```python
import torch

def _softmax(x, axis=-1):
    return torch.softmax(x, dim=axis)
def _gelu(x):
    return torch.nn.functional.gelu(x, approximate="tanh")
def _silu(x):
    return torch.nn.functional.silu(x)
def _sigmoid(x):
    return torch.sigmoid(x)
def _layernorm(x, axis=-1, eps=1e-5):
    m = x.mean(dim=axis, keepdim=True)
    v = x.var(dim=axis, keepdim=True, unbiased=False)
    return (x - m) / torch.sqrt(v + eps)
def _scatter_add(tgt, idx, src, axis=0):
    return tgt.clone().index_add_(axis, idx, src)
def _segment_sum(src, seg, n):
    out = torch.zeros((n,) + src.shape[1:], dtype=src.dtype)
    return out.index_add_(0, seg, src)
def _pad(x, pw):
    flat = tuple(v for pair in reversed(pw) for v in pair)
    return torch.nn.functional.pad(x, flat)

kernel_name = 'standalone__convnext_block__vision'

def setup_inputs():

    _w = (lambda a: (lambda t: t.to(torch.bfloat16) if t.is_floating_point() else t)(torch.as_tensor(a)))
    H = W = 512
    C = 96
    return {
        "x": _w((lambda *s: torch.randn(*s, dtype=torch.bfloat16) * 0.05)(1, H, W, C)),
        "dw": _w((lambda *s: torch.randn(*s, dtype=torch.bfloat16) * 0.05)(7, 7, C)),
        "w1": _w((lambda *s: torch.randn(*s, dtype=torch.bfloat16) * 0.05)(C, 4 * C)),
        "w2": _w((lambda *s: torch.randn(*s, dtype=torch.bfloat16) * 0.05)(4 * C, C)),
    }

def reference(x, dw, w1, w2):
    B, H, W, C = x.shape
    pad = 3
    xp = _pad(x, ((0,0),(pad,pad),(pad,pad),(0,0)))
    out = torch.zeros_like(x)
    for i in range(7):
        for j in range(7):
            out = out + xp[:, i:i+H, j:j+W, :] * dw[i, j][None, None, None, :]
    h = _layernorm(out, axis=-1)
    return x + _gelu(h @ w1) @ w2
```

```cpp
#include <hip/hip_runtime.h>
#include <hip/hip_bf16.h>
#include <stdint.h>

namespace {

typedef __attribute__((ext_vector_type(4))) float f32x4;
typedef __attribute__((ext_vector_type(4))) unsigned u32x4;
typedef __attribute__((ext_vector_type(8))) unsigned u32x8;
typedef __attribute__((ext_vector_type(2))) unsigned u32x2;
typedef __attribute__((address_space(3))) void* lds_ptr_t;

constexpr int IMG = 512;
constexpr int CH = 96;
constexpr int HID = 384;
constexpr int ROWB = IMG * CH * 2;
constexpr int LUTN = 3841;
constexpr int DWOFF = 15376;
constexpr int W1OFF = DWOFF + 96 * 192 + 32;
constexpr int XOFF = W1OFF + 2 * 6144;
constexpr int XROW = 8200;
constexpr int W2OFF = XOFF + 12 * XROW;
constexpr int PB = XOFF + 10 * XROW;
constexpr int PFOFF = W2OFF + 16384 + 2048;
constexpr int LDS_BYTES = PFOFF + 256;
static_assert(LUTN * 4 <= DWOFF && XOFF % 16 == 0 && LDS_BYTES <= 160 * 1024, "LDS map");
constexpr unsigned OOB = 0x40000000u;
constexpr unsigned GH1 = 0xcc9bcc9bu, GH2 = 0xd297d297u, GHH = 0x30003000u;

#define MAIN_ASM \
  "s_mov_b32 %[rnd], 0\n\t" \
  "s_mov_b32 %[op0], 0x40000000\n\t" \
  "s_mov_b32 %[op1], 0x40000000\n\t" \
  "v_add_u32 v232, 1536, %[aa]\n\t" \
  "ds_read2_b32 v[98:99], %[aa] offset0:0 offset1:6\n\t" \
  "ds_read2_b32 v[102:103], %[aa] offset0:12 offset1:18\n\t" \
  "ds_read2_b32 v[106:107], %[aa] offset0:24 offset1:30\n\t" \
  "ds_read2_b32 v[110:111], %[aa] offset0:36 offset1:42\n\t" \
  "ds_read2_b32 v[114:115], %[aa] offset0:192 offset1:198\n\t" \
  "ds_read2_b32 v[118:119], %[aa] offset0:204 offset1:210\n\t" \
  "ds_read2_b32 v[122:123], %[aa] offset0:216 offset1:222\n\t" \
  "ds_read2_b32 v[126:127], %[aa] offset0:228 offset1:234\n\t" \
  "ds_read2_b32 v[130:131], v232 offset0:0 offset1:6\n\t" \
  "ds_read2_b32 v[134:135], v232 offset0:12 offset1:18\n\t" \
  "ds_read2_b32 v[138:139], v232 offset0:24 offset1:30\n\t" \
  "ds_read2_b32 v[142:143], v232 offset0:36 offset1:42\n\t" \
  "v_mfma_f32_32x32x16_bf16 v[144:159], v[242:245], v[240:243], 0\n\t" \
  "v_mfma_f32_32x32x16_bf16 v[160:175], v[242:245], v[240:243], 0\n\t" \
  "s_waitcnt lgkmcnt(11)\n\t" \
  "v_and_b32 v96, 0xffff, v98\n\t" \
  "v_and_b32 v97, 0xffff0000, v98\n\t" \
  "v_and_b32 v98, 0xffff, v99\n\t" \
  "v_and_b32 v99, 0xffff0000, v99\n\t" \
  "s_waitcnt lgkmcnt(10)\n\t" \
  "v_and_b32 v100, 0xffff, v102\n\t" \
  "v_and_b32 v101, 0xffff0000, v102\n\t" \
  "v_and_b32 v102, 0xffff, v103\n\t" \
  "v_and_b32 v103, 0xffff0000, v103\n\t" \
  "s_waitcnt lgkmcnt(9)\n\t" \
  "v_and_b32 v104, 0xffff, v106\n\t" \
  "v_and_b32 v105, 0xffff0000, v106\n\t" \
  "v_and_b32 v106, 0xffff, v107\n\t" \
  "v_and_b32 v107, 0xffff0000, v107\n\t" \
  "s_waitcnt lgkmcnt(8)\n\t" \
  "v_and_b32 v108, 0xffff, v110\n\t" \
  "v_and_b32 v109, 0xffff0000, v110\n\t" \
  "v_and_b32 v110, 0xffff, v111\n\t" \
  "v_and_b32 v111, 0xffff0000, v111\n\t" \
  "s_waitcnt lgkmcnt(7)\n\t" \
  "v_and_b32 v112, 0xffff, v114\n\t" \
  "v_and_b32 v113, 0xffff0000, v114\n\t" \
  "v_and_b32 v114, 0xffff, v115\n\t" \
  "v_and_b32 v115, 0xffff0000, v115\n\t" \
  "s_waitcnt lgkmcnt(6)\n\t" \
  "v_and_b32 v116, 0xffff, v118\n\t" \
  "v_and_b32 v117, 0xffff0000, v118\n\t" \
  "v_and_b32 v118, 0xffff, v119\n\t" \
  "v_and_b32 v119, 0xffff0000, v119\n\t" \
  "s_waitcnt lgkmcnt(5)\n\t" \
  "v_and_b32 v120, 0xffff, v122\n\t" \
  "v_and_b32 v121, 0xffff0000, v122\n\t" \
  "v_and_b32 v122, 0xffff, v123\n\t" \
  "v_and_b32 v123, 0xffff0000, v123\n\t" \
  "s_waitcnt lgkmcnt(4)\n\t" \
  "v_and_b32 v124, 0xffff, v126\n\t" \
  "v_and_b32 v125, 0xffff0000, v126\n\t" \
  "v_and_b32 v126, 0xffff, v127\n\t" \
  "v_and_b32 v127, 0xffff0000, v127\n\t" \
  "s_waitcnt lgkmcnt(3)\n\t" \
  "v_and_b32 v128, 0xffff, v130\n\t" \
  "v_and_b32 v129, 0xffff0000, v130\n\t" \
  "v_and_b32 v130, 0xffff, v131\n\t" \
  "v_and_b32 v131, 0xffff0000, v131\n\t" \
  "s_waitcnt lgkmcnt(2)\n\t" \
  "v_and_b32 v132, 0xffff, v134\n\t" \
  "v_and_b32 v133, 0xffff0000, v134\n\t" \
  "v_and_b32 v134, 0xffff, v135\n\t" \
  "v_and_b32 v135, 0xffff0000, v135\n\t" \
  "s_waitcnt lgkmcnt(1)\n\t" \
  "v_and_b32 v136, 0xffff, v138\n\t" \
  "v_and_b32 v137, 0xffff0000, v138\n\t" \
  "v_and_b32 v138, 0xffff, v139\n\t" \
  "v_and_b32 v139, 0xffff0000, v139\n\t" \
  "s_waitcnt lgkmcnt(0)\n\t" \
  "v_and_b32 v140, 0xffff, v142\n\t" \
  "v_and_b32 v141, 0xffff0000, v142\n\t" \
  "v_and_b32 v142, 0xffff, v143\n\t" \
  "v_and_b32 v143, 0xffff0000, v143\n\t" \
  "v_bfe_u32 v236, %[l8], 3, 2\n\t" \
  "v_mul_u32_u24 v236, 0x5555, v236\n\t" \
  "s_waitcnt vmcnt(10)\n\t" \
  "s_barrier\n\t" \
  ".Lcnx_round:\n\t" \
  "v_add_u32 v232, 49200, %[cb]\n\t" \
  "v_add_u32 v233, %[w512], %[l8]\n\t" \
  "v_mfma_f32_32x32x16_bf16 v[176:191], v[242:245], v[240:243], 0\n\t" \
  "ds_read_b64 v[208:209], %[cb] offset:0\n\t" \
  "ds_read_b64 v[210:211], %[cb] offset:400\n\t" \
  "ds_read_b64 v[212:213], %[cb] offset:800\n\t" \
  "ds_read_b64 v[214:215], %[cb] offset:1200\n\t" \
  "ds_read_b64 v[216:217], %[cb] offset:8\n\t" \
  "ds_read_b64 v[218:219], %[cb] offset:408\n\t" \
  "ds_read_b64 v[220:221], %[cb] offset:808\n\t" \
  "ds_read_b64 v[222:223], %[cb] offset:1208\n\t" \
  "ds_read_b64 v[224:225], %[cb] offset:16\n\t" \
  "ds_read_b64 v[226:227], %[cb] offset:416\n\t" \
  "ds_read_b64 v[228:229], %[cb] offset:816\n\t" \
  "ds_read_b64 v[230:231], %[cb] offset:1216\n\t" \
  "s_waitcnt lgkmcnt(8)\n\t" \
  "v_smfmac_f32_16x16x64_bf16 v[144:147], v[96:99], v[208:215], v236\n\t" \
  "s_waitcnt lgkmcnt(4)\n\t" \
  "v_smfmac_f32_16x16x64_bf16 v[148:151], v[112:115], v[216:223], v236\n\t" \
  "s_waitcnt vmcnt(8) lgkmcnt(0)\n\t" \
  "s_barrier\n\t" \
  "ds_read_b64 v[208:209], %[cb] offset:16400\n\t" \
  "ds_read_b64 v[210:211], %[cb] offset:16800\n\t" \
  "ds_read_b64 v[212:213], %[cb] offset:17200\n\t" \
  "ds_read_b64 v[214:215], %[cb] offset:17600\n\t" \
  "ds_read_b64 v[216:217], %[cb] offset:16408\n\t" \
  "ds_read_b64 v[218:219], %[cb] offset:16808\n\t" \
  "ds_read_b64 v[220:221], %[cb] offset:17208\n\t" \
  "ds_read_b64 v[222:223], %[cb] offset:17608\n\t" \
  "s_add_u32 m0, %[xl], 0\n\t" \
  "s_add_u32 %[xo], %[xo], %[rs]\n\t" \
  "buffer_load_dwordx4 %[xv], %[xrs], %[xo] offen lds\n\t" \
  "s_add_u32 m0, %[xl], 8200\n\t" \
  "s_add_u32 %[xo], %[xo], %[rs]\n\t" \
  "buffer_load_dwordx4 %[xv], %[xrs], %[xo] offen lds\n\t" \
  "v_smfmac_f32_16x16x64_bf16 v[152:155], v[128:131], v[224:231], v236\n\t" \
  "ds_read_b64 v[224:225], %[cb] offset:16416\n\t" \
  "ds_read_b64 v[226:227], %[cb] offset:16816\n\t" \
  "ds_read_b64 v[228:229], %[cb] offset:17216\n\t" \
  "s_waitcnt lgkmcnt(7)\n\t" \
  "v_smfmac_f32_16x16x64_bf16 v[156:159], v[96:99], v[208:215], v236\n\t" \
  "ds_read_b64 v[230:231], %[cb] offset:17616\n\t" \
  "v_smfmac_f32_16x16x64_bf16 v[144:147], v[100:103], v[208:215], v236\n\t" \
  "s_waitcnt lgkmcnt(4)\n\t" \
  "v_smfmac_f32_16x16x64_bf16 v[160:163], v[112:115], v[216:223], v236\n\t" \
  "v_smfmac_f32_16x16x64_bf16 v[148:151], v[116:119], v[216:223], v236\n\t" \
  "s_waitcnt vmcnt(8) lgkmcnt(0)\n\t" \
  "s_barrier\n\t" \
  "ds_read_b64 v[208:209], %[cb] offset:32800\n\t" \
  "ds_read_b64 v[210:211], %[cb] offset:33200\n\t" \
  "ds_read_b64 v[212:213], %[cb] offset:33600\n\t" \
  "ds_read_b64 v[214:215], %[cb] offset:34000\n\t" \
  "ds_read_b64 v[216:217], %[cb] offset:32808\n\t" \
  "ds_read_b64 v[218:219], %[cb] offset:33208\n\t" \
  "ds_read_b64 v[220:221], %[cb] offset:33608\n\t" \
  "ds_read_b64 v[222:223], %[cb] offset:34008\n\t" \
  "s_add_u32 m0, %[xl], 16400\n\t" \
  "s_add_u32 %[xo], %[xo], %[rs]\n\t" \
  "buffer_load_dwordx4 %[xv], %[xrs], %[xo] offen lds\n\t" \
  "s_add_u32 m0, %[xl], 24600\n\t" \
  "s_add_u32 %[xo], %[xo], %[rs]\n\t" \
  "buffer_load_dwordx4 %[xv], %[xrs], %[xo] offen lds\n\t" \
  "v_smfmac_f32_16x16x64_bf16 v[164:167], v[128:131], v[224:231], v236\n\t" \
  "v_cvt_pk_bf16_f32 v48, v48, v49\n\t" \
  "v_cvt_pk_bf16_f32 v49, v50, v51\n\t" \
  "v_cvt_pk_bf16_f32 v50, v52, v53\n\t" \
  "v_cvt_pk_bf16_f32 v51, v54, v55\n\t" \
  "v_cvt_pk_bf16_f32 v52, v0, v1\n\t" \
  "v_cvt_pk_bf16_f32 v53, v2, v3\n\t" \
  "v_smfmac_f32_16x16x64_bf16 v[152:155], v[132:135], v[224:231], v236\n\t" \
  "ds_read_b64 v[224:225], %[cb] offset:32816\n\t" \
  "ds_read_b64 v[226:227], %[cb] offset:33216\n\t" \
  "ds_read_b64 v[228:229], %[cb] offset:33616\n\t" \
  "v_cvt_pk_bf16_f32 v54, v4, v5\n\t" \
  "v_cvt_pk_bf16_f32 v55, v6, v7\n\t" \
  "v_cvt_pk_bf16_f32 v56, v56, v57\n\t" \
  "s_waitcnt lgkmcnt(7)\n\t" \
  "v_smfmac_f32_16x16x64_bf16 v[168:171], v[96:99], v[208:215], v236\n\t" \
  "ds_read_b64 v[230:231], %[cb] offset:34016\n\t" \
  "v_cvt_pk_bf16_f32 v57, v58, v59\n\t" \
  "v_cvt_pk_bf16_f32 v58, v60, v61\n\t" \
  "v_cvt_pk_bf16_f32 v59, v62, v63\n\t" \
  "v_smfmac_f32_16x16x64_bf16 v[156:159], v[100:103], v[208:215], v236\n\t" \
  "v_cvt_pk_bf16_f32 v60, v8, v9\n\t" \
  "v_cvt_pk_bf16_f32 v61, v10, v11\n\t" \
  "v_cvt_pk_bf16_f32 v62, v12, v13\n\t" \
  "v_smfmac_f32_16x16x64_bf16 v[144:147], v[104:107], v[208:215], v236\n\t" \
  "v_cvt_pk_bf16_f32 v63, v14, v15\n\t" \
  "v_mfma_f32_32x32x16_bf16 v[0:15], v[242:245], v[240:243], 0\n\t" \
  "s_waitcnt lgkmcnt(4)\n\t" \
  "v_smfmac_f32_16x16x64_bf16 v[172:175], v[112:115], v[216:223], v236\n\t" \
  "v_smfmac_f32_16x16x64_bf16 v[160:163], v[116:119], v[216:223], v236\n\t" \
  "v_smfmac_f32_16x16x64_bf16 v[148:151], v[120:123], v[216:223], v236\n\t" \
  "s_cmp_lg_u32 %[rnd], 0\n\t" \
  "s_cbranch_scc1 .Lcnx_c1\n\t" \
  "v_lshlrev_b32 v88, 14, v233\n\t" \
  "v_lshl_add_u32 v89, v233, 1, %[lb]\n\t" \
  "v_mov_b32 v90, 0x40135761\n\t" \
  "v_add_u32 v48, 0xbc603000, v88\n\t" \
  "v_add_u32 v49, 0xbc60b000, v88\n\t" \
  "v_add_u32 v50, 0xbc613000, v88\n\t" \
  "v_add_u32 v51, 0xbc61b000, v88\n\t" \
  "v_add_u32 v52, 0xc0603000, v88\n\t" \
  "v_add_u32 v53, 0xc060b000, v88\n\t" \
  "v_add_u32 v54, 0xc0613000, v88\n\t" \
  "v_add_u32 v55, 0xc061b000, v88\n\t" \
  "v_fma_f32 v56, 2.0, v48, %[m11]\n\t" \
  "v_fma_f32 v57, 2.0, v49, %[m11]\n\t" \
  "v_fma_f32 v58, 2.0, v50, %[m11]\n\t" \
  "v_fma_f32 v59, 2.0, v51, %[m11]\n\t" \
  "v_min_f32 v48, v48, v56\n\t" \
  "v_min_f32 v49, v49, v57\n\t" \
  "v_min_f32 v50, v50, v58\n\t" \
  "v_min_f32 v51, v51, v59\n\t" \
  "v_mul_f32 v56, v48, v48\n\t" \
  "v_mul_f32 v57, v49, v49\n\t" \
  "v_mul_f32 v58, v50, v50\n\t" \
  "v_mul_f32 v59, v51, v51\n\t" \
  "v_mul_f32 v60, v52, v52\n\t" \
  "v_mul_f32 v61, v53, v53\n\t" \
  "v_mul_f32 v62, v54, v54\n\t" \
  "v_mul_f32 v63, v55, v55\n\t" \
  "v_fmamk_f32 v56, v56, 0x3dd2d3e8, v90\n\t" \
  "v_fmamk_f32 v57, v57, 0x3dd2d3e8, v90\n\t" \
  "v_fmamk_f32 v58, v58, 0x3dd2d3e8, v90\n\t" \
  "v_fmamk_f32 v59, v59, 0x3dd2d3e8, v90\n\t" \
  "v_fmamk_f32 v60, v60, 0x3dd2d3e8, v90\n\t" \
  "v_fmamk_f32 v61, v61, 0x3dd2d3e8, v90\n\t" \
  "v_fmamk_f32 v62, v62, 0x3dd2d3e8, v90\n\t" \
  "v_fmamk_f32 v63, v63, 0x3dd2d3e8, v90\n\t" \
  "v_mul_f32 v56, v56, v48\n\t" \
  "v_mul_f32 v57, v57, v49\n\t" \
  "v_mul_f32 v58, v58, v50\n\t" \
  "v_mul_f32 v59, v59, v51\n\t" \
  "v_mul_f32 v60, v60, v52\n\t" \
  "v_mul_f32 v61, v61, v53\n\t" \
  "v_mul_f32 v62, v62, v54\n\t" \
  "v_mul_f32 v63, v63, v55\n\t" \
  "v_exp_f32 v56, v56\n\t" \
  "v_exp_f32 v57, v57\n\t" \
  "v_exp_f32 v58, v58\n\t" \
  "v_exp_f32 v59, v59\n\t" \
  "v_exp_f32 v60, v60\n\t" \
  "v_exp_f32 v61, v61\n\t" \
  "v_exp_f32 v62, v62\n\t" \
  "v_exp_f32 v63, v63\n\t" \
  "v_add_f32 v56, 1.0, v56\n\t" \
  "v_add_f32 v57, 1.0, v57\n\t" \
  "v_add_f32 v58, 1.0, v58\n\t" \
  "v_add_f32 v59, 1.0, v59\n\t" \
  "v_add_f32 v60, 1.0, v60\n\t" \
  "v_add_f32 v61, 1.0, v61\n\t" \
  "v_add_f32 v62, 1.0, v62\n\t" \
  "v_add_f32 v63, 1.0, v63\n\t" \
  "v_rcp_f32 v56, v56\n\t" \
  "v_rcp_f32 v57, v57\n\t" \
  "v_rcp_f32 v58, v58\n\t" \
  "v_rcp_f32 v59, v59\n\t" \
  "v_rcp_f32 v60, v60\n\t" \
  "v_rcp_f32 v61, v61\n\t" \
  "v_rcp_f32 v62, v62\n\t" \
  "v_rcp_f32 v63, v63\n\t" \
  "v_fma_f32 v56, -2.0, v56, 1.0\n\t" \
  "v_fma_f32 v57, -2.0, v57, 1.0\n\t" \
  "v_fma_f32 v58, -2.0, v58, 1.0\n\t" \
  "v_fma_f32 v59, -2.0, v59, 1.0\n\t" \
  "v_fma_f32 v60, -2.0, v60, 1.0\n\t" \
  "v_fma_f32 v61, -2.0, v61, 1.0\n\t" \
  "v_fma_f32 v62, -2.0, v62, 1.0\n\t" \
  "v_fma_f32 v63, -2.0, v63, 1.0\n\t" \
  "ds_write_b128 v89, v[56:59]\n\t" \
  "s_cmp_eq_u32 %[w512], 3584\n\t" \
  "s_cselect_b64 exec, 1, -1\n\t" \
  "ds_write_b128 v89, v[60:63] offset:8192\n\t" \
  "s_mov_b64 exec, -1\n\t" \
  ".Lcnx_c1:\n\t" \
  "s_waitcnt vmcnt(8) lgkmcnt(0)\n\t" \
  "s_barrier\n\t" \
  "ds_read_b64 v[208:209], v232 offset:0\n\t" \
  "ds_read_b64 v[210:211], v232 offset:400\n\t" \
  "ds_read_b64 v[212:213], v232 offset:800\n\t" \
  "ds_read_b64 v[214:215], v232 offset:1200\n\t" \
  "ds_read_b64 v[216:217], v232 offset:8\n\t" \
  "ds_read_b64 v[218:219], v232 offset:408\n\t" \
  "ds_read_b64 v[220:221], v232 offset:808\n\t" \
  "ds_read_b64 v[222:223], v232 offset:1208\n\t" \
  "s_add_u32 m0, %[xl], 32800\n\t" \
  "s_add_u32 %[xo], %[xo], %[rs]\n\t" \
  "buffer_load_dwordx4 %[xv], %[xrs], %[xo] offen lds\n\t" \
  "s_add_u32 m0, %[xl], 41000\n\t" \
  "s_add_u32 %[xo], %[xo], %[rs]\n\t" \
  "buffer_load_dwordx4 %[xv], %[xrs], %[xo] offen lds\n\t" \
  "v_smfmac_f32_16x16x64_bf16 v[176:179], v[128:131], v[224:231], v236\n\t" \
  "v_smfmac_f32_16x16x64_bf16 v[164:167], v[132:135], v[224:231], v236\n\t" \
  "v_smfmac_f32_16x16x64_bf16 v[152:155], v[136:139], v[224:231], v236\n\t" \
  "ds_read_b64 v[224:225], v232 offset:16\n\t" \
  "s_waitcnt lgkmcnt(5)\n\t" \
  "v_smfmac_f32_16x16x64_bf16 v[180:183], v[96:99], v[208:215], v236\n\t" \
  "ds_read_b64 v[226:227], v232 offset:416\n\t" \
  "v_smfmac_f32_16x16x64_bf16 v[168:171], v[100:103], v[208:215], v236\n\t" \
  "ds_read_b64 v[228:229], v232 offset:816\n\t" \
  "v_cvt_pk_bf16_f32 v64, v64, v65\n\t" \
  "v_cvt_pk_bf16_f32 v65, v66, v67\n\t" \
  "v_cvt_pk_bf16_f32 v66, v68, v69\n\t" \
  "v_cvt_pk_bf16_f32 v67, v70, v71\n\t" \
  "v_cvt_pk_bf16_f32 v68, v16, v17\n\t" \
  "v_cvt_pk_bf16_f32 v69, v18, v19\n\t" \
  "v_smfmac_f32_16x16x64_bf16 v[156:159], v[104:107], v[208:215], v236\n\t" \
  "ds_read_b64 v[230:231], v232 offset:1216\n\t" \
  "v_cvt_pk_bf16_f32 v70, v20, v21\n\t" \
  "v_cvt_pk_bf16_f32 v71, v22, v23\n\t" \
  "v_cvt_pk_bf16_f32 v72, v72, v73\n\t" \
  "v_smfmac_f32_16x16x64_bf16 v[144:147], v[108:111], v[208:215], v236\n\t" \
  "v_cvt_pk_bf16_f32 v73, v74, v75\n\t" \
  "v_cvt_pk_bf16_f32 v74, v76, v77\n\t" \
  "v_cvt_pk_bf16_f32 v75, v78, v79\n\t" \
  "s_waitcnt lgkmcnt(4)\n\t" \
  "v_smfmac_f32_16x16x64_bf16 v[184:187], v[112:115], v[216:223], v236\n\t" \
  "v_cvt_pk_bf16_f32 v76, v24, v25\n\t" \
  "v_cvt_pk_bf16_f32 v77, v26, v27\n\t" \
  "v_cvt_pk_bf16_f32 v78, v28, v29\n\t" \
  "v_smfmac_f32_16x16x64_bf16 v[172:175], v[116:119], v[216:223], v236\n\t" \
  "v_cvt_pk_bf16_f32 v79, v30, v31\n\t" \
  "v_mfma_f32_32x32x16_bf16 v[16:31], v[242:245], v[240:243], 0\n\t" \
  "v_smfmac_f32_16x16x64_bf16 v[160:163], v[120:123], v[216:223], v236\n\t" \
  "v_mul_f32 v193, v144, v144\n\t" \
  "v_add_f32 v192, v144, v145\n\t" \
  "v_fmac_f32 v193, v145, v145\n\t" \
  "v_add_f32 v192, v192, v146\n\t" \
  "v_smfmac_f32_16x16x64_bf16 v[148:151], v[124:127], v[216:223], v236\n\t" \
  "v_fmac_f32 v193, v146, v146\n\t" \
  "v_add_f32 v192, v192, v147\n\t" \
  "v_fmac_f32 v193, v147, v147\n\t" \
  "s_waitcnt vmcnt(8) lgkmcnt(0)\n\t" \
  "s_barrier\n\t" \
  "ds_read_b64 v[208:209], v232 offset:16400\n\t" \
  "ds_read_b64 v[210:211], v232 offset:16800\n\t" \
  "ds_read_b64 v[212:213], v232 offset:17200\n\t" \
  "ds_read_b64 v[214:215], v232 offset:17600\n\t" \
  "ds_read_b64 v[216:217], v232 offset:16408\n\t" \
  "ds_read_b64 v[218:219], v232 offset:16808\n\t" \
  "ds_read_b64 v[220:221], v232 offset:17208\n\t" \
  "ds_read_b64 v[222:223], v232 offset:17608\n\t" \
  "s_add_u32 m0, %[xl], 49200\n\t" \
  "s_add_u32 %[xo], %[xo], %[rs]\n\t" \
  "buffer_load_dwordx4 %[xv], %[xrs], %[xo] offen lds\n\t" \
  "s_add_u32 m0, %[xl], 57400\n\t" \
  "s_add_u32 %[xo], %[xo], %[rs]\n\t" \
  "buffer_load_dwordx4 %[xv], %[xrs], %[xo] offen lds\n\t" \
  "v_smfmac_f32_16x16x64_bf16 v[188:191], v[128:131], v[224:231], v236\n\t" \
  "v_smfmac_f32_16x16x64_bf16 v[176:179], v[132:135], v[224:231], v236\n\t" \
  "v_smfmac_f32_16x16x64_bf16 v[164:167], v[136:139], v[224:231], v236\n\t" \
  "v_add_f32 v192, v192, v148\n\t" \
  "v_fmac_f32 v193, v148, v148\n\t" \
  "v_add_f32 v192, v192, v149\n\t" \
  "v_fmac_f32 v193, v149, v149\n\t" \
  "v_add_f32 v192, v192, v150\n\t" \
  "v_fmac_f32 v193, v150, v150\n\t" \
  "v_smfmac_f32_16x16x64_bf16 v[152:155], v[140:143], v[224:231], v236\n\t" \
  "ds_read_b64 v[224:225], v232 offset:16416\n\t" \
  "v_add_f32 v192, v192, v151\n\t" \
  "v_fmac_f32 v193, v151, v151\n\t" \
  "s_waitcnt lgkmcnt(5)\n\t" \
  "v_smfmac_f32_16x16x64_bf16 v[0:3], v[96:99], v[208:215], v236\n\t" \
  "ds_read_b64 v[226:227], v232 offset:16816\n\t" \
  "v_smfmac_f32_16x16x64_bf16 v[180:183], v[100:103], v[208:215], v236\n\t" \
  "ds_read_b64 v[228:229], v232 offset:17216\n\t" \
  "v_cvt_pk_bf16_f32 v80, v80, v81\n\t" \
  "v_cvt_pk_bf16_f32 v81, v82, v83\n\t" \
  "v_cvt_pk_bf16_f32 v82, v84, v85\n\t" \
  "v_cvt_pk_bf16_f32 v83, v86, v87\n\t" \
  "v_cvt_pk_bf16_f32 v84, v32, v33\n\t" \
  "v_cvt_pk_bf16_f32 v85, v34, v35\n\t" \
  "v_smfmac_f32_16x16x64_bf16 v[168:171], v[104:107], v[208:215], v236\n\t" \
  "ds_read_b64 v[230:231], v232 offset:17616\n\t" \
  "v_cvt_pk_bf16_f32 v86, v36, v37\n\t" \
  "v_add_f32 v192, v192, v152\n\t" \
  "v_cvt_pk_bf16_f32 v87, v38, v39\n\t" \
  "v_smfmac_f32_16x16x64_bf16 v[156:159], v[108:111], v[208:215], v236\n\t" \
  "v_cvt_pk_bf16_f32 v88, v88, v89\n\t" \
  "v_fmac_f32 v193, v152, v152\n\t" \
  "v_cvt_pk_bf16_f32 v89, v90, v91\n\t" \
  "s_waitcnt lgkmcnt(4)\n\t" \
  "v_smfmac_f32_16x16x64_bf16 v[4:7], v[112:115], v[216:223], v236\n\t" \
  "v_add_f32 v192, v192, v153\n\t" \
  "v_cvt_pk_bf16_f32 v90, v92, v93\n\t" \
  "v_fmac_f32 v193, v153, v153\n\t" \
  "v_smfmac_f32_16x16x64_bf16 v[184:187], v[116:119], v[216:223], v236\n\t" \
  "v_add_f32 v192, v192, v154\n\t" \
  "v_cvt_pk_bf16_f32 v91, v94, v95\n\t" \
  "v_fmac_f32 v193, v154, v154\n\t" \
  "v_smfmac_f32_16x16x64_bf16 v[172:175], v[120:123], v[216:223], v236\n\t" \
  "v_add_f32 v192, v192, v155\n\t" \
  "v_cvt_pk_bf16_f32 v92, v40, v41\n\t" \
  "v_fmac_f32 v193, v155, v155\n\t" \
  "v_smfmac_f32_16x16x64_bf16 v[160:163], v[124:127], v[216:223], v236\n\t" \
  "v_mul_f32 v195, v156, v156\n\t" \
  "v_cvt_pk_bf16_f32 v93, v42, v43\n\t" \
  "ds_write_b64 v233, v[192:193] offset:16400\n\t" \
  "s_waitcnt vmcnt(8) lgkmcnt(0)\n\t" \
  "s_barrier\n\t" \
  "ds_read_b64 v[208:209], v232 offset:32800\n\t" \
  "ds_read_b64 v[210:211], v232 offset:33200\n\t" \
  "ds_read_b64 v[212:213], v232 offset:33600\n\t" \
  "ds_read_b64 v[214:215], v232 offset:34000\n\t" \
  "ds_read_b64 v[216:217], v232 offset:32808\n\t" \
  "ds_read_b64 v[218:219], v232 offset:33208\n\t" \
  "ds_read_b64 v[220:221], v232 offset:33608\n\t" \
  "ds_read_b64 v[222:223], v232 offset:34008\n\t" \
  "s_add_u32 m0, %[xl], 65600\n\t" \
  "s_add_u32 %[xo], %[xo], %[rs]\n\t" \
  "buffer_load_dwordx4 %[xv], %[xrs], %[xo] offen lds\n\t" \
  "s_add_u32 m0, %[xl], 73800\n\t" \
  "s_add_u32 %[xo], %[xo], %[rs]\n\t" \
  "buffer_load_dwordx4 %[xv], %[xrs], %[xo] offen lds\n\t" \
  "v_smfmac_f32_16x16x64_bf16 v[8:11], v[128:131], v[224:231], v236\n\t" \
  "v_add_f32 v194, v156, v157\n\t" \
  "v_cvt_pk_bf16_f32 v94, v44, v45\n\t" \
  "v_fmac_f32 v195, v157, v157\n\t" \
  "v_smfmac_f32_16x16x64_bf16 v[188:191], v[132:135], v[224:231], v236\n\t" \
  "v_add_f32 v194, v194, v158\n\t" \
  "v_cvt_pk_bf16_f32 v95, v46, v47\n\t" \
  "v_fmac_f32 v195, v158, v158\n\t" \
  "v_smfmac_f32_16x16x64_bf16 v[176:179], v[136:139], v[224:231], v236\n\t" \
  "v_add_f32 v194, v194, v159\n\t" \
  "v_mfma_f32_32x32x16_bf16 v[32:47], v[242:245], v[240:243], 0\n\t" \
  "v_fmac_f32 v195, v159, v159\n\t" \
  "v_smfmac_f32_16x16x64_bf16 v[164:167], v[140:143], v[224:231], v236\n\t" \
  "ds_read_b64 v[224:225], v232 offset:32816\n\t" \
  "v_add_f32 v194, v194, v160\n\t" \
  "v_fmac_f32 v195, v160, v160\n\t" \
  "v_add_f32 v194, v194, v161\n\t" \
  "s_waitcnt lgkmcnt(5)\n\t" \
  "v_smfmac_f32_16x16x64_bf16 v[12:15], v[96:99], v[208:215], v236\n\t" \
  "ds_read_b64 v[226:227], v232 offset:33216\n\t" \
  "v_fmac_f32 v195, v161, v161\n\t" \
  "v_add_f32 v194, v194, v162\n\t" \
  "v_fmac_f32 v195, v162, v162\n\t" \
  "v_smfmac_f32_16x16x64_bf16 v[0:3], v[100:103], v[208:215], v236\n\t" \
  "ds_read_b64 v[228:229], v232 offset:33616\n\t" \
  "v_add_f32 v194, v194, v163\n\t" \
  "v_fmac_f32 v195, v163, v163\n\t" \
  "v_smfmac_f32_16x16x64_bf16 v[180:183], v[104:107], v[208:215], v236\n\t" \
  "buffer_store_dwordx4 v[52:55], %[vo], %[ors], %[op0] offen offset:0 sc1\n\t" \
  "ds_read_b64 v[230:231], v232 offset:34016\n\t" \
  "v_add_f32 v194, v194, v164\n\t" \
  "v_fmac_f32 v195, v164, v164\n\t" \
  "v_add_f32 v194, v194, v165\n\t" \
  "v_smfmac_f32_16x16x64_bf16 v[168:171], v[108:111], v[208:215], v236\n\t" \
  "v_fmac_f32 v195, v165, v165\n\t" \
  "v_add_f32 v194, v194, v166\n\t" \
  "v_fmac_f32 v195, v166, v166\n\t" \
  "s_waitcnt lgkmcnt(4)\n\t" \
  "v_smfmac_f32_16x16x64_bf16 v[16:19], v[112:115], v[216:223], v236\n\t" \
  "v_add_f32 v194, v194, v167\n\t" \
  "v_fmac_f32 v195, v167, v167\n\t" \
  "v_smfmac_f32_16x16x64_bf16 v[4:7], v[116:119], v[216:223], v236\n\t" \
  "ds_write_b64 v233, v[194:195] offset:20496\n\t" \
  "v_smfmac_f32_16x16x64_bf16 v[184:187], v[120:123], v[216:223], v236\n\t" \
  "v_mul_f32 v197, v168, v168\n\t" \
  "v_add_f32 v196, v168, v169\n\t" \
  "v_fmac_f32 v197, v169, v169\n\t" \
  "v_add_f32 v196, v196, v170\n\t" \
  "v_fmac_f32 v197, v170, v170\n\t" \
  "v_add_f32 v196, v196, v171\n\t" \
  "v_smfmac_f32_16x16x64_bf16 v[172:175], v[124:127], v[216:223], v236\n\t" \
  "v_fmac_f32 v197, v171, v171\n\t" \
  "s_waitcnt vmcnt(9) lgkmcnt(0)\n\t" \
  "s_barrier\n\t" \
  "ds_read_b64 v[208:209], %[cb] offset:0\n\t" \
  "ds_read_b64 v[210:211], %[cb] offset:400\n\t" \
  "ds_read_b64 v[212:213], %[cb] offset:800\n\t" \
  "ds_read_b64 v[214:215], %[cb] offset:1200\n\t" \
  "ds_read_b64 v[216:217], %[cb] offset:8\n\t" \
  "ds_read_b64 v[218:219], %[cb] offset:408\n\t" \
  "ds_read_b64 v[220:221], %[cb] offset:808\n\t" \
  "ds_read_b64 v[222:223], %[cb] offset:1208\n\t" \
  "v_smfmac_f32_16x16x64_bf16 v[20:23], v[128:131], v[224:231], v236\n\t" \
  "v_smfmac_f32_16x16x64_bf16 v[8:11], v[132:135], v[224:231], v236\n\t" \
  "buffer_store_dwordx4 v[60:63], %[vo], %[ors], %[op0] offen offset:64 sc1\n\t" \
  "v_smfmac_f32_16x16x64_bf16 v[188:191], v[136:139], v[224:231], v236\n\t" \
  "v_add_f32 v196, v196, v172\n\t" \
  "v_fmac_f32 v197, v172, v172\n\t" \
  "v_add_f32 v196, v196, v173\n\t" \
  "v_fmac_f32 v197, v173, v173\n\t" \
  "v_add_f32 v196, v196, v174\n\t" \
  "v_fmac_f32 v197, v174, v174\n\t" \
  "v_smfmac_f32_16x16x64_bf16 v[176:179], v[140:143], v[224:231], v236\n\t" \
  "ds_read_b64 v[224:225], %[cb] offset:16\n\t" \
  "v_add_f32 v196, v196, v175\n\t" \
  "v_fmac_f32 v197, v175, v175\n\t" \
  "s_waitcnt lgkmcnt(5)\n\t" \
  "v_smfmac_f32_16x16x64_bf16 v[24:27], v[96:99], v[208:215], v236\n\t" \
  "ds_read_b64 v[226:227], %[cb] offset:416\n\t" \
  "v_smfmac_f32_16x16x64_bf16 v[12:15], v[100:103], v[208:215], v236\n\t" \
  "ds_read_b64 v[228:229], %[cb] offset:816\n\t" \
  "v_smfmac_f32_16x16x64_bf16 v[0:3], v[104:107], v[208:215], v236\n\t" \
  "ds_read_b64 v[230:231], %[cb] offset:1216\n\t" \
  "v_add_f32 v196, v196, v176\n\t" \
  "v_fmac_f32 v197, v176, v176\n\t" \
  "v_add_f32 v196, v196, v177\n\t" \
  "v_fmac_f32 v197, v177, v177\n\t" \
  "v_add_f32 v196, v196, v178\n\t" \
  "v_fmac_f32 v197, v178, v178\n\t" \
  "v_smfmac_f32_16x16x64_bf16 v[180:183], v[108:111], v[208:215], v236\n\t" \
  "v_add_f32 v196, v196, v179\n\t" \
  "v_fmac_f32 v197, v179, v179\n\t" \
  "s_waitcnt lgkmcnt(4)\n\t" \
  "v_smfmac_f32_16x16x64_bf16 v[28:31], v[112:115], v[216:223], v236\n\t" \
  "buffer_store_dwordx4 v[68:71], %[vo], %[ors], %[op0] offen offset:128 sc1\n\t" \
  "v_smfmac_f32_16x16x64_bf16 v[16:19], v[116:119], v[216:223], v236\n\t" \
  "ds_write_b64 v233, v[196:197] offset:24592\n\t" \
  "v_smfmac_f32_16x16x64_bf16 v[4:7], v[120:123], v[216:223], v236\n\t" \
  "v_mul_f32 v199, v180, v180\n\t" \
  "v_add_f32 v198, v180, v181\n\t" \
  "v_fmac_f32 v199, v181, v181\n\t" \
  "v_add_f32 v198, v198, v182\n\t" \
  "v_fmac_f32 v199, v182, v182\n\t" \
  "v_add_f32 v198, v198, v183\n\t" \
  "v_smfmac_f32_16x16x64_bf16 v[184:187], v[124:127], v[216:223], v236\n\t" \
  "v_fmac_f32 v199, v183, v183\n\t" \
  "s_waitcnt vmcnt(9) lgkmcnt(0)\n\t" \
  "s_barrier\n\t" \
  "ds_read_b64 v[208:209], %[cb] offset:16400\n\t" \
  "ds_read_b64 v[210:211], %[cb] offset:16800\n\t" \
  "ds_read_b64 v[212:213], %[cb] offset:17200\n\t" \
  "ds_read_b64 v[214:215], %[cb] offset:17600\n\t" \
  "ds_read_b64 v[216:217], %[cb] offset:16408\n\t" \
  "ds_read_b64 v[218:219], %[cb] offset:16808\n\t" \
  "ds_read_b64 v[220:221], %[cb] offset:17208\n\t" \
  "ds_read_b64 v[222:223], %[cb] offset:17608\n\t" \
  "v_smfmac_f32_16x16x64_bf16 v[32:35], v[128:131], v[224:231], v236\n\t" \
  "v_smfmac_f32_16x16x64_bf16 v[20:23], v[132:135], v[224:231], v236\n\t" \
  "v_smfmac_f32_16x16x64_bf16 v[8:11], v[136:139], v[224:231], v236\n\t" \
  "v_add_f32 v198, v198, v184\n\t" \
  "v_fmac_f32 v199, v184, v184\n\t" \
  "v_add_f32 v198, v198, v185\n\t" \
  "v_fmac_f32 v199, v185, v185\n\t" \
  "v_add_f32 v198, v198, v186\n\t" \
  "v_smfmac_f32_16x16x64_bf16 v[188:191], v[140:143], v[224:231], v236\n\t" \
  "buffer_store_dwordx4 v[76:79], %[vo], %[ors], %[op0] offen offset:192 sc1\n\t" \
  "ds_read_b64 v[224:225], %[cb] offset:16416\n\t" \
  "v_fmac_f32 v199, v186, v186\n\t" \
  "v_add_f32 v198, v198, v187\n\t" \
  "v_fmac_f32 v199, v187, v187\n\t" \
  "s_waitcnt lgkmcnt(5)\n\t" \
  "v_smfmac_f32_16x16x64_bf16 v[36:39], v[96:99], v[208:215], v236\n\t" \
  "ds_read_b64 v[226:227], %[cb] offset:16816\n\t" \
  "v_smfmac_f32_16x16x64_bf16 v[24:27], v[100:103], v[208:215], v236\n\t" \
  "ds_read_b64 v[228:229], %[cb] offset:17216\n\t" \
  "v_smfmac_f32_16x16x64_bf16 v[12:15], v[104:107], v[208:215], v236\n\t" \
  "ds_read_b64 v[230:231], %[cb] offset:17616\n\t" \
  "v_add_f32 v198, v198, v188\n\t" \
  "v_fmac_f32 v199, v188, v188\n\t" \
  "v_add_f32 v198, v198, v189\n\t" \
  "v_fmac_f32 v199, v189, v189\n\t" \
  "v_add_f32 v198, v198, v190\n\t" \
  "v_fmac_f32 v199, v190, v190\n\t" \
  "v_smfmac_f32_16x16x64_bf16 v[0:3], v[108:111], v[208:215], v236\n\t" \
  "v_add_f32 v198, v198, v191\n\t" \
  "v_fmac_f32 v199, v191, v191\n\t" \
  "s_waitcnt lgkmcnt(4)\n\t" \
  "v_smfmac_f32_16x16x64_bf16 v[40:43], v[112:115], v[216:223], v236\n\t" \
  "ds_write_b64 v233, v[198:199] offset:28688\n\t" \
  "v_smfmac_f32_16x16x64_bf16 v[28:31], v[116:119], v[216:223], v236\n\t" \
  "v_smfmac_f32_16x16x64_bf16 v[16:19], v[120:123], v[216:223], v236\n\t" \
  "buffer_store_dwordx4 v[84:87], %[vo], %[ors], %[op0] offen offset:256 sc1\n\t" \
  "v_mul_f32 v201, v0, v0\n\t" \
  "v_add_f32 v200, v0, v1\n\t" \
  "v_fmac_f32 v201, v1, v1\n\t" \
  "v_add_f32 v200, v200, v2\n\t" \
  "v_fmac_f32 v201, v2, v2\n\t" \
  "v_add_f32 v200, v200, v3\n\t" \
  "v_smfmac_f32_16x16x64_bf16 v[4:7], v[124:127], v[216:223], v236\n\t" \
  "v_fmac_f32 v201, v3, v3\n\t" \
  "s_waitcnt vmcnt(9) lgkmcnt(0)\n\t" \
  "s_barrier\n\t" \
  "ds_read_b64 v[208:209], %[cb] offset:32800\n\t" \
  "ds_read_b64 v[210:211], %[cb] offset:33200\n\t" \
  "ds_read_b64 v[212:213], %[cb] offset:33600\n\t" \
  "ds_read_b64 v[214:215], %[cb] offset:34000\n\t" \
  "ds_read_b64 v[216:217], %[cb] offset:32808\n\t" \
  "ds_read_b64 v[218:219], %[cb] offset:33208\n\t" \
  "ds_read_b64 v[220:221], %[cb] offset:33608\n\t" \
  "ds_read_b64 v[222:223], %[cb] offset:34008\n\t" \
  "v_smfmac_f32_16x16x64_bf16 v[44:47], v[128:131], v[224:231], v236\n\t" \
  "v_smfmac_f32_16x16x64_bf16 v[32:35], v[132:135], v[224:231], v236\n\t" \
  "v_smfmac_f32_16x16x64_bf16 v[20:23], v[136:139], v[224:231], v236\n\t" \
  "v_add_f32 v200, v200, v4\n\t" \
  "v_fmac_f32 v201, v4, v4\n\t" \
  "v_add_f32 v200, v200, v5\n\t" \
  "v_fmac_f32 v201, v5, v5\n\t" \
  "v_add_f32 v200, v200, v6\n\t" \
  "v_fmac_f32 v201, v6, v6\n\t" \
  "v_smfmac_f32_16x16x64_bf16 v[8:11], v[140:143], v[224:231], v236\n\t" \
  "ds_read_b64 v[224:225], %[cb] offset:32816\n\t" \
  "v_add_f32 v200, v200, v7\n\t" \
  "v_fmac_f32 v201, v7, v7\n\t" \
  "s_waitcnt lgkmcnt(5)\n\t" \
  "v_smfmac_f32_16x16x64_bf16 v[36:39], v[100:103], v[208:215], v236\n\t" \
  "ds_read_b64 v[226:227], %[cb] offset:33216\n\t" \
  "v_smfmac_f32_16x16x64_bf16 v[24:27], v[104:107], v[208:215], v236\n\t" \
  "buffer_store_dwordx4 v[92:95], %[vo], %[ors], %[op0] offen offset:320 sc1\n\t" \
  "ds_read_b64 v[228:229], %[cb] offset:33616\n\t" \
  "v_smfmac_f32_16x16x64_bf16 v[12:15], v[108:111], v[208:215], v236\n\t" \
  "ds_read_b64 v[230:231], %[cb] offset:34016\n\t" \
  "v_add_f32 v200, v200, v8\n\t" \
  "v_fmac_f32 v201, v8, v8\n\t" \
  "v_add_f32 v200, v200, v9\n\t" \
  "v_fmac_f32 v201, v9, v9\n\t" \
  "v_add_f32 v200, v200, v10\n\t" \
  "v_fmac_f32 v201, v10, v10\n\t" \
  "s_waitcnt lgkmcnt(4)\n\t" \
  "v_smfmac_f32_16x16x64_bf16 v[40:43], v[116:119], v[216:223], v236\n\t" \
  "v_add_f32 v200, v200, v11\n\t" \
  "v_fmac_f32 v201, v11, v11\n\t" \
  "v_smfmac_f32_16x16x64_bf16 v[28:31], v[120:123], v[216:223], v236\n\t" \
  "ds_write_b64 v233, v[200:201] offset:0\n\t" \
  "v_smfmac_f32_16x16x64_bf16 v[16:19], v[124:127], v[216:223], v236\n\t" \
  "v_mul_f32 v203, v12, v12\n\t" \
  "v_add_f32 v202, v12, v13\n\t" \
  "v_fmac_f32 v203, v13, v13\n\t" \
  "v_add_f32 v202, v202, v14\n\t" \
  "v_fmac_f32 v203, v14, v14\n\t" \
  "v_add_f32 v202, v202, v15\n\t" \
  "s_waitcnt vmcnt(8) lgkmcnt(0)\n\t" \
  "s_barrier\n\t" \
  "ds_read_b64 v[208:209], v232 offset:0\n\t" \
  "ds_read_b64 v[210:211], v232 offset:400\n\t" \
  "ds_read_b64 v[212:213], v232 offset:800\n\t" \
  "ds_read_b64 v[214:215], v232 offset:1200\n\t" \
  "ds_read_b64 v[216:217], v232 offset:8\n\t" \
  "ds_read_b64 v[218:219], v232 offset:408\n\t" \
  "ds_read_b64 v[220:221], v232 offset:808\n\t" \
  "ds_read_b64 v[222:223], v232 offset:1208\n\t" \
  "s_cmp_lg_u32 %[grp], 0\n\t" \
  "s_cbranch_scc1 .Lcnx_c2\n\t" \
  "v_add_u32 v128, %[sr], %[l8]\n\t" \
  "ds_read_b64 v[96:97], v128 offset:0\n\t" \
  "ds_read_b64 v[98:99], v128 offset:512\n\t" \
  "ds_read_b64 v[100:101], v128 offset:1024\n\t" \
  "ds_read_b64 v[102:103], v128 offset:1536\n\t" \
  "ds_read_b64 v[112:113], v128 offset:2048\n\t" \
  "ds_read_b64 v[114:115], v128 offset:2560\n\t" \
  "ds_read_b64 v[116:117], v128 offset:3072\n\t" \
  "ds_read_b64 v[118:119], v128 offset:3584\n\t" \
  ".Lcnx_c2:\n\t" \
  "v_smfmac_f32_16x16x64_bf16 v[44:47], v[132:135], v[224:231], v236\n\t" \
  "v_fmac_f32 v203, v15, v15\n\t" \
  "v_smfmac_f32_16x16x64_bf16 v[32:35], v[136:139], v[224:231], v236\n\t" \
  "v_smfmac_f32_16x16x64_bf16 v[20:23], v[140:143], v[224:231], v236\n\t" \
  "buffer_store_dwordx4 v[48:51], %[vo], %[ors], %[op1] offen offset:0 sc1\n\t" \
  "ds_read_b64 v[224:225], v232 offset:16\n\t" \
  "v_add_f32 v202, v202, v16\n\t" \
  "v_fmac_f32 v203, v16, v16\n\t" \
  "v_add_f32 v202, v202, v17\n\t" \
  "v_fmac_f32 v203, v17, v17\n\t" \
  "v_add_f32 v202, v202, v18\n\t" \
  "v_fmac_f32 v203, v18, v18\n\t" \
  "s_waitcnt lgkmcnt(5)\n\t" \
  "v_smfmac_f32_16x16x64_bf16 v[36:39], v[104:107], v[208:215], v236\n\t" \
  "ds_read_b64 v[226:227], v232 offset:416\n\t" \
  "ds_read_b64 v[228:229], v232 offset:816\n\t" \
  "ds_read_b64 v[230:231], v232 offset:1216\n\t" \
  "v_add_f32 v202, v202, v19\n\t" \
  "v_fmac_f32 v203, v19, v19\n\t" \
  "v_smfmac_f32_16x16x64_bf16 v[24:27], v[108:111], v[208:215], v236\n\t" \
  "s_waitcnt lgkmcnt(4)\n\t" \
  "v_smfmac_f32_16x16x64_bf16 v[40:43], v[120:123], v[216:223], v236\n\t" \
  "v_add_f32 v202, v202, v20\n\t" \
  "v_fmac_f32 v203, v20, v20\n\t" \
  "v_add_f32 v202, v202, v21\n\t" \
  "v_fmac_f32 v203, v21, v21\n\t" \
  "v_add_f32 v202, v202, v22\n\t" \
  "v_fmac_f32 v203, v22, v22\n\t" \
  "v_smfmac_f32_16x16x64_bf16 v[28:31], v[124:127], v[216:223], v236\n\t" \
  "v_add_f32 v202, v202, v23\n\t" \
  "v_fmac_f32 v203, v23, v23\n\t" \
  "ds_write_b64 v233, v[202:203] offset:4096\n\t" \
  "s_waitcnt vmcnt(7) lgkmcnt(0)\n\t" \
  "s_barrier\n\t" \
  "ds_read_b64 v[208:209], v232 offset:16400\n\t" \
  "ds_read_b64 v[210:211], v232 offset:16800\n\t" \
  "ds_read_b64 v[212:213], v232 offset:17200\n\t" \
  "ds_read_b64 v[214:215], v232 offset:17600\n\t" \
  "ds_read_b64 v[216:217], v232 offset:16408\n\t" \
  "ds_read_b64 v[218:219], v232 offset:16808\n\t" \
  "ds_read_b64 v[220:221], v232 offset:17208\n\t" \
  "ds_read_b64 v[222:223], v232 offset:17608\n\t" \
  "s_cmp_ge_u32 %[f0], 2\n\t" \
  "s_cbranch_scc1 .Lcnx_c3\n\t" \
  "s_add_u32 m0, %[wl], %[ws]\n\t" \
  "s_nop 0\n\t" \
  "buffer_load_dwordx4 %[wv], %[wrs], %[so0] offen lds\n\t" \
  "buffer_load_dwordx4 %[wv], %[wrs], %[so1] offen offset:1024 lds\n\t" \
  "buffer_load_dwordx4 %[wv], %[wrs], %[so2] offen offset:2048 lds\n\t" \
  "s_add_u32 %[so0], %[so0], %[wc]\n\t" \
  "s_add_u32 %[so1], %[so1], %[wc]\n\t" \
  "s_add_u32 %[so2], %[so2], %[wc]\n\t" \
  "s_add_u32 m0, %[wl], 0\n\t" \
  "s_nop 0\n\t" \
  "buffer_load_dwordx4 %[wv], %[wrs], %[so0] offen lds\n\t" \
  "buffer_load_dwordx4 %[wv], %[wrs], %[so1] offen offset:1024 lds\n\t" \
  "buffer_load_dwordx4 %[wv], %[wrs], %[so2] offen offset:2048 lds\n\t" \
  "s_add_u32 %[so0], %[so0], %[wc]\n\t" \
  "s_add_u32 %[so1], %[so1], %[wc]\n\t" \
  "s_add_u32 %[so2], %[so2], %[wc]\n\t" \
  ".Lcnx_c3:\n\t" \
  "v_smfmac_f32_16x16x64_bf16 v[44:47], v[136:139], v[224:231], v236\n\t" \
  "v_mul_f32 v205, v24, v24\n\t" \
  "v_add_f32 v204, v24, v25\n\t" \
  "v_smfmac_f32_16x16x64_bf16 v[32:35], v[140:143], v[224:231], v236\n\t" \
  "buffer_store_dwordx4 v[56:59], %[vo], %[ors], %[op1] offen offset:64 sc1\n\t" \
  "ds_read_b64 v[224:225], v232 offset:16416\n\t" \
  "ds_read_b64 v[226:227], v232 offset:16816\n\t" \
  "v_fmac_f32 v205, v25, v25\n\t" \
  "v_add_f32 v204, v204, v26\n\t" \
  "v_fmac_f32 v205, v26, v26\n\t" \
  "s_cmp_lg_u32 %[grp], 0\n\t" \
  "s_cbranch_scc1 .Lcnx_c4\n\t" \
  "s_waitcnt lgkmcnt(15)\n\t" \
  "v_add_f32 v96, v96, v98\n\t" \
  "v_add_f32 v97, v97, v99\n\t" \
  "v_add_f32 v100, v100, v102\n\t" \
  "v_add_f32 v101, v101, v103\n\t" \
  "v_add_f32 v112, v112, v114\n\t" \
  "v_add_f32 v113, v113, v115\n\t" \
  "v_add_f32 v116, v116, v118\n\t" \
  "v_add_f32 v117, v117, v119\n\t" \
  "v_add_f32 v96, v96, v100\n\t" \
  "v_add_f32 v97, v97, v101\n\t" \
  "v_add_f32 v112, v112, v116\n\t" \
  "v_add_f32 v113, v113, v117\n\t" \
  "v_add_f32 v96, v96, v112\n\t" \
  "v_add_f32 v97, v97, v113\n\t" \
  "v_mul_f32 v96, 0x3c2aaaab, v96\n\t" \
  "v_mul_f32 v97, 0x3c2aaaab, v97\n\t" \
  "v_fma_f32 v97, -v96, v96, v97\n\t" \
  "v_max_f32 v97, 0, v97\n\t" \
  "v_add_f32 v97, 0x3727c5ac, v97\n\t" \
  "v_rsq_f32 v97, v97\n\t" \
  "s_nop 0\n\t" \
  "v_mul_f32 v98, 0x3e000000, v97\n\t" \
  "v_mul_f32 v99, -v96, v98\n\t" \
  "ds_write_b64 v233, v[98:99] offset:32784\n\t" \
  ".Lcnx_c4:\n\t" \
  "s_waitcnt lgkmcnt(6)\n\t" \
  "v_smfmac_f32_16x16x64_bf16 v[36:39], v[108:111], v[208:215], v236\n\t" \
  "ds_read_b64 v[228:229], v232 offset:17216\n\t" \
  "ds_read_b64 v[230:231], v232 offset:17616\n\t" \
  "v_add_f32 v204, v204, v27\n\t" \
  "v_fmac_f32 v205, v27, v27\n\t" \
  "v_add_f32 v204, v204, v28\n\t" \
  "s_waitcnt lgkmcnt(4)\n\t" \
  "v_smfmac_f32_16x16x64_bf16 v[40:43], v[124:127], v[216:223], v236\n\t" \
  "v_fmac_f32 v205, v28, v28\n\t" \
  "v_add_f32 v204, v204, v29\n\t" \
  "v_fmac_f32 v205, v29, v29\n\t" \
  "s_waitcnt lgkmcnt(0)\n\t" \
  "v_smfmac_f32_16x16x64_bf16 v[44:47], v[140:143], v[224:231], v236\n\t" \
  "v_add_f32 v204, v204, v30\n\t" \
  "v_fmac_f32 v205, v30, v30\n\t" \
  "v_add_f32 v204, v204, v31\n\t" \
  "v_fmac_f32 v205, v31, v31\n\t" \
  "v_add_f32 v204, v204, v32\n\t" \
  "v_fmac_f32 v205, v32, v32\n\t" \
  "v_add_f32 v204, v204, v33\n\t" \
  "v_fmac_f32 v205, v33, v33\n\t" \
  "v_add_f32 v204, v204, v34\n\t" \
  "v_fmac_f32 v205, v34, v34\n\t" \
  "v_add_f32 v204, v204, v35\n\t" \
  "v_fmac_f32 v205, v35, v35\n\t" \
  "v_mul_f32 v207, v36, v36\n\t" \
  "v_add_f32 v206, v36, v37\n\t" \
  "v_fmac_f32 v207, v37, v37\n\t" \
  "v_add_f32 v206, v206, v38\n\t" \
  "v_fmac_f32 v207, v38, v38\n\t" \
  "v_add_f32 v206, v206, v39\n\t" \
  "v_fmac_f32 v207, v39, v39\n\t" \
  "v_add_f32 v206, v206, v40\n\t" \
  "v_fmac_f32 v207, v40, v40\n\t" \
  "v_add_f32 v206, v206, v41\n\t" \
  "v_fmac_f32 v207, v41, v41\n\t" \
  "v_add_f32 v206, v206, v42\n\t" \
  "v_fmac_f32 v207, v42, v42\n\t" \
  "v_add_f32 v206, v206, v43\n\t" \
  "v_fmac_f32 v207, v43, v43\n\t" \
  "v_add_f32 v206, v206, v44\n\t" \
  "v_fmac_f32 v207, v44, v44\n\t" \
  "v_add_f32 v206, v206, v45\n\t" \
  "v_fmac_f32 v207, v45, v45\n\t" \
  "v_add_f32 v206, v206, v46\n\t" \
  "v_fmac_f32 v207, v46, v46\n\t" \
  "v_add_f32 v206, v206, v47\n\t" \
  "v_fmac_f32 v207, v47, v47\n\t" \
  "ds_write_b64 v233, v[204:205] offset:8192\n\t" \
  "ds_write_b64 v233, v[206:207] offset:12288\n\t" \
  "s_waitcnt lgkmcnt(0)\n\t" \
  "s_barrier\n\t" \
  "ds_read_b64 v[192:193], %[l8] offset:32784\n\t" \
  "ds_read_b64 v[194:195], %[l8] offset:33296\n\t" \
  "ds_read_b64 v[196:197], %[l8] offset:33808\n\t" \
  "ds_read_b64 v[198:199], %[l8] offset:34320\n\t" \
  "s_cmp_lg_u32 %[grp], 1\n\t" \
  "s_cbranch_scc1 .Lcnx_c5\n\t" \
  "v_add_u32 v128, %[sr], %[l8]\n\t" \
  "ds_read_b64 v[96:97], v128 offset:0\n\t" \
  "ds_read_b64 v[98:99], v128 offset:512\n\t" \
  "ds_read_b64 v[100:101], v128 offset:1024\n\t" \
  "ds_read_b64 v[102:103], v128 offset:1536\n\t" \
  "ds_read_b64 v[112:113], v128 offset:2048\n\t" \
  "ds_read_b64 v[114:115], v128 offset:2560\n\t" \
  "ds_read_b64 v[116:117], v128 offset:3072\n\t" \
  "ds_read_b64 v[118:119], v128 offset:3584\n\t" \
  ".Lcnx_c5:\n\t" \
  "v_lshl_add_u32 v208, %[l8], 1, %[z1]\n\t" \
  "v_lshl_add_u32 v209, %[l8], 1, %[z2]\n\t" \
  "s_waitcnt lgkmcnt(3)\n\t" \
  "v_fma_f32 v144, v144, v192, v193\n\t" \
  "v_fma_f32 v145, v145, v192, v193\n\t" \
  "v_fma_f32 v146, v146, v192, v193\n\t" \
  "v_fma_f32 v147, v147, v192, v193\n\t" \
  "v_fma_f32 v148, v148, v192, v193\n\t" \
  "v_fma_f32 v149, v149, v192, v193\n\t" \
  "v_fma_f32 v150, v150, v192, v193\n\t" \
  "v_fma_f32 v151, v151, v192, v193\n\t" \
  "v_fma_f32 v152, v152, v192, v193\n\t" \
  "v_fma_f32 v153, v153, v192, v193\n\t" \
  "v_fma_f32 v154, v154, v192, v193\n\t" \
  "v_fma_f32 v155, v155, v192, v193\n\t" \
  "v_cvt_pk_bf16_f32 v144, v144, v145\n\t" \
  "v_cvt_pk_bf16_f32 v145, v146, v147\n\t" \
  "v_cvt_pk_bf16_f32 v146, v148, v149\n\t" \
  "v_cvt_pk_bf16_f32 v147, v150, v151\n\t" \
  "v_cvt_pk_bf16_f32 v148, v152, v153\n\t" \
  "v_cvt_pk_bf16_f32 v149, v154, v155\n\t" \
  "ds_write_b128 v208, v[144:147] offset:0\n\t" \
  "ds_write_b64 v209, v[148:149] offset:0\n\t" \
  "buffer_store_dwordx4 v[64:67], %[vo], %[ors], %[op1] offen offset:128 sc1\n\t" \
  "s_cmp_lg_u32 %[grp], 1\n\t" \
  "s_cbranch_scc1 .Lcnx_c6\n\t" \
  "s_waitcnt lgkmcnt(2)\n\t" \
  "v_add_f32 v96, v96, v98\n\t" \
  "v_add_f32 v97, v97, v99\n\t" \
  "v_add_f32 v100, v100, v102\n\t" \
  "v_add_f32 v101, v101, v103\n\t" \
  "v_add_f32 v112, v112, v114\n\t" \
  "v_add_f32 v113, v113, v115\n\t" \
  "v_add_f32 v116, v116, v118\n\t" \
  "v_add_f32 v117, v117, v119\n\t" \
  "v_add_f32 v96, v96, v100\n\t" \
  "v_add_f32 v97, v97, v101\n\t" \
  "v_add_f32 v112, v112, v116\n\t" \
  "v_add_f32 v113, v113, v117\n\t" \
  "v_add_f32 v96, v96, v112\n\t" \
  "v_add_f32 v97, v97, v113\n\t" \
  "v_mul_f32 v96, 0x3c2aaaab, v96\n\t" \
  "v_mul_f32 v97, 0x3c2aaaab, v97\n\t" \
  "v_fma_f32 v97, -v96, v96, v97\n\t" \
  "v_max_f32 v97, 0, v97\n\t" \
  "v_add_f32 v97, 0x3727c5ac, v97\n\t" \
  "v_rsq_f32 v97, v97\n\t" \
  "s_nop 0\n\t" \
  "v_mul_f32 v98, 0x3e000000, v97\n\t" \
  "v_mul_f32 v99, -v96, v98\n\t" \
  "ds_write_b64 v233, v[98:99] offset:14352\n\t" \
  ".Lcnx_c6:\n\t" \
  "s_waitcnt lgkmcnt(4)\n\t" \
  "v_fma_f32 v156, v156, v194, v195\n\t" \
  "v_fma_f32 v157, v157, v194, v195\n\t" \
  "v_fma_f32 v158, v158, v194, v195\n\t" \
  "v_fma_f32 v159, v159, v194, v195\n\t" \
  "v_fma_f32 v160, v160, v194, v195\n\t" \
  "v_fma_f32 v161, v161, v194, v195\n\t" \
  "v_fma_f32 v162, v162, v194, v195\n\t" \
  "v_fma_f32 v163, v163, v194, v195\n\t" \
  "v_fma_f32 v164, v164, v194, v195\n\t" \
  "v_fma_f32 v165, v165, v194, v195\n\t" \
  "v_fma_f32 v166, v166, v194, v195\n\t" \
  "v_fma_f32 v167, v167, v194, v195\n\t" \
  "v_cvt_pk_bf16_f32 v156, v156, v157\n\t" \
  "v_cvt_pk_bf16_f32 v157, v158, v159\n\t" \
  "v_cvt_pk_bf16_f32 v158, v160, v161\n\t" \
  "v_cvt_pk_bf16_f32 v159, v162, v163\n\t" \
  "v_cvt_pk_bf16_f32 v160, v164, v165\n\t" \
  "v_cvt_pk_bf16_f32 v161, v166, v167\n\t" \
  "ds_write_b128 v208, v[156:159] offset:12288\n\t" \
  "ds_write_b64 v209, v[160:161] offset:12288\n\t" \
  "s_waitcnt lgkmcnt(5)\n\t" \
  "v_fma_f32 v168, v168, v196, v197\n\t" \
  "v_fma_f32 v169, v169, v196, v197\n\t" \
  "v_fma_f32 v170, v170, v196, v197\n\t" \
  "v_fma_f32 v171, v171, v196, v197\n\t" \
  "v_fma_f32 v172, v172, v196, v197\n\t" \
  "v_fma_f32 v173, v173, v196, v197\n\t" \
  "v_fma_f32 v174, v174, v196, v197\n\t" \
  "v_fma_f32 v175, v175, v196, v197\n\t" \
  "v_fma_f32 v176, v176, v196, v197\n\t" \
  "v_fma_f32 v177, v177, v196, v197\n\t" \
  "v_fma_f32 v178, v178, v196, v197\n\t" \
  "v_fma_f32 v179, v179, v196, v197\n\t" \
  "v_cvt_pk_bf16_f32 v168, v168, v169\n\t" \
  "v_cvt_pk_bf16_f32 v169, v170, v171\n\t" \
  "v_cvt_pk_bf16_f32 v170, v172, v173\n\t" \
  "v_cvt_pk_bf16_f32 v171, v174, v175\n\t" \
  "v_cvt_pk_bf16_f32 v172, v176, v177\n\t" \
  "v_cvt_pk_bf16_f32 v173, v178, v179\n\t" \
  "ds_write_b128 v208, v[168:171] offset:24576\n\t" \
  "ds_write_b64 v209, v[172:173] offset:24576\n\t" \
  "s_waitcnt lgkmcnt(6)\n\t" \
  "v_fma_f32 v180, v180, v198, v199\n\t" \
  "v_fma_f32 v181, v181, v198, v199\n\t" \
  "v_fma_f32 v182, v182, v198, v199\n\t" \
  "v_fma_f32 v183, v183, v198, v199\n\t" \
  "v_fma_f32 v184, v184, v198, v199\n\t" \
  "v_fma_f32 v185, v185, v198, v199\n\t" \
  "v_fma_f32 v186, v186, v198, v199\n\t" \
  "v_fma_f32 v187, v187, v198, v199\n\t" \
  "v_fma_f32 v188, v188, v198, v199\n\t" \
  "v_fma_f32 v189, v189, v198, v199\n\t" \
  "v_fma_f32 v190, v190, v198, v199\n\t" \
  "v_fma_f32 v191, v191, v198, v199\n\t" \
  "v_cvt_pk_bf16_f32 v180, v180, v181\n\t" \
  "v_cvt_pk_bf16_f32 v181, v182, v183\n\t" \
  "v_cvt_pk_bf16_f32 v182, v184, v185\n\t" \
  "v_cvt_pk_bf16_f32 v183, v186, v187\n\t" \
  "v_cvt_pk_bf16_f32 v184, v188, v189\n\t" \
  "v_cvt_pk_bf16_f32 v185, v190, v191\n\t" \
  "ds_write_b128 v208, v[180:183] offset:36864\n\t" \
  "ds_write_b64 v209, v[184:185] offset:36864\n\t" \
  "v_add_u32 v208, 49152, v208\n\t" \
  "v_add_u32 v209, 49152, v209\n\t" \
  "s_waitcnt lgkmcnt(0)\n\t" \
  "s_barrier\n\t" \
  "ds_read_b64 v[200:201], %[l8] offset:16400\n\t" \
  "ds_read_b64 v[202:203], %[l8] offset:16912\n\t" \
  "ds_read_b64 v[204:205], %[l8] offset:17424\n\t" \
  "ds_read_b64 v[206:207], %[l8] offset:17936\n\t" \
  "s_waitcnt lgkmcnt(3)\n\t" \
  "v_fma_f32 v0, v0, v200, v201\n\t" \
  "v_fma_f32 v1, v1, v200, v201\n\t" \
  "v_fma_f32 v2, v2, v200, v201\n\t" \
  "v_fma_f32 v3, v3, v200, v201\n\t" \
  "v_fma_f32 v4, v4, v200, v201\n\t" \
  "v_fma_f32 v5, v5, v200, v201\n\t" \
  "v_fma_f32 v6, v6, v200, v201\n\t" \
  "v_fma_f32 v7, v7, v200, v201\n\t" \
  "v_fma_f32 v8, v8, v200, v201\n\t" \
  "v_fma_f32 v9, v9, v200, v201\n\t" \
  "v_fma_f32 v10, v10, v200, v201\n\t" \
  "v_fma_f32 v11, v11, v200, v201\n\t" \
  "v_cvt_pk_bf16_f32 v0, v0, v1\n\t" \
  "v_cvt_pk_bf16_f32 v1, v2, v3\n\t" \
  "v_cvt_pk_bf16_f32 v2, v4, v5\n\t" \
  "v_cvt_pk_bf16_f32 v3, v6, v7\n\t" \
  "v_cvt_pk_bf16_f32 v4, v8, v9\n\t" \
  "v_cvt_pk_bf16_f32 v5, v10, v11\n\t" \
  "ds_write_b128 v208, v[0:3] offset:0\n\t" \
  "ds_write_b64 v209, v[4:5] offset:0\n\t" \
  "buffer_store_dwordx4 v[72:75], %[vo], %[ors], %[op1] offen offset:192 sc1\n\t" \
  "s_waitcnt lgkmcnt(4)\n\t" \
  "v_fma_f32 v12, v12, v202, v203\n\t" \
  "v_fma_f32 v13, v13, v202, v203\n\t" \
  "v_fma_f32 v14, v14, v202, v203\n\t" \
  "v_fma_f32 v15, v15, v202, v203\n\t" \
  "v_fma_f32 v16, v16, v202, v203\n\t" \
  "v_fma_f32 v17, v17, v202, v203\n\t" \
  "v_fma_f32 v18, v18, v202, v203\n\t" \
  "v_fma_f32 v19, v19, v202, v203\n\t" \
  "v_fma_f32 v20, v20, v202, v203\n\t" \
  "v_fma_f32 v21, v21, v202, v203\n\t" \
  "v_fma_f32 v22, v22, v202, v203\n\t" \
  "v_fma_f32 v23, v23, v202, v203\n\t" \
  "v_cvt_pk_bf16_f32 v12, v12, v13\n\t" \
  "v_cvt_pk_bf16_f32 v13, v14, v15\n\t" \
  "v_cvt_pk_bf16_f32 v14, v16, v17\n\t" \
  "v_cvt_pk_bf16_f32 v15, v18, v19\n\t" \
  "v_cvt_pk_bf16_f32 v16, v20, v21\n\t" \
  "v_cvt_pk_bf16_f32 v17, v22, v23\n\t" \
  "ds_write_b128 v208, v[12:15] offset:12288\n\t" \
  "ds_write_b64 v209, v[16:17] offset:12288\n\t" \
  "s_waitcnt lgkmcnt(5)\n\t" \
  "v_fma_f32 v24, v24, v204, v205\n\t" \
  "v_fma_f32 v25, v25, v204, v205\n\t" \
  "v_fma_f32 v26, v26, v204, v205\n\t" \
  "v_fma_f32 v27, v27, v204, v205\n\t" \
  "v_fma_f32 v28, v28, v204, v205\n\t" \
  "v_fma_f32 v29, v29, v204, v205\n\t" \
  "v_fma_f32 v30, v30, v204, v205\n\t" \
  "v_fma_f32 v31, v31, v204, v205\n\t" \
  "v_fma_f32 v32, v32, v204, v205\n\t" \
  "v_fma_f32 v33, v33, v204, v205\n\t" \
  "v_fma_f32 v34, v34, v204, v205\n\t" \
  "v_fma_f32 v35, v35, v204, v205\n\t" \
  "v_cvt_pk_bf16_f32 v24, v24, v25\n\t" \
  "v_cvt_pk_bf16_f32 v25, v26, v27\n\t" \
  "v_cvt_pk_bf16_f32 v26, v28, v29\n\t" \
  "v_cvt_pk_bf16_f32 v27, v30, v31\n\t" \
  "v_cvt_pk_bf16_f32 v28, v32, v33\n\t" \
  "v_cvt_pk_bf16_f32 v29, v34, v35\n\t" \
  "ds_write_b128 v208, v[24:27] offset:24576\n\t" \
  "ds_write_b64 v209, v[28:29] offset:24576\n\t" \
  "s_waitcnt lgkmcnt(6)\n\t" \
  "v_fma_f32 v36, v36, v206, v207\n\t" \
  "v_fma_f32 v37, v37, v206, v207\n\t" \
  "v_fma_f32 v38, v38, v206, v207\n\t" \
  "v_fma_f32 v39, v39, v206, v207\n\t" \
  "v_fma_f32 v40, v40, v206, v207\n\t" \
  "v_fma_f32 v41, v41, v206, v207\n\t" \
  "v_fma_f32 v42, v42, v206, v207\n\t" \
  "v_fma_f32 v43, v43, v206, v207\n\t" \
  "v_fma_f32 v44, v44, v206, v207\n\t" \
  "v_fma_f32 v45, v45, v206, v207\n\t" \
  "v_fma_f32 v46, v46, v206, v207\n\t" \
  "v_fma_f32 v47, v47, v206, v207\n\t" \
  "v_cvt_pk_bf16_f32 v36, v36, v37\n\t" \
  "v_cvt_pk_bf16_f32 v37, v38, v39\n\t" \
  "v_cvt_pk_bf16_f32 v38, v40, v41\n\t" \
  "v_cvt_pk_bf16_f32 v39, v42, v43\n\t" \
  "v_cvt_pk_bf16_f32 v40, v44, v45\n\t" \
  "v_cvt_pk_bf16_f32 v41, v46, v47\n\t" \
  "ds_write_b128 v208, v[36:39] offset:36864\n\t" \
  "ds_write_b64 v209, v[40:41] offset:36864\n\t" \
  "v_lshlrev_b32 v232, 1, %[l8]\n\t" \
  "v_lshl_add_u32 v237, %[l8], 3, %[zb]\n\t" \
  "v_and_b32 v232, 0xf0, v232\n\t" \
  "v_mad_i32_i24 v237, v232, -3, v237\n\t" \
  "s_cmp_lg_u32 %[rnd], 0\n\t" \
  "s_cbranch_scc1 .Lcnx_c7\n\t" \
  "s_waitcnt vmcnt(0)\n\t" \
  ".Lcnx_c7:\n\t" \
  "s_waitcnt lgkmcnt(0)\n\t" \
  "s_barrier\n\t" \
  "ds_read_b64_tr_b16 v[208:209], %[a1] offset:6144\n\t" \
  "ds_read_b64_tr_b16 v[210:211], %[a1] offset:7168\n\t" \
  "ds_read_b128 v[96:99], v237 offset:0\n\t" \
  "ds_read_b128 v[108:111], v237 offset:256\n\t" \
  "ds_read_b128 v[120:123], v237 offset:512\n\t" \
  "ds_read_b128 v[132:135], v237 offset:768\n\t" \
  "ds_read_b64_tr_b16 v[212:213], %[a1] offset:9216\n\t" \
  "ds_read_b64_tr_b16 v[214:215], %[a1] offset:10240\n\t" \
  "ds_read_b128 v[100:103], v237 offset:4096\n\t" \
  "ds_read_b128 v[112:115], v237 offset:4352\n\t" \
  "ds_read_b128 v[124:127], v237 offset:4608\n\t" \
  "ds_read_b128 v[136:139], v237 offset:4864\n\t" \
  "ds_read_b128 v[104:107], v237 offset:8192\n\t" \
  "ds_read_b128 v[116:119], v237 offset:8448\n\t" \
  "ds_read_b128 v[128:131], v237 offset:8704\n\t" \
  "ds_read_b128 v[140:143], v237 offset:8960\n\t" \
  "s_waitcnt lgkmcnt(13)\n\t" \
  "v_mfma_f32_16x16x32_bf16 v[144:147], v[208:211], v[96:99], 0\n\t" \
  "s_waitcnt lgkmcnt(12)\n\t" \
  "v_mfma_f32_16x16x32_bf16 v[152:155], v[208:211], v[108:111], 0\n\t" \
  "s_waitcnt lgkmcnt(11)\n\t" \
  "v_mfma_f32_16x16x32_bf16 v[160:163], v[208:211], v[120:123], 0\n\t" \
  "buffer_store_dwordx4 v[80:83], %[vo], %[ors], %[op1] offen offset:256 sc1\n\t" \
  "s_waitcnt lgkmcnt(10)\n\t" \
  "v_mfma_f32_16x16x32_bf16 v[168:171], v[208:211], v[132:135], 0\n\t" \
  "ds_read_b64_tr_b16 v[208:209], %[a1] offset:8192\n\t" \
  "s_waitcnt lgkmcnt(8)\n\t" \
  "v_mfma_f32_16x16x32_bf16 v[144:147], v[212:215], v[100:103], v[144:147]\n\t" \
  "ds_read_b64_tr_b16 v[210:211], %[a1] offset:11264\n\t" \
  "s_waitcnt lgkmcnt(8)\n\t" \
  "v_mfma_f32_16x16x32_bf16 v[152:155], v[212:215], v[112:115], v[152:155]\n\t" \
  "s_waitcnt lgkmcnt(7)\n\t" \
  "v_mfma_f32_16x16x32_bf16 v[160:163], v[212:215], v[124:127], v[160:163]\n\t" \
  "s_waitcnt lgkmcnt(6)\n\t" \
  "v_mfma_f32_16x16x32_bf16 v[168:171], v[212:215], v[136:139], v[168:171]\n\t" \
  "ds_read_b64_tr_b16 v[212:213], %[a1] offset:6656\n\t" \
  "s_waitcnt lgkmcnt(1)\n\t" \
  "v_mfma_f32_16x16x32_bf16 v[144:147], v[208:211], v[104:107], v[144:147]\n\t" \
  "ds_read_b64_tr_b16 v[214:215], %[a1] offset:7680\n\t" \
  "v_mfma_f32_16x16x32_bf16 v[152:155], v[208:211], v[116:119], v[152:155]\n\t" \
  "v_mfma_f32_16x16x32_bf16 v[160:163], v[208:211], v[128:131], v[160:163]\n\t" \
  "v_mfma_f32_16x16x32_bf16 v[168:171], v[208:211], v[140:143], v[168:171]\n\t" \
  "ds_read_b64_tr_b16 v[208:209], %[a1] offset:9728\n\t" \
  "s_waitcnt lgkmcnt(1)\n\t" \
  "v_mfma_f32_16x16x32_bf16 v[148:151], v[212:215], v[96:99], 0\n\t" \
  "buffer_store_dwordx4 v[88:91], %[vo], %[ors], %[op1] offen offset:320 sc1\n\t" \
  "ds_read_b64_tr_b16 v[210:211], %[a1] offset:10752\n\t" \
  "v_mfma_f32_16x16x32_bf16 v[156:159], v[212:215], v[108:111], 0\n\t" \
  "v_mfma_f32_16x16x32_bf16 v[164:167], v[212:215], v[120:123], 0\n\t" \
  "v_mfma_f32_16x16x32_bf16 v[172:175], v[212:215], v[132:135], 0\n\t" \
  "ds_read_b64_tr_b16 v[212:213], %[a1] offset:8704\n\t" \
  "s_waitcnt lgkmcnt(1)\n\t" \
  "v_mfma_f32_16x16x32_bf16 v[148:151], v[208:211], v[100:103], v[148:151]\n\t" \
  "ds_read_b64_tr_b16 v[214:215], %[a1] offset:11776\n\t" \
  "v_mfma_f32_16x16x32_bf16 v[156:159], v[208:211], v[112:115], v[156:159]\n\t" \
  "v_cvt_pk_f16_f32 v216, |v146|, |v147| clamp\n\t" \
  "v_cvt_pk_f16_f32 v232, v144, v145\n\t" \
  "v_mfma_f32_16x16x32_bf16 v[164:167], v[208:211], v[124:127], v[164:167]\n\t" \
  "v_cvt_pk_f16_f32 v218, |v154|, |v155| clamp\n\t" \
  "v_cvt_pk_f16_f32 v220, |v152|, |v153| clamp\n\t" \
  "v_pk_mul_f16 v233, v232, v232\n\t" \
  "v_mfma_f32_16x16x32_bf16 v[172:175], v[208:211], v[136:139], v[172:175]\n\t" \
  "v_and_b32_sdwa v217, %[mk], v216 dst_sel:DWORD dst_unused:UNUSED_PAD src0_sel:DWORD src1_sel:WORD_1\n\t" \
  "v_and_b32 v216, %[mk], v216\n\t" \
  "s_waitcnt lgkmcnt(0)\n\t" \
  "v_mfma_f32_16x16x32_bf16 v[148:151], v[212:215], v[104:107], v[148:151]\n\t" \
  "ds_read_b32 v217, v217 offset:0\n\t" \
  "v_and_b32_sdwa v219, %[mk], v218 dst_sel:DWORD dst_unused:UNUSED_PAD src0_sel:DWORD src1_sel:WORD_1\n\t" \
  "v_mfma_f32_16x16x32_bf16 v[156:159], v[212:215], v[116:119], v[156:159]\n\t" \
  "ds_read_b32 v216, v216 offset:0\n\t" \
  "v_and_b32 v218, %[mk], v218\n\t" \
  "v_and_b32_sdwa v221, %[mk], v220 dst_sel:DWORD dst_unused:UNUSED_PAD src0_sel:DWORD src1_sel:WORD_1\n\t" \
  "v_mfma_f32_16x16x32_bf16 v[164:167], v[212:215], v[128:131], v[164:167]\n\t" \
  "ds_read_b32 v219, v219 offset:0\n\t" \
  "v_and_b32 v220, %[mk], v220\n\t" \
  "v_mfma_f32_16x16x32_bf16 v[172:175], v[212:215], v[140:143], v[172:175]\n\t" \
  "ds_read_b32 v218, v218 offset:0\n\t" \
  "v_pk_fma_f16 v233, v233, %[h2], %[h1]\n\t" \
  "v_pk_mul_f16 v233, v232, v233\n\t" \
  "ds_read_b32 v221, v221 offset:0\n\t" \
  "ds_read_b32 v220, v220 offset:0\n\t" \
  "s_waitcnt lgkmcnt(0)\n\t" \
  "s_barrier\n\t" \
  "ds_read_b64_tr_b16 v[208:209], %[a1] offset:0\n\t" \
  "ds_read_b64_tr_b16 v[210:211], %[a1] offset:1024\n\t" \
  "ds_read_b64_tr_b16 v[212:213], %[a1] offset:3072\n\t" \
  "ds_read_b64_tr_b16 v[214:215], %[a1] offset:4096\n\t" \
  "s_setprio 2\n\t" \
  "v_exp_f16 v232, v233\n\t" \
  "v_cvt_pk_f16_f32 v224, |v162|, |v163| clamp\n\t" \
  "s_waitcnt lgkmcnt(2)\n\t" \
  "v_mfma_f32_16x16x32_bf16 v[176:179], v[208:211], v[96:99], 0\n\t" \
  "v_cvt_pk_f16_f32 v234, v160, v161\n\t" \
  "v_cvt_pk_f16_f32 v226, |v170|, |v171| clamp\n\t" \
  "v_cvt_pk_f16_f32 v228, |v168|, |v169| clamp\n\t" \
  "v_exp_f16_sdwa v232, v233 dst_sel:WORD_1 dst_unused:UNUSED_PRESERVE src0_sel:WORD_1\n\t" \
  "v_pk_mul_f16 v235, v234, v234\n\t" \
  "v_mfma_f32_16x16x32_bf16 v[184:187], v[208:211], v[108:111], 0\n\t" \
  "s_cmp_lg_u32 %[grp], 0\n\t" \
  "s_cbranch_scc1 .Lcnx_c8\n\t" \
  "s_add_u32 m0, %[wl], %[ws]\n\t" \
  "s_nop 0\n\t" \
  "buffer_load_dwordx4 %[wv], %[wrs], %[so0] offen lds\n\t" \
  "buffer_load_dwordx4 %[wv], %[wrs], %[so1] offen offset:1024 lds\n\t" \
  "buffer_load_dwordx4 %[wv], %[wrs], %[so2] offen offset:2048 lds\n\t" \
  "s_add_u32 %[so0], %[so0], %[wc]\n\t" \
  "s_add_u32 %[so1], %[so1], %[wc]\n\t" \
  "s_add_u32 %[so2], %[so2], %[wc]\n\t" \
  ".Lcnx_c8:\n\t" \
  "buffer_load_dwordx4 v[236:239], %[vo], %[xrs], %[oc0] offen offset:0\n\t" \
  "s_cmp_lg_u32 %[rnd], 0\n\t" \
  "s_cbranch_scc1 .Lcnx_c9\n\t" \
  "s_sub_u32 %[xo], %[xo], %[rs6]\n\t" \
  "s_add_u32 m0, %[xl], 0\n\t" \
  "s_add_u32 %[xo], %[xo], %[rs]\n\t" \
  "buffer_load_dwordx4 %[xv], %[xrs], %[xo] offen lds\n\t" \
  ".Lcnx_c9:\n\t" \
  "v_and_b32_sdwa v225, %[mk], v224 dst_sel:DWORD dst_unused:UNUSED_PAD src0_sel:DWORD src1_sel:WORD_1\n\t" \
  "v_and_b32 v224, %[mk], v224\n\t" \
  "v_and_b32_sdwa v227, %[mk], v226 dst_sel:DWORD dst_unused:UNUSED_PAD src0_sel:DWORD src1_sel:WORD_1\n\t" \
  "v_and_b32 v226, %[mk], v226\n\t" \
  "v_mfma_f32_16x16x32_bf16 v[192:195], v[208:211], v[120:123], 0\n\t" \
  "ds_read_b32 v225, v225 offset:0\n\t" \
  "v_and_b32_sdwa v229, %[mk], v228 dst_sel:DWORD dst_unused:UNUSED_PAD src0_sel:DWORD src1_sel:WORD_1\n\t" \
  "v_and_b32 v228, %[mk], v228\n\t" \
  "v_mfma_f32_16x16x32_bf16 v[200:203], v[208:211], v[132:135], 0\n\t" \
  "ds_read_b64_tr_b16 v[208:209], %[a1] offset:2048\n\t" \
  "ds_read_b64_tr_b16 v[210:211], %[a1] offset:5120\n\t" \
  "v_pk_fma_f16 v235, v235, %[h2], %[h1]\n\t" \
  "v_pk_mul_f16 v235, v234, v235\n\t" \
  "v_fma_f32 v147, |v147|, v217, v147 mul:4\n\t" \
  "v_fma_f32 v146, |v146|, v216, v146 mul:4\n\t" \
  "v_fma_f32 v155, |v155|, v219, v155 mul:4\n\t" \
  "s_waitcnt lgkmcnt(3)\n\t" \
  "v_mfma_f32_16x16x32_bf16 v[176:179], v[212:215], v[100:103], v[176:179]\n\t" \
  "ds_read_b32 v224, v224 offset:0\n\t" \
  "ds_read_b32 v227, v227 offset:0\n\t" \
  "v_fma_f32 v154, |v154|, v218, v154 mul:4\n\t" \
  "v_fma_f32 v153, |v153|, v221, v153 mul:4\n\t" \
  "v_fma_f32 v152, |v152|, v220, v152 mul:4\n\t" \
  "v_pk_fma_f16 v232, v232, %[hh], %[hh]\n\t" \
  "v_cvt_pk_bf16_f32 v147, v146, v147\n\t" \
  "v_mfma_f32_16x16x32_bf16 v[184:187], v[212:215], v[112:115], v[184:187]\n\t" \
  "ds_read_b32 v226, v226 offset:0\n\t" \
  "v_cvt_pk_bf16_f32 v155, v154, v155\n\t" \
  "v_cvt_pk_bf16_f32 v154, v152, v153\n\t" \
  "v_rcp_f16 v233, v232\n\t" \
  "v_exp_f16 v234, v235\n\t" \
  "v_cvt_pk_f16_f32 v216, |v148|, |v149| clamp\n\t" \
  "v_cvt_pk_f16_f32 v218, |v150|, |v151| clamp\n\t" \
  "v_mfma_f32_16x16x32_bf16 v[192:195], v[212:215], v[124:127], v[192:195]\n\t" \
  "s_setprio 2\n\t" \
  "ds_read_b32 v229, v229 offset:0\n\t" \
  "v_cvt_pk_f16_f32 v220, |v156|, |v157| clamp\n\t" \
  "v_cvt_pk_f16_f32 v222, |v158|, |v159| clamp\n\t" \
  "v_rcp_f16_sdwa v232, v232 dst_sel:DWORD dst_unused:UNUSED_PAD src0_sel:WORD_1\n\t" \
  "v_exp_f16_sdwa v234, v235 dst_sel:WORD_1 dst_unused:UNUSED_PRESERVE src0_sel:WORD_1\n\t" \
  "v_and_b32_sdwa v217, %[mk], v216 dst_sel:DWORD dst_unused:UNUSED_PAD src0_sel:DWORD src1_sel:WORD_1\n\t" \
  "v_and_b32 v216, %[mk], v216\n\t" \
  "v_mfma_f32_16x16x32_bf16 v[200:203], v[212:215], v[136:139], v[200:203]\n\t" \
  "ds_read_b64_tr_b16 v[212:213], %[a1] offset:512\n\t" \
  "v_and_b32_sdwa v219, %[mk], v218 dst_sel:DWORD dst_unused:UNUSED_PAD src0_sel:DWORD src1_sel:WORD_1\n\t" \
  "v_and_b32 v218, %[mk], v218\n\t" \
  "s_waitcnt lgkmcnt(5)\n\t" \
  "v_mfma_f32_16x16x32_bf16 v[176:179], v[208:211], v[104:107], v[176:179]\n\t" \
  "ds_read_b64_tr_b16 v[214:215], %[a1] offset:1536\n\t" \
  "ds_read_b32 v228, v228 offset:0\n\t" \
  "v_and_b32_sdwa v221, %[mk], v220 dst_sel:DWORD dst_unused:UNUSED_PAD src0_sel:DWORD src1_sel:WORD_1\n\t" \
  "v_and_b32 v220, %[mk], v220\n\t" \
  "v_and_b32_sdwa v223, %[mk], v222 dst_sel:DWORD dst_unused:UNUSED_PAD src0_sel:DWORD src1_sel:WORD_1\n\t" \
  "v_mfma_f32_16x16x32_bf16 v[184:187], v[208:211], v[116:119], v[184:187]\n\t" \
  "ds_read_b32 v217, v217 offset:0\n\t" \
  "ds_read_b32 v216, v216 offset:0\n\t" \
  "v_and_b32 v222, %[mk], v222\n\t" \
  "s_waitcnt lgkmcnt(7)\n\t" \
  "v_fma_f32 v163, |v163|, v225, v163 mul:4\n\t" \
  "v_fma_f32 v162, |v162|, v224, v162 mul:4\n\t" \
  "v_mfma_f32_16x16x32_bf16 v[192:195], v[208:211], v[128:131], v[192:195]\n\t" \
  "ds_read_b32 v219, v219 offset:0\n\t" \
  "ds_read_b32 v218, v218 offset:0\n\t" \
  "v_fma_f32 v171, |v171|, v227, v171 mul:4\n\t" \
  "s_waitcnt lgkmcnt(4)\n\t" \
  "v_fma_f32 v170, |v170|, v226, v170 mul:4\n\t" \
  "v_fma_f32 v169, |v169|, v229, v169 mul:4\n\t" \
  "v_fma_f32 v168, |v168|, v228, v168 mul:4\n\t" \
  "v_fma_mix_f32 v144, v144, v233, 0 op_sel_hi:[0,1,0]\n\t" \
  "v_fma_mix_f32 v145, v145, v232, 0 op_sel_hi:[0,1,0]\n\t" \
  "v_mfma_f32_16x16x32_bf16 v[200:203], v[208:211], v[140:143], v[200:203]\n\t" \
  "ds_read_b64_tr_b16 v[208:209], %[a1] offset:3584\n\t" \
  "ds_read_b64_tr_b16 v[210:211], %[a1] offset:4608\n\t" \
  "v_pk_fma_f16 v234, v234, %[hh], %[hh]\n\t" \
  "v_cvt_pk_bf16_f32 v146, v144, v145\n\t" \
  "v_cvt_pk_bf16_f32 v163, v162, v163\n\t" \
  "v_cvt_pk_bf16_f32 v171, v170, v171\n\t" \
  "v_cvt_pk_bf16_f32 v170, v168, v169\n\t" \
  "v_rcp_f16 v235, v234\n\t" \
  "v_mfma_f32_16x16x32_bf16 v[180:183], v[212:215], v[96:99], 0\n\t" \
  "s_setprio 1\n\t" \
  "ds_read_b32 v221, v221 offset:0\n\t" \
  "ds_read_b32 v220, v220 offset:0\n\t" \
  "v_cvt_pk_f16_f32 v224, |v164|, |v165| clamp\n\t" \
  "v_cvt_pk_f16_f32 v226, |v166|, |v167| clamp\n\t" \
  "v_cvt_pk_f16_f32 v228, |v172|, |v173| clamp\n\t" \
  "v_cvt_pk_f16_f32 v230, |v174|, |v175| clamp\n\t" \
  "v_rcp_f16_sdwa v234, v234 dst_sel:DWORD dst_unused:UNUSED_PAD src0_sel:WORD_1\n\t" \
  "v_mfma_f32_16x16x32_bf16 v[188:191], v[212:215], v[108:111], 0\n\t" \
  "ds_read_b32 v223, v223 offset:0\n\t" \
  "v_and_b32_sdwa v225, %[mk], v224 dst_sel:DWORD dst_unused:UNUSED_PAD src0_sel:DWORD src1_sel:WORD_1\n\t" \
  "v_and_b32 v224, %[mk], v224\n\t" \
  "v_and_b32_sdwa v227, %[mk], v226 dst_sel:DWORD dst_unused:UNUSED_PAD src0_sel:DWORD src1_sel:WORD_1\n\t" \
  "v_and_b32 v226, %[mk], v226\n\t" \
  "v_mfma_f32_16x16x32_bf16 v[196:199], v[212:215], v[120:123], 0\n\t" \
  "ds_read_b32 v222, v222 offset:0\n\t" \
  "v_and_b32_sdwa v229, %[mk], v228 dst_sel:DWORD dst_unused:UNUSED_PAD src0_sel:DWORD src1_sel:WORD_1\n\t" \
  "v_and_b32 v228, %[mk], v228\n\t" \
  "v_mfma_f32_16x16x32_bf16 v[204:207], v[212:215], v[132:135], 0\n\t" \
  "ds_read_b64_tr_b16 v[212:213], %[a1] offset:2560\n\t" \
  "ds_read_b64_tr_b16 v[214:215], %[a1] offset:5632\n\t" \
  "v_and_b32_sdwa v231, %[mk], v230 dst_sel:DWORD dst_unused:UNUSED_PAD src0_sel:DWORD src1_sel:WORD_1\n\t" \
  "v_and_b32 v230, %[mk], v230\n\t" \
  "s_waitcnt lgkmcnt(8)\n\t" \
  "v_fma_f32 v149, |v149|, v217, v149 mul:4\n\t" \
  "s_waitcnt lgkmcnt(6)\n\t" \
  "v_mfma_f32_16x16x32_bf16 v[180:183], v[208:211], v[100:103], v[180:183]\n\t" \
  "ds_read_b32 v225, v225 offset:0\n\t" \
  "ds_read_b32 v224, v224 offset:0\n\t" \
  "v_fma_f32 v148, |v148|, v216, v148 mul:4\n\t" \
  "v_fma_f32 v151, |v151|, v219, v151 mul:4\n\t" \
  "v_fma_f32 v150, |v150|, v218, v150 mul:4\n\t" \
  "s_waitcnt lgkmcnt(4)\n\t" \
  "v_fma_f32 v157, |v157|, v221, v157 mul:4\n\t" \
  "v_fma_f32 v156, |v156|, v220, v156 mul:4\n\t" \
  "v_mfma_f32_16x16x32_bf16 v[188:191], v[208:211], v[112:115], v[188:191]\n\t" \
  "ds_read_b32 v227, v227 offset:0\n\t" \
  "ds_read_b32 v226, v226 offset:0\n\t" \
  "v_fma_f32 v159, |v159|, v223, v159 mul:4\n\t" \
  "v_fma_f32 v158, |v158|, v222, v158 mul:4\n\t" \
  "v_fma_mix_f32 v160, v160, v235, 0 op_sel_hi:[0,1,0]\n\t" \
  "v_fma_mix_f32 v161, v161, v234, 0 op_sel_hi:[0,1,0]\n\t" \
  "v_cvt_pk_bf16_f32 v162, v160, v161\n\t" \
  "v_cvt_pk_bf16_f32 v148, v148, v149\n\t" \
  "v_mfma_f32_16x16x32_bf16 v[196:199], v[208:211], v[124:127], v[196:199]\n\t" \
  "s_setprio 0\n\t" \
  "ds_read_b32 v229, v229 offset:0\n\t" \
  "ds_read_b32 v228, v228 offset:0\n\t" \
  "v_cvt_pk_bf16_f32 v149, v150, v151\n\t" \
  "v_cvt_pk_bf16_f32 v156, v156, v157\n\t" \
  "v_cvt_pk_bf16_f32 v157, v158, v159\n\t" \
  "v_cvt_pk_f16_f32 v216, |v178|, |v179| clamp\n\t" \
  "v_cvt_pk_f16_f32 v232, v176, v177\n\t" \
  "v_cvt_pk_f16_f32 v218, |v186|, |v187| clamp\n\t" \
  "v_mfma_f32_16x16x32_bf16 v[204:207], v[208:211], v[136:139], v[204:207]\n\t" \
  "ds_read_b32 v231, v231 offset:0\n\t" \
  "v_cvt_pk_f16_f32 v220, |v184|, |v185| clamp\n\t" \
  "v_pk_mul_f16 v233, v232, v232\n\t" \
  "v_and_b32_sdwa v217, %[mk], v216 dst_sel:DWORD dst_unused:UNUSED_PAD src0_sel:DWORD src1_sel:WORD_1\n\t" \
  "v_and_b32 v216, %[mk], v216\n\t" \
  "s_waitcnt lgkmcnt(7)\n\t" \
  "v_mfma_f32_16x16x32_bf16 v[180:183], v[212:215], v[104:107], v[180:183]\n\t" \
  "ds_read_b32 v230, v230 offset:0\n\t" \
  "v_and_b32_sdwa v219, %[mk], v218 dst_sel:DWORD dst_unused:UNUSED_PAD src0_sel:DWORD src1_sel:WORD_1\n\t" \
  "v_and_b32 v218, %[mk], v218\n\t" \
  "v_and_b32_sdwa v221, %[mk], v220 dst_sel:DWORD dst_unused:UNUSED_PAD src0_sel:DWORD src1_sel:WORD_1\n\t" \
  "v_mfma_f32_16x16x32_bf16 v[188:191], v[212:215], v[116:119], v[188:191]\n\t" \
  "ds_read_b32 v217, v217 offset:0\n\t" \
  "ds_read_b32 v216, v216 offset:0\n\t" \
  "v_and_b32 v220, %[mk], v220\n\t" \
  "v_pk_fma_f16 v233, v233, %[h2], %[h1]\n\t" \
  "v_pk_mul_f16 v233, v232, v233\n\t" \
  "v_mfma_f32_16x16x32_bf16 v[196:199], v[212:215], v[128:131], v[196:199]\n\t" \
  "ds_read_b32 v219, v219 offset:0\n\t" \
  "ds_read_b32 v218, v218 offset:0\n\t" \
  "s_waitcnt lgkmcnt(8)\n\t" \
  "v_fma_f32 v165, |v165|, v225, v165 mul:4\n\t" \
  "v_fma_f32 v164, |v164|, v224, v164 mul:4\n\t" \
  "v_fma_f32 v167, |v167|, v227, v167 mul:4\n\t" \
  "v_fma_f32 v166, |v166|, v226, v166 mul:4\n\t" \
  "s_waitcnt lgkmcnt(4)\n\t" \
  "v_fma_f32 v173, |v173|, v229, v173 mul:4\n\t" \
  "v_fma_f32 v172, |v172|, v228, v172 mul:4\n\t" \
  "v_mfma_f32_16x16x32_bf16 v[204:207], v[212:215], v[140:143], v[204:207]\n\t" \
  "ds_read_b32 v221, v221 offset:0\n\t" \
  "v_fma_f32 v175, |v175|, v231, v175 mul:4\n\t" \
  "v_fma_f32 v174, |v174|, v230, v174 mul:4\n\t" \
  "v_cvt_pk_bf16_f32 v164, v164, v165\n\t" \
  "v_cvt_pk_bf16_f32 v165, v166, v167\n\t" \
  "v_cvt_pk_bf16_f32 v172, v172, v173\n\t" \
  "v_cvt_pk_bf16_f32 v173, v174, v175\n\t" \
  "s_waitcnt vmcnt(1)\n\t" \
  "s_cmp_lg_u32 %[rnd], 1\n\t" \
  "s_cbranch_scc1 .Lcnx_c13\n\t" \
  "s_waitcnt vmcnt(0)\n\t" \
  ".Lcnx_c13:\n\t" \
  "s_barrier\n\t" \
  "ds_read_b64_tr_b16 v[208:209], %[a2] offset:6160\n\t" \
  "ds_read_b64_tr_b16 v[210:211], %[a2] offset:7696\n\t" \
  "ds_read_b64_tr_b16 v[212:213], %[a2] offset:6168\n\t" \
  "ds_read_b64_tr_b16 v[214:215], %[a2] offset:7704\n\t" \
  "ds_read_b32 v220, v220 offset:0\n\t" \
  "s_setprio 2\n\t" \
  "v_exp_f16 v232, v233\n\t" \
  "v_cvt_pk_f16_f32 v224, |v194|, |v195| clamp\n\t" \
  "v_mfma_f32_16x16x32_bf16 v[0:3], v[242:245], v[236:239], 0\n\t" \
  "v_cvt_pk_f16_f32 v234, v192, v193\n\t" \
  "v_cvt_pk_f16_f32 v226, |v202|, |v203| clamp\n\t" \
  "v_mfma_f32_16x16x32_bf16 v[4:7], v[240:243], v[236:239], 0\n\t" \
  "s_cmp_lg_u32 %[grp], 0\n\t" \
  "s_cbranch_scc1 .Lcnx_c10\n\t" \
  "s_add_u32 m0, %[wl], 0\n\t" \
  "s_nop 0\n\t" \
  "buffer_load_dwordx4 %[wv], %[wrs], %[so0] offen lds\n\t" \
  "buffer_load_dwordx4 %[wv], %[wrs], %[so1] offen offset:1024 lds\n\t" \
  "buffer_load_dwordx4 %[wv], %[wrs], %[so2] offen offset:2048 lds\n\t" \
  "s_add_u32 %[so0], %[so0], %[wc]\n\t" \
  "s_add_u32 %[so1], %[so1], %[wc]\n\t" \
  "s_add_u32 %[so2], %[so2], %[wc]\n\t" \
  ".Lcnx_c10:\n\t" \
  "buffer_load_dwordx4 v[236:239], %[vo], %[xrs], %[oc0] offen offset:64\n\t" \
  "s_cmp_lg_u32 %[rnd], 0\n\t" \
  "s_cbranch_scc1 .Lcnx_c11\n\t" \
  "s_add_u32 m0, %[xl], 8200\n\t" \
  "s_add_u32 %[xo], %[xo], %[rs]\n\t" \
  "buffer_load_dwordx4 %[xv], %[xrs], %[xo] offen lds\n\t" \
  ".Lcnx_c11:\n\t" \
  "s_sub_u32 %[t0], %[grp], %[rnd]\n\t" \
  "s_cmp_lg_u32 %[t0], 1\n\t" \
  "s_cbranch_scc1 .Lcnx_c12\n\t" \
  "s_mov_b32 m0, %[pf]\n\t" \
  "s_add_u32 %[t0], %[wb], 0\n\t" \
  "buffer_load_dword %[wv], %[xrs], %[t0] offen lds\n\t" \
  ".Lcnx_c12:\n\t" \
  "v_cvt_pk_f16_f32 v228, |v200|, |v201| clamp\n\t" \
  "v_exp_f16_sdwa v232, v233 dst_sel:WORD_1 dst_unused:UNUSED_PRESERVE src0_sel:WORD_1\n\t" \
  "v_pk_mul_f16 v235, v234, v234\n\t" \
  "s_waitcnt lgkmcnt(3)\n\t" \
  "v_mfma_f32_16x16x32_bf16 v[0:3], v[208:211], v[146:149], v[0:3]\n\t" \
  "v_and_b32_sdwa v225, %[mk], v224 dst_sel:DWORD dst_unused:UNUSED_PAD src0_sel:DWORD src1_sel:WORD_1\n\t" \
  "v_and_b32 v224, %[mk], v224\n\t" \
  "v_mfma_f32_16x16x32_bf16 v[24:27], v[208:211], v[154:157], 0\n\t" \
  "ds_read_b32 v225, v225 offset:0\n\t" \
  "v_and_b32_sdwa v227, %[mk], v226 dst_sel:DWORD dst_unused:UNUSED_PAD src0_sel:DWORD src1_sel:WORD_1\n\t" \
  "v_mfma_f32_16x16x32_bf16 v[48:51], v[208:211], v[162:165], 0\n\t" \
  "ds_read_b32 v224, v224 offset:0\n\t" \
  "v_and_b32 v226, %[mk], v226\n\t" \
  "v_and_b32_sdwa v229, %[mk], v228 dst_sel:DWORD dst_unused:UNUSED_PAD src0_sel:DWORD src1_sel:WORD_1\n\t" \
  "v_mfma_f32_16x16x32_bf16 v[72:75], v[208:211], v[170:173], 0\n\t" \
  "ds_read_b64_tr_b16 v[208:209], %[a2] offset:6224\n\t" \
  "v_and_b32 v228, %[mk], v228\n\t" \
  "s_waitcnt lgkmcnt(4)\n\t" \
  "v_mfma_f32_16x16x32_bf16 v[4:7], v[212:215], v[146:149], v[4:7]\n\t" \
  "ds_read_b64_tr_b16 v[210:211], %[a2] offset:7760\n\t" \
  "ds_read_b32 v227, v227 offset:0\n\t" \
  "v_pk_fma_f16 v235, v235, %[h2], %[h1]\n\t" \
  "v_mfma_f32_16x16x32_bf16 v[28:31], v[212:215], v[154:157], 0\n\t" \
  "ds_read_b32 v226, v226 offset:0\n\t" \
  "v_pk_mul_f16 v235, v234, v235\n\t" \
  "v_fma_f32 v179, |v179|, v217, v179 mul:4\n\t" \
  "v_fma_f32 v178, |v178|, v216, v178 mul:4\n\t" \
  "v_mfma_f32_16x16x32_bf16 v[52:55], v[212:215], v[162:165], 0\n\t" \
  "ds_read_b32 v229, v229 offset:0\n\t" \
  "v_fma_f32 v187, |v187|, v219, v187 mul:4\n\t" \
  "s_waitcnt lgkmcnt(7)\n\t" \
  "v_fma_f32 v186, |v186|, v218, v186 mul:4\n\t" \
  "v_fma_f32 v185, |v185|, v221, v185 mul:4\n\t" \
  "v_mfma_f32_16x16x32_bf16 v[76:79], v[212:215], v[170:173], 0\n\t" \
  "ds_read_b64_tr_b16 v[212:213], %[a2] offset:6232\n\t" \
  "v_fma_f32 v184, |v184|, v220, v184 mul:4\n\t" \
  "v_pk_fma_f16 v232, v232, %[hh], %[hh]\n\t" \
  "s_waitcnt lgkmcnt(4)\n\t" \
  "v_mfma_f32_16x16x32_bf16 v[8:11], v[208:211], v[146:149], 0\n\t" \
  "ds_read_b64_tr_b16 v[214:215], %[a2] offset:7768\n\t" \
  "v_cvt_pk_bf16_f32 v179, v178, v179\n\t" \
  "v_cvt_pk_bf16_f32 v187, v186, v187\n\t" \
  "v_cvt_pk_bf16_f32 v186, v184, v185\n\t" \
  "v_mfma_f32_16x16x32_bf16 v[32:35], v[208:211], v[154:157], 0\n\t" \
  "ds_read_b32 v228, v228 offset:0\n\t" \
  "v_rcp_f16 v233, v232\n\t" \
  "v_exp_f16 v234, v235\n\t" \
  "v_cvt_pk_f16_f32 v216, |v180|, |v181| clamp\n\t" \
  "s_setprio 2\n\t" \
  "v_mfma_f32_16x16x32_bf16 v[56:59], v[208:211], v[162:165], 0\n\t" \
  "v_cvt_pk_f16_f32 v218, |v182|, |v183| clamp\n\t" \
  "v_cvt_pk_f16_f32 v220, |v188|, |v189| clamp\n\t" \
  "v_mfma_f32_16x16x32_bf16 v[80:83], v[208:211], v[170:173], 0\n\t" \
  "ds_read_b64_tr_b16 v[208:209], %[a2] offset:6288\n\t" \
  "v_cvt_pk_f16_f32 v222, |v190|, |v191| clamp\n\t" \
  "v_rcp_f16_sdwa v232, v232 dst_sel:DWORD dst_unused:UNUSED_PAD src0_sel:WORD_1\n\t" \
  "v_exp_f16_sdwa v234, v235 dst_sel:WORD_1 dst_unused:UNUSED_PRESERVE src0_sel:WORD_1\n\t" \
  "s_waitcnt lgkmcnt(2)\n\t" \
  "v_mfma_f32_16x16x32_bf16 v[12:15], v[212:215], v[146:149], 0\n\t" \
  "ds_read_b64_tr_b16 v[210:211], %[a2] offset:7824\n\t" \
  "v_and_b32_sdwa v217, %[mk], v216 dst_sel:DWORD dst_unused:UNUSED_PAD src0_sel:DWORD src1_sel:WORD_1\n\t" \
  "v_and_b32 v216, %[mk], v216\n\t" \
  "v_mfma_f32_16x16x32_bf16 v[36:39], v[212:215], v[154:157], 0\n\t" \
  "ds_read_b32 v217, v217 offset:0\n\t" \
  "v_and_b32_sdwa v219, %[mk], v218 dst_sel:DWORD dst_unused:UNUSED_PAD src0_sel:DWORD src1_sel:WORD_1\n\t" \
  "v_mfma_f32_16x16x32_bf16 v[60:63], v[212:215], v[162:165], 0\n\t" \
  "ds_read_b32 v216, v216 offset:0\n\t" \
  "v_and_b32 v218, %[mk], v218\n\t" \
  "v_and_b32_sdwa v221, %[mk], v220 dst_sel:DWORD dst_unused:UNUSED_PAD src0_sel:DWORD src1_sel:WORD_1\n\t" \
  "v_mfma_f32_16x16x32_bf16 v[84:87], v[212:215], v[170:173], 0\n\t" \
  "ds_read_b64_tr_b16 v[212:213], %[a2] offset:6296\n\t" \
  "v_and_b32 v220, %[mk], v220\n\t" \
  "s_waitcnt lgkmcnt(3)\n\t" \
  "v_mfma_f32_16x16x32_bf16 v[16:19], v[208:211], v[146:149], 0\n\t" \
  "ds_read_b64_tr_b16 v[214:215], %[a2] offset:7832\n\t" \
  "ds_read_b32 v219, v219 offset:0\n\t" \
  "v_and_b32_sdwa v223, %[mk], v222 dst_sel:DWORD dst_unused:UNUSED_PAD src0_sel:DWORD src1_sel:WORD_1\n\t" \
  "v_mfma_f32_16x16x32_bf16 v[40:43], v[208:211], v[154:157], 0\n\t" \
  "ds_read_b32 v218, v218 offset:0\n\t" \
  "v_and_b32 v222, %[mk], v222\n\t" \
  "v_fma_f32 v195, |v195|, v225, v195 mul:4\n\t" \
  "v_mfma_f32_16x16x32_bf16 v[64:67], v[208:211], v[162:165], 0\n\t" \
  "ds_read_b32 v221, v221 offset:0\n\t" \
  "v_fma_f32 v194, |v194|, v224, v194 mul:4\n\t" \
  "v_fma_f32 v203, |v203|, v227, v203 mul:4\n\t" \
  "v_mfma_f32_16x16x32_bf16 v[88:91], v[208:211], v[170:173], 0\n\t" \
  "ds_read_b64_tr_b16 v[208:209], %[a1] offset:6144\n\t" \
  "ds_read_b64_tr_b16 v[210:211], %[a1] offset:7168\n\t" \
  "v_fma_f32 v202, |v202|, v226, v202 mul:4\n\t" \
  "v_fma_f32 v201, |v201|, v229, v201 mul:4\n\t" \
  "s_waitcnt lgkmcnt(5)\n\t" \
  "v_mfma_f32_16x16x32_bf16 v[20:23], v[212:215], v[146:149], 0\n\t" \
  "ds_read_b32 v220, v220 offset:0\n\t" \
  "v_fma_f32 v200, |v200|, v228, v200 mul:4\n\t" \
  "v_fma_mix_f32 v176, v176, v233, 0 op_sel_hi:[0,1,0]\n\t" \
  "v_fma_mix_f32 v177, v177, v232, 0 op_sel_hi:[0,1,0]\n\t" \
  "v_mfma_f32_16x16x32_bf16 v[44:47], v[212:215], v[154:157], 0\n\t" \
  "ds_read_b32 v223, v223 offset:0\n\t" \
  "v_pk_fma_f16 v234, v234, %[hh], %[hh]\n\t" \
  "v_cvt_pk_bf16_f32 v178, v176, v177\n\t" \
  "v_cvt_pk_bf16_f32 v195, v194, v195\n\t" \
  "v_mfma_f32_16x16x32_bf16 v[68:71], v[212:215], v[162:165], 0\n\t" \
  "ds_read_b32 v222, v222 offset:0\n\t" \
  "v_cvt_pk_bf16_f32 v203, v202, v203\n\t" \
  "s_setprio 1\n\t" \
  "v_cvt_pk_bf16_f32 v202, v200, v201\n\t" \
  "v_rcp_f16 v235, v234\n\t" \
  "v_mfma_f32_16x16x32_bf16 v[92:95], v[212:215], v[170:173], 0\n\t" \
  "ds_read_b64_tr_b16 v[212:213], %[a1] offset:9216\n\t" \
  "v_cvt_pk_f16_f32 v224, |v196|, |v197| clamp\n\t" \
  "v_cvt_pk_f16_f32 v226, |v198|, |v199| clamp\n\t" \
  "s_waitcnt lgkmcnt(4)\n\t" \
  "v_mfma_f32_16x16x32_bf16 v[144:147], v[208:211], v[96:99], 0\n\t" \
  "ds_read_b64_tr_b16 v[214:215], %[a1] offset:10240\n\t" \
  "v_cvt_pk_f16_f32 v228, |v204|, |v205| clamp\n\t" \
  "v_cvt_pk_f16_f32 v230, |v206|, |v207| clamp\n\t" \
  "v_rcp_f16_sdwa v234, v234 dst_sel:DWORD dst_unused:UNUSED_PAD src0_sel:WORD_1\n\t" \
  "v_mfma_f32_16x16x32_bf16 v[152:155], v[208:211], v[108:111], 0\n\t" \
  "v_and_b32_sdwa v225, %[mk], v224 dst_sel:DWORD dst_unused:UNUSED_PAD src0_sel:DWORD src1_sel:WORD_1\n\t" \
  "v_and_b32 v224, %[mk], v224\n\t" \
  "v_mfma_f32_16x16x32_bf16 v[160:163], v[208:211], v[120:123], 0\n\t" \
  "ds_read_b32 v225, v225 offset:0\n\t" \
  "v_and_b32_sdwa v227, %[mk], v226 dst_sel:DWORD dst_unused:UNUSED_PAD src0_sel:DWORD src1_sel:WORD_1\n\t" \
  "v_mfma_f32_16x16x32_bf16 v[168:171], v[208:211], v[132:135], 0\n\t" \
  "ds_read_b64_tr_b16 v[208:209], %[a1] offset:8192\n\t" \
  "v_and_b32 v226, %[mk], v226\n\t" \
  "v_and_b32_sdwa v229, %[mk], v228 dst_sel:DWORD dst_unused:UNUSED_PAD src0_sel:DWORD src1_sel:WORD_1\n\t" \
  "s_waitcnt lgkmcnt(2)\n\t" \
  "v_mfma_f32_16x16x32_bf16 v[144:147], v[212:215], v[100:103], v[144:147]\n\t" \
  "ds_read_b64_tr_b16 v[210:211], %[a1] offset:11264\n\t" \
  "v_and_b32 v228, %[mk], v228\n\t" \
  "v_mfma_f32_16x16x32_bf16 v[152:155], v[212:215], v[112:115], v[152:155]\n\t" \
  "ds_read_b32 v224, v224 offset:0\n\t" \
  "ds_read_b32 v227, v227 offset:0\n\t" \
  "v_and_b32_sdwa v231, %[mk], v230 dst_sel:DWORD dst_unused:UNUSED_PAD src0_sel:DWORD src1_sel:WORD_1\n\t" \
  "v_mfma_f32_16x16x32_bf16 v[160:163], v[212:215], v[124:127], v[160:163]\n\t" \
  "ds_read_b32 v226, v226 offset:0\n\t" \
  "v_and_b32 v230, %[mk], v230\n\t" \
  "v_fma_f32 v181, |v181|, v217, v181 mul:4\n\t" \
  "v_mfma_f32_16x16x32_bf16 v[168:171], v[212:215], v[136:139], v[168:171]\n\t" \
  "ds_read_b64_tr_b16 v[212:213], %[a1] offset:6656\n\t" \
  "ds_read_b64_tr_b16 v[214:215], %[a1] offset:7680\n\t" \
  "v_fma_f32 v180, |v180|, v216, v180 mul:4\n\t" \
  "v_fma_f32 v183, |v183|, v219, v183 mul:4\n\t" \
  "s_waitcnt lgkmcnt(5)\n\t" \
  "v_mfma_f32_16x16x32_bf16 v[144:147], v[208:211], v[104:107], v[144:147]\n\t" \
  "ds_read_b32 v229, v229 offset:0\n\t" \
  "ds_read_b32 v228, v228 offset:0\n\t" \
  "v_fma_f32 v182, |v182|, v218, v182 mul:4\n\t" \
  "v_fma_f32 v189, |v189|, v221, v189 mul:4\n\t" \
  "v_mfma_f32_16x16x32_bf16 v[152:155], v[208:211], v[116:119], v[152:155]\n\t" \
  "ds_read_b32 v231, v231 offset:0\n\t" \
  "v_fma_f32 v188, |v188|, v220, v188 mul:4\n\t" \
  "v_fma_f32 v191, |v191|, v223, v191 mul:4\n\t" \
  "v_fma_f32 v190, |v190|, v222, v190 mul:4\n\t" \
  "v_mfma_f32_16x16x32_bf16 v[160:163], v[208:211], v[128:131], v[160:163]\n\t" \
  "ds_read_b32 v230, v230 offset:0\n\t" \
  "v_fma_mix_f32 v192, v192, v235, 0 op_sel_hi:[0,1,0]\n\t" \
  "v_fma_mix_f32 v193, v193, v234, 0 op_sel_hi:[0,1,0]\n\t" \
  "v_cvt_pk_bf16_f32 v194, v192, v193\n\t" \
  "s_setprio 0\n\t" \
  "v_mfma_f32_16x16x32_bf16 v[168:171], v[208:211], v[140:143], v[168:171]\n\t" \
  "ds_read_b64_tr_b16 v[208:209], %[a1] offset:9728\n\t" \
  "v_cvt_pk_bf16_f32 v180, v180, v181\n\t" \
  "v_cvt_pk_bf16_f32 v181, v182, v183\n\t" \
  "s_waitcnt lgkmcnt(5)\n\t" \
  "v_mfma_f32_16x16x32_bf16 v[148:151], v[212:215], v[96:99], 0\n\t" \
  "ds_read_b64_tr_b16 v[210:211], %[a1] offset:10752\n\t" \
  "v_cvt_pk_bf16_f32 v188, v188, v189\n\t" \
  "v_cvt_pk_bf16_f32 v189, v190, v191\n\t" \
  "v_cvt_pk_f16_f32 v216, |v146|, |v147| clamp\n\t" \
  "v_mfma_f32_16x16x32_bf16 v[156:159], v[212:215], v[108:111], 0\n\t" \
  "v_cvt_pk_f16_f32 v232, v144, v145\n\t" \
  "v_cvt_pk_f16_f32 v218, |v154|, |v155| clamp\n\t" \
  "v_cvt_pk_f16_f32 v220, |v152|, |v153| clamp\n\t" \
  "v_mfma_f32_16x16x32_bf16 v[164:167], v[212:215], v[120:123], 0\n\t" \
  "v_pk_mul_f16 v233, v232, v232\n\t" \
  "v_and_b32_sdwa v217, %[mk], v216 dst_sel:DWORD dst_unused:UNUSED_PAD src0_sel:DWORD src1_sel:WORD_1\n\t" \
  "v_mfma_f32_16x16x32_bf16 v[172:175], v[212:215], v[132:135], 0\n\t" \
  "ds_read_b64_tr_b16 v[212:213], %[a1] offset:8704\n\t" \
  "v_and_b32 v216, %[mk], v216\n\t" \
  "v_and_b32_sdwa v219, %[mk], v218 dst_sel:DWORD dst_unused:UNUSED_PAD src0_sel:DWORD src1_sel:WORD_1\n\t" \
  "s_waitcnt lgkmcnt(1)\n\t" \
  "v_mfma_f32_16x16x32_bf16 v[148:151], v[208:211], v[100:103], v[148:151]\n\t" \
  "ds_read_b64_tr_b16 v[214:215], %[a1] offset:11776\n\t" \
  "v_and_b32 v218, %[mk], v218\n\t" \
  "v_mfma_f32_16x16x32_bf16 v[156:159], v[208:211], v[112:115], v[156:159]\n\t" \
  "ds_read_b32 v217, v217 offset:0\n\t" \
  "v_and_b32_sdwa v221, %[mk], v220 dst_sel:DWORD dst_unused:UNUSED_PAD src0_sel:DWORD src1_sel:WORD_1\n\t" \
  "v_mfma_f32_16x16x32_bf16 v[164:167], v[208:211], v[124:127], v[164:167]\n\t" \
  "ds_read_b32 v216, v216 offset:0\n\t" \
  "v_and_b32 v220, %[mk], v220\n\t" \
  "v_pk_fma_f16 v233, v233, %[h2], %[h1]\n\t" \
  "v_mfma_f32_16x16x32_bf16 v[172:175], v[208:211], v[136:139], v[172:175]\n\t" \
  "ds_read_b32 v219, v219 offset:0\n\t" \
  "v_pk_mul_f16 v233, v232, v233\n\t" \
  "v_fma_f32 v197, |v197|, v225, v197 mul:4\n\t" \
  "s_waitcnt lgkmcnt(3)\n\t" \
  "v_mfma_f32_16x16x32_bf16 v[148:151], v[212:215], v[104:107], v[148:151]\n\t" \
  "ds_read_b32 v218, v218 offset:0\n\t" \
  "v_fma_f32 v196, |v196|, v224, v196 mul:4\n\t" \
  "v_fma_f32 v199, |v199|, v227, v199 mul:4\n\t" \
  "v_mfma_f32_16x16x32_bf16 v[156:159], v[212:215], v[116:119], v[156:159]\n\t" \
  "ds_read_b32 v221, v221 offset:0\n\t" \
  "v_fma_f32 v198, |v198|, v226, v198 mul:4\n\t" \
  "v_fma_f32 v205, |v205|, v229, v205 mul:4\n\t" \
  "v_fma_f32 v204, |v204|, v228, v204 mul:4\n\t" \
  "v_mfma_f32_16x16x32_bf16 v[164:167], v[212:215], v[128:131], v[164:167]\n\t" \
  "ds_read_b32 v220, v220 offset:0\n\t" \
  "v_fma_f32 v207, |v207|, v231, v207 mul:4\n\t" \
  "v_fma_f32 v206, |v206|, v230, v206 mul:4\n\t" \
  "v_cvt_pk_bf16_f32 v196, v196, v197\n\t" \
  "v_mfma_f32_16x16x32_bf16 v[172:175], v[212:215], v[140:143], v[172:175]\n\t" \
  "v_cvt_pk_bf16_f32 v197, v198, v199\n\t" \
  "v_cvt_pk_bf16_f32 v204, v204, v205\n\t" \
  "v_cvt_pk_bf16_f32 v205, v206, v207\n\t" \
  "s_waitcnt vmcnt(1)\n\t" \
  "s_cmp_lg_u32 %[rnd], 1\n\t" \
  "s_cbranch_scc1 .Lcnx_c17\n\t" \
  "s_waitcnt vmcnt(0)\n\t" \
  ".Lcnx_c17:\n\t" \
  "s_barrier\n\t" \
  "ds_read_b64_tr_b16 v[208:209], %[a2] offset:0\n\t" \
  "ds_read_b64_tr_b16 v[210:211], %[a2] offset:1536\n\t" \
  "ds_read_b64_tr_b16 v[212:213], %[a2] offset:8\n\t" \
  "ds_read_b64_tr_b16 v[214:215], %[a2] offset:1544\n\t" \
  "s_setprio 2\n\t" \
  "v_exp_f16 v232, v233\n\t" \
  "v_cvt_pk_f16_f32 v224, |v162|, |v163| clamp\n\t" \
  "v_mfma_f32_16x16x32_bf16 v[8:11], v[242:245], v[236:239], v[8:11]\n\t" \
  "v_cvt_pk_f16_f32 v234, v160, v161\n\t" \
  "v_cvt_pk_f16_f32 v226, |v170|, |v171| clamp\n\t" \
  "v_mfma_f32_16x16x32_bf16 v[12:15], v[240:243], v[236:239], v[12:15]\n\t" \
  "s_cmp_lg_u32 %[grp], 0\n\t" \
  "s_cbranch_scc1 .Lcnx_c14\n\t" \
  "s_add_u32 m0, %[wl], %[ws]\n\t" \
  "s_nop 0\n\t" \
  "buffer_load_dwordx4 %[wv], %[wrs], %[so0] offen lds\n\t" \
  "buffer_load_dwordx4 %[wv], %[wrs], %[so1] offen offset:1024 lds\n\t" \
  "buffer_load_dwordx4 %[wv], %[wrs], %[so2] offen offset:2048 lds\n\t" \
  "s_add_u32 %[so0], %[so0], %[wc]\n\t" \
  "s_add_u32 %[so1], %[so1], %[wc]\n\t" \
  "s_add_u32 %[so2], %[so2], %[wc]\n\t" \
  ".Lcnx_c14:\n\t" \
  "buffer_load_dwordx4 v[236:239], %[vo], %[xrs], %[oc0] offen offset:128\n\t" \
  "s_cmp_lg_u32 %[rnd], 0\n\t" \
  "s_cbranch_scc1 .Lcnx_c15\n\t" \
  "s_add_u32 m0, %[xl], 16400\n\t" \
  "s_add_u32 %[xo], %[xo], %[rs]\n\t" \
  "buffer_load_dwordx4 %[xv], %[xrs], %[xo] offen lds\n\t" \
  ".Lcnx_c15:\n\t" \
  "s_sub_u32 %[t0], %[grp], %[rnd]\n\t" \
  "s_cmp_lg_u32 %[t0], 1\n\t" \
  "s_cbranch_scc1 .Lcnx_c16\n\t" \
  "s_mov_b32 m0, %[pf]\n\t" \
  "s_add_u32 %[t0], %[wb], 491520\n\t" \
  "buffer_load_dword %[wv], %[xrs], %[t0] offen lds\n\t" \
  ".Lcnx_c16:\n\t" \
  "v_cvt_pk_f16_f32 v228, |v168|, |v169| clamp\n\t" \
  "v_exp_f16_sdwa v232, v233 dst_sel:WORD_1 dst_unused:UNUSED_PRESERVE src0_sel:WORD_1\n\t" \
  "v_pk_mul_f16 v235, v234, v234\n\t" \
  "s_waitcnt lgkmcnt(2)\n\t" \
  "v_mfma_f32_16x16x32_bf16 v[0:3], v[208:211], v[178:181], v[0:3]\n\t" \
  "v_and_b32_sdwa v225, %[mk], v224 dst_sel:DWORD dst_unused:UNUSED_PAD src0_sel:DWORD src1_sel:WORD_1\n\t" \
  "v_and_b32 v224, %[mk], v224\n\t" \
  "v_mfma_f32_16x16x32_bf16 v[24:27], v[208:211], v[186:189], v[24:27]\n\t" \
  "ds_read_b32 v225, v225 offset:0\n\t" \
  "v_and_b32_sdwa v227, %[mk], v226 dst_sel:DWORD dst_unused:UNUSED_PAD src0_sel:DWORD src1_sel:WORD_1\n\t" \
  "v_mfma_f32_16x16x32_bf16 v[48:51], v[208:211], v[194:197], v[48:51]\n\t" \
  "ds_read_b32 v224, v224 offset:0\n\t" \
  "v_and_b32 v226, %[mk], v226\n\t" \
  "v_and_b32_sdwa v229, %[mk], v228 dst_sel:DWORD dst_unused:UNUSED_PAD src0_sel:DWORD src1_sel:WORD_1\n\t" \
  "v_mfma_f32_16x16x32_bf16 v[72:75], v[208:211], v[202:205], v[72:75]\n\t" \
  "ds_read_b64_tr_b16 v[208:209], %[a2] offset:64\n\t" \
  "v_and_b32 v228, %[mk], v228\n\t" \
  "s_waitcnt lgkmcnt(3)\n\t" \
  "v_mfma_f32_16x16x32_bf16 v[4:7], v[212:215], v[178:181], v[4:7]\n\t" \
  "ds_read_b64_tr_b16 v[210:211], %[a2] offset:1600\n\t" \
  "ds_read_b32 v227, v227 offset:0\n\t" \
  "v_pk_fma_f16 v235, v235, %[h2], %[h1]\n\t" \
  "v_mfma_f32_16x16x32_bf16 v[28:31], v[212:215], v[186:189], v[28:31]\n\t" \
  "ds_read_b32 v226, v226 offset:0\n\t" \
  "v_pk_mul_f16 v235, v234, v235\n\t" \
  "v_fma_f32 v147, |v147|, v217, v147 mul:4\n\t" \
  "v_fma_f32 v146, |v146|, v216, v146 mul:4\n\t" \
  "v_mfma_f32_16x16x32_bf16 v[52:55], v[212:215], v[194:197], v[52:55]\n\t" \
  "ds_read_b32 v229, v229 offset:0\n\t" \
  "v_fma_f32 v155, |v155|, v219, v155 mul:4\n\t" \
  "v_fma_f32 v154, |v154|, v218, v154 mul:4\n\t" \
  "v_fma_f32 v153, |v153|, v221, v153 mul:4\n\t" \
  "v_mfma_f32_16x16x32_bf16 v[76:79], v[212:215], v[202:205], v[76:79]\n\t" \
  "ds_read_b64_tr_b16 v[212:213], %[a2] offset:72\n\t" \
  "v_fma_f32 v152, |v152|, v220, v152 mul:4\n\t" \
  "v_pk_fma_f16 v232, v232, %[hh], %[hh]\n\t" \
  "s_waitcnt lgkmcnt(4)\n\t" \
  "v_mfma_f32_16x16x32_bf16 v[8:11], v[208:211], v[178:181], v[8:11]\n\t" \
  "ds_read_b64_tr_b16 v[214:215], %[a2] offset:1608\n\t" \
  "v_cvt_pk_bf16_f32 v147, v146, v147\n\t" \
  "v_cvt_pk_bf16_f32 v155, v154, v155\n\t" \
  "v_cvt_pk_bf16_f32 v154, v152, v153\n\t" \
  "v_mfma_f32_16x16x32_bf16 v[32:35], v[208:211], v[186:189], v[32:35]\n\t" \
  "ds_read_b32 v228, v228 offset:0\n\t" \
  "v_rcp_f16 v233, v232\n\t" \
  "v_exp_f16 v234, v235\n\t" \
  "v_cvt_pk_f16_f32 v216, |v148|, |v149| clamp\n\t" \
  "v_mfma_f32_16x16x32_bf16 v[56:59], v[208:211], v[194:197], v[56:59]\n\t" \
  "s_setprio 2\n\t" \
  "v_cvt_pk_f16_f32 v218, |v150|, |v151| clamp\n\t" \
  "v_cvt_pk_f16_f32 v220, |v156|, |v157| clamp\n\t" \
  "v_mfma_f32_16x16x32_bf16 v[80:83], v[208:211], v[202:205], v[80:83]\n\t" \
  "ds_read_b64_tr_b16 v[208:209], %[a2] offset:128\n\t" \
  "v_cvt_pk_f16_f32 v222, |v158|, |v159| clamp\n\t" \
  "v_rcp_f16_sdwa v232, v232 dst_sel:DWORD dst_unused:UNUSED_PAD src0_sel:WORD_1\n\t" \
  "v_exp_f16_sdwa v234, v235 dst_sel:WORD_1 dst_unused:UNUSED_PRESERVE src0_sel:WORD_1\n\t" \
  "s_waitcnt lgkmcnt(2)\n\t" \
  "v_mfma_f32_16x16x32_bf16 v[12:15], v[212:215], v[178:181], v[12:15]\n\t" \
  "ds_read_b64_tr_b16 v[210:211], %[a2] offset:1664\n\t" \
  "v_and_b32_sdwa v217, %[mk], v216 dst_sel:DWORD dst_unused:UNUSED_PAD src0_sel:DWORD src1_sel:WORD_1\n\t" \
  "v_and_b32 v216, %[mk], v216\n\t" \
  "v_mfma_f32_16x16x32_bf16 v[36:39], v[212:215], v[186:189], v[36:39]\n\t" \
  "ds_read_b32 v217, v217 offset:0\n\t" \
  "v_and_b32_sdwa v219, %[mk], v218 dst_sel:DWORD dst_unused:UNUSED_PAD src0_sel:DWORD src1_sel:WORD_1\n\t" \
  "v_mfma_f32_16x16x32_bf16 v[60:63], v[212:215], v[194:197], v[60:63]\n\t" \
  "ds_read_b32 v216, v216 offset:0\n\t" \
  "v_and_b32 v218, %[mk], v218\n\t" \
  "v_and_b32_sdwa v221, %[mk], v220 dst_sel:DWORD dst_unused:UNUSED_PAD src0_sel:DWORD src1_sel:WORD_1\n\t" \
  "v_mfma_f32_16x16x32_bf16 v[84:87], v[212:215], v[202:205], v[84:87]\n\t" \
  "ds_read_b64_tr_b16 v[212:213], %[a2] offset:136\n\t" \
  "v_and_b32 v220, %[mk], v220\n\t" \
  "s_waitcnt lgkmcnt(3)\n\t" \
  "v_mfma_f32_16x16x32_bf16 v[16:19], v[208:211], v[178:181], v[16:19]\n\t" \
  "ds_read_b64_tr_b16 v[214:215], %[a2] offset:1672\n\t" \
  "ds_read_b32 v219, v219 offset:0\n\t" \
  "v_and_b32_sdwa v223, %[mk], v222 dst_sel:DWORD dst_unused:UNUSED_PAD src0_sel:DWORD src1_sel:WORD_1\n\t" \
  "v_mfma_f32_16x16x32_bf16 v[40:43], v[208:211], v[186:189], v[40:43]\n\t" \
  "ds_read_b32 v218, v218 offset:0\n\t" \
  "v_and_b32 v222, %[mk], v222\n\t" \
  "v_fma_f32 v163, |v163|, v225, v163 mul:4\n\t" \
  "v_mfma_f32_16x16x32_bf16 v[64:67], v[208:211], v[194:197], v[64:67]\n\t" \
  "ds_read_b32 v221, v221 offset:0\n\t" \
  "v_fma_f32 v162, |v162|, v224, v162 mul:4\n\t" \
  "v_fma_f32 v171, |v171|, v227, v171 mul:4\n\t" \
  "v_mfma_f32_16x16x32_bf16 v[88:91], v[208:211], v[202:205], v[88:91]\n\t" \
  "ds_read_b64_tr_b16 v[208:209], %[a1] offset:0\n\t" \
  "ds_read_b64_tr_b16 v[210:211], %[a1] offset:1024\n\t" \
  "v_fma_f32 v170, |v170|, v226, v170 mul:4\n\t" \
  "v_fma_f32 v169, |v169|, v229, v169 mul:4\n\t" \
  "s_waitcnt lgkmcnt(5)\n\t" \
  "v_mfma_f32_16x16x32_bf16 v[20:23], v[212:215], v[178:181], v[20:23]\n\t" \
  "ds_read_b32 v220, v220 offset:0\n\t" \
  "v_fma_f32 v168, |v168|, v228, v168 mul:4\n\t" \
  "v_fma_mix_f32 v144, v144, v233, 0 op_sel_hi:[0,1,0]\n\t" \
  "v_fma_mix_f32 v145, v145, v232, 0 op_sel_hi:[0,1,0]\n\t" \
  "v_mfma_f32_16x16x32_bf16 v[44:47], v[212:215], v[186:189], v[44:47]\n\t" \
  "ds_read_b32 v223, v223 offset:0\n\t" \
  "v_pk_fma_f16 v234, v234, %[hh], %[hh]\n\t" \
  "v_cvt_pk_bf16_f32 v146, v144, v145\n\t" \
  "v_cvt_pk_bf16_f32 v163, v162, v163\n\t" \
  "v_mfma_f32_16x16x32_bf16 v[68:71], v[212:215], v[194:197], v[68:71]\n\t" \
  "ds_read_b32 v222, v222 offset:0\n\t" \
  "v_cvt_pk_bf16_f32 v171, v170, v171\n\t" \
  "v_cvt_pk_bf16_f32 v170, v168, v169\n\t" \
  "s_setprio 1\n\t" \
  "v_rcp_f16 v235, v234\n\t" \
  "v_mfma_f32_16x16x32_bf16 v[92:95], v[212:215], v[202:205], v[92:95]\n\t" \
  "ds_read_b64_tr_b16 v[212:213], %[a1] offset:3072\n\t" \
  "v_cvt_pk_f16_f32 v224, |v164|, |v165| clamp\n\t" \
  "v_cvt_pk_f16_f32 v226, |v166|, |v167| clamp\n\t" \
  "s_waitcnt lgkmcnt(4)\n\t" \
  "v_mfma_f32_16x16x32_bf16 v[176:179], v[208:211], v[96:99], 0\n\t" \
  "ds_read_b64_tr_b16 v[214:215], %[a1] offset:4096\n\t" \
  "v_cvt_pk_f16_f32 v228, |v172|, |v173| clamp\n\t" \
  "v_cvt_pk_f16_f32 v230, |v174|, |v175| clamp\n\t" \
  "v_rcp_f16_sdwa v234, v234 dst_sel:DWORD dst_unused:UNUSED_PAD src0_sel:WORD_1\n\t" \
  "v_mfma_f32_16x16x32_bf16 v[184:187], v[208:211], v[108:111], 0\n\t" \
  "v_and_b32_sdwa v225, %[mk], v224 dst_sel:DWORD dst_unused:UNUSED_PAD src0_sel:DWORD src1_sel:WORD_1\n\t" \
  "v_and_b32 v224, %[mk], v224\n\t" \
  "v_mfma_f32_16x16x32_bf16 v[192:195], v[208:211], v[120:123], 0\n\t" \
  "ds_read_b32 v225, v225 offset:0\n\t" \
  "v_and_b32_sdwa v227, %[mk], v226 dst_sel:DWORD dst_unused:UNUSED_PAD src0_sel:DWORD src1_sel:WORD_1\n\t" \
  "v_mfma_f32_16x16x32_bf16 v[200:203], v[208:211], v[132:135], 0\n\t" \
  "ds_read_b64_tr_b16 v[208:209], %[a1] offset:2048\n\t" \
  "v_and_b32 v226, %[mk], v226\n\t" \
  "v_and_b32_sdwa v229, %[mk], v228 dst_sel:DWORD dst_unused:UNUSED_PAD src0_sel:DWORD src1_sel:WORD_1\n\t" \
  "s_waitcnt lgkmcnt(2)\n\t" \
  "v_mfma_f32_16x16x32_bf16 v[176:179], v[212:215], v[100:103], v[176:179]\n\t" \
  "ds_read_b64_tr_b16 v[210:211], %[a1] offset:5120\n\t" \
  "v_and_b32 v228, %[mk], v228\n\t" \
  "v_mfma_f32_16x16x32_bf16 v[184:187], v[212:215], v[112:115], v[184:187]\n\t" \
  "ds_read_b32 v224, v224 offset:0\n\t" \
  "ds_read_b32 v227, v227 offset:0\n\t" \
  "v_and_b32_sdwa v231, %[mk], v230 dst_sel:DWORD dst_unused:UNUSED_PAD src0_sel:DWORD src1_sel:WORD_1\n\t" \
  "v_mfma_f32_16x16x32_bf16 v[192:195], v[212:215], v[124:127], v[192:195]\n\t" \
  "ds_read_b32 v226, v226 offset:0\n\t" \
  "v_and_b32 v230, %[mk], v230\n\t" \
  "v_fma_f32 v149, |v149|, v217, v149 mul:4\n\t" \
  "v_mfma_f32_16x16x32_bf16 v[200:203], v[212:215], v[136:139], v[200:203]\n\t" \
  "ds_read_b64_tr_b16 v[212:213], %[a1] offset:512\n\t" \
  "ds_read_b64_tr_b16 v[214:215], %[a1] offset:1536\n\t" \
  "v_fma_f32 v148, |v148|, v216, v148 mul:4\n\t" \
  "v_fma_f32 v151, |v151|, v219, v151 mul:4\n\t" \
  "s_waitcnt lgkmcnt(5)\n\t" \
  "v_mfma_f32_16x16x32_bf16 v[176:179], v[208:211], v[104:107], v[176:179]\n\t" \
  "ds_read_b32 v229, v229 offset:0\n\t" \
  "ds_read_b32 v228, v228 offset:0\n\t" \
  "v_fma_f32 v150, |v150|, v218, v150 mul:4\n\t" \
  "v_fma_f32 v157, |v157|, v221, v157 mul:4\n\t" \
  "v_mfma_f32_16x16x32_bf16 v[184:187], v[208:211], v[116:119], v[184:187]\n\t" \
  "ds_read_b32 v231, v231 offset:0\n\t" \
  "v_fma_f32 v156, |v156|, v220, v156 mul:4\n\t" \
  "v_fma_f32 v159, |v159|, v223, v159 mul:4\n\t" \
  "v_fma_f32 v158, |v158|, v222, v158 mul:4\n\t" \
  "v_mfma_f32_16x16x32_bf16 v[192:195], v[208:211], v[128:131], v[192:195]\n\t" \
  "ds_read_b32 v230, v230 offset:0\n\t" \
  "v_fma_mix_f32 v160, v160, v235, 0 op_sel_hi:[0,1,0]\n\t" \
  "v_fma_mix_f32 v161, v161, v234, 0 op_sel_hi:[0,1,0]\n\t" \
  "v_cvt_pk_bf16_f32 v162, v160, v161\n\t" \
  "s_setprio 0\n\t" \
  "v_mfma_f32_16x16x32_bf16 v[200:203], v[208:211], v[140:143], v[200:203]\n\t" \
  "ds_read_b64_tr_b16 v[208:209], %[a1] offset:3584\n\t" \
  "v_cvt_pk_bf16_f32 v148, v148, v149\n\t" \
  "v_cvt_pk_bf16_f32 v149, v150, v151\n\t" \
  "s_waitcnt lgkmcnt(5)\n\t" \
  "v_mfma_f32_16x16x32_bf16 v[180:183], v[212:215], v[96:99], 0\n\t" \
  "ds_read_b64_tr_b16 v[210:211], %[a1] offset:4608\n\t" \
  "v_cvt_pk_bf16_f32 v156, v156, v157\n\t" \
  "v_cvt_pk_bf16_f32 v157, v158, v159\n\t" \
  "v_cvt_pk_f16_f32 v216, |v178|, |v179| clamp\n\t" \
  "v_mfma_f32_16x16x32_bf16 v[188:191], v[212:215], v[108:111], 0\n\t" \
  "v_cvt_pk_f16_f32 v232, v176, v177\n\t" \
  "v_cvt_pk_f16_f32 v218, |v186|, |v187| clamp\n\t" \
  "v_cvt_pk_f16_f32 v220, |v184|, |v185| clamp\n\t" \
  "v_mfma_f32_16x16x32_bf16 v[196:199], v[212:215], v[120:123], 0\n\t" \
  "v_pk_mul_f16 v233, v232, v232\n\t" \
  "v_and_b32_sdwa v217, %[mk], v216 dst_sel:DWORD dst_unused:UNUSED_PAD src0_sel:DWORD src1_sel:WORD_1\n\t" \
  "v_mfma_f32_16x16x32_bf16 v[204:207], v[212:215], v[132:135], 0\n\t" \
  "ds_read_b64_tr_b16 v[212:213], %[a1] offset:2560\n\t" \
  "v_and_b32 v216, %[mk], v216\n\t" \
  "v_and_b32_sdwa v219, %[mk], v218 dst_sel:DWORD dst_unused:UNUSED_PAD src0_sel:DWORD src1_sel:WORD_1\n\t" \
  "s_waitcnt lgkmcnt(1)\n\t" \
  "v_mfma_f32_16x16x32_bf16 v[180:183], v[208:211], v[100:103], v[180:183]\n\t" \
  "ds_read_b64_tr_b16 v[214:215], %[a1] offset:5632\n\t" \
  "v_and_b32 v218, %[mk], v218\n\t" \
  "v_mfma_f32_16x16x32_bf16 v[188:191], v[208:211], v[112:115], v[188:191]\n\t" \
  "ds_read_b32 v217, v217 offset:0\n\t" \
  "v_and_b32_sdwa v221, %[mk], v220 dst_sel:DWORD dst_unused:UNUSED_PAD src0_sel:DWORD src1_sel:WORD_1\n\t" \
  "v_mfma_f32_16x16x32_bf16 v[196:199], v[208:211], v[124:127], v[196:199]\n\t" \
  "ds_read_b32 v216, v216 offset:0\n\t" \
  "v_and_b32 v220, %[mk], v220\n\t" \
  "v_pk_fma_f16 v233, v233, %[h2], %[h1]\n\t" \
  "v_mfma_f32_16x16x32_bf16 v[204:207], v[208:211], v[136:139], v[204:207]\n\t" \
  "ds_read_b32 v219, v219 offset:0\n\t" \
  "v_pk_mul_f16 v233, v232, v233\n\t" \
  "v_fma_f32 v165, |v165|, v225, v165 mul:4\n\t" \
  "s_waitcnt lgkmcnt(3)\n\t" \
  "v_mfma_f32_16x16x32_bf16 v[180:183], v[212:215], v[104:107], v[180:183]\n\t" \
  "ds_read_b32 v218, v218 offset:0\n\t" \
  "v_fma_f32 v164, |v164|, v224, v164 mul:4\n\t" \
  "v_fma_f32 v167, |v167|, v227, v167 mul:4\n\t" \
  "v_mfma_f32_16x16x32_bf16 v[188:191], v[212:215], v[116:119], v[188:191]\n\t" \
  "ds_read_b32 v221, v221 offset:0\n\t" \
  "v_fma_f32 v166, |v166|, v226, v166 mul:4\n\t" \
  "v_fma_f32 v173, |v173|, v229, v173 mul:4\n\t" \
  "v_fma_f32 v172, |v172|, v228, v172 mul:4\n\t" \
  "v_mfma_f32_16x16x32_bf16 v[196:199], v[212:215], v[128:131], v[196:199]\n\t" \
  "ds_read_b32 v220, v220 offset:0\n\t" \
  "v_fma_f32 v175, |v175|, v231, v175 mul:4\n\t" \
  "v_fma_f32 v174, |v174|, v230, v174 mul:4\n\t" \
  "v_cvt_pk_bf16_f32 v164, v164, v165\n\t" \
  "v_mfma_f32_16x16x32_bf16 v[204:207], v[212:215], v[140:143], v[204:207]\n\t" \
  "v_cvt_pk_bf16_f32 v165, v166, v167\n\t" \
  "v_cvt_pk_bf16_f32 v172, v172, v173\n\t" \
  "v_cvt_pk_bf16_f32 v173, v174, v175\n\t" \
  "s_waitcnt vmcnt(1)\n\t" \
  "s_cmp_lg_u32 %[rnd], 1\n\t" \
  "s_cbranch_scc1 .Lcnx_c20\n\t" \
  "s_waitcnt vmcnt(0)\n\t" \
  ".Lcnx_c20:\n\t" \
  "s_barrier\n\t" \
  "ds_read_b64_tr_b16 v[208:209], %[a2] offset:6160\n\t" \
  "ds_read_b64_tr_b16 v[210:211], %[a2] offset:7696\n\t" \
  "ds_read_b64_tr_b16 v[212:213], %[a2] offset:6168\n\t" \
  "ds_read_b64_tr_b16 v[214:215], %[a2] offset:7704\n\t" \
  "s_setprio 2\n\t" \
  "v_exp_f16 v232, v233\n\t" \
  "v_cvt_pk_f16_f32 v224, |v194|, |v195| clamp\n\t" \
  "v_mfma_f32_16x16x32_bf16 v[16:19], v[242:245], v[236:239], v[16:19]\n\t" \
  "v_cvt_pk_f16_f32 v234, v192, v193\n\t" \
  "v_cvt_pk_f16_f32 v226, |v202|, |v203| clamp\n\t" \
  "v_mfma_f32_16x16x32_bf16 v[20:23], v[240:243], v[236:239], v[20:23]\n\t" \
  "s_cmp_lg_u32 %[grp], 0\n\t" \
  "s_cbranch_scc1 .Lcnx_c18\n\t" \
  "s_add_u32 m0, %[wl], 0\n\t" \
  "s_nop 0\n\t" \
  "buffer_load_dwordx4 %[wv], %[wrs], %[so0] offen lds\n\t" \
  "buffer_load_dwordx4 %[wv], %[wrs], %[so1] offen offset:1024 lds\n\t" \
  "buffer_load_dwordx4 %[wv], %[wrs], %[so2] offen offset:2048 lds\n\t" \
  "s_add_u32 %[so0], %[so0], %[wc]\n\t" \
  "s_add_u32 %[so1], %[so1], %[wc]\n\t" \
  "s_add_u32 %[so2], %[so2], %[wc]\n\t" \
  ".Lcnx_c18:\n\t" \
  "buffer_load_dwordx4 v[236:239], %[vo], %[xrs], %[oc0] offen offset:192\n\t" \
  "s_cmp_lg_u32 %[rnd], 0\n\t" \
  "s_cbranch_scc1 .Lcnx_c19\n\t" \
  "s_add_u32 m0, %[xl], 24600\n\t" \
  "s_add_u32 %[xo], %[xo], %[rs]\n\t" \
  "buffer_load_dwordx4 %[xv], %[xrs], %[xo] offen lds\n\t" \
  ".Lcnx_c19:\n\t" \
  "v_cvt_pk_f16_f32 v228, |v200|, |v201| clamp\n\t" \
  "v_exp_f16_sdwa v232, v233 dst_sel:WORD_1 dst_unused:UNUSED_PRESERVE src0_sel:WORD_1\n\t" \
  "v_pk_mul_f16 v235, v234, v234\n\t" \
  "s_waitcnt lgkmcnt(2)\n\t" \
  "v_mfma_f32_16x16x32_bf16 v[0:3], v[208:211], v[146:149], v[0:3]\n\t" \
  "v_and_b32_sdwa v225, %[mk], v224 dst_sel:DWORD dst_unused:UNUSED_PAD src0_sel:DWORD src1_sel:WORD_1\n\t" \
  "v_and_b32 v224, %[mk], v224\n\t" \
  "v_mfma_f32_16x16x32_bf16 v[24:27], v[208:211], v[154:157], v[24:27]\n\t" \
  "ds_read_b32 v225, v225 offset:0\n\t" \
  "v_and_b32_sdwa v227, %[mk], v226 dst_sel:DWORD dst_unused:UNUSED_PAD src0_sel:DWORD src1_sel:WORD_1\n\t" \
  "v_mfma_f32_16x16x32_bf16 v[48:51], v[208:211], v[162:165], v[48:51]\n\t" \
  "ds_read_b32 v224, v224 offset:0\n\t" \
  "v_and_b32 v226, %[mk], v226\n\t" \
  "v_and_b32_sdwa v229, %[mk], v228 dst_sel:DWORD dst_unused:UNUSED_PAD src0_sel:DWORD src1_sel:WORD_1\n\t" \
  "v_mfma_f32_16x16x32_bf16 v[72:75], v[208:211], v[170:173], v[72:75]\n\t" \
  "ds_read_b64_tr_b16 v[208:209], %[a2] offset:6224\n\t" \
  "v_and_b32 v228, %[mk], v228\n\t" \
  "s_waitcnt lgkmcnt(3)\n\t" \
  "v_mfma_f32_16x16x32_bf16 v[4:7], v[212:215], v[146:149], v[4:7]\n\t" \
  "ds_read_b64_tr_b16 v[210:211], %[a2] offset:7760\n\t" \
  "ds_read_b32 v227, v227 offset:0\n\t" \
  "v_pk_fma_f16 v235, v235, %[h2], %[h1]\n\t" \
  "v_mfma_f32_16x16x32_bf16 v[28:31], v[212:215], v[154:157], v[28:31]\n\t" \
  "ds_read_b32 v226, v226 offset:0\n\t" \
  "v_pk_mul_f16 v235, v234, v235\n\t" \
  "v_fma_f32 v179, |v179|, v217, v179 mul:4\n\t" \
  "v_fma_f32 v178, |v178|, v216, v178 mul:4\n\t" \
  "v_mfma_f32_16x16x32_bf16 v[52:55], v[212:215], v[162:165], v[52:55]\n\t" \
  "ds_read_b32 v229, v229 offset:0\n\t" \
  "v_fma_f32 v187, |v187|, v219, v187 mul:4\n\t" \
  "v_fma_f32 v186, |v186|, v218, v186 mul:4\n\t" \
  "v_fma_f32 v185, |v185|, v221, v185 mul:4\n\t" \
  "v_mfma_f32_16x16x32_bf16 v[76:79], v[212:215], v[170:173], v[76:79]\n\t" \
  "ds_read_b64_tr_b16 v[212:213], %[a2] offset:6232\n\t" \
  "v_fma_f32 v184, |v184|, v220, v184 mul:4\n\t" \
  "v_pk_fma_f16 v232, v232, %[hh], %[hh]\n\t" \
  "s_waitcnt lgkmcnt(4)\n\t" \
  "v_mfma_f32_16x16x32_bf16 v[8:11], v[208:211], v[146:149], v[8:11]\n\t" \
  "ds_read_b64_tr_b16 v[214:215], %[a2] offset:7768\n\t" \
  "v_cvt_pk_bf16_f32 v179, v178, v179\n\t" \
  "v_cvt_pk_bf16_f32 v187, v186, v187\n\t" \
  "v_cvt_pk_bf16_f32 v186, v184, v185\n\t" \
  "v_mfma_f32_16x16x32_bf16 v[32:35], v[208:211], v[154:157], v[32:35]\n\t" \
  "ds_read_b32 v228, v228 offset:0\n\t" \
  "v_rcp_f16 v233, v232\n\t" \
  "v_exp_f16 v234, v235\n\t" \
  "v_cvt_pk_f16_f32 v216, |v180|, |v181| clamp\n\t" \
  "v_mfma_f32_16x16x32_bf16 v[56:59], v[208:211], v[162:165], v[56:59]\n\t" \
  "s_setprio 2\n\t" \
  "v_cvt_pk_f16_f32 v218, |v182|, |v183| clamp\n\t" \
  "v_cvt_pk_f16_f32 v220, |v188|, |v189| clamp\n\t" \
  "v_mfma_f32_16x16x32_bf16 v[80:83], v[208:211], v[170:173], v[80:83]\n\t" \
  "ds_read_b64_tr_b16 v[208:209], %[a2] offset:6288\n\t" \
  "v_cvt_pk_f16_f32 v222, |v190|, |v191| clamp\n\t" \
  "v_rcp_f16_sdwa v232, v232 dst_sel:DWORD dst_unused:UNUSED_PAD src0_sel:WORD_1\n\t" \
  "v_exp_f16_sdwa v234, v235 dst_sel:WORD_1 dst_unused:UNUSED_PRESERVE src0_sel:WORD_1\n\t" \
  "s_waitcnt lgkmcnt(2)\n\t" \
  "v_mfma_f32_16x16x32_bf16 v[12:15], v[212:215], v[146:149], v[12:15]\n\t" \
  "ds_read_b64_tr_b16 v[210:211], %[a2] offset:7824\n\t" \
  "v_and_b32_sdwa v217, %[mk], v216 dst_sel:DWORD dst_unused:UNUSED_PAD src0_sel:DWORD src1_sel:WORD_1\n\t" \
  "v_and_b32 v216, %[mk], v216\n\t" \
  "v_mfma_f32_16x16x32_bf16 v[36:39], v[212:215], v[154:157], v[36:39]\n\t" \
  "ds_read_b32 v217, v217 offset:0\n\t" \
  "v_and_b32_sdwa v219, %[mk], v218 dst_sel:DWORD dst_unused:UNUSED_PAD src0_sel:DWORD src1_sel:WORD_1\n\t" \
  "v_mfma_f32_16x16x32_bf16 v[60:63], v[212:215], v[162:165], v[60:63]\n\t" \
  "ds_read_b32 v216, v216 offset:0\n\t" \
  "v_and_b32 v218, %[mk], v218\n\t" \
  "v_and_b32_sdwa v221, %[mk], v220 dst_sel:DWORD dst_unused:UNUSED_PAD src0_sel:DWORD src1_sel:WORD_1\n\t" \
  "v_mfma_f32_16x16x32_bf16 v[84:87], v[212:215], v[170:173], v[84:87]\n\t" \
  "ds_read_b64_tr_b16 v[212:213], %[a2] offset:6296\n\t" \
  "v_and_b32 v220, %[mk], v220\n\t" \
  "s_waitcnt lgkmcnt(3)\n\t" \
  "v_mfma_f32_16x16x32_bf16 v[16:19], v[208:211], v[146:149], v[16:19]\n\t" \
  "ds_read_b64_tr_b16 v[214:215], %[a2] offset:7832\n\t" \
  "ds_read_b32 v219, v219 offset:0\n\t" \
  "v_and_b32_sdwa v223, %[mk], v222 dst_sel:DWORD dst_unused:UNUSED_PAD src0_sel:DWORD src1_sel:WORD_1\n\t" \
  "v_mfma_f32_16x16x32_bf16 v[40:43], v[208:211], v[154:157], v[40:43]\n\t" \
  "ds_read_b32 v218, v218 offset:0\n\t" \
  "v_and_b32 v222, %[mk], v222\n\t" \
  "v_fma_f32 v195, |v195|, v225, v195 mul:4\n\t" \
  "v_mfma_f32_16x16x32_bf16 v[64:67], v[208:211], v[162:165], v[64:67]\n\t" \
  "ds_read_b32 v221, v221 offset:0\n\t" \
  "v_fma_f32 v194, |v194|, v224, v194 mul:4\n\t" \
  "v_fma_f32 v203, |v203|, v227, v203 mul:4\n\t" \
  "v_mfma_f32_16x16x32_bf16 v[88:91], v[208:211], v[170:173], v[88:91]\n\t" \
  "ds_read_b64_tr_b16 v[208:209], %[a1] offset:6144\n\t" \
  "ds_read_b64_tr_b16 v[210:211], %[a1] offset:7168\n\t" \
  "v_fma_f32 v202, |v202|, v226, v202 mul:4\n\t" \
  "v_fma_f32 v201, |v201|, v229, v201 mul:4\n\t" \
  "s_waitcnt lgkmcnt(5)\n\t" \
  "v_mfma_f32_16x16x32_bf16 v[20:23], v[212:215], v[146:149], v[20:23]\n\t" \
  "ds_read_b32 v220, v220 offset:0\n\t" \
  "v_fma_f32 v200, |v200|, v228, v200 mul:4\n\t" \
  "v_fma_mix_f32 v176, v176, v233, 0 op_sel_hi:[0,1,0]\n\t" \
  "v_fma_mix_f32 v177, v177, v232, 0 op_sel_hi:[0,1,0]\n\t" \
  "v_mfma_f32_16x16x32_bf16 v[44:47], v[212:215], v[154:157], v[44:47]\n\t" \
  "ds_read_b32 v223, v223 offset:0\n\t" \
  "v_pk_fma_f16 v234, v234, %[hh], %[hh]\n\t" \
  "v_cvt_pk_bf16_f32 v178, v176, v177\n\t" \
  "v_cvt_pk_bf16_f32 v195, v194, v195\n\t" \
  "v_mfma_f32_16x16x32_bf16 v[68:71], v[212:215], v[162:165], v[68:71]\n\t" \
  "ds_read_b32 v222, v222 offset:0\n\t" \
  "v_cvt_pk_bf16_f32 v203, v202, v203\n\t" \
  "v_cvt_pk_bf16_f32 v202, v200, v201\n\t" \
  "s_setprio 1\n\t" \
  "v_rcp_f16 v235, v234\n\t" \
  "v_mfma_f32_16x16x32_bf16 v[92:95], v[212:215], v[170:173], v[92:95]\n\t" \
  "ds_read_b64_tr_b16 v[212:213], %[a1] offset:9216\n\t" \
  "v_cvt_pk_f16_f32 v224, |v196|, |v197| clamp\n\t" \
  "v_cvt_pk_f16_f32 v226, |v198|, |v199| clamp\n\t" \
  "s_waitcnt lgkmcnt(4)\n\t" \
  "v_mfma_f32_16x16x32_bf16 v[144:147], v[208:211], v[96:99], 0\n\t" \
  "ds_read_b64_tr_b16 v[214:215], %[a1] offset:10240\n\t" \
  "v_cvt_pk_f16_f32 v228, |v204|, |v205| clamp\n\t" \
  "v_cvt_pk_f16_f32 v230, |v206|, |v207| clamp\n\t" \
  "v_rcp_f16_sdwa v234, v234 dst_sel:DWORD dst_unused:UNUSED_PAD src0_sel:WORD_1\n\t" \
  "v_mfma_f32_16x16x32_bf16 v[152:155], v[208:211], v[108:111], 0\n\t" \
  "v_and_b32_sdwa v225, %[mk], v224 dst_sel:DWORD dst_unused:UNUSED_PAD src0_sel:DWORD src1_sel:WORD_1\n\t" \
  "v_and_b32 v224, %[mk], v224\n\t" \
  "v_mfma_f32_16x16x32_bf16 v[160:163], v[208:211], v[120:123], 0\n\t" \
  "ds_read_b32 v225, v225 offset:0\n\t" \
  "v_and_b32_sdwa v227, %[mk], v226 dst_sel:DWORD dst_unused:UNUSED_PAD src0_sel:DWORD src1_sel:WORD_1\n\t" \
  "v_mfma_f32_16x16x32_bf16 v[168:171], v[208:211], v[132:135], 0\n\t" \
  "ds_read_b64_tr_b16 v[208:209], %[a1] offset:8192\n\t" \
  "v_and_b32 v226, %[mk], v226\n\t" \
  "v_and_b32_sdwa v229, %[mk], v228 dst_sel:DWORD dst_unused:UNUSED_PAD src0_sel:DWORD src1_sel:WORD_1\n\t" \
  "s_waitcnt lgkmcnt(2)\n\t" \
  "v_mfma_f32_16x16x32_bf16 v[144:147], v[212:215], v[100:103], v[144:147]\n\t" \
  "ds_read_b64_tr_b16 v[210:211], %[a1] offset:11264\n\t" \
  "v_and_b32 v228, %[mk], v228\n\t" \
  "v_mfma_f32_16x16x32_bf16 v[152:155], v[212:215], v[112:115], v[152:155]\n\t" \
  "ds_read_b32 v224, v224 offset:0\n\t" \
  "ds_read_b32 v227, v227 offset:0\n\t" \
  "v_and_b32_sdwa v231, %[mk], v230 dst_sel:DWORD dst_unused:UNUSED_PAD src0_sel:DWORD src1_sel:WORD_1\n\t" \
  "v_mfma_f32_16x16x32_bf16 v[160:163], v[212:215], v[124:127], v[160:163]\n\t" \
  "ds_read_b32 v226, v226 offset:0\n\t" \
  "v_and_b32 v230, %[mk], v230\n\t" \
  "v_fma_f32 v181, |v181|, v217, v181 mul:4\n\t" \
  "v_mfma_f32_16x16x32_bf16 v[168:171], v[212:215], v[136:139], v[168:171]\n\t" \
  "ds_read_b64_tr_b16 v[212:213], %[a1] offset:6656\n\t" \
  "ds_read_b64_tr_b16 v[214:215], %[a1] offset:7680\n\t" \
  "v_fma_f32 v180, |v180|, v216, v180 mul:4\n\t" \
  "v_fma_f32 v183, |v183|, v219, v183 mul:4\n\t" \
  "s_waitcnt lgkmcnt(5)\n\t" \
  "v_mfma_f32_16x16x32_bf16 v[144:147], v[208:211], v[104:107], v[144:147]\n\t" \
  "ds_read_b32 v229, v229 offset:0\n\t" \
  "ds_read_b32 v228, v228 offset:0\n\t" \
  "v_fma_f32 v182, |v182|, v218, v182 mul:4\n\t" \
  "v_fma_f32 v189, |v189|, v221, v189 mul:4\n\t" \
  "v_mfma_f32_16x16x32_bf16 v[152:155], v[208:211], v[116:119], v[152:155]\n\t" \
  "ds_read_b32 v231, v231 offset:0\n\t" \
  "v_fma_f32 v188, |v188|, v220, v188 mul:4\n\t" \
  "v_fma_f32 v191, |v191|, v223, v191 mul:4\n\t" \
  "v_fma_f32 v190, |v190|, v222, v190 mul:4\n\t" \
  "v_mfma_f32_16x16x32_bf16 v[160:163], v[208:211], v[128:131], v[160:163]\n\t" \
  "ds_read_b32 v230, v230 offset:0\n\t" \
  "v_fma_mix_f32 v192, v192, v235, 0 op_sel_hi:[0,1,0]\n\t" \
  "v_fma_mix_f32 v193, v193, v234, 0 op_sel_hi:[0,1,0]\n\t" \
  "v_cvt_pk_bf16_f32 v194, v192, v193\n\t" \
  "s_setprio 0\n\t" \
  "v_mfma_f32_16x16x32_bf16 v[168:171], v[208:211], v[140:143], v[168:171]\n\t" \
  "ds_read_b64_tr_b16 v[208:209], %[a1] offset:9728\n\t" \
  "v_cvt_pk_bf16_f32 v180, v180, v181\n\t" \
  "v_cvt_pk_bf16_f32 v181, v182, v183\n\t" \
  "s_waitcnt lgkmcnt(5)\n\t" \
  "v_mfma_f32_16x16x32_bf16 v[148:151], v[212:215], v[96:99], 0\n\t" \
  "ds_read_b64_tr_b16 v[210:211], %[a1] offset:10752\n\t" \
  "v_cvt_pk_bf16_f32 v188, v188, v189\n\t" \
  "v_cvt_pk_bf16_f32 v189, v190, v191\n\t" \
  "v_cvt_pk_f16_f32 v216, |v146|, |v147| clamp\n\t" \
  "v_mfma_f32_16x16x32_bf16 v[156:159], v[212:215], v[108:111], 0\n\t" \
  "v_cvt_pk_f16_f32 v232, v144, v145\n\t" \
  "v_cvt_pk_f16_f32 v218, |v154|, |v155| clamp\n\t" \
  "v_cvt_pk_f16_f32 v220, |v152|, |v153| clamp\n\t" \
  "v_mfma_f32_16x16x32_bf16 v[164:167], v[212:215], v[120:123], 0\n\t" \
  "v_pk_mul_f16 v233, v232, v232\n\t" \
  "v_and_b32_sdwa v217, %[mk], v216 dst_sel:DWORD dst_unused:UNUSED_PAD src0_sel:DWORD src1_sel:WORD_1\n\t" \
  "v_mfma_f32_16x16x32_bf16 v[172:175], v[212:215], v[132:135], 0\n\t" \
  "ds_read_b64_tr_b16 v[212:213], %[a1] offset:8704\n\t" \
  "v_and_b32 v216, %[mk], v216\n\t" \
  "v_and_b32_sdwa v219, %[mk], v218 dst_sel:DWORD dst_unused:UNUSED_PAD src0_sel:DWORD src1_sel:WORD_1\n\t" \
  "s_waitcnt lgkmcnt(1)\n\t" \
  "v_mfma_f32_16x16x32_bf16 v[148:151], v[208:211], v[100:103], v[148:151]\n\t" \
  "ds_read_b64_tr_b16 v[214:215], %[a1] offset:11776\n\t" \
  "v_and_b32 v218, %[mk], v218\n\t" \
  "v_mfma_f32_16x16x32_bf16 v[156:159], v[208:211], v[112:115], v[156:159]\n\t" \
  "ds_read_b32 v217, v217 offset:0\n\t" \
  "v_and_b32_sdwa v221, %[mk], v220 dst_sel:DWORD dst_unused:UNUSED_PAD src0_sel:DWORD src1_sel:WORD_1\n\t" \
  "v_mfma_f32_16x16x32_bf16 v[164:167], v[208:211], v[124:127], v[164:167]\n\t" \
  "ds_read_b32 v216, v216 offset:0\n\t" \
  "v_and_b32 v220, %[mk], v220\n\t" \
  "v_pk_fma_f16 v233, v233, %[h2], %[h1]\n\t" \
  "v_mfma_f32_16x16x32_bf16 v[172:175], v[208:211], v[136:139], v[172:175]\n\t" \
  "ds_read_b32 v219, v219 offset:0\n\t" \
  "v_pk_mul_f16 v233, v232, v233\n\t" \
  "v_fma_f32 v197, |v197|, v225, v197 mul:4\n\t" \
  "s_waitcnt lgkmcnt(3)\n\t" \
  "v_mfma_f32_16x16x32_bf16 v[148:151], v[212:215], v[104:107], v[148:151]\n\t" \
  "ds_read_b32 v218, v218 offset:0\n\t" \
  "v_fma_f32 v196, |v196|, v224, v196 mul:4\n\t" \
  "v_fma_f32 v199, |v199|, v227, v199 mul:4\n\t" \
  "v_mfma_f32_16x16x32_bf16 v[156:159], v[212:215], v[116:119], v[156:159]\n\t" \
  "ds_read_b32 v221, v221 offset:0\n\t" \
  "v_fma_f32 v198, |v198|, v226, v198 mul:4\n\t" \
  "v_fma_f32 v205, |v205|, v229, v205 mul:4\n\t" \
  "v_fma_f32 v204, |v204|, v228, v204 mul:4\n\t" \
  "v_mfma_f32_16x16x32_bf16 v[164:167], v[212:215], v[128:131], v[164:167]\n\t" \
  "ds_read_b32 v220, v220 offset:0\n\t" \
  "v_fma_f32 v207, |v207|, v231, v207 mul:4\n\t" \
  "v_fma_f32 v206, |v206|, v230, v206 mul:4\n\t" \
  "v_cvt_pk_bf16_f32 v196, v196, v197\n\t" \
  "v_mfma_f32_16x16x32_bf16 v[172:175], v[212:215], v[140:143], v[172:175]\n\t" \
  "v_cvt_pk_bf16_f32 v197, v198, v199\n\t" \
  "v_cvt_pk_bf16_f32 v204, v204, v205\n\t" \
  "v_cvt_pk_bf16_f32 v205, v206, v207\n\t" \
  "s_waitcnt vmcnt(1)\n\t" \
  "s_cmp_lg_u32 %[rnd], 1\n\t" \
  "s_cbranch_scc1 .Lcnx_c23\n\t" \
  "s_waitcnt vmcnt(0)\n\t" \
  ".Lcnx_c23:\n\t" \
  "s_barrier\n\t" \
  "ds_read_b64_tr_b16 v[208:209], %[a2] offset:0\n\t" \
  "ds_read_b64_tr_b16 v[210:211], %[a2] offset:1536\n\t" \
  "ds_read_b64_tr_b16 v[212:213], %[a2] offset:8\n\t" \
  "ds_read_b64_tr_b16 v[214:215], %[a2] offset:1544\n\t" \
  "s_setprio 2\n\t" \
  "v_exp_f16 v232, v233\n\t" \
  "v_cvt_pk_f16_f32 v224, |v162|, |v163| clamp\n\t" \
  "v_mfma_f32_16x16x32_bf16 v[24:27], v[242:245], v[236:239], v[24:27]\n\t" \
  "v_cvt_pk_f16_f32 v234, v160, v161\n\t" \
  "v_cvt_pk_f16_f32 v226, |v170|, |v171| clamp\n\t" \
  "v_mfma_f32_16x16x32_bf16 v[28:31], v[240:243], v[236:239], v[28:31]\n\t" \
  "s_cmp_lg_u32 %[grp], 0\n\t" \
  "s_cbranch_scc1 .Lcnx_c21\n\t" \
  "s_add_u32 m0, %[wl], %[ws]\n\t" \
  "s_nop 0\n\t" \
  "buffer_load_dwordx4 %[wv], %[wrs], %[so0] offen lds\n\t" \
  "buffer_load_dwordx4 %[wv], %[wrs], %[so1] offen offset:1024 lds\n\t" \
  "buffer_load_dwordx4 %[wv], %[wrs], %[so2] offen offset:2048 lds\n\t" \
  "s_add_u32 %[so0], %[so0], %[wc]\n\t" \
  "s_add_u32 %[so1], %[so1], %[wc]\n\t" \
  "s_add_u32 %[so2], %[so2], %[wc]\n\t" \
  ".Lcnx_c21:\n\t" \
  "buffer_load_dwordx4 v[236:239], %[vo], %[xrs], %[oc0] offen offset:256\n\t" \
  "s_cmp_lg_u32 %[rnd], 0\n\t" \
  "s_cbranch_scc1 .Lcnx_c22\n\t" \
  "s_add_u32 m0, %[xl], 32800\n\t" \
  "s_add_u32 %[xo], %[xo], %[rs]\n\t" \
  "buffer_load_dwordx4 %[xv], %[xrs], %[xo] offen lds\n\t" \
  ".Lcnx_c22:\n\t" \
  "v_cvt_pk_f16_f32 v228, |v168|, |v169| clamp\n\t" \
  "v_exp_f16_sdwa v232, v233 dst_sel:WORD_1 dst_unused:UNUSED_PRESERVE src0_sel:WORD_1\n\t" \
  "v_pk_mul_f16 v235, v234, v234\n\t" \
  "s_waitcnt lgkmcnt(2)\n\t" \
  "v_mfma_f32_16x16x32_bf16 v[0:3], v[208:211], v[178:181], v[0:3]\n\t" \
  "v_and_b32_sdwa v225, %[mk], v224 dst_sel:DWORD dst_unused:UNUSED_PAD src0_sel:DWORD src1_sel:WORD_1\n\t" \
  "v_and_b32 v224, %[mk], v224\n\t" \
  "v_mfma_f32_16x16x32_bf16 v[24:27], v[208:211], v[186:189], v[24:27]\n\t" \
  "ds_read_b32 v225, v225 offset:0\n\t" \
  "v_and_b32_sdwa v227, %[mk], v226 dst_sel:DWORD dst_unused:UNUSED_PAD src0_sel:DWORD src1_sel:WORD_1\n\t" \
  "v_mfma_f32_16x16x32_bf16 v[48:51], v[208:211], v[194:197], v[48:51]\n\t" \
  "ds_read_b32 v224, v224 offset:0\n\t" \
  "v_and_b32 v226, %[mk], v226\n\t" \
  "v_and_b32_sdwa v229, %[mk], v228 dst_sel:DWORD dst_unused:UNUSED_PAD src0_sel:DWORD src1_sel:WORD_1\n\t" \
  "v_mfma_f32_16x16x32_bf16 v[72:75], v[208:211], v[202:205], v[72:75]\n\t" \
  "ds_read_b64_tr_b16 v[208:209], %[a2] offset:64\n\t" \
  "v_and_b32 v228, %[mk], v228\n\t" \
  "s_waitcnt lgkmcnt(3)\n\t" \
  "v_mfma_f32_16x16x32_bf16 v[4:7], v[212:215], v[178:181], v[4:7]\n\t" \
  "ds_read_b64_tr_b16 v[210:211], %[a2] offset:1600\n\t" \
  "ds_read_b32 v227, v227 offset:0\n\t" \
  "v_pk_fma_f16 v235, v235, %[h2], %[h1]\n\t" \
  "v_mfma_f32_16x16x32_bf16 v[28:31], v[212:215], v[186:189], v[28:31]\n\t" \
  "ds_read_b32 v226, v226 offset:0\n\t" \
  "v_pk_mul_f16 v235, v234, v235\n\t" \
  "v_fma_f32 v147, |v147|, v217, v147 mul:4\n\t" \
  "v_fma_f32 v146, |v146|, v216, v146 mul:4\n\t" \
  "v_mfma_f32_16x16x32_bf16 v[52:55], v[212:215], v[194:197], v[52:55]\n\t" \
  "ds_read_b32 v229, v229 offset:0\n\t" \
  "v_fma_f32 v155, |v155|, v219, v155 mul:4\n\t" \
  "v_fma_f32 v154, |v154|, v218, v154 mul:4\n\t" \
  "v_fma_f32 v153, |v153|, v221, v153 mul:4\n\t" \
  "v_mfma_f32_16x16x32_bf16 v[76:79], v[212:215], v[202:205], v[76:79]\n\t" \
  "ds_read_b64_tr_b16 v[212:213], %[a2] offset:72\n\t" \
  "v_fma_f32 v152, |v152|, v220, v152 mul:4\n\t" \
  "v_pk_fma_f16 v232, v232, %[hh], %[hh]\n\t" \
  "s_waitcnt lgkmcnt(4)\n\t" \
  "v_mfma_f32_16x16x32_bf16 v[8:11], v[208:211], v[178:181], v[8:11]\n\t" \
  "ds_read_b64_tr_b16 v[214:215], %[a2] offset:1608\n\t" \
  "v_cvt_pk_bf16_f32 v147, v146, v147\n\t" \
  "v_cvt_pk_bf16_f32 v155, v154, v155\n\t" \
  "v_cvt_pk_bf16_f32 v154, v152, v153\n\t" \
  "v_mfma_f32_16x16x32_bf16 v[32:35], v[208:211], v[186:189], v[32:35]\n\t" \
  "ds_read_b32 v228, v228 offset:0\n\t" \
  "v_rcp_f16 v233, v232\n\t" \
  "v_exp_f16 v234, v235\n\t" \
  "v_cvt_pk_f16_f32 v216, |v148|, |v149| clamp\n\t" \
  "v_mfma_f32_16x16x32_bf16 v[56:59], v[208:211], v[194:197], v[56:59]\n\t" \
  "s_setprio 2\n\t" \
  "v_cvt_pk_f16_f32 v218, |v150|, |v151| clamp\n\t" \
  "v_cvt_pk_f16_f32 v220, |v156|, |v157| clamp\n\t" \
  "v_mfma_f32_16x16x32_bf16 v[80:83], v[208:211], v[202:205], v[80:83]\n\t" \
  "ds_read_b64_tr_b16 v[208:209], %[a2] offset:128\n\t" \
  "v_cvt_pk_f16_f32 v222, |v158|, |v159| clamp\n\t" \
  "v_rcp_f16_sdwa v232, v232 dst_sel:DWORD dst_unused:UNUSED_PAD src0_sel:WORD_1\n\t" \
  "v_exp_f16_sdwa v234, v235 dst_sel:WORD_1 dst_unused:UNUSED_PRESERVE src0_sel:WORD_1\n\t" \
  "s_waitcnt lgkmcnt(2)\n\t" \
  "v_mfma_f32_16x16x32_bf16 v[12:15], v[212:215], v[178:181], v[12:15]\n\t" \
  "ds_read_b64_tr_b16 v[210:211], %[a2] offset:1664\n\t" \
  "v_and_b32_sdwa v217, %[mk], v216 dst_sel:DWORD dst_unused:UNUSED_PAD src0_sel:DWORD src1_sel:WORD_1\n\t" \
  "v_and_b32 v216, %[mk], v216\n\t" \
  "v_mfma_f32_16x16x32_bf16 v[36:39], v[212:215], v[186:189], v[36:39]\n\t" \
  "ds_read_b32 v217, v217 offset:0\n\t" \
  "v_and_b32_sdwa v219, %[mk], v218 dst_sel:DWORD dst_unused:UNUSED_PAD src0_sel:DWORD src1_sel:WORD_1\n\t" \
  "v_mfma_f32_16x16x32_bf16 v[60:63], v[212:215], v[194:197], v[60:63]\n\t" \
  "ds_read_b32 v216, v216 offset:0\n\t" \
  "v_and_b32 v218, %[mk], v218\n\t" \
  "v_and_b32_sdwa v221, %[mk], v220 dst_sel:DWORD dst_unused:UNUSED_PAD src0_sel:DWORD src1_sel:WORD_1\n\t" \
  "v_mfma_f32_16x16x32_bf16 v[84:87], v[212:215], v[202:205], v[84:87]\n\t" \
  "ds_read_b64_tr_b16 v[212:213], %[a2] offset:136\n\t" \
  "v_and_b32 v220, %[mk], v220\n\t" \
  "s_waitcnt lgkmcnt(3)\n\t" \
  "v_mfma_f32_16x16x32_bf16 v[16:19], v[208:211], v[178:181], v[16:19]\n\t" \
  "ds_read_b64_tr_b16 v[214:215], %[a2] offset:1672\n\t" \
  "ds_read_b32 v219, v219 offset:0\n\t" \
  "v_and_b32_sdwa v223, %[mk], v222 dst_sel:DWORD dst_unused:UNUSED_PAD src0_sel:DWORD src1_sel:WORD_1\n\t" \
  "v_mfma_f32_16x16x32_bf16 v[40:43], v[208:211], v[186:189], v[40:43]\n\t" \
  "ds_read_b32 v218, v218 offset:0\n\t" \
  "v_and_b32 v222, %[mk], v222\n\t" \
  "v_fma_f32 v163, |v163|, v225, v163 mul:4\n\t" \
  "v_mfma_f32_16x16x32_bf16 v[64:67], v[208:211], v[194:197], v[64:67]\n\t" \
  "ds_read_b32 v221, v221 offset:0\n\t" \
  "v_fma_f32 v162, |v162|, v224, v162 mul:4\n\t" \
  "v_fma_f32 v171, |v171|, v227, v171 mul:4\n\t" \
  "v_mfma_f32_16x16x32_bf16 v[88:91], v[208:211], v[202:205], v[88:91]\n\t" \
  "ds_read_b64_tr_b16 v[208:209], %[a1] offset:0\n\t" \
  "ds_read_b64_tr_b16 v[210:211], %[a1] offset:1024\n\t" \
  "v_fma_f32 v170, |v170|, v226, v170 mul:4\n\t" \
  "v_fma_f32 v169, |v169|, v229, v169 mul:4\n\t" \
  "s_waitcnt lgkmcnt(5)\n\t" \
  "v_mfma_f32_16x16x32_bf16 v[20:23], v[212:215], v[178:181], v[20:23]\n\t" \
  "ds_read_b32 v220, v220 offset:0\n\t" \
  "v_fma_f32 v168, |v168|, v228, v168 mul:4\n\t" \
  "v_fma_mix_f32 v144, v144, v233, 0 op_sel_hi:[0,1,0]\n\t" \
  "v_fma_mix_f32 v145, v145, v232, 0 op_sel_hi:[0,1,0]\n\t" \
  "v_mfma_f32_16x16x32_bf16 v[44:47], v[212:215], v[186:189], v[44:47]\n\t" \
  "ds_read_b32 v223, v223 offset:0\n\t" \
  "v_pk_fma_f16 v234, v234, %[hh], %[hh]\n\t" \
  "v_cvt_pk_bf16_f32 v146, v144, v145\n\t" \
  "v_cvt_pk_bf16_f32 v163, v162, v163\n\t" \
  "v_mfma_f32_16x16x32_bf16 v[68:71], v[212:215], v[194:197], v[68:71]\n\t" \
  "ds_read_b32 v222, v222 offset:0\n\t" \
  "v_cvt_pk_bf16_f32 v171, v170, v171\n\t" \
  "v_cvt_pk_bf16_f32 v170, v168, v169\n\t" \
  "s_setprio 1\n\t" \
  "v_rcp_f16 v235, v234\n\t" \
  "v_mfma_f32_16x16x32_bf16 v[92:95], v[212:215], v[202:205], v[92:95]\n\t" \
  "ds_read_b64_tr_b16 v[212:213], %[a1] offset:3072\n\t" \
  "v_cvt_pk_f16_f32 v224, |v164|, |v165| clamp\n\t" \
  "v_cvt_pk_f16_f32 v226, |v166|, |v167| clamp\n\t" \
  "s_waitcnt lgkmcnt(4)\n\t" \
  "v_mfma_f32_16x16x32_bf16 v[176:179], v[208:211], v[96:99], 0\n\t" \
  "ds_read_b64_tr_b16 v[214:215], %[a1] offset:4096\n\t" \
  "v_cvt_pk_f16_f32 v228, |v172|, |v173| clamp\n\t" \
  "v_cvt_pk_f16_f32 v230, |v174|, |v175| clamp\n\t" \
  "v_rcp_f16_sdwa v234, v234 dst_sel:DWORD dst_unused:UNUSED_PAD src0_sel:WORD_1\n\t" \
  "v_mfma_f32_16x16x32_bf16 v[184:187], v[208:211], v[108:111], 0\n\t" \
  "v_and_b32_sdwa v225, %[mk], v224 dst_sel:DWORD dst_unused:UNUSED_PAD src0_sel:DWORD src1_sel:WORD_1\n\t" \
  "v_and_b32 v224, %[mk], v224\n\t" \
  "v_mfma_f32_16x16x32_bf16 v[192:195], v[208:211], v[120:123], 0\n\t" \
  "ds_read_b32 v225, v225 offset:0\n\t" \
  "v_and_b32_sdwa v227, %[mk], v226 dst_sel:DWORD dst_unused:UNUSED_PAD src0_sel:DWORD src1_sel:WORD_1\n\t" \
  "v_mfma_f32_16x16x32_bf16 v[200:203], v[208:211], v[132:135], 0\n\t" \
  "ds_read_b64_tr_b16 v[208:209], %[a1] offset:2048\n\t" \
  "v_and_b32 v226, %[mk], v226\n\t" \
  "v_and_b32_sdwa v229, %[mk], v228 dst_sel:DWORD dst_unused:UNUSED_PAD src0_sel:DWORD src1_sel:WORD_1\n\t" \
  "s_waitcnt lgkmcnt(2)\n\t" \
  "v_mfma_f32_16x16x32_bf16 v[176:179], v[212:215], v[100:103], v[176:179]\n\t" \
  "ds_read_b64_tr_b16 v[210:211], %[a1] offset:5120\n\t" \
  "v_and_b32 v228, %[mk], v228\n\t" \
  "v_mfma_f32_16x16x32_bf16 v[184:187], v[212:215], v[112:115], v[184:187]\n\t" \
  "ds_read_b32 v224, v224 offset:0\n\t" \
  "ds_read_b32 v227, v227 offset:0\n\t" \
  "v_and_b32_sdwa v231, %[mk], v230 dst_sel:DWORD dst_unused:UNUSED_PAD src0_sel:DWORD src1_sel:WORD_1\n\t" \
  "v_mfma_f32_16x16x32_bf16 v[192:195], v[212:215], v[124:127], v[192:195]\n\t" \
  "ds_read_b32 v226, v226 offset:0\n\t" \
  "v_and_b32 v230, %[mk], v230\n\t" \
  "v_fma_f32 v149, |v149|, v217, v149 mul:4\n\t" \
  "v_mfma_f32_16x16x32_bf16 v[200:203], v[212:215], v[136:139], v[200:203]\n\t" \
  "ds_read_b64_tr_b16 v[212:213], %[a1] offset:512\n\t" \
  "ds_read_b64_tr_b16 v[214:215], %[a1] offset:1536\n\t" \
  "v_fma_f32 v148, |v148|, v216, v148 mul:4\n\t" \
  "v_fma_f32 v151, |v151|, v219, v151 mul:4\n\t" \
  "s_waitcnt lgkmcnt(5)\n\t" \
  "v_mfma_f32_16x16x32_bf16 v[176:179], v[208:211], v[104:107], v[176:179]\n\t" \
  "ds_read_b32 v229, v229 offset:0\n\t" \
  "ds_read_b32 v228, v228 offset:0\n\t" \
  "v_fma_f32 v150, |v150|, v218, v150 mul:4\n\t" \
  "v_fma_f32 v157, |v157|, v221, v157 mul:4\n\t" \
  "v_mfma_f32_16x16x32_bf16 v[184:187], v[208:211], v[116:119], v[184:187]\n\t" \
  "ds_read_b32 v231, v231 offset:0\n\t" \
  "v_fma_f32 v156, |v156|, v220, v156 mul:4\n\t" \
  "v_fma_f32 v159, |v159|, v223, v159 mul:4\n\t" \
  "v_fma_f32 v158, |v158|, v222, v158 mul:4\n\t" \
  "v_mfma_f32_16x16x32_bf16 v[192:195], v[208:211], v[128:131], v[192:195]\n\t" \
  "ds_read_b32 v230, v230 offset:0\n\t" \
  "v_fma_mix_f32 v160, v160, v235, 0 op_sel_hi:[0,1,0]\n\t" \
  "v_fma_mix_f32 v161, v161, v234, 0 op_sel_hi:[0,1,0]\n\t" \
  "v_cvt_pk_bf16_f32 v162, v160, v161\n\t" \
  "s_setprio 0\n\t" \
  "v_mfma_f32_16x16x32_bf16 v[200:203], v[208:211], v[140:143], v[200:203]\n\t" \
  "ds_read_b64_tr_b16 v[208:209], %[a1] offset:3584\n\t" \
  "v_cvt_pk_bf16_f32 v148, v148, v149\n\t" \
  "v_cvt_pk_bf16_f32 v149, v150, v151\n\t" \
  "s_waitcnt lgkmcnt(5)\n\t" \
  "v_mfma_f32_16x16x32_bf16 v[180:183], v[212:215], v[96:99], 0\n\t" \
  "ds_read_b64_tr_b16 v[210:211], %[a1] offset:4608\n\t" \
  "v_cvt_pk_bf16_f32 v156, v156, v157\n\t" \
  "v_cvt_pk_bf16_f32 v157, v158, v159\n\t" \
  "v_cvt_pk_f16_f32 v216, |v178|, |v179| clamp\n\t" \
  "v_mfma_f32_16x16x32_bf16 v[188:191], v[212:215], v[108:111], 0\n\t" \
  "v_cvt_pk_f16_f32 v232, v176, v177\n\t" \
  "v_cvt_pk_f16_f32 v218, |v186|, |v187| clamp\n\t" \
  "v_cvt_pk_f16_f32 v220, |v184|, |v185| clamp\n\t" \
  "v_mfma_f32_16x16x32_bf16 v[196:199], v[212:215], v[120:123], 0\n\t" \
  "v_pk_mul_f16 v233, v232, v232\n\t" \
  "v_and_b32_sdwa v217, %[mk], v216 dst_sel:DWORD dst_unused:UNUSED_PAD src0_sel:DWORD src1_sel:WORD_1\n\t" \
  "v_mfma_f32_16x16x32_bf16 v[204:207], v[212:215], v[132:135], 0\n\t" \
  "ds_read_b64_tr_b16 v[212:213], %[a1] offset:2560\n\t" \
  "v_and_b32 v216, %[mk], v216\n\t" \
  "v_and_b32_sdwa v219, %[mk], v218 dst_sel:DWORD dst_unused:UNUSED_PAD src0_sel:DWORD src1_sel:WORD_1\n\t" \
  "s_waitcnt lgkmcnt(1)\n\t" \
  "v_mfma_f32_16x16x32_bf16 v[180:183], v[208:211], v[100:103], v[180:183]\n\t" \
  "ds_read_b64_tr_b16 v[214:215], %[a1] offset:5632\n\t" \
  "v_and_b32 v218, %[mk], v218\n\t" \
  "v_mfma_f32_16x16x32_bf16 v[188:191], v[208:211], v[112:115], v[188:191]\n\t" \
  "ds_read_b32 v217, v217 offset:0\n\t" \
  "v_and_b32_sdwa v221, %[mk], v220 dst_sel:DWORD dst_unused:UNUSED_PAD src0_sel:DWORD src1_sel:WORD_1\n\t" \
  "v_mfma_f32_16x16x32_bf16 v[196:199], v[208:211], v[124:127], v[196:199]\n\t" \
  "ds_read_b32 v216, v216 offset:0\n\t" \
  "v_and_b32 v220, %[mk], v220\n\t" \
  "v_pk_fma_f16 v233, v233, %[h2], %[h1]\n\t" \
  "v_mfma_f32_16x16x32_bf16 v[204:207], v[208:211], v[136:139], v[204:207]\n\t" \
  "ds_read_b32 v219, v219 offset:0\n\t" \
  "v_pk_mul_f16 v233, v232, v233\n\t" \
  "v_fma_f32 v165, |v165|, v225, v165 mul:4\n\t" \
  "s_waitcnt lgkmcnt(3)\n\t" \
  "v_mfma_f32_16x16x32_bf16 v[180:183], v[212:215], v[104:107], v[180:183]\n\t" \
  "ds_read_b32 v218, v218 offset:0\n\t" \
  "v_fma_f32 v164, |v164|, v224, v164 mul:4\n\t" \
  "v_fma_f32 v167, |v167|, v227, v167 mul:4\n\t" \
  "v_mfma_f32_16x16x32_bf16 v[188:191], v[212:215], v[116:119], v[188:191]\n\t" \
  "ds_read_b32 v221, v221 offset:0\n\t" \
  "v_fma_f32 v166, |v166|, v226, v166 mul:4\n\t" \
  "v_fma_f32 v173, |v173|, v229, v173 mul:4\n\t" \
  "v_fma_f32 v172, |v172|, v228, v172 mul:4\n\t" \
  "v_mfma_f32_16x16x32_bf16 v[196:199], v[212:215], v[128:131], v[196:199]\n\t" \
  "ds_read_b32 v220, v220 offset:0\n\t" \
  "v_fma_f32 v175, |v175|, v231, v175 mul:4\n\t" \
  "v_fma_f32 v174, |v174|, v230, v174 mul:4\n\t" \
  "v_cvt_pk_bf16_f32 v164, v164, v165\n\t" \
  "v_mfma_f32_16x16x32_bf16 v[204:207], v[212:215], v[140:143], v[204:207]\n\t" \
  "v_cvt_pk_bf16_f32 v165, v166, v167\n\t" \
  "v_cvt_pk_bf16_f32 v172, v172, v173\n\t" \
  "v_cvt_pk_bf16_f32 v173, v174, v175\n\t" \
  "s_waitcnt vmcnt(1)\n\t" \
  "s_cmp_lg_u32 %[rnd], 1\n\t" \
  "s_cbranch_scc1 .Lcnx_c26\n\t" \
  "s_waitcnt vmcnt(0)\n\t" \
  ".Lcnx_c26:\n\t" \
  "s_barrier\n\t" \
  "ds_read_b64_tr_b16 v[208:209], %[a2] offset:6160\n\t" \
  "ds_read_b64_tr_b16 v[210:211], %[a2] offset:7696\n\t" \
  "ds_read_b64_tr_b16 v[212:213], %[a2] offset:6168\n\t" \
  "ds_read_b64_tr_b16 v[214:215], %[a2] offset:7704\n\t" \
  "s_setprio 2\n\t" \
  "v_exp_f16 v232, v233\n\t" \
  "v_cvt_pk_f16_f32 v224, |v194|, |v195| clamp\n\t" \
  "v_mfma_f32_16x16x32_bf16 v[32:35], v[242:245], v[236:239], v[32:35]\n\t" \
  "v_cvt_pk_f16_f32 v234, v192, v193\n\t" \
  "v_cvt_pk_f16_f32 v226, |v202|, |v203| clamp\n\t" \
  "v_mfma_f32_16x16x32_bf16 v[36:39], v[240:243], v[236:239], v[36:39]\n\t" \
  "s_cmp_lg_u32 %[grp], 0\n\t" \
  "s_cbranch_scc1 .Lcnx_c24\n\t" \
  "s_add_u32 m0, %[wl], 0\n\t" \
  "s_nop 0\n\t" \
  "buffer_load_dwordx4 %[wv], %[wrs], %[so0] offen lds\n\t" \
  "buffer_load_dwordx4 %[wv], %[wrs], %[so1] offen offset:1024 lds\n\t" \
  "buffer_load_dwordx4 %[wv], %[wrs], %[so2] offen offset:2048 lds\n\t" \
  "s_add_u32 %[so0], %[so0], %[wc]\n\t" \
  "s_add_u32 %[so1], %[so1], %[wc]\n\t" \
  "s_add_u32 %[so2], %[so2], %[wc]\n\t" \
  ".Lcnx_c24:\n\t" \
  "buffer_load_dwordx4 v[236:239], %[vo], %[xrs], %[oc0] offen offset:320\n\t" \
  "s_cmp_lg_u32 %[rnd], 0\n\t" \
  "s_cbranch_scc1 .Lcnx_c25\n\t" \
  "s_add_u32 m0, %[xl], 41000\n\t" \
  "s_add_u32 %[xo], %[xo], %[rs]\n\t" \
  "buffer_load_dwordx4 %[xv], %[xrs], %[xo] offen lds\n\t" \
  ".Lcnx_c25:\n\t" \
  "v_cvt_pk_f16_f32 v228, |v200|, |v201| clamp\n\t" \
  "v_exp_f16_sdwa v232, v233 dst_sel:WORD_1 dst_unused:UNUSED_PRESERVE src0_sel:WORD_1\n\t" \
  "v_pk_mul_f16 v235, v234, v234\n\t" \
  "s_waitcnt lgkmcnt(2)\n\t" \
  "v_mfma_f32_16x16x32_bf16 v[0:3], v[208:211], v[146:149], v[0:3]\n\t" \
  "v_and_b32_sdwa v225, %[mk], v224 dst_sel:DWORD dst_unused:UNUSED_PAD src0_sel:DWORD src1_sel:WORD_1\n\t" \
  "v_and_b32 v224, %[mk], v224\n\t" \
  "v_mfma_f32_16x16x32_bf16 v[24:27], v[208:211], v[154:157], v[24:27]\n\t" \
  "ds_read_b32 v225, v225 offset:0\n\t" \
  "v_and_b32_sdwa v227, %[mk], v226 dst_sel:DWORD dst_unused:UNUSED_PAD src0_sel:DWORD src1_sel:WORD_1\n\t" \
  "v_mfma_f32_16x16x32_bf16 v[48:51], v[208:211], v[162:165], v[48:51]\n\t" \
  "ds_read_b32 v224, v224 offset:0\n\t" \
  "v_and_b32 v226, %[mk], v226\n\t" \
  "v_and_b32_sdwa v229, %[mk], v228 dst_sel:DWORD dst_unused:UNUSED_PAD src0_sel:DWORD src1_sel:WORD_1\n\t" \
  "v_mfma_f32_16x16x32_bf16 v[72:75], v[208:211], v[170:173], v[72:75]\n\t" \
  "ds_read_b64_tr_b16 v[208:209], %[a2] offset:6224\n\t" \
  "v_and_b32 v228, %[mk], v228\n\t" \
  "s_waitcnt lgkmcnt(3)\n\t" \
  "v_mfma_f32_16x16x32_bf16 v[4:7], v[212:215], v[146:149], v[4:7]\n\t" \
  "ds_read_b64_tr_b16 v[210:211], %[a2] offset:7760\n\t" \
  "ds_read_b32 v227, v227 offset:0\n\t" \
  "v_pk_fma_f16 v235, v235, %[h2], %[h1]\n\t" \
  "v_mfma_f32_16x16x32_bf16 v[28:31], v[212:215], v[154:157], v[28:31]\n\t" \
  "ds_read_b32 v226, v226 offset:0\n\t" \
  "v_pk_mul_f16 v235, v234, v235\n\t" \
  "v_fma_f32 v179, |v179|, v217, v179 mul:4\n\t" \
  "v_fma_f32 v178, |v178|, v216, v178 mul:4\n\t" \
  "v_mfma_f32_16x16x32_bf16 v[52:55], v[212:215], v[162:165], v[52:55]\n\t" \
  "ds_read_b32 v229, v229 offset:0\n\t" \
  "v_fma_f32 v187, |v187|, v219, v187 mul:4\n\t" \
  "v_fma_f32 v186, |v186|, v218, v186 mul:4\n\t" \
  "v_fma_f32 v185, |v185|, v221, v185 mul:4\n\t" \
  "v_mfma_f32_16x16x32_bf16 v[76:79], v[212:215], v[170:173], v[76:79]\n\t" \
  "ds_read_b64_tr_b16 v[212:213], %[a2] offset:6232\n\t" \
  "v_fma_f32 v184, |v184|, v220, v184 mul:4\n\t" \
  "v_pk_fma_f16 v232, v232, %[hh], %[hh]\n\t" \
  "s_waitcnt lgkmcnt(4)\n\t" \
  "v_mfma_f32_16x16x32_bf16 v[8:11], v[208:211], v[146:149], v[8:11]\n\t" \
  "ds_read_b64_tr_b16 v[214:215], %[a2] offset:7768\n\t" \
  "v_cvt_pk_bf16_f32 v179, v178, v179\n\t" \
  "v_cvt_pk_bf16_f32 v187, v186, v187\n\t" \
  "v_cvt_pk_bf16_f32 v186, v184, v185\n\t" \
  "v_mfma_f32_16x16x32_bf16 v[32:35], v[208:211], v[154:157], v[32:35]\n\t" \
  "ds_read_b32 v228, v228 offset:0\n\t" \
  "v_rcp_f16 v233, v232\n\t" \
  "v_exp_f16 v234, v235\n\t" \
  "v_cvt_pk_f16_f32 v216, |v180|, |v181| clamp\n\t" \
  "v_mfma_f32_16x16x32_bf16 v[56:59], v[208:211], v[162:165], v[56:59]\n\t" \
  "s_setprio 2\n\t" \
  "v_cvt_pk_f16_f32 v218, |v182|, |v183| clamp\n\t" \
  "v_cvt_pk_f16_f32 v220, |v188|, |v189| clamp\n\t" \
  "v_mfma_f32_16x16x32_bf16 v[80:83], v[208:211], v[170:173], v[80:83]\n\t" \
  "ds_read_b64_tr_b16 v[208:209], %[a2] offset:6288\n\t" \
  "v_cvt_pk_f16_f32 v222, |v190|, |v191| clamp\n\t" \
  "v_rcp_f16_sdwa v232, v232 dst_sel:DWORD dst_unused:UNUSED_PAD src0_sel:WORD_1\n\t" \
  "v_exp_f16_sdwa v234, v235 dst_sel:WORD_1 dst_unused:UNUSED_PRESERVE src0_sel:WORD_1\n\t" \
  "s_waitcnt lgkmcnt(2)\n\t" \
  "v_mfma_f32_16x16x32_bf16 v[12:15], v[212:215], v[146:149], v[12:15]\n\t" \
  "ds_read_b64_tr_b16 v[210:211], %[a2] offset:7824\n\t" \
  "v_and_b32_sdwa v217, %[mk], v216 dst_sel:DWORD dst_unused:UNUSED_PAD src0_sel:DWORD src1_sel:WORD_1\n\t" \
  "v_and_b32 v216, %[mk], v216\n\t" \
  "v_mfma_f32_16x16x32_bf16 v[36:39], v[212:215], v[154:157], v[36:39]\n\t" \
  "ds_read_b32 v217, v217 offset:0\n\t" \
  "v_and_b32_sdwa v219, %[mk], v218 dst_sel:DWORD dst_unused:UNUSED_PAD src0_sel:DWORD src1_sel:WORD_1\n\t" \
  "v_mfma_f32_16x16x32_bf16 v[60:63], v[212:215], v[162:165], v[60:63]\n\t" \
  "ds_read_b32 v216, v216 offset:0\n\t" \
  "v_and_b32 v218, %[mk], v218\n\t" \
  "v_and_b32_sdwa v221, %[mk], v220 dst_sel:DWORD dst_unused:UNUSED_PAD src0_sel:DWORD src1_sel:WORD_1\n\t" \
  "v_mfma_f32_16x16x32_bf16 v[84:87], v[212:215], v[170:173], v[84:87]\n\t" \
  "ds_read_b64_tr_b16 v[212:213], %[a2] offset:6296\n\t" \
  "v_and_b32 v220, %[mk], v220\n\t" \
  "s_waitcnt lgkmcnt(3)\n\t" \
  "v_mfma_f32_16x16x32_bf16 v[16:19], v[208:211], v[146:149], v[16:19]\n\t" \
  "ds_read_b64_tr_b16 v[214:215], %[a2] offset:7832\n\t" \
  "ds_read_b32 v219, v219 offset:0\n\t" \
  "v_and_b32_sdwa v223, %[mk], v222 dst_sel:DWORD dst_unused:UNUSED_PAD src0_sel:DWORD src1_sel:WORD_1\n\t" \
  "v_mfma_f32_16x16x32_bf16 v[40:43], v[208:211], v[154:157], v[40:43]\n\t" \
  "ds_read_b32 v218, v218 offset:0\n\t" \
  "v_and_b32 v222, %[mk], v222\n\t" \
  "v_fma_f32 v195, |v195|, v225, v195 mul:4\n\t" \
  "v_mfma_f32_16x16x32_bf16 v[64:67], v[208:211], v[162:165], v[64:67]\n\t" \
  "ds_read_b32 v221, v221 offset:0\n\t" \
  "v_fma_f32 v194, |v194|, v224, v194 mul:4\n\t" \
  "v_fma_f32 v203, |v203|, v227, v203 mul:4\n\t" \
  "v_mfma_f32_16x16x32_bf16 v[88:91], v[208:211], v[170:173], v[88:91]\n\t" \
  "ds_read_b64_tr_b16 v[208:209], %[a1] offset:6144\n\t" \
  "ds_read_b64_tr_b16 v[210:211], %[a1] offset:7168\n\t" \
  "v_fma_f32 v202, |v202|, v226, v202 mul:4\n\t" \
  "v_fma_f32 v201, |v201|, v229, v201 mul:4\n\t" \
  "s_waitcnt lgkmcnt(5)\n\t" \
  "v_mfma_f32_16x16x32_bf16 v[20:23], v[212:215], v[146:149], v[20:23]\n\t" \
  "ds_read_b32 v220, v220 offset:0\n\t" \
  "v_fma_f32 v200, |v200|, v228, v200 mul:4\n\t" \
  "v_fma_mix_f32 v176, v176, v233, 0 op_sel_hi:[0,1,0]\n\t" \
  "v_fma_mix_f32 v177, v177, v232, 0 op_sel_hi:[0,1,0]\n\t" \
  "v_mfma_f32_16x16x32_bf16 v[44:47], v[212:215], v[154:157], v[44:47]\n\t" \
  "ds_read_b32 v223, v223 offset:0\n\t" \
  "v_pk_fma_f16 v234, v234, %[hh], %[hh]\n\t" \
  "v_cvt_pk_bf16_f32 v178, v176, v177\n\t" \
  "v_cvt_pk_bf16_f32 v195, v194, v195\n\t" \
  "v_mfma_f32_16x16x32_bf16 v[68:71], v[212:215], v[162:165], v[68:71]\n\t" \
  "ds_read_b32 v222, v222 offset:0\n\t" \
  "v_cvt_pk_bf16_f32 v203, v202, v203\n\t" \
  "v_cvt_pk_bf16_f32 v202, v200, v201\n\t" \
  "s_setprio 1\n\t" \
  "v_rcp_f16 v235, v234\n\t" \
  "v_mfma_f32_16x16x32_bf16 v[92:95], v[212:215], v[170:173], v[92:95]\n\t" \
  "ds_read_b64_tr_b16 v[212:213], %[a1] offset:9216\n\t" \
  "v_cvt_pk_f16_f32 v224, |v196|, |v197| clamp\n\t" \
  "v_cvt_pk_f16_f32 v226, |v198|, |v199| clamp\n\t" \
  "s_waitcnt lgkmcnt(4)\n\t" \
  "v_mfma_f32_16x16x32_bf16 v[144:147], v[208:211], v[96:99], 0\n\t" \
  "ds_read_b64_tr_b16 v[214:215], %[a1] offset:10240\n\t" \
  "v_cvt_pk_f16_f32 v228, |v204|, |v205| clamp\n\t" \
  "v_cvt_pk_f16_f32 v230, |v206|, |v207| clamp\n\t" \
  "v_rcp_f16_sdwa v234, v234 dst_sel:DWORD dst_unused:UNUSED_PAD src0_sel:WORD_1\n\t" \
  "v_mfma_f32_16x16x32_bf16 v[152:155], v[208:211], v[108:111], 0\n\t" \
  "v_and_b32_sdwa v225, %[mk], v224 dst_sel:DWORD dst_unused:UNUSED_PAD src0_sel:DWORD src1_sel:WORD_1\n\t" \
  "v_and_b32 v224, %[mk], v224\n\t" \
  "v_mfma_f32_16x16x32_bf16 v[160:163], v[208:211], v[120:123], 0\n\t" \
  "ds_read_b32 v225, v225 offset:0\n\t" \
  "v_and_b32_sdwa v227, %[mk], v226 dst_sel:DWORD dst_unused:UNUSED_PAD src0_sel:DWORD src1_sel:WORD_1\n\t" \
  "v_mfma_f32_16x16x32_bf16 v[168:171], v[208:211], v[132:135], 0\n\t" \
  "ds_read_b64_tr_b16 v[208:209], %[a1] offset:8192\n\t" \
  "v_and_b32 v226, %[mk], v226\n\t" \
  "v_and_b32_sdwa v229, %[mk], v228 dst_sel:DWORD dst_unused:UNUSED_PAD src0_sel:DWORD src1_sel:WORD_1\n\t" \
  "s_waitcnt lgkmcnt(2)\n\t" \
  "v_mfma_f32_16x16x32_bf16 v[144:147], v[212:215], v[100:103], v[144:147]\n\t" \
  "ds_read_b64_tr_b16 v[210:211], %[a1] offset:11264\n\t" \
  "v_and_b32 v228, %[mk], v228\n\t" \
  "v_mfma_f32_16x16x32_bf16 v[152:155], v[212:215], v[112:115], v[152:155]\n\t" \
  "ds_read_b32 v224, v224 offset:0\n\t" \
  "ds_read_b32 v227, v227 offset:0\n\t" \
  "v_and_b32_sdwa v231, %[mk], v230 dst_sel:DWORD dst_unused:UNUSED_PAD src0_sel:DWORD src1_sel:WORD_1\n\t" \
  "v_mfma_f32_16x16x32_bf16 v[160:163], v[212:215], v[124:127], v[160:163]\n\t" \
  "ds_read_b32 v226, v226 offset:0\n\t" \
  "v_and_b32 v230, %[mk], v230\n\t" \
  "v_fma_f32 v181, |v181|, v217, v181 mul:4\n\t" \
  "v_mfma_f32_16x16x32_bf16 v[168:171], v[212:215], v[136:139], v[168:171]\n\t" \
  "ds_read_b64_tr_b16 v[212:213], %[a1] offset:6656\n\t" \
  "ds_read_b64_tr_b16 v[214:215], %[a1] offset:7680\n\t" \
  "v_fma_f32 v180, |v180|, v216, v180 mul:4\n\t" \
  "v_fma_f32 v183, |v183|, v219, v183 mul:4\n\t" \
  "s_waitcnt lgkmcnt(5)\n\t" \
  "v_mfma_f32_16x16x32_bf16 v[144:147], v[208:211], v[104:107], v[144:147]\n\t" \
  "ds_read_b32 v229, v229 offset:0\n\t" \
  "ds_read_b32 v228, v228 offset:0\n\t" \
  "v_fma_f32 v182, |v182|, v218, v182 mul:4\n\t" \
  "v_fma_f32 v189, |v189|, v221, v189 mul:4\n\t" \
  "v_mfma_f32_16x16x32_bf16 v[152:155], v[208:211], v[116:119], v[152:155]\n\t" \
  "ds_read_b32 v231, v231 offset:0\n\t" \
  "v_fma_f32 v188, |v188|, v220, v188 mul:4\n\t" \
  "v_fma_f32 v191, |v191|, v223, v191 mul:4\n\t" \
  "v_fma_f32 v190, |v190|, v222, v190 mul:4\n\t" \
  "v_mfma_f32_16x16x32_bf16 v[160:163], v[208:211], v[128:131], v[160:163]\n\t" \
  "ds_read_b32 v230, v230 offset:0\n\t" \
  "v_fma_mix_f32 v192, v192, v235, 0 op_sel_hi:[0,1,0]\n\t" \
  "v_fma_mix_f32 v193, v193, v234, 0 op_sel_hi:[0,1,0]\n\t" \
  "v_cvt_pk_bf16_f32 v194, v192, v193\n\t" \
  "s_setprio 0\n\t" \
  "v_mfma_f32_16x16x32_bf16 v[168:171], v[208:211], v[140:143], v[168:171]\n\t" \
  "ds_read_b64_tr_b16 v[208:209], %[a1] offset:9728\n\t" \
  "v_cvt_pk_bf16_f32 v180, v180, v181\n\t" \
  "v_cvt_pk_bf16_f32 v181, v182, v183\n\t" \
  "s_waitcnt lgkmcnt(5)\n\t" \
  "v_mfma_f32_16x16x32_bf16 v[148:151], v[212:215], v[96:99], 0\n\t" \
  "ds_read_b64_tr_b16 v[210:211], %[a1] offset:10752\n\t" \
  "v_cvt_pk_bf16_f32 v188, v188, v189\n\t" \
  "v_cvt_pk_bf16_f32 v189, v190, v191\n\t" \
  "v_cvt_pk_f16_f32 v216, |v146|, |v147| clamp\n\t" \
  "v_mfma_f32_16x16x32_bf16 v[156:159], v[212:215], v[108:111], 0\n\t" \
  "v_cvt_pk_f16_f32 v232, v144, v145\n\t" \
  "v_cvt_pk_f16_f32 v218, |v154|, |v155| clamp\n\t" \
  "v_cvt_pk_f16_f32 v220, |v152|, |v153| clamp\n\t" \
  "v_mfma_f32_16x16x32_bf16 v[164:167], v[212:215], v[120:123], 0\n\t" \
  "v_pk_mul_f16 v233, v232, v232\n\t" \
  "v_and_b32_sdwa v217, %[mk], v216 dst_sel:DWORD dst_unused:UNUSED_PAD src0_sel:DWORD src1_sel:WORD_1\n\t" \
  "v_mfma_f32_16x16x32_bf16 v[172:175], v[212:215], v[132:135], 0\n\t" \
  "ds_read_b64_tr_b16 v[212:213], %[a1] offset:8704\n\t" \
  "v_and_b32 v216, %[mk], v216\n\t" \
  "v_and_b32_sdwa v219, %[mk], v218 dst_sel:DWORD dst_unused:UNUSED_PAD src0_sel:DWORD src1_sel:WORD_1\n\t" \
  "s_waitcnt lgkmcnt(1)\n\t" \
  "v_mfma_f32_16x16x32_bf16 v[148:151], v[208:211], v[100:103], v[148:151]\n\t" \
  "ds_read_b64_tr_b16 v[214:215], %[a1] offset:11776\n\t" \
  "v_and_b32 v218, %[mk], v218\n\t" \
  "v_mfma_f32_16x16x32_bf16 v[156:159], v[208:211], v[112:115], v[156:159]\n\t" \
  "ds_read_b32 v217, v217 offset:0\n\t" \
  "v_and_b32_sdwa v221, %[mk], v220 dst_sel:DWORD dst_unused:UNUSED_PAD src0_sel:DWORD src1_sel:WORD_1\n\t" \
  "v_mfma_f32_16x16x32_bf16 v[164:167], v[208:211], v[124:127], v[164:167]\n\t" \
  "ds_read_b32 v216, v216 offset:0\n\t" \
  "v_and_b32 v220, %[mk], v220\n\t" \
  "v_pk_fma_f16 v233, v233, %[h2], %[h1]\n\t" \
  "v_mfma_f32_16x16x32_bf16 v[172:175], v[208:211], v[136:139], v[172:175]\n\t" \
  "ds_read_b32 v219, v219 offset:0\n\t" \
  "v_pk_mul_f16 v233, v232, v233\n\t" \
  "v_fma_f32 v197, |v197|, v225, v197 mul:4\n\t" \
  "s_waitcnt lgkmcnt(3)\n\t" \
  "v_mfma_f32_16x16x32_bf16 v[148:151], v[212:215], v[104:107], v[148:151]\n\t" \
  "ds_read_b32 v218, v218 offset:0\n\t" \
  "v_fma_f32 v196, |v196|, v224, v196 mul:4\n\t" \
  "v_fma_f32 v199, |v199|, v227, v199 mul:4\n\t" \
  "v_mfma_f32_16x16x32_bf16 v[156:159], v[212:215], v[116:119], v[156:159]\n\t" \
  "ds_read_b32 v221, v221 offset:0\n\t" \
  "v_fma_f32 v198, |v198|, v226, v198 mul:4\n\t" \
  "v_fma_f32 v205, |v205|, v229, v205 mul:4\n\t" \
  "v_fma_f32 v204, |v204|, v228, v204 mul:4\n\t" \
  "v_mfma_f32_16x16x32_bf16 v[164:167], v[212:215], v[128:131], v[164:167]\n\t" \
  "ds_read_b32 v220, v220 offset:0\n\t" \
  "v_fma_f32 v207, |v207|, v231, v207 mul:4\n\t" \
  "v_fma_f32 v206, |v206|, v230, v206 mul:4\n\t" \
  "v_cvt_pk_bf16_f32 v196, v196, v197\n\t" \
  "v_mfma_f32_16x16x32_bf16 v[172:175], v[212:215], v[140:143], v[172:175]\n\t" \
  "v_cvt_pk_bf16_f32 v197, v198, v199\n\t" \
  "v_cvt_pk_bf16_f32 v204, v204, v205\n\t" \
  "v_cvt_pk_bf16_f32 v205, v206, v207\n\t" \
  "s_waitcnt vmcnt(1)\n\t" \
  "s_cmp_lg_u32 %[rnd], 1\n\t" \
  "s_cbranch_scc1 .Lcnx_c29\n\t" \
  "s_waitcnt vmcnt(0)\n\t" \
  ".Lcnx_c29:\n\t" \
  "s_barrier\n\t" \
  "ds_read_b64_tr_b16 v[208:209], %[a2] offset:0\n\t" \
  "ds_read_b64_tr_b16 v[210:211], %[a2] offset:1536\n\t" \
  "ds_read_b64_tr_b16 v[212:213], %[a2] offset:8\n\t" \
  "ds_read_b64_tr_b16 v[214:215], %[a2] offset:1544\n\t" \
  "s_setprio 2\n\t" \
  "v_exp_f16 v232, v233\n\t" \
  "v_cvt_pk_f16_f32 v224, |v162|, |v163| clamp\n\t" \
  "v_mfma_f32_16x16x32_bf16 v[40:43], v[242:245], v[236:239], v[40:43]\n\t" \
  "v_cvt_pk_f16_f32 v234, v160, v161\n\t" \
  "v_cvt_pk_f16_f32 v226, |v170|, |v171| clamp\n\t" \
  "v_mfma_f32_16x16x32_bf16 v[44:47], v[240:243], v[236:239], v[44:47]\n\t" \
  "s_cmp_lg_u32 %[grp], 0\n\t" \
  "s_cbranch_scc1 .Lcnx_c27\n\t" \
  "s_add_u32 m0, %[wl], %[ws]\n\t" \
  "s_nop 0\n\t" \
  "buffer_load_dwordx4 %[wv], %[wrs], %[so0] offen lds\n\t" \
  "buffer_load_dwordx4 %[wv], %[wrs], %[so1] offen offset:1024 lds\n\t" \
  "buffer_load_dwordx4 %[wv], %[wrs], %[so2] offen offset:2048 lds\n\t" \
  "s_add_u32 %[so0], %[so0], %[wc]\n\t" \
  "s_add_u32 %[so1], %[so1], %[wc]\n\t" \
  "s_add_u32 %[so2], %[so2], %[wc]\n\t" \
  ".Lcnx_c27:\n\t" \
  "buffer_load_dwordx4 v[236:239], %[vo], %[xrs], %[oc1] offen offset:0\n\t" \
  "s_cmp_lg_u32 %[rnd], 0\n\t" \
  "s_cbranch_scc1 .Lcnx_c28\n\t" \
  "s_add_u32 m0, %[xl], 49200\n\t" \
  "s_add_u32 %[xo], %[xo], %[rs]\n\t" \
  "buffer_load_dwordx4 %[xv], %[xrs], %[xo] offen lds\n\t" \
  ".Lcnx_c28:\n\t" \
  "v_cvt_pk_f16_f32 v228, |v168|, |v169| clamp\n\t" \
  "v_exp_f16_sdwa v232, v233 dst_sel:WORD_1 dst_unused:UNUSED_PRESERVE src0_sel:WORD_1\n\t" \
  "v_pk_mul_f16 v235, v234, v234\n\t" \
  "s_waitcnt lgkmcnt(2)\n\t" \
  "v_mfma_f32_16x16x32_bf16 v[0:3], v[208:211], v[178:181], v[0:3]\n\t" \
  "v_and_b32_sdwa v225, %[mk], v224 dst_sel:DWORD dst_unused:UNUSED_PAD src0_sel:DWORD src1_sel:WORD_1\n\t" \
  "v_and_b32 v224, %[mk], v224\n\t" \
  "v_mfma_f32_16x16x32_bf16 v[24:27], v[208:211], v[186:189], v[24:27]\n\t" \
  "ds_read_b32 v225, v225 offset:0\n\t" \
  "v_and_b32_sdwa v227, %[mk], v226 dst_sel:DWORD dst_unused:UNUSED_PAD src0_sel:DWORD src1_sel:WORD_1\n\t" \
  "v_mfma_f32_16x16x32_bf16 v[48:51], v[208:211], v[194:197], v[48:51]\n\t" \
  "ds_read_b32 v224, v224 offset:0\n\t" \
  "v_and_b32 v226, %[mk], v226\n\t" \
  "v_and_b32_sdwa v229, %[mk], v228 dst_sel:DWORD dst_unused:UNUSED_PAD src0_sel:DWORD src1_sel:WORD_1\n\t" \
  "v_mfma_f32_16x16x32_bf16 v[72:75], v[208:211], v[202:205], v[72:75]\n\t" \
  "ds_read_b64_tr_b16 v[208:209], %[a2] offset:64\n\t" \
  "v_and_b32 v228, %[mk], v228\n\t" \
  "s_waitcnt lgkmcnt(3)\n\t" \
  "v_mfma_f32_16x16x32_bf16 v[4:7], v[212:215], v[178:181], v[4:7]\n\t" \
  "ds_read_b64_tr_b16 v[210:211], %[a2] offset:1600\n\t" \
  "ds_read_b32 v227, v227 offset:0\n\t" \
  "v_pk_fma_f16 v235, v235, %[h2], %[h1]\n\t" \
  "v_mfma_f32_16x16x32_bf16 v[28:31], v[212:215], v[186:189], v[28:31]\n\t" \
  "ds_read_b32 v226, v226 offset:0\n\t" \
  "v_pk_mul_f16 v235, v234, v235\n\t" \
  "v_fma_f32 v147, |v147|, v217, v147 mul:4\n\t" \
  "v_fma_f32 v146, |v146|, v216, v146 mul:4\n\t" \
  "v_mfma_f32_16x16x32_bf16 v[52:55], v[212:215], v[194:197], v[52:55]\n\t" \
  "ds_read_b32 v229, v229 offset:0\n\t" \
  "v_fma_f32 v155, |v155|, v219, v155 mul:4\n\t" \
  "v_fma_f32 v154, |v154|, v218, v154 mul:4\n\t" \
  "v_fma_f32 v153, |v153|, v221, v153 mul:4\n\t" \
  "v_mfma_f32_16x16x32_bf16 v[76:79], v[212:215], v[202:205], v[76:79]\n\t" \
  "ds_read_b64_tr_b16 v[212:213], %[a2] offset:72\n\t" \
  "v_fma_f32 v152, |v152|, v220, v152 mul:4\n\t" \
  "v_pk_fma_f16 v232, v232, %[hh], %[hh]\n\t" \
  "s_waitcnt lgkmcnt(4)\n\t" \
  "v_mfma_f32_16x16x32_bf16 v[8:11], v[208:211], v[178:181], v[8:11]\n\t" \
  "ds_read_b64_tr_b16 v[214:215], %[a2] offset:1608\n\t" \
  "v_cvt_pk_bf16_f32 v147, v146, v147\n\t" \
  "v_cvt_pk_bf16_f32 v155, v154, v155\n\t" \
  "v_cvt_pk_bf16_f32 v154, v152, v153\n\t" \
  "v_mfma_f32_16x16x32_bf16 v[32:35], v[208:211], v[186:189], v[32:35]\n\t" \
  "ds_read_b32 v228, v228 offset:0\n\t" \
  "v_rcp_f16 v233, v232\n\t" \
  "v_exp_f16 v234, v235\n\t" \
  "v_cvt_pk_f16_f32 v216, |v148|, |v149| clamp\n\t" \
  "v_mfma_f32_16x16x32_bf16 v[56:59], v[208:211], v[194:197], v[56:59]\n\t" \
  "s_setprio 2\n\t" \
  "v_cvt_pk_f16_f32 v218, |v150|, |v151| clamp\n\t" \
  "v_cvt_pk_f16_f32 v220, |v156|, |v157| clamp\n\t" \
  "v_mfma_f32_16x16x32_bf16 v[80:83], v[208:211], v[202:205], v[80:83]\n\t" \
  "ds_read_b64_tr_b16 v[208:209], %[a2] offset:128\n\t" \
  "v_cvt_pk_f16_f32 v222, |v158|, |v159| clamp\n\t" \
  "v_rcp_f16_sdwa v232, v232 dst_sel:DWORD dst_unused:UNUSED_PAD src0_sel:WORD_1\n\t" \
  "v_exp_f16_sdwa v234, v235 dst_sel:WORD_1 dst_unused:UNUSED_PRESERVE src0_sel:WORD_1\n\t" \
  "s_waitcnt lgkmcnt(2)\n\t" \
  "v_mfma_f32_16x16x32_bf16 v[12:15], v[212:215], v[178:181], v[12:15]\n\t" \
  "ds_read_b64_tr_b16 v[210:211], %[a2] offset:1664\n\t" \
  "v_and_b32_sdwa v217, %[mk], v216 dst_sel:DWORD dst_unused:UNUSED_PAD src0_sel:DWORD src1_sel:WORD_1\n\t" \
  "v_and_b32 v216, %[mk], v216\n\t" \
  "v_mfma_f32_16x16x32_bf16 v[36:39], v[212:215], v[186:189], v[36:39]\n\t" \
  "ds_read_b32 v217, v217 offset:0\n\t" \
  "v_and_b32_sdwa v219, %[mk], v218 dst_sel:DWORD dst_unused:UNUSED_PAD src0_sel:DWORD src1_sel:WORD_1\n\t" \
  "v_mfma_f32_16x16x32_bf16 v[60:63], v[212:215], v[194:197], v[60:63]\n\t" \
  "ds_read_b32 v216, v216 offset:0\n\t" \
  "v_and_b32 v218, %[mk], v218\n\t" \
  "v_and_b32_sdwa v221, %[mk], v220 dst_sel:DWORD dst_unused:UNUSED_PAD src0_sel:DWORD src1_sel:WORD_1\n\t" \
  "v_mfma_f32_16x16x32_bf16 v[84:87], v[212:215], v[202:205], v[84:87]\n\t" \
  "ds_read_b64_tr_b16 v[212:213], %[a2] offset:136\n\t" \
  "v_and_b32 v220, %[mk], v220\n\t" \
  "s_waitcnt lgkmcnt(3)\n\t" \
  "v_mfma_f32_16x16x32_bf16 v[16:19], v[208:211], v[178:181], v[16:19]\n\t" \
  "ds_read_b64_tr_b16 v[214:215], %[a2] offset:1672\n\t" \
  "ds_read_b32 v219, v219 offset:0\n\t" \
  "v_and_b32_sdwa v223, %[mk], v222 dst_sel:DWORD dst_unused:UNUSED_PAD src0_sel:DWORD src1_sel:WORD_1\n\t" \
  "v_mfma_f32_16x16x32_bf16 v[40:43], v[208:211], v[186:189], v[40:43]\n\t" \
  "ds_read_b32 v218, v218 offset:0\n\t" \
  "v_and_b32 v222, %[mk], v222\n\t" \
  "v_fma_f32 v163, |v163|, v225, v163 mul:4\n\t" \
  "v_mfma_f32_16x16x32_bf16 v[64:67], v[208:211], v[194:197], v[64:67]\n\t" \
  "ds_read_b32 v221, v221 offset:0\n\t" \
  "v_fma_f32 v162, |v162|, v224, v162 mul:4\n\t" \
  "v_fma_f32 v171, |v171|, v227, v171 mul:4\n\t" \
  "v_mfma_f32_16x16x32_bf16 v[88:91], v[208:211], v[202:205], v[88:91]\n\t" \
  "ds_read_b64_tr_b16 v[208:209], %[a1] offset:0\n\t" \
  "ds_read_b64_tr_b16 v[210:211], %[a1] offset:1024\n\t" \
  "v_fma_f32 v170, |v170|, v226, v170 mul:4\n\t" \
  "v_fma_f32 v169, |v169|, v229, v169 mul:4\n\t" \
  "s_waitcnt lgkmcnt(5)\n\t" \
  "v_mfma_f32_16x16x32_bf16 v[20:23], v[212:215], v[178:181], v[20:23]\n\t" \
  "ds_read_b32 v220, v220 offset:0\n\t" \
  "v_fma_f32 v168, |v168|, v228, v168 mul:4\n\t" \
  "v_fma_mix_f32 v144, v144, v233, 0 op_sel_hi:[0,1,0]\n\t" \
  "v_fma_mix_f32 v145, v145, v232, 0 op_sel_hi:[0,1,0]\n\t" \
  "v_mfma_f32_16x16x32_bf16 v[44:47], v[212:215], v[186:189], v[44:47]\n\t" \
  "ds_read_b32 v223, v223 offset:0\n\t" \
  "v_pk_fma_f16 v234, v234, %[hh], %[hh]\n\t" \
  "v_cvt_pk_bf16_f32 v146, v144, v145\n\t" \
  "v_cvt_pk_bf16_f32 v163, v162, v163\n\t" \
  "v_mfma_f32_16x16x32_bf16 v[68:71], v[212:215], v[194:197], v[68:71]\n\t" \
  "ds_read_b32 v222, v222 offset:0\n\t" \
  "v_cvt_pk_bf16_f32 v171, v170, v171\n\t" \
  "v_cvt_pk_bf16_f32 v170, v168, v169\n\t" \
  "s_setprio 1\n\t" \
  "v_rcp_f16 v235, v234\n\t" \
  "v_mfma_f32_16x16x32_bf16 v[92:95], v[212:215], v[202:205], v[92:95]\n\t" \
  "ds_read_b64_tr_b16 v[212:213], %[a1] offset:3072\n\t" \
  "v_cvt_pk_f16_f32 v224, |v164|, |v165| clamp\n\t" \
  "v_cvt_pk_f16_f32 v226, |v166|, |v167| clamp\n\t" \
  "s_waitcnt lgkmcnt(4)\n\t" \
  "v_mfma_f32_16x16x32_bf16 v[176:179], v[208:211], v[96:99], 0\n\t" \
  "ds_read_b64_tr_b16 v[214:215], %[a1] offset:4096\n\t" \
  "v_cvt_pk_f16_f32 v228, |v172|, |v173| clamp\n\t" \
  "v_cvt_pk_f16_f32 v230, |v174|, |v175| clamp\n\t" \
  "v_rcp_f16_sdwa v234, v234 dst_sel:DWORD dst_unused:UNUSED_PAD src0_sel:WORD_1\n\t" \
  "v_mfma_f32_16x16x32_bf16 v[184:187], v[208:211], v[108:111], 0\n\t" \
  "v_and_b32_sdwa v225, %[mk], v224 dst_sel:DWORD dst_unused:UNUSED_PAD src0_sel:DWORD src1_sel:WORD_1\n\t" \
  "v_and_b32 v224, %[mk], v224\n\t" \
  "v_mfma_f32_16x16x32_bf16 v[192:195], v[208:211], v[120:123], 0\n\t" \
  "ds_read_b32 v225, v225 offset:0\n\t" \
  "v_and_b32_sdwa v227, %[mk], v226 dst_sel:DWORD dst_unused:UNUSED_PAD src0_sel:DWORD src1_sel:WORD_1\n\t" \
  "v_mfma_f32_16x16x32_bf16 v[200:203], v[208:211], v[132:135], 0\n\t" \
  "ds_read_b64_tr_b16 v[208:209], %[a1] offset:2048\n\t" \
  "v_and_b32 v226, %[mk], v226\n\t" \
  "v_and_b32_sdwa v229, %[mk], v228 dst_sel:DWORD dst_unused:UNUSED_PAD src0_sel:DWORD src1_sel:WORD_1\n\t" \
  "s_waitcnt lgkmcnt(2)\n\t" \
  "v_mfma_f32_16x16x32_bf16 v[176:179], v[212:215], v[100:103], v[176:179]\n\t" \
  "ds_read_b64_tr_b16 v[210:211], %[a1] offset:5120\n\t" \
  "v_and_b32 v228, %[mk], v228\n\t" \
  "v_mfma_f32_16x16x32_bf16 v[184:187], v[212:215], v[112:115], v[184:187]\n\t" \
  "ds_read_b32 v224, v224 offset:0\n\t" \
  "ds_read_b32 v227, v227 offset:0\n\t" \
  "v_and_b32_sdwa v231, %[mk], v230 dst_sel:DWORD dst_unused:UNUSED_PAD src0_sel:DWORD src1_sel:WORD_1\n\t" \
  "v_mfma_f32_16x16x32_bf16 v[192:195], v[212:215], v[124:127], v[192:195]\n\t" \
  "ds_read_b32 v226, v226 offset:0\n\t" \
  "v_and_b32 v230, %[mk], v230\n\t" \
  "v_fma_f32 v149, |v149|, v217, v149 mul:4\n\t" \
  "v_mfma_f32_16x16x32_bf16 v[200:203], v[212:215], v[136:139], v[200:203]\n\t" \
  "ds_read_b64_tr_b16 v[212:213], %[a1] offset:512\n\t" \
  "ds_read_b64_tr_b16 v[214:215], %[a1] offset:1536\n\t" \
  "v_fma_f32 v148, |v148|, v216, v148 mul:4\n\t" \
  "v_fma_f32 v151, |v151|, v219, v151 mul:4\n\t" \
  "s_waitcnt lgkmcnt(5)\n\t" \
  "v_mfma_f32_16x16x32_bf16 v[176:179], v[208:211], v[104:107], v[176:179]\n\t" \
  "ds_read_b32 v229, v229 offset:0\n\t" \
  "ds_read_b32 v228, v228 offset:0\n\t" \
  "v_fma_f32 v150, |v150|, v218, v150 mul:4\n\t" \
  "v_fma_f32 v157, |v157|, v221, v157 mul:4\n\t" \
  "v_mfma_f32_16x16x32_bf16 v[184:187], v[208:211], v[116:119], v[184:187]\n\t" \
  "ds_read_b32 v231, v231 offset:0\n\t" \
  "v_fma_f32 v156, |v156|, v220, v156 mul:4\n\t" \
  "v_fma_f32 v159, |v159|, v223, v159 mul:4\n\t" \
  "v_fma_f32 v158, |v158|, v222, v158 mul:4\n\t" \
  "v_mfma_f32_16x16x32_bf16 v[192:195], v[208:211], v[128:131], v[192:195]\n\t" \
  "ds_read_b32 v230, v230 offset:0\n\t" \
  "v_fma_mix_f32 v160, v160, v235, 0 op_sel_hi:[0,1,0]\n\t" \
  "v_fma_mix_f32 v161, v161, v234, 0 op_sel_hi:[0,1,0]\n\t" \
  "v_cvt_pk_bf16_f32 v162, v160, v161\n\t" \
  "s_setprio 0\n\t" \
  "v_mfma_f32_16x16x32_bf16 v[200:203], v[208:211], v[140:143], v[200:203]\n\t" \
  "ds_read_b64_tr_b16 v[208:209], %[a1] offset:3584\n\t" \
  "v_cvt_pk_bf16_f32 v148, v148, v149\n\t" \
  "v_cvt_pk_bf16_f32 v149, v150, v151\n\t" \
  "s_waitcnt lgkmcnt(5)\n\t" \
  "v_mfma_f32_16x16x32_bf16 v[180:183], v[212:215], v[96:99], 0\n\t" \
  "ds_read_b64_tr_b16 v[210:211], %[a1] offset:4608\n\t" \
  "v_cvt_pk_bf16_f32 v156, v156, v157\n\t" \
  "v_cvt_pk_bf16_f32 v157, v158, v159\n\t" \
  "v_cvt_pk_f16_f32 v216, |v178|, |v179| clamp\n\t" \
  "v_mfma_f32_16x16x32_bf16 v[188:191], v[212:215], v[108:111], 0\n\t" \
  "v_cvt_pk_f16_f32 v232, v176, v177\n\t" \
  "v_cvt_pk_f16_f32 v218, |v186|, |v187| clamp\n\t" \
  "v_cvt_pk_f16_f32 v220, |v184|, |v185| clamp\n\t" \
  "v_mfma_f32_16x16x32_bf16 v[196:199], v[212:215], v[120:123], 0\n\t" \
  "v_pk_mul_f16 v233, v232, v232\n\t" \
  "v_and_b32_sdwa v217, %[mk], v216 dst_sel:DWORD dst_unused:UNUSED_PAD src0_sel:DWORD src1_sel:WORD_1\n\t" \
  "v_mfma_f32_16x16x32_bf16 v[204:207], v[212:215], v[132:135], 0\n\t" \
  "ds_read_b64_tr_b16 v[212:213], %[a1] offset:2560\n\t" \
  "v_and_b32 v216, %[mk], v216\n\t" \
  "v_and_b32_sdwa v219, %[mk], v218 dst_sel:DWORD dst_unused:UNUSED_PAD src0_sel:DWORD src1_sel:WORD_1\n\t" \
  "s_waitcnt lgkmcnt(1)\n\t" \
  "v_mfma_f32_16x16x32_bf16 v[180:183], v[208:211], v[100:103], v[180:183]\n\t" \
  "ds_read_b64_tr_b16 v[214:215], %[a1] offset:5632\n\t" \
  "v_and_b32 v218, %[mk], v218\n\t" \
  "v_mfma_f32_16x16x32_bf16 v[188:191], v[208:211], v[112:115], v[188:191]\n\t" \
  "ds_read_b32 v217, v217 offset:0\n\t" \
  "v_and_b32_sdwa v221, %[mk], v220 dst_sel:DWORD dst_unused:UNUSED_PAD src0_sel:DWORD src1_sel:WORD_1\n\t" \
  "v_mfma_f32_16x16x32_bf16 v[196:199], v[208:211], v[124:127], v[196:199]\n\t" \
  "ds_read_b32 v216, v216 offset:0\n\t" \
  "v_and_b32 v220, %[mk], v220\n\t" \
  "v_pk_fma_f16 v233, v233, %[h2], %[h1]\n\t" \
  "v_mfma_f32_16x16x32_bf16 v[204:207], v[208:211], v[136:139], v[204:207]\n\t" \
  "ds_read_b32 v219, v219 offset:0\n\t" \
  "v_pk_mul_f16 v233, v232, v233\n\t" \
  "v_fma_f32 v165, |v165|, v225, v165 mul:4\n\t" \
  "s_waitcnt lgkmcnt(3)\n\t" \
  "v_mfma_f32_16x16x32_bf16 v[180:183], v[212:215], v[104:107], v[180:183]\n\t" \
  "ds_read_b32 v218, v218 offset:0\n\t" \
  "v_fma_f32 v164, |v164|, v224, v164 mul:4\n\t" \
  "v_fma_f32 v167, |v167|, v227, v167 mul:4\n\t" \
  "v_mfma_f32_16x16x32_bf16 v[188:191], v[212:215], v[116:119], v[188:191]\n\t" \
  "ds_read_b32 v221, v221 offset:0\n\t" \
  "v_fma_f32 v166, |v166|, v226, v166 mul:4\n\t" \
  "v_fma_f32 v173, |v173|, v229, v173 mul:4\n\t" \
  "v_fma_f32 v172, |v172|, v228, v172 mul:4\n\t" \
  "v_mfma_f32_16x16x32_bf16 v[196:199], v[212:215], v[128:131], v[196:199]\n\t" \
  "ds_read_b32 v220, v220 offset:0\n\t" \
  "v_fma_f32 v175, |v175|, v231, v175 mul:4\n\t" \
  "v_fma_f32 v174, |v174|, v230, v174 mul:4\n\t" \
  "v_cvt_pk_bf16_f32 v164, v164, v165\n\t" \
  "v_mfma_f32_16x16x32_bf16 v[204:207], v[212:215], v[140:143], v[204:207]\n\t" \
  "v_cvt_pk_bf16_f32 v165, v166, v167\n\t" \
  "v_cvt_pk_bf16_f32 v172, v172, v173\n\t" \
  "v_cvt_pk_bf16_f32 v173, v174, v175\n\t" \
  "s_waitcnt vmcnt(1)\n\t" \
  "s_cmp_lg_u32 %[rnd], 1\n\t" \
  "s_cbranch_scc1 .Lcnx_c32\n\t" \
  "s_waitcnt vmcnt(0)\n\t" \
  ".Lcnx_c32:\n\t" \
  "s_barrier\n\t" \
  "ds_read_b64_tr_b16 v[208:209], %[a2] offset:6160\n\t" \
  "ds_read_b64_tr_b16 v[210:211], %[a2] offset:7696\n\t" \
  "ds_read_b64_tr_b16 v[212:213], %[a2] offset:6168\n\t" \
  "ds_read_b64_tr_b16 v[214:215], %[a2] offset:7704\n\t" \
  "s_setprio 2\n\t" \
  "v_exp_f16 v232, v233\n\t" \
  "v_cvt_pk_f16_f32 v224, |v194|, |v195| clamp\n\t" \
  "v_mfma_f32_16x16x32_bf16 v[48:51], v[242:245], v[236:239], v[48:51]\n\t" \
  "v_cvt_pk_f16_f32 v234, v192, v193\n\t" \
  "v_cvt_pk_f16_f32 v226, |v202|, |v203| clamp\n\t" \
  "v_mfma_f32_16x16x32_bf16 v[52:55], v[240:243], v[236:239], v[52:55]\n\t" \
  "s_cmp_lg_u32 %[grp], 0\n\t" \
  "s_cbranch_scc1 .Lcnx_c30\n\t" \
  "s_add_u32 m0, %[wl], 0\n\t" \
  "s_nop 0\n\t" \
  "buffer_load_dwordx4 %[wv], %[wrs], %[so0] offen lds\n\t" \
  "buffer_load_dwordx4 %[wv], %[wrs], %[so1] offen offset:1024 lds\n\t" \
  "buffer_load_dwordx4 %[wv], %[wrs], %[so2] offen offset:2048 lds\n\t" \
  "s_add_u32 %[so0], %[so0], %[wc]\n\t" \
  "s_add_u32 %[so1], %[so1], %[wc]\n\t" \
  "s_add_u32 %[so2], %[so2], %[wc]\n\t" \
  ".Lcnx_c30:\n\t" \
  "buffer_load_dwordx4 v[236:239], %[vo], %[xrs], %[oc1] offen offset:64\n\t" \
  "s_cmp_lg_u32 %[rnd], 0\n\t" \
  "s_cbranch_scc1 .Lcnx_c31\n\t" \
  "s_add_u32 m0, %[xl], 57400\n\t" \
  "s_add_u32 %[xo], %[xo], %[rs]\n\t" \
  "buffer_load_dwordx4 %[xv], %[xrs], %[xo] offen lds\n\t" \
  ".Lcnx_c31:\n\t" \
  "v_cvt_pk_f16_f32 v228, |v200|, |v201| clamp\n\t" \
  "v_exp_f16_sdwa v232, v233 dst_sel:WORD_1 dst_unused:UNUSED_PRESERVE src0_sel:WORD_1\n\t" \
  "v_pk_mul_f16 v235, v234, v234\n\t" \
  "s_waitcnt lgkmcnt(2)\n\t" \
  "v_mfma_f32_16x16x32_bf16 v[0:3], v[208:211], v[146:149], v[0:3]\n\t" \
  "v_and_b32_sdwa v225, %[mk], v224 dst_sel:DWORD dst_unused:UNUSED_PAD src0_sel:DWORD src1_sel:WORD_1\n\t" \
  "v_and_b32 v224, %[mk], v224\n\t" \
  "v_mfma_f32_16x16x32_bf16 v[24:27], v[208:211], v[154:157], v[24:27]\n\t" \
  "ds_read_b32 v225, v225 offset:0\n\t" \
  "v_and_b32_sdwa v227, %[mk], v226 dst_sel:DWORD dst_unused:UNUSED_PAD src0_sel:DWORD src1_sel:WORD_1\n\t" \
  "v_mfma_f32_16x16x32_bf16 v[48:51], v[208:211], v[162:165], v[48:51]\n\t" \
  "ds_read_b32 v224, v224 offset:0\n\t" \
  "v_and_b32 v226, %[mk], v226\n\t" \
  "v_and_b32_sdwa v229, %[mk], v228 dst_sel:DWORD dst_unused:UNUSED_PAD src0_sel:DWORD src1_sel:WORD_1\n\t" \
  "v_mfma_f32_16x16x32_bf16 v[72:75], v[208:211], v[170:173], v[72:75]\n\t" \
  "ds_read_b64_tr_b16 v[208:209], %[a2] offset:6224\n\t" \
  "v_and_b32 v228, %[mk], v228\n\t" \
  "s_waitcnt lgkmcnt(3)\n\t" \
  "v_mfma_f32_16x16x32_bf16 v[4:7], v[212:215], v[146:149], v[4:7]\n\t" \
  "ds_read_b64_tr_b16 v[210:211], %[a2] offset:7760\n\t" \
  "ds_read_b32 v227, v227 offset:0\n\t" \
  "v_pk_fma_f16 v235, v235, %[h2], %[h1]\n\t" \
  "v_mfma_f32_16x16x32_bf16 v[28:31], v[212:215], v[154:157], v[28:31]\n\t" \
  "ds_read_b32 v226, v226 offset:0\n\t" \
  "v_pk_mul_f16 v235, v234, v235\n\t" \
  "v_fma_f32 v179, |v179|, v217, v179 mul:4\n\t" \
  "v_fma_f32 v178, |v178|, v216, v178 mul:4\n\t" \
  "v_mfma_f32_16x16x32_bf16 v[52:55], v[212:215], v[162:165], v[52:55]\n\t" \
  "ds_read_b32 v229, v229 offset:0\n\t" \
  "v_fma_f32 v187, |v187|, v219, v187 mul:4\n\t" \
  "v_fma_f32 v186, |v186|, v218, v186 mul:4\n\t" \
  "v_fma_f32 v185, |v185|, v221, v185 mul:4\n\t" \
  "v_mfma_f32_16x16x32_bf16 v[76:79], v[212:215], v[170:173], v[76:79]\n\t" \
  "ds_read_b64_tr_b16 v[212:213], %[a2] offset:6232\n\t" \
  "v_fma_f32 v184, |v184|, v220, v184 mul:4\n\t" \
  "v_pk_fma_f16 v232, v232, %[hh], %[hh]\n\t" \
  "s_waitcnt lgkmcnt(4)\n\t" \
  "v_mfma_f32_16x16x32_bf16 v[8:11], v[208:211], v[146:149], v[8:11]\n\t" \
  "ds_read_b64_tr_b16 v[214:215], %[a2] offset:7768\n\t" \
  "v_cvt_pk_bf16_f32 v179, v178, v179\n\t" \
  "v_cvt_pk_bf16_f32 v187, v186, v187\n\t" \
  "v_cvt_pk_bf16_f32 v186, v184, v185\n\t" \
  "v_mfma_f32_16x16x32_bf16 v[32:35], v[208:211], v[154:157], v[32:35]\n\t" \
  "ds_read_b32 v228, v228 offset:0\n\t" \
  "v_rcp_f16 v233, v232\n\t" \
  "v_exp_f16 v234, v235\n\t" \
  "v_cvt_pk_f16_f32 v216, |v180|, |v181| clamp\n\t" \
  "v_mfma_f32_16x16x32_bf16 v[56:59], v[208:211], v[162:165], v[56:59]\n\t" \
  "s_setprio 2\n\t" \
  "v_cvt_pk_f16_f32 v218, |v182|, |v183| clamp\n\t" \
  "v_cvt_pk_f16_f32 v220, |v188|, |v189| clamp\n\t" \
  "v_mfma_f32_16x16x32_bf16 v[80:83], v[208:211], v[170:173], v[80:83]\n\t" \
  "ds_read_b64_tr_b16 v[208:209], %[a2] offset:6288\n\t" \
  "v_cvt_pk_f16_f32 v222, |v190|, |v191| clamp\n\t" \
  "v_rcp_f16_sdwa v232, v232 dst_sel:DWORD dst_unused:UNUSED_PAD src0_sel:WORD_1\n\t" \
  "v_exp_f16_sdwa v234, v235 dst_sel:WORD_1 dst_unused:UNUSED_PRESERVE src0_sel:WORD_1\n\t" \
  "s_waitcnt lgkmcnt(2)\n\t" \
  "v_mfma_f32_16x16x32_bf16 v[12:15], v[212:215], v[146:149], v[12:15]\n\t" \
  "ds_read_b64_tr_b16 v[210:211], %[a2] offset:7824\n\t" \
  "v_and_b32_sdwa v217, %[mk], v216 dst_sel:DWORD dst_unused:UNUSED_PAD src0_sel:DWORD src1_sel:WORD_1\n\t" \
  "v_and_b32 v216, %[mk], v216\n\t" \
  "v_mfma_f32_16x16x32_bf16 v[36:39], v[212:215], v[154:157], v[36:39]\n\t" \
  "ds_read_b32 v217, v217 offset:0\n\t" \
  "v_and_b32_sdwa v219, %[mk], v218 dst_sel:DWORD dst_unused:UNUSED_PAD src0_sel:DWORD src1_sel:WORD_1\n\t" \
  "v_mfma_f32_16x16x32_bf16 v[60:63], v[212:215], v[162:165], v[60:63]\n\t" \
  "ds_read_b32 v216, v216 offset:0\n\t" \
  "v_and_b32 v218, %[mk], v218\n\t" \
  "v_and_b32_sdwa v221, %[mk], v220 dst_sel:DWORD dst_unused:UNUSED_PAD src0_sel:DWORD src1_sel:WORD_1\n\t" \
  "v_mfma_f32_16x16x32_bf16 v[84:87], v[212:215], v[170:173], v[84:87]\n\t" \
  "ds_read_b64_tr_b16 v[212:213], %[a2] offset:6296\n\t" \
  "v_and_b32 v220, %[mk], v220\n\t" \
  "s_waitcnt lgkmcnt(3)\n\t" \
  "v_mfma_f32_16x16x32_bf16 v[16:19], v[208:211], v[146:149], v[16:19]\n\t" \
  "ds_read_b64_tr_b16 v[214:215], %[a2] offset:7832\n\t" \
  "ds_read_b32 v219, v219 offset:0\n\t" \
  "v_and_b32_sdwa v223, %[mk], v222 dst_sel:DWORD dst_unused:UNUSED_PAD src0_sel:DWORD src1_sel:WORD_1\n\t" \
  "v_mfma_f32_16x16x32_bf16 v[40:43], v[208:211], v[154:157], v[40:43]\n\t" \
  "ds_read_b32 v218, v218 offset:0\n\t" \
  "v_and_b32 v222, %[mk], v222\n\t" \
  "v_fma_f32 v195, |v195|, v225, v195 mul:4\n\t" \
  "v_mfma_f32_16x16x32_bf16 v[64:67], v[208:211], v[162:165], v[64:67]\n\t" \
  "ds_read_b32 v221, v221 offset:0\n\t" \
  "v_fma_f32 v194, |v194|, v224, v194 mul:4\n\t" \
  "v_fma_f32 v203, |v203|, v227, v203 mul:4\n\t" \
  "v_mfma_f32_16x16x32_bf16 v[88:91], v[208:211], v[170:173], v[88:91]\n\t" \
  "ds_read_b64_tr_b16 v[208:209], %[a1] offset:6144\n\t" \
  "ds_read_b64_tr_b16 v[210:211], %[a1] offset:7168\n\t" \
  "v_fma_f32 v202, |v202|, v226, v202 mul:4\n\t" \
  "v_fma_f32 v201, |v201|, v229, v201 mul:4\n\t" \
  "s_waitcnt lgkmcnt(5)\n\t" \
  "v_mfma_f32_16x16x32_bf16 v[20:23], v[212:215], v[146:149], v[20:23]\n\t" \
  "ds_read_b32 v220, v220 offset:0\n\t" \
  "v_fma_f32 v200, |v200|, v228, v200 mul:4\n\t" \
  "v_fma_mix_f32 v176, v176, v233, 0 op_sel_hi:[0,1,0]\n\t" \
  "v_fma_mix_f32 v177, v177, v232, 0 op_sel_hi:[0,1,0]\n\t" \
  "v_mfma_f32_16x16x32_bf16 v[44:47], v[212:215], v[154:157], v[44:47]\n\t" \
  "ds_read_b32 v223, v223 offset:0\n\t" \
  "v_pk_fma_f16 v234, v234, %[hh], %[hh]\n\t" \
  "v_cvt_pk_bf16_f32 v178, v176, v177\n\t" \
  "v_cvt_pk_bf16_f32 v195, v194, v195\n\t" \
  "v_mfma_f32_16x16x32_bf16 v[68:71], v[212:215], v[162:165], v[68:71]\n\t" \
  "ds_read_b32 v222, v222 offset:0\n\t" \
  "v_cvt_pk_bf16_f32 v203, v202, v203\n\t" \
  "v_cvt_pk_bf16_f32 v202, v200, v201\n\t" \
  "s_setprio 1\n\t" \
  "v_rcp_f16 v235, v234\n\t" \
  "v_mfma_f32_16x16x32_bf16 v[92:95], v[212:215], v[170:173], v[92:95]\n\t" \
  "ds_read_b64_tr_b16 v[212:213], %[a1] offset:9216\n\t" \
  "v_cvt_pk_f16_f32 v224, |v196|, |v197| clamp\n\t" \
  "v_cvt_pk_f16_f32 v226, |v198|, |v199| clamp\n\t" \
  "s_waitcnt lgkmcnt(4)\n\t" \
  "v_mfma_f32_16x16x32_bf16 v[144:147], v[208:211], v[96:99], 0\n\t" \
  "ds_read_b64_tr_b16 v[214:215], %[a1] offset:10240\n\t" \
  "v_cvt_pk_f16_f32 v228, |v204|, |v205| clamp\n\t" \
  "v_cvt_pk_f16_f32 v230, |v206|, |v207| clamp\n\t" \
  "v_rcp_f16_sdwa v234, v234 dst_sel:DWORD dst_unused:UNUSED_PAD src0_sel:WORD_1\n\t" \
  "v_mfma_f32_16x16x32_bf16 v[152:155], v[208:211], v[108:111], 0\n\t" \
  "v_and_b32_sdwa v225, %[mk], v224 dst_sel:DWORD dst_unused:UNUSED_PAD src0_sel:DWORD src1_sel:WORD_1\n\t" \
  "v_and_b32 v224, %[mk], v224\n\t" \
  "v_mfma_f32_16x16x32_bf16 v[160:163], v[208:211], v[120:123], 0\n\t" \
  "ds_read_b32 v225, v225 offset:0\n\t" \
  "v_and_b32_sdwa v227, %[mk], v226 dst_sel:DWORD dst_unused:UNUSED_PAD src0_sel:DWORD src1_sel:WORD_1\n\t" \
  "v_mfma_f32_16x16x32_bf16 v[168:171], v[208:211], v[132:135], 0\n\t" \
  "ds_read_b64_tr_b16 v[208:209], %[a1] offset:8192\n\t" \
  "v_and_b32 v226, %[mk], v226\n\t" \
  "v_and_b32_sdwa v229, %[mk], v228 dst_sel:DWORD dst_unused:UNUSED_PAD src0_sel:DWORD src1_sel:WORD_1\n\t" \
  "s_waitcnt lgkmcnt(2)\n\t" \
  "v_mfma_f32_16x16x32_bf16 v[144:147], v[212:215], v[100:103], v[144:147]\n\t" \
  "ds_read_b64_tr_b16 v[210:211], %[a1] offset:11264\n\t" \
  "v_and_b32 v228, %[mk], v228\n\t" \
  "v_mfma_f32_16x16x32_bf16 v[152:155], v[212:215], v[112:115], v[152:155]\n\t" \
  "ds_read_b32 v224, v224 offset:0\n\t" \
  "ds_read_b32 v227, v227 offset:0\n\t" \
  "v_and_b32_sdwa v231, %[mk], v230 dst_sel:DWORD dst_unused:UNUSED_PAD src0_sel:DWORD src1_sel:WORD_1\n\t" \
  "v_mfma_f32_16x16x32_bf16 v[160:163], v[212:215], v[124:127], v[160:163]\n\t" \
  "ds_read_b32 v226, v226 offset:0\n\t" \
  "v_and_b32 v230, %[mk], v230\n\t" \
  "v_fma_f32 v181, |v181|, v217, v181 mul:4\n\t" \
  "v_mfma_f32_16x16x32_bf16 v[168:171], v[212:215], v[136:139], v[168:171]\n\t" \
  "ds_read_b64_tr_b16 v[212:213], %[a1] offset:6656\n\t" \
  "ds_read_b64_tr_b16 v[214:215], %[a1] offset:7680\n\t" \
  "v_fma_f32 v180, |v180|, v216, v180 mul:4\n\t" \
  "v_fma_f32 v183, |v183|, v219, v183 mul:4\n\t" \
  "s_waitcnt lgkmcnt(5)\n\t" \
  "v_mfma_f32_16x16x32_bf16 v[144:147], v[208:211], v[104:107], v[144:147]\n\t" \
  "ds_read_b32 v229, v229 offset:0\n\t" \
  "ds_read_b32 v228, v228 offset:0\n\t" \
  "v_fma_f32 v182, |v182|, v218, v182 mul:4\n\t" \
  "v_fma_f32 v189, |v189|, v221, v189 mul:4\n\t" \
  "v_mfma_f32_16x16x32_bf16 v[152:155], v[208:211], v[116:119], v[152:155]\n\t" \
  "ds_read_b32 v231, v231 offset:0\n\t" \
  "v_fma_f32 v188, |v188|, v220, v188 mul:4\n\t" \
  "v_fma_f32 v191, |v191|, v223, v191 mul:4\n\t" \
  "v_fma_f32 v190, |v190|, v222, v190 mul:4\n\t" \
  "v_mfma_f32_16x16x32_bf16 v[160:163], v[208:211], v[128:131], v[160:163]\n\t" \
  "ds_read_b32 v230, v230 offset:0\n\t" \
  "v_fma_mix_f32 v192, v192, v235, 0 op_sel_hi:[0,1,0]\n\t" \
  "v_fma_mix_f32 v193, v193, v234, 0 op_sel_hi:[0,1,0]\n\t" \
  "v_cvt_pk_bf16_f32 v194, v192, v193\n\t" \
  "s_setprio 0\n\t" \
  "v_mfma_f32_16x16x32_bf16 v[168:171], v[208:211], v[140:143], v[168:171]\n\t" \
  "ds_read_b64_tr_b16 v[208:209], %[a1] offset:9728\n\t" \
  "v_cvt_pk_bf16_f32 v180, v180, v181\n\t" \
  "v_cvt_pk_bf16_f32 v181, v182, v183\n\t" \
  "s_waitcnt lgkmcnt(5)\n\t" \
  "v_mfma_f32_16x16x32_bf16 v[148:151], v[212:215], v[96:99], 0\n\t" \
  "ds_read_b64_tr_b16 v[210:211], %[a1] offset:10752\n\t" \
  "v_cvt_pk_bf16_f32 v188, v188, v189\n\t" \
  "v_cvt_pk_bf16_f32 v189, v190, v191\n\t" \
  "v_cvt_pk_f16_f32 v216, |v146|, |v147| clamp\n\t" \
  "v_mfma_f32_16x16x32_bf16 v[156:159], v[212:215], v[108:111], 0\n\t" \
  "v_cvt_pk_f16_f32 v232, v144, v145\n\t" \
  "v_cvt_pk_f16_f32 v218, |v154|, |v155| clamp\n\t" \
  "v_cvt_pk_f16_f32 v220, |v152|, |v153| clamp\n\t" \
  "v_mfma_f32_16x16x32_bf16 v[164:167], v[212:215], v[120:123], 0\n\t" \
  "v_pk_mul_f16 v233, v232, v232\n\t" \
  "v_and_b32_sdwa v217, %[mk], v216 dst_sel:DWORD dst_unused:UNUSED_PAD src0_sel:DWORD src1_sel:WORD_1\n\t" \
  "v_mfma_f32_16x16x32_bf16 v[172:175], v[212:215], v[132:135], 0\n\t" \
  "ds_read_b64_tr_b16 v[212:213], %[a1] offset:8704\n\t" \
  "v_and_b32 v216, %[mk], v216\n\t" \
  "v_and_b32_sdwa v219, %[mk], v218 dst_sel:DWORD dst_unused:UNUSED_PAD src0_sel:DWORD src1_sel:WORD_1\n\t" \
  "s_waitcnt lgkmcnt(1)\n\t" \
  "v_mfma_f32_16x16x32_bf16 v[148:151], v[208:211], v[100:103], v[148:151]\n\t" \
  "ds_read_b64_tr_b16 v[214:215], %[a1] offset:11776\n\t" \
  "v_and_b32 v218, %[mk], v218\n\t" \
  "v_mfma_f32_16x16x32_bf16 v[156:159], v[208:211], v[112:115], v[156:159]\n\t" \
  "ds_read_b32 v217, v217 offset:0\n\t" \
  "v_and_b32_sdwa v221, %[mk], v220 dst_sel:DWORD dst_unused:UNUSED_PAD src0_sel:DWORD src1_sel:WORD_1\n\t" \
  "v_mfma_f32_16x16x32_bf16 v[164:167], v[208:211], v[124:127], v[164:167]\n\t" \
  "ds_read_b32 v216, v216 offset:0\n\t" \
  "v_and_b32 v220, %[mk], v220\n\t" \
  "v_pk_fma_f16 v233, v233, %[h2], %[h1]\n\t" \
  "v_mfma_f32_16x16x32_bf16 v[172:175], v[208:211], v[136:139], v[172:175]\n\t" \
  "ds_read_b32 v219, v219 offset:0\n\t" \
  "v_pk_mul_f16 v233, v232, v233\n\t" \
  "v_fma_f32 v197, |v197|, v225, v197 mul:4\n\t" \
  "s_waitcnt lgkmcnt(3)\n\t" \
  "v_mfma_f32_16x16x32_bf16 v[148:151], v[212:215], v[104:107], v[148:151]\n\t" \
  "ds_read_b32 v218, v218 offset:0\n\t" \
  "v_fma_f32 v196, |v196|, v224, v196 mul:4\n\t" \
  "v_fma_f32 v199, |v199|, v227, v199 mul:4\n\t" \
  "v_mfma_f32_16x16x32_bf16 v[156:159], v[212:215], v[116:119], v[156:159]\n\t" \
  "ds_read_b32 v221, v221 offset:0\n\t" \
  "v_fma_f32 v198, |v198|, v226, v198 mul:4\n\t" \
  "v_fma_f32 v205, |v205|, v229, v205 mul:4\n\t" \
  "v_fma_f32 v204, |v204|, v228, v204 mul:4\n\t" \
  "v_mfma_f32_16x16x32_bf16 v[164:167], v[212:215], v[128:131], v[164:167]\n\t" \
  "ds_read_b32 v220, v220 offset:0\n\t" \
  "v_fma_f32 v207, |v207|, v231, v207 mul:4\n\t" \
  "v_fma_f32 v206, |v206|, v230, v206 mul:4\n\t" \
  "v_cvt_pk_bf16_f32 v196, v196, v197\n\t" \
  "v_mfma_f32_16x16x32_bf16 v[172:175], v[212:215], v[140:143], v[172:175]\n\t" \
  "v_cvt_pk_bf16_f32 v197, v198, v199\n\t" \
  "v_cvt_pk_bf16_f32 v204, v204, v205\n\t" \
  "v_cvt_pk_bf16_f32 v205, v206, v207\n\t" \
  "s_waitcnt vmcnt(1)\n\t" \
  "s_cmp_lg_u32 %[rnd], 1\n\t" \
  "s_cbranch_scc1 .Lcnx_c35\n\t" \
  "s_waitcnt vmcnt(0)\n\t" \
  ".Lcnx_c35:\n\t" \
  "s_barrier\n\t" \
  "ds_read_b64_tr_b16 v[208:209], %[a2] offset:0\n\t" \
  "ds_read_b64_tr_b16 v[210:211], %[a2] offset:1536\n\t" \
  "ds_read_b64_tr_b16 v[212:213], %[a2] offset:8\n\t" \
  "ds_read_b64_tr_b16 v[214:215], %[a2] offset:1544\n\t" \
  "s_setprio 2\n\t" \
  "v_exp_f16 v232, v233\n\t" \
  "v_cvt_pk_f16_f32 v224, |v162|, |v163| clamp\n\t" \
  "v_mfma_f32_16x16x32_bf16 v[56:59], v[242:245], v[236:239], v[56:59]\n\t" \
  "v_cvt_pk_f16_f32 v234, v160, v161\n\t" \
  "v_cvt_pk_f16_f32 v226, |v170|, |v171| clamp\n\t" \
  "v_mfma_f32_16x16x32_bf16 v[60:63], v[240:243], v[236:239], v[60:63]\n\t" \
  "s_cmp_lg_u32 %[grp], 0\n\t" \
  "s_cbranch_scc1 .Lcnx_c33\n\t" \
  "s_add_u32 m0, %[wl], %[ws]\n\t" \
  "s_nop 0\n\t" \
  "buffer_load_dwordx4 %[wv], %[wrs], %[so0] offen lds\n\t" \
  "buffer_load_dwordx4 %[wv], %[wrs], %[so1] offen offset:1024 lds\n\t" \
  "buffer_load_dwordx4 %[wv], %[wrs], %[so2] offen offset:2048 lds\n\t" \
  "s_add_u32 %[so0], %[so0], %[wc]\n\t" \
  "s_add_u32 %[so1], %[so1], %[wc]\n\t" \
  "s_add_u32 %[so2], %[so2], %[wc]\n\t" \
  ".Lcnx_c33:\n\t" \
  "buffer_load_dwordx4 v[236:239], %[vo], %[xrs], %[oc1] offen offset:128\n\t" \
  "s_cmp_lg_u32 %[rnd], 0\n\t" \
  "s_cbranch_scc1 .Lcnx_c34\n\t" \
  "s_add_u32 m0, %[xl], 65600\n\t" \
  "s_add_u32 %[xo], %[xo], %[rs]\n\t" \
  "buffer_load_dwordx4 %[xv], %[xrs], %[xo] offen lds\n\t" \
  ".Lcnx_c34:\n\t" \
  "v_cvt_pk_f16_f32 v228, |v168|, |v169| clamp\n\t" \
  "v_exp_f16_sdwa v232, v233 dst_sel:WORD_1 dst_unused:UNUSED_PRESERVE src0_sel:WORD_1\n\t" \
  "v_pk_mul_f16 v235, v234, v234\n\t" \
  "s_waitcnt lgkmcnt(2)\n\t" \
  "v_mfma_f32_16x16x32_bf16 v[0:3], v[208:211], v[178:181], v[0:3]\n\t" \
  "v_and_b32_sdwa v225, %[mk], v224 dst_sel:DWORD dst_unused:UNUSED_PAD src0_sel:DWORD src1_sel:WORD_1\n\t" \
  "v_and_b32 v224, %[mk], v224\n\t" \
  "v_mfma_f32_16x16x32_bf16 v[24:27], v[208:211], v[186:189], v[24:27]\n\t" \
  "ds_read_b32 v225, v225 offset:0\n\t" \
  "v_and_b32_sdwa v227, %[mk], v226 dst_sel:DWORD dst_unused:UNUSED_PAD src0_sel:DWORD src1_sel:WORD_1\n\t" \
  "v_mfma_f32_16x16x32_bf16 v[48:51], v[208:211], v[194:197], v[48:51]\n\t" \
  "ds_read_b32 v224, v224 offset:0\n\t" \
  "v_and_b32 v226, %[mk], v226\n\t" \
  "v_and_b32_sdwa v229, %[mk], v228 dst_sel:DWORD dst_unused:UNUSED_PAD src0_sel:DWORD src1_sel:WORD_1\n\t" \
  "v_mfma_f32_16x16x32_bf16 v[72:75], v[208:211], v[202:205], v[72:75]\n\t" \
  "ds_read_b64_tr_b16 v[208:209], %[a2] offset:64\n\t" \
  "v_and_b32 v228, %[mk], v228\n\t" \
  "s_waitcnt lgkmcnt(3)\n\t" \
  "v_mfma_f32_16x16x32_bf16 v[4:7], v[212:215], v[178:181], v[4:7]\n\t" \
  "ds_read_b64_tr_b16 v[210:211], %[a2] offset:1600\n\t" \
  "ds_read_b32 v227, v227 offset:0\n\t" \
  "v_pk_fma_f16 v235, v235, %[h2], %[h1]\n\t" \
  "v_mfma_f32_16x16x32_bf16 v[28:31], v[212:215], v[186:189], v[28:31]\n\t" \
  "ds_read_b32 v226, v226 offset:0\n\t" \
  "v_pk_mul_f16 v235, v234, v235\n\t" \
  "v_fma_f32 v147, |v147|, v217, v147 mul:4\n\t" \
  "v_fma_f32 v146, |v146|, v216, v146 mul:4\n\t" \
  "v_mfma_f32_16x16x32_bf16 v[52:55], v[212:215], v[194:197], v[52:55]\n\t" \
  "ds_read_b32 v229, v229 offset:0\n\t" \
  "v_fma_f32 v155, |v155|, v219, v155 mul:4\n\t" \
  "v_fma_f32 v154, |v154|, v218, v154 mul:4\n\t" \
  "v_fma_f32 v153, |v153|, v221, v153 mul:4\n\t" \
  "v_mfma_f32_16x16x32_bf16 v[76:79], v[212:215], v[202:205], v[76:79]\n\t" \
  "ds_read_b64_tr_b16 v[212:213], %[a2] offset:72\n\t" \
  "v_fma_f32 v152, |v152|, v220, v152 mul:4\n\t" \
  "v_pk_fma_f16 v232, v232, %[hh], %[hh]\n\t" \
  "s_waitcnt lgkmcnt(4)\n\t" \
  "v_mfma_f32_16x16x32_bf16 v[8:11], v[208:211], v[178:181], v[8:11]\n\t" \
  "ds_read_b64_tr_b16 v[214:215], %[a2] offset:1608\n\t" \
  "v_cvt_pk_bf16_f32 v147, v146, v147\n\t" \
  "v_cvt_pk_bf16_f32 v155, v154, v155\n\t" \
  "v_cvt_pk_bf16_f32 v154, v152, v153\n\t" \
  "v_mfma_f32_16x16x32_bf16 v[32:35], v[208:211], v[186:189], v[32:35]\n\t" \
  "ds_read_b32 v228, v228 offset:0\n\t" \
  "v_rcp_f16 v233, v232\n\t" \
  "v_exp_f16 v234, v235\n\t" \
  "v_cvt_pk_f16_f32 v216, |v148|, |v149| clamp\n\t" \
  "v_mfma_f32_16x16x32_bf16 v[56:59], v[208:211], v[194:197], v[56:59]\n\t" \
  "s_setprio 2\n\t" \
  "v_cvt_pk_f16_f32 v218, |v150|, |v151| clamp\n\t" \
  "v_cvt_pk_f16_f32 v220, |v156|, |v157| clamp\n\t" \
  "v_mfma_f32_16x16x32_bf16 v[80:83], v[208:211], v[202:205], v[80:83]\n\t" \
  "ds_read_b64_tr_b16 v[208:209], %[a2] offset:128\n\t" \
  "v_cvt_pk_f16_f32 v222, |v158|, |v159| clamp\n\t" \
  "v_rcp_f16_sdwa v232, v232 dst_sel:DWORD dst_unused:UNUSED_PAD src0_sel:WORD_1\n\t" \
  "v_exp_f16_sdwa v234, v235 dst_sel:WORD_1 dst_unused:UNUSED_PRESERVE src0_sel:WORD_1\n\t" \
  "s_waitcnt lgkmcnt(2)\n\t" \
  "v_mfma_f32_16x16x32_bf16 v[12:15], v[212:215], v[178:181], v[12:15]\n\t" \
  "ds_read_b64_tr_b16 v[210:211], %[a2] offset:1664\n\t" \
  "v_and_b32_sdwa v217, %[mk], v216 dst_sel:DWORD dst_unused:UNUSED_PAD src0_sel:DWORD src1_sel:WORD_1\n\t" \
  "v_and_b32 v216, %[mk], v216\n\t" \
  "v_mfma_f32_16x16x32_bf16 v[36:39], v[212:215], v[186:189], v[36:39]\n\t" \
  "ds_read_b32 v217, v217 offset:0\n\t" \
  "v_and_b32_sdwa v219, %[mk], v218 dst_sel:DWORD dst_unused:UNUSED_PAD src0_sel:DWORD src1_sel:WORD_1\n\t" \
  "v_mfma_f32_16x16x32_bf16 v[60:63], v[212:215], v[194:197], v[60:63]\n\t" \
  "ds_read_b32 v216, v216 offset:0\n\t" \
  "v_and_b32 v218, %[mk], v218\n\t" \
  "v_and_b32_sdwa v221, %[mk], v220 dst_sel:DWORD dst_unused:UNUSED_PAD src0_sel:DWORD src1_sel:WORD_1\n\t" \
  "v_mfma_f32_16x16x32_bf16 v[84:87], v[212:215], v[202:205], v[84:87]\n\t" \
  "ds_read_b64_tr_b16 v[212:213], %[a2] offset:136\n\t" \
  "v_and_b32 v220, %[mk], v220\n\t" \
  "s_waitcnt lgkmcnt(3)\n\t" \
  "v_mfma_f32_16x16x32_bf16 v[16:19], v[208:211], v[178:181], v[16:19]\n\t" \
  "ds_read_b64_tr_b16 v[214:215], %[a2] offset:1672\n\t" \
  "ds_read_b32 v219, v219 offset:0\n\t" \
  "v_and_b32_sdwa v223, %[mk], v222 dst_sel:DWORD dst_unused:UNUSED_PAD src0_sel:DWORD src1_sel:WORD_1\n\t" \
  "v_mfma_f32_16x16x32_bf16 v[40:43], v[208:211], v[186:189], v[40:43]\n\t" \
  "ds_read_b32 v218, v218 offset:0\n\t" \
  "v_and_b32 v222, %[mk], v222\n\t" \
  "v_fma_f32 v163, |v163|, v225, v163 mul:4\n\t" \
  "v_mfma_f32_16x16x32_bf16 v[64:67], v[208:211], v[194:197], v[64:67]\n\t" \
  "ds_read_b32 v221, v221 offset:0\n\t" \
  "v_fma_f32 v162, |v162|, v224, v162 mul:4\n\t" \
  "v_fma_f32 v171, |v171|, v227, v171 mul:4\n\t" \
  "v_mfma_f32_16x16x32_bf16 v[88:91], v[208:211], v[202:205], v[88:91]\n\t" \
  "ds_read_b64_tr_b16 v[208:209], %[a1] offset:0\n\t" \
  "ds_read_b64_tr_b16 v[210:211], %[a1] offset:1024\n\t" \
  "v_fma_f32 v170, |v170|, v226, v170 mul:4\n\t" \
  "v_fma_f32 v169, |v169|, v229, v169 mul:4\n\t" \
  "s_waitcnt lgkmcnt(5)\n\t" \
  "v_mfma_f32_16x16x32_bf16 v[20:23], v[212:215], v[178:181], v[20:23]\n\t" \
  "ds_read_b32 v220, v220 offset:0\n\t" \
  "v_fma_f32 v168, |v168|, v228, v168 mul:4\n\t" \
  "v_fma_mix_f32 v144, v144, v233, 0 op_sel_hi:[0,1,0]\n\t" \
  "v_fma_mix_f32 v145, v145, v232, 0 op_sel_hi:[0,1,0]\n\t" \
  "v_mfma_f32_16x16x32_bf16 v[44:47], v[212:215], v[186:189], v[44:47]\n\t" \
  "ds_read_b32 v223, v223 offset:0\n\t" \
  "v_pk_fma_f16 v234, v234, %[hh], %[hh]\n\t" \
  "v_cvt_pk_bf16_f32 v146, v144, v145\n\t" \
  "v_cvt_pk_bf16_f32 v163, v162, v163\n\t" \
  "v_mfma_f32_16x16x32_bf16 v[68:71], v[212:215], v[194:197], v[68:71]\n\t" \
  "ds_read_b32 v222, v222 offset:0\n\t" \
  "v_cvt_pk_bf16_f32 v171, v170, v171\n\t" \
  "v_cvt_pk_bf16_f32 v170, v168, v169\n\t" \
  "s_setprio 1\n\t" \
  "v_rcp_f16 v235, v234\n\t" \
  "v_mfma_f32_16x16x32_bf16 v[92:95], v[212:215], v[202:205], v[92:95]\n\t" \
  "ds_read_b64_tr_b16 v[212:213], %[a1] offset:3072\n\t" \
  "v_cvt_pk_f16_f32 v224, |v164|, |v165| clamp\n\t" \
  "v_cvt_pk_f16_f32 v226, |v166|, |v167| clamp\n\t" \
  "s_waitcnt lgkmcnt(4)\n\t" \
  "v_mfma_f32_16x16x32_bf16 v[176:179], v[208:211], v[96:99], 0\n\t" \
  "ds_read_b64_tr_b16 v[214:215], %[a1] offset:4096\n\t" \
  "v_cvt_pk_f16_f32 v228, |v172|, |v173| clamp\n\t" \
  "v_cvt_pk_f16_f32 v230, |v174|, |v175| clamp\n\t" \
  "v_rcp_f16_sdwa v234, v234 dst_sel:DWORD dst_unused:UNUSED_PAD src0_sel:WORD_1\n\t" \
  "v_mfma_f32_16x16x32_bf16 v[184:187], v[208:211], v[108:111], 0\n\t" \
  "v_and_b32_sdwa v225, %[mk], v224 dst_sel:DWORD dst_unused:UNUSED_PAD src0_sel:DWORD src1_sel:WORD_1\n\t" \
  "v_and_b32 v224, %[mk], v224\n\t" \
  "v_mfma_f32_16x16x32_bf16 v[192:195], v[208:211], v[120:123], 0\n\t" \
  "ds_read_b32 v225, v225 offset:0\n\t" \
  "v_and_b32_sdwa v227, %[mk], v226 dst_sel:DWORD dst_unused:UNUSED_PAD src0_sel:DWORD src1_sel:WORD_1\n\t" \
  "v_mfma_f32_16x16x32_bf16 v[200:203], v[208:211], v[132:135], 0\n\t" \
  "ds_read_b64_tr_b16 v[208:209], %[a1] offset:2048\n\t" \
  "v_and_b32 v226, %[mk], v226\n\t" \
  "v_and_b32_sdwa v229, %[mk], v228 dst_sel:DWORD dst_unused:UNUSED_PAD src0_sel:DWORD src1_sel:WORD_1\n\t" \
  "s_waitcnt lgkmcnt(2)\n\t" \
  "v_mfma_f32_16x16x32_bf16 v[176:179], v[212:215], v[100:103], v[176:179]\n\t" \
  "ds_read_b64_tr_b16 v[210:211], %[a1] offset:5120\n\t" \
  "v_and_b32 v228, %[mk], v228\n\t" \
  "v_mfma_f32_16x16x32_bf16 v[184:187], v[212:215], v[112:115], v[184:187]\n\t" \
  "ds_read_b32 v224, v224 offset:0\n\t" \
  "ds_read_b32 v227, v227 offset:0\n\t" \
  "v_and_b32_sdwa v231, %[mk], v230 dst_sel:DWORD dst_unused:UNUSED_PAD src0_sel:DWORD src1_sel:WORD_1\n\t" \
  "v_mfma_f32_16x16x32_bf16 v[192:195], v[212:215], v[124:127], v[192:195]\n\t" \
  "ds_read_b32 v226, v226 offset:0\n\t" \
  "v_and_b32 v230, %[mk], v230\n\t" \
  "v_fma_f32 v149, |v149|, v217, v149 mul:4\n\t" \
  "v_mfma_f32_16x16x32_bf16 v[200:203], v[212:215], v[136:139], v[200:203]\n\t" \
  "ds_read_b64_tr_b16 v[212:213], %[a1] offset:512\n\t" \
  "ds_read_b64_tr_b16 v[214:215], %[a1] offset:1536\n\t" \
  "v_fma_f32 v148, |v148|, v216, v148 mul:4\n\t" \
  "v_fma_f32 v151, |v151|, v219, v151 mul:4\n\t" \
  "s_waitcnt lgkmcnt(5)\n\t" \
  "v_mfma_f32_16x16x32_bf16 v[176:179], v[208:211], v[104:107], v[176:179]\n\t" \
  "ds_read_b32 v229, v229 offset:0\n\t" \
  "ds_read_b32 v228, v228 offset:0\n\t" \
  "v_fma_f32 v150, |v150|, v218, v150 mul:4\n\t" \
  "v_fma_f32 v157, |v157|, v221, v157 mul:4\n\t" \
  "v_mfma_f32_16x16x32_bf16 v[184:187], v[208:211], v[116:119], v[184:187]\n\t" \
  "ds_read_b32 v231, v231 offset:0\n\t" \
  "v_fma_f32 v156, |v156|, v220, v156 mul:4\n\t" \
  "v_fma_f32 v159, |v159|, v223, v159 mul:4\n\t" \
  "v_fma_f32 v158, |v158|, v222, v158 mul:4\n\t" \
  "v_mfma_f32_16x16x32_bf16 v[192:195], v[208:211], v[128:131], v[192:195]\n\t" \
  "ds_read_b32 v230, v230 offset:0\n\t" \
  "v_fma_mix_f32 v160, v160, v235, 0 op_sel_hi:[0,1,0]\n\t" \
  "v_fma_mix_f32 v161, v161, v234, 0 op_sel_hi:[0,1,0]\n\t" \
  "v_cvt_pk_bf16_f32 v162, v160, v161\n\t" \
  "s_setprio 0\n\t" \
  "v_mfma_f32_16x16x32_bf16 v[200:203], v[208:211], v[140:143], v[200:203]\n\t" \
  "ds_read_b64_tr_b16 v[208:209], %[a1] offset:3584\n\t" \
  "v_cvt_pk_bf16_f32 v148, v148, v149\n\t" \
  "v_cvt_pk_bf16_f32 v149, v150, v151\n\t" \
  "s_waitcnt lgkmcnt(5)\n\t" \
  "v_mfma_f32_16x16x32_bf16 v[180:183], v[212:215], v[96:99], 0\n\t" \
  "ds_read_b64_tr_b16 v[210:211], %[a1] offset:4608\n\t" \
  "v_cvt_pk_bf16_f32 v156, v156, v157\n\t" \
  "v_cvt_pk_bf16_f32 v157, v158, v159\n\t" \
  "v_cvt_pk_f16_f32 v216, |v178|, |v179| clamp\n\t" \
  "v_mfma_f32_16x16x32_bf16 v[188:191], v[212:215], v[108:111], 0\n\t" \
  "v_cvt_pk_f16_f32 v232, v176, v177\n\t" \
  "v_cvt_pk_f16_f32 v218, |v186|, |v187| clamp\n\t" \
  "v_cvt_pk_f16_f32 v220, |v184|, |v185| clamp\n\t" \
  "v_mfma_f32_16x16x32_bf16 v[196:199], v[212:215], v[120:123], 0\n\t" \
  "v_pk_mul_f16 v233, v232, v232\n\t" \
  "v_and_b32_sdwa v217, %[mk], v216 dst_sel:DWORD dst_unused:UNUSED_PAD src0_sel:DWORD src1_sel:WORD_1\n\t" \
  "v_mfma_f32_16x16x32_bf16 v[204:207], v[212:215], v[132:135], 0\n\t" \
  "ds_read_b64_tr_b16 v[212:213], %[a1] offset:2560\n\t" \
  "v_and_b32 v216, %[mk], v216\n\t" \
  "v_and_b32_sdwa v219, %[mk], v218 dst_sel:DWORD dst_unused:UNUSED_PAD src0_sel:DWORD src1_sel:WORD_1\n\t" \
  "s_waitcnt lgkmcnt(1)\n\t" \
  "v_mfma_f32_16x16x32_bf16 v[180:183], v[208:211], v[100:103], v[180:183]\n\t" \
  "ds_read_b64_tr_b16 v[214:215], %[a1] offset:5632\n\t" \
  "v_and_b32 v218, %[mk], v218\n\t" \
  "v_mfma_f32_16x16x32_bf16 v[188:191], v[208:211], v[112:115], v[188:191]\n\t" \
  "ds_read_b32 v217, v217 offset:0\n\t" \
  "v_and_b32_sdwa v221, %[mk], v220 dst_sel:DWORD dst_unused:UNUSED_PAD src0_sel:DWORD src1_sel:WORD_1\n\t" \
  "v_mfma_f32_16x16x32_bf16 v[196:199], v[208:211], v[124:127], v[196:199]\n\t" \
  "ds_read_b32 v216, v216 offset:0\n\t" \
  "v_and_b32 v220, %[mk], v220\n\t" \
  "v_pk_fma_f16 v233, v233, %[h2], %[h1]\n\t" \
  "v_mfma_f32_16x16x32_bf16 v[204:207], v[208:211], v[136:139], v[204:207]\n\t" \
  "ds_read_b32 v219, v219 offset:0\n\t" \
  "v_pk_mul_f16 v233, v232, v233\n\t" \
  "v_fma_f32 v165, |v165|, v225, v165 mul:4\n\t" \
  "s_waitcnt lgkmcnt(3)\n\t" \
  "v_mfma_f32_16x16x32_bf16 v[180:183], v[212:215], v[104:107], v[180:183]\n\t" \
  "ds_read_b32 v218, v218 offset:0\n\t" \
  "v_fma_f32 v164, |v164|, v224, v164 mul:4\n\t" \
  "v_fma_f32 v167, |v167|, v227, v167 mul:4\n\t" \
  "v_mfma_f32_16x16x32_bf16 v[188:191], v[212:215], v[116:119], v[188:191]\n\t" \
  "ds_read_b32 v221, v221 offset:0\n\t" \
  "v_fma_f32 v166, |v166|, v226, v166 mul:4\n\t" \
  "v_fma_f32 v173, |v173|, v229, v173 mul:4\n\t" \
  "v_fma_f32 v172, |v172|, v228, v172 mul:4\n\t" \
  "v_mfma_f32_16x16x32_bf16 v[196:199], v[212:215], v[128:131], v[196:199]\n\t" \
  "ds_read_b32 v220, v220 offset:0\n\t" \
  "v_fma_f32 v175, |v175|, v231, v175 mul:4\n\t" \
  "v_fma_f32 v174, |v174|, v230, v174 mul:4\n\t" \
  "v_cvt_pk_bf16_f32 v164, v164, v165\n\t" \
  "v_mfma_f32_16x16x32_bf16 v[204:207], v[212:215], v[140:143], v[204:207]\n\t" \
  "v_cvt_pk_bf16_f32 v165, v166, v167\n\t" \
  "v_cvt_pk_bf16_f32 v172, v172, v173\n\t" \
  "v_cvt_pk_bf16_f32 v173, v174, v175\n\t" \
  "s_waitcnt vmcnt(1)\n\t" \
  "s_cmp_lg_u32 %[rnd], 1\n\t" \
  "s_cbranch_scc1 .Lcnx_c38\n\t" \
  "s_waitcnt vmcnt(0)\n\t" \
  ".Lcnx_c38:\n\t" \
  "s_barrier\n\t" \
  "ds_read_b64_tr_b16 v[208:209], %[a2] offset:6160\n\t" \
  "ds_read_b64_tr_b16 v[210:211], %[a2] offset:7696\n\t" \
  "ds_read_b64_tr_b16 v[212:213], %[a2] offset:6168\n\t" \
  "ds_read_b64_tr_b16 v[214:215], %[a2] offset:7704\n\t" \
  "s_setprio 2\n\t" \
  "v_exp_f16 v232, v233\n\t" \
  "v_cvt_pk_f16_f32 v224, |v194|, |v195| clamp\n\t" \
  "v_mfma_f32_16x16x32_bf16 v[64:67], v[242:245], v[236:239], v[64:67]\n\t" \
  "v_cvt_pk_f16_f32 v234, v192, v193\n\t" \
  "v_cvt_pk_f16_f32 v226, |v202|, |v203| clamp\n\t" \
  "v_mfma_f32_16x16x32_bf16 v[68:71], v[240:243], v[236:239], v[68:71]\n\t" \
  "s_cmp_lg_u32 %[grp], 0\n\t" \
  "s_cbranch_scc1 .Lcnx_c36\n\t" \
  "s_add_u32 m0, %[wl], 0\n\t" \
  "s_nop 0\n\t" \
  "buffer_load_dwordx4 %[wv], %[wrs], %[so0] offen lds\n\t" \
  "buffer_load_dwordx4 %[wv], %[wrs], %[so1] offen offset:1024 lds\n\t" \
  "buffer_load_dwordx4 %[wv], %[wrs], %[so2] offen offset:2048 lds\n\t" \
  "s_add_u32 %[so0], %[so0], %[wc]\n\t" \
  "s_add_u32 %[so1], %[so1], %[wc]\n\t" \
  "s_add_u32 %[so2], %[so2], %[wc]\n\t" \
  ".Lcnx_c36:\n\t" \
  "s_sub_u32 %[so0], %[so0], %[wr1]\n\t" \
  "s_sub_u32 %[so1], %[so1], %[wr1]\n\t" \
  "s_sub_u32 %[so2], %[so2], %[wr1]\n\t" \
  "buffer_load_dwordx4 v[236:239], %[vo], %[xrs], %[oc1] offen offset:192\n\t" \
  "s_cmp_lg_u32 %[rnd], 0\n\t" \
  "s_cbranch_scc1 .Lcnx_c37\n\t" \
  "s_add_u32 m0, %[xl], 73800\n\t" \
  "s_add_u32 %[xo], %[xo], %[rs]\n\t" \
  "buffer_load_dwordx4 %[xv], %[xrs], %[xo] offen lds\n\t" \
  ".Lcnx_c37:\n\t" \
  "v_cvt_pk_f16_f32 v228, |v200|, |v201| clamp\n\t" \
  "v_exp_f16_sdwa v232, v233 dst_sel:WORD_1 dst_unused:UNUSED_PRESERVE src0_sel:WORD_1\n\t" \
  "v_pk_mul_f16 v235, v234, v234\n\t" \
  "s_waitcnt lgkmcnt(2)\n\t" \
  "v_mfma_f32_16x16x32_bf16 v[0:3], v[208:211], v[146:149], v[0:3]\n\t" \
  "v_and_b32_sdwa v225, %[mk], v224 dst_sel:DWORD dst_unused:UNUSED_PAD src0_sel:DWORD src1_sel:WORD_1\n\t" \
  "v_and_b32 v224, %[mk], v224\n\t" \
  "v_mfma_f32_16x16x32_bf16 v[24:27], v[208:211], v[154:157], v[24:27]\n\t" \
  "ds_read_b32 v225, v225 offset:0\n\t" \
  "v_and_b32_sdwa v227, %[mk], v226 dst_sel:DWORD dst_unused:UNUSED_PAD src0_sel:DWORD src1_sel:WORD_1\n\t" \
  "v_mfma_f32_16x16x32_bf16 v[48:51], v[208:211], v[162:165], v[48:51]\n\t" \
  "ds_read_b32 v224, v224 offset:0\n\t" \
  "v_and_b32 v226, %[mk], v226\n\t" \
  "v_and_b32_sdwa v229, %[mk], v228 dst_sel:DWORD dst_unused:UNUSED_PAD src0_sel:DWORD src1_sel:WORD_1\n\t" \
  "v_mfma_f32_16x16x32_bf16 v[72:75], v[208:211], v[170:173], v[72:75]\n\t" \
  "ds_read_b64_tr_b16 v[208:209], %[a2] offset:6224\n\t" \
  "v_and_b32 v228, %[mk], v228\n\t" \
  "s_waitcnt lgkmcnt(3)\n\t" \
  "v_mfma_f32_16x16x32_bf16 v[4:7], v[212:215], v[146:149], v[4:7]\n\t" \
  "ds_read_b64_tr_b16 v[210:211], %[a2] offset:7760\n\t" \
  "ds_read_b32 v227, v227 offset:0\n\t" \
  "v_pk_fma_f16 v235, v235, %[h2], %[h1]\n\t" \
  "v_mfma_f32_16x16x32_bf16 v[28:31], v[212:215], v[154:157], v[28:31]\n\t" \
  "ds_read_b32 v226, v226 offset:0\n\t" \
  "v_pk_mul_f16 v235, v234, v235\n\t" \
  "v_fma_f32 v179, |v179|, v217, v179 mul:4\n\t" \
  "v_fma_f32 v178, |v178|, v216, v178 mul:4\n\t" \
  "v_mfma_f32_16x16x32_bf16 v[52:55], v[212:215], v[162:165], v[52:55]\n\t" \
  "ds_read_b32 v229, v229 offset:0\n\t" \
  "v_fma_f32 v187, |v187|, v219, v187 mul:4\n\t" \
  "v_fma_f32 v186, |v186|, v218, v186 mul:4\n\t" \
  "v_fma_f32 v185, |v185|, v221, v185 mul:4\n\t" \
  "v_mfma_f32_16x16x32_bf16 v[76:79], v[212:215], v[170:173], v[76:79]\n\t" \
  "ds_read_b64_tr_b16 v[212:213], %[a2] offset:6232\n\t" \
  "v_fma_f32 v184, |v184|, v220, v184 mul:4\n\t" \
  "v_pk_fma_f16 v232, v232, %[hh], %[hh]\n\t" \
  "s_waitcnt lgkmcnt(4)\n\t" \
  "v_mfma_f32_16x16x32_bf16 v[8:11], v[208:211], v[146:149], v[8:11]\n\t" \
  "ds_read_b64_tr_b16 v[214:215], %[a2] offset:7768\n\t" \
  "v_cvt_pk_bf16_f32 v179, v178, v179\n\t" \
  "v_cvt_pk_bf16_f32 v187, v186, v187\n\t" \
  "v_cvt_pk_bf16_f32 v186, v184, v185\n\t" \
  "v_mfma_f32_16x16x32_bf16 v[32:35], v[208:211], v[154:157], v[32:35]\n\t" \
  "ds_read_b32 v228, v228 offset:0\n\t" \
  "v_rcp_f16 v233, v232\n\t" \
  "v_exp_f16 v234, v235\n\t" \
  "v_cvt_pk_f16_f32 v216, |v180|, |v181| clamp\n\t" \
  "v_mfma_f32_16x16x32_bf16 v[56:59], v[208:211], v[162:165], v[56:59]\n\t" \
  "s_setprio 2\n\t" \
  "v_cvt_pk_f16_f32 v218, |v182|, |v183| clamp\n\t" \
  "v_cvt_pk_f16_f32 v220, |v188|, |v189| clamp\n\t" \
  "v_mfma_f32_16x16x32_bf16 v[80:83], v[208:211], v[170:173], v[80:83]\n\t" \
  "ds_read_b64_tr_b16 v[208:209], %[a2] offset:6288\n\t" \
  "v_cvt_pk_f16_f32 v222, |v190|, |v191| clamp\n\t" \
  "v_rcp_f16_sdwa v232, v232 dst_sel:DWORD dst_unused:UNUSED_PAD src0_sel:WORD_1\n\t" \
  "v_exp_f16_sdwa v234, v235 dst_sel:WORD_1 dst_unused:UNUSED_PRESERVE src0_sel:WORD_1\n\t" \
  "s_waitcnt lgkmcnt(2)\n\t" \
  "v_mfma_f32_16x16x32_bf16 v[12:15], v[212:215], v[146:149], v[12:15]\n\t" \
  "ds_read_b64_tr_b16 v[210:211], %[a2] offset:7824\n\t" \
  "v_and_b32_sdwa v217, %[mk], v216 dst_sel:DWORD dst_unused:UNUSED_PAD src0_sel:DWORD src1_sel:WORD_1\n\t" \
  "v_and_b32 v216, %[mk], v216\n\t" \
  "v_mfma_f32_16x16x32_bf16 v[36:39], v[212:215], v[154:157], v[36:39]\n\t" \
  "ds_read_b32 v217, v217 offset:0\n\t" \
  "v_and_b32_sdwa v219, %[mk], v218 dst_sel:DWORD dst_unused:UNUSED_PAD src0_sel:DWORD src1_sel:WORD_1\n\t" \
  "v_mfma_f32_16x16x32_bf16 v[60:63], v[212:215], v[162:165], v[60:63]\n\t" \
  "ds_read_b32 v216, v216 offset:0\n\t" \
  "v_and_b32 v218, %[mk], v218\n\t" \
  "v_and_b32_sdwa v221, %[mk], v220 dst_sel:DWORD dst_unused:UNUSED_PAD src0_sel:DWORD src1_sel:WORD_1\n\t" \
  "v_mfma_f32_16x16x32_bf16 v[84:87], v[212:215], v[170:173], v[84:87]\n\t" \
  "ds_read_b64_tr_b16 v[212:213], %[a2] offset:6296\n\t" \
  "v_and_b32 v220, %[mk], v220\n\t" \
  "s_waitcnt lgkmcnt(3)\n\t" \
  "v_mfma_f32_16x16x32_bf16 v[16:19], v[208:211], v[146:149], v[16:19]\n\t" \
  "ds_read_b64_tr_b16 v[214:215], %[a2] offset:7832\n\t" \
  "ds_read_b32 v219, v219 offset:0\n\t" \
  "v_and_b32_sdwa v223, %[mk], v222 dst_sel:DWORD dst_unused:UNUSED_PAD src0_sel:DWORD src1_sel:WORD_1\n\t" \
  "v_mfma_f32_16x16x32_bf16 v[40:43], v[208:211], v[154:157], v[40:43]\n\t" \
  "ds_read_b32 v218, v218 offset:0\n\t" \
  "v_and_b32 v222, %[mk], v222\n\t" \
  "v_fma_f32 v195, |v195|, v225, v195 mul:4\n\t" \
  "v_mfma_f32_16x16x32_bf16 v[64:67], v[208:211], v[162:165], v[64:67]\n\t" \
  "ds_read_b32 v221, v221 offset:0\n\t" \
  "v_fma_f32 v194, |v194|, v224, v194 mul:4\n\t" \
  "v_fma_f32 v203, |v203|, v227, v203 mul:4\n\t" \
  "v_mfma_f32_16x16x32_bf16 v[88:91], v[208:211], v[170:173], v[88:91]\n\t" \
  "ds_read_b64_tr_b16 v[208:209], %[a1] offset:6144\n\t" \
  "ds_read_b64_tr_b16 v[210:211], %[a1] offset:7168\n\t" \
  "v_fma_f32 v202, |v202|, v226, v202 mul:4\n\t" \
  "v_fma_f32 v201, |v201|, v229, v201 mul:4\n\t" \
  "s_waitcnt lgkmcnt(5)\n\t" \
  "v_mfma_f32_16x16x32_bf16 v[20:23], v[212:215], v[146:149], v[20:23]\n\t" \
  "ds_read_b32 v220, v220 offset:0\n\t" \
  "v_fma_f32 v200, |v200|, v228, v200 mul:4\n\t" \
  "v_fma_mix_f32 v176, v176, v233, 0 op_sel_hi:[0,1,0]\n\t" \
  "v_fma_mix_f32 v177, v177, v232, 0 op_sel_hi:[0,1,0]\n\t" \
  "v_mfma_f32_16x16x32_bf16 v[44:47], v[212:215], v[154:157], v[44:47]\n\t" \
  "ds_read_b32 v223, v223 offset:0\n\t" \
  "v_pk_fma_f16 v234, v234, %[hh], %[hh]\n\t" \
  "v_cvt_pk_bf16_f32 v178, v176, v177\n\t" \
  "v_cvt_pk_bf16_f32 v195, v194, v195\n\t" \
  "v_mfma_f32_16x16x32_bf16 v[68:71], v[212:215], v[162:165], v[68:71]\n\t" \
  "ds_read_b32 v222, v222 offset:0\n\t" \
  "v_cvt_pk_bf16_f32 v203, v202, v203\n\t" \
  "v_cvt_pk_bf16_f32 v202, v200, v201\n\t" \
  "s_setprio 1\n\t" \
  "v_rcp_f16 v235, v234\n\t" \
  "v_mfma_f32_16x16x32_bf16 v[92:95], v[212:215], v[170:173], v[92:95]\n\t" \
  "ds_read_b64_tr_b16 v[212:213], %[a1] offset:9216\n\t" \
  "v_cvt_pk_f16_f32 v224, |v196|, |v197| clamp\n\t" \
  "v_cvt_pk_f16_f32 v226, |v198|, |v199| clamp\n\t" \
  "s_waitcnt lgkmcnt(4)\n\t" \
  "v_mfma_f32_16x16x32_bf16 v[144:147], v[208:211], v[96:99], 0\n\t" \
  "ds_read_b64_tr_b16 v[214:215], %[a1] offset:10240\n\t" \
  "v_cvt_pk_f16_f32 v228, |v204|, |v205| clamp\n\t" \
  "v_cvt_pk_f16_f32 v230, |v206|, |v207| clamp\n\t" \
  "v_rcp_f16_sdwa v234, v234 dst_sel:DWORD dst_unused:UNUSED_PAD src0_sel:WORD_1\n\t" \
  "v_mfma_f32_16x16x32_bf16 v[152:155], v[208:211], v[108:111], 0\n\t" \
  "v_and_b32_sdwa v225, %[mk], v224 dst_sel:DWORD dst_unused:UNUSED_PAD src0_sel:DWORD src1_sel:WORD_1\n\t" \
  "v_and_b32 v224, %[mk], v224\n\t" \
  "v_mfma_f32_16x16x32_bf16 v[160:163], v[208:211], v[120:123], 0\n\t" \
  "ds_read_b32 v225, v225 offset:0\n\t" \
  "v_and_b32_sdwa v227, %[mk], v226 dst_sel:DWORD dst_unused:UNUSED_PAD src0_sel:DWORD src1_sel:WORD_1\n\t" \
  "v_mfma_f32_16x16x32_bf16 v[168:171], v[208:211], v[132:135], 0\n\t" \
  "ds_read_b64_tr_b16 v[208:209], %[a1] offset:8192\n\t" \
  "v_and_b32 v226, %[mk], v226\n\t" \
  "v_and_b32_sdwa v229, %[mk], v228 dst_sel:DWORD dst_unused:UNUSED_PAD src0_sel:DWORD src1_sel:WORD_1\n\t" \
  "s_waitcnt lgkmcnt(2)\n\t" \
  "v_mfma_f32_16x16x32_bf16 v[144:147], v[212:215], v[100:103], v[144:147]\n\t" \
  "ds_read_b64_tr_b16 v[210:211], %[a1] offset:11264\n\t" \
  "v_and_b32 v228, %[mk], v228\n\t" \
  "v_mfma_f32_16x16x32_bf16 v[152:155], v[212:215], v[112:115], v[152:155]\n\t" \
  "ds_read_b32 v224, v224 offset:0\n\t" \
  "ds_read_b32 v227, v227 offset:0\n\t" \
  "v_and_b32_sdwa v231, %[mk], v230 dst_sel:DWORD dst_unused:UNUSED_PAD src0_sel:DWORD src1_sel:WORD_1\n\t" \
  "v_mfma_f32_16x16x32_bf16 v[160:163], v[212:215], v[124:127], v[160:163]\n\t" \
  "ds_read_b32 v226, v226 offset:0\n\t" \
  "v_and_b32 v230, %[mk], v230\n\t" \
  "v_fma_f32 v181, |v181|, v217, v181 mul:4\n\t" \
  "v_mfma_f32_16x16x32_bf16 v[168:171], v[212:215], v[136:139], v[168:171]\n\t" \
  "ds_read_b64_tr_b16 v[212:213], %[a1] offset:6656\n\t" \
  "ds_read_b64_tr_b16 v[214:215], %[a1] offset:7680\n\t" \
  "v_fma_f32 v180, |v180|, v216, v180 mul:4\n\t" \
  "v_fma_f32 v183, |v183|, v219, v183 mul:4\n\t" \
  "s_waitcnt lgkmcnt(5)\n\t" \
  "v_mfma_f32_16x16x32_bf16 v[144:147], v[208:211], v[104:107], v[144:147]\n\t" \
  "ds_read_b32 v229, v229 offset:0\n\t" \
  "ds_read_b32 v228, v228 offset:0\n\t" \
  "v_fma_f32 v182, |v182|, v218, v182 mul:4\n\t" \
  "v_fma_f32 v189, |v189|, v221, v189 mul:4\n\t" \
  "v_mfma_f32_16x16x32_bf16 v[152:155], v[208:211], v[116:119], v[152:155]\n\t" \
  "ds_read_b32 v231, v231 offset:0\n\t" \
  "v_fma_f32 v188, |v188|, v220, v188 mul:4\n\t" \
  "v_fma_f32 v191, |v191|, v223, v191 mul:4\n\t" \
  "v_fma_f32 v190, |v190|, v222, v190 mul:4\n\t" \
  "v_mfma_f32_16x16x32_bf16 v[160:163], v[208:211], v[128:131], v[160:163]\n\t" \
  "ds_read_b32 v230, v230 offset:0\n\t" \
  "v_fma_mix_f32 v192, v192, v235, 0 op_sel_hi:[0,1,0]\n\t" \
  "v_fma_mix_f32 v193, v193, v234, 0 op_sel_hi:[0,1,0]\n\t" \
  "v_cvt_pk_bf16_f32 v194, v192, v193\n\t" \
  "s_setprio 0\n\t" \
  "v_mfma_f32_16x16x32_bf16 v[168:171], v[208:211], v[140:143], v[168:171]\n\t" \
  "ds_read_b64_tr_b16 v[208:209], %[a1] offset:9728\n\t" \
  "v_cvt_pk_bf16_f32 v180, v180, v181\n\t" \
  "v_cvt_pk_bf16_f32 v181, v182, v183\n\t" \
  "s_waitcnt lgkmcnt(5)\n\t" \
  "v_mfma_f32_16x16x32_bf16 v[148:151], v[212:215], v[96:99], 0\n\t" \
  "ds_read_b64_tr_b16 v[210:211], %[a1] offset:10752\n\t" \
  "v_cvt_pk_bf16_f32 v188, v188, v189\n\t" \
  "v_cvt_pk_bf16_f32 v189, v190, v191\n\t" \
  "v_cvt_pk_f16_f32 v216, |v146|, |v147| clamp\n\t" \
  "v_mfma_f32_16x16x32_bf16 v[156:159], v[212:215], v[108:111], 0\n\t" \
  "v_cvt_pk_f16_f32 v232, v144, v145\n\t" \
  "v_cvt_pk_f16_f32 v218, |v154|, |v155| clamp\n\t" \
  "v_cvt_pk_f16_f32 v220, |v152|, |v153| clamp\n\t" \
  "v_mfma_f32_16x16x32_bf16 v[164:167], v[212:215], v[120:123], 0\n\t" \
  "v_pk_mul_f16 v233, v232, v232\n\t" \
  "v_and_b32_sdwa v217, %[mk], v216 dst_sel:DWORD dst_unused:UNUSED_PAD src0_sel:DWORD src1_sel:WORD_1\n\t" \
  "v_mfma_f32_16x16x32_bf16 v[172:175], v[212:215], v[132:135], 0\n\t" \
  "ds_read_b64_tr_b16 v[212:213], %[a1] offset:8704\n\t" \
  "v_and_b32 v216, %[mk], v216\n\t" \
  "v_and_b32_sdwa v219, %[mk], v218 dst_sel:DWORD dst_unused:UNUSED_PAD src0_sel:DWORD src1_sel:WORD_1\n\t" \
  "s_waitcnt lgkmcnt(1)\n\t" \
  "v_mfma_f32_16x16x32_bf16 v[148:151], v[208:211], v[100:103], v[148:151]\n\t" \
  "ds_read_b64_tr_b16 v[214:215], %[a1] offset:11776\n\t" \
  "v_and_b32 v218, %[mk], v218\n\t" \
  "v_mfma_f32_16x16x32_bf16 v[156:159], v[208:211], v[112:115], v[156:159]\n\t" \
  "ds_read_b32 v217, v217 offset:0\n\t" \
  "v_and_b32_sdwa v221, %[mk], v220 dst_sel:DWORD dst_unused:UNUSED_PAD src0_sel:DWORD src1_sel:WORD_1\n\t" \
  "v_mfma_f32_16x16x32_bf16 v[164:167], v[208:211], v[124:127], v[164:167]\n\t" \
  "ds_read_b32 v216, v216 offset:0\n\t" \
  "v_and_b32 v220, %[mk], v220\n\t" \
  "v_pk_fma_f16 v233, v233, %[h2], %[h1]\n\t" \
  "v_mfma_f32_16x16x32_bf16 v[172:175], v[208:211], v[136:139], v[172:175]\n\t" \
  "ds_read_b32 v219, v219 offset:0\n\t" \
  "v_pk_mul_f16 v233, v232, v233\n\t" \
  "v_fma_f32 v197, |v197|, v225, v197 mul:4\n\t" \
  "s_waitcnt lgkmcnt(3)\n\t" \
  "v_mfma_f32_16x16x32_bf16 v[148:151], v[212:215], v[104:107], v[148:151]\n\t" \
  "ds_read_b32 v218, v218 offset:0\n\t" \
  "v_fma_f32 v196, |v196|, v224, v196 mul:4\n\t" \
  "v_fma_f32 v199, |v199|, v227, v199 mul:4\n\t" \
  "v_mfma_f32_16x16x32_bf16 v[156:159], v[212:215], v[116:119], v[156:159]\n\t" \
  "ds_read_b32 v221, v221 offset:0\n\t" \
  "v_fma_f32 v198, |v198|, v226, v198 mul:4\n\t" \
  "v_fma_f32 v205, |v205|, v229, v205 mul:4\n\t" \
  "v_fma_f32 v204, |v204|, v228, v204 mul:4\n\t" \
  "v_mfma_f32_16x16x32_bf16 v[164:167], v[212:215], v[128:131], v[164:167]\n\t" \
  "ds_read_b32 v220, v220 offset:0\n\t" \
  "v_fma_f32 v207, |v207|, v231, v207 mul:4\n\t" \
  "v_fma_f32 v206, |v206|, v230, v206 mul:4\n\t" \
  "v_cvt_pk_bf16_f32 v196, v196, v197\n\t" \
  "v_mfma_f32_16x16x32_bf16 v[172:175], v[212:215], v[140:143], v[172:175]\n\t" \
  "v_cvt_pk_bf16_f32 v197, v198, v199\n\t" \
  "v_cvt_pk_bf16_f32 v204, v204, v205\n\t" \
  "v_cvt_pk_bf16_f32 v205, v206, v207\n\t" \
  "s_waitcnt vmcnt(1)\n\t" \
  "s_cmp_lg_u32 %[rnd], 1\n\t" \
  "s_cbranch_scc1 .Lcnx_c41\n\t" \
  "s_waitcnt vmcnt(0)\n\t" \
  ".Lcnx_c41:\n\t" \
  "s_barrier\n\t" \
  "ds_read_b64_tr_b16 v[208:209], %[a2] offset:0\n\t" \
  "ds_read_b64_tr_b16 v[210:211], %[a2] offset:1536\n\t" \
  "ds_read_b64_tr_b16 v[212:213], %[a2] offset:8\n\t" \
  "ds_read_b64_tr_b16 v[214:215], %[a2] offset:1544\n\t" \
  "s_setprio 2\n\t" \
  "v_exp_f16 v232, v233\n\t" \
  "v_cvt_pk_f16_f32 v224, |v162|, |v163| clamp\n\t" \
  "v_mfma_f32_16x16x32_bf16 v[72:75], v[242:245], v[236:239], v[72:75]\n\t" \
  "v_cvt_pk_f16_f32 v234, v160, v161\n\t" \
  "v_cvt_pk_f16_f32 v226, |v170|, |v171| clamp\n\t" \
  "v_mfma_f32_16x16x32_bf16 v[76:79], v[240:243], v[236:239], v[76:79]\n\t" \
  "s_cmp_lg_u32 %[grp], 0\n\t" \
  "s_cbranch_scc1 .Lcnx_c39\n\t" \
  "s_add_u32 m0, %[wl], %[ws]\n\t" \
  "s_nop 0\n\t" \
  "buffer_load_dwordx4 %[wv], %[wrs], %[so0] offen lds\n\t" \
  "buffer_load_dwordx4 %[wv], %[wrs], %[so1] offen offset:1024 lds\n\t" \
  "buffer_load_dwordx4 %[wv], %[wrs], %[so2] offen offset:2048 lds\n\t" \
  "s_add_u32 %[so0], %[so0], %[wc]\n\t" \
  "s_add_u32 %[so1], %[so1], %[wc]\n\t" \
  "s_add_u32 %[so2], %[so2], %[wc]\n\t" \
  ".Lcnx_c39:\n\t" \
  "buffer_load_dwordx4 v[236:239], %[vo], %[xrs], %[oc1] offen offset:256\n\t" \
  "s_cmp_lg_u32 %[rnd], 0\n\t" \
  "s_cbranch_scc1 .Lcnx_c40\n\t" \
  "s_add_u32 m0, %[xl], 82000\n\t" \
  "s_add_u32 %[xo], %[xo], %[rs]\n\t" \
  "buffer_load_dwordx4 %[xv], %[xrs], %[xo] offen lds\n\t" \
  ".Lcnx_c40:\n\t" \
  "v_cvt_pk_f16_f32 v228, |v168|, |v169| clamp\n\t" \
  "v_exp_f16_sdwa v232, v233 dst_sel:WORD_1 dst_unused:UNUSED_PRESERVE src0_sel:WORD_1\n\t" \
  "v_pk_mul_f16 v235, v234, v234\n\t" \
  "s_waitcnt lgkmcnt(2)\n\t" \
  "v_mfma_f32_16x16x32_bf16 v[0:3], v[208:211], v[178:181], v[0:3]\n\t" \
  "v_and_b32_sdwa v225, %[mk], v224 dst_sel:DWORD dst_unused:UNUSED_PAD src0_sel:DWORD src1_sel:WORD_1\n\t" \
  "v_and_b32 v224, %[mk], v224\n\t" \
  "v_mfma_f32_16x16x32_bf16 v[24:27], v[208:211], v[186:189], v[24:27]\n\t" \
  "ds_read_b32 v225, v225 offset:0\n\t" \
  "v_and_b32_sdwa v227, %[mk], v226 dst_sel:DWORD dst_unused:UNUSED_PAD src0_sel:DWORD src1_sel:WORD_1\n\t" \
  "v_mfma_f32_16x16x32_bf16 v[48:51], v[208:211], v[194:197], v[48:51]\n\t" \
  "ds_read_b32 v224, v224 offset:0\n\t" \
  "v_and_b32 v226, %[mk], v226\n\t" \
  "v_and_b32_sdwa v229, %[mk], v228 dst_sel:DWORD dst_unused:UNUSED_PAD src0_sel:DWORD src1_sel:WORD_1\n\t" \
  "v_mfma_f32_16x16x32_bf16 v[72:75], v[208:211], v[202:205], v[72:75]\n\t" \
  "ds_read_b64_tr_b16 v[208:209], %[a2] offset:64\n\t" \
  "v_and_b32 v228, %[mk], v228\n\t" \
  "s_waitcnt lgkmcnt(3)\n\t" \
  "v_mfma_f32_16x16x32_bf16 v[4:7], v[212:215], v[178:181], v[4:7]\n\t" \
  "ds_read_b64_tr_b16 v[210:211], %[a2] offset:1600\n\t" \
  "ds_read_b32 v227, v227 offset:0\n\t" \
  "v_pk_fma_f16 v235, v235, %[h2], %[h1]\n\t" \
  "v_mfma_f32_16x16x32_bf16 v[28:31], v[212:215], v[186:189], v[28:31]\n\t" \
  "ds_read_b32 v226, v226 offset:0\n\t" \
  "v_pk_mul_f16 v235, v234, v235\n\t" \
  "v_fma_f32 v147, |v147|, v217, v147 mul:4\n\t" \
  "v_fma_f32 v146, |v146|, v216, v146 mul:4\n\t" \
  "v_mfma_f32_16x16x32_bf16 v[52:55], v[212:215], v[194:197], v[52:55]\n\t" \
  "ds_read_b32 v229, v229 offset:0\n\t" \
  "v_fma_f32 v155, |v155|, v219, v155 mul:4\n\t" \
  "v_fma_f32 v154, |v154|, v218, v154 mul:4\n\t" \
  "v_fma_f32 v153, |v153|, v221, v153 mul:4\n\t" \
  "v_mfma_f32_16x16x32_bf16 v[76:79], v[212:215], v[202:205], v[76:79]\n\t" \
  "ds_read_b64_tr_b16 v[212:213], %[a2] offset:72\n\t" \
  "v_fma_f32 v152, |v152|, v220, v152 mul:4\n\t" \
  "v_pk_fma_f16 v232, v232, %[hh], %[hh]\n\t" \
  "s_waitcnt lgkmcnt(4)\n\t" \
  "v_mfma_f32_16x16x32_bf16 v[8:11], v[208:211], v[178:181], v[8:11]\n\t" \
  "ds_read_b64_tr_b16 v[214:215], %[a2] offset:1608\n\t" \
  "v_cvt_pk_bf16_f32 v147, v146, v147\n\t" \
  "v_cvt_pk_bf16_f32 v155, v154, v155\n\t" \
  "v_cvt_pk_bf16_f32 v154, v152, v153\n\t" \
  "v_mfma_f32_16x16x32_bf16 v[32:35], v[208:211], v[186:189], v[32:35]\n\t" \
  "ds_read_b32 v228, v228 offset:0\n\t" \
  "v_rcp_f16 v233, v232\n\t" \
  "v_exp_f16 v234, v235\n\t" \
  "v_cvt_pk_f16_f32 v216, |v148|, |v149| clamp\n\t" \
  "v_mfma_f32_16x16x32_bf16 v[56:59], v[208:211], v[194:197], v[56:59]\n\t" \
  "s_setprio 2\n\t" \
  "v_cvt_pk_f16_f32 v218, |v150|, |v151| clamp\n\t" \
  "v_cvt_pk_f16_f32 v220, |v156|, |v157| clamp\n\t" \
  "v_mfma_f32_16x16x32_bf16 v[80:83], v[208:211], v[202:205], v[80:83]\n\t" \
  "ds_read_b64_tr_b16 v[208:209], %[a2] offset:128\n\t" \
  "v_cvt_pk_f16_f32 v222, |v158|, |v159| clamp\n\t" \
  "v_rcp_f16_sdwa v232, v232 dst_sel:DWORD dst_unused:UNUSED_PAD src0_sel:WORD_1\n\t" \
  "v_exp_f16_sdwa v234, v235 dst_sel:WORD_1 dst_unused:UNUSED_PRESERVE src0_sel:WORD_1\n\t" \
  "s_waitcnt lgkmcnt(2)\n\t" \
  "v_mfma_f32_16x16x32_bf16 v[12:15], v[212:215], v[178:181], v[12:15]\n\t" \
  "ds_read_b64_tr_b16 v[210:211], %[a2] offset:1664\n\t" \
  "v_and_b32_sdwa v217, %[mk], v216 dst_sel:DWORD dst_unused:UNUSED_PAD src0_sel:DWORD src1_sel:WORD_1\n\t" \
  "v_and_b32 v216, %[mk], v216\n\t" \
  "v_mfma_f32_16x16x32_bf16 v[36:39], v[212:215], v[186:189], v[36:39]\n\t" \
  "ds_read_b32 v217, v217 offset:0\n\t" \
  "v_and_b32_sdwa v219, %[mk], v218 dst_sel:DWORD dst_unused:UNUSED_PAD src0_sel:DWORD src1_sel:WORD_1\n\t" \
  "v_mfma_f32_16x16x32_bf16 v[60:63], v[212:215], v[194:197], v[60:63]\n\t" \
  "ds_read_b32 v216, v216 offset:0\n\t" \
  "v_and_b32 v218, %[mk], v218\n\t" \
  "v_and_b32_sdwa v221, %[mk], v220 dst_sel:DWORD dst_unused:UNUSED_PAD src0_sel:DWORD src1_sel:WORD_1\n\t" \
  "v_mfma_f32_16x16x32_bf16 v[84:87], v[212:215], v[202:205], v[84:87]\n\t" \
  "ds_read_b64_tr_b16 v[212:213], %[a2] offset:136\n\t" \
  "v_and_b32 v220, %[mk], v220\n\t" \
  "s_waitcnt lgkmcnt(3)\n\t" \
  "v_mfma_f32_16x16x32_bf16 v[16:19], v[208:211], v[178:181], v[16:19]\n\t" \
  "ds_read_b64_tr_b16 v[214:215], %[a2] offset:1672\n\t" \
  "ds_read_b32 v219, v219 offset:0\n\t" \
  "v_and_b32_sdwa v223, %[mk], v222 dst_sel:DWORD dst_unused:UNUSED_PAD src0_sel:DWORD src1_sel:WORD_1\n\t" \
  "v_mfma_f32_16x16x32_bf16 v[40:43], v[208:211], v[186:189], v[40:43]\n\t" \
  "ds_read_b32 v218, v218 offset:0\n\t" \
  "v_and_b32 v222, %[mk], v222\n\t" \
  "v_fma_f32 v163, |v163|, v225, v163 mul:4\n\t" \
  "v_mfma_f32_16x16x32_bf16 v[64:67], v[208:211], v[194:197], v[64:67]\n\t" \
  "ds_read_b32 v221, v221 offset:0\n\t" \
  "v_fma_f32 v162, |v162|, v224, v162 mul:4\n\t" \
  "v_fma_f32 v171, |v171|, v227, v171 mul:4\n\t" \
  "v_mfma_f32_16x16x32_bf16 v[88:91], v[208:211], v[202:205], v[88:91]\n\t" \
  "ds_read_b64_tr_b16 v[208:209], %[a1] offset:0\n\t" \
  "ds_read_b64_tr_b16 v[210:211], %[a1] offset:1024\n\t" \
  "v_fma_f32 v170, |v170|, v226, v170 mul:4\n\t" \
  "v_fma_f32 v169, |v169|, v229, v169 mul:4\n\t" \
  "s_waitcnt lgkmcnt(5)\n\t" \
  "v_mfma_f32_16x16x32_bf16 v[20:23], v[212:215], v[178:181], v[20:23]\n\t" \
  "ds_read_b32 v220, v220 offset:0\n\t" \
  "v_fma_f32 v168, |v168|, v228, v168 mul:4\n\t" \
  "v_fma_mix_f32 v144, v144, v233, 0 op_sel_hi:[0,1,0]\n\t" \
  "v_fma_mix_f32 v145, v145, v232, 0 op_sel_hi:[0,1,0]\n\t" \
  "v_mfma_f32_16x16x32_bf16 v[44:47], v[212:215], v[186:189], v[44:47]\n\t" \
  "ds_read_b32 v223, v223 offset:0\n\t" \
  "v_pk_fma_f16 v234, v234, %[hh], %[hh]\n\t" \
  "v_cvt_pk_bf16_f32 v146, v144, v145\n\t" \
  "v_cvt_pk_bf16_f32 v163, v162, v163\n\t" \
  "v_mfma_f32_16x16x32_bf16 v[68:71], v[212:215], v[194:197], v[68:71]\n\t" \
  "ds_read_b32 v222, v222 offset:0\n\t" \
  "v_cvt_pk_bf16_f32 v171, v170, v171\n\t" \
  "v_cvt_pk_bf16_f32 v170, v168, v169\n\t" \
  "s_setprio 1\n\t" \
  "v_rcp_f16 v235, v234\n\t" \
  "v_mfma_f32_16x16x32_bf16 v[92:95], v[212:215], v[202:205], v[92:95]\n\t" \
  "ds_read_b64_tr_b16 v[212:213], %[a1] offset:3072\n\t" \
  "v_cvt_pk_f16_f32 v224, |v164|, |v165| clamp\n\t" \
  "v_cvt_pk_f16_f32 v226, |v166|, |v167| clamp\n\t" \
  "s_waitcnt lgkmcnt(4)\n\t" \
  "v_mfma_f32_16x16x32_bf16 v[176:179], v[208:211], v[96:99], 0\n\t" \
  "ds_read_b64_tr_b16 v[214:215], %[a1] offset:4096\n\t" \
  "v_cvt_pk_f16_f32 v228, |v172|, |v173| clamp\n\t" \
  "v_cvt_pk_f16_f32 v230, |v174|, |v175| clamp\n\t" \
  "v_rcp_f16_sdwa v234, v234 dst_sel:DWORD dst_unused:UNUSED_PAD src0_sel:WORD_1\n\t" \
  "v_mfma_f32_16x16x32_bf16 v[184:187], v[208:211], v[108:111], 0\n\t" \
  "v_and_b32_sdwa v225, %[mk], v224 dst_sel:DWORD dst_unused:UNUSED_PAD src0_sel:DWORD src1_sel:WORD_1\n\t" \
  "v_and_b32 v224, %[mk], v224\n\t" \
  "v_mfma_f32_16x16x32_bf16 v[192:195], v[208:211], v[120:123], 0\n\t" \
  "ds_read_b32 v225, v225 offset:0\n\t" \
  "v_and_b32_sdwa v227, %[mk], v226 dst_sel:DWORD dst_unused:UNUSED_PAD src0_sel:DWORD src1_sel:WORD_1\n\t" \
  "v_mfma_f32_16x16x32_bf16 v[200:203], v[208:211], v[132:135], 0\n\t" \
  "ds_read_b64_tr_b16 v[208:209], %[a1] offset:2048\n\t" \
  "v_and_b32 v226, %[mk], v226\n\t" \
  "v_and_b32_sdwa v229, %[mk], v228 dst_sel:DWORD dst_unused:UNUSED_PAD src0_sel:DWORD src1_sel:WORD_1\n\t" \
  "s_waitcnt lgkmcnt(2)\n\t" \
  "v_mfma_f32_16x16x32_bf16 v[176:179], v[212:215], v[100:103], v[176:179]\n\t" \
  "ds_read_b64_tr_b16 v[210:211], %[a1] offset:5120\n\t" \
  "v_and_b32 v228, %[mk], v228\n\t" \
  "v_mfma_f32_16x16x32_bf16 v[184:187], v[212:215], v[112:115], v[184:187]\n\t" \
  "ds_read_b32 v224, v224 offset:0\n\t" \
  "ds_read_b32 v227, v227 offset:0\n\t" \
  "v_and_b32_sdwa v231, %[mk], v230 dst_sel:DWORD dst_unused:UNUSED_PAD src0_sel:DWORD src1_sel:WORD_1\n\t" \
  "v_mfma_f32_16x16x32_bf16 v[192:195], v[212:215], v[124:127], v[192:195]\n\t" \
  "ds_read_b32 v226, v226 offset:0\n\t" \
  "v_and_b32 v230, %[mk], v230\n\t" \
  "v_fma_f32 v149, |v149|, v217, v149 mul:4\n\t" \
  "v_mfma_f32_16x16x32_bf16 v[200:203], v[212:215], v[136:139], v[200:203]\n\t" \
  "ds_read_b64_tr_b16 v[212:213], %[a1] offset:512\n\t" \
  "ds_read_b64_tr_b16 v[214:215], %[a1] offset:1536\n\t" \
  "v_fma_f32 v148, |v148|, v216, v148 mul:4\n\t" \
  "v_fma_f32 v151, |v151|, v219, v151 mul:4\n\t" \
  "s_waitcnt lgkmcnt(5)\n\t" \
  "v_mfma_f32_16x16x32_bf16 v[176:179], v[208:211], v[104:107], v[176:179]\n\t" \
  "ds_read_b32 v229, v229 offset:0\n\t" \
  "ds_read_b32 v228, v228 offset:0\n\t" \
  "v_fma_f32 v150, |v150|, v218, v150 mul:4\n\t" \
  "v_fma_f32 v157, |v157|, v221, v157 mul:4\n\t" \
  "v_mfma_f32_16x16x32_bf16 v[184:187], v[208:211], v[116:119], v[184:187]\n\t" \
  "ds_read_b32 v231, v231 offset:0\n\t" \
  "v_fma_f32 v156, |v156|, v220, v156 mul:4\n\t" \
  "v_fma_f32 v159, |v159|, v223, v159 mul:4\n\t" \
  "v_fma_f32 v158, |v158|, v222, v158 mul:4\n\t" \
  "v_mfma_f32_16x16x32_bf16 v[192:195], v[208:211], v[128:131], v[192:195]\n\t" \
  "ds_read_b32 v230, v230 offset:0\n\t" \
  "v_fma_mix_f32 v160, v160, v235, 0 op_sel_hi:[0,1,0]\n\t" \
  "v_fma_mix_f32 v161, v161, v234, 0 op_sel_hi:[0,1,0]\n\t" \
  "v_cvt_pk_bf16_f32 v162, v160, v161\n\t" \
  "s_setprio 0\n\t" \
  "v_mfma_f32_16x16x32_bf16 v[200:203], v[208:211], v[140:143], v[200:203]\n\t" \
  "ds_read_b64_tr_b16 v[208:209], %[a1] offset:3584\n\t" \
  "v_cvt_pk_bf16_f32 v148, v148, v149\n\t" \
  "v_cvt_pk_bf16_f32 v149, v150, v151\n\t" \
  "s_waitcnt lgkmcnt(5)\n\t" \
  "v_mfma_f32_16x16x32_bf16 v[180:183], v[212:215], v[96:99], 0\n\t" \
  "ds_read_b64_tr_b16 v[210:211], %[a1] offset:4608\n\t" \
  "v_cvt_pk_bf16_f32 v156, v156, v157\n\t" \
  "v_cvt_pk_bf16_f32 v157, v158, v159\n\t" \
  "v_cvt_pk_f16_f32 v216, |v178|, |v179| clamp\n\t" \
  "v_mfma_f32_16x16x32_bf16 v[188:191], v[212:215], v[108:111], 0\n\t" \
  "v_cvt_pk_f16_f32 v232, v176, v177\n\t" \
  "v_cvt_pk_f16_f32 v218, |v186|, |v187| clamp\n\t" \
  "v_cvt_pk_f16_f32 v220, |v184|, |v185| clamp\n\t" \
  "v_mfma_f32_16x16x32_bf16 v[196:199], v[212:215], v[120:123], 0\n\t" \
  "v_pk_mul_f16 v233, v232, v232\n\t" \
  "v_and_b32_sdwa v217, %[mk], v216 dst_sel:DWORD dst_unused:UNUSED_PAD src0_sel:DWORD src1_sel:WORD_1\n\t" \
  "v_mfma_f32_16x16x32_bf16 v[204:207], v[212:215], v[132:135], 0\n\t" \
  "ds_read_b64_tr_b16 v[212:213], %[a1] offset:2560\n\t" \
  "v_and_b32 v216, %[mk], v216\n\t" \
  "v_and_b32_sdwa v219, %[mk], v218 dst_sel:DWORD dst_unused:UNUSED_PAD src0_sel:DWORD src1_sel:WORD_1\n\t" \
  "s_waitcnt lgkmcnt(1)\n\t" \
  "v_mfma_f32_16x16x32_bf16 v[180:183], v[208:211], v[100:103], v[180:183]\n\t" \
  "ds_read_b64_tr_b16 v[214:215], %[a1] offset:5632\n\t" \
  "v_and_b32 v218, %[mk], v218\n\t" \
  "v_mfma_f32_16x16x32_bf16 v[188:191], v[208:211], v[112:115], v[188:191]\n\t" \
  "ds_read_b32 v217, v217 offset:0\n\t" \
  "v_and_b32_sdwa v221, %[mk], v220 dst_sel:DWORD dst_unused:UNUSED_PAD src0_sel:DWORD src1_sel:WORD_1\n\t" \
  "v_mfma_f32_16x16x32_bf16 v[196:199], v[208:211], v[124:127], v[196:199]\n\t" \
  "ds_read_b32 v216, v216 offset:0\n\t" \
  "v_and_b32 v220, %[mk], v220\n\t" \
  "v_pk_fma_f16 v233, v233, %[h2], %[h1]\n\t" \
  "v_mfma_f32_16x16x32_bf16 v[204:207], v[208:211], v[136:139], v[204:207]\n\t" \
  "ds_read_b32 v219, v219 offset:0\n\t" \
  "v_pk_mul_f16 v233, v232, v233\n\t" \
  "v_fma_f32 v165, |v165|, v225, v165 mul:4\n\t" \
  "s_waitcnt lgkmcnt(3)\n\t" \
  "v_mfma_f32_16x16x32_bf16 v[180:183], v[212:215], v[104:107], v[180:183]\n\t" \
  "ds_read_b32 v218, v218 offset:0\n\t" \
  "v_fma_f32 v164, |v164|, v224, v164 mul:4\n\t" \
  "v_fma_f32 v167, |v167|, v227, v167 mul:4\n\t" \
  "v_mfma_f32_16x16x32_bf16 v[188:191], v[212:215], v[116:119], v[188:191]\n\t" \
  "ds_read_b32 v221, v221 offset:0\n\t" \
  "v_fma_f32 v166, |v166|, v226, v166 mul:4\n\t" \
  "v_fma_f32 v173, |v173|, v229, v173 mul:4\n\t" \
  "v_fma_f32 v172, |v172|, v228, v172 mul:4\n\t" \
  "v_mfma_f32_16x16x32_bf16 v[196:199], v[212:215], v[128:131], v[196:199]\n\t" \
  "ds_read_b32 v220, v220 offset:0\n\t" \
  "v_fma_f32 v175, |v175|, v231, v175 mul:4\n\t" \
  "v_fma_f32 v174, |v174|, v230, v174 mul:4\n\t" \
  "v_cvt_pk_bf16_f32 v164, v164, v165\n\t" \
  "v_mfma_f32_16x16x32_bf16 v[204:207], v[212:215], v[140:143], v[204:207]\n\t" \
  "v_cvt_pk_bf16_f32 v165, v166, v167\n\t" \
  "v_cvt_pk_bf16_f32 v172, v172, v173\n\t" \
  "v_cvt_pk_bf16_f32 v173, v174, v175\n\t" \
  "s_waitcnt vmcnt(1)\n\t" \
  "s_cmp_lg_u32 %[rnd], 1\n\t" \
  "s_cbranch_scc1 .Lcnx_c44\n\t" \
  "s_waitcnt vmcnt(0)\n\t" \
  ".Lcnx_c44:\n\t" \
  "s_barrier\n\t" \
  "ds_read_b64_tr_b16 v[208:209], %[a2] offset:6160\n\t" \
  "ds_read_b64_tr_b16 v[210:211], %[a2] offset:7696\n\t" \
  "ds_read_b64_tr_b16 v[212:213], %[a2] offset:6168\n\t" \
  "ds_read_b64_tr_b16 v[214:215], %[a2] offset:7704\n\t" \
  "s_setprio 2\n\t" \
  "v_exp_f16 v232, v233\n\t" \
  "v_cvt_pk_f16_f32 v224, |v194|, |v195| clamp\n\t" \
  "v_mfma_f32_16x16x32_bf16 v[80:83], v[242:245], v[236:239], v[80:83]\n\t" \
  "v_cvt_pk_f16_f32 v234, v192, v193\n\t" \
  "v_cvt_pk_f16_f32 v226, |v202|, |v203| clamp\n\t" \
  "v_cvt_pk_f16_f32 v228, |v200|, |v201| clamp\n\t" \
  "v_exp_f16_sdwa v232, v233 dst_sel:WORD_1 dst_unused:UNUSED_PRESERVE src0_sel:WORD_1\n\t" \
  "v_mfma_f32_16x16x32_bf16 v[84:87], v[240:243], v[236:239], v[84:87]\n\t" \
  "s_cmp_lg_u32 %[grp], 0\n\t" \
  "s_cbranch_scc1 .Lcnx_c42\n\t" \
  "s_add_u32 m0, %[wl], 0\n\t" \
  "s_nop 0\n\t" \
  "buffer_load_dwordx4 %[wv], %[wrs], %[so0] offen lds\n\t" \
  "buffer_load_dwordx4 %[wv], %[wrs], %[so1] offen offset:1024 lds\n\t" \
  "buffer_load_dwordx4 %[wv], %[wrs], %[so2] offen offset:2048 lds\n\t" \
  "s_add_u32 %[so0], %[so0], %[wc]\n\t" \
  "s_add_u32 %[so1], %[so1], %[wc]\n\t" \
  "s_add_u32 %[so2], %[so2], %[wc]\n\t" \
  ".Lcnx_c42:\n\t" \
  "s_sub_u32 %[so0], %[so0], %[wr2]\n\t" \
  "s_sub_u32 %[so1], %[so1], %[wr2]\n\t" \
  "s_sub_u32 %[so2], %[so2], %[wr2]\n\t" \
  "buffer_load_dwordx4 v[236:239], %[vo], %[xrs], %[oc1] offen offset:320\n\t" \
  "s_cmp_lg_u32 %[rnd], 0\n\t" \
  "s_cbranch_scc1 .Lcnx_c43\n\t" \
  "s_add_u32 m0, %[xl], 90200\n\t" \
  "s_add_u32 %[xo], %[xo], %[rs]\n\t" \
  "buffer_load_dwordx4 %[xv], %[xrs], %[xo] offen lds\n\t" \
  ".Lcnx_c43:\n\t" \
  "v_pk_mul_f16 v235, v234, v234\n\t" \
  "v_and_b32_sdwa v225, %[mk], v224 dst_sel:DWORD dst_unused:UNUSED_PAD src0_sel:DWORD src1_sel:WORD_1\n\t" \
  "v_and_b32 v224, %[mk], v224\n\t" \
  "s_waitcnt lgkmcnt(2)\n\t" \
  "v_mfma_f32_16x16x32_bf16 v[0:3], v[208:211], v[146:149], v[0:3]\n\t" \
  "ds_read_b32 v225, v225 offset:0\n\t" \
  "v_and_b32_sdwa v227, %[mk], v226 dst_sel:DWORD dst_unused:UNUSED_PAD src0_sel:DWORD src1_sel:WORD_1\n\t" \
  "v_and_b32 v226, %[mk], v226\n\t" \
  "v_and_b32_sdwa v229, %[mk], v228 dst_sel:DWORD dst_unused:UNUSED_PAD src0_sel:DWORD src1_sel:WORD_1\n\t" \
  "v_mfma_f32_16x16x32_bf16 v[24:27], v[208:211], v[154:157], v[24:27]\n\t" \
  "ds_read_b32 v224, v224 offset:0\n\t" \
  "v_and_b32 v228, %[mk], v228\n\t" \
  "v_pk_fma_f16 v235, v235, %[h2], %[h1]\n\t" \
  "v_mfma_f32_16x16x32_bf16 v[48:51], v[208:211], v[162:165], v[48:51]\n\t" \
  "ds_read_b32 v227, v227 offset:0\n\t" \
  "v_pk_mul_f16 v235, v234, v235\n\t" \
  "v_fma_f32 v179, |v179|, v217, v179 mul:4\n\t" \
  "v_fma_f32 v178, |v178|, v216, v178 mul:4\n\t" \
  "v_fma_f32 v187, |v187|, v219, v187 mul:4\n\t" \
  "v_mfma_f32_16x16x32_bf16 v[72:75], v[208:211], v[170:173], v[72:75]\n\t" \
  "ds_read_b64_tr_b16 v[208:209], %[a2] offset:6224\n\t" \
  "ds_read_b64_tr_b16 v[210:211], %[a2] offset:7760\n\t" \
  "v_fma_f32 v186, |v186|, v218, v186 mul:4\n\t" \
  "v_fma_f32 v185, |v185|, v221, v185 mul:4\n\t" \
  "v_fma_f32 v184, |v184|, v220, v184 mul:4\n\t" \
  "v_pk_fma_f16 v232, v232, %[hh], %[hh]\n\t" \
  "s_waitcnt lgkmcnt(5)\n\t" \
  "v_mfma_f32_16x16x32_bf16 v[4:7], v[212:215], v[146:149], v[4:7]\n\t" \
  "ds_read_b32 v226, v226 offset:0\n\t" \
  "v_cvt_pk_bf16_f32 v179, v178, v179\n\t" \
  "v_cvt_pk_bf16_f32 v187, v186, v187\n\t" \
  "v_cvt_pk_bf16_f32 v186, v184, v185\n\t" \
  "s_setprio 2\n\t" \
  "v_rcp_f16 v233, v232\n\t" \
  "v_mfma_f32_16x16x32_bf16 v[28:31], v[212:215], v[154:157], v[28:31]\n\t" \
  "ds_read_b32 v229, v229 offset:0\n\t" \
  "v_exp_f16 v234, v235\n\t" \
  "v_cvt_pk_f16_f32 v216, |v180|, |v181| clamp\n\t" \
  "v_cvt_pk_f16_f32 v218, |v182|, |v183| clamp\n\t" \
  "v_cvt_pk_f16_f32 v220, |v188|, |v189| clamp\n\t" \
  "v_cvt_pk_f16_f32 v222, |v190|, |v191| clamp\n\t" \
  "v_mfma_f32_16x16x32_bf16 v[52:55], v[212:215], v[162:165], v[52:55]\n\t" \
  "ds_read_b32 v228, v228 offset:0\n\t" \
  "v_rcp_f16_sdwa v232, v232 dst_sel:DWORD dst_unused:UNUSED_PAD src0_sel:WORD_1\n\t" \
  "v_exp_f16_sdwa v234, v235 dst_sel:WORD_1 dst_unused:UNUSED_PRESERVE src0_sel:WORD_1\n\t" \
  "v_and_b32_sdwa v217, %[mk], v216 dst_sel:DWORD dst_unused:UNUSED_PAD src0_sel:DWORD src1_sel:WORD_1\n\t" \
  "v_and_b32 v216, %[mk], v216\n\t" \
  "v_mfma_f32_16x16x32_bf16 v[76:79], v[212:215], v[170:173], v[76:79]\n\t" \
  "ds_read_b64_tr_b16 v[212:213], %[a2] offset:6232\n\t" \
  "v_and_b32_sdwa v219, %[mk], v218 dst_sel:DWORD dst_unused:UNUSED_PAD src0_sel:DWORD src1_sel:WORD_1\n\t" \
  "v_and_b32 v218, %[mk], v218\n\t" \
  "s_waitcnt lgkmcnt(4)\n\t" \
  "v_mfma_f32_16x16x32_bf16 v[8:11], v[208:211], v[146:149], v[8:11]\n\t" \
  "ds_read_b64_tr_b16 v[214:215], %[a2] offset:7768\n\t" \
  "ds_read_b32 v217, v217 offset:0\n\t" \
  "v_and_b32_sdwa v221, %[mk], v220 dst_sel:DWORD dst_unused:UNUSED_PAD src0_sel:DWORD src1_sel:WORD_1\n\t" \
  "v_and_b32 v220, %[mk], v220\n\t" \
  "v_mfma_f32_16x16x32_bf16 v[32:35], v[208:211], v[154:157], v[32:35]\n\t" \
  "ds_read_b32 v216, v216 offset:0\n\t" \
  "ds_read_b32 v219, v219 offset:0\n\t" \
  "v_and_b32_sdwa v223, %[mk], v222 dst_sel:DWORD dst_unused:UNUSED_PAD src0_sel:DWORD src1_sel:WORD_1\n\t" \
  "v_and_b32 v222, %[mk], v222\n\t" \
  "v_fma_f32 v195, |v195|, v225, v195 mul:4\n\t" \
  "v_mfma_f32_16x16x32_bf16 v[56:59], v[208:211], v[162:165], v[56:59]\n\t" \
  "ds_read_b32 v218, v218 offset:0\n\t" \
  "ds_read_b32 v221, v221 offset:0\n\t" \
  "v_fma_f32 v194, |v194|, v224, v194 mul:4\n\t" \
  "v_fma_f32 v203, |v203|, v227, v203 mul:4\n\t" \
  "s_waitcnt lgkmcnt(7)\n\t" \
  "v_fma_f32 v202, |v202|, v226, v202 mul:4\n\t" \
  "v_mfma_f32_16x16x32_bf16 v[80:83], v[208:211], v[170:173], v[80:83]\n\t" \
  "ds_read_b64_tr_b16 v[208:209], %[a2] offset:6288\n\t" \
  "ds_read_b64_tr_b16 v[210:211], %[a2] offset:7824\n\t" \
  "s_setprio 1\n\t" \
  "v_fma_f32 v201, |v201|, v229, v201 mul:4\n\t" \
  "v_fma_f32 v200, |v200|, v228, v200 mul:4\n\t" \
  "v_fma_mix_f32 v176, v176, v233, 0 op_sel_hi:[0,1,0]\n\t" \
  "v_fma_mix_f32 v177, v177, v232, 0 op_sel_hi:[0,1,0]\n\t" \
  "s_waitcnt lgkmcnt(7)\n\t" \
  "v_mfma_f32_16x16x32_bf16 v[12:15], v[212:215], v[146:149], v[12:15]\n\t" \
  "ds_read_b32 v220, v220 offset:0\n\t" \
  "v_pk_fma_f16 v234, v234, %[hh], %[hh]\n\t" \
  "v_cvt_pk_bf16_f32 v178, v176, v177\n\t" \
  "v_cvt_pk_bf16_f32 v195, v194, v195\n\t" \
  "v_cvt_pk_bf16_f32 v203, v202, v203\n\t" \
  "v_cvt_pk_bf16_f32 v202, v200, v201\n\t" \
  "v_mfma_f32_16x16x32_bf16 v[36:39], v[212:215], v[154:157], v[36:39]\n\t" \
  "ds_read_b32 v223, v223 offset:0\n\t" \
  "v_rcp_f16 v235, v234\n\t" \
  "v_cvt_pk_f16_f32 v224, |v196|, |v197| clamp\n\t" \
  "v_cvt_pk_f16_f32 v226, |v198|, |v199| clamp\n\t" \
  "v_cvt_pk_f16_f32 v228, |v204|, |v205| clamp\n\t" \
  "v_mfma_f32_16x16x32_bf16 v[60:63], v[212:215], v[162:165], v[60:63]\n\t" \
  "ds_read_b32 v222, v222 offset:0\n\t" \
  "v_cvt_pk_f16_f32 v230, |v206|, |v207| clamp\n\t" \
  "v_rcp_f16_sdwa v234, v234 dst_sel:DWORD dst_unused:UNUSED_PAD src0_sel:WORD_1\n\t" \
  "v_and_b32_sdwa v225, %[mk], v224 dst_sel:DWORD dst_unused:UNUSED_PAD src0_sel:DWORD src1_sel:WORD_1\n\t" \
  "v_and_b32 v224, %[mk], v224\n\t" \
  "v_mfma_f32_16x16x32_bf16 v[84:87], v[212:215], v[170:173], v[84:87]\n\t" \
  "ds_read_b64_tr_b16 v[212:213], %[a2] offset:6296\n\t" \
  "v_and_b32_sdwa v227, %[mk], v226 dst_sel:DWORD dst_unused:UNUSED_PAD src0_sel:DWORD src1_sel:WORD_1\n\t" \
  "v_and_b32 v226, %[mk], v226\n\t" \
  "s_waitcnt lgkmcnt(4)\n\t" \
  "v_mfma_f32_16x16x32_bf16 v[16:19], v[208:211], v[146:149], v[16:19]\n\t" \
  "ds_read_b64_tr_b16 v[214:215], %[a2] offset:7832\n\t" \
  "ds_read_b32 v225, v225 offset:0\n\t" \
  "v_and_b32_sdwa v229, %[mk], v228 dst_sel:DWORD dst_unused:UNUSED_PAD src0_sel:DWORD src1_sel:WORD_1\n\t" \
  "v_and_b32 v228, %[mk], v228\n\t" \
  "v_and_b32_sdwa v231, %[mk], v230 dst_sel:DWORD dst_unused:UNUSED_PAD src0_sel:DWORD src1_sel:WORD_1\n\t" \
  "v_mfma_f32_16x16x32_bf16 v[40:43], v[208:211], v[154:157], v[40:43]\n\t" \
  "ds_read_b32 v224, v224 offset:0\n\t" \
  "ds_read_b32 v227, v227 offset:0\n\t" \
  "v_and_b32 v230, %[mk], v230\n\t" \
  "v_fma_f32 v181, |v181|, v217, v181 mul:4\n\t" \
  "s_setprio 0\n\t" \
  "v_mfma_f32_16x16x32_bf16 v[64:67], v[208:211], v[162:165], v[64:67]\n\t" \
  "ds_read_b32 v226, v226 offset:0\n\t" \
  "ds_read_b32 v229, v229 offset:0\n\t" \
  "v_fma_f32 v180, |v180|, v216, v180 mul:4\n\t" \
  "v_fma_f32 v183, |v183|, v219, v183 mul:4\n\t" \
  "v_fma_f32 v182, |v182|, v218, v182 mul:4\n\t" \
  "v_mfma_f32_16x16x32_bf16 v[88:91], v[208:211], v[170:173], v[88:91]\n\t" \
  "ds_read_b32 v228, v228 offset:0\n\t" \
  "s_waitcnt lgkmcnt(8)\n\t" \
  "v_fma_f32 v189, |v189|, v221, v189 mul:4\n\t" \
  "v_fma_f32 v188, |v188|, v220, v188 mul:4\n\t" \
  "v_fma_f32 v191, |v191|, v223, v191 mul:4\n\t" \
  "v_fma_f32 v190, |v190|, v222, v190 mul:4\n\t" \
  "v_fma_mix_f32 v192, v192, v235, 0 op_sel_hi:[0,1,0]\n\t" \
  "s_waitcnt lgkmcnt(6)\n\t" \
  "v_mfma_f32_16x16x32_bf16 v[20:23], v[212:215], v[146:149], v[20:23]\n\t" \
  "ds_read_b32 v231, v231 offset:0\n\t" \
  "v_fma_mix_f32 v193, v193, v234, 0 op_sel_hi:[0,1,0]\n\t" \
  "v_cvt_pk_bf16_f32 v194, v192, v193\n\t" \
  "v_cvt_pk_bf16_f32 v180, v180, v181\n\t" \
  "v_cvt_pk_bf16_f32 v181, v182, v183\n\t" \
  "v_mfma_f32_16x16x32_bf16 v[44:47], v[212:215], v[154:157], v[44:47]\n\t" \
  "ds_read_b32 v230, v230 offset:0\n\t" \
  "v_cvt_pk_bf16_f32 v188, v188, v189\n\t" \
  "v_cvt_pk_bf16_f32 v189, v190, v191\n\t" \
  "s_waitcnt lgkmcnt(4)\n\t" \
  "v_fma_f32 v197, |v197|, v225, v197 mul:4\n\t" \
  "v_fma_f32 v196, |v196|, v224, v196 mul:4\n\t" \
  "v_fma_f32 v199, |v199|, v227, v199 mul:4\n\t" \
  "v_mfma_f32_16x16x32_bf16 v[68:71], v[212:215], v[162:165], v[68:71]\n\t" \
  "v_fma_f32 v198, |v198|, v226, v198 mul:4\n\t" \
  "s_waitcnt lgkmcnt(0)\n\t" \
  "v_fma_f32 v205, |v205|, v229, v205 mul:4\n\t" \
  "v_fma_f32 v204, |v204|, v228, v204 mul:4\n\t" \
  "v_fma_f32 v207, |v207|, v231, v207 mul:4\n\t" \
  "v_mfma_f32_16x16x32_bf16 v[92:95], v[212:215], v[170:173], v[92:95]\n\t" \
  "v_fma_f32 v206, |v206|, v230, v206 mul:4\n\t" \
  "v_cvt_pk_bf16_f32 v196, v196, v197\n\t" \
  "v_cvt_pk_bf16_f32 v197, v198, v199\n\t" \
  "v_cvt_pk_bf16_f32 v204, v204, v205\n\t" \
  "v_cvt_pk_bf16_f32 v205, v206, v207\n\t" \
  "s_waitcnt vmcnt(0)\n\t" \
  "s_barrier\n\t" \
  "s_cmp_lg_u32 %[rnd], 0\n\t" \
  "s_cbranch_scc1 .Lcnx_fin\n\t" \
  "ds_read_b64_tr_b16 v[208:209], %[a2] offset:0\n\t" \
  "ds_read_b64_tr_b16 v[210:211], %[a2] offset:1536\n\t" \
  "ds_read_b64_tr_b16 v[212:213], %[a2] offset:8\n\t" \
  "ds_read_b64_tr_b16 v[214:215], %[a2] offset:1544\n\t" \
  "v_add_u32 v232, 1536, %[aa]\n\t" \
  "v_mfma_f32_16x16x32_bf16 v[88:91], v[242:245], v[236:239], v[88:91]\n\t" \
  "ds_read2_b32 v[98:99], %[aa] offset0:0 offset1:6\n\t" \
  "v_mfma_f32_16x16x32_bf16 v[92:95], v[240:243], v[236:239], v[92:95]\n\t" \
  "ds_read2_b32 v[102:103], %[aa] offset0:12 offset1:18\n\t" \
  "s_waitcnt lgkmcnt(4)\n\t" \
  "v_mfma_f32_16x16x32_bf16 v[0:3], v[208:211], v[178:181], v[0:3]\n\t" \
  "ds_read2_b32 v[106:107], %[aa] offset0:24 offset1:30\n\t" \
  "v_mfma_f32_16x16x32_bf16 v[24:27], v[208:211], v[186:189], v[24:27]\n\t" \
  "ds_read2_b32 v[110:111], %[aa] offset0:36 offset1:42\n\t" \
  "ds_read2_b32 v[114:115], %[aa] offset0:192 offset1:198\n\t" \
  "v_mfma_f32_16x16x32_bf16 v[48:51], v[208:211], v[194:197], v[48:51]\n\t" \
  "ds_read2_b32 v[118:119], %[aa] offset0:204 offset1:210\n\t" \
  "ds_read2_b32 v[122:123], %[aa] offset0:216 offset1:222\n\t" \
  "v_mfma_f32_32x32x16_bf16 v[144:159], v[242:245], v[240:243], 0\n\t" \
  "v_mfma_f32_16x16x32_bf16 v[72:75], v[208:211], v[202:205], v[72:75]\n\t" \
  "ds_read_b64_tr_b16 v[208:209], %[a2] offset:64\n\t" \
  "ds_read_b64_tr_b16 v[210:211], %[a2] offset:1600\n\t" \
  "v_mfma_f32_32x32x16_bf16 v[160:175], v[242:245], v[240:243], 0\n\t" \
  "s_waitcnt lgkmcnt(8)\n\t" \
  "v_and_b32 v96, 0xffff, v98\n\t" \
  "v_mfma_f32_16x16x32_bf16 v[4:7], v[212:215], v[178:181], v[4:7]\n\t" \
  "ds_read2_b32 v[126:127], %[aa] offset0:228 offset1:234\n\t" \
  "ds_read2_b32 v[130:131], v232 offset0:0 offset1:6\n\t" \
  "v_and_b32 v97, 0xffff0000, v98\n\t" \
  "v_and_b32 v98, 0xffff, v99\n\t" \
  "v_mfma_f32_16x16x32_bf16 v[28:31], v[212:215], v[186:189], v[28:31]\n\t" \
  "ds_read2_b32 v[134:135], v232 offset0:12 offset1:18\n\t" \
  "v_and_b32 v99, 0xffff0000, v99\n\t" \
  "s_waitcnt lgkmcnt(10)\n\t" \
  "v_and_b32 v100, 0xffff, v102\n\t" \
  "v_and_b32 v101, 0xffff0000, v102\n\t" \
  "v_mfma_f32_16x16x32_bf16 v[52:55], v[212:215], v[194:197], v[52:55]\n\t" \
  "ds_read2_b32 v[138:139], v232 offset0:24 offset1:30\n\t" \
  "v_and_b32 v102, 0xffff, v103\n\t" \
  "v_and_b32 v103, 0xffff0000, v103\n\t" \
  "v_mfma_f32_16x16x32_bf16 v[76:79], v[212:215], v[202:205], v[76:79]\n\t" \
  "ds_read_b64_tr_b16 v[212:213], %[a2] offset:72\n\t" \
  "s_waitcnt lgkmcnt(11)\n\t" \
  "v_and_b32 v104, 0xffff, v106\n\t" \
  "v_and_b32 v105, 0xffff0000, v106\n\t" \
  "v_and_b32 v106, 0xffff, v107\n\t" \
  "s_waitcnt lgkmcnt(5)\n\t" \
  "v_mfma_f32_16x16x32_bf16 v[8:11], v[208:211], v[178:181], v[8:11]\n\t" \
  "ds_read_b64_tr_b16 v[214:215], %[a2] offset:1608\n\t" \
  "v_and_b32 v107, 0xffff0000, v107\n\t" \
  "v_and_b32 v108, 0xffff, v110\n\t" \
  "v_mfma_f32_16x16x32_bf16 v[32:35], v[208:211], v[186:189], v[32:35]\n\t" \
  "ds_read2_b32 v[142:143], v232 offset0:36 offset1:42\n\t" \
  "v_and_b32 v109, 0xffff0000, v110\n\t" \
  "v_and_b32 v110, 0xffff, v111\n\t" \
  "v_and_b32 v111, 0xffff0000, v111\n\t" \
  "v_mfma_f32_16x16x32_bf16 v[56:59], v[208:211], v[194:197], v[56:59]\n\t" \
  "v_and_b32 v112, 0xffff, v114\n\t" \
  "v_and_b32 v113, 0xffff0000, v114\n\t" \
  "v_mfma_f32_16x16x32_bf16 v[80:83], v[208:211], v[202:205], v[80:83]\n\t" \
  "ds_read_b64_tr_b16 v[208:209], %[a2] offset:128\n\t" \
  "v_and_b32 v114, 0xffff, v115\n\t" \
  "v_and_b32 v115, 0xffff0000, v115\n\t" \
  "s_waitcnt lgkmcnt(2)\n\t" \
  "v_mfma_f32_16x16x32_bf16 v[12:15], v[212:215], v[178:181], v[12:15]\n\t" \
  "ds_read_b64_tr_b16 v[210:211], %[a2] offset:1664\n\t" \
  "v_and_b32 v116, 0xffff, v118\n\t" \
  "v_and_b32 v117, 0xffff0000, v118\n\t" \
  "v_and_b32 v118, 0xffff, v119\n\t" \
  "v_mfma_f32_16x16x32_bf16 v[36:39], v[212:215], v[186:189], v[36:39]\n\t" \
  "v_and_b32 v119, 0xffff0000, v119\n\t" \
  "v_and_b32 v120, 0xffff, v122\n\t" \
  "v_mfma_f32_16x16x32_bf16 v[60:63], v[212:215], v[194:197], v[60:63]\n\t" \
  "v_and_b32 v121, 0xffff0000, v122\n\t" \
  "v_and_b32 v122, 0xffff, v123\n\t" \
  "v_and_b32 v123, 0xffff0000, v123\n\t" \
  "v_mfma_f32_16x16x32_bf16 v[84:87], v[212:215], v[202:205], v[84:87]\n\t" \
  "ds_read_b64_tr_b16 v[212:213], %[a2] offset:136\n\t" \
  "v_and_b32 v124, 0xffff, v126\n\t" \
  "v_and_b32 v125, 0xffff0000, v126\n\t" \
  "s_waitcnt lgkmcnt(1)\n\t" \
  "v_mfma_f32_16x16x32_bf16 v[16:19], v[208:211], v[178:181], v[16:19]\n\t" \
  "ds_read_b64_tr_b16 v[214:215], %[a2] offset:1672\n\t" \
  "v_and_b32 v126, 0xffff, v127\n\t" \
  "v_and_b32 v127, 0xffff0000, v127\n\t" \
  "v_and_b32 v128, 0xffff, v130\n\t" \
  "v_mfma_f32_16x16x32_bf16 v[40:43], v[208:211], v[186:189], v[40:43]\n\t" \
  "v_and_b32 v129, 0xffff0000, v130\n\t" \
  "v_and_b32 v130, 0xffff, v131\n\t" \
  "v_mfma_f32_16x16x32_bf16 v[64:67], v[208:211], v[194:197], v[64:67]\n\t" \
  "v_and_b32 v131, 0xffff0000, v131\n\t" \
  "v_and_b32 v132, 0xffff, v134\n\t" \
  "v_mfma_f32_16x16x32_bf16 v[88:91], v[208:211], v[202:205], v[88:91]\n\t" \
  "v_and_b32 v133, 0xffff0000, v134\n\t" \
  "v_and_b32 v134, 0xffff, v135\n\t" \
  "v_and_b32 v135, 0xffff0000, v135\n\t" \
  "s_waitcnt lgkmcnt(0)\n\t" \
  "v_mfma_f32_16x16x32_bf16 v[20:23], v[212:215], v[178:181], v[20:23]\n\t" \
  "v_and_b32 v136, 0xffff, v138\n\t" \
  "v_and_b32 v137, 0xffff0000, v138\n\t" \
  "v_mfma_f32_16x16x32_bf16 v[44:47], v[212:215], v[186:189], v[44:47]\n\t" \
  "v_and_b32 v138, 0xffff, v139\n\t" \
  "v_and_b32 v139, 0xffff0000, v139\n\t" \
  "v_and_b32 v140, 0xffff, v142\n\t" \
  "v_mfma_f32_16x16x32_bf16 v[68:71], v[212:215], v[194:197], v[68:71]\n\t" \
  "v_and_b32 v141, 0xffff0000, v142\n\t" \
  "v_and_b32 v142, 0xffff, v143\n\t" \
  "v_mfma_f32_16x16x32_bf16 v[92:95], v[212:215], v[202:205], v[92:95]\n\t" \
  "v_and_b32 v143, 0xffff0000, v143\n\t" \
  "v_bfe_u32 v236, %[l8], 3, 2\n\t" \
  "v_mul_u32_u24 v236, 0x5555, v236\n\t" \
  "s_nop 7\n\t" \
  "s_mov_b32 %[rnd], 1\n\t" \
  "s_mov_b32 %[f0], 2\n\t" \
  "s_mov_b32 %[op0], %[oc0]\n\t" \
  "s_mov_b32 %[op1], %[oc1]\n\t" \
  "s_add_u32 %[oc0], %[oc0], %[dy]\n\t" \
  "s_add_u32 %[oc1], %[oc1], %[dy]\n\t" \
  "s_branch .Lcnx_round\n\t" \
  ".Lcnx_fin:\n\t" \
  "ds_read_b64_tr_b16 v[208:209], %[a2] offset:0\n\t" \
  "ds_read_b64_tr_b16 v[210:211], %[a2] offset:1536\n\t" \
  "ds_read_b64_tr_b16 v[212:213], %[a2] offset:8\n\t" \
  "ds_read_b64_tr_b16 v[214:215], %[a2] offset:1544\n\t" \
  "v_mfma_f32_16x16x32_bf16 v[88:91], v[242:245], v[236:239], v[88:91]\n\t" \
  "v_mfma_f32_16x16x32_bf16 v[92:95], v[240:243], v[236:239], v[92:95]\n\t" \
  "s_waitcnt lgkmcnt(2)\n\t" \
  "v_mfma_f32_16x16x32_bf16 v[0:3], v[208:211], v[178:181], v[0:3]\n\t" \
  "v_mfma_f32_16x16x32_bf16 v[24:27], v[208:211], v[186:189], v[24:27]\n\t" \
  "v_mfma_f32_16x16x32_bf16 v[48:51], v[208:211], v[194:197], v[48:51]\n\t" \
  "v_mfma_f32_16x16x32_bf16 v[72:75], v[208:211], v[202:205], v[72:75]\n\t" \
  "ds_read_b64_tr_b16 v[208:209], %[a2] offset:64\n\t" \
  "s_waitcnt lgkmcnt(1)\n\t" \
  "v_mfma_f32_16x16x32_bf16 v[4:7], v[212:215], v[178:181], v[4:7]\n\t" \
  "ds_read_b64_tr_b16 v[210:211], %[a2] offset:1600\n\t" \
  "v_mfma_f32_16x16x32_bf16 v[28:31], v[212:215], v[186:189], v[28:31]\n\t" \
  "v_mfma_f32_16x16x32_bf16 v[52:55], v[212:215], v[194:197], v[52:55]\n\t" \
  "v_mfma_f32_16x16x32_bf16 v[76:79], v[212:215], v[202:205], v[76:79]\n\t" \
  "ds_read_b64_tr_b16 v[212:213], %[a2] offset:72\n\t" \
  "s_waitcnt lgkmcnt(1)\n\t" \
  "v_mfma_f32_16x16x32_bf16 v[8:11], v[208:211], v[178:181], v[8:11]\n\t" \
  "ds_read_b64_tr_b16 v[214:215], %[a2] offset:1608\n\t" \
  "v_mfma_f32_16x16x32_bf16 v[32:35], v[208:211], v[186:189], v[32:35]\n\t" \
  "v_mfma_f32_16x16x32_bf16 v[56:59], v[208:211], v[194:197], v[56:59]\n\t" \
  "v_mfma_f32_16x16x32_bf16 v[80:83], v[208:211], v[202:205], v[80:83]\n\t" \
  "ds_read_b64_tr_b16 v[208:209], %[a2] offset:128\n\t" \
  "v_cvt_pk_bf16_f32 v0, v0, v1\n\t" \
  "v_cvt_pk_bf16_f32 v1, v2, v3\n\t" \
  "v_cvt_pk_bf16_f32 v2, v4, v5\n\t" \
  "v_cvt_pk_bf16_f32 v3, v6, v7\n\t" \
  "buffer_store_dwordx4 v[0:3], %[vo], %[ors], %[oc0] offen offset:0 sc1\n\t" \
  "v_cvt_pk_bf16_f32 v24, v24, v25\n\t" \
  "v_cvt_pk_bf16_f32 v25, v26, v27\n\t" \
  "v_cvt_pk_bf16_f32 v26, v28, v29\n\t" \
  "v_cvt_pk_bf16_f32 v27, v30, v31\n\t" \
  "buffer_store_dwordx4 v[24:27], %[vo], %[ors], %[oc0] offen offset:192 sc1\n\t" \
  "v_cvt_pk_bf16_f32 v48, v48, v49\n\t" \
  "v_cvt_pk_bf16_f32 v49, v50, v51\n\t" \
  "v_cvt_pk_bf16_f32 v50, v52, v53\n\t" \
  "v_cvt_pk_bf16_f32 v51, v54, v55\n\t" \
  "buffer_store_dwordx4 v[48:51], %[vo], %[ors], %[oc1] offen offset:0 sc1\n\t" \
  "v_cvt_pk_bf16_f32 v72, v72, v73\n\t" \
  "v_cvt_pk_bf16_f32 v73, v74, v75\n\t" \
  "v_cvt_pk_bf16_f32 v74, v76, v77\n\t" \
  "v_cvt_pk_bf16_f32 v75, v78, v79\n\t" \
  "buffer_store_dwordx4 v[72:75], %[vo], %[ors], %[oc1] offen offset:192 sc1\n\t" \
  "s_waitcnt lgkmcnt(1)\n\t" \
  "v_mfma_f32_16x16x32_bf16 v[12:15], v[212:215], v[178:181], v[12:15]\n\t" \
  "ds_read_b64_tr_b16 v[210:211], %[a2] offset:1664\n\t" \
  "v_mfma_f32_16x16x32_bf16 v[36:39], v[212:215], v[186:189], v[36:39]\n\t" \
  "v_mfma_f32_16x16x32_bf16 v[60:63], v[212:215], v[194:197], v[60:63]\n\t" \
  "v_mfma_f32_16x16x32_bf16 v[84:87], v[212:215], v[202:205], v[84:87]\n\t" \
  "ds_read_b64_tr_b16 v[212:213], %[a2] offset:136\n\t" \
  "s_waitcnt lgkmcnt(1)\n\t" \
  "v_mfma_f32_16x16x32_bf16 v[16:19], v[208:211], v[178:181], v[16:19]\n\t" \
  "ds_read_b64_tr_b16 v[214:215], %[a2] offset:1672\n\t" \
  "v_mfma_f32_16x16x32_bf16 v[40:43], v[208:211], v[186:189], v[40:43]\n\t" \
  "v_mfma_f32_16x16x32_bf16 v[64:67], v[208:211], v[194:197], v[64:67]\n\t" \
  "v_mfma_f32_16x16x32_bf16 v[88:91], v[208:211], v[202:205], v[88:91]\n\t" \
  "v_cvt_pk_bf16_f32 v8, v8, v9\n\t" \
  "v_cvt_pk_bf16_f32 v9, v10, v11\n\t" \
  "v_cvt_pk_bf16_f32 v10, v12, v13\n\t" \
  "v_cvt_pk_bf16_f32 v11, v14, v15\n\t" \
  "buffer_store_dwordx4 v[8:11], %[vo], %[ors], %[oc0] offen offset:64 sc1\n\t" \
  "v_cvt_pk_bf16_f32 v32, v32, v33\n\t" \
  "v_cvt_pk_bf16_f32 v33, v34, v35\n\t" \
  "v_cvt_pk_bf16_f32 v34, v36, v37\n\t" \
  "v_cvt_pk_bf16_f32 v35, v38, v39\n\t" \
  "buffer_store_dwordx4 v[32:35], %[vo], %[ors], %[oc0] offen offset:256 sc1\n\t" \
  "v_cvt_pk_bf16_f32 v56, v56, v57\n\t" \
  "v_cvt_pk_bf16_f32 v57, v58, v59\n\t" \
  "v_cvt_pk_bf16_f32 v58, v60, v61\n\t" \
  "v_cvt_pk_bf16_f32 v59, v62, v63\n\t" \
  "buffer_store_dwordx4 v[56:59], %[vo], %[ors], %[oc1] offen offset:64 sc1\n\t" \
  "v_cvt_pk_bf16_f32 v80, v80, v81\n\t" \
  "v_cvt_pk_bf16_f32 v81, v82, v83\n\t" \
  "v_cvt_pk_bf16_f32 v82, v84, v85\n\t" \
  "v_cvt_pk_bf16_f32 v83, v86, v87\n\t" \
  "buffer_store_dwordx4 v[80:83], %[vo], %[ors], %[oc1] offen offset:256 sc1\n\t" \
  "s_waitcnt lgkmcnt(0)\n\t" \
  "v_mfma_f32_16x16x32_bf16 v[20:23], v[212:215], v[178:181], v[20:23]\n\t" \
  "v_mfma_f32_16x16x32_bf16 v[44:47], v[212:215], v[186:189], v[44:47]\n\t" \
  "v_mfma_f32_16x16x32_bf16 v[68:71], v[212:215], v[194:197], v[68:71]\n\t" \
  "v_mfma_f32_16x16x32_bf16 v[92:95], v[212:215], v[202:205], v[92:95]\n\t" \
  "v_cvt_pk_bf16_f32 v16, v16, v17\n\t" \
  "v_cvt_pk_bf16_f32 v17, v18, v19\n\t" \
  "s_nop 2\n\t" \
  "v_cvt_pk_bf16_f32 v18, v20, v21\n\t" \
  "v_cvt_pk_bf16_f32 v19, v22, v23\n\t" \
  "buffer_store_dwordx4 v[16:19], %[vo], %[ors], %[oc0] offen offset:128 sc1\n\t" \
  "v_cvt_pk_bf16_f32 v40, v40, v41\n\t" \
  "v_cvt_pk_bf16_f32 v41, v42, v43\n\t" \
  "v_cvt_pk_bf16_f32 v42, v44, v45\n\t" \
  "v_cvt_pk_bf16_f32 v43, v46, v47\n\t" \
  "buffer_store_dwordx4 v[40:43], %[vo], %[ors], %[oc0] offen offset:320 sc1\n\t" \
  "v_cvt_pk_bf16_f32 v64, v64, v65\n\t" \
  "v_cvt_pk_bf16_f32 v65, v66, v67\n\t" \
  "v_cvt_pk_bf16_f32 v66, v68, v69\n\t" \
  "v_cvt_pk_bf16_f32 v67, v70, v71\n\t" \
  "buffer_store_dwordx4 v[64:67], %[vo], %[ors], %[oc1] offen offset:128 sc1\n\t" \
  "v_cvt_pk_bf16_f32 v88, v88, v89\n\t" \
  "v_cvt_pk_bf16_f32 v89, v90, v91\n\t" \
  "v_cvt_pk_bf16_f32 v90, v92, v93\n\t" \
  "v_cvt_pk_bf16_f32 v91, v94, v95\n\t" \
  "buffer_store_dwordx4 v[88:91], %[vo], %[ors], %[oc1] offen offset:320 sc1\n\t" \
  ""


__device__ __forceinline__ unsigned lds_addr(const char* p) { return (unsigned)(uintptr_t)(lds_ptr_t)p; }
__device__ __forceinline__ unsigned sg(unsigned v) { return __builtin_amdgcn_readfirstlane(v); }

template <bool NT> __device__ __forceinline__ void dma16(u32x4 rsrc, unsigned voff, unsigned soff, unsigned lds_dst) {
    if constexpr (NT)
        asm volatile("s_mov_b32 m0, %1\n\ts_nop 0\n\tbuffer_load_dwordx4 %0, %2, %3 offen nt lds"
                     : : "v"(voff), "s"(lds_dst), "s"(rsrc), "s"(soff) : "memory");
    else
        asm volatile("s_mov_b32 m0, %1\n\ts_nop 0\n\tbuffer_load_dwordx4 %0, %2, %3 offen lds"
                     : : "v"(voff), "s"(lds_dst), "s"(rsrc), "s"(soff) : "memory");
}
__device__ __forceinline__ void warm(u32x4 rsrc, unsigned voff, unsigned soff, unsigned lds_dst) {
    asm volatile("s_mov_b32 m0, %1\n\ts_nop 0\n\tbuffer_load_dword %0, %2, %3 offen lds"
                 : : "v"(voff), "s"(lds_dst), "s"(rsrc), "s"(soff) : "memory");
}
__device__ __forceinline__ u32x4 make_rsrc(const void* p, unsigned bytes) {
    const uint64_t a = (uint64_t)(uintptr_t)p;
    u32x4 r = {sg((unsigned)a), sg((unsigned)(a >> 32) & 0xffffu), sg(bytes), 0x00020000u};
    return r;
}

#define V10(a) "v" #a "0", "v" #a "1", "v" #a "2", "v" #a "3", "v" #a "4", "v" #a "5", "v" #a "6", "v" #a "7", "v" #a "8", "v" #a "9"
#define MAIN_CLOB "memory", "scc", V10(), V10(1), V10(2), V10(3), V10(4), V10(5), V10(6), V10(7), V10(8), V10(9), V10(10), V10(11), \
    V10(12), V10(13), V10(14), V10(15), V10(16), V10(17), V10(18), V10(19), V10(20), V10(21), V10(22),                            \
    V10(23)

__global__ __launch_bounds__(512, 2) void convnext_kernel(const __hip_bfloat16* __restrict__ x,
                                                          const __hip_bfloat16* __restrict__ dw,
                                                          const __hip_bfloat16* __restrict__ w1,
                                                          const __hip_bfloat16* __restrict__ w2,
                                                          __hip_bfloat16* __restrict__ out) {
    __shared__ __attribute__((aligned(16))) char smem[LDS_BYTES];
    const int tid = threadIdx.x;
    const int lane = tid & 63;
    const int wid = sg(tid >> 6);
    const int bid = blockIdx.x;

    const int x0 = ((bid >> 3) & 15) * 32, low = bid >> 7;
    const int ym = (bid & 7) * 64 + 31 + low;
    const int rs = low ? ROWB : -ROWB;
    const unsigned sbase = lds_addr(smem);
    asm volatile("s_setreg_imm32_b32 hwreg(HW_REG_MODE, 4, 2), 0\n\ts_setreg_imm32_b32 hwreg(HW_REG_MODE, 9, 1), 0\n\ts_nop 1");

    const u32x4 xrs = make_rsrc(x, IMG * ROWB);
    const bool isw1 = wid < 2;
    const u32x4 wrs = make_rsrc(isw1 ? w1 : w2, CH * HID * 2);
    const u32x4 ors = make_rsrc(out, IMG * ROWB);
    unsigned t[7];
    const int ks = tid & 7, kc = tid >> 3;
    if (wid < 6) {
        const __hip_bfloat16* src = dw + (ks == 0 ? 0 : low ? ks - 1 : 7 - ks) * (7 * CH) + 2 * kc;
#pragma unroll
        for (int j = 0; j < 7; ++j)
            asm volatile("global_load_dword %0, %1, off offset:%2" : "=&v"(t[j]) : "v"(src), "n"(j * CH * 2) : "memory");
    } else {
        unsigned long long pc;
        unsigned len;
        asm volatile("s_getpc_b64 %0\n.Lcnx_pc:\n\ts_mov_b32 %1, .Lcnx_end-.Lcnx_pc" : "=s"(pc), "=s"(len));
        if (wid == 6) warm(make_rsrc((const void*)pc, len & ~3u), lane < 20 ? lane * 64 : OOB, (bid >> 3) * 1280, sbase + PFOFF);
        warm(make_rsrc(wid == 6 ? w2 : w1, CH * HID * 2), lane < 36 ? lane * 64 : OOB, (bid >> 3) * 2304, sbase + PFOFF);
#pragma unroll
        for (int j = 0; j < 7; ++j) t[j] = 0;
    }
    const int pp = tid / 25, s = tid - pp * 25;
    const int gx = x0 - 3 + 2 * pp + (s >= 12);
    const unsigned xv = (s < 24 && pp < 19 && (unsigned)gx < (unsigned)IMG) ? (unsigned)((x0 - 3 + 2 * pp) * (CH * 2) + s * 16) : OOB;
    unsigned xo = (unsigned)(ym * ROWB - 3 * rs);
    const unsigned xl = sbase + XOFF + wid * 1024;
#pragma unroll
    for (int r = 0; r < 6; ++r) dma16<true>(xrs, xv, xo + r * rs, xl + r * XROW);


    const int G = lane >> 4, n = lane & 15, q = n >> 2, p = lane & 3;
    const int wl_ = (wid & 3) * 60 + lane;
    const unsigned warmv = lane < 60 ? (unsigned)((wl_ / 48) * ROWB + x0 * (CH * 2) + (wl_ % 48) * 128) : OOB;
    unsigned a1 = sbase + W1OFF + (lane >> 2) * 32 + (p & 1) * 16 + (p >> 1) * 8;
    unsigned a2 = sbase + W2OFF + (G & 1) * 3080 + ((G >> 1) * 4 + q) * 192 + p * 16;
    unsigned wv = wid >= 4 ? warmv : isw1 ? (unsigned)((12 * ((lane >> 3) & 3) + ((lane >> 1) & 3)) * (HID * 2) + (2 * (lane & 1) + (lane >> 5)) * 16)
                                          : (unsigned)(lane * 16);
    const unsigned wpd = isw1 ? 4 * HID * 2 - 1024 : 0, wc = isw1 ? 64 : 6144;
    unsigned so0 = (wid & 1) * (isw1 ? 48 * HID * 2 : 3072), so1 = so0 + wpd, so2 = so1 + wpd;
    const unsigned wl = sbase + (isw1 ? W1OFF + (wid & 1) * 3072 : W2OFF + (wid & 1) * 3080);
    unsigned cb = sbase + XOFF + n * 400 + (G & 1) * XROW + (G >> 1) * 192 + wid * 24;
    unsigned aa = sbase + DWOFF + (12 * wid + p) * 192 + (1 - (q >> 1)) * 24 + (1 + (G & 1) - (q & 1)) * 8 + (G >> 1) * 4;
    unsigned vo = n * 384 + G * 16;
    unsigned l8 = sbase + PB + lane * 8;
    const unsigned one = G == q ? (p & 1 ? 0x3f800000u : 0x3f80u) : 0u;
    u32x2 id2 = {p < 2 ? one : 0u, p < 2 ? 0u : one};
    const u32x2 zz = {0u, 0u};
    unsigned dwa = sbase + DWOFF + kc * 384 + ks * 24;
    asm volatile("" : "+v"(a1), "+v"(a2), "+v"(wv), "+v"(cb), "+v"(aa), "+v"(vo), "+v"(l8), "+v"(id2), "+v"(dwa));
    unsigned oc0 = (unsigned)(ym * ROWB + 2 * wid * rs + x0 * (CH * 2)), oc1 = oc0 + rs, op0, op1;
    unsigned rnd, f0 = wid, t0;
    so0 = sg(so0); so1 = sg(so1); so2 = sg(so2); oc0 = sg(oc0); oc1 = sg(oc1);
#pragma unroll
    for (int r = 6; r < 9; ++r) dma16<true>(xrs, xv, xo + r * rs, xl + r * XROW);

    asm volatile("s_waitcnt vmcnt(9)" ::: "memory");

#pragma unroll
    for (int j = 0; j < 7; ++j) {
        asm volatile("" : "+v"(t[j]));
        if (ks == 0) t[j] = 0;
    }
    if (tid <= 384) {
#pragma unroll
        for (int c = 0; c < 2; ++c) {
            const unsigned sel = c ? 0x07060302u : 0x05040100u;
            auto pk = [&](unsigned a, unsigned b) { return __builtin_amdgcn_perm(b, a, sel); };
            char* bp = smem + (dwa - sbase) + (tid == 384 ? 0 : c * 192);
            *(u32x2*)(bp) = (u32x2){pk(0u, t[1]), pk(t[3], t[5])};
            *(u32x2*)(bp + 8) = (u32x2){pk(t[0], t[2]), pk(t[4], t[6])};
            *(u32x2*)(bp + 16) = (u32x2){pk(t[1], t[3]), pk(t[5], 0u)};
        }
    }
    if (wid >= 4) __builtin_amdgcn_s_setprio(1);

    asm volatile("s_waitcnt lgkmcnt(0)\n\ts_barrier" ::: "memory");

#pragma unroll
    for (int r = 9; r < 12; ++r) dma16<true>(xrs, xv, xo + r * rs, xl + r * XROW);
    const unsigned wb = xo + (low ? 28 : -37) * ROWB;
    xo = sg(xo + 11 * rs);
    asm volatile(MAIN_ASM
                 : [xo] "+&s"(xo), [oc0] "+&s"(oc0), [oc1] "+&s"(oc1), [op0] "=&s"(op0), [op1] "=&s"(op1), [rnd] "=&s"(rnd), [f0] "+&s"(f0),
                   [so0] "+&s"(so0), [so1] "+&s"(so1), [so2] "+&s"(so2), [t0] "=&s"(t0)
                 : [a1] "v"(a1), [a2] "v"(a2), [wv] "v"(wv), [xv] "v"(xv), [h2] "v"(GH2), [cb] "v"(cb), [aa] "v"(aa),
                   [vo] "v"(vo), [l8] "v"(l8), "{v[240:241]}"(zz), "{v[242:243]}"(id2), "{v[244:245]}"(zz),
                   [xrs] "s"(xrs), [ors] "s"(ors), [wrs] "s"(wrs), [xl] "s"(sg(xl)), [rs] "s"(sg(rs)), [rs6] "s"(sg(6 * rs)), [dy] "s"(sg(16 * rs)), [wb] "s"(sg(wb)),
                   [zb] "s"(sg(XOFF - 8 * PB - 7 * sbase + wid * 12288)), [lb] "s"(sg(-2 * PB - sbase)), [m11] "s"(0xba000000u),
                   [grp] "s"(sg(wid >> 2)), [sr] "s"(sg((wid & 3) * 4096 + (wid < 4 ? 16400 : 0))), [w512] "s"(sg(wid * 512)),
                   [z1] "s"(sg(XOFF - 2 * PB - sbase + wid * 1024)), [z2] "s"(sg(XOFF - 2 * PB - sbase + (8 + (wid & 3)) * 1024 + (wid >> 2) * 8)),
                   [wl] "s"(sg(wl)), [ws] "s"(sg(isw1 ? 6144 : 6160)), [wc] "s"(sg(wc)), [wr1] "s"(sg(isw1 ? 12 * wc : 0)), [wr2] "s"(sg(isw1 ? 0 : 12 * wc)),
                   [pf] "s"(sg(sbase + PFOFF)), [h1] "s"(GH1), [hh] "s"(GHH), [mk] "s"(0x3ffcu)
                 : MAIN_CLOB);
    asm volatile("s_waitcnt vmcnt(0)\n.Lcnx_end:" ::: "memory");

}

}

extern "C" void kernel_launch(void* const* d_in, const int* in_sizes, int n_in, void* d_out, int out_size,
                              void* d_ws, size_t ws_size, hipStream_t stream) {
    (void)in_sizes; (void)n_in; (void)out_size; (void)d_ws; (void)ws_size;
    const __hip_bfloat16* x = (const __hip_bfloat16*)d_in[0];
    const __hip_bfloat16* dw = (const __hip_bfloat16*)d_in[1];
    const __hip_bfloat16* w1 = (const __hip_bfloat16*)d_in[2];
    const __hip_bfloat16* w2 = (const __hip_bfloat16*)d_in[3];
    __hip_bfloat16* out = (__hip_bfloat16*)d_out;
    convnext_kernel<<<256, 512, 0, stream>>>(x, dw, w1, w2, out);
}
```

```cpp
#include <hip/hip_runtime.h>
#include <hip/hip_bf16.h>
#include <stdint.h>

namespace {

typedef __attribute__((ext_vector_type(4))) float f32x4;
typedef __attribute__((ext_vector_type(4))) unsigned u32x4;
typedef __attribute__((ext_vector_type(8))) unsigned u32x8;
typedef __attribute__((ext_vector_type(2))) unsigned u32x2;
typedef __attribute__((address_space(3))) void* lds_ptr_t;

constexpr int IMG = 512;
constexpr int CH = 96;
constexpr int HID = 384;
constexpr int ROWB = IMG * CH * 2;
constexpr int LUTN = 3841;
constexpr int DWOFF = 15376;
constexpr int CNTOFF = DWOFF + 96 * 192 + 32;
constexpr int W1OFF = CNTOFF + 128;
constexpr int XOFF = W1OFF + 2 * 6144;
constexpr int XROW = 8200;
constexpr int W2OFF = XOFF + 12 * XROW;
constexpr int PB = XOFF + 10 * XROW;
constexpr int PFOFF = W2OFF + 12320;
constexpr int TABOFF = W2OFF + 16384 + 2048;
constexpr int LDS_BYTES = TABOFF + 512;
static_assert(CNTOFF == 33840 && PB == 128256 && LUTN * 4 <= DWOFF && XOFF % 16 == 0 && LDS_BYTES <= 160 * 1024, "LDS map");
constexpr unsigned OOB = 0x40000000u;
constexpr unsigned GH1 = 0xcc9bcc9bu, GH2 = 0xd297d297u, GHH = 0x30003000u;

#define MAIN_ASM \
  "s_mov_b32 %[tg], 8\n\t" \
  "s_mov_b32 %[rnd], 0\n\t" \
  "s_mov_b32 %[op0], 0x40000000\n\t" \
  "s_mov_b32 %[op1], 0x40000000\n\t" \
  "v_add_u32 v232, 1536, v247\n\t" \
  "ds_read2_b32 v[98:99], v247 offset0:0 offset1:6\n\t" \
  "ds_read2_b32 v[102:103], v247 offset0:12 offset1:18\n\t" \
  "ds_read2_b32 v[106:107], v247 offset0:24 offset1:30\n\t" \
  "ds_read2_b32 v[110:111], v247 offset0:36 offset1:42\n\t" \
  "ds_read2_b32 v[114:115], v247 offset0:192 offset1:198\n\t" \
  "ds_read2_b32 v[118:119], v247 offset0:204 offset1:210\n\t" \
  "ds_read2_b32 v[122:123], v247 offset0:216 offset1:222\n\t" \
  "ds_read2_b32 v[126:127], v247 offset0:228 offset1:234\n\t" \
  "ds_read2_b32 v[130:131], v232 offset0:0 offset1:6\n\t" \
  "ds_read2_b32 v[134:135], v232 offset0:12 offset1:18\n\t" \
  "ds_read2_b32 v[138:139], v232 offset0:24 offset1:30\n\t" \
  "ds_read2_b32 v[142:143], v232 offset0:36 offset1:42\n\t" \
  "v_mfma_f32_32x32x16_bf16 v[144:159], v[242:245], v[240:243], 0\n\t" \
  "v_mfma_f32_32x32x16_bf16 v[160:175], v[242:245], v[240:243], 0\n\t" \
  "s_waitcnt lgkmcnt(11)\n\t" \
  "v_and_b32 v96, 0xffff, v98\n\t" \
  "v_and_b32 v97, 0xffff0000, v98\n\t" \
  "v_and_b32 v98, 0xffff, v99\n\t" \
  "v_and_b32 v99, 0xffff0000, v99\n\t" \
  "s_waitcnt lgkmcnt(10)\n\t" \
  "v_and_b32 v100, 0xffff, v102\n\t" \
  "v_and_b32 v101, 0xffff0000, v102\n\t" \
  "v_and_b32 v102, 0xffff, v103\n\t" \
  "v_and_b32 v103, 0xffff0000, v103\n\t" \
  "s_waitcnt lgkmcnt(9)\n\t" \
  "v_and_b32 v104, 0xffff, v106\n\t" \
  "v_and_b32 v105, 0xffff0000, v106\n\t" \
  "v_and_b32 v106, 0xffff, v107\n\t" \
  "v_and_b32 v107, 0xffff0000, v107\n\t" \
  "s_waitcnt lgkmcnt(8)\n\t" \
  "v_and_b32 v108, 0xffff, v110\n\t" \
  "v_and_b32 v109, 0xffff0000, v110\n\t" \
  "v_and_b32 v110, 0xffff, v111\n\t" \
  "v_and_b32 v111, 0xffff0000, v111\n\t" \
  "s_waitcnt lgkmcnt(7)\n\t" \
  "v_and_b32 v112, 0xffff, v114\n\t" \
  "v_and_b32 v113, 0xffff0000, v114\n\t" \
  "v_and_b32 v114, 0xffff, v115\n\t" \
  "v_and_b32 v115, 0xffff0000, v115\n\t" \
  "s_waitcnt lgkmcnt(6)\n\t" \
  "v_and_b32 v116, 0xffff, v118\n\t" \
  "v_and_b32 v117, 0xffff0000, v118\n\t" \
  "v_and_b32 v118, 0xffff, v119\n\t" \
  "v_and_b32 v119, 0xffff0000, v119\n\t" \
  "s_waitcnt lgkmcnt(5)\n\t" \
  "v_and_b32 v120, 0xffff, v122\n\t" \
  "v_and_b32 v121, 0xffff0000, v122\n\t" \
  "v_and_b32 v122, 0xffff, v123\n\t" \
  "v_and_b32 v123, 0xffff0000, v123\n\t" \
  "s_waitcnt lgkmcnt(4)\n\t" \
  "v_and_b32 v124, 0xffff, v126\n\t" \
  "v_and_b32 v125, 0xffff0000, v126\n\t" \
  "v_and_b32 v126, 0xffff, v127\n\t" \
  "v_and_b32 v127, 0xffff0000, v127\n\t" \
  "s_waitcnt lgkmcnt(3)\n\t" \
  "v_and_b32 v128, 0xffff, v130\n\t" \
  "v_and_b32 v129, 0xffff0000, v130\n\t" \
  "v_and_b32 v130, 0xffff, v131\n\t" \
  "v_and_b32 v131, 0xffff0000, v131\n\t" \
  "s_waitcnt lgkmcnt(2)\n\t" \
  "v_and_b32 v132, 0xffff, v134\n\t" \
  "v_and_b32 v133, 0xffff0000, v134\n\t" \
  "v_and_b32 v134, 0xffff, v135\n\t" \
  "v_and_b32 v135, 0xffff0000, v135\n\t" \
  "s_waitcnt lgkmcnt(1)\n\t" \
  "v_and_b32 v136, 0xffff, v138\n\t" \
  "v_and_b32 v137, 0xffff0000, v138\n\t" \
  "v_and_b32 v138, 0xffff, v139\n\t" \
  "v_and_b32 v139, 0xffff0000, v139\n\t" \
  "s_waitcnt lgkmcnt(0)\n\t" \
  "v_and_b32 v140, 0xffff, v142\n\t" \
  "v_and_b32 v141, 0xffff0000, v142\n\t" \
  "v_and_b32 v142, 0xffff, v143\n\t" \
  "v_and_b32 v143, 0xffff0000, v143\n\t" \
  "v_bfe_u32 v236, %[l8], 3, 2\n\t" \
  "v_mul_u32_u24 v236, 0x5555, v236\n\t" \
  "s_waitcnt vmcnt(10)\n\t" \
  "s_barrier\n\t" \
  ".Lcnx_round:\n\t" \
  "v_add_u32 v232, 49200, v246\n\t" \
  "v_add_u32 v233, %[w512], %[l8]\n\t" \
  "v_mfma_f32_32x32x16_bf16 v[176:191], v[242:245], v[240:243], 0\n\t" \
  "ds_read_b64 v[208:209], v246 offset:0\n\t" \
  "ds_read_b64 v[210:211], v246 offset:400\n\t" \
  "ds_read_b64 v[212:213], v246 offset:800\n\t" \
  "ds_read_b64 v[214:215], v246 offset:1200\n\t" \
  "ds_read_b64 v[216:217], v246 offset:8\n\t" \
  "ds_read_b64 v[218:219], v246 offset:408\n\t" \
  "ds_read_b64 v[220:221], v246 offset:808\n\t" \
  "ds_read_b64 v[222:223], v246 offset:1208\n\t" \
  "ds_read_b64 v[224:225], v246 offset:16\n\t" \
  "ds_read_b64 v[226:227], v246 offset:416\n\t" \
  "ds_read_b64 v[228:229], v246 offset:816\n\t" \
  "ds_read_b64 v[230:231], v246 offset:1216\n\t" \
  "s_waitcnt lgkmcnt(8)\n\t" \
  "v_smfmac_f32_16x16x64_bf16 v[144:147], v[96:99], v[208:215], v236\n\t" \
  "s_waitcnt lgkmcnt(4)\n\t" \
  "v_smfmac_f32_16x16x64_bf16 v[148:151], v[112:115], v[216:223], v236\n\t" \
  "s_waitcnt vmcnt(8) lgkmcnt(0)\n\t" \
  "s_barrier\n\t" \
  "ds_read_b64 v[208:209], v246 offset:16400\n\t" \
  "ds_read_b64 v[210:211], v246 offset:16800\n\t" \
  "ds_read_b64 v[212:213], v246 offset:17200\n\t" \
  "ds_read_b64 v[214:215], v246 offset:17600\n\t" \
  "ds_read_b64 v[216:217], v246 offset:16408\n\t" \
  "ds_read_b64 v[218:219], v246 offset:16808\n\t" \
  "ds_read_b64 v[220:221], v246 offset:17208\n\t" \
  "ds_read_b64 v[222:223], v246 offset:17608\n\t" \
  "s_add_u32 m0, %[xl], 0\n\t" \
  "s_add_u32 %[xo], %[xo], %[rs]\n\t" \
  "buffer_load_dwordx4 %[xv], %[xrs], %[xo] offen lds\n\t" \
  "s_add_u32 m0, %[xl], 8200\n\t" \
  "s_add_u32 %[xo], %[xo], %[rs]\n\t" \
  "buffer_load_dwordx4 %[xv], %[xrs], %[xo] offen lds\n\t" \
  "v_smfmac_f32_16x16x64_bf16 v[152:155], v[128:131], v[224:231], v236\n\t" \
  "ds_read_b64 v[224:225], v246 offset:16416\n\t" \
  "ds_read_b64 v[226:227], v246 offset:16816\n\t" \
  "ds_read_b64 v[228:229], v246 offset:17216\n\t" \
  "s_waitcnt lgkmcnt(7)\n\t" \
  "v_smfmac_f32_16x16x64_bf16 v[156:159], v[96:99], v[208:215], v236\n\t" \
  "ds_read_b64 v[230:231], v246 offset:17616\n\t" \
  "v_smfmac_f32_16x16x64_bf16 v[144:147], v[100:103], v[208:215], v236\n\t" \
  "s_waitcnt lgkmcnt(4)\n\t" \
  "v_smfmac_f32_16x16x64_bf16 v[160:163], v[112:115], v[216:223], v236\n\t" \
  "v_smfmac_f32_16x16x64_bf16 v[148:151], v[116:119], v[216:223], v236\n\t" \
  "s_waitcnt vmcnt(8) lgkmcnt(0)\n\t" \
  "s_barrier\n\t" \
  "ds_read_b64 v[208:209], v246 offset:32800\n\t" \
  "ds_read_b64 v[210:211], v246 offset:33200\n\t" \
  "ds_read_b64 v[212:213], v246 offset:33600\n\t" \
  "ds_read_b64 v[214:215], v246 offset:34000\n\t" \
  "ds_read_b64 v[216:217], v246 offset:32808\n\t" \
  "ds_read_b64 v[218:219], v246 offset:33208\n\t" \
  "ds_read_b64 v[220:221], v246 offset:33608\n\t" \
  "ds_read_b64 v[222:223], v246 offset:34008\n\t" \
  "s_add_u32 m0, %[xl], 16400\n\t" \
  "s_add_u32 %[xo], %[xo], %[rs]\n\t" \
  "buffer_load_dwordx4 %[xv], %[xrs], %[xo] offen lds\n\t" \
  "s_add_u32 m0, %[xl], 24600\n\t" \
  "s_add_u32 %[xo], %[xo], %[rs]\n\t" \
  "buffer_load_dwordx4 %[xv], %[xrs], %[xo] offen lds\n\t" \
  "v_smfmac_f32_16x16x64_bf16 v[164:167], v[128:131], v[224:231], v236\n\t" \
  "v_cvt_pk_bf16_f32 v48, v48, v49\n\t" \
  "v_cvt_pk_bf16_f32 v49, v50, v51\n\t" \
  "v_cvt_pk_bf16_f32 v50, v52, v53\n\t" \
  "v_cvt_pk_bf16_f32 v51, v54, v55\n\t" \
  "v_cvt_pk_bf16_f32 v52, v0, v1\n\t" \
  "v_cvt_pk_bf16_f32 v53, v2, v3\n\t" \
  "v_smfmac_f32_16x16x64_bf16 v[152:155], v[132:135], v[224:231], v236\n\t" \
  "ds_read_b64 v[224:225], v246 offset:32816\n\t" \
  "ds_read_b64 v[226:227], v246 offset:33216\n\t" \
  "ds_read_b64 v[228:229], v246 offset:33616\n\t" \
  "v_cvt_pk_bf16_f32 v54, v4, v5\n\t" \
  "v_cvt_pk_bf16_f32 v55, v6, v7\n\t" \
  "v_cvt_pk_bf16_f32 v56, v56, v57\n\t" \
  "s_waitcnt lgkmcnt(7)\n\t" \
  "v_smfmac_f32_16x16x64_bf16 v[168:171], v[96:99], v[208:215], v236\n\t" \
  "ds_read_b64 v[230:231], v246 offset:34016\n\t" \
  "v_cvt_pk_bf16_f32 v57, v58, v59\n\t" \
  "v_cvt_pk_bf16_f32 v58, v60, v61\n\t" \
  "v_cvt_pk_bf16_f32 v59, v62, v63\n\t" \
  "v_smfmac_f32_16x16x64_bf16 v[156:159], v[100:103], v[208:215], v236\n\t" \
  "v_cvt_pk_bf16_f32 v60, v8, v9\n\t" \
  "v_cvt_pk_bf16_f32 v61, v10, v11\n\t" \
  "v_cvt_pk_bf16_f32 v62, v12, v13\n\t" \
  "v_smfmac_f32_16x16x64_bf16 v[144:147], v[104:107], v[208:215], v236\n\t" \
  "v_cvt_pk_bf16_f32 v63, v14, v15\n\t" \
  "v_mfma_f32_32x32x16_bf16 v[0:15], v[242:245], v[240:243], 0\n\t" \
  "s_waitcnt lgkmcnt(4)\n\t" \
  "v_smfmac_f32_16x16x64_bf16 v[172:175], v[112:115], v[216:223], v236\n\t" \
  "v_smfmac_f32_16x16x64_bf16 v[160:163], v[116:119], v[216:223], v236\n\t" \
  "v_smfmac_f32_16x16x64_bf16 v[148:151], v[120:123], v[216:223], v236\n\t" \
  "s_cmp_lg_u32 %[rnd], 0\n\t" \
  "s_cbranch_scc1 .Lcnx_c1\n\t" \
  "v_lshlrev_b32 v88, 14, v233\n\t" \
  "v_lshl_add_u32 v89, v233, 1, %[lb]\n\t" \
  "v_mov_b32 v90, 0x40135761\n\t" \
  "v_add_u32 v48, 0xbc403000, v88\n\t" \
  "v_add_u32 v49, 0xbc40b000, v88\n\t" \
  "v_add_u32 v50, 0xbc413000, v88\n\t" \
  "v_add_u32 v51, 0xbc41b000, v88\n\t" \
  "v_add_u32 v52, 0xc0403000, v88\n\t" \
  "v_add_u32 v53, 0xc040b000, v88\n\t" \
  "v_add_u32 v54, 0xc0413000, v88\n\t" \
  "v_add_u32 v55, 0xc041b000, v88\n\t" \
  "v_fma_f32 v56, 2.0, v48, %[m11]\n\t" \
  "v_fma_f32 v57, 2.0, v49, %[m11]\n\t" \
  "v_fma_f32 v58, 2.0, v50, %[m11]\n\t" \
  "v_fma_f32 v59, 2.0, v51, %[m11]\n\t" \
  "v_min_f32 v48, v48, v56\n\t" \
  "v_min_f32 v49, v49, v57\n\t" \
  "v_min_f32 v50, v50, v58\n\t" \
  "v_min_f32 v51, v51, v59\n\t" \
  "v_mul_f32 v56, v48, v48\n\t" \
  "v_mul_f32 v57, v49, v49\n\t" \
  "v_mul_f32 v58, v50, v50\n\t" \
  "v_mul_f32 v59, v51, v51\n\t" \
  "v_mul_f32 v60, v52, v52\n\t" \
  "v_mul_f32 v61, v53, v53\n\t" \
  "v_mul_f32 v62, v54, v54\n\t" \
  "v_mul_f32 v63, v55, v55\n\t" \
  "v_fmamk_f32 v56, v56, 0x3dd2d3e8, v90\n\t" \
  "v_fmamk_f32 v57, v57, 0x3dd2d3e8, v90\n\t" \
  "v_fmamk_f32 v58, v58, 0x3dd2d3e8, v90\n\t" \
  "v_fmamk_f32 v59, v59, 0x3dd2d3e8, v90\n\t" \
  "v_fmamk_f32 v60, v60, 0x3dd2d3e8, v90\n\t" \
  "v_fmamk_f32 v61, v61, 0x3dd2d3e8, v90\n\t" \
  "v_fmamk_f32 v62, v62, 0x3dd2d3e8, v90\n\t" \
  "v_fmamk_f32 v63, v63, 0x3dd2d3e8, v90\n\t" \
  "v_mul_f32 v56, v56, v48\n\t" \
  "v_mul_f32 v57, v57, v49\n\t" \
  "v_mul_f32 v58, v58, v50\n\t" \
  "v_mul_f32 v59, v59, v51\n\t" \
  "v_mul_f32 v60, v60, v52\n\t" \
  "v_mul_f32 v61, v61, v53\n\t" \
  "v_mul_f32 v62, v62, v54\n\t" \
  "v_mul_f32 v63, v63, v55\n\t" \
  "v_exp_f32 v56, v56\n\t" \
  "v_exp_f32 v57, v57\n\t" \
  "v_exp_f32 v58, v58\n\t" \
  "v_exp_f32 v59, v59\n\t" \
  "v_exp_f32 v60, v60\n\t" \
  "v_exp_f32 v61, v61\n\t" \
  "v_exp_f32 v62, v62\n\t" \
  "v_exp_f32 v63, v63\n\t" \
  "v_add_f32 v56, 1.0, v56\n\t" \
  "v_add_f32 v57, 1.0, v57\n\t" \
  "v_add_f32 v58, 1.0, v58\n\t" \
  "v_add_f32 v59, 1.0, v59\n\t" \
  "v_add_f32 v60, 1.0, v60\n\t" \
  "v_add_f32 v61, 1.0, v61\n\t" \
  "v_add_f32 v62, 1.0, v62\n\t" \
  "v_add_f32 v63, 1.0, v63\n\t" \
  "v_rcp_f32 v56, v56\n\t" \
  "v_rcp_f32 v57, v57\n\t" \
  "v_rcp_f32 v58, v58\n\t" \
  "v_rcp_f32 v59, v59\n\t" \
  "v_rcp_f32 v60, v60\n\t" \
  "v_rcp_f32 v61, v61\n\t" \
  "v_rcp_f32 v62, v62\n\t" \
  "v_rcp_f32 v63, v63\n\t" \
  "v_fma_f32 v56, -2.0, v56, 1.0\n\t" \
  "v_fma_f32 v57, -2.0, v57, 1.0\n\t" \
  "v_fma_f32 v58, -2.0, v58, 1.0\n\t" \
  "v_fma_f32 v59, -2.0, v59, 1.0\n\t" \
  "v_fma_f32 v60, -2.0, v60, 1.0\n\t" \
  "v_fma_f32 v61, -2.0, v61, 1.0\n\t" \
  "v_fma_f32 v62, -2.0, v62, 1.0\n\t" \
  "v_fma_f32 v63, -2.0, v63, 1.0\n\t" \
  "ds_write_b128 v89, v[56:59]\n\t" \
  "s_cmp_eq_u32 %[w512], 3584\n\t" \
  "s_cselect_b64 exec, 1, -1\n\t" \
  "ds_write_b128 v89, v[60:63] offset:8192\n\t" \
  "s_mov_b64 exec, -1\n\t" \
  ".Lcnx_c1:\n\t" \
  "s_waitcnt vmcnt(8) lgkmcnt(0)\n\t" \
  "s_barrier\n\t" \
  "ds_read_b64 v[208:209], v232 offset:0\n\t" \
  "ds_read_b64 v[210:211], v232 offset:400\n\t" \
  "ds_read_b64 v[212:213], v232 offset:800\n\t" \
  "ds_read_b64 v[214:215], v232 offset:1200\n\t" \
  "ds_read_b64 v[216:217], v232 offset:8\n\t" \
  "ds_read_b64 v[218:219], v232 offset:408\n\t" \
  "ds_read_b64 v[220:221], v232 offset:808\n\t" \
  "ds_read_b64 v[222:223], v232 offset:1208\n\t" \
  "s_add_u32 m0, %[xl], 32800\n\t" \
  "s_add_u32 %[xo], %[xo], %[rs]\n\t" \
  "buffer_load_dwordx4 %[xv], %[xrs], %[xo] offen lds\n\t" \
  "s_add_u32 m0, %[xl], 41000\n\t" \
  "s_add_u32 %[xo], %[xo], %[rs]\n\t" \
  "buffer_load_dwordx4 %[xv], %[xrs], %[xo] offen lds\n\t" \
  "v_smfmac_f32_16x16x64_bf16 v[176:179], v[128:131], v[224:231], v236\n\t" \
  "v_smfmac_f32_16x16x64_bf16 v[164:167], v[132:135], v[224:231], v236\n\t" \
  "v_smfmac_f32_16x16x64_bf16 v[152:155], v[136:139], v[224:231], v236\n\t" \
  "ds_read_b64 v[224:225], v232 offset:16\n\t" \
  "s_waitcnt lgkmcnt(5)\n\t" \
  "v_smfmac_f32_16x16x64_bf16 v[180:183], v[96:99], v[208:215], v236\n\t" \
  "ds_read_b64 v[226:227], v232 offset:416\n\t" \
  "v_smfmac_f32_16x16x64_bf16 v[168:171], v[100:103], v[208:215], v236\n\t" \
  "ds_read_b64 v[228:229], v232 offset:816\n\t" \
  "v_cvt_pk_bf16_f32 v64, v64, v65\n\t" \
  "v_cvt_pk_bf16_f32 v65, v66, v67\n\t" \
  "v_cvt_pk_bf16_f32 v66, v68, v69\n\t" \
  "v_cvt_pk_bf16_f32 v67, v70, v71\n\t" \
  "v_cvt_pk_bf16_f32 v68, v16, v17\n\t" \
  "v_cvt_pk_bf16_f32 v69, v18, v19\n\t" \
  "v_smfmac_f32_16x16x64_bf16 v[156:159], v[104:107], v[208:215], v236\n\t" \
  "ds_read_b64 v[230:231], v232 offset:1216\n\t" \
  "v_cvt_pk_bf16_f32 v70, v20, v21\n\t" \
  "v_cvt_pk_bf16_f32 v71, v22, v23\n\t" \
  "v_cvt_pk_bf16_f32 v72, v72, v73\n\t" \
  "v_smfmac_f32_16x16x64_bf16 v[144:147], v[108:111], v[208:215], v236\n\t" \
  "v_cvt_pk_bf16_f32 v73, v74, v75\n\t" \
  "v_cvt_pk_bf16_f32 v74, v76, v77\n\t" \
  "v_cvt_pk_bf16_f32 v75, v78, v79\n\t" \
  "s_waitcnt lgkmcnt(4)\n\t" \
  "v_smfmac_f32_16x16x64_bf16 v[184:187], v[112:115], v[216:223], v236\n\t" \
  "v_cvt_pk_bf16_f32 v76, v24, v25\n\t" \
  "v_cvt_pk_bf16_f32 v77, v26, v27\n\t" \
  "v_cvt_pk_bf16_f32 v78, v28, v29\n\t" \
  "v_smfmac_f32_16x16x64_bf16 v[172:175], v[116:119], v[216:223], v236\n\t" \
  "v_cvt_pk_bf16_f32 v79, v30, v31\n\t" \
  "v_mfma_f32_32x32x16_bf16 v[16:31], v[242:245], v[240:243], 0\n\t" \
  "v_smfmac_f32_16x16x64_bf16 v[160:163], v[120:123], v[216:223], v236\n\t" \
  "v_mul_f32 v193, v144, v144\n\t" \
  "v_add_f32 v192, v144, v145\n\t" \
  "v_fmac_f32 v193, v145, v145\n\t" \
  "v_add_f32 v192, v192, v146\n\t" \
  "v_smfmac_f32_16x16x64_bf16 v[148:151], v[124:127], v[216:223], v236\n\t" \
  "v_fmac_f32 v193, v146, v146\n\t" \
  "v_add_f32 v192, v192, v147\n\t" \
  "v_fmac_f32 v193, v147, v147\n\t" \
  "s_waitcnt vmcnt(8) lgkmcnt(0)\n\t" \
  "s_barrier\n\t" \
  "ds_read_b64 v[208:209], v232 offset:16400\n\t" \
  "ds_read_b64 v[210:211], v232 offset:16800\n\t" \
  "ds_read_b64 v[212:213], v232 offset:17200\n\t" \
  "ds_read_b64 v[214:215], v232 offset:17600\n\t" \
  "ds_read_b64 v[216:217], v232 offset:16408\n\t" \
  "ds_read_b64 v[218:219], v232 offset:16808\n\t" \
  "ds_read_b64 v[220:221], v232 offset:17208\n\t" \
  "ds_read_b64 v[222:223], v232 offset:17608\n\t" \
  "s_add_u32 m0, %[xl], 49200\n\t" \
  "s_add_u32 %[xo], %[xo], %[rs]\n\t" \
  "buffer_load_dwordx4 %[xv], %[xrs], %[xo] offen lds\n\t" \
  "s_add_u32 m0, %[xl], 57400\n\t" \
  "s_add_u32 %[xo], %[xo], %[rs]\n\t" \
  "buffer_load_dwordx4 %[xv], %[xrs], %[xo] offen lds\n\t" \
  "v_smfmac_f32_16x16x64_bf16 v[188:191], v[128:131], v[224:231], v236\n\t" \
  "v_smfmac_f32_16x16x64_bf16 v[176:179], v[132:135], v[224:231], v236\n\t" \
  "v_smfmac_f32_16x16x64_bf16 v[164:167], v[136:139], v[224:231], v236\n\t" \
  "v_add_f32 v192, v192, v148\n\t" \
  "v_fmac_f32 v193, v148, v148\n\t" \
  "v_add_f32 v192, v192, v149\n\t" \
  "v_fmac_f32 v193, v149, v149\n\t" \
  "v_add_f32 v192, v192, v150\n\t" \
  "v_fmac_f32 v193, v150, v150\n\t" \
  "v_smfmac_f32_16x16x64_bf16 v[152:155], v[140:143], v[224:231], v236\n\t" \
  "ds_read_b64 v[224:225], v232 offset:16416\n\t" \
  "v_add_f32 v192, v192, v151\n\t" \
  "v_fmac_f32 v193, v151, v151\n\t" \
  "s_waitcnt lgkmcnt(5)\n\t" \
  "v_smfmac_f32_16x16x64_bf16 v[0:3], v[96:99], v[208:215], v236\n\t" \
  "ds_read_b64 v[226:227], v232 offset:16816\n\t" \
  "v_smfmac_f32_16x16x64_bf16 v[180:183], v[100:103], v[208:215], v236\n\t" \
  "ds_read_b64 v[228:229], v232 offset:17216\n\t" \
  "v_cvt_pk_bf16_f32 v80, v80, v81\n\t" \
  "v_cvt_pk_bf16_f32 v81, v82, v83\n\t" \
  "v_cvt_pk_bf16_f32 v82, v84, v85\n\t" \
  "v_cvt_pk_bf16_f32 v83, v86, v87\n\t" \
  "v_cvt_pk_bf16_f32 v84, v32, v33\n\t" \
  "v_cvt_pk_bf16_f32 v85, v34, v35\n\t" \
  "v_smfmac_f32_16x16x64_bf16 v[168:171], v[104:107], v[208:215], v236\n\t" \
  "ds_read_b64 v[230:231], v232 offset:17616\n\t" \
  "v_cvt_pk_bf16_f32 v86, v36, v37\n\t" \
  "v_add_f32 v192, v192, v152\n\t" \
  "v_cvt_pk_bf16_f32 v87, v38, v39\n\t" \
  "v_smfmac_f32_16x16x64_bf16 v[156:159], v[108:111], v[208:215], v236\n\t" \
  "v_cvt_pk_bf16_f32 v88, v88, v89\n\t" \
  "v_fmac_f32 v193, v152, v152\n\t" \
  "v_cvt_pk_bf16_f32 v89, v90, v91\n\t" \
  "s_waitcnt lgkmcnt(4)\n\t" \
  "v_smfmac_f32_16x16x64_bf16 v[4:7], v[112:115], v[216:223], v236\n\t" \
  "v_add_f32 v192, v192, v153\n\t" \
  "v_cvt_pk_bf16_f32 v90, v92, v93\n\t" \
  "v_fmac_f32 v193, v153, v153\n\t" \
  "v_smfmac_f32_16x16x64_bf16 v[184:187], v[116:119], v[216:223], v236\n\t" \
  "v_add_f32 v192, v192, v154\n\t" \
  "v_cvt_pk_bf16_f32 v91, v94, v95\n\t" \
  "v_fmac_f32 v193, v154, v154\n\t" \
  "v_smfmac_f32_16x16x64_bf16 v[172:175], v[120:123], v[216:223], v236\n\t" \
  "v_add_f32 v192, v192, v155\n\t" \
  "v_cvt_pk_bf16_f32 v92, v40, v41\n\t" \
  "v_fmac_f32 v193, v155, v155\n\t" \
  "v_smfmac_f32_16x16x64_bf16 v[160:163], v[124:127], v[216:223], v236\n\t" \
  "v_mul_f32 v195, v156, v156\n\t" \
  "v_cvt_pk_bf16_f32 v93, v42, v43\n\t" \
  "ds_write_b64 v233, v[192:193] offset:16400\n\t" \
  "s_waitcnt vmcnt(8) lgkmcnt(0)\n\t" \
  "s_barrier\n\t" \
  "ds_read_b64 v[208:209], v232 offset:32800\n\t" \
  "ds_read_b64 v[210:211], v232 offset:33200\n\t" \
  "ds_read_b64 v[212:213], v232 offset:33600\n\t" \
  "ds_read_b64 v[214:215], v232 offset:34000\n\t" \
  "ds_read_b64 v[216:217], v232 offset:32808\n\t" \
  "ds_read_b64 v[218:219], v232 offset:33208\n\t" \
  "ds_read_b64 v[220:221], v232 offset:33608\n\t" \
  "ds_read_b64 v[222:223], v232 offset:34008\n\t" \
  "s_add_u32 m0, %[xl], 65600\n\t" \
  "s_add_u32 %[xo], %[xo], %[rs]\n\t" \
  "buffer_load_dwordx4 %[xv], %[xrs], %[xo] offen lds\n\t" \
  "s_add_u32 m0, %[xl], 73800\n\t" \
  "s_add_u32 %[xo], %[xo], %[rs]\n\t" \
  "buffer_load_dwordx4 %[xv], %[xrs], %[xo] offen lds\n\t" \
  "v_smfmac_f32_16x16x64_bf16 v[8:11], v[128:131], v[224:231], v236\n\t" \
  "v_add_f32 v194, v156, v157\n\t" \
  "v_cvt_pk_bf16_f32 v94, v44, v45\n\t" \
  "v_fmac_f32 v195, v157, v157\n\t" \
  "v_smfmac_f32_16x16x64_bf16 v[188:191], v[132:135], v[224:231], v236\n\t" \
  "v_add_f32 v194, v194, v158\n\t" \
  "v_cvt_pk_bf16_f32 v95, v46, v47\n\t" \
  "v_fmac_f32 v195, v158, v158\n\t" \
  "v_smfmac_f32_16x16x64_bf16 v[176:179], v[136:139], v[224:231], v236\n\t" \
  "v_add_f32 v194, v194, v159\n\t" \
  "v_mfma_f32_32x32x16_bf16 v[32:47], v[242:245], v[240:243], 0\n\t" \
  "v_fmac_f32 v195, v159, v159\n\t" \
  "v_smfmac_f32_16x16x64_bf16 v[164:167], v[140:143], v[224:231], v236\n\t" \
  "ds_read_b64 v[224:225], v232 offset:32816\n\t" \
  "v_add_f32 v194, v194, v160\n\t" \
  "v_fmac_f32 v195, v160, v160\n\t" \
  "v_add_f32 v194, v194, v161\n\t" \
  "s_waitcnt lgkmcnt(5)\n\t" \
  "v_smfmac_f32_16x16x64_bf16 v[12:15], v[96:99], v[208:215], v236\n\t" \
  "ds_read_b64 v[226:227], v232 offset:33216\n\t" \
  "v_fmac_f32 v195, v161, v161\n\t" \
  "v_add_f32 v194, v194, v162\n\t" \
  "v_fmac_f32 v195, v162, v162\n\t" \
  "v_smfmac_f32_16x16x64_bf16 v[0:3], v[100:103], v[208:215], v236\n\t" \
  "ds_read_b64 v[228:229], v232 offset:33616\n\t" \
  "v_add_f32 v194, v194, v163\n\t" \
  "v_fmac_f32 v195, v163, v163\n\t" \
  "v_smfmac_f32_16x16x64_bf16 v[180:183], v[104:107], v[208:215], v236\n\t" \
  "buffer_store_dwordx4 v[52:55], %[vo], %[ors], %[op0] offen offset:0 sc1\n\t" \
  "ds_read_b64 v[230:231], v232 offset:34016\n\t" \
  "v_add_f32 v194, v194, v164\n\t" \
  "v_fmac_f32 v195, v164, v164\n\t" \
  "v_add_f32 v194, v194, v165\n\t" \
  "v_smfmac_f32_16x16x64_bf16 v[168:171], v[108:111], v[208:215], v236\n\t" \
  "v_fmac_f32 v195, v165, v165\n\t" \
  "v_add_f32 v194, v194, v166\n\t" \
  "v_fmac_f32 v195, v166, v166\n\t" \
  "s_waitcnt lgkmcnt(4)\n\t" \
  "v_smfmac_f32_16x16x64_bf16 v[16:19], v[112:115], v[216:223], v236\n\t" \
  "v_add_f32 v194, v194, v167\n\t" \
  "v_fmac_f32 v195, v167, v167\n\t" \
  "v_smfmac_f32_16x16x64_bf16 v[4:7], v[116:119], v[216:223], v236\n\t" \
  "ds_write_b64 v233, v[194:195] offset:20496\n\t" \
  "v_smfmac_f32_16x16x64_bf16 v[184:187], v[120:123], v[216:223], v236\n\t" \
  "v_mul_f32 v197, v168, v168\n\t" \
  "v_add_f32 v196, v168, v169\n\t" \
  "v_fmac_f32 v197, v169, v169\n\t" \
  "v_add_f32 v196, v196, v170\n\t" \
  "v_fmac_f32 v197, v170, v170\n\t" \
  "v_add_f32 v196, v196, v171\n\t" \
  "v_smfmac_f32_16x16x64_bf16 v[172:175], v[124:127], v[216:223], v236\n\t" \
  "v_fmac_f32 v197, v171, v171\n\t" \
  "s_waitcnt vmcnt(9) lgkmcnt(0)\n\t" \
  "s_barrier\n\t" \
  "ds_read_b64 v[208:209], v246 offset:0\n\t" \
  "ds_read_b64 v[210:211], v246 offset:400\n\t" \
  "ds_read_b64 v[212:213], v246 offset:800\n\t" \
  "ds_read_b64 v[214:215], v246 offset:1200\n\t" \
  "ds_read_b64 v[216:217], v246 offset:8\n\t" \
  "ds_read_b64 v[218:219], v246 offset:408\n\t" \
  "ds_read_b64 v[220:221], v246 offset:808\n\t" \
  "ds_read_b64 v[222:223], v246 offset:1208\n\t" \
  "v_smfmac_f32_16x16x64_bf16 v[20:23], v[128:131], v[224:231], v236\n\t" \
  "v_smfmac_f32_16x16x64_bf16 v[8:11], v[132:135], v[224:231], v236\n\t" \
  "buffer_store_dwordx4 v[60:63], %[vo], %[ors], %[op0] offen offset:64 sc1\n\t" \
  "v_smfmac_f32_16x16x64_bf16 v[188:191], v[136:139], v[224:231], v236\n\t" \
  "v_add_f32 v196, v196, v172\n\t" \
  "v_fmac_f32 v197, v172, v172\n\t" \
  "v_add_f32 v196, v196, v173\n\t" \
  "v_fmac_f32 v197, v173, v173\n\t" \
  "v_add_f32 v196, v196, v174\n\t" \
  "v_fmac_f32 v197, v174, v174\n\t" \
  "v_smfmac_f32_16x16x64_bf16 v[176:179], v[140:143], v[224:231], v236\n\t" \
  "ds_read_b64 v[224:225], v246 offset:16\n\t" \
  "v_add_f32 v196, v196, v175\n\t" \
  "v_fmac_f32 v197, v175, v175\n\t" \
  "s_waitcnt lgkmcnt(5)\n\t" \
  "v_smfmac_f32_16x16x64_bf16 v[24:27], v[96:99], v[208:215], v236\n\t" \
  "ds_read_b64 v[226:227], v246 offset:416\n\t" \
  "v_smfmac_f32_16x16x64_bf16 v[12:15], v[100:103], v[208:215], v236\n\t" \
  "ds_read_b64 v[228:229], v246 offset:816\n\t" \
  "v_smfmac_f32_16x16x64_bf16 v[0:3], v[104:107], v[208:215], v236\n\t" \
  "ds_read_b64 v[230:231], v246 offset:1216\n\t" \
  "v_add_f32 v196, v196, v176\n\t" \
  "v_fmac_f32 v197, v176, v176\n\t" \
  "v_add_f32 v196, v196, v177\n\t" \
  "v_fmac_f32 v197, v177, v177\n\t" \
  "v_add_f32 v196, v196, v178\n\t" \
  "v_fmac_f32 v197, v178, v178\n\t" \
  "v_smfmac_f32_16x16x64_bf16 v[180:183], v[108:111], v[208:215], v236\n\t" \
  "v_add_f32 v196, v196, v179\n\t" \
  "v_fmac_f32 v197, v179, v179\n\t" \
  "s_waitcnt lgkmcnt(4)\n\t" \
  "v_smfmac_f32_16x16x64_bf16 v[28:31], v[112:115], v[216:223], v236\n\t" \
  "buffer_store_dwordx4 v[68:71], %[vo], %[ors], %[op0] offen offset:128 sc1\n\t" \
  "v_smfmac_f32_16x16x64_bf16 v[16:19], v[116:119], v[216:223], v236\n\t" \
  "ds_write_b64 v233, v[196:197] offset:24592\n\t" \
  "v_smfmac_f32_16x16x64_bf16 v[4:7], v[120:123], v[216:223], v236\n\t" \
  "v_mul_f32 v199, v180, v180\n\t" \
  "v_add_f32 v198, v180, v181\n\t" \
  "v_fmac_f32 v199, v181, v181\n\t" \
  "v_add_f32 v198, v198, v182\n\t" \
  "v_fmac_f32 v199, v182, v182\n\t" \
  "v_add_f32 v198, v198, v183\n\t" \
  "v_smfmac_f32_16x16x64_bf16 v[184:187], v[124:127], v[216:223], v236\n\t" \
  "v_fmac_f32 v199, v183, v183\n\t" \
  "s_waitcnt vmcnt(9) lgkmcnt(0)\n\t" \
  "s_barrier\n\t" \
  "ds_read_b64 v[208:209], v246 offset:16400\n\t" \
  "ds_read_b64 v[210:211], v246 offset:16800\n\t" \
  "ds_read_b64 v[212:213], v246 offset:17200\n\t" \
  "ds_read_b64 v[214:215], v246 offset:17600\n\t" \
  "ds_read_b64 v[216:217], v246 offset:16408\n\t" \
  "ds_read_b64 v[218:219], v246 offset:16808\n\t" \
  "ds_read_b64 v[220:221], v246 offset:17208\n\t" \
  "ds_read_b64 v[222:223], v246 offset:17608\n\t" \
  "v_smfmac_f32_16x16x64_bf16 v[32:35], v[128:131], v[224:231], v236\n\t" \
  "v_smfmac_f32_16x16x64_bf16 v[20:23], v[132:135], v[224:231], v236\n\t" \
  "v_smfmac_f32_16x16x64_bf16 v[8:11], v[136:139], v[224:231], v236\n\t" \
  "v_add_f32 v198, v198, v184\n\t" \
  "v_fmac_f32 v199, v184, v184\n\t" \
  "v_add_f32 v198, v198, v185\n\t" \
  "v_fmac_f32 v199, v185, v185\n\t" \
  "v_add_f32 v198, v198, v186\n\t" \
  "v_smfmac_f32_16x16x64_bf16 v[188:191], v[140:143], v[224:231], v236\n\t" \
  "buffer_store_dwordx4 v[76:79], %[vo], %[ors], %[op0] offen offset:192 sc1\n\t" \
  "ds_read_b64 v[224:225], v246 offset:16416\n\t" \
  "v_fmac_f32 v199, v186, v186\n\t" \
  "v_add_f32 v198, v198, v187\n\t" \
  "v_fmac_f32 v199, v187, v187\n\t" \
  "s_waitcnt lgkmcnt(5)\n\t" \
  "v_smfmac_f32_16x16x64_bf16 v[36:39], v[96:99], v[208:215], v236\n\t" \
  "ds_read_b64 v[226:227], v246 offset:16816\n\t" \
  "v_smfmac_f32_16x16x64_bf16 v[24:27], v[100:103], v[208:215], v236\n\t" \
  "ds_read_b64 v[228:229], v246 offset:17216\n\t" \
  "v_smfmac_f32_16x16x64_bf16 v[12:15], v[104:107], v[208:215], v236\n\t" \
  "ds_read_b64 v[230:231], v246 offset:17616\n\t" \
  "v_add_f32 v198, v198, v188\n\t" \
  "v_fmac_f32 v199, v188, v188\n\t" \
  "v_add_f32 v198, v198, v189\n\t" \
  "v_fmac_f32 v199, v189, v189\n\t" \
  "v_add_f32 v198, v198, v190\n\t" \
  "v_fmac_f32 v199, v190, v190\n\t" \
  "v_smfmac_f32_16x16x64_bf16 v[0:3], v[108:111], v[208:215], v236\n\t" \
  "v_add_f32 v198, v198, v191\n\t" \
  "v_fmac_f32 v199, v191, v191\n\t" \
  "s_waitcnt lgkmcnt(4)\n\t" \
  "v_smfmac_f32_16x16x64_bf16 v[40:43], v[112:115], v[216:223], v236\n\t" \
  "ds_write_b64 v233, v[198:199] offset:28688\n\t" \
  "v_smfmac_f32_16x16x64_bf16 v[28:31], v[116:119], v[216:223], v236\n\t" \
  "v_smfmac_f32_16x16x64_bf16 v[16:19], v[120:123], v[216:223], v236\n\t" \
  "buffer_store_dwordx4 v[84:87], %[vo], %[ors], %[op0] offen offset:256 sc1\n\t" \
  "v_mul_f32 v201, v0, v0\n\t" \
  "v_add_f32 v200, v0, v1\n\t" \
  "v_fmac_f32 v201, v1, v1\n\t" \
  "v_add_f32 v200, v200, v2\n\t" \
  "v_fmac_f32 v201, v2, v2\n\t" \
  "v_add_f32 v200, v200, v3\n\t" \
  "v_smfmac_f32_16x16x64_bf16 v[4:7], v[124:127], v[216:223], v236\n\t" \
  "v_fmac_f32 v201, v3, v3\n\t" \
  "s_waitcnt vmcnt(9) lgkmcnt(0)\n\t" \
  "s_barrier\n\t" \
  "ds_read_b64 v[208:209], v246 offset:32800\n\t" \
  "ds_read_b64 v[210:211], v246 offset:33200\n\t" \
  "ds_read_b64 v[212:213], v246 offset:33600\n\t" \
  "ds_read_b64 v[214:215], v246 offset:34000\n\t" \
  "ds_read_b64 v[216:217], v246 offset:32808\n\t" \
  "ds_read_b64 v[218:219], v246 offset:33208\n\t" \
  "ds_read_b64 v[220:221], v246 offset:33608\n\t" \
  "ds_read_b64 v[222:223], v246 offset:34008\n\t" \
  "v_smfmac_f32_16x16x64_bf16 v[44:47], v[128:131], v[224:231], v236\n\t" \
  "v_smfmac_f32_16x16x64_bf16 v[32:35], v[132:135], v[224:231], v236\n\t" \
  "v_smfmac_f32_16x16x64_bf16 v[20:23], v[136:139], v[224:231], v236\n\t" \
  "v_add_f32 v200, v200, v4\n\t" \
  "v_fmac_f32 v201, v4, v4\n\t" \
  "v_add_f32 v200, v200, v5\n\t" \
  "v_fmac_f32 v201, v5, v5\n\t" \
  "v_add_f32 v200, v200, v6\n\t" \
  "v_fmac_f32 v201, v6, v6\n\t" \
  "v_smfmac_f32_16x16x64_bf16 v[8:11], v[140:143], v[224:231], v236\n\t" \
  "ds_read_b64 v[224:225], v246 offset:32816\n\t" \
  "v_add_f32 v200, v200, v7\n\t" \
  "v_fmac_f32 v201, v7, v7\n\t" \
  "s_waitcnt lgkmcnt(5)\n\t" \
  "v_smfmac_f32_16x16x64_bf16 v[36:39], v[100:103], v[208:215], v236\n\t" \
  "ds_read_b64 v[226:227], v246 offset:33216\n\t" \
  "v_smfmac_f32_16x16x64_bf16 v[24:27], v[104:107], v[208:215], v236\n\t" \
  "buffer_store_dwordx4 v[92:95], %[vo], %[ors], %[op0] offen offset:320 sc1\n\t" \
  "ds_read_b64 v[228:229], v246 offset:33616\n\t" \
  "v_smfmac_f32_16x16x64_bf16 v[12:15], v[108:111], v[208:215], v236\n\t" \
  "ds_read_b64 v[230:231], v246 offset:34016\n\t" \
  "v_add_f32 v200, v200, v8\n\t" \
  "v_fmac_f32 v201, v8, v8\n\t" \
  "v_add_f32 v200, v200, v9\n\t" \
  "v_fmac_f32 v201, v9, v9\n\t" \
  "v_add_f32 v200, v200, v10\n\t" \
  "v_fmac_f32 v201, v10, v10\n\t" \
  "s_waitcnt lgkmcnt(4)\n\t" \
  "v_smfmac_f32_16x16x64_bf16 v[40:43], v[116:119], v[216:223], v236\n\t" \
  "v_add_f32 v200, v200, v11\n\t" \
  "v_fmac_f32 v201, v11, v11\n\t" \
  "v_smfmac_f32_16x16x64_bf16 v[28:31], v[120:123], v[216:223], v236\n\t" \
  "ds_write_b64 v233, v[200:201] offset:0\n\t" \
  "v_smfmac_f32_16x16x64_bf16 v[16:19], v[124:127], v[216:223], v236\n\t" \
  "v_mul_f32 v203, v12, v12\n\t" \
  "v_add_f32 v202, v12, v13\n\t" \
  "v_fmac_f32 v203, v13, v13\n\t" \
  "v_add_f32 v202, v202, v14\n\t" \
  "v_fmac_f32 v203, v14, v14\n\t" \
  "v_add_f32 v202, v202, v15\n\t" \
  "s_waitcnt vmcnt(8) lgkmcnt(0)\n\t" \
  "s_barrier\n\t" \
  "ds_read_b64 v[208:209], v232 offset:0\n\t" \
  "ds_read_b64 v[210:211], v232 offset:400\n\t" \
  "ds_read_b64 v[212:213], v232 offset:800\n\t" \
  "ds_read_b64 v[214:215], v232 offset:1200\n\t" \
  "ds_read_b64 v[216:217], v232 offset:8\n\t" \
  "ds_read_b64 v[218:219], v232 offset:408\n\t" \
  "ds_read_b64 v[220:221], v232 offset:808\n\t" \
  "ds_read_b64 v[222:223], v232 offset:1208\n\t" \
  "s_cmp_lg_u32 %[grp], 0\n\t" \
  "s_cbranch_scc1 .Lcnx_c2\n\t" \
  "v_add_u32 v128, %[sr], %[l8]\n\t" \
  "ds_read_b64 v[96:97], v128 offset:0\n\t" \
  "ds_read_b64 v[98:99], v128 offset:512\n\t" \
  "ds_read_b64 v[100:101], v128 offset:1024\n\t" \
  "ds_read_b64 v[102:103], v128 offset:1536\n\t" \
  "ds_read_b64 v[112:113], v128 offset:2048\n\t" \
  "ds_read_b64 v[114:115], v128 offset:2560\n\t" \
  "ds_read_b64 v[116:117], v128 offset:3072\n\t" \
  "ds_read_b64 v[118:119], v128 offset:3584\n\t" \
  ".Lcnx_c2:\n\t" \
  "v_smfmac_f32_16x16x64_bf16 v[44:47], v[132:135], v[224:231], v236\n\t" \
  "v_fmac_f32 v203, v15, v15\n\t" \
  "v_smfmac_f32_16x16x64_bf16 v[32:35], v[136:139], v[224:231], v236\n\t" \
  "v_smfmac_f32_16x16x64_bf16 v[20:23], v[140:143], v[224:231], v236\n\t" \
  "buffer_store_dwordx4 v[48:51], %[vo], %[ors], %[op1] offen offset:0 sc1\n\t" \
  "ds_read_b64 v[224:225], v232 offset:16\n\t" \
  "v_add_f32 v202, v202, v16\n\t" \
  "v_fmac_f32 v203, v16, v16\n\t" \
  "v_add_f32 v202, v202, v17\n\t" \
  "v_fmac_f32 v203, v17, v17\n\t" \
  "v_add_f32 v202, v202, v18\n\t" \
  "v_fmac_f32 v203, v18, v18\n\t" \
  "s_waitcnt lgkmcnt(5)\n\t" \
  "v_smfmac_f32_16x16x64_bf16 v[36:39], v[104:107], v[208:215], v236\n\t" \
  "ds_read_b64 v[226:227], v232 offset:416\n\t" \
  "ds_read_b64 v[228:229], v232 offset:816\n\t" \
  "ds_read_b64 v[230:231], v232 offset:1216\n\t" \
  "v_add_f32 v202, v202, v19\n\t" \
  "v_fmac_f32 v203, v19, v19\n\t" \
  "v_smfmac_f32_16x16x64_bf16 v[24:27], v[108:111], v[208:215], v236\n\t" \
  "s_waitcnt lgkmcnt(4)\n\t" \
  "v_smfmac_f32_16x16x64_bf16 v[40:43], v[120:123], v[216:223], v236\n\t" \
  "v_add_f32 v202, v202, v20\n\t" \
  "v_fmac_f32 v203, v20, v20\n\t" \
  "v_add_f32 v202, v202, v21\n\t" \
  "v_fmac_f32 v203, v21, v21\n\t" \
  "v_add_f32 v202, v202, v22\n\t" \
  "v_fmac_f32 v203, v22, v22\n\t" \
  "v_smfmac_f32_16x16x64_bf16 v[28:31], v[124:127], v[216:223], v236\n\t" \
  "v_add_f32 v202, v202, v23\n\t" \
  "v_fmac_f32 v203, v23, v23\n\t" \
  "ds_write_b64 v233, v[202:203] offset:4096\n\t" \
  "s_waitcnt vmcnt(7) lgkmcnt(0)\n\t" \
  "s_barrier\n\t" \
  "ds_read_b64 v[208:209], v232 offset:16400\n\t" \
  "ds_read_b64 v[210:211], v232 offset:16800\n\t" \
  "ds_read_b64 v[212:213], v232 offset:17200\n\t" \
  "ds_read_b64 v[214:215], v232 offset:17600\n\t" \
  "ds_read_b64 v[216:217], v232 offset:16408\n\t" \
  "ds_read_b64 v[218:219], v232 offset:16808\n\t" \
  "ds_read_b64 v[220:221], v232 offset:17208\n\t" \
  "ds_read_b64 v[222:223], v232 offset:17608\n\t" \
  "s_cmp_ge_u32 %[f0], 2\n\t" \
  "s_cbranch_scc1 .Lcnx_c3\n\t" \
  "s_add_u32 m0, %[wl], %[ws]\n\t" \
  "s_nop 0\n\t" \
  "buffer_load_dwordx4 %[wv], %[wrs], %[so0] offen lds\n\t" \
  "buffer_load_dwordx4 %[wv], %[wrs], %[so1] offen offset:1024 lds\n\t" \
  "buffer_load_dwordx4 %[wv], %[wrs], %[so2] offen offset:2048 lds\n\t" \
  "s_add_u32 %[so0], %[so0], %[wc]\n\t" \
  "s_add_u32 %[so1], %[so1], %[wc]\n\t" \
  "s_add_u32 %[so2], %[so2], %[wc]\n\t" \
  "s_add_u32 m0, %[wl], 0\n\t" \
  "s_nop 0\n\t" \
  "buffer_load_dwordx4 %[wv], %[wrs], %[so0] offen lds\n\t" \
  "buffer_load_dwordx4 %[wv], %[wrs], %[so1] offen offset:1024 lds\n\t" \
  "buffer_load_dwordx4 %[wv], %[wrs], %[so2] offen offset:2048 lds\n\t" \
  "s_add_u32 %[so0], %[so0], %[wc]\n\t" \
  "s_add_u32 %[so1], %[so1], %[wc]\n\t" \
  "s_add_u32 %[so2], %[so2], %[wc]\n\t" \
  ".Lcnx_c3:\n\t" \
  "v_smfmac_f32_16x16x64_bf16 v[44:47], v[136:139], v[224:231], v236\n\t" \
  "v_mul_f32 v205, v24, v24\n\t" \
  "v_add_f32 v204, v24, v25\n\t" \
  "v_smfmac_f32_16x16x64_bf16 v[32:35], v[140:143], v[224:231], v236\n\t" \
  "buffer_store_dwordx4 v[56:59], %[vo], %[ors], %[op1] offen offset:64 sc1\n\t" \
  "ds_read_b64 v[224:225], v232 offset:16416\n\t" \
  "ds_read_b64 v[226:227], v232 offset:16816\n\t" \
  "v_fmac_f32 v205, v25, v25\n\t" \
  "v_add_f32 v204, v204, v26\n\t" \
  "v_fmac_f32 v205, v26, v26\n\t" \
  "s_cmp_lg_u32 %[grp], 0\n\t" \
  "s_cbranch_scc1 .Lcnx_c4\n\t" \
  "s_waitcnt lgkmcnt(15)\n\t" \
  "v_add_f32 v96, v96, v98\n\t" \
  "v_add_f32 v97, v97, v99\n\t" \
  "v_add_f32 v100, v100, v102\n\t" \
  "v_add_f32 v101, v101, v103\n\t" \
  "v_add_f32 v112, v112, v114\n\t" \
  "v_add_f32 v113, v113, v115\n\t" \
  "v_add_f32 v116, v116, v118\n\t" \
  "v_add_f32 v117, v117, v119\n\t" \
  "v_add_f32 v96, v96, v100\n\t" \
  "v_add_f32 v97, v97, v101\n\t" \
  "v_add_f32 v112, v112, v116\n\t" \
  "v_add_f32 v113, v113, v117\n\t" \
  "v_add_f32 v96, v96, v112\n\t" \
  "v_add_f32 v97, v97, v113\n\t" \
  "v_mul_f32 v96, 0x3c2aaaab, v96\n\t" \
  "v_mul_f32 v97, 0x3c2aaaab, v97\n\t" \
  "v_fma_f32 v97, -v96, v96, v97\n\t" \
  "v_max_f32 v97, 0, v97\n\t" \
  "v_add_f32 v97, 0x3727c5ac, v97\n\t" \
  "v_rsq_f32 v97, v97\n\t" \
  "s_nop 0\n\t" \
  "v_mul_f32 v98, 0x3e000000, v97\n\t" \
  "v_mul_f32 v99, -v96, v98\n\t" \
  "ds_write_b64 v233, v[98:99] offset:32784\n\t" \
  ".Lcnx_c4:\n\t" \
  "s_waitcnt lgkmcnt(6)\n\t" \
  "v_smfmac_f32_16x16x64_bf16 v[36:39], v[108:111], v[208:215], v236\n\t" \
  "ds_read_b64 v[228:229], v232 offset:17216\n\t" \
  "ds_read_b64 v[230:231], v232 offset:17616\n\t" \
  "v_add_f32 v204, v204, v27\n\t" \
  "v_fmac_f32 v205, v27, v27\n\t" \
  "v_add_f32 v204, v204, v28\n\t" \
  "s_waitcnt lgkmcnt(4)\n\t" \
  "v_smfmac_f32_16x16x64_bf16 v[40:43], v[124:127], v[216:223], v236\n\t" \
  "v_fmac_f32 v205, v28, v28\n\t" \
  "v_add_f32 v204, v204, v29\n\t" \
  "v_fmac_f32 v205, v29, v29\n\t" \
  "s_waitcnt lgkmcnt(0)\n\t" \
  "v_smfmac_f32_16x16x64_bf16 v[44:47], v[140:143], v[224:231], v236\n\t" \
  "v_add_f32 v204, v204, v30\n\t" \
  "v_fmac_f32 v205, v30, v30\n\t" \
  "v_add_f32 v204, v204, v31\n\t" \
  "v_fmac_f32 v205, v31, v31\n\t" \
  "v_add_f32 v204, v204, v32\n\t" \
  "v_fmac_f32 v205, v32, v32\n\t" \
  "v_add_f32 v204, v204, v33\n\t" \
  "v_fmac_f32 v205, v33, v33\n\t" \
  "v_add_f32 v204, v204, v34\n\t" \
  "v_fmac_f32 v205, v34, v34\n\t" \
  "v_add_f32 v204, v204, v35\n\t" \
  "v_fmac_f32 v205, v35, v35\n\t" \
  "v_mul_f32 v207, v36, v36\n\t" \
  "v_add_f32 v206, v36, v37\n\t" \
  "v_fmac_f32 v207, v37, v37\n\t" \
  "v_add_f32 v206, v206, v38\n\t" \
  "v_fmac_f32 v207, v38, v38\n\t" \
  "v_add_f32 v206, v206, v39\n\t" \
  "v_fmac_f32 v207, v39, v39\n\t" \
  "v_add_f32 v206, v206, v40\n\t" \
  "v_fmac_f32 v207, v40, v40\n\t" \
  "v_add_f32 v206, v206, v41\n\t" \
  "v_fmac_f32 v207, v41, v41\n\t" \
  "v_add_f32 v206, v206, v42\n\t" \
  "v_fmac_f32 v207, v42, v42\n\t" \
  "v_add_f32 v206, v206, v43\n\t" \
  "v_fmac_f32 v207, v43, v43\n\t" \
  "v_add_f32 v206, v206, v44\n\t" \
  "v_fmac_f32 v207, v44, v44\n\t" \
  "v_add_f32 v206, v206, v45\n\t" \
  "v_fmac_f32 v207, v45, v45\n\t" \
  "v_add_f32 v206, v206, v46\n\t" \
  "v_fmac_f32 v207, v46, v46\n\t" \
  "v_add_f32 v206, v206, v47\n\t" \
  "v_fmac_f32 v207, v47, v47\n\t" \
  "ds_write_b64 v233, v[204:205] offset:8192\n\t" \
  "ds_write_b64 v233, v[206:207] offset:12288\n\t" \
  "s_waitcnt lgkmcnt(0)\n\t" \
  "s_barrier\n\t" \
  "ds_read_b64 v[192:193], %[l8] offset:32784\n\t" \
  "ds_read_b64 v[194:195], %[l8] offset:33296\n\t" \
  "ds_read_b64 v[196:197], %[l8] offset:33808\n\t" \
  "ds_read_b64 v[198:199], %[l8] offset:34320\n\t" \
  "s_cmp_lg_u32 %[grp], 1\n\t" \
  "s_cbranch_scc1 .Lcnx_c5\n\t" \
  "v_add_u32 v128, %[sr], %[l8]\n\t" \
  "ds_read_b64 v[96:97], v128 offset:0\n\t" \
  "ds_read_b64 v[98:99], v128 offset:512\n\t" \
  "ds_read_b64 v[100:101], v128 offset:1024\n\t" \
  "ds_read_b64 v[102:103], v128 offset:1536\n\t" \
  "ds_read_b64 v[112:113], v128 offset:2048\n\t" \
  "ds_read_b64 v[114:115], v128 offset:2560\n\t" \
  "ds_read_b64 v[116:117], v128 offset:3072\n\t" \
  "ds_read_b64 v[118:119], v128 offset:3584\n\t" \
  ".Lcnx_c5:\n\t" \
  "v_lshl_add_u32 v208, %[l8], 1, %[z1]\n\t" \
  "v_lshl_add_u32 v209, %[l8], 1, %[z2]\n\t" \
  "s_waitcnt lgkmcnt(3)\n\t" \
  "v_fma_f32 v144, v144, v192, v193\n\t" \
  "v_fma_f32 v145, v145, v192, v193\n\t" \
  "v_fma_f32 v146, v146, v192, v193\n\t" \
  "v_fma_f32 v147, v147, v192, v193\n\t" \
  "v_fma_f32 v148, v148, v192, v193\n\t" \
  "v_fma_f32 v149, v149, v192, v193\n\t" \
  "v_fma_f32 v150, v150, v192, v193\n\t" \
  "v_fma_f32 v151, v151, v192, v193\n\t" \
  "v_fma_f32 v152, v152, v192, v193\n\t" \
  "v_fma_f32 v153, v153, v192, v193\n\t" \
  "v_fma_f32 v154, v154, v192, v193\n\t" \
  "v_fma_f32 v155, v155, v192, v193\n\t" \
  "v_cvt_pk_bf16_f32 v144, v144, v145\n\t" \
  "v_cvt_pk_bf16_f32 v145, v146, v147\n\t" \
  "v_cvt_pk_bf16_f32 v146, v148, v149\n\t" \
  "v_cvt_pk_bf16_f32 v147, v150, v151\n\t" \
  "v_cvt_pk_bf16_f32 v148, v152, v153\n\t" \
  "v_cvt_pk_bf16_f32 v149, v154, v155\n\t" \
  "ds_write_b128 v208, v[144:147] offset:0\n\t" \
  "ds_write_b64 v209, v[148:149] offset:0\n\t" \
  "buffer_store_dwordx4 v[64:67], %[vo], %[ors], %[op1] offen offset:128 sc1\n\t" \
  "s_cmp_lg_u32 %[grp], 1\n\t" \
  "s_cbranch_scc1 .Lcnx_c6\n\t" \
  "s_waitcnt lgkmcnt(2)\n\t" \
  "v_add_f32 v96, v96, v98\n\t" \
  "v_add_f32 v97, v97, v99\n\t" \
  "v_add_f32 v100, v100, v102\n\t" \
  "v_add_f32 v101, v101, v103\n\t" \
  "v_add_f32 v112, v112, v114\n\t" \
  "v_add_f32 v113, v113, v115\n\t" \
  "v_add_f32 v116, v116, v118\n\t" \
  "v_add_f32 v117, v117, v119\n\t" \
  "v_add_f32 v96, v96, v100\n\t" \
  "v_add_f32 v97, v97, v101\n\t" \
  "v_add_f32 v112, v112, v116\n\t" \
  "v_add_f32 v113, v113, v117\n\t" \
  "v_add_f32 v96, v96, v112\n\t" \
  "v_add_f32 v97, v97, v113\n\t" \
  "v_mul_f32 v96, 0x3c2aaaab, v96\n\t" \
  "v_mul_f32 v97, 0x3c2aaaab, v97\n\t" \
  "v_fma_f32 v97, -v96, v96, v97\n\t" \
  "v_max_f32 v97, 0, v97\n\t" \
  "v_add_f32 v97, 0x3727c5ac, v97\n\t" \
  "v_rsq_f32 v97, v97\n\t" \
  "s_nop 0\n\t" \
  "v_mul_f32 v98, 0x3e000000, v97\n\t" \
  "v_mul_f32 v99, -v96, v98\n\t" \
  "ds_write_b64 v233, v[98:99] offset:14352\n\t" \
  ".Lcnx_c6:\n\t" \
  "s_waitcnt lgkmcnt(4)\n\t" \
  "v_fma_f32 v156, v156, v194, v195\n\t" \
  "v_fma_f32 v157, v157, v194, v195\n\t" \
  "v_fma_f32 v158, v158, v194, v195\n\t" \
  "v_fma_f32 v159, v159, v194, v195\n\t" \
  "v_fma_f32 v160, v160, v194, v195\n\t" \
  "v_fma_f32 v161, v161, v194, v195\n\t" \
  "v_fma_f32 v162, v162, v194, v195\n\t" \
  "v_fma_f32 v163, v163, v194, v195\n\t" \
  "v_fma_f32 v164, v164, v194, v195\n\t" \
  "v_fma_f32 v165, v165, v194, v195\n\t" \
  "v_fma_f32 v166, v166, v194, v195\n\t" \
  "v_fma_f32 v167, v167, v194, v195\n\t" \
  "v_cvt_pk_bf16_f32 v156, v156, v157\n\t" \
  "v_cvt_pk_bf16_f32 v157, v158, v159\n\t" \
  "v_cvt_pk_bf16_f32 v158, v160, v161\n\t" \
  "v_cvt_pk_bf16_f32 v159, v162, v163\n\t" \
  "v_cvt_pk_bf16_f32 v160, v164, v165\n\t" \
  "v_cvt_pk_bf16_f32 v161, v166, v167\n\t" \
  "ds_write_b128 v208, v[156:159] offset:12288\n\t" \
  "ds_write_b64 v209, v[160:161] offset:12288\n\t" \
  "s_waitcnt lgkmcnt(5)\n\t" \
  "v_fma_f32 v168, v168, v196, v197\n\t" \
  "v_fma_f32 v169, v169, v196, v197\n\t" \
  "v_fma_f32 v170, v170, v196, v197\n\t" \
  "v_fma_f32 v171, v171, v196, v197\n\t" \
  "v_fma_f32 v172, v172, v196, v197\n\t" \
  "v_fma_f32 v173, v173, v196, v197\n\t" \
  "v_fma_f32 v174, v174, v196, v197\n\t" \
  "v_fma_f32 v175, v175, v196, v197\n\t" \
  "v_fma_f32 v176, v176, v196, v197\n\t" \
  "v_fma_f32 v177, v177, v196, v197\n\t" \
  "v_fma_f32 v178, v178, v196, v197\n\t" \
  "v_fma_f32 v179, v179, v196, v197\n\t" \
  "v_cvt_pk_bf16_f32 v168, v168, v169\n\t" \
  "v_cvt_pk_bf16_f32 v169, v170, v171\n\t" \
  "v_cvt_pk_bf16_f32 v170, v172, v173\n\t" \
  "v_cvt_pk_bf16_f32 v171, v174, v175\n\t" \
  "v_cvt_pk_bf16_f32 v172, v176, v177\n\t" \
  "v_cvt_pk_bf16_f32 v173, v178, v179\n\t" \
  "ds_write_b128 v208, v[168:171] offset:24576\n\t" \
  "ds_write_b64 v209, v[172:173] offset:24576\n\t" \
  "s_waitcnt lgkmcnt(6)\n\t" \
  "v_fma_f32 v180, v180, v198, v199\n\t" \
  "v_fma_f32 v181, v181, v198, v199\n\t" \
  "v_fma_f32 v182, v182, v198, v199\n\t" \
  "v_fma_f32 v183, v183, v198, v199\n\t" \
  "v_fma_f32 v184, v184, v198, v199\n\t" \
  "v_fma_f32 v185, v185, v198, v199\n\t" \
  "v_fma_f32 v186, v186, v198, v199\n\t" \
  "v_fma_f32 v187, v187, v198, v199\n\t" \
  "v_fma_f32 v188, v188, v198, v199\n\t" \
  "v_fma_f32 v189, v189, v198, v199\n\t" \
  "v_fma_f32 v190, v190, v198, v199\n\t" \
  "v_fma_f32 v191, v191, v198, v199\n\t" \
  "v_cvt_pk_bf16_f32 v180, v180, v181\n\t" \
  "v_cvt_pk_bf16_f32 v181, v182, v183\n\t" \
  "v_cvt_pk_bf16_f32 v182, v184, v185\n\t" \
  "v_cvt_pk_bf16_f32 v183, v186, v187\n\t" \
  "v_cvt_pk_bf16_f32 v184, v188, v189\n\t" \
  "v_cvt_pk_bf16_f32 v185, v190, v191\n\t" \
  "ds_write_b128 v208, v[180:183] offset:36864\n\t" \
  "ds_write_b64 v209, v[184:185] offset:36864\n\t" \
  "v_add_u32 v208, 49152, v208\n\t" \
  "v_add_u32 v209, 49152, v209\n\t" \
  "s_waitcnt lgkmcnt(0)\n\t" \
  "s_barrier\n\t" \
  "ds_read_b64 v[200:201], %[l8] offset:16400\n\t" \
  "ds_read_b64 v[202:203], %[l8] offset:16912\n\t" \
  "ds_read_b64 v[204:205], %[l8] offset:17424\n\t" \
  "ds_read_b64 v[206:207], %[l8] offset:17936\n\t" \
  "s_waitcnt lgkmcnt(3)\n\t" \
  "v_fma_f32 v0, v0, v200, v201\n\t" \
  "v_fma_f32 v1, v1, v200, v201\n\t" \
  "v_fma_f32 v2, v2, v200, v201\n\t" \
  "v_fma_f32 v3, v3, v200, v201\n\t" \
  "v_fma_f32 v4, v4, v200, v201\n\t" \
  "v_fma_f32 v5, v5, v200, v201\n\t" \
  "v_fma_f32 v6, v6, v200, v201\n\t" \
  "v_fma_f32 v7, v7, v200, v201\n\t" \
  "v_fma_f32 v8, v8, v200, v201\n\t" \
  "v_fma_f32 v9, v9, v200, v201\n\t" \
  "v_fma_f32 v10, v10, v200, v201\n\t" \
  "v_fma_f32 v11, v11, v200, v201\n\t" \
  "v_cvt_pk_bf16_f32 v0, v0, v1\n\t" \
  "v_cvt_pk_bf16_f32 v1, v2, v3\n\t" \
  "v_cvt_pk_bf16_f32 v2, v4, v5\n\t" \
  "v_cvt_pk_bf16_f32 v3, v6, v7\n\t" \
  "v_cvt_pk_bf16_f32 v4, v8, v9\n\t" \
  "v_cvt_pk_bf16_f32 v5, v10, v11\n\t" \
  "ds_write_b128 v208, v[0:3] offset:0\n\t" \
  "ds_write_b64 v209, v[4:5] offset:0\n\t" \
  "buffer_store_dwordx4 v[72:75], %[vo], %[ors], %[op1] offen offset:192 sc1\n\t" \
  "s_waitcnt lgkmcnt(4)\n\t" \
  "v_fma_f32 v12, v12, v202, v203\n\t" \
  "v_fma_f32 v13, v13, v202, v203\n\t" \
  "v_fma_f32 v14, v14, v202, v203\n\t" \
  "v_fma_f32 v15, v15, v202, v203\n\t" \
  "v_fma_f32 v16, v16, v202, v203\n\t" \
  "v_fma_f32 v17, v17, v202, v203\n\t" \
  "v_fma_f32 v18, v18, v202, v203\n\t" \
  "v_fma_f32 v19, v19, v202, v203\n\t" \
  "v_fma_f32 v20, v20, v202, v203\n\t" \
  "v_fma_f32 v21, v21, v202, v203\n\t" \
  "v_fma_f32 v22, v22, v202, v203\n\t" \
  "v_fma_f32 v23, v23, v202, v203\n\t" \
  "v_cvt_pk_bf16_f32 v12, v12, v13\n\t" \
  "v_cvt_pk_bf16_f32 v13, v14, v15\n\t" \
  "v_cvt_pk_bf16_f32 v14, v16, v17\n\t" \
  "v_cvt_pk_bf16_f32 v15, v18, v19\n\t" \
  "v_cvt_pk_bf16_f32 v16, v20, v21\n\t" \
  "v_cvt_pk_bf16_f32 v17, v22, v23\n\t" \
  "ds_write_b128 v208, v[12:15] offset:12288\n\t" \
  "ds_write_b64 v209, v[16:17] offset:12288\n\t" \
  "s_waitcnt lgkmcnt(5)\n\t" \
  "v_fma_f32 v24, v24, v204, v205\n\t" \
  "v_fma_f32 v25, v25, v204, v205\n\t" \
  "v_fma_f32 v26, v26, v204, v205\n\t" \
  "v_fma_f32 v27, v27, v204, v205\n\t" \
  "v_fma_f32 v28, v28, v204, v205\n\t" \
  "v_fma_f32 v29, v29, v204, v205\n\t" \
  "v_fma_f32 v30, v30, v204, v205\n\t" \
  "v_fma_f32 v31, v31, v204, v205\n\t" \
  "v_fma_f32 v32, v32, v204, v205\n\t" \
  "v_fma_f32 v33, v33, v204, v205\n\t" \
  "v_fma_f32 v34, v34, v204, v205\n\t" \
  "v_fma_f32 v35, v35, v204, v205\n\t" \
  "v_cvt_pk_bf16_f32 v24, v24, v25\n\t" \
  "v_cvt_pk_bf16_f32 v25, v26, v27\n\t" \
  "v_cvt_pk_bf16_f32 v26, v28, v29\n\t" \
  "v_cvt_pk_bf16_f32 v27, v30, v31\n\t" \
  "v_cvt_pk_bf16_f32 v28, v32, v33\n\t" \
  "v_cvt_pk_bf16_f32 v29, v34, v35\n\t" \
  "ds_write_b128 v208, v[24:27] offset:24576\n\t" \
  "ds_write_b64 v209, v[28:29] offset:24576\n\t" \
  "s_waitcnt lgkmcnt(6)\n\t" \
  "v_fma_f32 v36, v36, v206, v207\n\t" \
  "v_fma_f32 v37, v37, v206, v207\n\t" \
  "v_fma_f32 v38, v38, v206, v207\n\t" \
  "v_fma_f32 v39, v39, v206, v207\n\t" \
  "v_fma_f32 v40, v40, v206, v207\n\t" \
  "v_fma_f32 v41, v41, v206, v207\n\t" \
  "v_fma_f32 v42, v42, v206, v207\n\t" \
  "v_fma_f32 v43, v43, v206, v207\n\t" \
  "v_fma_f32 v44, v44, v206, v207\n\t" \
  "v_fma_f32 v45, v45, v206, v207\n\t" \
  "v_fma_f32 v46, v46, v206, v207\n\t" \
  "v_fma_f32 v47, v47, v206, v207\n\t" \
  "v_cvt_pk_bf16_f32 v36, v36, v37\n\t" \
  "v_cvt_pk_bf16_f32 v37, v38, v39\n\t" \
  "v_cvt_pk_bf16_f32 v38, v40, v41\n\t" \
  "v_cvt_pk_bf16_f32 v39, v42, v43\n\t" \
  "v_cvt_pk_bf16_f32 v40, v44, v45\n\t" \
  "v_cvt_pk_bf16_f32 v41, v46, v47\n\t" \
  "ds_write_b128 v208, v[36:39] offset:36864\n\t" \
  "ds_write_b64 v209, v[40:41] offset:36864\n\t" \
  "v_lshlrev_b32 v232, 1, %[l8]\n\t" \
  "v_lshl_add_u32 v237, %[l8], 3, %[zb]\n\t" \
  "v_and_b32 v232, 0xf0, v232\n\t" \
  "v_mad_i32_i24 v237, v232, -3, v237\n\t" \
  "s_cmp_lg_u32 %[rnd], 0\n\t" \
  "s_cbranch_scc1 .Lcnx_c7\n\t" \
  "s_waitcnt vmcnt(0)\n\t" \
  ".Lcnx_c7:\n\t" \
  "s_waitcnt lgkmcnt(0)\n\t" \
  "s_barrier\n\t" \
  "ds_read_b64_tr_b16 v[208:209], %[a1] offset:6144\n\t" \
  "ds_read_b64_tr_b16 v[210:211], %[a1] offset:7168\n\t" \
  "ds_read_b128 v[96:99], v237 offset:0\n\t" \
  "ds_read_b128 v[108:111], v237 offset:256\n\t" \
  "ds_read_b128 v[120:123], v237 offset:512\n\t" \
  "ds_read_b128 v[132:135], v237 offset:768\n\t" \
  "ds_read_b64_tr_b16 v[212:213], %[a1] offset:9216\n\t" \
  "ds_read_b64_tr_b16 v[214:215], %[a1] offset:10240\n\t" \
  "ds_read_b128 v[100:103], v237 offset:4096\n\t" \
  "ds_read_b128 v[112:115], v237 offset:4352\n\t" \
  "ds_read_b128 v[124:127], v237 offset:4608\n\t" \
  "ds_read_b128 v[136:139], v237 offset:4864\n\t" \
  "ds_read_b128 v[104:107], v237 offset:8192\n\t" \
  "ds_read_b128 v[116:119], v237 offset:8448\n\t" \
  "ds_read_b128 v[128:131], v237 offset:8704\n\t" \
  "ds_read_b128 v[140:143], v237 offset:8960\n\t" \
  "s_waitcnt lgkmcnt(13)\n\t" \
  "v_mfma_f32_16x16x32_bf16 v[144:147], v[208:211], v[96:99], 0\n\t" \
  "s_waitcnt lgkmcnt(12)\n\t" \
  "v_mfma_f32_16x16x32_bf16 v[152:155], v[208:211], v[108:111], 0\n\t" \
  "s_waitcnt lgkmcnt(11)\n\t" \
  "v_mfma_f32_16x16x32_bf16 v[160:163], v[208:211], v[120:123], 0\n\t" \
  "buffer_store_dwordx4 v[80:83], %[vo], %[ors], %[op1] offen offset:256 sc1\n\t" \
  "s_waitcnt lgkmcnt(10)\n\t" \
  "v_mfma_f32_16x16x32_bf16 v[168:171], v[208:211], v[132:135], 0\n\t" \
  "ds_read_b64_tr_b16 v[208:209], %[a1] offset:8192\n\t" \
  "s_waitcnt lgkmcnt(8)\n\t" \
  "v_mfma_f32_16x16x32_bf16 v[144:147], v[212:215], v[100:103], v[144:147]\n\t" \
  "ds_read_b64_tr_b16 v[210:211], %[a1] offset:11264\n\t" \
  "s_waitcnt lgkmcnt(8)\n\t" \
  "v_mfma_f32_16x16x32_bf16 v[152:155], v[212:215], v[112:115], v[152:155]\n\t" \
  "s_waitcnt lgkmcnt(7)\n\t" \
  "v_mfma_f32_16x16x32_bf16 v[160:163], v[212:215], v[124:127], v[160:163]\n\t" \
  "s_waitcnt lgkmcnt(6)\n\t" \
  "v_mfma_f32_16x16x32_bf16 v[168:171], v[212:215], v[136:139], v[168:171]\n\t" \
  "ds_read_b64_tr_b16 v[212:213], %[a1] offset:6656\n\t" \
  "s_waitcnt lgkmcnt(1)\n\t" \
  "v_mfma_f32_16x16x32_bf16 v[144:147], v[208:211], v[104:107], v[144:147]\n\t" \
  "ds_read_b64_tr_b16 v[214:215], %[a1] offset:7680\n\t" \
  "v_mfma_f32_16x16x32_bf16 v[152:155], v[208:211], v[116:119], v[152:155]\n\t" \
  "v_mfma_f32_16x16x32_bf16 v[160:163], v[208:211], v[128:131], v[160:163]\n\t" \
  "v_mfma_f32_16x16x32_bf16 v[168:171], v[208:211], v[140:143], v[168:171]\n\t" \
  "ds_read_b64_tr_b16 v[208:209], %[a1] offset:9728\n\t" \
  "s_waitcnt lgkmcnt(1)\n\t" \
  "v_mfma_f32_16x16x32_bf16 v[148:151], v[212:215], v[96:99], 0\n\t" \
  "buffer_store_dwordx4 v[88:91], %[vo], %[ors], %[op1] offen offset:320 sc1\n\t" \
  "ds_read_b64_tr_b16 v[210:211], %[a1] offset:10752\n\t" \
  "v_mfma_f32_16x16x32_bf16 v[156:159], v[212:215], v[108:111], 0\n\t" \
  "v_mfma_f32_16x16x32_bf16 v[164:167], v[212:215], v[120:123], 0\n\t" \
  "v_mfma_f32_16x16x32_bf16 v[172:175], v[212:215], v[132:135], 0\n\t" \
  "ds_read_b64_tr_b16 v[212:213], %[a1] offset:8704\n\t" \
  "s_waitcnt lgkmcnt(1)\n\t" \
  "v_mfma_f32_16x16x32_bf16 v[148:151], v[208:211], v[100:103], v[148:151]\n\t" \
  "ds_read_b64_tr_b16 v[214:215], %[a1] offset:11776\n\t" \
  "v_mfma_f32_16x16x32_bf16 v[156:159], v[208:211], v[112:115], v[156:159]\n\t" \
  "v_cvt_pk_f16_f32 v216, |v146|, |v147| clamp\n\t" \
  "v_cvt_pk_f16_f32 v232, v144, v145\n\t" \
  "v_mfma_f32_16x16x32_bf16 v[164:167], v[208:211], v[124:127], v[164:167]\n\t" \
  "v_cvt_pk_f16_f32 v218, |v154|, |v155| clamp\n\t" \
  "v_cvt_pk_f16_f32 v220, |v152|, |v153| clamp\n\t" \
  "v_pk_mul_f16 v233, v232, v232\n\t" \
  "v_mfma_f32_16x16x32_bf16 v[172:175], v[208:211], v[136:139], v[172:175]\n\t" \
  "ds_read_b64_tr_b16 v[208:209], %[a1] offset:0\n\t" \
  "v_and_b32_sdwa v217, %[mk], v216 dst_sel:DWORD dst_unused:UNUSED_PAD src0_sel:DWORD src1_sel:WORD_1\n\t" \
  "v_and_b32 v216, %[mk], v216\n\t" \
  "s_waitcnt lgkmcnt(1)\n\t" \
  "v_mfma_f32_16x16x32_bf16 v[148:151], v[212:215], v[104:107], v[148:151]\n\t" \
  "s_mov_b64 exec, 1\n\t" \
  "ds_inc_u32 v240, %[h2] offset:33848\n\t" \
  "s_mov_b64 exec, -1\n\t" \
  "s_cmp_lg_u32 %[wq], 1\n\t" \
  "s_cbranch_scc1 .Lcnx_c8\n\t" \
  "s_add_u32 m0, %[wl], %[ws]\n\t" \
  "s_nop 0\n\t" \
  "buffer_load_dwordx4 %[wv], %[wrs], %[so0] offen lds\n\t" \
  "buffer_load_dwordx4 %[wv], %[wrs], %[so1] offen offset:1024 lds\n\t" \
  "buffer_load_dwordx4 %[wv], %[wrs], %[so2] offen offset:2048 lds\n\t" \
  "s_add_u32 %[so0], %[so0], %[wc]\n\t" \
  "s_add_u32 %[so1], %[so1], %[wc]\n\t" \
  "s_add_u32 %[so2], %[so2], %[wc]\n\t" \
  ".Lcnx_c8:\n\t" \
  "ds_read_b64_tr_b16 v[210:211], %[a1] offset:1024\n\t" \
  "v_and_b32_sdwa v219, %[mk], v218 dst_sel:DWORD dst_unused:UNUSED_PAD src0_sel:DWORD src1_sel:WORD_1\n\t" \
  "v_mfma_f32_16x16x32_bf16 v[156:159], v[212:215], v[116:119], v[156:159]\n\t" \
  "ds_read_b32 v217, v217 offset:0\n\t" \
  "v_and_b32 v218, %[mk], v218\n\t" \
  "v_and_b32_sdwa v221, %[mk], v220 dst_sel:DWORD dst_unused:UNUSED_PAD src0_sel:DWORD src1_sel:WORD_1\n\t" \
  "v_mfma_f32_16x16x32_bf16 v[164:167], v[212:215], v[128:131], v[164:167]\n\t" \
  "ds_read_b32 v216, v216 offset:0\n\t" \
  "v_and_b32 v220, %[mk], v220\n\t" \
  "v_mfma_f32_16x16x32_bf16 v[172:175], v[212:215], v[140:143], v[172:175]\n\t" \
  "ds_read_b64_tr_b16 v[212:213], %[a1] offset:3072\n\t" \
  "ds_read_b64_tr_b16 v[214:215], %[a1] offset:4096\n\t" \
  "v_pk_fma_f16 v233, v233, %[h2], %[h1]\n\t" \
  "v_pk_mul_f16 v233, v232, v233\n\t" \
  "s_setprio 2\n\t" \
  "v_exp_f16 v232, v233\n\t" \
  "v_cvt_pk_f16_f32 v224, |v162|, |v163| clamp\n\t" \
  "s_waitcnt lgkmcnt(4)\n\t" \
  "v_mfma_f32_16x16x32_bf16 v[176:179], v[208:211], v[96:99], 0\n\t" \
  "ds_read_b32 v219, v219 offset:0\n\t" \
  "ds_read_b32 v218, v218 offset:0\n\t" \
  "v_cvt_pk_f16_f32 v234, v160, v161\n\t" \
  "v_cvt_pk_f16_f32 v226, |v170|, |v171| clamp\n\t" \
  "v_cvt_pk_f16_f32 v228, |v168|, |v169| clamp\n\t" \
  "v_exp_f16_sdwa v232, v233 dst_sel:WORD_1 dst_unused:UNUSED_PRESERVE src0_sel:WORD_1\n\t" \
  "v_pk_mul_f16 v235, v234, v234\n\t" \
  "v_mfma_f32_16x16x32_bf16 v[184:187], v[208:211], v[108:111], 0\n\t" \
  "buffer_load_dwordx4 v[236:239], %[vo], %[xrs], %[oc0] offen offset:0\n\t" \
  "ds_read_b32 v221, v221 offset:0\n\t" \
  "v_and_b32_sdwa v225, %[mk], v224 dst_sel:DWORD dst_unused:UNUSED_PAD src0_sel:DWORD src1_sel:WORD_1\n\t" \
  "v_and_b32 v224, %[mk], v224\n\t" \
  "v_and_b32_sdwa v227, %[mk], v226 dst_sel:DWORD dst_unused:UNUSED_PAD src0_sel:DWORD src1_sel:WORD_1\n\t" \
  "v_and_b32 v226, %[mk], v226\n\t" \
  "v_mfma_f32_16x16x32_bf16 v[192:195], v[208:211], v[120:123], 0\n\t" \
  "ds_read_b32 v220, v220 offset:0\n\t" \
  "v_and_b32_sdwa v229, %[mk], v228 dst_sel:DWORD dst_unused:UNUSED_PAD src0_sel:DWORD src1_sel:WORD_1\n\t" \
  "v_and_b32 v228, %[mk], v228\n\t" \
  "v_mfma_f32_16x16x32_bf16 v[200:203], v[208:211], v[132:135], 0\n\t" \
  "ds_read_b64_tr_b16 v[208:209], %[a1] offset:2048\n\t" \
  "ds_read_b64_tr_b16 v[210:211], %[a1] offset:5120\n\t" \
  "v_pk_fma_f16 v235, v235, %[h2], %[h1]\n\t" \
  "v_pk_mul_f16 v235, v234, v235\n\t" \
  "s_waitcnt lgkmcnt(5)\n\t" \
  "v_fma_f32 v147, |v147|, v217, v147 mul:4\n\t" \
  "v_fma_f32 v146, |v146|, v216, v146 mul:4\n\t" \
  "v_fma_f32 v155, |v155|, v219, v155 mul:4\n\t" \
  "v_mfma_f32_16x16x32_bf16 v[176:179], v[212:215], v[100:103], v[176:179]\n\t" \
  "ds_read_b32 v246, v240 offset:33848\n\t" \
  "ds_read_b32 v225, v225 offset:0\n\t" \
  "ds_read_b32 v224, v224 offset:0\n\t" \
  "s_waitcnt lgkmcnt(5)\n\t" \
  "v_fma_f32 v154, |v154|, v218, v154 mul:4\n\t" \
  "v_fma_f32 v153, |v153|, v221, v153 mul:4\n\t" \
  "v_fma_f32 v152, |v152|, v220, v152 mul:4\n\t" \
  "v_pk_fma_f16 v232, v232, %[hh], %[hh]\n\t" \
  "v_cvt_pk_bf16_f32 v147, v146, v147\n\t" \
  "v_mfma_f32_16x16x32_bf16 v[184:187], v[212:215], v[112:115], v[184:187]\n\t" \
  "ds_read_b32 v227, v227 offset:0\n\t" \
  "ds_read_b32 v226, v226 offset:0\n\t" \
  "v_cvt_pk_bf16_f32 v155, v154, v155\n\t" \
  "v_cvt_pk_bf16_f32 v154, v152, v153\n\t" \
  "v_rcp_f16 v233, v232\n\t" \
  "v_exp_f16 v234, v235\n\t" \
  "v_cvt_pk_f16_f32 v216, |v148|, |v149| clamp\n\t" \
  "v_cvt_pk_f16_f32 v218, |v150|, |v151| clamp\n\t" \
  "v_mfma_f32_16x16x32_bf16 v[192:195], v[212:215], v[124:127], v[192:195]\n\t" \
  "s_setprio 2\n\t" \
  "ds_read_b32 v229, v229 offset:0\n\t" \
  "v_cvt_pk_f16_f32 v220, |v156|, |v157| clamp\n\t" \
  "v_cvt_pk_f16_f32 v222, |v158|, |v159| clamp\n\t" \
  "v_rcp_f16_sdwa v232, v232 dst_sel:DWORD dst_unused:UNUSED_PAD src0_sel:WORD_1\n\t" \
  "v_exp_f16_sdwa v234, v235 dst_sel:WORD_1 dst_unused:UNUSED_PRESERVE src0_sel:WORD_1\n\t" \
  "v_and_b32_sdwa v217, %[mk], v216 dst_sel:DWORD dst_unused:UNUSED_PAD src0_sel:DWORD src1_sel:WORD_1\n\t" \
  "v_and_b32 v216, %[mk], v216\n\t" \
  "v_mfma_f32_16x16x32_bf16 v[200:203], v[212:215], v[136:139], v[200:203]\n\t" \
  "s_waitcnt lgkmcnt(5)\n\t" \
  "v_cmp_gt_u32 vcc, %[tg], v246\n\t" \
  "s_cbranch_vccnz .Lcnx_w2\n.Lcnx_b2:\n\t" \
  "ds_read_b64_tr_b16 v[212:213], %[a1] offset:512\n\t" \
  "v_and_b32_sdwa v219, %[mk], v218 dst_sel:DWORD dst_unused:UNUSED_PAD src0_sel:DWORD src1_sel:WORD_1\n\t" \
  "v_and_b32 v218, %[mk], v218\n\t" \
  "v_mfma_f32_16x16x32_bf16 v[176:179], v[208:211], v[104:107], v[176:179]\n\t" \
  "s_waitcnt vmcnt(1)\n\t" \
  "s_mov_b64 exec, 1\n\t" \
  "ds_inc_u32 v240, %[h2] offset:33852\n\t" \
  "s_mov_b64 exec, -1\n\t" \
  "s_cmp_lg_u32 %[wq], 0\n\t" \
  "s_cbranch_scc1 .Lcnx_c9\n\t" \
  "s_add_u32 m0, %[wl], %[ws]\n\t" \
  "s_nop 0\n\t" \
  "buffer_load_dwordx4 %[wv], %[wrs], %[so0] offen lds\n\t" \
  "buffer_load_dwordx4 %[wv], %[wrs], %[so1] offen offset:1024 lds\n\t" \
  "buffer_load_dwordx4 %[wv], %[wrs], %[so2] offen offset:2048 lds\n\t" \
  "s_add_u32 %[so0], %[so0], %[wc]\n\t" \
  "s_add_u32 %[so1], %[so1], %[wc]\n\t" \
  "s_add_u32 %[so2], %[so2], %[wc]\n\t" \
  ".Lcnx_c9:\n\t" \
  "s_cmp_lg_u32 %[rnd], 0\n\t" \
  "s_cbranch_scc1 .Lcnx_c10\n\t" \
  "s_sub_u32 %[xo], %[xo], %[rs6]\n\t" \
  "s_add_u32 m0, %[xl], 0\n\t" \
  "s_add_u32 %[xo], %[xo], %[rs]\n\t" \
  "buffer_load_dwordx4 %[xv], %[xrs], %[xo] offen lds\n\t" \
  ".Lcnx_c10:\n\t" \
  "ds_read_b64_tr_b16 v[214:215], %[a1] offset:1536\n\t" \
  "ds_read_b32 v228, v228 offset:0\n\t" \
  "v_and_b32_sdwa v221, %[mk], v220 dst_sel:DWORD dst_unused:UNUSED_PAD src0_sel:DWORD src1_sel:WORD_1\n\t" \
  "v_and_b32 v220, %[mk], v220\n\t" \
  "v_and_b32_sdwa v223, %[mk], v222 dst_sel:DWORD dst_unused:UNUSED_PAD src0_sel:DWORD src1_sel:WORD_1\n\t" \
  "v_mfma_f32_16x16x32_bf16 v[184:187], v[208:211], v[116:119], v[184:187]\n\t" \
  "ds_read_b32 v217, v217 offset:0\n\t" \
  "ds_read_b32 v216, v216 offset:0\n\t" \
  "v_and_b32 v222, %[mk], v222\n\t" \
  "s_waitcnt lgkmcnt(8)\n\t" \
  "v_fma_f32 v163, |v163|, v225, v163 mul:4\n\t" \
  "v_fma_f32 v162, |v162|, v224, v162 mul:4\n\t" \
  "v_mfma_f32_16x16x32_bf16 v[192:195], v[208:211], v[128:131], v[192:195]\n\t" \
  "ds_read_b32 v219, v219 offset:0\n\t" \
  "ds_read_b32 v218, v218 offset:0\n\t" \
  "v_fma_f32 v171, |v171|, v227, v171 mul:4\n\t" \
  "s_waitcnt lgkmcnt(4)\n\t" \
  "v_fma_f32 v170, |v170|, v226, v170 mul:4\n\t" \
  "v_fma_f32 v169, |v169|, v229, v169 mul:4\n\t" \
  "v_fma_f32 v168, |v168|, v228, v168 mul:4\n\t" \
  "v_fma_mix_f32 v144, v144, v233, 0 op_sel_hi:[0,1,0]\n\t" \
  "v_fma_mix_f32 v145, v145, v232, 0 op_sel_hi:[0,1,0]\n\t" \
  "v_mfma_f32_16x16x32_bf16 v[200:203], v[208:211], v[140:143], v[200:203]\n\t" \
  "ds_read_b64_tr_b16 v[208:209], %[a1] offset:3584\n\t" \
  "ds_read_b64_tr_b16 v[210:211], %[a1] offset:4608\n\t" \
  "v_pk_fma_f16 v234, v234, %[hh], %[hh]\n\t" \
  "v_cvt_pk_bf16_f32 v146, v144, v145\n\t" \
  "v_cvt_pk_bf16_f32 v163, v162, v163\n\t" \
  "v_cvt_pk_bf16_f32 v171, v170, v171\n\t" \
  "v_cvt_pk_bf16_f32 v170, v168, v169\n\t" \
  "s_setprio 1\n\t" \
  "v_rcp_f16 v235, v234\n\t" \
  "v_mfma_f32_16x16x32_bf16 v[180:183], v[212:215], v[96:99], 0\n\t" \
  "ds_read_b32 v221, v221 offset:0\n\t" \
  "ds_read_b32 v220, v220 offset:0\n\t" \
  "v_cvt_pk_f16_f32 v224, |v164|, |v165| clamp\n\t" \
  "v_cvt_pk_f16_f32 v226, |v166|, |v167| clamp\n\t" \
  "v_cvt_pk_f16_f32 v228, |v172|, |v173| clamp\n\t" \
  "v_cvt_pk_f16_f32 v230, |v174|, |v175| clamp\n\t" \
  "v_rcp_f16_sdwa v234, v234 dst_sel:DWORD dst_unused:UNUSED_PAD src0_sel:WORD_1\n\t" \
  "v_mfma_f32_16x16x32_bf16 v[188:191], v[212:215], v[108:111], 0\n\t" \
  "ds_read_b32 v223, v223 offset:0\n\t" \
  "v_and_b32_sdwa v225, %[mk], v224 dst_sel:DWORD dst_unused:UNUSED_PAD src0_sel:DWORD src1_sel:WORD_1\n\t" \
  "v_and_b32 v224, %[mk], v224\n\t" \
  "v_and_b32_sdwa v227, %[mk], v226 dst_sel:DWORD dst_unused:UNUSED_PAD src0_sel:DWORD src1_sel:WORD_1\n\t" \
  "v_and_b32 v226, %[mk], v226\n\t" \
  "v_mfma_f32_16x16x32_bf16 v[196:199], v[212:215], v[120:123], 0\n\t" \
  "ds_read_b32 v222, v222 offset:0\n\t" \
  "v_and_b32_sdwa v229, %[mk], v228 dst_sel:DWORD dst_unused:UNUSED_PAD src0_sel:DWORD src1_sel:WORD_1\n\t" \
  "v_and_b32 v228, %[mk], v228\n\t" \
  "v_mfma_f32_16x16x32_bf16 v[204:207], v[212:215], v[132:135], 0\n\t" \
  "ds_read_b64_tr_b16 v[212:213], %[a1] offset:2560\n\t" \
  "ds_read_b64_tr_b16 v[214:215], %[a1] offset:5632\n\t" \
  "v_and_b32_sdwa v231, %[mk], v230 dst_sel:DWORD dst_unused:UNUSED_PAD src0_sel:DWORD src1_sel:WORD_1\n\t" \
  "v_and_b32 v230, %[mk], v230\n\t" \
  "s_waitcnt lgkmcnt(8)\n\t" \
  "v_fma_f32 v149, |v149|, v217, v149 mul:4\n\t" \
  "s_waitcnt lgkmcnt(6)\n\t" \
  "v_mfma_f32_16x16x32_bf16 v[180:183], v[208:211], v[100:103], v[180:183]\n\t" \
  "ds_read_b32 v246, v240 offset:33852\n\t" \
  "ds_read_b32 v225, v225 offset:0\n\t" \
  "ds_read_b32 v224, v224 offset:0\n\t" \
  "v_fma_f32 v148, |v148|, v216, v148 mul:4\n\t" \
  "v_fma_f32 v151, |v151|, v219, v151 mul:4\n\t" \
  "v_fma_f32 v150, |v150|, v218, v150 mul:4\n\t" \
  "s_waitcnt lgkmcnt(5)\n\t" \
  "v_fma_f32 v157, |v157|, v221, v157 mul:4\n\t" \
  "v_fma_f32 v156, |v156|, v220, v156 mul:4\n\t" \
  "v_mfma_f32_16x16x32_bf16 v[188:191], v[208:211], v[112:115], v[188:191]\n\t" \
  "ds_read_b32 v227, v227 offset:0\n\t" \
  "ds_read_b32 v226, v226 offset:0\n\t" \
  "v_fma_f32 v159, |v159|, v223, v159 mul:4\n\t" \
  "v_fma_f32 v158, |v158|, v222, v158 mul:4\n\t" \
  "v_fma_mix_f32 v160, v160, v235, 0 op_sel_hi:[0,1,0]\n\t" \
  "v_fma_mix_f32 v161, v161, v234, 0 op_sel_hi:[0,1,0]\n\t" \
  "v_cvt_pk_bf16_f32 v162, v160, v161\n\t" \
  "v_cvt_pk_bf16_f32 v148, v148, v149\n\t" \
  "v_mfma_f32_16x16x32_bf16 v[196:199], v[208:211], v[124:127], v[196:199]\n\t" \
  "ds_read_b32 v229, v229 offset:0\n\t" \
  "ds_read_b32 v228, v228 offset:0\n\t" \
  "v_cvt_pk_bf16_f32 v149, v150, v151\n\t" \
  "s_setprio 0\n\t" \
  "v_cvt_pk_bf16_f32 v156, v156, v157\n\t" \
  "v_cvt_pk_bf16_f32 v157, v158, v159\n\t" \
  "v_cvt_pk_f16_f32 v216, |v178|, |v179| clamp\n\t" \
  "v_cvt_pk_f16_f32 v232, v176, v177\n\t" \
  "v_cvt_pk_f16_f32 v218, |v186|, |v187| clamp\n\t" \
  "v_mfma_f32_16x16x32_bf16 v[204:207], v[208:211], v[136:139], v[204:207]\n\t" \
  "s_waitcnt lgkmcnt(6)\n\t" \
  "v_cmp_gt_u32 vcc, %[tg], v246\n\t" \
  "s_cbranch_vccnz .Lcnx_w3\n.Lcnx_b3:\n\t" \
  "ds_read_b64_tr_b16 v[208:209], %[a2] offset:6160\n\t" \
  "ds_read_b64_tr_b16 v[210:211], %[a2] offset:7696\n\t" \
  "v_cvt_pk_f16_f32 v220, |v184|, |v185| clamp\n\t" \
  "v_pk_mul_f16 v233, v232, v232\n\t" \
  "v_and_b32_sdwa v217, %[mk], v216 dst_sel:DWORD dst_unused:UNUSED_PAD src0_sel:DWORD src1_sel:WORD_1\n\t" \
  "v_and_b32 v216, %[mk], v216\n\t" \
  "v_mfma_f32_16x16x32_bf16 v[180:183], v[212:215], v[104:107], v[180:183]\n\t" \
  "s_waitcnt vmcnt(1)\n\t" \
  "s_cmp_lg_u32 %[rnd], 1\n\t" \
  "s_cbranch_scc1 .Lcnx_c11\n\t" \
  "s_waitcnt vmcnt(0)\n\t" \
  ".Lcnx_c11:\n\t" \
  "s_mov_b64 exec, 1\n\t" \
  "ds_inc_u32 v240, %[h2] offset:33856\n\t" \
  "s_mov_b64 exec, -1\n\t" \
  "s_cmp_lg_u32 %[wq], 1\n\t" \
  "s_cbranch_scc1 .Lcnx_c12\n\t" \
  "s_add_u32 m0, %[wl], 0\n\t" \
  "s_nop 0\n\t" \
  "buffer_load_dwordx4 %[wv], %[wrs], %[so0] offen lds\n\t" \
  "buffer_load_dwordx4 %[wv], %[wrs], %[so1] offen offset:1024 lds\n\t" \
  "buffer_load_dwordx4 %[wv], %[wrs], %[so2] offen offset:2048 lds\n\t" \
  "s_add_u32 %[so0], %[so0], %[wc]\n\t" \
  "s_add_u32 %[so1], %[so1], %[wc]\n\t" \
  "s_add_u32 %[so2], %[so2], %[wc]\n\t" \
  ".Lcnx_c12:\n\t" \
  "ds_read_b32 v231, v231 offset:0\n\t" \
  "v_and_b32_sdwa v219, %[mk], v218 dst_sel:DWORD dst_unused:UNUSED_PAD src0_sel:DWORD src1_sel:WORD_1\n\t" \
  "v_and_b32 v218, %[mk], v218\n\t" \
  "v_and_b32_sdwa v221, %[mk], v220 dst_sel:DWORD dst_unused:UNUSED_PAD src0_sel:DWORD src1_sel:WORD_1\n\t" \
  "v_mfma_f32_16x16x32_bf16 v[188:191], v[212:215], v[116:119], v[188:191]\n\t" \
  "ds_read_b32 v230, v230 offset:0\n\t" \
  "ds_read_b32 v217, v217 offset:0\n\t" \
  "v_and_b32 v220, %[mk], v220\n\t" \
  "v_pk_fma_f16 v233, v233, %[h2], %[h1]\n\t" \
  "v_pk_mul_f16 v233, v232, v233\n\t" \
  "v_mfma_f32_16x16x32_bf16 v[196:199], v[212:215], v[128:131], v[196:199]\n\t" \
  "ds_read_b32 v216, v216 offset:0\n\t" \
  "ds_read_b32 v219, v219 offset:0\n\t" \
  "s_waitcnt lgkmcnt(10)\n\t" \
  "v_fma_f32 v165, |v165|, v225, v165 mul:4\n\t" \
  "v_fma_f32 v164, |v164|, v224, v164 mul:4\n\t" \
  "v_fma_f32 v167, |v167|, v227, v167 mul:4\n\t" \
  "v_fma_f32 v166, |v166|, v226, v166 mul:4\n\t" \
  "s_waitcnt lgkmcnt(3)\n\t" \
  "v_fma_f32 v173, |v173|, v229, v173 mul:4\n\t" \
  "v_fma_f32 v172, |v172|, v228, v172 mul:4\n\t" \
  "v_mfma_f32_16x16x32_bf16 v[204:207], v[212:215], v[140:143], v[204:207]\n\t" \
  "ds_read_b64_tr_b16 v[212:213], %[a2] offset:6168\n\t" \
  "ds_read_b64_tr_b16 v[214:215], %[a2] offset:7704\n\t" \
  "v_fma_f32 v175, |v175|, v231, v175 mul:4\n\t" \
  "v_fma_f32 v174, |v174|, v230, v174 mul:4\n\t" \
  "v_cvt_pk_bf16_f32 v164, v164, v165\n\t" \
  "v_cvt_pk_bf16_f32 v165, v166, v167\n\t" \
  "v_cvt_pk_bf16_f32 v172, v172, v173\n\t" \
  "v_cvt_pk_bf16_f32 v173, v174, v175\n\t" \
  "s_setprio 2\n\t" \
  "v_exp_f16 v232, v233\n\t" \
  "v_cvt_pk_f16_f32 v224, |v194|, |v195| clamp\n\t" \
  "v_mfma_f32_16x16x32_bf16 v[0:3], v[242:245], v[236:239], 0\n\t" \
  "ds_read_b32 v218, v218 offset:0\n\t" \
  "v_cvt_pk_f16_f32 v234, v192, v193\n\t" \
  "v_cvt_pk_f16_f32 v226, |v202|, |v203| clamp\n\t" \
  "v_mfma_f32_16x16x32_bf16 v[4:7], v[240:243], v[236:239], 0\n\t" \
  "buffer_load_dwordx4 v[236:239], %[vo], %[xrs], %[oc0] offen offset:64\n\t" \
  "ds_read_b32 v221, v221 offset:0\n\t" \
  "v_cvt_pk_f16_f32 v228, |v200|, |v201| clamp\n\t" \
  "v_exp_f16_sdwa v232, v233 dst_sel:WORD_1 dst_unused:UNUSED_PRESERVE src0_sel:WORD_1\n\t" \
  "v_pk_mul_f16 v235, v234, v234\n\t" \
  "v_mfma_f32_16x16x32_bf16 v[0:3], v[208:211], v[146:149], v[0:3]\n\t" \
  "ds_read_b32 v220, v220 offset:0\n\t" \
  "v_and_b32_sdwa v225, %[mk], v224 dst_sel:DWORD dst_unused:UNUSED_PAD src0_sel:DWORD src1_sel:WORD_1\n\t" \
  "v_and_b32 v224, %[mk], v224\n\t" \
  "v_mfma_f32_16x16x32_bf16 v[24:27], v[208:211], v[154:157], 0\n\t" \
  "ds_read_b32 v225, v225 offset:0\n\t" \
  "v_and_b32_sdwa v227, %[mk], v226 dst_sel:DWORD dst_unused:UNUSED_PAD src0_sel:DWORD src1_sel:WORD_1\n\t" \
  "v_mfma_f32_16x16x32_bf16 v[48:51], v[208:211], v[162:165], 0\n\t" \
  "ds_read_b32 v224, v224 offset:0\n\t" \
  "v_and_b32 v226, %[mk], v226\n\t" \
  "v_and_b32_sdwa v229, %[mk], v228 dst_sel:DWORD dst_unused:UNUSED_PAD src0_sel:DWORD src1_sel:WORD_1\n\t" \
  "v_mfma_f32_16x16x32_bf16 v[72:75], v[208:211], v[170:173], 0\n\t" \
  "ds_read_b64_tr_b16 v[208:209], %[a2] offset:6224\n\t" \
  "v_and_b32 v228, %[mk], v228\n\t" \
  "s_waitcnt lgkmcnt(6)\n\t" \
  "v_mfma_f32_16x16x32_bf16 v[4:7], v[212:215], v[146:149], v[4:7]\n\t" \
  "ds_read_b64_tr_b16 v[210:211], %[a2] offset:7760\n\t" \
  "ds_read_b32 v227, v227 offset:0\n\t" \
  "v_pk_fma_f16 v235, v235, %[h2], %[h1]\n\t" \
  "v_mfma_f32_16x16x32_bf16 v[28:31], v[212:215], v[154:157], 0\n\t" \
  "ds_read_b32 v226, v226 offset:0\n\t" \
  "v_pk_mul_f16 v235, v234, v235\n\t" \
  "v_fma_f32 v179, |v179|, v217, v179 mul:4\n\t" \
  "v_fma_f32 v178, |v178|, v216, v178 mul:4\n\t" \
  "v_mfma_f32_16x16x32_bf16 v[52:55], v[212:215], v[162:165], 0\n\t" \
  "ds_read_b32 v229, v229 offset:0\n\t" \
  "v_fma_f32 v187, |v187|, v219, v187 mul:4\n\t" \
  "s_waitcnt lgkmcnt(7)\n\t" \
  "v_fma_f32 v186, |v186|, v218, v186 mul:4\n\t" \
  "v_fma_f32 v185, |v185|, v221, v185 mul:4\n\t" \
  "v_mfma_f32_16x16x32_bf16 v[76:79], v[212:215], v[170:173], 0\n\t" \
  "ds_read_b64_tr_b16 v[212:213], %[a2] offset:6232\n\t" \
  "v_fma_f32 v184, |v184|, v220, v184 mul:4\n\t" \
  "v_pk_fma_f16 v232, v232, %[hh], %[hh]\n\t" \
  "s_waitcnt lgkmcnt(4)\n\t" \
  "v_mfma_f32_16x16x32_bf16 v[8:11], v[208:211], v[146:149], 0\n\t" \
  "ds_read_b64_tr_b16 v[214:215], %[a2] offset:7768\n\t" \
  "v_cvt_pk_bf16_f32 v179, v178, v179\n\t" \
  "v_cvt_pk_bf16_f32 v187, v186, v187\n\t" \
  "v_cvt_pk_bf16_f32 v186, v184, v185\n\t" \
  "v_mfma_f32_16x16x32_bf16 v[32:35], v[208:211], v[154:157], 0\n\t" \
  "ds_read_b32 v228, v228 offset:0\n\t" \
  "v_rcp_f16 v233, v232\n\t" \
  "v_exp_f16 v234, v235\n\t" \
  "v_cvt_pk_f16_f32 v216, |v180|, |v181| clamp\n\t" \
  "v_mfma_f32_16x16x32_bf16 v[56:59], v[208:211], v[162:165], 0\n\t" \
  "v_cvt_pk_f16_f32 v218, |v182|, |v183| clamp\n\t" \
  "v_cvt_pk_f16_f32 v220, |v188|, |v189| clamp\n\t" \
  "v_mfma_f32_16x16x32_bf16 v[80:83], v[208:211], v[170:173], 0\n\t" \
  "s_setprio 2\n\t" \
  "ds_read_b64_tr_b16 v[208:209], %[a2] offset:6288\n\t" \
  "v_cvt_pk_f16_f32 v222, |v190|, |v191| clamp\n\t" \
  "v_rcp_f16_sdwa v232, v232 dst_sel:DWORD dst_unused:UNUSED_PAD src0_sel:WORD_1\n\t" \
  "v_exp_f16_sdwa v234, v235 dst_sel:WORD_1 dst_unused:UNUSED_PRESERVE src0_sel:WORD_1\n\t" \
  "s_waitcnt lgkmcnt(2)\n\t" \
  "v_mfma_f32_16x16x32_bf16 v[12:15], v[212:215], v[146:149], 0\n\t" \
  "ds_read_b64_tr_b16 v[210:211], %[a2] offset:7824\n\t" \
  "v_and_b32_sdwa v217, %[mk], v216 dst_sel:DWORD dst_unused:UNUSED_PAD src0_sel:DWORD src1_sel:WORD_1\n\t" \
  "v_and_b32 v216, %[mk], v216\n\t" \
  "v_mfma_f32_16x16x32_bf16 v[36:39], v[212:215], v[154:157], 0\n\t" \
  "ds_read_b32 v217, v217 offset:0\n\t" \
  "v_and_b32_sdwa v219, %[mk], v218 dst_sel:DWORD dst_unused:UNUSED_PAD src0_sel:DWORD src1_sel:WORD_1\n\t" \
  "v_mfma_f32_16x16x32_bf16 v[60:63], v[212:215], v[162:165], 0\n\t" \
  "ds_read_b32 v216, v216 offset:0\n\t" \
  "v_and_b32 v218, %[mk], v218\n\t" \
  "v_and_b32_sdwa v221, %[mk], v220 dst_sel:DWORD dst_unused:UNUSED_PAD src0_sel:DWORD src1_sel:WORD_1\n\t" \
  "v_mfma_f32_16x16x32_bf16 v[84:87], v[212:215], v[170:173], 0\n\t" \
  "ds_read_b64_tr_b16 v[212:213], %[a2] offset:6296\n\t" \
  "v_and_b32 v220, %[mk], v220\n\t" \
  "s_waitcnt lgkmcnt(3)\n\t" \
  "v_mfma_f32_16x16x32_bf16 v[16:19], v[208:211], v[146:149], 0\n\t" \
  "ds_read_b32 v246, v240 offset:33856\n\t" \
  "ds_read_b64_tr_b16 v[214:215], %[a2] offset:7832\n\t" \
  "ds_read_b32 v219, v219 offset:0\n\t" \
  "v_and_b32_sdwa v223, %[mk], v222 dst_sel:DWORD dst_unused:UNUSED_PAD src0_sel:DWORD src1_sel:WORD_1\n\t" \
  "v_mfma_f32_16x16x32_bf16 v[40:43], v[208:211], v[154:157], 0\n\t" \
  "ds_read_b32 v218, v218 offset:0\n\t" \
  "v_and_b32 v222, %[mk], v222\n\t" \
  "v_fma_f32 v195, |v195|, v225, v195 mul:4\n\t" \
  "v_mfma_f32_16x16x32_bf16 v[64:67], v[208:211], v[162:165], 0\n\t" \
  "ds_read_b32 v221, v221 offset:0\n\t" \
  "v_fma_f32 v194, |v194|, v224, v194 mul:4\n\t" \
  "v_fma_f32 v203, |v203|, v227, v203 mul:4\n\t" \
  "v_mfma_f32_16x16x32_bf16 v[88:91], v[208:211], v[170:173], 0\n\t" \
  "s_waitcnt lgkmcnt(4)\n\t" \
  "v_cmp_gt_u32 vcc, %[tg], v246\n\t" \
  "s_cbranch_vccnz .Lcnx_w4\n.Lcnx_b4:\n\t" \
  "ds_read_b64_tr_b16 v[208:209], %[a1] offset:6144\n\t" \
  "ds_read_b64_tr_b16 v[210:211], %[a1] offset:7168\n\t" \
  "v_fma_f32 v202, |v202|, v226, v202 mul:4\n\t" \
  "v_fma_f32 v201, |v201|, v229, v201 mul:4\n\t" \
  "s_waitcnt lgkmcnt(5)\n\t" \
  "v_mfma_f32_16x16x32_bf16 v[20:23], v[212:215], v[146:149], 0\n\t" \
  "s_waitcnt vmcnt(1)\n\t" \
  "s_mov_b64 exec, 1\n\t" \
  "ds_inc_u32 v240, %[h2] offset:33860\n\t" \
  "s_mov_b64 exec, -1\n\t" \
  "s_cmp_lg_u32 %[wq], 0\n\t" \
  "s_cbranch_scc1 .Lcnx_c13\n\t" \
  "s_add_u32 m0, %[wl], 0\n\t" \
  "s_nop 0\n\t" \
  "buffer_load_dwordx4 %[wv], %[wrs], %[so0] offen lds\n\t" \
  "buffer_load_dwordx4 %[wv], %[wrs], %[so1] offen offset:1024 lds\n\t" \
  "buffer_load_dwordx4 %[wv], %[wrs], %[so2] offen offset:2048 lds\n\t" \
  "s_add_u32 %[so0], %[so0], %[wc]\n\t" \
  "s_add_u32 %[so1], %[so1], %[wc]\n\t" \
  "s_add_u32 %[so2], %[so2], %[wc]\n\t" \
  ".Lcnx_c13:\n\t" \
  "s_cmp_lg_u32 %[rnd], 0\n\t" \
  "s_cbranch_scc1 .Lcnx_c14\n\t" \
  "s_add_u32 m0, %[xl], 8200\n\t" \
  "s_add_u32 %[xo], %[xo], %[rs]\n\t" \
  "buffer_load_dwordx4 %[xv], %[xrs], %[xo] offen lds\n\t" \
  "s_cmp_lg_u32 %[w512], 3072\n\t" \
  "s_cbranch_scc1 .Lcnx_c14\n\t" \
  "s_mov_b32 m0, %[pf]\n\t" \
  "s_add_u32 %[t0], %[xo], %[wb]\n\t" \
  "buffer_load_dword %[wv], %[xrs], %[t0] offen lds\n\t" \
  ".Lcnx_c14:\n\t" \
  "ds_read_b32 v220, v220 offset:0\n\t" \
  "v_fma_f32 v200, |v200|, v228, v200 mul:4\n\t" \
  "v_fma_mix_f32 v176, v176, v233, 0 op_sel_hi:[0,1,0]\n\t" \
  "v_fma_mix_f32 v177, v177, v232, 0 op_sel_hi:[0,1,0]\n\t" \
  "v_mfma_f32_16x16x32_bf16 v[44:47], v[212:215], v[154:157], 0\n\t" \
  "ds_read_b32 v223, v223 offset:0\n\t" \
  "v_pk_fma_f16 v234, v234, %[hh], %[hh]\n\t" \
  "v_cvt_pk_bf16_f32 v178, v176, v177\n\t" \
  "v_cvt_pk_bf16_f32 v195, v194, v195\n\t" \
  "v_mfma_f32_16x16x32_bf16 v[68:71], v[212:215], v[162:165], 0\n\t" \
  "ds_read_b32 v222, v222 offset:0\n\t" \
  "v_cvt_pk_bf16_f32 v203, v202, v203\n\t" \
  "v_cvt_pk_bf16_f32 v202, v200, v201\n\t" \
  "s_setprio 1\n\t" \
  "v_rcp_f16 v235, v234\n\t" \
  "v_mfma_f32_16x16x32_bf16 v[92:95], v[212:215], v[170:173], 0\n\t" \
  "ds_read_b64_tr_b16 v[212:213], %[a1] offset:9216\n\t" \
  "v_cvt_pk_f16_f32 v224, |v196|, |v197| clamp\n\t" \
  "v_cvt_pk_f16_f32 v226, |v198|, |v199| clamp\n\t" \
  "s_waitcnt lgkmcnt(5)\n\t" \
  "v_mfma_f32_16x16x32_bf16 v[144:147], v[208:211], v[96:99], 0\n\t" \
  "ds_read_b64_tr_b16 v[214:215], %[a1] offset:10240\n\t" \
  "v_cvt_pk_f16_f32 v228, |v204|, |v205| clamp\n\t" \
  "v_cvt_pk_f16_f32 v230, |v206|, |v207| clamp\n\t" \
  "v_rcp_f16_sdwa v234, v234 dst_sel:DWORD dst_unused:UNUSED_PAD src0_sel:WORD_1\n\t" \
  "v_mfma_f32_16x16x32_bf16 v[152:155], v[208:211], v[108:111], 0\n\t" \
  "v_and_b32_sdwa v225, %[mk], v224 dst_sel:DWORD dst_unused:UNUSED_PAD src0_sel:DWORD src1_sel:WORD_1\n\t" \
  "v_and_b32 v224, %[mk], v224\n\t" \
  "v_mfma_f32_16x16x32_bf16 v[160:163], v[208:211], v[120:123], 0\n\t" \
  "ds_read_b32 v225, v225 offset:0\n\t" \
  "v_and_b32_sdwa v227, %[mk], v226 dst_sel:DWORD dst_unused:UNUSED_PAD src0_sel:DWORD src1_sel:WORD_1\n\t" \
  "v_mfma_f32_16x16x32_bf16 v[168:171], v[208:211], v[132:135], 0\n\t" \
  "ds_read_b64_tr_b16 v[208:209], %[a1] offset:8192\n\t" \
  "v_and_b32 v226, %[mk], v226\n\t" \
  "v_and_b32_sdwa v229, %[mk], v228 dst_sel:DWORD dst_unused:UNUSED_PAD src0_sel:DWORD src1_sel:WORD_1\n\t" \
  "s_waitcnt lgkmcnt(2)\n\t" \
  "v_mfma_f32_16x16x32_bf16 v[144:147], v[212:215], v[100:103], v[144:147]\n\t" \
  "ds_read_b64_tr_b16 v[210:211], %[a1] offset:11264\n\t" \
  "v_and_b32 v228, %[mk], v228\n\t" \
  "v_mfma_f32_16x16x32_bf16 v[152:155], v[212:215], v[112:115], v[152:155]\n\t" \
  "ds_read_b32 v224, v224 offset:0\n\t" \
  "ds_read_b32 v227, v227 offset:0\n\t" \
  "v_and_b32_sdwa v231, %[mk], v230 dst_sel:DWORD dst_unused:UNUSED_PAD src0_sel:DWORD src1_sel:WORD_1\n\t" \
  "v_mfma_f32_16x16x32_bf16 v[160:163], v[212:215], v[124:127], v[160:163]\n\t" \
  "ds_read_b32 v226, v226 offset:0\n\t" \
  "v_and_b32 v230, %[mk], v230\n\t" \
  "v_fma_f32 v181, |v181|, v217, v181 mul:4\n\t" \
  "v_mfma_f32_16x16x32_bf16 v[168:171], v[212:215], v[136:139], v[168:171]\n\t" \
  "ds_read_b64_tr_b16 v[212:213], %[a1] offset:6656\n\t" \
  "ds_read_b64_tr_b16 v[214:215], %[a1] offset:7680\n\t" \
  "v_fma_f32 v180, |v180|, v216, v180 mul:4\n\t" \
  "v_fma_f32 v183, |v183|, v219, v183 mul:4\n\t" \
  "s_waitcnt lgkmcnt(5)\n\t" \
  "v_mfma_f32_16x16x32_bf16 v[144:147], v[208:211], v[104:107], v[144:147]\n\t" \
  "ds_read_b32 v229, v229 offset:0\n\t" \
  "ds_read_b32 v228, v228 offset:0\n\t" \
  "v_fma_f32 v182, |v182|, v218, v182 mul:4\n\t" \
  "v_fma_f32 v189, |v189|, v221, v189 mul:4\n\t" \
  "v_mfma_f32_16x16x32_bf16 v[152:155], v[208:211], v[116:119], v[152:155]\n\t" \
  "ds_read_b32 v231, v231 offset:0\n\t" \
  "v_fma_f32 v188, |v188|, v220, v188 mul:4\n\t" \
  "v_fma_f32 v191, |v191|, v223, v191 mul:4\n\t" \
  "v_fma_f32 v190, |v190|, v222, v190 mul:4\n\t" \
  "v_mfma_f32_16x16x32_bf16 v[160:163], v[208:211], v[128:131], v[160:163]\n\t" \
  "ds_read_b32 v230, v230 offset:0\n\t" \
  "v_fma_mix_f32 v192, v192, v235, 0 op_sel_hi:[0,1,0]\n\t" \
  "v_fma_mix_f32 v193, v193, v234, 0 op_sel_hi:[0,1,0]\n\t" \
  "v_cvt_pk_bf16_f32 v194, v192, v193\n\t" \
  "v_mfma_f32_16x16x32_bf16 v[168:171], v[208:211], v[140:143], v[168:171]\n\t" \
  "ds_read_b64_tr_b16 v[208:209], %[a1] offset:9728\n\t" \
  "v_cvt_pk_bf16_f32 v180, v180, v181\n\t" \
  "v_cvt_pk_bf16_f32 v181, v182, v183\n\t" \
  "s_waitcnt lgkmcnt(5)\n\t" \
  "v_mfma_f32_16x16x32_bf16 v[148:151], v[212:215], v[96:99], 0\n\t" \
  "s_setprio 0\n\t" \
  "ds_read_b64_tr_b16 v[210:211], %[a1] offset:10752\n\t" \
  "v_cvt_pk_bf16_f32 v188, v188, v189\n\t" \
  "v_cvt_pk_bf16_f32 v189, v190, v191\n\t" \
  "v_cvt_pk_f16_f32 v216, |v146|, |v147| clamp\n\t" \
  "v_mfma_f32_16x16x32_bf16 v[156:159], v[212:215], v[108:111], 0\n\t" \
  "v_cvt_pk_f16_f32 v232, v144, v145\n\t" \
  "v_cvt_pk_f16_f32 v218, |v154|, |v155| clamp\n\t" \
  "v_cvt_pk_f16_f32 v220, |v152|, |v153| clamp\n\t" \
  "v_mfma_f32_16x16x32_bf16 v[164:167], v[212:215], v[120:123], 0\n\t" \
  "v_pk_mul_f16 v233, v232, v232\n\t" \
  "v_and_b32_sdwa v217, %[mk], v216 dst_sel:DWORD dst_unused:UNUSED_PAD src0_sel:DWORD src1_sel:WORD_1\n\t" \
  "v_mfma_f32_16x16x32_bf16 v[172:175], v[212:215], v[132:135], 0\n\t" \
  "ds_read_b64_tr_b16 v[212:213], %[a1] offset:8704\n\t" \
  "v_and_b32 v216, %[mk], v216\n\t" \
  "v_and_b32_sdwa v219, %[mk], v218 dst_sel:DWORD dst_unused:UNUSED_PAD src0_sel:DWORD src1_sel:WORD_1\n\t" \
  "s_waitcnt lgkmcnt(1)\n\t" \
  "v_mfma_f32_16x16x32_bf16 v[148:151], v[208:211], v[100:103], v[148:151]\n\t" \
  "ds_read_b32 v246, v240 offset:33860\n\t" \
  "ds_read_b64_tr_b16 v[214:215], %[a1] offset:11776\n\t" \
  "v_and_b32 v218, %[mk], v218\n\t" \
  "v_mfma_f32_16x16x32_bf16 v[156:159], v[208:211], v[112:115], v[156:159]\n\t" \
  "ds_read_b32 v217, v217 offset:0\n\t" \
  "v_and_b32_sdwa v221, %[mk], v220 dst_sel:DWORD dst_unused:UNUSED_PAD src0_sel:DWORD src1_sel:WORD_1\n\t" \
  "v_mfma_f32_16x16x32_bf16 v[164:167], v[208:211], v[124:127], v[164:167]\n\t" \
  "ds_read_b32 v216, v216 offset:0\n\t" \
  "v_and_b32 v220, %[mk], v220\n\t" \
  "v_pk_fma_f16 v233, v233, %[h2], %[h1]\n\t" \
  "v_mfma_f32_16x16x32_bf16 v[172:175], v[208:211], v[136:139], v[172:175]\n\t" \
  "s_waitcnt lgkmcnt(3)\n\t" \
  "v_cmp_gt_u32 vcc, %[tg], v246\n\t" \
  "s_cbranch_vccnz .Lcnx_w5\n.Lcnx_b5:\n\t" \
  "ds_read_b64_tr_b16 v[208:209], %[a2] offset:0\n\t" \
  "ds_read_b64_tr_b16 v[210:211], %[a2] offset:1536\n\t" \
  "v_pk_mul_f16 v233, v232, v233\n\t" \
  "v_fma_f32 v197, |v197|, v225, v197 mul:4\n\t" \
  "s_waitcnt lgkmcnt(4)\n\t" \
  "v_mfma_f32_16x16x32_bf16 v[148:151], v[212:215], v[104:107], v[148:151]\n\t" \
  "s_waitcnt vmcnt(1)\n\t" \
  "s_cmp_lg_u32 %[rnd], 1\n\t" \
  "s_cbranch_scc1 .Lcnx_c16\n\t" \
  "s_waitcnt vmcnt(0)\n\t" \
  ".Lcnx_c16:\n\t" \
  "s_mov_b64 exec, 1\n\t" \
  "ds_inc_u32 v240, %[h2] offset:33864\n\t" \
  "s_mov_b64 exec, -1\n\t" \
  "s_cmp_lg_u32 %[wq], 1\n\t" \
  "s_cbranch_scc1 .Lcnx_c17\n\t" \
  "s_add_u32 m0, %[wl], %[ws]\n\t" \
  "s_nop 0\n\t" \
  "buffer_load_dwordx4 %[wv], %[wrs], %[so0] offen lds\n\t" \
  "buffer_load_dwordx4 %[wv], %[wrs], %[so1] offen offset:1024 lds\n\t" \
  "buffer_load_dwordx4 %[wv], %[wrs], %[so2] offen offset:2048 lds\n\t" \
  "s_add_u32 %[so0], %[so0], %[wc]\n\t" \
  "s_add_u32 %[so1], %[so1], %[wc]\n\t" \
  "s_add_u32 %[so2], %[so2], %[wc]\n\t" \
  ".Lcnx_c17:\n\t" \
  "ds_read_b32 v219, v219 offset:0\n\t" \
  "ds_read_b32 v218, v218 offset:0\n\t" \
  "v_fma_f32 v196, |v196|, v224, v196 mul:4\n\t" \
  "v_fma_f32 v199, |v199|, v227, v199 mul:4\n\t" \
  "v_mfma_f32_16x16x32_bf16 v[156:159], v[212:215], v[116:119], v[156:159]\n\t" \
  "ds_read_b32 v221, v221 offset:0\n\t" \
  "v_fma_f32 v198, |v198|, v226, v198 mul:4\n\t" \
  "v_fma_f32 v205, |v205|, v229, v205 mul:4\n\t" \
  "v_fma_f32 v204, |v204|, v228, v204 mul:4\n\t" \
  "v_mfma_f32_16x16x32_bf16 v[164:167], v[212:215], v[128:131], v[164:167]\n\t" \
  "ds_read_b32 v220, v220 offset:0\n\t" \
  "v_fma_f32 v207, |v207|, v231, v207 mul:4\n\t" \
  "v_fma_f32 v206, |v206|, v230, v206 mul:4\n\t" \
  "v_cvt_pk_bf16_f32 v196, v196, v197\n\t" \
  "v_mfma_f32_16x16x32_bf16 v[172:175], v[212:215], v[140:143], v[172:175]\n\t" \
  "ds_read_b64_tr_b16 v[212:213], %[a2] offset:8\n\t" \
  "v_cvt_pk_bf16_f32 v197, v198, v199\n\t" \
  "v_cvt_pk_bf16_f32 v204, v204, v205\n\t" \
  "v_cvt_pk_bf16_f32 v205, v206, v207\n\t" \
  "s_setprio 2\n\t" \
  "v_exp_f16 v232, v233\n\t" \
  "v_cvt_pk_f16_f32 v224, |v162|, |v163| clamp\n\t" \
  "v_mfma_f32_16x16x32_bf16 v[8:11], v[242:245], v[236:239], v[8:11]\n\t" \
  "ds_read_b64_tr_b16 v[214:215], %[a2] offset:1544\n\t" \
  "v_cvt_pk_f16_f32 v234, v160, v161\n\t" \
  "v_cvt_pk_f16_f32 v226, |v170|, |v171| clamp\n\t" \
  "v_mfma_f32_16x16x32_bf16 v[12:15], v[240:243], v[236:239], v[12:15]\n\t" \
  "buffer_load_dwordx4 v[236:239], %[vo], %[xrs], %[oc0] offen offset:128\n\t" \
  "v_cvt_pk_f16_f32 v228, |v168|, |v169| clamp\n\t" \
  "v_exp_f16_sdwa v232, v233 dst_sel:WORD_1 dst_unused:UNUSED_PRESERVE src0_sel:WORD_1\n\t" \
  "v_pk_mul_f16 v235, v234, v234\n\t" \
  "s_waitcnt lgkmcnt(7)\n\t" \
  "v_mfma_f32_16x16x32_bf16 v[0:3], v[208:211], v[178:181], v[0:3]\n\t" \
  "v_and_b32_sdwa v225, %[mk], v224 dst_sel:DWORD dst_unused:UNUSED_PAD src0_sel:DWORD src1_sel:WORD_1\n\t" \
  "v_and_b32 v224, %[mk], v224\n\t" \
  "v_mfma_f32_16x16x32_bf16 v[24:27], v[208:211], v[186:189], v[24:27]\n\t" \
  "ds_read_b32 v225, v225 offset:0\n\t" \
  "v_and_b32_sdwa v227, %[mk], v226 dst_sel:DWORD dst_unused:UNUSED_PAD src0_sel:DWORD src1_sel:WORD_1\n\t" \
  "v_mfma_f32_16x16x32_bf16 v[48:51], v[208:211], v[194:197], v[48:51]\n\t" \
  "ds_read_b32 v224, v224 offset:0\n\t" \
  "v_and_b32 v226, %[mk], v226\n\t" \
  "v_and_b32_sdwa v229, %[mk], v228 dst_sel:DWORD dst_unused:UNUSED_PAD src0_sel:DWORD src1_sel:WORD_1\n\t" \
  "v_mfma_f32_16x16x32_bf16 v[72:75], v[208:211], v[202:205], v[72:75]\n\t" \
  "ds_read_b64_tr_b16 v[208:209], %[a2] offset:64\n\t" \
  "v_and_b32 v228, %[mk], v228\n\t" \
  "s_waitcnt lgkmcnt(3)\n\t" \
  "v_mfma_f32_16x16x32_bf16 v[4:7], v[212:215], v[178:181], v[4:7]\n\t" \
  "ds_read_b64_tr_b16 v[210:211], %[a2] offset:1600\n\t" \
  "ds_read_b32 v227, v227 offset:0\n\t" \
  "v_pk_fma_f16 v235, v235, %[h2], %[h1]\n\t" \
  "v_mfma_f32_16x16x32_bf16 v[28:31], v[212:215], v[186:189], v[28:31]\n\t" \
  "ds_read_b32 v226, v226 offset:0\n\t" \
  "v_pk_mul_f16 v235, v234, v235\n\t" \
  "v_fma_f32 v147, |v147|, v217, v147 mul:4\n\t" \
  "v_fma_f32 v146, |v146|, v216, v146 mul:4\n\t" \
  "v_mfma_f32_16x16x32_bf16 v[52:55], v[212:215], v[194:197], v[52:55]\n\t" \
  "ds_read_b32 v229, v229 offset:0\n\t" \
  "v_fma_f32 v155, |v155|, v219, v155 mul:4\n\t" \
  "v_fma_f32 v154, |v154|, v218, v154 mul:4\n\t" \
  "v_fma_f32 v153, |v153|, v221, v153 mul:4\n\t" \
  "v_mfma_f32_16x16x32_bf16 v[76:79], v[212:215], v[202:205], v[76:79]\n\t" \
  "ds_read_b64_tr_b16 v[212:213], %[a2] offset:72\n\t" \
  "v_fma_f32 v152, |v152|, v220, v152 mul:4\n\t" \
  "v_pk_fma_f16 v232, v232, %[hh], %[hh]\n\t" \
  "s_waitcnt lgkmcnt(4)\n\t" \
  "v_mfma_f32_16x16x32_bf16 v[8:11], v[208:211], v[178:181], v[8:11]\n\t" \
  "ds_read_b64_tr_b16 v[214:215], %[a2] offset:1608\n\t" \
  "v_cvt_pk_bf16_f32 v147, v146, v147\n\t" \
  "v_cvt_pk_bf16_f32 v155, v154, v155\n\t" \
  "v_cvt_pk_bf16_f32 v154, v152, v153\n\t" \
  "v_mfma_f32_16x16x32_bf16 v[32:35], v[208:211], v[186:189], v[32:35]\n\t" \
  "ds_read_b32 v228, v228 offset:0\n\t" \
  "v_rcp_f16 v233, v232\n\t" \
  "v_exp_f16 v234, v235\n\t" \
  "v_cvt_pk_f16_f32 v216, |v148|, |v149| clamp\n\t" \
  "v_mfma_f32_16x16x32_bf16 v[56:59], v[208:211], v[194:197], v[56:59]\n\t" \
  "v_cvt_pk_f16_f32 v218, |v150|, |v151| clamp\n\t" \
  "v_cvt_pk_f16_f32 v220, |v156|, |v157| clamp\n\t" \
  "v_mfma_f32_16x16x32_bf16 v[80:83], v[208:211], v[202:205], v[80:83]\n\t" \
  "ds_read_b64_tr_b16 v[208:209], %[a2] offset:128\n\t" \
  "v_cvt_pk_f16_f32 v222, |v158|, |v159| clamp\n\t" \
  "s_setprio 2\n\t" \
  "v_rcp_f16_sdwa v232, v232 dst_sel:DWORD dst_unused:UNUSED_PAD src0_sel:WORD_1\n\t" \
  "v_exp_f16_sdwa v234, v235 dst_sel:WORD_1 dst_unused:UNUSED_PRESERVE src0_sel:WORD_1\n\t" \
  "s_waitcnt lgkmcnt(2)\n\t" \
  "v_mfma_f32_16x16x32_bf16 v[12:15], v[212:215], v[178:181], v[12:15]\n\t" \
  "ds_read_b64_tr_b16 v[210:211], %[a2] offset:1664\n\t" \
  "v_and_b32_sdwa v217, %[mk], v216 dst_sel:DWORD dst_unused:UNUSED_PAD src0_sel:DWORD src1_sel:WORD_1\n\t" \
  "v_and_b32 v216, %[mk], v216\n\t" \
  "v_mfma_f32_16x16x32_bf16 v[36:39], v[212:215], v[186:189], v[36:39]\n\t" \
  "ds_read_b32 v217, v217 offset:0\n\t" \
  "v_and_b32_sdwa v219, %[mk], v218 dst_sel:DWORD dst_unused:UNUSED_PAD src0_sel:DWORD src1_sel:WORD_1\n\t" \
  "v_mfma_f32_16x16x32_bf16 v[60:63], v[212:215], v[194:197], v[60:63]\n\t" \
  "ds_read_b32 v216, v216 offset:0\n\t" \
  "v_and_b32 v218, %[mk], v218\n\t" \
  "v_and_b32_sdwa v221, %[mk], v220 dst_sel:DWORD dst_unused:UNUSED_PAD src0_sel:DWORD src1_sel:WORD_1\n\t" \
  "v_mfma_f32_16x16x32_bf16 v[84:87], v[212:215], v[202:205], v[84:87]\n\t" \
  "ds_read_b64_tr_b16 v[212:213], %[a2] offset:136\n\t" \
  "v_and_b32 v220, %[mk], v220\n\t" \
  "s_waitcnt lgkmcnt(3)\n\t" \
  "v_mfma_f32_16x16x32_bf16 v[16:19], v[208:211], v[178:181], v[16:19]\n\t" \
  "ds_read_b32 v246, v240 offset:33864\n\t" \
  "ds_read_b64_tr_b16 v[214:215], %[a2] offset:1672\n\t" \
  "ds_read_b32 v219, v219 offset:0\n\t" \
  "v_and_b32_sdwa v223, %[mk], v222 dst_sel:DWORD dst_unused:UNUSED_PAD src0_sel:DWORD src1_sel:WORD_1\n\t" \
  "v_mfma_f32_16x16x32_bf16 v[40:43], v[208:211], v[186:189], v[40:43]\n\t" \
  "ds_read_b32 v218, v218 offset:0\n\t" \
  "v_and_b32 v222, %[mk], v222\n\t" \
  "v_fma_f32 v163, |v163|, v225, v163 mul:4\n\t" \
  "v_mfma_f32_16x16x32_bf16 v[64:67], v[208:211], v[194:197], v[64:67]\n\t" \
  "ds_read_b32 v221, v221 offset:0\n\t" \
  "v_fma_f32 v162, |v162|, v224, v162 mul:4\n\t" \
  "v_fma_f32 v171, |v171|, v227, v171 mul:4\n\t" \
  "v_mfma_f32_16x16x32_bf16 v[88:91], v[208:211], v[202:205], v[88:91]\n\t" \
  "s_waitcnt lgkmcnt(4)\n\t" \
  "v_cmp_gt_u32 vcc, %[tg], v246\n\t" \
  "s_cbranch_vccnz .Lcnx_w6\n.Lcnx_b6:\n\t" \
  "ds_read_b64_tr_b16 v[208:209], %[a1] offset:0\n\t" \
  "ds_read_b64_tr_b16 v[210:211], %[a1] offset:1024\n\t" \
  "v_fma_f32 v170, |v170|, v226, v170 mul:4\n\t" \
  "v_fma_f32 v169, |v169|, v229, v169 mul:4\n\t" \
  "s_waitcnt lgkmcnt(5)\n\t" \
  "v_mfma_f32_16x16x32_bf16 v[20:23], v[212:215], v[178:181], v[20:23]\n\t" \
  "s_waitcnt vmcnt(1)\n\t" \
  "s_mov_b64 exec, 1\n\t" \
  "ds_inc_u32 v240, %[h2] offset:33868\n\t" \
  "s_mov_b64 exec, -1\n\t" \
  "s_cmp_lg_u32 %[wq], 0\n\t" \
  "s_cbranch_scc1 .Lcnx_c18\n\t" \
  "s_add_u32 m0, %[wl], %[ws]\n\t" \
  "s_nop 0\n\t" \
  "buffer_load_dwordx4 %[wv], %[wrs], %[so0] offen lds\n\t" \
  "buffer_load_dwordx4 %[wv], %[wrs], %[so1] offen offset:1024 lds\n\t" \
  "buffer_load_dwordx4 %[wv], %[wrs], %[so2] offen offset:2048 lds\n\t" \
  "s_add_u32 %[so0], %[so0], %[wc]\n\t" \
  "s_add_u32 %[so1], %[so1], %[wc]\n\t" \
  "s_add_u32 %[so2], %[so2], %[wc]\n\t" \
  ".Lcnx_c18:\n\t" \
  "s_cmp_lg_u32 %[rnd], 0\n\t" \
  "s_cbranch_scc1 .Lcnx_c19\n\t" \
  "s_add_u32 m0, %[xl], 16400\n\t" \
  "s_add_u32 %[xo], %[xo], %[rs]\n\t" \
  "buffer_load_dwordx4 %[xv], %[xrs], %[xo] offen lds\n\t" \
  "s_cmp_lg_u32 %[w512], 3584\n\t" \
  "s_cbranch_scc1 .Lcnx_c19\n\t" \
  "s_mov_b32 m0, %[pf]\n\t" \
  "s_add_u32 %[t0], %[xo], %[wb]\n\t" \
  "buffer_load_dword %[wv], %[xrs], %[t0] offen lds\n\t" \
  ".Lcnx_c19:\n\t" \
  "ds_read_b32 v220, v220 offset:0\n\t" \
  "v_fma_f32 v168, |v168|, v228, v168 mul:4\n\t" \
  "v_fma_mix_f32 v144, v144, v233, 0 op_sel_hi:[0,1,0]\n\t" \
  "v_fma_mix_f32 v145, v145, v232, 0 op_sel_hi:[0,1,0]\n\t" \
  "v_mfma_f32_16x16x32_bf16 v[44:47], v[212:215], v[186:189], v[44:47]\n\t" \
  "ds_read_b32 v223, v223 offset:0\n\t" \
  "v_pk_fma_f16 v234, v234, %[hh], %[hh]\n\t" \
  "v_cvt_pk_bf16_f32 v146, v144, v145\n\t" \
  "v_cvt_pk_bf16_f32 v163, v162, v163\n\t" \
  "v_mfma_f32_16x16x32_bf16 v[68:71], v[212:215], v[194:197], v[68:71]\n\t" \
  "ds_read_b32 v222, v222 offset:0\n\t" \
  "v_cvt_pk_bf16_f32 v171, v170, v171\n\t" \
  "v_cvt_pk_bf16_f32 v170, v168, v169\n\t" \
  "v_rcp_f16 v235, v234\n\t" \
  "s_setprio 1\n\t" \
  "v_mfma_f32_16x16x32_bf16 v[92:95], v[212:215], v[202:205], v[92:95]\n\t" \
  "ds_read_b64_tr_b16 v[212:213], %[a1] offset:3072\n\t" \
  "v_cvt_pk_f16_f32 v224, |v164|, |v165| clamp\n\t" \
  "v_cvt_pk_f16_f32 v226, |v166|, |v167| clamp\n\t" \
  "s_waitcnt lgkmcnt(5)\n\t" \
  "v_mfma_f32_16x16x32_bf16 v[176:179], v[208:211], v[96:99], 0\n\t" \
  "ds_read_b64_tr_b16 v[214:215], %[a1] offset:4096\n\t" \
  "v_cvt_pk_f16_f32 v228, |v172|, |v173| clamp\n\t" \
  "v_cvt_pk_f16_f32 v230, |v174|, |v175| clamp\n\t" \
  "v_rcp_f16_sdwa v234, v234 dst_sel:DWORD dst_unused:UNUSED_PAD src0_sel:WORD_1\n\t" \
  "v_mfma_f32_16x16x32_bf16 v[184:187], v[208:211], v[108:111], 0\n\t" \
  "v_and_b32_sdwa v225, %[mk], v224 dst_sel:DWORD dst_unused:UNUSED_PAD src0_sel:DWORD src1_sel:WORD_1\n\t" \
  "v_and_b32 v224, %[mk], v224\n\t" \
  "v_mfma_f32_16x16x32_bf16 v[192:195], v[208:211], v[120:123], 0\n\t" \
  "ds_read_b32 v225, v225 offset:0\n\t" \
  "v_and_b32_sdwa v227, %[mk], v226 dst_sel:DWORD dst_unused:UNUSED_PAD src0_sel:DWORD src1_sel:WORD_1\n\t" \
  "v_mfma_f32_16x16x32_bf16 v[200:203], v[208:211], v[132:135], 0\n\t" \
  "ds_read_b64_tr_b16 v[208:209], %[a1] offset:2048\n\t" \
  "v_and_b32 v226, %[mk], v226\n\t" \
  "v_and_b32_sdwa v229, %[mk], v228 dst_sel:DWORD dst_unused:UNUSED_PAD src0_sel:DWORD src1_sel:WORD_1\n\t" \
  "s_waitcnt lgkmcnt(2)\n\t" \
  "v_mfma_f32_16x16x32_bf16 v[176:179], v[212:215], v[100:103], v[176:179]\n\t" \
  "ds_read_b64_tr_b16 v[210:211], %[a1] offset:5120\n\t" \
  "v_and_b32 v228, %[mk], v228\n\t" \
  "v_mfma_f32_16x16x32_bf16 v[184:187], v[212:215], v[112:115], v[184:187]\n\t" \
  "ds_read_b32 v224, v224 offset:0\n\t" \
  "ds_read_b32 v227, v227 offset:0\n\t" \
  "v_and_b32_sdwa v231, %[mk], v230 dst_sel:DWORD dst_unused:UNUSED_PAD src0_sel:DWORD src1_sel:WORD_1\n\t" \
  "v_mfma_f32_16x16x32_bf16 v[192:195], v[212:215], v[124:127], v[192:195]\n\t" \
  "ds_read_b32 v226, v226 offset:0\n\t" \
  "v_and_b32 v230, %[mk], v230\n\t" \
  "v_fma_f32 v149, |v149|, v217, v149 mul:4\n\t" \
  "v_mfma_f32_16x16x32_bf16 v[200:203], v[212:215], v[136:139], v[200:203]\n\t" \
  "ds_read_b64_tr_b16 v[212:213], %[a1] offset:512\n\t" \
  "ds_read_b64_tr_b16 v[214:215], %[a1] offset:1536\n\t" \
  "v_fma_f32 v148, |v148|, v216, v148 mul:4\n\t" \
  "v_fma_f32 v151, |v151|, v219, v151 mul:4\n\t" \
  "s_waitcnt lgkmcnt(5)\n\t" \
  "v_mfma_f32_16x16x32_bf16 v[176:179], v[208:211], v[104:107], v[176:179]\n\t" \
  "ds_read_b32 v229, v229 offset:0\n\t" \
  "ds_read_b32 v228, v228 offset:0\n\t" \
  "v_fma_f32 v150, |v150|, v218, v150 mul:4\n\t" \
  "v_fma_f32 v157, |v157|, v221, v157 mul:4\n\t" \
  "v_mfma_f32_16x16x32_bf16 v[184:187], v[208:211], v[116:119], v[184:187]\n\t" \
  "ds_read_b32 v231, v231 offset:0\n\t" \
  "v_fma_f32 v156, |v156|, v220, v156 mul:4\n\t" \
  "v_fma_f32 v159, |v159|, v223, v159 mul:4\n\t" \
  "v_fma_f32 v158, |v158|, v222, v158 mul:4\n\t" \
  "v_mfma_f32_16x16x32_bf16 v[192:195], v[208:211], v[128:131], v[192:195]\n\t" \
  "ds_read_b32 v230, v230 offset:0\n\t" \
  "v_fma_mix_f32 v160, v160, v235, 0 op_sel_hi:[0,1,0]\n\t" \
  "v_fma_mix_f32 v161, v161, v234, 0 op_sel_hi:[0,1,0]\n\t" \
  "v_cvt_pk_bf16_f32 v162, v160, v161\n\t" \
  "v_mfma_f32_16x16x32_bf16 v[200:203], v[208:211], v[140:143], v[200:203]\n\t" \
  "ds_read_b64_tr_b16 v[208:209], %[a1] offset:3584\n\t" \
  "v_cvt_pk_bf16_f32 v148, v148, v149\n\t" \
  "v_cvt_pk_bf16_f32 v149, v150, v151\n\t" \
  "s_waitcnt lgkmcnt(5)\n\t" \
  "v_mfma_f32_16x16x32_bf16 v[180:183], v[212:215], v[96:99], 0\n\t" \
  "ds_read_b64_tr_b16 v[210:211], %[a1] offset:4608\n\t" \
  "s_setprio 0\n\t" \
  "v_cvt_pk_bf16_f32 v156, v156, v157\n\t" \
  "v_cvt_pk_bf16_f32 v157, v158, v159\n\t" \
  "v_cvt_pk_f16_f32 v216, |v178|, |v179| clamp\n\t" \
  "v_mfma_f32_16x16x32_bf16 v[188:191], v[212:215], v[108:111], 0\n\t" \
  "v_cvt_pk_f16_f32 v232, v176, v177\n\t" \
  "v_cvt_pk_f16_f32 v218, |v186|, |v187| clamp\n\t" \
  "v_cvt_pk_f16_f32 v220, |v184|, |v185| clamp\n\t" \
  "v_mfma_f32_16x16x32_bf16 v[196:199], v[212:215], v[120:123], 0\n\t" \
  "v_pk_mul_f16 v233, v232, v232\n\t" \
  "v_and_b32_sdwa v217, %[mk], v216 dst_sel:DWORD dst_unused:UNUSED_PAD src0_sel:DWORD src1_sel:WORD_1\n\t" \
  "v_mfma_f32_16x16x32_bf16 v[204:207], v[212:215], v[132:135], 0\n\t" \
  "ds_read_b64_tr_b16 v[212:213], %[a1] offset:2560\n\t" \
  "v_and_b32 v216, %[mk], v216\n\t" \
  "v_and_b32_sdwa v219, %[mk], v218 dst_sel:DWORD dst_unused:UNUSED_PAD src0_sel:DWORD src1_sel:WORD_1\n\t" \
  "s_waitcnt lgkmcnt(1)\n\t" \
  "v_mfma_f32_16x16x32_bf16 v[180:183], v[208:211], v[100:103], v[180:183]\n\t" \
  "ds_read_b32 v246, v240 offset:33868\n\t" \
  "ds_read_b64_tr_b16 v[214:215], %[a1] offset:5632\n\t" \
  "v_and_b32 v218, %[mk], v218\n\t" \
  "v_mfma_f32_16x16x32_bf16 v[188:191], v[208:211], v[112:115], v[188:191]\n\t" \
  "ds_read_b32 v217, v217 offset:0\n\t" \
  "v_and_b32_sdwa v221, %[mk], v220 dst_sel:DWORD dst_unused:UNUSED_PAD src0_sel:DWORD src1_sel:WORD_1\n\t" \
  "v_mfma_f32_16x16x32_bf16 v[196:199], v[208:211], v[124:127], v[196:199]\n\t" \
  "ds_read_b32 v216, v216 offset:0\n\t" \
  "v_and_b32 v220, %[mk], v220\n\t" \
  "v_pk_fma_f16 v233, v233, %[h2], %[h1]\n\t" \
  "v_mfma_f32_16x16x32_bf16 v[204:207], v[208:211], v[136:139], v[204:207]\n\t" \
  "s_waitcnt lgkmcnt(3)\n\t" \
  "v_cmp_gt_u32 vcc, %[tg], v246\n\t" \
  "s_cbranch_vccnz .Lcnx_w7\n.Lcnx_b7:\n\t" \
  "ds_read_b64_tr_b16 v[208:209], %[a2] offset:6160\n\t" \
  "ds_read_b64_tr_b16 v[210:211], %[a2] offset:7696\n\t" \
  "v_pk_mul_f16 v233, v232, v233\n\t" \
  "v_fma_f32 v165, |v165|, v225, v165 mul:4\n\t" \
  "s_waitcnt lgkmcnt(4)\n\t" \
  "v_mfma_f32_16x16x32_bf16 v[180:183], v[212:215], v[104:107], v[180:183]\n\t" \
  "s_waitcnt vmcnt(1)\n\t" \
  "s_cmp_lg_u32 %[rnd], 1\n\t" \
  "s_cbranch_scc1 .Lcnx_c21\n\t" \
  "s_waitcnt vmcnt(0)\n\t" \
  ".Lcnx_c21:\n\t" \
  "s_mov_b64 exec, 1\n\t" \
  "ds_inc_u32 v240, %[h2] offset:33872\n\t" \
  "s_mov_b64 exec, -1\n\t" \
  "s_cmp_lg_u32 %[wq], 1\n\t" \
  "s_cbranch_scc1 .Lcnx_c22\n\t" \
  "s_add_u32 m0, %[wl], 0\n\t" \
  "s_nop 0\n\t" \
  "buffer_load_dwordx4 %[wv], %[wrs], %[so0] offen lds\n\t" \
  "buffer_load_dwordx4 %[wv], %[wrs], %[so1] offen offset:1024 lds\n\t" \
  "buffer_load_dwordx4 %[wv], %[wrs], %[so2] offen offset:2048 lds\n\t" \
  "s_add_u32 %[so0], %[so0], %[wc]\n\t" \
  "s_add_u32 %[so1], %[so1], %[wc]\n\t" \
  "s_add_u32 %[so2], %[so2], %[wc]\n\t" \
  ".Lcnx_c22:\n\t" \
  "ds_read_b32 v219, v219 offset:0\n\t" \
  "ds_read_b32 v218, v218 offset:0\n\t" \
  "v_fma_f32 v164, |v164|, v224, v164 mul:4\n\t" \
  "v_fma_f32 v167, |v167|, v227, v167 mul:4\n\t" \
  "v_mfma_f32_16x16x32_bf16 v[188:191], v[212:215], v[116:119], v[188:191]\n\t" \
  "ds_read_b32 v221, v221 offset:0\n\t" \
  "v_fma_f32 v166, |v166|, v226, v166 mul:4\n\t" \
  "v_fma_f32 v173, |v173|, v229, v173 mul:4\n\t" \
  "v_fma_f32 v172, |v172|, v228, v172 mul:4\n\t" \
  "v_mfma_f32_16x16x32_bf16 v[196:199], v[212:215], v[128:131], v[196:199]\n\t" \
  "ds_read_b32 v220, v220 offset:0\n\t" \
  "v_fma_f32 v175, |v175|, v231, v175 mul:4\n\t" \
  "v_fma_f32 v174, |v174|, v230, v174 mul:4\n\t" \
  "v_cvt_pk_bf16_f32 v164, v164, v165\n\t" \
  "v_mfma_f32_16x16x32_bf16 v[204:207], v[212:215], v[140:143], v[204:207]\n\t" \
  "ds_read_b64_tr_b16 v[212:213], %[a2] offset:6168\n\t" \
  "v_cvt_pk_bf16_f32 v165, v166, v167\n\t" \
  "v_cvt_pk_bf16_f32 v172, v172, v173\n\t" \
  "v_cvt_pk_bf16_f32 v173, v174, v175\n\t" \
  "s_setprio 2\n\t" \
  "v_exp_f16 v232, v233\n\t" \
  "v_cvt_pk_f16_f32 v224, |v194|, |v195| clamp\n\t" \
  "v_mfma_f32_16x16x32_bf16 v[16:19], v[242:245], v[236:239], v[16:19]\n\t" \
  "ds_read_b64_tr_b16 v[214:215], %[a2] offset:7704\n\t" \
  "v_cvt_pk_f16_f32 v234, v192, v193\n\t" \
  "v_cvt_pk_f16_f32 v226, |v202|, |v203| clamp\n\t" \
  "v_mfma_f32_16x16x32_bf16 v[20:23], v[240:243], v[236:239], v[20:23]\n\t" \
  "buffer_load_dwordx4 v[236:239], %[vo], %[xrs], %[oc0] offen offset:192\n\t" \
  "v_cvt_pk_f16_f32 v228, |v200|, |v201| clamp\n\t" \
  "v_exp_f16_sdwa v232, v233 dst_sel:WORD_1 dst_unused:UNUSED_PRESERVE src0_sel:WORD_1\n\t" \
  "v_pk_mul_f16 v235, v234, v234\n\t" \
  "s_waitcnt lgkmcnt(7)\n\t" \
  "v_mfma_f32_16x16x32_bf16 v[0:3], v[208:211], v[146:149], v[0:3]\n\t" \
  "v_and_b32_sdwa v225, %[mk], v224 dst_sel:DWORD dst_unused:UNUSED_PAD src0_sel:DWORD src1_sel:WORD_1\n\t" \
  "v_and_b32 v224, %[mk], v224\n\t" \
  "v_mfma_f32_16x16x32_bf16 v[24:27], v[208:211], v[154:157], v[24:27]\n\t" \
  "ds_read_b32 v225, v225 offset:0\n\t" \
  "v_and_b32_sdwa v227, %[mk], v226 dst_sel:DWORD dst_unused:UNUSED_PAD src0_sel:DWORD src1_sel:WORD_1\n\t" \
  "v_mfma_f32_16x16x32_bf16 v[48:51], v[208:211], v[162:165], v[48:51]\n\t" \
  "ds_read_b32 v224, v224 offset:0\n\t" \
  "v_and_b32 v226, %[mk], v226\n\t" \
  "v_and_b32_sdwa v229, %[mk], v228 dst_sel:DWORD dst_unused:UNUSED_PAD src0_sel:DWORD src1_sel:WORD_1\n\t" \
  "v_mfma_f32_16x16x32_bf16 v[72:75], v[208:211], v[170:173], v[72:75]\n\t" \
  "ds_read_b64_tr_b16 v[208:209], %[a2] offset:6224\n\t" \
  "v_and_b32 v228, %[mk], v228\n\t" \
  "s_waitcnt lgkmcnt(3)\n\t" \
  "v_mfma_f32_16x16x32_bf16 v[4:7], v[212:215], v[146:149], v[4:7]\n\t" \
  "ds_read_b64_tr_b16 v[210:211], %[a2] offset:7760\n\t" \
  "ds_read_b32 v227, v227 offset:0\n\t" \
  "v_pk_fma_f16 v235, v235, %[h2], %[h1]\n\t" \
  "v_mfma_f32_16x16x32_bf16 v[28:31], v[212:215], v[154:157], v[28:31]\n\t" \
  "ds_read_b32 v226, v226 offset:0\n\t" \
  "v_pk_mul_f16 v235, v234, v235\n\t" \
  "v_fma_f32 v179, |v179|, v217, v179 mul:4\n\t" \
  "v_fma_f32 v178, |v178|, v216, v178 mul:4\n\t" \
  "v_mfma_f32_16x16x32_bf16 v[52:55], v[212:215], v[162:165], v[52:55]\n\t" \
  "ds_read_b32 v229, v229 offset:0\n\t" \
  "v_fma_f32 v187, |v187|, v219, v187 mul:4\n\t" \
  "v_fma_f32 v186, |v186|, v218, v186 mul:4\n\t" \
  "v_fma_f32 v185, |v185|, v221, v185 mul:4\n\t" \
  "v_mfma_f32_16x16x32_bf16 v[76:79], v[212:215], v[170:173], v[76:79]\n\t" \
  "ds_read_b64_tr_b16 v[212:213], %[a2] offset:6232\n\t" \
  "v_fma_f32 v184, |v184|, v220, v184 mul:4\n\t" \
  "v_pk_fma_f16 v232, v232, %[hh], %[hh]\n\t" \
  "s_waitcnt lgkmcnt(4)\n\t" \
  "v_mfma_f32_16x16x32_bf16 v[8:11], v[208:211], v[146:149], v[8:11]\n\t" \
  "ds_read_b64_tr_b16 v[214:215], %[a2] offset:7768\n\t" \
  "v_cvt_pk_bf16_f32 v179, v178, v179\n\t" \
  "v_cvt_pk_bf16_f32 v187, v186, v187\n\t" \
  "v_cvt_pk_bf16_f32 v186, v184, v185\n\t" \
  "v_mfma_f32_16x16x32_bf16 v[32:35], v[208:211], v[154:157], v[32:35]\n\t" \
  "ds_read_b32 v228, v228 offset:0\n\t" \
  "v_rcp_f16 v233, v232\n\t" \
  "v_exp_f16 v234, v235\n\t" \
  "v_cvt_pk_f16_f32 v216, |v180|, |v181| clamp\n\t" \
  "v_mfma_f32_16x16x32_bf16 v[56:59], v[208:211], v[162:165], v[56:59]\n\t" \
  "v_cvt_pk_f16_f32 v218, |v182|, |v183| clamp\n\t" \
  "v_cvt_pk_f16_f32 v220, |v188|, |v189| clamp\n\t" \
  "v_mfma_f32_16x16x32_bf16 v[80:83], v[208:211], v[170:173], v[80:83]\n\t" \
  "ds_read_b64_tr_b16 v[208:209], %[a2] offset:6288\n\t" \
  "v_cvt_pk_f16_f32 v222, |v190|, |v191| clamp\n\t" \
  "s_setprio 2\n\t" \
  "v_rcp_f16_sdwa v232, v232 dst_sel:DWORD dst_unused:UNUSED_PAD src0_sel:WORD_1\n\t" \
  "v_exp_f16_sdwa v234, v235 dst_sel:WORD_1 dst_unused:UNUSED_PRESERVE src0_sel:WORD_1\n\t" \
  "s_waitcnt lgkmcnt(2)\n\t" \
  "v_mfma_f32_16x16x32_bf16 v[12:15], v[212:215], v[146:149], v[12:15]\n\t" \
  "ds_read_b64_tr_b16 v[210:211], %[a2] offset:7824\n\t" \
  "v_and_b32_sdwa v217, %[mk], v216 dst_sel:DWORD dst_unused:UNUSED_PAD src0_sel:DWORD src1_sel:WORD_1\n\t" \
  "v_and_b32 v216, %[mk], v216\n\t" \
  "v_mfma_f32_16x16x32_bf16 v[36:39], v[212:215], v[154:157], v[36:39]\n\t" \
  "ds_read_b32 v217, v217 offset:0\n\t" \
  "v_and_b32_sdwa v219, %[mk], v218 dst_sel:DWORD dst_unused:UNUSED_PAD src0_sel:DWORD src1_sel:WORD_1\n\t" \
  "v_mfma_f32_16x16x32_bf16 v[60:63], v[212:215], v[162:165], v[60:63]\n\t" \
  "ds_read_b32 v216, v216 offset:0\n\t" \
  "v_and_b32 v218, %[mk], v218\n\t" \
  "v_and_b32_sdwa v221, %[mk], v220 dst_sel:DWORD dst_unused:UNUSED_PAD src0_sel:DWORD src1_sel:WORD_1\n\t" \
  "v_mfma_f32_16x16x32_bf16 v[84:87], v[212:215], v[170:173], v[84:87]\n\t" \
  "ds_read_b64_tr_b16 v[212:213], %[a2] offset:6296\n\t" \
  "v_and_b32 v220, %[mk], v220\n\t" \
  "s_waitcnt lgkmcnt(3)\n\t" \
  "v_mfma_f32_16x16x32_bf16 v[16:19], v[208:211], v[146:149], v[16:19]\n\t" \
  "ds_read_b32 v246, v240 offset:33872\n\t" \
  "ds_read_b64_tr_b16 v[214:215], %[a2] offset:7832\n\t" \
  "ds_read_b32 v219, v219 offset:0\n\t" \
  "v_and_b32_sdwa v223, %[mk], v222 dst_sel:DWORD dst_unused:UNUSED_PAD src0_sel:DWORD src1_sel:WORD_1\n\t" \
  "v_mfma_f32_16x16x32_bf16 v[40:43], v[208:211], v[154:157], v[40:43]\n\t" \
  "ds_read_b32 v218, v218 offset:0\n\t" \
  "v_and_b32 v222, %[mk], v222\n\t" \
  "v_fma_f32 v195, |v195|, v225, v195 mul:4\n\t" \
  "v_mfma_f32_16x16x32_bf16 v[64:67], v[208:211], v[162:165], v[64:67]\n\t" \
  "ds_read_b32 v221, v221 offset:0\n\t" \
  "v_fma_f32 v194, |v194|, v224, v194 mul:4\n\t" \
  "v_fma_f32 v203, |v203|, v227, v203 mul:4\n\t" \
  "v_mfma_f32_16x16x32_bf16 v[88:91], v[208:211], v[170:173], v[88:91]\n\t" \
  "s_waitcnt lgkmcnt(4)\n\t" \
  "v_cmp_gt_u32 vcc, %[tg], v246\n\t" \
  "s_cbranch_vccnz .Lcnx_w8\n.Lcnx_b8:\n\t" \
  "ds_read_b64_tr_b16 v[208:209], %[a1] offset:6144\n\t" \
  "ds_read_b64_tr_b16 v[210:211], %[a1] offset:7168\n\t" \
  "v_fma_f32 v202, |v202|, v226, v202 mul:4\n\t" \
  "v_fma_f32 v201, |v201|, v229, v201 mul:4\n\t" \
  "s_waitcnt lgkmcnt(5)\n\t" \
  "v_mfma_f32_16x16x32_bf16 v[20:23], v[212:215], v[146:149], v[20:23]\n\t" \
  "s_waitcnt vmcnt(1)\n\t" \
  "s_mov_b64 exec, 1\n\t" \
  "ds_inc_u32 v240, %[h2] offset:33876\n\t" \
  "s_mov_b64 exec, -1\n\t" \
  "s_cmp_lg_u32 %[wq], 0\n\t" \
  "s_cbranch_scc1 .Lcnx_c23\n\t" \
  "s_add_u32 m0, %[wl], 0\n\t" \
  "s_nop 0\n\t" \
  "buffer_load_dwordx4 %[wv], %[wrs], %[so0] offen lds\n\t" \
  "buffer_load_dwordx4 %[wv], %[wrs], %[so1] offen offset:1024 lds\n\t" \
  "buffer_load_dwordx4 %[wv], %[wrs], %[so2] offen offset:2048 lds\n\t" \
  "s_add_u32 %[so0], %[so0], %[wc]\n\t" \
  "s_add_u32 %[so1], %[so1], %[wc]\n\t" \
  "s_add_u32 %[so2], %[so2], %[wc]\n\t" \
  ".Lcnx_c23:\n\t" \
  "s_cmp_lg_u32 %[rnd], 0\n\t" \
  "s_cbranch_scc1 .Lcnx_c24\n\t" \
  "s_add_u32 m0, %[xl], 24600\n\t" \
  "s_add_u32 %[xo], %[xo], %[rs]\n\t" \
  "buffer_load_dwordx4 %[xv], %[xrs], %[xo] offen lds\n\t" \
  "s_cmp_lg_u32 %[w512], 2048\n\t" \
  "s_cbranch_scc1 .Lcnx_c24\n\t" \
  "s_mov_b32 m0, %[pf]\n\t" \
  "s_add_u32 %[t0], %[xo], %[wb]\n\t" \
  "buffer_load_dword %[wv], %[xrs], %[t0] offen lds\n\t" \
  ".Lcnx_c24:\n\t" \
  "ds_read_b32 v220, v220 offset:0\n\t" \
  "v_fma_f32 v200, |v200|, v228, v200 mul:4\n\t" \
  "v_fma_mix_f32 v176, v176, v233, 0 op_sel_hi:[0,1,0]\n\t" \
  "v_fma_mix_f32 v177, v177, v232, 0 op_sel_hi:[0,1,0]\n\t" \
  "v_mfma_f32_16x16x32_bf16 v[44:47], v[212:215], v[154:157], v[44:47]\n\t" \
  "ds_read_b32 v223, v223 offset:0\n\t" \
  "v_pk_fma_f16 v234, v234, %[hh], %[hh]\n\t" \
  "v_cvt_pk_bf16_f32 v178, v176, v177\n\t" \
  "v_cvt_pk_bf16_f32 v195, v194, v195\n\t" \
  "v_mfma_f32_16x16x32_bf16 v[68:71], v[212:215], v[162:165], v[68:71]\n\t" \
  "ds_read_b32 v222, v222 offset:0\n\t" \
  "v_cvt_pk_bf16_f32 v203, v202, v203\n\t" \
  "v_cvt_pk_bf16_f32 v202, v200, v201\n\t" \
  "v_rcp_f16 v235, v234\n\t" \
  "s_setprio 1\n\t" \
  "v_mfma_f32_16x16x32_bf16 v[92:95], v[212:215], v[170:173], v[92:95]\n\t" \
  "ds_read_b64_tr_b16 v[212:213], %[a1] offset:9216\n\t" \
  "v_cvt_pk_f16_f32 v224, |v196|, |v197| clamp\n\t" \
  "v_cvt_pk_f16_f32 v226, |v198|, |v199| clamp\n\t" \
  "s_waitcnt lgkmcnt(5)\n\t" \
  "v_mfma_f32_16x16x32_bf16 v[144:147], v[208:211], v[96:99], 0\n\t" \
  "ds_read_b64_tr_b16 v[214:215], %[a1] offset:10240\n\t" \
  "v_cvt_pk_f16_f32 v228, |v204|, |v205| clamp\n\t" \
  "v_cvt_pk_f16_f32 v230, |v206|, |v207| clamp\n\t" \
  "v_rcp_f16_sdwa v234, v234 dst_sel:DWORD dst_unused:UNUSED_PAD src0_sel:WORD_1\n\t" \
  "v_mfma_f32_16x16x32_bf16 v[152:155], v[208:211], v[108:111], 0\n\t" \
  "v_and_b32_sdwa v225, %[mk], v224 dst_sel:DWORD dst_unused:UNUSED_PAD src0_sel:DWORD src1_sel:WORD_1\n\t" \
  "v_and_b32 v224, %[mk], v224\n\t" \
  "v_mfma_f32_16x16x32_bf16 v[160:163], v[208:211], v[120:123], 0\n\t" \
  "ds_read_b32 v225, v225 offset:0\n\t" \
  "v_and_b32_sdwa v227, %[mk], v226 dst_sel:DWORD dst_unused:UNUSED_PAD src0_sel:DWORD src1_sel:WORD_1\n\t" \
  "v_mfma_f32_16x16x32_bf16 v[168:171], v[208:211], v[132:135], 0\n\t" \
  "ds_read_b64_tr_b16 v[208:209], %[a1] offset:8192\n\t" \
  "v_and_b32 v226, %[mk], v226\n\t" \
  "v_and_b32_sdwa v229, %[mk], v228 dst_sel:DWORD dst_unused:UNUSED_PAD src0_sel:DWORD src1_sel:WORD_1\n\t" \
  "s_waitcnt lgkmcnt(2)\n\t" \
  "v_mfma_f32_16x16x32_bf16 v[144:147], v[212:215], v[100:103], v[144:147]\n\t" \
  "ds_read_b64_tr_b16 v[210:211], %[a1] offset:11264\n\t" \
  "v_and_b32 v228, %[mk], v228\n\t" \
  "v_mfma_f32_16x16x32_bf16 v[152:155], v[212:215], v[112:115], v[152:155]\n\t" \
  "ds_read_b32 v224, v224 offset:0\n\t" \
  "ds_read_b32 v227, v227 offset:0\n\t" \
  "v_and_b32_sdwa v231, %[mk], v230 dst_sel:DWORD dst_unused:UNUSED_PAD src0_sel:DWORD src1_sel:WORD_1\n\t" \
  "v_mfma_f32_16x16x32_bf16 v[160:163], v[212:215], v[124:127], v[160:163]\n\t" \
  "ds_read_b32 v226, v226 offset:0\n\t" \
  "v_and_b32 v230, %[mk], v230\n\t" \
  "v_fma_f32 v181, |v181|, v217, v181 mul:4\n\t" \
  "v_mfma_f32_16x16x32_bf16 v[168:171], v[212:215], v[136:139], v[168:171]\n\t" \
  "ds_read_b64_tr_b16 v[212:213], %[a1] offset:6656\n\t" \
  "ds_read_b64_tr_b16 v[214:215], %[a1] offset:7680\n\t" \
  "v_fma_f32 v180, |v180|, v216, v180 mul:4\n\t" \
  "v_fma_f32 v183, |v183|, v219, v183 mul:4\n\t" \
  "s_waitcnt lgkmcnt(5)\n\t" \
  "v_mfma_f32_16x16x32_bf16 v[144:147], v[208:211], v[104:107], v[144:147]\n\t" \
  "ds_read_b32 v229, v229 offset:0\n\t" \
  "ds_read_b32 v228, v228 offset:0\n\t" \
  "v_fma_f32 v182, |v182|, v218, v182 mul:4\n\t" \
  "v_fma_f32 v189, |v189|, v221, v189 mul:4\n\t" \
  "v_mfma_f32_16x16x32_bf16 v[152:155], v[208:211], v[116:119], v[152:155]\n\t" \
  "ds_read_b32 v231, v231 offset:0\n\t" \
  "v_fma_f32 v188, |v188|, v220, v188 mul:4\n\t" \
  "v_fma_f32 v191, |v191|, v223, v191 mul:4\n\t" \
  "v_fma_f32 v190, |v190|, v222, v190 mul:4\n\t" \
  "v_mfma_f32_16x16x32_bf16 v[160:163], v[208:211], v[128:131], v[160:163]\n\t" \
  "ds_read_b32 v230, v230 offset:0\n\t" \
  "v_fma_mix_f32 v192, v192, v235, 0 op_sel_hi:[0,1,0]\n\t" \
  "v_fma_mix_f32 v193, v193, v234, 0 op_sel_hi:[0,1,0]\n\t" \
  "v_cvt_pk_bf16_f32 v194, v192, v193\n\t" \
  "v_mfma_f32_16x16x32_bf16 v[168:171], v[208:211], v[140:143], v[168:171]\n\t" \
  "ds_read_b64_tr_b16 v[208:209], %[a1] offset:9728\n\t" \
  "v_cvt_pk_bf16_f32 v180, v180, v181\n\t" \
  "v_cvt_pk_bf16_f32 v181, v182, v183\n\t" \
  "s_waitcnt lgkmcnt(5)\n\t" \
  "v_mfma_f32_16x16x32_bf16 v[148:151], v[212:215], v[96:99], 0\n\t" \
  "ds_read_b64_tr_b16 v[210:211], %[a1] offset:10752\n\t" \
  "s_setprio 0\n\t" \
  "v_cvt_pk_bf16_f32 v188, v188, v189\n\t" \
  "v_cvt_pk_bf16_f32 v189, v190, v191\n\t" \
  "v_cvt_pk_f16_f32 v216, |v146|, |v147| clamp\n\t" \
  "v_mfma_f32_16x16x32_bf16 v[156:159], v[212:215], v[108:111], 0\n\t" \
  "v_cvt_pk_f16_f32 v232, v144, v145\n\t" \
  "v_cvt_pk_f16_f32 v218, |v154|, |v155| clamp\n\t" \
  "v_cvt_pk_f16_f32 v220, |v152|, |v153| clamp\n\t" \
  "v_mfma_f32_16x16x32_bf16 v[164:167], v[212:215], v[120:123], 0\n\t" \
  "v_pk_mul_f16 v233, v232, v232\n\t" \
  "v_and_b32_sdwa v217, %[mk], v216 dst_sel:DWORD dst_unused:UNUSED_PAD src0_sel:DWORD src1_sel:WORD_1\n\t" \
  "v_mfma_f32_16x16x32_bf16 v[172:175], v[212:215], v[132:135], 0\n\t" \
  "ds_read_b64_tr_b16 v[212:213], %[a1] offset:8704\n\t" \
  "v_and_b32 v216, %[mk], v216\n\t" \
  "v_and_b32_sdwa v219, %[mk], v218 dst_sel:DWORD dst_unused:UNUSED_PAD src0_sel:DWORD src1_sel:WORD_1\n\t" \
  "s_waitcnt lgkmcnt(1)\n\t" \
  "v_mfma_f32_16x16x32_bf16 v[148:151], v[208:211], v[100:103], v[148:151]\n\t" \
  "ds_read_b32 v246, v240 offset:33876\n\t" \
  "ds_read_b64_tr_b16 v[214:215], %[a1] offset:11776\n\t" \
  "v_and_b32 v218, %[mk], v218\n\t" \
  "v_mfma_f32_16x16x32_bf16 v[156:159], v[208:211], v[112:115], v[156:159]\n\t" \
  "ds_read_b32 v217, v217 offset:0\n\t" \
  "v_and_b32_sdwa v221, %[mk], v220 dst_sel:DWORD dst_unused:UNUSED_PAD src0_sel:DWORD src1_sel:WORD_1\n\t" \
  "v_mfma_f32_16x16x32_bf16 v[164:167], v[208:211], v[124:127], v[164:167]\n\t" \
  "ds_read_b32 v216, v216 offset:0\n\t" \
  "v_and_b32 v220, %[mk], v220\n\t" \
  "v_pk_fma_f16 v233, v233, %[h2], %[h1]\n\t" \
  "v_mfma_f32_16x16x32_bf16 v[172:175], v[208:211], v[136:139], v[172:175]\n\t" \
  "s_waitcnt lgkmcnt(3)\n\t" \
  "v_cmp_gt_u32 vcc, %[tg], v246\n\t" \
  "s_cbranch_vccnz .Lcnx_w9\n.Lcnx_b9:\n\t" \
  "ds_read_b64_tr_b16 v[208:209], %[a2] offset:0\n\t" \
  "ds_read_b64_tr_b16 v[210:211], %[a2] offset:1536\n\t" \
  "v_pk_mul_f16 v233, v232, v233\n\t" \
  "v_fma_f32 v197, |v197|, v225, v197 mul:4\n\t" \
  "s_waitcnt lgkmcnt(4)\n\t" \
  "v_mfma_f32_16x16x32_bf16 v[148:151], v[212:215], v[104:107], v[148:151]\n\t" \
  "s_waitcnt vmcnt(1)\n\t" \
  "s_cmp_lg_u32 %[rnd], 1\n\t" \
  "s_cbranch_scc1 .Lcnx_c26\n\t" \
  "s_waitcnt vmcnt(0)\n\t" \
  ".Lcnx_c26:\n\t" \
  "s_mov_b64 exec, 1\n\t" \
  "ds_inc_u32 v240, %[h2] offset:33880\n\t" \
  "s_mov_b64 exec, -1\n\t" \
  "s_cmp_lg_u32 %[wq], 1\n\t" \
  "s_cbranch_scc1 .Lcnx_c27\n\t" \
  "s_add_u32 m0, %[wl], %[ws]\n\t" \
  "s_nop 0\n\t" \
  "buffer_load_dwordx4 %[wv], %[wrs], %[so0] offen lds\n\t" \
  "buffer_load_dwordx4 %[wv], %[wrs], %[so1] offen offset:1024 lds\n\t" \
  "buffer_load_dwordx4 %[wv], %[wrs], %[so2] offen offset:2048 lds\n\t" \
  "s_add_u32 %[so0], %[so0], %[wc]\n\t" \
  "s_add_u32 %[so1], %[so1], %[wc]\n\t" \
  "s_add_u32 %[so2], %[so2], %[wc]\n\t" \
  ".Lcnx_c27:\n\t" \
  "ds_read_b32 v219, v219 offset:0\n\t" \
  "ds_read_b32 v218, v218 offset:0\n\t" \
  "v_fma_f32 v196, |v196|, v224, v196 mul:4\n\t" \
  "v_fma_f32 v199, |v199|, v227, v199 mul:4\n\t" \
  "v_mfma_f32_16x16x32_bf16 v[156:159], v[212:215], v[116:119], v[156:159]\n\t" \
  "ds_read_b32 v221, v221 offset:0\n\t" \
  "v_fma_f32 v198, |v198|, v226, v198 mul:4\n\t" \
  "v_fma_f32 v205, |v205|, v229, v205 mul:4\n\t" \
  "v_fma_f32 v204, |v204|, v228, v204 mul:4\n\t" \
  "v_mfma_f32_16x16x32_bf16 v[164:167], v[212:215], v[128:131], v[164:167]\n\t" \
  "ds_read_b32 v220, v220 offset:0\n\t" \
  "v_fma_f32 v207, |v207|, v231, v207 mul:4\n\t" \
  "v_fma_f32 v206, |v206|, v230, v206 mul:4\n\t" \
  "v_cvt_pk_bf16_f32 v196, v196, v197\n\t" \
  "v_mfma_f32_16x16x32_bf16 v[172:175], v[212:215], v[140:143], v[172:175]\n\t" \
  "ds_read_b64_tr_b16 v[212:213], %[a2] offset:8\n\t" \
  "v_cvt_pk_bf16_f32 v197, v198, v199\n\t" \
  "v_cvt_pk_bf16_f32 v204, v204, v205\n\t" \
  "v_cvt_pk_bf16_f32 v205, v206, v207\n\t" \
  "s_setprio 2\n\t" \
  "v_exp_f16 v232, v233\n\t" \
  "v_cvt_pk_f16_f32 v224, |v162|, |v163| clamp\n\t" \
  "v_mfma_f32_16x16x32_bf16 v[24:27], v[242:245], v[236:239], v[24:27]\n\t" \
  "ds_read_b64_tr_b16 v[214:215], %[a2] offset:1544\n\t" \
  "v_cvt_pk_f16_f32 v234, v160, v161\n\t" \
  "v_cvt_pk_f16_f32 v226, |v170|, |v171| clamp\n\t" \
  "v_mfma_f32_16x16x32_bf16 v[28:31], v[240:243], v[236:239], v[28:31]\n\t" \
  "buffer_load_dwordx4 v[236:239], %[vo], %[xrs], %[oc0] offen offset:256\n\t" \
  "v_cvt_pk_f16_f32 v228, |v168|, |v169| clamp\n\t" \
  "v_exp_f16_sdwa v232, v233 dst_sel:WORD_1 dst_unused:UNUSED_PRESERVE src0_sel:WORD_1\n\t" \
  "v_pk_mul_f16 v235, v234, v234\n\t" \
  "s_waitcnt lgkmcnt(7)\n\t" \
  "v_mfma_f32_16x16x32_bf16 v[0:3], v[208:211], v[178:181], v[0:3]\n\t" \
  "v_and_b32_sdwa v225, %[mk], v224 dst_sel:DWORD dst_unused:UNUSED_PAD src0_sel:DWORD src1_sel:WORD_1\n\t" \
  "v_and_b32 v224, %[mk], v224\n\t" \
  "v_mfma_f32_16x16x32_bf16 v[24:27], v[208:211], v[186:189], v[24:27]\n\t" \
  "ds_read_b32 v225, v225 offset:0\n\t" \
  "v_and_b32_sdwa v227, %[mk], v226 dst_sel:DWORD dst_unused:UNUSED_PAD src0_sel:DWORD src1_sel:WORD_1\n\t" \
  "v_mfma_f32_16x16x32_bf16 v[48:51], v[208:211], v[194:197], v[48:51]\n\t" \
  "ds_read_b32 v224, v224 offset:0\n\t" \
  "v_and_b32 v226, %[mk], v226\n\t" \
  "v_and_b32_sdwa v229, %[mk], v228 dst_sel:DWORD dst_unused:UNUSED_PAD src0_sel:DWORD src1_sel:WORD_1\n\t" \
  "v_mfma_f32_16x16x32_bf16 v[72:75], v[208:211], v[202:205], v[72:75]\n\t" \
  "ds_read_b64_tr_b16 v[208:209], %[a2] offset:64\n\t" \
  "v_and_b32 v228, %[mk], v228\n\t" \
  "s_waitcnt lgkmcnt(3)\n\t" \
  "v_mfma_f32_16x16x32_bf16 v[4:7], v[212:215], v[178:181], v[4:7]\n\t" \
  "ds_read_b64_tr_b16 v[210:211], %[a2] offset:1600\n\t" \
  "ds_read_b32 v227, v227 offset:0\n\t" \
  "v_pk_fma_f16 v235, v235, %[h2], %[h1]\n\t" \
  "v_mfma_f32_16x16x32_bf16 v[28:31], v[212:215], v[186:189], v[28:31]\n\t" \
  "ds_read_b32 v226, v226 offset:0\n\t" \
  "v_pk_mul_f16 v235, v234, v235\n\t" \
  "v_fma_f32 v147, |v147|, v217, v147 mul:4\n\t" \
  "v_fma_f32 v146, |v146|, v216, v146 mul:4\n\t" \
  "v_mfma_f32_16x16x32_bf16 v[52:55], v[212:215], v[194:197], v[52:55]\n\t" \
  "ds_read_b32 v229, v229 offset:0\n\t" \
  "v_fma_f32 v155, |v155|, v219, v155 mul:4\n\t" \
  "v_fma_f32 v154, |v154|, v218, v154 mul:4\n\t" \
  "v_fma_f32 v153, |v153|, v221, v153 mul:4\n\t" \
  "v_mfma_f32_16x16x32_bf16 v[76:79], v[212:215], v[202:205], v[76:79]\n\t" \
  "ds_read_b64_tr_b16 v[212:213], %[a2] offset:72\n\t" \
  "v_fma_f32 v152, |v152|, v220, v152 mul:4\n\t" \
  "v_pk_fma_f16 v232, v232, %[hh], %[hh]\n\t" \
  "s_waitcnt lgkmcnt(4)\n\t" \
  "v_mfma_f32_16x16x32_bf16 v[8:11], v[208:211], v[178:181], v[8:11]\n\t" \
  "ds_read_b64_tr_b16 v[214:215], %[a2] offset:1608\n\t" \
  "v_cvt_pk_bf16_f32 v147, v146, v147\n\t" \
  "v_cvt_pk_bf16_f32 v155, v154, v155\n\t" \
  "v_cvt_pk_bf16_f32 v154, v152, v153\n\t" \
  "v_mfma_f32_16x16x32_bf16 v[32:35], v[208:211], v[186:189], v[32:35]\n\t" \
  "ds_read_b32 v228, v228 offset:0\n\t" \
  "v_rcp_f16 v233, v232\n\t" \
  "v_exp_f16 v234, v235\n\t" \
  "v_cvt_pk_f16_f32 v216, |v148|, |v149| clamp\n\t" \
  "v_mfma_f32_16x16x32_bf16 v[56:59], v[208:211], v[194:197], v[56:59]\n\t" \
  "v_cvt_pk_f16_f32 v218, |v150|, |v151| clamp\n\t" \
  "v_cvt_pk_f16_f32 v220, |v156|, |v157| clamp\n\t" \
  "v_mfma_f32_16x16x32_bf16 v[80:83], v[208:211], v[202:205], v[80:83]\n\t" \
  "ds_read_b64_tr_b16 v[208:209], %[a2] offset:128\n\t" \
  "v_cvt_pk_f16_f32 v222, |v158|, |v159| clamp\n\t" \
  "s_setprio 2\n\t" \
  "v_rcp_f16_sdwa v232, v232 dst_sel:DWORD dst_unused:UNUSED_PAD src0_sel:WORD_1\n\t" \
  "v_exp_f16_sdwa v234, v235 dst_sel:WORD_1 dst_unused:UNUSED_PRESERVE src0_sel:WORD_1\n\t" \
  "s_waitcnt lgkmcnt(2)\n\t" \
  "v_mfma_f32_16x16x32_bf16 v[12:15], v[212:215], v[178:181], v[12:15]\n\t" \
  "ds_read_b64_tr_b16 v[210:211], %[a2] offset:1664\n\t" \
  "v_and_b32_sdwa v217, %[mk], v216 dst_sel:DWORD dst_unused:UNUSED_PAD src0_sel:DWORD src1_sel:WORD_1\n\t" \
  "v_and_b32 v216, %[mk], v216\n\t" \
  "v_mfma_f32_16x16x32_bf16 v[36:39], v[212:215], v[186:189], v[36:39]\n\t" \
  "ds_read_b32 v217, v217 offset:0\n\t" \
  "v_and_b32_sdwa v219, %[mk], v218 dst_sel:DWORD dst_unused:UNUSED_PAD src0_sel:DWORD src1_sel:WORD_1\n\t" \
  "v_mfma_f32_16x16x32_bf16 v[60:63], v[212:215], v[194:197], v[60:63]\n\t" \
  "ds_read_b32 v216, v216 offset:0\n\t" \
  "v_and_b32 v218, %[mk], v218\n\t" \
  "v_and_b32_sdwa v221, %[mk], v220 dst_sel:DWORD dst_unused:UNUSED_PAD src0_sel:DWORD src1_sel:WORD_1\n\t" \
  "v_mfma_f32_16x16x32_bf16 v[84:87], v[212:215], v[202:205], v[84:87]\n\t" \
  "ds_read_b64_tr_b16 v[212:213], %[a2] offset:136\n\t" \
  "v_and_b32 v220, %[mk], v220\n\t" \
  "s_waitcnt lgkmcnt(3)\n\t" \
  "v_mfma_f32_16x16x32_bf16 v[16:19], v[208:211], v[178:181], v[16:19]\n\t" \
  "ds_read_b32 v246, v240 offset:33880\n\t" \
  "ds_read_b64_tr_b16 v[214:215], %[a2] offset:1672\n\t" \
  "ds_read_b32 v219, v219 offset:0\n\t" \
  "v_and_b32_sdwa v223, %[mk], v222 dst_sel:DWORD dst_unused:UNUSED_PAD src0_sel:DWORD src1_sel:WORD_1\n\t" \
  "v_mfma_f32_16x16x32_bf16 v[40:43], v[208:211], v[186:189], v[40:43]\n\t" \
  "ds_read_b32 v218, v218 offset:0\n\t" \
  "v_and_b32 v222, %[mk], v222\n\t" \
  "v_fma_f32 v163, |v163|, v225, v163 mul:4\n\t" \
  "v_mfma_f32_16x16x32_bf16 v[64:67], v[208:211], v[194:197], v[64:67]\n\t" \
  "ds_read_b32 v221, v221 offset:0\n\t" \
  "v_fma_f32 v162, |v162|, v224, v162 mul:4\n\t" \
  "v_fma_f32 v171, |v171|, v227, v171 mul:4\n\t" \
  "v_mfma_f32_16x16x32_bf16 v[88:91], v[208:211], v[202:205], v[88:91]\n\t" \
  "s_waitcnt lgkmcnt(4)\n\t" \
  "v_cmp_gt_u32 vcc, %[tg], v246\n\t" \
  "s_cbranch_vccnz .Lcnx_w10\n.Lcnx_b10:\n\t" \
  "ds_read_b64_tr_b16 v[208:209], %[a1] offset:0\n\t" \
  "ds_read_b64_tr_b16 v[210:211], %[a1] offset:1024\n\t" \
  "v_fma_f32 v170, |v170|, v226, v170 mul:4\n\t" \
  "v_fma_f32 v169, |v169|, v229, v169 mul:4\n\t" \
  "s_waitcnt lgkmcnt(5)\n\t" \
  "v_mfma_f32_16x16x32_bf16 v[20:23], v[212:215], v[178:181], v[20:23]\n\t" \
  "s_waitcnt vmcnt(1)\n\t" \
  "s_mov_b64 exec, 1\n\t" \
  "ds_inc_u32 v240, %[h2] offset:33884\n\t" \
  "s_mov_b64 exec, -1\n\t" \
  "s_cmp_lg_u32 %[wq], 0\n\t" \
  "s_cbranch_scc1 .Lcnx_c28\n\t" \
  "s_add_u32 m0, %[wl], %[ws]\n\t" \
  "s_nop 0\n\t" \
  "buffer_load_dwordx4 %[wv], %[wrs], %[so0] offen lds\n\t" \
  "buffer_load_dwordx4 %[wv], %[wrs], %[so1] offen offset:1024 lds\n\t" \
  "buffer_load_dwordx4 %[wv], %[wrs], %[so2] offen offset:2048 lds\n\t" \
  "s_add_u32 %[so0], %[so0], %[wc]\n\t" \
  "s_add_u32 %[so1], %[so1], %[wc]\n\t" \
  "s_add_u32 %[so2], %[so2], %[wc]\n\t" \
  ".Lcnx_c28:\n\t" \
  "s_cmp_lg_u32 %[rnd], 0\n\t" \
  "s_cbranch_scc1 .Lcnx_c29\n\t" \
  "s_add_u32 m0, %[xl], 32800\n\t" \
  "s_add_u32 %[xo], %[xo], %[rs]\n\t" \
  "buffer_load_dwordx4 %[xv], %[xrs], %[xo] offen lds\n\t" \
  "s_cmp_lg_u32 %[w512], 2560\n\t" \
  "s_cbranch_scc1 .Lcnx_c29\n\t" \
  "s_mov_b32 m0, %[pf]\n\t" \
  "s_add_u32 %[t0], %[xo], %[wb]\n\t" \
  "buffer_load_dword %[wv], %[xrs], %[t0] offen lds\n\t" \
  ".Lcnx_c29:\n\t" \
  "ds_read_b32 v220, v220 offset:0\n\t" \
  "v_fma_f32 v168, |v168|, v228, v168 mul:4\n\t" \
  "v_fma_mix_f32 v144, v144, v233, 0 op_sel_hi:[0,1,0]\n\t" \
  "v_fma_mix_f32 v145, v145, v232, 0 op_sel_hi:[0,1,0]\n\t" \
  "v_mfma_f32_16x16x32_bf16 v[44:47], v[212:215], v[186:189], v[44:47]\n\t" \
  "ds_read_b32 v223, v223 offset:0\n\t" \
  "v_pk_fma_f16 v234, v234, %[hh], %[hh]\n\t" \
  "v_cvt_pk_bf16_f32 v146, v144, v145\n\t" \
  "v_cvt_pk_bf16_f32 v163, v162, v163\n\t" \
  "v_mfma_f32_16x16x32_bf16 v[68:71], v[212:215], v[194:197], v[68:71]\n\t" \
  "ds_read_b32 v222, v222 offset:0\n\t" \
  "v_cvt_pk_bf16_f32 v171, v170, v171\n\t" \
  "v_cvt_pk_bf16_f32 v170, v168, v169\n\t" \
  "v_rcp_f16 v235, v234\n\t" \
  "s_setprio 1\n\t" \
  "v_mfma_f32_16x16x32_bf16 v[92:95], v[212:215], v[202:205], v[92:95]\n\t" \
  "ds_read_b64_tr_b16 v[212:213], %[a1] offset:3072\n\t" \
  "v_cvt_pk_f16_f32 v224, |v164|, |v165| clamp\n\t" \
  "v_cvt_pk_f16_f32 v226, |v166|, |v167| clamp\n\t" \
  "s_waitcnt lgkmcnt(5)\n\t" \
  "v_mfma_f32_16x16x32_bf16 v[176:179], v[208:211], v[96:99], 0\n\t" \
  "ds_read_b64_tr_b16 v[214:215], %[a1] offset:4096\n\t" \
  "v_cvt_pk_f16_f32 v228, |v172|, |v173| clamp\n\t" \
  "v_cvt_pk_f16_f32 v230, |v174|, |v175| clamp\n\t" \
  "v_rcp_f16_sdwa v234, v234 dst_sel:DWORD dst_unused:UNUSED_PAD src0_sel:WORD_1\n\t" \
  "v_mfma_f32_16x16x32_bf16 v[184:187], v[208:211], v[108:111], 0\n\t" \
  "v_and_b32_sdwa v225, %[mk], v224 dst_sel:DWORD dst_unused:UNUSED_PAD src0_sel:DWORD src1_sel:WORD_1\n\t" \
  "v_and_b32 v224, %[mk], v224\n\t" \
  "v_mfma_f32_16x16x32_bf16 v[192:195], v[208:211], v[120:123], 0\n\t" \
  "ds_read_b32 v225, v225 offset:0\n\t" \
  "v_and_b32_sdwa v227, %[mk], v226 dst_sel:DWORD dst_unused:UNUSED_PAD src0_sel:DWORD src1_sel:WORD_1\n\t" \
  "v_mfma_f32_16x16x32_bf16 v[200:203], v[208:211], v[132:135], 0\n\t" \
  "ds_read_b64_tr_b16 v[208:209], %[a1] offset:2048\n\t" \
  "v_and_b32 v226, %[mk], v226\n\t" \
  "v_and_b32_sdwa v229, %[mk], v228 dst_sel:DWORD dst_unused:UNUSED_PAD src0_sel:DWORD src1_sel:WORD_1\n\t" \
  "s_waitcnt lgkmcnt(2)\n\t" \
  "v_mfma_f32_16x16x32_bf16 v[176:179], v[212:215], v[100:103], v[176:179]\n\t" \
  "ds_read_b64_tr_b16 v[210:211], %[a1] offset:5120\n\t" \
  "v_and_b32 v228, %[mk], v228\n\t" \
  "v_mfma_f32_16x16x32_bf16 v[184:187], v[212:215], v[112:115], v[184:187]\n\t" \
  "ds_read_b32 v224, v224 offset:0\n\t" \
  "ds_read_b32 v227, v227 offset:0\n\t" \
  "v_and_b32_sdwa v231, %[mk], v230 dst_sel:DWORD dst_unused:UNUSED_PAD src0_sel:DWORD src1_sel:WORD_1\n\t" \
  "v_mfma_f32_16x16x32_bf16 v[192:195], v[212:215], v[124:127], v[192:195]\n\t" \
  "ds_read_b32 v226, v226 offset:0\n\t" \
  "v_and_b32 v230, %[mk], v230\n\t" \
  "v_fma_f32 v149, |v149|, v217, v149 mul:4\n\t" \
  "v_mfma_f32_16x16x32_bf16 v[200:203], v[212:215], v[136:139], v[200:203]\n\t" \
  "ds_read_b64_tr_b16 v[212:213], %[a1] offset:512\n\t" \
  "ds_read_b64_tr_b16 v[214:215], %[a1] offset:1536\n\t" \
  "v_fma_f32 v148, |v148|, v216, v148 mul:4\n\t" \
  "v_fma_f32 v151, |v151|, v219, v151 mul:4\n\t" \
  "s_waitcnt lgkmcnt(5)\n\t" \
  "v_mfma_f32_16x16x32_bf16 v[176:179], v[208:211], v[104:107], v[176:179]\n\t" \
  "ds_read_b32 v229, v229 offset:0\n\t" \
  "ds_read_b32 v228, v228 offset:0\n\t" \
  "v_fma_f32 v150, |v150|, v218, v150 mul:4\n\t" \
  "v_fma_f32 v157, |v157|, v221, v157 mul:4\n\t" \
  "v_mfma_f32_16x16x32_bf16 v[184:187], v[208:211], v[116:119], v[184:187]\n\t" \
  "ds_read_b32 v231, v231 offset:0\n\t" \
  "v_fma_f32 v156, |v156|, v220, v156 mul:4\n\t" \
  "v_fma_f32 v159, |v159|, v223, v159 mul:4\n\t" \
  "v_fma_f32 v158, |v158|, v222, v158 mul:4\n\t" \
  "v_mfma_f32_16x16x32_bf16 v[192:195], v[208:211], v[128:131], v[192:195]\n\t" \
  "ds_read_b32 v230, v230 offset:0\n\t" \
  "v_fma_mix_f32 v160, v160, v235, 0 op_sel_hi:[0,1,0]\n\t" \
  "v_fma_mix_f32 v161, v161, v234, 0 op_sel_hi:[0,1,0]\n\t" \
  "v_cvt_pk_bf16_f32 v162, v160, v161\n\t" \
  "v_mfma_f32_16x16x32_bf16 v[200:203], v[208:211], v[140:143], v[200:203]\n\t" \
  "ds_read_b64_tr_b16 v[208:209], %[a1] offset:3584\n\t" \
  "v_cvt_pk_bf16_f32 v148, v148, v149\n\t" \
  "v_cvt_pk_bf16_f32 v149, v150, v151\n\t" \
  "s_waitcnt lgkmcnt(5)\n\t" \
  "v_mfma_f32_16x16x32_bf16 v[180:183], v[212:215], v[96:99], 0\n\t" \
  "ds_read_b64_tr_b16 v[210:211], %[a1] offset:4608\n\t" \
  "s_setprio 0\n\t" \
  "v_cvt_pk_bf16_f32 v156, v156, v157\n\t" \
  "v_cvt_pk_bf16_f32 v157, v158, v159\n\t" \
  "v_cvt_pk_f16_f32 v216, |v178|, |v179| clamp\n\t" \
  "v_mfma_f32_16x16x32_bf16 v[188:191], v[212:215], v[108:111], 0\n\t" \
  "v_cvt_pk_f16_f32 v232, v176, v177\n\t" \
  "v_cvt_pk_f16_f32 v218, |v186|, |v187| clamp\n\t" \
  "v_cvt_pk_f16_f32 v220, |v184|, |v185| clamp\n\t" \
  "v_mfma_f32_16x16x32_bf16 v[196:199], v[212:215], v[120:123], 0\n\t" \
  "v_pk_mul_f16 v233, v232, v232\n\t" \
  "v_and_b32_sdwa v217, %[mk], v216 dst_sel:DWORD dst_unused:UNUSED_PAD src0_sel:DWORD src1_sel:WORD_1\n\t" \
  "v_mfma_f32_16x16x32_bf16 v[204:207], v[212:215], v[132:135], 0\n\t" \
  "ds_read_b64_tr_b16 v[212:213], %[a1] offset:2560\n\t" \
  "v_and_b32 v216, %[mk], v216\n\t" \
  "v_and_b32_sdwa v219, %[mk], v218 dst_sel:DWORD dst_unused:UNUSED_PAD src0_sel:DWORD src1_sel:WORD_1\n\t" \
  "s_waitcnt lgkmcnt(1)\n\t" \
  "v_mfma_f32_16x16x32_bf16 v[180:183], v[208:211], v[100:103], v[180:183]\n\t" \
  "ds_read_b32 v246, v240 offset:33884\n\t" \
  "ds_read_b64_tr_b16 v[214:215], %[a1] offset:5632\n\t" \
  "v_and_b32 v218, %[mk], v218\n\t" \
  "v_mfma_f32_16x16x32_bf16 v[188:191], v[208:211], v[112:115], v[188:191]\n\t" \
  "ds_read_b32 v217, v217 offset:0\n\t" \
  "v_and_b32_sdwa v221, %[mk], v220 dst_sel:DWORD dst_unused:UNUSED_PAD src0_sel:DWORD src1_sel:WORD_1\n\t" \
  "v_mfma_f32_16x16x32_bf16 v[196:199], v[208:211], v[124:127], v[196:199]\n\t" \
  "ds_read_b32 v216, v216 offset:0\n\t" \
  "v_and_b32 v220, %[mk], v220\n\t" \
  "v_pk_fma_f16 v233, v233, %[h2], %[h1]\n\t" \
  "v_mfma_f32_16x16x32_bf16 v[204:207], v[208:211], v[136:139], v[204:207]\n\t" \
  "s_waitcnt lgkmcnt(3)\n\t" \
  "v_cmp_gt_u32 vcc, %[tg], v246\n\t" \
  "s_cbranch_vccnz .Lcnx_w11\n.Lcnx_b11:\n\t" \
  "ds_read_b64_tr_b16 v[208:209], %[a2] offset:6160\n\t" \
  "ds_read_b64_tr_b16 v[210:211], %[a2] offset:7696\n\t" \
  "v_pk_mul_f16 v233, v232, v233\n\t" \
  "v_fma_f32 v165, |v165|, v225, v165 mul:4\n\t" \
  "s_waitcnt lgkmcnt(4)\n\t" \
  "v_mfma_f32_16x16x32_bf16 v[180:183], v[212:215], v[104:107], v[180:183]\n\t" \
  "s_waitcnt vmcnt(1)\n\t" \
  "s_cmp_lg_u32 %[rnd], 1\n\t" \
  "s_cbranch_scc1 .Lcnx_c31\n\t" \
  "s_waitcnt vmcnt(0)\n\t" \
  ".Lcnx_c31:\n\t" \
  "s_mov_b64 exec, 1\n\t" \
  "ds_inc_u32 v240, %[h2] offset:33888\n\t" \
  "s_mov_b64 exec, -1\n\t" \
  "s_cmp_lg_u32 %[wq], 1\n\t" \
  "s_cbranch_scc1 .Lcnx_c32\n\t" \
  "s_add_u32 m0, %[wl], 0\n\t" \
  "s_nop 0\n\t" \
  "buffer_load_dwordx4 %[wv], %[wrs], %[so0] offen lds\n\t" \
  "buffer_load_dwordx4 %[wv], %[wrs], %[so1] offen offset:1024 lds\n\t" \
  "buffer_load_dwordx4 %[wv], %[wrs], %[so2] offen offset:2048 lds\n\t" \
  "s_add_u32 %[so0], %[so0], %[wc]\n\t" \
  "s_add_u32 %[so1], %[so1], %[wc]\n\t" \
  "s_add_u32 %[so2], %[so2], %[wc]\n\t" \
  ".Lcnx_c32:\n\t" \
  "ds_read_b32 v219, v219 offset:0\n\t" \
  "ds_read_b32 v218, v218 offset:0\n\t" \
  "v_fma_f32 v164, |v164|, v224, v164 mul:4\n\t" \
  "v_fma_f32 v167, |v167|, v227, v167 mul:4\n\t" \
  "v_mfma_f32_16x16x32_bf16 v[188:191], v[212:215], v[116:119], v[188:191]\n\t" \
  "ds_read_b32 v221, v221 offset:0\n\t" \
  "v_fma_f32 v166, |v166|, v226, v166 mul:4\n\t" \
  "v_fma_f32 v173, |v173|, v229, v173 mul:4\n\t" \
  "v_fma_f32 v172, |v172|, v228, v172 mul:4\n\t" \
  "v_mfma_f32_16x16x32_bf16 v[196:199], v[212:215], v[128:131], v[196:199]\n\t" \
  "ds_read_b32 v220, v220 offset:0\n\t" \
  "v_fma_f32 v175, |v175|, v231, v175 mul:4\n\t" \
  "v_fma_f32 v174, |v174|, v230, v174 mul:4\n\t" \
  "v_cvt_pk_bf16_f32 v164, v164, v165\n\t" \
  "v_mfma_f32_16x16x32_bf16 v[204:207], v[212:215], v[140:143], v[204:207]\n\t" \
  "ds_read_b64_tr_b16 v[212:213], %[a2] offset:6168\n\t" \
  "v_cvt_pk_bf16_f32 v165, v166, v167\n\t" \
  "v_cvt_pk_bf16_f32 v172, v172, v173\n\t" \
  "v_cvt_pk_bf16_f32 v173, v174, v175\n\t" \
  "s_setprio 2\n\t" \
  "v_exp_f16 v232, v233\n\t" \
  "v_cvt_pk_f16_f32 v224, |v194|, |v195| clamp\n\t" \
  "v_mfma_f32_16x16x32_bf16 v[32:35], v[242:245], v[236:239], v[32:35]\n\t" \
  "ds_read_b64_tr_b16 v[214:215], %[a2] offset:7704\n\t" \
  "v_cvt_pk_f16_f32 v234, v192, v193\n\t" \
  "v_cvt_pk_f16_f32 v226, |v202|, |v203| clamp\n\t" \
  "v_mfma_f32_16x16x32_bf16 v[36:39], v[240:243], v[236:239], v[36:39]\n\t" \
  "buffer_load_dwordx4 v[236:239], %[vo], %[xrs], %[oc0] offen offset:320\n\t" \
  "v_cvt_pk_f16_f32 v228, |v200|, |v201| clamp\n\t" \
  "v_exp_f16_sdwa v232, v233 dst_sel:WORD_1 dst_unused:UNUSED_PRESERVE src0_sel:WORD_1\n\t" \
  "v_pk_mul_f16 v235, v234, v234\n\t" \
  "s_waitcnt lgkmcnt(7)\n\t" \
  "v_mfma_f32_16x16x32_bf16 v[0:3], v[208:211], v[146:149], v[0:3]\n\t" \
  "v_and_b32_sdwa v225, %[mk], v224 dst_sel:DWORD dst_unused:UNUSED_PAD src0_sel:DWORD src1_sel:WORD_1\n\t" \
  "v_and_b32 v224, %[mk], v224\n\t" \
  "v_mfma_f32_16x16x32_bf16 v[24:27], v[208:211], v[154:157], v[24:27]\n\t" \
  "ds_read_b32 v225, v225 offset:0\n\t" \
  "v_and_b32_sdwa v227, %[mk], v226 dst_sel:DWORD dst_unused:UNUSED_PAD src0_sel:DWORD src1_sel:WORD_1\n\t" \
  "v_mfma_f32_16x16x32_bf16 v[48:51], v[208:211], v[162:165], v[48:51]\n\t" \
  "ds_read_b32 v224, v224 offset:0\n\t" \
  "v_and_b32 v226, %[mk], v226\n\t" \
  "v_and_b32_sdwa v229, %[mk], v228 dst_sel:DWORD dst_unused:UNUSED_PAD src0_sel:DWORD src1_sel:WORD_1\n\t" \
  "v_mfma_f32_16x16x32_bf16 v[72:75], v[208:211], v[170:173], v[72:75]\n\t" \
  "ds_read_b64_tr_b16 v[208:209], %[a2] offset:6224\n\t" \
  "v_and_b32 v228, %[mk], v228\n\t" \
  "s_waitcnt lgkmcnt(3)\n\t" \
  "v_mfma_f32_16x16x32_bf16 v[4:7], v[212:215], v[146:149], v[4:7]\n\t" \
  "ds_read_b64_tr_b16 v[210:211], %[a2] offset:7760\n\t" \
  "ds_read_b32 v227, v227 offset:0\n\t" \
  "v_pk_fma_f16 v235, v235, %[h2], %[h1]\n\t" \
  "v_mfma_f32_16x16x32_bf16 v[28:31], v[212:215], v[154:157], v[28:31]\n\t" \
  "ds_read_b32 v226, v226 offset:0\n\t" \
  "v_pk_mul_f16 v235, v234, v235\n\t" \
  "v_fma_f32 v179, |v179|, v217, v179 mul:4\n\t" \
  "v_fma_f32 v178, |v178|, v216, v178 mul:4\n\t" \
  "v_mfma_f32_16x16x32_bf16 v[52:55], v[212:215], v[162:165], v[52:55]\n\t" \
  "ds_read_b32 v229, v229 offset:0\n\t" \
  "v_fma_f32 v187, |v187|, v219, v187 mul:4\n\t" \
  "v_fma_f32 v186, |v186|, v218, v186 mul:4\n\t" \
  "v_fma_f32 v185, |v185|, v221, v185 mul:4\n\t" \
  "v_mfma_f32_16x16x32_bf16 v[76:79], v[212:215], v[170:173], v[76:79]\n\t" \
  "ds_read_b64_tr_b16 v[212:213], %[a2] offset:6232\n\t" \
  "v_fma_f32 v184, |v184|, v220, v184 mul:4\n\t" \
  "v_pk_fma_f16 v232, v232, %[hh], %[hh]\n\t" \
  "s_waitcnt lgkmcnt(4)\n\t" \
  "v_mfma_f32_16x16x32_bf16 v[8:11], v[208:211], v[146:149], v[8:11]\n\t" \
  "ds_read_b64_tr_b16 v[214:215], %[a2] offset:7768\n\t" \
  "v_cvt_pk_bf16_f32 v179, v178, v179\n\t" \
  "v_cvt_pk_bf16_f32 v187, v186, v187\n\t" \
  "v_cvt_pk_bf16_f32 v186, v184, v185\n\t" \
  "v_mfma_f32_16x16x32_bf16 v[32:35], v[208:211], v[154:157], v[32:35]\n\t" \
  "ds_read_b32 v228, v228 offset:0\n\t" \
  "v_rcp_f16 v233, v232\n\t" \
  "v_exp_f16 v234, v235\n\t" \
  "v_cvt_pk_f16_f32 v216, |v180|, |v181| clamp\n\t" \
  "v_mfma_f32_16x16x32_bf16 v[56:59], v[208:211], v[162:165], v[56:59]\n\t" \
  "v_cvt_pk_f16_f32 v218, |v182|, |v183| clamp\n\t" \
  "v_cvt_pk_f16_f32 v220, |v188|, |v189| clamp\n\t" \
  "v_mfma_f32_16x16x32_bf16 v[80:83], v[208:211], v[170:173], v[80:83]\n\t" \
  "ds_read_b64_tr_b16 v[208:209], %[a2] offset:6288\n\t" \
  "v_cvt_pk_f16_f32 v222, |v190|, |v191| clamp\n\t" \
  "s_setprio 2\n\t" \
  "v_rcp_f16_sdwa v232, v232 dst_sel:DWORD dst_unused:UNUSED_PAD src0_sel:WORD_1\n\t" \
  "v_exp_f16_sdwa v234, v235 dst_sel:WORD_1 dst_unused:UNUSED_PRESERVE src0_sel:WORD_1\n\t" \
  "s_waitcnt lgkmcnt(2)\n\t" \
  "v_mfma_f32_16x16x32_bf16 v[12:15], v[212:215], v[146:149], v[12:15]\n\t" \
  "ds_read_b64_tr_b16 v[210:211], %[a2] offset:7824\n\t" \
  "v_and_b32_sdwa v217, %[mk], v216 dst_sel:DWORD dst_unused:UNUSED_PAD src0_sel:DWORD src1_sel:WORD_1\n\t" \
  "v_and_b32 v216, %[mk], v216\n\t" \
  "v_mfma_f32_16x16x32_bf16 v[36:39], v[212:215], v[154:157], v[36:39]\n\t" \
  "ds_read_b32 v217, v217 offset:0\n\t" \
  "v_and_b32_sdwa v219, %[mk], v218 dst_sel:DWORD dst_unused:UNUSED_PAD src0_sel:DWORD src1_sel:WORD_1\n\t" \
  "v_mfma_f32_16x16x32_bf16 v[60:63], v[212:215], v[162:165], v[60:63]\n\t" \
  "ds_read_b32 v216, v216 offset:0\n\t" \
  "v_and_b32 v218, %[mk], v218\n\t" \
  "v_and_b32_sdwa v221, %[mk], v220 dst_sel:DWORD dst_unused:UNUSED_PAD src0_sel:DWORD src1_sel:WORD_1\n\t" \
  "v_mfma_f32_16x16x32_bf16 v[84:87], v[212:215], v[170:173], v[84:87]\n\t" \
  "ds_read_b64_tr_b16 v[212:213], %[a2] offset:6296\n\t" \
  "v_and_b32 v220, %[mk], v220\n\t" \
  "s_waitcnt lgkmcnt(3)\n\t" \
  "v_mfma_f32_16x16x32_bf16 v[16:19], v[208:211], v[146:149], v[16:19]\n\t" \
  "ds_read_b32 v246, v240 offset:33888\n\t" \
  "ds_read_b64_tr_b16 v[214:215], %[a2] offset:7832\n\t" \
  "ds_read_b32 v219, v219 offset:0\n\t" \
  "v_and_b32_sdwa v223, %[mk], v222 dst_sel:DWORD dst_unused:UNUSED_PAD src0_sel:DWORD src1_sel:WORD_1\n\t" \
  "v_mfma_f32_16x16x32_bf16 v[40:43], v[208:211], v[154:157], v[40:43]\n\t" \
  "ds_read_b32 v218, v218 offset:0\n\t" \
  "v_and_b32 v222, %[mk], v222\n\t" \
  "v_fma_f32 v195, |v195|, v225, v195 mul:4\n\t" \
  "v_mfma_f32_16x16x32_bf16 v[64:67], v[208:211], v[162:165], v[64:67]\n\t" \
  "ds_read_b32 v221, v221 offset:0\n\t" \
  "v_fma_f32 v194, |v194|, v224, v194 mul:4\n\t" \
  "v_fma_f32 v203, |v203|, v227, v203 mul:4\n\t" \
  "v_mfma_f32_16x16x32_bf16 v[88:91], v[208:211], v[170:173], v[88:91]\n\t" \
  "s_waitcnt lgkmcnt(4)\n\t" \
  "v_cmp_gt_u32 vcc, %[tg], v246\n\t" \
  "s_cbranch_vccnz .Lcnx_w12\n.Lcnx_b12:\n\t" \
  "ds_read_b64_tr_b16 v[208:209], %[a1] offset:6144\n\t" \
  "ds_read_b64_tr_b16 v[210:211], %[a1] offset:7168\n\t" \
  "v_fma_f32 v202, |v202|, v226, v202 mul:4\n\t" \
  "v_fma_f32 v201, |v201|, v229, v201 mul:4\n\t" \
  "s_waitcnt lgkmcnt(5)\n\t" \
  "v_mfma_f32_16x16x32_bf16 v[20:23], v[212:215], v[146:149], v[20:23]\n\t" \
  "s_waitcnt vmcnt(1)\n\t" \
  "s_mov_b64 exec, 1\n\t" \
  "ds_inc_u32 v240, %[h2] offset:33892\n\t" \
  "s_mov_b64 exec, -1\n\t" \
  "s_cmp_lg_u32 %[wq], 0\n\t" \
  "s_cbranch_scc1 .Lcnx_c33\n\t" \
  "s_add_u32 m0, %[wl], 0\n\t" \
  "s_nop 0\n\t" \
  "buffer_load_dwordx4 %[wv], %[wrs], %[so0] offen lds\n\t" \
  "buffer_load_dwordx4 %[wv], %[wrs], %[so1] offen offset:1024 lds\n\t" \
  "buffer_load_dwordx4 %[wv], %[wrs], %[so2] offen offset:2048 lds\n\t" \
  "s_add_u32 %[so0], %[so0], %[wc]\n\t" \
  "s_add_u32 %[so1], %[so1], %[wc]\n\t" \
  "s_add_u32 %[so2], %[so2], %[wc]\n\t" \
  ".Lcnx_c33:\n\t" \
  "s_cmp_lg_u32 %[rnd], 0\n\t" \
  "s_cbranch_scc1 .Lcnx_c34\n\t" \
  "s_add_u32 m0, %[xl], 41000\n\t" \
  "s_add_u32 %[xo], %[xo], %[rs]\n\t" \
  "buffer_load_dwordx4 %[xv], %[xrs], %[xo] offen lds\n\t" \
  "s_cmp_lg_u32 %[w512], 3072\n\t" \
  "s_cbranch_scc1 .Lcnx_c34\n\t" \
  "s_mov_b32 m0, %[pf]\n\t" \
  "s_add_u32 %[t0], %[xo], %[wb]\n\t" \
  "buffer_load_dword %[wv], %[xrs], %[t0] offen lds\n\t" \
  ".Lcnx_c34:\n\t" \
  "ds_read_b32 v220, v220 offset:0\n\t" \
  "v_fma_f32 v200, |v200|, v228, v200 mul:4\n\t" \
  "v_fma_mix_f32 v176, v176, v233, 0 op_sel_hi:[0,1,0]\n\t" \
  "v_fma_mix_f32 v177, v177, v232, 0 op_sel_hi:[0,1,0]\n\t" \
  "v_mfma_f32_16x16x32_bf16 v[44:47], v[212:215], v[154:157], v[44:47]\n\t" \
  "ds_read_b32 v223, v223 offset:0\n\t" \
  "v_pk_fma_f16 v234, v234, %[hh], %[hh]\n\t" \
  "v_cvt_pk_bf16_f32 v178, v176, v177\n\t" \
  "v_cvt_pk_bf16_f32 v195, v194, v195\n\t" \
  "v_mfma_f32_16x16x32_bf16 v[68:71], v[212:215], v[162:165], v[68:71]\n\t" \
  "ds_read_b32 v222, v222 offset:0\n\t" \
  "v_cvt_pk_bf16_f32 v203, v202, v203\n\t" \
  "v_cvt_pk_bf16_f32 v202, v200, v201\n\t" \
  "v_rcp_f16 v235, v234\n\t" \
  "s_setprio 1\n\t" \
  "v_mfma_f32_16x16x32_bf16 v[92:95], v[212:215], v[170:173], v[92:95]\n\t" \
  "ds_read_b64_tr_b16 v[212:213], %[a1] offset:9216\n\t" \
  "v_cvt_pk_f16_f32 v224, |v196|, |v197| clamp\n\t" \
  "v_cvt_pk_f16_f32 v226, |v198|, |v199| clamp\n\t" \
  "s_waitcnt lgkmcnt(5)\n\t" \
  "v_mfma_f32_16x16x32_bf16 v[144:147], v[208:211], v[96:99], 0\n\t" \
  "ds_read_b64_tr_b16 v[214:215], %[a1] offset:10240\n\t" \
  "v_cvt_pk_f16_f32 v228, |v204|, |v205| clamp\n\t" \
  "v_cvt_pk_f16_f32 v230, |v206|, |v207| clamp\n\t" \
  "v_rcp_f16_sdwa v234, v234 dst_sel:DWORD dst_unused:UNUSED_PAD src0_sel:WORD_1\n\t" \
  "v_mfma_f32_16x16x32_bf16 v[152:155], v[208:211], v[108:111], 0\n\t" \
  "v_and_b32_sdwa v225, %[mk], v224 dst_sel:DWORD dst_unused:UNUSED_PAD src0_sel:DWORD src1_sel:WORD_1\n\t" \
  "v_and_b32 v224, %[mk], v224\n\t" \
  "v_mfma_f32_16x16x32_bf16 v[160:163], v[208:211], v[120:123], 0\n\t" \
  "ds_read_b32 v225, v225 offset:0\n\t" \
  "v_and_b32_sdwa v227, %[mk], v226 dst_sel:DWORD dst_unused:UNUSED_PAD src0_sel:DWORD src1_sel:WORD_1\n\t" \
  "v_mfma_f32_16x16x32_bf16 v[168:171], v[208:211], v[132:135], 0\n\t" \
  "ds_read_b64_tr_b16 v[208:209], %[a1] offset:8192\n\t" \
  "v_and_b32 v226, %[mk], v226\n\t" \
  "v_and_b32_sdwa v229, %[mk], v228 dst_sel:DWORD dst_unused:UNUSED_PAD src0_sel:DWORD src1_sel:WORD_1\n\t" \
  "s_waitcnt lgkmcnt(2)\n\t" \
  "v_mfma_f32_16x16x32_bf16 v[144:147], v[212:215], v[100:103], v[144:147]\n\t" \
  "ds_read_b64_tr_b16 v[210:211], %[a1] offset:11264\n\t" \
  "v_and_b32 v228, %[mk], v228\n\t" \
  "v_mfma_f32_16x16x32_bf16 v[152:155], v[212:215], v[112:115], v[152:155]\n\t" \
  "ds_read_b32 v224, v224 offset:0\n\t" \
  "ds_read_b32 v227, v227 offset:0\n\t" \
  "v_and_b32_sdwa v231, %[mk], v230 dst_sel:DWORD dst_unused:UNUSED_PAD src0_sel:DWORD src1_sel:WORD_1\n\t" \
  "v_mfma_f32_16x16x32_bf16 v[160:163], v[212:215], v[124:127], v[160:163]\n\t" \
  "ds_read_b32 v226, v226 offset:0\n\t" \
  "v_and_b32 v230, %[mk], v230\n\t" \
  "v_fma_f32 v181, |v181|, v217, v181 mul:4\n\t" \
  "v_mfma_f32_16x16x32_bf16 v[168:171], v[212:215], v[136:139], v[168:171]\n\t" \
  "ds_read_b64_tr_b16 v[212:213], %[a1] offset:6656\n\t" \
  "ds_read_b64_tr_b16 v[214:215], %[a1] offset:7680\n\t" \
  "v_fma_f32 v180, |v180|, v216, v180 mul:4\n\t" \
  "v_fma_f32 v183, |v183|, v219, v183 mul:4\n\t" \
  "s_waitcnt lgkmcnt(5)\n\t" \
  "v_mfma_f32_16x16x32_bf16 v[144:147], v[208:211], v[104:107], v[144:147]\n\t" \
  "ds_read_b32 v229, v229 offset:0\n\t" \
  "ds_read_b32 v228, v228 offset:0\n\t" \
  "v_fma_f32 v182, |v182|, v218, v182 mul:4\n\t" \
  "v_fma_f32 v189, |v189|, v221, v189 mul:4\n\t" \
  "v_mfma_f32_16x16x32_bf16 v[152:155], v[208:211], v[116:119], v[152:155]\n\t" \
  "ds_read_b32 v231, v231 offset:0\n\t" \
  "v_fma_f32 v188, |v188|, v220, v188 mul:4\n\t" \
  "v_fma_f32 v191, |v191|, v223, v191 mul:4\n\t" \
  "v_fma_f32 v190, |v190|, v222, v190 mul:4\n\t" \
  "v_mfma_f32_16x16x32_bf16 v[160:163], v[208:211], v[128:131], v[160:163]\n\t" \
  "ds_read_b32 v230, v230 offset:0\n\t" \
  "v_fma_mix_f32 v192, v192, v235, 0 op_sel_hi:[0,1,0]\n\t" \
  "v_fma_mix_f32 v193, v193, v234, 0 op_sel_hi:[0,1,0]\n\t" \
  "v_cvt_pk_bf16_f32 v194, v192, v193\n\t" \
  "v_mfma_f32_16x16x32_bf16 v[168:171], v[208:211], v[140:143], v[168:171]\n\t" \
  "ds_read_b64_tr_b16 v[208:209], %[a1] offset:9728\n\t" \
  "v_cvt_pk_bf16_f32 v180, v180, v181\n\t" \
  "v_cvt_pk_bf16_f32 v181, v182, v183\n\t" \
  "s_waitcnt lgkmcnt(5)\n\t" \
  "v_mfma_f32_16x16x32_bf16 v[148:151], v[212:215], v[96:99], 0\n\t" \
  "ds_read_b64_tr_b16 v[210:211], %[a1] offset:10752\n\t" \
  "s_setprio 0\n\t" \
  "v_cvt_pk_bf16_f32 v188, v188, v189\n\t" \
  "v_cvt_pk_bf16_f32 v189, v190, v191\n\t" \
  "v_cvt_pk_f16_f32 v216, |v146|, |v147| clamp\n\t" \
  "v_mfma_f32_16x16x32_bf16 v[156:159], v[212:215], v[108:111], 0\n\t" \
  "v_cvt_pk_f16_f32 v232, v144, v145\n\t" \
  "v_cvt_pk_f16_f32 v218, |v154|, |v155| clamp\n\t" \
  "v_cvt_pk_f16_f32 v220, |v152|, |v153| clamp\n\t" \
  "v_mfma_f32_16x16x32_bf16 v[164:167], v[212:215], v[120:123], 0\n\t" \
  "v_pk_mul_f16 v233, v232, v232\n\t" \
  "v_and_b32_sdwa v217, %[mk], v216 dst_sel:DWORD dst_unused:UNUSED_PAD src0_sel:DWORD src1_sel:WORD_1\n\t" \
  "v_mfma_f32_16x16x32_bf16 v[172:175], v[212:215], v[132:135], 0\n\t" \
  "ds_read_b64_tr_b16 v[212:213], %[a1] offset:8704\n\t" \
  "v_and_b32 v216, %[mk], v216\n\t" \
  "v_and_b32_sdwa v219, %[mk], v218 dst_sel:DWORD dst_unused:UNUSED_PAD src0_sel:DWORD src1_sel:WORD_1\n\t" \
  "s_waitcnt lgkmcnt(1)\n\t" \
  "v_mfma_f32_16x16x32_bf16 v[148:151], v[208:211], v[100:103], v[148:151]\n\t" \
  "ds_read_b32 v246, v240 offset:33892\n\t" \
  "ds_read_b64_tr_b16 v[214:215], %[a1] offset:11776\n\t" \
  "v_and_b32 v218, %[mk], v218\n\t" \
  "v_mfma_f32_16x16x32_bf16 v[156:159], v[208:211], v[112:115], v[156:159]\n\t" \
  "ds_read_b32 v217, v217 offset:0\n\t" \
  "v_and_b32_sdwa v221, %[mk], v220 dst_sel:DWORD dst_unused:UNUSED_PAD src0_sel:DWORD src1_sel:WORD_1\n\t" \
  "v_mfma_f32_16x16x32_bf16 v[164:167], v[208:211], v[124:127], v[164:167]\n\t" \
  "ds_read_b32 v216, v216 offset:0\n\t" \
  "v_and_b32 v220, %[mk], v220\n\t" \
  "v_pk_fma_f16 v233, v233, %[h2], %[h1]\n\t" \
  "v_mfma_f32_16x16x32_bf16 v[172:175], v[208:211], v[136:139], v[172:175]\n\t" \
  "s_waitcnt lgkmcnt(3)\n\t" \
  "v_cmp_gt_u32 vcc, %[tg], v246\n\t" \
  "s_cbranch_vccnz .Lcnx_w13\n.Lcnx_b13:\n\t" \
  "ds_read_b64_tr_b16 v[208:209], %[a2] offset:0\n\t" \
  "ds_read_b64_tr_b16 v[210:211], %[a2] offset:1536\n\t" \
  "v_pk_mul_f16 v233, v232, v233\n\t" \
  "v_fma_f32 v197, |v197|, v225, v197 mul:4\n\t" \
  "s_waitcnt lgkmcnt(4)\n\t" \
  "v_mfma_f32_16x16x32_bf16 v[148:151], v[212:215], v[104:107], v[148:151]\n\t" \
  "s_waitcnt vmcnt(1)\n\t" \
  "s_cmp_lg_u32 %[rnd], 1\n\t" \
  "s_cbranch_scc1 .Lcnx_c36\n\t" \
  "s_waitcnt vmcnt(0)\n\t" \
  ".Lcnx_c36:\n\t" \
  "s_mov_b64 exec, 1\n\t" \
  "ds_inc_u32 v240, %[h2] offset:33896\n\t" \
  "s_mov_b64 exec, -1\n\t" \
  "s_cmp_lg_u32 %[wq], 1\n\t" \
  "s_cbranch_scc1 .Lcnx_c37\n\t" \
  "s_add_u32 m0, %[wl], %[ws]\n\t" \
  "s_nop 0\n\t" \
  "buffer_load_dwordx4 %[wv], %[wrs], %[so0] offen lds\n\t" \
  "buffer_load_dwordx4 %[wv], %[wrs], %[so1] offen offset:1024 lds\n\t" \
  "buffer_load_dwordx4 %[wv], %[wrs], %[so2] offen offset:2048 lds\n\t" \
  "s_add_u32 %[so0], %[so0], %[wc]\n\t" \
  "s_add_u32 %[so1], %[so1], %[wc]\n\t" \
  "s_add_u32 %[so2], %[so2], %[wc]\n\t" \
  ".Lcnx_c37:\n\t" \
  "ds_read_b32 v219, v219 offset:0\n\t" \
  "ds_read_b32 v218, v218 offset:0\n\t" \
  "v_fma_f32 v196, |v196|, v224, v196 mul:4\n\t" \
  "v_fma_f32 v199, |v199|, v227, v199 mul:4\n\t" \
  "v_mfma_f32_16x16x32_bf16 v[156:159], v[212:215], v[116:119], v[156:159]\n\t" \
  "ds_read_b32 v221, v221 offset:0\n\t" \
  "v_fma_f32 v198, |v198|, v226, v198 mul:4\n\t" \
  "v_fma_f32 v205, |v205|, v229, v205 mul:4\n\t" \
  "v_fma_f32 v204, |v204|, v228, v204 mul:4\n\t" \
  "v_mfma_f32_16x16x32_bf16 v[164:167], v[212:215], v[128:131], v[164:167]\n\t" \
  "ds_read_b32 v220, v220 offset:0\n\t" \
  "v_fma_f32 v207, |v207|, v231, v207 mul:4\n\t" \
  "v_fma_f32 v206, |v206|, v230, v206 mul:4\n\t" \
  "v_cvt_pk_bf16_f32 v196, v196, v197\n\t" \
  "v_mfma_f32_16x16x32_bf16 v[172:175], v[212:215], v[140:143], v[172:175]\n\t" \
  "ds_read_b64_tr_b16 v[212:213], %[a2] offset:8\n\t" \
  "v_cvt_pk_bf16_f32 v197, v198, v199\n\t" \
  "v_cvt_pk_bf16_f32 v204, v204, v205\n\t" \
  "v_cvt_pk_bf16_f32 v205, v206, v207\n\t" \
  "s_setprio 2\n\t" \
  "v_exp_f16 v232, v233\n\t" \
  "v_cvt_pk_f16_f32 v224, |v162|, |v163| clamp\n\t" \
  "v_mfma_f32_16x16x32_bf16 v[40:43], v[242:245], v[236:239], v[40:43]\n\t" \
  "ds_read_b64_tr_b16 v[214:215], %[a2] offset:1544\n\t" \
  "v_cvt_pk_f16_f32 v234, v160, v161\n\t" \
  "v_cvt_pk_f16_f32 v226, |v170|, |v171| clamp\n\t" \
  "v_mfma_f32_16x16x32_bf16 v[44:47], v[240:243], v[236:239], v[44:47]\n\t" \
  "buffer_load_dwordx4 v[236:239], %[vo], %[xrs], %[oc1] offen offset:0\n\t" \
  "v_cvt_pk_f16_f32 v228, |v168|, |v169| clamp\n\t" \
  "v_exp_f16_sdwa v232, v233 dst_sel:WORD_1 dst_unused:UNUSED_PRESERVE src0_sel:WORD_1\n\t" \
  "v_pk_mul_f16 v235, v234, v234\n\t" \
  "s_waitcnt lgkmcnt(7)\n\t" \
  "v_mfma_f32_16x16x32_bf16 v[0:3], v[208:211], v[178:181], v[0:3]\n\t" \
  "v_and_b32_sdwa v225, %[mk], v224 dst_sel:DWORD dst_unused:UNUSED_PAD src0_sel:DWORD src1_sel:WORD_1\n\t" \
  "v_and_b32 v224, %[mk], v224\n\t" \
  "v_mfma_f32_16x16x32_bf16 v[24:27], v[208:211], v[186:189], v[24:27]\n\t" \
  "ds_read_b32 v225, v225 offset:0\n\t" \
  "v_and_b32_sdwa v227, %[mk], v226 dst_sel:DWORD dst_unused:UNUSED_PAD src0_sel:DWORD src1_sel:WORD_1\n\t" \
  "v_mfma_f32_16x16x32_bf16 v[48:51], v[208:211], v[194:197], v[48:51]\n\t" \
  "ds_read_b32 v224, v224 offset:0\n\t" \
  "v_and_b32 v226, %[mk], v226\n\t" \
  "v_and_b32_sdwa v229, %[mk], v228 dst_sel:DWORD dst_unused:UNUSED_PAD src0_sel:DWORD src1_sel:WORD_1\n\t" \
  "v_mfma_f32_16x16x32_bf16 v[72:75], v[208:211], v[202:205], v[72:75]\n\t" \
  "ds_read_b64_tr_b16 v[208:209], %[a2] offset:64\n\t" \
  "v_and_b32 v228, %[mk], v228\n\t" \
  "s_waitcnt lgkmcnt(3)\n\t" \
  "v_mfma_f32_16x16x32_bf16 v[4:7], v[212:215], v[178:181], v[4:7]\n\t" \
  "ds_read_b64_tr_b16 v[210:211], %[a2] offset:1600\n\t" \
  "ds_read_b32 v227, v227 offset:0\n\t" \
  "v_pk_fma_f16 v235, v235, %[h2], %[h1]\n\t" \
  "v_mfma_f32_16x16x32_bf16 v[28:31], v[212:215], v[186:189], v[28:31]\n\t" \
  "ds_read_b32 v226, v226 offset:0\n\t" \
  "v_pk_mul_f16 v235, v234, v235\n\t" \
  "v_fma_f32 v147, |v147|, v217, v147 mul:4\n\t" \
  "v_fma_f32 v146, |v146|, v216, v146 mul:4\n\t" \
  "v_mfma_f32_16x16x32_bf16 v[52:55], v[212:215], v[194:197], v[52:55]\n\t" \
  "ds_read_b32 v229, v229 offset:0\n\t" \
  "v_fma_f32 v155, |v155|, v219, v155 mul:4\n\t" \
  "v_fma_f32 v154, |v154|, v218, v154 mul:4\n\t" \
  "v_fma_f32 v153, |v153|, v221, v153 mul:4\n\t" \
  "v_mfma_f32_16x16x32_bf16 v[76:79], v[212:215], v[202:205], v[76:79]\n\t" \
  "ds_read_b64_tr_b16 v[212:213], %[a2] offset:72\n\t" \
  "v_fma_f32 v152, |v152|, v220, v152 mul:4\n\t" \
  "v_pk_fma_f16 v232, v232, %[hh], %[hh]\n\t" \
  "s_waitcnt lgkmcnt(4)\n\t" \
  "v_mfma_f32_16x16x32_bf16 v[8:11], v[208:211], v[178:181], v[8:11]\n\t" \
  "ds_read_b64_tr_b16 v[214:215], %[a2] offset:1608\n\t" \
  "v_cvt_pk_bf16_f32 v147, v146, v147\n\t" \
  "v_cvt_pk_bf16_f32 v155, v154, v155\n\t" \
  "v_cvt_pk_bf16_f32 v154, v152, v153\n\t" \
  "v_mfma_f32_16x16x32_bf16 v[32:35], v[208:211], v[186:189], v[32:35]\n\t" \
  "ds_read_b32 v228, v228 offset:0\n\t" \
  "v_rcp_f16 v233, v232\n\t" \
  "v_exp_f16 v234, v235\n\t" \
  "v_cvt_pk_f16_f32 v216, |v148|, |v149| clamp\n\t" \
  "v_mfma_f32_16x16x32_bf16 v[56:59], v[208:211], v[194:197], v[56:59]\n\t" \
  "v_cvt_pk_f16_f32 v218, |v150|, |v151| clamp\n\t" \
  "v_cvt_pk_f16_f32 v220, |v156|, |v157| clamp\n\t" \
  "v_mfma_f32_16x16x32_bf16 v[80:83], v[208:211], v[202:205], v[80:83]\n\t" \
  "ds_read_b64_tr_b16 v[208:209], %[a2] offset:128\n\t" \
  "v_cvt_pk_f16_f32 v222, |v158|, |v159| clamp\n\t" \
  "s_setprio 2\n\t" \
  "v_rcp_f16_sdwa v232, v232 dst_sel:DWORD dst_unused:UNUSED_PAD src0_sel:WORD_1\n\t" \
  "v_exp_f16_sdwa v234, v235 dst_sel:WORD_1 dst_unused:UNUSED_PRESERVE src0_sel:WORD_1\n\t" \
  "s_waitcnt lgkmcnt(2)\n\t" \
  "v_mfma_f32_16x16x32_bf16 v[12:15], v[212:215], v[178:181], v[12:15]\n\t" \
  "ds_read_b64_tr_b16 v[210:211], %[a2] offset:1664\n\t" \
  "v_and_b32_sdwa v217, %[mk], v216 dst_sel:DWORD dst_unused:UNUSED_PAD src0_sel:DWORD src1_sel:WORD_1\n\t" \
  "v_and_b32 v216, %[mk], v216\n\t" \
  "v_mfma_f32_16x16x32_bf16 v[36:39], v[212:215], v[186:189], v[36:39]\n\t" \
  "ds_read_b32 v217, v217 offset:0\n\t" \
  "v_and_b32_sdwa v219, %[mk], v218 dst_sel:DWORD dst_unused:UNUSED_PAD src0_sel:DWORD src1_sel:WORD_1\n\t" \
  "v_mfma_f32_16x16x32_bf16 v[60:63], v[212:215], v[194:197], v[60:63]\n\t" \
  "ds_read_b32 v216, v216 offset:0\n\t" \
  "v_and_b32 v218, %[mk], v218\n\t" \
  "v_and_b32_sdwa v221, %[mk], v220 dst_sel:DWORD dst_unused:UNUSED_PAD src0_sel:DWORD src1_sel:WORD_1\n\t" \
  "v_mfma_f32_16x16x32_bf16 v[84:87], v[212:215], v[202:205], v[84:87]\n\t" \
  "ds_read_b64_tr_b16 v[212:213], %[a2] offset:136\n\t" \
  "v_and_b32 v220, %[mk], v220\n\t" \
  "s_waitcnt lgkmcnt(3)\n\t" \
  "v_mfma_f32_16x16x32_bf16 v[16:19], v[208:211], v[178:181], v[16:19]\n\t" \
  "ds_read_b32 v246, v240 offset:33896\n\t" \
  "ds_read_b64_tr_b16 v[214:215], %[a2] offset:1672\n\t" \
  "ds_read_b32 v219, v219 offset:0\n\t" \
  "v_and_b32_sdwa v223, %[mk], v222 dst_sel:DWORD dst_unused:UNUSED_PAD src0_sel:DWORD src1_sel:WORD_1\n\t" \
  "v_mfma_f32_16x16x32_bf16 v[40:43], v[208:211], v[186:189], v[40:43]\n\t" \
  "ds_read_b32 v218, v218 offset:0\n\t" \
  "v_and_b32 v222, %[mk], v222\n\t" \
  "v_fma_f32 v163, |v163|, v225, v163 mul:4\n\t" \
  "v_mfma_f32_16x16x32_bf16 v[64:67], v[208:211], v[194:197], v[64:67]\n\t" \
  "ds_read_b32 v221, v221 offset:0\n\t" \
  "v_fma_f32 v162, |v162|, v224, v162 mul:4\n\t" \
  "v_fma_f32 v171, |v171|, v227, v171 mul:4\n\t" \
  "v_mfma_f32_16x16x32_bf16 v[88:91], v[208:211], v[202:205], v[88:91]\n\t" \
  "s_waitcnt lgkmcnt(4)\n\t" \
  "v_cmp_gt_u32 vcc, %[tg], v246\n\t" \
  "s_cbranch_vccnz .Lcnx_w14\n.Lcnx_b14:\n\t" \
  "ds_read_b64_tr_b16 v[208:209], %[a1] offset:0\n\t" \
  "ds_read_b64_tr_b16 v[210:211], %[a1] offset:1024\n\t" \
  "v_fma_f32 v170, |v170|, v226, v170 mul:4\n\t" \
  "v_fma_f32 v169, |v169|, v229, v169 mul:4\n\t" \
  "s_waitcnt lgkmcnt(5)\n\t" \
  "v_mfma_f32_16x16x32_bf16 v[20:23], v[212:215], v[178:181], v[20:23]\n\t" \
  "s_waitcnt vmcnt(1)\n\t" \
  "s_mov_b64 exec, 1\n\t" \
  "ds_inc_u32 v240, %[h2] offset:33900\n\t" \
  "s_mov_b64 exec, -1\n\t" \
  "s_cmp_lg_u32 %[wq], 0\n\t" \
  "s_cbranch_scc1 .Lcnx_c38\n\t" \
  "s_add_u32 m0, %[wl], %[ws]\n\t" \
  "s_nop 0\n\t" \
  "buffer_load_dwordx4 %[wv], %[wrs], %[so0] offen lds\n\t" \
  "buffer_load_dwordx4 %[wv], %[wrs], %[so1] offen offset:1024 lds\n\t" \
  "buffer_load_dwordx4 %[wv], %[wrs], %[so2] offen offset:2048 lds\n\t" \
  "s_add_u32 %[so0], %[so0], %[wc]\n\t" \
  "s_add_u32 %[so1], %[so1], %[wc]\n\t" \
  "s_add_u32 %[so2], %[so2], %[wc]\n\t" \
  ".Lcnx_c38:\n\t" \
  "s_cmp_lg_u32 %[rnd], 0\n\t" \
  "s_cbranch_scc1 .Lcnx_c39\n\t" \
  "s_add_u32 m0, %[xl], 49200\n\t" \
  "s_add_u32 %[xo], %[xo], %[rs]\n\t" \
  "buffer_load_dwordx4 %[xv], %[xrs], %[xo] offen lds\n\t" \
  "s_cmp_lg_u32 %[w512], 3584\n\t" \
  "s_cbranch_scc1 .Lcnx_c39\n\t" \
  "s_mov_b32 m0, %[pf]\n\t" \
  "s_add_u32 %[t0], %[xo], %[wb]\n\t" \
  "buffer_load_dword %[wv], %[xrs], %[t0] offen lds\n\t" \
  ".Lcnx_c39:\n\t" \
  "ds_read_b32 v220, v220 offset:0\n\t" \
  "v_fma_f32 v168, |v168|, v228, v168 mul:4\n\t" \
  "v_fma_mix_f32 v144, v144, v233, 0 op_sel_hi:[0,1,0]\n\t" \
  "v_fma_mix_f32 v145, v145, v232, 0 op_sel_hi:[0,1,0]\n\t" \
  "v_mfma_f32_16x16x32_bf16 v[44:47], v[212:215], v[186:189], v[44:47]\n\t" \
  "ds_read_b32 v223, v223 offset:0\n\t" \
  "v_pk_fma_f16 v234, v234, %[hh], %[hh]\n\t" \
  "v_cvt_pk_bf16_f32 v146, v144, v145\n\t" \
  "v_cvt_pk_bf16_f32 v163, v162, v163\n\t" \
  "v_mfma_f32_16x16x32_bf16 v[68:71], v[212:215], v[194:197], v[68:71]\n\t" \
  "ds_read_b32 v222, v222 offset:0\n\t" \
  "v_cvt_pk_bf16_f32 v171, v170, v171\n\t" \
  "v_cvt_pk_bf16_f32 v170, v168, v169\n\t" \
  "v_rcp_f16 v235, v234\n\t" \
  "s_setprio 1\n\t" \
  "v_mfma_f32_16x16x32_bf16 v[92:95], v[212:215], v[202:205], v[92:95]\n\t" \
  "ds_read_b64_tr_b16 v[212:213], %[a1] offset:3072\n\t" \
  "v_cvt_pk_f16_f32 v224, |v164|, |v165| clamp\n\t" \
  "v_cvt_pk_f16_f32 v226, |v166|, |v167| clamp\n\t" \
  "s_waitcnt lgkmcnt(5)\n\t" \
  "v_mfma_f32_16x16x32_bf16 v[176:179], v[208:211], v[96:99], 0\n\t" \
  "ds_read_b64_tr_b16 v[214:215], %[a1] offset:4096\n\t" \
  "v_cvt_pk_f16_f32 v228, |v172|, |v173| clamp\n\t" \
  "v_cvt_pk_f16_f32 v230, |v174|, |v175| clamp\n\t" \
  "v_rcp_f16_sdwa v234, v234 dst_sel:DWORD dst_unused:UNUSED_PAD src0_sel:WORD_1\n\t" \
  "v_mfma_f32_16x16x32_bf16 v[184:187], v[208:211], v[108:111], 0\n\t" \
  "v_and_b32_sdwa v225, %[mk], v224 dst_sel:DWORD dst_unused:UNUSED_PAD src0_sel:DWORD src1_sel:WORD_1\n\t" \
  "v_and_b32 v224, %[mk], v224\n\t" \
  "v_mfma_f32_16x16x32_bf16 v[192:195], v[208:211], v[120:123], 0\n\t" \
  "ds_read_b32 v225, v225 offset:0\n\t" \
  "v_and_b32_sdwa v227, %[mk], v226 dst_sel:DWORD dst_unused:UNUSED_PAD src0_sel:DWORD src1_sel:WORD_1\n\t" \
  "v_mfma_f32_16x16x32_bf16 v[200:203], v[208:211], v[132:135], 0\n\t" \
  "ds_read_b64_tr_b16 v[208:209], %[a1] offset:2048\n\t" \
  "v_and_b32 v226, %[mk], v226\n\t" \
  "v_and_b32_sdwa v229, %[mk], v228 dst_sel:DWORD dst_unused:UNUSED_PAD src0_sel:DWORD src1_sel:WORD_1\n\t" \
  "s_waitcnt lgkmcnt(2)\n\t" \
  "v_mfma_f32_16x16x32_bf16 v[176:179], v[212:215], v[100:103], v[176:179]\n\t" \
  "ds_read_b64_tr_b16 v[210:211], %[a1] offset:5120\n\t" \
  "v_and_b32 v228, %[mk], v228\n\t" \
  "v_mfma_f32_16x16x32_bf16 v[184:187], v[212:215], v[112:115], v[184:187]\n\t" \
  "ds_read_b32 v224, v224 offset:0\n\t" \
  "ds_read_b32 v227, v227 offset:0\n\t" \
  "v_and_b32_sdwa v231, %[mk], v230 dst_sel:DWORD dst_unused:UNUSED_PAD src0_sel:DWORD src1_sel:WORD_1\n\t" \
  "v_mfma_f32_16x16x32_bf16 v[192:195], v[212:215], v[124:127], v[192:195]\n\t" \
  "ds_read_b32 v226, v226 offset:0\n\t" \
  "v_and_b32 v230, %[mk], v230\n\t" \
  "v_fma_f32 v149, |v149|, v217, v149 mul:4\n\t" \
  "v_mfma_f32_16x16x32_bf16 v[200:203], v[212:215], v[136:139], v[200:203]\n\t" \
  "ds_read_b64_tr_b16 v[212:213], %[a1] offset:512\n\t" \
  "ds_read_b64_tr_b16 v[214:215], %[a1] offset:1536\n\t" \
  "v_fma_f32 v148, |v148|, v216, v148 mul:4\n\t" \
  "v_fma_f32 v151, |v151|, v219, v151 mul:4\n\t" \
  "s_waitcnt lgkmcnt(5)\n\t" \
  "v_mfma_f32_16x16x32_bf16 v[176:179], v[208:211], v[104:107], v[176:179]\n\t" \
  "ds_read_b32 v229, v229 offset:0\n\t" \
  "ds_read_b32 v228, v228 offset:0\n\t" \
  "v_fma_f32 v150, |v150|, v218, v150 mul:4\n\t" \
  "v_fma_f32 v157, |v157|, v221, v157 mul:4\n\t" \
  "v_mfma_f32_16x16x32_bf16 v[184:187], v[208:211], v[116:119], v[184:187]\n\t" \
  "ds_read_b32 v231, v231 offset:0\n\t" \
  "v_fma_f32 v156, |v156|, v220, v156 mul:4\n\t" \
  "v_fma_f32 v159, |v159|, v223, v159 mul:4\n\t" \
  "v_fma_f32 v158, |v158|, v222, v158 mul:4\n\t" \
  "v_mfma_f32_16x16x32_bf16 v[192:195], v[208:211], v[128:131], v[192:195]\n\t" \
  "ds_read_b32 v230, v230 offset:0\n\t" \
  "v_fma_mix_f32 v160, v160, v235, 0 op_sel_hi:[0,1,0]\n\t" \
  "v_fma_mix_f32 v161, v161, v234, 0 op_sel_hi:[0,1,0]\n\t" \
  "v_cvt_pk_bf16_f32 v162, v160, v161\n\t" \
  "v_mfma_f32_16x16x32_bf16 v[200:203], v[208:211], v[140:143], v[200:203]\n\t" \
  "ds_read_b64_tr_b16 v[208:209], %[a1] offset:3584\n\t" \
  "v_cvt_pk_bf16_f32 v148, v148, v149\n\t" \
  "v_cvt_pk_bf16_f32 v149, v150, v151\n\t" \
  "s_waitcnt lgkmcnt(5)\n\t" \
  "v_mfma_f32_16x16x32_bf16 v[180:183], v[212:215], v[96:99], 0\n\t" \
  "ds_read_b64_tr_b16 v[210:211], %[a1] offset:4608\n\t" \
  "s_setprio 0\n\t" \
  "v_cvt_pk_bf16_f32 v156, v156, v157\n\t" \
  "v_cvt_pk_bf16_f32 v157, v158, v159\n\t" \
  "v_cvt_pk_f16_f32 v216, |v178|, |v179| clamp\n\t" \
  "v_mfma_f32_16x16x32_bf16 v[188:191], v[212:215], v[108:111], 0\n\t" \
  "v_cvt_pk_f16_f32 v232, v176, v177\n\t" \
  "v_cvt_pk_f16_f32 v218, |v186|, |v187| clamp\n\t" \
  "v_cvt_pk_f16_f32 v220, |v184|, |v185| clamp\n\t" \
  "v_mfma_f32_16x16x32_bf16 v[196:199], v[212:215], v[120:123], 0\n\t" \
  "v_pk_mul_f16 v233, v232, v232\n\t" \
  "v_and_b32_sdwa v217, %[mk], v216 dst_sel:DWORD dst_unused:UNUSED_PAD src0_sel:DWORD src1_sel:WORD_1\n\t" \
  "v_mfma_f32_16x16x32_bf16 v[204:207], v[212:215], v[132:135], 0\n\t" \
  "ds_read_b64_tr_b16 v[212:213], %[a1] offset:2560\n\t" \
  "v_and_b32 v216, %[mk], v216\n\t" \
  "v_and_b32_sdwa v219, %[mk], v218 dst_sel:DWORD dst_unused:UNUSED_PAD src0_sel:DWORD src1_sel:WORD_1\n\t" \
  "s_waitcnt lgkmcnt(1)\n\t" \
  "v_mfma_f32_16x16x32_bf16 v[180:183], v[208:211], v[100:103], v[180:183]\n\t" \
  "ds_read_b32 v246, v240 offset:33900\n\t" \
  "ds_read_b64_tr_b16 v[214:215], %[a1] offset:5632\n\t" \
  "v_and_b32 v218, %[mk], v218\n\t" \
  "v_mfma_f32_16x16x32_bf16 v[188:191], v[208:211], v[112:115], v[188:191]\n\t" \
  "ds_read_b32 v217, v217 offset:0\n\t" \
  "v_and_b32_sdwa v221, %[mk], v220 dst_sel:DWORD dst_unused:UNUSED_PAD src0_sel:DWORD src1_sel:WORD_1\n\t" \
  "v_mfma_f32_16x16x32_bf16 v[196:199], v[208:211], v[124:127], v[196:199]\n\t" \
  "ds_read_b32 v216, v216 offset:0\n\t" \
  "v_and_b32 v220, %[mk], v220\n\t" \
  "v_pk_fma_f16 v233, v233, %[h2], %[h1]\n\t" \
  "v_mfma_f32_16x16x32_bf16 v[204:207], v[208:211], v[136:139], v[204:207]\n\t" \
  "s_waitcnt lgkmcnt(3)\n\t" \
  "v_cmp_gt_u32 vcc, %[tg], v246\n\t" \
  "s_cbranch_vccnz .Lcnx_w15\n.Lcnx_b15:\n\t" \
  "ds_read_b64_tr_b16 v[208:209], %[a2] offset:6160\n\t" \
  "ds_read_b64_tr_b16 v[210:211], %[a2] offset:7696\n\t" \
  "v_pk_mul_f16 v233, v232, v233\n\t" \
  "v_fma_f32 v165, |v165|, v225, v165 mul:4\n\t" \
  "s_waitcnt lgkmcnt(4)\n\t" \
  "v_mfma_f32_16x16x32_bf16 v[180:183], v[212:215], v[104:107], v[180:183]\n\t" \
  "s_waitcnt vmcnt(1)\n\t" \
  "s_cmp_lg_u32 %[rnd], 1\n\t" \
  "s_cbranch_scc1 .Lcnx_c41\n\t" \
  "s_waitcnt vmcnt(0)\n\t" \
  ".Lcnx_c41:\n\t" \
  "s_mov_b64 exec, 1\n\t" \
  "ds_inc_u32 v240, %[h2] offset:33904\n\t" \
  "s_mov_b64 exec, -1\n\t" \
  "s_cmp_lg_u32 %[wq], 1\n\t" \
  "s_cbranch_scc1 .Lcnx_c42\n\t" \
  "s_add_u32 m0, %[wl], 0\n\t" \
  "s_nop 0\n\t" \
  "buffer_load_dwordx4 %[wv], %[wrs], %[so0] offen lds\n\t" \
  "buffer_load_dwordx4 %[wv], %[wrs], %[so1] offen offset:1024 lds\n\t" \
  "buffer_load_dwordx4 %[wv], %[wrs], %[so2] offen offset:2048 lds\n\t" \
  "s_add_u32 %[so0], %[so0], %[wc]\n\t" \
  "s_add_u32 %[so1], %[so1], %[wc]\n\t" \
  "s_add_u32 %[so2], %[so2], %[wc]\n\t" \
  ".Lcnx_c42:\n\t" \
  "ds_read_b32 v219, v219 offset:0\n\t" \
  "ds_read_b32 v218, v218 offset:0\n\t" \
  "v_fma_f32 v164, |v164|, v224, v164 mul:4\n\t" \
  "v_fma_f32 v167, |v167|, v227, v167 mul:4\n\t" \
  "v_mfma_f32_16x16x32_bf16 v[188:191], v[212:215], v[116:119], v[188:191]\n\t" \
  "ds_read_b32 v221, v221 offset:0\n\t" \
  "v_fma_f32 v166, |v166|, v226, v166 mul:4\n\t" \
  "v_fma_f32 v173, |v173|, v229, v173 mul:4\n\t" \
  "v_fma_f32 v172, |v172|, v228, v172 mul:4\n\t" \
  "v_mfma_f32_16x16x32_bf16 v[196:199], v[212:215], v[128:131], v[196:199]\n\t" \
  "ds_read_b32 v220, v220 offset:0\n\t" \
  "v_fma_f32 v175, |v175|, v231, v175 mul:4\n\t" \
  "v_fma_f32 v174, |v174|, v230, v174 mul:4\n\t" \
  "v_cvt_pk_bf16_f32 v164, v164, v165\n\t" \
  "v_mfma_f32_16x16x32_bf16 v[204:207], v[212:215], v[140:143], v[204:207]\n\t" \
  "ds_read_b64_tr_b16 v[212:213], %[a2] offset:6168\n\t" \
  "v_cvt_pk_bf16_f32 v165, v166, v167\n\t" \
  "v_cvt_pk_bf16_f32 v172, v172, v173\n\t" \
  "v_cvt_pk_bf16_f32 v173, v174, v175\n\t" \
  "s_setprio 2\n\t" \
  "v_exp_f16 v232, v233\n\t" \
  "v_cvt_pk_f16_f32 v224, |v194|, |v195| clamp\n\t" \
  "v_mfma_f32_16x16x32_bf16 v[48:51], v[242:245], v[236:239], v[48:51]\n\t" \
  "ds_read_b64_tr_b16 v[214:215], %[a2] offset:7704\n\t" \
  "v_cvt_pk_f16_f32 v234, v192, v193\n\t" \
  "v_cvt_pk_f16_f32 v226, |v202|, |v203| clamp\n\t" \
  "v_mfma_f32_16x16x32_bf16 v[52:55], v[240:243], v[236:239], v[52:55]\n\t" \
  "buffer_load_dwordx4 v[236:239], %[vo], %[xrs], %[oc1] offen offset:64\n\t" \
  "v_cvt_pk_f16_f32 v228, |v200|, |v201| clamp\n\t" \
  "v_exp_f16_sdwa v232, v233 dst_sel:WORD_1 dst_unused:UNUSED_PRESERVE src0_sel:WORD_1\n\t" \
  "v_pk_mul_f16 v235, v234, v234\n\t" \
  "s_waitcnt lgkmcnt(7)\n\t" \
  "v_mfma_f32_16x16x32_bf16 v[0:3], v[208:211], v[146:149], v[0:3]\n\t" \
  "v_and_b32_sdwa v225, %[mk], v224 dst_sel:DWORD dst_unused:UNUSED_PAD src0_sel:DWORD src1_sel:WORD_1\n\t" \
  "v_and_b32 v224, %[mk], v224\n\t" \
  "v_mfma_f32_16x16x32_bf16 v[24:27], v[208:211], v[154:157], v[24:27]\n\t" \
  "ds_read_b32 v225, v225 offset:0\n\t" \
  "v_and_b32_sdwa v227, %[mk], v226 dst_sel:DWORD dst_unused:UNUSED_PAD src0_sel:DWORD src1_sel:WORD_1\n\t" \
  "v_mfma_f32_16x16x32_bf16 v[48:51], v[208:211], v[162:165], v[48:51]\n\t" \
  "ds_read_b32 v224, v224 offset:0\n\t" \
  "v_and_b32 v226, %[mk], v226\n\t" \
  "v_and_b32_sdwa v229, %[mk], v228 dst_sel:DWORD dst_unused:UNUSED_PAD src0_sel:DWORD src1_sel:WORD_1\n\t" \
  "v_mfma_f32_16x16x32_bf16 v[72:75], v[208:211], v[170:173], v[72:75]\n\t" \
  "ds_read_b64_tr_b16 v[208:209], %[a2] offset:6224\n\t" \
  "v_and_b32 v228, %[mk], v228\n\t" \
  "s_waitcnt lgkmcnt(3)\n\t" \
  "v_mfma_f32_16x16x32_bf16 v[4:7], v[212:215], v[146:149], v[4:7]\n\t" \
  "ds_read_b64_tr_b16 v[210:211], %[a2] offset:7760\n\t" \
  "ds_read_b32 v227, v227 offset:0\n\t" \
  "v_pk_fma_f16 v235, v235, %[h2], %[h1]\n\t" \
  "v_mfma_f32_16x16x32_bf16 v[28:31], v[212:215], v[154:157], v[28:31]\n\t" \
  "ds_read_b32 v226, v226 offset:0\n\t" \
  "v_pk_mul_f16 v235, v234, v235\n\t" \
  "v_fma_f32 v179, |v179|, v217, v179 mul:4\n\t" \
  "v_fma_f32 v178, |v178|, v216, v178 mul:4\n\t" \
  "v_mfma_f32_16x16x32_bf16 v[52:55], v[212:215], v[162:165], v[52:55]\n\t" \
  "ds_read_b32 v229, v229 offset:0\n\t" \
  "v_fma_f32 v187, |v187|, v219, v187 mul:4\n\t" \
  "v_fma_f32 v186, |v186|, v218, v186 mul:4\n\t" \
  "v_fma_f32 v185, |v185|, v221, v185 mul:4\n\t" \
  "v_mfma_f32_16x16x32_bf16 v[76:79], v[212:215], v[170:173], v[76:79]\n\t" \
  "ds_read_b64_tr_b16 v[212:213], %[a2] offset:6232\n\t" \
  "v_fma_f32 v184, |v184|, v220, v184 mul:4\n\t" \
  "v_pk_fma_f16 v232, v232, %[hh], %[hh]\n\t" \
  "s_waitcnt lgkmcnt(4)\n\t" \
  "v_mfma_f32_16x16x32_bf16 v[8:11], v[208:211], v[146:149], v[8:11]\n\t" \
  "ds_read_b64_tr_b16 v[214:215], %[a2] offset:7768\n\t" \
  "v_cvt_pk_bf16_f32 v179, v178, v179\n\t" \
  "v_cvt_pk_bf16_f32 v187, v186, v187\n\t" \
  "v_cvt_pk_bf16_f32 v186, v184, v185\n\t" \
  "v_mfma_f32_16x16x32_bf16 v[32:35], v[208:211], v[154:157], v[32:35]\n\t" \
  "ds_read_b32 v228, v228 offset:0\n\t" \
  "v_rcp_f16 v233, v232\n\t" \
  "v_exp_f16 v234, v235\n\t" \
  "v_cvt_pk_f16_f32 v216, |v180|, |v181| clamp\n\t" \
  "v_mfma_f32_16x16x32_bf16 v[56:59], v[208:211], v[162:165], v[56:59]\n\t" \
  "v_cvt_pk_f16_f32 v218, |v182|, |v183| clamp\n\t" \
  "v_cvt_pk_f16_f32 v220, |v188|, |v189| clamp\n\t" \
  "v_mfma_f32_16x16x32_bf16 v[80:83], v[208:211], v[170:173], v[80:83]\n\t" \
  "ds_read_b64_tr_b16 v[208:209], %[a2] offset:6288\n\t" \
  "v_cvt_pk_f16_f32 v222, |v190|, |v191| clamp\n\t" \
  "s_setprio 2\n\t" \
  "v_rcp_f16_sdwa v232, v232 dst_sel:DWORD dst_unused:UNUSED_PAD src0_sel:WORD_1\n\t" \
  "v_exp_f16_sdwa v234, v235 dst_sel:WORD_1 dst_unused:UNUSED_PRESERVE src0_sel:WORD_1\n\t" \
  "s_waitcnt lgkmcnt(2)\n\t" \
  "v_mfma_f32_16x16x32_bf16 v[12:15], v[212:215], v[146:149], v[12:15]\n\t" \
  "ds_read_b64_tr_b16 v[210:211], %[a2] offset:7824\n\t" \
  "v_and_b32_sdwa v217, %[mk], v216 dst_sel:DWORD dst_unused:UNUSED_PAD src0_sel:DWORD src1_sel:WORD_1\n\t" \
  "v_and_b32 v216, %[mk], v216\n\t" \
  "v_mfma_f32_16x16x32_bf16 v[36:39], v[212:215], v[154:157], v[36:39]\n\t" \
  "ds_read_b32 v217, v217 offset:0\n\t" \
  "v_and_b32_sdwa v219, %[mk], v218 dst_sel:DWORD dst_unused:UNUSED_PAD src0_sel:DWORD src1_sel:WORD_1\n\t" \
  "v_mfma_f32_16x16x32_bf16 v[60:63], v[212:215], v[162:165], v[60:63]\n\t" \
  "ds_read_b32 v216, v216 offset:0\n\t" \
  "v_and_b32 v218, %[mk], v218\n\t" \
  "v_and_b32_sdwa v221, %[mk], v220 dst_sel:DWORD dst_unused:UNUSED_PAD src0_sel:DWORD src1_sel:WORD_1\n\t" \
  "v_mfma_f32_16x16x32_bf16 v[84:87], v[212:215], v[170:173], v[84:87]\n\t" \
  "ds_read_b64_tr_b16 v[212:213], %[a2] offset:6296\n\t" \
  "v_and_b32 v220, %[mk], v220\n\t" \
  "s_waitcnt lgkmcnt(3)\n\t" \
  "v_mfma_f32_16x16x32_bf16 v[16:19], v[208:211], v[146:149], v[16:19]\n\t" \
  "ds_read_b32 v246, v240 offset:33904\n\t" \
  "ds_read_b64_tr_b16 v[214:215], %[a2] offset:7832\n\t" \
  "ds_read_b32 v219, v219 offset:0\n\t" \
  "v_and_b32_sdwa v223, %[mk], v222 dst_sel:DWORD dst_unused:UNUSED_PAD src0_sel:DWORD src1_sel:WORD_1\n\t" \
  "v_mfma_f32_16x16x32_bf16 v[40:43], v[208:211], v[154:157], v[40:43]\n\t" \
  "ds_read_b32 v218, v218 offset:0\n\t" \
  "v_and_b32 v222, %[mk], v222\n\t" \
  "v_fma_f32 v195, |v195|, v225, v195 mul:4\n\t" \
  "v_mfma_f32_16x16x32_bf16 v[64:67], v[208:211], v[162:165], v[64:67]\n\t" \
  "ds_read_b32 v221, v221 offset:0\n\t" \
  "v_fma_f32 v194, |v194|, v224, v194 mul:4\n\t" \
  "v_fma_f32 v203, |v203|, v227, v203 mul:4\n\t" \
  "v_mfma_f32_16x16x32_bf16 v[88:91], v[208:211], v[170:173], v[88:91]\n\t" \
  "s_waitcnt lgkmcnt(4)\n\t" \
  "v_cmp_gt_u32 vcc, %[tg], v246\n\t" \
  "s_cbranch_vccnz .Lcnx_w16\n.Lcnx_b16:\n\t" \
  "ds_read_b64_tr_b16 v[208:209], %[a1] offset:6144\n\t" \
  "ds_read_b64_tr_b16 v[210:211], %[a1] offset:7168\n\t" \
  "v_fma_f32 v202, |v202|, v226, v202 mul:4\n\t" \
  "v_fma_f32 v201, |v201|, v229, v201 mul:4\n\t" \
  "s_waitcnt lgkmcnt(5)\n\t" \
  "v_mfma_f32_16x16x32_bf16 v[20:23], v[212:215], v[146:149], v[20:23]\n\t" \
  "s_waitcnt vmcnt(1)\n\t" \
  "s_mov_b64 exec, 1\n\t" \
  "ds_inc_u32 v240, %[h2] offset:33908\n\t" \
  "s_mov_b64 exec, -1\n\t" \
  "s_cmp_lg_u32 %[wq], 0\n\t" \
  "s_cbranch_scc1 .Lcnx_c43\n\t" \
  "s_add_u32 m0, %[wl], 0\n\t" \
  "s_nop 0\n\t" \
  "buffer_load_dwordx4 %[wv], %[wrs], %[so0] offen lds\n\t" \
  "buffer_load_dwordx4 %[wv], %[wrs], %[so1] offen offset:1024 lds\n\t" \
  "buffer_load_dwordx4 %[wv], %[wrs], %[so2] offen offset:2048 lds\n\t" \
  "s_add_u32 %[so0], %[so0], %[wc]\n\t" \
  "s_add_u32 %[so1], %[so1], %[wc]\n\t" \
  "s_add_u32 %[so2], %[so2], %[wc]\n\t" \
  ".Lcnx_c43:\n\t" \
  "s_cmp_lg_u32 %[rnd], 0\n\t" \
  "s_cbranch_scc1 .Lcnx_c44\n\t" \
  "s_add_u32 m0, %[xl], 57400\n\t" \
  "s_add_u32 %[xo], %[xo], %[rs]\n\t" \
  "buffer_load_dwordx4 %[xv], %[xrs], %[xo] offen lds\n\t" \
  "s_cmp_lg_u32 %[w512], 2048\n\t" \
  "s_cbranch_scc1 .Lcnx_c44\n\t" \
  "s_mov_b32 m0, %[pf]\n\t" \
  "s_add_u32 %[t0], %[xo], %[wb]\n\t" \
  "buffer_load_dword %[wv], %[xrs], %[t0] offen lds\n\t" \
  ".Lcnx_c44:\n\t" \
  "ds_read_b32 v220, v220 offset:0\n\t" \
  "v_fma_f32 v200, |v200|, v228, v200 mul:4\n\t" \
  "v_fma_mix_f32 v176, v176, v233, 0 op_sel_hi:[0,1,0]\n\t" \
  "v_fma_mix_f32 v177, v177, v232, 0 op_sel_hi:[0,1,0]\n\t" \
  "v_mfma_f32_16x16x32_bf16 v[44:47], v[212:215], v[154:157], v[44:47]\n\t" \
  "ds_read_b32 v223, v223 offset:0\n\t" \
  "v_pk_fma_f16 v234, v234, %[hh], %[hh]\n\t" \
  "v_cvt_pk_bf16_f32 v178, v176, v177\n\t" \
  "v_cvt_pk_bf16_f32 v195, v194, v195\n\t" \
  "v_mfma_f32_16x16x32_bf16 v[68:71], v[212:215], v[162:165], v[68:71]\n\t" \
  "ds_read_b32 v222, v222 offset:0\n\t" \
  "v_cvt_pk_bf16_f32 v203, v202, v203\n\t" \
  "v_cvt_pk_bf16_f32 v202, v200, v201\n\t" \
  "v_rcp_f16 v235, v234\n\t" \
  "s_setprio 1\n\t" \
  "v_mfma_f32_16x16x32_bf16 v[92:95], v[212:215], v[170:173], v[92:95]\n\t" \
  "ds_read_b64_tr_b16 v[212:213], %[a1] offset:9216\n\t" \
  "v_cvt_pk_f16_f32 v224, |v196|, |v197| clamp\n\t" \
  "v_cvt_pk_f16_f32 v226, |v198|, |v199| clamp\n\t" \
  "s_waitcnt lgkmcnt(5)\n\t" \
  "v_mfma_f32_16x16x32_bf16 v[144:147], v[208:211], v[96:99], 0\n\t" \
  "ds_read_b64_tr_b16 v[214:215], %[a1] offset:10240\n\t" \
  "v_cvt_pk_f16_f32 v228, |v204|, |v205| clamp\n\t" \
  "v_cvt_pk_f16_f32 v230, |v206|, |v207| clamp\n\t" \
  "v_rcp_f16_sdwa v234, v234 dst_sel:DWORD dst_unused:UNUSED_PAD src0_sel:WORD_1\n\t" \
  "v_mfma_f32_16x16x32_bf16 v[152:155], v[208:211], v[108:111], 0\n\t" \
  "v_and_b32_sdwa v225, %[mk], v224 dst_sel:DWORD dst_unused:UNUSED_PAD src0_sel:DWORD src1_sel:WORD_1\n\t" \
  "v_and_b32 v224, %[mk], v224\n\t" \
  "v_mfma_f32_16x16x32_bf16 v[160:163], v[208:211], v[120:123], 0\n\t" \
  "ds_read_b32 v225, v225 offset:0\n\t" \
  "v_and_b32_sdwa v227, %[mk], v226 dst_sel:DWORD dst_unused:UNUSED_PAD src0_sel:DWORD src1_sel:WORD_1\n\t" \
  "v_mfma_f32_16x16x32_bf16 v[168:171], v[208:211], v[132:135], 0\n\t" \
  "ds_read_b64_tr_b16 v[208:209], %[a1] offset:8192\n\t" \
  "v_and_b32 v226, %[mk], v226\n\t" \
  "v_and_b32_sdwa v229, %[mk], v228 dst_sel:DWORD dst_unused:UNUSED_PAD src0_sel:DWORD src1_sel:WORD_1\n\t" \
  "s_waitcnt lgkmcnt(2)\n\t" \
  "v_mfma_f32_16x16x32_bf16 v[144:147], v[212:215], v[100:103], v[144:147]\n\t" \
  "ds_read_b64_tr_b16 v[210:211], %[a1] offset:11264\n\t" \
  "v_and_b32 v228, %[mk], v228\n\t" \
  "v_mfma_f32_16x16x32_bf16 v[152:155], v[212:215], v[112:115], v[152:155]\n\t" \
  "ds_read_b32 v224, v224 offset:0\n\t" \
  "ds_read_b32 v227, v227 offset:0\n\t" \
  "v_and_b32_sdwa v231, %[mk], v230 dst_sel:DWORD dst_unused:UNUSED_PAD src0_sel:DWORD src1_sel:WORD_1\n\t" \
  "v_mfma_f32_16x16x32_bf16 v[160:163], v[212:215], v[124:127], v[160:163]\n\t" \
  "ds_read_b32 v226, v226 offset:0\n\t" \
  "v_and_b32 v230, %[mk], v230\n\t" \
  "v_fma_f32 v181, |v181|, v217, v181 mul:4\n\t" \
  "v_mfma_f32_16x16x32_bf16 v[168:171], v[212:215], v[136:139], v[168:171]\n\t" \
  "ds_read_b64_tr_b16 v[212:213], %[a1] offset:6656\n\t" \
  "ds_read_b64_tr_b16 v[214:215], %[a1] offset:7680\n\t" \
  "v_fma_f32 v180, |v180|, v216, v180 mul:4\n\t" \
  "v_fma_f32 v183, |v183|, v219, v183 mul:4\n\t" \
  "s_waitcnt lgkmcnt(5)\n\t" \
  "v_mfma_f32_16x16x32_bf16 v[144:147], v[208:211], v[104:107], v[144:147]\n\t" \
  "ds_read_b32 v229, v229 offset:0\n\t" \
  "ds_read_b32 v228, v228 offset:0\n\t" \
  "v_fma_f32 v182, |v182|, v218, v182 mul:4\n\t" \
  "v_fma_f32 v189, |v189|, v221, v189 mul:4\n\t" \
  "v_mfma_f32_16x16x32_bf16 v[152:155], v[208:211], v[116:119], v[152:155]\n\t" \
  "ds_read_b32 v231, v231 offset:0\n\t" \
  "v_fma_f32 v188, |v188|, v220, v188 mul:4\n\t" \
  "v_fma_f32 v191, |v191|, v223, v191 mul:4\n\t" \
  "v_fma_f32 v190, |v190|, v222, v190 mul:4\n\t" \
  "v_mfma_f32_16x16x32_bf16 v[160:163], v[208:211], v[128:131], v[160:163]\n\t" \
  "ds_read_b32 v230, v230 offset:0\n\t" \
  "v_fma_mix_f32 v192, v192, v235, 0 op_sel_hi:[0,1,0]\n\t" \
  "v_fma_mix_f32 v193, v193, v234, 0 op_sel_hi:[0,1,0]\n\t" \
  "v_cvt_pk_bf16_f32 v194, v192, v193\n\t" \
  "v_mfma_f32_16x16x32_bf16 v[168:171], v[208:211], v[140:143], v[168:171]\n\t" \
  "ds_read_b64_tr_b16 v[208:209], %[a1] offset:9728\n\t" \
  "v_cvt_pk_bf16_f32 v180, v180, v181\n\t" \
  "v_cvt_pk_bf16_f32 v181, v182, v183\n\t" \
  "s_waitcnt lgkmcnt(5)\n\t" \
  "v_mfma_f32_16x16x32_bf16 v[148:151], v[212:215], v[96:99], 0\n\t" \
  "ds_read_b64_tr_b16 v[210:211], %[a1] offset:10752\n\t" \
  "s_setprio 0\n\t" \
  "v_cvt_pk_bf16_f32 v188, v188, v189\n\t" \
  "v_cvt_pk_bf16_f32 v189, v190, v191\n\t" \
  "v_cvt_pk_f16_f32 v216, |v146|, |v147| clamp\n\t" \
  "v_mfma_f32_16x16x32_bf16 v[156:159], v[212:215], v[108:111], 0\n\t" \
  "v_cvt_pk_f16_f32 v232, v144, v145\n\t" \
  "v_cvt_pk_f16_f32 v218, |v154|, |v155| clamp\n\t" \
  "v_cvt_pk_f16_f32 v220, |v152|, |v153| clamp\n\t" \
  "v_mfma_f32_16x16x32_bf16 v[164:167], v[212:215], v[120:123], 0\n\t" \
  "v_pk_mul_f16 v233, v232, v232\n\t" \
  "v_and_b32_sdwa v217, %[mk], v216 dst_sel:DWORD dst_unused:UNUSED_PAD src0_sel:DWORD src1_sel:WORD_1\n\t" \
  "v_mfma_f32_16x16x32_bf16 v[172:175], v[212:215], v[132:135], 0\n\t" \
  "ds_read_b64_tr_b16 v[212:213], %[a1] offset:8704\n\t" \
  "v_and_b32 v216, %[mk], v216\n\t" \
  "v_and_b32_sdwa v219, %[mk], v218 dst_sel:DWORD dst_unused:UNUSED_PAD src0_sel:DWORD src1_sel:WORD_1\n\t" \
  "s_waitcnt lgkmcnt(1)\n\t" \
  "v_mfma_f32_16x16x32_bf16 v[148:151], v[208:211], v[100:103], v[148:151]\n\t" \
  "ds_read_b32 v246, v240 offset:33908\n\t" \
  "ds_read_b64_tr_b16 v[214:215], %[a1] offset:11776\n\t" \
  "v_and_b32 v218, %[mk], v218\n\t" \
  "v_mfma_f32_16x16x32_bf16 v[156:159], v[208:211], v[112:115], v[156:159]\n\t" \
  "ds_read_b32 v217, v217 offset:0\n\t" \
  "v_and_b32_sdwa v221, %[mk], v220 dst_sel:DWORD dst_unused:UNUSED_PAD src0_sel:DWORD src1_sel:WORD_1\n\t" \
  "v_mfma_f32_16x16x32_bf16 v[164:167], v[208:211], v[124:127], v[164:167]\n\t" \
  "ds_read_b32 v216, v216 offset:0\n\t" \
  "v_and_b32 v220, %[mk], v220\n\t" \
  "v_pk_fma_f16 v233, v233, %[h2], %[h1]\n\t" \
  "v_mfma_f32_16x16x32_bf16 v[172:175], v[208:211], v[136:139], v[172:175]\n\t" \
  "s_waitcnt lgkmcnt(3)\n\t" \
  "v_cmp_gt_u32 vcc, %[tg], v246\n\t" \
  "s_cbranch_vccnz .Lcnx_w17\n.Lcnx_b17:\n\t" \
  "ds_read_b64_tr_b16 v[208:209], %[a2] offset:0\n\t" \
  "ds_read_b64_tr_b16 v[210:211], %[a2] offset:1536\n\t" \
  "v_pk_mul_f16 v233, v232, v233\n\t" \
  "v_fma_f32 v197, |v197|, v225, v197 mul:4\n\t" \
  "s_waitcnt lgkmcnt(4)\n\t" \
  "v_mfma_f32_16x16x32_bf16 v[148:151], v[212:215], v[104:107], v[148:151]\n\t" \
  "s_waitcnt vmcnt(1)\n\t" \
  "s_cmp_lg_u32 %[rnd], 1\n\t" \
  "s_cbranch_scc1 .Lcnx_c46\n\t" \
  "s_waitcnt vmcnt(0)\n\t" \
  ".Lcnx_c46:\n\t" \
  "s_mov_b64 exec, 1\n\t" \
  "ds_inc_u32 v240, %[h2] offset:33912\n\t" \
  "s_mov_b64 exec, -1\n\t" \
  "s_cmp_lg_u32 %[wq], 1\n\t" \
  "s_cbranch_scc1 .Lcnx_c47\n\t" \
  "s_add_u32 m0, %[wl], %[ws]\n\t" \
  "s_nop 0\n\t" \
  "buffer_load_dwordx4 %[wv], %[wrs], %[so0] offen lds\n\t" \
  "buffer_load_dwordx4 %[wv], %[wrs], %[so1] offen offset:1024 lds\n\t" \
  "buffer_load_dwordx4 %[wv], %[wrs], %[so2] offen offset:2048 lds\n\t" \
  "s_add_u32 %[so0], %[so0], %[wc]\n\t" \
  "s_add_u32 %[so1], %[so1], %[wc]\n\t" \
  "s_add_u32 %[so2], %[so2], %[wc]\n\t" \
  ".Lcnx_c47:\n\t" \
  "ds_read_b32 v219, v219 offset:0\n\t" \
  "ds_read_b32 v218, v218 offset:0\n\t" \
  "v_fma_f32 v196, |v196|, v224, v196 mul:4\n\t" \
  "v_fma_f32 v199, |v199|, v227, v199 mul:4\n\t" \
  "v_mfma_f32_16x16x32_bf16 v[156:159], v[212:215], v[116:119], v[156:159]\n\t" \
  "ds_read_b32 v221, v221 offset:0\n\t" \
  "v_fma_f32 v198, |v198|, v226, v198 mul:4\n\t" \
  "v_fma_f32 v205, |v205|, v229, v205 mul:4\n\t" \
  "v_fma_f32 v204, |v204|, v228, v204 mul:4\n\t" \
  "v_mfma_f32_16x16x32_bf16 v[164:167], v[212:215], v[128:131], v[164:167]\n\t" \
  "ds_read_b32 v220, v220 offset:0\n\t" \
  "v_fma_f32 v207, |v207|, v231, v207 mul:4\n\t" \
  "v_fma_f32 v206, |v206|, v230, v206 mul:4\n\t" \
  "v_cvt_pk_bf16_f32 v196, v196, v197\n\t" \
  "v_mfma_f32_16x16x32_bf16 v[172:175], v[212:215], v[140:143], v[172:175]\n\t" \
  "ds_read_b64_tr_b16 v[212:213], %[a2] offset:8\n\t" \
  "v_cvt_pk_bf16_f32 v197, v198, v199\n\t" \
  "v_cvt_pk_bf16_f32 v204, v204, v205\n\t" \
  "v_cvt_pk_bf16_f32 v205, v206, v207\n\t" \
  "s_setprio 2\n\t" \
  "v_exp_f16 v232, v233\n\t" \
  "v_cvt_pk_f16_f32 v224, |v162|, |v163| clamp\n\t" \
  "v_mfma_f32_16x16x32_bf16 v[56:59], v[242:245], v[236:239], v[56:59]\n\t" \
  "ds_read_b64_tr_b16 v[214:215], %[a2] offset:1544\n\t" \
  "v_cvt_pk_f16_f32 v234, v160, v161\n\t" \
  "v_cvt_pk_f16_f32 v226, |v170|, |v171| clamp\n\t" \
  "v_mfma_f32_16x16x32_bf16 v[60:63], v[240:243], v[236:239], v[60:63]\n\t" \
  "buffer_load_dwordx4 v[236:239], %[vo], %[xrs], %[oc1] offen offset:128\n\t" \
  "v_cvt_pk_f16_f32 v228, |v168|, |v169| clamp\n\t" \
  "v_exp_f16_sdwa v232, v233 dst_sel:WORD_1 dst_unused:UNUSED_PRESERVE src0_sel:WORD_1\n\t" \
  "v_pk_mul_f16 v235, v234, v234\n\t" \
  "s_waitcnt lgkmcnt(7)\n\t" \
  "v_mfma_f32_16x16x32_bf16 v[0:3], v[208:211], v[178:181], v[0:3]\n\t" \
  "v_and_b32_sdwa v225, %[mk], v224 dst_sel:DWORD dst_unused:UNUSED_PAD src0_sel:DWORD src1_sel:WORD_1\n\t" \
  "v_and_b32 v224, %[mk], v224\n\t" \
  "v_mfma_f32_16x16x32_bf16 v[24:27], v[208:211], v[186:189], v[24:27]\n\t" \
  "ds_read_b32 v225, v225 offset:0\n\t" \
  "v_and_b32_sdwa v227, %[mk], v226 dst_sel:DWORD dst_unused:UNUSED_PAD src0_sel:DWORD src1_sel:WORD_1\n\t" \
  "v_mfma_f32_16x16x32_bf16 v[48:51], v[208:211], v[194:197], v[48:51]\n\t" \
  "ds_read_b32 v224, v224 offset:0\n\t" \
  "v_and_b32 v226, %[mk], v226\n\t" \
  "v_and_b32_sdwa v229, %[mk], v228 dst_sel:DWORD dst_unused:UNUSED_PAD src0_sel:DWORD src1_sel:WORD_1\n\t" \
  "v_mfma_f32_16x16x32_bf16 v[72:75], v[208:211], v[202:205], v[72:75]\n\t" \
  "ds_read_b64_tr_b16 v[208:209], %[a2] offset:64\n\t" \
  "v_and_b32 v228, %[mk], v228\n\t" \
  "s_waitcnt lgkmcnt(3)\n\t" \
  "v_mfma_f32_16x16x32_bf16 v[4:7], v[212:215], v[178:181], v[4:7]\n\t" \
  "ds_read_b64_tr_b16 v[210:211], %[a2] offset:1600\n\t" \
  "ds_read_b32 v227, v227 offset:0\n\t" \
  "v_pk_fma_f16 v235, v235, %[h2], %[h1]\n\t" \
  "v_mfma_f32_16x16x32_bf16 v[28:31], v[212:215], v[186:189], v[28:31]\n\t" \
  "ds_read_b32 v226, v226 offset:0\n\t" \
  "v_pk_mul_f16 v235, v234, v235\n\t" \
  "v_fma_f32 v147, |v147|, v217, v147 mul:4\n\t" \
  "v_fma_f32 v146, |v146|, v216, v146 mul:4\n\t" \
  "v_mfma_f32_16x16x32_bf16 v[52:55], v[212:215], v[194:197], v[52:55]\n\t" \
  "ds_read_b32 v229, v229 offset:0\n\t" \
  "v_fma_f32 v155, |v155|, v219, v155 mul:4\n\t" \
  "v_fma_f32 v154, |v154|, v218, v154 mul:4\n\t" \
  "v_fma_f32 v153, |v153|, v221, v153 mul:4\n\t" \
  "v_mfma_f32_16x16x32_bf16 v[76:79], v[212:215], v[202:205], v[76:79]\n\t" \
  "ds_read_b64_tr_b16 v[212:213], %[a2] offset:72\n\t" \
  "v_fma_f32 v152, |v152|, v220, v152 mul:4\n\t" \
  "v_pk_fma_f16 v232, v232, %[hh], %[hh]\n\t" \
  "s_waitcnt lgkmcnt(4)\n\t" \
  "v_mfma_f32_16x16x32_bf16 v[8:11], v[208:211], v[178:181], v[8:11]\n\t" \
  "ds_read_b64_tr_b16 v[214:215], %[a2] offset:1608\n\t" \
  "v_cvt_pk_bf16_f32 v147, v146, v147\n\t" \
  "v_cvt_pk_bf16_f32 v155, v154, v155\n\t" \
  "v_cvt_pk_bf16_f32 v154, v152, v153\n\t" \
  "v_mfma_f32_16x16x32_bf16 v[32:35], v[208:211], v[186:189], v[32:35]\n\t" \
  "ds_read_b32 v228, v228 offset:0\n\t" \
  "v_rcp_f16 v233, v232\n\t" \
  "v_exp_f16 v234, v235\n\t" \
  "v_cvt_pk_f16_f32 v216, |v148|, |v149| clamp\n\t" \
  "v_mfma_f32_16x16x32_bf16 v[56:59], v[208:211], v[194:197], v[56:59]\n\t" \
  "v_cvt_pk_f16_f32 v218, |v150|, |v151| clamp\n\t" \
  "v_cvt_pk_f16_f32 v220, |v156|, |v157| clamp\n\t" \
  "v_mfma_f32_16x16x32_bf16 v[80:83], v[208:211], v[202:205], v[80:83]\n\t" \
  "ds_read_b64_tr_b16 v[208:209], %[a2] offset:128\n\t" \
  "v_cvt_pk_f16_f32 v222, |v158|, |v159| clamp\n\t" \
  "s_setprio 2\n\t" \
  "v_rcp_f16_sdwa v232, v232 dst_sel:DWORD dst_unused:UNUSED_PAD src0_sel:WORD_1\n\t" \
  "v_exp_f16_sdwa v234, v235 dst_sel:WORD_1 dst_unused:UNUSED_PRESERVE src0_sel:WORD_1\n\t" \
  "s_waitcnt lgkmcnt(2)\n\t" \
  "v_mfma_f32_16x16x32_bf16 v[12:15], v[212:215], v[178:181], v[12:15]\n\t" \
  "ds_read_b64_tr_b16 v[210:211], %[a2] offset:1664\n\t" \
  "v_and_b32_sdwa v217, %[mk], v216 dst_sel:DWORD dst_unused:UNUSED_PAD src0_sel:DWORD src1_sel:WORD_1\n\t" \
  "v_and_b32 v216, %[mk], v216\n\t" \
  "v_mfma_f32_16x16x32_bf16 v[36:39], v[212:215], v[186:189], v[36:39]\n\t" \
  "ds_read_b32 v217, v217 offset:0\n\t" \
  "v_and_b32_sdwa v219, %[mk], v218 dst_sel:DWORD dst_unused:UNUSED_PAD src0_sel:DWORD src1_sel:WORD_1\n\t" \
  "v_mfma_f32_16x16x32_bf16 v[60:63], v[212:215], v[194:197], v[60:63]\n\t" \
  "ds_read_b32 v216, v216 offset:0\n\t" \
  "v_and_b32 v218, %[mk], v218\n\t" \
  "v_and_b32_sdwa v221, %[mk], v220 dst_sel:DWORD dst_unused:UNUSED_PAD src0_sel:DWORD src1_sel:WORD_1\n\t" \
  "v_mfma_f32_16x16x32_bf16 v[84:87], v[212:215], v[202:205], v[84:87]\n\t" \
  "ds_read_b64_tr_b16 v[212:213], %[a2] offset:136\n\t" \
  "v_and_b32 v220, %[mk], v220\n\t" \
  "s_waitcnt lgkmcnt(3)\n\t" \
  "v_mfma_f32_16x16x32_bf16 v[16:19], v[208:211], v[178:181], v[16:19]\n\t" \
  "ds_read_b32 v246, v240 offset:33912\n\t" \
  "ds_read_b64_tr_b16 v[214:215], %[a2] offset:1672\n\t" \
  "ds_read_b32 v219, v219 offset:0\n\t" \
  "v_and_b32_sdwa v223, %[mk], v222 dst_sel:DWORD dst_unused:UNUSED_PAD src0_sel:DWORD src1_sel:WORD_1\n\t" \
  "v_mfma_f32_16x16x32_bf16 v[40:43], v[208:211], v[186:189], v[40:43]\n\t" \
  "ds_read_b32 v218, v218 offset:0\n\t" \
  "v_and_b32 v222, %[mk], v222\n\t" \
  "v_fma_f32 v163, |v163|, v225, v163 mul:4\n\t" \
  "v_mfma_f32_16x16x32_bf16 v[64:67], v[208:211], v[194:197], v[64:67]\n\t" \
  "ds_read_b32 v221, v221 offset:0\n\t" \
  "v_fma_f32 v162, |v162|, v224, v162 mul:4\n\t" \
  "v_fma_f32 v171, |v171|, v227, v171 mul:4\n\t" \
  "v_mfma_f32_16x16x32_bf16 v[88:91], v[208:211], v[202:205], v[88:91]\n\t" \
  "s_waitcnt lgkmcnt(4)\n\t" \
  "v_cmp_gt_u32 vcc, %[tg], v246\n\t" \
  "s_cbranch_vccnz .Lcnx_w18\n.Lcnx_b18:\n\t" \
  "ds_read_b64_tr_b16 v[208:209], %[a1] offset:0\n\t" \
  "ds_read_b64_tr_b16 v[210:211], %[a1] offset:1024\n\t" \
  "v_fma_f32 v170, |v170|, v226, v170 mul:4\n\t" \
  "v_fma_f32 v169, |v169|, v229, v169 mul:4\n\t" \
  "s_waitcnt lgkmcnt(5)\n\t" \
  "v_mfma_f32_16x16x32_bf16 v[20:23], v[212:215], v[178:181], v[20:23]\n\t" \
  "s_waitcnt vmcnt(1)\n\t" \
  "s_mov_b64 exec, 1\n\t" \
  "ds_inc_u32 v240, %[h2] offset:33916\n\t" \
  "s_mov_b64 exec, -1\n\t" \
  "s_cmp_lg_u32 %[wq], 0\n\t" \
  "s_cbranch_scc1 .Lcnx_c48\n\t" \
  "s_add_u32 m0, %[wl], %[ws]\n\t" \
  "s_nop 0\n\t" \
  "buffer_load_dwordx4 %[wv], %[wrs], %[so0] offen lds\n\t" \
  "buffer_load_dwordx4 %[wv], %[wrs], %[so1] offen offset:1024 lds\n\t" \
  "buffer_load_dwordx4 %[wv], %[wrs], %[so2] offen offset:2048 lds\n\t" \
  "s_add_u32 %[so0], %[so0], %[wc]\n\t" \
  "s_add_u32 %[so1], %[so1], %[wc]\n\t" \
  "s_add_u32 %[so2], %[so2], %[wc]\n\t" \
  ".Lcnx_c48:\n\t" \
  "s_cmp_lg_u32 %[rnd], 0\n\t" \
  "s_cbranch_scc1 .Lcnx_c49\n\t" \
  "s_add_u32 m0, %[xl], 65600\n\t" \
  "s_add_u32 %[xo], %[xo], %[rs]\n\t" \
  "buffer_load_dwordx4 %[xv], %[xrs], %[xo] offen lds\n\t" \
  "s_cmp_lg_u32 %[w512], 2560\n\t" \
  "s_cbranch_scc1 .Lcnx_c49\n\t" \
  "s_mov_b32 m0, %[pf]\n\t" \
  "s_add_u32 %[t0], %[xo], %[wb]\n\t" \
  "buffer_load_dword %[wv], %[xrs], %[t0] offen lds\n\t" \
  ".Lcnx_c49:\n\t" \
  "ds_read_b32 v220, v220 offset:0\n\t" \
  "v_fma_f32 v168, |v168|, v228, v168 mul:4\n\t" \
  "v_fma_mix_f32 v144, v144, v233, 0 op_sel_hi:[0,1,0]\n\t" \
  "v_fma_mix_f32 v145, v145, v232, 0 op_sel_hi:[0,1,0]\n\t" \
  "v_mfma_f32_16x16x32_bf16 v[44:47], v[212:215], v[186:189], v[44:47]\n\t" \
  "ds_read_b32 v223, v223 offset:0\n\t" \
  "v_pk_fma_f16 v234, v234, %[hh], %[hh]\n\t" \
  "v_cvt_pk_bf16_f32 v146, v144, v145\n\t" \
  "v_cvt_pk_bf16_f32 v163, v162, v163\n\t" \
  "v_mfma_f32_16x16x32_bf16 v[68:71], v[212:215], v[194:197], v[68:71]\n\t" \
  "ds_read_b32 v222, v222 offset:0\n\t" \
  "v_cvt_pk_bf16_f32 v171, v170, v171\n\t" \
  "v_cvt_pk_bf16_f32 v170, v168, v169\n\t" \
  "v_rcp_f16 v235, v234\n\t" \
  "s_setprio 1\n\t" \
  "v_mfma_f32_16x16x32_bf16 v[92:95], v[212:215], v[202:205], v[92:95]\n\t" \
  "ds_read_b64_tr_b16 v[212:213], %[a1] offset:3072\n\t" \
  "v_cvt_pk_f16_f32 v224, |v164|, |v165| clamp\n\t" \
  "v_cvt_pk_f16_f32 v226, |v166|, |v167| clamp\n\t" \
  "s_waitcnt lgkmcnt(5)\n\t" \
  "v_mfma_f32_16x16x32_bf16 v[176:179], v[208:211], v[96:99], 0\n\t" \
  "ds_read_b64_tr_b16 v[214:215], %[a1] offset:4096\n\t" \
  "v_cvt_pk_f16_f32 v228, |v172|, |v173| clamp\n\t" \
  "v_cvt_pk_f16_f32 v230, |v174|, |v175| clamp\n\t" \
  "v_rcp_f16_sdwa v234, v234 dst_sel:DWORD dst_unused:UNUSED_PAD src0_sel:WORD_1\n\t" \
  "v_mfma_f32_16x16x32_bf16 v[184:187], v[208:211], v[108:111], 0\n\t" \
  "v_and_b32_sdwa v225, %[mk], v224 dst_sel:DWORD dst_unused:UNUSED_PAD src0_sel:DWORD src1_sel:WORD_1\n\t" \
  "v_and_b32 v224, %[mk], v224\n\t" \
  "v_mfma_f32_16x16x32_bf16 v[192:195], v[208:211], v[120:123], 0\n\t" \
  "ds_read_b32 v225, v225 offset:0\n\t" \
  "v_and_b32_sdwa v227, %[mk], v226 dst_sel:DWORD dst_unused:UNUSED_PAD src0_sel:DWORD src1_sel:WORD_1\n\t" \
  "v_mfma_f32_16x16x32_bf16 v[200:203], v[208:211], v[132:135], 0\n\t" \
  "ds_read_b64_tr_b16 v[208:209], %[a1] offset:2048\n\t" \
  "v_and_b32 v226, %[mk], v226\n\t" \
  "v_and_b32_sdwa v229, %[mk], v228 dst_sel:DWORD dst_unused:UNUSED_PAD src0_sel:DWORD src1_sel:WORD_1\n\t" \
  "s_waitcnt lgkmcnt(2)\n\t" \
  "v_mfma_f32_16x16x32_bf16 v[176:179], v[212:215], v[100:103], v[176:179]\n\t" \
  "ds_read_b64_tr_b16 v[210:211], %[a1] offset:5120\n\t" \
  "v_and_b32 v228, %[mk], v228\n\t" \
  "v_mfma_f32_16x16x32_bf16 v[184:187], v[212:215], v[112:115], v[184:187]\n\t" \
  "ds_read_b32 v224, v224 offset:0\n\t" \
  "ds_read_b32 v227, v227 offset:0\n\t" \
  "v_and_b32_sdwa v231, %[mk], v230 dst_sel:DWORD dst_unused:UNUSED_PAD src0_sel:DWORD src1_sel:WORD_1\n\t" \
  "v_mfma_f32_16x16x32_bf16 v[192:195], v[212:215], v[124:127], v[192:195]\n\t" \
  "ds_read_b32 v226, v226 offset:0\n\t" \
  "v_and_b32 v230, %[mk], v230\n\t" \
  "v_fma_f32 v149, |v149|, v217, v149 mul:4\n\t" \
  "v_mfma_f32_16x16x32_bf16 v[200:203], v[212:215], v[136:139], v[200:203]\n\t" \
  "ds_read_b64_tr_b16 v[212:213], %[a1] offset:512\n\t" \
  "ds_read_b64_tr_b16 v[214:215], %[a1] offset:1536\n\t" \
  "v_fma_f32 v148, |v148|, v216, v148 mul:4\n\t" \
  "v_fma_f32 v151, |v151|, v219, v151 mul:4\n\t" \
  "s_waitcnt lgkmcnt(5)\n\t" \
  "v_mfma_f32_16x16x32_bf16 v[176:179], v[208:211], v[104:107], v[176:179]\n\t" \
  "ds_read_b32 v229, v229 offset:0\n\t" \
  "ds_read_b32 v228, v228 offset:0\n\t" \
  "v_fma_f32 v150, |v150|, v218, v150 mul:4\n\t" \
  "v_fma_f32 v157, |v157|, v221, v157 mul:4\n\t" \
  "v_mfma_f32_16x16x32_bf16 v[184:187], v[208:211], v[116:119], v[184:187]\n\t" \
  "ds_read_b32 v231, v231 offset:0\n\t" \
  "v_fma_f32 v156, |v156|, v220, v156 mul:4\n\t" \
  "v_fma_f32 v159, |v159|, v223, v159 mul:4\n\t" \
  "v_fma_f32 v158, |v158|, v222, v158 mul:4\n\t" \
  "v_mfma_f32_16x16x32_bf16 v[192:195], v[208:211], v[128:131], v[192:195]\n\t" \
  "ds_read_b32 v230, v230 offset:0\n\t" \
  "v_fma_mix_f32 v160, v160, v235, 0 op_sel_hi:[0,1,0]\n\t" \
  "v_fma_mix_f32 v161, v161, v234, 0 op_sel_hi:[0,1,0]\n\t" \
  "v_cvt_pk_bf16_f32 v162, v160, v161\n\t" \
  "v_mfma_f32_16x16x32_bf16 v[200:203], v[208:211], v[140:143], v[200:203]\n\t" \
  "ds_read_b64_tr_b16 v[208:209], %[a1] offset:3584\n\t" \
  "v_cvt_pk_bf16_f32 v148, v148, v149\n\t" \
  "v_cvt_pk_bf16_f32 v149, v150, v151\n\t" \
  "s_waitcnt lgkmcnt(5)\n\t" \
  "v_mfma_f32_16x16x32_bf16 v[180:183], v[212:215], v[96:99], 0\n\t" \
  "ds_read_b64_tr_b16 v[210:211], %[a1] offset:4608\n\t" \
  "s_setprio 0\n\t" \
  "v_cvt_pk_bf16_f32 v156, v156, v157\n\t" \
  "v_cvt_pk_bf16_f32 v157, v158, v159\n\t" \
  "v_cvt_pk_f16_f32 v216, |v178|, |v179| clamp\n\t" \
  "v_mfma_f32_16x16x32_bf16 v[188:191], v[212:215], v[108:111], 0\n\t" \
  "v_cvt_pk_f16_f32 v232, v176, v177\n\t" \
  "v_cvt_pk_f16_f32 v218, |v186|, |v187| clamp\n\t" \
  "v_cvt_pk_f16_f32 v220, |v184|, |v185| clamp\n\t" \
  "v_mfma_f32_16x16x32_bf16 v[196:199], v[212:215], v[120:123], 0\n\t" \
  "v_pk_mul_f16 v233, v232, v232\n\t" \
  "v_and_b32_sdwa v217, %[mk], v216 dst_sel:DWORD dst_unused:UNUSED_PAD src0_sel:DWORD src1_sel:WORD_1\n\t" \
  "v_mfma_f32_16x16x32_bf16 v[204:207], v[212:215], v[132:135], 0\n\t" \
  "ds_read_b64_tr_b16 v[212:213], %[a1] offset:2560\n\t" \
  "v_and_b32 v216, %[mk], v216\n\t" \
  "v_and_b32_sdwa v219, %[mk], v218 dst_sel:DWORD dst_unused:UNUSED_PAD src0_sel:DWORD src1_sel:WORD_1\n\t" \
  "s_waitcnt lgkmcnt(1)\n\t" \
  "v_mfma_f32_16x16x32_bf16 v[180:183], v[208:211], v[100:103], v[180:183]\n\t" \
  "ds_read_b32 v246, v240 offset:33916\n\t" \
  "ds_read_b64_tr_b16 v[214:215], %[a1] offset:5632\n\t" \
  "v_and_b32 v218, %[mk], v218\n\t" \
  "v_mfma_f32_16x16x32_bf16 v[188:191], v[208:211], v[112:115], v[188:191]\n\t" \
  "ds_read_b32 v217, v217 offset:0\n\t" \
  "v_and_b32_sdwa v221, %[mk], v220 dst_sel:DWORD dst_unused:UNUSED_PAD src0_sel:DWORD src1_sel:WORD_1\n\t" \
  "v_mfma_f32_16x16x32_bf16 v[196:199], v[208:211], v[124:127], v[196:199]\n\t" \
  "ds_read_b32 v216, v216 offset:0\n\t" \
  "v_and_b32 v220, %[mk], v220\n\t" \
  "v_pk_fma_f16 v233, v233, %[h2], %[h1]\n\t" \
  "v_mfma_f32_16x16x32_bf16 v[204:207], v[208:211], v[136:139], v[204:207]\n\t" \
  "s_waitcnt lgkmcnt(3)\n\t" \
  "v_cmp_gt_u32 vcc, %[tg], v246\n\t" \
  "s_cbranch_vccnz .Lcnx_w19\n.Lcnx_b19:\n\t" \
  "ds_read_b64_tr_b16 v[208:209], %[a2] offset:6160\n\t" \
  "ds_read_b64_tr_b16 v[210:211], %[a2] offset:7696\n\t" \
  "v_pk_mul_f16 v233, v232, v233\n\t" \
  "v_fma_f32 v165, |v165|, v225, v165 mul:4\n\t" \
  "s_waitcnt lgkmcnt(4)\n\t" \
  "v_mfma_f32_16x16x32_bf16 v[180:183], v[212:215], v[104:107], v[180:183]\n\t" \
  "s_waitcnt vmcnt(1)\n\t" \
  "s_cmp_lg_u32 %[rnd], 1\n\t" \
  "s_cbranch_scc1 .Lcnx_c51\n\t" \
  "s_waitcnt vmcnt(0)\n\t" \
  ".Lcnx_c51:\n\t" \
  "s_mov_b64 exec, 1\n\t" \
  "ds_inc_u32 v240, %[h2] offset:33920\n\t" \
  "s_mov_b64 exec, -1\n\t" \
  "s_cmp_lg_u32 %[wq], 1\n\t" \
  "s_cbranch_scc1 .Lcnx_c52\n\t" \
  "s_add_u32 m0, %[wl], 0\n\t" \
  "s_nop 0\n\t" \
  "buffer_load_dwordx4 %[wv], %[wrs], %[so0] offen lds\n\t" \
  "buffer_load_dwordx4 %[wv], %[wrs], %[so1] offen offset:1024 lds\n\t" \
  "buffer_load_dwordx4 %[wv], %[wrs], %[so2] offen offset:2048 lds\n\t" \
  "s_add_u32 %[so0], %[so0], %[wc]\n\t" \
  "s_add_u32 %[so1], %[so1], %[wc]\n\t" \
  "s_add_u32 %[so2], %[so2], %[wc]\n\t" \
  ".Lcnx_c52:\n\t" \
  "ds_read_b32 v219, v219 offset:0\n\t" \
  "ds_read_b32 v218, v218 offset:0\n\t" \
  "v_fma_f32 v164, |v164|, v224, v164 mul:4\n\t" \
  "v_fma_f32 v167, |v167|, v227, v167 mul:4\n\t" \
  "v_mfma_f32_16x16x32_bf16 v[188:191], v[212:215], v[116:119], v[188:191]\n\t" \
  "ds_read_b32 v221, v221 offset:0\n\t" \
  "v_fma_f32 v166, |v166|, v226, v166 mul:4\n\t" \
  "v_fma_f32 v173, |v173|, v229, v173 mul:4\n\t" \
  "v_fma_f32 v172, |v172|, v228, v172 mul:4\n\t" \
  "v_mfma_f32_16x16x32_bf16 v[196:199], v[212:215], v[128:131], v[196:199]\n\t" \
  "ds_read_b32 v220, v220 offset:0\n\t" \
  "v_fma_f32 v175, |v175|, v231, v175 mul:4\n\t" \
  "v_fma_f32 v174, |v174|, v230, v174 mul:4\n\t" \
  "v_cvt_pk_bf16_f32 v164, v164, v165\n\t" \
  "v_mfma_f32_16x16x32_bf16 v[204:207], v[212:215], v[140:143], v[204:207]\n\t" \
  "ds_read_b64_tr_b16 v[212:213], %[a2] offset:6168\n\t" \
  "v_cvt_pk_bf16_f32 v165, v166, v167\n\t" \
  "v_cvt_pk_bf16_f32 v172, v172, v173\n\t" \
  "v_cvt_pk_bf16_f32 v173, v174, v175\n\t" \
  "s_setprio 2\n\t" \
  "v_exp_f16 v232, v233\n\t" \
  "v_cvt_pk_f16_f32 v224, |v194|, |v195| clamp\n\t" \
  "v_mfma_f32_16x16x32_bf16 v[64:67], v[242:245], v[236:239], v[64:67]\n\t" \
  "ds_read_b64_tr_b16 v[214:215], %[a2] offset:7704\n\t" \
  "v_cvt_pk_f16_f32 v234, v192, v193\n\t" \
  "v_cvt_pk_f16_f32 v226, |v202|, |v203| clamp\n\t" \
  "v_mfma_f32_16x16x32_bf16 v[68:71], v[240:243], v[236:239], v[68:71]\n\t" \
  "buffer_load_dwordx4 v[236:239], %[vo], %[xrs], %[oc1] offen offset:192\n\t" \
  "v_cvt_pk_f16_f32 v228, |v200|, |v201| clamp\n\t" \
  "v_exp_f16_sdwa v232, v233 dst_sel:WORD_1 dst_unused:UNUSED_PRESERVE src0_sel:WORD_1\n\t" \
  "v_pk_mul_f16 v235, v234, v234\n\t" \
  "s_waitcnt lgkmcnt(7)\n\t" \
  "v_mfma_f32_16x16x32_bf16 v[0:3], v[208:211], v[146:149], v[0:3]\n\t" \
  "v_and_b32_sdwa v225, %[mk], v224 dst_sel:DWORD dst_unused:UNUSED_PAD src0_sel:DWORD src1_sel:WORD_1\n\t" \
  "v_and_b32 v224, %[mk], v224\n\t" \
  "v_mfma_f32_16x16x32_bf16 v[24:27], v[208:211], v[154:157], v[24:27]\n\t" \
  "ds_read_b32 v225, v225 offset:0\n\t" \
  "v_and_b32_sdwa v227, %[mk], v226 dst_sel:DWORD dst_unused:UNUSED_PAD src0_sel:DWORD src1_sel:WORD_1\n\t" \
  "v_mfma_f32_16x16x32_bf16 v[48:51], v[208:211], v[162:165], v[48:51]\n\t" \
  "ds_read_b32 v224, v224 offset:0\n\t" \
  "v_and_b32 v226, %[mk], v226\n\t" \
  "v_and_b32_sdwa v229, %[mk], v228 dst_sel:DWORD dst_unused:UNUSED_PAD src0_sel:DWORD src1_sel:WORD_1\n\t" \
  "v_mfma_f32_16x16x32_bf16 v[72:75], v[208:211], v[170:173], v[72:75]\n\t" \
  "ds_read_b64_tr_b16 v[208:209], %[a2] offset:6224\n\t" \
  "v_and_b32 v228, %[mk], v228\n\t" \
  "s_waitcnt lgkmcnt(3)\n\t" \
  "v_mfma_f32_16x16x32_bf16 v[4:7], v[212:215], v[146:149], v[4:7]\n\t" \
  "ds_read_b64_tr_b16 v[210:211], %[a2] offset:7760\n\t" \
  "ds_read_b32 v227, v227 offset:0\n\t" \
  "v_pk_fma_f16 v235, v235, %[h2], %[h1]\n\t" \
  "v_mfma_f32_16x16x32_bf16 v[28:31], v[212:215], v[154:157], v[28:31]\n\t" \
  "ds_read_b32 v226, v226 offset:0\n\t" \
  "v_pk_mul_f16 v235, v234, v235\n\t" \
  "v_fma_f32 v179, |v179|, v217, v179 mul:4\n\t" \
  "v_fma_f32 v178, |v178|, v216, v178 mul:4\n\t" \
  "v_mfma_f32_16x16x32_bf16 v[52:55], v[212:215], v[162:165], v[52:55]\n\t" \
  "ds_read_b32 v229, v229 offset:0\n\t" \
  "v_fma_f32 v187, |v187|, v219, v187 mul:4\n\t" \
  "v_fma_f32 v186, |v186|, v218, v186 mul:4\n\t" \
  "v_fma_f32 v185, |v185|, v221, v185 mul:4\n\t" \
  "v_mfma_f32_16x16x32_bf16 v[76:79], v[212:215], v[170:173], v[76:79]\n\t" \
  "ds_read_b64_tr_b16 v[212:213], %[a2] offset:6232\n\t" \
  "v_fma_f32 v184, |v184|, v220, v184 mul:4\n\t" \
  "v_pk_fma_f16 v232, v232, %[hh], %[hh]\n\t" \
  "s_waitcnt lgkmcnt(4)\n\t" \
  "v_mfma_f32_16x16x32_bf16 v[8:11], v[208:211], v[146:149], v[8:11]\n\t" \
  "ds_read_b64_tr_b16 v[214:215], %[a2] offset:7768\n\t" \
  "v_cvt_pk_bf16_f32 v179, v178, v179\n\t" \
  "v_cvt_pk_bf16_f32 v187, v186, v187\n\t" \
  "v_cvt_pk_bf16_f32 v186, v184, v185\n\t" \
  "v_mfma_f32_16x16x32_bf16 v[32:35], v[208:211], v[154:157], v[32:35]\n\t" \
  "ds_read_b32 v228, v228 offset:0\n\t" \
  "v_rcp_f16 v233, v232\n\t" \
  "v_exp_f16 v234, v235\n\t" \
  "v_cvt_pk_f16_f32 v216, |v180|, |v181| clamp\n\t" \
  "v_mfma_f32_16x16x32_bf16 v[56:59], v[208:211], v[162:165], v[56:59]\n\t" \
  "v_cvt_pk_f16_f32 v218, |v182|, |v183| clamp\n\t" \
  "v_cvt_pk_f16_f32 v220, |v188|, |v189| clamp\n\t" \
  "v_mfma_f32_16x16x32_bf16 v[80:83], v[208:211], v[170:173], v[80:83]\n\t" \
  "ds_read_b64_tr_b16 v[208:209], %[a2] offset:6288\n\t" \
  "v_cvt_pk_f16_f32 v222, |v190|, |v191| clamp\n\t" \
  "s_setprio 2\n\t" \
  "v_rcp_f16_sdwa v232, v232 dst_sel:DWORD dst_unused:UNUSED_PAD src0_sel:WORD_1\n\t" \
  "v_exp_f16_sdwa v234, v235 dst_sel:WORD_1 dst_unused:UNUSED_PRESERVE src0_sel:WORD_1\n\t" \
  "s_waitcnt lgkmcnt(2)\n\t" \
  "v_mfma_f32_16x16x32_bf16 v[12:15], v[212:215], v[146:149], v[12:15]\n\t" \
  "ds_read_b64_tr_b16 v[210:211], %[a2] offset:7824\n\t" \
  "v_and_b32_sdwa v217, %[mk], v216 dst_sel:DWORD dst_unused:UNUSED_PAD src0_sel:DWORD src1_sel:WORD_1\n\t" \
  "v_and_b32 v216, %[mk], v216\n\t" \
  "v_mfma_f32_16x16x32_bf16 v[36:39], v[212:215], v[154:157], v[36:39]\n\t" \
  "ds_read_b32 v217, v217 offset:0\n\t" \
  "v_and_b32_sdwa v219, %[mk], v218 dst_sel:DWORD dst_unused:UNUSED_PAD src0_sel:DWORD src1_sel:WORD_1\n\t" \
  "v_mfma_f32_16x16x32_bf16 v[60:63], v[212:215], v[162:165], v[60:63]\n\t" \
  "ds_read_b32 v216, v216 offset:0\n\t" \
  "v_and_b32 v218, %[mk], v218\n\t" \
  "v_and_b32_sdwa v221, %[mk], v220 dst_sel:DWORD dst_unused:UNUSED_PAD src0_sel:DWORD src1_sel:WORD_1\n\t" \
  "v_mfma_f32_16x16x32_bf16 v[84:87], v[212:215], v[170:173], v[84:87]\n\t" \
  "ds_read_b64_tr_b16 v[212:213], %[a2] offset:6296\n\t" \
  "v_and_b32 v220, %[mk], v220\n\t" \
  "s_waitcnt lgkmcnt(3)\n\t" \
  "v_mfma_f32_16x16x32_bf16 v[16:19], v[208:211], v[146:149], v[16:19]\n\t" \
  "ds_read_b32 v246, v240 offset:33920\n\t" \
  "ds_read_b64_tr_b16 v[214:215], %[a2] offset:7832\n\t" \
  "ds_read_b32 v219, v219 offset:0\n\t" \
  "v_and_b32_sdwa v223, %[mk], v222 dst_sel:DWORD dst_unused:UNUSED_PAD src0_sel:DWORD src1_sel:WORD_1\n\t" \
  "v_mfma_f32_16x16x32_bf16 v[40:43], v[208:211], v[154:157], v[40:43]\n\t" \
  "ds_read_b32 v218, v218 offset:0\n\t" \
  "v_and_b32 v222, %[mk], v222\n\t" \
  "v_fma_f32 v195, |v195|, v225, v195 mul:4\n\t" \
  "v_mfma_f32_16x16x32_bf16 v[64:67], v[208:211], v[162:165], v[64:67]\n\t" \
  "ds_read_b32 v221, v221 offset:0\n\t" \
  "v_fma_f32 v194, |v194|, v224, v194 mul:4\n\t" \
  "v_fma_f32 v203, |v203|, v227, v203 mul:4\n\t" \
  "v_mfma_f32_16x16x32_bf16 v[88:91], v[208:211], v[170:173], v[88:91]\n\t" \
  "s_waitcnt lgkmcnt(4)\n\t" \
  "v_cmp_gt_u32 vcc, %[tg], v246\n\t" \
  "s_cbranch_vccnz .Lcnx_w20\n.Lcnx_b20:\n\t" \
  "ds_read_b64_tr_b16 v[208:209], %[a1] offset:6144\n\t" \
  "ds_read_b64_tr_b16 v[210:211], %[a1] offset:7168\n\t" \
  "v_fma_f32 v202, |v202|, v226, v202 mul:4\n\t" \
  "v_fma_f32 v201, |v201|, v229, v201 mul:4\n\t" \
  "s_waitcnt lgkmcnt(5)\n\t" \
  "v_mfma_f32_16x16x32_bf16 v[20:23], v[212:215], v[146:149], v[20:23]\n\t" \
  "s_waitcnt vmcnt(1)\n\t" \
  "s_mov_b64 exec, 1\n\t" \
  "ds_inc_u32 v240, %[h2] offset:33924\n\t" \
  "s_mov_b64 exec, -1\n\t" \
  "s_cmp_lg_u32 %[wq], 0\n\t" \
  "s_cbranch_scc1 .Lcnx_c53\n\t" \
  "s_add_u32 m0, %[wl], 0\n\t" \
  "s_nop 0\n\t" \
  "buffer_load_dwordx4 %[wv], %[wrs], %[so0] offen lds\n\t" \
  "buffer_load_dwordx4 %[wv], %[wrs], %[so1] offen offset:1024 lds\n\t" \
  "buffer_load_dwordx4 %[wv], %[wrs], %[so2] offen offset:2048 lds\n\t" \
  "s_add_u32 %[so0], %[so0], %[wc]\n\t" \
  "s_add_u32 %[so1], %[so1], %[wc]\n\t" \
  "s_add_u32 %[so2], %[so2], %[wc]\n\t" \
  "s_sub_u32 %[so0], %[so0], 768\n\t" \
  "s_sub_u32 %[so1], %[so1], 768\n\t" \
  "s_sub_u32 %[so2], %[so2], 768\n\t" \
  ".Lcnx_c53:\n\t" \
  "s_cmp_lg_u32 %[rnd], 0\n\t" \
  "s_cbranch_scc1 .Lcnx_c54\n\t" \
  "s_add_u32 m0, %[xl], 73800\n\t" \
  "s_add_u32 %[xo], %[xo], %[rs]\n\t" \
  "buffer_load_dwordx4 %[xv], %[xrs], %[xo] offen lds\n\t" \
  "s_cmp_lg_u32 %[w512], 3072\n\t" \
  "s_cbranch_scc1 .Lcnx_c54\n\t" \
  "s_mov_b32 m0, %[pf]\n\t" \
  "s_add_u32 %[t0], %[xo], %[wb]\n\t" \
  "buffer_load_dword %[wv], %[xrs], %[t0] offen lds\n\t" \
  ".Lcnx_c54:\n\t" \
  "ds_read_b32 v220, v220 offset:0\n\t" \
  "v_fma_f32 v200, |v200|, v228, v200 mul:4\n\t" \
  "v_fma_mix_f32 v176, v176, v233, 0 op_sel_hi:[0,1,0]\n\t" \
  "v_fma_mix_f32 v177, v177, v232, 0 op_sel_hi:[0,1,0]\n\t" \
  "v_mfma_f32_16x16x32_bf16 v[44:47], v[212:215], v[154:157], v[44:47]\n\t" \
  "ds_read_b32 v223, v223 offset:0\n\t" \
  "v_pk_fma_f16 v234, v234, %[hh], %[hh]\n\t" \
  "v_cvt_pk_bf16_f32 v178, v176, v177\n\t" \
  "v_cvt_pk_bf16_f32 v195, v194, v195\n\t" \
  "v_mfma_f32_16x16x32_bf16 v[68:71], v[212:215], v[162:165], v[68:71]\n\t" \
  "ds_read_b32 v222, v222 offset:0\n\t" \
  "v_cvt_pk_bf16_f32 v203, v202, v203\n\t" \
  "v_cvt_pk_bf16_f32 v202, v200, v201\n\t" \
  "v_rcp_f16 v235, v234\n\t" \
  "s_setprio 1\n\t" \
  "v_mfma_f32_16x16x32_bf16 v[92:95], v[212:215], v[170:173], v[92:95]\n\t" \
  "ds_read_b64_tr_b16 v[212:213], %[a1] offset:9216\n\t" \
  "v_cvt_pk_f16_f32 v224, |v196|, |v197| clamp\n\t" \
  "v_cvt_pk_f16_f32 v226, |v198|, |v199| clamp\n\t" \
  "s_waitcnt lgkmcnt(5)\n\t" \
  "v_mfma_f32_16x16x32_bf16 v[144:147], v[208:211], v[96:99], 0\n\t" \
  "ds_read_b64_tr_b16 v[214:215], %[a1] offset:10240\n\t" \
  "v_cvt_pk_f16_f32 v228, |v204|, |v205| clamp\n\t" \
  "v_cvt_pk_f16_f32 v230, |v206|, |v207| clamp\n\t" \
  "v_rcp_f16_sdwa v234, v234 dst_sel:DWORD dst_unused:UNUSED_PAD src0_sel:WORD_1\n\t" \
  "v_mfma_f32_16x16x32_bf16 v[152:155], v[208:211], v[108:111], 0\n\t" \
  "v_and_b32_sdwa v225, %[mk], v224 dst_sel:DWORD dst_unused:UNUSED_PAD src0_sel:DWORD src1_sel:WORD_1\n\t" \
  "v_and_b32 v224, %[mk], v224\n\t" \
  "v_mfma_f32_16x16x32_bf16 v[160:163], v[208:211], v[120:123], 0\n\t" \
  "ds_read_b32 v225, v225 offset:0\n\t" \
  "v_and_b32_sdwa v227, %[mk], v226 dst_sel:DWORD dst_unused:UNUSED_PAD src0_sel:DWORD src1_sel:WORD_1\n\t" \
  "v_mfma_f32_16x16x32_bf16 v[168:171], v[208:211], v[132:135], 0\n\t" \
  "ds_read_b64_tr_b16 v[208:209], %[a1] offset:8192\n\t" \
  "v_and_b32 v226, %[mk], v226\n\t" \
  "v_and_b32_sdwa v229, %[mk], v228 dst_sel:DWORD dst_unused:UNUSED_PAD src0_sel:DWORD src1_sel:WORD_1\n\t" \
  "s_waitcnt lgkmcnt(2)\n\t" \
  "v_mfma_f32_16x16x32_bf16 v[144:147], v[212:215], v[100:103], v[144:147]\n\t" \
  "ds_read_b64_tr_b16 v[210:211], %[a1] offset:11264\n\t" \
  "v_and_b32 v228, %[mk], v228\n\t" \
  "v_mfma_f32_16x16x32_bf16 v[152:155], v[212:215], v[112:115], v[152:155]\n\t" \
  "ds_read_b32 v224, v224 offset:0\n\t" \
  "ds_read_b32 v227, v227 offset:0\n\t" \
  "v_and_b32_sdwa v231, %[mk], v230 dst_sel:DWORD dst_unused:UNUSED_PAD src0_sel:DWORD src1_sel:WORD_1\n\t" \
  "v_mfma_f32_16x16x32_bf16 v[160:163], v[212:215], v[124:127], v[160:163]\n\t" \
  "ds_read_b32 v226, v226 offset:0\n\t" \
  "v_and_b32 v230, %[mk], v230\n\t" \
  "v_fma_f32 v181, |v181|, v217, v181 mul:4\n\t" \
  "v_mfma_f32_16x16x32_bf16 v[168:171], v[212:215], v[136:139], v[168:171]\n\t" \
  "ds_read_b64_tr_b16 v[212:213], %[a1] offset:6656\n\t" \
  "ds_read_b64_tr_b16 v[214:215], %[a1] offset:7680\n\t" \
  "v_fma_f32 v180, |v180|, v216, v180 mul:4\n\t" \
  "v_fma_f32 v183, |v183|, v219, v183 mul:4\n\t" \
  "s_waitcnt lgkmcnt(5)\n\t" \
  "v_mfma_f32_16x16x32_bf16 v[144:147], v[208:211], v[104:107], v[144:147]\n\t" \
  "ds_read_b32 v229, v229 offset:0\n\t" \
  "ds_read_b32 v228, v228 offset:0\n\t" \
  "v_fma_f32 v182, |v182|, v218, v182 mul:4\n\t" \
  "v_fma_f32 v189, |v189|, v221, v189 mul:4\n\t" \
  "v_mfma_f32_16x16x32_bf16 v[152:155], v[208:211], v[116:119], v[152:155]\n\t" \
  "ds_read_b32 v231, v231 offset:0\n\t" \
  "v_fma_f32 v188, |v188|, v220, v188 mul:4\n\t" \
  "v_fma_f32 v191, |v191|, v223, v191 mul:4\n\t" \
  "v_fma_f32 v190, |v190|, v222, v190 mul:4\n\t" \
  "v_mfma_f32_16x16x32_bf16 v[160:163], v[208:211], v[128:131], v[160:163]\n\t" \
  "ds_read_b32 v230, v230 offset:0\n\t" \
  "v_fma_mix_f32 v192, v192, v235, 0 op_sel_hi:[0,1,0]\n\t" \
  "v_fma_mix_f32 v193, v193, v234, 0 op_sel_hi:[0,1,0]\n\t" \
  "v_cvt_pk_bf16_f32 v194, v192, v193\n\t" \
  "v_mfma_f32_16x16x32_bf16 v[168:171], v[208:211], v[140:143], v[168:171]\n\t" \
  "ds_read_b64_tr_b16 v[208:209], %[a1] offset:9728\n\t" \
  "v_cvt_pk_bf16_f32 v180, v180, v181\n\t" \
  "v_cvt_pk_bf16_f32 v181, v182, v183\n\t" \
  "s_waitcnt lgkmcnt(5)\n\t" \
  "v_mfma_f32_16x16x32_bf16 v[148:151], v[212:215], v[96:99], 0\n\t" \
  "ds_read_b64_tr_b16 v[210:211], %[a1] offset:10752\n\t" \
  "s_setprio 0\n\t" \
  "v_cvt_pk_bf16_f32 v188, v188, v189\n\t" \
  "v_cvt_pk_bf16_f32 v189, v190, v191\n\t" \
  "v_cvt_pk_f16_f32 v216, |v146|, |v147| clamp\n\t" \
  "v_mfma_f32_16x16x32_bf16 v[156:159], v[212:215], v[108:111], 0\n\t" \
  "v_cvt_pk_f16_f32 v232, v144, v145\n\t" \
  "v_cvt_pk_f16_f32 v218, |v154|, |v155| clamp\n\t" \
  "v_cvt_pk_f16_f32 v220, |v152|, |v153| clamp\n\t" \
  "v_mfma_f32_16x16x32_bf16 v[164:167], v[212:215], v[120:123], 0\n\t" \
  "v_pk_mul_f16 v233, v232, v232\n\t" \
  "v_and_b32_sdwa v217, %[mk], v216 dst_sel:DWORD dst_unused:UNUSED_PAD src0_sel:DWORD src1_sel:WORD_1\n\t" \
  "v_mfma_f32_16x16x32_bf16 v[172:175], v[212:215], v[132:135], 0\n\t" \
  "ds_read_b64_tr_b16 v[212:213], %[a1] offset:8704\n\t" \
  "v_and_b32 v216, %[mk], v216\n\t" \
  "v_and_b32_sdwa v219, %[mk], v218 dst_sel:DWORD dst_unused:UNUSED_PAD src0_sel:DWORD src1_sel:WORD_1\n\t" \
  "s_waitcnt lgkmcnt(1)\n\t" \
  "v_mfma_f32_16x16x32_bf16 v[148:151], v[208:211], v[100:103], v[148:151]\n\t" \
  "ds_read_b32 v246, v240 offset:33924\n\t" \
  "ds_read_b64_tr_b16 v[214:215], %[a1] offset:11776\n\t" \
  "v_and_b32 v218, %[mk], v218\n\t" \
  "v_mfma_f32_16x16x32_bf16 v[156:159], v[208:211], v[112:115], v[156:159]\n\t" \
  "ds_read_b32 v217, v217 offset:0\n\t" \
  "v_and_b32_sdwa v221, %[mk], v220 dst_sel:DWORD dst_unused:UNUSED_PAD src0_sel:DWORD src1_sel:WORD_1\n\t" \
  "v_mfma_f32_16x16x32_bf16 v[164:167], v[208:211], v[124:127], v[164:167]\n\t" \
  "ds_read_b32 v216, v216 offset:0\n\t" \
  "v_and_b32 v220, %[mk], v220\n\t" \
  "v_pk_fma_f16 v233, v233, %[h2], %[h1]\n\t" \
  "v_mfma_f32_16x16x32_bf16 v[172:175], v[208:211], v[136:139], v[172:175]\n\t" \
  "s_waitcnt lgkmcnt(3)\n\t" \
  "v_cmp_gt_u32 vcc, %[tg], v246\n\t" \
  "s_cbranch_vccnz .Lcnx_w21\n.Lcnx_b21:\n\t" \
  "ds_read_b64_tr_b16 v[208:209], %[a2] offset:0\n\t" \
  "ds_read_b64_tr_b16 v[210:211], %[a2] offset:1536\n\t" \
  "v_pk_mul_f16 v233, v232, v233\n\t" \
  "v_fma_f32 v197, |v197|, v225, v197 mul:4\n\t" \
  "s_waitcnt lgkmcnt(4)\n\t" \
  "v_mfma_f32_16x16x32_bf16 v[148:151], v[212:215], v[104:107], v[148:151]\n\t" \
  "s_waitcnt vmcnt(1)\n\t" \
  "s_cmp_lg_u32 %[rnd], 1\n\t" \
  "s_cbranch_scc1 .Lcnx_c56\n\t" \
  "s_waitcnt vmcnt(0)\n\t" \
  ".Lcnx_c56:\n\t" \
  "s_mov_b64 exec, 1\n\t" \
  "ds_inc_u32 v240, %[h2] offset:33928\n\t" \
  "s_mov_b64 exec, -1\n\t" \
  "s_cmp_lg_u32 %[wq], 1\n\t" \
  "s_cbranch_scc1 .Lcnx_c57\n\t" \
  "s_add_u32 m0, %[wl], %[ws]\n\t" \
  "s_nop 0\n\t" \
  "buffer_load_dwordx4 %[wv], %[wrs], %[so0] offen lds\n\t" \
  "buffer_load_dwordx4 %[wv], %[wrs], %[so1] offen offset:1024 lds\n\t" \
  "buffer_load_dwordx4 %[wv], %[wrs], %[so2] offen offset:2048 lds\n\t" \
  "s_add_u32 %[so0], %[so0], %[wc]\n\t" \
  "s_add_u32 %[so1], %[so1], %[wc]\n\t" \
  "s_add_u32 %[so2], %[so2], %[wc]\n\t" \
  ".Lcnx_c57:\n\t" \
  "ds_read_b32 v219, v219 offset:0\n\t" \
  "ds_read_b32 v218, v218 offset:0\n\t" \
  "v_fma_f32 v196, |v196|, v224, v196 mul:4\n\t" \
  "v_fma_f32 v199, |v199|, v227, v199 mul:4\n\t" \
  "v_mfma_f32_16x16x32_bf16 v[156:159], v[212:215], v[116:119], v[156:159]\n\t" \
  "ds_read_b32 v221, v221 offset:0\n\t" \
  "v_fma_f32 v198, |v198|, v226, v198 mul:4\n\t" \
  "v_fma_f32 v205, |v205|, v229, v205 mul:4\n\t" \
  "v_fma_f32 v204, |v204|, v228, v204 mul:4\n\t" \
  "v_mfma_f32_16x16x32_bf16 v[164:167], v[212:215], v[128:131], v[164:167]\n\t" \
  "ds_read_b32 v220, v220 offset:0\n\t" \
  "v_fma_f32 v207, |v207|, v231, v207 mul:4\n\t" \
  "v_fma_f32 v206, |v206|, v230, v206 mul:4\n\t" \
  "v_cvt_pk_bf16_f32 v196, v196, v197\n\t" \
  "v_mfma_f32_16x16x32_bf16 v[172:175], v[212:215], v[140:143], v[172:175]\n\t" \
  "ds_read_b64_tr_b16 v[212:213], %[a2] offset:8\n\t" \
  "v_cvt_pk_bf16_f32 v197, v198, v199\n\t" \
  "v_cvt_pk_bf16_f32 v204, v204, v205\n\t" \
  "v_cvt_pk_bf16_f32 v205, v206, v207\n\t" \
  "s_setprio 2\n\t" \
  "v_exp_f16 v232, v233\n\t" \
  "v_cvt_pk_f16_f32 v224, |v162|, |v163| clamp\n\t" \
  "v_mfma_f32_16x16x32_bf16 v[72:75], v[242:245], v[236:239], v[72:75]\n\t" \
  "ds_read_b64_tr_b16 v[214:215], %[a2] offset:1544\n\t" \
  "v_cvt_pk_f16_f32 v234, v160, v161\n\t" \
  "v_cvt_pk_f16_f32 v226, |v170|, |v171| clamp\n\t" \
  "v_mfma_f32_16x16x32_bf16 v[76:79], v[240:243], v[236:239], v[76:79]\n\t" \
  "buffer_load_dwordx4 v[236:239], %[vo], %[xrs], %[oc1] offen offset:256\n\t" \
  "v_cvt_pk_f16_f32 v228, |v168|, |v169| clamp\n\t" \
  "v_exp_f16_sdwa v232, v233 dst_sel:WORD_1 dst_unused:UNUSED_PRESERVE src0_sel:WORD_1\n\t" \
  "v_pk_mul_f16 v235, v234, v234\n\t" \
  "s_waitcnt lgkmcnt(7)\n\t" \
  "v_mfma_f32_16x16x32_bf16 v[0:3], v[208:211], v[178:181], v[0:3]\n\t" \
  "v_and_b32_sdwa v225, %[mk], v224 dst_sel:DWORD dst_unused:UNUSED_PAD src0_sel:DWORD src1_sel:WORD_1\n\t" \
  "v_and_b32 v224, %[mk], v224\n\t" \
  "v_mfma_f32_16x16x32_bf16 v[24:27], v[208:211], v[186:189], v[24:27]\n\t" \
  "ds_read_b32 v225, v225 offset:0\n\t" \
  "v_and_b32_sdwa v227, %[mk], v226 dst_sel:DWORD dst_unused:UNUSED_PAD src0_sel:DWORD src1_sel:WORD_1\n\t" \
  "v_mfma_f32_16x16x32_bf16 v[48:51], v[208:211], v[194:197], v[48:51]\n\t" \
  "ds_read_b32 v224, v224 offset:0\n\t" \
  "v_and_b32 v226, %[mk], v226\n\t" \
  "v_and_b32_sdwa v229, %[mk], v228 dst_sel:DWORD dst_unused:UNUSED_PAD src0_sel:DWORD src1_sel:WORD_1\n\t" \
  "v_mfma_f32_16x16x32_bf16 v[72:75], v[208:211], v[202:205], v[72:75]\n\t" \
  "ds_read_b64_tr_b16 v[208:209], %[a2] offset:64\n\t" \
  "v_and_b32 v228, %[mk], v228\n\t" \
  "s_waitcnt lgkmcnt(3)\n\t" \
  "v_mfma_f32_16x16x32_bf16 v[4:7], v[212:215], v[178:181], v[4:7]\n\t" \
  "ds_read_b64_tr_b16 v[210:211], %[a2] offset:1600\n\t" \
  "ds_read_b32 v227, v227 offset:0\n\t" \
  "v_pk_fma_f16 v235, v235, %[h2], %[h1]\n\t" \
  "v_mfma_f32_16x16x32_bf16 v[28:31], v[212:215], v[186:189], v[28:31]\n\t" \
  "ds_read_b32 v226, v226 offset:0\n\t" \
  "v_pk_mul_f16 v235, v234, v235\n\t" \
  "v_fma_f32 v147, |v147|, v217, v147 mul:4\n\t" \
  "v_fma_f32 v146, |v146|, v216, v146 mul:4\n\t" \
  "v_mfma_f32_16x16x32_bf16 v[52:55], v[212:215], v[194:197], v[52:55]\n\t" \
  "ds_read_b32 v229, v229 offset:0\n\t" \
  "v_fma_f32 v155, |v155|, v219, v155 mul:4\n\t" \
  "v_fma_f32 v154, |v154|, v218, v154 mul:4\n\t" \
  "v_fma_f32 v153, |v153|, v221, v153 mul:4\n\t" \
  "v_mfma_f32_16x16x32_bf16 v[76:79], v[212:215], v[202:205], v[76:79]\n\t" \
  "ds_read_b64_tr_b16 v[212:213], %[a2] offset:72\n\t" \
  "v_fma_f32 v152, |v152|, v220, v152 mul:4\n\t" \
  "v_pk_fma_f16 v232, v232, %[hh], %[hh]\n\t" \
  "s_waitcnt lgkmcnt(4)\n\t" \
  "v_mfma_f32_16x16x32_bf16 v[8:11], v[208:211], v[178:181], v[8:11]\n\t" \
  "ds_read_b64_tr_b16 v[214:215], %[a2] offset:1608\n\t" \
  "v_cvt_pk_bf16_f32 v147, v146, v147\n\t" \
  "v_cvt_pk_bf16_f32 v155, v154, v155\n\t" \
  "v_cvt_pk_bf16_f32 v154, v152, v153\n\t" \
  "v_mfma_f32_16x16x32_bf16 v[32:35], v[208:211], v[186:189], v[32:35]\n\t" \
  "ds_read_b32 v228, v228 offset:0\n\t" \
  "v_rcp_f16 v233, v232\n\t" \
  "v_exp_f16 v234, v235\n\t" \
  "v_cvt_pk_f16_f32 v216, |v148|, |v149| clamp\n\t" \
  "v_mfma_f32_16x16x32_bf16 v[56:59], v[208:211], v[194:197], v[56:59]\n\t" \
  "v_cvt_pk_f16_f32 v218, |v150|, |v151| clamp\n\t" \
  "v_cvt_pk_f16_f32 v220, |v156|, |v157| clamp\n\t" \
  "v_mfma_f32_16x16x32_bf16 v[80:83], v[208:211], v[202:205], v[80:83]\n\t" \
  "ds_read_b64_tr_b16 v[208:209], %[a2] offset:128\n\t" \
  "v_cvt_pk_f16_f32 v222, |v158|, |v159| clamp\n\t" \
  "s_setprio 2\n\t" \
  "v_rcp_f16_sdwa v232, v232 dst_sel:DWORD dst_unused:UNUSED_PAD src0_sel:WORD_1\n\t" \
  "v_exp_f16_sdwa v234, v235 dst_sel:WORD_1 dst_unused:UNUSED_PRESERVE src0_sel:WORD_1\n\t" \
  "s_waitcnt lgkmcnt(2)\n\t" \
  "v_mfma_f32_16x16x32_bf16 v[12:15], v[212:215], v[178:181], v[12:15]\n\t" \
  "ds_read_b64_tr_b16 v[210:211], %[a2] offset:1664\n\t" \
  "v_and_b32_sdwa v217, %[mk], v216 dst_sel:DWORD dst_unused:UNUSED_PAD src0_sel:DWORD src1_sel:WORD_1\n\t" \
  "v_and_b32 v216, %[mk], v216\n\t" \
  "v_mfma_f32_16x16x32_bf16 v[36:39], v[212:215], v[186:189], v[36:39]\n\t" \
  "ds_read_b32 v217, v217 offset:0\n\t" \
  "v_and_b32_sdwa v219, %[mk], v218 dst_sel:DWORD dst_unused:UNUSED_PAD src0_sel:DWORD src1_sel:WORD_1\n\t" \
  "v_mfma_f32_16x16x32_bf16 v[60:63], v[212:215], v[194:197], v[60:63]\n\t" \
  "ds_read_b32 v216, v216 offset:0\n\t" \
  "v_and_b32 v218, %[mk], v218\n\t" \
  "v_and_b32_sdwa v221, %[mk], v220 dst_sel:DWORD dst_unused:UNUSED_PAD src0_sel:DWORD src1_sel:WORD_1\n\t" \
  "v_mfma_f32_16x16x32_bf16 v[84:87], v[212:215], v[202:205], v[84:87]\n\t" \
  "ds_read_b64_tr_b16 v[212:213], %[a2] offset:136\n\t" \
  "v_and_b32 v220, %[mk], v220\n\t" \
  "s_waitcnt lgkmcnt(3)\n\t" \
  "v_mfma_f32_16x16x32_bf16 v[16:19], v[208:211], v[178:181], v[16:19]\n\t" \
  "ds_read_b32 v246, v240 offset:33928\n\t" \
  "ds_read_b64_tr_b16 v[214:215], %[a2] offset:1672\n\t" \
  "ds_read_b32 v219, v219 offset:0\n\t" \
  "v_and_b32_sdwa v223, %[mk], v222 dst_sel:DWORD dst_unused:UNUSED_PAD src0_sel:DWORD src1_sel:WORD_1\n\t" \
  "v_mfma_f32_16x16x32_bf16 v[40:43], v[208:211], v[186:189], v[40:43]\n\t" \
  "ds_read_b32 v218, v218 offset:0\n\t" \
  "v_and_b32 v222, %[mk], v222\n\t" \
  "v_fma_f32 v163, |v163|, v225, v163 mul:4\n\t" \
  "v_mfma_f32_16x16x32_bf16 v[64:67], v[208:211], v[194:197], v[64:67]\n\t" \
  "ds_read_b32 v221, v221 offset:0\n\t" \
  "v_fma_f32 v162, |v162|, v224, v162 mul:4\n\t" \
  "v_fma_f32 v171, |v171|, v227, v171 mul:4\n\t" \
  "v_mfma_f32_16x16x32_bf16 v[88:91], v[208:211], v[202:205], v[88:91]\n\t" \
  "s_waitcnt lgkmcnt(4)\n\t" \
  "v_cmp_gt_u32 vcc, %[tg], v246\n\t" \
  "s_cbranch_vccnz .Lcnx_w22\n.Lcnx_b22:\n\t" \
  "ds_read_b64_tr_b16 v[208:209], %[a1] offset:0\n\t" \
  "ds_read_b64_tr_b16 v[210:211], %[a1] offset:1024\n\t" \
  "v_fma_f32 v170, |v170|, v226, v170 mul:4\n\t" \
  "v_fma_f32 v169, |v169|, v229, v169 mul:4\n\t" \
  "s_waitcnt lgkmcnt(5)\n\t" \
  "v_mfma_f32_16x16x32_bf16 v[20:23], v[212:215], v[178:181], v[20:23]\n\t" \
  "s_waitcnt vmcnt(1)\n\t" \
  "s_mov_b64 exec, 1\n\t" \
  "ds_inc_u32 v240, %[h2] offset:33932\n\t" \
  "s_mov_b64 exec, -1\n\t" \
  "s_cmp_lg_u32 %[wq], 0\n\t" \
  "s_cbranch_scc1 .Lcnx_c58\n\t" \
  "s_add_u32 m0, %[wl], %[ws]\n\t" \
  "s_nop 0\n\t" \
  "buffer_load_dwordx4 %[wv], %[wrs], %[so0] offen lds\n\t" \
  "buffer_load_dwordx4 %[wv], %[wrs], %[so1] offen offset:1024 lds\n\t" \
  "buffer_load_dwordx4 %[wv], %[wrs], %[so2] offen offset:2048 lds\n\t" \
  "s_add_u32 %[so0], %[so0], %[wc]\n\t" \
  "s_add_u32 %[so1], %[so1], %[wc]\n\t" \
  "s_add_u32 %[so2], %[so2], %[wc]\n\t" \
  ".Lcnx_c58:\n\t" \
  "s_cmp_lg_u32 %[rnd], 0\n\t" \
  "s_cbranch_scc1 .Lcnx_c59\n\t" \
  "s_add_u32 m0, %[xl], 82000\n\t" \
  "s_add_u32 %[xo], %[xo], %[rs]\n\t" \
  "buffer_load_dwordx4 %[xv], %[xrs], %[xo] offen lds\n\t" \
  "s_cmp_lg_u32 %[w512], 3584\n\t" \
  "s_cbranch_scc1 .Lcnx_c59\n\t" \
  "s_mov_b32 m0, %[pf]\n\t" \
  "s_add_u32 %[t0], %[xo], %[wb]\n\t" \
  "buffer_load_dword %[wv], %[xrs], %[t0] offen lds\n\t" \
  ".Lcnx_c59:\n\t" \
  "ds_read_b32 v220, v220 offset:0\n\t" \
  "v_fma_f32 v168, |v168|, v228, v168 mul:4\n\t" \
  "v_fma_mix_f32 v144, v144, v233, 0 op_sel_hi:[0,1,0]\n\t" \
  "v_fma_mix_f32 v145, v145, v232, 0 op_sel_hi:[0,1,0]\n\t" \
  "v_mfma_f32_16x16x32_bf16 v[44:47], v[212:215], v[186:189], v[44:47]\n\t" \
  "ds_read_b32 v223, v223 offset:0\n\t" \
  "v_pk_fma_f16 v234, v234, %[hh], %[hh]\n\t" \
  "v_cvt_pk_bf16_f32 v146, v144, v145\n\t" \
  "v_cvt_pk_bf16_f32 v163, v162, v163\n\t" \
  "v_mfma_f32_16x16x32_bf16 v[68:71], v[212:215], v[194:197], v[68:71]\n\t" \
  "ds_read_b32 v222, v222 offset:0\n\t" \
  "v_cvt_pk_bf16_f32 v171, v170, v171\n\t" \
  "v_cvt_pk_bf16_f32 v170, v168, v169\n\t" \
  "v_rcp_f16 v235, v234\n\t" \
  "s_setprio 1\n\t" \
  "v_mfma_f32_16x16x32_bf16 v[92:95], v[212:215], v[202:205], v[92:95]\n\t" \
  "ds_read_b64_tr_b16 v[212:213], %[a1] offset:3072\n\t" \
  "v_cvt_pk_f16_f32 v224, |v164|, |v165| clamp\n\t" \
  "v_cvt_pk_f16_f32 v226, |v166|, |v167| clamp\n\t" \
  "s_waitcnt lgkmcnt(5)\n\t" \
  "v_mfma_f32_16x16x32_bf16 v[176:179], v[208:211], v[96:99], 0\n\t" \
  "ds_read_b64_tr_b16 v[214:215], %[a1] offset:4096\n\t" \
  "v_cvt_pk_f16_f32 v228, |v172|, |v173| clamp\n\t" \
  "v_cvt_pk_f16_f32 v230, |v174|, |v175| clamp\n\t" \
  "v_rcp_f16_sdwa v234, v234 dst_sel:DWORD dst_unused:UNUSED_PAD src0_sel:WORD_1\n\t" \
  "v_mfma_f32_16x16x32_bf16 v[184:187], v[208:211], v[108:111], 0\n\t" \
  "v_and_b32_sdwa v225, %[mk], v224 dst_sel:DWORD dst_unused:UNUSED_PAD src0_sel:DWORD src1_sel:WORD_1\n\t" \
  "v_and_b32 v224, %[mk], v224\n\t" \
  "v_mfma_f32_16x16x32_bf16 v[192:195], v[208:211], v[120:123], 0\n\t" \
  "ds_read_b32 v225, v225 offset:0\n\t" \
  "v_and_b32_sdwa v227, %[mk], v226 dst_sel:DWORD dst_unused:UNUSED_PAD src0_sel:DWORD src1_sel:WORD_1\n\t" \
  "v_mfma_f32_16x16x32_bf16 v[200:203], v[208:211], v[132:135], 0\n\t" \
  "ds_read_b64_tr_b16 v[208:209], %[a1] offset:2048\n\t" \
  "v_and_b32 v226, %[mk], v226\n\t" \
  "v_and_b32_sdwa v229, %[mk], v228 dst_sel:DWORD dst_unused:UNUSED_PAD src0_sel:DWORD src1_sel:WORD_1\n\t" \
  "s_waitcnt lgkmcnt(2)\n\t" \
  "v_mfma_f32_16x16x32_bf16 v[176:179], v[212:215], v[100:103], v[176:179]\n\t" \
  "ds_read_b64_tr_b16 v[210:211], %[a1] offset:5120\n\t" \
  "v_and_b32 v228, %[mk], v228\n\t" \
  "v_mfma_f32_16x16x32_bf16 v[184:187], v[212:215], v[112:115], v[184:187]\n\t" \
  "ds_read_b32 v224, v224 offset:0\n\t" \
  "ds_read_b32 v227, v227 offset:0\n\t" \
  "v_and_b32_sdwa v231, %[mk], v230 dst_sel:DWORD dst_unused:UNUSED_PAD src0_sel:DWORD src1_sel:WORD_1\n\t" \
  "v_mfma_f32_16x16x32_bf16 v[192:195], v[212:215], v[124:127], v[192:195]\n\t" \
  "ds_read_b32 v226, v226 offset:0\n\t" \
  "v_and_b32 v230, %[mk], v230\n\t" \
  "v_fma_f32 v149, |v149|, v217, v149 mul:4\n\t" \
  "v_mfma_f32_16x16x32_bf16 v[200:203], v[212:215], v[136:139], v[200:203]\n\t" \
  "ds_read_b64_tr_b16 v[212:213], %[a1] offset:512\n\t" \
  "ds_read_b64_tr_b16 v[214:215], %[a1] offset:1536\n\t" \
  "v_fma_f32 v148, |v148|, v216, v148 mul:4\n\t" \
  "v_fma_f32 v151, |v151|, v219, v151 mul:4\n\t" \
  "s_waitcnt lgkmcnt(5)\n\t" \
  "v_mfma_f32_16x16x32_bf16 v[176:179], v[208:211], v[104:107], v[176:179]\n\t" \
  "ds_read_b32 v229, v229 offset:0\n\t" \
  "ds_read_b32 v228, v228 offset:0\n\t" \
  "v_fma_f32 v150, |v150|, v218, v150 mul:4\n\t" \
  "v_fma_f32 v157, |v157|, v221, v157 mul:4\n\t" \
  "v_mfma_f32_16x16x32_bf16 v[184:187], v[208:211], v[116:119], v[184:187]\n\t" \
  "ds_read_b32 v231, v231 offset:0\n\t" \
  "v_fma_f32 v156, |v156|, v220, v156 mul:4\n\t" \
  "v_fma_f32 v159, |v159|, v223, v159 mul:4\n\t" \
  "v_fma_f32 v158, |v158|, v222, v158 mul:4\n\t" \
  "v_mfma_f32_16x16x32_bf16 v[192:195], v[208:211], v[128:131], v[192:195]\n\t" \
  "ds_read_b32 v230, v230 offset:0\n\t" \
  "v_fma_mix_f32 v160, v160, v235, 0 op_sel_hi:[0,1,0]\n\t" \
  "v_fma_mix_f32 v161, v161, v234, 0 op_sel_hi:[0,1,0]\n\t" \
  "v_cvt_pk_bf16_f32 v162, v160, v161\n\t" \
  "v_mfma_f32_16x16x32_bf16 v[200:203], v[208:211], v[140:143], v[200:203]\n\t" \
  "ds_read_b64_tr_b16 v[208:209], %[a1] offset:3584\n\t" \
  "v_cvt_pk_bf16_f32 v148, v148, v149\n\t" \
  "v_cvt_pk_bf16_f32 v149, v150, v151\n\t" \
  "s_waitcnt lgkmcnt(5)\n\t" \
  "v_mfma_f32_16x16x32_bf16 v[180:183], v[212:215], v[96:99], 0\n\t" \
  "ds_read_b64_tr_b16 v[210:211], %[a1] offset:4608\n\t" \
  "s_setprio 0\n\t" \
  "v_cvt_pk_bf16_f32 v156, v156, v157\n\t" \
  "v_cvt_pk_bf16_f32 v157, v158, v159\n\t" \
  "v_cvt_pk_f16_f32 v216, |v178|, |v179| clamp\n\t" \
  "v_mfma_f32_16x16x32_bf16 v[188:191], v[212:215], v[108:111], 0\n\t" \
  "v_cvt_pk_f16_f32 v232, v176, v177\n\t" \
  "v_cvt_pk_f16_f32 v218, |v186|, |v187| clamp\n\t" \
  "v_cvt_pk_f16_f32 v220, |v184|, |v185| clamp\n\t" \
  "v_mfma_f32_16x16x32_bf16 v[196:199], v[212:215], v[120:123], 0\n\t" \
  "v_pk_mul_f16 v233, v232, v232\n\t" \
  "v_and_b32_sdwa v217, %[mk], v216 dst_sel:DWORD dst_unused:UNUSED_PAD src0_sel:DWORD src1_sel:WORD_1\n\t" \
  "v_mfma_f32_16x16x32_bf16 v[204:207], v[212:215], v[132:135], 0\n\t" \
  "ds_read_b64_tr_b16 v[212:213], %[a1] offset:2560\n\t" \
  "v_and_b32 v216, %[mk], v216\n\t" \
  "v_and_b32_sdwa v219, %[mk], v218 dst_sel:DWORD dst_unused:UNUSED_PAD src0_sel:DWORD src1_sel:WORD_1\n\t" \
  "s_waitcnt lgkmcnt(1)\n\t" \
  "v_mfma_f32_16x16x32_bf16 v[180:183], v[208:211], v[100:103], v[180:183]\n\t" \
  "ds_read_b32 v246, v240 offset:33932\n\t" \
  "ds_read_b64_tr_b16 v[214:215], %[a1] offset:5632\n\t" \
  "v_and_b32 v218, %[mk], v218\n\t" \
  "v_mfma_f32_16x16x32_bf16 v[188:191], v[208:211], v[112:115], v[188:191]\n\t" \
  "ds_read_b32 v217, v217 offset:0\n\t" \
  "v_and_b32_sdwa v221, %[mk], v220 dst_sel:DWORD dst_unused:UNUSED_PAD src0_sel:DWORD src1_sel:WORD_1\n\t" \
  "v_mfma_f32_16x16x32_bf16 v[196:199], v[208:211], v[124:127], v[196:199]\n\t" \
  "ds_read_b32 v216, v216 offset:0\n\t" \
  "v_and_b32 v220, %[mk], v220\n\t" \
  "v_pk_fma_f16 v233, v233, %[h2], %[h1]\n\t" \
  "v_mfma_f32_16x16x32_bf16 v[204:207], v[208:211], v[136:139], v[204:207]\n\t" \
  "s_waitcnt lgkmcnt(3)\n\t" \
  "v_cmp_gt_u32 vcc, %[tg], v246\n\t" \
  "s_cbranch_vccnz .Lcnx_w23\n.Lcnx_b23:\n\t" \
  "ds_read_b64_tr_b16 v[208:209], %[a2] offset:6160\n\t" \
  "ds_read_b64_tr_b16 v[210:211], %[a2] offset:7696\n\t" \
  "v_pk_mul_f16 v233, v232, v233\n\t" \
  "v_fma_f32 v165, |v165|, v225, v165 mul:4\n\t" \
  "s_waitcnt lgkmcnt(4)\n\t" \
  "v_mfma_f32_16x16x32_bf16 v[180:183], v[212:215], v[104:107], v[180:183]\n\t" \
  "s_waitcnt vmcnt(1)\n\t" \
  "s_cmp_lg_u32 %[rnd], 1\n\t" \
  "s_cbranch_scc1 .Lcnx_c61\n\t" \
  "s_waitcnt vmcnt(0)\n\t" \
  ".Lcnx_c61:\n\t" \
  "s_mov_b64 exec, 1\n\t" \
  "ds_inc_u32 v240, %[h2] offset:33936\n\t" \
  "s_mov_b64 exec, -1\n\t" \
  "s_cmp_lg_u32 %[wq], 1\n\t" \
  "s_cbranch_scc1 .Lcnx_c62\n\t" \
  "s_add_u32 m0, %[wl], 0\n\t" \
  "s_nop 0\n\t" \
  "buffer_load_dwordx4 %[wv], %[wrs], %[so0] offen lds\n\t" \
  "buffer_load_dwordx4 %[wv], %[wrs], %[so1] offen offset:1024 lds\n\t" \
  "buffer_load_dwordx4 %[wv], %[wrs], %[so2] offen offset:2048 lds\n\t" \
  "s_add_u32 %[so0], %[so0], %[wc]\n\t" \
  "s_add_u32 %[so1], %[so1], %[wc]\n\t" \
  "s_add_u32 %[so2], %[so2], %[wc]\n\t" \
  "s_sub_u32 %[so0], %[so0], 73728\n\t" \
  "s_sub_u32 %[so1], %[so1], 73728\n\t" \
  "s_sub_u32 %[so2], %[so2], 73728\n\t" \
  ".Lcnx_c62:\n\t" \
  "ds_read_b32 v219, v219 offset:0\n\t" \
  "ds_read_b32 v218, v218 offset:0\n\t" \
  "v_fma_f32 v164, |v164|, v224, v164 mul:4\n\t" \
  "v_fma_f32 v167, |v167|, v227, v167 mul:4\n\t" \
  "v_mfma_f32_16x16x32_bf16 v[188:191], v[212:215], v[116:119], v[188:191]\n\t" \
  "ds_read_b32 v221, v221 offset:0\n\t" \
  "v_fma_f32 v166, |v166|, v226, v166 mul:4\n\t" \
  "v_fma_f32 v173, |v173|, v229, v173 mul:4\n\t" \
  "v_fma_f32 v172, |v172|, v228, v172 mul:4\n\t" \
  "v_mfma_f32_16x16x32_bf16 v[196:199], v[212:215], v[128:131], v[196:199]\n\t" \
  "ds_read_b32 v220, v220 offset:0\n\t" \
  "v_fma_f32 v175, |v175|, v231, v175 mul:4\n\t" \
  "v_fma_f32 v174, |v174|, v230, v174 mul:4\n\t" \
  "v_cvt_pk_bf16_f32 v164, v164, v165\n\t" \
  "v_mfma_f32_16x16x32_bf16 v[204:207], v[212:215], v[140:143], v[204:207]\n\t" \
  "ds_read_b64_tr_b16 v[212:213], %[a2] offset:6168\n\t" \
  "v_cvt_pk_bf16_f32 v165, v166, v167\n\t" \
  "v_cvt_pk_bf16_f32 v172, v172, v173\n\t" \
  "v_cvt_pk_bf16_f32 v173, v174, v175\n\t" \
  "s_setprio 2\n\t" \
  "v_exp_f16 v232, v233\n\t" \
  "v_cvt_pk_f16_f32 v224, |v194|, |v195| clamp\n\t" \
  "v_mfma_f32_16x16x32_bf16 v[80:83], v[242:245], v[236:239], v[80:83]\n\t" \
  "ds_read_b64_tr_b16 v[214:215], %[a2] offset:7704\n\t" \
  "v_cvt_pk_f16_f32 v234, v192, v193\n\t" \
  "v_cvt_pk_f16_f32 v226, |v202|, |v203| clamp\n\t" \
  "v_cvt_pk_f16_f32 v228, |v200|, |v201| clamp\n\t" \
  "v_exp_f16_sdwa v232, v233 dst_sel:WORD_1 dst_unused:UNUSED_PRESERVE src0_sel:WORD_1\n\t" \
  "v_mfma_f32_16x16x32_bf16 v[84:87], v[240:243], v[236:239], v[84:87]\n\t" \
  "buffer_load_dwordx4 v[236:239], %[vo], %[xrs], %[oc1] offen offset:320\n\t" \
  "v_pk_mul_f16 v235, v234, v234\n\t" \
  "v_and_b32_sdwa v225, %[mk], v224 dst_sel:DWORD dst_unused:UNUSED_PAD src0_sel:DWORD src1_sel:WORD_1\n\t" \
  "v_and_b32 v224, %[mk], v224\n\t" \
  "s_waitcnt lgkmcnt(7)\n\t" \
  "v_mfma_f32_16x16x32_bf16 v[0:3], v[208:211], v[146:149], v[0:3]\n\t" \
  "ds_read_b32 v225, v225 offset:0\n\t" \
  "v_and_b32_sdwa v227, %[mk], v226 dst_sel:DWORD dst_unused:UNUSED_PAD src0_sel:DWORD src1_sel:WORD_1\n\t" \
  "v_and_b32 v226, %[mk], v226\n\t" \
  "v_and_b32_sdwa v229, %[mk], v228 dst_sel:DWORD dst_unused:UNUSED_PAD src0_sel:DWORD src1_sel:WORD_1\n\t" \
  "v_mfma_f32_16x16x32_bf16 v[24:27], v[208:211], v[154:157], v[24:27]\n\t" \
  "ds_read_b32 v224, v224 offset:0\n\t" \
  "v_and_b32 v228, %[mk], v228\n\t" \
  "v_pk_fma_f16 v235, v235, %[h2], %[h1]\n\t" \
  "v_mfma_f32_16x16x32_bf16 v[48:51], v[208:211], v[162:165], v[48:51]\n\t" \
  "ds_read_b32 v227, v227 offset:0\n\t" \
  "v_pk_mul_f16 v235, v234, v235\n\t" \
  "s_waitcnt lgkmcnt(8)\n\t" \
  "v_fma_f32 v179, |v179|, v217, v179 mul:4\n\t" \
  "v_fma_f32 v178, |v178|, v216, v178 mul:4\n\t" \
  "v_fma_f32 v187, |v187|, v219, v187 mul:4\n\t" \
  "v_mfma_f32_16x16x32_bf16 v[72:75], v[208:211], v[170:173], v[72:75]\n\t" \
  "ds_read_b64_tr_b16 v[208:209], %[a2] offset:6224\n\t" \
  "ds_read_b64_tr_b16 v[210:211], %[a2] offset:7760\n\t" \
  "s_waitcnt lgkmcnt(7)\n\t" \
  "v_fma_f32 v186, |v186|, v218, v186 mul:4\n\t" \
  "v_fma_f32 v185, |v185|, v221, v185 mul:4\n\t" \
  "v_fma_f32 v184, |v184|, v220, v184 mul:4\n\t" \
  "v_pk_fma_f16 v232, v232, %[hh], %[hh]\n\t" \
  "s_waitcnt lgkmcnt(5)\n\t" \
  "v_mfma_f32_16x16x32_bf16 v[4:7], v[212:215], v[146:149], v[4:7]\n\t" \
  "ds_read_b32 v246, v240 offset:33936\n\t" \
  "ds_read_b32 v226, v226 offset:0\n\t" \
  "v_cvt_pk_bf16_f32 v179, v178, v179\n\t" \
  "v_cvt_pk_bf16_f32 v187, v186, v187\n\t" \
  "v_cvt_pk_bf16_f32 v186, v184, v185\n\t" \
  "v_rcp_f16 v233, v232\n\t" \
  "s_setprio 2\n\t" \
  "v_mfma_f32_16x16x32_bf16 v[28:31], v[212:215], v[154:157], v[28:31]\n\t" \
  "ds_read_b32 v229, v229 offset:0\n\t" \
  "v_exp_f16 v234, v235\n\t" \
  "v_cvt_pk_f16_f32 v216, |v180|, |v181| clamp\n\t" \
  "v_cvt_pk_f16_f32 v218, |v182|, |v183| clamp\n\t" \
  "v_cvt_pk_f16_f32 v220, |v188|, |v189| clamp\n\t" \
  "v_cvt_pk_f16_f32 v222, |v190|, |v191| clamp\n\t" \
  "v_mfma_f32_16x16x32_bf16 v[52:55], v[212:215], v[162:165], v[52:55]\n\t" \
  "ds_read_b32 v228, v228 offset:0\n\t" \
  "v_rcp_f16_sdwa v232, v232 dst_sel:DWORD dst_unused:UNUSED_PAD src0_sel:WORD_1\n\t" \
  "v_exp_f16_sdwa v234, v235 dst_sel:WORD_1 dst_unused:UNUSED_PRESERVE src0_sel:WORD_1\n\t" \
  "v_and_b32_sdwa v217, %[mk], v216 dst_sel:DWORD dst_unused:UNUSED_PAD src0_sel:DWORD src1_sel:WORD_1\n\t" \
  "v_and_b32 v216, %[mk], v216\n\t" \
  "v_mfma_f32_16x16x32_bf16 v[76:79], v[212:215], v[170:173], v[76:79]\n\t" \
  "s_waitcnt lgkmcnt(3)\n\t" \
  "v_cmp_gt_u32 vcc, %[tg], v246\n\t" \
  "s_cbranch_vccnz .Lcnx_w24\n.Lcnx_b24:\n\t" \
  "ds_read_b64_tr_b16 v[212:213], %[a2] offset:6232\n\t" \
  "v_and_b32_sdwa v219, %[mk], v218 dst_sel:DWORD dst_unused:UNUSED_PAD src0_sel:DWORD src1_sel:WORD_1\n\t" \
  "v_and_b32 v218, %[mk], v218\n\t" \
  "v_mfma_f32_16x16x32_bf16 v[8:11], v[208:211], v[146:149], v[8:11]\n\t" \
  "s_waitcnt vmcnt(1)\n\t" \
  "s_mov_b64 exec, 1\n\t" \
  "ds_inc_u32 v240, %[h2] offset:33940\n\t" \
  "s_mov_b64 exec, -1\n\t" \
  "s_cmp_lg_u32 %[wq], 0\n\t" \
  "s_cbranch_scc1 .Lcnx_c63\n\t" \
  "s_add_u32 m0, %[wl], 0\n\t" \
  "s_nop 0\n\t" \
  "buffer_load_dwordx4 %[wv], %[wrs], %[so0] offen lds\n\t" \
  "buffer_load_dwordx4 %[wv], %[wrs], %[so1] offen offset:1024 lds\n\t" \
  "buffer_load_dwordx4 %[wv], %[wrs], %[so2] offen offset:2048 lds\n\t" \
  "s_add_u32 %[so0], %[so0], %[wc]\n\t" \
  "s_add_u32 %[so1], %[so1], %[wc]\n\t" \
  "s_add_u32 %[so2], %[so2], %[wc]\n\t" \
  ".Lcnx_c63:\n\t" \
  "s_cmp_lg_u32 %[rnd], 0\n\t" \
  "s_cbranch_scc1 .Lcnx_c64\n\t" \
  "s_add_u32 m0, %[xl], 90200\n\t" \
  "s_add_u32 %[xo], %[xo], %[rs]\n\t" \
  "buffer_load_dwordx4 %[xv], %[xrs], %[xo] offen lds\n\t" \
  ".Lcnx_c64:\n\t" \
  "ds_read_b64_tr_b16 v[214:215], %[a2] offset:7768\n\t" \
  "ds_read_b32 v217, v217 offset:0\n\t" \
  "v_and_b32_sdwa v221, %[mk], v220 dst_sel:DWORD dst_unused:UNUSED_PAD src0_sel:DWORD src1_sel:WORD_1\n\t" \
  "v_and_b32 v220, %[mk], v220\n\t" \
  "v_mfma_f32_16x16x32_bf16 v[32:35], v[208:211], v[154:157], v[32:35]\n\t" \
  "ds_read_b32 v216, v216 offset:0\n\t" \
  "ds_read_b32 v219, v219 offset:0\n\t" \
  "v_and_b32_sdwa v223, %[mk], v222 dst_sel:DWORD dst_unused:UNUSED_PAD src0_sel:DWORD src1_sel:WORD_1\n\t" \
  "v_and_b32 v222, %[mk], v222\n\t" \
  "v_fma_f32 v195, |v195|, v225, v195 mul:4\n\t" \
  "v_mfma_f32_16x16x32_bf16 v[56:59], v[208:211], v[162:165], v[56:59]\n\t" \
  "ds_read_b32 v218, v218 offset:0\n\t" \
  "ds_read_b32 v221, v221 offset:0\n\t" \
  "v_fma_f32 v194, |v194|, v224, v194 mul:4\n\t" \
  "v_fma_f32 v203, |v203|, v227, v203 mul:4\n\t" \
  "s_waitcnt lgkmcnt(8)\n\t" \
  "v_fma_f32 v202, |v202|, v226, v202 mul:4\n\t" \
  "v_mfma_f32_16x16x32_bf16 v[80:83], v[208:211], v[170:173], v[80:83]\n\t" \
  "ds_read_b64_tr_b16 v[208:209], %[a2] offset:6288\n\t" \
  "s_setprio 1\n\t" \
  "ds_read_b64_tr_b16 v[210:211], %[a2] offset:7824\n\t" \
  "v_fma_f32 v201, |v201|, v229, v201 mul:4\n\t" \
  "v_fma_f32 v200, |v200|, v228, v200 mul:4\n\t" \
  "v_fma_mix_f32 v176, v176, v233, 0 op_sel_hi:[0,1,0]\n\t" \
  "v_fma_mix_f32 v177, v177, v232, 0 op_sel_hi:[0,1,0]\n\t" \
  "s_waitcnt lgkmcnt(7)\n\t" \
  "v_mfma_f32_16x16x32_bf16 v[12:15], v[212:215], v[146:149], v[12:15]\n\t" \
  "ds_read_b32 v220, v220 offset:0\n\t" \
  "v_pk_fma_f16 v234, v234, %[hh], %[hh]\n\t" \
  "v_cvt_pk_bf16_f32 v178, v176, v177\n\t" \
  "v_cvt_pk_bf16_f32 v195, v194, v195\n\t" \
  "v_cvt_pk_bf16_f32 v203, v202, v203\n\t" \
  "v_cvt_pk_bf16_f32 v202, v200, v201\n\t" \
  "v_mfma_f32_16x16x32_bf16 v[36:39], v[212:215], v[154:157], v[36:39]\n\t" \
  "ds_read_b32 v223, v223 offset:0\n\t" \
  "v_rcp_f16 v235, v234\n\t" \
  "v_cvt_pk_f16_f32 v224, |v196|, |v197| clamp\n\t" \
  "v_cvt_pk_f16_f32 v226, |v198|, |v199| clamp\n\t" \
  "v_cvt_pk_f16_f32 v228, |v204|, |v205| clamp\n\t" \
  "v_mfma_f32_16x16x32_bf16 v[60:63], v[212:215], v[162:165], v[60:63]\n\t" \
  "ds_read_b32 v222, v222 offset:0\n\t" \
  "v_cvt_pk_f16_f32 v230, |v206|, |v207| clamp\n\t" \
  "v_rcp_f16_sdwa v234, v234 dst_sel:DWORD dst_unused:UNUSED_PAD src0_sel:WORD_1\n\t" \
  "v_and_b32_sdwa v225, %[mk], v224 dst_sel:DWORD dst_unused:UNUSED_PAD src0_sel:DWORD src1_sel:WORD_1\n\t" \
  "v_and_b32 v224, %[mk], v224\n\t" \
  "v_mfma_f32_16x16x32_bf16 v[84:87], v[212:215], v[170:173], v[84:87]\n\t" \
  "ds_read_b64_tr_b16 v[212:213], %[a2] offset:6296\n\t" \
  "v_and_b32_sdwa v227, %[mk], v226 dst_sel:DWORD dst_unused:UNUSED_PAD src0_sel:DWORD src1_sel:WORD_1\n\t" \
  "v_and_b32 v226, %[mk], v226\n\t" \
  "s_waitcnt lgkmcnt(4)\n\t" \
  "v_mfma_f32_16x16x32_bf16 v[16:19], v[208:211], v[146:149], v[16:19]\n\t" \
  "ds_read_b32 v246, v240 offset:33940\n\t" \
  "ds_read_b64_tr_b16 v[214:215], %[a2] offset:7832\n\t" \
  "ds_read_b32 v225, v225 offset:0\n\t" \
  "v_and_b32_sdwa v229, %[mk], v228 dst_sel:DWORD dst_unused:UNUSED_PAD src0_sel:DWORD src1_sel:WORD_1\n\t" \
  "v_and_b32 v228, %[mk], v228\n\t" \
  "v_and_b32_sdwa v231, %[mk], v230 dst_sel:DWORD dst_unused:UNUSED_PAD src0_sel:DWORD src1_sel:WORD_1\n\t" \
  "v_mfma_f32_16x16x32_bf16 v[40:43], v[208:211], v[154:157], v[40:43]\n\t" \
  "ds_read_b32 v224, v224 offset:0\n\t" \
  "ds_read_b32 v227, v227 offset:0\n\t" \
  "v_and_b32 v230, %[mk], v230\n\t" \
  "v_fma_f32 v181, |v181|, v217, v181 mul:4\n\t" \
  "v_mfma_f32_16x16x32_bf16 v[64:67], v[208:211], v[162:165], v[64:67]\n\t" \
  "ds_read_b32 v226, v226 offset:0\n\t" \
  "ds_read_b32 v229, v229 offset:0\n\t" \
  "s_setprio 0\n\t" \
  "v_fma_f32 v180, |v180|, v216, v180 mul:4\n\t" \
  "v_fma_f32 v183, |v183|, v219, v183 mul:4\n\t" \
  "v_fma_f32 v182, |v182|, v218, v182 mul:4\n\t" \
  "v_mfma_f32_16x16x32_bf16 v[88:91], v[208:211], v[170:173], v[88:91]\n\t" \
  "s_waitcnt lgkmcnt(6)\n\t" \
  "v_cmp_gt_u32 vcc, %[tg], v246\n\t" \
  "s_cbranch_vccnz .Lcnx_w25\n.Lcnx_b25:\n\t" \
  "ds_read_b64_tr_b16 v[208:209], %[a2] offset:0\n\t" \
  "ds_read_b64_tr_b16 v[210:211], %[a2] offset:1536\n\t" \
  "v_fma_f32 v189, |v189|, v221, v189 mul:4\n\t" \
  "v_fma_f32 v188, |v188|, v220, v188 mul:4\n\t" \
  "v_fma_f32 v191, |v191|, v223, v191 mul:4\n\t" \
  "v_fma_f32 v190, |v190|, v222, v190 mul:4\n\t" \
  "v_fma_mix_f32 v192, v192, v235, 0 op_sel_hi:[0,1,0]\n\t" \
  "s_waitcnt lgkmcnt(7)\n\t" \
  "v_mfma_f32_16x16x32_bf16 v[20:23], v[212:215], v[146:149], v[20:23]\n\t" \
  "s_waitcnt vmcnt(1)\n\t" \
  "s_cmp_lg_u32 %[rnd], 1\n\t" \
  "s_cbranch_scc1 .Lcnx_c65\n\t" \
  "s_waitcnt vmcnt(0)\n\t" \
  ".Lcnx_c65:\n\t" \
  "ds_read_b32 v228, v228 offset:0\n\t" \
  "v_fma_mix_f32 v193, v193, v234, 0 op_sel_hi:[0,1,0]\n\t" \
  "v_cvt_pk_bf16_f32 v194, v192, v193\n\t" \
  "v_cvt_pk_bf16_f32 v180, v180, v181\n\t" \
  "v_cvt_pk_bf16_f32 v181, v182, v183\n\t" \
  "v_mfma_f32_16x16x32_bf16 v[44:47], v[212:215], v[154:157], v[44:47]\n\t" \
  "ds_read_b32 v231, v231 offset:0\n\t" \
  "v_cvt_pk_bf16_f32 v188, v188, v189\n\t" \
  "v_cvt_pk_bf16_f32 v189, v190, v191\n\t" \
  "ds_read_b32 v230, v230 offset:0\n\t" \
  "s_waitcnt lgkmcnt(6)\n\t" \
  "v_fma_f32 v197, |v197|, v225, v197 mul:4\n\t" \
  "v_fma_f32 v196, |v196|, v224, v196 mul:4\n\t" \
  "v_fma_f32 v199, |v199|, v227, v199 mul:4\n\t" \
  "v_mfma_f32_16x16x32_bf16 v[68:71], v[212:215], v[162:165], v[68:71]\n\t" \
  "v_fma_f32 v198, |v198|, v226, v198 mul:4\n\t" \
  "s_waitcnt lgkmcnt(0)\n\t" \
  "v_fma_f32 v205, |v205|, v229, v205 mul:4\n\t" \
  "v_fma_f32 v204, |v204|, v228, v204 mul:4\n\t" \
  "v_fma_f32 v207, |v207|, v231, v207 mul:4\n\t" \
  "v_mfma_f32_16x16x32_bf16 v[92:95], v[212:215], v[170:173], v[92:95]\n\t" \
  "ds_read_b64_tr_b16 v[212:213], %[a2] offset:8\n\t" \
  "v_fma_f32 v206, |v206|, v230, v206 mul:4\n\t" \
  "v_cvt_pk_bf16_f32 v196, v196, v197\n\t" \
  "v_cvt_pk_bf16_f32 v197, v198, v199\n\t" \
  "v_cvt_pk_bf16_f32 v204, v204, v205\n\t" \
  "v_cvt_pk_bf16_f32 v205, v206, v207\n\t" \
  "s_cmp_lg_u32 %[rnd], 0\n\t" \
  "s_cbranch_scc1 .Lcnx_fin\n\t" \
  "v_mfma_f32_32x32x16_bf16 v[144:159], v[242:245], v[240:243], 0\n\t" \
  "v_mfma_f32_16x16x32_bf16 v[88:91], v[242:245], v[236:239], v[88:91]\n\t" \
  "ds_read_b64_tr_b16 v[214:215], %[a2] offset:1544\n\t" \
  "v_mfma_f32_32x32x16_bf16 v[160:175], v[242:245], v[240:243], 0\n\t" \
  "ds_read_b64 v[246:247], %[l8] offset:34832\n\t" \
  "s_waitcnt lgkmcnt(0)\n\t" \
  "v_add_u32 v246, %[c24], v246\n\t" \
  "v_mfma_f32_16x16x32_bf16 v[92:95], v[240:243], v[236:239], v[92:95]\n\t" \
  "v_add_u32 v247, %[c23], v247\n\t" \
  "v_add_u32 v232, 1536, v247\n\t" \
  "v_mfma_f32_16x16x32_bf16 v[0:3], v[208:211], v[178:181], v[0:3]\n\t" \
  "ds_read2_b32 v[98:99], v247 offset0:0 offset1:6\n\t" \
  "v_mfma_f32_16x16x32_bf16 v[24:27], v[208:211], v[186:189], v[24:27]\n\t" \
  "ds_read2_b32 v[102:103], v247 offset0:12 offset1:18\n\t" \
  "v_mfma_f32_16x16x32_bf16 v[48:51], v[208:211], v[194:197], v[48:51]\n\t" \
  "ds_read2_b32 v[106:107], v247 offset0:24 offset1:30\n\t" \
  "ds_read2_b32 v[110:111], v247 offset0:36 offset1:42\n\t" \
  "v_mfma_f32_16x16x32_bf16 v[72:75], v[208:211], v[202:205], v[72:75]\n\t" \
  "ds_read_b64_tr_b16 v[208:209], %[a2] offset:64\n\t" \
  "ds_read_b64_tr_b16 v[210:211], %[a2] offset:1600\n\t" \
  "v_mfma_f32_16x16x32_bf16 v[4:7], v[212:215], v[178:181], v[4:7]\n\t" \
  "ds_read2_b32 v[114:115], v247 offset0:192 offset1:198\n\t" \
  "ds_read2_b32 v[118:119], v247 offset0:204 offset1:210\n\t" \
  "s_waitcnt lgkmcnt(7)\n\t" \
  "v_and_b32 v96, 0xffff, v98\n\t" \
  "v_mfma_f32_16x16x32_bf16 v[28:31], v[212:215], v[186:189], v[28:31]\n\t" \
  "ds_read2_b32 v[122:123], v247 offset0:216 offset1:222\n\t" \
  "ds_read2_b32 v[126:127], v247 offset0:228 offset1:234\n\t" \
  "v_and_b32 v97, 0xffff0000, v98\n\t" \
  "v_and_b32 v98, 0xffff, v99\n\t" \
  "v_and_b32 v99, 0xffff0000, v99\n\t" \
  "v_mfma_f32_16x16x32_bf16 v[52:55], v[212:215], v[194:197], v[52:55]\n\t" \
  "ds_read2_b32 v[130:131], v232 offset0:0 offset1:6\n\t" \
  "ds_read2_b32 v[134:135], v232 offset0:12 offset1:18\n\t" \
  "s_waitcnt lgkmcnt(10)\n\t" \
  "v_and_b32 v100, 0xffff, v102\n\t" \
  "v_and_b32 v101, 0xffff0000, v102\n\t" \
  "v_mfma_f32_16x16x32_bf16 v[76:79], v[212:215], v[202:205], v[76:79]\n\t" \
  "ds_read_b64_tr_b16 v[212:213], %[a2] offset:72\n\t" \
  "ds_read_b64_tr_b16 v[214:215], %[a2] offset:1608\n\t" \
  "v_and_b32 v102, 0xffff, v103\n\t" \
  "v_and_b32 v103, 0xffff0000, v103\n\t" \
  "s_waitcnt lgkmcnt(11)\n\t" \
  "v_and_b32 v104, 0xffff, v106\n\t" \
  "s_waitcnt lgkmcnt(8)\n\t" \
  "v_mfma_f32_16x16x32_bf16 v[8:11], v[208:211], v[178:181], v[8:11]\n\t" \
  "ds_read2_b32 v[138:139], v232 offset0:24 offset1:30\n\t" \
  "v_and_b32 v105, 0xffff0000, v106\n\t" \
  "v_and_b32 v106, 0xffff, v107\n\t" \
  "v_mfma_f32_16x16x32_bf16 v[32:35], v[208:211], v[186:189], v[32:35]\n\t" \
  "ds_read2_b32 v[142:143], v232 offset0:36 offset1:42\n\t" \
  "v_and_b32 v107, 0xffff0000, v107\n\t" \
  "v_and_b32 v108, 0xffff, v110\n\t" \
  "v_and_b32 v109, 0xffff0000, v110\n\t" \
  "v_mfma_f32_16x16x32_bf16 v[56:59], v[208:211], v[194:197], v[56:59]\n\t" \
  "v_and_b32 v110, 0xffff, v111\n\t" \
  "v_and_b32 v111, 0xffff0000, v111\n\t" \
  "s_waitcnt lgkmcnt(9)\n\t" \
  "v_and_b32 v112, 0xffff, v114\n\t" \
  "v_mfma_f32_16x16x32_bf16 v[80:83], v[208:211], v[202:205], v[80:83]\n\t" \
  "ds_read_b64_tr_b16 v[208:209], %[a2] offset:128\n\t" \
  "v_and_b32 v113, 0xffff0000, v114\n\t" \
  "v_and_b32 v114, 0xffff, v115\n\t" \
  "s_waitcnt lgkmcnt(3)\n\t" \
  "v_mfma_f32_16x16x32_bf16 v[12:15], v[212:215], v[178:181], v[12:15]\n\t" \
  "ds_read_b64_tr_b16 v[210:211], %[a2] offset:1664\n\t" \
  "v_and_b32 v115, 0xffff0000, v115\n\t" \
  "v_and_b32 v116, 0xffff, v118\n\t" \
  "v_and_b32 v117, 0xffff0000, v118\n\t" \
  "v_mfma_f32_16x16x32_bf16 v[36:39], v[212:215], v[186:189], v[36:39]\n\t" \
  "v_and_b32 v118, 0xffff, v119\n\t" \
  "v_and_b32 v119, 0xffff0000, v119\n\t" \
  "v_mfma_f32_16x16x32_bf16 v[60:63], v[212:215], v[194:197], v[60:63]\n\t" \
  "v_and_b32 v120, 0xffff, v122\n\t" \
  "v_and_b32 v121, 0xffff0000, v122\n\t" \
  "v_and_b32 v122, 0xffff, v123\n\t" \
  "v_mfma_f32_16x16x32_bf16 v[84:87], v[212:215], v[202:205], v[84:87]\n\t" \
  "ds_read_b64_tr_b16 v[212:213], %[a2] offset:136\n\t" \
  "v_and_b32 v123, 0xffff0000, v123\n\t" \
  "v_and_b32 v124, 0xffff, v126\n\t" \
  "s_waitcnt lgkmcnt(1)\n\t" \
  "v_mfma_f32_16x16x32_bf16 v[16:19], v[208:211], v[178:181], v[16:19]\n\t" \
  "ds_read_b64_tr_b16 v[214:215], %[a2] offset:1672\n\t" \
  "v_and_b32 v125, 0xffff0000, v126\n\t" \
  "v_and_b32 v126, 0xffff, v127\n\t" \
  "v_and_b32 v127, 0xffff0000, v127\n\t" \
  "v_mfma_f32_16x16x32_bf16 v[40:43], v[208:211], v[186:189], v[40:43]\n\t" \
  "v_and_b32 v128, 0xffff, v130\n\t" \
  "v_and_b32 v129, 0xffff0000, v130\n\t" \
  "v_mfma_f32_16x16x32_bf16 v[64:67], v[208:211], v[194:197], v[64:67]\n\t" \
  "v_and_b32 v130, 0xffff, v131\n\t" \
  "v_and_b32 v131, 0xffff0000, v131\n\t" \
  "v_and_b32 v132, 0xffff, v134\n\t" \
  "v_mfma_f32_16x16x32_bf16 v[88:91], v[208:211], v[202:205], v[88:91]\n\t" \
  "v_and_b32 v133, 0xffff0000, v134\n\t" \
  "v_and_b32 v134, 0xffff, v135\n\t" \
  "s_waitcnt lgkmcnt(0)\n\t" \
  "v_mfma_f32_16x16x32_bf16 v[20:23], v[212:215], v[178:181], v[20:23]\n\t" \
  "v_and_b32 v135, 0xffff0000, v135\n\t" \
  "v_and_b32 v136, 0xffff, v138\n\t" \
  "v_and_b32 v137, 0xffff0000, v138\n\t" \
  "v_mfma_f32_16x16x32_bf16 v[44:47], v[212:215], v[186:189], v[44:47]\n\t" \
  "v_and_b32 v138, 0xffff, v139\n\t" \
  "v_and_b32 v139, 0xffff0000, v139\n\t" \
  "v_mfma_f32_16x16x32_bf16 v[68:71], v[212:215], v[194:197], v[68:71]\n\t" \
  "v_and_b32 v140, 0xffff, v142\n\t" \
  "v_and_b32 v141, 0xffff0000, v142\n\t" \
  "v_and_b32 v142, 0xffff, v143\n\t" \
  "v_mfma_f32_16x16x32_bf16 v[92:95], v[212:215], v[202:205], v[92:95]\n\t" \
  "v_and_b32 v143, 0xffff0000, v143\n\t" \
  "v_bfe_u32 v236, %[l8], 3, 2\n\t" \
  "v_mul_u32_u24 v236, 0x5555, v236\n\t" \
  "s_nop 7\n\t" \
  "s_mov_b32 %[tg], 16\n\t" \
  "s_mov_b32 %[rnd], 1\n\t" \
  "s_mov_b32 %[f0], 2\n\t" \
  "s_mov_b32 %[op0], %[oc0]\n\t" \
  "s_mov_b32 %[op1], %[oc1]\n\t" \
  "s_add_u32 %[oc0], %[oc0], %[dy]\n\t" \
  "s_add_u32 %[oc1], %[oc1], %[dy]\n\t" \
  "s_branch .Lcnx_round\n\t" \
  ".Lcnx_w2:\n\t" \
  "ds_read_b32 v246, v240 offset:33848\n\t" \
  "s_waitcnt lgkmcnt(0)\n\t" \
  "v_cmp_gt_u32 vcc, %[tg], v246\n\t" \
  "s_cbranch_vccnz .Lcnx_w2\n\t" \
  "s_branch .Lcnx_b2\n\t" \
  ".Lcnx_w3:\n\t" \
  "ds_read_b32 v246, v240 offset:33852\n\t" \
  "s_waitcnt lgkmcnt(0)\n\t" \
  "v_cmp_gt_u32 vcc, %[tg], v246\n\t" \
  "s_cbranch_vccnz .Lcnx_w3\n\t" \
  "s_branch .Lcnx_b3\n\t" \
  ".Lcnx_w4:\n\t" \
  "ds_read_b32 v246, v240 offset:33856\n\t" \
  "s_waitcnt lgkmcnt(0)\n\t" \
  "v_cmp_gt_u32 vcc, %[tg], v246\n\t" \
  "s_cbranch_vccnz .Lcnx_w4\n\t" \
  "s_branch .Lcnx_b4\n\t" \
  ".Lcnx_w5:\n\t" \
  "ds_read_b32 v246, v240 offset:33860\n\t" \
  "s_waitcnt lgkmcnt(0)\n\t" \
  "v_cmp_gt_u32 vcc, %[tg], v246\n\t" \
  "s_cbranch_vccnz .Lcnx_w5\n\t" \
  "s_branch .Lcnx_b5\n\t" \
  ".Lcnx_w6:\n\t" \
  "ds_read_b32 v246, v240 offset:33864\n\t" \
  "s_waitcnt lgkmcnt(0)\n\t" \
  "v_cmp_gt_u32 vcc, %[tg], v246\n\t" \
  "s_cbranch_vccnz .Lcnx_w6\n\t" \
  "s_branch .Lcnx_b6\n\t" \
  ".Lcnx_w7:\n\t" \
  "ds_read_b32 v246, v240 offset:33868\n\t" \
  "s_waitcnt lgkmcnt(0)\n\t" \
  "v_cmp_gt_u32 vcc, %[tg], v246\n\t" \
  "s_cbranch_vccnz .Lcnx_w7\n\t" \
  "s_branch .Lcnx_b7\n\t" \
  ".Lcnx_w8:\n\t" \
  "ds_read_b32 v246, v240 offset:33872\n\t" \
  "s_waitcnt lgkmcnt(0)\n\t" \
  "v_cmp_gt_u32 vcc, %[tg], v246\n\t" \
  "s_cbranch_vccnz .Lcnx_w8\n\t" \
  "s_branch .Lcnx_b8\n\t" \
  ".Lcnx_w9:\n\t" \
  "ds_read_b32 v246, v240 offset:33876\n\t" \
  "s_waitcnt lgkmcnt(0)\n\t" \
  "v_cmp_gt_u32 vcc, %[tg], v246\n\t" \
  "s_cbranch_vccnz .Lcnx_w9\n\t" \
  "s_branch .Lcnx_b9\n\t" \
  ".Lcnx_w10:\n\t" \
  "ds_read_b32 v246, v240 offset:33880\n\t" \
  "s_waitcnt lgkmcnt(0)\n\t" \
  "v_cmp_gt_u32 vcc, %[tg], v246\n\t" \
  "s_cbranch_vccnz .Lcnx_w10\n\t" \
  "s_branch .Lcnx_b10\n\t" \
  ".Lcnx_w11:\n\t" \
  "ds_read_b32 v246, v240 offset:33884\n\t" \
  "s_waitcnt lgkmcnt(0)\n\t" \
  "v_cmp_gt_u32 vcc, %[tg], v246\n\t" \
  "s_cbranch_vccnz .Lcnx_w11\n\t" \
  "s_branch .Lcnx_b11\n\t" \
  ".Lcnx_w12:\n\t" \
  "ds_read_b32 v246, v240 offset:33888\n\t" \
  "s_waitcnt lgkmcnt(0)\n\t" \
  "v_cmp_gt_u32 vcc, %[tg], v246\n\t" \
  "s_cbranch_vccnz .Lcnx_w12\n\t" \
  "s_branch .Lcnx_b12\n\t" \
  ".Lcnx_w13:\n\t" \
  "ds_read_b32 v246, v240 offset:33892\n\t" \
  "s_waitcnt lgkmcnt(0)\n\t" \
  "v_cmp_gt_u32 vcc, %[tg], v246\n\t" \
  "s_cbranch_vccnz .Lcnx_w13\n\t" \
  "s_branch .Lcnx_b13\n\t" \
  ".Lcnx_w14:\n\t" \
  "ds_read_b32 v246, v240 offset:33896\n\t" \
  "s_waitcnt lgkmcnt(0)\n\t" \
  "v_cmp_gt_u32 vcc, %[tg], v246\n\t" \
  "s_cbranch_vccnz .Lcnx_w14\n\t" \
  "s_branch .Lcnx_b14\n\t" \
  ".Lcnx_w15:\n\t" \
  "ds_read_b32 v246, v240 offset:33900\n\t" \
  "s_waitcnt lgkmcnt(0)\n\t" \
  "v_cmp_gt_u32 vcc, %[tg], v246\n\t" \
  "s_cbranch_vccnz .Lcnx_w15\n\t" \
  "s_branch .Lcnx_b15\n\t" \
  ".Lcnx_w16:\n\t" \
  "ds_read_b32 v246, v240 offset:33904\n\t" \
  "s_waitcnt lgkmcnt(0)\n\t" \
  "v_cmp_gt_u32 vcc, %[tg], v246\n\t" \
  "s_cbranch_vccnz .Lcnx_w16\n\t" \
  "s_branch .Lcnx_b16\n\t" \
  ".Lcnx_w17:\n\t" \
  "ds_read_b32 v246, v240 offset:33908\n\t" \
  "s_waitcnt lgkmcnt(0)\n\t" \
  "v_cmp_gt_u32 vcc, %[tg], v246\n\t" \
  "s_cbranch_vccnz .Lcnx_w17\n\t" \
  "s_branch .Lcnx_b17\n\t" \
  ".Lcnx_w18:\n\t" \
  "ds_read_b32 v246, v240 offset:33912\n\t" \
  "s_waitcnt lgkmcnt(0)\n\t" \
  "v_cmp_gt_u32 vcc, %[tg], v246\n\t" \
  "s_cbranch_vccnz .Lcnx_w18\n\t" \
  "s_branch .Lcnx_b18\n\t" \
  ".Lcnx_w19:\n\t" \
  "ds_read_b32 v246, v240 offset:33916\n\t" \
  "s_waitcnt lgkmcnt(0)\n\t" \
  "v_cmp_gt_u32 vcc, %[tg], v246\n\t" \
  "s_cbranch_vccnz .Lcnx_w19\n\t" \
  "s_branch .Lcnx_b19\n\t" \
  ".Lcnx_w20:\n\t" \
  "ds_read_b32 v246, v240 offset:33920\n\t" \
  "s_waitcnt lgkmcnt(0)\n\t" \
  "v_cmp_gt_u32 vcc, %[tg], v246\n\t" \
  "s_cbranch_vccnz .Lcnx_w20\n\t" \
  "s_branch .Lcnx_b20\n\t" \
  ".Lcnx_w21:\n\t" \
  "ds_read_b32 v246, v240 offset:33924\n\t" \
  "s_waitcnt lgkmcnt(0)\n\t" \
  "v_cmp_gt_u32 vcc, %[tg], v246\n\t" \
  "s_cbranch_vccnz .Lcnx_w21\n\t" \
  "s_branch .Lcnx_b21\n\t" \
  ".Lcnx_w22:\n\t" \
  "ds_read_b32 v246, v240 offset:33928\n\t" \
  "s_waitcnt lgkmcnt(0)\n\t" \
  "v_cmp_gt_u32 vcc, %[tg], v246\n\t" \
  "s_cbranch_vccnz .Lcnx_w22\n\t" \
  "s_branch .Lcnx_b22\n\t" \
  ".Lcnx_w23:\n\t" \
  "ds_read_b32 v246, v240 offset:33932\n\t" \
  "s_waitcnt lgkmcnt(0)\n\t" \
  "v_cmp_gt_u32 vcc, %[tg], v246\n\t" \
  "s_cbranch_vccnz .Lcnx_w23\n\t" \
  "s_branch .Lcnx_b23\n\t" \
  ".Lcnx_w24:\n\t" \
  "ds_read_b32 v246, v240 offset:33936\n\t" \
  "s_waitcnt lgkmcnt(0)\n\t" \
  "v_cmp_gt_u32 vcc, %[tg], v246\n\t" \
  "s_cbranch_vccnz .Lcnx_w24\n\t" \
  "s_branch .Lcnx_b24\n\t" \
  ".Lcnx_w25:\n\t" \
  "ds_read_b32 v246, v240 offset:33940\n\t" \
  "s_waitcnt lgkmcnt(0)\n\t" \
  "v_cmp_gt_u32 vcc, %[tg], v246\n\t" \
  "s_cbranch_vccnz .Lcnx_w25\n\t" \
  "s_branch .Lcnx_b25\n\t" \
  ".Lcnx_fin:\n\t" \
  "v_mfma_f32_16x16x32_bf16 v[88:91], v[242:245], v[236:239], v[88:91]\n\t" \
  "ds_read_b64_tr_b16 v[214:215], %[a2] offset:1544\n\t" \
  "v_mfma_f32_16x16x32_bf16 v[92:95], v[240:243], v[236:239], v[92:95]\n\t" \
  "v_mfma_f32_16x16x32_bf16 v[0:3], v[208:211], v[178:181], v[0:3]\n\t" \
  "v_mfma_f32_16x16x32_bf16 v[24:27], v[208:211], v[186:189], v[24:27]\n\t" \
  "v_mfma_f32_16x16x32_bf16 v[48:51], v[208:211], v[194:197], v[48:51]\n\t" \
  "v_mfma_f32_16x16x32_bf16 v[72:75], v[208:211], v[202:205], v[72:75]\n\t" \
  "ds_read_b64_tr_b16 v[208:209], %[a2] offset:64\n\t" \
  "s_waitcnt lgkmcnt(1)\n\t" \
  "v_mfma_f32_16x16x32_bf16 v[4:7], v[212:215], v[178:181], v[4:7]\n\t" \
  "ds_read_b64_tr_b16 v[210:211], %[a2] offset:1600\n\t" \
  "v_mfma_f32_16x16x32_bf16 v[28:31], v[212:215], v[186:189], v[28:31]\n\t" \
  "v_mfma_f32_16x16x32_bf16 v[52:55], v[212:215], v[194:197], v[52:55]\n\t" \
  "v_mfma_f32_16x16x32_bf16 v[76:79], v[212:215], v[202:205], v[76:79]\n\t" \
  "ds_read_b64_tr_b16 v[212:213], %[a2] offset:72\n\t" \
  "s_waitcnt lgkmcnt(1)\n\t" \
  "v_mfma_f32_16x16x32_bf16 v[8:11], v[208:211], v[178:181], v[8:11]\n\t" \
  "ds_read_b64_tr_b16 v[214:215], %[a2] offset:1608\n\t" \
  "v_mfma_f32_16x16x32_bf16 v[32:35], v[208:211], v[186:189], v[32:35]\n\t" \
  "v_mfma_f32_16x16x32_bf16 v[56:59], v[208:211], v[194:197], v[56:59]\n\t" \
  "v_mfma_f32_16x16x32_bf16 v[80:83], v[208:211], v[202:205], v[80:83]\n\t" \
  "ds_read_b64_tr_b16 v[208:209], %[a2] offset:128\n\t" \
  "v_cvt_pk_bf16_f32 v0, v0, v1\n\t" \
  "v_cvt_pk_bf16_f32 v1, v2, v3\n\t" \
  "v_cvt_pk_bf16_f32 v2, v4, v5\n\t" \
  "v_cvt_pk_bf16_f32 v3, v6, v7\n\t" \
  "buffer_store_dwordx4 v[0:3], %[vo], %[ors], %[oc0] offen offset:0 sc1\n\t" \
  "v_cvt_pk_bf16_f32 v24, v24, v25\n\t" \
  "v_cvt_pk_bf16_f32 v25, v26, v27\n\t" \
  "v_cvt_pk_bf16_f32 v26, v28, v29\n\t" \
  "v_cvt_pk_bf16_f32 v27, v30, v31\n\t" \
  "buffer_store_dwordx4 v[24:27], %[vo], %[ors], %[oc0] offen offset:192 sc1\n\t" \
  "v_cvt_pk_bf16_f32 v48, v48, v49\n\t" \
  "v_cvt_pk_bf16_f32 v49, v50, v51\n\t" \
  "v_cvt_pk_bf16_f32 v50, v52, v53\n\t" \
  "v_cvt_pk_bf16_f32 v51, v54, v55\n\t" \
  "buffer_store_dwordx4 v[48:51], %[vo], %[ors], %[oc1] offen offset:0 sc1\n\t" \
  "v_cvt_pk_bf16_f32 v72, v72, v73\n\t" \
  "v_cvt_pk_bf16_f32 v73, v74, v75\n\t" \
  "v_cvt_pk_bf16_f32 v74, v76, v77\n\t" \
  "v_cvt_pk_bf16_f32 v75, v78, v79\n\t" \
  "buffer_store_dwordx4 v[72:75], %[vo], %[ors], %[oc1] offen offset:192 sc1\n\t" \
  "s_waitcnt lgkmcnt(1)\n\t" \
  "v_mfma_f32_16x16x32_bf16 v[12:15], v[212:215], v[178:181], v[12:15]\n\t" \
  "ds_read_b64_tr_b16 v[210:211], %[a2] offset:1664\n\t" \
  "v_mfma_f32_16x16x32_bf16 v[36:39], v[212:215], v[186:189], v[36:39]\n\t" \
  "v_mfma_f32_16x16x32_bf16 v[60:63], v[212:215], v[194:197], v[60:63]\n\t" \
  "v_mfma_f32_16x16x32_bf16 v[84:87], v[212:215], v[202:205], v[84:87]\n\t" \
  "ds_read_b64_tr_b16 v[212:213], %[a2] offset:136\n\t" \
  "s_waitcnt lgkmcnt(1)\n\t" \
  "v_mfma_f32_16x16x32_bf16 v[16:19], v[208:211], v[178:181], v[16:19]\n\t" \
  "ds_read_b64_tr_b16 v[214:215], %[a2] offset:1672\n\t" \
  "v_mfma_f32_16x16x32_bf16 v[40:43], v[208:211], v[186:189], v[40:43]\n\t" \
  "v_mfma_f32_16x16x32_bf16 v[64:67], v[208:211], v[194:197], v[64:67]\n\t" \
  "v_mfma_f32_16x16x32_bf16 v[88:91], v[208:211], v[202:205], v[88:91]\n\t" \
  "v_cvt_pk_bf16_f32 v8, v8, v9\n\t" \
  "v_cvt_pk_bf16_f32 v9, v10, v11\n\t" \
  "v_cvt_pk_bf16_f32 v10, v12, v13\n\t" \
  "v_cvt_pk_bf16_f32 v11, v14, v15\n\t" \
  "buffer_store_dwordx4 v[8:11], %[vo], %[ors], %[oc0] offen offset:64 sc1\n\t" \
  "v_cvt_pk_bf16_f32 v32, v32, v33\n\t" \
  "v_cvt_pk_bf16_f32 v33, v34, v35\n\t" \
  "v_cvt_pk_bf16_f32 v34, v36, v37\n\t" \
  "v_cvt_pk_bf16_f32 v35, v38, v39\n\t" \
  "buffer_store_dwordx4 v[32:35], %[vo], %[ors], %[oc0] offen offset:256 sc1\n\t" \
  "v_cvt_pk_bf16_f32 v56, v56, v57\n\t" \
  "v_cvt_pk_bf16_f32 v57, v58, v59\n\t" \
  "v_cvt_pk_bf16_f32 v58, v60, v61\n\t" \
  "v_cvt_pk_bf16_f32 v59, v62, v63\n\t" \
  "buffer_store_dwordx4 v[56:59], %[vo], %[ors], %[oc1] offen offset:64 sc1\n\t" \
  "v_cvt_pk_bf16_f32 v80, v80, v81\n\t" \
  "v_cvt_pk_bf16_f32 v81, v82, v83\n\t" \
  "v_cvt_pk_bf16_f32 v82, v84, v85\n\t" \
  "v_cvt_pk_bf16_f32 v83, v86, v87\n\t" \
  "buffer_store_dwordx4 v[80:83], %[vo], %[ors], %[oc1] offen offset:256 sc1\n\t" \
  "s_waitcnt lgkmcnt(0)\n\t" \
  "v_mfma_f32_16x16x32_bf16 v[20:23], v[212:215], v[178:181], v[20:23]\n\t" \
  "v_mfma_f32_16x16x32_bf16 v[44:47], v[212:215], v[186:189], v[44:47]\n\t" \
  "v_mfma_f32_16x16x32_bf16 v[68:71], v[212:215], v[194:197], v[68:71]\n\t" \
  "v_mfma_f32_16x16x32_bf16 v[92:95], v[212:215], v[202:205], v[92:95]\n\t" \
  "v_cvt_pk_bf16_f32 v16, v16, v17\n\t" \
  "v_cvt_pk_bf16_f32 v17, v18, v19\n\t" \
  "s_nop 2\n\t" \
  "v_cvt_pk_bf16_f32 v18, v20, v21\n\t" \
  "v_cvt_pk_bf16_f32 v19, v22, v23\n\t" \
  "buffer_store_dwordx4 v[16:19], %[vo], %[ors], %[oc0] offen offset:128 sc1\n\t" \
  "v_cvt_pk_bf16_f32 v40, v40, v41\n\t" \
  "v_cvt_pk_bf16_f32 v41, v42, v43\n\t" \
  "v_cvt_pk_bf16_f32 v42, v44, v45\n\t" \
  "v_cvt_pk_bf16_f32 v43, v46, v47\n\t" \
  "buffer_store_dwordx4 v[40:43], %[vo], %[ors], %[oc0] offen offset:320 sc1\n\t" \
  "v_cvt_pk_bf16_f32 v64, v64, v65\n\t" \
  "v_cvt_pk_bf16_f32 v65, v66, v67\n\t" \
  "v_cvt_pk_bf16_f32 v66, v68, v69\n\t" \
  "v_cvt_pk_bf16_f32 v67, v70, v71\n\t" \
  "buffer_store_dwordx4 v[64:67], %[vo], %[ors], %[oc1] offen offset:128 sc1\n\t" \
  "v_cvt_pk_bf16_f32 v88, v88, v89\n\t" \
  "v_cvt_pk_bf16_f32 v89, v90, v91\n\t" \
  "v_cvt_pk_bf16_f32 v90, v92, v93\n\t" \
  "v_cvt_pk_bf16_f32 v91, v94, v95\n\t" \
  "buffer_store_dwordx4 v[88:91], %[vo], %[ors], %[oc1] offen offset:320 sc1\n\t" \
  ""


__device__ __forceinline__ unsigned lds_addr(const char* p) { return (unsigned)(uintptr_t)(lds_ptr_t)p; }
__device__ __forceinline__ unsigned sg(unsigned v) { return __builtin_amdgcn_readfirstlane(v); }

template <bool NT> __device__ __forceinline__ void dma16(u32x4 rsrc, unsigned voff, unsigned soff, unsigned lds_dst) {
    if constexpr (NT)
        asm volatile("s_mov_b32 m0, %1\n\ts_nop 0\n\tbuffer_load_dwordx4 %0, %2, %3 offen nt lds"
                     : : "v"(voff), "s"(lds_dst), "s"(rsrc), "s"(soff) : "memory");
    else
        asm volatile("s_mov_b32 m0, %1\n\ts_nop 0\n\tbuffer_load_dwordx4 %0, %2, %3 offen lds"
                     : : "v"(voff), "s"(lds_dst), "s"(rsrc), "s"(soff) : "memory");
}
__device__ __forceinline__ void warm(u32x4 rsrc, unsigned voff, unsigned soff, unsigned lds_dst) {
    asm volatile("s_mov_b32 m0, %1\n\ts_nop 0\n\tbuffer_load_dword %0, %2, %3 offen lds"
                 : : "v"(voff), "s"(lds_dst), "s"(rsrc), "s"(soff) : "memory");
}
__device__ __forceinline__ u32x4 make_rsrc(const void* p, unsigned bytes) {
    const uint64_t a = (uint64_t)(uintptr_t)p;
    u32x4 r = {sg((unsigned)a), sg((unsigned)(a >> 32) & 0xffffu), sg(bytes), 0x00020000u};
    return r;
}

#define V10(a) "v" #a "0", "v" #a "1", "v" #a "2", "v" #a "3", "v" #a "4", "v" #a "5", "v" #a "6", "v" #a "7", "v" #a "8", "v" #a "9"
#define MAIN_CLOB "memory", "scc", V10(), "vcc", V10(1), V10(2), V10(3), V10(4), V10(5), V10(6), V10(7), V10(8), V10(9), V10(10), V10(11), \
    V10(12), V10(13), V10(14), V10(15), V10(16), V10(17), V10(18), V10(19), V10(20), V10(21), V10(22),                            \
    V10(23)

__global__ __launch_bounds__(512, 2) void convnext_kernel(const __hip_bfloat16* __restrict__ x,
                                                          const __hip_bfloat16* __restrict__ dw,
                                                          const __hip_bfloat16* __restrict__ w1,
                                                          const __hip_bfloat16* __restrict__ w2,
                                                          __hip_bfloat16* __restrict__ out) {
    __shared__ __attribute__((aligned(16))) char smem[LDS_BYTES];
    const int tid = threadIdx.x;
    const int lane = tid & 63;
    const int wid = sg(tid >> 6);
    const int bid = blockIdx.x;

    const int x0 = ((bid >> 3) & 15) * 32, low = bid >> 7;
    const int ym = (bid & 7) * 64 + 31 + low;
    const int rs = low ? ROWB : -ROWB;
    const unsigned sbase = lds_addr(smem);
    asm volatile("s_setreg_imm32_b32 hwreg(HW_REG_MODE, 4, 2), 0\n\ts_setreg_imm32_b32 hwreg(HW_REG_MODE, 9, 1), 0\n\ts_nop 1");

    const u32x4 xrs = make_rsrc(x, IMG * ROWB);
    const bool isw1 = wid < 2;
    const u32x4 wrs = make_rsrc(isw1 ? w1 : w2, CH * HID * 2);
    const u32x4 ors = make_rsrc(out, IMG * ROWB);
    unsigned t[7];
    const int ks = tid & 7, kc = tid >> 3;
    if (wid < 6) {
        const __hip_bfloat16* src = dw + (ks == 0 ? 0 : low ? ks - 1 : 7 - ks) * (7 * CH) + 2 * kc;
#pragma unroll
        for (int j = 0; j < 7; ++j)
            asm volatile("global_load_dword %0, %1, off offset:%2" : "=&v"(t[j]) : "v"(src), "n"(j * CH * 2) : "memory");
    } else {
        unsigned long long pc;
        unsigned len;
        asm volatile("s_getpc_b64 %0\n.Lcnx_pc:\n\ts_mov_b32 %1, .Lcnx_end-.Lcnx_pc" : "=s"(pc), "=s"(len));
        if (wid == 6) warm(make_rsrc((const void*)pc, len & ~3u), lane < 20 ? lane * 64 : OOB, (bid >> 3) * 1280, sbase + PFOFF);
        warm(make_rsrc(wid == 6 ? w2 : w1, CH * HID * 2), lane < 36 ? lane * 64 : OOB, (bid >> 3) * 2304, sbase + PFOFF);
#pragma unroll
        for (int j = 0; j < 7; ++j) t[j] = 0;
    }
    const int pp = tid / 25, s = tid - pp * 25;
    const int gx = x0 - 3 + 2 * pp + (s >= 12);
    const unsigned xv = (s < 24 && pp < 19 && (unsigned)gx < (unsigned)IMG) ? (unsigned)((x0 - 3 + 2 * pp) * (CH * 2) + s * 16) : OOB;
    unsigned xo = (unsigned)(ym * ROWB - 3 * rs);
    const unsigned xl = sbase + XOFF + wid * 1024;
#pragma unroll
    for (int r = 0; r < 6; ++r) dma16<true>(xrs, xv, xo + r * rs, xl + r * XROW);


    const int G = lane >> 4, n = lane & 15, q = n >> 2, p = lane & 3;
    const unsigned warmv = lane < 48 ? (unsigned)(x0 * (CH * 2) + lane * 128) : OOB;
    unsigned a1 = sbase + W1OFF + (lane >> 2) * 32 + (p & 1) * 16 + (p >> 1) * 8;
    unsigned a2 = sbase + W2OFF + (G & 1) * 3080 + ((G >> 1) * 4 + q) * 192 + p * 16;
    unsigned wv = wid >= 4 ? warmv : isw1 ? (unsigned)((12 * ((lane >> 3) & 3) + ((lane >> 1) & 3)) * (HID * 2) + (2 * (lane & 1) + (lane >> 5)) * 16)
                                          : (unsigned)(lane * 16);
    const unsigned wpd = isw1 ? 4 * HID * 2 - 1024 : 0, wc = isw1 ? 64 : 6144;
    unsigned so0 = (wid & 1) * (isw1 ? 48 * HID * 2 : 3072), so1 = so0 + wpd, so2 = so1 + wpd;
    const unsigned wl = sbase + (isw1 ? W1OFF + (wid & 1) * 3072 : W2OFF + (wid & 1) * 3080);
    u32x2 ca = {sbase + XOFF + n * 400 + (G & 1) * XROW + (G >> 1) * 192,
                sbase + DWOFF + p * 192 + (1 - (q >> 1)) * 24 + (1 + (G & 1) - (q & 1)) * 8 + (G >> 1) * 4};
    if (wid == 7) {
        *(u32x2*)(smem + TABOFF + lane * 8) = ca;
        if (lane < 32) *(unsigned*)(smem + CNTOFF + lane * 4) = 0;
    }
    ca += (u32x2){(unsigned)wid * 24, (unsigned)wid * 2304};
    unsigned vo = n * 384 + G * 16;
    unsigned l8 = sbase + PB + lane * 8;
    const unsigned one = G == q ? (p & 1 ? 0x3f800000u : 0x3f80u) : 0u;
    u32x2 id2 = {p < 2 ? one : 0u, p < 2 ? 0u : one};
    const u32x2 zz = {0u, 0u};
    unsigned dwa = sbase + DWOFF + kc * 384 + ks * 24;
    asm volatile("" : "+v"(a1), "+v"(a2), "+v"(wv), "+v"(ca), "+v"(vo), "+v"(l8), "+v"(id2), "+v"(dwa));
    unsigned oc0 = (unsigned)(ym * ROWB + 2 * wid * rs + x0 * (CH * 2)), oc1 = oc0 + rs, op0, op1;
    unsigned rnd, f0 = wid, t0, tg;
    so0 = sg(so0); so1 = sg(so1); so2 = sg(so2); oc0 = sg(oc0); oc1 = sg(oc1);
#pragma unroll
    for (int r = 6; r < 9; ++r) dma16<true>(xrs, xv, xo + r * rs, xl + r * XROW);

    asm volatile("s_waitcnt vmcnt(9)" ::: "memory");

#pragma unroll
    for (int j = 0; j < 7; ++j) {
        asm volatile("" : "+v"(t[j]));
        if (ks == 0) t[j] = 0;
    }
    if (tid <= 384) {
#pragma unroll
        for (int c = 0; c < 2; ++c) {
            const unsigned sel = c ? 0x07060302u : 0x05040100u;
            auto pk = [&](unsigned a, unsigned b) { return __builtin_amdgcn_perm(b, a, sel); };
            char* bp = smem + (dwa - sbase) + (tid == 384 ? 0 : c * 192);
            *(u32x2*)(bp) = (u32x2){pk(0u, t[1]), pk(t[3], t[5])};
            *(u32x2*)(bp + 8) = (u32x2){pk(t[0], t[2]), pk(t[4], t[6])};
            *(u32x2*)(bp + 16) = (u32x2){pk(t[1], t[3]), pk(t[5], 0u)};
        }
    }
    if (wid >= 4) __builtin_amdgcn_s_setprio(1);

    asm volatile("s_waitcnt lgkmcnt(0)\n\ts_barrier" ::: "memory");

#pragma unroll
    for (int r = 9; r < 12; ++r) dma16<true>(xrs, xv, xo + r * rs, xl + r * XROW);
    const unsigned wb = (13 - 2) * rs;
    xo = sg(xo + 11 * rs);
    asm volatile(MAIN_ASM
                 : [xo] "+&s"(xo), [oc0] "+&s"(oc0), [oc1] "+&s"(oc1), [op0] "=&s"(op0), [op1] "=&s"(op1), [rnd] "=&s"(rnd), [f0] "+&s"(f0),
                   [so0] "+&s"(so0), [so1] "+&s"(so1), [so2] "+&s"(so2), [t0] "=&s"(t0), [tg] "=&s"(tg), "+{v[246:247]}"(ca)
                 : [a1] "v"(a1), [a2] "v"(a2), [wv] "v"(wv), [xv] "v"(xv), [h2] "v"(GH2),
                   [vo] "v"(vo), [l8] "v"(l8), "{v[240:241]}"(zz), "{v[242:243]}"(id2), "{v[244:245]}"(zz),
                   [xrs] "s"(xrs), [ors] "s"(ors), [wrs] "s"(wrs), [xl] "s"(sg(xl)), [rs] "s"(sg(rs)), [rs6] "s"(sg(6 * rs)), [dy] "s"(sg(16 * rs)), [wb] "s"(sg(wb)),
                   [zb] "s"(sg(XOFF - 8 * PB - 7 * sbase + wid * 12288)), [lb] "s"(sg(-2 * PB - sbase)), [m11] "s"(0xba000000u),
                   [grp] "s"(sg(wid >> 2)), [sr] "s"(sg((wid & 3) * 4096 + (wid < 4 ? 16400 : 0))), [w512] "s"(sg(wid * 512)),
                   [z1] "s"(sg(XOFF - 2 * PB - sbase + wid * 1024)), [z2] "s"(sg(XOFF - 2 * PB - sbase + (8 + (wid & 3)) * 1024 + (wid >> 2) * 8)),
                   [wl] "s"(sg(wl)), [ws] "s"(sg(isw1 ? 6144 : 6160)), [wc] "s"(sg(wc)), [wq] "s"(sg(wid >> 1)), [c24] "s"(sg(wid * 24)), [c23] "s"(sg(wid * 2304)),
                   [pf] "s"(sg(sbase + PFOFF)), [h1] "s"(GH1), [hh] "s"(GHH), [mk] "s"(0x3ffcu)
                 : MAIN_CLOB);
    asm volatile("s_waitcnt vmcnt(0)\n.Lcnx_end:" ::: "memory");

}

}

extern "C" void kernel_launch(void* const* d_in, const int* in_sizes, int n_in, void* d_out, int out_size,
                              void* d_ws, size_t ws_size, hipStream_t stream) {
    (void)in_sizes; (void)n_in; (void)out_size; (void)d_ws; (void)ws_size;
    const __hip_bfloat16* x = (const __hip_bfloat16*)d_in[0];
    const __hip_bfloat16* dw = (const __hip_bfloat16*)d_in[1];
    const __hip_bfloat16* w1 = (const __hip_bfloat16*)d_in[2];
    const __hip_bfloat16* w2 = (const __hip_bfloat16*)d_in[3];
    __hip_bfloat16* out = (__hip_bfloat16*)d_out;
    convnext_kernel<<<256, 512, 0, stream>>>(x, dw, w1, w2, out);
}
```

```cpp
#include <hip/hip_runtime.h>
#include <hip/hip_bf16.h>
#include <stdint.h>

namespace {

typedef __attribute__((ext_vector_type(4))) float f32x4;
typedef __attribute__((ext_vector_type(4))) unsigned u32x4;
typedef __attribute__((ext_vector_type(8))) unsigned u32x8;
typedef __attribute__((ext_vector_type(2))) unsigned u32x2;
typedef __attribute__((address_space(3))) void* lds_ptr_t;

constexpr int IMG = 512;
constexpr int CH = 96;
constexpr int HID = 384;
constexpr int ROWB = IMG * CH * 2;
constexpr int LUTN = 3841;
constexpr int DWOFF = 15376;
constexpr int XOFF = DWOFF + 96 * 192 + 32;
constexpr int XROW = 8200;
constexpr int PB = XOFF + 10 * XROW;
constexpr int WB = XOFF + 9 * XROW;
constexpr int WA = 3 * XROW;
constexpr int TABOFF = WB + WA + 18448 + 12288;
constexpr int CNTOFF = TABOFF + 512;
constexpr int PFOFF = CNTOFF + 144;
constexpr int LDS_BYTES = 160 * 1024;
static_assert(PB == 115840 && CNTOFF - PB == 47648 && LUTN * 4 <= DWOFF && XOFF % 16 == 0 && PFOFF + 48 * 4 <= LDS_BYTES, "LDS map");
constexpr unsigned OOB = 0x40000000u;
constexpr unsigned GH1 = 0xcc9bcc9bu, GH2 = 0xd297d297u, GHH = 0x30003000u;

#define XA_ASM \
  "s_add_u32 m0, %[xl], 0\n\t" \
  "s_add_u32 %[xo], %[xo], %[rs]\n\t" \
  "buffer_load_dwordx4 %[xv], %[xrs], %[xo] offen nt lds\n\t" \
  "s_add_u32 m0, %[xl], 8200\n\t" \
  "s_add_u32 %[xo], %[xo], %[rs]\n\t" \
  "buffer_load_dwordx4 %[xv], %[xrs], %[xo] offen nt lds\n\t" \
  "s_add_u32 m0, %[xl], 16400\n\t" \
  "s_add_u32 %[xo], %[xo], %[rs]\n\t" \
  "buffer_load_dwordx4 %[xv], %[xrs], %[xo] offen nt lds\n\t" \
  "s_add_u32 m0, %[xl], 24600\n\t" \
  "s_add_u32 %[xo], %[xo], %[rs]\n\t" \
  "buffer_load_dwordx4 %[xv], %[xrs], %[xo] offen nt lds\n\t" \
  ""
#define XB_ASM \
  "s_add_u32 m0, %[xl], 32800\n\t" \
  "s_add_u32 %[xo], %[xo], %[rs]\n\t" \
  "buffer_load_dwordx4 %[xv], %[xrs], %[xo] offen nt lds\n\t" \
  "s_add_u32 m0, %[xl], 41000\n\t" \
  "s_add_u32 %[xo], %[xo], %[rs]\n\t" \
  "buffer_load_dwordx4 %[xv], %[xrs], %[xo] offen nt lds\n\t" \
  "s_add_u32 m0, %[xl], 49200\n\t" \
  "s_add_u32 %[xo], %[xo], %[rs]\n\t" \
  "buffer_load_dwordx4 %[xv], %[xrs], %[xo] offen nt lds\n\t" \
  "s_add_u32 m0, %[xl], 57400\n\t" \
  "s_add_u32 %[xo], %[xo], %[rs]\n\t" \
  "buffer_load_dwordx4 %[xv], %[xrs], %[xo] offen nt lds\n\t" \
  ""
#define MAIN_ASM \
  "s_mov_b32 %[tg], 3072\n\t" \
  "s_mov_b32 %[rnd], 0\n\t" \
  "s_mov_b32 %[d1], 0\n\t" \
  "s_mov_b32 %[d2], 0\n\t" \
  "s_mov_b64 vcc, 0\n\t" \
  "s_mov_b32 %[op0], 0x40000000\n\t" \
  "s_mov_b32 %[op1], 0x40000000\n\t" \
  "v_mfma_f32_32x32x16_bf16 v[144:159], v[242:245], v[240:243], 0\n\t" \
  "v_mfma_f32_32x32x16_bf16 v[160:175], v[242:245], v[240:243], 0\n\t" \
  "s_waitcnt lgkmcnt(0)\n\t" \
  "s_barrier\n\t" \
  "v_add_u32 v232, 1536, v247\n\t" \
  "ds_read2_b32 v[98:99], v247 offset0:0 offset1:6\n\t" \
  "ds_read2_b32 v[102:103], v247 offset0:12 offset1:18\n\t" \
  "ds_read2_b32 v[106:107], v247 offset0:24 offset1:30\n\t" \
  "ds_read2_b32 v[110:111], v247 offset0:36 offset1:42\n\t" \
  "ds_read2_b32 v[114:115], v247 offset0:192 offset1:198\n\t" \
  "ds_read2_b32 v[118:119], v247 offset0:204 offset1:210\n\t" \
  "ds_read2_b32 v[122:123], v247 offset0:216 offset1:222\n\t" \
  "ds_read2_b32 v[126:127], v247 offset0:228 offset1:234\n\t" \
  "ds_read2_b32 v[130:131], v232 offset0:0 offset1:6\n\t" \
  "ds_read2_b32 v[134:135], v232 offset0:12 offset1:18\n\t" \
  "ds_read2_b32 v[138:139], v232 offset0:24 offset1:30\n\t" \
  "ds_read2_b32 v[142:143], v232 offset0:36 offset1:42\n\t" \
  "s_add_u32 m0, %[xl], 65600\n\t" \
  "s_add_u32 %[xo], %[xo], %[rs]\n\t" \
  "buffer_load_dwordx4 %[xv], %[xrs], %[xo] offen nt lds\n\t" \
  "s_add_u32 m0, %[xl], 73800\n\t" \
  "s_add_u32 %[xo], %[xo], %[rs]\n\t" \
  "buffer_load_dwordx4 %[xv], %[xrs], %[xo] offen nt lds\n\t" \
  "s_add_u32 m0, %[xl], 82000\n\t" \
  "s_add_u32 %[xo], %[xo], %[rs]\n\t" \
  "buffer_load_dwordx4 %[xv], %[xrs], %[xo] offen nt lds\n\t" \
  "s_add_u32 m0, %[xl], 90200\n\t" \
  "s_add_u32 %[xo], %[xo], %[rs]\n\t" \
  "buffer_load_dwordx4 %[xv], %[xrs], %[xo] offen nt lds\n\t" \
  "s_waitcnt lgkmcnt(11)\n\t" \
  "v_and_b32 v96, 0xffff, v98\n\t" \
  "v_and_b32 v97, 0xffff0000, v98\n\t" \
  "v_and_b32 v98, 0xffff, v99\n\t" \
  "v_and_b32 v99, 0xffff0000, v99\n\t" \
  "s_waitcnt lgkmcnt(10)\n\t" \
  "v_and_b32 v100, 0xffff, v102\n\t" \
  "v_and_b32 v101, 0xffff0000, v102\n\t" \
  "v_and_b32 v102, 0xffff, v103\n\t" \
  "v_and_b32 v103, 0xffff0000, v103\n\t" \
  "s_waitcnt lgkmcnt(9)\n\t" \
  "v_and_b32 v104, 0xffff, v106\n\t" \
  "v_and_b32 v105, 0xffff0000, v106\n\t" \
  "v_and_b32 v106, 0xffff, v107\n\t" \
  "v_and_b32 v107, 0xffff0000, v107\n\t" \
  "s_waitcnt lgkmcnt(8)\n\t" \
  "v_and_b32 v108, 0xffff, v110\n\t" \
  "v_and_b32 v109, 0xffff0000, v110\n\t" \
  "v_and_b32 v110, 0xffff, v111\n\t" \
  "v_and_b32 v111, 0xffff0000, v111\n\t" \
  "s_waitcnt lgkmcnt(7)\n\t" \
  "v_and_b32 v112, 0xffff, v114\n\t" \
  "v_and_b32 v113, 0xffff0000, v114\n\t" \
  "v_and_b32 v114, 0xffff, v115\n\t" \
  "v_and_b32 v115, 0xffff0000, v115\n\t" \
  "s_waitcnt lgkmcnt(6)\n\t" \
  "v_and_b32 v116, 0xffff, v118\n\t" \
  "v_and_b32 v117, 0xffff0000, v118\n\t" \
  "v_and_b32 v118, 0xffff, v119\n\t" \
  "v_and_b32 v119, 0xffff0000, v119\n\t" \
  "s_waitcnt lgkmcnt(5)\n\t" \
  "v_and_b32 v120, 0xffff, v122\n\t" \
  "v_and_b32 v121, 0xffff0000, v122\n\t" \
  "v_and_b32 v122, 0xffff, v123\n\t" \
  "v_and_b32 v123, 0xffff0000, v123\n\t" \
  "s_waitcnt lgkmcnt(4)\n\t" \
  "v_and_b32 v124, 0xffff, v126\n\t" \
  "v_and_b32 v125, 0xffff0000, v126\n\t" \
  "v_and_b32 v126, 0xffff, v127\n\t" \
  "v_and_b32 v127, 0xffff0000, v127\n\t" \
  "s_waitcnt lgkmcnt(3)\n\t" \
  "v_and_b32 v128, 0xffff, v130\n\t" \
  "v_and_b32 v129, 0xffff0000, v130\n\t" \
  "v_and_b32 v130, 0xffff, v131\n\t" \
  "v_and_b32 v131, 0xffff0000, v131\n\t" \
  "s_waitcnt lgkmcnt(2)\n\t" \
  "v_and_b32 v132, 0xffff, v134\n\t" \
  "v_and_b32 v133, 0xffff0000, v134\n\t" \
  "v_and_b32 v134, 0xffff, v135\n\t" \
  "v_and_b32 v135, 0xffff0000, v135\n\t" \
  "s_waitcnt lgkmcnt(1)\n\t" \
  "v_and_b32 v136, 0xffff, v138\n\t" \
  "v_and_b32 v137, 0xffff0000, v138\n\t" \
  "v_and_b32 v138, 0xffff, v139\n\t" \
  "v_and_b32 v139, 0xffff0000, v139\n\t" \
  "s_waitcnt lgkmcnt(0)\n\t" \
  "v_and_b32 v140, 0xffff, v142\n\t" \
  "v_and_b32 v141, 0xffff0000, v142\n\t" \
  "v_and_b32 v142, 0xffff, v143\n\t" \
  "v_and_b32 v143, 0xffff0000, v143\n\t" \
  "v_bfe_u32 v236, %[l8], 3, 2\n\t" \
  "v_mul_u32_u24 v236, 0x5555, v236\n\t" \
  "s_waitcnt vmcnt(10)\n\t" \
  "s_barrier\n\t" \
  ".Lcnx_round:\n\t" \
  "v_add_u32 v232, 49200, v246\n\t" \
  "v_add_u32 v233, %[w512], %[l8]\n\t" \
  "v_mfma_f32_32x32x16_bf16 v[176:191], v[242:245], v[240:243], 0\n\t" \
  "ds_read_b64 v[208:209], v246 offset:0\n\t" \
  "ds_read_b64 v[210:211], v246 offset:400\n\t" \
  "ds_read_b64 v[212:213], v246 offset:800\n\t" \
  "ds_read_b64 v[214:215], v246 offset:1200\n\t" \
  "ds_read_b64 v[216:217], v246 offset:8\n\t" \
  "ds_read_b64 v[218:219], v246 offset:408\n\t" \
  "ds_read_b64 v[220:221], v246 offset:808\n\t" \
  "ds_read_b64 v[222:223], v246 offset:1208\n\t" \
  "ds_read_b64 v[224:225], v246 offset:16\n\t" \
  "ds_read_b64 v[226:227], v246 offset:416\n\t" \
  "ds_read_b64 v[228:229], v246 offset:816\n\t" \
  "ds_read_b64 v[230:231], v246 offset:1216\n\t" \
  "s_waitcnt lgkmcnt(8)\n\t" \
  "v_smfmac_f32_16x16x64_bf16 v[144:147], v[96:99], v[208:215], v236\n\t" \
  "s_waitcnt lgkmcnt(4)\n\t" \
  "v_smfmac_f32_16x16x64_bf16 v[148:151], v[112:115], v[216:223], v236\n\t" \
  "s_waitcnt vmcnt(5) lgkmcnt(0)\n\t" \
  "s_barrier\n\t" \
  "ds_read_b64 v[208:209], v246 offset:16400\n\t" \
  "ds_read_b64 v[210:211], v246 offset:16800\n\t" \
  "ds_read_b64 v[212:213], v246 offset:17200\n\t" \
  "ds_read_b64 v[214:215], v246 offset:17600\n\t" \
  "ds_read_b64 v[216:217], v246 offset:16408\n\t" \
  "ds_read_b64 v[218:219], v246 offset:16808\n\t" \
  "ds_read_b64 v[220:221], v246 offset:17208\n\t" \
  "ds_read_b64 v[222:223], v246 offset:17608\n\t" \
  "s_cmp_lg_u32 %[rnd], 1\n\t" \
  "s_cbranch_scc1 .Lcnx_c1\n\t" \
  "s_add_u32 m0, %[xl], 73800\n\t" \
  "s_add_u32 %[xo], %[xo], %[rs]\n\t" \
  "buffer_load_dwordx4 %[xv], %[xrs], %[xo] offen lds\n\t" \
  "s_add_u32 m0, %[xl], 82000\n\t" \
  "s_add_u32 %[xo], %[xo], %[rs]\n\t" \
  "buffer_load_dwordx4 %[xv], %[xrs], %[xo] offen lds\n\t" \
  "s_add_u32 m0, %[xl], 90200\n\t" \
  "s_add_u32 %[xo], %[xo], %[rs]\n\t" \
  "buffer_load_dwordx4 %[xv], %[xrs], %[xo] offen lds\n\t" \
  ".Lcnx_c1:\n\t" \
  "s_add_u32 m0, %[xl], 0\n\t" \
  "s_add_u32 %[xo], %[xo], %[rs]\n\t" \
  "buffer_load_dwordx4 %[xv], %[xrs], %[xo] offen lds\n\t" \
  "s_add_u32 m0, %[xl], 8200\n\t" \
  "s_add_u32 %[xo], %[xo], %[rs]\n\t" \
  "buffer_load_dwordx4 %[xv], %[xrs], %[xo] offen lds\n\t" \
  "v_smfmac_f32_16x16x64_bf16 v[152:155], v[128:131], v[224:231], v236\n\t" \
  "ds_read_b64 v[224:225], v246 offset:16416\n\t" \
  "ds_read_b64 v[226:227], v246 offset:16816\n\t" \
  "ds_read_b64 v[228:229], v246 offset:17216\n\t" \
  "s_waitcnt lgkmcnt(7)\n\t" \
  "v_smfmac_f32_16x16x64_bf16 v[156:159], v[96:99], v[208:215], v236\n\t" \
  "ds_read_b64 v[230:231], v246 offset:17616\n\t" \
  "v_smfmac_f32_16x16x64_bf16 v[144:147], v[100:103], v[208:215], v236\n\t" \
  "s_waitcnt lgkmcnt(4)\n\t" \
  "v_smfmac_f32_16x16x64_bf16 v[160:163], v[112:115], v[216:223], v236\n\t" \
  "v_smfmac_f32_16x16x64_bf16 v[148:151], v[116:119], v[216:223], v236\n\t" \
  "s_waitcnt vmcnt(8) lgkmcnt(0)\n\t" \
  "s_barrier\n\t" \
  "ds_read_b64 v[208:209], v246 offset:32800\n\t" \
  "ds_read_b64 v[210:211], v246 offset:33200\n\t" \
  "ds_read_b64 v[212:213], v246 offset:33600\n\t" \
  "ds_read_b64 v[214:215], v246 offset:34000\n\t" \
  "ds_read_b64 v[216:217], v246 offset:32808\n\t" \
  "ds_read_b64 v[218:219], v246 offset:33208\n\t" \
  "ds_read_b64 v[220:221], v246 offset:33608\n\t" \
  "ds_read_b64 v[222:223], v246 offset:34008\n\t" \
  "s_add_u32 m0, %[xl], 16400\n\t" \
  "s_add_u32 %[xo], %[xo], %[rs]\n\t" \
  "buffer_load_dwordx4 %[xv], %[xrs], %[xo] offen lds\n\t" \
  "s_add_u32 m0, %[xl], 24600\n\t" \
  "s_add_u32 %[xo], %[xo], %[rs]\n\t" \
  "buffer_load_dwordx4 %[xv], %[xrs], %[xo] offen lds\n\t" \
  "v_smfmac_f32_16x16x64_bf16 v[164:167], v[128:131], v[224:231], v236\n\t" \
  "v_cvt_pk_bf16_f32 v48, v48, v49\n\t" \
  "v_cvt_pk_bf16_f32 v49, v50, v51\n\t" \
  "v_cvt_pk_bf16_f32 v50, v52, v53\n\t" \
  "v_cvt_pk_bf16_f32 v51, v54, v55\n\t" \
  "v_cvt_pk_bf16_f32 v52, v0, v1\n\t" \
  "v_cvt_pk_bf16_f32 v53, v2, v3\n\t" \
  "v_smfmac_f32_16x16x64_bf16 v[152:155], v[132:135], v[224:231], v236\n\t" \
  "ds_read_b64 v[224:225], v246 offset:32816\n\t" \
  "ds_read_b64 v[226:227], v246 offset:33216\n\t" \
  "ds_read_b64 v[228:229], v246 offset:33616\n\t" \
  "v_cvt_pk_bf16_f32 v54, v4, v5\n\t" \
  "v_cvt_pk_bf16_f32 v55, v6, v7\n\t" \
  "v_cvt_pk_bf16_f32 v56, v56, v57\n\t" \
  "s_waitcnt lgkmcnt(7)\n\t" \
  "v_smfmac_f32_16x16x64_bf16 v[168:171], v[96:99], v[208:215], v236\n\t" \
  "ds_read_b64 v[230:231], v246 offset:34016\n\t" \
  "v_cvt_pk_bf16_f32 v57, v58, v59\n\t" \
  "v_cvt_pk_bf16_f32 v58, v60, v61\n\t" \
  "v_cvt_pk_bf16_f32 v59, v62, v63\n\t" \
  "v_smfmac_f32_16x16x64_bf16 v[156:159], v[100:103], v[208:215], v236\n\t" \
  "v_cvt_pk_bf16_f32 v60, v8, v9\n\t" \
  "v_cvt_pk_bf16_f32 v61, v10, v11\n\t" \
  "v_cvt_pk_bf16_f32 v62, v12, v13\n\t" \
  "v_smfmac_f32_16x16x64_bf16 v[144:147], v[104:107], v[208:215], v236\n\t" \
  "v_cvt_pk_bf16_f32 v63, v14, v15\n\t" \
  "v_mfma_f32_32x32x16_bf16 v[0:15], v[242:245], v[240:243], 0\n\t" \
  "s_waitcnt lgkmcnt(4)\n\t" \
  "v_smfmac_f32_16x16x64_bf16 v[172:175], v[112:115], v[216:223], v236\n\t" \
  "v_smfmac_f32_16x16x64_bf16 v[160:163], v[116:119], v[216:223], v236\n\t" \
  "v_smfmac_f32_16x16x64_bf16 v[148:151], v[120:123], v[216:223], v236\n\t" \
  "s_waitcnt vmcnt(8) lgkmcnt(0)\n\t" \
  "s_barrier\n\t" \
  "ds_read_b64 v[208:209], v232 offset:0\n\t" \
  "ds_read_b64 v[210:211], v232 offset:400\n\t" \
  "ds_read_b64 v[212:213], v232 offset:800\n\t" \
  "ds_read_b64 v[214:215], v232 offset:1200\n\t" \
  "ds_read_b64 v[216:217], v232 offset:8\n\t" \
  "ds_read_b64 v[218:219], v232 offset:408\n\t" \
  "ds_read_b64 v[220:221], v232 offset:808\n\t" \
  "ds_read_b64 v[222:223], v232 offset:1208\n\t" \
  "s_add_u32 m0, %[xl], 32800\n\t" \
  "s_add_u32 %[xo], %[xo], %[rs]\n\t" \
  "buffer_load_dwordx4 %[xv], %[xrs], %[xo] offen lds\n\t" \
  "s_add_u32 m0, %[xl], 41000\n\t" \
  "s_add_u32 %[xo], %[xo], %[rs]\n\t" \
  "buffer_load_dwordx4 %[xv], %[xrs], %[xo] offen lds\n\t" \
  "v_smfmac_f32_16x16x64_bf16 v[176:179], v[128:131], v[224:231], v236\n\t" \
  "v_smfmac_f32_16x16x64_bf16 v[164:167], v[132:135], v[224:231], v236\n\t" \
  "v_smfmac_f32_16x16x64_bf16 v[152:155], v[136:139], v[224:231], v236\n\t" \
  "ds_read_b64 v[224:225], v232 offset:16\n\t" \
  "s_waitcnt lgkmcnt(5)\n\t" \
  "v_smfmac_f32_16x16x64_bf16 v[180:183], v[96:99], v[208:215], v236\n\t" \
  "ds_read_b64 v[226:227], v232 offset:416\n\t" \
  "v_smfmac_f32_16x16x64_bf16 v[168:171], v[100:103], v[208:215], v236\n\t" \
  "ds_read_b64 v[228:229], v232 offset:816\n\t" \
  "v_cvt_pk_bf16_f32 v64, v64, v65\n\t" \
  "v_cvt_pk_bf16_f32 v65, v66, v67\n\t" \
  "v_cvt_pk_bf16_f32 v66, v68, v69\n\t" \
  "v_cvt_pk_bf16_f32 v67, v70, v71\n\t" \
  "v_cvt_pk_bf16_f32 v68, v16, v17\n\t" \
  "v_cvt_pk_bf16_f32 v69, v18, v19\n\t" \
  "v_smfmac_f32_16x16x64_bf16 v[156:159], v[104:107], v[208:215], v236\n\t" \
  "ds_read_b64 v[230:231], v232 offset:1216\n\t" \
  "v_cvt_pk_bf16_f32 v70, v20, v21\n\t" \
  "v_cvt_pk_bf16_f32 v71, v22, v23\n\t" \
  "v_cvt_pk_bf16_f32 v72, v72, v73\n\t" \
  "v_smfmac_f32_16x16x64_bf16 v[144:147], v[108:111], v[208:215], v236\n\t" \
  "v_cvt_pk_bf16_f32 v73, v74, v75\n\t" \
  "v_cvt_pk_bf16_f32 v74, v76, v77\n\t" \
  "v_cvt_pk_bf16_f32 v75, v78, v79\n\t" \
  "s_waitcnt lgkmcnt(4)\n\t" \
  "v_smfmac_f32_16x16x64_bf16 v[184:187], v[112:115], v[216:223], v236\n\t" \
  "v_cvt_pk_bf16_f32 v76, v24, v25\n\t" \
  "v_cvt_pk_bf16_f32 v77, v26, v27\n\t" \
  "v_cvt_pk_bf16_f32 v78, v28, v29\n\t" \
  "v_smfmac_f32_16x16x64_bf16 v[172:175], v[116:119], v[216:223], v236\n\t" \
  "v_cvt_pk_bf16_f32 v79, v30, v31\n\t" \
  "v_mfma_f32_32x32x16_bf16 v[16:31], v[242:245], v[240:243], 0\n\t" \
  "v_smfmac_f32_16x16x64_bf16 v[160:163], v[120:123], v[216:223], v236\n\t" \
  "v_mul_f32 v193, v144, v144\n\t" \
  "v_add_f32 v192, v144, v145\n\t" \
  "v_fmac_f32 v193, v145, v145\n\t" \
  "v_add_f32 v192, v192, v146\n\t" \
  "v_smfmac_f32_16x16x64_bf16 v[148:151], v[124:127], v[216:223], v236\n\t" \
  "v_fmac_f32 v193, v146, v146\n\t" \
  "v_add_f32 v192, v192, v147\n\t" \
  "v_fmac_f32 v193, v147, v147\n\t" \
  "s_waitcnt vmcnt(8) lgkmcnt(0)\n\t" \
  "s_barrier\n\t" \
  "ds_read_b64 v[208:209], v232 offset:16400\n\t" \
  "ds_read_b64 v[210:211], v232 offset:16800\n\t" \
  "ds_read_b64 v[212:213], v232 offset:17200\n\t" \
  "ds_read_b64 v[214:215], v232 offset:17600\n\t" \
  "ds_read_b64 v[216:217], v232 offset:16408\n\t" \
  "ds_read_b64 v[218:219], v232 offset:16808\n\t" \
  "ds_read_b64 v[220:221], v232 offset:17208\n\t" \
  "ds_read_b64 v[222:223], v232 offset:17608\n\t" \
  "s_add_u32 m0, %[xl], 49200\n\t" \
  "s_add_u32 %[xo], %[xo], %[rs]\n\t" \
  "buffer_load_dwordx4 %[xv], %[xrs], %[xo] offen lds\n\t" \
  "s_add_u32 m0, %[xl], 57400\n\t" \
  "s_add_u32 %[xo], %[xo], %[rs]\n\t" \
  "buffer_load_dwordx4 %[xv], %[xrs], %[xo] offen lds\n\t" \
  "v_smfmac_f32_16x16x64_bf16 v[188:191], v[128:131], v[224:231], v236\n\t" \
  "v_smfmac_f32_16x16x64_bf16 v[176:179], v[132:135], v[224:231], v236\n\t" \
  "v_smfmac_f32_16x16x64_bf16 v[164:167], v[136:139], v[224:231], v236\n\t" \
  "v_add_f32 v192, v192, v148\n\t" \
  "v_fmac_f32 v193, v148, v148\n\t" \
  "v_add_f32 v192, v192, v149\n\t" \
  "v_fmac_f32 v193, v149, v149\n\t" \
  "v_add_f32 v192, v192, v150\n\t" \
  "v_fmac_f32 v193, v150, v150\n\t" \
  "v_smfmac_f32_16x16x64_bf16 v[152:155], v[140:143], v[224:231], v236\n\t" \
  "ds_read_b64 v[224:225], v232 offset:16416\n\t" \
  "v_add_f32 v192, v192, v151\n\t" \
  "v_fmac_f32 v193, v151, v151\n\t" \
  "s_waitcnt lgkmcnt(5)\n\t" \
  "v_smfmac_f32_16x16x64_bf16 v[0:3], v[96:99], v[208:215], v236\n\t" \
  "ds_read_b64 v[226:227], v232 offset:16816\n\t" \
  "v_smfmac_f32_16x16x64_bf16 v[180:183], v[100:103], v[208:215], v236\n\t" \
  "ds_read_b64 v[228:229], v232 offset:17216\n\t" \
  "v_cvt_pk_bf16_f32 v80, v80, v81\n\t" \
  "v_cvt_pk_bf16_f32 v81, v82, v83\n\t" \
  "v_cvt_pk_bf16_f32 v82, v84, v85\n\t" \
  "v_cvt_pk_bf16_f32 v83, v86, v87\n\t" \
  "v_cvt_pk_bf16_f32 v84, v32, v33\n\t" \
  "v_cvt_pk_bf16_f32 v85, v34, v35\n\t" \
  "v_smfmac_f32_16x16x64_bf16 v[168:171], v[104:107], v[208:215], v236\n\t" \
  "ds_read_b64 v[230:231], v232 offset:17616\n\t" \
  "v_add_f32 v192, v192, v152\n\t" \
  "v_cvt_pk_bf16_f32 v86, v36, v37\n\t" \
  "v_fmac_f32 v193, v152, v152\n\t" \
  "v_smfmac_f32_16x16x64_bf16 v[156:159], v[108:111], v[208:215], v236\n\t" \
  "v_add_f32 v192, v192, v153\n\t" \
  "v_cvt_pk_bf16_f32 v87, v38, v39\n\t" \
  "v_fmac_f32 v193, v153, v153\n\t" \
  "s_waitcnt lgkmcnt(4)\n\t" \
  "v_smfmac_f32_16x16x64_bf16 v[4:7], v[112:115], v[216:223], v236\n\t" \
  "v_cvt_pk_bf16_f32 v88, v88, v89\n\t" \
  "v_add_f32 v192, v192, v154\n\t" \
  "v_cvt_pk_bf16_f32 v89, v90, v91\n\t" \
  "v_smfmac_f32_16x16x64_bf16 v[184:187], v[116:119], v[216:223], v236\n\t" \
  "v_cvt_pk_bf16_f32 v90, v92, v93\n\t" \
  "v_fmac_f32 v193, v154, v154\n\t" \
  "v_cvt_pk_bf16_f32 v91, v94, v95\n\t" \
  "v_smfmac_f32_16x16x64_bf16 v[172:175], v[120:123], v[216:223], v236\n\t" \
  "v_cvt_pk_bf16_f32 v92, v40, v41\n\t" \
  "v_add_f32 v192, v192, v155\n\t" \
  "v_cvt_pk_bf16_f32 v93, v42, v43\n\t" \
  "v_smfmac_f32_16x16x64_bf16 v[160:163], v[124:127], v[216:223], v236\n\t" \
  "v_cvt_pk_bf16_f32 v94, v44, v45\n\t" \
  "v_fmac_f32 v193, v155, v155\n\t" \
  "v_cvt_pk_bf16_f32 v95, v46, v47\n\t" \
  "s_waitcnt vmcnt(8) lgkmcnt(0)\n\t" \
  "s_barrier\n\t" \
  "ds_read_b64 v[208:209], v232 offset:32800\n\t" \
  "ds_read_b64 v[210:211], v232 offset:33200\n\t" \
  "ds_read_b64 v[212:213], v232 offset:33600\n\t" \
  "ds_read_b64 v[214:215], v232 offset:34000\n\t" \
  "ds_read_b64 v[216:217], v232 offset:32808\n\t" \
  "ds_read_b64 v[218:219], v232 offset:33208\n\t" \
  "ds_read_b64 v[220:221], v232 offset:33608\n\t" \
  "ds_read_b64 v[222:223], v232 offset:34008\n\t" \
  "s_add_u32 m0, %[xl], 65600\n\t" \
  "s_add_u32 %[xo], %[xo], %[rs]\n\t" \
  "buffer_load_dwordx4 %[xv], %[xrs], %[xo] offen lds\n\t" \
  "s_add_u32 m0, %[xl], 73800\n\t" \
  "s_add_u32 %[xo], %[xo], %[rs]\n\t" \
  "buffer_load_dwordx4 %[xv], %[xrs], %[xo] offen lds\n\t" \
  "v_smfmac_f32_16x16x64_bf16 v[8:11], v[128:131], v[224:231], v236\n\t" \
  "v_mfma_f32_32x32x16_bf16 v[32:47], v[242:245], v[240:243], 0\n\t" \
  "v_mul_f32 v195, v156, v156\n\t" \
  "v_smfmac_f32_16x16x64_bf16 v[188:191], v[132:135], v[224:231], v236\n\t" \
  "ds_write_b64 v233, v[192:193] offset:16400\n\t" \
  "v_add_f32 v194, v156, v157\n\t" \
  "v_fmac_f32 v195, v157, v157\n\t" \
  "v_add_f32 v194, v194, v158\n\t" \
  "v_smfmac_f32_16x16x64_bf16 v[176:179], v[136:139], v[224:231], v236\n\t" \
  "v_fmac_f32 v195, v158, v158\n\t" \
  "v_add_f32 v194, v194, v159\n\t" \
  "v_fmac_f32 v195, v159, v159\n\t" \
  "v_smfmac_f32_16x16x64_bf16 v[164:167], v[140:143], v[224:231], v236\n\t" \
  "ds_read_b64 v[224:225], v232 offset:32816\n\t" \
  "v_add_f32 v194, v194, v160\n\t" \
  "v_fmac_f32 v195, v160, v160\n\t" \
  "v_add_f32 v194, v194, v161\n\t" \
  "s_waitcnt lgkmcnt(6)\n\t" \
  "v_smfmac_f32_16x16x64_bf16 v[12:15], v[96:99], v[208:215], v236\n\t" \
  "ds_read_b64 v[226:227], v232 offset:33216\n\t" \
  "v_fmac_f32 v195, v161, v161\n\t" \
  "v_add_f32 v194, v194, v162\n\t" \
  "v_fmac_f32 v195, v162, v162\n\t" \
  "v_smfmac_f32_16x16x64_bf16 v[0:3], v[100:103], v[208:215], v236\n\t" \
  "ds_read_b64 v[228:229], v232 offset:33616\n\t" \
  "v_add_f32 v194, v194, v163\n\t" \
  "v_fmac_f32 v195, v163, v163\n\t" \
  "v_smfmac_f32_16x16x64_bf16 v[180:183], v[104:107], v[208:215], v236\n\t" \
  "buffer_store_dwordx4 v[52:55], %[vo], %[ors], %[op0] offen offset:0 sc1\n\t" \
  "ds_read_b64 v[230:231], v232 offset:34016\n\t" \
  "v_add_f32 v194, v194, v164\n\t" \
  "v_fmac_f32 v195, v164, v164\n\t" \
  "v_add_f32 v194, v194, v165\n\t" \
  "v_smfmac_f32_16x16x64_bf16 v[168:171], v[108:111], v[208:215], v236\n\t" \
  "v_fmac_f32 v195, v165, v165\n\t" \
  "v_add_f32 v194, v194, v166\n\t" \
  "v_fmac_f32 v195, v166, v166\n\t" \
  "s_waitcnt lgkmcnt(5)\n\t" \
  "v_smfmac_f32_16x16x64_bf16 v[16:19], v[112:115], v[216:223], v236\n\t" \
  "v_add_f32 v194, v194, v167\n\t" \
  "v_fmac_f32 v195, v167, v167\n\t" \
  "v_smfmac_f32_16x16x64_bf16 v[4:7], v[116:119], v[216:223], v236\n\t" \
  "ds_write_b64 v233, v[194:195] offset:20496\n\t" \
  "v_smfmac_f32_16x16x64_bf16 v[184:187], v[120:123], v[216:223], v236\n\t" \
  "v_mul_f32 v197, v168, v168\n\t" \
  "v_add_f32 v196, v168, v169\n\t" \
  "v_fmac_f32 v197, v169, v169\n\t" \
  "v_add_f32 v196, v196, v170\n\t" \
  "v_fmac_f32 v197, v170, v170\n\t" \
  "v_add_f32 v196, v196, v171\n\t" \
  "v_smfmac_f32_16x16x64_bf16 v[172:175], v[124:127], v[216:223], v236\n\t" \
  "v_fmac_f32 v197, v171, v171\n\t" \
  "s_waitcnt vmcnt(9) lgkmcnt(0)\n\t" \
  "s_barrier\n\t" \
  "ds_read_b64 v[208:209], v246 offset:0\n\t" \
  "ds_read_b64 v[210:211], v246 offset:400\n\t" \
  "ds_read_b64 v[212:213], v246 offset:800\n\t" \
  "ds_read_b64 v[214:215], v246 offset:1200\n\t" \
  "ds_read_b64 v[216:217], v246 offset:8\n\t" \
  "ds_read_b64 v[218:219], v246 offset:408\n\t" \
  "ds_read_b64 v[220:221], v246 offset:808\n\t" \
  "ds_read_b64 v[222:223], v246 offset:1208\n\t" \
  "v_smfmac_f32_16x16x64_bf16 v[20:23], v[128:131], v[224:231], v236\n\t" \
  "v_smfmac_f32_16x16x64_bf16 v[8:11], v[132:135], v[224:231], v236\n\t" \
  "buffer_store_dwordx4 v[60:63], %[vo], %[ors], %[op0] offen offset:64 sc1\n\t" \
  "v_smfmac_f32_16x16x64_bf16 v[188:191], v[136:139], v[224:231], v236\n\t" \
  "v_add_f32 v196, v196, v172\n\t" \
  "v_fmac_f32 v197, v172, v172\n\t" \
  "v_add_f32 v196, v196, v173\n\t" \
  "v_fmac_f32 v197, v173, v173\n\t" \
  "v_add_f32 v196, v196, v174\n\t" \
  "v_fmac_f32 v197, v174, v174\n\t" \
  "v_smfmac_f32_16x16x64_bf16 v[176:179], v[140:143], v[224:231], v236\n\t" \
  "ds_read_b64 v[224:225], v246 offset:16\n\t" \
  "v_add_f32 v196, v196, v175\n\t" \
  "v_fmac_f32 v197, v175, v175\n\t" \
  "s_waitcnt lgkmcnt(5)\n\t" \
  "v_smfmac_f32_16x16x64_bf16 v[24:27], v[96:99], v[208:215], v236\n\t" \
  "ds_read_b64 v[226:227], v246 offset:416\n\t" \
  "v_smfmac_f32_16x16x64_bf16 v[12:15], v[100:103], v[208:215], v236\n\t" \
  "ds_read_b64 v[228:229], v246 offset:816\n\t" \
  "v_smfmac_f32_16x16x64_bf16 v[0:3], v[104:107], v[208:215], v236\n\t" \
  "ds_read_b64 v[230:231], v246 offset:1216\n\t" \
  "v_add_f32 v196, v196, v176\n\t" \
  "v_fmac_f32 v197, v176, v176\n\t" \
  "v_add_f32 v196, v196, v177\n\t" \
  "v_fmac_f32 v197, v177, v177\n\t" \
  "v_add_f32 v196, v196, v178\n\t" \
  "v_fmac_f32 v197, v178, v178\n\t" \
  "v_smfmac_f32_16x16x64_bf16 v[180:183], v[108:111], v[208:215], v236\n\t" \
  "v_add_f32 v196, v196, v179\n\t" \
  "v_fmac_f32 v197, v179, v179\n\t" \
  "s_waitcnt lgkmcnt(4)\n\t" \
  "v_smfmac_f32_16x16x64_bf16 v[28:31], v[112:115], v[216:223], v236\n\t" \
  "buffer_store_dwordx4 v[68:71], %[vo], %[ors], %[op0] offen offset:128 sc1\n\t" \
  "v_smfmac_f32_16x16x64_bf16 v[16:19], v[116:119], v[216:223], v236\n\t" \
  "ds_write_b64 v233, v[196:197] offset:24592\n\t" \
  "v_smfmac_f32_16x16x64_bf16 v[4:7], v[120:123], v[216:223], v236\n\t" \
  "v_mul_f32 v199, v180, v180\n\t" \
  "v_add_f32 v198, v180, v181\n\t" \
  "v_fmac_f32 v199, v181, v181\n\t" \
  "v_add_f32 v198, v198, v182\n\t" \
  "v_fmac_f32 v199, v182, v182\n\t" \
  "v_add_f32 v198, v198, v183\n\t" \
  "v_smfmac_f32_16x16x64_bf16 v[184:187], v[124:127], v[216:223], v236\n\t" \
  "v_fmac_f32 v199, v183, v183\n\t" \
  "s_waitcnt vmcnt(9) lgkmcnt(0)\n\t" \
  "s_barrier\n\t" \
  "ds_read_b64 v[208:209], v246 offset:16400\n\t" \
  "ds_read_b64 v[210:211], v246 offset:16800\n\t" \
  "ds_read_b64 v[212:213], v246 offset:17200\n\t" \
  "ds_read_b64 v[214:215], v246 offset:17600\n\t" \
  "ds_read_b64 v[216:217], v246 offset:16408\n\t" \
  "ds_read_b64 v[218:219], v246 offset:16808\n\t" \
  "ds_read_b64 v[220:221], v246 offset:17208\n\t" \
  "ds_read_b64 v[222:223], v246 offset:17608\n\t" \
  "v_smfmac_f32_16x16x64_bf16 v[32:35], v[128:131], v[224:231], v236\n\t" \
  "v_smfmac_f32_16x16x64_bf16 v[20:23], v[132:135], v[224:231], v236\n\t" \
  "v_smfmac_f32_16x16x64_bf16 v[8:11], v[136:139], v[224:231], v236\n\t" \
  "v_add_f32 v198, v198, v184\n\t" \
  "v_fmac_f32 v199, v184, v184\n\t" \
  "v_add_f32 v198, v198, v185\n\t" \
  "v_fmac_f32 v199, v185, v185\n\t" \
  "v_add_f32 v198, v198, v186\n\t" \
  "v_smfmac_f32_16x16x64_bf16 v[188:191], v[140:143], v[224:231], v236\n\t" \
  "buffer_store_dwordx4 v[76:79], %[vo], %[ors], %[op0] offen offset:192 sc1\n\t" \
  "ds_read_b64 v[224:225], v246 offset:16416\n\t" \
  "v_fmac_f32 v199, v186, v186\n\t" \
  "v_add_f32 v198, v198, v187\n\t" \
  "v_fmac_f32 v199, v187, v187\n\t" \
  "s_waitcnt lgkmcnt(5)\n\t" \
  "v_smfmac_f32_16x16x64_bf16 v[36:39], v[96:99], v[208:215], v236\n\t" \
  "ds_read_b64 v[226:227], v246 offset:16816\n\t" \
  "v_smfmac_f32_16x16x64_bf16 v[24:27], v[100:103], v[208:215], v236\n\t" \
  "ds_read_b64 v[228:229], v246 offset:17216\n\t" \
  "v_smfmac_f32_16x16x64_bf16 v[12:15], v[104:107], v[208:215], v236\n\t" \
  "ds_read_b64 v[230:231], v246 offset:17616\n\t" \
  "v_add_f32 v198, v198, v188\n\t" \
  "v_fmac_f32 v199, v188, v188\n\t" \
  "v_add_f32 v198, v198, v189\n\t" \
  "v_fmac_f32 v199, v189, v189\n\t" \
  "v_add_f32 v198, v198, v190\n\t" \
  "v_fmac_f32 v199, v190, v190\n\t" \
  "v_smfmac_f32_16x16x64_bf16 v[0:3], v[108:111], v[208:215], v236\n\t" \
  "v_add_f32 v198, v198, v191\n\t" \
  "v_fmac_f32 v199, v191, v191\n\t" \
  "s_waitcnt lgkmcnt(4)\n\t" \
  "v_smfmac_f32_16x16x64_bf16 v[40:43], v[112:115], v[216:223], v236\n\t" \
  "ds_write_b64 v233, v[198:199] offset:28688\n\t" \
  "v_smfmac_f32_16x16x64_bf16 v[28:31], v[116:119], v[216:223], v236\n\t" \
  "v_smfmac_f32_16x16x64_bf16 v[16:19], v[120:123], v[216:223], v236\n\t" \
  "buffer_store_dwordx4 v[84:87], %[vo], %[ors], %[op0] offen offset:256 sc1\n\t" \
  "v_mul_f32 v201, v0, v0\n\t" \
  "v_add_f32 v200, v0, v1\n\t" \
  "v_fmac_f32 v201, v1, v1\n\t" \
  "v_add_f32 v200, v200, v2\n\t" \
  "v_fmac_f32 v201, v2, v2\n\t" \
  "v_add_f32 v200, v200, v3\n\t" \
  "v_smfmac_f32_16x16x64_bf16 v[4:7], v[124:127], v[216:223], v236\n\t" \
  "v_fmac_f32 v201, v3, v3\n\t" \
  "s_waitcnt vmcnt(9) lgkmcnt(0)\n\t" \
  "s_barrier\n\t" \
  "ds_read_b64 v[208:209], v246 offset:32800\n\t" \
  "ds_read_b64 v[210:211], v246 offset:33200\n\t" \
  "ds_read_b64 v[212:213], v246 offset:33600\n\t" \
  "ds_read_b64 v[214:215], v246 offset:34000\n\t" \
  "ds_read_b64 v[216:217], v246 offset:32808\n\t" \
  "ds_read_b64 v[218:219], v246 offset:33208\n\t" \
  "ds_read_b64 v[220:221], v246 offset:33608\n\t" \
  "ds_read_b64 v[222:223], v246 offset:34008\n\t" \
  "v_smfmac_f32_16x16x64_bf16 v[44:47], v[128:131], v[224:231], v236\n\t" \
  "v_smfmac_f32_16x16x64_bf16 v[32:35], v[132:135], v[224:231], v236\n\t" \
  "v_smfmac_f32_16x16x64_bf16 v[20:23], v[136:139], v[224:231], v236\n\t" \
  "v_add_f32 v200, v200, v4\n\t" \
  "v_fmac_f32 v201, v4, v4\n\t" \
  "v_add_f32 v200, v200, v5\n\t" \
  "v_fmac_f32 v201, v5, v5\n\t" \
  "v_add_f32 v200, v200, v6\n\t" \
  "v_fmac_f32 v201, v6, v6\n\t" \
  "v_smfmac_f32_16x16x64_bf16 v[8:11], v[140:143], v[224:231], v236\n\t" \
  "ds_read_b64 v[224:225], v246 offset:32816\n\t" \
  "v_add_f32 v200, v200, v7\n\t" \
  "v_fmac_f32 v201, v7, v7\n\t" \
  "s_waitcnt lgkmcnt(5)\n\t" \
  "v_smfmac_f32_16x16x64_bf16 v[36:39], v[100:103], v[208:215], v236\n\t" \
  "ds_read_b64 v[226:227], v246 offset:33216\n\t" \
  "v_smfmac_f32_16x16x64_bf16 v[24:27], v[104:107], v[208:215], v236\n\t" \
  "buffer_store_dwordx4 v[92:95], %[vo], %[ors], %[op0] offen offset:320 sc1\n\t" \
  "ds_read_b64 v[228:229], v246 offset:33616\n\t" \
  "v_smfmac_f32_16x16x64_bf16 v[12:15], v[108:111], v[208:215], v236\n\t" \
  "ds_read_b64 v[230:231], v246 offset:34016\n\t" \
  "v_add_f32 v200, v200, v8\n\t" \
  "v_fmac_f32 v201, v8, v8\n\t" \
  "v_add_f32 v200, v200, v9\n\t" \
  "v_fmac_f32 v201, v9, v9\n\t" \
  "v_add_f32 v200, v200, v10\n\t" \
  "v_fmac_f32 v201, v10, v10\n\t" \
  "s_waitcnt lgkmcnt(4)\n\t" \
  "v_smfmac_f32_16x16x64_bf16 v[40:43], v[116:119], v[216:223], v236\n\t" \
  "v_add_f32 v200, v200, v11\n\t" \
  "v_fmac_f32 v201, v11, v11\n\t" \
  "v_smfmac_f32_16x16x64_bf16 v[28:31], v[120:123], v[216:223], v236\n\t" \
  "ds_write_b64 v233, v[200:201] offset:0\n\t" \
  "v_smfmac_f32_16x16x64_bf16 v[16:19], v[124:127], v[216:223], v236\n\t" \
  "v_mul_f32 v203, v12, v12\n\t" \
  "v_add_f32 v202, v12, v13\n\t" \
  "v_fmac_f32 v203, v13, v13\n\t" \
  "v_add_f32 v202, v202, v14\n\t" \
  "v_fmac_f32 v203, v14, v14\n\t" \
  "v_add_f32 v202, v202, v15\n\t" \
  "s_waitcnt vmcnt(8) lgkmcnt(0)\n\t" \
  "s_barrier\n\t" \
  "ds_read_b64 v[208:209], v232 offset:0\n\t" \
  "ds_read_b64 v[210:211], v232 offset:400\n\t" \
  "ds_read_b64 v[212:213], v232 offset:800\n\t" \
  "ds_read_b64 v[214:215], v232 offset:1200\n\t" \
  "ds_read_b64 v[216:217], v232 offset:8\n\t" \
  "ds_read_b64 v[218:219], v232 offset:408\n\t" \
  "ds_read_b64 v[220:221], v232 offset:808\n\t" \
  "ds_read_b64 v[222:223], v232 offset:1208\n\t" \
  "s_cmp_lg_u32 %[grp], 0\n\t" \
  "s_cbranch_scc1 .Lcnx_c2\n\t" \
  "v_add_u32 v128, %[sr], %[l8]\n\t" \
  "ds_read_b64 v[96:97], v128 offset:0\n\t" \
  "ds_read_b64 v[98:99], v128 offset:512\n\t" \
  "ds_read_b64 v[100:101], v128 offset:1024\n\t" \
  "ds_read_b64 v[102:103], v128 offset:1536\n\t" \
  "ds_read_b64 v[112:113], v128 offset:2048\n\t" \
  "ds_read_b64 v[114:115], v128 offset:2560\n\t" \
  "ds_read_b64 v[116:117], v128 offset:3072\n\t" \
  "ds_read_b64 v[118:119], v128 offset:3584\n\t" \
  ".Lcnx_c2:\n\t" \
  "v_smfmac_f32_16x16x64_bf16 v[44:47], v[132:135], v[224:231], v236\n\t" \
  "v_fmac_f32 v203, v15, v15\n\t" \
  "v_smfmac_f32_16x16x64_bf16 v[32:35], v[136:139], v[224:231], v236\n\t" \
  "v_smfmac_f32_16x16x64_bf16 v[20:23], v[140:143], v[224:231], v236\n\t" \
  "buffer_store_dwordx4 v[48:51], %[vo], %[ors], %[op1] offen offset:0 sc1\n\t" \
  "ds_read_b64 v[224:225], v232 offset:16\n\t" \
  "v_add_f32 v202, v202, v16\n\t" \
  "v_fmac_f32 v203, v16, v16\n\t" \
  "v_add_f32 v202, v202, v17\n\t" \
  "v_fmac_f32 v203, v17, v17\n\t" \
  "v_add_f32 v202, v202, v18\n\t" \
  "v_fmac_f32 v203, v18, v18\n\t" \
  "s_waitcnt lgkmcnt(5)\n\t" \
  "v_smfmac_f32_16x16x64_bf16 v[36:39], v[104:107], v[208:215], v236\n\t" \
  "ds_read_b64 v[226:227], v232 offset:416\n\t" \
  "ds_read_b64 v[228:229], v232 offset:816\n\t" \
  "ds_read_b64 v[230:231], v232 offset:1216\n\t" \
  "v_add_f32 v202, v202, v19\n\t" \
  "v_fmac_f32 v203, v19, v19\n\t" \
  "v_smfmac_f32_16x16x64_bf16 v[24:27], v[108:111], v[208:215], v236\n\t" \
  "s_waitcnt lgkmcnt(4)\n\t" \
  "v_smfmac_f32_16x16x64_bf16 v[40:43], v[120:123], v[216:223], v236\n\t" \
  "v_add_f32 v202, v202, v20\n\t" \
  "v_fmac_f32 v203, v20, v20\n\t" \
  "v_add_f32 v202, v202, v21\n\t" \
  "v_fmac_f32 v203, v21, v21\n\t" \
  "v_add_f32 v202, v202, v22\n\t" \
  "v_fmac_f32 v203, v22, v22\n\t" \
  "v_smfmac_f32_16x16x64_bf16 v[28:31], v[124:127], v[216:223], v236\n\t" \
  "v_add_f32 v202, v202, v23\n\t" \
  "v_fmac_f32 v203, v23, v23\n\t" \
  "ds_write_b64 v233, v[202:203] offset:4096\n\t" \
  "s_waitcnt vmcnt(7) lgkmcnt(0)\n\t" \
  "s_barrier\n\t" \
  "ds_read_b64 v[208:209], v232 offset:16400\n\t" \
  "ds_read_b64 v[210:211], v232 offset:16800\n\t" \
  "ds_read_b64 v[212:213], v232 offset:17200\n\t" \
  "ds_read_b64 v[214:215], v232 offset:17600\n\t" \
  "ds_read_b64 v[216:217], v232 offset:16408\n\t" \
  "ds_read_b64 v[218:219], v232 offset:16808\n\t" \
  "ds_read_b64 v[220:221], v232 offset:17208\n\t" \
  "ds_read_b64 v[222:223], v232 offset:17608\n\t" \
  "s_cmp_ge_u32 %[f0], 2\n\t" \
  "s_cbranch_scc1 .Lcnx_c3\n\t" \
  "s_mov_b32 m0, %[wd2]\n\t" \
  "s_nop 0\n\t" \
  "buffer_load_dwordx4 %[wv], %[wrs], %[so0] offen lds\n\t" \
  "buffer_load_dwordx4 %[wv], %[wrs], %[so1] offen offset:1024 lds\n\t" \
  "buffer_load_dwordx4 %[wv], %[wrs], %[so2] offen offset:2048 lds\n\t" \
  "s_add_u32 %[so0], %[so0], %[wc]\n\t" \
  "s_add_u32 %[so1], %[so1], %[wc]\n\t" \
  "s_add_u32 %[so2], %[so2], %[wc]\n\t" \
  "s_mov_b32 m0, %[wd3]\n\t" \
  "s_nop 0\n\t" \
  "buffer_load_dwordx4 %[wv], %[wrs], %[so0] offen lds\n\t" \
  "buffer_load_dwordx4 %[wv], %[wrs], %[so1] offen offset:1024 lds\n\t" \
  "buffer_load_dwordx4 %[wv], %[wrs], %[so2] offen offset:2048 lds\n\t" \
  "s_add_u32 %[so0], %[so0], %[wc]\n\t" \
  "s_add_u32 %[so1], %[so1], %[wc]\n\t" \
  "s_add_u32 %[so2], %[so2], %[wc]\n\t" \
  ".Lcnx_c3:\n\t" \
  "v_smfmac_f32_16x16x64_bf16 v[44:47], v[136:139], v[224:231], v236\n\t" \
  "v_mul_f32 v205, v24, v24\n\t" \
  "v_add_f32 v204, v24, v25\n\t" \
  "v_smfmac_f32_16x16x64_bf16 v[32:35], v[140:143], v[224:231], v236\n\t" \
  "buffer_store_dwordx4 v[56:59], %[vo], %[ors], %[op1] offen offset:64 sc1\n\t" \
  "ds_read_b64 v[224:225], v232 offset:16416\n\t" \
  "ds_read_b64 v[226:227], v232 offset:16816\n\t" \
  "v_fmac_f32 v205, v25, v25\n\t" \
  "v_add_f32 v204, v204, v26\n\t" \
  "v_fmac_f32 v205, v26, v26\n\t" \
  "s_cmp_lg_u32 %[grp], 0\n\t" \
  "s_cbranch_scc1 .Lcnx_c4\n\t" \
  "s_waitcnt lgkmcnt(15)\n\t" \
  "v_add_f32 v96, v96, v98\n\t" \
  "v_add_f32 v97, v97, v99\n\t" \
  "v_add_f32 v100, v100, v102\n\t" \
  "v_add_f32 v101, v101, v103\n\t" \
  "v_add_f32 v112, v112, v114\n\t" \
  "v_add_f32 v113, v113, v115\n\t" \
  "v_add_f32 v116, v116, v118\n\t" \
  "v_add_f32 v117, v117, v119\n\t" \
  "v_add_f32 v96, v96, v100\n\t" \
  "v_add_f32 v97, v97, v101\n\t" \
  "v_add_f32 v112, v112, v116\n\t" \
  "v_add_f32 v113, v113, v117\n\t" \
  "v_add_f32 v96, v96, v112\n\t" \
  "v_add_f32 v97, v97, v113\n\t" \
  "v_mul_f32 v96, 0x3c2aaaab, v96\n\t" \
  "v_mul_f32 v97, 0x3c2aaaab, v97\n\t" \
  "v_fma_f32 v97, -v96, v96, v97\n\t" \
  "v_max_f32 v97, 0, v97\n\t" \
  "v_add_f32 v97, 0x3727c5ac, v97\n\t" \
  "v_rsq_f32 v97, v97\n\t" \
  "s_nop 0\n\t" \
  "v_mul_f32 v98, 0x3e000000, v97\n\t" \
  "v_mul_f32 v99, -v96, v98\n\t" \
  "ds_write_b64 v233, v[98:99] offset:32784\n\t" \
  ".Lcnx_c4:\n\t" \
  "s_waitcnt lgkmcnt(6)\n\t" \
  "v_smfmac_f32_16x16x64_bf16 v[36:39], v[108:111], v[208:215], v236\n\t" \
  "ds_read_b64 v[228:229], v232 offset:17216\n\t" \
  "ds_read_b64 v[230:231], v232 offset:17616\n\t" \
  "v_add_f32 v204, v204, v27\n\t" \
  "v_fmac_f32 v205, v27, v27\n\t" \
  "v_add_f32 v204, v204, v28\n\t" \
  "s_waitcnt lgkmcnt(4)\n\t" \
  "v_smfmac_f32_16x16x64_bf16 v[40:43], v[124:127], v[216:223], v236\n\t" \
  "v_fmac_f32 v205, v28, v28\n\t" \
  "v_add_f32 v204, v204, v29\n\t" \
  "v_fmac_f32 v205, v29, v29\n\t" \
  "s_waitcnt lgkmcnt(0)\n\t" \
  "v_smfmac_f32_16x16x64_bf16 v[44:47], v[140:143], v[224:231], v236\n\t" \
  "v_add_f32 v204, v204, v30\n\t" \
  "v_fmac_f32 v205, v30, v30\n\t" \
  "v_add_f32 v204, v204, v31\n\t" \
  "v_fmac_f32 v205, v31, v31\n\t" \
  "v_add_f32 v204, v204, v32\n\t" \
  "v_fmac_f32 v205, v32, v32\n\t" \
  "v_add_f32 v204, v204, v33\n\t" \
  "v_fmac_f32 v205, v33, v33\n\t" \
  "v_add_f32 v204, v204, v34\n\t" \
  "v_fmac_f32 v205, v34, v34\n\t" \
  "v_add_f32 v204, v204, v35\n\t" \
  "v_fmac_f32 v205, v35, v35\n\t" \
  "v_mul_f32 v207, v36, v36\n\t" \
  "v_add_f32 v206, v36, v37\n\t" \
  "v_fmac_f32 v207, v37, v37\n\t" \
  "v_add_f32 v206, v206, v38\n\t" \
  "v_fmac_f32 v207, v38, v38\n\t" \
  "v_add_f32 v206, v206, v39\n\t" \
  "v_fmac_f32 v207, v39, v39\n\t" \
  "v_add_f32 v206, v206, v40\n\t" \
  "v_fmac_f32 v207, v40, v40\n\t" \
  "v_add_f32 v206, v206, v41\n\t" \
  "v_fmac_f32 v207, v41, v41\n\t" \
  "v_add_f32 v206, v206, v42\n\t" \
  "v_fmac_f32 v207, v42, v42\n\t" \
  "v_add_f32 v206, v206, v43\n\t" \
  "v_fmac_f32 v207, v43, v43\n\t" \
  "v_add_f32 v206, v206, v44\n\t" \
  "v_fmac_f32 v207, v44, v44\n\t" \
  "v_add_f32 v206, v206, v45\n\t" \
  "v_fmac_f32 v207, v45, v45\n\t" \
  "v_add_f32 v206, v206, v46\n\t" \
  "v_fmac_f32 v207, v46, v46\n\t" \
  "v_add_f32 v206, v206, v47\n\t" \
  "v_fmac_f32 v207, v47, v47\n\t" \
  "ds_write_b64 v233, v[204:205] offset:8192\n\t" \
  "ds_write_b64 v233, v[206:207] offset:12288\n\t" \
  "s_waitcnt lgkmcnt(0)\n\t" \
  "s_barrier\n\t" \
  "ds_read_b64 v[192:193], %[l8] offset:32784\n\t" \
  "ds_read_b64 v[194:195], %[l8] offset:33296\n\t" \
  "ds_read_b64 v[196:197], %[l8] offset:33808\n\t" \
  "ds_read_b64 v[198:199], %[l8] offset:34320\n\t" \
  "s_cmp_lg_u32 %[grp], 1\n\t" \
  "s_cbranch_scc1 .Lcnx_c5\n\t" \
  "v_add_u32 v128, %[sr], %[l8]\n\t" \
  "ds_read_b64 v[96:97], v128 offset:0\n\t" \
  "ds_read_b64 v[98:99], v128 offset:512\n\t" \
  "ds_read_b64 v[100:101], v128 offset:1024\n\t" \
  "ds_read_b64 v[102:103], v128 offset:1536\n\t" \
  "ds_read_b64 v[112:113], v128 offset:2048\n\t" \
  "ds_read_b64 v[114:115], v128 offset:2560\n\t" \
  "ds_read_b64 v[116:117], v128 offset:3072\n\t" \
  "ds_read_b64 v[118:119], v128 offset:3584\n\t" \
  ".Lcnx_c5:\n\t" \
  "v_lshl_add_u32 v208, %[l8], 1, %[z1]\n\t" \
  "v_lshl_add_u32 v209, %[l8], 1, %[z2]\n\t" \
  "s_waitcnt lgkmcnt(3)\n\t" \
  "v_fma_f32 v144, v144, v192, v193\n\t" \
  "v_fma_f32 v145, v145, v192, v193\n\t" \
  "v_fma_f32 v146, v146, v192, v193\n\t" \
  "v_fma_f32 v147, v147, v192, v193\n\t" \
  "v_fma_f32 v148, v148, v192, v193\n\t" \
  "v_fma_f32 v149, v149, v192, v193\n\t" \
  "v_fma_f32 v150, v150, v192, v193\n\t" \
  "v_fma_f32 v151, v151, v192, v193\n\t" \
  "v_fma_f32 v152, v152, v192, v193\n\t" \
  "v_fma_f32 v153, v153, v192, v193\n\t" \
  "v_fma_f32 v154, v154, v192, v193\n\t" \
  "v_fma_f32 v155, v155, v192, v193\n\t" \
  "v_cvt_pk_bf16_f32 v144, v144, v145\n\t" \
  "v_cvt_pk_bf16_f32 v145, v146, v147\n\t" \
  "v_cvt_pk_bf16_f32 v146, v148, v149\n\t" \
  "v_cvt_pk_bf16_f32 v147, v150, v151\n\t" \
  "v_cvt_pk_bf16_f32 v148, v152, v153\n\t" \
  "v_cvt_pk_bf16_f32 v149, v154, v155\n\t" \
  "ds_write_b128 v208, v[144:147] offset:0\n\t" \
  "ds_write_b64 v209, v[148:149] offset:0\n\t" \
  "buffer_store_dwordx4 v[64:67], %[vo], %[ors], %[op1] offen offset:128 sc1\n\t" \
  "s_cmp_lg_u32 %[grp], 1\n\t" \
  "s_cbranch_scc1 .Lcnx_c6\n\t" \
  "s_waitcnt lgkmcnt(2)\n\t" \
  "v_add_f32 v96, v96, v98\n\t" \
  "v_add_f32 v97, v97, v99\n\t" \
  "v_add_f32 v100, v100, v102\n\t" \
  "v_add_f32 v101, v101, v103\n\t" \
  "v_add_f32 v112, v112, v114\n\t" \
  "v_add_f32 v113, v113, v115\n\t" \
  "v_add_f32 v116, v116, v118\n\t" \
  "v_add_f32 v117, v117, v119\n\t" \
  "v_add_f32 v96, v96, v100\n\t" \
  "v_add_f32 v97, v97, v101\n\t" \
  "v_add_f32 v112, v112, v116\n\t" \
  "v_add_f32 v113, v113, v117\n\t" \
  "v_add_f32 v96, v96, v112\n\t" \
  "v_add_f32 v97, v97, v113\n\t" \
  "v_mul_f32 v96, 0x3c2aaaab, v96\n\t" \
  "v_mul_f32 v97, 0x3c2aaaab, v97\n\t" \
  "v_fma_f32 v97, -v96, v96, v97\n\t" \
  "v_max_f32 v97, 0, v97\n\t" \
  "v_add_f32 v97, 0x3727c5ac, v97\n\t" \
  "v_rsq_f32 v97, v97\n\t" \
  "s_nop 0\n\t" \
  "v_mul_f32 v98, 0x3e000000, v97\n\t" \
  "v_mul_f32 v99, -v96, v98\n\t" \
  "ds_write_b64 v233, v[98:99] offset:14352\n\t" \
  ".Lcnx_c6:\n\t" \
  "s_waitcnt lgkmcnt(4)\n\t" \
  "v_fma_f32 v156, v156, v194, v195\n\t" \
  "v_fma_f32 v157, v157, v194, v195\n\t" \
  "v_fma_f32 v158, v158, v194, v195\n\t" \
  "v_fma_f32 v159, v159, v194, v195\n\t" \
  "v_fma_f32 v160, v160, v194, v195\n\t" \
  "v_fma_f32 v161, v161, v194, v195\n\t" \
  "v_fma_f32 v162, v162, v194, v195\n\t" \
  "v_fma_f32 v163, v163, v194, v195\n\t" \
  "v_fma_f32 v164, v164, v194, v195\n\t" \
  "v_fma_f32 v165, v165, v194, v195\n\t" \
  "v_fma_f32 v166, v166, v194, v195\n\t" \
  "v_fma_f32 v167, v167, v194, v195\n\t" \
  "v_cvt_pk_bf16_f32 v156, v156, v157\n\t" \
  "v_cvt_pk_bf16_f32 v157, v158, v159\n\t" \
  "v_cvt_pk_bf16_f32 v158, v160, v161\n\t" \
  "v_cvt_pk_bf16_f32 v159, v162, v163\n\t" \
  "v_cvt_pk_bf16_f32 v160, v164, v165\n\t" \
  "v_cvt_pk_bf16_f32 v161, v166, v167\n\t" \
  "ds_write_b128 v208, v[156:159] offset:12288\n\t" \
  "ds_write_b64 v209, v[160:161] offset:12288\n\t" \
  "s_waitcnt lgkmcnt(5)\n\t" \
  "v_fma_f32 v168, v168, v196, v197\n\t" \
  "v_fma_f32 v169, v169, v196, v197\n\t" \
  "v_fma_f32 v170, v170, v196, v197\n\t" \
  "v_fma_f32 v171, v171, v196, v197\n\t" \
  "v_fma_f32 v172, v172, v196, v197\n\t" \
  "v_fma_f32 v173, v173, v196, v197\n\t" \
  "v_fma_f32 v174, v174, v196, v197\n\t" \
  "v_fma_f32 v175, v175, v196, v197\n\t" \
  "v_fma_f32 v176, v176, v196, v197\n\t" \
  "v_fma_f32 v177, v177, v196, v197\n\t" \
  "v_fma_f32 v178, v178, v196, v197\n\t" \
  "v_fma_f32 v179, v179, v196, v197\n\t" \
  "v_cvt_pk_bf16_f32 v168, v168, v169\n\t" \
  "v_cvt_pk_bf16_f32 v169, v170, v171\n\t" \
  "v_cvt_pk_bf16_f32 v170, v172, v173\n\t" \
  "v_cvt_pk_bf16_f32 v171, v174, v175\n\t" \
  "v_cvt_pk_bf16_f32 v172, v176, v177\n\t" \
  "v_cvt_pk_bf16_f32 v173, v178, v179\n\t" \
  "ds_write_b128 v208, v[168:171] offset:24576\n\t" \
  "ds_write_b64 v209, v[172:173] offset:24576\n\t" \
  "s_waitcnt lgkmcnt(6)\n\t" \
  "v_fma_f32 v180, v180, v198, v199\n\t" \
  "v_fma_f32 v181, v181, v198, v199\n\t" \
  "v_fma_f32 v182, v182, v198, v199\n\t" \
  "v_fma_f32 v183, v183, v198, v199\n\t" \
  "v_fma_f32 v184, v184, v198, v199\n\t" \
  "v_fma_f32 v185, v185, v198, v199\n\t" \
  "v_fma_f32 v186, v186, v198, v199\n\t" \
  "v_fma_f32 v187, v187, v198, v199\n\t" \
  "v_fma_f32 v188, v188, v198, v199\n\t" \
  "v_fma_f32 v189, v189, v198, v199\n\t" \
  "v_fma_f32 v190, v190, v198, v199\n\t" \
  "v_fma_f32 v191, v191, v198, v199\n\t" \
  "v_cvt_pk_bf16_f32 v180, v180, v181\n\t" \
  "v_cvt_pk_bf16_f32 v181, v182, v183\n\t" \
  "v_cvt_pk_bf16_f32 v182, v184, v185\n\t" \
  "v_cvt_pk_bf16_f32 v183, v186, v187\n\t" \
  "v_cvt_pk_bf16_f32 v184, v188, v189\n\t" \
  "v_cvt_pk_bf16_f32 v185, v190, v191\n\t" \
  "ds_write_b128 v208, v[180:183] offset:36864\n\t" \
  "ds_write_b64 v209, v[184:185] offset:36864\n\t" \
  "v_add_u32 v208, 49152, v208\n\t" \
  "v_add_u32 v209, 49152, v209\n\t" \
  "s_waitcnt lgkmcnt(0)\n\t" \
  "s_barrier\n\t" \
  "ds_read_b64 v[200:201], %[l8] offset:16400\n\t" \
  "ds_read_b64 v[202:203], %[l8] offset:16912\n\t" \
  "ds_read_b64 v[204:205], %[l8] offset:17424\n\t" \
  "ds_read_b64 v[206:207], %[l8] offset:17936\n\t" \
  "s_waitcnt lgkmcnt(3)\n\t" \
  "v_fma_f32 v0, v0, v200, v201\n\t" \
  "v_fma_f32 v1, v1, v200, v201\n\t" \
  "v_fma_f32 v2, v2, v200, v201\n\t" \
  "v_fma_f32 v3, v3, v200, v201\n\t" \
  "v_fma_f32 v4, v4, v200, v201\n\t" \
  "v_fma_f32 v5, v5, v200, v201\n\t" \
  "v_fma_f32 v6, v6, v200, v201\n\t" \
  "v_fma_f32 v7, v7, v200, v201\n\t" \
  "v_fma_f32 v8, v8, v200, v201\n\t" \
  "v_fma_f32 v9, v9, v200, v201\n\t" \
  "v_fma_f32 v10, v10, v200, v201\n\t" \
  "v_fma_f32 v11, v11, v200, v201\n\t" \
  "v_cvt_pk_bf16_f32 v0, v0, v1\n\t" \
  "v_cvt_pk_bf16_f32 v1, v2, v3\n\t" \
  "v_cvt_pk_bf16_f32 v2, v4, v5\n\t" \
  "v_cvt_pk_bf16_f32 v3, v6, v7\n\t" \
  "v_cvt_pk_bf16_f32 v4, v8, v9\n\t" \
  "v_cvt_pk_bf16_f32 v5, v10, v11\n\t" \
  "ds_write_b128 v208, v[0:3] offset:0\n\t" \
  "ds_write_b64 v209, v[4:5] offset:0\n\t" \
  "buffer_store_dwordx4 v[72:75], %[vo], %[ors], %[op1] offen offset:192 sc1\n\t" \
  "s_waitcnt lgkmcnt(4)\n\t" \
  "v_fma_f32 v12, v12, v202, v203\n\t" \
  "v_fma_f32 v13, v13, v202, v203\n\t" \
  "v_fma_f32 v14, v14, v202, v203\n\t" \
  "v_fma_f32 v15, v15, v202, v203\n\t" \
  "v_fma_f32 v16, v16, v202, v203\n\t" \
  "v_fma_f32 v17, v17, v202, v203\n\t" \
  "v_fma_f32 v18, v18, v202, v203\n\t" \
  "v_fma_f32 v19, v19, v202, v203\n\t" \
  "v_fma_f32 v20, v20, v202, v203\n\t" \
  "v_fma_f32 v21, v21, v202, v203\n\t" \
  "v_fma_f32 v22, v22, v202, v203\n\t" \
  "v_fma_f32 v23, v23, v202, v203\n\t" \
  "v_cvt_pk_bf16_f32 v12, v12, v13\n\t" \
  "v_cvt_pk_bf16_f32 v13, v14, v15\n\t" \
  "v_cvt_pk_bf16_f32 v14, v16, v17\n\t" \
  "v_cvt_pk_bf16_f32 v15, v18, v19\n\t" \
  "v_cvt_pk_bf16_f32 v16, v20, v21\n\t" \
  "v_cvt_pk_bf16_f32 v17, v22, v23\n\t" \
  "ds_write_b128 v208, v[12:15] offset:12288\n\t" \
  "ds_write_b64 v209, v[16:17] offset:12288\n\t" \
  "s_waitcnt lgkmcnt(5)\n\t" \
  "v_fma_f32 v24, v24, v204, v205\n\t" \
  "v_fma_f32 v25, v25, v204, v205\n\t" \
  "v_fma_f32 v26, v26, v204, v205\n\t" \
  "v_fma_f32 v27, v27, v204, v205\n\t" \
  "v_fma_f32 v28, v28, v204, v205\n\t" \
  "v_fma_f32 v29, v29, v204, v205\n\t" \
  "v_fma_f32 v30, v30, v204, v205\n\t" \
  "v_fma_f32 v31, v31, v204, v205\n\t" \
  "v_fma_f32 v32, v32, v204, v205\n\t" \
  "v_fma_f32 v33, v33, v204, v205\n\t" \
  "v_fma_f32 v34, v34, v204, v205\n\t" \
  "v_fma_f32 v35, v35, v204, v205\n\t" \
  "v_cvt_pk_bf16_f32 v24, v24, v25\n\t" \
  "v_cvt_pk_bf16_f32 v25, v26, v27\n\t" \
  "v_cvt_pk_bf16_f32 v26, v28, v29\n\t" \
  "v_cvt_pk_bf16_f32 v27, v30, v31\n\t" \
  "v_cvt_pk_bf16_f32 v28, v32, v33\n\t" \
  "v_cvt_pk_bf16_f32 v29, v34, v35\n\t" \
  "ds_write_b128 v208, v[24:27] offset:24576\n\t" \
  "ds_write_b64 v209, v[28:29] offset:24576\n\t" \
  "s_waitcnt lgkmcnt(6)\n\t" \
  "v_fma_f32 v36, v36, v206, v207\n\t" \
  "v_fma_f32 v37, v37, v206, v207\n\t" \
  "v_fma_f32 v38, v38, v206, v207\n\t" \
  "v_fma_f32 v39, v39, v206, v207\n\t" \
  "v_fma_f32 v40, v40, v206, v207\n\t" \
  "v_fma_f32 v41, v41, v206, v207\n\t" \
  "v_fma_f32 v42, v42, v206, v207\n\t" \
  "v_fma_f32 v43, v43, v206, v207\n\t" \
  "v_fma_f32 v44, v44, v206, v207\n\t" \
  "v_fma_f32 v45, v45, v206, v207\n\t" \
  "v_fma_f32 v46, v46, v206, v207\n\t" \
  "v_fma_f32 v47, v47, v206, v207\n\t" \
  "v_cvt_pk_bf16_f32 v36, v36, v37\n\t" \
  "v_cvt_pk_bf16_f32 v37, v38, v39\n\t" \
  "v_cvt_pk_bf16_f32 v38, v40, v41\n\t" \
  "v_cvt_pk_bf16_f32 v39, v42, v43\n\t" \
  "v_cvt_pk_bf16_f32 v40, v44, v45\n\t" \
  "v_cvt_pk_bf16_f32 v41, v46, v47\n\t" \
  "ds_write_b128 v208, v[36:39] offset:36864\n\t" \
  "ds_write_b64 v209, v[40:41] offset:36864\n\t" \
  "v_lshlrev_b32 v232, 1, %[l8]\n\t" \
  "v_lshl_add_u32 v237, %[l8], 3, %[zb]\n\t" \
  "v_and_b32 v232, 0xf0, v232\n\t" \
  "v_mad_i32_i24 v237, v232, -3, v237\n\t" \
  "s_cmp_lg_u32 %[rnd], 0\n\t" \
  "s_cbranch_scc1 .Lcnx_c7\n\t" \
  "s_waitcnt vmcnt(0)\n\t" \
  ".Lcnx_c7:\n\t" \
  "s_waitcnt lgkmcnt(0)\n\t" \
  "s_barrier\n\t" \
  "ds_read_b64_tr_b16 v[208:209], %[a1] offset:43048\n\t" \
  "ds_read_b64_tr_b16 v[210:211], %[a1] offset:44072\n\t" \
  "ds_read_b128 v[96:99], v237 offset:0\n\t" \
  "ds_read_b128 v[108:111], v237 offset:256\n\t" \
  "ds_read_b128 v[120:123], v237 offset:512\n\t" \
  "ds_read_b128 v[132:135], v237 offset:768\n\t" \
  "ds_read_b64_tr_b16 v[212:213], %[a1] offset:46120\n\t" \
  "ds_read_b64_tr_b16 v[214:215], %[a1] offset:47144\n\t" \
  "ds_read_b128 v[100:103], v237 offset:4096\n\t" \
  "ds_read_b128 v[112:115], v237 offset:4352\n\t" \
  "ds_read_b128 v[124:127], v237 offset:4608\n\t" \
  "ds_read_b128 v[136:139], v237 offset:4864\n\t" \
  "ds_read_b128 v[104:107], v237 offset:8192\n\t" \
  "ds_read_b128 v[116:119], v237 offset:8448\n\t" \
  "ds_read_b128 v[128:131], v237 offset:8704\n\t" \
  "ds_read_b128 v[140:143], v237 offset:8960\n\t" \
  "s_waitcnt lgkmcnt(13)\n\t" \
  "v_mfma_f32_16x16x32_bf16 v[144:147], v[208:211], v[96:99], 0\n\t" \
  "s_waitcnt lgkmcnt(12)\n\t" \
  "v_mfma_f32_16x16x32_bf16 v[152:155], v[208:211], v[108:111], 0\n\t" \
  "s_cmp_eq_u32 %[grp], 0\n\t" \
  "s_cbranch_scc1 .Lcnx_f0\n.Lcnx_g0:\n\t" \
  "s_waitcnt lgkmcnt(11)\n\t" \
  "v_mfma_f32_16x16x32_bf16 v[160:163], v[208:211], v[120:123], 0\n\t" \
  "buffer_store_dwordx4 v[80:83], %[vo], %[ors], %[op1] offen offset:256 sc1\n\t" \
  "s_waitcnt lgkmcnt(10)\n\t" \
  "v_mfma_f32_16x16x32_bf16 v[168:171], v[208:211], v[132:135], 0\n\t" \
  "ds_read_b64_tr_b16 v[208:209], %[a1] offset:45096\n\t" \
  "s_waitcnt lgkmcnt(8)\n\t" \
  "v_mfma_f32_16x16x32_bf16 v[144:147], v[212:215], v[100:103], v[144:147]\n\t" \
  "ds_read_b64_tr_b16 v[210:211], %[a1] offset:48168\n\t" \
  "s_waitcnt lgkmcnt(8)\n\t" \
  "v_mfma_f32_16x16x32_bf16 v[152:155], v[212:215], v[112:115], v[152:155]\n\t" \
  "s_waitcnt lgkmcnt(7)\n\t" \
  "v_mfma_f32_16x16x32_bf16 v[160:163], v[212:215], v[124:127], v[160:163]\n\t" \
  "s_waitcnt lgkmcnt(6)\n\t" \
  "v_mfma_f32_16x16x32_bf16 v[168:171], v[212:215], v[136:139], v[168:171]\n\t" \
  "ds_read_b64_tr_b16 v[212:213], %[a1] offset:43560\n\t" \
  "s_waitcnt lgkmcnt(1)\n\t" \
  "v_mfma_f32_16x16x32_bf16 v[144:147], v[208:211], v[104:107], v[144:147]\n\t" \
  "ds_read_b64_tr_b16 v[214:215], %[a1] offset:44584\n\t" \
  "v_mfma_f32_16x16x32_bf16 v[152:155], v[208:211], v[116:119], v[152:155]\n\t" \
  "v_mfma_f32_16x16x32_bf16 v[160:163], v[208:211], v[128:131], v[160:163]\n\t" \
  "v_mfma_f32_16x16x32_bf16 v[168:171], v[208:211], v[140:143], v[168:171]\n\t" \
  "ds_read_b64_tr_b16 v[208:209], %[a1] offset:46632\n\t" \
  "s_waitcnt lgkmcnt(1)\n\t" \
  "v_mfma_f32_16x16x32_bf16 v[148:151], v[212:215], v[96:99], 0\n\t" \
  "buffer_store_dwordx4 v[88:91], %[vo], %[ors], %[op1] offen offset:320 sc1\n\t" \
  "ds_read_b64_tr_b16 v[210:211], %[a1] offset:47656\n\t" \
  "v_mfma_f32_16x16x32_bf16 v[156:159], v[212:215], v[108:111], 0\n\t" \
  "v_mfma_f32_16x16x32_bf16 v[164:167], v[212:215], v[120:123], 0\n\t" \
  "v_mfma_f32_16x16x32_bf16 v[172:175], v[212:215], v[132:135], 0\n\t" \
  "ds_read_b64_tr_b16 v[212:213], %[a1] offset:45608\n\t" \
  "s_waitcnt lgkmcnt(1)\n\t" \
  "v_mfma_f32_16x16x32_bf16 v[148:151], v[208:211], v[100:103], v[148:151]\n\t" \
  "ds_read_b64_tr_b16 v[214:215], %[a1] offset:48680\n\t" \
  "v_mfma_f32_16x16x32_bf16 v[156:159], v[208:211], v[112:115], v[156:159]\n\t" \
  "s_mov_b64 exec, 3\n\t" \
  "ds_add_rtn_u32 v246, %[l8], %[vo] offset:47648\n\t" \
  "s_mov_b64 exec, -1\n\t" \
  "v_cvt_pk_f16_f32 v216, |v146|, |v147| clamp\n\t" \
  "v_cvt_pk_f16_f32 v232, v144, v145\n\t" \
  "v_mfma_f32_16x16x32_bf16 v[164:167], v[208:211], v[124:127], v[164:167]\n\t" \
  "v_cvt_pk_f16_f32 v218, |v154|, |v155| clamp\n\t" \
  "v_cvt_pk_f16_f32 v220, |v152|, |v153| clamp\n\t" \
  "v_pk_mul_f16 v233, v232, v232\n\t" \
  "v_mfma_f32_16x16x32_bf16 v[172:175], v[208:211], v[136:139], v[172:175]\n\t" \
  "ds_read_b64_tr_b16 v[208:209], %[a1] offset:49192\n\t" \
  "v_and_b32_sdwa v217, %[mk], v216 dst_sel:DWORD dst_unused:UNUSED_PAD src0_sel:DWORD src1_sel:WORD_1\n\t" \
  "v_and_b32 v216, %[mk], v216\n\t" \
  "s_waitcnt lgkmcnt(2)\n\t" \
  "v_mfma_f32_16x16x32_bf16 v[148:151], v[212:215], v[104:107], v[148:151]\n\t" \
  "ds_read_b64_tr_b16 v[210:211], %[a1] offset:50216\n\t" \
  "v_and_b32_sdwa v219, %[mk], v218 dst_sel:DWORD dst_unused:UNUSED_PAD src0_sel:DWORD src1_sel:WORD_1\n\t" \
  "v_mfma_f32_16x16x32_bf16 v[156:159], v[212:215], v[116:119], v[156:159]\n\t" \
  "ds_read_b32 v217, v217 offset:0\n\t" \
  "v_and_b32 v218, %[mk], v218\n\t" \
  "v_and_b32_sdwa v221, %[mk], v220 dst_sel:DWORD dst_unused:UNUSED_PAD src0_sel:DWORD src1_sel:WORD_1\n\t" \
  "v_mfma_f32_16x16x32_bf16 v[164:167], v[212:215], v[128:131], v[164:167]\n\t" \
  "ds_read_b32 v216, v216 offset:0\n\t" \
  "v_and_b32 v220, %[mk], v220\n\t" \
  "v_mfma_f32_16x16x32_bf16 v[172:175], v[212:215], v[140:143], v[172:175]\n\t" \
  "ds_read_b64_tr_b16 v[212:213], %[a1] offset:52264\n\t" \
  "v_pk_fma_f16 v233, v233, %[h2], %[h1]\n\t" \
  "v_pk_mul_f16 v233, v232, v233\n\t" \
  "s_cmp_lg_u32 %[wq], 4\n\t" \
  "s_cbranch_scc1 .Lcnx_c19\n\t" \
  "s_mov_b32 m0, %[pf]\n\t" \
  "s_add_u32 %[wb], %[wb], %[rs4]\n\t" \
  "buffer_load_dword %[wv], %[xrs], %[wb] offen lds\n\t" \
  ".Lcnx_c19:\n\t" \
  "s_setprio 0\n\t" \
  "v_exp_f16 v232, v233\n\t" \
  "v_cvt_pk_f16_f32 v224, |v162|, |v163| clamp\n\t" \
  "s_waitcnt lgkmcnt(3)\n\t" \
  "v_mfma_f32_16x16x32_bf16 v[176:179], v[208:211], v[96:99], 0\n\t" \
  "ds_read_b64_tr_b16 v[214:215], %[a1] offset:53288\n\t" \
  "v_cvt_pk_f16_f32 v234, v160, v161\n\t" \
  "v_cvt_pk_f16_f32 v226, |v170|, |v171| clamp\n\t" \
  "v_cvt_pk_f16_f32 v228, |v168|, |v169| clamp\n\t" \
  "v_exp_f16_sdwa v232, v233 dst_sel:WORD_1 dst_unused:UNUSED_PRESERVE src0_sel:WORD_1\n\t" \
  "v_pk_mul_f16 v235, v234, v234\n\t" \
  "v_mfma_f32_16x16x32_bf16 v[184:187], v[208:211], v[108:111], 0\n\t" \
  "buffer_load_dwordx4 v[236:239], %[vo], %[xrs], %[oc0] offen offset:0\n\t" \
  "s_cmp_lg_u32 %[wq], 5\n\t" \
  "s_cbranch_scc1 .Lcnx_c20\n\t" \
  "s_mov_b32 m0, %[pf]\n\t" \
  "s_add_u32 %[wb], %[wb], %[rs4]\n\t" \
  "buffer_load_dword %[wv], %[xrs], %[wb] offen lds\n\t" \
  ".Lcnx_c20:\n\t" \
  "ds_read_b32 v219, v219 offset:0\n\t" \
  "v_and_b32_sdwa v225, %[mk], v224 dst_sel:DWORD dst_unused:UNUSED_PAD src0_sel:DWORD src1_sel:WORD_1\n\t" \
  "v_and_b32 v224, %[mk], v224\n\t" \
  "v_and_b32_sdwa v227, %[mk], v226 dst_sel:DWORD dst_unused:UNUSED_PAD src0_sel:DWORD src1_sel:WORD_1\n\t" \
  "v_and_b32 v226, %[mk], v226\n\t" \
  "v_mfma_f32_16x16x32_bf16 v[192:195], v[208:211], v[120:123], 0\n\t" \
  "ds_read_b32 v218, v218 offset:0\n\t" \
  "v_and_b32_sdwa v229, %[mk], v228 dst_sel:DWORD dst_unused:UNUSED_PAD src0_sel:DWORD src1_sel:WORD_1\n\t" \
  "v_and_b32 v228, %[mk], v228\n\t" \
  "v_mfma_f32_16x16x32_bf16 v[200:203], v[208:211], v[132:135], 0\n\t" \
  "ds_read_b64_tr_b16 v[208:209], %[a1] offset:51240\n\t" \
  "ds_read_b64_tr_b16 v[210:211], %[a1] offset:54312\n\t" \
  "v_pk_fma_f16 v235, v235, %[h2], %[h1]\n\t" \
  "v_pk_mul_f16 v235, v234, v235\n\t" \
  "ds_read_b32 v221, v221 offset:0\n\t" \
  "ds_read_b32 v220, v220 offset:0\n\t" \
  "ds_read_b32 v225, v225 offset:0\n\t" \
  "ds_read_b32 v224, v224 offset:0\n\t" \
  "ds_read_b32 v227, v227 offset:0\n\t" \
  "ds_read_b32 v226, v226 offset:0\n\t" \
  "ds_read_b32 v229, v229 offset:0\n\t" \
  "ds_read_b32 v228, v228 offset:0\n\t" \
  "s_waitcnt lgkmcnt(11)\n\t" \
  "v_fma_f32 v147, |v147|, v217, v147 mul:4\n\t" \
  "v_fma_f32 v146, |v146|, v216, v146 mul:4\n\t" \
  "v_fma_f32 v155, |v155|, v219, v155 mul:4\n\t" \
  "v_mfma_f32_16x16x32_bf16 v[176:179], v[212:215], v[100:103], v[176:179]\n\t" \
  "s_waitcnt lgkmcnt(6)\n\t" \
  "v_fma_f32 v154, |v154|, v218, v154 mul:4\n\t" \
  "v_fma_f32 v153, |v153|, v221, v153 mul:4\n\t" \
  "v_fma_f32 v152, |v152|, v220, v152 mul:4\n\t" \
  "v_pk_fma_f16 v232, v232, %[hh], %[hh]\n\t" \
  "v_cvt_pk_bf16_f32 v147, v146, v147\n\t" \
  "v_mfma_f32_16x16x32_bf16 v[184:187], v[212:215], v[112:115], v[184:187]\n\t" \
  "s_cmp_lg_u32 %[grp], 0\n\t" \
  "s_cbranch_scc1 .Lcnx_c8\n\t" \
  "s_waitcnt vmcnt(3)\n\t" \
  ".Lcnx_c8:\n\t" \
  "s_mov_b64 exec, 3\n\t" \
  "ds_add_rtn_u32 v246, %[l8], %[vo] offset:47656\n\t" \
  "s_mov_b64 exec, -1\n\t" \
  "v_cvt_pk_bf16_f32 v155, v154, v155\n\t" \
  "s_setprio 1\n\t" \
  "v_cvt_pk_bf16_f32 v154, v152, v153\n\t" \
  "v_rcp_f16 v233, v232\n\t" \
  "v_exp_f16 v234, v235\n\t" \
  "v_cvt_pk_f16_f32 v216, |v148|, |v149| clamp\n\t" \
  "v_cvt_pk_f16_f32 v218, |v150|, |v151| clamp\n\t" \
  "v_mfma_f32_16x16x32_bf16 v[192:195], v[212:215], v[124:127], v[192:195]\n\t" \
  "v_cvt_pk_f16_f32 v220, |v156|, |v157| clamp\n\t" \
  "v_cvt_pk_f16_f32 v222, |v158|, |v159| clamp\n\t" \
  "v_rcp_f16_sdwa v232, v232 dst_sel:DWORD dst_unused:UNUSED_PAD src0_sel:WORD_1\n\t" \
  "v_exp_f16_sdwa v234, v235 dst_sel:WORD_1 dst_unused:UNUSED_PRESERVE src0_sel:WORD_1\n\t" \
  "v_and_b32_sdwa v217, %[mk], v216 dst_sel:DWORD dst_unused:UNUSED_PAD src0_sel:DWORD src1_sel:WORD_1\n\t" \
  "v_and_b32 v216, %[mk], v216\n\t" \
  "v_mfma_f32_16x16x32_bf16 v[200:203], v[212:215], v[136:139], v[200:203]\n\t" \
  "s_waitcnt lgkmcnt(0)\n\t" \
  "v_readfirstlane_b32 %[t0], v246\n\t" \
  "s_cmp_lt_u32 %[t0], %[tg]\n\t" \
  "s_cbranch_scc1 .Lcnx_w9\n.Lcnx_b9:\n\t" \
  "ds_read_b64_tr_b16 v[212:213], %[a1] offset:49704\n\t" \
  "v_and_b32_sdwa v219, %[mk], v218 dst_sel:DWORD dst_unused:UNUSED_PAD src0_sel:DWORD src1_sel:WORD_1\n\t" \
  "v_and_b32 v218, %[mk], v218\n\t" \
  "v_mfma_f32_16x16x32_bf16 v[176:179], v[208:211], v[104:107], v[176:179]\n\t" \
  "ds_read_b64_tr_b16 v[214:215], %[a1] offset:50728\n\t" \
  "v_and_b32_sdwa v221, %[mk], v220 dst_sel:DWORD dst_unused:UNUSED_PAD src0_sel:DWORD src1_sel:WORD_1\n\t" \
  "v_and_b32 v220, %[mk], v220\n\t" \
  "v_and_b32_sdwa v223, %[mk], v222 dst_sel:DWORD dst_unused:UNUSED_PAD src0_sel:DWORD src1_sel:WORD_1\n\t" \
  "v_mfma_f32_16x16x32_bf16 v[184:187], v[208:211], v[116:119], v[184:187]\n\t" \
  "s_cmp_eq_u32 %[grp], 0\n\t" \
  "s_cbranch_scc1 .Lcnx_f1\n.Lcnx_g1:\n\t" \
  "ds_read_b32 v217, v217 offset:0\n\t" \
  "ds_read_b32 v216, v216 offset:0\n\t" \
  "v_and_b32 v222, %[mk], v222\n\t" \
  "v_fma_f32 v163, |v163|, v225, v163 mul:4\n\t" \
  "v_fma_f32 v162, |v162|, v224, v162 mul:4\n\t" \
  "v_mfma_f32_16x16x32_bf16 v[192:195], v[208:211], v[128:131], v[192:195]\n\t" \
  "ds_read_b32 v219, v219 offset:0\n\t" \
  "ds_read_b32 v218, v218 offset:0\n\t" \
  "v_fma_f32 v171, |v171|, v227, v171 mul:4\n\t" \
  "v_fma_f32 v170, |v170|, v226, v170 mul:4\n\t" \
  "v_fma_f32 v169, |v169|, v229, v169 mul:4\n\t" \
  "v_fma_f32 v168, |v168|, v228, v168 mul:4\n\t" \
  "v_fma_mix_f32 v144, v144, v233, 0 op_sel_hi:[0,1,0]\n\t" \
  "v_fma_mix_f32 v145, v145, v232, 0 op_sel_hi:[0,1,0]\n\t" \
  "v_mfma_f32_16x16x32_bf16 v[200:203], v[208:211], v[140:143], v[200:203]\n\t" \
  "ds_read_b64_tr_b16 v[208:209], %[a1] offset:52776\n\t" \
  "ds_read_b64_tr_b16 v[210:211], %[a1] offset:53800\n\t" \
  "v_pk_fma_f16 v234, v234, %[hh], %[hh]\n\t" \
  "v_cvt_pk_bf16_f32 v146, v144, v145\n\t" \
  "v_cvt_pk_bf16_f32 v163, v162, v163\n\t" \
  "v_cvt_pk_bf16_f32 v171, v170, v171\n\t" \
  "v_cvt_pk_bf16_f32 v170, v168, v169\n\t" \
  "s_setprio 2\n\t" \
  "v_rcp_f16 v235, v234\n\t" \
  "s_waitcnt lgkmcnt(6)\n\t" \
  "v_mfma_f32_16x16x32_bf16 v[180:183], v[212:215], v[96:99], 0\n\t" \
  "ds_read_b32 v221, v221 offset:0\n\t" \
  "v_cvt_pk_f16_f32 v224, |v164|, |v165| clamp\n\t" \
  "v_cvt_pk_f16_f32 v226, |v166|, |v167| clamp\n\t" \
  "v_cvt_pk_f16_f32 v228, |v172|, |v173| clamp\n\t" \
  "v_cvt_pk_f16_f32 v230, |v174|, |v175| clamp\n\t" \
  "v_rcp_f16_sdwa v234, v234 dst_sel:DWORD dst_unused:UNUSED_PAD src0_sel:WORD_1\n\t" \
  "v_mfma_f32_16x16x32_bf16 v[188:191], v[212:215], v[108:111], 0\n\t" \
  "ds_read_b32 v220, v220 offset:0\n\t" \
  "v_and_b32_sdwa v225, %[mk], v224 dst_sel:DWORD dst_unused:UNUSED_PAD src0_sel:DWORD src1_sel:WORD_1\n\t" \
  "v_and_b32 v224, %[mk], v224\n\t" \
  "v_and_b32_sdwa v227, %[mk], v226 dst_sel:DWORD dst_unused:UNUSED_PAD src0_sel:DWORD src1_sel:WORD_1\n\t" \
  "v_and_b32 v226, %[mk], v226\n\t" \
  "v_mfma_f32_16x16x32_bf16 v[196:199], v[212:215], v[120:123], 0\n\t" \
  "ds_read_b32 v223, v223 offset:0\n\t" \
  "v_and_b32_sdwa v229, %[mk], v228 dst_sel:DWORD dst_unused:UNUSED_PAD src0_sel:DWORD src1_sel:WORD_1\n\t" \
  "v_and_b32 v228, %[mk], v228\n\t" \
  "v_mfma_f32_16x16x32_bf16 v[204:207], v[212:215], v[132:135], 0\n\t" \
  "ds_read_b64_tr_b16 v[212:213], %[a1] offset:51752\n\t" \
  "ds_read_b64_tr_b16 v[214:215], %[a1] offset:54824\n\t" \
  "v_and_b32_sdwa v231, %[mk], v230 dst_sel:DWORD dst_unused:UNUSED_PAD src0_sel:DWORD src1_sel:WORD_1\n\t" \
  "v_and_b32 v230, %[mk], v230\n\t" \
  "ds_read_b32 v222, v222 offset:0\n\t" \
  "ds_read_b32 v225, v225 offset:0\n\t" \
  "ds_read_b32 v224, v224 offset:0\n\t" \
  "ds_read_b32 v227, v227 offset:0\n\t" \
  "ds_read_b32 v226, v226 offset:0\n\t" \
  "ds_read_b32 v229, v229 offset:0\n\t" \
  "ds_read_b32 v228, v228 offset:0\n\t" \
  "ds_read_b32 v231, v231 offset:0\n\t" \
  "s_waitcnt lgkmcnt(15)\n\t" \
  "v_fma_f32 v149, |v149|, v217, v149 mul:4\n\t" \
  "s_waitcnt lgkmcnt(13)\n\t" \
  "v_mfma_f32_16x16x32_bf16 v[180:183], v[208:211], v[100:103], v[180:183]\n\t" \
  "ds_read_b32 v230, v230 offset:0\n\t" \
  "v_fma_f32 v148, |v148|, v216, v148 mul:4\n\t" \
  "v_fma_f32 v151, |v151|, v219, v151 mul:4\n\t" \
  "v_fma_f32 v150, |v150|, v218, v150 mul:4\n\t" \
  "s_waitcnt lgkmcnt(8)\n\t" \
  "v_fma_f32 v157, |v157|, v221, v157 mul:4\n\t" \
  "v_fma_f32 v156, |v156|, v220, v156 mul:4\n\t" \
  "v_mfma_f32_16x16x32_bf16 v[188:191], v[208:211], v[112:115], v[188:191]\n\t" \
  "s_waitcnt vmcnt(0)\n\t" \
  "s_mov_b64 exec, 3\n\t" \
  "ds_add_rtn_u32 v246, %[l8], %[vo] offset:47664\n\t" \
  "s_mov_b64 exec, -1\n\t" \
  "v_fma_f32 v159, |v159|, v223, v159 mul:4\n\t" \
  "v_fma_f32 v158, |v158|, v222, v158 mul:4\n\t" \
  "s_setprio 3\n\t" \
  "v_fma_mix_f32 v160, v160, v235, 0 op_sel_hi:[0,1,0]\n\t" \
  "v_fma_mix_f32 v161, v161, v234, 0 op_sel_hi:[0,1,0]\n\t" \
  "v_cvt_pk_bf16_f32 v162, v160, v161\n\t" \
  "v_cvt_pk_bf16_f32 v148, v148, v149\n\t" \
  "v_mfma_f32_16x16x32_bf16 v[196:199], v[208:211], v[124:127], v[196:199]\n\t" \
  "v_cvt_pk_bf16_f32 v149, v150, v151\n\t" \
  "v_cvt_pk_bf16_f32 v156, v156, v157\n\t" \
  "v_cvt_pk_bf16_f32 v157, v158, v159\n\t" \
  "v_cvt_pk_f16_f32 v216, |v178|, |v179| clamp\n\t" \
  "v_cvt_pk_f16_f32 v232, v176, v177\n\t" \
  "v_cvt_pk_f16_f32 v218, |v186|, |v187| clamp\n\t" \
  "v_mfma_f32_16x16x32_bf16 v[204:207], v[208:211], v[136:139], v[204:207]\n\t" \
  "s_waitcnt lgkmcnt(0)\n\t" \
  "v_readfirstlane_b32 %[t0], v246\n\t" \
  "s_cmp_lt_u32 %[t0], %[tg]\n\t" \
  "s_cbranch_scc1 .Lcnx_w12\n.Lcnx_b12:\n\t" \
  "ds_read_b64_tr_b16 v[208:209], %[a2] offset:24600\n\t" \
  "v_cvt_pk_f16_f32 v220, |v184|, |v185| clamp\n\t" \
  "v_pk_mul_f16 v233, v232, v232\n\t" \
  "v_and_b32_sdwa v217, %[mk], v216 dst_sel:DWORD dst_unused:UNUSED_PAD src0_sel:DWORD src1_sel:WORD_1\n\t" \
  "v_and_b32 v216, %[mk], v216\n\t" \
  "v_mfma_f32_16x16x32_bf16 v[180:183], v[212:215], v[104:107], v[180:183]\n\t" \
  "ds_read_b64_tr_b16 v[210:211], %[a2] offset:26136\n\t" \
  "v_and_b32_sdwa v219, %[mk], v218 dst_sel:DWORD dst_unused:UNUSED_PAD src0_sel:DWORD src1_sel:WORD_1\n\t" \
  "v_and_b32 v218, %[mk], v218\n\t" \
  "v_and_b32_sdwa v221, %[mk], v220 dst_sel:DWORD dst_unused:UNUSED_PAD src0_sel:DWORD src1_sel:WORD_1\n\t" \
  "v_mfma_f32_16x16x32_bf16 v[188:191], v[212:215], v[116:119], v[188:191]\n\t" \
  "s_cmp_eq_u32 %[grp], 0\n\t" \
  "s_cbranch_scc1 .Lcnx_f2\n.Lcnx_g2:\n\t" \
  "ds_read_b32 v217, v217 offset:0\n\t" \
  "v_and_b32 v220, %[mk], v220\n\t" \
  "v_pk_fma_f16 v233, v233, %[h2], %[h1]\n\t" \
  "v_pk_mul_f16 v233, v232, v233\n\t" \
  "v_mfma_f32_16x16x32_bf16 v[196:199], v[212:215], v[128:131], v[196:199]\n\t" \
  "ds_read_b32 v216, v216 offset:0\n\t" \
  "v_fma_f32 v165, |v165|, v225, v165 mul:4\n\t" \
  "v_fma_f32 v164, |v164|, v224, v164 mul:4\n\t" \
  "v_fma_f32 v167, |v167|, v227, v167 mul:4\n\t" \
  "v_fma_f32 v166, |v166|, v226, v166 mul:4\n\t" \
  "v_fma_f32 v173, |v173|, v229, v173 mul:4\n\t" \
  "v_fma_f32 v172, |v172|, v228, v172 mul:4\n\t" \
  "v_mfma_f32_16x16x32_bf16 v[204:207], v[212:215], v[140:143], v[204:207]\n\t" \
  "ds_read_b64_tr_b16 v[212:213], %[a2] offset:24608\n\t" \
  "ds_read_b64_tr_b16 v[214:215], %[a2] offset:26144\n\t" \
  "v_fma_f32 v175, |v175|, v231, v175 mul:4\n\t" \
  "v_fma_f32 v174, |v174|, v230, v174 mul:4\n\t" \
  "v_cvt_pk_bf16_f32 v164, v164, v165\n\t" \
  "v_cvt_pk_bf16_f32 v165, v166, v167\n\t" \
  "v_cvt_pk_bf16_f32 v172, v172, v173\n\t" \
  "v_cvt_pk_bf16_f32 v173, v174, v175\n\t" \
  "s_setprio 0\n\t" \
  "v_exp_f16 v232, v233\n\t" \
  "v_cvt_pk_f16_f32 v224, |v194|, |v195| clamp\n\t" \
  "v_mfma_f32_16x16x32_bf16 v[0:3], v[242:245], v[236:239], 0\n\t" \
  "ds_read_b32 v219, v219 offset:0\n\t" \
  "v_cvt_pk_f16_f32 v234, v192, v193\n\t" \
  "v_cvt_pk_f16_f32 v226, |v202|, |v203| clamp\n\t" \
  "v_mfma_f32_16x16x32_bf16 v[4:7], v[240:243], v[236:239], 0\n\t" \
  "buffer_load_dwordx4 v[236:239], %[vo], %[xrs], %[oc0] offen offset:64\n\t" \
  "s_cmp_lg_u32 %[rnd], 0\n\t" \
  "s_cbranch_scc1 .Lcnx_c21\n\t" \
  "s_sub_u32 %[xo], %[xo], %[rs6]\n\t" \
  "s_add_u32 m0, %[xl], 0\n\t" \
  "s_add_u32 %[xo], %[xo], %[rs]\n\t" \
  "buffer_load_dwordx4 %[xv], %[xrs], %[xo] offen lds\n\t" \
  "s_cmp_lg_u32 %[wq], 6\n\t" \
  "s_cbranch_scc1 .Lcnx_c21\n\t" \
  "s_mov_b32 m0, %[pf]\n\t" \
  "s_add_u32 %[wb], %[wb], %[rs4]\n\t" \
  "buffer_load_dword %[wv], %[xrs], %[wb] offen lds\n\t" \
  ".Lcnx_c21:\n\t" \
  "ds_read_b32 v218, v218 offset:0\n\t" \
  "v_cvt_pk_f16_f32 v228, |v200|, |v201| clamp\n\t" \
  "v_exp_f16_sdwa v232, v233 dst_sel:WORD_1 dst_unused:UNUSED_PRESERVE src0_sel:WORD_1\n\t" \
  "v_pk_mul_f16 v235, v234, v234\n\t" \
  "s_waitcnt lgkmcnt(6)\n\t" \
  "v_mfma_f32_16x16x32_bf16 v[0:3], v[208:211], v[146:149], v[0:3]\n\t" \
  "ds_read_b32 v221, v221 offset:0\n\t" \
  "v_and_b32_sdwa v225, %[mk], v224 dst_sel:DWORD dst_unused:UNUSED_PAD src0_sel:DWORD src1_sel:WORD_1\n\t" \
  "v_and_b32 v224, %[mk], v224\n\t" \
  "v_mfma_f32_16x16x32_bf16 v[24:27], v[208:211], v[154:157], 0\n\t" \
  "ds_read_b32 v220, v220 offset:0\n\t" \
  "v_and_b32_sdwa v227, %[mk], v226 dst_sel:DWORD dst_unused:UNUSED_PAD src0_sel:DWORD src1_sel:WORD_1\n\t" \
  "v_mfma_f32_16x16x32_bf16 v[48:51], v[208:211], v[162:165], 0\n\t" \
  "ds_read_b32 v225, v225 offset:0\n\t" \
  "v_and_b32 v226, %[mk], v226\n\t" \
  "v_and_b32_sdwa v229, %[mk], v228 dst_sel:DWORD dst_unused:UNUSED_PAD src0_sel:DWORD src1_sel:WORD_1\n\t" \
  "v_mfma_f32_16x16x32_bf16 v[72:75], v[208:211], v[170:173], 0\n\t" \
  "ds_read_b64_tr_b16 v[208:209], %[a2] offset:24664\n\t" \
  "v_and_b32 v228, %[mk], v228\n\t" \
  "s_waitcnt lgkmcnt(6)\n\t" \
  "v_mfma_f32_16x16x32_bf16 v[4:7], v[212:215], v[146:149], v[4:7]\n\t" \
  "ds_read_b64_tr_b16 v[210:211], %[a2] offset:26200\n\t" \
  "v_pk_fma_f16 v235, v235, %[h2], %[h1]\n\t" \
  "v_mfma_f32_16x16x32_bf16 v[28:31], v[212:215], v[154:157], 0\n\t" \
  "ds_read_b32 v224, v224 offset:0\n\t" \
  "v_pk_mul_f16 v235, v234, v235\n\t" \
  "s_waitcnt lgkmcnt(7)\n\t" \
  "v_fma_f32 v179, |v179|, v217, v179 mul:4\n\t" \
  "v_fma_f32 v178, |v178|, v216, v178 mul:4\n\t" \
  "v_mfma_f32_16x16x32_bf16 v[52:55], v[212:215], v[162:165], 0\n\t" \
  "ds_read_b32 v227, v227 offset:0\n\t" \
  "v_fma_f32 v187, |v187|, v219, v187 mul:4\n\t" \
  "s_waitcnt lgkmcnt(5)\n\t" \
  "v_fma_f32 v186, |v186|, v218, v186 mul:4\n\t" \
  "v_fma_f32 v185, |v185|, v221, v185 mul:4\n\t" \
  "v_mfma_f32_16x16x32_bf16 v[76:79], v[212:215], v[170:173], 0\n\t" \
  "ds_read_b64_tr_b16 v[212:213], %[a2] offset:24672\n\t" \
  "v_fma_f32 v184, |v184|, v220, v184 mul:4\n\t" \
  "v_pk_fma_f16 v232, v232, %[hh], %[hh]\n\t" \
  "s_waitcnt lgkmcnt(3)\n\t" \
  "v_mfma_f32_16x16x32_bf16 v[8:11], v[208:211], v[146:149], 0\n\t" \
  "ds_read_b64_tr_b16 v[214:215], %[a2] offset:26208\n\t" \
  "v_cvt_pk_bf16_f32 v179, v178, v179\n\t" \
  "v_cvt_pk_bf16_f32 v187, v186, v187\n\t" \
  "v_cvt_pk_bf16_f32 v186, v184, v185\n\t" \
  "v_mfma_f32_16x16x32_bf16 v[32:35], v[208:211], v[154:157], 0\n\t" \
  "ds_read_b32 v226, v226 offset:0\n\t" \
  "v_rcp_f16 v233, v232\n\t" \
  "v_exp_f16 v234, v235\n\t" \
  "v_cvt_pk_f16_f32 v216, |v180|, |v181| clamp\n\t" \
  "v_mfma_f32_16x16x32_bf16 v[56:59], v[208:211], v[162:165], 0\n\t" \
  "s_setprio 1\n\t" \
  "ds_read_b32 v229, v229 offset:0\n\t" \
  "v_cvt_pk_f16_f32 v218, |v182|, |v183| clamp\n\t" \
  "v_cvt_pk_f16_f32 v220, |v188|, |v189| clamp\n\t" \
  "v_mfma_f32_16x16x32_bf16 v[80:83], v[208:211], v[170:173], 0\n\t" \
  "ds_read_b64_tr_b16 v[208:209], %[a2] offset:24728\n\t" \
  "v_cvt_pk_f16_f32 v222, |v190|, |v191| clamp\n\t" \
  "v_rcp_f16_sdwa v232, v232 dst_sel:DWORD dst_unused:UNUSED_PAD src0_sel:WORD_1\n\t" \
  "v_exp_f16_sdwa v234, v235 dst_sel:WORD_1 dst_unused:UNUSED_PRESERVE src0_sel:WORD_1\n\t" \
  "s_waitcnt lgkmcnt(3)\n\t" \
  "v_mfma_f32_16x16x32_bf16 v[12:15], v[212:215], v[146:149], 0\n\t" \
  "ds_read_b64_tr_b16 v[210:211], %[a2] offset:26264\n\t" \
  "v_and_b32_sdwa v217, %[mk], v216 dst_sel:DWORD dst_unused:UNUSED_PAD src0_sel:DWORD src1_sel:WORD_1\n\t" \
  "v_and_b32 v216, %[mk], v216\n\t" \
  "v_mfma_f32_16x16x32_bf16 v[36:39], v[212:215], v[154:157], 0\n\t" \
  "ds_read_b32 v228, v228 offset:0\n\t" \
  "v_and_b32_sdwa v219, %[mk], v218 dst_sel:DWORD dst_unused:UNUSED_PAD src0_sel:DWORD src1_sel:WORD_1\n\t" \
  "v_mfma_f32_16x16x32_bf16 v[60:63], v[212:215], v[162:165], 0\n\t" \
  "ds_read_b32 v217, v217 offset:0\n\t" \
  "v_and_b32 v218, %[mk], v218\n\t" \
  "v_and_b32_sdwa v221, %[mk], v220 dst_sel:DWORD dst_unused:UNUSED_PAD src0_sel:DWORD src1_sel:WORD_1\n\t" \
  "v_mfma_f32_16x16x32_bf16 v[84:87], v[212:215], v[170:173], 0\n\t" \
  "ds_read_b64_tr_b16 v[212:213], %[a2] offset:24736\n\t" \
  "v_and_b32 v220, %[mk], v220\n\t" \
  "s_waitcnt lgkmcnt(3)\n\t" \
  "v_mfma_f32_16x16x32_bf16 v[16:19], v[208:211], v[146:149], 0\n\t" \
  "ds_read_b64_tr_b16 v[214:215], %[a2] offset:26272\n\t" \
  "v_and_b32_sdwa v223, %[mk], v222 dst_sel:DWORD dst_unused:UNUSED_PAD src0_sel:DWORD src1_sel:WORD_1\n\t" \
  "v_mfma_f32_16x16x32_bf16 v[40:43], v[208:211], v[154:157], 0\n\t" \
  "ds_read_b32 v216, v216 offset:0\n\t" \
  "v_and_b32 v222, %[mk], v222\n\t" \
  "v_fma_f32 v195, |v195|, v225, v195 mul:4\n\t" \
  "v_mfma_f32_16x16x32_bf16 v[64:67], v[208:211], v[162:165], 0\n\t" \
  "ds_read_b32 v219, v219 offset:0\n\t" \
  "v_fma_f32 v194, |v194|, v224, v194 mul:4\n\t" \
  "v_fma_f32 v203, |v203|, v227, v203 mul:4\n\t" \
  "v_mfma_f32_16x16x32_bf16 v[88:91], v[208:211], v[170:173], 0\n\t" \
  "ds_read_b64_tr_b16 v[208:209], %[a1] offset:36904\n\t" \
  "ds_read_b64_tr_b16 v[210:211], %[a1] offset:37928\n\t" \
  "s_waitcnt lgkmcnt(7)\n\t" \
  "v_fma_f32 v202, |v202|, v226, v202 mul:4\n\t" \
  "v_fma_f32 v201, |v201|, v229, v201 mul:4\n\t" \
  "s_waitcnt lgkmcnt(4)\n\t" \
  "v_mfma_f32_16x16x32_bf16 v[20:23], v[212:215], v[146:149], 0\n\t" \
  "ds_read_b32 v218, v218 offset:0\n\t" \
  "v_fma_f32 v200, |v200|, v228, v200 mul:4\n\t" \
  "v_fma_mix_f32 v176, v176, v233, 0 op_sel_hi:[0,1,0]\n\t" \
  "v_fma_mix_f32 v177, v177, v232, 0 op_sel_hi:[0,1,0]\n\t" \
  "v_mfma_f32_16x16x32_bf16 v[44:47], v[212:215], v[154:157], 0\n\t" \
  "ds_read_b32 v221, v221 offset:0\n\t" \
  "v_pk_fma_f16 v234, v234, %[hh], %[hh]\n\t" \
  "v_cvt_pk_bf16_f32 v178, v176, v177\n\t" \
  "v_cvt_pk_bf16_f32 v195, v194, v195\n\t" \
  "v_mfma_f32_16x16x32_bf16 v[68:71], v[212:215], v[162:165], 0\n\t" \
  "ds_read_b32 v220, v220 offset:0\n\t" \
  "v_cvt_pk_bf16_f32 v203, v202, v203\n\t" \
  "v_cvt_pk_bf16_f32 v202, v200, v201\n\t" \
  "v_rcp_f16 v235, v234\n\t" \
  "v_mfma_f32_16x16x32_bf16 v[92:95], v[212:215], v[170:173], 0\n\t" \
  "ds_read_b64_tr_b16 v[212:213], %[a1] offset:39976\n\t" \
  "s_setprio 2\n\t" \
  "v_cvt_pk_f16_f32 v224, |v196|, |v197| clamp\n\t" \
  "v_cvt_pk_f16_f32 v226, |v198|, |v199| clamp\n\t" \
  "s_waitcnt lgkmcnt(4)\n\t" \
  "v_mfma_f32_16x16x32_bf16 v[144:147], v[208:211], v[96:99], 0\n\t" \
  "ds_read_b64_tr_b16 v[214:215], %[a1] offset:41000\n\t" \
  "v_cvt_pk_f16_f32 v228, |v204|, |v205| clamp\n\t" \
  "v_cvt_pk_f16_f32 v230, |v206|, |v207| clamp\n\t" \
  "v_rcp_f16_sdwa v234, v234 dst_sel:DWORD dst_unused:UNUSED_PAD src0_sel:WORD_1\n\t" \
  "v_mfma_f32_16x16x32_bf16 v[152:155], v[208:211], v[108:111], 0\n\t" \
  "ds_read_b32 v223, v223 offset:0\n\t" \
  "v_and_b32_sdwa v225, %[mk], v224 dst_sel:DWORD dst_unused:UNUSED_PAD src0_sel:DWORD src1_sel:WORD_1\n\t" \
  "v_and_b32 v224, %[mk], v224\n\t" \
  "v_mfma_f32_16x16x32_bf16 v[160:163], v[208:211], v[120:123], 0\n\t" \
  "ds_read_b32 v222, v222 offset:0\n\t" \
  "v_and_b32_sdwa v227, %[mk], v226 dst_sel:DWORD dst_unused:UNUSED_PAD src0_sel:DWORD src1_sel:WORD_1\n\t" \
  "v_mfma_f32_16x16x32_bf16 v[168:171], v[208:211], v[132:135], 0\n\t" \
  "ds_read_b64_tr_b16 v[208:209], %[a1] offset:38952\n\t" \
  "v_and_b32 v226, %[mk], v226\n\t" \
  "v_and_b32_sdwa v229, %[mk], v228 dst_sel:DWORD dst_unused:UNUSED_PAD src0_sel:DWORD src1_sel:WORD_1\n\t" \
  "s_waitcnt lgkmcnt(3)\n\t" \
  "v_mfma_f32_16x16x32_bf16 v[144:147], v[212:215], v[100:103], v[144:147]\n\t" \
  "ds_read_b64_tr_b16 v[210:211], %[a1] offset:42024\n\t" \
  "v_and_b32 v228, %[mk], v228\n\t" \
  "v_mfma_f32_16x16x32_bf16 v[152:155], v[212:215], v[112:115], v[152:155]\n\t" \
  "ds_read_b32 v225, v225 offset:0\n\t" \
  "v_and_b32_sdwa v231, %[mk], v230 dst_sel:DWORD dst_unused:UNUSED_PAD src0_sel:DWORD src1_sel:WORD_1\n\t" \
  "v_mfma_f32_16x16x32_bf16 v[160:163], v[212:215], v[124:127], v[160:163]\n\t" \
  "ds_read_b32 v224, v224 offset:0\n\t" \
  "v_and_b32 v230, %[mk], v230\n\t" \
  "v_fma_f32 v181, |v181|, v217, v181 mul:4\n\t" \
  "v_mfma_f32_16x16x32_bf16 v[168:171], v[212:215], v[136:139], v[168:171]\n\t" \
  "ds_read_b64_tr_b16 v[212:213], %[a1] offset:37416\n\t" \
  "ds_read_b64_tr_b16 v[214:215], %[a1] offset:38440\n\t" \
  "v_fma_f32 v180, |v180|, v216, v180 mul:4\n\t" \
  "v_fma_f32 v183, |v183|, v219, v183 mul:4\n\t" \
  "s_waitcnt lgkmcnt(4)\n\t" \
  "v_mfma_f32_16x16x32_bf16 v[144:147], v[208:211], v[104:107], v[144:147]\n\t" \
  "ds_read_b32 v227, v227 offset:0\n\t" \
  "ds_read_b32 v226, v226 offset:0\n\t" \
  "v_fma_f32 v182, |v182|, v218, v182 mul:4\n\t" \
  "v_fma_f32 v189, |v189|, v221, v189 mul:4\n\t" \
  "v_mfma_f32_16x16x32_bf16 v[152:155], v[208:211], v[116:119], v[152:155]\n\t" \
  "ds_read_b32 v229, v229 offset:0\n\t" \
  "v_fma_f32 v188, |v188|, v220, v188 mul:4\n\t" \
  "v_fma_f32 v191, |v191|, v223, v191 mul:4\n\t" \
  "v_fma_f32 v190, |v190|, v222, v190 mul:4\n\t" \
  "v_mfma_f32_16x16x32_bf16 v[160:163], v[208:211], v[128:131], v[160:163]\n\t" \
  "ds_read_b32 v228, v228 offset:0\n\t" \
  "v_fma_mix_f32 v192, v192, v235, 0 op_sel_hi:[0,1,0]\n\t" \
  "v_fma_mix_f32 v193, v193, v234, 0 op_sel_hi:[0,1,0]\n\t" \
  "v_cvt_pk_bf16_f32 v194, v192, v193\n\t" \
  "v_mfma_f32_16x16x32_bf16 v[168:171], v[208:211], v[140:143], v[168:171]\n\t" \
  "ds_read_b64_tr_b16 v[208:209], %[a1] offset:40488\n\t" \
  "v_cvt_pk_bf16_f32 v180, v180, v181\n\t" \
  "v_cvt_pk_bf16_f32 v181, v182, v183\n\t" \
  "s_waitcnt lgkmcnt(5)\n\t" \
  "v_mfma_f32_16x16x32_bf16 v[148:151], v[212:215], v[96:99], 0\n\t" \
  "ds_read_b64_tr_b16 v[210:211], %[a1] offset:41512\n\t" \
  "v_cvt_pk_bf16_f32 v188, v188, v189\n\t" \
  "s_setprio 3\n\t" \
  "v_cvt_pk_bf16_f32 v189, v190, v191\n\t" \
  "v_cvt_pk_f16_f32 v216, |v146|, |v147| clamp\n\t" \
  "v_mfma_f32_16x16x32_bf16 v[156:159], v[212:215], v[108:111], 0\n\t" \
  "ds_read_b32 v231, v231 offset:0\n\t" \
  "v_cvt_pk_f16_f32 v232, v144, v145\n\t" \
  "v_cvt_pk_f16_f32 v218, |v154|, |v155| clamp\n\t" \
  "v_cvt_pk_f16_f32 v220, |v152|, |v153| clamp\n\t" \
  "v_mfma_f32_16x16x32_bf16 v[164:167], v[212:215], v[120:123], 0\n\t" \
  "ds_read_b32 v230, v230 offset:0\n\t" \
  "v_pk_mul_f16 v233, v232, v232\n\t" \
  "v_and_b32_sdwa v217, %[mk], v216 dst_sel:DWORD dst_unused:UNUSED_PAD src0_sel:DWORD src1_sel:WORD_1\n\t" \
  "v_mfma_f32_16x16x32_bf16 v[172:175], v[212:215], v[132:135], 0\n\t" \
  "ds_read_b64_tr_b16 v[212:213], %[a1] offset:39464\n\t" \
  "v_and_b32 v216, %[mk], v216\n\t" \
  "v_and_b32_sdwa v219, %[mk], v218 dst_sel:DWORD dst_unused:UNUSED_PAD src0_sel:DWORD src1_sel:WORD_1\n\t" \
  "s_waitcnt lgkmcnt(3)\n\t" \
  "v_mfma_f32_16x16x32_bf16 v[148:151], v[208:211], v[100:103], v[148:151]\n\t" \
  "ds_read_b64_tr_b16 v[214:215], %[a1] offset:42536\n\t" \
  "v_and_b32 v218, %[mk], v218\n\t" \
  "v_mfma_f32_16x16x32_bf16 v[156:159], v[208:211], v[112:115], v[156:159]\n\t" \
  "s_waitcnt vmcnt(1)\n\t" \
  "s_cmp_lg_u32 %[rnd], 1\n\t" \
  "s_cbranch_scc1 .Lcnx_c9\n\t" \
  "s_waitcnt vmcnt(0)\n\t" \
  ".Lcnx_c9:\n\t" \
  "s_mov_b64 exec, 3\n\t" \
  "ds_add_rtn_u32 v246, %[l8], %[vo] offset:47672\n\t" \
  "s_mov_b64 exec, -1\n\t" \
  "ds_read_b32 v217, v217 offset:0\n\t" \
  "v_and_b32_sdwa v221, %[mk], v220 dst_sel:DWORD dst_unused:UNUSED_PAD src0_sel:DWORD src1_sel:WORD_1\n\t" \
  "v_mfma_f32_16x16x32_bf16 v[164:167], v[208:211], v[124:127], v[164:167]\n\t" \
  "ds_read_b32 v216, v216 offset:0\n\t" \
  "v_and_b32 v220, %[mk], v220\n\t" \
  "v_pk_fma_f16 v233, v233, %[h2], %[h1]\n\t" \
  "v_mfma_f32_16x16x32_bf16 v[172:175], v[208:211], v[136:139], v[172:175]\n\t" \
  "s_waitcnt lgkmcnt(2)\n\t" \
  "v_readfirstlane_b32 %[t0], v246\n\t" \
  "s_cmp_lt_u32 %[t0], %[tg]\n\t" \
  "s_cbranch_scc1 .Lcnx_w24\n.Lcnx_b24:\n\t" \
  "ds_read_b64_tr_b16 v[208:209], %[a2] offset:30752\n\t" \
  "v_pk_mul_f16 v233, v232, v233\n\t" \
  "v_fma_f32 v197, |v197|, v225, v197 mul:4\n\t" \
  "v_mfma_f32_16x16x32_bf16 v[148:151], v[212:215], v[104:107], v[148:151]\n\t" \
  "ds_read_b64_tr_b16 v[210:211], %[a2] offset:32288\n\t" \
  "v_fma_f32 v196, |v196|, v224, v196 mul:4\n\t" \
  "v_fma_f32 v199, |v199|, v227, v199 mul:4\n\t" \
  "v_mfma_f32_16x16x32_bf16 v[156:159], v[212:215], v[116:119], v[156:159]\n\t" \
  "s_cmp_eq_u32 %[grp], 0\n\t" \
  "s_cbranch_scc1 .Lcnx_f3\n.Lcnx_g3:\n\t" \
  "ds_read_b32 v219, v219 offset:0\n\t" \
  "v_fma_f32 v198, |v198|, v226, v198 mul:4\n\t" \
  "v_fma_f32 v205, |v205|, v229, v205 mul:4\n\t" \
  "v_fma_f32 v204, |v204|, v228, v204 mul:4\n\t" \
  "v_mfma_f32_16x16x32_bf16 v[164:167], v[212:215], v[128:131], v[164:167]\n\t" \
  "ds_read_b32 v218, v218 offset:0\n\t" \
  "v_fma_f32 v207, |v207|, v231, v207 mul:4\n\t" \
  "v_fma_f32 v206, |v206|, v230, v206 mul:4\n\t" \
  "v_cvt_pk_bf16_f32 v196, v196, v197\n\t" \
  "v_mfma_f32_16x16x32_bf16 v[172:175], v[212:215], v[140:143], v[172:175]\n\t" \
  "ds_read_b64_tr_b16 v[212:213], %[a2] offset:30760\n\t" \
  "v_cvt_pk_bf16_f32 v197, v198, v199\n\t" \
  "v_cvt_pk_bf16_f32 v204, v204, v205\n\t" \
  "v_cvt_pk_bf16_f32 v205, v206, v207\n\t" \
  "s_setprio 0\n\t" \
  "v_exp_f16 v232, v233\n\t" \
  "v_cvt_pk_f16_f32 v224, |v162|, |v163| clamp\n\t" \
  "v_mfma_f32_16x16x32_bf16 v[8:11], v[242:245], v[236:239], v[8:11]\n\t" \
  "ds_read_b64_tr_b16 v[214:215], %[a2] offset:32296\n\t" \
  "v_cvt_pk_f16_f32 v234, v160, v161\n\t" \
  "v_cvt_pk_f16_f32 v226, |v170|, |v171| clamp\n\t" \
  "v_mfma_f32_16x16x32_bf16 v[12:15], v[240:243], v[236:239], v[12:15]\n\t" \
  "buffer_load_dwordx4 v[236:239], %[vo], %[xrs], %[oc0] offen offset:128\n\t" \
  "s_cmp_lg_u32 %[rnd], 0\n\t" \
  "s_cbranch_scc1 .Lcnx_c22\n\t" \
  "s_add_u32 m0, %[xl], 8200\n\t" \
  "s_add_u32 %[xo], %[xo], %[rs]\n\t" \
  "buffer_load_dwordx4 %[xv], %[xrs], %[xo] offen lds\n\t" \
  "s_cmp_lg_u32 %[wq], 7\n\t" \
  "s_cbranch_scc1 .Lcnx_c22\n\t" \
  "s_mov_b32 m0, %[pf]\n\t" \
  "s_add_u32 %[wb], %[wb], %[rs4]\n\t" \
  "buffer_load_dword %[wv], %[xrs], %[wb] offen lds\n\t" \
  ".Lcnx_c22:\n\t" \
  "ds_read_b32 v221, v221 offset:0\n\t" \
  "v_cvt_pk_f16_f32 v228, |v168|, |v169| clamp\n\t" \
  "v_exp_f16_sdwa v232, v233 dst_sel:WORD_1 dst_unused:UNUSED_PRESERVE src0_sel:WORD_1\n\t" \
  "v_pk_mul_f16 v235, v234, v234\n\t" \
  "s_waitcnt lgkmcnt(5)\n\t" \
  "v_mfma_f32_16x16x32_bf16 v[0:3], v[208:211], v[178:181], v[0:3]\n\t" \
  "ds_read_b32 v220, v220 offset:0\n\t" \
  "v_and_b32_sdwa v225, %[mk], v224 dst_sel:DWORD dst_unused:UNUSED_PAD src0_sel:DWORD src1_sel:WORD_1\n\t" \
  "v_and_b32 v224, %[mk], v224\n\t" \
  "v_mfma_f32_16x16x32_bf16 v[24:27], v[208:211], v[186:189], v[24:27]\n\t" \
  "ds_read_b32 v225, v225 offset:0\n\t" \
  "v_and_b32_sdwa v227, %[mk], v226 dst_sel:DWORD dst_unused:UNUSED_PAD src0_sel:DWORD src1_sel:WORD_1\n\t" \
  "v_mfma_f32_16x16x32_bf16 v[48:51], v[208:211], v[194:197], v[48:51]\n\t" \
  "ds_read_b32 v224, v224 offset:0\n\t" \
  "v_and_b32 v226, %[mk], v226\n\t" \
  "v_and_b32_sdwa v229, %[mk], v228 dst_sel:DWORD dst_unused:UNUSED_PAD src0_sel:DWORD src1_sel:WORD_1\n\t" \
  "v_mfma_f32_16x16x32_bf16 v[72:75], v[208:211], v[202:205], v[72:75]\n\t" \
  "ds_read_b64_tr_b16 v[208:209], %[a2] offset:30816\n\t" \
  "v_and_b32 v228, %[mk], v228\n\t" \
  "s_waitcnt lgkmcnt(5)\n\t" \
  "v_mfma_f32_16x16x32_bf16 v[4:7], v[212:215], v[178:181], v[4:7]\n\t" \
  "ds_read_b64_tr_b16 v[210:211], %[a2] offset:32352\n\t" \
  "v_pk_fma_f16 v235, v235, %[h2], %[h1]\n\t" \
  "v_mfma_f32_16x16x32_bf16 v[28:31], v[212:215], v[186:189], v[28:31]\n\t" \
  "ds_read_b32 v227, v227 offset:0\n\t" \
  "v_pk_mul_f16 v235, v234, v235\n\t" \
  "v_fma_f32 v147, |v147|, v217, v147 mul:4\n\t" \
  "v_fma_f32 v146, |v146|, v216, v146 mul:4\n\t" \
  "v_mfma_f32_16x16x32_bf16 v[52:55], v[212:215], v[194:197], v[52:55]\n\t" \
  "ds_read_b32 v226, v226 offset:0\n\t" \
  "v_fma_f32 v155, |v155|, v219, v155 mul:4\n\t" \
  "s_waitcnt lgkmcnt(6)\n\t" \
  "v_fma_f32 v154, |v154|, v218, v154 mul:4\n\t" \
  "v_fma_f32 v153, |v153|, v221, v153 mul:4\n\t" \
  "v_mfma_f32_16x16x32_bf16 v[76:79], v[212:215], v[202:205], v[76:79]\n\t" \
  "ds_read_b64_tr_b16 v[212:213], %[a2] offset:30824\n\t" \
  "v_fma_f32 v152, |v152|, v220, v152 mul:4\n\t" \
  "v_pk_fma_f16 v232, v232, %[hh], %[hh]\n\t" \
  "s_waitcnt lgkmcnt(3)\n\t" \
  "v_mfma_f32_16x16x32_bf16 v[8:11], v[208:211], v[178:181], v[8:11]\n\t" \
  "ds_read_b64_tr_b16 v[214:215], %[a2] offset:32360\n\t" \
  "v_cvt_pk_bf16_f32 v147, v146, v147\n\t" \
  "v_cvt_pk_bf16_f32 v155, v154, v155\n\t" \
  "v_cvt_pk_bf16_f32 v154, v152, v153\n\t" \
  "v_mfma_f32_16x16x32_bf16 v[32:35], v[208:211], v[186:189], v[32:35]\n\t" \
  "ds_read_b32 v229, v229 offset:0\n\t" \
  "v_rcp_f16 v233, v232\n\t" \
  "v_exp_f16 v234, v235\n\t" \
  "v_cvt_pk_f16_f32 v216, |v148|, |v149| clamp\n\t" \
  "v_mfma_f32_16x16x32_bf16 v[56:59], v[208:211], v[194:197], v[56:59]\n\t" \
  "ds_read_b32 v228, v228 offset:0\n\t" \
  "s_setprio 1\n\t" \
  "v_cvt_pk_f16_f32 v218, |v150|, |v151| clamp\n\t" \
  "v_cvt_pk_f16_f32 v220, |v156|, |v157| clamp\n\t" \
  "v_mfma_f32_16x16x32_bf16 v[80:83], v[208:211], v[202:205], v[80:83]\n\t" \
  "ds_read_b64_tr_b16 v[208:209], %[a2] offset:30880\n\t" \
  "v_cvt_pk_f16_f32 v222, |v158|, |v159| clamp\n\t" \
  "v_rcp_f16_sdwa v232, v232 dst_sel:DWORD dst_unused:UNUSED_PAD src0_sel:WORD_1\n\t" \
  "v_exp_f16_sdwa v234, v235 dst_sel:WORD_1 dst_unused:UNUSED_PRESERVE src0_sel:WORD_1\n\t" \
  "s_waitcnt lgkmcnt(3)\n\t" \
  "v_mfma_f32_16x16x32_bf16 v[12:15], v[212:215], v[178:181], v[12:15]\n\t" \
  "ds_read_b64_tr_b16 v[210:211], %[a2] offset:32416\n\t" \
  "v_and_b32_sdwa v217, %[mk], v216 dst_sel:DWORD dst_unused:UNUSED_PAD src0_sel:DWORD src1_sel:WORD_1\n\t" \
  "v_and_b32 v216, %[mk], v216\n\t" \
  "v_mfma_f32_16x16x32_bf16 v[36:39], v[212:215], v[186:189], v[36:39]\n\t" \
  "ds_read_b32 v217, v217 offset:0\n\t" \
  "v_and_b32_sdwa v219, %[mk], v218 dst_sel:DWORD dst_unused:UNUSED_PAD src0_sel:DWORD src1_sel:WORD_1\n\t" \
  "v_mfma_f32_16x16x32_bf16 v[60:63], v[212:215], v[194:197], v[60:63]\n\t" \
  "ds_read_b32 v216, v216 offset:0\n\t" \
  "v_and_b32 v218, %[mk], v218\n\t" \
  "v_and_b32_sdwa v221, %[mk], v220 dst_sel:DWORD dst_unused:UNUSED_PAD src0_sel:DWORD src1_sel:WORD_1\n\t" \
  "v_mfma_f32_16x16x32_bf16 v[84:87], v[212:215], v[202:205], v[84:87]\n\t" \
  "ds_read_b64_tr_b16 v[212:213], %[a2] offset:30888\n\t" \
  "v_and_b32 v220, %[mk], v220\n\t" \
  "s_waitcnt lgkmcnt(3)\n\t" \
  "v_mfma_f32_16x16x32_bf16 v[16:19], v[208:211], v[178:181], v[16:19]\n\t" \
  "ds_read_b64_tr_b16 v[214:215], %[a2] offset:32424\n\t" \
  "v_and_b32_sdwa v223, %[mk], v222 dst_sel:DWORD dst_unused:UNUSED_PAD src0_sel:DWORD src1_sel:WORD_1\n\t" \
  "v_mfma_f32_16x16x32_bf16 v[40:43], v[208:211], v[186:189], v[40:43]\n\t" \
  "ds_read_b32 v219, v219 offset:0\n\t" \
  "v_and_b32 v222, %[mk], v222\n\t" \
  "v_fma_f32 v163, |v163|, v225, v163 mul:4\n\t" \
  "v_mfma_f32_16x16x32_bf16 v[64:67], v[208:211], v[194:197], v[64:67]\n\t" \
  "ds_read_b32 v218, v218 offset:0\n\t" \
  "v_fma_f32 v162, |v162|, v224, v162 mul:4\n\t" \
  "v_fma_f32 v171, |v171|, v227, v171 mul:4\n\t" \
  "v_mfma_f32_16x16x32_bf16 v[88:91], v[208:211], v[202:205], v[88:91]\n\t" \
  "ds_read_b64_tr_b16 v[208:209], %[a1] offset:12304\n\t" \
  "ds_read_b64_tr_b16 v[210:211], %[a1] offset:13328\n\t" \
  "v_fma_f32 v170, |v170|, v226, v170 mul:4\n\t" \
  "v_fma_f32 v169, |v169|, v229, v169 mul:4\n\t" \
  "s_waitcnt lgkmcnt(4)\n\t" \
  "v_mfma_f32_16x16x32_bf16 v[20:23], v[212:215], v[178:181], v[20:23]\n\t" \
  "ds_read_b32 v221, v221 offset:0\n\t" \
  "v_fma_f32 v168, |v168|, v228, v168 mul:4\n\t" \
  "v_fma_mix_f32 v144, v144, v233, 0 op_sel_hi:[0,1,0]\n\t" \
  "v_fma_mix_f32 v145, v145, v232, 0 op_sel_hi:[0,1,0]\n\t" \
  "v_mfma_f32_16x16x32_bf16 v[44:47], v[212:215], v[186:189], v[44:47]\n\t" \
  "ds_read_b32 v220, v220 offset:0\n\t" \
  "v_pk_fma_f16 v234, v234, %[hh], %[hh]\n\t" \
  "v_cvt_pk_bf16_f32 v146, v144, v145\n\t" \
  "v_cvt_pk_bf16_f32 v163, v162, v163\n\t" \
  "v_mfma_f32_16x16x32_bf16 v[68:71], v[212:215], v[194:197], v[68:71]\n\t" \
  "ds_read_b32 v223, v223 offset:0\n\t" \
  "v_cvt_pk_bf16_f32 v171, v170, v171\n\t" \
  "v_cvt_pk_bf16_f32 v170, v168, v169\n\t" \
  "v_rcp_f16 v235, v234\n\t" \
  "v_mfma_f32_16x16x32_bf16 v[92:95], v[212:215], v[202:205], v[92:95]\n\t" \
  "ds_read_b64_tr_b16 v[212:213], %[a1] offset:15376\n\t" \
  "v_cvt_pk_f16_f32 v224, |v164|, |v165| clamp\n\t" \
  "s_setprio 2\n\t" \
  "v_cvt_pk_f16_f32 v226, |v166|, |v167| clamp\n\t" \
  "s_waitcnt lgkmcnt(4)\n\t" \
  "v_mfma_f32_16x16x32_bf16 v[176:179], v[208:211], v[96:99], 0\n\t" \
  "ds_read_b64_tr_b16 v[214:215], %[a1] offset:16400\n\t" \
  "v_cvt_pk_f16_f32 v228, |v172|, |v173| clamp\n\t" \
  "v_cvt_pk_f16_f32 v230, |v174|, |v175| clamp\n\t" \
  "v_rcp_f16_sdwa v234, v234 dst_sel:DWORD dst_unused:UNUSED_PAD src0_sel:WORD_1\n\t" \
  "v_mfma_f32_16x16x32_bf16 v[184:187], v[208:211], v[108:111], 0\n\t" \
  "ds_read_b32 v222, v222 offset:0\n\t" \
  "v_and_b32_sdwa v225, %[mk], v224 dst_sel:DWORD dst_unused:UNUSED_PAD src0_sel:DWORD src1_sel:WORD_1\n\t" \
  "v_and_b32 v224, %[mk], v224\n\t" \
  "v_mfma_f32_16x16x32_bf16 v[192:195], v[208:211], v[120:123], 0\n\t" \
  "ds_read_b32 v225, v225 offset:0\n\t" \
  "v_and_b32_sdwa v227, %[mk], v226 dst_sel:DWORD dst_unused:UNUSED_PAD src0_sel:DWORD src1_sel:WORD_1\n\t" \
  "v_mfma_f32_16x16x32_bf16 v[200:203], v[208:211], v[132:135], 0\n\t" \
  "ds_read_b64_tr_b16 v[208:209], %[a1] offset:14352\n\t" \
  "v_and_b32 v226, %[mk], v226\n\t" \
  "v_and_b32_sdwa v229, %[mk], v228 dst_sel:DWORD dst_unused:UNUSED_PAD src0_sel:DWORD src1_sel:WORD_1\n\t" \
  "s_waitcnt lgkmcnt(3)\n\t" \
  "v_mfma_f32_16x16x32_bf16 v[176:179], v[212:215], v[100:103], v[176:179]\n\t" \
  "ds_read_b64_tr_b16 v[210:211], %[a1] offset:17424\n\t" \
  "v_and_b32 v228, %[mk], v228\n\t" \
  "v_mfma_f32_16x16x32_bf16 v[184:187], v[212:215], v[112:115], v[184:187]\n\t" \
  "ds_read_b32 v224, v224 offset:0\n\t" \
  "v_and_b32_sdwa v231, %[mk], v230 dst_sel:DWORD dst_unused:UNUSED_PAD src0_sel:DWORD src1_sel:WORD_1\n\t" \
  "v_mfma_f32_16x16x32_bf16 v[192:195], v[212:215], v[124:127], v[192:195]\n\t" \
  "ds_read_b32 v227, v227 offset:0\n\t" \
  "v_and_b32 v230, %[mk], v230\n\t" \
  "v_fma_f32 v149, |v149|, v217, v149 mul:4\n\t" \
  "v_mfma_f32_16x16x32_bf16 v[200:203], v[212:215], v[136:139], v[200:203]\n\t" \
  "ds_read_b64_tr_b16 v[212:213], %[a1] offset:12816\n\t" \
  "ds_read_b64_tr_b16 v[214:215], %[a1] offset:13840\n\t" \
  "v_fma_f32 v148, |v148|, v216, v148 mul:4\n\t" \
  "v_fma_f32 v151, |v151|, v219, v151 mul:4\n\t" \
  "s_waitcnt lgkmcnt(4)\n\t" \
  "v_mfma_f32_16x16x32_bf16 v[176:179], v[208:211], v[104:107], v[176:179]\n\t" \
  "ds_read_b32 v226, v226 offset:0\n\t" \
  "v_fma_f32 v150, |v150|, v218, v150 mul:4\n\t" \
  "v_fma_f32 v157, |v157|, v221, v157 mul:4\n\t" \
  "v_mfma_f32_16x16x32_bf16 v[184:187], v[208:211], v[116:119], v[184:187]\n\t" \
  "ds_read_b32 v229, v229 offset:0\n\t" \
  "v_fma_f32 v156, |v156|, v220, v156 mul:4\n\t" \
  "v_fma_f32 v159, |v159|, v223, v159 mul:4\n\t" \
  "v_fma_f32 v158, |v158|, v222, v158 mul:4\n\t" \
  "v_mfma_f32_16x16x32_bf16 v[192:195], v[208:211], v[128:131], v[192:195]\n\t" \
  "ds_read_b32 v228, v228 offset:0\n\t" \
  "v_fma_mix_f32 v160, v160, v235, 0 op_sel_hi:[0,1,0]\n\t" \
  "v_fma_mix_f32 v161, v161, v234, 0 op_sel_hi:[0,1,0]\n\t" \
  "v_cvt_pk_bf16_f32 v162, v160, v161\n\t" \
  "v_mfma_f32_16x16x32_bf16 v[200:203], v[208:211], v[140:143], v[200:203]\n\t" \
  "ds_read_b64_tr_b16 v[208:209], %[a1] offset:15888\n\t" \
  "v_cvt_pk_bf16_f32 v148, v148, v149\n\t" \
  "v_cvt_pk_bf16_f32 v149, v150, v151\n\t" \
  "s_waitcnt lgkmcnt(4)\n\t" \
  "v_mfma_f32_16x16x32_bf16 v[180:183], v[212:215], v[96:99], 0\n\t" \
  "ds_read_b64_tr_b16 v[210:211], %[a1] offset:16912\n\t" \
  "v_cvt_pk_bf16_f32 v156, v156, v157\n\t" \
  "v_cvt_pk_bf16_f32 v157, v158, v159\n\t" \
  "s_setprio 3\n\t" \
  "v_cvt_pk_f16_f32 v216, |v178|, |v179| clamp\n\t" \
  "v_mfma_f32_16x16x32_bf16 v[188:191], v[212:215], v[108:111], 0\n\t" \
  "ds_read_b32 v231, v231 offset:0\n\t" \
  "v_cvt_pk_f16_f32 v232, v176, v177\n\t" \
  "v_cvt_pk_f16_f32 v218, |v186|, |v187| clamp\n\t" \
  "v_cvt_pk_f16_f32 v220, |v184|, |v185| clamp\n\t" \
  "v_mfma_f32_16x16x32_bf16 v[196:199], v[212:215], v[120:123], 0\n\t" \
  "ds_read_b32 v230, v230 offset:0\n\t" \
  "v_pk_mul_f16 v233, v232, v232\n\t" \
  "v_and_b32_sdwa v217, %[mk], v216 dst_sel:DWORD dst_unused:UNUSED_PAD src0_sel:DWORD src1_sel:WORD_1\n\t" \
  "v_mfma_f32_16x16x32_bf16 v[204:207], v[212:215], v[132:135], 0\n\t" \
  "ds_read_b64_tr_b16 v[212:213], %[a1] offset:14864\n\t" \
  "v_and_b32 v216, %[mk], v216\n\t" \
  "v_and_b32_sdwa v219, %[mk], v218 dst_sel:DWORD dst_unused:UNUSED_PAD src0_sel:DWORD src1_sel:WORD_1\n\t" \
  "s_waitcnt lgkmcnt(3)\n\t" \
  "v_mfma_f32_16x16x32_bf16 v[180:183], v[208:211], v[100:103], v[180:183]\n\t" \
  "ds_read_b64_tr_b16 v[214:215], %[a1] offset:17936\n\t" \
  "v_and_b32 v218, %[mk], v218\n\t" \
  "v_mfma_f32_16x16x32_bf16 v[188:191], v[208:211], v[112:115], v[188:191]\n\t" \
  "s_waitcnt vmcnt(1)\n\t" \
  "s_cmp_lg_u32 %[rnd], 1\n\t" \
  "s_cbranch_scc1 .Lcnx_c10\n\t" \
  "s_waitcnt vmcnt(0)\n\t" \
  ".Lcnx_c10:\n\t" \
  "s_mov_b64 exec, 3\n\t" \
  "ds_add_rtn_u32 v246, %[l8], %[vo] offset:47680\n\t" \
  "s_mov_b64 exec, -1\n\t" \
  "ds_read_b32 v217, v217 offset:0\n\t" \
  "v_and_b32_sdwa v221, %[mk], v220 dst_sel:DWORD dst_unused:UNUSED_PAD src0_sel:DWORD src1_sel:WORD_1\n\t" \
  "v_mfma_f32_16x16x32_bf16 v[196:199], v[208:211], v[124:127], v[196:199]\n\t" \
  "ds_read_b32 v216, v216 offset:0\n\t" \
  "v_and_b32 v220, %[mk], v220\n\t" \
  "v_pk_fma_f16 v233, v233, %[h2], %[h1]\n\t" \
  "v_mfma_f32_16x16x32_bf16 v[204:207], v[208:211], v[136:139], v[204:207]\n\t" \
  "s_waitcnt lgkmcnt(2)\n\t" \
  "v_readfirstlane_b32 %[t0], v246\n\t" \
  "s_cmp_lt_u32 %[t0], %[tg]\n\t" \
  "s_cbranch_scc1 .Lcnx_w36\n.Lcnx_b36:\n\t" \
  "ds_read_b64_tr_b16 v[208:209], %[a2] offset:0\n\t" \
  "v_pk_mul_f16 v233, v232, v233\n\t" \
  "v_fma_f32 v165, |v165|, v225, v165 mul:4\n\t" \
  "v_mfma_f32_16x16x32_bf16 v[180:183], v[212:215], v[104:107], v[180:183]\n\t" \
  "ds_read_b64_tr_b16 v[210:211], %[a2] offset:1536\n\t" \
  "v_fma_f32 v164, |v164|, v224, v164 mul:4\n\t" \
  "v_fma_f32 v167, |v167|, v227, v167 mul:4\n\t" \
  "v_mfma_f32_16x16x32_bf16 v[188:191], v[212:215], v[116:119], v[188:191]\n\t" \
  "s_cmp_eq_u32 %[grp], 0\n\t" \
  "s_cbranch_scc1 .Lcnx_f4\n.Lcnx_g4:\n\t" \
  "ds_read_b32 v219, v219 offset:0\n\t" \
  "v_fma_f32 v166, |v166|, v226, v166 mul:4\n\t" \
  "v_fma_f32 v173, |v173|, v229, v173 mul:4\n\t" \
  "v_fma_f32 v172, |v172|, v228, v172 mul:4\n\t" \
  "v_mfma_f32_16x16x32_bf16 v[196:199], v[212:215], v[128:131], v[196:199]\n\t" \
  "ds_read_b32 v218, v218 offset:0\n\t" \
  "v_fma_f32 v175, |v175|, v231, v175 mul:4\n\t" \
  "v_fma_f32 v174, |v174|, v230, v174 mul:4\n\t" \
  "v_cvt_pk_bf16_f32 v164, v164, v165\n\t" \
  "v_mfma_f32_16x16x32_bf16 v[204:207], v[212:215], v[140:143], v[204:207]\n\t" \
  "ds_read_b64_tr_b16 v[212:213], %[a2] offset:8\n\t" \
  "v_cvt_pk_bf16_f32 v165, v166, v167\n\t" \
  "v_cvt_pk_bf16_f32 v172, v172, v173\n\t" \
  "v_cvt_pk_bf16_f32 v173, v174, v175\n\t" \
  "s_setprio 0\n\t" \
  "v_exp_f16 v232, v233\n\t" \
  "v_cvt_pk_f16_f32 v224, |v194|, |v195| clamp\n\t" \
  "v_mfma_f32_16x16x32_bf16 v[16:19], v[242:245], v[236:239], v[16:19]\n\t" \
  "ds_read_b64_tr_b16 v[214:215], %[a2] offset:1544\n\t" \
  "v_cvt_pk_f16_f32 v234, v192, v193\n\t" \
  "v_cvt_pk_f16_f32 v226, |v202|, |v203| clamp\n\t" \
  "v_mfma_f32_16x16x32_bf16 v[20:23], v[240:243], v[236:239], v[20:23]\n\t" \
  "buffer_load_dwordx4 v[236:239], %[vo], %[xrs], %[oc0] offen offset:192\n\t" \
  "s_cmp_lg_u32 %[rnd], 0\n\t" \
  "s_cbranch_scc1 .Lcnx_c23\n\t" \
  "s_add_u32 m0, %[xl], 16400\n\t" \
  "s_add_u32 %[xo], %[xo], %[rs]\n\t" \
  "buffer_load_dwordx4 %[xv], %[xrs], %[xo] offen lds\n\t" \
  "s_cmp_lg_u32 %[wq], 4\n\t" \
  "s_cbranch_scc1 .Lcnx_c23\n\t" \
  "s_mov_b32 m0, %[pf]\n\t" \
  "s_add_u32 %[wb], %[wb], %[rs4]\n\t" \
  "buffer_load_dword %[wv], %[xrs], %[wb] offen lds\n\t" \
  ".Lcnx_c23:\n\t" \
  "ds_read_b32 v221, v221 offset:0\n\t" \
  "v_cvt_pk_f16_f32 v228, |v200|, |v201| clamp\n\t" \
  "v_exp_f16_sdwa v232, v233 dst_sel:WORD_1 dst_unused:UNUSED_PRESERVE src0_sel:WORD_1\n\t" \
  "v_pk_mul_f16 v235, v234, v234\n\t" \
  "s_waitcnt lgkmcnt(5)\n\t" \
  "v_mfma_f32_16x16x32_bf16 v[0:3], v[208:211], v[146:149], v[0:3]\n\t" \
  "ds_read_b32 v220, v220 offset:0\n\t" \
  "v_and_b32_sdwa v225, %[mk], v224 dst_sel:DWORD dst_unused:UNUSED_PAD src0_sel:DWORD src1_sel:WORD_1\n\t" \
  "v_and_b32 v224, %[mk], v224\n\t" \
  "v_mfma_f32_16x16x32_bf16 v[24:27], v[208:211], v[154:157], v[24:27]\n\t" \
  "ds_read_b32 v225, v225 offset:0\n\t" \
  "v_and_b32_sdwa v227, %[mk], v226 dst_sel:DWORD dst_unused:UNUSED_PAD src0_sel:DWORD src1_sel:WORD_1\n\t" \
  "v_mfma_f32_16x16x32_bf16 v[48:51], v[208:211], v[162:165], v[48:51]\n\t" \
  "ds_read_b32 v224, v224 offset:0\n\t" \
  "v_and_b32 v226, %[mk], v226\n\t" \
  "v_and_b32_sdwa v229, %[mk], v228 dst_sel:DWORD dst_unused:UNUSED_PAD src0_sel:DWORD src1_sel:WORD_1\n\t" \
  "v_mfma_f32_16x16x32_bf16 v[72:75], v[208:211], v[170:173], v[72:75]\n\t" \
  "ds_read_b64_tr_b16 v[208:209], %[a2] offset:64\n\t" \
  "v_and_b32 v228, %[mk], v228\n\t" \
  "s_waitcnt lgkmcnt(5)\n\t" \
  "v_mfma_f32_16x16x32_bf16 v[4:7], v[212:215], v[146:149], v[4:7]\n\t" \
  "ds_read_b64_tr_b16 v[210:211], %[a2] offset:1600\n\t" \
  "v_pk_fma_f16 v235, v235, %[h2], %[h1]\n\t" \
  "v_mfma_f32_16x16x32_bf16 v[28:31], v[212:215], v[154:157], v[28:31]\n\t" \
  "ds_read_b32 v227, v227 offset:0\n\t" \
  "v_pk_mul_f16 v235, v234, v235\n\t" \
  "v_fma_f32 v179, |v179|, v217, v179 mul:4\n\t" \
  "v_fma_f32 v178, |v178|, v216, v178 mul:4\n\t" \
  "v_mfma_f32_16x16x32_bf16 v[52:55], v[212:215], v[162:165], v[52:55]\n\t" \
  "ds_read_b32 v226, v226 offset:0\n\t" \
  "v_fma_f32 v187, |v187|, v219, v187 mul:4\n\t" \
  "s_waitcnt lgkmcnt(6)\n\t" \
  "v_fma_f32 v186, |v186|, v218, v186 mul:4\n\t" \
  "v_fma_f32 v185, |v185|, v221, v185 mul:4\n\t" \
  "v_mfma_f32_16x16x32_bf16 v[76:79], v[212:215], v[170:173], v[76:79]\n\t" \
  "ds_read_b64_tr_b16 v[212:213], %[a2] offset:72\n\t" \
  "v_fma_f32 v184, |v184|, v220, v184 mul:4\n\t" \
  "v_pk_fma_f16 v232, v232, %[hh], %[hh]\n\t" \
  "s_waitcnt lgkmcnt(3)\n\t" \
  "v_mfma_f32_16x16x32_bf16 v[8:11], v[208:211], v[146:149], v[8:11]\n\t" \
  "ds_read_b64_tr_b16 v[214:215], %[a2] offset:1608\n\t" \
  "v_cvt_pk_bf16_f32 v179, v178, v179\n\t" \
  "v_cvt_pk_bf16_f32 v187, v186, v187\n\t" \
  "v_cvt_pk_bf16_f32 v186, v184, v185\n\t" \
  "v_mfma_f32_16x16x32_bf16 v[32:35], v[208:211], v[154:157], v[32:35]\n\t" \
  "ds_read_b32 v229, v229 offset:0\n\t" \
  "v_rcp_f16 v233, v232\n\t" \
  "v_exp_f16 v234, v235\n\t" \
  "v_cvt_pk_f16_f32 v216, |v180|, |v181| clamp\n\t" \
  "v_mfma_f32_16x16x32_bf16 v[56:59], v[208:211], v[162:165], v[56:59]\n\t" \
  "ds_read_b32 v228, v228 offset:0\n\t" \
  "s_setprio 1\n\t" \
  "v_cvt_pk_f16_f32 v218, |v182|, |v183| clamp\n\t" \
  "v_cvt_pk_f16_f32 v220, |v188|, |v189| clamp\n\t" \
  "v_mfma_f32_16x16x32_bf16 v[80:83], v[208:211], v[170:173], v[80:83]\n\t" \
  "ds_read_b64_tr_b16 v[208:209], %[a2] offset:128\n\t" \
  "v_cvt_pk_f16_f32 v222, |v190|, |v191| clamp\n\t" \
  "v_rcp_f16_sdwa v232, v232 dst_sel:DWORD dst_unused:UNUSED_PAD src0_sel:WORD_1\n\t" \
  "v_exp_f16_sdwa v234, v235 dst_sel:WORD_1 dst_unused:UNUSED_PRESERVE src0_sel:WORD_1\n\t" \
  "s_waitcnt lgkmcnt(3)\n\t" \
  "v_mfma_f32_16x16x32_bf16 v[12:15], v[212:215], v[146:149], v[12:15]\n\t" \
  "ds_read_b64_tr_b16 v[210:211], %[a2] offset:1664\n\t" \
  "v_and_b32_sdwa v217, %[mk], v216 dst_sel:DWORD dst_unused:UNUSED_PAD src0_sel:DWORD src1_sel:WORD_1\n\t" \
  "v_and_b32 v216, %[mk], v216\n\t" \
  "v_mfma_f32_16x16x32_bf16 v[36:39], v[212:215], v[154:157], v[36:39]\n\t" \
  "ds_read_b32 v217, v217 offset:0\n\t" \
  "v_and_b32_sdwa v219, %[mk], v218 dst_sel:DWORD dst_unused:UNUSED_PAD src0_sel:DWORD src1_sel:WORD_1\n\t" \
  "v_mfma_f32_16x16x32_bf16 v[60:63], v[212:215], v[162:165], v[60:63]\n\t" \
  "ds_read_b32 v216, v216 offset:0\n\t" \
  "v_and_b32 v218, %[mk], v218\n\t" \
  "v_and_b32_sdwa v221, %[mk], v220 dst_sel:DWORD dst_unused:UNUSED_PAD src0_sel:DWORD src1_sel:WORD_1\n\t" \
  "v_mfma_f32_16x16x32_bf16 v[84:87], v[212:215], v[170:173], v[84:87]\n\t" \
  "ds_read_b64_tr_b16 v[212:213], %[a2] offset:136\n\t" \
  "v_and_b32 v220, %[mk], v220\n\t" \
  "s_waitcnt lgkmcnt(3)\n\t" \
  "v_mfma_f32_16x16x32_bf16 v[16:19], v[208:211], v[146:149], v[16:19]\n\t" \
  "ds_read_b64_tr_b16 v[214:215], %[a2] offset:1672\n\t" \
  "v_and_b32_sdwa v223, %[mk], v222 dst_sel:DWORD dst_unused:UNUSED_PAD src0_sel:DWORD src1_sel:WORD_1\n\t" \
  "v_mfma_f32_16x16x32_bf16 v[40:43], v[208:211], v[154:157], v[40:43]\n\t" \
  "ds_read_b32 v219, v219 offset:0\n\t" \
  "v_and_b32 v222, %[mk], v222\n\t" \
  "v_fma_f32 v195, |v195|, v225, v195 mul:4\n\t" \
  "v_mfma_f32_16x16x32_bf16 v[64:67], v[208:211], v[162:165], v[64:67]\n\t" \
  "ds_read_b32 v218, v218 offset:0\n\t" \
  "v_fma_f32 v194, |v194|, v224, v194 mul:4\n\t" \
  "v_fma_f32 v203, |v203|, v227, v203 mul:4\n\t" \
  "v_mfma_f32_16x16x32_bf16 v[88:91], v[208:211], v[170:173], v[88:91]\n\t" \
  "ds_read_b64_tr_b16 v[208:209], %[a1] offset:43048\n\t" \
  "ds_read_b64_tr_b16 v[210:211], %[a1] offset:44072\n\t" \
  "v_fma_f32 v202, |v202|, v226, v202 mul:4\n\t" \
  "v_fma_f32 v201, |v201|, v229, v201 mul:4\n\t" \
  "s_waitcnt lgkmcnt(4)\n\t" \
  "v_mfma_f32_16x16x32_bf16 v[20:23], v[212:215], v[146:149], v[20:23]\n\t" \
  "ds_read_b32 v221, v221 offset:0\n\t" \
  "v_fma_f32 v200, |v200|, v228, v200 mul:4\n\t" \
  "v_fma_mix_f32 v176, v176, v233, 0 op_sel_hi:[0,1,0]\n\t" \
  "v_fma_mix_f32 v177, v177, v232, 0 op_sel_hi:[0,1,0]\n\t" \
  "v_mfma_f32_16x16x32_bf16 v[44:47], v[212:215], v[154:157], v[44:47]\n\t" \
  "ds_read_b32 v220, v220 offset:0\n\t" \
  "v_pk_fma_f16 v234, v234, %[hh], %[hh]\n\t" \
  "v_cvt_pk_bf16_f32 v178, v176, v177\n\t" \
  "v_cvt_pk_bf16_f32 v195, v194, v195\n\t" \
  "v_mfma_f32_16x16x32_bf16 v[68:71], v[212:215], v[162:165], v[68:71]\n\t" \
  "ds_read_b32 v223, v223 offset:0\n\t" \
  "v_cvt_pk_bf16_f32 v203, v202, v203\n\t" \
  "v_cvt_pk_bf16_f32 v202, v200, v201\n\t" \
  "v_rcp_f16 v235, v234\n\t" \
  "v_mfma_f32_16x16x32_bf16 v[92:95], v[212:215], v[170:173], v[92:95]\n\t" \
  "ds_read_b64_tr_b16 v[212:213], %[a1] offset:46120\n\t" \
  "v_cvt_pk_f16_f32 v224, |v196|, |v197| clamp\n\t" \
  "s_setprio 2\n\t" \
  "v_cvt_pk_f16_f32 v226, |v198|, |v199| clamp\n\t" \
  "s_waitcnt lgkmcnt(4)\n\t" \
  "v_mfma_f32_16x16x32_bf16 v[144:147], v[208:211], v[96:99], 0\n\t" \
  "ds_read_b64_tr_b16 v[214:215], %[a1] offset:47144\n\t" \
  "v_cvt_pk_f16_f32 v228, |v204|, |v205| clamp\n\t" \
  "v_cvt_pk_f16_f32 v230, |v206|, |v207| clamp\n\t" \
  "v_rcp_f16_sdwa v234, v234 dst_sel:DWORD dst_unused:UNUSED_PAD src0_sel:WORD_1\n\t" \
  "v_mfma_f32_16x16x32_bf16 v[152:155], v[208:211], v[108:111], 0\n\t" \
  "ds_read_b32 v222, v222 offset:0\n\t" \
  "v_and_b32_sdwa v225, %[mk], v224 dst_sel:DWORD dst_unused:UNUSED_PAD src0_sel:DWORD src1_sel:WORD_1\n\t" \
  "v_and_b32 v224, %[mk], v224\n\t" \
  "v_mfma_f32_16x16x32_bf16 v[160:163], v[208:211], v[120:123], 0\n\t" \
  "ds_read_b32 v225, v225 offset:0\n\t" \
  "v_and_b32_sdwa v227, %[mk], v226 dst_sel:DWORD dst_unused:UNUSED_PAD src0_sel:DWORD src1_sel:WORD_1\n\t" \
  "v_mfma_f32_16x16x32_bf16 v[168:171], v[208:211], v[132:135], 0\n\t" \
  "ds_read_b64_tr_b16 v[208:209], %[a1] offset:45096\n\t" \
  "v_and_b32 v226, %[mk], v226\n\t" \
  "v_and_b32_sdwa v229, %[mk], v228 dst_sel:DWORD dst_unused:UNUSED_PAD src0_sel:DWORD src1_sel:WORD_1\n\t" \
  "s_waitcnt lgkmcnt(3)\n\t" \
  "v_mfma_f32_16x16x32_bf16 v[144:147], v[212:215], v[100:103], v[144:147]\n\t" \
  "ds_read_b64_tr_b16 v[210:211], %[a1] offset:48168\n\t" \
  "v_and_b32 v228, %[mk], v228\n\t" \
  "v_mfma_f32_16x16x32_bf16 v[152:155], v[212:215], v[112:115], v[152:155]\n\t" \
  "ds_read_b32 v224, v224 offset:0\n\t" \
  "v_and_b32_sdwa v231, %[mk], v230 dst_sel:DWORD dst_unused:UNUSED_PAD src0_sel:DWORD src1_sel:WORD_1\n\t" \
  "v_mfma_f32_16x16x32_bf16 v[160:163], v[212:215], v[124:127], v[160:163]\n\t" \
  "ds_read_b32 v227, v227 offset:0\n\t" \
  "v_and_b32 v230, %[mk], v230\n\t" \
  "v_fma_f32 v181, |v181|, v217, v181 mul:4\n\t" \
  "v_mfma_f32_16x16x32_bf16 v[168:171], v[212:215], v[136:139], v[168:171]\n\t" \
  "ds_read_b64_tr_b16 v[212:213], %[a1] offset:43560\n\t" \
  "ds_read_b64_tr_b16 v[214:215], %[a1] offset:44584\n\t" \
  "v_fma_f32 v180, |v180|, v216, v180 mul:4\n\t" \
  "v_fma_f32 v183, |v183|, v219, v183 mul:4\n\t" \
  "s_waitcnt lgkmcnt(4)\n\t" \
  "v_mfma_f32_16x16x32_bf16 v[144:147], v[208:211], v[104:107], v[144:147]\n\t" \
  "ds_read_b32 v226, v226 offset:0\n\t" \
  "v_fma_f32 v182, |v182|, v218, v182 mul:4\n\t" \
  "v_fma_f32 v189, |v189|, v221, v189 mul:4\n\t" \
  "v_mfma_f32_16x16x32_bf16 v[152:155], v[208:211], v[116:119], v[152:155]\n\t" \
  "ds_read_b32 v229, v229 offset:0\n\t" \
  "v_fma_f32 v188, |v188|, v220, v188 mul:4\n\t" \
  "v_fma_f32 v191, |v191|, v223, v191 mul:4\n\t" \
  "v_fma_f32 v190, |v190|, v222, v190 mul:4\n\t" \
  "v_mfma_f32_16x16x32_bf16 v[160:163], v[208:211], v[128:131], v[160:163]\n\t" \
  "ds_read_b32 v228, v228 offset:0\n\t" \
  "v_fma_mix_f32 v192, v192, v235, 0 op_sel_hi:[0,1,0]\n\t" \
  "v_fma_mix_f32 v193, v193, v234, 0 op_sel_hi:[0,1,0]\n\t" \
  "v_cvt_pk_bf16_f32 v194, v192, v193\n\t" \
  "v_mfma_f32_16x16x32_bf16 v[168:171], v[208:211], v[140:143], v[168:171]\n\t" \
  "ds_read_b64_tr_b16 v[208:209], %[a1] offset:46632\n\t" \
  "v_cvt_pk_bf16_f32 v180, v180, v181\n\t" \
  "v_cvt_pk_bf16_f32 v181, v182, v183\n\t" \
  "s_waitcnt lgkmcnt(4)\n\t" \
  "v_mfma_f32_16x16x32_bf16 v[148:151], v[212:215], v[96:99], 0\n\t" \
  "ds_read_b64_tr_b16 v[210:211], %[a1] offset:47656\n\t" \
  "v_cvt_pk_bf16_f32 v188, v188, v189\n\t" \
  "v_cvt_pk_bf16_f32 v189, v190, v191\n\t" \
  "s_setprio 3\n\t" \
  "v_cvt_pk_f16_f32 v216, |v146|, |v147| clamp\n\t" \
  "v_mfma_f32_16x16x32_bf16 v[156:159], v[212:215], v[108:111], 0\n\t" \
  "ds_read_b32 v231, v231 offset:0\n\t" \
  "v_cvt_pk_f16_f32 v232, v144, v145\n\t" \
  "v_cvt_pk_f16_f32 v218, |v154|, |v155| clamp\n\t" \
  "v_cvt_pk_f16_f32 v220, |v152|, |v153| clamp\n\t" \
  "v_mfma_f32_16x16x32_bf16 v[164:167], v[212:215], v[120:123], 0\n\t" \
  "ds_read_b32 v230, v230 offset:0\n\t" \
  "v_pk_mul_f16 v233, v232, v232\n\t" \
  "v_and_b32_sdwa v217, %[mk], v216 dst_sel:DWORD dst_unused:UNUSED_PAD src0_sel:DWORD src1_sel:WORD_1\n\t" \
  "v_mfma_f32_16x16x32_bf16 v[172:175], v[212:215], v[132:135], 0\n\t" \
  "ds_read_b64_tr_b16 v[212:213], %[a1] offset:45608\n\t" \
  "v_and_b32 v216, %[mk], v216\n\t" \
  "v_and_b32_sdwa v219, %[mk], v218 dst_sel:DWORD dst_unused:UNUSED_PAD src0_sel:DWORD src1_sel:WORD_1\n\t" \
  "s_waitcnt lgkmcnt(3)\n\t" \
  "v_mfma_f32_16x16x32_bf16 v[148:151], v[208:211], v[100:103], v[148:151]\n\t" \
  "ds_read_b64_tr_b16 v[214:215], %[a1] offset:48680\n\t" \
  "v_and_b32 v218, %[mk], v218\n\t" \
  "v_mfma_f32_16x16x32_bf16 v[156:159], v[208:211], v[112:115], v[156:159]\n\t" \
  "s_waitcnt vmcnt(1)\n\t" \
  "s_cmp_lg_u32 %[rnd], 1\n\t" \
  "s_cbranch_scc1 .Lcnx_c11\n\t" \
  "s_waitcnt vmcnt(0)\n\t" \
  ".Lcnx_c11:\n\t" \
  "s_mov_b64 exec, 3\n\t" \
  "ds_add_rtn_u32 v246, %[l8], %[vo] offset:47688\n\t" \
  "s_mov_b64 exec, -1\n\t" \
  "ds_read_b32 v217, v217 offset:0\n\t" \
  "v_and_b32_sdwa v221, %[mk], v220 dst_sel:DWORD dst_unused:UNUSED_PAD src0_sel:DWORD src1_sel:WORD_1\n\t" \
  "v_mfma_f32_16x16x32_bf16 v[164:167], v[208:211], v[124:127], v[164:167]\n\t" \
  "ds_read_b32 v216, v216 offset:0\n\t" \
  "v_and_b32 v220, %[mk], v220\n\t" \
  "v_pk_fma_f16 v233, v233, %[h2], %[h1]\n\t" \
  "v_mfma_f32_16x16x32_bf16 v[172:175], v[208:211], v[136:139], v[172:175]\n\t" \
  "s_waitcnt lgkmcnt(2)\n\t" \
  "v_readfirstlane_b32 %[t0], v246\n\t" \
  "s_cmp_lt_u32 %[t0], %[tg]\n\t" \
  "s_cbranch_scc1 .Lcnx_w48\n.Lcnx_b48:\n\t" \
  "ds_read_b64_tr_b16 v[208:209], %[a2] offset:6152\n\t" \
  "v_pk_mul_f16 v233, v232, v233\n\t" \
  "v_fma_f32 v197, |v197|, v225, v197 mul:4\n\t" \
  "v_mfma_f32_16x16x32_bf16 v[148:151], v[212:215], v[104:107], v[148:151]\n\t" \
  "ds_read_b64_tr_b16 v[210:211], %[a2] offset:7688\n\t" \
  "v_fma_f32 v196, |v196|, v224, v196 mul:4\n\t" \
  "v_fma_f32 v199, |v199|, v227, v199 mul:4\n\t" \
  "v_mfma_f32_16x16x32_bf16 v[156:159], v[212:215], v[116:119], v[156:159]\n\t" \
  "s_cmp_eq_u32 %[grp], 0\n\t" \
  "s_cbranch_scc1 .Lcnx_f5\n.Lcnx_g5:\n\t" \
  "ds_read_b32 v219, v219 offset:0\n\t" \
  "v_fma_f32 v198, |v198|, v226, v198 mul:4\n\t" \
  "v_fma_f32 v205, |v205|, v229, v205 mul:4\n\t" \
  "v_fma_f32 v204, |v204|, v228, v204 mul:4\n\t" \
  "v_mfma_f32_16x16x32_bf16 v[164:167], v[212:215], v[128:131], v[164:167]\n\t" \
  "ds_read_b32 v218, v218 offset:0\n\t" \
  "v_fma_f32 v207, |v207|, v231, v207 mul:4\n\t" \
  "v_fma_f32 v206, |v206|, v230, v206 mul:4\n\t" \
  "v_cvt_pk_bf16_f32 v196, v196, v197\n\t" \
  "v_mfma_f32_16x16x32_bf16 v[172:175], v[212:215], v[140:143], v[172:175]\n\t" \
  "ds_read_b64_tr_b16 v[212:213], %[a2] offset:6160\n\t" \
  "v_cvt_pk_bf16_f32 v197, v198, v199\n\t" \
  "v_cvt_pk_bf16_f32 v204, v204, v205\n\t" \
  "v_cvt_pk_bf16_f32 v205, v206, v207\n\t" \
  "s_setprio 0\n\t" \
  "v_exp_f16 v232, v233\n\t" \
  "v_cvt_pk_f16_f32 v224, |v162|, |v163| clamp\n\t" \
  "v_mfma_f32_16x16x32_bf16 v[24:27], v[242:245], v[236:239], v[24:27]\n\t" \
  "ds_read_b64_tr_b16 v[214:215], %[a2] offset:7696\n\t" \
  "v_cvt_pk_f16_f32 v234, v160, v161\n\t" \
  "v_cvt_pk_f16_f32 v226, |v170|, |v171| clamp\n\t" \
  "v_mfma_f32_16x16x32_bf16 v[28:31], v[240:243], v[236:239], v[28:31]\n\t" \
  "buffer_load_dwordx4 v[236:239], %[vo], %[xrs], %[oc0] offen offset:256\n\t" \
  "s_cmp_lg_u32 %[rnd], 0\n\t" \
  "s_cbranch_scc1 .Lcnx_c24\n\t" \
  "s_add_u32 m0, %[xl], 24600\n\t" \
  "s_add_u32 %[xo], %[xo], %[rs]\n\t" \
  "buffer_load_dwordx4 %[xv], %[xrs], %[xo] offen lds\n\t" \
  "s_cmp_lg_u32 %[wq], 5\n\t" \
  "s_cbranch_scc1 .Lcnx_c24\n\t" \
  "s_mov_b32 m0, %[pf]\n\t" \
  "s_add_u32 %[wb], %[wb], %[rs4]\n\t" \
  "buffer_load_dword %[wv], %[xrs], %[wb] offen lds\n\t" \
  ".Lcnx_c24:\n\t" \
  "ds_read_b32 v221, v221 offset:0\n\t" \
  "v_cvt_pk_f16_f32 v228, |v168|, |v169| clamp\n\t" \
  "v_exp_f16_sdwa v232, v233 dst_sel:WORD_1 dst_unused:UNUSED_PRESERVE src0_sel:WORD_1\n\t" \
  "v_pk_mul_f16 v235, v234, v234\n\t" \
  "s_waitcnt lgkmcnt(5)\n\t" \
  "v_mfma_f32_16x16x32_bf16 v[0:3], v[208:211], v[178:181], v[0:3]\n\t" \
  "ds_read_b32 v220, v220 offset:0\n\t" \
  "v_and_b32_sdwa v225, %[mk], v224 dst_sel:DWORD dst_unused:UNUSED_PAD src0_sel:DWORD src1_sel:WORD_1\n\t" \
  "v_and_b32 v224, %[mk], v224\n\t" \
  "v_mfma_f32_16x16x32_bf16 v[24:27], v[208:211], v[186:189], v[24:27]\n\t" \
  "ds_read_b32 v225, v225 offset:0\n\t" \
  "v_and_b32_sdwa v227, %[mk], v226 dst_sel:DWORD dst_unused:UNUSED_PAD src0_sel:DWORD src1_sel:WORD_1\n\t" \
  "v_mfma_f32_16x16x32_bf16 v[48:51], v[208:211], v[194:197], v[48:51]\n\t" \
  "ds_read_b32 v224, v224 offset:0\n\t" \
  "v_and_b32 v226, %[mk], v226\n\t" \
  "v_and_b32_sdwa v229, %[mk], v228 dst_sel:DWORD dst_unused:UNUSED_PAD src0_sel:DWORD src1_sel:WORD_1\n\t" \
  "v_mfma_f32_16x16x32_bf16 v[72:75], v[208:211], v[202:205], v[72:75]\n\t" \
  "ds_read_b64_tr_b16 v[208:209], %[a2] offset:6216\n\t" \
  "v_and_b32 v228, %[mk], v228\n\t" \
  "s_waitcnt lgkmcnt(5)\n\t" \
  "v_mfma_f32_16x16x32_bf16 v[4:7], v[212:215], v[178:181], v[4:7]\n\t" \
  "ds_read_b64_tr_b16 v[210:211], %[a2] offset:7752\n\t" \
  "v_pk_fma_f16 v235, v235, %[h2], %[h1]\n\t" \
  "v_mfma_f32_16x16x32_bf16 v[28:31], v[212:215], v[186:189], v[28:31]\n\t" \
  "ds_read_b32 v227, v227 offset:0\n\t" \
  "v_pk_mul_f16 v235, v234, v235\n\t" \
  "v_fma_f32 v147, |v147|, v217, v147 mul:4\n\t" \
  "v_fma_f32 v146, |v146|, v216, v146 mul:4\n\t" \
  "v_mfma_f32_16x16x32_bf16 v[52:55], v[212:215], v[194:197], v[52:55]\n\t" \
  "ds_read_b32 v226, v226 offset:0\n\t" \
  "v_fma_f32 v155, |v155|, v219, v155 mul:4\n\t" \
  "s_waitcnt lgkmcnt(6)\n\t" \
  "v_fma_f32 v154, |v154|, v218, v154 mul:4\n\t" \
  "v_fma_f32 v153, |v153|, v221, v153 mul:4\n\t" \
  "v_mfma_f32_16x16x32_bf16 v[76:79], v[212:215], v[202:205], v[76:79]\n\t" \
  "ds_read_b64_tr_b16 v[212:213], %[a2] offset:6224\n\t" \
  "v_fma_f32 v152, |v152|, v220, v152 mul:4\n\t" \
  "v_pk_fma_f16 v232, v232, %[hh], %[hh]\n\t" \
  "s_waitcnt lgkmcnt(3)\n\t" \
  "v_mfma_f32_16x16x32_bf16 v[8:11], v[208:211], v[178:181], v[8:11]\n\t" \
  "ds_read_b64_tr_b16 v[214:215], %[a2] offset:7760\n\t" \
  "v_cvt_pk_bf16_f32 v147, v146, v147\n\t" \
  "v_cvt_pk_bf16_f32 v155, v154, v155\n\t" \
  "v_cvt_pk_bf16_f32 v154, v152, v153\n\t" \
  "v_mfma_f32_16x16x32_bf16 v[32:35], v[208:211], v[186:189], v[32:35]\n\t" \
  "ds_read_b32 v229, v229 offset:0\n\t" \
  "v_rcp_f16 v233, v232\n\t" \
  "v_exp_f16 v234, v235\n\t" \
  "v_cvt_pk_f16_f32 v216, |v148|, |v149| clamp\n\t" \
  "v_mfma_f32_16x16x32_bf16 v[56:59], v[208:211], v[194:197], v[56:59]\n\t" \
  "ds_read_b32 v228, v228 offset:0\n\t" \
  "s_setprio 1\n\t" \
  "v_cvt_pk_f16_f32 v218, |v150|, |v151| clamp\n\t" \
  "v_cvt_pk_f16_f32 v220, |v156|, |v157| clamp\n\t" \
  "v_mfma_f32_16x16x32_bf16 v[80:83], v[208:211], v[202:205], v[80:83]\n\t" \
  "ds_read_b64_tr_b16 v[208:209], %[a2] offset:6280\n\t" \
  "v_cvt_pk_f16_f32 v222, |v158|, |v159| clamp\n\t" \
  "v_rcp_f16_sdwa v232, v232 dst_sel:DWORD dst_unused:UNUSED_PAD src0_sel:WORD_1\n\t" \
  "v_exp_f16_sdwa v234, v235 dst_sel:WORD_1 dst_unused:UNUSED_PRESERVE src0_sel:WORD_1\n\t" \
  "s_waitcnt lgkmcnt(3)\n\t" \
  "v_mfma_f32_16x16x32_bf16 v[12:15], v[212:215], v[178:181], v[12:15]\n\t" \
  "ds_read_b64_tr_b16 v[210:211], %[a2] offset:7816\n\t" \
  "v_and_b32_sdwa v217, %[mk], v216 dst_sel:DWORD dst_unused:UNUSED_PAD src0_sel:DWORD src1_sel:WORD_1\n\t" \
  "v_and_b32 v216, %[mk], v216\n\t" \
  "v_mfma_f32_16x16x32_bf16 v[36:39], v[212:215], v[186:189], v[36:39]\n\t" \
  "ds_read_b32 v217, v217 offset:0\n\t" \
  "v_and_b32_sdwa v219, %[mk], v218 dst_sel:DWORD dst_unused:UNUSED_PAD src0_sel:DWORD src1_sel:WORD_1\n\t" \
  "v_mfma_f32_16x16x32_bf16 v[60:63], v[212:215], v[194:197], v[60:63]\n\t" \
  "ds_read_b32 v216, v216 offset:0\n\t" \
  "v_and_b32 v218, %[mk], v218\n\t" \
  "v_and_b32_sdwa v221, %[mk], v220 dst_sel:DWORD dst_unused:UNUSED_PAD src0_sel:DWORD src1_sel:WORD_1\n\t" \
  "v_mfma_f32_16x16x32_bf16 v[84:87], v[212:215], v[202:205], v[84:87]\n\t" \
  "ds_read_b64_tr_b16 v[212:213], %[a2] offset:6288\n\t" \
  "v_and_b32 v220, %[mk], v220\n\t" \
  "s_waitcnt lgkmcnt(3)\n\t" \
  "v_mfma_f32_16x16x32_bf16 v[16:19], v[208:211], v[178:181], v[16:19]\n\t" \
  "ds_read_b64_tr_b16 v[214:215], %[a2] offset:7824\n\t" \
  "v_and_b32_sdwa v223, %[mk], v222 dst_sel:DWORD dst_unused:UNUSED_PAD src0_sel:DWORD src1_sel:WORD_1\n\t" \
  "v_mfma_f32_16x16x32_bf16 v[40:43], v[208:211], v[186:189], v[40:43]\n\t" \
  "ds_read_b32 v219, v219 offset:0\n\t" \
  "v_and_b32 v222, %[mk], v222\n\t" \
  "v_fma_f32 v163, |v163|, v225, v163 mul:4\n\t" \
  "v_mfma_f32_16x16x32_bf16 v[64:67], v[208:211], v[194:197], v[64:67]\n\t" \
  "ds_read_b32 v218, v218 offset:0\n\t" \
  "v_fma_f32 v162, |v162|, v224, v162 mul:4\n\t" \
  "v_fma_f32 v171, |v171|, v227, v171 mul:4\n\t" \
  "v_mfma_f32_16x16x32_bf16 v[88:91], v[208:211], v[202:205], v[88:91]\n\t" \
  "ds_read_b64_tr_b16 v[208:209], %[a1] offset:49192\n\t" \
  "ds_read_b64_tr_b16 v[210:211], %[a1] offset:50216\n\t" \
  "v_fma_f32 v170, |v170|, v226, v170 mul:4\n\t" \
  "v_fma_f32 v169, |v169|, v229, v169 mul:4\n\t" \
  "s_waitcnt lgkmcnt(4)\n\t" \
  "v_mfma_f32_16x16x32_bf16 v[20:23], v[212:215], v[178:181], v[20:23]\n\t" \
  "ds_read_b32 v221, v221 offset:0\n\t" \
  "v_fma_f32 v168, |v168|, v228, v168 mul:4\n\t" \
  "v_fma_mix_f32 v144, v144, v233, 0 op_sel_hi:[0,1,0]\n\t" \
  "v_fma_mix_f32 v145, v145, v232, 0 op_sel_hi:[0,1,0]\n\t" \
  "v_mfma_f32_16x16x32_bf16 v[44:47], v[212:215], v[186:189], v[44:47]\n\t" \
  "ds_read_b32 v220, v220 offset:0\n\t" \
  "v_pk_fma_f16 v234, v234, %[hh], %[hh]\n\t" \
  "v_cvt_pk_bf16_f32 v146, v144, v145\n\t" \
  "v_cvt_pk_bf16_f32 v163, v162, v163\n\t" \
  "v_mfma_f32_16x16x32_bf16 v[68:71], v[212:215], v[194:197], v[68:71]\n\t" \
  "ds_read_b32 v223, v223 offset:0\n\t" \
  "v_cvt_pk_bf16_f32 v171, v170, v171\n\t" \
  "v_cvt_pk_bf16_f32 v170, v168, v169\n\t" \
  "v_rcp_f16 v235, v234\n\t" \
  "v_mfma_f32_16x16x32_bf16 v[92:95], v[212:215], v[202:205], v[92:95]\n\t" \
  "ds_read_b64_tr_b16 v[212:213], %[a1] offset:52264\n\t" \
  "v_cvt_pk_f16_f32 v224, |v164|, |v165| clamp\n\t" \
  "s_setprio 2\n\t" \
  "v_cvt_pk_f16_f32 v226, |v166|, |v167| clamp\n\t" \
  "s_waitcnt lgkmcnt(4)\n\t" \
  "v_mfma_f32_16x16x32_bf16 v[176:179], v[208:211], v[96:99], 0\n\t" \
  "ds_read_b64_tr_b16 v[214:215], %[a1] offset:53288\n\t" \
  "v_cvt_pk_f16_f32 v228, |v172|, |v173| clamp\n\t" \
  "v_cvt_pk_f16_f32 v230, |v174|, |v175| clamp\n\t" \
  "v_rcp_f16_sdwa v234, v234 dst_sel:DWORD dst_unused:UNUSED_PAD src0_sel:WORD_1\n\t" \
  "v_mfma_f32_16x16x32_bf16 v[184:187], v[208:211], v[108:111], 0\n\t" \
  "ds_read_b32 v222, v222 offset:0\n\t" \
  "v_and_b32_sdwa v225, %[mk], v224 dst_sel:DWORD dst_unused:UNUSED_PAD src0_sel:DWORD src1_sel:WORD_1\n\t" \
  "v_and_b32 v224, %[mk], v224\n\t" \
  "v_mfma_f32_16x16x32_bf16 v[192:195], v[208:211], v[120:123], 0\n\t" \
  "ds_read_b32 v225, v225 offset:0\n\t" \
  "v_and_b32_sdwa v227, %[mk], v226 dst_sel:DWORD dst_unused:UNUSED_PAD src0_sel:DWORD src1_sel:WORD_1\n\t" \
  "v_mfma_f32_16x16x32_bf16 v[200:203], v[208:211], v[132:135], 0\n\t" \
  "ds_read_b64_tr_b16 v[208:209], %[a1] offset:51240\n\t" \
  "v_and_b32 v226, %[mk], v226\n\t" \
  "v_and_b32_sdwa v229, %[mk], v228 dst_sel:DWORD dst_unused:UNUSED_PAD src0_sel:DWORD src1_sel:WORD_1\n\t" \
  "s_waitcnt lgkmcnt(3)\n\t" \
  "v_mfma_f32_16x16x32_bf16 v[176:179], v[212:215], v[100:103], v[176:179]\n\t" \
  "ds_read_b64_tr_b16 v[210:211], %[a1] offset:54312\n\t" \
  "v_and_b32 v228, %[mk], v228\n\t" \
  "v_mfma_f32_16x16x32_bf16 v[184:187], v[212:215], v[112:115], v[184:187]\n\t" \
  "ds_read_b32 v224, v224 offset:0\n\t" \
  "v_and_b32_sdwa v231, %[mk], v230 dst_sel:DWORD dst_unused:UNUSED_PAD src0_sel:DWORD src1_sel:WORD_1\n\t" \
  "v_mfma_f32_16x16x32_bf16 v[192:195], v[212:215], v[124:127], v[192:195]\n\t" \
  "ds_read_b32 v227, v227 offset:0\n\t" \
  "v_and_b32 v230, %[mk], v230\n\t" \
  "v_fma_f32 v149, |v149|, v217, v149 mul:4\n\t" \
  "v_mfma_f32_16x16x32_bf16 v[200:203], v[212:215], v[136:139], v[200:203]\n\t" \
  "ds_read_b64_tr_b16 v[212:213], %[a1] offset:49704\n\t" \
  "ds_read_b64_tr_b16 v[214:215], %[a1] offset:50728\n\t" \
  "v_fma_f32 v148, |v148|, v216, v148 mul:4\n\t" \
  "v_fma_f32 v151, |v151|, v219, v151 mul:4\n\t" \
  "s_waitcnt lgkmcnt(4)\n\t" \
  "v_mfma_f32_16x16x32_bf16 v[176:179], v[208:211], v[104:107], v[176:179]\n\t" \
  "ds_read_b32 v226, v226 offset:0\n\t" \
  "v_fma_f32 v150, |v150|, v218, v150 mul:4\n\t" \
  "v_fma_f32 v157, |v157|, v221, v157 mul:4\n\t" \
  "v_mfma_f32_16x16x32_bf16 v[184:187], v[208:211], v[116:119], v[184:187]\n\t" \
  "ds_read_b32 v229, v229 offset:0\n\t" \
  "v_fma_f32 v156, |v156|, v220, v156 mul:4\n\t" \
  "v_fma_f32 v159, |v159|, v223, v159 mul:4\n\t" \
  "v_fma_f32 v158, |v158|, v222, v158 mul:4\n\t" \
  "v_mfma_f32_16x16x32_bf16 v[192:195], v[208:211], v[128:131], v[192:195]\n\t" \
  "ds_read_b32 v228, v228 offset:0\n\t" \
  "v_fma_mix_f32 v160, v160, v235, 0 op_sel_hi:[0,1,0]\n\t" \
  "v_fma_mix_f32 v161, v161, v234, 0 op_sel_hi:[0,1,0]\n\t" \
  "v_cvt_pk_bf16_f32 v162, v160, v161\n\t" \
  "v_mfma_f32_16x16x32_bf16 v[200:203], v[208:211], v[140:143], v[200:203]\n\t" \
  "ds_read_b64_tr_b16 v[208:209], %[a1] offset:52776\n\t" \
  "v_cvt_pk_bf16_f32 v148, v148, v149\n\t" \
  "v_cvt_pk_bf16_f32 v149, v150, v151\n\t" \
  "s_waitcnt lgkmcnt(4)\n\t" \
  "v_mfma_f32_16x16x32_bf16 v[180:183], v[212:215], v[96:99], 0\n\t" \
  "ds_read_b64_tr_b16 v[210:211], %[a1] offset:53800\n\t" \
  "v_cvt_pk_bf16_f32 v156, v156, v157\n\t" \
  "v_cvt_pk_bf16_f32 v157, v158, v159\n\t" \
  "s_setprio 3\n\t" \
  "v_cvt_pk_f16_f32 v216, |v178|, |v179| clamp\n\t" \
  "v_mfma_f32_16x16x32_bf16 v[188:191], v[212:215], v[108:111], 0\n\t" \
  "ds_read_b32 v231, v231 offset:0\n\t" \
  "v_cvt_pk_f16_f32 v232, v176, v177\n\t" \
  "v_cvt_pk_f16_f32 v218, |v186|, |v187| clamp\n\t" \
  "v_cvt_pk_f16_f32 v220, |v184|, |v185| clamp\n\t" \
  "v_mfma_f32_16x16x32_bf16 v[196:199], v[212:215], v[120:123], 0\n\t" \
  "ds_read_b32 v230, v230 offset:0\n\t" \
  "v_pk_mul_f16 v233, v232, v232\n\t" \
  "v_and_b32_sdwa v217, %[mk], v216 dst_sel:DWORD dst_unused:UNUSED_PAD src0_sel:DWORD src1_sel:WORD_1\n\t" \
  "v_mfma_f32_16x16x32_bf16 v[204:207], v[212:215], v[132:135], 0\n\t" \
  "ds_read_b64_tr_b16 v[212:213], %[a1] offset:51752\n\t" \
  "v_and_b32 v216, %[mk], v216\n\t" \
  "v_and_b32_sdwa v219, %[mk], v218 dst_sel:DWORD dst_unused:UNUSED_PAD src0_sel:DWORD src1_sel:WORD_1\n\t" \
  "s_waitcnt lgkmcnt(3)\n\t" \
  "v_mfma_f32_16x16x32_bf16 v[180:183], v[208:211], v[100:103], v[180:183]\n\t" \
  "ds_read_b64_tr_b16 v[214:215], %[a1] offset:54824\n\t" \
  "v_and_b32 v218, %[mk], v218\n\t" \
  "v_mfma_f32_16x16x32_bf16 v[188:191], v[208:211], v[112:115], v[188:191]\n\t" \
  "s_waitcnt vmcnt(1)\n\t" \
  "s_cmp_lg_u32 %[rnd], 1\n\t" \
  "s_cbranch_scc1 .Lcnx_c12\n\t" \
  "s_waitcnt vmcnt(0)\n\t" \
  ".Lcnx_c12:\n\t" \
  "s_mov_b64 exec, 3\n\t" \
  "ds_add_rtn_u32 v246, %[l8], %[vo] offset:47696\n\t" \
  "s_mov_b64 exec, -1\n\t" \
  "ds_read_b32 v217, v217 offset:0\n\t" \
  "v_and_b32_sdwa v221, %[mk], v220 dst_sel:DWORD dst_unused:UNUSED_PAD src0_sel:DWORD src1_sel:WORD_1\n\t" \
  "v_mfma_f32_16x16x32_bf16 v[196:199], v[208:211], v[124:127], v[196:199]\n\t" \
  "ds_read_b32 v216, v216 offset:0\n\t" \
  "v_and_b32 v220, %[mk], v220\n\t" \
  "v_pk_fma_f16 v233, v233, %[h2], %[h1]\n\t" \
  "v_mfma_f32_16x16x32_bf16 v[204:207], v[208:211], v[136:139], v[204:207]\n\t" \
  "s_waitcnt lgkmcnt(2)\n\t" \
  "v_readfirstlane_b32 %[t0], v246\n\t" \
  "s_cmp_lt_u32 %[t0], %[tg]\n\t" \
  "s_cbranch_scc1 .Lcnx_w60\n.Lcnx_b60:\n\t" \
  "ds_read_b64_tr_b16 v[208:209], %[a2] offset:24600\n\t" \
  "v_pk_mul_f16 v233, v232, v233\n\t" \
  "v_fma_f32 v165, |v165|, v225, v165 mul:4\n\t" \
  "v_mfma_f32_16x16x32_bf16 v[180:183], v[212:215], v[104:107], v[180:183]\n\t" \
  "ds_read_b64_tr_b16 v[210:211], %[a2] offset:26136\n\t" \
  "v_fma_f32 v164, |v164|, v224, v164 mul:4\n\t" \
  "v_fma_f32 v167, |v167|, v227, v167 mul:4\n\t" \
  "v_mfma_f32_16x16x32_bf16 v[188:191], v[212:215], v[116:119], v[188:191]\n\t" \
  "s_cmp_eq_u32 %[grp], 0\n\t" \
  "s_cbranch_scc1 .Lcnx_f6\n.Lcnx_g6:\n\t" \
  "ds_read_b32 v219, v219 offset:0\n\t" \
  "v_fma_f32 v166, |v166|, v226, v166 mul:4\n\t" \
  "v_fma_f32 v173, |v173|, v229, v173 mul:4\n\t" \
  "v_fma_f32 v172, |v172|, v228, v172 mul:4\n\t" \
  "v_mfma_f32_16x16x32_bf16 v[196:199], v[212:215], v[128:131], v[196:199]\n\t" \
  "ds_read_b32 v218, v218 offset:0\n\t" \
  "v_fma_f32 v175, |v175|, v231, v175 mul:4\n\t" \
  "v_fma_f32 v174, |v174|, v230, v174 mul:4\n\t" \
  "v_cvt_pk_bf16_f32 v164, v164, v165\n\t" \
  "v_mfma_f32_16x16x32_bf16 v[204:207], v[212:215], v[140:143], v[204:207]\n\t" \
  "ds_read_b64_tr_b16 v[212:213], %[a2] offset:24608\n\t" \
  "v_cvt_pk_bf16_f32 v165, v166, v167\n\t" \
  "v_cvt_pk_bf16_f32 v172, v172, v173\n\t" \
  "v_cvt_pk_bf16_f32 v173, v174, v175\n\t" \
  "s_setprio 0\n\t" \
  "v_exp_f16 v232, v233\n\t" \
  "v_cvt_pk_f16_f32 v224, |v194|, |v195| clamp\n\t" \
  "v_mfma_f32_16x16x32_bf16 v[32:35], v[242:245], v[236:239], v[32:35]\n\t" \
  "ds_read_b64_tr_b16 v[214:215], %[a2] offset:26144\n\t" \
  "v_cvt_pk_f16_f32 v234, v192, v193\n\t" \
  "v_cvt_pk_f16_f32 v226, |v202|, |v203| clamp\n\t" \
  "v_mfma_f32_16x16x32_bf16 v[36:39], v[240:243], v[236:239], v[36:39]\n\t" \
  "buffer_load_dwordx4 v[236:239], %[vo], %[xrs], %[oc0] offen offset:320\n\t" \
  "s_cmp_lg_u32 %[rnd], 0\n\t" \
  "s_cbranch_scc1 .Lcnx_c25\n\t" \
  "s_add_u32 m0, %[xl], 32800\n\t" \
  "s_add_u32 %[xo], %[xo], %[rs]\n\t" \
  "buffer_load_dwordx4 %[xv], %[xrs], %[xo] offen lds\n\t" \
  "s_cmp_lg_u32 %[wq], 6\n\t" \
  "s_cbranch_scc1 .Lcnx_c25\n\t" \
  "s_mov_b32 m0, %[pf]\n\t" \
  "s_add_u32 %[wb], %[wb], %[rs4]\n\t" \
  "buffer_load_dword %[wv], %[xrs], %[wb] offen lds\n\t" \
  ".Lcnx_c25:\n\t" \
  "ds_read_b32 v221, v221 offset:0\n\t" \
  "v_cvt_pk_f16_f32 v228, |v200|, |v201| clamp\n\t" \
  "v_exp_f16_sdwa v232, v233 dst_sel:WORD_1 dst_unused:UNUSED_PRESERVE src0_sel:WORD_1\n\t" \
  "v_pk_mul_f16 v235, v234, v234\n\t" \
  "s_waitcnt lgkmcnt(5)\n\t" \
  "v_mfma_f32_16x16x32_bf16 v[0:3], v[208:211], v[146:149], v[0:3]\n\t" \
  "ds_read_b32 v220, v220 offset:0\n\t" \
  "v_and_b32_sdwa v225, %[mk], v224 dst_sel:DWORD dst_unused:UNUSED_PAD src0_sel:DWORD src1_sel:WORD_1\n\t" \
  "v_and_b32 v224, %[mk], v224\n\t" \
  "v_mfma_f32_16x16x32_bf16 v[24:27], v[208:211], v[154:157], v[24:27]\n\t" \
  "ds_read_b32 v225, v225 offset:0\n\t" \
  "v_and_b32_sdwa v227, %[mk], v226 dst_sel:DWORD dst_unused:UNUSED_PAD src0_sel:DWORD src1_sel:WORD_1\n\t" \
  "v_mfma_f32_16x16x32_bf16 v[48:51], v[208:211], v[162:165], v[48:51]\n\t" \
  "ds_read_b32 v224, v224 offset:0\n\t" \
  "v_and_b32 v226, %[mk], v226\n\t" \
  "v_and_b32_sdwa v229, %[mk], v228 dst_sel:DWORD dst_unused:UNUSED_PAD src0_sel:DWORD src1_sel:WORD_1\n\t" \
  "v_mfma_f32_16x16x32_bf16 v[72:75], v[208:211], v[170:173], v[72:75]\n\t" \
  "ds_read_b64_tr_b16 v[208:209], %[a2] offset:24664\n\t" \
  "v_and_b32 v228, %[mk], v228\n\t" \
  "s_waitcnt lgkmcnt(5)\n\t" \
  "v_mfma_f32_16x16x32_bf16 v[4:7], v[212:215], v[146:149], v[4:7]\n\t" \
  "ds_read_b64_tr_b16 v[210:211], %[a2] offset:26200\n\t" \
  "v_pk_fma_f16 v235, v235, %[h2], %[h1]\n\t" \
  "v_mfma_f32_16x16x32_bf16 v[28:31], v[212:215], v[154:157], v[28:31]\n\t" \
  "ds_read_b32 v227, v227 offset:0\n\t" \
  "v_pk_mul_f16 v235, v234, v235\n\t" \
  "v_fma_f32 v179, |v179|, v217, v179 mul:4\n\t" \
  "v_fma_f32 v178, |v178|, v216, v178 mul:4\n\t" \
  "v_mfma_f32_16x16x32_bf16 v[52:55], v[212:215], v[162:165], v[52:55]\n\t" \
  "ds_read_b32 v226, v226 offset:0\n\t" \
  "v_fma_f32 v187, |v187|, v219, v187 mul:4\n\t" \
  "s_waitcnt lgkmcnt(6)\n\t" \
  "v_fma_f32 v186, |v186|, v218, v186 mul:4\n\t" \
  "v_fma_f32 v185, |v185|, v221, v185 mul:4\n\t" \
  "v_mfma_f32_16x16x32_bf16 v[76:79], v[212:215], v[170:173], v[76:79]\n\t" \
  "ds_read_b64_tr_b16 v[212:213], %[a2] offset:24672\n\t" \
  "v_fma_f32 v184, |v184|, v220, v184 mul:4\n\t" \
  "v_pk_fma_f16 v232, v232, %[hh], %[hh]\n\t" \
  "s_waitcnt lgkmcnt(3)\n\t" \
  "v_mfma_f32_16x16x32_bf16 v[8:11], v[208:211], v[146:149], v[8:11]\n\t" \
  "ds_read_b64_tr_b16 v[214:215], %[a2] offset:26208\n\t" \
  "v_cvt_pk_bf16_f32 v179, v178, v179\n\t" \
  "v_cvt_pk_bf16_f32 v187, v186, v187\n\t" \
  "v_cvt_pk_bf16_f32 v186, v184, v185\n\t" \
  "v_mfma_f32_16x16x32_bf16 v[32:35], v[208:211], v[154:157], v[32:35]\n\t" \
  "ds_read_b32 v229, v229 offset:0\n\t" \
  "v_rcp_f16 v233, v232\n\t" \
  "v_exp_f16 v234, v235\n\t" \
  "v_cvt_pk_f16_f32 v216, |v180|, |v181| clamp\n\t" \
  "v_mfma_f32_16x16x32_bf16 v[56:59], v[208:211], v[162:165], v[56:59]\n\t" \
  "ds_read_b32 v228, v228 offset:0\n\t" \
  "s_setprio 1\n\t" \
  "v_cvt_pk_f16_f32 v218, |v182|, |v183| clamp\n\t" \
  "v_cvt_pk_f16_f32 v220, |v188|, |v189| clamp\n\t" \
  "v_mfma_f32_16x16x32_bf16 v[80:83], v[208:211], v[170:173], v[80:83]\n\t" \
  "ds_read_b64_tr_b16 v[208:209], %[a2] offset:24728\n\t" \
  "v_cvt_pk_f16_f32 v222, |v190|, |v191| clamp\n\t" \
  "v_rcp_f16_sdwa v232, v232 dst_sel:DWORD dst_unused:UNUSED_PAD src0_sel:WORD_1\n\t" \
  "v_exp_f16_sdwa v234, v235 dst_sel:WORD_1 dst_unused:UNUSED_PRESERVE src0_sel:WORD_1\n\t" \
  "s_waitcnt lgkmcnt(3)\n\t" \
  "v_mfma_f32_16x16x32_bf16 v[12:15], v[212:215], v[146:149], v[12:15]\n\t" \
  "ds_read_b64_tr_b16 v[210:211], %[a2] offset:26264\n\t" \
  "v_and_b32_sdwa v217, %[mk], v216 dst_sel:DWORD dst_unused:UNUSED_PAD src0_sel:DWORD src1_sel:WORD_1\n\t" \
  "v_and_b32 v216, %[mk], v216\n\t" \
  "v_mfma_f32_16x16x32_bf16 v[36:39], v[212:215], v[154:157], v[36:39]\n\t" \
  "ds_read_b32 v217, v217 offset:0\n\t" \
  "v_and_b32_sdwa v219, %[mk], v218 dst_sel:DWORD dst_unused:UNUSED_PAD src0_sel:DWORD src1_sel:WORD_1\n\t" \
  "v_mfma_f32_16x16x32_bf16 v[60:63], v[212:215], v[162:165], v[60:63]\n\t" \
  "ds_read_b32 v216, v216 offset:0\n\t" \
  "v_and_b32 v218, %[mk], v218\n\t" \
  "v_and_b32_sdwa v221, %[mk], v220 dst_sel:DWORD dst_unused:UNUSED_PAD src0_sel:DWORD src1_sel:WORD_1\n\t" \
  "v_mfma_f32_16x16x32_bf16 v[84:87], v[212:215], v[170:173], v[84:87]\n\t" \
  "ds_read_b64_tr_b16 v[212:213], %[a2] offset:24736\n\t" \
  "v_and_b32 v220, %[mk], v220\n\t" \
  "s_waitcnt lgkmcnt(3)\n\t" \
  "v_mfma_f32_16x16x32_bf16 v[16:19], v[208:211], v[146:149], v[16:19]\n\t" \
  "ds_read_b64_tr_b16 v[214:215], %[a2] offset:26272\n\t" \
  "v_and_b32_sdwa v223, %[mk], v222 dst_sel:DWORD dst_unused:UNUSED_PAD src0_sel:DWORD src1_sel:WORD_1\n\t" \
  "v_mfma_f32_16x16x32_bf16 v[40:43], v[208:211], v[154:157], v[40:43]\n\t" \
  "ds_read_b32 v219, v219 offset:0\n\t" \
  "v_and_b32 v222, %[mk], v222\n\t" \
  "v_fma_f32 v195, |v195|, v225, v195 mul:4\n\t" \
  "v_mfma_f32_16x16x32_bf16 v[64:67], v[208:211], v[162:165], v[64:67]\n\t" \
  "ds_read_b32 v218, v218 offset:0\n\t" \
  "v_fma_f32 v194, |v194|, v224, v194 mul:4\n\t" \
  "v_fma_f32 v203, |v203|, v227, v203 mul:4\n\t" \
  "v_mfma_f32_16x16x32_bf16 v[88:91], v[208:211], v[170:173], v[88:91]\n\t" \
  "ds_read_b64_tr_b16 v[208:209], %[a1] offset:36904\n\t" \
  "ds_read_b64_tr_b16 v[210:211], %[a1] offset:37928\n\t" \
  "v_fma_f32 v202, |v202|, v226, v202 mul:4\n\t" \
  "v_fma_f32 v201, |v201|, v229, v201 mul:4\n\t" \
  "s_waitcnt lgkmcnt(4)\n\t" \
  "v_mfma_f32_16x16x32_bf16 v[20:23], v[212:215], v[146:149], v[20:23]\n\t" \
  "ds_read_b32 v221, v221 offset:0\n\t" \
  "v_fma_f32 v200, |v200|, v228, v200 mul:4\n\t" \
  "v_fma_mix_f32 v176, v176, v233, 0 op_sel_hi:[0,1,0]\n\t" \
  "v_fma_mix_f32 v177, v177, v232, 0 op_sel_hi:[0,1,0]\n\t" \
  "v_mfma_f32_16x16x32_bf16 v[44:47], v[212:215], v[154:157], v[44:47]\n\t" \
  "ds_read_b32 v220, v220 offset:0\n\t" \
  "v_pk_fma_f16 v234, v234, %[hh], %[hh]\n\t" \
  "v_cvt_pk_bf16_f32 v178, v176, v177\n\t" \
  "v_cvt_pk_bf16_f32 v195, v194, v195\n\t" \
  "v_mfma_f32_16x16x32_bf16 v[68:71], v[212:215], v[162:165], v[68:71]\n\t" \
  "ds_read_b32 v223, v223 offset:0\n\t" \
  "v_cvt_pk_bf16_f32 v203, v202, v203\n\t" \
  "v_cvt_pk_bf16_f32 v202, v200, v201\n\t" \
  "v_rcp_f16 v235, v234\n\t" \
  "v_mfma_f32_16x16x32_bf16 v[92:95], v[212:215], v[170:173], v[92:95]\n\t" \
  "ds_read_b64_tr_b16 v[212:213], %[a1] offset:39976\n\t" \
  "v_cvt_pk_f16_f32 v224, |v196|, |v197| clamp\n\t" \
  "s_setprio 2\n\t" \
  "v_cvt_pk_f16_f32 v226, |v198|, |v199| clamp\n\t" \
  "s_waitcnt lgkmcnt(4)\n\t" \
  "v_mfma_f32_16x16x32_bf16 v[144:147], v[208:211], v[96:99], 0\n\t" \
  "ds_read_b64_tr_b16 v[214:215], %[a1] offset:41000\n\t" \
  "v_cvt_pk_f16_f32 v228, |v204|, |v205| clamp\n\t" \
  "v_cvt_pk_f16_f32 v230, |v206|, |v207| clamp\n\t" \
  "v_rcp_f16_sdwa v234, v234 dst_sel:DWORD dst_unused:UNUSED_PAD src0_sel:WORD_1\n\t" \
  "v_mfma_f32_16x16x32_bf16 v[152:155], v[208:211], v[108:111], 0\n\t" \
  "ds_read_b32 v222, v222 offset:0\n\t" \
  "v_and_b32_sdwa v225, %[mk], v224 dst_sel:DWORD dst_unused:UNUSED_PAD src0_sel:DWORD src1_sel:WORD_1\n\t" \
  "v_and_b32 v224, %[mk], v224\n\t" \
  "v_mfma_f32_16x16x32_bf16 v[160:163], v[208:211], v[120:123], 0\n\t" \
  "ds_read_b32 v225, v225 offset:0\n\t" \
  "v_and_b32_sdwa v227, %[mk], v226 dst_sel:DWORD dst_unused:UNUSED_PAD src0_sel:DWORD src1_sel:WORD_1\n\t" \
  "v_mfma_f32_16x16x32_bf16 v[168:171], v[208:211], v[132:135], 0\n\t" \
  "ds_read_b64_tr_b16 v[208:209], %[a1] offset:38952\n\t" \
  "v_and_b32 v226, %[mk], v226\n\t" \
  "v_and_b32_sdwa v229, %[mk], v228 dst_sel:DWORD dst_unused:UNUSED_PAD src0_sel:DWORD src1_sel:WORD_1\n\t" \
  "s_waitcnt lgkmcnt(3)\n\t" \
  "v_mfma_f32_16x16x32_bf16 v[144:147], v[212:215], v[100:103], v[144:147]\n\t" \
  "ds_read_b64_tr_b16 v[210:211], %[a1] offset:42024\n\t" \
  "v_and_b32 v228, %[mk], v228\n\t" \
  "v_mfma_f32_16x16x32_bf16 v[152:155], v[212:215], v[112:115], v[152:155]\n\t" \
  "ds_read_b32 v224, v224 offset:0\n\t" \
  "v_and_b32_sdwa v231, %[mk], v230 dst_sel:DWORD dst_unused:UNUSED_PAD src0_sel:DWORD src1_sel:WORD_1\n\t" \
  "v_mfma_f32_16x16x32_bf16 v[160:163], v[212:215], v[124:127], v[160:163]\n\t" \
  "ds_read_b32 v227, v227 offset:0\n\t" \
  "v_and_b32 v230, %[mk], v230\n\t" \
  "v_fma_f32 v181, |v181|, v217, v181 mul:4\n\t" \
  "v_mfma_f32_16x16x32_bf16 v[168:171], v[212:215], v[136:139], v[168:171]\n\t" \
  "ds_read_b64_tr_b16 v[212:213], %[a1] offset:37416\n\t" \
  "ds_read_b64_tr_b16 v[214:215], %[a1] offset:38440\n\t" \
  "v_fma_f32 v180, |v180|, v216, v180 mul:4\n\t" \
  "v_fma_f32 v183, |v183|, v219, v183 mul:4\n\t" \
  "s_waitcnt lgkmcnt(4)\n\t" \
  "v_mfma_f32_16x16x32_bf16 v[144:147], v[208:211], v[104:107], v[144:147]\n\t" \
  "ds_read_b32 v226, v226 offset:0\n\t" \
  "v_fma_f32 v182, |v182|, v218, v182 mul:4\n\t" \
  "v_fma_f32 v189, |v189|, v221, v189 mul:4\n\t" \
  "v_mfma_f32_16x16x32_bf16 v[152:155], v[208:211], v[116:119], v[152:155]\n\t" \
  "ds_read_b32 v229, v229 offset:0\n\t" \
  "v_fma_f32 v188, |v188|, v220, v188 mul:4\n\t" \
  "v_fma_f32 v191, |v191|, v223, v191 mul:4\n\t" \
  "v_fma_f32 v190, |v190|, v222, v190 mul:4\n\t" \
  "v_mfma_f32_16x16x32_bf16 v[160:163], v[208:211], v[128:131], v[160:163]\n\t" \
  "ds_read_b32 v228, v228 offset:0\n\t" \
  "v_fma_mix_f32 v192, v192, v235, 0 op_sel_hi:[0,1,0]\n\t" \
  "v_fma_mix_f32 v193, v193, v234, 0 op_sel_hi:[0,1,0]\n\t" \
  "v_cvt_pk_bf16_f32 v194, v192, v193\n\t" \
  "v_mfma_f32_16x16x32_bf16 v[168:171], v[208:211], v[140:143], v[168:171]\n\t" \
  "ds_read_b64_tr_b16 v[208:209], %[a1] offset:40488\n\t" \
  "v_cvt_pk_bf16_f32 v180, v180, v181\n\t" \
  "v_cvt_pk_bf16_f32 v181, v182, v183\n\t" \
  "s_waitcnt lgkmcnt(4)\n\t" \
  "v_mfma_f32_16x16x32_bf16 v[148:151], v[212:215], v[96:99], 0\n\t" \
  "ds_read_b64_tr_b16 v[210:211], %[a1] offset:41512\n\t" \
  "v_cvt_pk_bf16_f32 v188, v188, v189\n\t" \
  "v_cvt_pk_bf16_f32 v189, v190, v191\n\t" \
  "s_setprio 3\n\t" \
  "v_cvt_pk_f16_f32 v216, |v146|, |v147| clamp\n\t" \
  "v_mfma_f32_16x16x32_bf16 v[156:159], v[212:215], v[108:111], 0\n\t" \
  "ds_read_b32 v231, v231 offset:0\n\t" \
  "v_cvt_pk_f16_f32 v232, v144, v145\n\t" \
  "v_cvt_pk_f16_f32 v218, |v154|, |v155| clamp\n\t" \
  "v_cvt_pk_f16_f32 v220, |v152|, |v153| clamp\n\t" \
  "v_mfma_f32_16x16x32_bf16 v[164:167], v[212:215], v[120:123], 0\n\t" \
  "ds_read_b32 v230, v230 offset:0\n\t" \
  "v_pk_mul_f16 v233, v232, v232\n\t" \
  "v_and_b32_sdwa v217, %[mk], v216 dst_sel:DWORD dst_unused:UNUSED_PAD src0_sel:DWORD src1_sel:WORD_1\n\t" \
  "v_mfma_f32_16x16x32_bf16 v[172:175], v[212:215], v[132:135], 0\n\t" \
  "ds_read_b64_tr_b16 v[212:213], %[a1] offset:39464\n\t" \
  "v_and_b32 v216, %[mk], v216\n\t" \
  "v_and_b32_sdwa v219, %[mk], v218 dst_sel:DWORD dst_unused:UNUSED_PAD src0_sel:DWORD src1_sel:WORD_1\n\t" \
  "s_waitcnt lgkmcnt(3)\n\t" \
  "v_mfma_f32_16x16x32_bf16 v[148:151], v[208:211], v[100:103], v[148:151]\n\t" \
  "ds_read_b64_tr_b16 v[214:215], %[a1] offset:42536\n\t" \
  "v_and_b32 v218, %[mk], v218\n\t" \
  "v_mfma_f32_16x16x32_bf16 v[156:159], v[208:211], v[112:115], v[156:159]\n\t" \
  "s_waitcnt vmcnt(1)\n\t" \
  "s_cmp_lg_u32 %[rnd], 1\n\t" \
  "s_cbranch_scc1 .Lcnx_c13\n\t" \
  "s_waitcnt vmcnt(0)\n\t" \
  ".Lcnx_c13:\n\t" \
  "s_mov_b64 exec, 3\n\t" \
  "ds_add_rtn_u32 v246, %[l8], %[vo] offset:47704\n\t" \
  "s_mov_b64 exec, -1\n\t" \
  "ds_read_b32 v217, v217 offset:0\n\t" \
  "v_and_b32_sdwa v221, %[mk], v220 dst_sel:DWORD dst_unused:UNUSED_PAD src0_sel:DWORD src1_sel:WORD_1\n\t" \
  "v_mfma_f32_16x16x32_bf16 v[164:167], v[208:211], v[124:127], v[164:167]\n\t" \
  "ds_read_b32 v216, v216 offset:0\n\t" \
  "v_and_b32 v220, %[mk], v220\n\t" \
  "v_pk_fma_f16 v233, v233, %[h2], %[h1]\n\t" \
  "v_mfma_f32_16x16x32_bf16 v[172:175], v[208:211], v[136:139], v[172:175]\n\t" \
  "s_waitcnt lgkmcnt(2)\n\t" \
  "v_readfirstlane_b32 %[t0], v246\n\t" \
  "s_cmp_lt_u32 %[t0], %[tg]\n\t" \
  "s_cbranch_scc1 .Lcnx_w72\n.Lcnx_b72:\n\t" \
  "ds_read_b64_tr_b16 v[208:209], %[a2] offset:30752\n\t" \
  "v_pk_mul_f16 v233, v232, v233\n\t" \
  "v_fma_f32 v197, |v197|, v225, v197 mul:4\n\t" \
  "v_mfma_f32_16x16x32_bf16 v[148:151], v[212:215], v[104:107], v[148:151]\n\t" \
  "ds_read_b64_tr_b16 v[210:211], %[a2] offset:32288\n\t" \
  "v_fma_f32 v196, |v196|, v224, v196 mul:4\n\t" \
  "v_fma_f32 v199, |v199|, v227, v199 mul:4\n\t" \
  "v_mfma_f32_16x16x32_bf16 v[156:159], v[212:215], v[116:119], v[156:159]\n\t" \
  "s_cmp_eq_u32 %[grp], 0\n\t" \
  "s_cbranch_scc1 .Lcnx_f7\n.Lcnx_g7:\n\t" \
  "ds_read_b32 v219, v219 offset:0\n\t" \
  "v_fma_f32 v198, |v198|, v226, v198 mul:4\n\t" \
  "v_fma_f32 v205, |v205|, v229, v205 mul:4\n\t" \
  "v_fma_f32 v204, |v204|, v228, v204 mul:4\n\t" \
  "v_mfma_f32_16x16x32_bf16 v[164:167], v[212:215], v[128:131], v[164:167]\n\t" \
  "ds_read_b32 v218, v218 offset:0\n\t" \
  "v_fma_f32 v207, |v207|, v231, v207 mul:4\n\t" \
  "v_fma_f32 v206, |v206|, v230, v206 mul:4\n\t" \
  "v_cvt_pk_bf16_f32 v196, v196, v197\n\t" \
  "v_mfma_f32_16x16x32_bf16 v[172:175], v[212:215], v[140:143], v[172:175]\n\t" \
  "ds_read_b64_tr_b16 v[212:213], %[a2] offset:30760\n\t" \
  "v_cvt_pk_bf16_f32 v197, v198, v199\n\t" \
  "v_cvt_pk_bf16_f32 v204, v204, v205\n\t" \
  "v_cvt_pk_bf16_f32 v205, v206, v207\n\t" \
  "s_setprio 0\n\t" \
  "v_exp_f16 v232, v233\n\t" \
  "v_cvt_pk_f16_f32 v224, |v162|, |v163| clamp\n\t" \
  "v_mfma_f32_16x16x32_bf16 v[40:43], v[242:245], v[236:239], v[40:43]\n\t" \
  "ds_read_b64_tr_b16 v[214:215], %[a2] offset:32296\n\t" \
  "v_cvt_pk_f16_f32 v234, v160, v161\n\t" \
  "v_cvt_pk_f16_f32 v226, |v170|, |v171| clamp\n\t" \
  "v_mfma_f32_16x16x32_bf16 v[44:47], v[240:243], v[236:239], v[44:47]\n\t" \
  "buffer_load_dwordx4 v[236:239], %[vo], %[xrs], %[oc1] offen offset:0\n\t" \
  "s_cmp_lg_u32 %[rnd], 0\n\t" \
  "s_cbranch_scc1 .Lcnx_c26\n\t" \
  "s_add_u32 m0, %[xl], 41000\n\t" \
  "s_add_u32 %[xo], %[xo], %[rs]\n\t" \
  "buffer_load_dwordx4 %[xv], %[xrs], %[xo] offen lds\n\t" \
  "s_cmp_lg_u32 %[wq], 7\n\t" \
  "s_cbranch_scc1 .Lcnx_c26\n\t" \
  "s_mov_b32 m0, %[pf]\n\t" \
  "s_add_u32 %[wb], %[wb], %[rs4]\n\t" \
  "buffer_load_dword %[wv], %[xrs], %[wb] offen lds\n\t" \
  ".Lcnx_c26:\n\t" \
  "ds_read_b32 v221, v221 offset:0\n\t" \
  "v_cvt_pk_f16_f32 v228, |v168|, |v169| clamp\n\t" \
  "v_exp_f16_sdwa v232, v233 dst_sel:WORD_1 dst_unused:UNUSED_PRESERVE src0_sel:WORD_1\n\t" \
  "v_pk_mul_f16 v235, v234, v234\n\t" \
  "s_waitcnt lgkmcnt(5)\n\t" \
  "v_mfma_f32_16x16x32_bf16 v[0:3], v[208:211], v[178:181], v[0:3]\n\t" \
  "ds_read_b32 v220, v220 offset:0\n\t" \
  "v_and_b32_sdwa v225, %[mk], v224 dst_sel:DWORD dst_unused:UNUSED_PAD src0_sel:DWORD src1_sel:WORD_1\n\t" \
  "v_and_b32 v224, %[mk], v224\n\t" \
  "v_mfma_f32_16x16x32_bf16 v[24:27], v[208:211], v[186:189], v[24:27]\n\t" \
  "ds_read_b32 v225, v225 offset:0\n\t" \
  "v_and_b32_sdwa v227, %[mk], v226 dst_sel:DWORD dst_unused:UNUSED_PAD src0_sel:DWORD src1_sel:WORD_1\n\t" \
  "v_mfma_f32_16x16x32_bf16 v[48:51], v[208:211], v[194:197], v[48:51]\n\t" \
  "ds_read_b32 v224, v224 offset:0\n\t" \
  "v_and_b32 v226, %[mk], v226\n\t" \
  "v_and_b32_sdwa v229, %[mk], v228 dst_sel:DWORD dst_unused:UNUSED_PAD src0_sel:DWORD src1_sel:WORD_1\n\t" \
  "v_mfma_f32_16x16x32_bf16 v[72:75], v[208:211], v[202:205], v[72:75]\n\t" \
  "ds_read_b64_tr_b16 v[208:209], %[a2] offset:30816\n\t" \
  "v_and_b32 v228, %[mk], v228\n\t" \
  "s_waitcnt lgkmcnt(5)\n\t" \
  "v_mfma_f32_16x16x32_bf16 v[4:7], v[212:215], v[178:181], v[4:7]\n\t" \
  "ds_read_b64_tr_b16 v[210:211], %[a2] offset:32352\n\t" \
  "v_pk_fma_f16 v235, v235, %[h2], %[h1]\n\t" \
  "v_mfma_f32_16x16x32_bf16 v[28:31], v[212:215], v[186:189], v[28:31]\n\t" \
  "ds_read_b32 v227, v227 offset:0\n\t" \
  "v_pk_mul_f16 v235, v234, v235\n\t" \
  "v_fma_f32 v147, |v147|, v217, v147 mul:4\n\t" \
  "v_fma_f32 v146, |v146|, v216, v146 mul:4\n\t" \
  "v_mfma_f32_16x16x32_bf16 v[52:55], v[212:215], v[194:197], v[52:55]\n\t" \
  "ds_read_b32 v226, v226 offset:0\n\t" \
  "v_fma_f32 v155, |v155|, v219, v155 mul:4\n\t" \
  "s_waitcnt lgkmcnt(6)\n\t" \
  "v_fma_f32 v154, |v154|, v218, v154 mul:4\n\t" \
  "v_fma_f32 v153, |v153|, v221, v153 mul:4\n\t" \
  "v_mfma_f32_16x16x32_bf16 v[76:79], v[212:215], v[202:205], v[76:79]\n\t" \
  "ds_read_b64_tr_b16 v[212:213], %[a2] offset:30824\n\t" \
  "v_fma_f32 v152, |v152|, v220, v152 mul:4\n\t" \
  "v_pk_fma_f16 v232, v232, %[hh], %[hh]\n\t" \
  "s_waitcnt lgkmcnt(3)\n\t" \
  "v_mfma_f32_16x16x32_bf16 v[8:11], v[208:211], v[178:181], v[8:11]\n\t" \
  "ds_read_b64_tr_b16 v[214:215], %[a2] offset:32360\n\t" \
  "v_cvt_pk_bf16_f32 v147, v146, v147\n\t" \
  "v_cvt_pk_bf16_f32 v155, v154, v155\n\t" \
  "v_cvt_pk_bf16_f32 v154, v152, v153\n\t" \
  "v_mfma_f32_16x16x32_bf16 v[32:35], v[208:211], v[186:189], v[32:35]\n\t" \
  "ds_read_b32 v229, v229 offset:0\n\t" \
  "v_rcp_f16 v233, v232\n\t" \
  "v_exp_f16 v234, v235\n\t" \
  "v_cvt_pk_f16_f32 v216, |v148|, |v149| clamp\n\t" \
  "v_mfma_f32_16x16x32_bf16 v[56:59], v[208:211], v[194:197], v[56:59]\n\t" \
  "ds_read_b32 v228, v228 offset:0\n\t" \
  "s_setprio 1\n\t" \
  "v_cvt_pk_f16_f32 v218, |v150|, |v151| clamp\n\t" \
  "v_cvt_pk_f16_f32 v220, |v156|, |v157| clamp\n\t" \
  "v_mfma_f32_16x16x32_bf16 v[80:83], v[208:211], v[202:205], v[80:83]\n\t" \
  "ds_read_b64_tr_b16 v[208:209], %[a2] offset:30880\n\t" \
  "v_cvt_pk_f16_f32 v222, |v158|, |v159| clamp\n\t" \
  "v_rcp_f16_sdwa v232, v232 dst_sel:DWORD dst_unused:UNUSED_PAD src0_sel:WORD_1\n\t" \
  "v_exp_f16_sdwa v234, v235 dst_sel:WORD_1 dst_unused:UNUSED_PRESERVE src0_sel:WORD_1\n\t" \
  "s_waitcnt lgkmcnt(3)\n\t" \
  "v_mfma_f32_16x16x32_bf16 v[12:15], v[212:215], v[178:181], v[12:15]\n\t" \
  "ds_read_b64_tr_b16 v[210:211], %[a2] offset:32416\n\t" \
  "v_and_b32_sdwa v217, %[mk], v216 dst_sel:DWORD dst_unused:UNUSED_PAD src0_sel:DWORD src1_sel:WORD_1\n\t" \
  "v_and_b32 v216, %[mk], v216\n\t" \
  "v_mfma_f32_16x16x32_bf16 v[36:39], v[212:215], v[186:189], v[36:39]\n\t" \
  "ds_read_b32 v217, v217 offset:0\n\t" \
  "v_and_b32_sdwa v219, %[mk], v218 dst_sel:DWORD dst_unused:UNUSED_PAD src0_sel:DWORD src1_sel:WORD_1\n\t" \
  "v_mfma_f32_16x16x32_bf16 v[60:63], v[212:215], v[194:197], v[60:63]\n\t" \
  "ds_read_b32 v216, v216 offset:0\n\t" \
  "v_and_b32 v218, %[mk], v218\n\t" \
  "v_and_b32_sdwa v221, %[mk], v220 dst_sel:DWORD dst_unused:UNUSED_PAD src0_sel:DWORD src1_sel:WORD_1\n\t" \
  "v_mfma_f32_16x16x32_bf16 v[84:87], v[212:215], v[202:205], v[84:87]\n\t" \
  "ds_read_b64_tr_b16 v[212:213], %[a2] offset:30888\n\t" \
  "v_and_b32 v220, %[mk], v220\n\t" \
  "s_waitcnt lgkmcnt(3)\n\t" \
  "v_mfma_f32_16x16x32_bf16 v[16:19], v[208:211], v[178:181], v[16:19]\n\t" \
  "ds_read_b64_tr_b16 v[214:215], %[a2] offset:32424\n\t" \
  "v_and_b32_sdwa v223, %[mk], v222 dst_sel:DWORD dst_unused:UNUSED_PAD src0_sel:DWORD src1_sel:WORD_1\n\t" \
  "v_mfma_f32_16x16x32_bf16 v[40:43], v[208:211], v[186:189], v[40:43]\n\t" \
  "ds_read_b32 v219, v219 offset:0\n\t" \
  "v_and_b32 v222, %[mk], v222\n\t" \
  "v_fma_f32 v163, |v163|, v225, v163 mul:4\n\t" \
  "v_mfma_f32_16x16x32_bf16 v[64:67], v[208:211], v[194:197], v[64:67]\n\t" \
  "ds_read_b32 v218, v218 offset:0\n\t" \
  "v_fma_f32 v162, |v162|, v224, v162 mul:4\n\t" \
  "v_fma_f32 v171, |v171|, v227, v171 mul:4\n\t" \
  "v_mfma_f32_16x16x32_bf16 v[88:91], v[208:211], v[202:205], v[88:91]\n\t" \
  "ds_read_b64_tr_b16 v[208:209], %[a1] offset:12304\n\t" \
  "ds_read_b64_tr_b16 v[210:211], %[a1] offset:13328\n\t" \
  "v_fma_f32 v170, |v170|, v226, v170 mul:4\n\t" \
  "v_fma_f32 v169, |v169|, v229, v169 mul:4\n\t" \
  "s_waitcnt lgkmcnt(4)\n\t" \
  "v_mfma_f32_16x16x32_bf16 v[20:23], v[212:215], v[178:181], v[20:23]\n\t" \
  "ds_read_b32 v221, v221 offset:0\n\t" \
  "v_fma_f32 v168, |v168|, v228, v168 mul:4\n\t" \
  "v_fma_mix_f32 v144, v144, v233, 0 op_sel_hi:[0,1,0]\n\t" \
  "v_fma_mix_f32 v145, v145, v232, 0 op_sel_hi:[0,1,0]\n\t" \
  "v_mfma_f32_16x16x32_bf16 v[44:47], v[212:215], v[186:189], v[44:47]\n\t" \
  "ds_read_b32 v220, v220 offset:0\n\t" \
  "v_pk_fma_f16 v234, v234, %[hh], %[hh]\n\t" \
  "v_cvt_pk_bf16_f32 v146, v144, v145\n\t" \
  "v_cvt_pk_bf16_f32 v163, v162, v163\n\t" \
  "v_mfma_f32_16x16x32_bf16 v[68:71], v[212:215], v[194:197], v[68:71]\n\t" \
  "ds_read_b32 v223, v223 offset:0\n\t" \
  "v_cvt_pk_bf16_f32 v171, v170, v171\n\t" \
  "v_cvt_pk_bf16_f32 v170, v168, v169\n\t" \
  "v_rcp_f16 v235, v234\n\t" \
  "v_mfma_f32_16x16x32_bf16 v[92:95], v[212:215], v[202:205], v[92:95]\n\t" \
  "ds_read_b64_tr_b16 v[212:213], %[a1] offset:15376\n\t" \
  "v_cvt_pk_f16_f32 v224, |v164|, |v165| clamp\n\t" \
  "s_setprio 2\n\t" \
  "v_cvt_pk_f16_f32 v226, |v166|, |v167| clamp\n\t" \
  "s_waitcnt lgkmcnt(4)\n\t" \
  "v_mfma_f32_16x16x32_bf16 v[176:179], v[208:211], v[96:99], 0\n\t" \
  "ds_read_b64_tr_b16 v[214:215], %[a1] offset:16400\n\t" \
  "v_cvt_pk_f16_f32 v228, |v172|, |v173| clamp\n\t" \
  "v_cvt_pk_f16_f32 v230, |v174|, |v175| clamp\n\t" \
  "v_rcp_f16_sdwa v234, v234 dst_sel:DWORD dst_unused:UNUSED_PAD src0_sel:WORD_1\n\t" \
  "v_mfma_f32_16x16x32_bf16 v[184:187], v[208:211], v[108:111], 0\n\t" \
  "ds_read_b32 v222, v222 offset:0\n\t" \
  "v_and_b32_sdwa v225, %[mk], v224 dst_sel:DWORD dst_unused:UNUSED_PAD src0_sel:DWORD src1_sel:WORD_1\n\t" \
  "v_and_b32 v224, %[mk], v224\n\t" \
  "v_mfma_f32_16x16x32_bf16 v[192:195], v[208:211], v[120:123], 0\n\t" \
  "ds_read_b32 v225, v225 offset:0\n\t" \
  "v_and_b32_sdwa v227, %[mk], v226 dst_sel:DWORD dst_unused:UNUSED_PAD src0_sel:DWORD src1_sel:WORD_1\n\t" \
  "v_mfma_f32_16x16x32_bf16 v[200:203], v[208:211], v[132:135], 0\n\t" \
  "ds_read_b64_tr_b16 v[208:209], %[a1] offset:14352\n\t" \
  "v_and_b32 v226, %[mk], v226\n\t" \
  "v_and_b32_sdwa v229, %[mk], v228 dst_sel:DWORD dst_unused:UNUSED_PAD src0_sel:DWORD src1_sel:WORD_1\n\t" \
  "s_waitcnt lgkmcnt(3)\n\t" \
  "v_mfma_f32_16x16x32_bf16 v[176:179], v[212:215], v[100:103], v[176:179]\n\t" \
  "ds_read_b64_tr_b16 v[210:211], %[a1] offset:17424\n\t" \
  "v_and_b32 v228, %[mk], v228\n\t" \
  "v_mfma_f32_16x16x32_bf16 v[184:187], v[212:215], v[112:115], v[184:187]\n\t" \
  "ds_read_b32 v224, v224 offset:0\n\t" \
  "v_and_b32_sdwa v231, %[mk], v230 dst_sel:DWORD dst_unused:UNUSED_PAD src0_sel:DWORD src1_sel:WORD_1\n\t" \
  "v_mfma_f32_16x16x32_bf16 v[192:195], v[212:215], v[124:127], v[192:195]\n\t" \
  "ds_read_b32 v227, v227 offset:0\n\t" \
  "v_and_b32 v230, %[mk], v230\n\t" \
  "v_fma_f32 v149, |v149|, v217, v149 mul:4\n\t" \
  "v_mfma_f32_16x16x32_bf16 v[200:203], v[212:215], v[136:139], v[200:203]\n\t" \
  "ds_read_b64_tr_b16 v[212:213], %[a1] offset:12816\n\t" \
  "ds_read_b64_tr_b16 v[214:215], %[a1] offset:13840\n\t" \
  "v_fma_f32 v148, |v148|, v216, v148 mul:4\n\t" \
  "v_fma_f32 v151, |v151|, v219, v151 mul:4\n\t" \
  "s_waitcnt lgkmcnt(4)\n\t" \
  "v_mfma_f32_16x16x32_bf16 v[176:179], v[208:211], v[104:107], v[176:179]\n\t" \
  "ds_read_b32 v226, v226 offset:0\n\t" \
  "v_fma_f32 v150, |v150|, v218, v150 mul:4\n\t" \
  "v_fma_f32 v157, |v157|, v221, v157 mul:4\n\t" \
  "v_mfma_f32_16x16x32_bf16 v[184:187], v[208:211], v[116:119], v[184:187]\n\t" \
  "ds_read_b32 v229, v229 offset:0\n\t" \
  "v_fma_f32 v156, |v156|, v220, v156 mul:4\n\t" \
  "v_fma_f32 v159, |v159|, v223, v159 mul:4\n\t" \
  "v_fma_f32 v158, |v158|, v222, v158 mul:4\n\t" \
  "v_mfma_f32_16x16x32_bf16 v[192:195], v[208:211], v[128:131], v[192:195]\n\t" \
  "ds_read_b32 v228, v228 offset:0\n\t" \
  "v_fma_mix_f32 v160, v160, v235, 0 op_sel_hi:[0,1,0]\n\t" \
  "v_fma_mix_f32 v161, v161, v234, 0 op_sel_hi:[0,1,0]\n\t" \
  "v_cvt_pk_bf16_f32 v162, v160, v161\n\t" \
  "v_mfma_f32_16x16x32_bf16 v[200:203], v[208:211], v[140:143], v[200:203]\n\t" \
  "ds_read_b64_tr_b16 v[208:209], %[a1] offset:15888\n\t" \
  "v_cvt_pk_bf16_f32 v148, v148, v149\n\t" \
  "v_cvt_pk_bf16_f32 v149, v150, v151\n\t" \
  "s_waitcnt lgkmcnt(4)\n\t" \
  "v_mfma_f32_16x16x32_bf16 v[180:183], v[212:215], v[96:99], 0\n\t" \
  "ds_read_b64_tr_b16 v[210:211], %[a1] offset:16912\n\t" \
  "v_cvt_pk_bf16_f32 v156, v156, v157\n\t" \
  "v_cvt_pk_bf16_f32 v157, v158, v159\n\t" \
  "s_setprio 3\n\t" \
  "v_cvt_pk_f16_f32 v216, |v178|, |v179| clamp\n\t" \
  "v_mfma_f32_16x16x32_bf16 v[188:191], v[212:215], v[108:111], 0\n\t" \
  "ds_read_b32 v231, v231 offset:0\n\t" \
  "v_cvt_pk_f16_f32 v232, v176, v177\n\t" \
  "v_cvt_pk_f16_f32 v218, |v186|, |v187| clamp\n\t" \
  "v_cvt_pk_f16_f32 v220, |v184|, |v185| clamp\n\t" \
  "v_mfma_f32_16x16x32_bf16 v[196:199], v[212:215], v[120:123], 0\n\t" \
  "ds_read_b32 v230, v230 offset:0\n\t" \
  "v_pk_mul_f16 v233, v232, v232\n\t" \
  "v_and_b32_sdwa v217, %[mk], v216 dst_sel:DWORD dst_unused:UNUSED_PAD src0_sel:DWORD src1_sel:WORD_1\n\t" \
  "v_mfma_f32_16x16x32_bf16 v[204:207], v[212:215], v[132:135], 0\n\t" \
  "ds_read_b64_tr_b16 v[212:213], %[a1] offset:14864\n\t" \
  "v_and_b32 v216, %[mk], v216\n\t" \
  "v_and_b32_sdwa v219, %[mk], v218 dst_sel:DWORD dst_unused:UNUSED_PAD src0_sel:DWORD src1_sel:WORD_1\n\t" \
  "s_waitcnt lgkmcnt(3)\n\t" \
  "v_mfma_f32_16x16x32_bf16 v[180:183], v[208:211], v[100:103], v[180:183]\n\t" \
  "ds_read_b64_tr_b16 v[214:215], %[a1] offset:17936\n\t" \
  "v_and_b32 v218, %[mk], v218\n\t" \
  "v_mfma_f32_16x16x32_bf16 v[188:191], v[208:211], v[112:115], v[188:191]\n\t" \
  "s_waitcnt vmcnt(1)\n\t" \
  "s_cmp_lg_u32 %[rnd], 1\n\t" \
  "s_cbranch_scc1 .Lcnx_c14\n\t" \
  "s_waitcnt vmcnt(0)\n\t" \
  ".Lcnx_c14:\n\t" \
  "s_mov_b64 exec, 3\n\t" \
  "ds_add_rtn_u32 v246, %[l8], %[vo] offset:47712\n\t" \
  "s_mov_b64 exec, -1\n\t" \
  "ds_read_b32 v217, v217 offset:0\n\t" \
  "v_and_b32_sdwa v221, %[mk], v220 dst_sel:DWORD dst_unused:UNUSED_PAD src0_sel:DWORD src1_sel:WORD_1\n\t" \
  "v_mfma_f32_16x16x32_bf16 v[196:199], v[208:211], v[124:127], v[196:199]\n\t" \
  "ds_read_b32 v216, v216 offset:0\n\t" \
  "v_and_b32 v220, %[mk], v220\n\t" \
  "v_pk_fma_f16 v233, v233, %[h2], %[h1]\n\t" \
  "v_mfma_f32_16x16x32_bf16 v[204:207], v[208:211], v[136:139], v[204:207]\n\t" \
  "s_waitcnt lgkmcnt(2)\n\t" \
  "v_readfirstlane_b32 %[t0], v246\n\t" \
  "s_cmp_lt_u32 %[t0], %[tg]\n\t" \
  "s_cbranch_scc1 .Lcnx_w84\n.Lcnx_b84:\n\t" \
  "s_or_b32 %[grp], %[grp], %[rnd]\n\t" \
  "ds_read_b64_tr_b16 v[208:209], %[a2] offset:0\n\t" \
  "v_pk_mul_f16 v233, v232, v233\n\t" \
  "v_fma_f32 v165, |v165|, v225, v165 mul:4\n\t" \
  "v_mfma_f32_16x16x32_bf16 v[180:183], v[212:215], v[104:107], v[180:183]\n\t" \
  "ds_read_b64_tr_b16 v[210:211], %[a2] offset:1536\n\t" \
  "v_fma_f32 v164, |v164|, v224, v164 mul:4\n\t" \
  "v_fma_f32 v167, |v167|, v227, v167 mul:4\n\t" \
  "v_mfma_f32_16x16x32_bf16 v[188:191], v[212:215], v[116:119], v[188:191]\n\t" \
  "s_cmp_eq_u32 %[grp], 0\n\t" \
  "s_cbranch_scc1 .Lcnx_f8\n.Lcnx_g8:\n\t" \
  "ds_read_b32 v219, v219 offset:0\n\t" \
  "v_fma_f32 v166, |v166|, v226, v166 mul:4\n\t" \
  "v_fma_f32 v173, |v173|, v229, v173 mul:4\n\t" \
  "v_fma_f32 v172, |v172|, v228, v172 mul:4\n\t" \
  "v_mfma_f32_16x16x32_bf16 v[196:199], v[212:215], v[128:131], v[196:199]\n\t" \
  "ds_read_b32 v218, v218 offset:0\n\t" \
  "v_fma_f32 v175, |v175|, v231, v175 mul:4\n\t" \
  "v_fma_f32 v174, |v174|, v230, v174 mul:4\n\t" \
  "v_cvt_pk_bf16_f32 v164, v164, v165\n\t" \
  "v_mfma_f32_16x16x32_bf16 v[204:207], v[212:215], v[140:143], v[204:207]\n\t" \
  "ds_read_b64_tr_b16 v[212:213], %[a2] offset:8\n\t" \
  "v_cvt_pk_bf16_f32 v165, v166, v167\n\t" \
  "v_cvt_pk_bf16_f32 v172, v172, v173\n\t" \
  "v_cvt_pk_bf16_f32 v173, v174, v175\n\t" \
  "s_setprio 0\n\t" \
  "v_exp_f16 v232, v233\n\t" \
  "v_cvt_pk_f16_f32 v224, |v194|, |v195| clamp\n\t" \
  "v_mfma_f32_16x16x32_bf16 v[48:51], v[242:245], v[236:239], v[48:51]\n\t" \
  "ds_read_b64_tr_b16 v[214:215], %[a2] offset:1544\n\t" \
  "v_cvt_pk_f16_f32 v234, v192, v193\n\t" \
  "v_cvt_pk_f16_f32 v226, |v202|, |v203| clamp\n\t" \
  "v_mfma_f32_16x16x32_bf16 v[52:55], v[240:243], v[236:239], v[52:55]\n\t" \
  "buffer_load_dwordx4 v[236:239], %[vo], %[xrs], %[oc1] offen offset:64\n\t" \
  "s_cmp_lg_u32 %[rnd], 0\n\t" \
  "s_cbranch_scc1 .Lcnx_c27\n\t" \
  "s_add_u32 m0, %[xl], 49200\n\t" \
  "s_add_u32 %[xo], %[xo], %[rs]\n\t" \
  "buffer_load_dwordx4 %[xv], %[xrs], %[xo] offen lds\n\t" \
  "s_cmp_lg_u32 %[wq], 4\n\t" \
  "s_cbranch_scc1 .Lcnx_c27\n\t" \
  "s_mov_b32 m0, %[pf]\n\t" \
  "s_add_u32 %[wb], %[wb], %[rs4]\n\t" \
  "buffer_load_dword %[wv], %[xrs], %[wb] offen lds\n\t" \
  ".Lcnx_c27:\n\t" \
  "ds_read_b32 v221, v221 offset:0\n\t" \
  "v_cvt_pk_f16_f32 v228, |v200|, |v201| clamp\n\t" \
  "v_exp_f16_sdwa v232, v233 dst_sel:WORD_1 dst_unused:UNUSED_PRESERVE src0_sel:WORD_1\n\t" \
  "v_pk_mul_f16 v235, v234, v234\n\t" \
  "s_waitcnt lgkmcnt(5)\n\t" \
  "v_mfma_f32_16x16x32_bf16 v[0:3], v[208:211], v[146:149], v[0:3]\n\t" \
  "ds_read_b32 v220, v220 offset:0\n\t" \
  "v_and_b32_sdwa v225, %[mk], v224 dst_sel:DWORD dst_unused:UNUSED_PAD src0_sel:DWORD src1_sel:WORD_1\n\t" \
  "v_and_b32 v224, %[mk], v224\n\t" \
  "v_mfma_f32_16x16x32_bf16 v[24:27], v[208:211], v[154:157], v[24:27]\n\t" \
  "ds_read_b32 v225, v225 offset:0\n\t" \
  "v_and_b32_sdwa v227, %[mk], v226 dst_sel:DWORD dst_unused:UNUSED_PAD src0_sel:DWORD src1_sel:WORD_1\n\t" \
  "v_mfma_f32_16x16x32_bf16 v[48:51], v[208:211], v[162:165], v[48:51]\n\t" \
  "ds_read_b32 v224, v224 offset:0\n\t" \
  "v_and_b32 v226, %[mk], v226\n\t" \
  "v_and_b32_sdwa v229, %[mk], v228 dst_sel:DWORD dst_unused:UNUSED_PAD src0_sel:DWORD src1_sel:WORD_1\n\t" \
  "v_mfma_f32_16x16x32_bf16 v[72:75], v[208:211], v[170:173], v[72:75]\n\t" \
  "ds_read_b64_tr_b16 v[208:209], %[a2] offset:64\n\t" \
  "v_and_b32 v228, %[mk], v228\n\t" \
  "s_waitcnt lgkmcnt(5)\n\t" \
  "v_mfma_f32_16x16x32_bf16 v[4:7], v[212:215], v[146:149], v[4:7]\n\t" \
  "ds_read_b64_tr_b16 v[210:211], %[a2] offset:1600\n\t" \
  "v_pk_fma_f16 v235, v235, %[h2], %[h1]\n\t" \
  "v_mfma_f32_16x16x32_bf16 v[28:31], v[212:215], v[154:157], v[28:31]\n\t" \
  "ds_read_b32 v227, v227 offset:0\n\t" \
  "v_pk_mul_f16 v235, v234, v235\n\t" \
  "v_fma_f32 v179, |v179|, v217, v179 mul:4\n\t" \
  "v_fma_f32 v178, |v178|, v216, v178 mul:4\n\t" \
  "v_mfma_f32_16x16x32_bf16 v[52:55], v[212:215], v[162:165], v[52:55]\n\t" \
  "ds_read_b32 v226, v226 offset:0\n\t" \
  "v_fma_f32 v187, |v187|, v219, v187 mul:4\n\t" \
  "s_waitcnt lgkmcnt(6)\n\t" \
  "v_fma_f32 v186, |v186|, v218, v186 mul:4\n\t" \
  "v_fma_f32 v185, |v185|, v221, v185 mul:4\n\t" \
  "v_mfma_f32_16x16x32_bf16 v[76:79], v[212:215], v[170:173], v[76:79]\n\t" \
  "ds_read_b64_tr_b16 v[212:213], %[a2] offset:72\n\t" \
  "v_fma_f32 v184, |v184|, v220, v184 mul:4\n\t" \
  "v_pk_fma_f16 v232, v232, %[hh], %[hh]\n\t" \
  "s_waitcnt lgkmcnt(3)\n\t" \
  "v_mfma_f32_16x16x32_bf16 v[8:11], v[208:211], v[146:149], v[8:11]\n\t" \
  "ds_read_b64_tr_b16 v[214:215], %[a2] offset:1608\n\t" \
  "v_cvt_pk_bf16_f32 v179, v178, v179\n\t" \
  "v_cvt_pk_bf16_f32 v187, v186, v187\n\t" \
  "v_cvt_pk_bf16_f32 v186, v184, v185\n\t" \
  "v_mfma_f32_16x16x32_bf16 v[32:35], v[208:211], v[154:157], v[32:35]\n\t" \
  "ds_read_b32 v229, v229 offset:0\n\t" \
  "v_rcp_f16 v233, v232\n\t" \
  "v_exp_f16 v234, v235\n\t" \
  "v_cvt_pk_f16_f32 v216, |v180|, |v181| clamp\n\t" \
  "v_mfma_f32_16x16x32_bf16 v[56:59], v[208:211], v[162:165], v[56:59]\n\t" \
  "ds_read_b32 v228, v228 offset:0\n\t" \
  "s_setprio 1\n\t" \
  "v_cvt_pk_f16_f32 v218, |v182|, |v183| clamp\n\t" \
  "v_cvt_pk_f16_f32 v220, |v188|, |v189| clamp\n\t" \
  "v_mfma_f32_16x16x32_bf16 v[80:83], v[208:211], v[170:173], v[80:83]\n\t" \
  "ds_read_b64_tr_b16 v[208:209], %[a2] offset:128\n\t" \
  "v_cvt_pk_f16_f32 v222, |v190|, |v191| clamp\n\t" \
  "v_rcp_f16_sdwa v232, v232 dst_sel:DWORD dst_unused:UNUSED_PAD src0_sel:WORD_1\n\t" \
  "v_exp_f16_sdwa v234, v235 dst_sel:WORD_1 dst_unused:UNUSED_PRESERVE src0_sel:WORD_1\n\t" \
  "s_waitcnt lgkmcnt(3)\n\t" \
  "v_mfma_f32_16x16x32_bf16 v[12:15], v[212:215], v[146:149], v[12:15]\n\t" \
  "ds_read_b64_tr_b16 v[210:211], %[a2] offset:1664\n\t" \
  "v_and_b32_sdwa v217, %[mk], v216 dst_sel:DWORD dst_unused:UNUSED_PAD src0_sel:DWORD src1_sel:WORD_1\n\t" \
  "v_and_b32 v216, %[mk], v216\n\t" \
  "v_mfma_f32_16x16x32_bf16 v[36:39], v[212:215], v[154:157], v[36:39]\n\t" \
  "ds_read_b32 v217, v217 offset:0\n\t" \
  "v_and_b32_sdwa v219, %[mk], v218 dst_sel:DWORD dst_unused:UNUSED_PAD src0_sel:DWORD src1_sel:WORD_1\n\t" \
  "v_mfma_f32_16x16x32_bf16 v[60:63], v[212:215], v[162:165], v[60:63]\n\t" \
  "ds_read_b32 v216, v216 offset:0\n\t" \
  "v_and_b32 v218, %[mk], v218\n\t" \
  "v_and_b32_sdwa v221, %[mk], v220 dst_sel:DWORD dst_unused:UNUSED_PAD src0_sel:DWORD src1_sel:WORD_1\n\t" \
  "v_mfma_f32_16x16x32_bf16 v[84:87], v[212:215], v[170:173], v[84:87]\n\t" \
  "ds_read_b64_tr_b16 v[212:213], %[a2] offset:136\n\t" \
  "v_and_b32 v220, %[mk], v220\n\t" \
  "s_waitcnt lgkmcnt(3)\n\t" \
  "v_mfma_f32_16x16x32_bf16 v[16:19], v[208:211], v[146:149], v[16:19]\n\t" \
  "ds_read_b64_tr_b16 v[214:215], %[a2] offset:1672\n\t" \
  "v_and_b32_sdwa v223, %[mk], v222 dst_sel:DWORD dst_unused:UNUSED_PAD src0_sel:DWORD src1_sel:WORD_1\n\t" \
  "v_mfma_f32_16x16x32_bf16 v[40:43], v[208:211], v[154:157], v[40:43]\n\t" \
  "ds_read_b32 v219, v219 offset:0\n\t" \
  "v_and_b32 v222, %[mk], v222\n\t" \
  "v_fma_f32 v195, |v195|, v225, v195 mul:4\n\t" \
  "v_mfma_f32_16x16x32_bf16 v[64:67], v[208:211], v[162:165], v[64:67]\n\t" \
  "ds_read_b32 v218, v218 offset:0\n\t" \
  "v_fma_f32 v194, |v194|, v224, v194 mul:4\n\t" \
  "v_fma_f32 v203, |v203|, v227, v203 mul:4\n\t" \
  "v_mfma_f32_16x16x32_bf16 v[88:91], v[208:211], v[170:173], v[88:91]\n\t" \
  "ds_read_b64_tr_b16 v[208:209], %[a1] offset:43048\n\t" \
  "ds_read_b64_tr_b16 v[210:211], %[a1] offset:44072\n\t" \
  "v_fma_f32 v202, |v202|, v226, v202 mul:4\n\t" \
  "v_fma_f32 v201, |v201|, v229, v201 mul:4\n\t" \
  "s_waitcnt lgkmcnt(4)\n\t" \
  "v_mfma_f32_16x16x32_bf16 v[20:23], v[212:215], v[146:149], v[20:23]\n\t" \
  "ds_read_b32 v221, v221 offset:0\n\t" \
  "v_fma_f32 v200, |v200|, v228, v200 mul:4\n\t" \
  "v_fma_mix_f32 v176, v176, v233, 0 op_sel_hi:[0,1,0]\n\t" \
  "v_fma_mix_f32 v177, v177, v232, 0 op_sel_hi:[0,1,0]\n\t" \
  "v_mfma_f32_16x16x32_bf16 v[44:47], v[212:215], v[154:157], v[44:47]\n\t" \
  "ds_read_b32 v220, v220 offset:0\n\t" \
  "v_pk_fma_f16 v234, v234, %[hh], %[hh]\n\t" \
  "v_cvt_pk_bf16_f32 v178, v176, v177\n\t" \
  "v_cvt_pk_bf16_f32 v195, v194, v195\n\t" \
  "v_mfma_f32_16x16x32_bf16 v[68:71], v[212:215], v[162:165], v[68:71]\n\t" \
  "ds_read_b32 v223, v223 offset:0\n\t" \
  "v_cvt_pk_bf16_f32 v203, v202, v203\n\t" \
  "v_cvt_pk_bf16_f32 v202, v200, v201\n\t" \
  "v_rcp_f16 v235, v234\n\t" \
  "v_mfma_f32_16x16x32_bf16 v[92:95], v[212:215], v[170:173], v[92:95]\n\t" \
  "ds_read_b64_tr_b16 v[212:213], %[a1] offset:46120\n\t" \
  "v_cvt_pk_f16_f32 v224, |v196|, |v197| clamp\n\t" \
  "s_setprio 2\n\t" \
  "v_cvt_pk_f16_f32 v226, |v198|, |v199| clamp\n\t" \
  "s_waitcnt lgkmcnt(4)\n\t" \
  "v_mfma_f32_16x16x32_bf16 v[144:147], v[208:211], v[96:99], 0\n\t" \
  "ds_read_b64_tr_b16 v[214:215], %[a1] offset:47144\n\t" \
  "v_cvt_pk_f16_f32 v228, |v204|, |v205| clamp\n\t" \
  "v_cvt_pk_f16_f32 v230, |v206|, |v207| clamp\n\t" \
  "v_rcp_f16_sdwa v234, v234 dst_sel:DWORD dst_unused:UNUSED_PAD src0_sel:WORD_1\n\t" \
  "v_mfma_f32_16x16x32_bf16 v[152:155], v[208:211], v[108:111], 0\n\t" \
  "ds_read_b32 v222, v222 offset:0\n\t" \
  "v_and_b32_sdwa v225, %[mk], v224 dst_sel:DWORD dst_unused:UNUSED_PAD src0_sel:DWORD src1_sel:WORD_1\n\t" \
  "v_and_b32 v224, %[mk], v224\n\t" \
  "v_mfma_f32_16x16x32_bf16 v[160:163], v[208:211], v[120:123], 0\n\t" \
  "ds_read_b32 v225, v225 offset:0\n\t" \
  "v_and_b32_sdwa v227, %[mk], v226 dst_sel:DWORD dst_unused:UNUSED_PAD src0_sel:DWORD src1_sel:WORD_1\n\t" \
  "v_mfma_f32_16x16x32_bf16 v[168:171], v[208:211], v[132:135], 0\n\t" \
  "ds_read_b64_tr_b16 v[208:209], %[a1] offset:45096\n\t" \
  "v_and_b32 v226, %[mk], v226\n\t" \
  "v_and_b32_sdwa v229, %[mk], v228 dst_sel:DWORD dst_unused:UNUSED_PAD src0_sel:DWORD src1_sel:WORD_1\n\t" \
  "s_waitcnt lgkmcnt(3)\n\t" \
  "v_mfma_f32_16x16x32_bf16 v[144:147], v[212:215], v[100:103], v[144:147]\n\t" \
  "ds_read_b64_tr_b16 v[210:211], %[a1] offset:48168\n\t" \
  "v_and_b32 v228, %[mk], v228\n\t" \
  "v_mfma_f32_16x16x32_bf16 v[152:155], v[212:215], v[112:115], v[152:155]\n\t" \
  "ds_read_b32 v224, v224 offset:0\n\t" \
  "v_and_b32_sdwa v231, %[mk], v230 dst_sel:DWORD dst_unused:UNUSED_PAD src0_sel:DWORD src1_sel:WORD_1\n\t" \
  "v_mfma_f32_16x16x32_bf16 v[160:163], v[212:215], v[124:127], v[160:163]\n\t" \
  "ds_read_b32 v227, v227 offset:0\n\t" \
  "v_and_b32 v230, %[mk], v230\n\t" \
  "v_fma_f32 v181, |v181|, v217, v181 mul:4\n\t" \
  "v_mfma_f32_16x16x32_bf16 v[168:171], v[212:215], v[136:139], v[168:171]\n\t" \
  "ds_read_b64_tr_b16 v[212:213], %[a1] offset:43560\n\t" \
  "ds_read_b64_tr_b16 v[214:215], %[a1] offset:44584\n\t" \
  "v_fma_f32 v180, |v180|, v216, v180 mul:4\n\t" \
  "v_fma_f32 v183, |v183|, v219, v183 mul:4\n\t" \
  "s_waitcnt lgkmcnt(4)\n\t" \
  "v_mfma_f32_16x16x32_bf16 v[144:147], v[208:211], v[104:107], v[144:147]\n\t" \
  "ds_read_b32 v226, v226 offset:0\n\t" \
  "v_fma_f32 v182, |v182|, v218, v182 mul:4\n\t" \
  "v_fma_f32 v189, |v189|, v221, v189 mul:4\n\t" \
  "v_mfma_f32_16x16x32_bf16 v[152:155], v[208:211], v[116:119], v[152:155]\n\t" \
  "ds_read_b32 v229, v229 offset:0\n\t" \
  "v_fma_f32 v188, |v188|, v220, v188 mul:4\n\t" \
  "v_fma_f32 v191, |v191|, v223, v191 mul:4\n\t" \
  "v_fma_f32 v190, |v190|, v222, v190 mul:4\n\t" \
  "v_mfma_f32_16x16x32_bf16 v[160:163], v[208:211], v[128:131], v[160:163]\n\t" \
  "ds_read_b32 v228, v228 offset:0\n\t" \
  "v_fma_mix_f32 v192, v192, v235, 0 op_sel_hi:[0,1,0]\n\t" \
  "v_fma_mix_f32 v193, v193, v234, 0 op_sel_hi:[0,1,0]\n\t" \
  "v_cvt_pk_bf16_f32 v194, v192, v193\n\t" \
  "v_mfma_f32_16x16x32_bf16 v[168:171], v[208:211], v[140:143], v[168:171]\n\t" \
  "ds_read_b64_tr_b16 v[208:209], %[a1] offset:46632\n\t" \
  "v_cvt_pk_bf16_f32 v180, v180, v181\n\t" \
  "v_cvt_pk_bf16_f32 v181, v182, v183\n\t" \
  "s_waitcnt lgkmcnt(4)\n\t" \
  "v_mfma_f32_16x16x32_bf16 v[148:151], v[212:215], v[96:99], 0\n\t" \
  "ds_read_b64_tr_b16 v[210:211], %[a1] offset:47656\n\t" \
  "v_cvt_pk_bf16_f32 v188, v188, v189\n\t" \
  "v_cvt_pk_bf16_f32 v189, v190, v191\n\t" \
  "s_setprio 3\n\t" \
  "v_cvt_pk_f16_f32 v216, |v146|, |v147| clamp\n\t" \
  "v_mfma_f32_16x16x32_bf16 v[156:159], v[212:215], v[108:111], 0\n\t" \
  "ds_read_b32 v231, v231 offset:0\n\t" \
  "v_cvt_pk_f16_f32 v232, v144, v145\n\t" \
  "v_cvt_pk_f16_f32 v218, |v154|, |v155| clamp\n\t" \
  "v_cvt_pk_f16_f32 v220, |v152|, |v153| clamp\n\t" \
  "v_mfma_f32_16x16x32_bf16 v[164:167], v[212:215], v[120:123], 0\n\t" \
  "ds_read_b32 v230, v230 offset:0\n\t" \
  "v_pk_mul_f16 v233, v232, v232\n\t" \
  "v_and_b32_sdwa v217, %[mk], v216 dst_sel:DWORD dst_unused:UNUSED_PAD src0_sel:DWORD src1_sel:WORD_1\n\t" \
  "v_mfma_f32_16x16x32_bf16 v[172:175], v[212:215], v[132:135], 0\n\t" \
  "ds_read_b64_tr_b16 v[212:213], %[a1] offset:45608\n\t" \
  "v_and_b32 v216, %[mk], v216\n\t" \
  "v_and_b32_sdwa v219, %[mk], v218 dst_sel:DWORD dst_unused:UNUSED_PAD src0_sel:DWORD src1_sel:WORD_1\n\t" \
  "s_waitcnt lgkmcnt(3)\n\t" \
  "v_mfma_f32_16x16x32_bf16 v[148:151], v[208:211], v[100:103], v[148:151]\n\t" \
  "ds_read_b64_tr_b16 v[214:215], %[a1] offset:48680\n\t" \
  "v_and_b32 v218, %[mk], v218\n\t" \
  "v_mfma_f32_16x16x32_bf16 v[156:159], v[208:211], v[112:115], v[156:159]\n\t" \
  "s_waitcnt vmcnt(1)\n\t" \
  "s_cmp_lg_u32 %[rnd], 1\n\t" \
  "s_cbranch_scc1 .Lcnx_c15\n\t" \
  "s_waitcnt vmcnt(0)\n\t" \
  ".Lcnx_c15:\n\t" \
  "s_mov_b64 exec, 3\n\t" \
  "ds_add_rtn_u32 v246, %[l8], %[vo] offset:47720\n\t" \
  "s_mov_b64 exec, -1\n\t" \
  "ds_read_b32 v217, v217 offset:0\n\t" \
  "v_and_b32_sdwa v221, %[mk], v220 dst_sel:DWORD dst_unused:UNUSED_PAD src0_sel:DWORD src1_sel:WORD_1\n\t" \
  "v_mfma_f32_16x16x32_bf16 v[164:167], v[208:211], v[124:127], v[164:167]\n\t" \
  "ds_read_b32 v216, v216 offset:0\n\t" \
  "v_and_b32 v220, %[mk], v220\n\t" \
  "v_pk_fma_f16 v233, v233, %[h2], %[h1]\n\t" \
  "v_mfma_f32_16x16x32_bf16 v[172:175], v[208:211], v[136:139], v[172:175]\n\t" \
  "s_waitcnt lgkmcnt(2)\n\t" \
  "v_readfirstlane_b32 %[t0], v246\n\t" \
  "s_cmp_lt_u32 %[t0], %[tg]\n\t" \
  "s_cbranch_scc1 .Lcnx_w96\n.Lcnx_b96:\n\t" \
  "s_cmp_lg_u32 %[rnd], 1\n\t" \
  "s_cselect_b32 %[tg], %[tg], 0\n\t" \
  "ds_read_b64_tr_b16 v[208:209], %[a2] offset:6152\n\t" \
  "v_pk_mul_f16 v233, v232, v233\n\t" \
  "v_fma_f32 v197, |v197|, v225, v197 mul:4\n\t" \
  "v_mfma_f32_16x16x32_bf16 v[148:151], v[212:215], v[104:107], v[148:151]\n\t" \
  "ds_read_b64_tr_b16 v[210:211], %[a2] offset:7688\n\t" \
  "v_fma_f32 v196, |v196|, v224, v196 mul:4\n\t" \
  "v_fma_f32 v199, |v199|, v227, v199 mul:4\n\t" \
  "v_mfma_f32_16x16x32_bf16 v[156:159], v[212:215], v[116:119], v[156:159]\n\t" \
  "s_cmp_eq_u32 %[grp], 0\n\t" \
  "s_cbranch_scc1 .Lcnx_f9\n.Lcnx_g9:\n\t" \
  "ds_read_b32 v219, v219 offset:0\n\t" \
  "v_fma_f32 v198, |v198|, v226, v198 mul:4\n\t" \
  "v_fma_f32 v205, |v205|, v229, v205 mul:4\n\t" \
  "v_fma_f32 v204, |v204|, v228, v204 mul:4\n\t" \
  "v_mfma_f32_16x16x32_bf16 v[164:167], v[212:215], v[128:131], v[164:167]\n\t" \
  "ds_read_b32 v218, v218 offset:0\n\t" \
  "v_fma_f32 v207, |v207|, v231, v207 mul:4\n\t" \
  "v_fma_f32 v206, |v206|, v230, v206 mul:4\n\t" \
  "v_cvt_pk_bf16_f32 v196, v196, v197\n\t" \
  "v_mfma_f32_16x16x32_bf16 v[172:175], v[212:215], v[140:143], v[172:175]\n\t" \
  "ds_read_b64_tr_b16 v[212:213], %[a2] offset:6160\n\t" \
  "v_cvt_pk_bf16_f32 v197, v198, v199\n\t" \
  "v_cvt_pk_bf16_f32 v204, v204, v205\n\t" \
  "v_cvt_pk_bf16_f32 v205, v206, v207\n\t" \
  "s_cmp_lg_u32 %[rnd], 1\n\t" \
  "s_cbranch_scc1 .Lcnx_c16\n\t" \
  "s_mov_b64 vcc, -1\n\t" \
  "s_setprio 0\n\t" \
  "s_cmp_ge_u32 %[w512], 2048\n\t" \
  "s_cbranch_scc1 .Lcnx_c16\n\t" \
  "s_setprio 3\n\t" \
  ".Lcnx_c16:\n\t" \
  "s_cbranch_vccnz .Lcnx_p9_0\n\ts_setprio 0\n.Lcnx_p9_0:\n\t" \
  "v_exp_f16 v232, v233\n\t" \
  "v_cvt_pk_f16_f32 v224, |v162|, |v163| clamp\n\t" \
  "v_mfma_f32_16x16x32_bf16 v[56:59], v[242:245], v[236:239], v[56:59]\n\t" \
  "ds_read_b64_tr_b16 v[214:215], %[a2] offset:7696\n\t" \
  "v_cvt_pk_f16_f32 v234, v160, v161\n\t" \
  "v_cvt_pk_f16_f32 v226, |v170|, |v171| clamp\n\t" \
  "v_mfma_f32_16x16x32_bf16 v[60:63], v[240:243], v[236:239], v[60:63]\n\t" \
  "buffer_load_dwordx4 v[236:239], %[vo], %[xrs], %[oc1] offen offset:128\n\t" \
  "s_cmp_lg_u32 %[rnd], 0\n\t" \
  "s_cbranch_scc1 .Lcnx_c28\n\t" \
  "s_add_u32 m0, %[xl], 57400\n\t" \
  "s_add_u32 %[xo], %[xo], %[rs]\n\t" \
  "buffer_load_dwordx4 %[xv], %[xrs], %[xo] offen lds\n\t" \
  "s_cmp_lg_u32 %[wq], 5\n\t" \
  "s_cbranch_scc1 .Lcnx_c28\n\t" \
  "s_mov_b32 m0, %[pf]\n\t" \
  "s_add_u32 %[wb], %[wb], %[rs4]\n\t" \
  "buffer_load_dword %[wv], %[xrs], %[wb] offen lds\n\t" \
  ".Lcnx_c28:\n\t" \
  "ds_read_b32 v221, v221 offset:0\n\t" \
  "v_cvt_pk_f16_f32 v228, |v168|, |v169| clamp\n\t" \
  "v_exp_f16_sdwa v232, v233 dst_sel:WORD_1 dst_unused:UNUSED_PRESERVE src0_sel:WORD_1\n\t" \
  "v_pk_mul_f16 v235, v234, v234\n\t" \
  "s_waitcnt lgkmcnt(5)\n\t" \
  "v_mfma_f32_16x16x32_bf16 v[0:3], v[208:211], v[178:181], v[0:3]\n\t" \
  "ds_read_b32 v220, v220 offset:0\n\t" \
  "v_and_b32_sdwa v225, %[mk], v224 dst_sel:DWORD dst_unused:UNUSED_PAD src0_sel:DWORD src1_sel:WORD_1\n\t" \
  "v_and_b32 v224, %[mk], v224\n\t" \
  "v_mfma_f32_16x16x32_bf16 v[24:27], v[208:211], v[186:189], v[24:27]\n\t" \
  "ds_read_b32 v225, v225 offset:0\n\t" \
  "v_and_b32_sdwa v227, %[mk], v226 dst_sel:DWORD dst_unused:UNUSED_PAD src0_sel:DWORD src1_sel:WORD_1\n\t" \
  "v_mfma_f32_16x16x32_bf16 v[48:51], v[208:211], v[194:197], v[48:51]\n\t" \
  "ds_read_b32 v224, v224 offset:0\n\t" \
  "v_and_b32 v226, %[mk], v226\n\t" \
  "v_and_b32_sdwa v229, %[mk], v228 dst_sel:DWORD dst_unused:UNUSED_PAD src0_sel:DWORD src1_sel:WORD_1\n\t" \
  "v_mfma_f32_16x16x32_bf16 v[72:75], v[208:211], v[202:205], v[72:75]\n\t" \
  "ds_read_b64_tr_b16 v[208:209], %[a2] offset:6216\n\t" \
  "v_and_b32 v228, %[mk], v228\n\t" \
  "s_waitcnt lgkmcnt(5)\n\t" \
  "v_mfma_f32_16x16x32_bf16 v[4:7], v[212:215], v[178:181], v[4:7]\n\t" \
  "ds_read_b64_tr_b16 v[210:211], %[a2] offset:7752\n\t" \
  "v_pk_fma_f16 v235, v235, %[h2], %[h1]\n\t" \
  "v_mfma_f32_16x16x32_bf16 v[28:31], v[212:215], v[186:189], v[28:31]\n\t" \
  "ds_read_b32 v227, v227 offset:0\n\t" \
  "v_pk_mul_f16 v235, v234, v235\n\t" \
  "v_fma_f32 v147, |v147|, v217, v147 mul:4\n\t" \
  "v_fma_f32 v146, |v146|, v216, v146 mul:4\n\t" \
  "v_mfma_f32_16x16x32_bf16 v[52:55], v[212:215], v[194:197], v[52:55]\n\t" \
  "ds_read_b32 v226, v226 offset:0\n\t" \
  "v_fma_f32 v155, |v155|, v219, v155 mul:4\n\t" \
  "s_waitcnt lgkmcnt(6)\n\t" \
  "v_fma_f32 v154, |v154|, v218, v154 mul:4\n\t" \
  "v_fma_f32 v153, |v153|, v221, v153 mul:4\n\t" \
  "v_mfma_f32_16x16x32_bf16 v[76:79], v[212:215], v[202:205], v[76:79]\n\t" \
  "ds_read_b64_tr_b16 v[212:213], %[a2] offset:6224\n\t" \
  "v_fma_f32 v152, |v152|, v220, v152 mul:4\n\t" \
  "v_pk_fma_f16 v232, v232, %[hh], %[hh]\n\t" \
  "s_waitcnt lgkmcnt(3)\n\t" \
  "v_mfma_f32_16x16x32_bf16 v[8:11], v[208:211], v[178:181], v[8:11]\n\t" \
  "ds_read_b64_tr_b16 v[214:215], %[a2] offset:7760\n\t" \
  "v_cvt_pk_bf16_f32 v147, v146, v147\n\t" \
  "v_cvt_pk_bf16_f32 v155, v154, v155\n\t" \
  "v_cvt_pk_bf16_f32 v154, v152, v153\n\t" \
  "v_mfma_f32_16x16x32_bf16 v[32:35], v[208:211], v[186:189], v[32:35]\n\t" \
  "ds_read_b32 v229, v229 offset:0\n\t" \
  "v_rcp_f16 v233, v232\n\t" \
  "v_exp_f16 v234, v235\n\t" \
  "v_cvt_pk_f16_f32 v216, |v148|, |v149| clamp\n\t" \
  "v_mfma_f32_16x16x32_bf16 v[56:59], v[208:211], v[194:197], v[56:59]\n\t" \
  "ds_read_b32 v228, v228 offset:0\n\t" \
  "s_cbranch_vccnz .Lcnx_p9_1\n\ts_setprio 1\n.Lcnx_p9_1:\n\t" \
  "v_cvt_pk_f16_f32 v218, |v150|, |v151| clamp\n\t" \
  "v_cvt_pk_f16_f32 v220, |v156|, |v157| clamp\n\t" \
  "v_mfma_f32_16x16x32_bf16 v[80:83], v[208:211], v[202:205], v[80:83]\n\t" \
  "ds_read_b64_tr_b16 v[208:209], %[a2] offset:6280\n\t" \
  "v_cvt_pk_f16_f32 v222, |v158|, |v159| clamp\n\t" \
  "v_rcp_f16_sdwa v232, v232 dst_sel:DWORD dst_unused:UNUSED_PAD src0_sel:WORD_1\n\t" \
  "v_exp_f16_sdwa v234, v235 dst_sel:WORD_1 dst_unused:UNUSED_PRESERVE src0_sel:WORD_1\n\t" \
  "s_waitcnt lgkmcnt(3)\n\t" \
  "v_mfma_f32_16x16x32_bf16 v[12:15], v[212:215], v[178:181], v[12:15]\n\t" \
  "ds_read_b64_tr_b16 v[210:211], %[a2] offset:7816\n\t" \
  "v_and_b32_sdwa v217, %[mk], v216 dst_sel:DWORD dst_unused:UNUSED_PAD src0_sel:DWORD src1_sel:WORD_1\n\t" \
  "v_and_b32 v216, %[mk], v216\n\t" \
  "v_mfma_f32_16x16x32_bf16 v[36:39], v[212:215], v[186:189], v[36:39]\n\t" \
  "ds_read_b32 v217, v217 offset:0\n\t" \
  "v_and_b32_sdwa v219, %[mk], v218 dst_sel:DWORD dst_unused:UNUSED_PAD src0_sel:DWORD src1_sel:WORD_1\n\t" \
  "v_mfma_f32_16x16x32_bf16 v[60:63], v[212:215], v[194:197], v[60:63]\n\t" \
  "ds_read_b32 v216, v216 offset:0\n\t" \
  "v_and_b32 v218, %[mk], v218\n\t" \
  "v_and_b32_sdwa v221, %[mk], v220 dst_sel:DWORD dst_unused:UNUSED_PAD src0_sel:DWORD src1_sel:WORD_1\n\t" \
  "v_mfma_f32_16x16x32_bf16 v[84:87], v[212:215], v[202:205], v[84:87]\n\t" \
  "ds_read_b64_tr_b16 v[212:213], %[a2] offset:6288\n\t" \
  "v_and_b32 v220, %[mk], v220\n\t" \
  "s_waitcnt lgkmcnt(3)\n\t" \
  "v_mfma_f32_16x16x32_bf16 v[16:19], v[208:211], v[178:181], v[16:19]\n\t" \
  "ds_read_b64_tr_b16 v[214:215], %[a2] offset:7824\n\t" \
  "v_and_b32_sdwa v223, %[mk], v222 dst_sel:DWORD dst_unused:UNUSED_PAD src0_sel:DWORD src1_sel:WORD_1\n\t" \
  "v_mfma_f32_16x16x32_bf16 v[40:43], v[208:211], v[186:189], v[40:43]\n\t" \
  "ds_read_b32 v219, v219 offset:0\n\t" \
  "v_and_b32 v222, %[mk], v222\n\t" \
  "v_fma_f32 v163, |v163|, v225, v163 mul:4\n\t" \
  "v_mfma_f32_16x16x32_bf16 v[64:67], v[208:211], v[194:197], v[64:67]\n\t" \
  "ds_read_b32 v218, v218 offset:0\n\t" \
  "v_fma_f32 v162, |v162|, v224, v162 mul:4\n\t" \
  "v_fma_f32 v171, |v171|, v227, v171 mul:4\n\t" \
  "v_mfma_f32_16x16x32_bf16 v[88:91], v[208:211], v[202:205], v[88:91]\n\t" \
  "ds_read_b64_tr_b16 v[208:209], %[a1] offset:49192\n\t" \
  "ds_read_b64_tr_b16 v[210:211], %[a1] offset:50216\n\t" \
  "v_fma_f32 v170, |v170|, v226, v170 mul:4\n\t" \
  "v_fma_f32 v169, |v169|, v229, v169 mul:4\n\t" \
  "s_waitcnt lgkmcnt(4)\n\t" \
  "v_mfma_f32_16x16x32_bf16 v[20:23], v[212:215], v[178:181], v[20:23]\n\t" \
  "ds_read_b32 v221, v221 offset:0\n\t" \
  "v_fma_f32 v168, |v168|, v228, v168 mul:4\n\t" \
  "v_fma_mix_f32 v144, v144, v233, 0 op_sel_hi:[0,1,0]\n\t" \
  "v_fma_mix_f32 v145, v145, v232, 0 op_sel_hi:[0,1,0]\n\t" \
  "v_mfma_f32_16x16x32_bf16 v[44:47], v[212:215], v[186:189], v[44:47]\n\t" \
  "ds_read_b32 v220, v220 offset:0\n\t" \
  "v_pk_fma_f16 v234, v234, %[hh], %[hh]\n\t" \
  "v_cvt_pk_bf16_f32 v146, v144, v145\n\t" \
  "v_cvt_pk_bf16_f32 v163, v162, v163\n\t" \
  "v_mfma_f32_16x16x32_bf16 v[68:71], v[212:215], v[194:197], v[68:71]\n\t" \
  "ds_read_b32 v223, v223 offset:0\n\t" \
  "v_cvt_pk_bf16_f32 v171, v170, v171\n\t" \
  "v_cvt_pk_bf16_f32 v170, v168, v169\n\t" \
  "v_rcp_f16 v235, v234\n\t" \
  "v_mfma_f32_16x16x32_bf16 v[92:95], v[212:215], v[202:205], v[92:95]\n\t" \
  "ds_read_b64_tr_b16 v[212:213], %[a1] offset:52264\n\t" \
  "v_cvt_pk_f16_f32 v224, |v164|, |v165| clamp\n\t" \
  "s_cbranch_vccnz .Lcnx_p9_2\n\ts_setprio 2\n.Lcnx_p9_2:\n\t" \
  "v_cvt_pk_f16_f32 v226, |v166|, |v167| clamp\n\t" \
  "s_waitcnt lgkmcnt(4)\n\t" \
  "v_mfma_f32_16x16x32_bf16 v[176:179], v[208:211], v[96:99], 0\n\t" \
  "ds_read_b64_tr_b16 v[214:215], %[a1] offset:53288\n\t" \
  "v_cvt_pk_f16_f32 v228, |v172|, |v173| clamp\n\t" \
  "v_cvt_pk_f16_f32 v230, |v174|, |v175| clamp\n\t" \
  "v_rcp_f16_sdwa v234, v234 dst_sel:DWORD dst_unused:UNUSED_PAD src0_sel:WORD_1\n\t" \
  "v_mfma_f32_16x16x32_bf16 v[184:187], v[208:211], v[108:111], 0\n\t" \
  "ds_read_b32 v222, v222 offset:0\n\t" \
  "v_and_b32_sdwa v225, %[mk], v224 dst_sel:DWORD dst_unused:UNUSED_PAD src0_sel:DWORD src1_sel:WORD_1\n\t" \
  "v_and_b32 v224, %[mk], v224\n\t" \
  "v_mfma_f32_16x16x32_bf16 v[192:195], v[208:211], v[120:123], 0\n\t" \
  "ds_read_b32 v225, v225 offset:0\n\t" \
  "v_and_b32_sdwa v227, %[mk], v226 dst_sel:DWORD dst_unused:UNUSED_PAD src0_sel:DWORD src1_sel:WORD_1\n\t" \
  "v_mfma_f32_16x16x32_bf16 v[200:203], v[208:211], v[132:135], 0\n\t" \
  "ds_read_b64_tr_b16 v[208:209], %[a1] offset:51240\n\t" \
  "v_and_b32 v226, %[mk], v226\n\t" \
  "v_and_b32_sdwa v229, %[mk], v228 dst_sel:DWORD dst_unused:UNUSED_PAD src0_sel:DWORD src1_sel:WORD_1\n\t" \
  "s_waitcnt lgkmcnt(3)\n\t" \
  "v_mfma_f32_16x16x32_bf16 v[176:179], v[212:215], v[100:103], v[176:179]\n\t" \
  "ds_read_b64_tr_b16 v[210:211], %[a1] offset:54312\n\t" \
  "v_and_b32 v228, %[mk], v228\n\t" \
  "v_mfma_f32_16x16x32_bf16 v[184:187], v[212:215], v[112:115], v[184:187]\n\t" \
  "ds_read_b32 v224, v224 offset:0\n\t" \
  "v_and_b32_sdwa v231, %[mk], v230 dst_sel:DWORD dst_unused:UNUSED_PAD src0_sel:DWORD src1_sel:WORD_1\n\t" \
  "v_mfma_f32_16x16x32_bf16 v[192:195], v[212:215], v[124:127], v[192:195]\n\t" \
  "ds_read_b32 v227, v227 offset:0\n\t" \
  "v_and_b32 v230, %[mk], v230\n\t" \
  "v_fma_f32 v149, |v149|, v217, v149 mul:4\n\t" \
  "v_mfma_f32_16x16x32_bf16 v[200:203], v[212:215], v[136:139], v[200:203]\n\t" \
  "ds_read_b64_tr_b16 v[212:213], %[a1] offset:49704\n\t" \
  "ds_read_b64_tr_b16 v[214:215], %[a1] offset:50728\n\t" \
  "v_fma_f32 v148, |v148|, v216, v148 mul:4\n\t" \
  "v_fma_f32 v151, |v151|, v219, v151 mul:4\n\t" \
  "s_waitcnt lgkmcnt(4)\n\t" \
  "v_mfma_f32_16x16x32_bf16 v[176:179], v[208:211], v[104:107], v[176:179]\n\t" \
  "ds_read_b32 v226, v226 offset:0\n\t" \
  "v_fma_f32 v150, |v150|, v218, v150 mul:4\n\t" \
  "v_fma_f32 v157, |v157|, v221, v157 mul:4\n\t" \
  "v_mfma_f32_16x16x32_bf16 v[184:187], v[208:211], v[116:119], v[184:187]\n\t" \
  "ds_read_b32 v229, v229 offset:0\n\t" \
  "v_fma_f32 v156, |v156|, v220, v156 mul:4\n\t" \
  "v_fma_f32 v159, |v159|, v223, v159 mul:4\n\t" \
  "v_fma_f32 v158, |v158|, v222, v158 mul:4\n\t" \
  "v_mfma_f32_16x16x32_bf16 v[192:195], v[208:211], v[128:131], v[192:195]\n\t" \
  "ds_read_b32 v228, v228 offset:0\n\t" \
  "v_fma_mix_f32 v160, v160, v235, 0 op_sel_hi:[0,1,0]\n\t" \
  "v_fma_mix_f32 v161, v161, v234, 0 op_sel_hi:[0,1,0]\n\t" \
  "v_cvt_pk_bf16_f32 v162, v160, v161\n\t" \
  "v_mfma_f32_16x16x32_bf16 v[200:203], v[208:211], v[140:143], v[200:203]\n\t" \
  "ds_read_b64_tr_b16 v[208:209], %[a1] offset:52776\n\t" \
  "v_cvt_pk_bf16_f32 v148, v148, v149\n\t" \
  "v_cvt_pk_bf16_f32 v149, v150, v151\n\t" \
  "s_waitcnt lgkmcnt(4)\n\t" \
  "v_mfma_f32_16x16x32_bf16 v[180:183], v[212:215], v[96:99], 0\n\t" \
  "ds_read_b64_tr_b16 v[210:211], %[a1] offset:53800\n\t" \
  "v_cvt_pk_bf16_f32 v156, v156, v157\n\t" \
  "v_cvt_pk_bf16_f32 v157, v158, v159\n\t" \
  "s_cbranch_vccnz .Lcnx_p9_3\n\ts_setprio 3\n.Lcnx_p9_3:\n\t" \
  "v_cvt_pk_f16_f32 v216, |v178|, |v179| clamp\n\t" \
  "v_mfma_f32_16x16x32_bf16 v[188:191], v[212:215], v[108:111], 0\n\t" \
  "ds_read_b32 v231, v231 offset:0\n\t" \
  "v_cvt_pk_f16_f32 v232, v176, v177\n\t" \
  "v_cvt_pk_f16_f32 v218, |v186|, |v187| clamp\n\t" \
  "v_cvt_pk_f16_f32 v220, |v184|, |v185| clamp\n\t" \
  "v_mfma_f32_16x16x32_bf16 v[196:199], v[212:215], v[120:123], 0\n\t" \
  "ds_read_b32 v230, v230 offset:0\n\t" \
  "v_pk_mul_f16 v233, v232, v232\n\t" \
  "v_and_b32_sdwa v217, %[mk], v216 dst_sel:DWORD dst_unused:UNUSED_PAD src0_sel:DWORD src1_sel:WORD_1\n\t" \
  "v_mfma_f32_16x16x32_bf16 v[204:207], v[212:215], v[132:135], 0\n\t" \
  "ds_read_b64_tr_b16 v[212:213], %[a1] offset:51752\n\t" \
  "v_and_b32 v216, %[mk], v216\n\t" \
  "v_and_b32_sdwa v219, %[mk], v218 dst_sel:DWORD dst_unused:UNUSED_PAD src0_sel:DWORD src1_sel:WORD_1\n\t" \
  "s_waitcnt lgkmcnt(3)\n\t" \
  "v_mfma_f32_16x16x32_bf16 v[180:183], v[208:211], v[100:103], v[180:183]\n\t" \
  "ds_read_b64_tr_b16 v[214:215], %[a1] offset:54824\n\t" \
  "v_and_b32 v218, %[mk], v218\n\t" \
  "v_mfma_f32_16x16x32_bf16 v[188:191], v[208:211], v[112:115], v[188:191]\n\t" \
  "s_waitcnt vmcnt(1)\n\t" \
  "s_cmp_lg_u32 %[rnd], 1\n\t" \
  "s_cbranch_scc1 .Lcnx_c17\n\t" \
  "s_waitcnt vmcnt(0)\n\t" \
  ".Lcnx_c17:\n\t" \
  "s_mov_b64 exec, 3\n\t" \
  "ds_add_rtn_u32 v246, %[l8], %[vo] offset:47728\n\t" \
  "s_mov_b64 exec, -1\n\t" \
  "ds_read_b32 v217, v217 offset:0\n\t" \
  "v_and_b32_sdwa v221, %[mk], v220 dst_sel:DWORD dst_unused:UNUSED_PAD src0_sel:DWORD src1_sel:WORD_1\n\t" \
  "v_mfma_f32_16x16x32_bf16 v[196:199], v[208:211], v[124:127], v[196:199]\n\t" \
  "ds_read_b32 v216, v216 offset:0\n\t" \
  "v_and_b32 v220, %[mk], v220\n\t" \
  "v_pk_fma_f16 v233, v233, %[h2], %[h1]\n\t" \
  "v_mfma_f32_16x16x32_bf16 v[204:207], v[208:211], v[136:139], v[204:207]\n\t" \
  "s_waitcnt lgkmcnt(2)\n\t" \
  "v_readfirstlane_b32 %[t0], v246\n\t" \
  "s_cmp_lt_u32 %[t0], %[tg]\n\t" \
  "s_cbranch_scc1 .Lcnx_w108\n.Lcnx_b108:\n\t" \
  "v_add_u32 %[a1], %[d1], %[a1]\n\t" \
  "v_add_u32 %[a2], %[d2], %[a2]\n\t" \
  "ds_read_b64_tr_b16 v[208:209], %[a2] offset:24600\n\t" \
  "v_pk_mul_f16 v233, v232, v233\n\t" \
  "v_fma_f32 v165, |v165|, v225, v165 mul:4\n\t" \
  "v_mfma_f32_16x16x32_bf16 v[180:183], v[212:215], v[104:107], v[180:183]\n\t" \
  "ds_read_b64_tr_b16 v[210:211], %[a2] offset:26136\n\t" \
  "v_fma_f32 v164, |v164|, v224, v164 mul:4\n\t" \
  "v_fma_f32 v167, |v167|, v227, v167 mul:4\n\t" \
  "v_mfma_f32_16x16x32_bf16 v[188:191], v[212:215], v[116:119], v[188:191]\n\t" \
  "s_cmp_eq_u32 %[grp], 0\n\t" \
  "s_cbranch_scc1 .Lcnx_f10\n.Lcnx_g10:\n\t" \
  "ds_read_b32 v219, v219 offset:0\n\t" \
  "v_fma_f32 v166, |v166|, v226, v166 mul:4\n\t" \
  "v_fma_f32 v173, |v173|, v229, v173 mul:4\n\t" \
  "v_fma_f32 v172, |v172|, v228, v172 mul:4\n\t" \
  "v_mfma_f32_16x16x32_bf16 v[196:199], v[212:215], v[128:131], v[196:199]\n\t" \
  "ds_read_b32 v218, v218 offset:0\n\t" \
  "v_fma_f32 v175, |v175|, v231, v175 mul:4\n\t" \
  "v_fma_f32 v174, |v174|, v230, v174 mul:4\n\t" \
  "v_cvt_pk_bf16_f32 v164, v164, v165\n\t" \
  "v_mfma_f32_16x16x32_bf16 v[204:207], v[212:215], v[140:143], v[204:207]\n\t" \
  "ds_read_b64_tr_b16 v[212:213], %[a2] offset:24608\n\t" \
  "v_cvt_pk_bf16_f32 v165, v166, v167\n\t" \
  "v_cvt_pk_bf16_f32 v172, v172, v173\n\t" \
  "v_cvt_pk_bf16_f32 v173, v174, v175\n\t" \
  "s_cbranch_vccnz .Lcnx_p10_0\n\ts_setprio 0\n.Lcnx_p10_0:\n\t" \
  "v_exp_f16 v232, v233\n\t" \
  "v_cvt_pk_f16_f32 v224, |v194|, |v195| clamp\n\t" \
  "v_mfma_f32_16x16x32_bf16 v[64:67], v[242:245], v[236:239], v[64:67]\n\t" \
  "ds_read_b64_tr_b16 v[214:215], %[a2] offset:26144\n\t" \
  "v_cvt_pk_f16_f32 v234, v192, v193\n\t" \
  "v_cvt_pk_f16_f32 v226, |v202|, |v203| clamp\n\t" \
  "v_mfma_f32_16x16x32_bf16 v[68:71], v[240:243], v[236:239], v[68:71]\n\t" \
  "buffer_load_dwordx4 v[236:239], %[vo], %[xrs], %[oc1] offen offset:192\n\t" \
  "s_cmp_lg_u32 %[rnd], 0\n\t" \
  "s_cbranch_scc1 .Lcnx_c29\n\t" \
  "s_add_u32 m0, %[xl], 65600\n\t" \
  "s_add_u32 %[xo], %[xo], %[rs]\n\t" \
  "buffer_load_dwordx4 %[xv], %[xrs], %[xo] offen lds\n\t" \
  "s_cmp_lg_u32 %[wq], 6\n\t" \
  "s_cbranch_scc1 .Lcnx_c29\n\t" \
  "s_mov_b32 m0, %[pf]\n\t" \
  "s_add_u32 %[wb], %[wb], %[rs4]\n\t" \
  "buffer_load_dword %[wv], %[xrs], %[wb] offen lds\n\t" \
  ".Lcnx_c29:\n\t" \
  "ds_read_b32 v221, v221 offset:0\n\t" \
  "v_cvt_pk_f16_f32 v228, |v200|, |v201| clamp\n\t" \
  "v_exp_f16_sdwa v232, v233 dst_sel:WORD_1 dst_unused:UNUSED_PRESERVE src0_sel:WORD_1\n\t" \
  "v_pk_mul_f16 v235, v234, v234\n\t" \
  "s_waitcnt lgkmcnt(5)\n\t" \
  "v_mfma_f32_16x16x32_bf16 v[0:3], v[208:211], v[146:149], v[0:3]\n\t" \
  "ds_read_b32 v220, v220 offset:0\n\t" \
  "v_and_b32_sdwa v225, %[mk], v224 dst_sel:DWORD dst_unused:UNUSED_PAD src0_sel:DWORD src1_sel:WORD_1\n\t" \
  "v_and_b32 v224, %[mk], v224\n\t" \
  "v_mfma_f32_16x16x32_bf16 v[24:27], v[208:211], v[154:157], v[24:27]\n\t" \
  "ds_read_b32 v225, v225 offset:0\n\t" \
  "v_and_b32_sdwa v227, %[mk], v226 dst_sel:DWORD dst_unused:UNUSED_PAD src0_sel:DWORD src1_sel:WORD_1\n\t" \
  "v_mfma_f32_16x16x32_bf16 v[48:51], v[208:211], v[162:165], v[48:51]\n\t" \
  "ds_read_b32 v224, v224 offset:0\n\t" \
  "v_and_b32 v226, %[mk], v226\n\t" \
  "v_and_b32_sdwa v229, %[mk], v228 dst_sel:DWORD dst_unused:UNUSED_PAD src0_sel:DWORD src1_sel:WORD_1\n\t" \
  "v_mfma_f32_16x16x32_bf16 v[72:75], v[208:211], v[170:173], v[72:75]\n\t" \
  "ds_read_b64_tr_b16 v[208:209], %[a2] offset:24664\n\t" \
  "v_and_b32 v228, %[mk], v228\n\t" \
  "s_waitcnt lgkmcnt(5)\n\t" \
  "v_mfma_f32_16x16x32_bf16 v[4:7], v[212:215], v[146:149], v[4:7]\n\t" \
  "ds_read_b64_tr_b16 v[210:211], %[a2] offset:26200\n\t" \
  "v_pk_fma_f16 v235, v235, %[h2], %[h1]\n\t" \
  "v_mfma_f32_16x16x32_bf16 v[28:31], v[212:215], v[154:157], v[28:31]\n\t" \
  "ds_read_b32 v227, v227 offset:0\n\t" \
  "v_pk_mul_f16 v235, v234, v235\n\t" \
  "v_fma_f32 v179, |v179|, v217, v179 mul:4\n\t" \
  "v_fma_f32 v178, |v178|, v216, v178 mul:4\n\t" \
  "v_mfma_f32_16x16x32_bf16 v[52:55], v[212:215], v[162:165], v[52:55]\n\t" \
  "ds_read_b32 v226, v226 offset:0\n\t" \
  "v_fma_f32 v187, |v187|, v219, v187 mul:4\n\t" \
  "s_waitcnt lgkmcnt(6)\n\t" \
  "v_fma_f32 v186, |v186|, v218, v186 mul:4\n\t" \
  "v_fma_f32 v185, |v185|, v221, v185 mul:4\n\t" \
  "v_mfma_f32_16x16x32_bf16 v[76:79], v[212:215], v[170:173], v[76:79]\n\t" \
  "ds_read_b64_tr_b16 v[212:213], %[a2] offset:24672\n\t" \
  "v_fma_f32 v184, |v184|, v220, v184 mul:4\n\t" \
  "v_pk_fma_f16 v232, v232, %[hh], %[hh]\n\t" \
  "s_waitcnt lgkmcnt(3)\n\t" \
  "v_mfma_f32_16x16x32_bf16 v[8:11], v[208:211], v[146:149], v[8:11]\n\t" \
  "ds_read_b64_tr_b16 v[214:215], %[a2] offset:26208\n\t" \
  "v_cvt_pk_bf16_f32 v179, v178, v179\n\t" \
  "v_cvt_pk_bf16_f32 v187, v186, v187\n\t" \
  "v_cvt_pk_bf16_f32 v186, v184, v185\n\t" \
  "v_mfma_f32_16x16x32_bf16 v[32:35], v[208:211], v[154:157], v[32:35]\n\t" \
  "ds_read_b32 v229, v229 offset:0\n\t" \
  "v_rcp_f16 v233, v232\n\t" \
  "v_exp_f16 v234, v235\n\t" \
  "v_cvt_pk_f16_f32 v216, |v180|, |v181| clamp\n\t" \
  "v_mfma_f32_16x16x32_bf16 v[56:59], v[208:211], v[162:165], v[56:59]\n\t" \
  "ds_read_b32 v228, v228 offset:0\n\t" \
  "s_cbranch_vccnz .Lcnx_p10_1\n\ts_setprio 1\n.Lcnx_p10_1:\n\t" \
  "v_cvt_pk_f16_f32 v218, |v182|, |v183| clamp\n\t" \
  "v_cvt_pk_f16_f32 v220, |v188|, |v189| clamp\n\t" \
  "v_mfma_f32_16x16x32_bf16 v[80:83], v[208:211], v[170:173], v[80:83]\n\t" \
  "ds_read_b64_tr_b16 v[208:209], %[a2] offset:24728\n\t" \
  "v_cvt_pk_f16_f32 v222, |v190|, |v191| clamp\n\t" \
  "v_rcp_f16_sdwa v232, v232 dst_sel:DWORD dst_unused:UNUSED_PAD src0_sel:WORD_1\n\t" \
  "v_exp_f16_sdwa v234, v235 dst_sel:WORD_1 dst_unused:UNUSED_PRESERVE src0_sel:WORD_1\n\t" \
  "s_waitcnt lgkmcnt(3)\n\t" \
  "v_mfma_f32_16x16x32_bf16 v[12:15], v[212:215], v[146:149], v[12:15]\n\t" \
  "ds_read_b64_tr_b16 v[210:211], %[a2] offset:26264\n\t" \
  "v_and_b32_sdwa v217, %[mk], v216 dst_sel:DWORD dst_unused:UNUSED_PAD src0_sel:DWORD src1_sel:WORD_1\n\t" \
  "v_and_b32 v216, %[mk], v216\n\t" \
  "v_mfma_f32_16x16x32_bf16 v[36:39], v[212:215], v[154:157], v[36:39]\n\t" \
  "ds_read_b32 v217, v217 offset:0\n\t" \
  "v_and_b32_sdwa v219, %[mk], v218 dst_sel:DWORD dst_unused:UNUSED_PAD src0_sel:DWORD src1_sel:WORD_1\n\t" \
  "v_mfma_f32_16x16x32_bf16 v[60:63], v[212:215], v[162:165], v[60:63]\n\t" \
  "ds_read_b32 v216, v216 offset:0\n\t" \
  "v_and_b32 v218, %[mk], v218\n\t" \
  "v_and_b32_sdwa v221, %[mk], v220 dst_sel:DWORD dst_unused:UNUSED_PAD src0_sel:DWORD src1_sel:WORD_1\n\t" \
  "v_mfma_f32_16x16x32_bf16 v[84:87], v[212:215], v[170:173], v[84:87]\n\t" \
  "ds_read_b64_tr_b16 v[212:213], %[a2] offset:24736\n\t" \
  "v_and_b32 v220, %[mk], v220\n\t" \
  "s_waitcnt lgkmcnt(3)\n\t" \
  "v_mfma_f32_16x16x32_bf16 v[16:19], v[208:211], v[146:149], v[16:19]\n\t" \
  "ds_read_b64_tr_b16 v[214:215], %[a2] offset:26272\n\t" \
  "v_and_b32_sdwa v223, %[mk], v222 dst_sel:DWORD dst_unused:UNUSED_PAD src0_sel:DWORD src1_sel:WORD_1\n\t" \
  "v_mfma_f32_16x16x32_bf16 v[40:43], v[208:211], v[154:157], v[40:43]\n\t" \
  "ds_read_b32 v219, v219 offset:0\n\t" \
  "v_and_b32 v222, %[mk], v222\n\t" \
  "v_fma_f32 v195, |v195|, v225, v195 mul:4\n\t" \
  "v_mfma_f32_16x16x32_bf16 v[64:67], v[208:211], v[162:165], v[64:67]\n\t" \
  "ds_read_b32 v218, v218 offset:0\n\t" \
  "v_fma_f32 v194, |v194|, v224, v194 mul:4\n\t" \
  "v_fma_f32 v203, |v203|, v227, v203 mul:4\n\t" \
  "v_mfma_f32_16x16x32_bf16 v[88:91], v[208:211], v[170:173], v[88:91]\n\t" \
  "ds_read_b64_tr_b16 v[208:209], %[a1] offset:36904\n\t" \
  "ds_read_b64_tr_b16 v[210:211], %[a1] offset:37928\n\t" \
  "v_fma_f32 v202, |v202|, v226, v202 mul:4\n\t" \
  "v_fma_f32 v201, |v201|, v229, v201 mul:4\n\t" \
  "s_waitcnt lgkmcnt(4)\n\t" \
  "v_mfma_f32_16x16x32_bf16 v[20:23], v[212:215], v[146:149], v[20:23]\n\t" \
  "ds_read_b32 v221, v221 offset:0\n\t" \
  "v_fma_f32 v200, |v200|, v228, v200 mul:4\n\t" \
  "v_fma_mix_f32 v176, v176, v233, 0 op_sel_hi:[0,1,0]\n\t" \
  "v_fma_mix_f32 v177, v177, v232, 0 op_sel_hi:[0,1,0]\n\t" \
  "v_mfma_f32_16x16x32_bf16 v[44:47], v[212:215], v[154:157], v[44:47]\n\t" \
  "ds_read_b32 v220, v220 offset:0\n\t" \
  "v_pk_fma_f16 v234, v234, %[hh], %[hh]\n\t" \
  "v_cvt_pk_bf16_f32 v178, v176, v177\n\t" \
  "v_cvt_pk_bf16_f32 v195, v194, v195\n\t" \
  "v_mfma_f32_16x16x32_bf16 v[68:71], v[212:215], v[162:165], v[68:71]\n\t" \
  "ds_read_b32 v223, v223 offset:0\n\t" \
  "v_cvt_pk_bf16_f32 v203, v202, v203\n\t" \
  "v_cvt_pk_bf16_f32 v202, v200, v201\n\t" \
  "v_rcp_f16 v235, v234\n\t" \
  "v_mfma_f32_16x16x32_bf16 v[92:95], v[212:215], v[170:173], v[92:95]\n\t" \
  "ds_read_b64_tr_b16 v[212:213], %[a1] offset:39976\n\t" \
  "v_cvt_pk_f16_f32 v224, |v196|, |v197| clamp\n\t" \
  "s_cbranch_vccnz .Lcnx_p10_2\n\ts_setprio 2\n.Lcnx_p10_2:\n\t" \
  "v_cvt_pk_f16_f32 v226, |v198|, |v199| clamp\n\t" \
  "s_waitcnt lgkmcnt(4)\n\t" \
  "v_mfma_f32_16x16x32_bf16 v[144:147], v[208:211], v[96:99], 0\n\t" \
  "ds_read_b64_tr_b16 v[214:215], %[a1] offset:41000\n\t" \
  "v_cvt_pk_f16_f32 v228, |v204|, |v205| clamp\n\t" \
  "v_cvt_pk_f16_f32 v230, |v206|, |v207| clamp\n\t" \
  "v_rcp_f16_sdwa v234, v234 dst_sel:DWORD dst_unused:UNUSED_PAD src0_sel:WORD_1\n\t" \
  "v_mfma_f32_16x16x32_bf16 v[152:155], v[208:211], v[108:111], 0\n\t" \
  "ds_read_b32 v222, v222 offset:0\n\t" \
  "v_and_b32_sdwa v225, %[mk], v224 dst_sel:DWORD dst_unused:UNUSED_PAD src0_sel:DWORD src1_sel:WORD_1\n\t" \
  "v_and_b32 v224, %[mk], v224\n\t" \
  "v_mfma_f32_16x16x32_bf16 v[160:163], v[208:211], v[120:123], 0\n\t" \
  "ds_read_b32 v225, v225 offset:0\n\t" \
  "v_and_b32_sdwa v227, %[mk], v226 dst_sel:DWORD dst_unused:UNUSED_PAD src0_sel:DWORD src1_sel:WORD_1\n\t" \
  "v_mfma_f32_16x16x32_bf16 v[168:171], v[208:211], v[132:135], 0\n\t" \
  "ds_read_b64_tr_b16 v[208:209], %[a1] offset:38952\n\t" \
  "v_and_b32 v226, %[mk], v226\n\t" \
  "v_and_b32_sdwa v229, %[mk], v228 dst_sel:DWORD dst_unused:UNUSED_PAD src0_sel:DWORD src1_sel:WORD_1\n\t" \
  "s_waitcnt lgkmcnt(3)\n\t" \
  "v_mfma_f32_16x16x32_bf16 v[144:147], v[212:215], v[100:103], v[144:147]\n\t" \
  "ds_read_b64_tr_b16 v[210:211], %[a1] offset:42024\n\t" \
  "v_and_b32 v228, %[mk], v228\n\t" \
  "v_mfma_f32_16x16x32_bf16 v[152:155], v[212:215], v[112:115], v[152:155]\n\t" \
  "ds_read_b32 v224, v224 offset:0\n\t" \
  "v_and_b32_sdwa v231, %[mk], v230 dst_sel:DWORD dst_unused:UNUSED_PAD src0_sel:DWORD src1_sel:WORD_1\n\t" \
  "v_mfma_f32_16x16x32_bf16 v[160:163], v[212:215], v[124:127], v[160:163]\n\t" \
  "ds_read_b32 v227, v227 offset:0\n\t" \
  "v_and_b32 v230, %[mk], v230\n\t" \
  "v_fma_f32 v181, |v181|, v217, v181 mul:4\n\t" \
  "v_mfma_f32_16x16x32_bf16 v[168:171], v[212:215], v[136:139], v[168:171]\n\t" \
  "ds_read_b64_tr_b16 v[212:213], %[a1] offset:37416\n\t" \
  "ds_read_b64_tr_b16 v[214:215], %[a1] offset:38440\n\t" \
  "v_fma_f32 v180, |v180|, v216, v180 mul:4\n\t" \
  "v_fma_f32 v183, |v183|, v219, v183 mul:4\n\t" \
  "s_waitcnt lgkmcnt(4)\n\t" \
  "v_mfma_f32_16x16x32_bf16 v[144:147], v[208:211], v[104:107], v[144:147]\n\t" \
  "ds_read_b32 v226, v226 offset:0\n\t" \
  "v_fma_f32 v182, |v182|, v218, v182 mul:4\n\t" \
  "v_fma_f32 v189, |v189|, v221, v189 mul:4\n\t" \
  "v_mfma_f32_16x16x32_bf16 v[152:155], v[208:211], v[116:119], v[152:155]\n\t" \
  "ds_read_b32 v229, v229 offset:0\n\t" \
  "v_fma_f32 v188, |v188|, v220, v188 mul:4\n\t" \
  "v_fma_f32 v191, |v191|, v223, v191 mul:4\n\t" \
  "v_fma_f32 v190, |v190|, v222, v190 mul:4\n\t" \
  "v_mfma_f32_16x16x32_bf16 v[160:163], v[208:211], v[128:131], v[160:163]\n\t" \
  "ds_read_b32 v228, v228 offset:0\n\t" \
  "v_fma_mix_f32 v192, v192, v235, 0 op_sel_hi:[0,1,0]\n\t" \
  "v_fma_mix_f32 v193, v193, v234, 0 op_sel_hi:[0,1,0]\n\t" \
  "v_cvt_pk_bf16_f32 v194, v192, v193\n\t" \
  "v_mfma_f32_16x16x32_bf16 v[168:171], v[208:211], v[140:143], v[168:171]\n\t" \
  "ds_read_b64_tr_b16 v[208:209], %[a1] offset:40488\n\t" \
  "v_cvt_pk_bf16_f32 v180, v180, v181\n\t" \
  "v_cvt_pk_bf16_f32 v181, v182, v183\n\t" \
  "s_waitcnt lgkmcnt(4)\n\t" \
  "v_mfma_f32_16x16x32_bf16 v[148:151], v[212:215], v[96:99], 0\n\t" \
  "ds_read_b64_tr_b16 v[210:211], %[a1] offset:41512\n\t" \
  "v_cvt_pk_bf16_f32 v188, v188, v189\n\t" \
  "v_cvt_pk_bf16_f32 v189, v190, v191\n\t" \
  "s_cbranch_vccnz .Lcnx_p10_3\n\ts_setprio 3\n.Lcnx_p10_3:\n\t" \
  "v_cvt_pk_f16_f32 v216, |v146|, |v147| clamp\n\t" \
  "v_mfma_f32_16x16x32_bf16 v[156:159], v[212:215], v[108:111], 0\n\t" \
  "ds_read_b32 v231, v231 offset:0\n\t" \
  "v_cvt_pk_f16_f32 v232, v144, v145\n\t" \
  "v_cvt_pk_f16_f32 v218, |v154|, |v155| clamp\n\t" \
  "v_cvt_pk_f16_f32 v220, |v152|, |v153| clamp\n\t" \
  "v_mfma_f32_16x16x32_bf16 v[164:167], v[212:215], v[120:123], 0\n\t" \
  "ds_read_b32 v230, v230 offset:0\n\t" \
  "v_pk_mul_f16 v233, v232, v232\n\t" \
  "v_and_b32_sdwa v217, %[mk], v216 dst_sel:DWORD dst_unused:UNUSED_PAD src0_sel:DWORD src1_sel:WORD_1\n\t" \
  "v_mfma_f32_16x16x32_bf16 v[172:175], v[212:215], v[132:135], 0\n\t" \
  "ds_read_b64_tr_b16 v[212:213], %[a1] offset:39464\n\t" \
  "v_and_b32 v216, %[mk], v216\n\t" \
  "v_and_b32_sdwa v219, %[mk], v218 dst_sel:DWORD dst_unused:UNUSED_PAD src0_sel:DWORD src1_sel:WORD_1\n\t" \
  "s_waitcnt lgkmcnt(3)\n\t" \
  "v_mfma_f32_16x16x32_bf16 v[148:151], v[208:211], v[100:103], v[148:151]\n\t" \
  "ds_read_b64_tr_b16 v[214:215], %[a1] offset:42536\n\t" \
  "v_and_b32 v218, %[mk], v218\n\t" \
  "v_mfma_f32_16x16x32_bf16 v[156:159], v[208:211], v[112:115], v[156:159]\n\t" \
  "s_waitcnt vmcnt(1)\n\t" \
  "s_cmp_lg_u32 %[rnd], 1\n\t" \
  "s_cbranch_scc1 .Lcnx_c18\n\t" \
  "s_waitcnt vmcnt(0)\n\t" \
  ".Lcnx_c18:\n\t" \
  "s_mov_b64 exec, 3\n\t" \
  "ds_add_rtn_u32 v246, %[l8], %[vo] offset:47736\n\t" \
  "s_mov_b64 exec, -1\n\t" \
  "ds_read_b32 v217, v217 offset:0\n\t" \
  "v_and_b32_sdwa v221, %[mk], v220 dst_sel:DWORD dst_unused:UNUSED_PAD src0_sel:DWORD src1_sel:WORD_1\n\t" \
  "v_mfma_f32_16x16x32_bf16 v[164:167], v[208:211], v[124:127], v[164:167]\n\t" \
  "ds_read_b32 v216, v216 offset:0\n\t" \
  "v_and_b32 v220, %[mk], v220\n\t" \
  "v_pk_fma_f16 v233, v233, %[h2], %[h1]\n\t" \
  "v_mfma_f32_16x16x32_bf16 v[172:175], v[208:211], v[136:139], v[172:175]\n\t" \
  "s_waitcnt lgkmcnt(2)\n\t" \
  "v_readfirstlane_b32 %[t0], v246\n\t" \
  "s_cmp_lt_u32 %[t0], %[tg]\n\t" \
  "s_cbranch_scc1 .Lcnx_w120\n.Lcnx_b120:\n\t" \
  "ds_read_b64_tr_b16 v[208:209], %[a2] offset:30752\n\t" \
  "v_pk_mul_f16 v233, v232, v233\n\t" \
  "v_fma_f32 v197, |v197|, v225, v197 mul:4\n\t" \
  "v_mfma_f32_16x16x32_bf16 v[148:151], v[212:215], v[104:107], v[148:151]\n\t" \
  "ds_read_b64_tr_b16 v[210:211], %[a2] offset:32288\n\t" \
  "v_fma_f32 v196, |v196|, v224, v196 mul:4\n\t" \
  "v_fma_f32 v199, |v199|, v227, v199 mul:4\n\t" \
  "v_mfma_f32_16x16x32_bf16 v[156:159], v[212:215], v[116:119], v[156:159]\n\t" \
  "s_cmp_eq_u32 %[grp], 0\n\t" \
  "s_cbranch_scc1 .Lcnx_f11\n.Lcnx_g11:\n\t" \
  "ds_read_b32 v219, v219 offset:0\n\t" \
  "v_fma_f32 v198, |v198|, v226, v198 mul:4\n\t" \
  "v_fma_f32 v205, |v205|, v229, v205 mul:4\n\t" \
  "v_fma_f32 v204, |v204|, v228, v204 mul:4\n\t" \
  "v_mfma_f32_16x16x32_bf16 v[164:167], v[212:215], v[128:131], v[164:167]\n\t" \
  "ds_read_b32 v218, v218 offset:0\n\t" \
  "v_fma_f32 v207, |v207|, v231, v207 mul:4\n\t" \
  "v_fma_f32 v206, |v206|, v230, v206 mul:4\n\t" \
  "v_cvt_pk_bf16_f32 v196, v196, v197\n\t" \
  "v_mfma_f32_16x16x32_bf16 v[172:175], v[212:215], v[140:143], v[172:175]\n\t" \
  "ds_read_b64_tr_b16 v[212:213], %[a2] offset:30760\n\t" \
  "v_cvt_pk_bf16_f32 v197, v198, v199\n\t" \
  "v_cvt_pk_bf16_f32 v204, v204, v205\n\t" \
  "v_cvt_pk_bf16_f32 v205, v206, v207\n\t" \
  "s_cbranch_vccnz .Lcnx_p11_0\n\ts_setprio 0\n.Lcnx_p11_0:\n\t" \
  "v_exp_f16 v232, v233\n\t" \
  "v_cvt_pk_f16_f32 v224, |v162|, |v163| clamp\n\t" \
  "v_mfma_f32_16x16x32_bf16 v[72:75], v[242:245], v[236:239], v[72:75]\n\t" \
  "ds_read_b64_tr_b16 v[214:215], %[a2] offset:32296\n\t" \
  "v_cvt_pk_f16_f32 v234, v160, v161\n\t" \
  "v_cvt_pk_f16_f32 v226, |v170|, |v171| clamp\n\t" \
  "v_mfma_f32_16x16x32_bf16 v[76:79], v[240:243], v[236:239], v[76:79]\n\t" \
  "buffer_load_dwordx4 v[236:239], %[vo], %[xrs], %[oc1] offen offset:256\n\t" \
  "s_cmp_lg_u32 %[wq], 7\n\t" \
  "s_cbranch_scc1 .Lcnx_c30\n\t" \
  "s_mov_b32 m0, %[pf]\n\t" \
  "s_add_u32 %[wb], %[wb], %[rs4]\n\t" \
  "buffer_load_dword %[wv], %[xrs], %[wb] offen lds\n\t" \
  ".Lcnx_c30:\n\t" \
  "ds_read_b32 v221, v221 offset:0\n\t" \
  "v_cvt_pk_f16_f32 v228, |v168|, |v169| clamp\n\t" \
  "v_exp_f16_sdwa v232, v233 dst_sel:WORD_1 dst_unused:UNUSED_PRESERVE src0_sel:WORD_1\n\t" \
  "v_pk_mul_f16 v235, v234, v234\n\t" \
  "s_waitcnt lgkmcnt(5)\n\t" \
  "v_mfma_f32_16x16x32_bf16 v[0:3], v[208:211], v[178:181], v[0:3]\n\t" \
  "ds_read_b32 v220, v220 offset:0\n\t" \
  "v_and_b32_sdwa v225, %[mk], v224 dst_sel:DWORD dst_unused:UNUSED_PAD src0_sel:DWORD src1_sel:WORD_1\n\t" \
  "v_and_b32 v224, %[mk], v224\n\t" \
  "v_mfma_f32_16x16x32_bf16 v[24:27], v[208:211], v[186:189], v[24:27]\n\t" \
  "ds_read_b32 v225, v225 offset:0\n\t" \
  "v_and_b32_sdwa v227, %[mk], v226 dst_sel:DWORD dst_unused:UNUSED_PAD src0_sel:DWORD src1_sel:WORD_1\n\t" \
  "v_mfma_f32_16x16x32_bf16 v[48:51], v[208:211], v[194:197], v[48:51]\n\t" \
  "ds_read_b32 v224, v224 offset:0\n\t" \
  "v_and_b32 v226, %[mk], v226\n\t" \
  "v_and_b32_sdwa v229, %[mk], v228 dst_sel:DWORD dst_unused:UNUSED_PAD src0_sel:DWORD src1_sel:WORD_1\n\t" \
  "v_mfma_f32_16x16x32_bf16 v[72:75], v[208:211], v[202:205], v[72:75]\n\t" \
  "ds_read_b64_tr_b16 v[208:209], %[a2] offset:30816\n\t" \
  "v_and_b32 v228, %[mk], v228\n\t" \
  "s_waitcnt lgkmcnt(5)\n\t" \
  "v_mfma_f32_16x16x32_bf16 v[4:7], v[212:215], v[178:181], v[4:7]\n\t" \
  "ds_read_b64_tr_b16 v[210:211], %[a2] offset:32352\n\t" \
  "v_pk_fma_f16 v235, v235, %[h2], %[h1]\n\t" \
  "v_mfma_f32_16x16x32_bf16 v[28:31], v[212:215], v[186:189], v[28:31]\n\t" \
  "ds_read_b32 v227, v227 offset:0\n\t" \
  "v_pk_mul_f16 v235, v234, v235\n\t" \
  "v_fma_f32 v147, |v147|, v217, v147 mul:4\n\t" \
  "v_fma_f32 v146, |v146|, v216, v146 mul:4\n\t" \
  "v_mfma_f32_16x16x32_bf16 v[52:55], v[212:215], v[194:197], v[52:55]\n\t" \
  "ds_read_b32 v226, v226 offset:0\n\t" \
  "v_fma_f32 v155, |v155|, v219, v155 mul:4\n\t" \
  "s_waitcnt lgkmcnt(6)\n\t" \
  "v_fma_f32 v154, |v154|, v218, v154 mul:4\n\t" \
  "v_fma_f32 v153, |v153|, v221, v153 mul:4\n\t" \
  "v_mfma_f32_16x16x32_bf16 v[76:79], v[212:215], v[202:205], v[76:79]\n\t" \
  "ds_read_b64_tr_b16 v[212:213], %[a2] offset:30824\n\t" \
  "v_fma_f32 v152, |v152|, v220, v152 mul:4\n\t" \
  "v_pk_fma_f16 v232, v232, %[hh], %[hh]\n\t" \
  "s_waitcnt lgkmcnt(3)\n\t" \
  "v_mfma_f32_16x16x32_bf16 v[8:11], v[208:211], v[178:181], v[8:11]\n\t" \
  "ds_read_b64_tr_b16 v[214:215], %[a2] offset:32360\n\t" \
  "v_cvt_pk_bf16_f32 v147, v146, v147\n\t" \
  "v_cvt_pk_bf16_f32 v155, v154, v155\n\t" \
  "v_cvt_pk_bf16_f32 v154, v152, v153\n\t" \
  "v_mfma_f32_16x16x32_bf16 v[32:35], v[208:211], v[186:189], v[32:35]\n\t" \
  "ds_read_b32 v229, v229 offset:0\n\t" \
  "v_rcp_f16 v233, v232\n\t" \
  "v_exp_f16 v234, v235\n\t" \
  "v_cvt_pk_f16_f32 v216, |v148|, |v149| clamp\n\t" \
  "v_mfma_f32_16x16x32_bf16 v[56:59], v[208:211], v[194:197], v[56:59]\n\t" \
  "ds_read_b32 v228, v228 offset:0\n\t" \
  "s_cbranch_vccnz .Lcnx_p11_1\n\ts_setprio 1\n.Lcnx_p11_1:\n\t" \
  "v_cvt_pk_f16_f32 v218, |v150|, |v151| clamp\n\t" \
  "v_cvt_pk_f16_f32 v220, |v156|, |v157| clamp\n\t" \
  "v_mfma_f32_16x16x32_bf16 v[80:83], v[208:211], v[202:205], v[80:83]\n\t" \
  "ds_read_b64_tr_b16 v[208:209], %[a2] offset:30880\n\t" \
  "v_cvt_pk_f16_f32 v222, |v158|, |v159| clamp\n\t" \
  "v_rcp_f16_sdwa v232, v232 dst_sel:DWORD dst_unused:UNUSED_PAD src0_sel:WORD_1\n\t" \
  "v_exp_f16_sdwa v234, v235 dst_sel:WORD_1 dst_unused:UNUSED_PRESERVE src0_sel:WORD_1\n\t" \
  "s_waitcnt lgkmcnt(3)\n\t" \
  "v_mfma_f32_16x16x32_bf16 v[12:15], v[212:215], v[178:181], v[12:15]\n\t" \
  "ds_read_b64_tr_b16 v[210:211], %[a2] offset:32416\n\t" \
  "v_and_b32_sdwa v217, %[mk], v216 dst_sel:DWORD dst_unused:UNUSED_PAD src0_sel:DWORD src1_sel:WORD_1\n\t" \
  "v_and_b32 v216, %[mk], v216\n\t" \
  "v_mfma_f32_16x16x32_bf16 v[36:39], v[212:215], v[186:189], v[36:39]\n\t" \
  "ds_read_b32 v217, v217 offset:0\n\t" \
  "v_and_b32_sdwa v219, %[mk], v218 dst_sel:DWORD dst_unused:UNUSED_PAD src0_sel:DWORD src1_sel:WORD_1\n\t" \
  "v_mfma_f32_16x16x32_bf16 v[60:63], v[212:215], v[194:197], v[60:63]\n\t" \
  "ds_read_b32 v216, v216 offset:0\n\t" \
  "v_and_b32 v218, %[mk], v218\n\t" \
  "v_and_b32_sdwa v221, %[mk], v220 dst_sel:DWORD dst_unused:UNUSED_PAD src0_sel:DWORD src1_sel:WORD_1\n\t" \
  "v_mfma_f32_16x16x32_bf16 v[84:87], v[212:215], v[202:205], v[84:87]\n\t" \
  "ds_read_b64_tr_b16 v[212:213], %[a2] offset:30888\n\t" \
  "v_and_b32 v220, %[mk], v220\n\t" \
  "s_waitcnt lgkmcnt(3)\n\t" \
  "v_mfma_f32_16x16x32_bf16 v[16:19], v[208:211], v[178:181], v[16:19]\n\t" \
  "ds_read_b64_tr_b16 v[214:215], %[a2] offset:32424\n\t" \
  "v_and_b32_sdwa v223, %[mk], v222 dst_sel:DWORD dst_unused:UNUSED_PAD src0_sel:DWORD src1_sel:WORD_1\n\t" \
  "v_mfma_f32_16x16x32_bf16 v[40:43], v[208:211], v[186:189], v[40:43]\n\t" \
  "ds_read_b32 v219, v219 offset:0\n\t" \
  "v_and_b32 v222, %[mk], v222\n\t" \
  "v_fma_f32 v163, |v163|, v225, v163 mul:4\n\t" \
  "v_mfma_f32_16x16x32_bf16 v[64:67], v[208:211], v[194:197], v[64:67]\n\t" \
  "ds_read_b32 v218, v218 offset:0\n\t" \
  "v_fma_f32 v162, |v162|, v224, v162 mul:4\n\t" \
  "v_fma_f32 v171, |v171|, v227, v171 mul:4\n\t" \
  "v_mfma_f32_16x16x32_bf16 v[88:91], v[208:211], v[202:205], v[88:91]\n\t" \
  "ds_read_b64_tr_b16 v[208:209], %[a1] offset:12304\n\t" \
  "ds_read_b64_tr_b16 v[210:211], %[a1] offset:13328\n\t" \
  "v_fma_f32 v170, |v170|, v226, v170 mul:4\n\t" \
  "v_fma_f32 v169, |v169|, v229, v169 mul:4\n\t" \
  "s_waitcnt lgkmcnt(4)\n\t" \
  "v_mfma_f32_16x16x32_bf16 v[20:23], v[212:215], v[178:181], v[20:23]\n\t" \
  "ds_read_b32 v221, v221 offset:0\n\t" \
  "v_fma_f32 v168, |v168|, v228, v168 mul:4\n\t" \
  "v_fma_mix_f32 v144, v144, v233, 0 op_sel_hi:[0,1,0]\n\t" \
  "v_fma_mix_f32 v145, v145, v232, 0 op_sel_hi:[0,1,0]\n\t" \
  "v_mfma_f32_16x16x32_bf16 v[44:47], v[212:215], v[186:189], v[44:47]\n\t" \
  "ds_read_b32 v220, v220 offset:0\n\t" \
  "v_pk_fma_f16 v234, v234, %[hh], %[hh]\n\t" \
  "v_cvt_pk_bf16_f32 v146, v144, v145\n\t" \
  "v_cvt_pk_bf16_f32 v163, v162, v163\n\t" \
  "v_mfma_f32_16x16x32_bf16 v[68:71], v[212:215], v[194:197], v[68:71]\n\t" \
  "ds_read_b32 v223, v223 offset:0\n\t" \
  "v_cvt_pk_bf16_f32 v171, v170, v171\n\t" \
  "v_cvt_pk_bf16_f32 v170, v168, v169\n\t" \
  "v_rcp_f16 v235, v234\n\t" \
  "v_mfma_f32_16x16x32_bf16 v[92:95], v[212:215], v[202:205], v[92:95]\n\t" \
  "ds_read_b64_tr_b16 v[212:213], %[a1] offset:15376\n\t" \
  "v_cvt_pk_f16_f32 v224, |v164|, |v165| clamp\n\t" \
  "s_cbranch_vccnz .Lcnx_p11_2\n\ts_setprio 2\n.Lcnx_p11_2:\n\t" \
  "v_cvt_pk_f16_f32 v226, |v166|, |v167| clamp\n\t" \
  "s_waitcnt lgkmcnt(4)\n\t" \
  "v_mfma_f32_16x16x32_bf16 v[176:179], v[208:211], v[96:99], 0\n\t" \
  "ds_read_b64_tr_b16 v[214:215], %[a1] offset:16400\n\t" \
  "v_cvt_pk_f16_f32 v228, |v172|, |v173| clamp\n\t" \
  "v_cvt_pk_f16_f32 v230, |v174|, |v175| clamp\n\t" \
  "v_rcp_f16_sdwa v234, v234 dst_sel:DWORD dst_unused:UNUSED_PAD src0_sel:WORD_1\n\t" \
  "v_mfma_f32_16x16x32_bf16 v[184:187], v[208:211], v[108:111], 0\n\t" \
  "ds_read_b32 v222, v222 offset:0\n\t" \
  "v_and_b32_sdwa v225, %[mk], v224 dst_sel:DWORD dst_unused:UNUSED_PAD src0_sel:DWORD src1_sel:WORD_1\n\t" \
  "v_and_b32 v224, %[mk], v224\n\t" \
  "v_mfma_f32_16x16x32_bf16 v[192:195], v[208:211], v[120:123], 0\n\t" \
  "ds_read_b32 v225, v225 offset:0\n\t" \
  "v_and_b32_sdwa v227, %[mk], v226 dst_sel:DWORD dst_unused:UNUSED_PAD src0_sel:DWORD src1_sel:WORD_1\n\t" \
  "v_mfma_f32_16x16x32_bf16 v[200:203], v[208:211], v[132:135], 0\n\t" \
  "ds_read_b64_tr_b16 v[208:209], %[a1] offset:14352\n\t" \
  "v_and_b32 v226, %[mk], v226\n\t" \
  "v_and_b32_sdwa v229, %[mk], v228 dst_sel:DWORD dst_unused:UNUSED_PAD src0_sel:DWORD src1_sel:WORD_1\n\t" \
  "s_waitcnt lgkmcnt(3)\n\t" \
  "v_mfma_f32_16x16x32_bf16 v[176:179], v[212:215], v[100:103], v[176:179]\n\t" \
  "ds_read_b64_tr_b16 v[210:211], %[a1] offset:17424\n\t" \
  "v_and_b32 v228, %[mk], v228\n\t" \
  "v_mfma_f32_16x16x32_bf16 v[184:187], v[212:215], v[112:115], v[184:187]\n\t" \
  "ds_read_b32 v224, v224 offset:0\n\t" \
  "v_and_b32_sdwa v231, %[mk], v230 dst_sel:DWORD dst_unused:UNUSED_PAD src0_sel:DWORD src1_sel:WORD_1\n\t" \
  "v_mfma_f32_16x16x32_bf16 v[192:195], v[212:215], v[124:127], v[192:195]\n\t" \
  "ds_read_b32 v227, v227 offset:0\n\t" \
  "v_and_b32 v230, %[mk], v230\n\t" \
  "v_fma_f32 v149, |v149|, v217, v149 mul:4\n\t" \
  "v_mfma_f32_16x16x32_bf16 v[200:203], v[212:215], v[136:139], v[200:203]\n\t" \
  "ds_read_b64_tr_b16 v[212:213], %[a1] offset:12816\n\t" \
  "ds_read_b64_tr_b16 v[214:215], %[a1] offset:13840\n\t" \
  "v_fma_f32 v148, |v148|, v216, v148 mul:4\n\t" \
  "v_fma_f32 v151, |v151|, v219, v151 mul:4\n\t" \
  "s_waitcnt lgkmcnt(4)\n\t" \
  "v_mfma_f32_16x16x32_bf16 v[176:179], v[208:211], v[104:107], v[176:179]\n\t" \
  "ds_read_b32 v226, v226 offset:0\n\t" \
  "v_fma_f32 v150, |v150|, v218, v150 mul:4\n\t" \
  "v_fma_f32 v157, |v157|, v221, v157 mul:4\n\t" \
  "v_mfma_f32_16x16x32_bf16 v[184:187], v[208:211], v[116:119], v[184:187]\n\t" \
  "ds_read_b32 v229, v229 offset:0\n\t" \
  "v_fma_f32 v156, |v156|, v220, v156 mul:4\n\t" \
  "v_fma_f32 v159, |v159|, v223, v159 mul:4\n\t" \
  "v_fma_f32 v158, |v158|, v222, v158 mul:4\n\t" \
  "v_mfma_f32_16x16x32_bf16 v[192:195], v[208:211], v[128:131], v[192:195]\n\t" \
  "ds_read_b32 v228, v228 offset:0\n\t" \
  "v_fma_mix_f32 v160, v160, v235, 0 op_sel_hi:[0,1,0]\n\t" \
  "v_fma_mix_f32 v161, v161, v234, 0 op_sel_hi:[0,1,0]\n\t" \
  "v_cvt_pk_bf16_f32 v162, v160, v161\n\t" \
  "v_mfma_f32_16x16x32_bf16 v[200:203], v[208:211], v[140:143], v[200:203]\n\t" \
  "ds_read_b64_tr_b16 v[208:209], %[a1] offset:15888\n\t" \
  "v_cvt_pk_bf16_f32 v148, v148, v149\n\t" \
  "v_cvt_pk_bf16_f32 v149, v150, v151\n\t" \
  "s_waitcnt lgkmcnt(4)\n\t" \
  "v_mfma_f32_16x16x32_bf16 v[180:183], v[212:215], v[96:99], 0\n\t" \
  "ds_read_b64_tr_b16 v[210:211], %[a1] offset:16912\n\t" \
  "v_cvt_pk_bf16_f32 v156, v156, v157\n\t" \
  "v_cvt_pk_bf16_f32 v157, v158, v159\n\t" \
  "s_cbranch_vccnz .Lcnx_p11_3\n\ts_setprio 3\n.Lcnx_p11_3:\n\t" \
  "v_cvt_pk_f16_f32 v216, |v178|, |v179| clamp\n\t" \
  "v_mfma_f32_16x16x32_bf16 v[188:191], v[212:215], v[108:111], 0\n\t" \
  "ds_read_b32 v231, v231 offset:0\n\t" \
  "v_cvt_pk_f16_f32 v232, v176, v177\n\t" \
  "v_cvt_pk_f16_f32 v218, |v186|, |v187| clamp\n\t" \
  "v_cvt_pk_f16_f32 v220, |v184|, |v185| clamp\n\t" \
  "v_mfma_f32_16x16x32_bf16 v[196:199], v[212:215], v[120:123], 0\n\t" \
  "ds_read_b32 v230, v230 offset:0\n\t" \
  "v_pk_mul_f16 v233, v232, v232\n\t" \
  "v_and_b32_sdwa v217, %[mk], v216 dst_sel:DWORD dst_unused:UNUSED_PAD src0_sel:DWORD src1_sel:WORD_1\n\t" \
  "v_mfma_f32_16x16x32_bf16 v[204:207], v[212:215], v[132:135], 0\n\t" \
  "ds_read_b64_tr_b16 v[212:213], %[a1] offset:14864\n\t" \
  "v_and_b32 v216, %[mk], v216\n\t" \
  "v_and_b32_sdwa v219, %[mk], v218 dst_sel:DWORD dst_unused:UNUSED_PAD src0_sel:DWORD src1_sel:WORD_1\n\t" \
  "s_waitcnt lgkmcnt(3)\n\t" \
  "v_mfma_f32_16x16x32_bf16 v[180:183], v[208:211], v[100:103], v[180:183]\n\t" \
  "ds_read_b64_tr_b16 v[214:215], %[a1] offset:17936\n\t" \
  "v_and_b32 v218, %[mk], v218\n\t" \
  "v_mfma_f32_16x16x32_bf16 v[188:191], v[208:211], v[112:115], v[188:191]\n\t" \
  "s_waitcnt vmcnt(0)\n\t" \
  "s_mov_b64 exec, 3\n\t" \
  "ds_add_rtn_u32 v246, %[l8], %[vo] offset:47744\n\t" \
  "s_mov_b64 exec, -1\n\t" \
  "ds_read_b32 v217, v217 offset:0\n\t" \
  "v_and_b32_sdwa v221, %[mk], v220 dst_sel:DWORD dst_unused:UNUSED_PAD src0_sel:DWORD src1_sel:WORD_1\n\t" \
  "v_mfma_f32_16x16x32_bf16 v[196:199], v[208:211], v[124:127], v[196:199]\n\t" \
  "ds_read_b32 v216, v216 offset:0\n\t" \
  "v_and_b32 v220, %[mk], v220\n\t" \
  "v_pk_fma_f16 v233, v233, %[h2], %[h1]\n\t" \
  "v_mfma_f32_16x16x32_bf16 v[204:207], v[208:211], v[136:139], v[204:207]\n\t" \
  "s_waitcnt lgkmcnt(2)\n\t" \
  "v_readfirstlane_b32 %[t0], v246\n\t" \
  "s_cmp_lt_u32 %[t0], %[tg]\n\t" \
  "s_cbranch_scc1 .Lcnx_w132\n.Lcnx_b132:\n\t" \
  "ds_read_b64_tr_b16 v[208:209], %[a2] offset:0\n\t" \
  "v_pk_mul_f16 v233, v232, v233\n\t" \
  "v_fma_f32 v165, |v165|, v225, v165 mul:4\n\t" \
  "v_mfma_f32_16x16x32_bf16 v[180:183], v[212:215], v[104:107], v[180:183]\n\t" \
  "ds_read_b64_tr_b16 v[210:211], %[a2] offset:1536\n\t" \
  "v_fma_f32 v164, |v164|, v224, v164 mul:4\n\t" \
  "v_fma_f32 v167, |v167|, v227, v167 mul:4\n\t" \
  "v_mfma_f32_16x16x32_bf16 v[188:191], v[212:215], v[116:119], v[188:191]\n\t" \
  "ds_read_b32 v219, v219 offset:0\n\t" \
  "v_fma_f32 v166, |v166|, v226, v166 mul:4\n\t" \
  "v_fma_f32 v173, |v173|, v229, v173 mul:4\n\t" \
  "v_fma_f32 v172, |v172|, v228, v172 mul:4\n\t" \
  "v_mfma_f32_16x16x32_bf16 v[196:199], v[212:215], v[128:131], v[196:199]\n\t" \
  "ds_read_b32 v218, v218 offset:0\n\t" \
  "v_fma_f32 v175, |v175|, v231, v175 mul:4\n\t" \
  "v_fma_f32 v174, |v174|, v230, v174 mul:4\n\t" \
  "v_cvt_pk_bf16_f32 v164, v164, v165\n\t" \
  "v_mfma_f32_16x16x32_bf16 v[204:207], v[212:215], v[140:143], v[204:207]\n\t" \
  "ds_read_b64_tr_b16 v[212:213], %[a2] offset:8\n\t" \
  "v_cvt_pk_bf16_f32 v165, v166, v167\n\t" \
  "v_cvt_pk_bf16_f32 v172, v172, v173\n\t" \
  "v_cvt_pk_bf16_f32 v173, v174, v175\n\t" \
  "s_cbranch_vccnz .Lcnx_p12_0\n\ts_setprio 0\n.Lcnx_p12_0:\n\t" \
  "v_exp_f16 v232, v233\n\t" \
  "v_cvt_pk_f16_f32 v224, |v194|, |v195| clamp\n\t" \
  "v_mfma_f32_16x16x32_bf16 v[80:83], v[242:245], v[236:239], v[80:83]\n\t" \
  "ds_read_b64_tr_b16 v[214:215], %[a2] offset:1544\n\t" \
  "v_cvt_pk_f16_f32 v234, v192, v193\n\t" \
  "v_cvt_pk_f16_f32 v226, |v202|, |v203| clamp\n\t" \
  "v_cvt_pk_f16_f32 v228, |v200|, |v201| clamp\n\t" \
  "v_exp_f16_sdwa v232, v233 dst_sel:WORD_1 dst_unused:UNUSED_PRESERVE src0_sel:WORD_1\n\t" \
  "v_mfma_f32_16x16x32_bf16 v[84:87], v[240:243], v[236:239], v[84:87]\n\t" \
  "buffer_load_dwordx4 v[236:239], %[vo], %[xrs], %[oc1] offen offset:320\n\t" \
  "s_cmp_lg_u32 %[wq], 4\n\t" \
  "s_cbranch_scc1 .Lcnx_c31\n\t" \
  "s_mov_b32 m0, %[pf]\n\t" \
  "s_add_u32 %[wb], %[wb], %[rs4]\n\t" \
  "buffer_load_dword %[wv], %[xrs], %[wb] offen lds\n\t" \
  ".Lcnx_c31:\n\t" \
  "ds_read_b32 v221, v221 offset:0\n\t" \
  "v_pk_mul_f16 v235, v234, v234\n\t" \
  "v_and_b32_sdwa v225, %[mk], v224 dst_sel:DWORD dst_unused:UNUSED_PAD src0_sel:DWORD src1_sel:WORD_1\n\t" \
  "v_and_b32 v224, %[mk], v224\n\t" \
  "s_waitcnt lgkmcnt(5)\n\t" \
  "v_mfma_f32_16x16x32_bf16 v[0:3], v[208:211], v[146:149], v[0:3]\n\t" \
  "ds_read_b32 v220, v220 offset:0\n\t" \
  "v_and_b32_sdwa v227, %[mk], v226 dst_sel:DWORD dst_unused:UNUSED_PAD src0_sel:DWORD src1_sel:WORD_1\n\t" \
  "v_and_b32 v226, %[mk], v226\n\t" \
  "v_and_b32_sdwa v229, %[mk], v228 dst_sel:DWORD dst_unused:UNUSED_PAD src0_sel:DWORD src1_sel:WORD_1\n\t" \
  "v_mfma_f32_16x16x32_bf16 v[24:27], v[208:211], v[154:157], v[24:27]\n\t" \
  "ds_read_b32 v225, v225 offset:0\n\t" \
  "v_and_b32 v228, %[mk], v228\n\t" \
  "v_pk_fma_f16 v235, v235, %[h2], %[h1]\n\t" \
  "v_mfma_f32_16x16x32_bf16 v[48:51], v[208:211], v[162:165], v[48:51]\n\t" \
  "ds_read_b32 v224, v224 offset:0\n\t" \
  "v_pk_mul_f16 v235, v234, v235\n\t" \
  "s_waitcnt lgkmcnt(7)\n\t" \
  "v_fma_f32 v179, |v179|, v217, v179 mul:4\n\t" \
  "v_fma_f32 v178, |v178|, v216, v178 mul:4\n\t" \
  "v_fma_f32 v187, |v187|, v219, v187 mul:4\n\t" \
  "v_mfma_f32_16x16x32_bf16 v[72:75], v[208:211], v[170:173], v[72:75]\n\t" \
  "ds_read_b64_tr_b16 v[208:209], %[a2] offset:64\n\t" \
  "ds_read_b64_tr_b16 v[210:211], %[a2] offset:1600\n\t" \
  "s_waitcnt lgkmcnt(4)\n\t" \
  "v_fma_f32 v186, |v186|, v218, v186 mul:4\n\t" \
  "v_fma_f32 v185, |v185|, v221, v185 mul:4\n\t" \
  "v_fma_f32 v184, |v184|, v220, v184 mul:4\n\t" \
  "v_pk_fma_f16 v232, v232, %[hh], %[hh]\n\t" \
  "v_mfma_f32_16x16x32_bf16 v[4:7], v[212:215], v[146:149], v[4:7]\n\t" \
  "ds_read_b32 v227, v227 offset:0\n\t" \
  "v_cvt_pk_bf16_f32 v179, v178, v179\n\t" \
  "v_cvt_pk_bf16_f32 v187, v186, v187\n\t" \
  "v_cvt_pk_bf16_f32 v186, v184, v185\n\t" \
  "v_rcp_f16 v233, v232\n\t" \
  "s_cbranch_vccnz .Lcnx_p12_1\n\ts_setprio 1\n.Lcnx_p12_1:\n\t" \
  "v_mfma_f32_16x16x32_bf16 v[28:31], v[212:215], v[154:157], v[28:31]\n\t" \
  "ds_read_b32 v226, v226 offset:0\n\t" \
  "v_exp_f16 v234, v235\n\t" \
  "v_cvt_pk_f16_f32 v216, |v180|, |v181| clamp\n\t" \
  "v_cvt_pk_f16_f32 v218, |v182|, |v183| clamp\n\t" \
  "v_cvt_pk_f16_f32 v220, |v188|, |v189| clamp\n\t" \
  "v_cvt_pk_f16_f32 v222, |v190|, |v191| clamp\n\t" \
  "v_mfma_f32_16x16x32_bf16 v[52:55], v[212:215], v[162:165], v[52:55]\n\t" \
  "ds_read_b32 v229, v229 offset:0\n\t" \
  "v_rcp_f16_sdwa v232, v232 dst_sel:DWORD dst_unused:UNUSED_PAD src0_sel:WORD_1\n\t" \
  "v_exp_f16_sdwa v234, v235 dst_sel:WORD_1 dst_unused:UNUSED_PRESERVE src0_sel:WORD_1\n\t" \
  "v_and_b32_sdwa v217, %[mk], v216 dst_sel:DWORD dst_unused:UNUSED_PAD src0_sel:DWORD src1_sel:WORD_1\n\t" \
  "v_and_b32 v216, %[mk], v216\n\t" \
  "v_mfma_f32_16x16x32_bf16 v[76:79], v[212:215], v[170:173], v[76:79]\n\t" \
  "ds_read_b64_tr_b16 v[212:213], %[a2] offset:72\n\t" \
  "v_and_b32_sdwa v219, %[mk], v218 dst_sel:DWORD dst_unused:UNUSED_PAD src0_sel:DWORD src1_sel:WORD_1\n\t" \
  "v_and_b32 v218, %[mk], v218\n\t" \
  "s_waitcnt lgkmcnt(4)\n\t" \
  "v_mfma_f32_16x16x32_bf16 v[8:11], v[208:211], v[146:149], v[8:11]\n\t" \
  "ds_read_b64_tr_b16 v[214:215], %[a2] offset:1608\n\t" \
  "v_and_b32_sdwa v221, %[mk], v220 dst_sel:DWORD dst_unused:UNUSED_PAD src0_sel:DWORD src1_sel:WORD_1\n\t" \
  "v_and_b32 v220, %[mk], v220\n\t" \
  "v_mfma_f32_16x16x32_bf16 v[32:35], v[208:211], v[154:157], v[32:35]\n\t" \
  "ds_read_b32 v228, v228 offset:0\n\t" \
  "ds_read_b32 v217, v217 offset:0\n\t" \
  "v_and_b32_sdwa v223, %[mk], v222 dst_sel:DWORD dst_unused:UNUSED_PAD src0_sel:DWORD src1_sel:WORD_1\n\t" \
  "v_and_b32 v222, %[mk], v222\n\t" \
  "s_waitcnt lgkmcnt(6)\n\t" \
  "v_fma_f32 v195, |v195|, v225, v195 mul:4\n\t" \
  "v_mfma_f32_16x16x32_bf16 v[56:59], v[208:211], v[162:165], v[56:59]\n\t" \
  "ds_read_b32 v216, v216 offset:0\n\t" \
  "ds_read_b32 v219, v219 offset:0\n\t" \
  "v_fma_f32 v194, |v194|, v224, v194 mul:4\n\t" \
  "v_fma_f32 v203, |v203|, v227, v203 mul:4\n\t" \
  "s_waitcnt lgkmcnt(3)\n\t" \
  "v_fma_f32 v202, |v202|, v226, v202 mul:4\n\t" \
  "v_mfma_f32_16x16x32_bf16 v[80:83], v[208:211], v[170:173], v[80:83]\n\t" \
  "ds_read_b64_tr_b16 v[208:209], %[a2] offset:128\n\t" \
  "ds_read_b64_tr_b16 v[210:211], %[a2] offset:1664\n\t" \
  "v_fma_f32 v201, |v201|, v229, v201 mul:4\n\t" \
  "v_fma_f32 v200, |v200|, v228, v200 mul:4\n\t" \
  "v_fma_mix_f32 v176, v176, v233, 0 op_sel_hi:[0,1,0]\n\t" \
  "v_fma_mix_f32 v177, v177, v232, 0 op_sel_hi:[0,1,0]\n\t" \
  "v_mfma_f32_16x16x32_bf16 v[12:15], v[212:215], v[146:149], v[12:15]\n\t" \
  "ds_read_b32 v218, v218 offset:0\n\t" \
  "s_cbranch_vccnz .Lcnx_p12_2\n\ts_setprio 2\n.Lcnx_p12_2:\n\t" \
  "v_pk_fma_f16 v234, v234, %[hh], %[hh]\n\t" \
  "v_cvt_pk_bf16_f32 v178, v176, v177\n\t" \
  "v_cvt_pk_bf16_f32 v195, v194, v195\n\t" \
  "v_cvt_pk_bf16_f32 v203, v202, v203\n\t" \
  "v_cvt_pk_bf16_f32 v202, v200, v201\n\t" \
  "v_mfma_f32_16x16x32_bf16 v[36:39], v[212:215], v[154:157], v[36:39]\n\t" \
  "ds_read_b32 v221, v221 offset:0\n\t" \
  "v_rcp_f16 v235, v234\n\t" \
  "v_cvt_pk_f16_f32 v224, |v196|, |v197| clamp\n\t" \
  "v_cvt_pk_f16_f32 v226, |v198|, |v199| clamp\n\t" \
  "v_cvt_pk_f16_f32 v228, |v204|, |v205| clamp\n\t" \
  "v_mfma_f32_16x16x32_bf16 v[60:63], v[212:215], v[162:165], v[60:63]\n\t" \
  "ds_read_b32 v220, v220 offset:0\n\t" \
  "v_cvt_pk_f16_f32 v230, |v206|, |v207| clamp\n\t" \
  "v_rcp_f16_sdwa v234, v234 dst_sel:DWORD dst_unused:UNUSED_PAD src0_sel:WORD_1\n\t" \
  "v_and_b32_sdwa v225, %[mk], v224 dst_sel:DWORD dst_unused:UNUSED_PAD src0_sel:DWORD src1_sel:WORD_1\n\t" \
  "v_and_b32 v224, %[mk], v224\n\t" \
  "v_mfma_f32_16x16x32_bf16 v[84:87], v[212:215], v[170:173], v[84:87]\n\t" \
  "ds_read_b64_tr_b16 v[212:213], %[a2] offset:136\n\t" \
  "v_and_b32_sdwa v227, %[mk], v226 dst_sel:DWORD dst_unused:UNUSED_PAD src0_sel:DWORD src1_sel:WORD_1\n\t" \
  "v_and_b32 v226, %[mk], v226\n\t" \
  "s_waitcnt lgkmcnt(4)\n\t" \
  "v_mfma_f32_16x16x32_bf16 v[16:19], v[208:211], v[146:149], v[16:19]\n\t" \
  "ds_read_b64_tr_b16 v[214:215], %[a2] offset:1672\n\t" \
  "ds_read_b32 v223, v223 offset:0\n\t" \
  "v_and_b32_sdwa v229, %[mk], v228 dst_sel:DWORD dst_unused:UNUSED_PAD src0_sel:DWORD src1_sel:WORD_1\n\t" \
  "v_and_b32 v228, %[mk], v228\n\t" \
  "v_and_b32_sdwa v231, %[mk], v230 dst_sel:DWORD dst_unused:UNUSED_PAD src0_sel:DWORD src1_sel:WORD_1\n\t" \
  "v_mfma_f32_16x16x32_bf16 v[40:43], v[208:211], v[154:157], v[40:43]\n\t" \
  "s_waitcnt vmcnt(0)\n\t" \
  "s_mov_b64 exec, 3\n\t" \
  "ds_add_rtn_u32 v246, %[l8], %[vo] offset:47752\n\t" \
  "s_mov_b64 exec, -1\n\t" \
  "ds_read_b32 v222, v222 offset:0\n\t" \
  "ds_read_b32 v225, v225 offset:0\n\t" \
  "v_and_b32 v230, %[mk], v230\n\t" \
  "s_waitcnt lgkmcnt(8)\n\t" \
  "v_fma_f32 v181, |v181|, v217, v181 mul:4\n\t" \
  "v_mfma_f32_16x16x32_bf16 v[64:67], v[208:211], v[162:165], v[64:67]\n\t" \
  "ds_read_b32 v224, v224 offset:0\n\t" \
  "ds_read_b32 v227, v227 offset:0\n\t" \
  "v_fma_f32 v180, |v180|, v216, v180 mul:4\n\t" \
  "v_fma_f32 v183, |v183|, v219, v183 mul:4\n\t" \
  "v_fma_f32 v182, |v182|, v218, v182 mul:4\n\t" \
  "s_cbranch_vccnz .Lcnx_p12_3\n\ts_setprio 3\n.Lcnx_p12_3:\n\t" \
  "v_mfma_f32_16x16x32_bf16 v[88:91], v[208:211], v[170:173], v[88:91]\n\t" \
  "s_waitcnt lgkmcnt(4)\n\t" \
  "v_readfirstlane_b32 %[t0], v246\n\t" \
  "s_cmp_lt_u32 %[t0], %[tg]\n\t" \
  "s_cbranch_scc1 .Lcnx_w138\n.Lcnx_b138:\n\t" \
  "ds_read_b64_tr_b16 v[208:209], %[a2] offset:6152\n\t" \
  "ds_read_b64_tr_b16 v[210:211], %[a2] offset:7688\n\t" \
  "s_waitcnt lgkmcnt(5)\n\t" \
  "v_fma_f32 v189, |v189|, v221, v189 mul:4\n\t" \
  "v_fma_f32 v188, |v188|, v220, v188 mul:4\n\t" \
  "v_fma_f32 v191, |v191|, v223, v191 mul:4\n\t" \
  "v_fma_f32 v190, |v190|, v222, v190 mul:4\n\t" \
  "v_fma_mix_f32 v192, v192, v235, 0 op_sel_hi:[0,1,0]\n\t" \
  "v_mfma_f32_16x16x32_bf16 v[20:23], v[212:215], v[146:149], v[20:23]\n\t" \
  "ds_read_b32 v226, v226 offset:0\n\t" \
  "v_fma_mix_f32 v193, v193, v234, 0 op_sel_hi:[0,1,0]\n\t" \
  "v_cvt_pk_bf16_f32 v194, v192, v193\n\t" \
  "v_cvt_pk_bf16_f32 v180, v180, v181\n\t" \
  "v_cvt_pk_bf16_f32 v181, v182, v183\n\t" \
  "v_mfma_f32_16x16x32_bf16 v[44:47], v[212:215], v[154:157], v[44:47]\n\t" \
  "ds_read_b32 v229, v229 offset:0\n\t" \
  "v_cvt_pk_bf16_f32 v188, v188, v189\n\t" \
  "v_cvt_pk_bf16_f32 v189, v190, v191\n\t" \
  "ds_read_b32 v228, v228 offset:0\n\t" \
  "ds_read_b32 v231, v231 offset:0\n\t" \
  "ds_read_b32 v230, v230 offset:0\n\t" \
  "s_waitcnt lgkmcnt(4)\n\t" \
  "v_fma_f32 v197, |v197|, v225, v197 mul:4\n\t" \
  "v_fma_f32 v196, |v196|, v224, v196 mul:4\n\t" \
  "v_fma_f32 v199, |v199|, v227, v199 mul:4\n\t" \
  "v_mfma_f32_16x16x32_bf16 v[68:71], v[212:215], v[162:165], v[68:71]\n\t" \
  "v_fma_f32 v198, |v198|, v226, v198 mul:4\n\t" \
  "s_waitcnt lgkmcnt(0)\n\t" \
  "v_fma_f32 v205, |v205|, v229, v205 mul:4\n\t" \
  "v_fma_f32 v204, |v204|, v228, v204 mul:4\n\t" \
  "v_fma_f32 v207, |v207|, v231, v207 mul:4\n\t" \
  "v_mfma_f32_16x16x32_bf16 v[92:95], v[212:215], v[170:173], v[92:95]\n\t" \
  "ds_read_b64_tr_b16 v[212:213], %[a2] offset:6160\n\t" \
  "v_fma_f32 v206, |v206|, v230, v206 mul:4\n\t" \
  "v_cvt_pk_bf16_f32 v196, v196, v197\n\t" \
  "v_cvt_pk_bf16_f32 v197, v198, v199\n\t" \
  "v_cvt_pk_bf16_f32 v204, v204, v205\n\t" \
  "v_cvt_pk_bf16_f32 v205, v206, v207\n\t" \
  "s_cmp_lg_u32 %[rnd], 0\n\t" \
  "s_cbranch_scc1 .Lcnx_fin\n\t" \
  "v_mfma_f32_32x32x16_bf16 v[144:159], v[242:245], v[240:243], 0\n\t" \
  "v_mfma_f32_16x16x32_bf16 v[88:91], v[242:245], v[236:239], v[88:91]\n\t" \
  "ds_read_b64_tr_b16 v[214:215], %[a2] offset:7696\n\t" \
  "v_mfma_f32_32x32x16_bf16 v[160:175], v[242:245], v[240:243], 0\n\t" \
  "ds_read_b64 v[246:247], %[l8] offset:47136\n\t" \
  "s_waitcnt lgkmcnt(0)\n\t" \
  "v_add_u32 v246, %[c24], v246\n\t" \
  "v_mfma_f32_16x16x32_bf16 v[92:95], v[240:243], v[236:239], v[92:95]\n\t" \
  "v_add_u32 v247, %[c23], v247\n\t" \
  "v_add_u32 v232, 1536, v247\n\t" \
  "v_mfma_f32_16x16x32_bf16 v[0:3], v[208:211], v[178:181], v[0:3]\n\t" \
  "ds_read2_b32 v[98:99], v247 offset0:0 offset1:6\n\t" \
  "v_mfma_f32_16x16x32_bf16 v[24:27], v[208:211], v[186:189], v[24:27]\n\t" \
  "ds_read2_b32 v[102:103], v247 offset0:12 offset1:18\n\t" \
  "v_mfma_f32_16x16x32_bf16 v[48:51], v[208:211], v[194:197], v[48:51]\n\t" \
  "ds_read2_b32 v[106:107], v247 offset0:24 offset1:30\n\t" \
  "v_mfma_f32_16x16x32_bf16 v[72:75], v[208:211], v[202:205], v[72:75]\n\t" \
  "ds_read_b64_tr_b16 v[208:209], %[a2] offset:6216\n\t" \
  "ds_read_b64_tr_b16 v[210:211], %[a2] offset:7752\n\t" \
  "v_mfma_f32_16x16x32_bf16 v[4:7], v[212:215], v[178:181], v[4:7]\n\t" \
  "ds_read2_b32 v[110:111], v247 offset0:36 offset1:42\n\t" \
  "ds_read2_b32 v[114:115], v247 offset0:192 offset1:198\n\t" \
  "s_waitcnt lgkmcnt(6)\n\t" \
  "v_and_b32 v96, 0xffff, v98\n\t" \
  "v_mfma_f32_16x16x32_bf16 v[28:31], v[212:215], v[186:189], v[28:31]\n\t" \
  "ds_read2_b32 v[118:119], v247 offset0:204 offset1:210\n\t" \
  "ds_read2_b32 v[122:123], v247 offset0:216 offset1:222\n\t" \
  "v_and_b32 v97, 0xffff0000, v98\n\t" \
  "v_and_b32 v98, 0xffff, v99\n\t" \
  "v_and_b32 v99, 0xffff0000, v99\n\t" \
  "v_mfma_f32_16x16x32_bf16 v[52:55], v[212:215], v[194:197], v[52:55]\n\t" \
  "ds_read2_b32 v[126:127], v247 offset0:228 offset1:234\n\t" \
  "s_waitcnt lgkmcnt(8)\n\t" \
  "v_and_b32 v100, 0xffff, v102\n\t" \
  "v_and_b32 v101, 0xffff0000, v102\n\t" \
  "v_mfma_f32_16x16x32_bf16 v[76:79], v[212:215], v[202:205], v[76:79]\n\t" \
  "ds_read_b64_tr_b16 v[212:213], %[a2] offset:6224\n\t" \
  "ds_read_b64_tr_b16 v[214:215], %[a2] offset:7760\n\t" \
  "v_and_b32 v102, 0xffff, v103\n\t" \
  "v_and_b32 v103, 0xffff0000, v103\n\t" \
  "s_waitcnt lgkmcnt(9)\n\t" \
  "v_and_b32 v104, 0xffff, v106\n\t" \
  "s_waitcnt lgkmcnt(7)\n\t" \
  "v_mfma_f32_16x16x32_bf16 v[8:11], v[208:211], v[178:181], v[8:11]\n\t" \
  "ds_read2_b32 v[130:131], v232 offset0:0 offset1:6\n\t" \
  "v_and_b32 v105, 0xffff0000, v106\n\t" \
  "v_and_b32 v106, 0xffff, v107\n\t" \
  "v_mfma_f32_16x16x32_bf16 v[32:35], v[208:211], v[186:189], v[32:35]\n\t" \
  "ds_read2_b32 v[134:135], v232 offset0:12 offset1:18\n\t" \
  "v_and_b32 v107, 0xffff0000, v107\n\t" \
  "s_waitcnt lgkmcnt(8)\n\t" \
  "v_and_b32 v108, 0xffff, v110\n\t" \
  "v_and_b32 v109, 0xffff0000, v110\n\t" \
  "v_mfma_f32_16x16x32_bf16 v[56:59], v[208:211], v[194:197], v[56:59]\n\t" \
  "ds_read2_b32 v[138:139], v232 offset0:24 offset1:30\n\t" \
  "v_and_b32 v110, 0xffff, v111\n\t" \
  "v_and_b32 v111, 0xffff0000, v111\n\t" \
  "s_waitcnt lgkmcnt(8)\n\t" \
  "v_and_b32 v112, 0xffff, v114\n\t" \
  "v_mfma_f32_16x16x32_bf16 v[80:83], v[208:211], v[202:205], v[80:83]\n\t" \
  "ds_read_b64_tr_b16 v[208:209], %[a2] offset:6280\n\t" \
  "v_and_b32 v113, 0xffff0000, v114\n\t" \
  "v_and_b32 v114, 0xffff, v115\n\t" \
  "s_waitcnt lgkmcnt(4)\n\t" \
  "v_mfma_f32_16x16x32_bf16 v[12:15], v[212:215], v[178:181], v[12:15]\n\t" \
  "ds_read_b64_tr_b16 v[210:211], %[a2] offset:7816\n\t" \
  "v_and_b32 v115, 0xffff0000, v115\n\t" \
  "v_and_b32 v116, 0xffff, v118\n\t" \
  "v_and_b32 v117, 0xffff0000, v118\n\t" \
  "v_mfma_f32_16x16x32_bf16 v[36:39], v[212:215], v[186:189], v[36:39]\n\t" \
  "ds_read2_b32 v[142:143], v232 offset0:36 offset1:42\n\t" \
  "v_and_b32 v118, 0xffff, v119\n\t" \
  "v_and_b32 v119, 0xffff0000, v119\n\t" \
  "v_mfma_f32_16x16x32_bf16 v[60:63], v[212:215], v[194:197], v[60:63]\n\t" \
  "v_and_b32 v120, 0xffff, v122\n\t" \
  "v_and_b32 v121, 0xffff0000, v122\n\t" \
  "v_and_b32 v122, 0xffff, v123\n\t" \
  "v_mfma_f32_16x16x32_bf16 v[84:87], v[212:215], v[202:205], v[84:87]\n\t" \
  "ds_read_b64_tr_b16 v[212:213], %[a2] offset:6288\n\t" \
  "v_and_b32 v123, 0xffff0000, v123\n\t" \
  "v_and_b32 v124, 0xffff, v126\n\t" \
  "s_waitcnt lgkmcnt(2)\n\t" \
  "v_mfma_f32_16x16x32_bf16 v[16:19], v[208:211], v[178:181], v[16:19]\n\t" \
  "ds_read_b64_tr_b16 v[214:215], %[a2] offset:7824\n\t" \
  "v_and_b32 v125, 0xffff0000, v126\n\t" \
  "v_and_b32 v126, 0xffff, v127\n\t" \
  "v_and_b32 v127, 0xffff0000, v127\n\t" \
  "v_mfma_f32_16x16x32_bf16 v[40:43], v[208:211], v[186:189], v[40:43]\n\t" \
  "v_and_b32 v128, 0xffff, v130\n\t" \
  "v_and_b32 v129, 0xffff0000, v130\n\t" \
  "v_mfma_f32_16x16x32_bf16 v[64:67], v[208:211], v[194:197], v[64:67]\n\t" \
  "v_and_b32 v130, 0xffff, v131\n\t" \
  "v_and_b32 v131, 0xffff0000, v131\n\t" \
  "v_and_b32 v132, 0xffff, v134\n\t" \
  "v_mfma_f32_16x16x32_bf16 v[88:91], v[208:211], v[202:205], v[88:91]\n\t" \
  "v_and_b32 v133, 0xffff0000, v134\n\t" \
  "v_and_b32 v134, 0xffff, v135\n\t" \
  "s_waitcnt lgkmcnt(0)\n\t" \
  "v_mfma_f32_16x16x32_bf16 v[20:23], v[212:215], v[178:181], v[20:23]\n\t" \
  "v_and_b32 v135, 0xffff0000, v135\n\t" \
  "v_and_b32 v136, 0xffff, v138\n\t" \
  "v_and_b32 v137, 0xffff0000, v138\n\t" \
  "v_mfma_f32_16x16x32_bf16 v[44:47], v[212:215], v[186:189], v[44:47]\n\t" \
  "v_and_b32 v138, 0xffff, v139\n\t" \
  "v_and_b32 v139, 0xffff0000, v139\n\t" \
  "v_mfma_f32_16x16x32_bf16 v[68:71], v[212:215], v[194:197], v[68:71]\n\t" \
  "v_and_b32 v140, 0xffff, v142\n\t" \
  "v_and_b32 v141, 0xffff0000, v142\n\t" \
  "v_and_b32 v142, 0xffff, v143\n\t" \
  "v_mfma_f32_16x16x32_bf16 v[92:95], v[212:215], v[202:205], v[92:95]\n\t" \
  "v_and_b32 v143, 0xffff0000, v143\n\t" \
  "v_bfe_u32 v236, %[l8], 3, 2\n\t" \
  "v_mul_u32_u24 v236, 0x5555, v236\n\t" \
  "s_nop 7\n\t" \
  "s_mov_b32 %[tg], 6144\n\t" \
  "s_mov_b32 %[rnd], 1\n\t" \
  "s_mov_b32 %[wq], 0\n\t" \
  "s_mov_b32 %[d1], -86104\n\t" \
  "s_mov_b32 %[d2], -36904\n\t" \
  "s_mov_b32 %[f0], 2\n\t" \
  "s_mov_b32 %[op0], %[oc0]\n\t" \
  "s_mov_b32 %[op1], %[oc1]\n\t" \
  "s_add_u32 %[oc0], %[oc0], %[dy]\n\t" \
  "s_add_u32 %[oc1], %[oc1], %[dy]\n\t" \
  "s_branch .Lcnx_round\n\t" \
  ".Lcnx_w9:\n\t" \
  "ds_read_b32 v246, %[l8] offset:47656\n\t" \
  "s_waitcnt lgkmcnt(0)\n\t" \
  "v_readfirstlane_b32 %[t0], v246\n\t" \
  "s_cmp_lt_u32 %[t0], %[tg]\n\t" \
  "s_cbranch_scc1 .Lcnx_w9\n\t" \
  "s_branch .Lcnx_b9\n\t" \
  ".Lcnx_w12:\n\t" \
  "ds_read_b32 v246, %[l8] offset:47664\n\t" \
  "s_waitcnt lgkmcnt(0)\n\t" \
  "v_readfirstlane_b32 %[t0], v246\n\t" \
  "s_cmp_lt_u32 %[t0], %[tg]\n\t" \
  "s_cbranch_scc1 .Lcnx_w12\n\t" \
  "s_branch .Lcnx_b12\n\t" \
  ".Lcnx_w24:\n\t" \
  "ds_read_b32 v246, %[l8] offset:47672\n\t" \
  "s_waitcnt lgkmcnt(0)\n\t" \
  "v_readfirstlane_b32 %[t0], v246\n\t" \
  "s_cmp_lt_u32 %[t0], %[tg]\n\t" \
  "s_cbranch_scc1 .Lcnx_w24\n\t" \
  "s_branch .Lcnx_b24\n\t" \
  ".Lcnx_w36:\n\t" \
  "ds_read_b32 v246, %[l8] offset:47680\n\t" \
  "s_waitcnt lgkmcnt(0)\n\t" \
  "v_readfirstlane_b32 %[t0], v246\n\t" \
  "s_cmp_lt_u32 %[t0], %[tg]\n\t" \
  "s_cbranch_scc1 .Lcnx_w36\n\t" \
  "s_branch .Lcnx_b36\n\t" \
  ".Lcnx_w48:\n\t" \
  "ds_read_b32 v246, %[l8] offset:47688\n\t" \
  "s_waitcnt lgkmcnt(0)\n\t" \
  "v_readfirstlane_b32 %[t0], v246\n\t" \
  "s_cmp_lt_u32 %[t0], %[tg]\n\t" \
  "s_cbranch_scc1 .Lcnx_w48\n\t" \
  "s_branch .Lcnx_b48\n\t" \
  ".Lcnx_w60:\n\t" \
  "ds_read_b32 v246, %[l8] offset:47696\n\t" \
  "s_waitcnt lgkmcnt(0)\n\t" \
  "v_readfirstlane_b32 %[t0], v246\n\t" \
  "s_cmp_lt_u32 %[t0], %[tg]\n\t" \
  "s_cbranch_scc1 .Lcnx_w60\n\t" \
  "s_branch .Lcnx_b60\n\t" \
  ".Lcnx_w72:\n\t" \
  "ds_read_b32 v246, %[l8] offset:47704\n\t" \
  "s_waitcnt lgkmcnt(0)\n\t" \
  "v_readfirstlane_b32 %[t0], v246\n\t" \
  "s_cmp_lt_u32 %[t0], %[tg]\n\t" \
  "s_cbranch_scc1 .Lcnx_w72\n\t" \
  "s_branch .Lcnx_b72\n\t" \
  ".Lcnx_w84:\n\t" \
  "ds_read_b32 v246, %[l8] offset:47712\n\t" \
  "s_waitcnt lgkmcnt(0)\n\t" \
  "v_readfirstlane_b32 %[t0], v246\n\t" \
  "s_cmp_lt_u32 %[t0], %[tg]\n\t" \
  "s_cbranch_scc1 .Lcnx_w84\n\t" \
  "s_branch .Lcnx_b84\n\t" \
  ".Lcnx_w96:\n\t" \
  "ds_read_b32 v246, %[l8] offset:47720\n\t" \
  "s_waitcnt lgkmcnt(0)\n\t" \
  "v_readfirstlane_b32 %[t0], v246\n\t" \
  "s_cmp_lt_u32 %[t0], %[tg]\n\t" \
  "s_cbranch_scc1 .Lcnx_w96\n\t" \
  "s_branch .Lcnx_b96\n\t" \
  ".Lcnx_w108:\n\t" \
  "ds_read_b32 v246, %[l8] offset:47728\n\t" \
  "s_waitcnt lgkmcnt(0)\n\t" \
  "v_readfirstlane_b32 %[t0], v246\n\t" \
  "s_cmp_lt_u32 %[t0], %[tg]\n\t" \
  "s_cbranch_scc1 .Lcnx_w108\n\t" \
  "s_branch .Lcnx_b108\n\t" \
  ".Lcnx_w120:\n\t" \
  "ds_read_b32 v246, %[l8] offset:47736\n\t" \
  "s_waitcnt lgkmcnt(0)\n\t" \
  "v_readfirstlane_b32 %[t0], v246\n\t" \
  "s_cmp_lt_u32 %[t0], %[tg]\n\t" \
  "s_cbranch_scc1 .Lcnx_w120\n\t" \
  "s_branch .Lcnx_b120\n\t" \
  ".Lcnx_w132:\n\t" \
  "ds_read_b32 v246, %[l8] offset:47744\n\t" \
  "s_waitcnt lgkmcnt(0)\n\t" \
  "v_readfirstlane_b32 %[t0], v246\n\t" \
  "s_cmp_lt_u32 %[t0], %[tg]\n\t" \
  "s_cbranch_scc1 .Lcnx_w132\n\t" \
  "s_branch .Lcnx_b132\n\t" \
  ".Lcnx_w138:\n\t" \
  "ds_read_b32 v246, %[l8] offset:47752\n\t" \
  "s_waitcnt lgkmcnt(0)\n\t" \
  "v_readfirstlane_b32 %[t0], v246\n\t" \
  "s_cmp_lt_u32 %[t0], %[tg]\n\t" \
  "s_cbranch_scc1 .Lcnx_w138\n\t" \
  "s_branch .Lcnx_b138\n\t" \
  ".Lcnx_f0:\n\t" \
  "s_mov_b32 m0, %[wd0]\n\t" \
  "s_nop 0\n\t" \
  "buffer_load_dwordx4 %[wv], %[wrs], %[so0] offen lds\n\t" \
  "buffer_load_dwordx4 %[wv], %[wrs], %[so1] offen offset:1024 lds\n\t" \
  "buffer_load_dwordx4 %[wv], %[wrs], %[so2] offen offset:2048 lds\n\t" \
  "s_add_u32 %[so0], %[so0], %[wc]\n\t" \
  "s_add_u32 %[so1], %[so1], %[wc]\n\t" \
  "s_add_u32 %[so2], %[so2], %[wc]\n\t" \
  "s_branch .Lcnx_g0\n\t" \
  ".Lcnx_f1:\n\t" \
  "s_mov_b32 m0, %[wd1]\n\t" \
  "s_nop 0\n\t" \
  "buffer_load_dwordx4 %[wv], %[wrs], %[so0] offen lds\n\t" \
  "buffer_load_dwordx4 %[wv], %[wrs], %[so1] offen offset:1024 lds\n\t" \
  "buffer_load_dwordx4 %[wv], %[wrs], %[so2] offen offset:2048 lds\n\t" \
  "s_add_u32 %[so0], %[so0], %[wc]\n\t" \
  "s_add_u32 %[so1], %[so1], %[wc]\n\t" \
  "s_add_u32 %[so2], %[so2], %[wc]\n\t" \
  "s_branch .Lcnx_g1\n\t" \
  ".Lcnx_f2:\n\t" \
  "s_mov_b32 m0, %[wd2]\n\t" \
  "s_nop 0\n\t" \
  "buffer_load_dwordx4 %[wv], %[wrs], %[so0] offen lds\n\t" \
  "buffer_load_dwordx4 %[wv], %[wrs], %[so1] offen offset:1024 lds\n\t" \
  "buffer_load_dwordx4 %[wv], %[wrs], %[so2] offen offset:2048 lds\n\t" \
  "s_add_u32 %[so0], %[so0], %[wc]\n\t" \
  "s_add_u32 %[so1], %[so1], %[wc]\n\t" \
  "s_add_u32 %[so2], %[so2], %[wc]\n\t" \
  "s_branch .Lcnx_g2\n\t" \
  ".Lcnx_f3:\n\t" \
  "s_mov_b32 m0, %[wd3]\n\t" \
  "s_nop 0\n\t" \
  "buffer_load_dwordx4 %[wv], %[wrs], %[so0] offen lds\n\t" \
  "buffer_load_dwordx4 %[wv], %[wrs], %[so1] offen offset:1024 lds\n\t" \
  "buffer_load_dwordx4 %[wv], %[wrs], %[so2] offen offset:2048 lds\n\t" \
  "s_add_u32 %[so0], %[so0], %[wc]\n\t" \
  "s_add_u32 %[so1], %[so1], %[wc]\n\t" \
  "s_add_u32 %[so2], %[so2], %[wc]\n\t" \
  "s_branch .Lcnx_g3\n\t" \
  ".Lcnx_f4:\n\t" \
  "s_mov_b32 m0, %[wd0]\n\t" \
  "s_nop 0\n\t" \
  "buffer_load_dwordx4 %[wv], %[wrs], %[so0] offen lds\n\t" \
  "buffer_load_dwordx4 %[wv], %[wrs], %[so1] offen offset:1024 lds\n\t" \
  "buffer_load_dwordx4 %[wv], %[wrs], %[so2] offen offset:2048 lds\n\t" \
  "s_add_u32 %[so0], %[so0], %[wc]\n\t" \
  "s_add_u32 %[so1], %[so1], %[wc]\n\t" \
  "s_add_u32 %[so2], %[so2], %[wc]\n\t" \
  "s_cmp_lg_u32 %[rnd], 1\n\t" \
  "s_cbranch_scc1 .Lcnx_g4\n\t" \
  "s_add_u32 m0, %[wd0], %[wdl]\n\t" \
  "s_add_u32 %[t0], %[so0], %[wc3]\n\t" \
  "buffer_load_dwordx4 %[wv], %[wrs], %[t0] offen offset:0 lds\n\t" \
  "s_add_u32 %[t0], %[so1], %[wc3]\n\t" \
  "buffer_load_dwordx4 %[wv], %[wrs], %[t0] offen offset:1024 lds\n\t" \
  "s_add_u32 %[t0], %[so2], %[wc3]\n\t" \
  "buffer_load_dwordx4 %[wv], %[wrs], %[t0] offen offset:2048 lds\n\t" \
  "s_branch .Lcnx_g4\n\t" \
  ".Lcnx_f5:\n\t" \
  "s_mov_b32 m0, %[wd1]\n\t" \
  "s_nop 0\n\t" \
  "buffer_load_dwordx4 %[wv], %[wrs], %[so0] offen lds\n\t" \
  "buffer_load_dwordx4 %[wv], %[wrs], %[so1] offen offset:1024 lds\n\t" \
  "buffer_load_dwordx4 %[wv], %[wrs], %[so2] offen offset:2048 lds\n\t" \
  "s_add_u32 %[so0], %[so0], %[wc]\n\t" \
  "s_add_u32 %[so1], %[so1], %[wc]\n\t" \
  "s_add_u32 %[so2], %[so2], %[wc]\n\t" \
  "s_cmp_lg_u32 %[rnd], 1\n\t" \
  "s_cbranch_scc1 .Lcnx_g5\n\t" \
  "s_add_u32 m0, %[wd1], %[wdl]\n\t" \
  "s_add_u32 %[t0], %[so0], %[wc3]\n\t" \
  "buffer_load_dwordx4 %[wv], %[wrs], %[t0] offen offset:0 lds\n\t" \
  "s_add_u32 %[t0], %[so1], %[wc3]\n\t" \
  "buffer_load_dwordx4 %[wv], %[wrs], %[t0] offen offset:1024 lds\n\t" \
  "s_add_u32 %[t0], %[so2], %[wc3]\n\t" \
  "buffer_load_dwordx4 %[wv], %[wrs], %[t0] offen offset:2048 lds\n\t" \
  "s_branch .Lcnx_g5\n\t" \
  ".Lcnx_f6:\n\t" \
  "s_mov_b32 m0, %[wd2]\n\t" \
  "s_nop 0\n\t" \
  "buffer_load_dwordx4 %[wv], %[wrs], %[so0] offen lds\n\t" \
  "buffer_load_dwordx4 %[wv], %[wrs], %[so1] offen offset:1024 lds\n\t" \
  "buffer_load_dwordx4 %[wv], %[wrs], %[so2] offen offset:2048 lds\n\t" \
  "s_add_u32 %[so0], %[so0], %[wc]\n\t" \
  "s_add_u32 %[so1], %[so1], %[wc]\n\t" \
  "s_add_u32 %[so2], %[so2], %[wc]\n\t" \
  "s_cmp_lg_u32 %[rnd], 1\n\t" \
  "s_cbranch_scc1 .Lcnx_g6\n\t" \
  "s_cmp_lt_u32 %[w512], 1024\n\t" \
  "s_cbranch_scc1 .Lcnx_g6\n\t" \
  "s_add_u32 m0, %[wd2], %[wdl]\n\t" \
  "s_add_u32 %[t0], %[so0], %[wc3]\n\t" \
  "buffer_load_dwordx4 %[wv], %[wrs], %[t0] offen offset:0 lds\n\t" \
  "s_add_u32 %[t0], %[so1], %[wc3]\n\t" \
  "buffer_load_dwordx4 %[wv], %[wrs], %[t0] offen offset:1024 lds\n\t" \
  "s_add_u32 %[t0], %[so2], %[wc3]\n\t" \
  "buffer_load_dwordx4 %[wv], %[wrs], %[t0] offen offset:2048 lds\n\t" \
  "s_branch .Lcnx_g6\n\t" \
  ".Lcnx_f7:\n\t" \
  "s_mov_b32 m0, %[wd3]\n\t" \
  "s_nop 0\n\t" \
  "buffer_load_dwordx4 %[wv], %[wrs], %[so0] offen lds\n\t" \
  "buffer_load_dwordx4 %[wv], %[wrs], %[so1] offen offset:1024 lds\n\t" \
  "buffer_load_dwordx4 %[wv], %[wrs], %[so2] offen offset:2048 lds\n\t" \
  "s_add_u32 %[so0], %[so0], %[wc]\n\t" \
  "s_add_u32 %[so1], %[so1], %[wc]\n\t" \
  "s_add_u32 %[so2], %[so2], %[wc]\n\t" \
  "s_cmp_lg_u32 %[rnd], 1\n\t" \
  "s_cbranch_scc1 .Lcnx_g7\n\t" \
  "s_cmp_lt_u32 %[w512], 1024\n\t" \
  "s_cbranch_scc1 .Lcnx_g7\n\t" \
  "s_add_u32 m0, %[wd3], %[wdl]\n\t" \
  "s_add_u32 %[t0], %[so0], %[wc3]\n\t" \
  "buffer_load_dwordx4 %[wv], %[wrs], %[t0] offen offset:0 lds\n\t" \
  "s_add_u32 %[t0], %[so1], %[wc3]\n\t" \
  "buffer_load_dwordx4 %[wv], %[wrs], %[t0] offen offset:1024 lds\n\t" \
  "s_add_u32 %[t0], %[so2], %[wc3]\n\t" \
  "buffer_load_dwordx4 %[wv], %[wrs], %[t0] offen offset:2048 lds\n\t" \
  "s_branch .Lcnx_g7\n\t" \
  ".Lcnx_f8:\n\t" \
  "s_mov_b32 m0, %[wd0]\n\t" \
  "s_nop 0\n\t" \
  "buffer_load_dwordx4 %[wv], %[wrs], %[so0] offen lds\n\t" \
  "buffer_load_dwordx4 %[wv], %[wrs], %[so1] offen offset:1024 lds\n\t" \
  "buffer_load_dwordx4 %[wv], %[wrs], %[so2] offen offset:2048 lds\n\t" \
  "s_add_u32 %[so0], %[so0], %[wc]\n\t" \
  "s_add_u32 %[so1], %[so1], %[wc]\n\t" \
  "s_add_u32 %[so2], %[so2], %[wc]\n\t" \
  "s_branch .Lcnx_g8\n\t" \
  ".Lcnx_f9:\n\t" \
  "s_mov_b32 m0, %[wd1]\n\t" \
  "s_nop 0\n\t" \
  "buffer_load_dwordx4 %[wv], %[wrs], %[so0] offen lds\n\t" \
  "buffer_load_dwordx4 %[wv], %[wrs], %[so1] offen offset:1024 lds\n\t" \
  "buffer_load_dwordx4 %[wv], %[wrs], %[so2] offen offset:2048 lds\n\t" \
  "s_add_u32 %[so0], %[so0], %[wc]\n\t" \
  "s_add_u32 %[so1], %[so1], %[wc]\n\t" \
  "s_add_u32 %[so2], %[so2], %[wc]\n\t" \
  "s_sub_u32 %[so0], %[so0], %[wr1]\n\t" \
  "s_sub_u32 %[so1], %[so1], %[wr1]\n\t" \
  "s_sub_u32 %[so2], %[so2], %[wr1]\n\t" \
  "s_branch .Lcnx_g9\n\t" \
  ".Lcnx_f10:\n\t" \
  "s_mov_b32 m0, %[wd2]\n\t" \
  "s_nop 0\n\t" \
  "buffer_load_dwordx4 %[wv], %[wrs], %[so0] offen lds\n\t" \
  "buffer_load_dwordx4 %[wv], %[wrs], %[so1] offen offset:1024 lds\n\t" \
  "buffer_load_dwordx4 %[wv], %[wrs], %[so2] offen offset:2048 lds\n\t" \
  "s_add_u32 %[so0], %[so0], %[wc]\n\t" \
  "s_add_u32 %[so1], %[so1], %[wc]\n\t" \
  "s_add_u32 %[so2], %[so2], %[wc]\n\t" \
  "s_branch .Lcnx_g10\n\t" \
  ".Lcnx_f11:\n\t" \
  "s_mov_b32 m0, %[wd3]\n\t" \
  "s_nop 0\n\t" \
  "buffer_load_dwordx4 %[wv], %[wrs], %[so0] offen lds\n\t" \
  "buffer_load_dwordx4 %[wv], %[wrs], %[so1] offen offset:1024 lds\n\t" \
  "buffer_load_dwordx4 %[wv], %[wrs], %[so2] offen offset:2048 lds\n\t" \
  "s_add_u32 %[so0], %[so0], %[wc]\n\t" \
  "s_add_u32 %[so1], %[so1], %[wc]\n\t" \
  "s_add_u32 %[so2], %[so2], %[wc]\n\t" \
  "s_sub_u32 %[so0], %[so0], %[wr2]\n\t" \
  "s_sub_u32 %[so1], %[so1], %[wr2]\n\t" \
  "s_sub_u32 %[so2], %[so2], %[wr2]\n\t" \
  "s_branch .Lcnx_g11\n\t" \
  ".Lcnx_fin:\n\t" \
  "v_mfma_f32_16x16x32_bf16 v[88:91], v[242:245], v[236:239], v[88:91]\n\t" \
  "ds_read_b64_tr_b16 v[214:215], %[a2] offset:7696\n\t" \
  "v_mfma_f32_16x16x32_bf16 v[92:95], v[240:243], v[236:239], v[92:95]\n\t" \
  "v_mfma_f32_16x16x32_bf16 v[0:3], v[208:211], v[178:181], v[0:3]\n\t" \
  "v_mfma_f32_16x16x32_bf16 v[24:27], v[208:211], v[186:189], v[24:27]\n\t" \
  "v_mfma_f32_16x16x32_bf16 v[48:51], v[208:211], v[194:197], v[48:51]\n\t" \
  "v_mfma_f32_16x16x32_bf16 v[72:75], v[208:211], v[202:205], v[72:75]\n\t" \
  "ds_read_b64_tr_b16 v[208:209], %[a2] offset:6216\n\t" \
  "s_waitcnt lgkmcnt(1)\n\t" \
  "v_mfma_f32_16x16x32_bf16 v[4:7], v[212:215], v[178:181], v[4:7]\n\t" \
  "ds_read_b64_tr_b16 v[210:211], %[a2] offset:7752\n\t" \
  "v_mfma_f32_16x16x32_bf16 v[28:31], v[212:215], v[186:189], v[28:31]\n\t" \
  "v_mfma_f32_16x16x32_bf16 v[52:55], v[212:215], v[194:197], v[52:55]\n\t" \
  "v_mfma_f32_16x16x32_bf16 v[76:79], v[212:215], v[202:205], v[76:79]\n\t" \
  "ds_read_b64_tr_b16 v[212:213], %[a2] offset:6224\n\t" \
  "s_waitcnt lgkmcnt(1)\n\t" \
  "v_mfma_f32_16x16x32_bf16 v[8:11], v[208:211], v[178:181], v[8:11]\n\t" \
  "ds_read_b64_tr_b16 v[214:215], %[a2] offset:7760\n\t" \
  "v_mfma_f32_16x16x32_bf16 v[32:35], v[208:211], v[186:189], v[32:35]\n\t" \
  "v_mfma_f32_16x16x32_bf16 v[56:59], v[208:211], v[194:197], v[56:59]\n\t" \
  "v_mfma_f32_16x16x32_bf16 v[80:83], v[208:211], v[202:205], v[80:83]\n\t" \
  "ds_read_b64_tr_b16 v[208:209], %[a2] offset:6280\n\t" \
  "v_cvt_pk_bf16_f32 v0, v0, v1\n\t" \
  "v_cvt_pk_bf16_f32 v1, v2, v3\n\t" \
  "v_cvt_pk_bf16_f32 v2, v4, v5\n\t" \
  "v_cvt_pk_bf16_f32 v3, v6, v7\n\t" \
  "buffer_store_dwordx4 v[0:3], %[vo], %[ors], %[oc0] offen offset:0 sc1\n\t" \
  "v_cvt_pk_bf16_f32 v24, v24, v25\n\t" \
  "v_cvt_pk_bf16_f32 v25, v26, v27\n\t" \
  "v_cvt_pk_bf16_f32 v26, v28, v29\n\t" \
  "v_cvt_pk_bf16_f32 v27, v30, v31\n\t" \
  "buffer_store_dwordx4 v[24:27], %[vo], %[ors], %[oc0] offen offset:192 sc1\n\t" \
  "v_cvt_pk_bf16_f32 v48, v48, v49\n\t" \
  "v_cvt_pk_bf16_f32 v49, v50, v51\n\t" \
  "v_cvt_pk_bf16_f32 v50, v52, v53\n\t" \
  "v_cvt_pk_bf16_f32 v51, v54, v55\n\t" \
  "buffer_store_dwordx4 v[48:51], %[vo], %[ors], %[oc1] offen offset:0 sc1\n\t" \
  "v_cvt_pk_bf16_f32 v72, v72, v73\n\t" \
  "v_cvt_pk_bf16_f32 v73, v74, v75\n\t" \
  "v_cvt_pk_bf16_f32 v74, v76, v77\n\t" \
  "v_cvt_pk_bf16_f32 v75, v78, v79\n\t" \
  "buffer_store_dwordx4 v[72:75], %[vo], %[ors], %[oc1] offen offset:192 sc1\n\t" \
  "s_waitcnt lgkmcnt(1)\n\t" \
  "v_mfma_f32_16x16x32_bf16 v[12:15], v[212:215], v[178:181], v[12:15]\n\t" \
  "ds_read_b64_tr_b16 v[210:211], %[a2] offset:7816\n\t" \
  "v_mfma_f32_16x16x32_bf16 v[36:39], v[212:215], v[186:189], v[36:39]\n\t" \
  "v_mfma_f32_16x16x32_bf16 v[60:63], v[212:215], v[194:197], v[60:63]\n\t" \
  "v_mfma_f32_16x16x32_bf16 v[84:87], v[212:215], v[202:205], v[84:87]\n\t" \
  "ds_read_b64_tr_b16 v[212:213], %[a2] offset:6288\n\t" \
  "s_waitcnt lgkmcnt(1)\n\t" \
  "v_mfma_f32_16x16x32_bf16 v[16:19], v[208:211], v[178:181], v[16:19]\n\t" \
  "ds_read_b64_tr_b16 v[214:215], %[a2] offset:7824\n\t" \
  "v_mfma_f32_16x16x32_bf16 v[40:43], v[208:211], v[186:189], v[40:43]\n\t" \
  "v_mfma_f32_16x16x32_bf16 v[64:67], v[208:211], v[194:197], v[64:67]\n\t" \
  "v_mfma_f32_16x16x32_bf16 v[88:91], v[208:211], v[202:205], v[88:91]\n\t" \
  "v_cvt_pk_bf16_f32 v8, v8, v9\n\t" \
  "v_cvt_pk_bf16_f32 v9, v10, v11\n\t" \
  "v_cvt_pk_bf16_f32 v10, v12, v13\n\t" \
  "v_cvt_pk_bf16_f32 v11, v14, v15\n\t" \
  "buffer_store_dwordx4 v[8:11], %[vo], %[ors], %[oc0] offen offset:64 sc1\n\t" \
  "v_cvt_pk_bf16_f32 v32, v32, v33\n\t" \
  "v_cvt_pk_bf16_f32 v33, v34, v35\n\t" \
  "v_cvt_pk_bf16_f32 v34, v36, v37\n\t" \
  "v_cvt_pk_bf16_f32 v35, v38, v39\n\t" \
  "buffer_store_dwordx4 v[32:35], %[vo], %[ors], %[oc0] offen offset:256 sc1\n\t" \
  "v_cvt_pk_bf16_f32 v56, v56, v57\n\t" \
  "v_cvt_pk_bf16_f32 v57, v58, v59\n\t" \
  "v_cvt_pk_bf16_f32 v58, v60, v61\n\t" \
  "v_cvt_pk_bf16_f32 v59, v62, v63\n\t" \
  "buffer_store_dwordx4 v[56:59], %[vo], %[ors], %[oc1] offen offset:64 sc1\n\t" \
  "v_cvt_pk_bf16_f32 v80, v80, v81\n\t" \
  "v_cvt_pk_bf16_f32 v81, v82, v83\n\t" \
  "v_cvt_pk_bf16_f32 v82, v84, v85\n\t" \
  "v_cvt_pk_bf16_f32 v83, v86, v87\n\t" \
  "buffer_store_dwordx4 v[80:83], %[vo], %[ors], %[oc1] offen offset:256 sc1\n\t" \
  "s_waitcnt lgkmcnt(0)\n\t" \
  "v_mfma_f32_16x16x32_bf16 v[20:23], v[212:215], v[178:181], v[20:23]\n\t" \
  "v_mfma_f32_16x16x32_bf16 v[44:47], v[212:215], v[186:189], v[44:47]\n\t" \
  "v_mfma_f32_16x16x32_bf16 v[68:71], v[212:215], v[194:197], v[68:71]\n\t" \
  "v_mfma_f32_16x16x32_bf16 v[92:95], v[212:215], v[202:205], v[92:95]\n\t" \
  "v_cvt_pk_bf16_f32 v16, v16, v17\n\t" \
  "v_cvt_pk_bf16_f32 v17, v18, v19\n\t" \
  "s_nop 2\n\t" \
  "v_cvt_pk_bf16_f32 v18, v20, v21\n\t" \
  "v_cvt_pk_bf16_f32 v19, v22, v23\n\t" \
  "buffer_store_dwordx4 v[16:19], %[vo], %[ors], %[oc0] offen offset:128 sc1\n\t" \
  "v_cvt_pk_bf16_f32 v40, v40, v41\n\t" \
  "v_cvt_pk_bf16_f32 v41, v42, v43\n\t" \
  "v_cvt_pk_bf16_f32 v42, v44, v45\n\t" \
  "v_cvt_pk_bf16_f32 v43, v46, v47\n\t" \
  "buffer_store_dwordx4 v[40:43], %[vo], %[ors], %[oc0] offen offset:320 sc1\n\t" \
  "v_cvt_pk_bf16_f32 v64, v64, v65\n\t" \
  "v_cvt_pk_bf16_f32 v65, v66, v67\n\t" \
  "v_cvt_pk_bf16_f32 v66, v68, v69\n\t" \
  "v_cvt_pk_bf16_f32 v67, v70, v71\n\t" \
  "buffer_store_dwordx4 v[64:67], %[vo], %[ors], %[oc1] offen offset:128 sc1\n\t" \
  "v_cvt_pk_bf16_f32 v88, v88, v89\n\t" \
  "v_cvt_pk_bf16_f32 v89, v90, v91\n\t" \
  "v_cvt_pk_bf16_f32 v90, v92, v93\n\t" \
  "v_cvt_pk_bf16_f32 v91, v94, v95\n\t" \
  "buffer_store_dwordx4 v[88:91], %[vo], %[ors], %[oc1] offen offset:320 sc1\n\t" \
  ""


__device__ __forceinline__ unsigned lds_addr(const char* p) { return (unsigned)(uintptr_t)(lds_ptr_t)p; }
__device__ __forceinline__ unsigned sg(unsigned v) { return __builtin_amdgcn_readfirstlane(v); }

__device__ __forceinline__ void warm(u32x4 rsrc, unsigned voff, unsigned soff, unsigned lds_dst) {
    asm volatile("s_mov_b32 m0, %1\n\ts_nop 0\n\tbuffer_load_dword %0, %2, %3 offen lds"
                 : : "v"(voff), "s"(lds_dst), "s"(rsrc), "s"(soff) : "memory");
}
__device__ __forceinline__ u32x4 make_rsrc(unsigned lo, unsigned hi, unsigned bytes) {
    u32x4 r = {sg(lo), sg(hi & 0xffffu), sg(bytes), 0x00020000u};
    return r;
}

#define V10(a) "v" #a "0", "v" #a "1", "v" #a "2", "v" #a "3", "v" #a "4", "v" #a "5", "v" #a "6", "v" #a "7", "v" #a "8", "v" #a "9"
#define MAIN_CLOB "memory", "scc", V10(), "vcc", V10(1), V10(2), V10(3), V10(4), V10(5), V10(6), V10(7), V10(8), V10(9), V10(10), V10(11), \
    V10(12), V10(13), V10(14), V10(15), V10(16), V10(17), V10(18), V10(19), V10(20), V10(21), V10(22),                            \
    V10(23)

__global__ __launch_bounds__(512, 2) void convnext_kernel(const __hip_bfloat16* __restrict__ x,
                                                          const __hip_bfloat16* __restrict__ dw,
                                                          const __hip_bfloat16* __restrict__ w1,
                                                          const __hip_bfloat16* __restrict__ w2,
                                                          __hip_bfloat16* __restrict__ out) {
    __shared__ __attribute__((aligned(16))) char smem[LDS_BYTES];
    u32x8 ka;
    u32x2 ko;
    asm volatile("s_load_dwordx8 %0, %2, 0x0\n\ts_load_dwordx2 %1, %2, 0x20"
                 : "=&s"(ka), "=&s"(ko) : "s"(__builtin_amdgcn_kernarg_segment_ptr()) : "memory");
    const int tid = threadIdx.x;
    const int lane = tid & 63;
    const int wid = sg(tid >> 6);
    const int bid = blockIdx.x;

    const unsigned sbase = lds_addr(smem);
    if (wid == 3) {
        unsigned long long pc;
        unsigned len;
        asm volatile("s_getpc_b64 %0\n.Lcnx_pc:\n\ts_mov_b32 %1, .Lcnx_end-.Lcnx_pc" : "=s"(pc), "=s"(len));
        warm(make_rsrc((unsigned)pc, (unsigned)(pc >> 32), len & ~3u), lane < 20 ? lane * 64 : OOB, (bid >> 3) * 1280, sbase + PFOFF);
    }
    const int x0 = ((bid >> 3) & 15) * 32, low = bid >> 7;
    const int ym = (bid & 7) * 64 + 31 + low;
    unsigned rs = low ? ROWB : -ROWB;
    asm volatile("s_setreg_imm32_b32 hwreg(HW_REG_MODE, 4, 2), 0\n\ts_setreg_imm32_b32 hwreg(HW_REG_MODE, 9, 1), 0\n\ts_nop 1");

    const int ks = (tid >> 2) & 7, kq = (tid >> 5) * 4 + (tid & 3);
    unsigned dwv = ks == 0 ? OOB : (unsigned)((low ? ks - 1 : 7 - ks) * (7 * CH * 2) + kq * 8);
    unsigned dwa = kq * 768 + ks * 24 + (tid & 1) * 192, dwb = kq * 768 + ks * 24 + (~tid & 1) * 192, dsel = tid & 1 ? 0x07060302u : 0x05040100u;
    const int pp = tid / 25, s = tid - pp * 25;
    const int gx = x0 - 3 + 2 * pp + (s >= 12);
    unsigned xv = (s < 24 && pp < 19 && (unsigned)gx < (unsigned)IMG) ? (unsigned)((x0 - 3 + 2 * pp) * (CH * 2) + s * 16) : OOB;
    unsigned xo = ym * ROWB - 4 * rs, xl = sbase + XOFF + wid * 1024;

    const bool isw1 = wid < 2;
    const int G = lane >> 4, n = lane & 15, q = n >> 2, p = lane & 3;
    unsigned a1 = sbase + WB + (lane >> 2) * 32 + (p & 1) * 16 + (p >> 1) * 8;
    unsigned a2 = sbase + WB + (G & 1) * 3080 + ((G >> 1) * 4 + q) * 192 + p * 16;
    unsigned wv = wid >= 4 ? (lane < 48 ? (unsigned)(x0 * (CH * 2) + lane * 128) : OOB)
                : isw1     ? (unsigned)((12 * ((lane >> 3) & 3) + ((lane >> 1) & 3)) * (HID * 2) + (2 * (lane & 1) + (lane >> 5)) * 16)
                           : (unsigned)(lane * 16);
    u32x2 ca = {sbase + XOFF + n * 400 + (G & 1) * XROW + (G >> 1) * 192,
                sbase + DWOFF + p * 192 + (1 - (q >> 1)) * 24 + (1 + (G & 1) - (q & 1)) * 8 + (G >> 1) * 4};
    if (wid == 3) {
        *(u32x2*)(smem + TABOFF + lane * 8) = ca;
        *(unsigned*)(smem + (lane < 36 ? CNTOFF : DWOFF + 96 * 192 - 36 * 4) + (lane < 44 ? lane : 36) * 4) = 0;
    }
    ca += (u32x2){(unsigned)wid * 24, (unsigned)wid * 2304};
    unsigned vo = n * 384 + G * 16;
    unsigned l8 = sbase + PB + lane * 8;
    const unsigned one = G == q ? (p & 1 ? 0x3f800000u : 0x3f80u) : 0u;
    u32x2 id2 = {p < 2 ? one : 0u, p < 2 ? 0u : one}, z0 = {0u, 0u}, z1 = {0u, 0u};
    unsigned h2 = GH2;
    asm volatile("" : "+v"(a1), "+v"(a2), "+v"(wv), "+{v[246:247]}"(ca), "+v"(vo), "+v"(l8), "+{v[242:243]}"(id2), "+{v[240:241]}"(z0),
                      "+{v[244:245]}"(z1), "+v"(h2), "+v"(dwa), "+v"(dwb), "+v"(dwv), "+v"(dsel), "+v"(xv));
    const unsigned wpd = isw1 ? 4 * HID * 2 - 1024 : 0, wl = sbase + WB + (wid & 1) * (isw1 ? 3072 : 3080);
    unsigned wc = isw1 ? 64 : 6144;
    unsigned so0 = (wid & 1) * (isw1 ? 48 * HID * 2 : 3072), so1 = so0 + wpd, so2 = so1 + wpd;
    unsigned oc0 = ym * ROWB + 2 * wid * rs + x0 * (CH * 2), oc1 = oc0 + rs, op0, op1;
    unsigned rnd, f0 = wid, t0, tg, d1, d2, grp = wid >> 2, wq = wid, wb = xo + (22 + (wid & 3)) * rs;
    unsigned rs4 = 4 * rs, rs6 = 6 * rs, dy = 16 * rs, zb = XOFF - 8 * PB - 7 * sbase + wid * 12288;
    unsigned sr = (wid & 3) * 4096 + (wid < 4 ? 16400 : 0), w512 = wid * 512;
    unsigned zw1 = XOFF - 2 * PB - sbase + wid * 1024, zw2 = XOFF - 2 * PB - sbase + (8 + (wid & 3)) * 1024 + (wid >> 2) * 8;
    unsigned wd0 = wl + (isw1 ? WA + 12304 : WA), wd1 = wl + (isw1 ? 12304 : WA + 6152);
    unsigned wd2 = wl + (isw1 ? WA + 18448 : 0), wd3 = wl + (isw1 ? WA + 18448 + 6144 : 6152);
    unsigned wr1 = isw1 ? 12 * wc : 0, wr2 = isw1 ? 0 : 12 * wc, wdl = isw1 ? -86104 : -36904, wc3 = 3 * wc;
    unsigned c24 = wid * 24, c23 = wid * 2304;
    asm volatile("" : "+s"(wc), "+s"(so0), "+s"(so1), "+s"(so2), "+s"(oc0), "+s"(oc1), "+s"(f0), "+s"(grp), "+s"(wq), "+s"(wb), "+s"(rs4),
                      "+s"(rs6), "+s"(dy), "+s"(zb), "+s"(sr), "+s"(w512), "+s"(zw1), "+s"(zw2));
    asm volatile("" : "+s"(wd0), "+s"(wd1), "+s"(wd2), "+s"(wd3), "+s"(wr1), "+s"(wr2), "+s"(wdl), "+s"(wc3), "+s"(c24), "+s"(c23),
                      "+s"(xo), "+s"(xl), "+s"(rs));

    asm volatile("s_waitcnt lgkmcnt(0)" : "+s"(ka), "+s"(ko) : : "memory");

    const u32x4 xrs = make_rsrc(ka[0], ka[1], IMG * ROWB);
#define XROWS(name) asm volatile(name : [xo] "+&s"(xo) : [xv] "v"(xv), [xrs] "s"(xrs), [xl] "s"(xl), [rs] "s"(rs) : "memory", "scc")
    if (wid < 3) {
        const u32x4 drs = make_rsrc(ka[2], ka[3], 49 * CH * 2);
        u32x2 t[7];
#pragma unroll
        for (int j = 0; j < 7; ++j)
            asm volatile("buffer_load_dwordx2 %0, %1, %2, 0 offen offset:%3" : "=&v"(t[j]) : "v"(dwv), "s"(drs), "n"(j * CH * 2) : "memory");
        XROWS(XA_ASM);
        asm volatile("s_waitcnt vmcnt(4)" ::: "memory");
#pragma unroll
        for (int j = 0; j < 7; ++j) asm volatile("" : "+v"(t[j]));
#pragma unroll
        for (int c = 0; c < 4; ++c) {
            const unsigned sel = c & 1 ? dsel ^ 0x02020202u : dsel;
            auto pk = [&](int i, int j) { return __builtin_amdgcn_perm(j < 0 ? 0u : t[j][c >> 1], i < 0 ? 0u : t[i][c >> 1], sel); };
            char* bp = smem + DWOFF + (c & 1 ? dwb : dwa) + (c >> 1) * 384;
            *(u32x2*)(bp) = (u32x2){pk(-1, 1), pk(3, 5)};
            *(u32x2*)(bp + 8) = (u32x2){pk(0, 2), pk(4, 6)};
            *(u32x2*)(bp + 16) = (u32x2){pk(1, 3), pk(5, -1)};
        }
        XROWS(XB_ASM);
    } else {
        if (wid == 3) {
            warm(make_rsrc(ka[4], ka[5], CH * HID * 2), lane < 36 ? lane * 64 : OOB, (bid >> 3) * 2304, sbase + PFOFF);
            warm(make_rsrc(ka[6], ka[7], CH * HID * 2), lane < 36 ? lane * 64 : OOB, (bid >> 3) * 2304, sbase + PFOFF);
        }
        XROWS(XA_ASM);
        if (0) XROWS(XB_ASM);
        if (wid >= 4) {
#pragma unroll
            for (int h = 0; h < 4; ++h) {
                f32x4 tv4;
                const unsigned k0 = h * 1024 + (tid - 256) * 4;
#pragma unroll
                for (int j = 0; j < 4; ++j) {
                    float v = __builtin_bit_cast(float, (k0 + j) * 32768 + 12288 + (115u << 23));
                    if (h == 0) v = __builtin_fminf(v, __builtin_fmaf(v, 2.0f, -0x1p-11f));
                    const float e = __builtin_amdgcn_exp2f(v * __builtin_fmaf(v * v, 0.10294324f, 2.3022082f));
                    tv4[j] = __builtin_fmaf(__builtin_amdgcn_rcpf(1.0f + e), -2.0f, 1.0f);
                }
                if (h < 3 || k0 < LUTN) *(f32x4*)(smem + k0 * 4) = tv4;
            }
        }
        if (!0) XROWS(XB_ASM);
    }

    const u32x4 wrs = make_rsrc(isw1 ? ka[4] : ka[6], isw1 ? ka[5] : ka[7], CH * HID * 2);
    const u32x4 ors = make_rsrc(ko[0], ko[1], IMG * ROWB);
    if (wid >= 4) __builtin_amdgcn_s_setprio(1);


    asm volatile(MAIN_ASM
                 : [xo] "+&s"(xo), [oc0] "+&s"(oc0), [oc1] "+&s"(oc1), [op0] "=&s"(op0), [op1] "=&s"(op1), [rnd] "=&s"(rnd), [f0] "+&s"(f0),
                   [so0] "+&s"(so0), [so1] "+&s"(so1), [so2] "+&s"(so2), [t0] "=&s"(t0), [tg] "=&s"(tg), [d1] "=&s"(d1), [d2] "=&s"(d2),
                   [grp] "+&s"(grp), [a1] "+&v"(a1), [a2] "+&v"(a2), [wq] "+&s"(wq), [wb] "+&s"(wb), "+{v[246:247]}"(ca)
                 : [wv] "v"(wv), [xv] "v"(xv), [h2] "v"(h2), [vo] "v"(vo), [l8] "v"(l8), "{v[240:241]}"(z0), "{v[242:243]}"(id2),
                   "{v[244:245]}"(z1), [xrs] "s"(xrs), [ors] "s"(ors), [wrs] "s"(wrs), [xl] "s"(xl), [rs] "s"(rs), [rs6] "s"(rs6),
                   [dy] "s"(dy), [zb] "s"(zb), [sr] "s"(sr), [w512] "s"(w512), [z1] "s"(zw1), [z2] "s"(zw2), [wd0] "s"(wd0),
                   [wd1] "s"(wd1), [wd2] "s"(wd2), [wd3] "s"(wd3), [wr1] "s"(wr1), [wr2] "s"(wr2), [rs4] "s"(rs4), [wdl] "s"(wdl),
                   [wc3] "s"(wc3), [wc] "s"(wc), [c24] "s"(c24), [c23] "s"(c23), [pf] "n"(PFOFF), [h1] "s"(GH1), [hh] "s"(GHH),
                   [mk] "s"(0x3ffcu)
                 : MAIN_CLOB);
    asm volatile("s_waitcnt vmcnt(0)\n.Lcnx_end:" ::: "memory");

}

}

extern "C" void kernel_launch(void* const* d_in, const int* in_sizes, int n_in, void* d_out, int out_size,
                              void* d_ws, size_t ws_size, hipStream_t stream) {
    (void)in_sizes; (void)n_in; (void)out_size; (void)d_ws; (void)ws_size;
    const __hip_bfloat16* x = (const __hip_bfloat16*)d_in[0];
    const __hip_bfloat16* dw = (const __hip_bfloat16*)d_in[1];
    const __hip_bfloat16* w1 = (const __hip_bfloat16*)d_in[2];
    const __hip_bfloat16* w2 = (const __hip_bfloat16*)d_in[3];
    __hip_bfloat16* out = (__hip_bfloat16*)d_out;
    convnext_kernel<<<256, 512, 0, stream>>>(x, dw, w1, w2, out);
}
```

```cpp
#include <hip/hip_runtime.h>
#include <hip/hip_bf16.h>
#include <stdint.h>

namespace {

typedef __attribute__((address_space(3))) void* lds_ptr_t;

constexpr int IMG = 512;
constexpr int CH = 96;
constexpr int HID = 384;
constexpr int ROWB = IMG * CH * 2;
constexpr int LUTN = 3841;
constexpr int DWOFF = 15376;
constexpr int XOFF = DWOFF + 96 * 192 + 32;
constexpr int XROW = 8200;
constexpr int PB = XOFF + 10 * XROW;
constexpr int WB = XOFF + 9 * XROW;
constexpr int WA = 3 * XROW;
constexpr int TABOFF = WB + WA + 18448 + 12288;
constexpr int CNTOFF = TABOFF + 512;
constexpr int PFOFF = CNTOFF + 144;
constexpr int LDS_BYTES = 160 * 1024;
static_assert(PB == 115840 && CNTOFF - PB == 47648 && LUTN * 4 <= DWOFF && XOFF % 16 == 0 && PFOFF + 48 * 4 <= LDS_BYTES, "LDS map");
constexpr unsigned OOB = 0x40000000u;
constexpr unsigned GH1 = 0xcc9bcc9bu, GH2 = 0xd297d297u, GHH = 0x30003000u;

#define MAIN_ASM \
  "s_load_dwordx2 s[8:9], s[0:1], 0x0\n\t" \
  "s_load_dwordx2 s[12:13], s[0:1], 0x8\n\t" \
  "s_load_dwordx2 s[20:21], s[0:1], 0x20\n\t" \
  "v_readfirstlane_b32 s5, v0\n\t" \
  "v_and_b32 v21, 63, v0\n\t" \
  "s_mov_b32 m0, 0\n\t" \
  "s_cmp_lt_u32 s5, 128\n\t" \
  "s_cselect_b32 s3, 16, 24\n\t" \
  "v_lshlrev_b32 v22, 4, v21\n\t" \
  "s_load_dwordx2 s[16:17], s[0:1], s3\n\t" \
  "s_mul_i32 s4, s5, 34\n\t" \
  "s_add_u32 s66, s6, s4\n\t" \
  "s_addc_u32 s67, s7, 0\n\t" \
  "global_load_dwordx4 v[244:247], v22, s[66:67] offset:2048\n\t" \
  "global_load_dwordx4 v[16:19], v22, s[66:67] offset:3072\n\t" \
  "global_load_dwordx4 v[240:243], v22, s[6:7]\n\t" \
  "global_load_dwordx4 v[248:251], v22, s[6:7] offset:1024\n\t" \
  "s_cmp_lg_u32 s5, 192\n\t" \
  "s_cbranch_scc1 .Lcnx_nc\n\t" \
  "s_getpc_b64 s[14:15]\n\t" \
  ".Lcnx_pc:\n\t" \
  "s_lshr_b32 s3, s2, 3\n\t" \
  "s_lshl_b32 s3, s3, 11\n\t" \
  "v_lshl_add_u32 v1, v21, 5, s3\n\t" \
  "v_min_u32 v1, .Lcnx_end-.Lcnx_pc, v1\n\t" \
  "global_load_lds_dword v1, s[14:15]\n\t" \
  ".Lcnx_nc:\n\t" \
  "v_mul_u32_u24 v254, 0x147b, v0\n\t" \
  "s_bfe_u32 s27, s2, 0x40003\n\t" \
  "v_bfe_u32 v1, v0, 2, 3\n\t" \
  "s_mul_i32 s27, s27, 6144\n\t" \
  "v_lshrrev_b32 v254, 17, v254\n\t" \
  "s_sub_u32 s3, s27, 576\n\t" \
  "v_and_b32 v20, 3, v0\n\t" \
  "s_add_u32 s4, s27, 6720\n\t" \
  "v_sub_u32 v254, v0, v254\n\t" \
  "s_min_u32 s4, s4, 0x18000\n\t" \
  "v_and_b32 v2, 0x1e0, v0\n\t" \
  "s_and_b32 s24, s2, 7\n\t" \
  "v_lshl_add_u32 v254, v254, 4, s3\n\t" \
  "s_lshl_b32 s24, s24, 6\n\t" \
  "v_lshl_or_b32 v20, v20, 3, v2\n\t" \
  "s_mov_b32 s26, 0x18000\n\t" \
  "v_cmp_gt_u32 vcc, s4, v254\n\t" \
  "s_cmp_ge_u32 s2, 128\n\t" \
  "s_cselect_b32 s3, 7, 0\n\t" \
  "s_cselect_b32 s4, 0, 7\n\t" \
  "v_add_u32 v1, s3, v1\n\t" \
  "s_cselect_b32 s3, 28, 35\n\t" \
  "v_cndmask_b32 v254, 2.0, v254, vcc\n\t" \
  "s_cselect_b32 s26, s26, 0xfffe8000\n\t" \
  "v_xor_b32 v1, s4, v1\n\t" \
  "s_add_u32 s24, s24, s3\n\t" \
  "v_and_b32 v1, 7, v1\n\t" \
  "s_movk_i32 s3, 1344\n\t" \
  "s_mul_i32 s24, s24, 0x18000\n\t" \
  "v_mad_u32_u24 v1, v1, s3, v20\n\t" \
  "s_lshl4_add_u32 s25, s5, 33840\n\t" \
  "s_mov_b32 s10, 0x3000000\n\t" \
  "s_mov_b32 s11, 0x20000\n\t" \
  "s_mov_b32 s14, 9408\n\t" \
  "s_mov_b32 s15, 0x20000\n\t" \
  "s_mov_b32 s18, 73728\n\t" \
  "s_mov_b32 s19, 0x20000\n\t" \
  "s_mov_b32 s22, 0x3000000\n\t" \
  "s_mov_b32 s23, 0x20000\n\t" \
  "s_setreg_imm32_b32 hwreg(HW_REG_MODE, 4, 2), 0\n\t" \
  "s_setreg_imm32_b32 hwreg(HW_REG_MODE, 9, 1), 0\n\t" \
  "s_lshl_b32 s65, s26, 4\n\t" \
  "s_lshr_b32 s3, s5, 5\n\t" \
  "s_add_u32 s3, s3, 4\n\t" \
  "s_mul_i32 s3, s3, s26\n\t" \
  "s_add_u32 s28, s24, s27\n\t" \
  "s_add_u32 s28, s28, s3\n\t" \
  "s_add_u32 s29, s28, s26\n\t" \
  "s_bfe_u32 s3, s5, 0x20006\n\t" \
  "s_add_u32 s3, s3, 6\n\t" \
  "s_mul_i32 s3, s3, s26\n\t" \
  "s_add_u32 s30, s24, s65\n\t" \
  "s_add_u32 s30, s30, s3\n\t" \
  "s_lshl_b32 s31, s26, 2\n\t" \
  "s_mul_i32 s64, s26, 22\n\t" \
  "s_sub_u32 s64, s64, s65\n\t" \
  "v_mov_b32 v253, 0xd297d297\n\t" \
  "s_cmp_ge_u32 s5, 256\n\t" \
  "s_cselect_b32 s27, s27, 0\n\t" \
  "s_mov_b64 vcc, 0\n\t" \
  "s_waitcnt lgkmcnt(0)\n\t" \
  "s_and_b32 s13, s13, 0xffff\n\t" \
  "s_and_b32 s9, s9, 0xffff\n\t" \
  "s_and_b32 s17, s17, 0xffff\n\t" \
  "s_cmp_ge_u32 s5, 192\n\t" \
  "s_cbranch_scc1 .Lcnx_nd\n\t" \
  "buffer_load_dwordx2 v[2:3], v1, s[12:15], 0 offen offset:0\n\t" \
  "buffer_load_dwordx2 v[4:5], v1, s[12:15], 0 offen offset:192\n\t" \
  "buffer_load_dwordx2 v[6:7], v1, s[12:15], 0 offen offset:384\n\t" \
  "buffer_load_dwordx2 v[8:9], v1, s[12:15], 0 offen offset:576\n\t" \
  "buffer_load_dwordx2 v[10:11], v1, s[12:15], 0 offen offset:768\n\t" \
  "buffer_load_dwordx2 v[12:13], v1, s[12:15], 0 offen offset:960\n\t" \
  "buffer_load_dwordx2 v[14:15], v1, s[12:15], 0 offen offset:1152\n\t" \
  ".Lcnx_nd:\n\t" \
  "s_and_b32 s3, s5, 0x140\n\t" \
  "s_cmp_lg_u32 s3, 64\n\t" \
  "s_cbranch_scc1 .Lcnx_nw\n\t" \
  "s_lshr_b32 s3, s2, 3\n\t" \
  "s_mul_i32 s3, s3, 2304\n\t" \
  "s_mov_b32 m0, 0\n\t" \
  "v_mul_u32_u24 v21, 36, v21\n\t" \
  "buffer_load_dword v21, s[16:19], s3 offen lds\n\t" \
  ".Lcnx_nw:\n\t" \
  "s_add_u32 m0, s25, 0\n\t" \
  "s_add_u32 s24, s24, s26\n\t" \
  "buffer_load_dwordx4 v254, s[8:11], s24 offen nt lds\n\t" \
  "s_add_u32 m0, s25, 8200\n\t" \
  "s_add_u32 s24, s24, s26\n\t" \
  "buffer_load_dwordx4 v254, s[8:11], s24 offen nt lds\n\t" \
  "s_add_u32 m0, s25, 16400\n\t" \
  "s_add_u32 s24, s24, s26\n\t" \
  "buffer_load_dwordx4 v254, s[8:11], s24 offen nt lds\n\t" \
  "s_add_u32 m0, s25, 24600\n\t" \
  "s_add_u32 s24, s24, s26\n\t" \
  "buffer_load_dwordx4 v254, s[8:11], s24 offen nt lds\n\t" \
  "s_load_dwordx16 s[32:47], s[66:67], 0x1000\n\t" \
  "s_load_dwordx16 s[48:63], s[66:67], 0x1040\n\t" \
  "s_and_b32 s21, s21, 0xffff\n\t" \
  "s_waitcnt vmcnt(4)\n\t" \
  "v_mfma_f32_32x32x16_bf16 v[144:159], v[242:245], v[240:243], 0\n\t" \
  "v_mfma_f32_32x32x16_bf16 v[160:175], v[242:245], v[240:243], 0\n\t" \
  "v_bfe_u32 v236, v251, 3, 2\n\t" \
  "v_mul_u32_u24 v236, 0x5555, v236\n\t" \
  "v_add_u32 v252, s27, v16\n\t" \
  "s_cmp_gt_u32 s5, 192\n\t" \
  "s_cbranch_scc1 .Lcnx_e2\n\t" \
  "s_cmp_eq_u32 s5, 192\n\t" \
  "s_cbranch_scc1 .Lcnx_e1\n\t" \
  "v_xor_b32 v23, 0x02020202, v19\n\t" \
  "v_perm_b32 v24, v4, 0, v19\n\t" \
  "v_perm_b32 v25, v12, v8, v19\n\t" \
  "v_perm_b32 v26, v6, v2, v19\n\t" \
  "v_perm_b32 v27, v14, v10, v19\n\t" \
  "v_perm_b32 v28, v8, v4, v19\n\t" \
  "v_perm_b32 v29, 0, v12, v19\n\t" \
  "ds_write_b64 v17, v[24:25] offset:0\n\t" \
  "ds_write_b64 v17, v[26:27] offset:8\n\t" \
  "ds_write_b64 v17, v[28:29] offset:16\n\t" \
  "v_perm_b32 v30, v4, 0, v23\n\t" \
  "v_perm_b32 v31, v12, v8, v23\n\t" \
  "v_perm_b32 v32, v6, v2, v23\n\t" \
  "v_perm_b32 v33, v14, v10, v23\n\t" \
  "v_perm_b32 v34, v8, v4, v23\n\t" \
  "v_perm_b32 v35, 0, v12, v23\n\t" \
  "ds_write_b64 v18, v[30:31] offset:0\n\t" \
  "ds_write_b64 v18, v[32:33] offset:8\n\t" \
  "ds_write_b64 v18, v[34:35] offset:16\n\t" \
  "v_perm_b32 v37, v5, 0, v19\n\t" \
  "v_perm_b32 v36, v13, v9, v19\n\t" \
  "v_perm_b32 v39, v7, v3, v19\n\t" \
  "v_perm_b32 v38, v15, v11, v19\n\t" \
  "v_perm_b32 v41, v9, v5, v19\n\t" \
  "v_perm_b32 v40, 0, v13, v19\n\t" \
  "ds_write_b64 v17, v[36:37] offset:384\n\t" \
  "ds_write_b64 v17, v[38:39] offset:392\n\t" \
  "ds_write_b64 v17, v[40:41] offset:400\n\t" \
  "v_perm_b32 v43, v5, 0, v23\n\t" \
  "v_perm_b32 v42, v13, v9, v23\n\t" \
  "v_perm_b32 v45, v7, v3, v23\n\t" \
  "v_perm_b32 v44, v15, v11, v23\n\t" \
  "v_perm_b32 v47, v9, v5, v23\n\t" \
  "v_perm_b32 v46, 0, v13, v23\n\t" \
  "ds_write_b64 v18, v[42:43] offset:384\n\t" \
  "ds_write_b64 v18, v[44:45] offset:392\n\t" \
  "ds_write_b64 v18, v[46:47] offset:400\n\t" \
  "s_branch .Lcnx_e3\n\t" \
  ".Lcnx_e1:\n\t" \
  "ds_write_b64 v251, v[246:247] offset:47136\n\t" \
  "s_mov_b64 exec, 0x3ffff\n\t" \
  "ds_write_b64 v251, v[240:241] offset:47648\n\t" \
  "s_mov_b64 exec, 15\n\t" \
  "ds_write_b64 v17, v[240:241]\n\t" \
  "s_mov_b64 exec, -1\n\t" \
  "s_branch .Lcnx_e3\n\t" \
  ".Lcnx_e2:\n\t" \
  "s_setprio 1\n\t" \
  ".Lcnx_e3:\n\t" \
  "s_add_u32 m0, s25, 32800\n\t" \
  "s_add_u32 s24, s24, s26\n\t" \
  "buffer_load_dwordx4 v254, s[8:11], s24 offen nt lds\n\t" \
  "s_add_u32 m0, s25, 41000\n\t" \
  "s_add_u32 s24, s24, s26\n\t" \
  "buffer_load_dwordx4 v254, s[8:11], s24 offen nt lds\n\t" \
  "s_add_u32 m0, s25, 49200\n\t" \
  "s_add_u32 s24, s24, s26\n\t" \
  "buffer_load_dwordx4 v254, s[8:11], s24 offen nt lds\n\t" \
  "s_add_u32 m0, s25, 57400\n\t" \
  "s_add_u32 s24, s24, s26\n\t" \
  "buffer_load_dwordx4 v254, s[8:11], s24 offen nt lds\n\t" \
  "s_waitcnt vmcnt(6) lgkmcnt(0)\n\t" \
  "s_barrier\n\t" \
  "v_add_u32 v232, 1536, v247\n\t" \
  "ds_read2_b32 v[98:99], v247 offset0:0 offset1:6\n\t" \
  "ds_read2_b32 v[114:115], v247 offset0:192 offset1:198\n\t" \
  "ds_read2_b32 v[130:131], v232 offset0:0 offset1:6\n\t" \
  "ds_read2_b32 v[102:103], v247 offset0:12 offset1:18\n\t" \
  "ds_read2_b32 v[118:119], v247 offset0:204 offset1:210\n\t" \
  "ds_read2_b32 v[134:135], v232 offset0:12 offset1:18\n\t" \
  "ds_read2_b32 v[106:107], v247 offset0:24 offset1:30\n\t" \
  "ds_read2_b32 v[122:123], v247 offset0:216 offset1:222\n\t" \
  "ds_read2_b32 v[138:139], v232 offset0:24 offset1:30\n\t" \
  "ds_read2_b32 v[110:111], v247 offset0:36 offset1:42\n\t" \
  "ds_read2_b32 v[126:127], v247 offset0:228 offset1:234\n\t" \
  "ds_read2_b32 v[142:143], v232 offset0:36 offset1:42\n\t" \
  "s_add_u32 m0, s25, 65600\n\t" \
  "s_add_u32 s24, s24, s26\n\t" \
  "buffer_load_dwordx4 v254, s[8:11], s24 offen nt lds\n\t" \
  "s_add_u32 m0, s25, 73800\n\t" \
  "s_add_u32 s24, s24, s26\n\t" \
  "buffer_load_dwordx4 v254, s[8:11], s24 offen nt lds\n\t" \
  "s_add_u32 m0, s25, 82000\n\t" \
  "s_add_u32 s24, s24, s26\n\t" \
  "buffer_load_dwordx4 v254, s[8:11], s24 offen nt lds\n\t" \
  "s_add_u32 m0, s25, 90200\n\t" \
  "s_add_u32 s24, s24, s26\n\t" \
  "buffer_load_dwordx4 v254, s[8:11], s24 offen nt lds\n\t" \
  ".Lcnx_round:\n\t" \
  "v_add_u32 v232, 49200, v246\n\t" \
  "v_add_u32 v233, s41, v251\n\t" \
  "v_mfma_f32_32x32x16_bf16 v[176:191], v[242:245], v[240:243], 0\n\t" \
  "ds_read_b64 v[208:209], v246 offset:0\n\t" \
  "ds_read_b64 v[210:211], v246 offset:400\n\t" \
  "ds_read_b64 v[212:213], v246 offset:800\n\t" \
  "ds_read_b64 v[214:215], v246 offset:1200\n\t" \
  "ds_read_b64 v[216:217], v246 offset:8\n\t" \
  "ds_read_b64 v[218:219], v246 offset:408\n\t" \
  "ds_read_b64 v[220:221], v246 offset:808\n\t" \
  "ds_read_b64 v[222:223], v246 offset:1208\n\t" \
  "ds_read_b64 v[224:225], v246 offset:16\n\t" \
  "ds_read_b64 v[226:227], v246 offset:416\n\t" \
  "ds_read_b64 v[228:229], v246 offset:816\n\t" \
  "ds_read_b64 v[230:231], v246 offset:1216\n\t" \
  "s_waitcnt lgkmcnt(15)\n\t" \
  "v_and_b32 v96, 0xffff, v98\n\t" \
  "v_and_b32 v97, 0xffff0000, v98\n\t" \
  "v_and_b32 v98, 0xffff, v99\n\t" \
  "v_and_b32 v99, 0xffff0000, v99\n\t" \
  "v_and_b32 v112, 0xffff, v114\n\t" \
  "v_and_b32 v113, 0xffff0000, v114\n\t" \
  "v_and_b32 v114, 0xffff, v115\n\t" \
  "v_and_b32 v115, 0xffff0000, v115\n\t" \
  "v_and_b32 v128, 0xffff, v130\n\t" \
  "v_and_b32 v129, 0xffff0000, v130\n\t" \
  "v_and_b32 v130, 0xffff, v131\n\t" \
  "v_and_b32 v131, 0xffff0000, v131\n\t" \
  "s_waitcnt lgkmcnt(8)\n\t" \
  "v_smfmac_f32_16x16x64_bf16 v[144:147], v[96:99], v[208:215], v236\n\t" \
  "v_and_b32 v100, 0xffff, v102\n\t" \
  "v_and_b32 v101, 0xffff0000, v102\n\t" \
  "v_and_b32 v102, 0xffff, v103\n\t" \
  "v_and_b32 v103, 0xffff0000, v103\n\t" \
  "s_waitcnt lgkmcnt(4)\n\t" \
  "v_smfmac_f32_16x16x64_bf16 v[148:151], v[112:115], v[216:223], v236\n\t" \
  "v_and_b32 v116, 0xffff, v118\n\t" \
  "v_and_b32 v117, 0xffff0000, v118\n\t" \
  "v_and_b32 v118, 0xffff, v119\n\t" \
  "v_and_b32 v119, 0xffff0000, v119\n\t" \
  "s_waitcnt vmcnt(5) lgkmcnt(0)\n\t" \
  "s_barrier\n\t" \
  "ds_read_b64 v[208:209], v246 offset:16400\n\t" \
  "ds_read_b64 v[210:211], v246 offset:16800\n\t" \
  "ds_read_b64 v[212:213], v246 offset:17200\n\t" \
  "ds_read_b64 v[214:215], v246 offset:17600\n\t" \
  "ds_read_b64 v[216:217], v246 offset:16408\n\t" \
  "ds_read_b64 v[218:219], v246 offset:16808\n\t" \
  "ds_read_b64 v[220:221], v246 offset:17208\n\t" \
  "ds_read_b64 v[222:223], v246 offset:17608\n\t" \
  "v_smfmac_f32_16x16x64_bf16 v[152:155], v[128:131], v[224:231], v236\n\t" \
  "s_cmp_lg_u32 s58, 1\n\t" \
  "s_cbranch_scc1 .Lcnx_c1\n\t" \
  "s_add_u32 m0, s25, 73800\n\t" \
  "s_add_u32 s24, s24, s26\n\t" \
  "buffer_load_dwordx4 v254, s[8:11], s24 offen lds\n\t" \
  ".Lcnx_c1:\n\t" \
  "ds_read_b64 v[224:225], v246 offset:16416\n\t" \
  "ds_read_b64 v[226:227], v246 offset:16816\n\t" \
  "ds_read_b64 v[228:229], v246 offset:17216\n\t" \
  "v_and_b32 v132, 0xffff, v134\n\t" \
  "v_and_b32 v133, 0xffff0000, v134\n\t" \
  "v_and_b32 v134, 0xffff, v135\n\t" \
  "v_and_b32 v135, 0xffff0000, v135\n\t" \
  "s_waitcnt lgkmcnt(7)\n\t" \
  "v_smfmac_f32_16x16x64_bf16 v[156:159], v[96:99], v[208:215], v236\n\t" \
  "ds_read_b64 v[230:231], v246 offset:17616\n\t" \
  "v_and_b32 v104, 0xffff, v106\n\t" \
  "v_and_b32 v105, 0xffff0000, v106\n\t" \
  "v_and_b32 v106, 0xffff, v107\n\t" \
  "v_and_b32 v107, 0xffff0000, v107\n\t" \
  "v_smfmac_f32_16x16x64_bf16 v[144:147], v[100:103], v[208:215], v236\n\t" \
  "s_cmp_lg_u32 s58, 1\n\t" \
  "s_cbranch_scc1 .Lcnx_c2\n\t" \
  "s_add_u32 m0, s25, 82000\n\t" \
  "s_add_u32 s24, s24, s26\n\t" \
  "buffer_load_dwordx4 v254, s[8:11], s24 offen lds\n\t" \
  ".Lcnx_c2:\n\t" \
  "s_waitcnt lgkmcnt(4)\n\t" \
  "v_smfmac_f32_16x16x64_bf16 v[160:163], v[112:115], v[216:223], v236\n\t" \
  "v_and_b32 v120, 0xffff, v122\n\t" \
  "v_and_b32 v121, 0xffff0000, v122\n\t" \
  "v_and_b32 v122, 0xffff, v123\n\t" \
  "v_and_b32 v123, 0xffff0000, v123\n\t" \
  "v_smfmac_f32_16x16x64_bf16 v[148:151], v[116:119], v[216:223], v236\n\t" \
  "s_cmp_lg_u32 s58, 1\n\t" \
  "s_cbranch_scc1 .Lcnx_c3\n\t" \
  "s_add_u32 m0, s25, 90200\n\t" \
  "s_add_u32 s24, s24, s26\n\t" \
  "buffer_load_dwordx4 v254, s[8:11], s24 offen lds\n\t" \
  ".Lcnx_c3:\n\t" \
  "s_waitcnt vmcnt(6) lgkmcnt(0)\n\t" \
  "s_barrier\n\t" \
  "ds_read_b64 v[208:209], v246 offset:32800\n\t" \
  "ds_read_b64 v[210:211], v246 offset:33200\n\t" \
  "ds_read_b64 v[212:213], v246 offset:33600\n\t" \
  "ds_read_b64 v[214:215], v246 offset:34000\n\t" \
  "ds_read_b64 v[216:217], v246 offset:32808\n\t" \
  "ds_read_b64 v[218:219], v246 offset:33208\n\t" \
  "ds_read_b64 v[220:221], v246 offset:33608\n\t" \
  "ds_read_b64 v[222:223], v246 offset:34008\n\t" \
  "v_smfmac_f32_16x16x64_bf16 v[164:167], v[128:131], v[224:231], v236\n\t" \
  "v_and_b32 v136, 0xffff, v138\n\t" \
  "v_and_b32 v137, 0xffff0000, v138\n\t" \
  "v_and_b32 v138, 0xffff, v139\n\t" \
  "v_and_b32 v139, 0xffff0000, v139\n\t" \
  "v_cvt_pk_bf16_f32 v48, v48, v49\n\t" \
  "v_cvt_pk_bf16_f32 v49, v50, v51\n\t" \
  "v_cvt_pk_bf16_f32 v50, v52, v53\n\t" \
  "v_cvt_pk_bf16_f32 v51, v54, v55\n\t" \
  "v_cvt_pk_bf16_f32 v52, v0, v1\n\t" \
  "v_cvt_pk_bf16_f32 v53, v2, v3\n\t" \
  "s_add_u32 m0, s25, 0\n\t" \
  "s_add_u32 s24, s24, s26\n\t" \
  "v_smfmac_f32_16x16x64_bf16 v[152:155], v[132:135], v[224:231], v236\n\t" \
  "buffer_load_dwordx4 v254, s[8:11], s24 offen lds\n\t" \
  "ds_read_b64 v[224:225], v246 offset:32816\n\t" \
  "ds_read_b64 v[226:227], v246 offset:33216\n\t" \
  "ds_read_b64 v[228:229], v246 offset:33616\n\t" \
  "v_cvt_pk_bf16_f32 v54, v4, v5\n\t" \
  "v_cvt_pk_bf16_f32 v55, v6, v7\n\t" \
  "v_cvt_pk_bf16_f32 v56, v56, v57\n\t" \
  "s_waitcnt lgkmcnt(7)\n\t" \
  "v_smfmac_f32_16x16x64_bf16 v[168:171], v[96:99], v[208:215], v236\n\t" \
  "ds_read_b64 v[230:231], v246 offset:34016\n\t" \
  "v_and_b32 v108, 0xffff, v110\n\t" \
  "v_and_b32 v109, 0xffff0000, v110\n\t" \
  "v_and_b32 v110, 0xffff, v111\n\t" \
  "v_and_b32 v111, 0xffff0000, v111\n\t" \
  "v_cvt_pk_bf16_f32 v57, v58, v59\n\t" \
  "v_cvt_pk_bf16_f32 v58, v60, v61\n\t" \
  "v_cvt_pk_bf16_f32 v59, v62, v63\n\t" \
  "v_smfmac_f32_16x16x64_bf16 v[156:159], v[100:103], v[208:215], v236\n\t" \
  "v_cvt_pk_bf16_f32 v60, v8, v9\n\t" \
  "v_cvt_pk_bf16_f32 v61, v10, v11\n\t" \
  "v_cvt_pk_bf16_f32 v62, v12, v13\n\t" \
  "s_add_u32 m0, s25, 8200\n\t" \
  "s_add_u32 s24, s24, s26\n\t" \
  "v_smfmac_f32_16x16x64_bf16 v[144:147], v[104:107], v[208:215], v236\n\t" \
  "buffer_load_dwordx4 v254, s[8:11], s24 offen lds\n\t" \
  "v_cvt_pk_bf16_f32 v63, v14, v15\n\t" \
  "v_mfma_f32_32x32x16_bf16 v[0:15], v[242:245], v[240:243], 0\n\t" \
  "s_waitcnt lgkmcnt(4)\n\t" \
  "v_smfmac_f32_16x16x64_bf16 v[172:175], v[112:115], v[216:223], v236\n\t" \
  "v_and_b32 v124, 0xffff, v126\n\t" \
  "v_and_b32 v125, 0xffff0000, v126\n\t" \
  "v_and_b32 v126, 0xffff, v127\n\t" \
  "v_and_b32 v127, 0xffff0000, v127\n\t" \
  "s_add_u32 m0, s25, 16400\n\t" \
  "s_add_u32 s24, s24, s26\n\t" \
  "v_smfmac_f32_16x16x64_bf16 v[160:163], v[116:119], v[216:223], v236\n\t" \
  "buffer_load_dwordx4 v254, s[8:11], s24 offen lds\n\t" \
  "v_smfmac_f32_16x16x64_bf16 v[148:151], v[120:123], v[216:223], v236\n\t" \
  "s_waitcnt vmcnt(7) lgkmcnt(0)\n\t" \
  "s_barrier\n\t" \
  "ds_read_b64 v[208:209], v232 offset:0\n\t" \
  "ds_read_b64 v[210:211], v232 offset:400\n\t" \
  "ds_read_b64 v[212:213], v232 offset:800\n\t" \
  "ds_read_b64 v[214:215], v232 offset:1200\n\t" \
  "ds_read_b64 v[216:217], v232 offset:8\n\t" \
  "ds_read_b64 v[218:219], v232 offset:408\n\t" \
  "ds_read_b64 v[220:221], v232 offset:808\n\t" \
  "ds_read_b64 v[222:223], v232 offset:1208\n\t" \
  "v_smfmac_f32_16x16x64_bf16 v[176:179], v[128:131], v[224:231], v236\n\t" \
  "v_and_b32 v140, 0xffff, v142\n\t" \
  "v_and_b32 v141, 0xffff0000, v142\n\t" \
  "v_and_b32 v142, 0xffff, v143\n\t" \
  "v_and_b32 v143, 0xffff0000, v143\n\t" \
  "s_add_u32 m0, s25, 24600\n\t" \
  "s_add_u32 s24, s24, s26\n\t" \
  "v_smfmac_f32_16x16x64_bf16 v[164:167], v[132:135], v[224:231], v236\n\t" \
  "buffer_load_dwordx4 v254, s[8:11], s24 offen lds\n\t" \
  "v_smfmac_f32_16x16x64_bf16 v[152:155], v[136:139], v[224:231], v236\n\t" \
  "ds_read_b64 v[224:225], v232 offset:16\n\t" \
  "s_waitcnt lgkmcnt(5)\n\t" \
  "v_smfmac_f32_16x16x64_bf16 v[180:183], v[96:99], v[208:215], v236\n\t" \
  "ds_read_b64 v[226:227], v232 offset:416\n\t" \
  "v_smfmac_f32_16x16x64_bf16 v[168:171], v[100:103], v[208:215], v236\n\t" \
  "ds_read_b64 v[228:229], v232 offset:816\n\t" \
  "v_cvt_pk_bf16_f32 v64, v64, v65\n\t" \
  "v_cvt_pk_bf16_f32 v65, v66, v67\n\t" \
  "v_cvt_pk_bf16_f32 v66, v68, v69\n\t" \
  "v_cvt_pk_bf16_f32 v67, v70, v71\n\t" \
  "v_cvt_pk_bf16_f32 v68, v16, v17\n\t" \
  "v_cvt_pk_bf16_f32 v69, v18, v19\n\t" \
  "s_add_u32 m0, s25, 32800\n\t" \
  "s_add_u32 s24, s24, s26\n\t" \
  "v_smfmac_f32_16x16x64_bf16 v[156:159], v[104:107], v[208:215], v236\n\t" \
  "buffer_load_dwordx4 v254, s[8:11], s24 offen lds\n\t" \
  "ds_read_b64 v[230:231], v232 offset:1216\n\t" \
  "v_cvt_pk_bf16_f32 v70, v20, v21\n\t" \
  "v_cvt_pk_bf16_f32 v71, v22, v23\n\t" \
  "v_cvt_pk_bf16_f32 v72, v72, v73\n\t" \
  "v_smfmac_f32_16x16x64_bf16 v[144:147], v[108:111], v[208:215], v236\n\t" \
  "v_cvt_pk_bf16_f32 v73, v74, v75\n\t" \
  "v_cvt_pk_bf16_f32 v74, v76, v77\n\t" \
  "v_cvt_pk_bf16_f32 v75, v78, v79\n\t" \
  "s_waitcnt lgkmcnt(4)\n\t" \
  "v_smfmac_f32_16x16x64_bf16 v[184:187], v[112:115], v[216:223], v236\n\t" \
  "v_cvt_pk_bf16_f32 v76, v24, v25\n\t" \
  "v_cvt_pk_bf16_f32 v77, v26, v27\n\t" \
  "v_cvt_pk_bf16_f32 v78, v28, v29\n\t" \
  "v_smfmac_f32_16x16x64_bf16 v[172:175], v[116:119], v[216:223], v236\n\t" \
  "v_cvt_pk_bf16_f32 v79, v30, v31\n\t" \
  "v_mfma_f32_32x32x16_bf16 v[16:31], v[242:245], v[240:243], 0\n\t" \
  "s_add_u32 m0, s25, 41000\n\t" \
  "s_add_u32 s24, s24, s26\n\t" \
  "v_smfmac_f32_16x16x64_bf16 v[160:163], v[120:123], v[216:223], v236\n\t" \
  "buffer_load_dwordx4 v254, s[8:11], s24 offen lds\n\t" \
  "v_mul_f32 v193, v144, v144\n\t" \
  "v_add_f32 v192, v144, v145\n\t" \
  "v_fmac_f32 v193, v145, v145\n\t" \
  "v_add_f32 v192, v192, v146\n\t" \
  "v_smfmac_f32_16x16x64_bf16 v[148:151], v[124:127], v[216:223], v236\n\t" \
  "v_fmac_f32 v193, v146, v146\n\t" \
  "v_add_f32 v192, v192, v147\n\t" \
  "v_fmac_f32 v193, v147, v147\n\t" \
  "s_waitcnt vmcnt(8) lgkmcnt(0)\n\t" \
  "s_barrier\n\t" \
  "ds_read_b64 v[208:209], v232 offset:16400\n\t" \
  "ds_read_b64 v[210:211], v232 offset:16800\n\t" \
  "ds_read_b64 v[212:213], v232 offset:17200\n\t" \
  "ds_read_b64 v[214:215], v232 offset:17600\n\t" \
  "ds_read_b64 v[216:217], v232 offset:16408\n\t" \
  "ds_read_b64 v[218:219], v232 offset:16808\n\t" \
  "ds_read_b64 v[220:221], v232 offset:17208\n\t" \
  "ds_read_b64 v[222:223], v232 offset:17608\n\t" \
  "v_smfmac_f32_16x16x64_bf16 v[188:191], v[128:131], v[224:231], v236\n\t" \
  "s_add_u32 m0, s25, 49200\n\t" \
  "s_add_u32 s24, s24, s26\n\t" \
  "v_smfmac_f32_16x16x64_bf16 v[176:179], v[132:135], v[224:231], v236\n\t" \
  "buffer_load_dwordx4 v254, s[8:11], s24 offen lds\n\t" \
  "v_smfmac_f32_16x16x64_bf16 v[164:167], v[136:139], v[224:231], v236\n\t" \
  "v_add_f32 v192, v192, v148\n\t" \
  "v_fmac_f32 v193, v148, v148\n\t" \
  "v_add_f32 v192, v192, v149\n\t" \
  "v_fmac_f32 v193, v149, v149\n\t" \
  "v_add_f32 v192, v192, v150\n\t" \
  "v_fmac_f32 v193, v150, v150\n\t" \
  "v_smfmac_f32_16x16x64_bf16 v[152:155], v[140:143], v[224:231], v236\n\t" \
  "ds_read_b64 v[224:225], v232 offset:16416\n\t" \
  "v_add_f32 v192, v192, v151\n\t" \
  "v_fmac_f32 v193, v151, v151\n\t" \
  "s_waitcnt lgkmcnt(5)\n\t" \
  "v_smfmac_f32_16x16x64_bf16 v[0:3], v[96:99], v[208:215], v236\n\t" \
  "ds_read_b64 v[226:227], v232 offset:16816\n\t" \
  "v_smfmac_f32_16x16x64_bf16 v[180:183], v[100:103], v[208:215], v236\n\t" \
  "ds_read_b64 v[228:229], v232 offset:17216\n\t" \
  "v_cvt_pk_bf16_f32 v80, v80, v81\n\t" \
  "v_cvt_pk_bf16_f32 v81, v82, v83\n\t" \
  "v_cvt_pk_bf16_f32 v82, v84, v85\n\t" \
  "v_cvt_pk_bf16_f32 v83, v86, v87\n\t" \
  "v_cvt_pk_bf16_f32 v84, v32, v33\n\t" \
  "v_cvt_pk_bf16_f32 v85, v34, v35\n\t" \
  "s_add_u32 m0, s25, 57400\n\t" \
  "s_add_u32 s24, s24, s26\n\t" \
  "v_smfmac_f32_16x16x64_bf16 v[168:171], v[104:107], v[208:215], v236\n\t" \
  "buffer_load_dwordx4 v254, s[8:11], s24 offen lds\n\t" \
  "ds_read_b64 v[230:231], v232 offset:17616\n\t" \
  "v_cvt_pk_bf16_f32 v86, v36, v37\n\t" \
  "v_add_f32 v192, v192, v152\n\t" \
  "v_cvt_pk_bf16_f32 v87, v38, v39\n\t" \
  "v_smfmac_f32_16x16x64_bf16 v[156:159], v[108:111], v[208:215], v236\n\t" \
  "v_cvt_pk_bf16_f32 v88, v88, v89\n\t" \
  "v_fmac_f32 v193, v152, v152\n\t" \
  "v_cvt_pk_bf16_f32 v89, v90, v91\n\t" \
  "s_waitcnt lgkmcnt(4)\n\t" \
  "v_smfmac_f32_16x16x64_bf16 v[4:7], v[112:115], v[216:223], v236\n\t" \
  "v_add_f32 v192, v192, v153\n\t" \
  "v_cvt_pk_bf16_f32 v90, v92, v93\n\t" \
  "v_fmac_f32 v193, v153, v153\n\t" \
  "v_smfmac_f32_16x16x64_bf16 v[184:187], v[116:119], v[216:223], v236\n\t" \
  "v_add_f32 v192, v192, v154\n\t" \
  "v_cvt_pk_bf16_f32 v91, v94, v95\n\t" \
  "v_fmac_f32 v193, v154, v154\n\t" \
  "v_smfmac_f32_16x16x64_bf16 v[172:175], v[120:123], v[216:223], v236\n\t" \
  "v_add_f32 v192, v192, v155\n\t" \
  "v_cvt_pk_bf16_f32 v92, v40, v41\n\t" \
  "v_fmac_f32 v193, v155, v155\n\t" \
  "v_smfmac_f32_16x16x64_bf16 v[160:163], v[124:127], v[216:223], v236\n\t" \
  "v_mul_f32 v195, v156, v156\n\t" \
  "v_cvt_pk_bf16_f32 v93, v42, v43\n\t" \
  "ds_write_b64 v233, v[192:193] offset:16400\n\t" \
  "s_waitcnt vmcnt(8) lgkmcnt(0)\n\t" \
  "s_barrier\n\t" \
  "ds_read_b64 v[208:209], v232 offset:32800\n\t" \
  "ds_read_b64 v[210:211], v232 offset:33200\n\t" \
  "ds_read_b64 v[212:213], v232 offset:33600\n\t" \
  "ds_read_b64 v[214:215], v232 offset:34000\n\t" \
  "ds_read_b64 v[216:217], v232 offset:32808\n\t" \
  "ds_read_b64 v[218:219], v232 offset:33208\n\t" \
  "ds_read_b64 v[220:221], v232 offset:33608\n\t" \
  "ds_read_b64 v[222:223], v232 offset:34008\n\t" \
  "s_add_u32 m0, s25, 65600\n\t" \
  "s_add_u32 s24, s24, s26\n\t" \
  "v_smfmac_f32_16x16x64_bf16 v[8:11], v[128:131], v[224:231], v236\n\t" \
  "buffer_load_dwordx4 v254, s[8:11], s24 offen lds\n\t" \
  "v_cvt_pk_bf16_f32 v94, v44, v45\n\t" \
  "v_add_f32 v194, v156, v157\n\t" \
  "v_cvt_pk_bf16_f32 v95, v46, v47\n\t" \
  "v_smfmac_f32_16x16x64_bf16 v[188:191], v[132:135], v[224:231], v236\n\t" \
  "v_mfma_f32_32x32x16_bf16 v[32:47], v[242:245], v[240:243], 0\n\t" \
  "v_fmac_f32 v195, v157, v157\n\t" \
  "v_add_f32 v194, v194, v158\n\t" \
  "v_smfmac_f32_16x16x64_bf16 v[176:179], v[136:139], v[224:231], v236\n\t" \
  "v_fmac_f32 v195, v158, v158\n\t" \
  "v_add_f32 v194, v194, v159\n\t" \
  "v_fmac_f32 v195, v159, v159\n\t" \
  "v_smfmac_f32_16x16x64_bf16 v[164:167], v[140:143], v[224:231], v236\n\t" \
  "ds_read_b64 v[224:225], v232 offset:32816\n\t" \
  "v_add_f32 v194, v194, v160\n\t" \
  "v_fmac_f32 v195, v160, v160\n\t" \
  "v_add_f32 v194, v194, v161\n\t" \
  "s_waitcnt lgkmcnt(5)\n\t" \
  "s_add_u32 m0, s25, 73800\n\t" \
  "s_add_u32 s24, s24, s26\n\t" \
  "v_smfmac_f32_16x16x64_bf16 v[12:15], v[96:99], v[208:215], v236\n\t" \
  "buffer_load_dwordx4 v254, s[8:11], s24 offen lds\n\t" \
  "ds_read_b64 v[226:227], v232 offset:33216\n\t" \
  "v_fmac_f32 v195, v161, v161\n\t" \
  "v_add_f32 v194, v194, v162\n\t" \
  "v_fmac_f32 v195, v162, v162\n\t" \
  "v_smfmac_f32_16x16x64_bf16 v[0:3], v[100:103], v[208:215], v236\n\t" \
  "ds_read_b64 v[228:229], v232 offset:33616\n\t" \
  "v_add_f32 v194, v194, v163\n\t" \
  "v_fmac_f32 v195, v163, v163\n\t" \
  "v_smfmac_f32_16x16x64_bf16 v[180:183], v[104:107], v[208:215], v236\n\t" \
  "buffer_store_dwordx4 v[52:55], v250, s[20:23], s61 offen offset:0 sc1\n\t" \
  "ds_read_b64 v[230:231], v232 offset:34016\n\t" \
  "v_add_f32 v194, v194, v164\n\t" \
  "v_fmac_f32 v195, v164, v164\n\t" \
  "v_add_f32 v194, v194, v165\n\t" \
  "v_smfmac_f32_16x16x64_bf16 v[168:171], v[108:111], v[208:215], v236\n\t" \
  "v_fmac_f32 v195, v165, v165\n\t" \
  "v_add_f32 v194, v194, v166\n\t" \
  "v_fmac_f32 v195, v166, v166\n\t" \
  "s_waitcnt lgkmcnt(4)\n\t" \
  "v_smfmac_f32_16x16x64_bf16 v[16:19], v[112:115], v[216:223], v236\n\t" \
  "v_add_f32 v194, v194, v167\n\t" \
  "v_fmac_f32 v195, v167, v167\n\t" \
  "v_smfmac_f32_16x16x64_bf16 v[4:7], v[116:119], v[216:223], v236\n\t" \
  "ds_write_b64 v233, v[194:195] offset:20496\n\t" \
  "v_smfmac_f32_16x16x64_bf16 v[184:187], v[120:123], v[216:223], v236\n\t" \
  "v_mul_f32 v197, v168, v168\n\t" \
  "v_add_f32 v196, v168, v169\n\t" \
  "v_fmac_f32 v197, v169, v169\n\t" \
  "v_add_f32 v196, v196, v170\n\t" \
  "v_fmac_f32 v197, v170, v170\n\t" \
  "v_add_f32 v196, v196, v171\n\t" \
  "v_smfmac_f32_16x16x64_bf16 v[172:175], v[124:127], v[216:223], v236\n\t" \
  "v_fmac_f32 v197, v171, v171\n\t" \
  "s_waitcnt vmcnt(9) lgkmcnt(0)\n\t" \
  "s_barrier\n\t" \
  "ds_read_b64 v[208:209], v246 offset:0\n\t" \
  "ds_read_b64 v[210:211], v246 offset:400\n\t" \
  "ds_read_b64 v[212:213], v246 offset:800\n\t" \
  "ds_read_b64 v[214:215], v246 offset:1200\n\t" \
  "ds_read_b64 v[216:217], v246 offset:8\n\t" \
  "ds_read_b64 v[218:219], v246 offset:408\n\t" \
  "ds_read_b64 v[220:221], v246 offset:808\n\t" \
  "ds_read_b64 v[222:223], v246 offset:1208\n\t" \
  "v_smfmac_f32_16x16x64_bf16 v[20:23], v[128:131], v[224:231], v236\n\t" \
  "v_smfmac_f32_16x16x64_bf16 v[8:11], v[132:135], v[224:231], v236\n\t" \
  "buffer_store_dwordx4 v[60:63], v250, s[20:23], s61 offen offset:64 sc1\n\t" \
  "v_smfmac_f32_16x16x64_bf16 v[188:191], v[136:139], v[224:231], v236\n\t" \
  "v_add_f32 v196, v196, v172\n\t" \
  "v_fmac_f32 v197, v172, v172\n\t" \
  "v_add_f32 v196, v196, v173\n\t" \
  "v_fmac_f32 v197, v173, v173\n\t" \
  "v_add_f32 v196, v196, v174\n\t" \
  "v_fmac_f32 v197, v174, v174\n\t" \
  "v_smfmac_f32_16x16x64_bf16 v[176:179], v[140:143], v[224:231], v236\n\t" \
  "ds_read_b64 v[224:225], v246 offset:16\n\t" \
  "v_add_f32 v196, v196, v175\n\t" \
  "v_fmac_f32 v197, v175, v175\n\t" \
  "s_waitcnt lgkmcnt(5)\n\t" \
  "v_smfmac_f32_16x16x64_bf16 v[24:27], v[96:99], v[208:215], v236\n\t" \
  "ds_read_b64 v[226:227], v246 offset:416\n\t" \
  "v_smfmac_f32_16x16x64_bf16 v[12:15], v[100:103], v[208:215], v236\n\t" \
  "ds_read_b64 v[228:229], v246 offset:816\n\t" \
  "v_smfmac_f32_16x16x64_bf16 v[0:3], v[104:107], v[208:215], v236\n\t" \
  "ds_read_b64 v[230:231], v246 offset:1216\n\t" \
  "v_add_f32 v196, v196, v176\n\t" \
  "v_fmac_f32 v197, v176, v176\n\t" \
  "v_add_f32 v196, v196, v177\n\t" \
  "v_fmac_f32 v197, v177, v177\n\t" \
  "v_add_f32 v196, v196, v178\n\t" \
  "v_fmac_f32 v197, v178, v178\n\t" \
  "v_smfmac_f32_16x16x64_bf16 v[180:183], v[108:111], v[208:215], v236\n\t" \
  "v_add_f32 v196, v196, v179\n\t" \
  "v_fmac_f32 v197, v179, v179\n\t" \
  "s_waitcnt lgkmcnt(4)\n\t" \
  "v_smfmac_f32_16x16x64_bf16 v[28:31], v[112:115], v[216:223], v236\n\t" \
  "buffer_store_dwordx4 v[68:71], v250, s[20:23], s61 offen offset:128 sc1\n\t" \
  "v_smfmac_f32_16x16x64_bf16 v[16:19], v[116:119], v[216:223], v236\n\t" \
  "ds_write_b64 v233, v[196:197] offset:24592\n\t" \
  "v_smfmac_f32_16x16x64_bf16 v[4:7], v[120:123], v[216:223], v236\n\t" \
  "v_mul_f32 v199, v180, v180\n\t" \
  "v_add_f32 v198, v180, v181\n\t" \
  "v_fmac_f32 v199, v181, v181\n\t" \
  "v_add_f32 v198, v198, v182\n\t" \
  "v_fmac_f32 v199, v182, v182\n\t" \
  "v_add_f32 v198, v198, v183\n\t" \
  "v_smfmac_f32_16x16x64_bf16 v[184:187], v[124:127], v[216:223], v236\n\t" \
  "v_fmac_f32 v199, v183, v183\n\t" \
  "s_waitcnt vmcnt(9) lgkmcnt(0)\n\t" \
  "s_barrier\n\t" \
  "ds_read_b64 v[208:209], v246 offset:16400\n\t" \
  "ds_read_b64 v[210:211], v246 offset:16800\n\t" \
  "ds_read_b64 v[212:213], v246 offset:17200\n\t" \
  "ds_read_b64 v[214:215], v246 offset:17600\n\t" \
  "ds_read_b64 v[216:217], v246 offset:16408\n\t" \
  "ds_read_b64 v[218:219], v246 offset:16808\n\t" \
  "ds_read_b64 v[220:221], v246 offset:17208\n\t" \
  "ds_read_b64 v[222:223], v246 offset:17608\n\t" \
  "v_smfmac_f32_16x16x64_bf16 v[32:35], v[128:131], v[224:231], v236\n\t" \
  "v_smfmac_f32_16x16x64_bf16 v[20:23], v[132:135], v[224:231], v236\n\t" \
  "v_smfmac_f32_16x16x64_bf16 v[8:11], v[136:139], v[224:231], v236\n\t" \
  "v_add_f32 v198, v198, v184\n\t" \
  "v_fmac_f32 v199, v184, v184\n\t" \
  "v_add_f32 v198, v198, v185\n\t" \
  "v_fmac_f32 v199, v185, v185\n\t" \
  "v_add_f32 v198, v198, v186\n\t" \
  "v_smfmac_f32_16x16x64_bf16 v[188:191], v[140:143], v[224:231], v236\n\t" \
  "buffer_store_dwordx4 v[76:79], v250, s[20:23], s61 offen offset:192 sc1\n\t" \
  "ds_read_b64 v[224:225], v246 offset:16416\n\t" \
  "v_fmac_f32 v199, v186, v186\n\t" \
  "v_add_f32 v198, v198, v187\n\t" \
  "v_fmac_f32 v199, v187, v187\n\t" \
  "s_waitcnt lgkmcnt(5)\n\t" \
  "v_smfmac_f32_16x16x64_bf16 v[36:39], v[96:99], v[208:215], v236\n\t" \
  "ds_read_b64 v[226:227], v246 offset:16816\n\t" \
  "v_smfmac_f32_16x16x64_bf16 v[24:27], v[100:103], v[208:215], v236\n\t" \
  "ds_read_b64 v[228:229], v246 offset:17216\n\t" \
  "v_smfmac_f32_16x16x64_bf16 v[12:15], v[104:107], v[208:215], v236\n\t" \
  "ds_read_b64 v[230:231], v246 offset:17616\n\t" \
  "v_add_f32 v198, v198, v188\n\t" \
  "v_fmac_f32 v199, v188, v188\n\t" \
  "v_add_f32 v198, v198, v189\n\t" \
  "v_fmac_f32 v199, v189, v189\n\t" \
  "v_add_f32 v198, v198, v190\n\t" \
  "v_fmac_f32 v199, v190, v190\n\t" \
  "v_smfmac_f32_16x16x64_bf16 v[0:3], v[108:111], v[208:215], v236\n\t" \
  "v_add_f32 v198, v198, v191\n\t" \
  "v_fmac_f32 v199, v191, v191\n\t" \
  "s_waitcnt lgkmcnt(4)\n\t" \
  "v_smfmac_f32_16x16x64_bf16 v[40:43], v[112:115], v[216:223], v236\n\t" \
  "ds_write_b64 v233, v[198:199] offset:28688\n\t" \
  "v_smfmac_f32_16x16x64_bf16 v[28:31], v[116:119], v[216:223], v236\n\t" \
  "v_smfmac_f32_16x16x64_bf16 v[16:19], v[120:123], v[216:223], v236\n\t" \
  "buffer_store_dwordx4 v[84:87], v250, s[20:23], s61 offen offset:256 sc1\n\t" \
  "v_mul_f32 v201, v0, v0\n\t" \
  "v_add_f32 v200, v0, v1\n\t" \
  "v_fmac_f32 v201, v1, v1\n\t" \
  "v_add_f32 v200, v200, v2\n\t" \
  "v_fmac_f32 v201, v2, v2\n\t" \
  "v_add_f32 v200, v200, v3\n\t" \
  "v_smfmac_f32_16x16x64_bf16 v[4:7], v[124:127], v[216:223], v236\n\t" \
  "v_fmac_f32 v201, v3, v3\n\t" \
  "s_waitcnt vmcnt(9) lgkmcnt(0)\n\t" \
  "s_barrier\n\t" \
  "ds_read_b64 v[208:209], v246 offset:32800\n\t" \
  "ds_read_b64 v[210:211], v246 offset:33200\n\t" \
  "ds_read_b64 v[212:213], v246 offset:33600\n\t" \
  "ds_read_b64 v[214:215], v246 offset:34000\n\t" \
  "ds_read_b64 v[216:217], v246 offset:32808\n\t" \
  "ds_read_b64 v[218:219], v246 offset:33208\n\t" \
  "ds_read_b64 v[220:221], v246 offset:33608\n\t" \
  "ds_read_b64 v[222:223], v246 offset:34008\n\t" \
  "v_smfmac_f32_16x16x64_bf16 v[44:47], v[128:131], v[224:231], v236\n\t" \
  "v_smfmac_f32_16x16x64_bf16 v[32:35], v[132:135], v[224:231], v236\n\t" \
  "v_smfmac_f32_16x16x64_bf16 v[20:23], v[136:139], v[224:231], v236\n\t" \
  "v_add_f32 v200, v200, v4\n\t" \
  "v_fmac_f32 v201, v4, v4\n\t" \
  "v_add_f32 v200, v200, v5\n\t" \
  "v_fmac_f32 v201, v5, v5\n\t" \
  "v_add_f32 v200, v200, v6\n\t" \
  "v_fmac_f32 v201, v6, v6\n\t" \
  "v_smfmac_f32_16x16x64_bf16 v[8:11], v[140:143], v[224:231], v236\n\t" \
  "ds_read_b64 v[224:225], v246 offset:32816\n\t" \
  "v_add_f32 v200, v200, v7\n\t" \
  "v_fmac_f32 v201, v7, v7\n\t" \
  "s_waitcnt lgkmcnt(5)\n\t" \
  "v_smfmac_f32_16x16x64_bf16 v[36:39], v[100:103], v[208:215], v236\n\t" \
  "ds_read_b64 v[226:227], v246 offset:33216\n\t" \
  "v_smfmac_f32_16x16x64_bf16 v[24:27], v[104:107], v[208:215], v236\n\t" \
  "buffer_store_dwordx4 v[92:95], v250, s[20:23], s61 offen offset:320 sc1\n\t" \
  "ds_read_b64 v[228:229], v246 offset:33616\n\t" \
  "v_smfmac_f32_16x16x64_bf16 v[12:15], v[108:111], v[208:215], v236\n\t" \
  "ds_read_b64 v[230:231], v246 offset:34016\n\t" \
  "v_add_f32 v200, v200, v8\n\t" \
  "v_fmac_f32 v201, v8, v8\n\t" \
  "v_add_f32 v200, v200, v9\n\t" \
  "v_fmac_f32 v201, v9, v9\n\t" \
  "v_add_f32 v200, v200, v10\n\t" \
  "v_fmac_f32 v201, v10, v10\n\t" \
  "s_waitcnt lgkmcnt(4)\n\t" \
  "v_smfmac_f32_16x16x64_bf16 v[40:43], v[116:119], v[216:223], v236\n\t" \
  "v_add_f32 v200, v200, v11\n\t" \
  "v_fmac_f32 v201, v11, v11\n\t" \
  "v_smfmac_f32_16x16x64_bf16 v[28:31], v[120:123], v[216:223], v236\n\t" \
  "ds_write_b64 v233, v[200:201] offset:0\n\t" \
  "v_smfmac_f32_16x16x64_bf16 v[16:19], v[124:127], v[216:223], v236\n\t" \
  "v_mul_f32 v203, v12, v12\n\t" \
  "v_add_f32 v202, v12, v13\n\t" \
  "v_fmac_f32 v203, v13, v13\n\t" \
  "v_add_f32 v202, v202, v14\n\t" \
  "v_fmac_f32 v203, v14, v14\n\t" \
  "v_add_f32 v202, v202, v15\n\t" \
  "s_waitcnt vmcnt(8) lgkmcnt(0)\n\t" \
  "s_barrier\n\t" \
  "ds_read_b64 v[208:209], v232 offset:0\n\t" \
  "ds_read_b64 v[210:211], v232 offset:400\n\t" \
  "ds_read_b64 v[212:213], v232 offset:800\n\t" \
  "ds_read_b64 v[214:215], v232 offset:1200\n\t" \
  "ds_read_b64 v[216:217], v232 offset:8\n\t" \
  "ds_read_b64 v[218:219], v232 offset:408\n\t" \
  "ds_read_b64 v[220:221], v232 offset:808\n\t" \
  "ds_read_b64 v[222:223], v232 offset:1208\n\t" \
  "s_cmp_lg_u32 s58, 0\n\t" \
  "s_cbranch_scc1 .Lcnx_c4\n\t" \
  "v_lshl_add_u32 v48, v233, 1, s63\n\t" \
  "s_lshl_b32 m0, s41, 1\n\t" \
  "v_add_u32 v49, 0x2000, v48\n\t" \
  "global_load_lds_dwordx4 v48, s[6:7]\n\t" \
  "s_add_u32 m0, m0, 0x2000\n\t" \
  "s_cmp_eq_u32 s41, 3584\n\t" \
  "s_cselect_b64 exec, 1, -1\n\t" \
  "global_load_lds_dwordx4 v49, s[6:7]\n\t" \
  "s_mov_b64 exec, -1\n\t" \
  ".Lcnx_c4:\n\t" \
  "s_cmp_lg_u32 s37, 0\n\t" \
  "s_cbranch_scc1 .Lcnx_c5\n\t" \
  "v_add_u32 v128, s40, v251\n\t" \
  "ds_read_b64 v[96:97], v128 offset:0\n\t" \
  "ds_read_b64 v[98:99], v128 offset:512\n\t" \
  "ds_read_b64 v[100:101], v128 offset:1024\n\t" \
  "ds_read_b64 v[102:103], v128 offset:1536\n\t" \
  "ds_read_b64 v[112:113], v128 offset:2048\n\t" \
  "ds_read_b64 v[114:115], v128 offset:2560\n\t" \
  "ds_read_b64 v[116:117], v128 offset:3072\n\t" \
  "ds_read_b64 v[118:119], v128 offset:3584\n\t" \
  ".Lcnx_c5:\n\t" \
  "v_smfmac_f32_16x16x64_bf16 v[44:47], v[132:135], v[224:231], v236\n\t" \
  "v_fmac_f32 v203, v15, v15\n\t" \
  "v_smfmac_f32_16x16x64_bf16 v[32:35], v[136:139], v[224:231], v236\n\t" \
  "v_smfmac_f32_16x16x64_bf16 v[20:23], v[140:143], v[224:231], v236\n\t" \
  "buffer_store_dwordx4 v[48:51], v250, s[20:23], s62 offen offset:0 sc1\n\t" \
  "ds_read_b64 v[224:225], v232 offset:16\n\t" \
  "v_add_f32 v202, v202, v16\n\t" \
  "v_fmac_f32 v203, v16, v16\n\t" \
  "v_add_f32 v202, v202, v17\n\t" \
  "v_fmac_f32 v203, v17, v17\n\t" \
  "v_add_f32 v202, v202, v18\n\t" \
  "v_fmac_f32 v203, v18, v18\n\t" \
  "s_waitcnt lgkmcnt(5)\n\t" \
  "v_smfmac_f32_16x16x64_bf16 v[36:39], v[104:107], v[208:215], v236\n\t" \
  "ds_read_b64 v[226:227], v232 offset:416\n\t" \
  "ds_read_b64 v[228:229], v232 offset:816\n\t" \
  "ds_read_b64 v[230:231], v232 offset:1216\n\t" \
  "v_add_f32 v202, v202, v19\n\t" \
  "v_fmac_f32 v203, v19, v19\n\t" \
  "v_smfmac_f32_16x16x64_bf16 v[24:27], v[108:111], v[208:215], v236\n\t" \
  "s_waitcnt lgkmcnt(4)\n\t" \
  "v_smfmac_f32_16x16x64_bf16 v[40:43], v[120:123], v[216:223], v236\n\t" \
  "v_add_f32 v202, v202, v20\n\t" \
  "v_fmac_f32 v203, v20, v20\n\t" \
  "v_add_f32 v202, v202, v21\n\t" \
  "v_fmac_f32 v203, v21, v21\n\t" \
  "v_add_f32 v202, v202, v22\n\t" \
  "v_fmac_f32 v203, v22, v22\n\t" \
  "v_smfmac_f32_16x16x64_bf16 v[28:31], v[124:127], v[216:223], v236\n\t" \
  "v_add_f32 v202, v202, v23\n\t" \
  "v_fmac_f32 v203, v23, v23\n\t" \
  "ds_write_b64 v233, v[202:203] offset:4096\n\t" \
  "s_waitcnt vmcnt(7) lgkmcnt(0)\n\t" \
  "s_barrier\n\t" \
  "ds_read_b64 v[208:209], v232 offset:16400\n\t" \
  "ds_read_b64 v[210:211], v232 offset:16800\n\t" \
  "ds_read_b64 v[212:213], v232 offset:17200\n\t" \
  "ds_read_b64 v[214:215], v232 offset:17600\n\t" \
  "ds_read_b64 v[216:217], v232 offset:16408\n\t" \
  "ds_read_b64 v[218:219], v232 offset:16808\n\t" \
  "ds_read_b64 v[220:221], v232 offset:17208\n\t" \
  "ds_read_b64 v[222:223], v232 offset:17608\n\t" \
  "s_cmp_ge_u32 s36, 2\n\t" \
  "s_cbranch_scc1 .Lcnx_c6\n\t" \
  "s_mov_b32 m0, s46\n\t" \
  "s_nop 0\n\t" \
  "buffer_load_dwordx4 v252, s[16:19], s33 offen lds\n\t" \
  "buffer_load_dwordx4 v252, s[16:19], s34 offen offset:1024 lds\n\t" \
  "buffer_load_dwordx4 v252, s[16:19], s35 offen offset:2048 lds\n\t" \
  "s_add_u32 s33, s33, s32\n\t" \
  "s_add_u32 s34, s34, s32\n\t" \
  "s_add_u32 s35, s35, s32\n\t" \
  "s_mov_b32 m0, s47\n\t" \
  "s_nop 0\n\t" \
  "buffer_load_dwordx4 v252, s[16:19], s33 offen lds\n\t" \
  "buffer_load_dwordx4 v252, s[16:19], s34 offen offset:1024 lds\n\t" \
  "buffer_load_dwordx4 v252, s[16:19], s35 offen offset:2048 lds\n\t" \
  "s_add_u32 s33, s33, s32\n\t" \
  "s_add_u32 s34, s34, s32\n\t" \
  "s_add_u32 s35, s35, s32\n\t" \
  ".Lcnx_c6:\n\t" \
  "v_smfmac_f32_16x16x64_bf16 v[44:47], v[136:139], v[224:231], v236\n\t" \
  "v_mul_f32 v205, v24, v24\n\t" \
  "v_add_f32 v204, v24, v25\n\t" \
  "v_smfmac_f32_16x16x64_bf16 v[32:35], v[140:143], v[224:231], v236\n\t" \
  "buffer_store_dwordx4 v[56:59], v250, s[20:23], s62 offen offset:64 sc1\n\t" \
  "ds_read_b64 v[224:225], v232 offset:16416\n\t" \
  "ds_read_b64 v[226:227], v232 offset:16816\n\t" \
  "v_fmac_f32 v205, v25, v25\n\t" \
  "v_add_f32 v204, v204, v26\n\t" \
  "v_fmac_f32 v205, v26, v26\n\t" \
  "s_cmp_lg_u32 s37, 0\n\t" \
  "s_cbranch_scc1 .Lcnx_c7\n\t" \
  "s_waitcnt lgkmcnt(15)\n\t" \
  "v_add_f32 v96, v96, v98\n\t" \
  "v_add_f32 v97, v97, v99\n\t" \
  "v_add_f32 v100, v100, v102\n\t" \
  "v_add_f32 v101, v101, v103\n\t" \
  "v_add_f32 v112, v112, v114\n\t" \
  "v_add_f32 v113, v113, v115\n\t" \
  "v_add_f32 v116, v116, v118\n\t" \
  "v_add_f32 v117, v117, v119\n\t" \
  "v_add_f32 v96, v96, v100\n\t" \
  "v_add_f32 v97, v97, v101\n\t" \
  "v_add_f32 v112, v112, v116\n\t" \
  "v_add_f32 v113, v113, v117\n\t" \
  "v_add_f32 v96, v96, v112\n\t" \
  "v_add_f32 v97, v97, v113\n\t" \
  "v_mul_f32 v96, 0x3c2aaaab, v96\n\t" \
  "v_mul_f32 v97, 0x3c2aaaab, v97\n\t" \
  "v_fma_f32 v97, -v96, v96, v97\n\t" \
  "v_max_f32 v97, 0, v97\n\t" \
  "v_add_f32 v97, 0x3727c5ac, v97\n\t" \
  "v_rsq_f32 v97, v97\n\t" \
  "s_nop 0\n\t" \
  "v_mul_f32 v98, 0x3e000000, v97\n\t" \
  "v_mul_f32 v99, -v96, v98\n\t" \
  "ds_write_b64 v233, v[98:99] offset:32784\n\t" \
  ".Lcnx_c7:\n\t" \
  "s_waitcnt lgkmcnt(6)\n\t" \
  "v_smfmac_f32_16x16x64_bf16 v[36:39], v[108:111], v[208:215], v236\n\t" \
  "ds_read_b64 v[228:229], v232 offset:17216\n\t" \
  "ds_read_b64 v[230:231], v232 offset:17616\n\t" \
  "v_add_f32 v204, v204, v27\n\t" \
  "v_fmac_f32 v205, v27, v27\n\t" \
  "v_add_f32 v204, v204, v28\n\t" \
  "s_waitcnt lgkmcnt(4)\n\t" \
  "v_smfmac_f32_16x16x64_bf16 v[40:43], v[124:127], v[216:223], v236\n\t" \
  "v_fmac_f32 v205, v28, v28\n\t" \
  "v_add_f32 v204, v204, v29\n\t" \
  "v_fmac_f32 v205, v29, v29\n\t" \
  "s_waitcnt lgkmcnt(0)\n\t" \
  "v_smfmac_f32_16x16x64_bf16 v[44:47], v[140:143], v[224:231], v236\n\t" \
  "v_add_f32 v204, v204, v30\n\t" \
  "v_fmac_f32 v205, v30, v30\n\t" \
  "v_add_f32 v204, v204, v31\n\t" \
  "v_fmac_f32 v205, v31, v31\n\t" \
  "v_add_f32 v204, v204, v32\n\t" \
  "v_fmac_f32 v205, v32, v32\n\t" \
  "v_add_f32 v204, v204, v33\n\t" \
  "v_fmac_f32 v205, v33, v33\n\t" \
  "v_add_f32 v204, v204, v34\n\t" \
  "v_fmac_f32 v205, v34, v34\n\t" \
  "v_add_f32 v204, v204, v35\n\t" \
  "v_fmac_f32 v205, v35, v35\n\t" \
  "v_mul_f32 v207, v36, v36\n\t" \
  "v_add_f32 v206, v36, v37\n\t" \
  "v_fmac_f32 v207, v37, v37\n\t" \
  "v_add_f32 v206, v206, v38\n\t" \
  "v_fmac_f32 v207, v38, v38\n\t" \
  "v_add_f32 v206, v206, v39\n\t" \
  "v_fmac_f32 v207, v39, v39\n\t" \
  "v_add_f32 v206, v206, v40\n\t" \
  "v_fmac_f32 v207, v40, v40\n\t" \
  "v_add_f32 v206, v206, v41\n\t" \
  "v_fmac_f32 v207, v41, v41\n\t" \
  "v_add_f32 v206, v206, v42\n\t" \
  "v_fmac_f32 v207, v42, v42\n\t" \
  "v_add_f32 v206, v206, v43\n\t" \
  "v_fmac_f32 v207, v43, v43\n\t" \
  "v_add_f32 v206, v206, v44\n\t" \
  "v_fmac_f32 v207, v44, v44\n\t" \
  "v_add_f32 v206, v206, v45\n\t" \
  "v_fmac_f32 v207, v45, v45\n\t" \
  "v_add_f32 v206, v206, v46\n\t" \
  "v_fmac_f32 v207, v46, v46\n\t" \
  "v_add_f32 v206, v206, v47\n\t" \
  "v_fmac_f32 v207, v47, v47\n\t" \
  "ds_write_b64 v233, v[204:205] offset:8192\n\t" \
  "ds_write_b64 v233, v[206:207] offset:12288\n\t" \
  "s_waitcnt lgkmcnt(0)\n\t" \
  "s_barrier\n\t" \
  "ds_read_b64 v[192:193], v251 offset:32784\n\t" \
  "ds_read_b64 v[194:195], v251 offset:33296\n\t" \
  "ds_read_b64 v[196:197], v251 offset:33808\n\t" \
  "ds_read_b64 v[198:199], v251 offset:34320\n\t" \
  "s_cmp_lg_u32 s37, 1\n\t" \
  "s_cbranch_scc1 .Lcnx_c8\n\t" \
  "v_add_u32 v128, s40, v251\n\t" \
  "ds_read_b64 v[96:97], v128 offset:0\n\t" \
  "ds_read_b64 v[98:99], v128 offset:512\n\t" \
  "ds_read_b64 v[100:101], v128 offset:1024\n\t" \
  "ds_read_b64 v[102:103], v128 offset:1536\n\t" \
  "ds_read_b64 v[112:113], v128 offset:2048\n\t" \
  "ds_read_b64 v[114:115], v128 offset:2560\n\t" \
  "ds_read_b64 v[116:117], v128 offset:3072\n\t" \
  "ds_read_b64 v[118:119], v128 offset:3584\n\t" \
  ".Lcnx_c8:\n\t" \
  "v_lshl_add_u32 v208, v251, 1, s42\n\t" \
  "v_lshl_add_u32 v209, v251, 1, s43\n\t" \
  "s_waitcnt lgkmcnt(3)\n\t" \
  ".p2align 3\n\t" \
  "v_fma_f32 v144, v144, v192, v193\n\t" \
  "v_fma_f32 v145, v145, v192, v193\n\t" \
  "v_fma_f32 v146, v146, v192, v193\n\t" \
  "v_fma_f32 v147, v147, v192, v193\n\t" \
  "v_fma_f32 v148, v148, v192, v193\n\t" \
  "v_fma_f32 v149, v149, v192, v193\n\t" \
  "v_fma_f32 v150, v150, v192, v193\n\t" \
  "v_fma_f32 v151, v151, v192, v193\n\t" \
  "v_fma_f32 v152, v152, v192, v193\n\t" \
  "v_fma_f32 v153, v153, v192, v193\n\t" \
  "v_fma_f32 v154, v154, v192, v193\n\t" \
  "v_fma_f32 v155, v155, v192, v193\n\t" \
  "v_cvt_pk_bf16_f32 v144, v144, v145\n\t" \
  "v_cvt_pk_bf16_f32 v145, v146, v147\n\t" \
  "v_cvt_pk_bf16_f32 v146, v148, v149\n\t" \
  "v_cvt_pk_bf16_f32 v147, v150, v151\n\t" \
  "v_cvt_pk_bf16_f32 v148, v152, v153\n\t" \
  "v_cvt_pk_bf16_f32 v149, v154, v155\n\t" \
  "ds_write_b128 v208, v[144:147] offset:0\n\t" \
  "ds_write_b64 v209, v[148:149] offset:0\n\t" \
  "buffer_store_dwordx4 v[64:67], v250, s[20:23], s62 offen offset:128 sc1\n\t" \
  "s_cmp_lg_u32 s37, 1\n\t" \
  "s_cbranch_scc1 .Lcnx_c9\n\t" \
  "s_waitcnt lgkmcnt(2)\n\t" \
  "v_add_f32 v96, v96, v98\n\t" \
  "v_add_f32 v97, v97, v99\n\t" \
  "v_add_f32 v100, v100, v102\n\t" \
  "v_add_f32 v101, v101, v103\n\t" \
  "v_add_f32 v112, v112, v114\n\t" \
  "v_add_f32 v113, v113, v115\n\t" \
  "v_add_f32 v116, v116, v118\n\t" \
  "v_add_f32 v117, v117, v119\n\t" \
  "v_add_f32 v96, v96, v100\n\t" \
  "v_add_f32 v97, v97, v101\n\t" \
  "v_add_f32 v112, v112, v116\n\t" \
  "v_add_f32 v113, v113, v117\n\t" \
  "v_add_f32 v96, v96, v112\n\t" \
  "v_add_f32 v97, v97, v113\n\t" \
  "v_mul_f32 v96, 0x3c2aaaab, v96\n\t" \
  "v_mul_f32 v97, 0x3c2aaaab, v97\n\t" \
  "v_fma_f32 v97, -v96, v96, v97\n\t" \
  "v_max_f32 v97, 0, v97\n\t" \
  "v_add_f32 v97, 0x3727c5ac, v97\n\t" \
  "v_rsq_f32 v97, v97\n\t" \
  "s_nop 0\n\t" \
  "v_mul_f32 v98, 0x3e000000, v97\n\t" \
  "v_mul_f32 v99, -v96, v98\n\t" \
  "ds_write_b64 v233, v[98:99] offset:14352\n\t" \
  ".Lcnx_c9:\n\t" \
  "s_waitcnt lgkmcnt(4)\n\t" \
  ".p2align 3\n\t" \
  "v_fma_f32 v156, v156, v194, v195\n\t" \
  "v_fma_f32 v157, v157, v194, v195\n\t" \
  "v_fma_f32 v158, v158, v194, v195\n\t" \
  "v_fma_f32 v159, v159, v194, v195\n\t" \
  "v_fma_f32 v160, v160, v194, v195\n\t" \
  "v_fma_f32 v161, v161, v194, v195\n\t" \
  "v_fma_f32 v162, v162, v194, v195\n\t" \
  "v_fma_f32 v163, v163, v194, v195\n\t" \
  "v_fma_f32 v164, v164, v194, v195\n\t" \
  "v_fma_f32 v165, v165, v194, v195\n\t" \
  "v_fma_f32 v166, v166, v194, v195\n\t" \
  "v_fma_f32 v167, v167, v194, v195\n\t" \
  "v_cvt_pk_bf16_f32 v156, v156, v157\n\t" \
  "v_cvt_pk_bf16_f32 v157, v158, v159\n\t" \
  "v_cvt_pk_bf16_f32 v158, v160, v161\n\t" \
  "v_cvt_pk_bf16_f32 v159, v162, v163\n\t" \
  "v_cvt_pk_bf16_f32 v160, v164, v165\n\t" \
  "v_cvt_pk_bf16_f32 v161, v166, v167\n\t" \
  "ds_write_b128 v208, v[156:159] offset:12288\n\t" \
  "ds_write_b64 v209, v[160:161] offset:12288\n\t" \
  "s_waitcnt lgkmcnt(5)\n\t" \
  ".p2align 3\n\t" \
  "v_fma_f32 v168, v168, v196, v197\n\t" \
  "v_fma_f32 v169, v169, v196, v197\n\t" \
  "v_fma_f32 v170, v170, v196, v197\n\t" \
  "v_fma_f32 v171, v171, v196, v197\n\t" \
  "v_fma_f32 v172, v172, v196, v197\n\t" \
  "v_fma_f32 v173, v173, v196, v197\n\t" \
  "v_fma_f32 v174, v174, v196, v197\n\t" \
  "v_fma_f32 v175, v175, v196, v197\n\t" \
  "v_fma_f32 v176, v176, v196, v197\n\t" \
  "v_fma_f32 v177, v177, v196, v197\n\t" \
  "v_fma_f32 v178, v178, v196, v197\n\t" \
  "v_fma_f32 v179, v179, v196, v197\n\t" \
  "v_cvt_pk_bf16_f32 v168, v168, v169\n\t" \
  "v_cvt_pk_bf16_f32 v169, v170, v171\n\t" \
  "v_cvt_pk_bf16_f32 v170, v172, v173\n\t" \
  "v_cvt_pk_bf16_f32 v171, v174, v175\n\t" \
  "v_cvt_pk_bf16_f32 v172, v176, v177\n\t" \
  "v_cvt_pk_bf16_f32 v173, v178, v179\n\t" \
  "ds_write_b128 v208, v[168:171] offset:24576\n\t" \
  "ds_write_b64 v209, v[172:173] offset:24576\n\t" \
  "s_waitcnt lgkmcnt(6)\n\t" \
  ".p2align 3\n\t" \
  "v_fma_f32 v180, v180, v198, v199\n\t" \
  "v_fma_f32 v181, v181, v198, v199\n\t" \
  "v_fma_f32 v182, v182, v198, v199\n\t" \
  "v_fma_f32 v183, v183, v198, v199\n\t" \
  "v_fma_f32 v184, v184, v198, v199\n\t" \
  "v_fma_f32 v185, v185, v198, v199\n\t" \
  "v_fma_f32 v186, v186, v198, v199\n\t" \
  "v_fma_f32 v187, v187, v198, v199\n\t" \
  "v_fma_f32 v188, v188, v198, v199\n\t" \
  "v_fma_f32 v189, v189, v198, v199\n\t" \
  "v_fma_f32 v190, v190, v198, v199\n\t" \
  "v_fma_f32 v191, v191, v198, v199\n\t" \
  "v_cvt_pk_bf16_f32 v180, v180, v181\n\t" \
  "v_cvt_pk_bf16_f32 v181, v182, v183\n\t" \
  "v_cvt_pk_bf16_f32 v182, v184, v185\n\t" \
  "v_cvt_pk_bf16_f32 v183, v186, v187\n\t" \
  "v_cvt_pk_bf16_f32 v184, v188, v189\n\t" \
  "v_cvt_pk_bf16_f32 v185, v190, v191\n\t" \
  "ds_write_b128 v208, v[180:183] offset:36864\n\t" \
  "ds_write_b64 v209, v[184:185] offset:36864\n\t" \
  "v_add_u32 v208, 49152, v208\n\t" \
  "v_add_u32 v209, 49152, v209\n\t" \
  "s_waitcnt lgkmcnt(0)\n\t" \
  "s_barrier\n\t" \
  "ds_read_b64 v[200:201], v251 offset:16400\n\t" \
  "ds_read_b64 v[202:203], v251 offset:16912\n\t" \
  "ds_read_b64 v[204:205], v251 offset:17424\n\t" \
  "ds_read_b64 v[206:207], v251 offset:17936\n\t" \
  "s_waitcnt lgkmcnt(3)\n\t" \
  ".p2align 3\n\t" \
  "v_fma_f32 v0, v0, v200, v201\n\t" \
  "v_fma_f32 v1, v1, v200, v201\n\t" \
  "v_fma_f32 v2, v2, v200, v201\n\t" \
  "v_fma_f32 v3, v3, v200, v201\n\t" \
  "v_fma_f32 v4, v4, v200, v201\n\t" \
  "v_fma_f32 v5, v5, v200, v201\n\t" \
  "v_fma_f32 v6, v6, v200, v201\n\t" \
  "v_fma_f32 v7, v7, v200, v201\n\t" \
  "v_fma_f32 v8, v8, v200, v201\n\t" \
  "v_fma_f32 v9, v9, v200, v201\n\t" \
  "v_fma_f32 v10, v10, v200, v201\n\t" \
  "v_fma_f32 v11, v11, v200, v201\n\t" \
  "v_cvt_pk_bf16_f32 v0, v0, v1\n\t" \
  "v_cvt_pk_bf16_f32 v1, v2, v3\n\t" \
  "v_cvt_pk_bf16_f32 v2, v4, v5\n\t" \
  "v_cvt_pk_bf16_f32 v3, v6, v7\n\t" \
  "v_cvt_pk_bf16_f32 v4, v8, v9\n\t" \
  "v_cvt_pk_bf16_f32 v5, v10, v11\n\t" \
  "ds_write_b128 v208, v[0:3] offset:0\n\t" \
  "ds_write_b64 v209, v[4:5] offset:0\n\t" \
  "buffer_store_dwordx4 v[72:75], v250, s[20:23], s62 offen offset:192 sc1\n\t" \
  "s_waitcnt lgkmcnt(4)\n\t" \
  ".p2align 3\n\t" \
  "v_fma_f32 v12, v12, v202, v203\n\t" \
  "v_fma_f32 v13, v13, v202, v203\n\t" \
  "v_fma_f32 v14, v14, v202, v203\n\t" \
  "v_fma_f32 v15, v15, v202, v203\n\t" \
  "v_fma_f32 v16, v16, v202, v203\n\t" \
  "v_fma_f32 v17, v17, v202, v203\n\t" \
  "v_fma_f32 v18, v18, v202, v203\n\t" \
  "v_fma_f32 v19, v19, v202, v203\n\t" \
  "v_fma_f32 v20, v20, v202, v203\n\t" \
  "v_fma_f32 v21, v21, v202, v203\n\t" \
  "v_fma_f32 v22, v22, v202, v203\n\t" \
  "v_fma_f32 v23, v23, v202, v203\n\t" \
  "v_cvt_pk_bf16_f32 v12, v12, v13\n\t" \
  "v_cvt_pk_bf16_f32 v13, v14, v15\n\t" \
  "v_cvt_pk_bf16_f32 v14, v16, v17\n\t" \
  "v_cvt_pk_bf16_f32 v15, v18, v19\n\t" \
  "v_cvt_pk_bf16_f32 v16, v20, v21\n\t" \
  "v_cvt_pk_bf16_f32 v17, v22, v23\n\t" \
  "ds_write_b128 v208, v[12:15] offset:12288\n\t" \
  "ds_write_b64 v209, v[16:17] offset:12288\n\t" \
  "s_waitcnt lgkmcnt(5)\n\t" \
  ".p2align 3\n\t" \
  "v_fma_f32 v24, v24, v204, v205\n\t" \
  "v_fma_f32 v25, v25, v204, v205\n\t" \
  "v_fma_f32 v26, v26, v204, v205\n\t" \
  "v_fma_f32 v27, v27, v204, v205\n\t" \
  "v_fma_f32 v28, v28, v204, v205\n\t" \
  "v_fma_f32 v29, v29, v204, v205\n\t" \
  "v_fma_f32 v30, v30, v204, v205\n\t" \
  "v_fma_f32 v31, v31, v204, v205\n\t" \
  "v_fma_f32 v32, v32, v204, v205\n\t" \
  "v_fma_f32 v33, v33, v204, v205\n\t" \
  "v_fma_f32 v34, v34, v204, v205\n\t" \
  "v_fma_f32 v35, v35, v204, v205\n\t" \
  "v_cvt_pk_bf16_f32 v24, v24, v25\n\t" \
  "v_cvt_pk_bf16_f32 v25, v26, v27\n\t" \
  "v_cvt_pk_bf16_f32 v26, v28, v29\n\t" \
  "v_cvt_pk_bf16_f32 v27, v30, v31\n\t" \
  "v_cvt_pk_bf16_f32 v28, v32, v33\n\t" \
  "v_cvt_pk_bf16_f32 v29, v34, v35\n\t" \
  "ds_write_b128 v208, v[24:27] offset:24576\n\t" \
  "ds_write_b64 v209, v[28:29] offset:24576\n\t" \
  "s_waitcnt lgkmcnt(6)\n\t" \
  ".p2align 3\n\t" \
  "v_fma_f32 v36, v36, v206, v207\n\t" \
  "v_fma_f32 v37, v37, v206, v207\n\t" \
  "v_fma_f32 v38, v38, v206, v207\n\t" \
  "v_fma_f32 v39, v39, v206, v207\n\t" \
  "v_fma_f32 v40, v40, v206, v207\n\t" \
  "v_fma_f32 v41, v41, v206, v207\n\t" \
  "v_fma_f32 v42, v42, v206, v207\n\t" \
  "v_fma_f32 v43, v43, v206, v207\n\t" \
  "v_fma_f32 v44, v44, v206, v207\n\t" \
  "v_fma_f32 v45, v45, v206, v207\n\t" \
  "v_fma_f32 v46, v46, v206, v207\n\t" \
  "v_fma_f32 v47, v47, v206, v207\n\t" \
  "v_cvt_pk_bf16_f32 v36, v36, v37\n\t" \
  "v_cvt_pk_bf16_f32 v37, v38, v39\n\t" \
  "v_cvt_pk_bf16_f32 v38, v40, v41\n\t" \
  "v_cvt_pk_bf16_f32 v39, v42, v43\n\t" \
  "v_cvt_pk_bf16_f32 v40, v44, v45\n\t" \
  "v_cvt_pk_bf16_f32 v41, v46, v47\n\t" \
  "ds_write_b128 v208, v[36:39] offset:36864\n\t" \
  "ds_write_b64 v209, v[40:41] offset:36864\n\t" \
  "v_lshlrev_b32 v232, 1, v251\n\t" \
  "v_lshl_add_u32 v237, v251, 3, s39\n\t" \
  "v_and_b32 v232, 0xf0, v232\n\t" \
  "v_mad_i32_i24 v237, v232, -3, v237\n\t" \
  "s_cmp_lg_u32 s58, 0\n\t" \
  "s_cbranch_scc1 .Lcnx_c10\n\t" \
  "s_waitcnt vmcnt(0)\n\t" \
  ".Lcnx_c10:\n\t" \
  "s_waitcnt lgkmcnt(0)\n\t" \
  "s_barrier\n\t" \
  "ds_read_b64_tr_b16 v[208:209], v248 offset:43048\n\t" \
  "ds_read_b64_tr_b16 v[210:211], v248 offset:44072\n\t" \
  "ds_read_b128 v[96:99], v237 offset:0\n\t" \
  "ds_read_b128 v[108:111], v237 offset:256\n\t" \
  "ds_read_b128 v[120:123], v237 offset:512\n\t" \
  "ds_read_b128 v[132:135], v237 offset:768\n\t" \
  "ds_read_b64_tr_b16 v[212:213], v248 offset:46120\n\t" \
  "ds_read_b64_tr_b16 v[214:215], v248 offset:47144\n\t" \
  "ds_read_b128 v[100:103], v237 offset:4096\n\t" \
  "ds_read_b128 v[112:115], v237 offset:4352\n\t" \
  "ds_read_b128 v[124:127], v237 offset:4608\n\t" \
  "ds_read_b128 v[136:139], v237 offset:4864\n\t" \
  "ds_read_b128 v[104:107], v237 offset:8192\n\t" \
  "ds_read_b128 v[116:119], v237 offset:8448\n\t" \
  "ds_read_b128 v[128:131], v237 offset:8704\n\t" \
  "ds_read_b128 v[140:143], v237 offset:8960\n\t" \
  "s_waitcnt lgkmcnt(13)\n\t" \
  "v_mfma_f32_16x16x32_bf16 v[144:147], v[208:211], v[96:99], 0\n\t" \
  "s_waitcnt lgkmcnt(12)\n\t" \
  "v_mfma_f32_16x16x32_bf16 v[152:155], v[208:211], v[108:111], 0\n\t" \
  "s_cmp_eq_u32 s37, 0\n\t" \
  "s_cbranch_scc1 .Lcnx_f0\n.Lcnx_g0:\n\t" \
  "s_waitcnt lgkmcnt(11)\n\t" \
  "v_mfma_f32_16x16x32_bf16 v[160:163], v[208:211], v[120:123], 0\n\t" \
  "buffer_store_dwordx4 v[80:83], v250, s[20:23], s62 offen offset:256 sc1\n\t" \
  "s_waitcnt lgkmcnt(10)\n\t" \
  "v_mfma_f32_16x16x32_bf16 v[168:171], v[208:211], v[132:135], 0\n\t" \
  "ds_read_b64_tr_b16 v[208:209], v248 offset:45096\n\t" \
  "s_waitcnt lgkmcnt(8)\n\t" \
  "v_mfma_f32_16x16x32_bf16 v[144:147], v[212:215], v[100:103], v[144:147]\n\t" \
  "ds_read_b64_tr_b16 v[210:211], v248 offset:48168\n\t" \
  "s_waitcnt lgkmcnt(8)\n\t" \
  "v_mfma_f32_16x16x32_bf16 v[152:155], v[212:215], v[112:115], v[152:155]\n\t" \
  "s_waitcnt lgkmcnt(7)\n\t" \
  "v_mfma_f32_16x16x32_bf16 v[160:163], v[212:215], v[124:127], v[160:163]\n\t" \
  "s_waitcnt lgkmcnt(6)\n\t" \
  "v_mfma_f32_16x16x32_bf16 v[168:171], v[212:215], v[136:139], v[168:171]\n\t" \
  "ds_read_b64_tr_b16 v[212:213], v248 offset:43560\n\t" \
  "s_waitcnt lgkmcnt(1)\n\t" \
  "v_mfma_f32_16x16x32_bf16 v[144:147], v[208:211], v[104:107], v[144:147]\n\t" \
  "ds_read_b64_tr_b16 v[214:215], v248 offset:44584\n\t" \
  "v_mfma_f32_16x16x32_bf16 v[152:155], v[208:211], v[116:119], v[152:155]\n\t" \
  "v_mfma_f32_16x16x32_bf16 v[160:163], v[208:211], v[128:131], v[160:163]\n\t" \
  "v_mfma_f32_16x16x32_bf16 v[168:171], v[208:211], v[140:143], v[168:171]\n\t" \
  "ds_read_b64_tr_b16 v[208:209], v248 offset:46632\n\t" \
  "s_waitcnt lgkmcnt(1)\n\t" \
  "v_mfma_f32_16x16x32_bf16 v[148:151], v[212:215], v[96:99], 0\n\t" \
  "buffer_store_dwordx4 v[88:91], v250, s[20:23], s62 offen offset:320 sc1\n\t" \
  "ds_read_b64_tr_b16 v[210:211], v248 offset:47656\n\t" \
  "v_mfma_f32_16x16x32_bf16 v[156:159], v[212:215], v[108:111], 0\n\t" \
  "v_mfma_f32_16x16x32_bf16 v[164:167], v[212:215], v[120:123], 0\n\t" \
  "v_mfma_f32_16x16x32_bf16 v[172:175], v[212:215], v[132:135], 0\n\t" \
  "ds_read_b64_tr_b16 v[212:213], v248 offset:45608\n\t" \
  "s_waitcnt lgkmcnt(1)\n\t" \
  "v_mfma_f32_16x16x32_bf16 v[148:151], v[208:211], v[100:103], v[148:151]\n\t" \
  "ds_read_b64_tr_b16 v[214:215], v248 offset:48680\n\t" \
  "v_mfma_f32_16x16x32_bf16 v[156:159], v[208:211], v[112:115], v[156:159]\n\t" \
  "s_mov_b64 exec, 3\n\t" \
  "ds_add_rtn_u32 v246, v251, v250 offset:47648\n\t" \
  "s_mov_b64 exec, -1\n\t" \
  "v_cvt_pk_f16_f32 v216, |v146|, |v147| clamp\n\t" \
  "v_cvt_pk_f16_f32 v232, v144, v145\n\t" \
  "v_mfma_f32_16x16x32_bf16 v[164:167], v[208:211], v[124:127], v[164:167]\n\t" \
  "v_cvt_pk_f16_f32 v218, |v154|, |v155| clamp\n\t" \
  "v_cvt_pk_f16_f32 v220, |v152|, |v153| clamp\n\t" \
  "v_pk_mul_f16 v233, v232, v232\n\t" \
  "v_mfma_f32_16x16x32_bf16 v[172:175], v[208:211], v[136:139], v[172:175]\n\t" \
  "ds_read_b64_tr_b16 v[208:209], v248 offset:49192\n\t" \
  "v_and_b32_sdwa v217, s56, v216 dst_sel:DWORD dst_unused:UNUSED_PAD src0_sel:DWORD src1_sel:WORD_1\n\t" \
  "v_and_b32 v216, s56, v216\n\t" \
  "s_waitcnt lgkmcnt(2)\n\t" \
  "v_mfma_f32_16x16x32_bf16 v[148:151], v[212:215], v[104:107], v[148:151]\n\t" \
  "ds_read_b64_tr_b16 v[210:211], v248 offset:50216\n\t" \
  "v_and_b32_sdwa v219, s56, v218 dst_sel:DWORD dst_unused:UNUSED_PAD src0_sel:DWORD src1_sel:WORD_1\n\t" \
  "v_mfma_f32_16x16x32_bf16 v[156:159], v[212:215], v[116:119], v[156:159]\n\t" \
  "ds_read_b32 v217, v217 offset:0\n\t" \
  "v_and_b32 v218, s56, v218\n\t" \
  "v_and_b32_sdwa v221, s56, v220 dst_sel:DWORD dst_unused:UNUSED_PAD src0_sel:DWORD src1_sel:WORD_1\n\t" \
  "v_mfma_f32_16x16x32_bf16 v[164:167], v[212:215], v[128:131], v[164:167]\n\t" \
  "ds_read_b32 v216, v216 offset:0\n\t" \
  "v_and_b32 v220, s56, v220\n\t" \
  "v_mfma_f32_16x16x32_bf16 v[172:175], v[212:215], v[140:143], v[172:175]\n\t" \
  "ds_read_b64_tr_b16 v[212:213], v248 offset:52264\n\t" \
  "v_pk_fma_f16 v233, v233, v253, s54\n\t" \
  "v_pk_mul_f16 v233, v232, v233\n\t" \
  "s_cmp_lg_u32 s38, 4\n\t" \
  "s_cbranch_scc1 .Lcnx_c22\n\t" \
  "s_mov_b32 m0, 163632\n\t" \
  "s_add_u32 s30, s30, s31\n\t" \
  "buffer_load_dword v252, s[8:11], s30 offen lds\n\t" \
  ".Lcnx_c22:\n\t" \
  "s_setprio 0\n\t" \
  "v_exp_f16 v232, v233\n\t" \
  "v_cvt_pk_f16_f32 v224, |v162|, |v163| clamp\n\t" \
  "s_waitcnt lgkmcnt(3)\n\t" \
  "v_mfma_f32_16x16x32_bf16 v[176:179], v[208:211], v[96:99], 0\n\t" \
  "ds_read_b64_tr_b16 v[214:215], v248 offset:53288\n\t" \
  "v_cvt_pk_f16_f32 v234, v160, v161\n\t" \
  "v_cvt_pk_f16_f32 v226, |v170|, |v171| clamp\n\t" \
  "v_cvt_pk_f16_f32 v228, |v168|, |v169| clamp\n\t" \
  "v_exp_f16_sdwa v232, v233 dst_sel:WORD_1 dst_unused:UNUSED_PRESERVE src0_sel:WORD_1\n\t" \
  "v_pk_mul_f16 v235, v234, v234\n\t" \
  "v_mfma_f32_16x16x32_bf16 v[184:187], v[208:211], v[108:111], 0\n\t" \
  "buffer_load_dwordx4 v[236:239], v250, s[8:11], s28 offen offset:0\n\t" \
  "s_cmp_lg_u32 s38, 5\n\t" \
  "s_cbranch_scc1 .Lcnx_c23\n\t" \
  "s_mov_b32 m0, 163632\n\t" \
  "s_add_u32 s30, s30, s31\n\t" \
  "buffer_load_dword v252, s[8:11], s30 offen lds\n\t" \
  ".Lcnx_c23:\n\t" \
  "ds_read_b32 v219, v219 offset:0\n\t" \
  "v_and_b32_sdwa v225, s56, v224 dst_sel:DWORD dst_unused:UNUSED_PAD src0_sel:DWORD src1_sel:WORD_1\n\t" \
  "v_and_b32 v224, s56, v224\n\t" \
  "v_and_b32_sdwa v227, s56, v226 dst_sel:DWORD dst_unused:UNUSED_PAD src0_sel:DWORD src1_sel:WORD_1\n\t" \
  "v_and_b32 v226, s56, v226\n\t" \
  "v_mfma_f32_16x16x32_bf16 v[192:195], v[208:211], v[120:123], 0\n\t" \
  "ds_read_b32 v218, v218 offset:0\n\t" \
  "v_and_b32_sdwa v229, s56, v228 dst_sel:DWORD dst_unused:UNUSED_PAD src0_sel:DWORD src1_sel:WORD_1\n\t" \
  "v_and_b32 v228, s56, v228\n\t" \
  "v_mfma_f32_16x16x32_bf16 v[200:203], v[208:211], v[132:135], 0\n\t" \
  "ds_read_b64_tr_b16 v[208:209], v248 offset:51240\n\t" \
  "ds_read_b64_tr_b16 v[210:211], v248 offset:54312\n\t" \
  "v_pk_fma_f16 v235, v235, v253, s54\n\t" \
  "v_pk_mul_f16 v235, v234, v235\n\t" \
  "ds_read_b32 v221, v221 offset:0\n\t" \
  "ds_read_b32 v220, v220 offset:0\n\t" \
  "ds_read_b32 v225, v225 offset:0\n\t" \
  "ds_read_b32 v224, v224 offset:0\n\t" \
  "ds_read_b32 v227, v227 offset:0\n\t" \
  "ds_read_b32 v226, v226 offset:0\n\t" \
  "ds_read_b32 v229, v229 offset:0\n\t" \
  "ds_read_b32 v228, v228 offset:0\n\t" \
  "s_waitcnt lgkmcnt(11)\n\t" \
  "v_fma_f32 v147, |v147|, v217, v147 mul:4\n\t" \
  "v_fma_f32 v146, |v146|, v216, v146 mul:4\n\t" \
  "v_fma_f32 v155, |v155|, v219, v155 mul:4\n\t" \
  "v_mfma_f32_16x16x32_bf16 v[176:179], v[212:215], v[100:103], v[176:179]\n\t" \
  "s_waitcnt lgkmcnt(6)\n\t" \
  "v_fma_f32 v154, |v154|, v218, v154 mul:4\n\t" \
  "v_fma_f32 v153, |v153|, v221, v153 mul:4\n\t" \
  "v_fma_f32 v152, |v152|, v220, v152 mul:4\n\t" \
  "v_pk_fma_f16 v232, v232, s55, s55\n\t" \
  "v_cvt_pk_bf16_f32 v147, v146, v147\n\t" \
  "v_mfma_f32_16x16x32_bf16 v[184:187], v[212:215], v[112:115], v[184:187]\n\t" \
  "s_cmp_lg_u32 s37, 0\n\t" \
  "s_cbranch_scc1 .Lcnx_c11\n\t" \
  "s_waitcnt vmcnt(3)\n\t" \
  ".Lcnx_c11:\n\t" \
  "s_mov_b64 exec, 3\n\t" \
  "ds_add_rtn_u32 v246, v251, v250 offset:47656\n\t" \
  "s_mov_b64 exec, -1\n\t" \
  "v_cvt_pk_bf16_f32 v155, v154, v155\n\t" \
  "s_setprio 1\n\t" \
  "v_cvt_pk_bf16_f32 v154, v152, v153\n\t" \
  "v_rcp_f16 v233, v232\n\t" \
  "v_exp_f16 v234, v235\n\t" \
  "v_cvt_pk_f16_f32 v216, |v148|, |v149| clamp\n\t" \
  "v_cvt_pk_f16_f32 v218, |v150|, |v151| clamp\n\t" \
  "v_mfma_f32_16x16x32_bf16 v[192:195], v[212:215], v[124:127], v[192:195]\n\t" \
  "v_cvt_pk_f16_f32 v220, |v156|, |v157| clamp\n\t" \
  "v_cvt_pk_f16_f32 v222, |v158|, |v159| clamp\n\t" \
  "v_rcp_f16_sdwa v232, v232 dst_sel:DWORD dst_unused:UNUSED_PAD src0_sel:WORD_1\n\t" \
  "v_exp_f16_sdwa v234, v235 dst_sel:WORD_1 dst_unused:UNUSED_PRESERVE src0_sel:WORD_1\n\t" \
  "v_and_b32_sdwa v217, s56, v216 dst_sel:DWORD dst_unused:UNUSED_PAD src0_sel:DWORD src1_sel:WORD_1\n\t" \
  "v_and_b32 v216, s56, v216\n\t" \
  "v_mfma_f32_16x16x32_bf16 v[200:203], v[212:215], v[136:139], v[200:203]\n\t" \
  "s_waitcnt lgkmcnt(0)\n\t" \
  "v_readfirstlane_b32 s4, v246\n\t" \
  "s_cmp_lt_u32 s4, s57\n\t" \
  "s_cbranch_scc1 .Lcnx_w9\n.Lcnx_b9:\n\t" \
  "ds_read_b64_tr_b16 v[212:213], v248 offset:49704\n\t" \
  "v_and_b32_sdwa v219, s56, v218 dst_sel:DWORD dst_unused:UNUSED_PAD src0_sel:DWORD src1_sel:WORD_1\n\t" \
  "v_and_b32 v218, s56, v218\n\t" \
  "v_mfma_f32_16x16x32_bf16 v[176:179], v[208:211], v[104:107], v[176:179]\n\t" \
  "ds_read_b64_tr_b16 v[214:215], v248 offset:50728\n\t" \
  "v_and_b32_sdwa v221, s56, v220 dst_sel:DWORD dst_unused:UNUSED_PAD src0_sel:DWORD src1_sel:WORD_1\n\t" \
  "v_and_b32 v220, s56, v220\n\t" \
  "v_and_b32_sdwa v223, s56, v222 dst_sel:DWORD dst_unused:UNUSED_PAD src0_sel:DWORD src1_sel:WORD_1\n\t" \
  "v_mfma_f32_16x16x32_bf16 v[184:187], v[208:211], v[116:119], v[184:187]\n\t" \
  "s_cmp_eq_u32 s37, 0\n\t" \
  "s_cbranch_scc1 .Lcnx_f1\n.Lcnx_g1:\n\t" \
  "ds_read_b32 v217, v217 offset:0\n\t" \
  "ds_read_b32 v216, v216 offset:0\n\t" \
  "v_and_b32 v222, s56, v222\n\t" \
  "v_fma_f32 v163, |v163|, v225, v163 mul:4\n\t" \
  "v_fma_f32 v162, |v162|, v224, v162 mul:4\n\t" \
  "v_mfma_f32_16x16x32_bf16 v[192:195], v[208:211], v[128:131], v[192:195]\n\t" \
  "ds_read_b32 v219, v219 offset:0\n\t" \
  "ds_read_b32 v218, v218 offset:0\n\t" \
  "v_fma_f32 v171, |v171|, v227, v171 mul:4\n\t" \
  "v_fma_f32 v170, |v170|, v226, v170 mul:4\n\t" \
  "v_fma_f32 v169, |v169|, v229, v169 mul:4\n\t" \
  "v_fma_f32 v168, |v168|, v228, v168 mul:4\n\t" \
  "v_fma_mix_f32 v144, v144, v233, 0 op_sel_hi:[0,1,0]\n\t" \
  "v_fma_mix_f32 v145, v145, v232, 0 op_sel_hi:[0,1,0]\n\t" \
  "v_mfma_f32_16x16x32_bf16 v[200:203], v[208:211], v[140:143], v[200:203]\n\t" \
  "ds_read_b64_tr_b16 v[208:209], v248 offset:52776\n\t" \
  "ds_read_b64_tr_b16 v[210:211], v248 offset:53800\n\t" \
  "v_pk_fma_f16 v234, v234, s55, s55\n\t" \
  "v_cvt_pk_bf16_f32 v146, v144, v145\n\t" \
  "v_cvt_pk_bf16_f32 v163, v162, v163\n\t" \
  "v_cvt_pk_bf16_f32 v171, v170, v171\n\t" \
  "v_cvt_pk_bf16_f32 v170, v168, v169\n\t" \
  "s_setprio 2\n\t" \
  "v_rcp_f16 v235, v234\n\t" \
  "s_waitcnt lgkmcnt(6)\n\t" \
  "v_mfma_f32_16x16x32_bf16 v[180:183], v[212:215], v[96:99], 0\n\t" \
  "ds_read_b32 v221, v221 offset:0\n\t" \
  "v_cvt_pk_f16_f32 v224, |v164|, |v165| clamp\n\t" \
  "v_cvt_pk_f16_f32 v226, |v166|, |v167| clamp\n\t" \
  "v_cvt_pk_f16_f32 v228, |v172|, |v173| clamp\n\t" \
  "v_cvt_pk_f16_f32 v230, |v174|, |v175| clamp\n\t" \
  "v_rcp_f16_sdwa v234, v234 dst_sel:DWORD dst_unused:UNUSED_PAD src0_sel:WORD_1\n\t" \
  "v_mfma_f32_16x16x32_bf16 v[188:191], v[212:215], v[108:111], 0\n\t" \
  "ds_read_b32 v220, v220 offset:0\n\t" \
  "v_and_b32_sdwa v225, s56, v224 dst_sel:DWORD dst_unused:UNUSED_PAD src0_sel:DWORD src1_sel:WORD_1\n\t" \
  "v_and_b32 v224, s56, v224\n\t" \
  "v_and_b32_sdwa v227, s56, v226 dst_sel:DWORD dst_unused:UNUSED_PAD src0_sel:DWORD src1_sel:WORD_1\n\t" \
  "v_and_b32 v226, s56, v226\n\t" \
  "v_mfma_f32_16x16x32_bf16 v[196:199], v[212:215], v[120:123], 0\n\t" \
  "ds_read_b32 v223, v223 offset:0\n\t" \
  "v_and_b32_sdwa v229, s56, v228 dst_sel:DWORD dst_unused:UNUSED_PAD src0_sel:DWORD src1_sel:WORD_1\n\t" \
  "v_and_b32 v228, s56, v228\n\t" \
  "v_mfma_f32_16x16x32_bf16 v[204:207], v[212:215], v[132:135], 0\n\t" \
  "ds_read_b64_tr_b16 v[212:213], v248 offset:51752\n\t" \
  "ds_read_b64_tr_b16 v[214:215], v248 offset:54824\n\t" \
  "v_and_b32_sdwa v231, s56, v230 dst_sel:DWORD dst_unused:UNUSED_PAD src0_sel:DWORD src1_sel:WORD_1\n\t" \
  "v_and_b32 v230, s56, v230\n\t" \
  "ds_read_b32 v222, v222 offset:0\n\t" \
  "ds_read_b32 v225, v225 offset:0\n\t" \
  "ds_read_b32 v224, v224 offset:0\n\t" \
  "ds_read_b32 v227, v227 offset:0\n\t" \
  "ds_read_b32 v226, v226 offset:0\n\t" \
  "ds_read_b32 v229, v229 offset:0\n\t" \
  "ds_read_b32 v228, v228 offset:0\n\t" \
  "ds_read_b32 v231, v231 offset:0\n\t" \
  "s_waitcnt lgkmcnt(15)\n\t" \
  "v_fma_f32 v149, |v149|, v217, v149 mul:4\n\t" \
  "s_waitcnt lgkmcnt(13)\n\t" \
  "v_mfma_f32_16x16x32_bf16 v[180:183], v[208:211], v[100:103], v[180:183]\n\t" \
  "ds_read_b32 v230, v230 offset:0\n\t" \
  "v_fma_f32 v148, |v148|, v216, v148 mul:4\n\t" \
  "v_fma_f32 v151, |v151|, v219, v151 mul:4\n\t" \
  "v_fma_f32 v150, |v150|, v218, v150 mul:4\n\t" \
  "s_waitcnt lgkmcnt(8)\n\t" \
  "v_fma_f32 v157, |v157|, v221, v157 mul:4\n\t" \
  "v_fma_f32 v156, |v156|, v220, v156 mul:4\n\t" \
  "v_mfma_f32_16x16x32_bf16 v[188:191], v[208:211], v[112:115], v[188:191]\n\t" \
  "s_waitcnt vmcnt(0)\n\t" \
  "s_mov_b64 exec, 3\n\t" \
  "ds_add_rtn_u32 v246, v251, v250 offset:47664\n\t" \
  "s_mov_b64 exec, -1\n\t" \
  "v_fma_f32 v159, |v159|, v223, v159 mul:4\n\t" \
  "v_fma_f32 v158, |v158|, v222, v158 mul:4\n\t" \
  "s_setprio 3\n\t" \
  "v_fma_mix_f32 v160, v160, v235, 0 op_sel_hi:[0,1,0]\n\t" \
  "v_fma_mix_f32 v161, v161, v234, 0 op_sel_hi:[0,1,0]\n\t" \
  "v_cvt_pk_bf16_f32 v162, v160, v161\n\t" \
  "v_cvt_pk_bf16_f32 v148, v148, v149\n\t" \
  "v_mfma_f32_16x16x32_bf16 v[196:199], v[208:211], v[124:127], v[196:199]\n\t" \
  "v_cvt_pk_bf16_f32 v149, v150, v151\n\t" \
  "v_cvt_pk_bf16_f32 v156, v156, v157\n\t" \
  "v_cvt_pk_bf16_f32 v157, v158, v159\n\t" \
  "v_cvt_pk_f16_f32 v216, |v178|, |v179| clamp\n\t" \
  "v_cvt_pk_f16_f32 v232, v176, v177\n\t" \
  "v_cvt_pk_f16_f32 v218, |v186|, |v187| clamp\n\t" \
  "v_mfma_f32_16x16x32_bf16 v[204:207], v[208:211], v[136:139], v[204:207]\n\t" \
  "s_waitcnt lgkmcnt(0)\n\t" \
  "v_readfirstlane_b32 s4, v246\n\t" \
  "s_cmp_lt_u32 s4, s57\n\t" \
  "s_cbranch_scc1 .Lcnx_w12\n.Lcnx_b12:\n\t" \
  "ds_read_b64_tr_b16 v[208:209], v249 offset:24600\n\t" \
  "v_cvt_pk_f16_f32 v220, |v184|, |v185| clamp\n\t" \
  "v_pk_mul_f16 v233, v232, v232\n\t" \
  "v_and_b32_sdwa v217, s56, v216 dst_sel:DWORD dst_unused:UNUSED_PAD src0_sel:DWORD src1_sel:WORD_1\n\t" \
  "v_and_b32 v216, s56, v216\n\t" \
  "v_mfma_f32_16x16x32_bf16 v[180:183], v[212:215], v[104:107], v[180:183]\n\t" \
  "ds_read_b64_tr_b16 v[210:211], v249 offset:26136\n\t" \
  "v_and_b32_sdwa v219, s56, v218 dst_sel:DWORD dst_unused:UNUSED_PAD src0_sel:DWORD src1_sel:WORD_1\n\t" \
  "v_and_b32 v218, s56, v218\n\t" \
  "v_and_b32_sdwa v221, s56, v220 dst_sel:DWORD dst_unused:UNUSED_PAD src0_sel:DWORD src1_sel:WORD_1\n\t" \
  "v_mfma_f32_16x16x32_bf16 v[188:191], v[212:215], v[116:119], v[188:191]\n\t" \
  "s_cmp_eq_u32 s37, 0\n\t" \
  "s_cbranch_scc1 .Lcnx_f2\n.Lcnx_g2:\n\t" \
  "ds_read_b32 v217, v217 offset:0\n\t" \
  "v_and_b32 v220, s56, v220\n\t" \
  "v_pk_fma_f16 v233, v233, v253, s54\n\t" \
  "v_pk_mul_f16 v233, v232, v233\n\t" \
  "v_mfma_f32_16x16x32_bf16 v[196:199], v[212:215], v[128:131], v[196:199]\n\t" \
  "ds_read_b32 v216, v216 offset:0\n\t" \
  "v_fma_f32 v165, |v165|, v225, v165 mul:4\n\t" \
  "v_fma_f32 v164, |v164|, v224, v164 mul:4\n\t" \
  "v_fma_f32 v167, |v167|, v227, v167 mul:4\n\t" \
  "v_fma_f32 v166, |v166|, v226, v166 mul:4\n\t" \
  "v_fma_f32 v173, |v173|, v229, v173 mul:4\n\t" \
  "v_fma_f32 v172, |v172|, v228, v172 mul:4\n\t" \
  "v_mfma_f32_16x16x32_bf16 v[204:207], v[212:215], v[140:143], v[204:207]\n\t" \
  "ds_read_b64_tr_b16 v[212:213], v249 offset:24608\n\t" \
  "ds_read_b64_tr_b16 v[214:215], v249 offset:26144\n\t" \
  "v_fma_f32 v175, |v175|, v231, v175 mul:4\n\t" \
  "v_fma_f32 v174, |v174|, v230, v174 mul:4\n\t" \
  "v_cvt_pk_bf16_f32 v164, v164, v165\n\t" \
  "v_cvt_pk_bf16_f32 v165, v166, v167\n\t" \
  "v_cvt_pk_bf16_f32 v172, v172, v173\n\t" \
  "v_cvt_pk_bf16_f32 v173, v174, v175\n\t" \
  "s_setprio 0\n\t" \
  "v_exp_f16 v232, v233\n\t" \
  "v_cvt_pk_f16_f32 v224, |v194|, |v195| clamp\n\t" \
  "v_mfma_f32_16x16x32_bf16 v[0:3], v[242:245], v[236:239], 0\n\t" \
  "ds_read_b32 v219, v219 offset:0\n\t" \
  "v_cvt_pk_f16_f32 v234, v192, v193\n\t" \
  "v_cvt_pk_f16_f32 v226, |v202|, |v203| clamp\n\t" \
  "v_mfma_f32_16x16x32_bf16 v[4:7], v[240:243], v[236:239], 0\n\t" \
  "buffer_load_dwordx4 v[236:239], v250, s[8:11], s28 offen offset:64\n\t" \
  "s_cmp_lg_u32 s58, 0\n\t" \
  "s_cbranch_scc1 .Lcnx_c24\n\t" \
  "s_sub_u32 s24, s24, s64\n\t" \
  "s_add_u32 m0, s25, 0\n\t" \
  "s_add_u32 s24, s24, s26\n\t" \
  "buffer_load_dwordx4 v254, s[8:11], s24 offen lds\n\t" \
  "s_cmp_lg_u32 s38, 6\n\t" \
  "s_cbranch_scc1 .Lcnx_c24\n\t" \
  "s_mov_b32 m0, 163632\n\t" \
  "s_add_u32 s30, s30, s31\n\t" \
  "buffer_load_dword v252, s[8:11], s30 offen lds\n\t" \
  ".Lcnx_c24:\n\t" \
  "ds_read_b32 v218, v218 offset:0\n\t" \
  "v_cvt_pk_f16_f32 v228, |v200|, |v201| clamp\n\t" \
  "v_exp_f16_sdwa v232, v233 dst_sel:WORD_1 dst_unused:UNUSED_PRESERVE src0_sel:WORD_1\n\t" \
  "v_pk_mul_f16 v235, v234, v234\n\t" \
  "s_waitcnt lgkmcnt(6)\n\t" \
  "v_mfma_f32_16x16x32_bf16 v[0:3], v[208:211], v[146:149], v[0:3]\n\t" \
  "ds_read_b32 v221, v221 offset:0\n\t" \
  "v_and_b32_sdwa v225, s56, v224 dst_sel:DWORD dst_unused:UNUSED_PAD src0_sel:DWORD src1_sel:WORD_1\n\t" \
  "v_and_b32 v224, s56, v224\n\t" \
  "v_mfma_f32_16x16x32_bf16 v[24:27], v[208:211], v[154:157], 0\n\t" \
  "ds_read_b32 v220, v220 offset:0\n\t" \
  "v_and_b32_sdwa v227, s56, v226 dst_sel:DWORD dst_unused:UNUSED_PAD src0_sel:DWORD src1_sel:WORD_1\n\t" \
  "v_mfma_f32_16x16x32_bf16 v[48:51], v[208:211], v[162:165], 0\n\t" \
  "ds_read_b32 v225, v225 offset:0\n\t" \
  "v_and_b32 v226, s56, v226\n\t" \
  "v_and_b32_sdwa v229, s56, v228 dst_sel:DWORD dst_unused:UNUSED_PAD src0_sel:DWORD src1_sel:WORD_1\n\t" \
  "v_mfma_f32_16x16x32_bf16 v[72:75], v[208:211], v[170:173], 0\n\t" \
  "ds_read_b64_tr_b16 v[208:209], v249 offset:24664\n\t" \
  "v_and_b32 v228, s56, v228\n\t" \
  "s_waitcnt lgkmcnt(6)\n\t" \
  "v_mfma_f32_16x16x32_bf16 v[4:7], v[212:215], v[146:149], v[4:7]\n\t" \
  "ds_read_b64_tr_b16 v[210:211], v249 offset:26200\n\t" \
  "v_pk_fma_f16 v235, v235, v253, s54\n\t" \
  "v_mfma_f32_16x16x32_bf16 v[28:31], v[212:215], v[154:157], 0\n\t" \
  "ds_read_b32 v224, v224 offset:0\n\t" \
  "v_pk_mul_f16 v235, v234, v235\n\t" \
  "s_waitcnt lgkmcnt(7)\n\t" \
  "v_fma_f32 v179, |v179|, v217, v179 mul:4\n\t" \
  "v_fma_f32 v178, |v178|, v216, v178 mul:4\n\t" \
  "v_mfma_f32_16x16x32_bf16 v[52:55], v[212:215], v[162:165], 0\n\t" \
  "ds_read_b32 v227, v227 offset:0\n\t" \
  "v_fma_f32 v187, |v187|, v219, v187 mul:4\n\t" \
  "s_waitcnt lgkmcnt(5)\n\t" \
  "v_fma_f32 v186, |v186|, v218, v186 mul:4\n\t" \
  "v_fma_f32 v185, |v185|, v221, v185 mul:4\n\t" \
  "v_mfma_f32_16x16x32_bf16 v[76:79], v[212:215], v[170:173], 0\n\t" \
  "ds_read_b64_tr_b16 v[212:213], v249 offset:24672\n\t" \
  "v_fma_f32 v184, |v184|, v220, v184 mul:4\n\t" \
  "v_pk_fma_f16 v232, v232, s55, s55\n\t" \
  "s_waitcnt lgkmcnt(3)\n\t" \
  "v_mfma_f32_16x16x32_bf16 v[8:11], v[208:211], v[146:149], 0\n\t" \
  "ds_read_b64_tr_b16 v[214:215], v249 offset:26208\n\t" \
  "v_cvt_pk_bf16_f32 v179, v178, v179\n\t" \
  "v_cvt_pk_bf16_f32 v187, v186, v187\n\t" \
  "v_cvt_pk_bf16_f32 v186, v184, v185\n\t" \
  "v_mfma_f32_16x16x32_bf16 v[32:35], v[208:211], v[154:157], 0\n\t" \
  "ds_read_b32 v226, v226 offset:0\n\t" \
  "v_rcp_f16 v233, v232\n\t" \
  "v_exp_f16 v234, v235\n\t" \
  "v_cvt_pk_f16_f32 v216, |v180|, |v181| clamp\n\t" \
  "v_mfma_f32_16x16x32_bf16 v[56:59], v[208:211], v[162:165], 0\n\t" \
  "s_setprio 1\n\t" \
  "ds_read_b32 v229, v229 offset:0\n\t" \
  "v_cvt_pk_f16_f32 v218, |v182|, |v183| clamp\n\t" \
  "v_cvt_pk_f16_f32 v220, |v188|, |v189| clamp\n\t" \
  "v_mfma_f32_16x16x32_bf16 v[80:83], v[208:211], v[170:173], 0\n\t" \
  "ds_read_b64_tr_b16 v[208:209], v249 offset:24728\n\t" \
  "v_cvt_pk_f16_f32 v222, |v190|, |v191| clamp\n\t" \
  "v_rcp_f16_sdwa v232, v232 dst_sel:DWORD dst_unused:UNUSED_PAD src0_sel:WORD_1\n\t" \
  "v_exp_f16_sdwa v234, v235 dst_sel:WORD_1 dst_unused:UNUSED_PRESERVE src0_sel:WORD_1\n\t" \
  "s_waitcnt lgkmcnt(3)\n\t" \
  "v_mfma_f32_16x16x32_bf16 v[12:15], v[212:215], v[146:149], 0\n\t" \
  "ds_read_b64_tr_b16 v[210:211], v249 offset:26264\n\t" \
  "v_and_b32_sdwa v217, s56, v216 dst_sel:DWORD dst_unused:UNUSED_PAD src0_sel:DWORD src1_sel:WORD_1\n\t" \
  "v_and_b32 v216, s56, v216\n\t" \
  "v_mfma_f32_16x16x32_bf16 v[36:39], v[212:215], v[154:157], 0\n\t" \
  "ds_read_b32 v228, v228 offset:0\n\t" \
  "v_and_b32_sdwa v219, s56, v218 dst_sel:DWORD dst_unused:UNUSED_PAD src0_sel:DWORD src1_sel:WORD_1\n\t" \
  "v_mfma_f32_16x16x32_bf16 v[60:63], v[212:215], v[162:165], 0\n\t" \
  "ds_read_b32 v217, v217 offset:0\n\t" \
  "v_and_b32 v218, s56, v218\n\t" \
  "v_and_b32_sdwa v221, s56, v220 dst_sel:DWORD dst_unused:UNUSED_PAD src0_sel:DWORD src1_sel:WORD_1\n\t" \
  "v_mfma_f32_16x16x32_bf16 v[84:87], v[212:215], v[170:173], 0\n\t" \
  "ds_read_b64_tr_b16 v[212:213], v249 offset:24736\n\t" \
  "v_and_b32 v220, s56, v220\n\t" \
  "s_waitcnt lgkmcnt(3)\n\t" \
  "v_mfma_f32_16x16x32_bf16 v[16:19], v[208:211], v[146:149], 0\n\t" \
  "ds_read_b64_tr_b16 v[214:215], v249 offset:26272\n\t" \
  "v_and_b32_sdwa v223, s56, v222 dst_sel:DWORD dst_unused:UNUSED_PAD src0_sel:DWORD src1_sel:WORD_1\n\t" \
  "v_mfma_f32_16x16x32_bf16 v[40:43], v[208:211], v[154:157], 0\n\t" \
  "ds_read_b32 v216, v216 offset:0\n\t" \
  "v_and_b32 v222, s56, v222\n\t" \
  "v_fma_f32 v195, |v195|, v225, v195 mul:4\n\t" \
  "v_mfma_f32_16x16x32_bf16 v[64:67], v[208:211], v[162:165], 0\n\t" \
  "ds_read_b32 v219, v219 offset:0\n\t" \
  "v_fma_f32 v194, |v194|, v224, v194 mul:4\n\t" \
  "v_fma_f32 v203, |v203|, v227, v203 mul:4\n\t" \
  "v_mfma_f32_16x16x32_bf16 v[88:91], v[208:211], v[170:173], 0\n\t" \
  "ds_read_b64_tr_b16 v[208:209], v248 offset:36904\n\t" \
  "ds_read_b64_tr_b16 v[210:211], v248 offset:37928\n\t" \
  "s_waitcnt lgkmcnt(7)\n\t" \
  "v_fma_f32 v202, |v202|, v226, v202 mul:4\n\t" \
  "v_fma_f32 v201, |v201|, v229, v201 mul:4\n\t" \
  "s_waitcnt lgkmcnt(4)\n\t" \
  "v_mfma_f32_16x16x32_bf16 v[20:23], v[212:215], v[146:149], 0\n\t" \
  "ds_read_b32 v218, v218 offset:0\n\t" \
  "v_fma_f32 v200, |v200|, v228, v200 mul:4\n\t" \
  "v_fma_mix_f32 v176, v176, v233, 0 op_sel_hi:[0,1,0]\n\t" \
  "v_fma_mix_f32 v177, v177, v232, 0 op_sel_hi:[0,1,0]\n\t" \
  "v_mfma_f32_16x16x32_bf16 v[44:47], v[212:215], v[154:157], 0\n\t" \
  "ds_read_b32 v221, v221 offset:0\n\t" \
  "v_pk_fma_f16 v234, v234, s55, s55\n\t" \
  "v_cvt_pk_bf16_f32 v178, v176, v177\n\t" \
  "v_cvt_pk_bf16_f32 v195, v194, v195\n\t" \
  "v_mfma_f32_16x16x32_bf16 v[68:71], v[212:215], v[162:165], 0\n\t" \
  "ds_read_b32 v220, v220 offset:0\n\t" \
  "v_cvt_pk_bf16_f32 v203, v202, v203\n\t" \
  "v_cvt_pk_bf16_f32 v202, v200, v201\n\t" \
  "v_rcp_f16 v235, v234\n\t" \
  "v_mfma_f32_16x16x32_bf16 v[92:95], v[212:215], v[170:173], 0\n\t" \
  "ds_read_b64_tr_b16 v[212:213], v248 offset:39976\n\t" \
  "s_setprio 2\n\t" \
  "v_cvt_pk_f16_f32 v224, |v196|, |v197| clamp\n\t" \
  "v_cvt_pk_f16_f32 v226, |v198|, |v199| clamp\n\t" \
  "s_waitcnt lgkmcnt(4)\n\t" \
  "v_mfma_f32_16x16x32_bf16 v[144:147], v[208:211], v[96:99], 0\n\t" \
  "ds_read_b64_tr_b16 v[214:215], v248 offset:41000\n\t" \
  "v_cvt_pk_f16_f32 v228, |v204|, |v205| clamp\n\t" \
  "v_cvt_pk_f16_f32 v230, |v206|, |v207| clamp\n\t" \
  "v_rcp_f16_sdwa v234, v234 dst_sel:DWORD dst_unused:UNUSED_PAD src0_sel:WORD_1\n\t" \
  "v_mfma_f32_16x16x32_bf16 v[152:155], v[208:211], v[108:111], 0\n\t" \
  "ds_read_b32 v223, v223 offset:0\n\t" \
  "v_and_b32_sdwa v225, s56, v224 dst_sel:DWORD dst_unused:UNUSED_PAD src0_sel:DWORD src1_sel:WORD_1\n\t" \
  "v_and_b32 v224, s56, v224\n\t" \
  "v_mfma_f32_16x16x32_bf16 v[160:163], v[208:211], v[120:123], 0\n\t" \
  "ds_read_b32 v222, v222 offset:0\n\t" \
  "v_and_b32_sdwa v227, s56, v226 dst_sel:DWORD dst_unused:UNUSED_PAD src0_sel:DWORD src1_sel:WORD_1\n\t" \
  "v_mfma_f32_16x16x32_bf16 v[168:171], v[208:211], v[132:135], 0\n\t" \
  "ds_read_b64_tr_b16 v[208:209], v248 offset:38952\n\t" \
  "v_and_b32 v226, s56, v226\n\t" \
  "v_and_b32_sdwa v229, s56, v228 dst_sel:DWORD dst_unused:UNUSED_PAD src0_sel:DWORD src1_sel:WORD_1\n\t" \
  "s_waitcnt lgkmcnt(3)\n\t" \
  "v_mfma_f32_16x16x32_bf16 v[144:147], v[212:215], v[100:103], v[144:147]\n\t" \
  "ds_read_b64_tr_b16 v[210:211], v248 offset:42024\n\t" \
  "v_and_b32 v228, s56, v228\n\t" \
  "v_mfma_f32_16x16x32_bf16 v[152:155], v[212:215], v[112:115], v[152:155]\n\t" \
  "ds_read_b32 v225, v225 offset:0\n\t" \
  "v_and_b32_sdwa v231, s56, v230 dst_sel:DWORD dst_unused:UNUSED_PAD src0_sel:DWORD src1_sel:WORD_1\n\t" \
  "v_mfma_f32_16x16x32_bf16 v[160:163], v[212:215], v[124:127], v[160:163]\n\t" \
  "ds_read_b32 v224, v224 offset:0\n\t" \
  "v_and_b32 v230, s56, v230\n\t" \
  "v_fma_f32 v181, |v181|, v217, v181 mul:4\n\t" \
  "v_mfma_f32_16x16x32_bf16 v[168:171], v[212:215], v[136:139], v[168:171]\n\t" \
  "ds_read_b64_tr_b16 v[212:213], v248 offset:37416\n\t" \
  "ds_read_b64_tr_b16 v[214:215], v248 offset:38440\n\t" \
  "v_fma_f32 v180, |v180|, v216, v180 mul:4\n\t" \
  "v_fma_f32 v183, |v183|, v219, v183 mul:4\n\t" \
  "s_waitcnt lgkmcnt(4)\n\t" \
  "v_mfma_f32_16x16x32_bf16 v[144:147], v[208:211], v[104:107], v[144:147]\n\t" \
  "ds_read_b32 v227, v227 offset:0\n\t" \
  "ds_read_b32 v226, v226 offset:0\n\t" \
  "v_fma_f32 v182, |v182|, v218, v182 mul:4\n\t" \
  "v_fma_f32 v189, |v189|, v221, v189 mul:4\n\t" \
  "v_mfma_f32_16x16x32_bf16 v[152:155], v[208:211], v[116:119], v[152:155]\n\t" \
  "ds_read_b32 v229, v229 offset:0\n\t" \
  "v_fma_f32 v188, |v188|, v220, v188 mul:4\n\t" \
  "v_fma_f32 v191, |v191|, v223, v191 mul:4\n\t" \
  "v_fma_f32 v190, |v190|, v222, v190 mul:4\n\t" \
  "v_mfma_f32_16x16x32_bf16 v[160:163], v[208:211], v[128:131], v[160:163]\n\t" \
  "ds_read_b32 v228, v228 offset:0\n\t" \
  "v_fma_mix_f32 v192, v192, v235, 0 op_sel_hi:[0,1,0]\n\t" \
  "v_fma_mix_f32 v193, v193, v234, 0 op_sel_hi:[0,1,0]\n\t" \
  "v_cvt_pk_bf16_f32 v194, v192, v193\n\t" \
  "v_mfma_f32_16x16x32_bf16 v[168:171], v[208:211], v[140:143], v[168:171]\n\t" \
  "ds_read_b64_tr_b16 v[208:209], v248 offset:40488\n\t" \
  "v_cvt_pk_bf16_f32 v180, v180, v181\n\t" \
  "v_cvt_pk_bf16_f32 v181, v182, v183\n\t" \
  "s_waitcnt lgkmcnt(5)\n\t" \
  "v_mfma_f32_16x16x32_bf16 v[148:151], v[212:215], v[96:99], 0\n\t" \
  "ds_read_b64_tr_b16 v[210:211], v248 offset:41512\n\t" \
  "v_cvt_pk_bf16_f32 v188, v188, v189\n\t" \
  "s_setprio 3\n\t" \
  "v_cvt_pk_bf16_f32 v189, v190, v191\n\t" \
  "v_cvt_pk_f16_f32 v216, |v146|, |v147| clamp\n\t" \
  "v_mfma_f32_16x16x32_bf16 v[156:159], v[212:215], v[108:111], 0\n\t" \
  "ds_read_b32 v231, v231 offset:0\n\t" \
  "v_cvt_pk_f16_f32 v232, v144, v145\n\t" \
  "v_cvt_pk_f16_f32 v218, |v154|, |v155| clamp\n\t" \
  "v_cvt_pk_f16_f32 v220, |v152|, |v153| clamp\n\t" \
  "v_mfma_f32_16x16x32_bf16 v[164:167], v[212:215], v[120:123], 0\n\t" \
  "ds_read_b32 v230, v230 offset:0\n\t" \
  "v_pk_mul_f16 v233, v232, v232\n\t" \
  "v_and_b32_sdwa v217, s56, v216 dst_sel:DWORD dst_unused:UNUSED_PAD src0_sel:DWORD src1_sel:WORD_1\n\t" \
  "v_mfma_f32_16x16x32_bf16 v[172:175], v[212:215], v[132:135], 0\n\t" \
  "ds_read_b64_tr_b16 v[212:213], v248 offset:39464\n\t" \
  "v_and_b32 v216, s56, v216\n\t" \
  "v_and_b32_sdwa v219, s56, v218 dst_sel:DWORD dst_unused:UNUSED_PAD src0_sel:DWORD src1_sel:WORD_1\n\t" \
  "s_waitcnt lgkmcnt(3)\n\t" \
  "v_mfma_f32_16x16x32_bf16 v[148:151], v[208:211], v[100:103], v[148:151]\n\t" \
  "ds_read_b64_tr_b16 v[214:215], v248 offset:42536\n\t" \
  "v_and_b32 v218, s56, v218\n\t" \
  "v_mfma_f32_16x16x32_bf16 v[156:159], v[208:211], v[112:115], v[156:159]\n\t" \
  "s_waitcnt vmcnt(1)\n\t" \
  "s_cmp_lg_u32 s58, 1\n\t" \
  "s_cbranch_scc1 .Lcnx_c12\n\t" \
  "s_waitcnt vmcnt(0)\n\t" \
  ".Lcnx_c12:\n\t" \
  "s_mov_b64 exec, 3\n\t" \
  "ds_add_rtn_u32 v246, v251, v250 offset:47672\n\t" \
  "s_mov_b64 exec, -1\n\t" \
  "ds_read_b32 v217, v217 offset:0\n\t" \
  "v_and_b32_sdwa v221, s56, v220 dst_sel:DWORD dst_unused:UNUSED_PAD src0_sel:DWORD src1_sel:WORD_1\n\t" \
  "v_mfma_f32_16x16x32_bf16 v[164:167], v[208:211], v[124:127], v[164:167]\n\t" \
  "ds_read_b32 v216, v216 offset:0\n\t" \
  "v_and_b32 v220, s56, v220\n\t" \
  "v_pk_fma_f16 v233, v233, v253, s54\n\t" \
  "v_mfma_f32_16x16x32_bf16 v[172:175], v[208:211], v[136:139], v[172:175]\n\t" \
  "s_waitcnt lgkmcnt(2)\n\t" \
  "v_readfirstlane_b32 s4, v246\n\t" \
  "s_cmp_lt_u32 s4, s57\n\t" \
  "s_cbranch_scc1 .Lcnx_w24\n.Lcnx_b24:\n\t" \
  "ds_read_b64_tr_b16 v[208:209], v249 offset:30752\n\t" \
  "v_pk_mul_f16 v233, v232, v233\n\t" \
  "v_fma_f32 v197, |v197|, v225, v197 mul:4\n\t" \
  "v_mfma_f32_16x16x32_bf16 v[148:151], v[212:215], v[104:107], v[148:151]\n\t" \
  "ds_read_b64_tr_b16 v[210:211], v249 offset:32288\n\t" \
  "v_fma_f32 v196, |v196|, v224, v196 mul:4\n\t" \
  "v_fma_f32 v199, |v199|, v227, v199 mul:4\n\t" \
  "v_mfma_f32_16x16x32_bf16 v[156:159], v[212:215], v[116:119], v[156:159]\n\t" \
  "s_cmp_eq_u32 s37, 0\n\t" \
  "s_cbranch_scc1 .Lcnx_f3\n.Lcnx_g3:\n\t" \
  "ds_read_b32 v219, v219 offset:0\n\t" \
  "v_fma_f32 v198, |v198|, v226, v198 mul:4\n\t" \
  "v_fma_f32 v205, |v205|, v229, v205 mul:4\n\t" \
  "v_fma_f32 v204, |v204|, v228, v204 mul:4\n\t" \
  "v_mfma_f32_16x16x32_bf16 v[164:167], v[212:215], v[128:131], v[164:167]\n\t" \
  "ds_read_b32 v218, v218 offset:0\n\t" \
  "v_fma_f32 v207, |v207|, v231, v207 mul:4\n\t" \
  "v_fma_f32 v206, |v206|, v230, v206 mul:4\n\t" \
  "v_cvt_pk_bf16_f32 v196, v196, v197\n\t" \
  "v_mfma_f32_16x16x32_bf16 v[172:175], v[212:215], v[140:143], v[172:175]\n\t" \
  "ds_read_b64_tr_b16 v[212:213], v249 offset:30760\n\t" \
  "v_cvt_pk_bf16_f32 v197, v198, v199\n\t" \
  "v_cvt_pk_bf16_f32 v204, v204, v205\n\t" \
  "v_cvt_pk_bf16_f32 v205, v206, v207\n\t" \
  "s_setprio 0\n\t" \
  "v_exp_f16 v232, v233\n\t" \
  "v_cvt_pk_f16_f32 v224, |v162|, |v163| clamp\n\t" \
  "v_mfma_f32_16x16x32_bf16 v[8:11], v[242:245], v[236:239], v[8:11]\n\t" \
  "ds_read_b64_tr_b16 v[214:215], v249 offset:32296\n\t" \
  "v_cvt_pk_f16_f32 v234, v160, v161\n\t" \
  "v_cvt_pk_f16_f32 v226, |v170|, |v171| clamp\n\t" \
  "v_mfma_f32_16x16x32_bf16 v[12:15], v[240:243], v[236:239], v[12:15]\n\t" \
  "buffer_load_dwordx4 v[236:239], v250, s[8:11], s28 offen offset:128\n\t" \
  "s_cmp_lg_u32 s58, 0\n\t" \
  "s_cbranch_scc1 .Lcnx_c25\n\t" \
  "s_add_u32 m0, s25, 8200\n\t" \
  "s_add_u32 s24, s24, s26\n\t" \
  "buffer_load_dwordx4 v254, s[8:11], s24 offen lds\n\t" \
  "s_cmp_lg_u32 s38, 7\n\t" \
  "s_cbranch_scc1 .Lcnx_c25\n\t" \
  "s_mov_b32 m0, 163632\n\t" \
  "s_add_u32 s30, s30, s31\n\t" \
  "buffer_load_dword v252, s[8:11], s30 offen lds\n\t" \
  ".Lcnx_c25:\n\t" \
  "ds_read_b32 v221, v221 offset:0\n\t" \
  "v_cvt_pk_f16_f32 v228, |v168|, |v169| clamp\n\t" \
  "v_exp_f16_sdwa v232, v233 dst_sel:WORD_1 dst_unused:UNUSED_PRESERVE src0_sel:WORD_1\n\t" \
  "v_pk_mul_f16 v235, v234, v234\n\t" \
  "s_waitcnt lgkmcnt(5)\n\t" \
  "v_mfma_f32_16x16x32_bf16 v[0:3], v[208:211], v[178:181], v[0:3]\n\t" \
  "ds_read_b32 v220, v220 offset:0\n\t" \
  "v_and_b32_sdwa v225, s56, v224 dst_sel:DWORD dst_unused:UNUSED_PAD src0_sel:DWORD src1_sel:WORD_1\n\t" \
  "v_and_b32 v224, s56, v224\n\t" \
  "v_mfma_f32_16x16x32_bf16 v[24:27], v[208:211], v[186:189], v[24:27]\n\t" \
  "ds_read_b32 v225, v225 offset:0\n\t" \
  "v_and_b32_sdwa v227, s56, v226 dst_sel:DWORD dst_unused:UNUSED_PAD src0_sel:DWORD src1_sel:WORD_1\n\t" \
  "v_mfma_f32_16x16x32_bf16 v[48:51], v[208:211], v[194:197], v[48:51]\n\t" \
  "ds_read_b32 v224, v224 offset:0\n\t" \
  "v_and_b32 v226, s56, v226\n\t" \
  "v_and_b32_sdwa v229, s56, v228 dst_sel:DWORD dst_unused:UNUSED_PAD src0_sel:DWORD src1_sel:WORD_1\n\t" \
  "v_mfma_f32_16x16x32_bf16 v[72:75], v[208:211], v[202:205], v[72:75]\n\t" \
  "ds_read_b64_tr_b16 v[208:209], v249 offset:30816\n\t" \
  "v_and_b32 v228, s56, v228\n\t" \
  "s_waitcnt lgkmcnt(5)\n\t" \
  "v_mfma_f32_16x16x32_bf16 v[4:7], v[212:215], v[178:181], v[4:7]\n\t" \
  "ds_read_b64_tr_b16 v[210:211], v249 offset:32352\n\t" \
  "v_pk_fma_f16 v235, v235, v253, s54\n\t" \
  "v_mfma_f32_16x16x32_bf16 v[28:31], v[212:215], v[186:189], v[28:31]\n\t" \
  "ds_read_b32 v227, v227 offset:0\n\t" \
  "v_pk_mul_f16 v235, v234, v235\n\t" \
  "v_fma_f32 v147, |v147|, v217, v147 mul:4\n\t" \
  "v_fma_f32 v146, |v146|, v216, v146 mul:4\n\t" \
  "v_mfma_f32_16x16x32_bf16 v[52:55], v[212:215], v[194:197], v[52:55]\n\t" \
  "ds_read_b32 v226, v226 offset:0\n\t" \
  "v_fma_f32 v155, |v155|, v219, v155 mul:4\n\t" \
  "s_waitcnt lgkmcnt(6)\n\t" \
  "v_fma_f32 v154, |v154|, v218, v154 mul:4\n\t" \
  "v_fma_f32 v153, |v153|, v221, v153 mul:4\n\t" \
  "v_mfma_f32_16x16x32_bf16 v[76:79], v[212:215], v[202:205], v[76:79]\n\t" \
  "ds_read_b64_tr_b16 v[212:213], v249 offset:30824\n\t" \
  "v_fma_f32 v152, |v152|, v220, v152 mul:4\n\t" \
  "v_pk_fma_f16 v232, v232, s55, s55\n\t" \
  "s_waitcnt lgkmcnt(3)\n\t" \
  "v_mfma_f32_16x16x32_bf16 v[8:11], v[208:211], v[178:181], v[8:11]\n\t" \
  "ds_read_b64_tr_b16 v[214:215], v249 offset:32360\n\t" \
  "v_cvt_pk_bf16_f32 v147, v146, v147\n\t" \
  "v_cvt_pk_bf16_f32 v155, v154, v155\n\t" \
  "v_cvt_pk_bf16_f32 v154, v152, v153\n\t" \
  "v_mfma_f32_16x16x32_bf16 v[32:35], v[208:211], v[186:189], v[32:35]\n\t" \
  "ds_read_b32 v229, v229 offset:0\n\t" \
  "v_rcp_f16 v233, v232\n\t" \
  "v_exp_f16 v234, v235\n\t" \
  "v_cvt_pk_f16_f32 v216, |v148|, |v149| clamp\n\t" \
  "v_mfma_f32_16x16x32_bf16 v[56:59], v[208:211], v[194:197], v[56:59]\n\t" \
  "ds_read_b32 v228, v228 offset:0\n\t" \
  "s_setprio 1\n\t" \
  "v_cvt_pk_f16_f32 v218, |v150|, |v151| clamp\n\t" \
  "v_cvt_pk_f16_f32 v220, |v156|, |v157| clamp\n\t" \
  "v_mfma_f32_16x16x32_bf16 v[80:83], v[208:211], v[202:205], v[80:83]\n\t" \
  "ds_read_b64_tr_b16 v[208:209], v249 offset:30880\n\t" \
  "v_cvt_pk_f16_f32 v222, |v158|, |v159| clamp\n\t" \
  "v_rcp_f16_sdwa v232, v232 dst_sel:DWORD dst_unused:UNUSED_PAD src0_sel:WORD_1\n\t" \
  "v_exp_f16_sdwa v234, v235 dst_sel:WORD_1 dst_unused:UNUSED_PRESERVE src0_sel:WORD_1\n\t" \
  "s_waitcnt lgkmcnt(3)\n\t" \
  "v_mfma_f32_16x16x32_bf16 v[12:15], v[212:215], v[178:181], v[12:15]\n\t" \
  "ds_read_b64_tr_b16 v[210:211], v249 offset:32416\n\t" \
  "v_and_b32_sdwa v217, s56, v216 dst_sel:DWORD dst_unused:UNUSED_PAD src0_sel:DWORD src1_sel:WORD_1\n\t" \
  "v_and_b32 v216, s56, v216\n\t" \
  "v_mfma_f32_16x16x32_bf16 v[36:39], v[212:215], v[186:189], v[36:39]\n\t" \
  "ds_read_b32 v217, v217 offset:0\n\t" \
  "v_and_b32_sdwa v219, s56, v218 dst_sel:DWORD dst_unused:UNUSED_PAD src0_sel:DWORD src1_sel:WORD_1\n\t" \
  "v_mfma_f32_16x16x32_bf16 v[60:63], v[212:215], v[194:197], v[60:63]\n\t" \
  "ds_read_b32 v216, v216 offset:0\n\t" \
  "v_and_b32 v218, s56, v218\n\t" \
  "v_and_b32_sdwa v221, s56, v220 dst_sel:DWORD dst_unused:UNUSED_PAD src0_sel:DWORD src1_sel:WORD_1\n\t" \
  "v_mfma_f32_16x16x32_bf16 v[84:87], v[212:215], v[202:205], v[84:87]\n\t" \
  "ds_read_b64_tr_b16 v[212:213], v249 offset:30888\n\t" \
  "v_and_b32 v220, s56, v220\n\t" \
  "s_waitcnt lgkmcnt(3)\n\t" \
  "v_mfma_f32_16x16x32_bf16 v[16:19], v[208:211], v[178:181], v[16:19]\n\t" \
  "ds_read_b64_tr_b16 v[214:215], v249 offset:32424\n\t" \
  "v_and_b32_sdwa v223, s56, v222 dst_sel:DWORD dst_unused:UNUSED_PAD src0_sel:DWORD src1_sel:WORD_1\n\t" \
  "v_mfma_f32_16x16x32_bf16 v[40:43], v[208:211], v[186:189], v[40:43]\n\t" \
  "ds_read_b32 v219, v219 offset:0\n\t" \
  "v_and_b32 v222, s56, v222\n\t" \
  "v_fma_f32 v163, |v163|, v225, v163 mul:4\n\t" \
  "v_mfma_f32_16x16x32_bf16 v[64:67], v[208:211], v[194:197], v[64:67]\n\t" \
  "ds_read_b32 v218, v218 offset:0\n\t" \
  "v_fma_f32 v162, |v162|, v224, v162 mul:4\n\t" \
  "v_fma_f32 v171, |v171|, v227, v171 mul:4\n\t" \
  "v_mfma_f32_16x16x32_bf16 v[88:91], v[208:211], v[202:205], v[88:91]\n\t" \
  "ds_read_b64_tr_b16 v[208:209], v248 offset:12304\n\t" \
  "ds_read_b64_tr_b16 v[210:211], v248 offset:13328\n\t" \
  "v_fma_f32 v170, |v170|, v226, v170 mul:4\n\t" \
  "v_fma_f32 v169, |v169|, v229, v169 mul:4\n\t" \
  "s_waitcnt lgkmcnt(4)\n\t" \
  "v_mfma_f32_16x16x32_bf16 v[20:23], v[212:215], v[178:181], v[20:23]\n\t" \
  "ds_read_b32 v221, v221 offset:0\n\t" \
  "v_fma_f32 v168, |v168|, v228, v168 mul:4\n\t" \
  "v_fma_mix_f32 v144, v144, v233, 0 op_sel_hi:[0,1,0]\n\t" \
  "v_fma_mix_f32 v145, v145, v232, 0 op_sel_hi:[0,1,0]\n\t" \
  "v_mfma_f32_16x16x32_bf16 v[44:47], v[212:215], v[186:189], v[44:47]\n\t" \
  "ds_read_b32 v220, v220 offset:0\n\t" \
  "v_pk_fma_f16 v234, v234, s55, s55\n\t" \
  "v_cvt_pk_bf16_f32 v146, v144, v145\n\t" \
  "v_cvt_pk_bf16_f32 v163, v162, v163\n\t" \
  "v_mfma_f32_16x16x32_bf16 v[68:71], v[212:215], v[194:197], v[68:71]\n\t" \
  "ds_read_b32 v223, v223 offset:0\n\t" \
  "v_cvt_pk_bf16_f32 v171, v170, v171\n\t" \
  "v_cvt_pk_bf16_f32 v170, v168, v169\n\t" \
  "v_rcp_f16 v235, v234\n\t" \
  "v_mfma_f32_16x16x32_bf16 v[92:95], v[212:215], v[202:205], v[92:95]\n\t" \
  "ds_read_b64_tr_b16 v[212:213], v248 offset:15376\n\t" \
  "v_cvt_pk_f16_f32 v224, |v164|, |v165| clamp\n\t" \
  "s_setprio 2\n\t" \
  "v_cvt_pk_f16_f32 v226, |v166|, |v167| clamp\n\t" \
  "s_waitcnt lgkmcnt(4)\n\t" \
  "v_mfma_f32_16x16x32_bf16 v[176:179], v[208:211], v[96:99], 0\n\t" \
  "ds_read_b64_tr_b16 v[214:215], v248 offset:16400\n\t" \
  "v_cvt_pk_f16_f32 v228, |v172|, |v173| clamp\n\t" \
  "v_cvt_pk_f16_f32 v230, |v174|, |v175| clamp\n\t" \
  "v_rcp_f16_sdwa v234, v234 dst_sel:DWORD dst_unused:UNUSED_PAD src0_sel:WORD_1\n\t" \
  "v_mfma_f32_16x16x32_bf16 v[184:187], v[208:211], v[108:111], 0\n\t" \
  "ds_read_b32 v222, v222 offset:0\n\t" \
  "v_and_b32_sdwa v225, s56, v224 dst_sel:DWORD dst_unused:UNUSED_PAD src0_sel:DWORD src1_sel:WORD_1\n\t" \
  "v_and_b32 v224, s56, v224\n\t" \
  "v_mfma_f32_16x16x32_bf16 v[192:195], v[208:211], v[120:123], 0\n\t" \
  "ds_read_b32 v225, v225 offset:0\n\t" \
  "v_and_b32_sdwa v227, s56, v226 dst_sel:DWORD dst_unused:UNUSED_PAD src0_sel:DWORD src1_sel:WORD_1\n\t" \
  "v_mfma_f32_16x16x32_bf16 v[200:203], v[208:211], v[132:135], 0\n\t" \
  "ds_read_b64_tr_b16 v[208:209], v248 offset:14352\n\t" \
  "v_and_b32 v226, s56, v226\n\t" \
  "v_and_b32_sdwa v229, s56, v228 dst_sel:DWORD dst_unused:UNUSED_PAD src0_sel:DWORD src1_sel:WORD_1\n\t" \
  "s_waitcnt lgkmcnt(3)\n\t" \
  "v_mfma_f32_16x16x32_bf16 v[176:179], v[212:215], v[100:103], v[176:179]\n\t" \
  "ds_read_b64_tr_b16 v[210:211], v248 offset:17424\n\t" \
  "v_and_b32 v228, s56, v228\n\t" \
  "v_mfma_f32_16x16x32_bf16 v[184:187], v[212:215], v[112:115], v[184:187]\n\t" \
  "ds_read_b32 v224, v224 offset:0\n\t" \
  "v_and_b32_sdwa v231, s56, v230 dst_sel:DWORD dst_unused:UNUSED_PAD src0_sel:DWORD src1_sel:WORD_1\n\t" \
  "v_mfma_f32_16x16x32_bf16 v[192:195], v[212:215], v[124:127], v[192:195]\n\t" \
  "ds_read_b32 v227, v227 offset:0\n\t" \
  "v_and_b32 v230, s56, v230\n\t" \
  "v_fma_f32 v149, |v149|, v217, v149 mul:4\n\t" \
  "v_mfma_f32_16x16x32_bf16 v[200:203], v[212:215], v[136:139], v[200:203]\n\t" \
  "ds_read_b64_tr_b16 v[212:213], v248 offset:12816\n\t" \
  "ds_read_b64_tr_b16 v[214:215], v248 offset:13840\n\t" \
  "v_fma_f32 v148, |v148|, v216, v148 mul:4\n\t" \
  "v_fma_f32 v151, |v151|, v219, v151 mul:4\n\t" \
  "s_waitcnt lgkmcnt(4)\n\t" \
  "v_mfma_f32_16x16x32_bf16 v[176:179], v[208:211], v[104:107], v[176:179]\n\t" \
  "ds_read_b32 v226, v226 offset:0\n\t" \
  "v_fma_f32 v150, |v150|, v218, v150 mul:4\n\t" \
  "v_fma_f32 v157, |v157|, v221, v157 mul:4\n\t" \
  "v_mfma_f32_16x16x32_bf16 v[184:187], v[208:211], v[116:119], v[184:187]\n\t" \
  "ds_read_b32 v229, v229 offset:0\n\t" \
  "v_fma_f32 v156, |v156|, v220, v156 mul:4\n\t" \
  "v_fma_f32 v159, |v159|, v223, v159 mul:4\n\t" \
  "v_fma_f32 v158, |v158|, v222, v158 mul:4\n\t" \
  "v_mfma_f32_16x16x32_bf16 v[192:195], v[208:211], v[128:131], v[192:195]\n\t" \
  "ds_read_b32 v228, v228 offset:0\n\t" \
  "v_fma_mix_f32 v160, v160, v235, 0 op_sel_hi:[0,1,0]\n\t" \
  "v_fma_mix_f32 v161, v161, v234, 0 op_sel_hi:[0,1,0]\n\t" \
  "v_cvt_pk_bf16_f32 v162, v160, v161\n\t" \
  "v_mfma_f32_16x16x32_bf16 v[200:203], v[208:211], v[140:143], v[200:203]\n\t" \
  "ds_read_b64_tr_b16 v[208:209], v248 offset:15888\n\t" \
  "v_cvt_pk_bf16_f32 v148, v148, v149\n\t" \
  "v_cvt_pk_bf16_f32 v149, v150, v151\n\t" \
  "s_waitcnt lgkmcnt(4)\n\t" \
  "v_mfma_f32_16x16x32_bf16 v[180:183], v[212:215], v[96:99], 0\n\t" \
  "ds_read_b64_tr_b16 v[210:211], v248 offset:16912\n\t" \
  "v_cvt_pk_bf16_f32 v156, v156, v157\n\t" \
  "v_cvt_pk_bf16_f32 v157, v158, v159\n\t" \
  "s_setprio 3\n\t" \
  "v_cvt_pk_f16_f32 v216, |v178|, |v179| clamp\n\t" \
  "v_mfma_f32_16x16x32_bf16 v[188:191], v[212:215], v[108:111], 0\n\t" \
  "ds_read_b32 v231, v231 offset:0\n\t" \
  "v_cvt_pk_f16_f32 v232, v176, v177\n\t" \
  "v_cvt_pk_f16_f32 v218, |v186|, |v187| clamp\n\t" \
  "v_cvt_pk_f16_f32 v220, |v184|, |v185| clamp\n\t" \
  "v_mfma_f32_16x16x32_bf16 v[196:199], v[212:215], v[120:123], 0\n\t" \
  "ds_read_b32 v230, v230 offset:0\n\t" \
  "v_pk_mul_f16 v233, v232, v232\n\t" \
  "v_and_b32_sdwa v217, s56, v216 dst_sel:DWORD dst_unused:UNUSED_PAD src0_sel:DWORD src1_sel:WORD_1\n\t" \
  "v_mfma_f32_16x16x32_bf16 v[204:207], v[212:215], v[132:135], 0\n\t" \
  "ds_read_b64_tr_b16 v[212:213], v248 offset:14864\n\t" \
  "v_and_b32 v216, s56, v216\n\t" \
  "v_and_b32_sdwa v219, s56, v218 dst_sel:DWORD dst_unused:UNUSED_PAD src0_sel:DWORD src1_sel:WORD_1\n\t" \
  "s_waitcnt lgkmcnt(3)\n\t" \
  "v_mfma_f32_16x16x32_bf16 v[180:183], v[208:211], v[100:103], v[180:183]\n\t" \
  "ds_read_b64_tr_b16 v[214:215], v248 offset:17936\n\t" \
  "v_and_b32 v218, s56, v218\n\t" \
  "v_mfma_f32_16x16x32_bf16 v[188:191], v[208:211], v[112:115], v[188:191]\n\t" \
  "s_waitcnt vmcnt(1)\n\t" \
  "s_cmp_lg_u32 s58, 1\n\t" \
  "s_cbranch_scc1 .Lcnx_c13\n\t" \
  "s_waitcnt vmcnt(0)\n\t" \
  ".Lcnx_c13:\n\t" \
  "s_mov_b64 exec, 3\n\t" \
  "ds_add_rtn_u32 v246, v251, v250 offset:47680\n\t" \
  "s_mov_b64 exec, -1\n\t" \
  "ds_read_b32 v217, v217 offset:0\n\t" \
  "v_and_b32_sdwa v221, s56, v220 dst_sel:DWORD dst_unused:UNUSED_PAD src0_sel:DWORD src1_sel:WORD_1\n\t" \
  "v_mfma_f32_16x16x32_bf16 v[196:199], v[208:211], v[124:127], v[196:199]\n\t" \
  "ds_read_b32 v216, v216 offset:0\n\t" \
  "v_and_b32 v220, s56, v220\n\t" \
  "v_pk_fma_f16 v233, v233, v253, s54\n\t" \
  "v_mfma_f32_16x16x32_bf16 v[204:207], v[208:211], v[136:139], v[204:207]\n\t" \
  "s_waitcnt lgkmcnt(2)\n\t" \
  "v_readfirstlane_b32 s4, v246\n\t" \
  "s_cmp_lt_u32 s4, s57\n\t" \
  "s_cbranch_scc1 .Lcnx_w36\n.Lcnx_b36:\n\t" \
  "ds_read_b64_tr_b16 v[208:209], v249 offset:0\n\t" \
  "v_pk_mul_f16 v233, v232, v233\n\t" \
  "v_fma_f32 v165, |v165|, v225, v165 mul:4\n\t" \
  "v_mfma_f32_16x16x32_bf16 v[180:183], v[212:215], v[104:107], v[180:183]\n\t" \
  "ds_read_b64_tr_b16 v[210:211], v249 offset:1536\n\t" \
  "v_fma_f32 v164, |v164|, v224, v164 mul:4\n\t" \
  "v_fma_f32 v167, |v167|, v227, v167 mul:4\n\t" \
  "v_mfma_f32_16x16x32_bf16 v[188:191], v[212:215], v[116:119], v[188:191]\n\t" \
  "s_cmp_eq_u32 s37, 0\n\t" \
  "s_cbranch_scc1 .Lcnx_f4\n.Lcnx_g4:\n\t" \
  "ds_read_b32 v219, v219 offset:0\n\t" \
  "v_fma_f32 v166, |v166|, v226, v166 mul:4\n\t" \
  "v_fma_f32 v173, |v173|, v229, v173 mul:4\n\t" \
  "v_fma_f32 v172, |v172|, v228, v172 mul:4\n\t" \
  "v_mfma_f32_16x16x32_bf16 v[196:199], v[212:215], v[128:131], v[196:199]\n\t" \
  "ds_read_b32 v218, v218 offset:0\n\t" \
  "v_fma_f32 v175, |v175|, v231, v175 mul:4\n\t" \
  "v_fma_f32 v174, |v174|, v230, v174 mul:4\n\t" \
  "v_cvt_pk_bf16_f32 v164, v164, v165\n\t" \
  "v_mfma_f32_16x16x32_bf16 v[204:207], v[212:215], v[140:143], v[204:207]\n\t" \
  "ds_read_b64_tr_b16 v[212:213], v249 offset:8\n\t" \
  "v_cvt_pk_bf16_f32 v165, v166, v167\n\t" \
  "v_cvt_pk_bf16_f32 v172, v172, v173\n\t" \
  "v_cvt_pk_bf16_f32 v173, v174, v175\n\t" \
  "s_setprio 0\n\t" \
  "v_exp_f16 v232, v233\n\t" \
  "v_cvt_pk_f16_f32 v224, |v194|, |v195| clamp\n\t" \
  "v_mfma_f32_16x16x32_bf16 v[16:19], v[242:245], v[236:239], v[16:19]\n\t" \
  "ds_read_b64_tr_b16 v[214:215], v249 offset:1544\n\t" \
  "v_cvt_pk_f16_f32 v234, v192, v193\n\t" \
  "v_cvt_pk_f16_f32 v226, |v202|, |v203| clamp\n\t" \
  "v_mfma_f32_16x16x32_bf16 v[20:23], v[240:243], v[236:239], v[20:23]\n\t" \
  "buffer_load_dwordx4 v[236:239], v250, s[8:11], s28 offen offset:192\n\t" \
  "s_cmp_lg_u32 s58, 0\n\t" \
  "s_cbranch_scc1 .Lcnx_c26\n\t" \
  "s_add_u32 m0, s25, 16400\n\t" \
  "s_add_u32 s24, s24, s26\n\t" \
  "buffer_load_dwordx4 v254, s[8:11], s24 offen lds\n\t" \
  "s_cmp_lg_u32 s38, 4\n\t" \
  "s_cbranch_scc1 .Lcnx_c26\n\t" \
  "s_mov_b32 m0, 163632\n\t" \
  "s_add_u32 s30, s30, s31\n\t" \
  "buffer_load_dword v252, s[8:11], s30 offen lds\n\t" \
  ".Lcnx_c26:\n\t" \
  "ds_read_b32 v221, v221 offset:0\n\t" \
  "v_cvt_pk_f16_f32 v228, |v200|, |v201| clamp\n\t" \
  "v_exp_f16_sdwa v232, v233 dst_sel:WORD_1 dst_unused:UNUSED_PRESERVE src0_sel:WORD_1\n\t" \
  "v_pk_mul_f16 v235, v234, v234\n\t" \
  "s_waitcnt lgkmcnt(5)\n\t" \
  "v_mfma_f32_16x16x32_bf16 v[0:3], v[208:211], v[146:149], v[0:3]\n\t" \
  "ds_read_b32 v220, v220 offset:0\n\t" \
  "v_and_b32_sdwa v225, s56, v224 dst_sel:DWORD dst_unused:UNUSED_PAD src0_sel:DWORD src1_sel:WORD_1\n\t" \
  "v_and_b32 v224, s56, v224\n\t" \
  "v_mfma_f32_16x16x32_bf16 v[24:27], v[208:211], v[154:157], v[24:27]\n\t" \
  "ds_read_b32 v225, v225 offset:0\n\t" \
  "v_and_b32_sdwa v227, s56, v226 dst_sel:DWORD dst_unused:UNUSED_PAD src0_sel:DWORD src1_sel:WORD_1\n\t" \
  "v_mfma_f32_16x16x32_bf16 v[48:51], v[208:211], v[162:165], v[48:51]\n\t" \
  "ds_read_b32 v224, v224 offset:0\n\t" \
  "v_and_b32 v226, s56, v226\n\t" \
  "v_and_b32_sdwa v229, s56, v228 dst_sel:DWORD dst_unused:UNUSED_PAD src0_sel:DWORD src1_sel:WORD_1\n\t" \
  "v_mfma_f32_16x16x32_bf16 v[72:75], v[208:211], v[170:173], v[72:75]\n\t" \
  "ds_read_b64_tr_b16 v[208:209], v249 offset:64\n\t" \
  "v_and_b32 v228, s56, v228\n\t" \
  "s_waitcnt lgkmcnt(5)\n\t" \
  "v_mfma_f32_16x16x32_bf16 v[4:7], v[212:215], v[146:149], v[4:7]\n\t" \
  "ds_read_b64_tr_b16 v[210:211], v249 offset:1600\n\t" \
  "v_pk_fma_f16 v235, v235, v253, s54\n\t" \
  "v_mfma_f32_16x16x32_bf16 v[28:31], v[212:215], v[154:157], v[28:31]\n\t" \
  "ds_read_b32 v227, v227 offset:0\n\t" \
  "v_pk_mul_f16 v235, v234, v235\n\t" \
  "v_fma_f32 v179, |v179|, v217, v179 mul:4\n\t" \
  "v_fma_f32 v178, |v178|, v216, v178 mul:4\n\t" \
  "v_mfma_f32_16x16x32_bf16 v[52:55], v[212:215], v[162:165], v[52:55]\n\t" \
  "ds_read_b32 v226, v226 offset:0\n\t" \
  "v_fma_f32 v187, |v187|, v219, v187 mul:4\n\t" \
  "s_waitcnt lgkmcnt(6)\n\t" \
  "v_fma_f32 v186, |v186|, v218, v186 mul:4\n\t" \
  "v_fma_f32 v185, |v185|, v221, v185 mul:4\n\t" \
  "v_mfma_f32_16x16x32_bf16 v[76:79], v[212:215], v[170:173], v[76:79]\n\t" \
  "ds_read_b64_tr_b16 v[212:213], v249 offset:72\n\t" \
  "v_fma_f32 v184, |v184|, v220, v184 mul:4\n\t" \
  "v_pk_fma_f16 v232, v232, s55, s55\n\t" \
  "s_waitcnt lgkmcnt(3)\n\t" \
  "v_mfma_f32_16x16x32_bf16 v[8:11], v[208:211], v[146:149], v[8:11]\n\t" \
  "ds_read_b64_tr_b16 v[214:215], v249 offset:1608\n\t" \
  "v_cvt_pk_bf16_f32 v179, v178, v179\n\t" \
  "v_cvt_pk_bf16_f32 v187, v186, v187\n\t" \
  "v_cvt_pk_bf16_f32 v186, v184, v185\n\t" \
  "v_mfma_f32_16x16x32_bf16 v[32:35], v[208:211], v[154:157], v[32:35]\n\t" \
  "ds_read_b32 v229, v229 offset:0\n\t" \
  "v_rcp_f16 v233, v232\n\t" \
  "v_exp_f16 v234, v235\n\t" \
  "v_cvt_pk_f16_f32 v216, |v180|, |v181| clamp\n\t" \
  "v_mfma_f32_16x16x32_bf16 v[56:59], v[208:211], v[162:165], v[56:59]\n\t" \
  "ds_read_b32 v228, v228 offset:0\n\t" \
  "s_setprio 1\n\t" \
  "v_cvt_pk_f16_f32 v218, |v182|, |v183| clamp\n\t" \
  "v_cvt_pk_f16_f32 v220, |v188|, |v189| clamp\n\t" \
  "v_mfma_f32_16x16x32_bf16 v[80:83], v[208:211], v[170:173], v[80:83]\n\t" \
  "ds_read_b64_tr_b16 v[208:209], v249 offset:128\n\t" \
  "v_cvt_pk_f16_f32 v222, |v190|, |v191| clamp\n\t" \
  "v_rcp_f16_sdwa v232, v232 dst_sel:DWORD dst_unused:UNUSED_PAD src0_sel:WORD_1\n\t" \
  "v_exp_f16_sdwa v234, v235 dst_sel:WORD_1 dst_unused:UNUSED_PRESERVE src0_sel:WORD_1\n\t" \
  "s_waitcnt lgkmcnt(3)\n\t" \
  "v_mfma_f32_16x16x32_bf16 v[12:15], v[212:215], v[146:149], v[12:15]\n\t" \
  "ds_read_b64_tr_b16 v[210:211], v249 offset:1664\n\t" \
  "v_and_b32_sdwa v217, s56, v216 dst_sel:DWORD dst_unused:UNUSED_PAD src0_sel:DWORD src1_sel:WORD_1\n\t" \
  "v_and_b32 v216, s56, v216\n\t" \
  "v_mfma_f32_16x16x32_bf16 v[36:39], v[212:215], v[154:157], v[36:39]\n\t" \
  "ds_read_b32 v217, v217 offset:0\n\t" \
  "v_and_b32_sdwa v219, s56, v218 dst_sel:DWORD dst_unused:UNUSED_PAD src0_sel:DWORD src1_sel:WORD_1\n\t" \
  "v_mfma_f32_16x16x32_bf16 v[60:63], v[212:215], v[162:165], v[60:63]\n\t" \
  "ds_read_b32 v216, v216 offset:0\n\t" \
  "v_and_b32 v218, s56, v218\n\t" \
  "v_and_b32_sdwa v221, s56, v220 dst_sel:DWORD dst_unused:UNUSED_PAD src0_sel:DWORD src1_sel:WORD_1\n\t" \
  "v_mfma_f32_16x16x32_bf16 v[84:87], v[212:215], v[170:173], v[84:87]\n\t" \
  "ds_read_b64_tr_b16 v[212:213], v249 offset:136\n\t" \
  "v_and_b32 v220, s56, v220\n\t" \
  "s_waitcnt lgkmcnt(3)\n\t" \
  "v_mfma_f32_16x16x32_bf16 v[16:19], v[208:211], v[146:149], v[16:19]\n\t" \
  "ds_read_b64_tr_b16 v[214:215], v249 offset:1672\n\t" \
  "v_and_b32_sdwa v223, s56, v222 dst_sel:DWORD dst_unused:UNUSED_PAD src0_sel:DWORD src1_sel:WORD_1\n\t" \
  "v_mfma_f32_16x16x32_bf16 v[40:43], v[208:211], v[154:157], v[40:43]\n\t" \
  "ds_read_b32 v219, v219 offset:0\n\t" \
  "v_and_b32 v222, s56, v222\n\t" \
  "v_fma_f32 v195, |v195|, v225, v195 mul:4\n\t" \
  "v_mfma_f32_16x16x32_bf16 v[64:67], v[208:211], v[162:165], v[64:67]\n\t" \
  "ds_read_b32 v218, v218 offset:0\n\t" \
  "v_fma_f32 v194, |v194|, v224, v194 mul:4\n\t" \
  "v_fma_f32 v203, |v203|, v227, v203 mul:4\n\t" \
  "v_mfma_f32_16x16x32_bf16 v[88:91], v[208:211], v[170:173], v[88:91]\n\t" \
  "ds_read_b64_tr_b16 v[208:209], v248 offset:43048\n\t" \
  "ds_read_b64_tr_b16 v[210:211], v248 offset:44072\n\t" \
  "v_fma_f32 v202, |v202|, v226, v202 mul:4\n\t" \
  "v_fma_f32 v201, |v201|, v229, v201 mul:4\n\t" \
  "s_waitcnt lgkmcnt(4)\n\t" \
  "v_mfma_f32_16x16x32_bf16 v[20:23], v[212:215], v[146:149], v[20:23]\n\t" \
  "ds_read_b32 v221, v221 offset:0\n\t" \
  "v_fma_f32 v200, |v200|, v228, v200 mul:4\n\t" \
  "v_fma_mix_f32 v176, v176, v233, 0 op_sel_hi:[0,1,0]\n\t" \
  "v_fma_mix_f32 v177, v177, v232, 0 op_sel_hi:[0,1,0]\n\t" \
  "v_mfma_f32_16x16x32_bf16 v[44:47], v[212:215], v[154:157], v[44:47]\n\t" \
  "ds_read_b32 v220, v220 offset:0\n\t" \
  "v_pk_fma_f16 v234, v234, s55, s55\n\t" \
  "v_cvt_pk_bf16_f32 v178, v176, v177\n\t" \
  "v_cvt_pk_bf16_f32 v195, v194, v195\n\t" \
  "v_mfma_f32_16x16x32_bf16 v[68:71], v[212:215], v[162:165], v[68:71]\n\t" \
  "ds_read_b32 v223, v223 offset:0\n\t" \
  "v_cvt_pk_bf16_f32 v203, v202, v203\n\t" \
  "v_cvt_pk_bf16_f32 v202, v200, v201\n\t" \
  "v_rcp_f16 v235, v234\n\t" \
  "v_mfma_f32_16x16x32_bf16 v[92:95], v[212:215], v[170:173], v[92:95]\n\t" \
  "ds_read_b64_tr_b16 v[212:213], v248 offset:46120\n\t" \
  "v_cvt_pk_f16_f32 v224, |v196|, |v197| clamp\n\t" \
  "s_setprio 2\n\t" \
  "v_cvt_pk_f16_f32 v226, |v198|, |v199| clamp\n\t" \
  "s_waitcnt lgkmcnt(4)\n\t" \
  "v_mfma_f32_16x16x32_bf16 v[144:147], v[208:211], v[96:99], 0\n\t" \
  "ds_read_b64_tr_b16 v[214:215], v248 offset:47144\n\t" \
  "v_cvt_pk_f16_f32 v228, |v204|, |v205| clamp\n\t" \
  "v_cvt_pk_f16_f32 v230, |v206|, |v207| clamp\n\t" \
  "v_rcp_f16_sdwa v234, v234 dst_sel:DWORD dst_unused:UNUSED_PAD src0_sel:WORD_1\n\t" \
  "v_mfma_f32_16x16x32_bf16 v[152:155], v[208:211], v[108:111], 0\n\t" \
  "ds_read_b32 v222, v222 offset:0\n\t" \
  "v_and_b32_sdwa v225, s56, v224 dst_sel:DWORD dst_unused:UNUSED_PAD src0_sel:DWORD src1_sel:WORD_1\n\t" \
  "v_and_b32 v224, s56, v224\n\t" \
  "v_mfma_f32_16x16x32_bf16 v[160:163], v[208:211], v[120:123], 0\n\t" \
  "ds_read_b32 v225, v225 offset:0\n\t" \
  "v_and_b32_sdwa v227, s56, v226 dst_sel:DWORD dst_unused:UNUSED_PAD src0_sel:DWORD src1_sel:WORD_1\n\t" \
  "v_mfma_f32_16x16x32_bf16 v[168:171], v[208:211], v[132:135], 0\n\t" \
  "ds_read_b64_tr_b16 v[208:209], v248 offset:45096\n\t" \
  "v_and_b32 v226, s56, v226\n\t" \
  "v_and_b32_sdwa v229, s56, v228 dst_sel:DWORD dst_unused:UNUSED_PAD src0_sel:DWORD src1_sel:WORD_1\n\t" \
  "s_waitcnt lgkmcnt(3)\n\t" \
  "v_mfma_f32_16x16x32_bf16 v[144:147], v[212:215], v[100:103], v[144:147]\n\t" \
  "ds_read_b64_tr_b16 v[210:211], v248 offset:48168\n\t" \
  "v_and_b32 v228, s56, v228\n\t" \
  "v_mfma_f32_16x16x32_bf16 v[152:155], v[212:215], v[112:115], v[152:155]\n\t" \
  "ds_read_b32 v224, v224 offset:0\n\t" \
  "v_and_b32_sdwa v231, s56, v230 dst_sel:DWORD dst_unused:UNUSED_PAD src0_sel:DWORD src1_sel:WORD_1\n\t" \
  "v_mfma_f32_16x16x32_bf16 v[160:163], v[212:215], v[124:127], v[160:163]\n\t" \
  "ds_read_b32 v227, v227 offset:0\n\t" \
  "v_and_b32 v230, s56, v230\n\t" \
  "v_fma_f32 v181, |v181|, v217, v181 mul:4\n\t" \
  "v_mfma_f32_16x16x32_bf16 v[168:171], v[212:215], v[136:139], v[168:171]\n\t" \
  "ds_read_b64_tr_b16 v[212:213], v248 offset:43560\n\t" \
  "ds_read_b64_tr_b16 v[214:215], v248 offset:44584\n\t" \
  "v_fma_f32 v180, |v180|, v216, v180 mul:4\n\t" \
  "v_fma_f32 v183, |v183|, v219, v183 mul:4\n\t" \
  "s_waitcnt lgkmcnt(4)\n\t" \
  "v_mfma_f32_16x16x32_bf16 v[144:147], v[208:211], v[104:107], v[144:147]\n\t" \
  "ds_read_b32 v226, v226 offset:0\n\t" \
  "v_fma_f32 v182, |v182|, v218, v182 mul:4\n\t" \
  "v_fma_f32 v189, |v189|, v221, v189 mul:4\n\t" \
  "v_mfma_f32_16x16x32_bf16 v[152:155], v[208:211], v[116:119], v[152:155]\n\t" \
  "ds_read_b32 v229, v229 offset:0\n\t" \
  "v_fma_f32 v188, |v188|, v220, v188 mul:4\n\t" \
  "v_fma_f32 v191, |v191|, v223, v191 mul:4\n\t" \
  "v_fma_f32 v190, |v190|, v222, v190 mul:4\n\t" \
  "v_mfma_f32_16x16x32_bf16 v[160:163], v[208:211], v[128:131], v[160:163]\n\t" \
  "ds_read_b32 v228, v228 offset:0\n\t" \
  "v_fma_mix_f32 v192, v192, v235, 0 op_sel_hi:[0,1,0]\n\t" \
  "v_fma_mix_f32 v193, v193, v234, 0 op_sel_hi:[0,1,0]\n\t" \
  "v_cvt_pk_bf16_f32 v194, v192, v193\n\t" \
  "v_mfma_f32_16x16x32_bf16 v[168:171], v[208:211], v[140:143], v[168:171]\n\t" \
  "ds_read_b64_tr_b16 v[208:209], v248 offset:46632\n\t" \
  "v_cvt_pk_bf16_f32 v180, v180, v181\n\t" \
  "v_cvt_pk_bf16_f32 v181, v182, v183\n\t" \
  "s_waitcnt lgkmcnt(4)\n\t" \
  "v_mfma_f32_16x16x32_bf16 v[148:151], v[212:215], v[96:99], 0\n\t" \
  "ds_read_b64_tr_b16 v[210:211], v248 offset:47656\n\t" \
  "v_cvt_pk_bf16_f32 v188, v188, v189\n\t" \
  "v_cvt_pk_bf16_f32 v189, v190, v191\n\t" \
  "s_setprio 3\n\t" \
  "v_cvt_pk_f16_f32 v216, |v146|, |v147| clamp\n\t" \
  "v_mfma_f32_16x16x32_bf16 v[156:159], v[212:215], v[108:111], 0\n\t" \
  "ds_read_b32 v231, v231 offset:0\n\t" \
  "v_cvt_pk_f16_f32 v232, v144, v145\n\t" \
  "v_cvt_pk_f16_f32 v218, |v154|, |v155| clamp\n\t" \
  "v_cvt_pk_f16_f32 v220, |v152|, |v153| clamp\n\t" \
  "v_mfma_f32_16x16x32_bf16 v[164:167], v[212:215], v[120:123], 0\n\t" \
  "ds_read_b32 v230, v230 offset:0\n\t" \
  "v_pk_mul_f16 v233, v232, v232\n\t" \
  "v_and_b32_sdwa v217, s56, v216 dst_sel:DWORD dst_unused:UNUSED_PAD src0_sel:DWORD src1_sel:WORD_1\n\t" \
  "v_mfma_f32_16x16x32_bf16 v[172:175], v[212:215], v[132:135], 0\n\t" \
  "ds_read_b64_tr_b16 v[212:213], v248 offset:45608\n\t" \
  "v_and_b32 v216, s56, v216\n\t" \
  "v_and_b32_sdwa v219, s56, v218 dst_sel:DWORD dst_unused:UNUSED_PAD src0_sel:DWORD src1_sel:WORD_1\n\t" \
  "s_waitcnt lgkmcnt(3)\n\t" \
  "v_mfma_f32_16x16x32_bf16 v[148:151], v[208:211], v[100:103], v[148:151]\n\t" \
  "ds_read_b64_tr_b16 v[214:215], v248 offset:48680\n\t" \
  "v_and_b32 v218, s56, v218\n\t" \
  "v_mfma_f32_16x16x32_bf16 v[156:159], v[208:211], v[112:115], v[156:159]\n\t" \
  "s_waitcnt vmcnt(1)\n\t" \
  "s_cmp_lg_u32 s58, 1\n\t" \
  "s_cbranch_scc1 .Lcnx_c14\n\t" \
  "s_waitcnt vmcnt(0)\n\t" \
  ".Lcnx_c14:\n\t" \
  "s_mov_b64 exec, 3\n\t" \
  "ds_add_rtn_u32 v246, v251, v250 offset:47688\n\t" \
  "s_mov_b64 exec, -1\n\t" \
  "ds_read_b32 v217, v217 offset:0\n\t" \
  "v_and_b32_sdwa v221, s56, v220 dst_sel:DWORD dst_unused:UNUSED_PAD src0_sel:DWORD src1_sel:WORD_1\n\t" \
  "v_mfma_f32_16x16x32_bf16 v[164:167], v[208:211], v[124:127], v[164:167]\n\t" \
  "ds_read_b32 v216, v216 offset:0\n\t" \
  "v_and_b32 v220, s56, v220\n\t" \
  "v_pk_fma_f16 v233, v233, v253, s54\n\t" \
  "v_mfma_f32_16x16x32_bf16 v[172:175], v[208:211], v[136:139], v[172:175]\n\t" \
  "s_waitcnt lgkmcnt(2)\n\t" \
  "v_readfirstlane_b32 s4, v246\n\t" \
  "s_cmp_lt_u32 s4, s57\n\t" \
  "s_cbranch_scc1 .Lcnx_w48\n.Lcnx_b48:\n\t" \
  "ds_read_b64_tr_b16 v[208:209], v249 offset:6152\n\t" \
  "v_pk_mul_f16 v233, v232, v233\n\t" \
  "v_fma_f32 v197, |v197|, v225, v197 mul:4\n\t" \
  "v_mfma_f32_16x16x32_bf16 v[148:151], v[212:215], v[104:107], v[148:151]\n\t" \
  "ds_read_b64_tr_b16 v[210:211], v249 offset:7688\n\t" \
  "v_fma_f32 v196, |v196|, v224, v196 mul:4\n\t" \
  "v_fma_f32 v199, |v199|, v227, v199 mul:4\n\t" \
  "v_mfma_f32_16x16x32_bf16 v[156:159], v[212:215], v[116:119], v[156:159]\n\t" \
  "s_cmp_eq_u32 s37, 0\n\t" \
  "s_cbranch_scc1 .Lcnx_f5\n.Lcnx_g5:\n\t" \
  "ds_read_b32 v219, v219 offset:0\n\t" \
  "v_fma_f32 v198, |v198|, v226, v198 mul:4\n\t" \
  "v_fma_f32 v205, |v205|, v229, v205 mul:4\n\t" \
  "v_fma_f32 v204, |v204|, v228, v204 mul:4\n\t" \
  "v_mfma_f32_16x16x32_bf16 v[164:167], v[212:215], v[128:131], v[164:167]\n\t" \
  "ds_read_b32 v218, v218 offset:0\n\t" \
  "v_fma_f32 v207, |v207|, v231, v207 mul:4\n\t" \
  "v_fma_f32 v206, |v206|, v230, v206 mul:4\n\t" \
  "v_cvt_pk_bf16_f32 v196, v196, v197\n\t" \
  "v_mfma_f32_16x16x32_bf16 v[172:175], v[212:215], v[140:143], v[172:175]\n\t" \
  "ds_read_b64_tr_b16 v[212:213], v249 offset:6160\n\t" \
  "v_cvt_pk_bf16_f32 v197, v198, v199\n\t" \
  "v_cvt_pk_bf16_f32 v204, v204, v205\n\t" \
  "v_cvt_pk_bf16_f32 v205, v206, v207\n\t" \
  "s_setprio 0\n\t" \
  "v_exp_f16 v232, v233\n\t" \
  "v_cvt_pk_f16_f32 v224, |v162|, |v163| clamp\n\t" \
  "v_mfma_f32_16x16x32_bf16 v[24:27], v[242:245], v[236:239], v[24:27]\n\t" \
  "ds_read_b64_tr_b16 v[214:215], v249 offset:7696\n\t" \
  "v_cvt_pk_f16_f32 v234, v160, v161\n\t" \
  "v_cvt_pk_f16_f32 v226, |v170|, |v171| clamp\n\t" \
  "v_mfma_f32_16x16x32_bf16 v[28:31], v[240:243], v[236:239], v[28:31]\n\t" \
  "buffer_load_dwordx4 v[236:239], v250, s[8:11], s28 offen offset:256\n\t" \
  "s_cmp_lg_u32 s58, 0\n\t" \
  "s_cbranch_scc1 .Lcnx_c27\n\t" \
  "s_add_u32 m0, s25, 24600\n\t" \
  "s_add_u32 s24, s24, s26\n\t" \
  "buffer_load_dwordx4 v254, s[8:11], s24 offen lds\n\t" \
  "s_cmp_lg_u32 s38, 5\n\t" \
  "s_cbranch_scc1 .Lcnx_c27\n\t" \
  "s_mov_b32 m0, 163632\n\t" \
  "s_add_u32 s30, s30, s31\n\t" \
  "buffer_load_dword v252, s[8:11], s30 offen lds\n\t" \
  ".Lcnx_c27:\n\t" \
  "ds_read_b32 v221, v221 offset:0\n\t" \
  "v_cvt_pk_f16_f32 v228, |v168|, |v169| clamp\n\t" \
  "v_exp_f16_sdwa v232, v233 dst_sel:WORD_1 dst_unused:UNUSED_PRESERVE src0_sel:WORD_1\n\t" \
  "v_pk_mul_f16 v235, v234, v234\n\t" \
  "s_waitcnt lgkmcnt(5)\n\t" \
  "v_mfma_f32_16x16x32_bf16 v[0:3], v[208:211], v[178:181], v[0:3]\n\t" \
  "ds_read_b32 v220, v220 offset:0\n\t" \
  "v_and_b32_sdwa v225, s56, v224 dst_sel:DWORD dst_unused:UNUSED_PAD src0_sel:DWORD src1_sel:WORD_1\n\t" \
  "v_and_b32 v224, s56, v224\n\t" \
  "v_mfma_f32_16x16x32_bf16 v[24:27], v[208:211], v[186:189], v[24:27]\n\t" \
  "ds_read_b32 v225, v225 offset:0\n\t" \
  "v_and_b32_sdwa v227, s56, v226 dst_sel:DWORD dst_unused:UNUSED_PAD src0_sel:DWORD src1_sel:WORD_1\n\t" \
  "v_mfma_f32_16x16x32_bf16 v[48:51], v[208:211], v[194:197], v[48:51]\n\t" \
  "ds_read_b32 v224, v224 offset:0\n\t" \
  "v_and_b32 v226, s56, v226\n\t" \
  "v_and_b32_sdwa v229, s56, v228 dst_sel:DWORD dst_unused:UNUSED_PAD src0_sel:DWORD src1_sel:WORD_1\n\t" \
  "v_mfma_f32_16x16x32_bf16 v[72:75], v[208:211], v[202:205], v[72:75]\n\t" \
  "ds_read_b64_tr_b16 v[208:209], v249 offset:6216\n\t" \
  "v_and_b32 v228, s56, v228\n\t" \
  "s_waitcnt lgkmcnt(5)\n\t" \
  "v_mfma_f32_16x16x32_bf16 v[4:7], v[212:215], v[178:181], v[4:7]\n\t" \
  "ds_read_b64_tr_b16 v[210:211], v249 offset:7752\n\t" \
  "v_pk_fma_f16 v235, v235, v253, s54\n\t" \
  "v_mfma_f32_16x16x32_bf16 v[28:31], v[212:215], v[186:189], v[28:31]\n\t" \
  "ds_read_b32 v227, v227 offset:0\n\t" \
  "v_pk_mul_f16 v235, v234, v235\n\t" \
  "v_fma_f32 v147, |v147|, v217, v147 mul:4\n\t" \
  "v_fma_f32 v146, |v146|, v216, v146 mul:4\n\t" \
  "v_mfma_f32_16x16x32_bf16 v[52:55], v[212:215], v[194:197], v[52:55]\n\t" \
  "ds_read_b32 v226, v226 offset:0\n\t" \
  "v_fma_f32 v155, |v155|, v219, v155 mul:4\n\t" \
  "s_waitcnt lgkmcnt(6)\n\t" \
  "v_fma_f32 v154, |v154|, v218, v154 mul:4\n\t" \
  "v_fma_f32 v153, |v153|, v221, v153 mul:4\n\t" \
  "v_mfma_f32_16x16x32_bf16 v[76:79], v[212:215], v[202:205], v[76:79]\n\t" \
  "ds_read_b64_tr_b16 v[212:213], v249 offset:6224\n\t" \
  "v_fma_f32 v152, |v152|, v220, v152 mul:4\n\t" \
  "v_pk_fma_f16 v232, v232, s55, s55\n\t" \
  "s_waitcnt lgkmcnt(3)\n\t" \
  "v_mfma_f32_16x16x32_bf16 v[8:11], v[208:211], v[178:181], v[8:11]\n\t" \
  "ds_read_b64_tr_b16 v[214:215], v249 offset:7760\n\t" \
  "v_cvt_pk_bf16_f32 v147, v146, v147\n\t" \
  "v_cvt_pk_bf16_f32 v155, v154, v155\n\t" \
  "v_cvt_pk_bf16_f32 v154, v152, v153\n\t" \
  "v_mfma_f32_16x16x32_bf16 v[32:35], v[208:211], v[186:189], v[32:35]\n\t" \
  "ds_read_b32 v229, v229 offset:0\n\t" \
  "v_rcp_f16 v233, v232\n\t" \
  "v_exp_f16 v234, v235\n\t" \
  "v_cvt_pk_f16_f32 v216, |v148|, |v149| clamp\n\t" \
  "v_mfma_f32_16x16x32_bf16 v[56:59], v[208:211], v[194:197], v[56:59]\n\t" \
  "ds_read_b32 v228, v228 offset:0\n\t" \
  "s_setprio 1\n\t" \
  "v_cvt_pk_f16_f32 v218, |v150|, |v151| clamp\n\t" \
  "v_cvt_pk_f16_f32 v220, |v156|, |v157| clamp\n\t" \
  "v_mfma_f32_16x16x32_bf16 v[80:83], v[208:211], v[202:205], v[80:83]\n\t" \
  "ds_read_b64_tr_b16 v[208:209], v249 offset:6280\n\t" \
  "v_cvt_pk_f16_f32 v222, |v158|, |v159| clamp\n\t" \
  "v_rcp_f16_sdwa v232, v232 dst_sel:DWORD dst_unused:UNUSED_PAD src0_sel:WORD_1\n\t" \
  "v_exp_f16_sdwa v234, v235 dst_sel:WORD_1 dst_unused:UNUSED_PRESERVE src0_sel:WORD_1\n\t" \
  "s_waitcnt lgkmcnt(3)\n\t" \
  "v_mfma_f32_16x16x32_bf16 v[12:15], v[212:215], v[178:181], v[12:15]\n\t" \
  "ds_read_b64_tr_b16 v[210:211], v249 offset:7816\n\t" \
  "v_and_b32_sdwa v217, s56, v216 dst_sel:DWORD dst_unused:UNUSED_PAD src0_sel:DWORD src1_sel:WORD_1\n\t" \
  "v_and_b32 v216, s56, v216\n\t" \
  "v_mfma_f32_16x16x32_bf16 v[36:39], v[212:215], v[186:189], v[36:39]\n\t" \
  "ds_read_b32 v217, v217 offset:0\n\t" \
  "v_and_b32_sdwa v219, s56, v218 dst_sel:DWORD dst_unused:UNUSED_PAD src0_sel:DWORD src1_sel:WORD_1\n\t" \
  "v_mfma_f32_16x16x32_bf16 v[60:63], v[212:215], v[194:197], v[60:63]\n\t" \
  "ds_read_b32 v216, v216 offset:0\n\t" \
  "v_and_b32 v218, s56, v218\n\t" \
  "v_and_b32_sdwa v221, s56, v220 dst_sel:DWORD dst_unused:UNUSED_PAD src0_sel:DWORD src1_sel:WORD_1\n\t" \
  "v_mfma_f32_16x16x32_bf16 v[84:87], v[212:215], v[202:205], v[84:87]\n\t" \
  "ds_read_b64_tr_b16 v[212:213], v249 offset:6288\n\t" \
  "v_and_b32 v220, s56, v220\n\t" \
  "s_waitcnt lgkmcnt(3)\n\t" \
  "v_mfma_f32_16x16x32_bf16 v[16:19], v[208:211], v[178:181], v[16:19]\n\t" \
  "ds_read_b64_tr_b16 v[214:215], v249 offset:7824\n\t" \
  "v_and_b32_sdwa v223, s56, v222 dst_sel:DWORD dst_unused:UNUSED_PAD src0_sel:DWORD src1_sel:WORD_1\n\t" \
  "v_mfma_f32_16x16x32_bf16 v[40:43], v[208:211], v[186:189], v[40:43]\n\t" \
  "ds_read_b32 v219, v219 offset:0\n\t" \
  "v_and_b32 v222, s56, v222\n\t" \
  "v_fma_f32 v163, |v163|, v225, v163 mul:4\n\t" \
  "v_mfma_f32_16x16x32_bf16 v[64:67], v[208:211], v[194:197], v[64:67]\n\t" \
  "ds_read_b32 v218, v218 offset:0\n\t" \
  "v_fma_f32 v162, |v162|, v224, v162 mul:4\n\t" \
  "v_fma_f32 v171, |v171|, v227, v171 mul:4\n\t" \
  "v_mfma_f32_16x16x32_bf16 v[88:91], v[208:211], v[202:205], v[88:91]\n\t" \
  "ds_read_b64_tr_b16 v[208:209], v248 offset:49192\n\t" \
  "ds_read_b64_tr_b16 v[210:211], v248 offset:50216\n\t" \
  "v_fma_f32 v170, |v170|, v226, v170 mul:4\n\t" \
  "v_fma_f32 v169, |v169|, v229, v169 mul:4\n\t" \
  "s_waitcnt lgkmcnt(4)\n\t" \
  "v_mfma_f32_16x16x32_bf16 v[20:23], v[212:215], v[178:181], v[20:23]\n\t" \
  "ds_read_b32 v221, v221 offset:0\n\t" \
  "v_fma_f32 v168, |v168|, v228, v168 mul:4\n\t" \
  "v_fma_mix_f32 v144, v144, v233, 0 op_sel_hi:[0,1,0]\n\t" \
  "v_fma_mix_f32 v145, v145, v232, 0 op_sel_hi:[0,1,0]\n\t" \
  "v_mfma_f32_16x16x32_bf16 v[44:47], v[212:215], v[186:189], v[44:47]\n\t" \
  "ds_read_b32 v220, v220 offset:0\n\t" \
  "v_pk_fma_f16 v234, v234, s55, s55\n\t" \
  "v_cvt_pk_bf16_f32 v146, v144, v145\n\t" \
  "v_cvt_pk_bf16_f32 v163, v162, v163\n\t" \
  "v_mfma_f32_16x16x32_bf16 v[68:71], v[212:215], v[194:197], v[68:71]\n\t" \
  "ds_read_b32 v223, v223 offset:0\n\t" \
  "v_cvt_pk_bf16_f32 v171, v170, v171\n\t" \
  "v_cvt_pk_bf16_f32 v170, v168, v169\n\t" \
  "v_rcp_f16 v235, v234\n\t" \
  "v_mfma_f32_16x16x32_bf16 v[92:95], v[212:215], v[202:205], v[92:95]\n\t" \
  "ds_read_b64_tr_b16 v[212:213], v248 offset:52264\n\t" \
  "v_cvt_pk_f16_f32 v224, |v164|, |v165| clamp\n\t" \
  "s_setprio 2\n\t" \
  "v_cvt_pk_f16_f32 v226, |v166|, |v167| clamp\n\t" \
  "s_waitcnt lgkmcnt(4)\n\t" \
  "v_mfma_f32_16x16x32_bf16 v[176:179], v[208:211], v[96:99], 0\n\t" \
  "ds_read_b64_tr_b16 v[214:215], v248 offset:53288\n\t" \
  "v_cvt_pk_f16_f32 v228, |v172|, |v173| clamp\n\t" \
  "v_cvt_pk_f16_f32 v230, |v174|, |v175| clamp\n\t" \
  "v_rcp_f16_sdwa v234, v234 dst_sel:DWORD dst_unused:UNUSED_PAD src0_sel:WORD_1\n\t" \
  "v_mfma_f32_16x16x32_bf16 v[184:187], v[208:211], v[108:111], 0\n\t" \
  "ds_read_b32 v222, v222 offset:0\n\t" \
  "v_and_b32_sdwa v225, s56, v224 dst_sel:DWORD dst_unused:UNUSED_PAD src0_sel:DWORD src1_sel:WORD_1\n\t" \
  "v_and_b32 v224, s56, v224\n\t" \
  "v_mfma_f32_16x16x32_bf16 v[192:195], v[208:211], v[120:123], 0\n\t" \
  "ds_read_b32 v225, v225 offset:0\n\t" \
  "v_and_b32_sdwa v227, s56, v226 dst_sel:DWORD dst_unused:UNUSED_PAD src0_sel:DWORD src1_sel:WORD_1\n\t" \
  "v_mfma_f32_16x16x32_bf16 v[200:203], v[208:211], v[132:135], 0\n\t" \
  "ds_read_b64_tr_b16 v[208:209], v248 offset:51240\n\t" \
  "v_and_b32 v226, s56, v226\n\t" \
  "v_and_b32_sdwa v229, s56, v228 dst_sel:DWORD dst_unused:UNUSED_PAD src0_sel:DWORD src1_sel:WORD_1\n\t" \
  "s_waitcnt lgkmcnt(3)\n\t" \
  "v_mfma_f32_16x16x32_bf16 v[176:179], v[212:215], v[100:103], v[176:179]\n\t" \
  "ds_read_b64_tr_b16 v[210:211], v248 offset:54312\n\t" \
  "v_and_b32 v228, s56, v228\n\t" \
  "v_mfma_f32_16x16x32_bf16 v[184:187], v[212:215], v[112:115], v[184:187]\n\t" \
  "ds_read_b32 v224, v224 offset:0\n\t" \
  "v_and_b32_sdwa v231, s56, v230 dst_sel:DWORD dst_unused:UNUSED_PAD src0_sel:DWORD src1_sel:WORD_1\n\t" \
  "v_mfma_f32_16x16x32_bf16 v[192:195], v[212:215], v[124:127], v[192:195]\n\t" \
  "ds_read_b32 v227, v227 offset:0\n\t" \
  "v_and_b32 v230, s56, v230\n\t" \
  "v_fma_f32 v149, |v149|, v217, v149 mul:4\n\t" \
  "v_mfma_f32_16x16x32_bf16 v[200:203], v[212:215], v[136:139], v[200:203]\n\t" \
  "ds_read_b64_tr_b16 v[212:213], v248 offset:49704\n\t" \
  "ds_read_b64_tr_b16 v[214:215], v248 offset:50728\n\t" \
  "v_fma_f32 v148, |v148|, v216, v148 mul:4\n\t" \
  "v_fma_f32 v151, |v151|, v219, v151 mul:4\n\t" \
  "s_waitcnt lgkmcnt(4)\n\t" \
  "v_mfma_f32_16x16x32_bf16 v[176:179], v[208:211], v[104:107], v[176:179]\n\t" \
  "ds_read_b32 v226, v226 offset:0\n\t" \
  "v_fma_f32 v150, |v150|, v218, v150 mul:4\n\t" \
  "v_fma_f32 v157, |v157|, v221, v157 mul:4\n\t" \
  "v_mfma_f32_16x16x32_bf16 v[184:187], v[208:211], v[116:119], v[184:187]\n\t" \
  "ds_read_b32 v229, v229 offset:0\n\t" \
  "v_fma_f32 v156, |v156|, v220, v156 mul:4\n\t" \
  "v_fma_f32 v159, |v159|, v223, v159 mul:4\n\t" \
  "v_fma_f32 v158, |v158|, v222, v158 mul:4\n\t" \
  "v_mfma_f32_16x16x32_bf16 v[192:195], v[208:211], v[128:131], v[192:195]\n\t" \
  "ds_read_b32 v228, v228 offset:0\n\t" \
  "v_fma_mix_f32 v160, v160, v235, 0 op_sel_hi:[0,1,0]\n\t" \
  "v_fma_mix_f32 v161, v161, v234, 0 op_sel_hi:[0,1,0]\n\t" \
  "v_cvt_pk_bf16_f32 v162, v160, v161\n\t" \
  "v_mfma_f32_16x16x32_bf16 v[200:203], v[208:211], v[140:143], v[200:203]\n\t" \
  "ds_read_b64_tr_b16 v[208:209], v248 offset:52776\n\t" \
  "v_cvt_pk_bf16_f32 v148, v148, v149\n\t" \
  "v_cvt_pk_bf16_f32 v149, v150, v151\n\t" \
  "s_waitcnt lgkmcnt(4)\n\t" \
  "v_mfma_f32_16x16x32_bf16 v[180:183], v[212:215], v[96:99], 0\n\t" \
  "ds_read_b64_tr_b16 v[210:211], v248 offset:53800\n\t" \
  "v_cvt_pk_bf16_f32 v156, v156, v157\n\t" \
  "v_cvt_pk_bf16_f32 v157, v158, v159\n\t" \
  "s_setprio 3\n\t" \
  "v_cvt_pk_f16_f32 v216, |v178|, |v179| clamp\n\t" \
  "v_mfma_f32_16x16x32_bf16 v[188:191], v[212:215], v[108:111], 0\n\t" \
  "ds_read_b32 v231, v231 offset:0\n\t" \
  "v_cvt_pk_f16_f32 v232, v176, v177\n\t" \
  "v_cvt_pk_f16_f32 v218, |v186|, |v187| clamp\n\t" \
  "v_cvt_pk_f16_f32 v220, |v184|, |v185| clamp\n\t" \
  "v_mfma_f32_16x16x32_bf16 v[196:199], v[212:215], v[120:123], 0\n\t" \
  "ds_read_b32 v230, v230 offset:0\n\t" \
  "v_pk_mul_f16 v233, v232, v232\n\t" \
  "v_and_b32_sdwa v217, s56, v216 dst_sel:DWORD dst_unused:UNUSED_PAD src0_sel:DWORD src1_sel:WORD_1\n\t" \
  "v_mfma_f32_16x16x32_bf16 v[204:207], v[212:215], v[132:135], 0\n\t" \
  "ds_read_b64_tr_b16 v[212:213], v248 offset:51752\n\t" \
  "v_and_b32 v216, s56, v216\n\t" \
  "v_and_b32_sdwa v219, s56, v218 dst_sel:DWORD dst_unused:UNUSED_PAD src0_sel:DWORD src1_sel:WORD_1\n\t" \
  "s_waitcnt lgkmcnt(3)\n\t" \
  "v_mfma_f32_16x16x32_bf16 v[180:183], v[208:211], v[100:103], v[180:183]\n\t" \
  "ds_read_b64_tr_b16 v[214:215], v248 offset:54824\n\t" \
  "v_and_b32 v218, s56, v218\n\t" \
  "v_mfma_f32_16x16x32_bf16 v[188:191], v[208:211], v[112:115], v[188:191]\n\t" \
  "s_waitcnt vmcnt(1)\n\t" \
  "s_cmp_lg_u32 s58, 1\n\t" \
  "s_cbranch_scc1 .Lcnx_c15\n\t" \
  "s_waitcnt vmcnt(0)\n\t" \
  ".Lcnx_c15:\n\t" \
  "s_mov_b64 exec, 3\n\t" \
  "ds_add_rtn_u32 v246, v251, v250 offset:47696\n\t" \
  "s_mov_b64 exec, -1\n\t" \
  "ds_read_b32 v217, v217 offset:0\n\t" \
  "v_and_b32_sdwa v221, s56, v220 dst_sel:DWORD dst_unused:UNUSED_PAD src0_sel:DWORD src1_sel:WORD_1\n\t" \
  "v_mfma_f32_16x16x32_bf16 v[196:199], v[208:211], v[124:127], v[196:199]\n\t" \
  "ds_read_b32 v216, v216 offset:0\n\t" \
  "v_and_b32 v220, s56, v220\n\t" \
  "v_pk_fma_f16 v233, v233, v253, s54\n\t" \
  "v_mfma_f32_16x16x32_bf16 v[204:207], v[208:211], v[136:139], v[204:207]\n\t" \
  "s_waitcnt lgkmcnt(2)\n\t" \
  "v_readfirstlane_b32 s4, v246\n\t" \
  "s_cmp_lt_u32 s4, s57\n\t" \
  "s_cbranch_scc1 .Lcnx_w60\n.Lcnx_b60:\n\t" \
  "ds_read_b64_tr_b16 v[208:209], v249 offset:24600\n\t" \
  "v_pk_mul_f16 v233, v232, v233\n\t" \
  "v_fma_f32 v165, |v165|, v225, v165 mul:4\n\t" \
  "v_mfma_f32_16x16x32_bf16 v[180:183], v[212:215], v[104:107], v[180:183]\n\t" \
  "ds_read_b64_tr_b16 v[210:211], v249 offset:26136\n\t" \
  "v_fma_f32 v164, |v164|, v224, v164 mul:4\n\t" \
  "v_fma_f32 v167, |v167|, v227, v167 mul:4\n\t" \
  "v_mfma_f32_16x16x32_bf16 v[188:191], v[212:215], v[116:119], v[188:191]\n\t" \
  "s_cmp_eq_u32 s37, 0\n\t" \
  "s_cbranch_scc1 .Lcnx_f6\n.Lcnx_g6:\n\t" \
  "ds_read_b32 v219, v219 offset:0\n\t" \
  "v_fma_f32 v166, |v166|, v226, v166 mul:4\n\t" \
  "v_fma_f32 v173, |v173|, v229, v173 mul:4\n\t" \
  "v_fma_f32 v172, |v172|, v228, v172 mul:4\n\t" \
  "v_mfma_f32_16x16x32_bf16 v[196:199], v[212:215], v[128:131], v[196:199]\n\t" \
  "ds_read_b32 v218, v218 offset:0\n\t" \
  "v_fma_f32 v175, |v175|, v231, v175 mul:4\n\t" \
  "v_fma_f32 v174, |v174|, v230, v174 mul:4\n\t" \
  "v_cvt_pk_bf16_f32 v164, v164, v165\n\t" \
  "v_mfma_f32_16x16x32_bf16 v[204:207], v[212:215], v[140:143], v[204:207]\n\t" \
  "ds_read_b64_tr_b16 v[212:213], v249 offset:24608\n\t" \
  "v_cvt_pk_bf16_f32 v165, v166, v167\n\t" \
  "v_cvt_pk_bf16_f32 v172, v172, v173\n\t" \
  "v_cvt_pk_bf16_f32 v173, v174, v175\n\t" \
  "s_setprio 0\n\t" \
  "v_exp_f16 v232, v233\n\t" \
  "v_cvt_pk_f16_f32 v224, |v194|, |v195| clamp\n\t" \
  "v_mfma_f32_16x16x32_bf16 v[32:35], v[242:245], v[236:239], v[32:35]\n\t" \
  "ds_read_b64_tr_b16 v[214:215], v249 offset:26144\n\t" \
  "v_cvt_pk_f16_f32 v234, v192, v193\n\t" \
  "v_cvt_pk_f16_f32 v226, |v202|, |v203| clamp\n\t" \
  "v_mfma_f32_16x16x32_bf16 v[36:39], v[240:243], v[236:239], v[36:39]\n\t" \
  "buffer_load_dwordx4 v[236:239], v250, s[8:11], s28 offen offset:320\n\t" \
  "s_cmp_lg_u32 s58, 0\n\t" \
  "s_cbranch_scc1 .Lcnx_c28\n\t" \
  "s_add_u32 m0, s25, 32800\n\t" \
  "s_add_u32 s24, s24, s26\n\t" \
  "buffer_load_dwordx4 v254, s[8:11], s24 offen lds\n\t" \
  "s_cmp_lg_u32 s38, 6\n\t" \
  "s_cbranch_scc1 .Lcnx_c28\n\t" \
  "s_mov_b32 m0, 163632\n\t" \
  "s_add_u32 s30, s30, s31\n\t" \
  "buffer_load_dword v252, s[8:11], s30 offen lds\n\t" \
  ".Lcnx_c28:\n\t" \
  "ds_read_b32 v221, v221 offset:0\n\t" \
  "v_cvt_pk_f16_f32 v228, |v200|, |v201| clamp\n\t" \
  "v_exp_f16_sdwa v232, v233 dst_sel:WORD_1 dst_unused:UNUSED_PRESERVE src0_sel:WORD_1\n\t" \
  "v_pk_mul_f16 v235, v234, v234\n\t" \
  "s_waitcnt lgkmcnt(5)\n\t" \
  "v_mfma_f32_16x16x32_bf16 v[0:3], v[208:211], v[146:149], v[0:3]\n\t" \
  "ds_read_b32 v220, v220 offset:0\n\t" \
  "v_and_b32_sdwa v225, s56, v224 dst_sel:DWORD dst_unused:UNUSED_PAD src0_sel:DWORD src1_sel:WORD_1\n\t" \
  "v_and_b32 v224, s56, v224\n\t" \
  "v_mfma_f32_16x16x32_bf16 v[24:27], v[208:211], v[154:157], v[24:27]\n\t" \
  "ds_read_b32 v225, v225 offset:0\n\t" \
  "v_and_b32_sdwa v227, s56, v226 dst_sel:DWORD dst_unused:UNUSED_PAD src0_sel:DWORD src1_sel:WORD_1\n\t" \
  "v_mfma_f32_16x16x32_bf16 v[48:51], v[208:211], v[162:165], v[48:51]\n\t" \
  "ds_read_b32 v224, v224 offset:0\n\t" \
  "v_and_b32 v226, s56, v226\n\t" \
  "v_and_b32_sdwa v229, s56, v228 dst_sel:DWORD dst_unused:UNUSED_PAD src0_sel:DWORD src1_sel:WORD_1\n\t" \
  "v_mfma_f32_16x16x32_bf16 v[72:75], v[208:211], v[170:173], v[72:75]\n\t" \
  "ds_read_b64_tr_b16 v[208:209], v249 offset:24664\n\t" \
  "v_and_b32 v228, s56, v228\n\t" \
  "s_waitcnt lgkmcnt(5)\n\t" \
  "v_mfma_f32_16x16x32_bf16 v[4:7], v[212:215], v[146:149], v[4:7]\n\t" \
  "ds_read_b64_tr_b16 v[210:211], v249 offset:26200\n\t" \
  "v_pk_fma_f16 v235, v235, v253, s54\n\t" \
  "v_mfma_f32_16x16x32_bf16 v[28:31], v[212:215], v[154:157], v[28:31]\n\t" \
  "ds_read_b32 v227, v227 offset:0\n\t" \
  "v_pk_mul_f16 v235, v234, v235\n\t" \
  "v_fma_f32 v179, |v179|, v217, v179 mul:4\n\t" \
  "v_fma_f32 v178, |v178|, v216, v178 mul:4\n\t" \
  "v_mfma_f32_16x16x32_bf16 v[52:55], v[212:215], v[162:165], v[52:55]\n\t" \
  "ds_read_b32 v226, v226 offset:0\n\t" \
  "v_fma_f32 v187, |v187|, v219, v187 mul:4\n\t" \
  "s_waitcnt lgkmcnt(6)\n\t" \
  "v_fma_f32 v186, |v186|, v218, v186 mul:4\n\t" \
  "v_fma_f32 v185, |v185|, v221, v185 mul:4\n\t" \
  "v_mfma_f32_16x16x32_bf16 v[76:79], v[212:215], v[170:173], v[76:79]\n\t" \
  "ds_read_b64_tr_b16 v[212:213], v249 offset:24672\n\t" \
  "v_fma_f32 v184, |v184|, v220, v184 mul:4\n\t" \
  "v_pk_fma_f16 v232, v232, s55, s55\n\t" \
  "s_waitcnt lgkmcnt(3)\n\t" \
  "v_mfma_f32_16x16x32_bf16 v[8:11], v[208:211], v[146:149], v[8:11]\n\t" \
  "ds_read_b64_tr_b16 v[214:215], v249 offset:26208\n\t" \
  "v_cvt_pk_bf16_f32 v179, v178, v179\n\t" \
  "v_cvt_pk_bf16_f32 v187, v186, v187\n\t" \
  "v_cvt_pk_bf16_f32 v186, v184, v185\n\t" \
  "v_mfma_f32_16x16x32_bf16 v[32:35], v[208:211], v[154:157], v[32:35]\n\t" \
  "ds_read_b32 v229, v229 offset:0\n\t" \
  "v_rcp_f16 v233, v232\n\t" \
  "v_exp_f16 v234, v235\n\t" \
  "v_cvt_pk_f16_f32 v216, |v180|, |v181| clamp\n\t" \
  "v_mfma_f32_16x16x32_bf16 v[56:59], v[208:211], v[162:165], v[56:59]\n\t" \
  "ds_read_b32 v228, v228 offset:0\n\t" \
  "s_setprio 1\n\t" \
  "v_cvt_pk_f16_f32 v218, |v182|, |v183| clamp\n\t" \
  "v_cvt_pk_f16_f32 v220, |v188|, |v189| clamp\n\t" \
  "v_mfma_f32_16x16x32_bf16 v[80:83], v[208:211], v[170:173], v[80:83]\n\t" \
  "ds_read_b64_tr_b16 v[208:209], v249 offset:24728\n\t" \
  "v_cvt_pk_f16_f32 v222, |v190|, |v191| clamp\n\t" \
  "v_rcp_f16_sdwa v232, v232 dst_sel:DWORD dst_unused:UNUSED_PAD src0_sel:WORD_1\n\t" \
  "v_exp_f16_sdwa v234, v235 dst_sel:WORD_1 dst_unused:UNUSED_PRESERVE src0_sel:WORD_1\n\t" \
  "s_waitcnt lgkmcnt(3)\n\t" \
  "v_mfma_f32_16x16x32_bf16 v[12:15], v[212:215], v[146:149], v[12:15]\n\t" \
  "ds_read_b64_tr_b16 v[210:211], v249 offset:26264\n\t" \
  "v_and_b32_sdwa v217, s56, v216 dst_sel:DWORD dst_unused:UNUSED_PAD src0_sel:DWORD src1_sel:WORD_1\n\t" \
  "v_and_b32 v216, s56, v216\n\t" \
  "v_mfma_f32_16x16x32_bf16 v[36:39], v[212:215], v[154:157], v[36:39]\n\t" \
  "ds_read_b32 v217, v217 offset:0\n\t" \
  "v_and_b32_sdwa v219, s56, v218 dst_sel:DWORD dst_unused:UNUSED_PAD src0_sel:DWORD src1_sel:WORD_1\n\t" \
  "v_mfma_f32_16x16x32_bf16 v[60:63], v[212:215], v[162:165], v[60:63]\n\t" \
  "ds_read_b32 v216, v216 offset:0\n\t" \
  "v_and_b32 v218, s56, v218\n\t" \
  "v_and_b32_sdwa v221, s56, v220 dst_sel:DWORD dst_unused:UNUSED_PAD src0_sel:DWORD src1_sel:WORD_1\n\t" \
  "v_mfma_f32_16x16x32_bf16 v[84:87], v[212:215], v[170:173], v[84:87]\n\t" \
  "ds_read_b64_tr_b16 v[212:213], v249 offset:24736\n\t" \
  "v_and_b32 v220, s56, v220\n\t" \
  "s_waitcnt lgkmcnt(3)\n\t" \
  "v_mfma_f32_16x16x32_bf16 v[16:19], v[208:211], v[146:149], v[16:19]\n\t" \
  "ds_read_b64_tr_b16 v[214:215], v249 offset:26272\n\t" \
  "v_and_b32_sdwa v223, s56, v222 dst_sel:DWORD dst_unused:UNUSED_PAD src0_sel:DWORD src1_sel:WORD_1\n\t" \
  "v_mfma_f32_16x16x32_bf16 v[40:43], v[208:211], v[154:157], v[40:43]\n\t" \
  "ds_read_b32 v219, v219 offset:0\n\t" \
  "v_and_b32 v222, s56, v222\n\t" \
  "v_fma_f32 v195, |v195|, v225, v195 mul:4\n\t" \
  "v_mfma_f32_16x16x32_bf16 v[64:67], v[208:211], v[162:165], v[64:67]\n\t" \
  "ds_read_b32 v218, v218 offset:0\n\t" \
  "v_fma_f32 v194, |v194|, v224, v194 mul:4\n\t" \
  "v_fma_f32 v203, |v203|, v227, v203 mul:4\n\t" \
  "v_mfma_f32_16x16x32_bf16 v[88:91], v[208:211], v[170:173], v[88:91]\n\t" \
  "ds_read_b64_tr_b16 v[208:209], v248 offset:36904\n\t" \
  "ds_read_b64_tr_b16 v[210:211], v248 offset:37928\n\t" \
  "v_fma_f32 v202, |v202|, v226, v202 mul:4\n\t" \
  "v_fma_f32 v201, |v201|, v229, v201 mul:4\n\t" \
  "s_waitcnt lgkmcnt(4)\n\t" \
  "v_mfma_f32_16x16x32_bf16 v[20:23], v[212:215], v[146:149], v[20:23]\n\t" \
  "ds_read_b32 v221, v221 offset:0\n\t" \
  "v_fma_f32 v200, |v200|, v228, v200 mul:4\n\t" \
  "v_fma_mix_f32 v176, v176, v233, 0 op_sel_hi:[0,1,0]\n\t" \
  "v_fma_mix_f32 v177, v177, v232, 0 op_sel_hi:[0,1,0]\n\t" \
  "v_mfma_f32_16x16x32_bf16 v[44:47], v[212:215], v[154:157], v[44:47]\n\t" \
  "ds_read_b32 v220, v220 offset:0\n\t" \
  "v_pk_fma_f16 v234, v234, s55, s55\n\t" \
  "v_cvt_pk_bf16_f32 v178, v176, v177\n\t" \
  "v_cvt_pk_bf16_f32 v195, v194, v195\n\t" \
  "v_mfma_f32_16x16x32_bf16 v[68:71], v[212:215], v[162:165], v[68:71]\n\t" \
  "ds_read_b32 v223, v223 offset:0\n\t" \
  "v_cvt_pk_bf16_f32 v203, v202, v203\n\t" \
  "v_cvt_pk_bf16_f32 v202, v200, v201\n\t" \
  "v_rcp_f16 v235, v234\n\t" \
  "v_mfma_f32_16x16x32_bf16 v[92:95], v[212:215], v[170:173], v[92:95]\n\t" \
  "ds_read_b64_tr_b16 v[212:213], v248 offset:39976\n\t" \
  "v_cvt_pk_f16_f32 v224, |v196|, |v197| clamp\n\t" \
  "s_setprio 2\n\t" \
  "v_cvt_pk_f16_f32 v226, |v198|, |v199| clamp\n\t" \
  "s_waitcnt lgkmcnt(4)\n\t" \
  "v_mfma_f32_16x16x32_bf16 v[144:147], v[208:211], v[96:99], 0\n\t" \
  "ds_read_b64_tr_b16 v[214:215], v248 offset:41000\n\t" \
  "v_cvt_pk_f16_f32 v228, |v204|, |v205| clamp\n\t" \
  "v_cvt_pk_f16_f32 v230, |v206|, |v207| clamp\n\t" \
  "v_rcp_f16_sdwa v234, v234 dst_sel:DWORD dst_unused:UNUSED_PAD src0_sel:WORD_1\n\t" \
  "v_mfma_f32_16x16x32_bf16 v[152:155], v[208:211], v[108:111], 0\n\t" \
  "ds_read_b32 v222, v222 offset:0\n\t" \
  "v_and_b32_sdwa v225, s56, v224 dst_sel:DWORD dst_unused:UNUSED_PAD src0_sel:DWORD src1_sel:WORD_1\n\t" \
  "v_and_b32 v224, s56, v224\n\t" \
  "v_mfma_f32_16x16x32_bf16 v[160:163], v[208:211], v[120:123], 0\n\t" \
  "ds_read_b32 v225, v225 offset:0\n\t" \
  "v_and_b32_sdwa v227, s56, v226 dst_sel:DWORD dst_unused:UNUSED_PAD src0_sel:DWORD src1_sel:WORD_1\n\t" \
  "v_mfma_f32_16x16x32_bf16 v[168:171], v[208:211], v[132:135], 0\n\t" \
  "ds_read_b64_tr_b16 v[208:209], v248 offset:38952\n\t" \
  "v_and_b32 v226, s56, v226\n\t" \
  "v_and_b32_sdwa v229, s56, v228 dst_sel:DWORD dst_unused:UNUSED_PAD src0_sel:DWORD src1_sel:WORD_1\n\t" \
  "s_waitcnt lgkmcnt(3)\n\t" \
  "v_mfma_f32_16x16x32_bf16 v[144:147], v[212:215], v[100:103], v[144:147]\n\t" \
  "ds_read_b64_tr_b16 v[210:211], v248 offset:42024\n\t" \
  "v_and_b32 v228, s56, v228\n\t" \
  "v_mfma_f32_16x16x32_bf16 v[152:155], v[212:215], v[112:115], v[152:155]\n\t" \
  "ds_read_b32 v224, v224 offset:0\n\t" \
  "v_and_b32_sdwa v231, s56, v230 dst_sel:DWORD dst_unused:UNUSED_PAD src0_sel:DWORD src1_sel:WORD_1\n\t" \
  "v_mfma_f32_16x16x32_bf16 v[160:163], v[212:215], v[124:127], v[160:163]\n\t" \
  "ds_read_b32 v227, v227 offset:0\n\t" \
  "v_and_b32 v230, s56, v230\n\t" \
  "v_fma_f32 v181, |v181|, v217, v181 mul:4\n\t" \
  "v_mfma_f32_16x16x32_bf16 v[168:171], v[212:215], v[136:139], v[168:171]\n\t" \
  "ds_read_b64_tr_b16 v[212:213], v248 offset:37416\n\t" \
  "ds_read_b64_tr_b16 v[214:215], v248 offset:38440\n\t" \
  "v_fma_f32 v180, |v180|, v216, v180 mul:4\n\t" \
  "v_fma_f32 v183, |v183|, v219, v183 mul:4\n\t" \
  "s_waitcnt lgkmcnt(4)\n\t" \
  "v_mfma_f32_16x16x32_bf16 v[144:147], v[208:211], v[104:107], v[144:147]\n\t" \
  "ds_read_b32 v226, v226 offset:0\n\t" \
  "v_fma_f32 v182, |v182|, v218, v182 mul:4\n\t" \
  "v_fma_f32 v189, |v189|, v221, v189 mul:4\n\t" \
  "v_mfma_f32_16x16x32_bf16 v[152:155], v[208:211], v[116:119], v[152:155]\n\t" \
  "ds_read_b32 v229, v229 offset:0\n\t" \
  "v_fma_f32 v188, |v188|, v220, v188 mul:4\n\t" \
  "v_fma_f32 v191, |v191|, v223, v191 mul:4\n\t" \
  "v_fma_f32 v190, |v190|, v222, v190 mul:4\n\t" \
  "v_mfma_f32_16x16x32_bf16 v[160:163], v[208:211], v[128:131], v[160:163]\n\t" \
  "ds_read_b32 v228, v228 offset:0\n\t" \
  "v_fma_mix_f32 v192, v192, v235, 0 op_sel_hi:[0,1,0]\n\t" \
  "v_fma_mix_f32 v193, v193, v234, 0 op_sel_hi:[0,1,0]\n\t" \
  "v_cvt_pk_bf16_f32 v194, v192, v193\n\t" \
  "v_mfma_f32_16x16x32_bf16 v[168:171], v[208:211], v[140:143], v[168:171]\n\t" \
  "ds_read_b64_tr_b16 v[208:209], v248 offset:40488\n\t" \
  "v_cvt_pk_bf16_f32 v180, v180, v181\n\t" \
  "v_cvt_pk_bf16_f32 v181, v182, v183\n\t" \
  "s_waitcnt lgkmcnt(4)\n\t" \
  "v_mfma_f32_16x16x32_bf16 v[148:151], v[212:215], v[96:99], 0\n\t" \
  "ds_read_b64_tr_b16 v[210:211], v248 offset:41512\n\t" \
  "v_cvt_pk_bf16_f32 v188, v188, v189\n\t" \
  "v_cvt_pk_bf16_f32 v189, v190, v191\n\t" \
  "s_setprio 3\n\t" \
  "v_cvt_pk_f16_f32 v216, |v146|, |v147| clamp\n\t" \
  "v_mfma_f32_16x16x32_bf16 v[156:159], v[212:215], v[108:111], 0\n\t" \
  "ds_read_b32 v231, v231 offset:0\n\t" \
  "v_cvt_pk_f16_f32 v232, v144, v145\n\t" \
  "v_cvt_pk_f16_f32 v218, |v154|, |v155| clamp\n\t" \
  "v_cvt_pk_f16_f32 v220, |v152|, |v153| clamp\n\t" \
  "v_mfma_f32_16x16x32_bf16 v[164:167], v[212:215], v[120:123], 0\n\t" \
  "ds_read_b32 v230, v230 offset:0\n\t" \
  "v_pk_mul_f16 v233, v232, v232\n\t" \
  "v_and_b32_sdwa v217, s56, v216 dst_sel:DWORD dst_unused:UNUSED_PAD src0_sel:DWORD src1_sel:WORD_1\n\t" \
  "v_mfma_f32_16x16x32_bf16 v[172:175], v[212:215], v[132:135], 0\n\t" \
  "ds_read_b64_tr_b16 v[212:213], v248 offset:39464\n\t" \
  "v_and_b32 v216, s56, v216\n\t" \
  "v_and_b32_sdwa v219, s56, v218 dst_sel:DWORD dst_unused:UNUSED_PAD src0_sel:DWORD src1_sel:WORD_1\n\t" \
  "s_waitcnt lgkmcnt(3)\n\t" \
  "v_mfma_f32_16x16x32_bf16 v[148:151], v[208:211], v[100:103], v[148:151]\n\t" \
  "ds_read_b64_tr_b16 v[214:215], v248 offset:42536\n\t" \
  "v_and_b32 v218, s56, v218\n\t" \
  "v_mfma_f32_16x16x32_bf16 v[156:159], v[208:211], v[112:115], v[156:159]\n\t" \
  "s_waitcnt vmcnt(1)\n\t" \
  "s_cmp_lg_u32 s58, 1\n\t" \
  "s_cbranch_scc1 .Lcnx_c16\n\t" \
  "s_waitcnt vmcnt(0)\n\t" \
  ".Lcnx_c16:\n\t" \
  "s_mov_b64 exec, 3\n\t" \
  "ds_add_rtn_u32 v246, v251, v250 offset:47704\n\t" \
  "s_mov_b64 exec, -1\n\t" \
  "ds_read_b32 v217, v217 offset:0\n\t" \
  "v_and_b32_sdwa v221, s56, v220 dst_sel:DWORD dst_unused:UNUSED_PAD src0_sel:DWORD src1_sel:WORD_1\n\t" \
  "v_mfma_f32_16x16x32_bf16 v[164:167], v[208:211], v[124:127], v[164:167]\n\t" \
  "ds_read_b32 v216, v216 offset:0\n\t" \
  "v_and_b32 v220, s56, v220\n\t" \
  "v_pk_fma_f16 v233, v233, v253, s54\n\t" \
  "v_mfma_f32_16x16x32_bf16 v[172:175], v[208:211], v[136:139], v[172:175]\n\t" \
  "s_waitcnt lgkmcnt(2)\n\t" \
  "v_readfirstlane_b32 s4, v246\n\t" \
  "s_cmp_lt_u32 s4, s57\n\t" \
  "s_cbranch_scc1 .Lcnx_w72\n.Lcnx_b72:\n\t" \
  "ds_read_b64_tr_b16 v[208:209], v249 offset:30752\n\t" \
  "v_pk_mul_f16 v233, v232, v233\n\t" \
  "v_fma_f32 v197, |v197|, v225, v197 mul:4\n\t" \
  "v_mfma_f32_16x16x32_bf16 v[148:151], v[212:215], v[104:107], v[148:151]\n\t" \
  "ds_read_b64_tr_b16 v[210:211], v249 offset:32288\n\t" \
  "v_fma_f32 v196, |v196|, v224, v196 mul:4\n\t" \
  "v_fma_f32 v199, |v199|, v227, v199 mul:4\n\t" \
  "v_mfma_f32_16x16x32_bf16 v[156:159], v[212:215], v[116:119], v[156:159]\n\t" \
  "s_cmp_eq_u32 s37, 0\n\t" \
  "s_cbranch_scc1 .Lcnx_f7\n.Lcnx_g7:\n\t" \
  "ds_read_b32 v219, v219 offset:0\n\t" \
  "v_fma_f32 v198, |v198|, v226, v198 mul:4\n\t" \
  "v_fma_f32 v205, |v205|, v229, v205 mul:4\n\t" \
  "v_fma_f32 v204, |v204|, v228, v204 mul:4\n\t" \
  "v_mfma_f32_16x16x32_bf16 v[164:167], v[212:215], v[128:131], v[164:167]\n\t" \
  "ds_read_b32 v218, v218 offset:0\n\t" \
  "v_fma_f32 v207, |v207|, v231, v207 mul:4\n\t" \
  "v_fma_f32 v206, |v206|, v230, v206 mul:4\n\t" \
  "v_cvt_pk_bf16_f32 v196, v196, v197\n\t" \
  "v_mfma_f32_16x16x32_bf16 v[172:175], v[212:215], v[140:143], v[172:175]\n\t" \
  "ds_read_b64_tr_b16 v[212:213], v249 offset:30760\n\t" \
  "v_cvt_pk_bf16_f32 v197, v198, v199\n\t" \
  "v_cvt_pk_bf16_f32 v204, v204, v205\n\t" \
  "v_cvt_pk_bf16_f32 v205, v206, v207\n\t" \
  "s_setprio 0\n\t" \
  "v_exp_f16 v232, v233\n\t" \
  "v_cvt_pk_f16_f32 v224, |v162|, |v163| clamp\n\t" \
  "v_mfma_f32_16x16x32_bf16 v[40:43], v[242:245], v[236:239], v[40:43]\n\t" \
  "ds_read_b64_tr_b16 v[214:215], v249 offset:32296\n\t" \
  "v_cvt_pk_f16_f32 v234, v160, v161\n\t" \
  "v_cvt_pk_f16_f32 v226, |v170|, |v171| clamp\n\t" \
  "v_mfma_f32_16x16x32_bf16 v[44:47], v[240:243], v[236:239], v[44:47]\n\t" \
  "buffer_load_dwordx4 v[236:239], v250, s[8:11], s29 offen offset:0\n\t" \
  "s_cmp_lg_u32 s58, 0\n\t" \
  "s_cbranch_scc1 .Lcnx_c29\n\t" \
  "s_add_u32 m0, s25, 41000\n\t" \
  "s_add_u32 s24, s24, s26\n\t" \
  "buffer_load_dwordx4 v254, s[8:11], s24 offen lds\n\t" \
  "s_cmp_lg_u32 s38, 7\n\t" \
  "s_cbranch_scc1 .Lcnx_c29\n\t" \
  "s_mov_b32 m0, 163632\n\t" \
  "s_add_u32 s30, s30, s31\n\t" \
  "buffer_load_dword v252, s[8:11], s30 offen lds\n\t" \
  ".Lcnx_c29:\n\t" \
  "ds_read_b32 v221, v221 offset:0\n\t" \
  "v_cvt_pk_f16_f32 v228, |v168|, |v169| clamp\n\t" \
  "v_exp_f16_sdwa v232, v233 dst_sel:WORD_1 dst_unused:UNUSED_PRESERVE src0_sel:WORD_1\n\t" \
  "v_pk_mul_f16 v235, v234, v234\n\t" \
  "s_waitcnt lgkmcnt(5)\n\t" \
  "v_mfma_f32_16x16x32_bf16 v[0:3], v[208:211], v[178:181], v[0:3]\n\t" \
  "ds_read_b32 v220, v220 offset:0\n\t" \
  "v_and_b32_sdwa v225, s56, v224 dst_sel:DWORD dst_unused:UNUSED_PAD src0_sel:DWORD src1_sel:WORD_1\n\t" \
  "v_and_b32 v224, s56, v224\n\t" \
  "v_mfma_f32_16x16x32_bf16 v[24:27], v[208:211], v[186:189], v[24:27]\n\t" \
  "ds_read_b32 v225, v225 offset:0\n\t" \
  "v_and_b32_sdwa v227, s56, v226 dst_sel:DWORD dst_unused:UNUSED_PAD src0_sel:DWORD src1_sel:WORD_1\n\t" \
  "v_mfma_f32_16x16x32_bf16 v[48:51], v[208:211], v[194:197], v[48:51]\n\t" \
  "ds_read_b32 v224, v224 offset:0\n\t" \
  "v_and_b32 v226, s56, v226\n\t" \
  "v_and_b32_sdwa v229, s56, v228 dst_sel:DWORD dst_unused:UNUSED_PAD src0_sel:DWORD src1_sel:WORD_1\n\t" \
  "v_mfma_f32_16x16x32_bf16 v[72:75], v[208:211], v[202:205], v[72:75]\n\t" \
  "ds_read_b64_tr_b16 v[208:209], v249 offset:30816\n\t" \
  "v_and_b32 v228, s56, v228\n\t" \
  "s_waitcnt lgkmcnt(5)\n\t" \
  "v_mfma_f32_16x16x32_bf16 v[4:7], v[212:215], v[178:181], v[4:7]\n\t" \
  "ds_read_b64_tr_b16 v[210:211], v249 offset:32352\n\t" \
  "v_pk_fma_f16 v235, v235, v253, s54\n\t" \
  "v_mfma_f32_16x16x32_bf16 v[28:31], v[212:215], v[186:189], v[28:31]\n\t" \
  "ds_read_b32 v227, v227 offset:0\n\t" \
  "v_pk_mul_f16 v235, v234, v235\n\t" \
  "v_fma_f32 v147, |v147|, v217, v147 mul:4\n\t" \
  "v_fma_f32 v146, |v146|, v216, v146 mul:4\n\t" \
  "v_mfma_f32_16x16x32_bf16 v[52:55], v[212:215], v[194:197], v[52:55]\n\t" \
  "ds_read_b32 v226, v226 offset:0\n\t" \
  "v_fma_f32 v155, |v155|, v219, v155 mul:4\n\t" \
  "s_waitcnt lgkmcnt(6)\n\t" \
  "v_fma_f32 v154, |v154|, v218, v154 mul:4\n\t" \
  "v_fma_f32 v153, |v153|, v221, v153 mul:4\n\t" \
  "v_mfma_f32_16x16x32_bf16 v[76:79], v[212:215], v[202:205], v[76:79]\n\t" \
  "ds_read_b64_tr_b16 v[212:213], v249 offset:30824\n\t" \
  "v_fma_f32 v152, |v152|, v220, v152 mul:4\n\t" \
  "v_pk_fma_f16 v232, v232, s55, s55\n\t" \
  "s_waitcnt lgkmcnt(3)\n\t" \
  "v_mfma_f32_16x16x32_bf16 v[8:11], v[208:211], v[178:181], v[8:11]\n\t" \
  "ds_read_b64_tr_b16 v[214:215], v249 offset:32360\n\t" \
  "v_cvt_pk_bf16_f32 v147, v146, v147\n\t" \
  "v_cvt_pk_bf16_f32 v155, v154, v155\n\t" \
  "v_cvt_pk_bf16_f32 v154, v152, v153\n\t" \
  "v_mfma_f32_16x16x32_bf16 v[32:35], v[208:211], v[186:189], v[32:35]\n\t" \
  "ds_read_b32 v229, v229 offset:0\n\t" \
  "v_rcp_f16 v233, v232\n\t" \
  "v_exp_f16 v234, v235\n\t" \
  "v_cvt_pk_f16_f32 v216, |v148|, |v149| clamp\n\t" \
  "v_mfma_f32_16x16x32_bf16 v[56:59], v[208:211], v[194:197], v[56:59]\n\t" \
  "ds_read_b32 v228, v228 offset:0\n\t" \
  "s_setprio 1\n\t" \
  "v_cvt_pk_f16_f32 v218, |v150|, |v151| clamp\n\t" \
  "v_cvt_pk_f16_f32 v220, |v156|, |v157| clamp\n\t" \
  "v_mfma_f32_16x16x32_bf16 v[80:83], v[208:211], v[202:205], v[80:83]\n\t" \
  "ds_read_b64_tr_b16 v[208:209], v249 offset:30880\n\t" \
  "v_cvt_pk_f16_f32 v222, |v158|, |v159| clamp\n\t" \
  "v_rcp_f16_sdwa v232, v232 dst_sel:DWORD dst_unused:UNUSED_PAD src0_sel:WORD_1\n\t" \
  "v_exp_f16_sdwa v234, v235 dst_sel:WORD_1 dst_unused:UNUSED_PRESERVE src0_sel:WORD_1\n\t" \
  "s_waitcnt lgkmcnt(3)\n\t" \
  "v_mfma_f32_16x16x32_bf16 v[12:15], v[212:215], v[178:181], v[12:15]\n\t" \
  "ds_read_b64_tr_b16 v[210:211], v249 offset:32416\n\t" \
  "v_and_b32_sdwa v217, s56, v216 dst_sel:DWORD dst_unused:UNUSED_PAD src0_sel:DWORD src1_sel:WORD_1\n\t" \
  "v_and_b32 v216, s56, v216\n\t" \
  "v_mfma_f32_16x16x32_bf16 v[36:39], v[212:215], v[186:189], v[36:39]\n\t" \
  "ds_read_b32 v217, v217 offset:0\n\t" \
  "v_and_b32_sdwa v219, s56, v218 dst_sel:DWORD dst_unused:UNUSED_PAD src0_sel:DWORD src1_sel:WORD_1\n\t" \
  "v_mfma_f32_16x16x32_bf16 v[60:63], v[212:215], v[194:197], v[60:63]\n\t" \
  "ds_read_b32 v216, v216 offset:0\n\t" \
  "v_and_b32 v218, s56, v218\n\t" \
  "v_and_b32_sdwa v221, s56, v220 dst_sel:DWORD dst_unused:UNUSED_PAD src0_sel:DWORD src1_sel:WORD_1\n\t" \
  "v_mfma_f32_16x16x32_bf16 v[84:87], v[212:215], v[202:205], v[84:87]\n\t" \
  "ds_read_b64_tr_b16 v[212:213], v249 offset:30888\n\t" \
  "v_and_b32 v220, s56, v220\n\t" \
  "s_waitcnt lgkmcnt(3)\n\t" \
  "v_mfma_f32_16x16x32_bf16 v[16:19], v[208:211], v[178:181], v[16:19]\n\t" \
  "ds_read_b64_tr_b16 v[214:215], v249 offset:32424\n\t" \
  "v_and_b32_sdwa v223, s56, v222 dst_sel:DWORD dst_unused:UNUSED_PAD src0_sel:DWORD src1_sel:WORD_1\n\t" \
  "v_mfma_f32_16x16x32_bf16 v[40:43], v[208:211], v[186:189], v[40:43]\n\t" \
  "ds_read_b32 v219, v219 offset:0\n\t" \
  "v_and_b32 v222, s56, v222\n\t" \
  "v_fma_f32 v163, |v163|, v225, v163 mul:4\n\t" \
  "v_mfma_f32_16x16x32_bf16 v[64:67], v[208:211], v[194:197], v[64:67]\n\t" \
  "ds_read_b32 v218, v218 offset:0\n\t" \
  "v_fma_f32 v162, |v162|, v224, v162 mul:4\n\t" \
  "v_fma_f32 v171, |v171|, v227, v171 mul:4\n\t" \
  "v_mfma_f32_16x16x32_bf16 v[88:91], v[208:211], v[202:205], v[88:91]\n\t" \
  "ds_read_b64_tr_b16 v[208:209], v248 offset:12304\n\t" \
  "ds_read_b64_tr_b16 v[210:211], v248 offset:13328\n\t" \
  "v_fma_f32 v170, |v170|, v226, v170 mul:4\n\t" \
  "v_fma_f32 v169, |v169|, v229, v169 mul:4\n\t" \
  "s_waitcnt lgkmcnt(4)\n\t" \
  "v_mfma_f32_16x16x32_bf16 v[20:23], v[212:215], v[178:181], v[20:23]\n\t" \
  "ds_read_b32 v221, v221 offset:0\n\t" \
  "v_fma_f32 v168, |v168|, v228, v168 mul:4\n\t" \
  "v_fma_mix_f32 v144, v144, v233, 0 op_sel_hi:[0,1,0]\n\t" \
  "v_fma_mix_f32 v145, v145, v232, 0 op_sel_hi:[0,1,0]\n\t" \
  "v_mfma_f32_16x16x32_bf16 v[44:47], v[212:215], v[186:189], v[44:47]\n\t" \
  "ds_read_b32 v220, v220 offset:0\n\t" \
  "v_pk_fma_f16 v234, v234, s55, s55\n\t" \
  "v_cvt_pk_bf16_f32 v146, v144, v145\n\t" \
  "v_cvt_pk_bf16_f32 v163, v162, v163\n\t" \
  "v_mfma_f32_16x16x32_bf16 v[68:71], v[212:215], v[194:197], v[68:71]\n\t" \
  "ds_read_b32 v223, v223 offset:0\n\t" \
  "v_cvt_pk_bf16_f32 v171, v170, v171\n\t" \
  "v_cvt_pk_bf16_f32 v170, v168, v169\n\t" \
  "v_rcp_f16 v235, v234\n\t" \
  "v_mfma_f32_16x16x32_bf16 v[92:95], v[212:215], v[202:205], v[92:95]\n\t" \
  "ds_read_b64_tr_b16 v[212:213], v248 offset:15376\n\t" \
  "v_cvt_pk_f16_f32 v224, |v164|, |v165| clamp\n\t" \
  "s_setprio 2\n\t" \
  "v_cvt_pk_f16_f32 v226, |v166|, |v167| clamp\n\t" \
  "s_waitcnt lgkmcnt(4)\n\t" \
  "v_mfma_f32_16x16x32_bf16 v[176:179], v[208:211], v[96:99], 0\n\t" \
  "ds_read_b64_tr_b16 v[214:215], v248 offset:16400\n\t" \
  "v_cvt_pk_f16_f32 v228, |v172|, |v173| clamp\n\t" \
  "v_cvt_pk_f16_f32 v230, |v174|, |v175| clamp\n\t" \
  "v_rcp_f16_sdwa v234, v234 dst_sel:DWORD dst_unused:UNUSED_PAD src0_sel:WORD_1\n\t" \
  "v_mfma_f32_16x16x32_bf16 v[184:187], v[208:211], v[108:111], 0\n\t" \
  "ds_read_b32 v222, v222 offset:0\n\t" \
  "v_and_b32_sdwa v225, s56, v224 dst_sel:DWORD dst_unused:UNUSED_PAD src0_sel:DWORD src1_sel:WORD_1\n\t" \
  "v_and_b32 v224, s56, v224\n\t" \
  "v_mfma_f32_16x16x32_bf16 v[192:195], v[208:211], v[120:123], 0\n\t" \
  "ds_read_b32 v225, v225 offset:0\n\t" \
  "v_and_b32_sdwa v227, s56, v226 dst_sel:DWORD dst_unused:UNUSED_PAD src0_sel:DWORD src1_sel:WORD_1\n\t" \
  "v_mfma_f32_16x16x32_bf16 v[200:203], v[208:211], v[132:135], 0\n\t" \
  "ds_read_b64_tr_b16 v[208:209], v248 offset:14352\n\t" \
  "v_and_b32 v226, s56, v226\n\t" \
  "v_and_b32_sdwa v229, s56, v228 dst_sel:DWORD dst_unused:UNUSED_PAD src0_sel:DWORD src1_sel:WORD_1\n\t" \
  "s_waitcnt lgkmcnt(3)\n\t" \
  "v_mfma_f32_16x16x32_bf16 v[176:179], v[212:215], v[100:103], v[176:179]\n\t" \
  "ds_read_b64_tr_b16 v[210:211], v248 offset:17424\n\t" \
  "v_and_b32 v228, s56, v228\n\t" \
  "v_mfma_f32_16x16x32_bf16 v[184:187], v[212:215], v[112:115], v[184:187]\n\t" \
  "ds_read_b32 v224, v224 offset:0\n\t" \
  "v_and_b32_sdwa v231, s56, v230 dst_sel:DWORD dst_unused:UNUSED_PAD src0_sel:DWORD src1_sel:WORD_1\n\t" \
  "v_mfma_f32_16x16x32_bf16 v[192:195], v[212:215], v[124:127], v[192:195]\n\t" \
  "ds_read_b32 v227, v227 offset:0\n\t" \
  "v_and_b32 v230, s56, v230\n\t" \
  "v_fma_f32 v149, |v149|, v217, v149 mul:4\n\t" \
  "v_mfma_f32_16x16x32_bf16 v[200:203], v[212:215], v[136:139], v[200:203]\n\t" \
  "ds_read_b64_tr_b16 v[212:213], v248 offset:12816\n\t" \
  "ds_read_b64_tr_b16 v[214:215], v248 offset:13840\n\t" \
  "v_fma_f32 v148, |v148|, v216, v148 mul:4\n\t" \
  "v_fma_f32 v151, |v151|, v219, v151 mul:4\n\t" \
  "s_waitcnt lgkmcnt(4)\n\t" \
  "v_mfma_f32_16x16x32_bf16 v[176:179], v[208:211], v[104:107], v[176:179]\n\t" \
  "ds_read_b32 v226, v226 offset:0\n\t" \
  "v_fma_f32 v150, |v150|, v218, v150 mul:4\n\t" \
  "v_fma_f32 v157, |v157|, v221, v157 mul:4\n\t" \
  "v_mfma_f32_16x16x32_bf16 v[184:187], v[208:211], v[116:119], v[184:187]\n\t" \
  "ds_read_b32 v229, v229 offset:0\n\t" \
  "v_fma_f32 v156, |v156|, v220, v156 mul:4\n\t" \
  "v_fma_f32 v159, |v159|, v223, v159 mul:4\n\t" \
  "v_fma_f32 v158, |v158|, v222, v158 mul:4\n\t" \
  "v_mfma_f32_16x16x32_bf16 v[192:195], v[208:211], v[128:131], v[192:195]\n\t" \
  "ds_read_b32 v228, v228 offset:0\n\t" \
  "v_fma_mix_f32 v160, v160, v235, 0 op_sel_hi:[0,1,0]\n\t" \
  "v_fma_mix_f32 v161, v161, v234, 0 op_sel_hi:[0,1,0]\n\t" \
  "v_cvt_pk_bf16_f32 v162, v160, v161\n\t" \
  "v_mfma_f32_16x16x32_bf16 v[200:203], v[208:211], v[140:143], v[200:203]\n\t" \
  "ds_read_b64_tr_b16 v[208:209], v248 offset:15888\n\t" \
  "v_cvt_pk_bf16_f32 v148, v148, v149\n\t" \
  "v_cvt_pk_bf16_f32 v149, v150, v151\n\t" \
  "s_waitcnt lgkmcnt(4)\n\t" \
  "v_mfma_f32_16x16x32_bf16 v[180:183], v[212:215], v[96:99], 0\n\t" \
  "ds_read_b64_tr_b16 v[210:211], v248 offset:16912\n\t" \
  "v_cvt_pk_bf16_f32 v156, v156, v157\n\t" \
  "v_cvt_pk_bf16_f32 v157, v158, v159\n\t" \
  "s_setprio 3\n\t" \
  "v_cvt_pk_f16_f32 v216, |v178|, |v179| clamp\n\t" \
  "v_mfma_f32_16x16x32_bf16 v[188:191], v[212:215], v[108:111], 0\n\t" \
  "ds_read_b32 v231, v231 offset:0\n\t" \
  "v_cvt_pk_f16_f32 v232, v176, v177\n\t" \
  "v_cvt_pk_f16_f32 v218, |v186|, |v187| clamp\n\t" \
  "v_cvt_pk_f16_f32 v220, |v184|, |v185| clamp\n\t" \
  "v_mfma_f32_16x16x32_bf16 v[196:199], v[212:215], v[120:123], 0\n\t" \
  "ds_read_b32 v230, v230 offset:0\n\t" \
  "v_pk_mul_f16 v233, v232, v232\n\t" \
  "v_and_b32_sdwa v217, s56, v216 dst_sel:DWORD dst_unused:UNUSED_PAD src0_sel:DWORD src1_sel:WORD_1\n\t" \
  "v_mfma_f32_16x16x32_bf16 v[204:207], v[212:215], v[132:135], 0\n\t" \
  "ds_read_b64_tr_b16 v[212:213], v248 offset:14864\n\t" \
  "v_and_b32 v216, s56, v216\n\t" \
  "v_and_b32_sdwa v219, s56, v218 dst_sel:DWORD dst_unused:UNUSED_PAD src0_sel:DWORD src1_sel:WORD_1\n\t" \
  "s_waitcnt lgkmcnt(3)\n\t" \
  "v_mfma_f32_16x16x32_bf16 v[180:183], v[208:211], v[100:103], v[180:183]\n\t" \
  "ds_read_b64_tr_b16 v[214:215], v248 offset:17936\n\t" \
  "v_and_b32 v218, s56, v218\n\t" \
  "v_mfma_f32_16x16x32_bf16 v[188:191], v[208:211], v[112:115], v[188:191]\n\t" \
  "s_waitcnt vmcnt(1)\n\t" \
  "s_cmp_lg_u32 s58, 1\n\t" \
  "s_cbranch_scc1 .Lcnx_c17\n\t" \
  "s_waitcnt vmcnt(0)\n\t" \
  ".Lcnx_c17:\n\t" \
  "s_mov_b64 exec, 3\n\t" \
  "ds_add_rtn_u32 v246, v251, v250 offset:47712\n\t" \
  "s_mov_b64 exec, -1\n\t" \
  "ds_read_b32 v217, v217 offset:0\n\t" \
  "v_and_b32_sdwa v221, s56, v220 dst_sel:DWORD dst_unused:UNUSED_PAD src0_sel:DWORD src1_sel:WORD_1\n\t" \
  "v_mfma_f32_16x16x32_bf16 v[196:199], v[208:211], v[124:127], v[196:199]\n\t" \
  "ds_read_b32 v216, v216 offset:0\n\t" \
  "v_and_b32 v220, s56, v220\n\t" \
  "v_pk_fma_f16 v233, v233, v253, s54\n\t" \
  "v_mfma_f32_16x16x32_bf16 v[204:207], v[208:211], v[136:139], v[204:207]\n\t" \
  "s_waitcnt lgkmcnt(2)\n\t" \
  "v_readfirstlane_b32 s4, v246\n\t" \
  "s_cmp_lt_u32 s4, s57\n\t" \
  "s_cbranch_scc1 .Lcnx_w84\n.Lcnx_b84:\n\t" \
  "s_or_b32 s37, s37, s58\n\t" \
  "ds_read_b64_tr_b16 v[208:209], v249 offset:0\n\t" \
  "v_pk_mul_f16 v233, v232, v233\n\t" \
  "v_fma_f32 v165, |v165|, v225, v165 mul:4\n\t" \
  "v_mfma_f32_16x16x32_bf16 v[180:183], v[212:215], v[104:107], v[180:183]\n\t" \
  "ds_read_b64_tr_b16 v[210:211], v249 offset:1536\n\t" \
  "v_fma_f32 v164, |v164|, v224, v164 mul:4\n\t" \
  "v_fma_f32 v167, |v167|, v227, v167 mul:4\n\t" \
  "v_mfma_f32_16x16x32_bf16 v[188:191], v[212:215], v[116:119], v[188:191]\n\t" \
  "s_cmp_eq_u32 s37, 0\n\t" \
  "s_cbranch_scc1 .Lcnx_f8\n.Lcnx_g8:\n\t" \
  "ds_read_b32 v219, v219 offset:0\n\t" \
  "v_fma_f32 v166, |v166|, v226, v166 mul:4\n\t" \
  "v_fma_f32 v173, |v173|, v229, v173 mul:4\n\t" \
  "v_fma_f32 v172, |v172|, v228, v172 mul:4\n\t" \
  "v_mfma_f32_16x16x32_bf16 v[196:199], v[212:215], v[128:131], v[196:199]\n\t" \
  "ds_read_b32 v218, v218 offset:0\n\t" \
  "v_fma_f32 v175, |v175|, v231, v175 mul:4\n\t" \
  "v_fma_f32 v174, |v174|, v230, v174 mul:4\n\t" \
  "v_cvt_pk_bf16_f32 v164, v164, v165\n\t" \
  "v_mfma_f32_16x16x32_bf16 v[204:207], v[212:215], v[140:143], v[204:207]\n\t" \
  "ds_read_b64_tr_b16 v[212:213], v249 offset:8\n\t" \
  "v_cvt_pk_bf16_f32 v165, v166, v167\n\t" \
  "v_cvt_pk_bf16_f32 v172, v172, v173\n\t" \
  "v_cvt_pk_bf16_f32 v173, v174, v175\n\t" \
  "s_setprio 0\n\t" \
  "v_exp_f16 v232, v233\n\t" \
  "v_cvt_pk_f16_f32 v224, |v194|, |v195| clamp\n\t" \
  "v_mfma_f32_16x16x32_bf16 v[48:51], v[242:245], v[236:239], v[48:51]\n\t" \
  "ds_read_b64_tr_b16 v[214:215], v249 offset:1544\n\t" \
  "v_cvt_pk_f16_f32 v234, v192, v193\n\t" \
  "v_cvt_pk_f16_f32 v226, |v202|, |v203| clamp\n\t" \
  "v_mfma_f32_16x16x32_bf16 v[52:55], v[240:243], v[236:239], v[52:55]\n\t" \
  "buffer_load_dwordx4 v[236:239], v250, s[8:11], s29 offen offset:64\n\t" \
  "s_cmp_lg_u32 s58, 0\n\t" \
  "s_cbranch_scc1 .Lcnx_c30\n\t" \
  "s_add_u32 m0, s25, 49200\n\t" \
  "s_add_u32 s24, s24, s26\n\t" \
  "buffer_load_dwordx4 v254, s[8:11], s24 offen lds\n\t" \
  "s_cmp_lg_u32 s38, 4\n\t" \
  "s_cbranch_scc1 .Lcnx_c30\n\t" \
  "s_mov_b32 m0, 163632\n\t" \
  "s_add_u32 s30, s30, s31\n\t" \
  "buffer_load_dword v252, s[8:11], s30 offen lds\n\t" \
  ".Lcnx_c30:\n\t" \
  "ds_read_b32 v221, v221 offset:0\n\t" \
  "v_cvt_pk_f16_f32 v228, |v200|, |v201| clamp\n\t" \
  "v_exp_f16_sdwa v232, v233 dst_sel:WORD_1 dst_unused:UNUSED_PRESERVE src0_sel:WORD_1\n\t" \
  "v_pk_mul_f16 v235, v234, v234\n\t" \
  "s_waitcnt lgkmcnt(5)\n\t" \
  "v_mfma_f32_16x16x32_bf16 v[0:3], v[208:211], v[146:149], v[0:3]\n\t" \
  "ds_read_b32 v220, v220 offset:0\n\t" \
  "v_and_b32_sdwa v225, s56, v224 dst_sel:DWORD dst_unused:UNUSED_PAD src0_sel:DWORD src1_sel:WORD_1\n\t" \
  "v_and_b32 v224, s56, v224\n\t" \
  "v_mfma_f32_16x16x32_bf16 v[24:27], v[208:211], v[154:157], v[24:27]\n\t" \
  "ds_read_b32 v225, v225 offset:0\n\t" \
  "v_and_b32_sdwa v227, s56, v226 dst_sel:DWORD dst_unused:UNUSED_PAD src0_sel:DWORD src1_sel:WORD_1\n\t" \
  "v_mfma_f32_16x16x32_bf16 v[48:51], v[208:211], v[162:165], v[48:51]\n\t" \
  "ds_read_b32 v224, v224 offset:0\n\t" \
  "v_and_b32 v226, s56, v226\n\t" \
  "v_and_b32_sdwa v229, s56, v228 dst_sel:DWORD dst_unused:UNUSED_PAD src0_sel:DWORD src1_sel:WORD_1\n\t" \
  "v_mfma_f32_16x16x32_bf16 v[72:75], v[208:211], v[170:173], v[72:75]\n\t" \
  "ds_read_b64_tr_b16 v[208:209], v249 offset:64\n\t" \
  "v_and_b32 v228, s56, v228\n\t" \
  "s_waitcnt lgkmcnt(5)\n\t" \
  "v_mfma_f32_16x16x32_bf16 v[4:7], v[212:215], v[146:149], v[4:7]\n\t" \
  "ds_read_b64_tr_b16 v[210:211], v249 offset:1600\n\t" \
  "v_pk_fma_f16 v235, v235, v253, s54\n\t" \
  "v_mfma_f32_16x16x32_bf16 v[28:31], v[212:215], v[154:157], v[28:31]\n\t" \
  "ds_read_b32 v227, v227 offset:0\n\t" \
  "v_pk_mul_f16 v235, v234, v235\n\t" \
  "v_fma_f32 v179, |v179|, v217, v179 mul:4\n\t" \
  "v_fma_f32 v178, |v178|, v216, v178 mul:4\n\t" \
  "v_mfma_f32_16x16x32_bf16 v[52:55], v[212:215], v[162:165], v[52:55]\n\t" \
  "ds_read_b32 v226, v226 offset:0\n\t" \
  "v_fma_f32 v187, |v187|, v219, v187 mul:4\n\t" \
  "s_waitcnt lgkmcnt(6)\n\t" \
  "v_fma_f32 v186, |v186|, v218, v186 mul:4\n\t" \
  "v_fma_f32 v185, |v185|, v221, v185 mul:4\n\t" \
  "v_mfma_f32_16x16x32_bf16 v[76:79], v[212:215], v[170:173], v[76:79]\n\t" \
  "ds_read_b64_tr_b16 v[212:213], v249 offset:72\n\t" \
  "v_fma_f32 v184, |v184|, v220, v184 mul:4\n\t" \
  "v_pk_fma_f16 v232, v232, s55, s55\n\t" \
  "s_waitcnt lgkmcnt(3)\n\t" \
  "v_mfma_f32_16x16x32_bf16 v[8:11], v[208:211], v[146:149], v[8:11]\n\t" \
  "ds_read_b64_tr_b16 v[214:215], v249 offset:1608\n\t" \
  "v_cvt_pk_bf16_f32 v179, v178, v179\n\t" \
  "v_cvt_pk_bf16_f32 v187, v186, v187\n\t" \
  "v_cvt_pk_bf16_f32 v186, v184, v185\n\t" \
  "v_mfma_f32_16x16x32_bf16 v[32:35], v[208:211], v[154:157], v[32:35]\n\t" \
  "ds_read_b32 v229, v229 offset:0\n\t" \
  "v_rcp_f16 v233, v232\n\t" \
  "v_exp_f16 v234, v235\n\t" \
  "v_cvt_pk_f16_f32 v216, |v180|, |v181| clamp\n\t" \
  "v_mfma_f32_16x16x32_bf16 v[56:59], v[208:211], v[162:165], v[56:59]\n\t" \
  "ds_read_b32 v228, v228 offset:0\n\t" \
  "s_setprio 1\n\t" \
  "v_cvt_pk_f16_f32 v218, |v182|, |v183| clamp\n\t" \
  "v_cvt_pk_f16_f32 v220, |v188|, |v189| clamp\n\t" \
  "v_mfma_f32_16x16x32_bf16 v[80:83], v[208:211], v[170:173], v[80:83]\n\t" \
  "ds_read_b64_tr_b16 v[208:209], v249 offset:128\n\t" \
  "v_cvt_pk_f16_f32 v222, |v190|, |v191| clamp\n\t" \
  "v_rcp_f16_sdwa v232, v232 dst_sel:DWORD dst_unused:UNUSED_PAD src0_sel:WORD_1\n\t" \
  "v_exp_f16_sdwa v234, v235 dst_sel:WORD_1 dst_unused:UNUSED_PRESERVE src0_sel:WORD_1\n\t" \
  "s_waitcnt lgkmcnt(3)\n\t" \
  "v_mfma_f32_16x16x32_bf16 v[12:15], v[212:215], v[146:149], v[12:15]\n\t" \
  "ds_read_b64_tr_b16 v[210:211], v249 offset:1664\n\t" \
  "v_and_b32_sdwa v217, s56, v216 dst_sel:DWORD dst_unused:UNUSED_PAD src0_sel:DWORD src1_sel:WORD_1\n\t" \
  "v_and_b32 v216, s56, v216\n\t" \
  "v_mfma_f32_16x16x32_bf16 v[36:39], v[212:215], v[154:157], v[36:39]\n\t" \
  "ds_read_b32 v217, v217 offset:0\n\t" \
  "v_and_b32_sdwa v219, s56, v218 dst_sel:DWORD dst_unused:UNUSED_PAD src0_sel:DWORD src1_sel:WORD_1\n\t" \
  "v_mfma_f32_16x16x32_bf16 v[60:63], v[212:215], v[162:165], v[60:63]\n\t" \
  "ds_read_b32 v216, v216 offset:0\n\t" \
  "v_and_b32 v218, s56, v218\n\t" \
  "v_and_b32_sdwa v221, s56, v220 dst_sel:DWORD dst_unused:UNUSED_PAD src0_sel:DWORD src1_sel:WORD_1\n\t" \
  "v_mfma_f32_16x16x32_bf16 v[84:87], v[212:215], v[170:173], v[84:87]\n\t" \
  "ds_read_b64_tr_b16 v[212:213], v249 offset:136\n\t" \
  "v_and_b32 v220, s56, v220\n\t" \
  "s_waitcnt lgkmcnt(3)\n\t" \
  "v_mfma_f32_16x16x32_bf16 v[16:19], v[208:211], v[146:149], v[16:19]\n\t" \
  "ds_read_b64_tr_b16 v[214:215], v249 offset:1672\n\t" \
  "v_and_b32_sdwa v223, s56, v222 dst_sel:DWORD dst_unused:UNUSED_PAD src0_sel:DWORD src1_sel:WORD_1\n\t" \
  "v_mfma_f32_16x16x32_bf16 v[40:43], v[208:211], v[154:157], v[40:43]\n\t" \
  "ds_read_b32 v219, v219 offset:0\n\t" \
  "v_and_b32 v222, s56, v222\n\t" \
  "v_fma_f32 v195, |v195|, v225, v195 mul:4\n\t" \
  "v_mfma_f32_16x16x32_bf16 v[64:67], v[208:211], v[162:165], v[64:67]\n\t" \
  "ds_read_b32 v218, v218 offset:0\n\t" \
  "v_fma_f32 v194, |v194|, v224, v194 mul:4\n\t" \
  "v_fma_f32 v203, |v203|, v227, v203 mul:4\n\t" \
  "v_mfma_f32_16x16x32_bf16 v[88:91], v[208:211], v[170:173], v[88:91]\n\t" \
  "ds_read_b64_tr_b16 v[208:209], v248 offset:43048\n\t" \
  "ds_read_b64_tr_b16 v[210:211], v248 offset:44072\n\t" \
  "v_fma_f32 v202, |v202|, v226, v202 mul:4\n\t" \
  "v_fma_f32 v201, |v201|, v229, v201 mul:4\n\t" \
  "s_waitcnt lgkmcnt(4)\n\t" \
  "v_mfma_f32_16x16x32_bf16 v[20:23], v[212:215], v[146:149], v[20:23]\n\t" \
  "ds_read_b32 v221, v221 offset:0\n\t" \
  "v_fma_f32 v200, |v200|, v228, v200 mul:4\n\t" \
  "v_fma_mix_f32 v176, v176, v233, 0 op_sel_hi:[0,1,0]\n\t" \
  "v_fma_mix_f32 v177, v177, v232, 0 op_sel_hi:[0,1,0]\n\t" \
  "v_mfma_f32_16x16x32_bf16 v[44:47], v[212:215], v[154:157], v[44:47]\n\t" \
  "ds_read_b32 v220, v220 offset:0\n\t" \
  "v_pk_fma_f16 v234, v234, s55, s55\n\t" \
  "v_cvt_pk_bf16_f32 v178, v176, v177\n\t" \
  "v_cvt_pk_bf16_f32 v195, v194, v195\n\t" \
  "v_mfma_f32_16x16x32_bf16 v[68:71], v[212:215], v[162:165], v[68:71]\n\t" \
  "ds_read_b32 v223, v223 offset:0\n\t" \
  "v_cvt_pk_bf16_f32 v203, v202, v203\n\t" \
  "v_cvt_pk_bf16_f32 v202, v200, v201\n\t" \
  "v_rcp_f16 v235, v234\n\t" \
  "v_mfma_f32_16x16x32_bf16 v[92:95], v[212:215], v[170:173], v[92:95]\n\t" \
  "ds_read_b64_tr_b16 v[212:213], v248 offset:46120\n\t" \
  "v_cvt_pk_f16_f32 v224, |v196|, |v197| clamp\n\t" \
  "s_setprio 2\n\t" \
  "v_cvt_pk_f16_f32 v226, |v198|, |v199| clamp\n\t" \
  "s_waitcnt lgkmcnt(4)\n\t" \
  "v_mfma_f32_16x16x32_bf16 v[144:147], v[208:211], v[96:99], 0\n\t" \
  "ds_read_b64_tr_b16 v[214:215], v248 offset:47144\n\t" \
  "v_cvt_pk_f16_f32 v228, |v204|, |v205| clamp\n\t" \
  "v_cvt_pk_f16_f32 v230, |v206|, |v207| clamp\n\t" \
  "v_rcp_f16_sdwa v234, v234 dst_sel:DWORD dst_unused:UNUSED_PAD src0_sel:WORD_1\n\t" \
  "v_mfma_f32_16x16x32_bf16 v[152:155], v[208:211], v[108:111], 0\n\t" \
  "ds_read_b32 v222, v222 offset:0\n\t" \
  "v_and_b32_sdwa v225, s56, v224 dst_sel:DWORD dst_unused:UNUSED_PAD src0_sel:DWORD src1_sel:WORD_1\n\t" \
  "v_and_b32 v224, s56, v224\n\t" \
  "v_mfma_f32_16x16x32_bf16 v[160:163], v[208:211], v[120:123], 0\n\t" \
  "ds_read_b32 v225, v225 offset:0\n\t" \
  "v_and_b32_sdwa v227, s56, v226 dst_sel:DWORD dst_unused:UNUSED_PAD src0_sel:DWORD src1_sel:WORD_1\n\t" \
  "v_mfma_f32_16x16x32_bf16 v[168:171], v[208:211], v[132:135], 0\n\t" \
  "ds_read_b64_tr_b16 v[208:209], v248 offset:45096\n\t" \
  "v_and_b32 v226, s56, v226\n\t" \
  "v_and_b32_sdwa v229, s56, v228 dst_sel:DWORD dst_unused:UNUSED_PAD src0_sel:DWORD src1_sel:WORD_1\n\t" \
  "s_waitcnt lgkmcnt(3)\n\t" \
  "v_mfma_f32_16x16x32_bf16 v[144:147], v[212:215], v[100:103], v[144:147]\n\t" \
  "ds_read_b64_tr_b16 v[210:211], v248 offset:48168\n\t" \
  "v_and_b32 v228, s56, v228\n\t" \
  "v_mfma_f32_16x16x32_bf16 v[152:155], v[212:215], v[112:115], v[152:155]\n\t" \
  "ds_read_b32 v224, v224 offset:0\n\t" \
  "v_and_b32_sdwa v231, s56, v230 dst_sel:DWORD dst_unused:UNUSED_PAD src0_sel:DWORD src1_sel:WORD_1\n\t" \
  "v_mfma_f32_16x16x32_bf16 v[160:163], v[212:215], v[124:127], v[160:163]\n\t" \
  "ds_read_b32 v227, v227 offset:0\n\t" \
  "v_and_b32 v230, s56, v230\n\t" \
  "v_fma_f32 v181, |v181|, v217, v181 mul:4\n\t" \
  "v_mfma_f32_16x16x32_bf16 v[168:171], v[212:215], v[136:139], v[168:171]\n\t" \
  "ds_read_b64_tr_b16 v[212:213], v248 offset:43560\n\t" \
  "ds_read_b64_tr_b16 v[214:215], v248 offset:44584\n\t" \
  "v_fma_f32 v180, |v180|, v216, v180 mul:4\n\t" \
  "v_fma_f32 v183, |v183|, v219, v183 mul:4\n\t" \
  "s_waitcnt lgkmcnt(4)\n\t" \
  "v_mfma_f32_16x16x32_bf16 v[144:147], v[208:211], v[104:107], v[144:147]\n\t" \
  "ds_read_b32 v226, v226 offset:0\n\t" \
  "v_fma_f32 v182, |v182|, v218, v182 mul:4\n\t" \
  "v_fma_f32 v189, |v189|, v221, v189 mul:4\n\t" \
  "v_mfma_f32_16x16x32_bf16 v[152:155], v[208:211], v[116:119], v[152:155]\n\t" \
  "ds_read_b32 v229, v229 offset:0\n\t" \
  "v_fma_f32 v188, |v188|, v220, v188 mul:4\n\t" \
  "v_fma_f32 v191, |v191|, v223, v191 mul:4\n\t" \
  "v_fma_f32 v190, |v190|, v222, v190 mul:4\n\t" \
  "v_mfma_f32_16x16x32_bf16 v[160:163], v[208:211], v[128:131], v[160:163]\n\t" \
  "ds_read_b32 v228, v228 offset:0\n\t" \
  "v_fma_mix_f32 v192, v192, v235, 0 op_sel_hi:[0,1,0]\n\t" \
  "v_fma_mix_f32 v193, v193, v234, 0 op_sel_hi:[0,1,0]\n\t" \
  "v_cvt_pk_bf16_f32 v194, v192, v193\n\t" \
  "v_mfma_f32_16x16x32_bf16 v[168:171], v[208:211], v[140:143], v[168:171]\n\t" \
  "ds_read_b64_tr_b16 v[208:209], v248 offset:46632\n\t" \
  "v_cvt_pk_bf16_f32 v180, v180, v181\n\t" \
  "v_cvt_pk_bf16_f32 v181, v182, v183\n\t" \
  "s_waitcnt lgkmcnt(4)\n\t" \
  "v_mfma_f32_16x16x32_bf16 v[148:151], v[212:215], v[96:99], 0\n\t" \
  "ds_read_b64_tr_b16 v[210:211], v248 offset:47656\n\t" \
  "v_cvt_pk_bf16_f32 v188, v188, v189\n\t" \
  "v_cvt_pk_bf16_f32 v189, v190, v191\n\t" \
  "s_setprio 3\n\t" \
  "v_cvt_pk_f16_f32 v216, |v146|, |v147| clamp\n\t" \
  "v_mfma_f32_16x16x32_bf16 v[156:159], v[212:215], v[108:111], 0\n\t" \
  "ds_read_b32 v231, v231 offset:0\n\t" \
  "v_cvt_pk_f16_f32 v232, v144, v145\n\t" \
  "v_cvt_pk_f16_f32 v218, |v154|, |v155| clamp\n\t" \
  "v_cvt_pk_f16_f32 v220, |v152|, |v153| clamp\n\t" \
  "v_mfma_f32_16x16x32_bf16 v[164:167], v[212:215], v[120:123], 0\n\t" \
  "ds_read_b32 v230, v230 offset:0\n\t" \
  "v_pk_mul_f16 v233, v232, v232\n\t" \
  "v_and_b32_sdwa v217, s56, v216 dst_sel:DWORD dst_unused:UNUSED_PAD src0_sel:DWORD src1_sel:WORD_1\n\t" \
  "v_mfma_f32_16x16x32_bf16 v[172:175], v[212:215], v[132:135], 0\n\t" \
  "ds_read_b64_tr_b16 v[212:213], v248 offset:45608\n\t" \
  "v_and_b32 v216, s56, v216\n\t" \
  "v_and_b32_sdwa v219, s56, v218 dst_sel:DWORD dst_unused:UNUSED_PAD src0_sel:DWORD src1_sel:WORD_1\n\t" \
  "s_waitcnt lgkmcnt(3)\n\t" \
  "v_mfma_f32_16x16x32_bf16 v[148:151], v[208:211], v[100:103], v[148:151]\n\t" \
  "ds_read_b64_tr_b16 v[214:215], v248 offset:48680\n\t" \
  "v_and_b32 v218, s56, v218\n\t" \
  "v_mfma_f32_16x16x32_bf16 v[156:159], v[208:211], v[112:115], v[156:159]\n\t" \
  "s_waitcnt vmcnt(1)\n\t" \
  "s_cmp_lg_u32 s58, 1\n\t" \
  "s_cbranch_scc1 .Lcnx_c18\n\t" \
  "s_waitcnt vmcnt(0)\n\t" \
  ".Lcnx_c18:\n\t" \
  "s_mov_b64 exec, 3\n\t" \
  "ds_add_rtn_u32 v246, v251, v250 offset:47720\n\t" \
  "s_mov_b64 exec, -1\n\t" \
  "ds_read_b32 v217, v217 offset:0\n\t" \
  "v_and_b32_sdwa v221, s56, v220 dst_sel:DWORD dst_unused:UNUSED_PAD src0_sel:DWORD src1_sel:WORD_1\n\t" \
  "v_mfma_f32_16x16x32_bf16 v[164:167], v[208:211], v[124:127], v[164:167]\n\t" \
  "ds_read_b32 v216, v216 offset:0\n\t" \
  "v_and_b32 v220, s56, v220\n\t" \
  "v_pk_fma_f16 v233, v233, v253, s54\n\t" \
  "v_mfma_f32_16x16x32_bf16 v[172:175], v[208:211], v[136:139], v[172:175]\n\t" \
  "s_waitcnt lgkmcnt(2)\n\t" \
  "v_readfirstlane_b32 s4, v246\n\t" \
  "s_cmp_lt_u32 s4, s57\n\t" \
  "s_cbranch_scc1 .Lcnx_w96\n.Lcnx_b96:\n\t" \
  "s_cmp_lg_u32 s58, 1\n\t" \
  "s_cselect_b32 s57, s57, 0\n\t" \
  "ds_read_b64_tr_b16 v[208:209], v249 offset:6152\n\t" \
  "v_pk_mul_f16 v233, v232, v233\n\t" \
  "v_fma_f32 v197, |v197|, v225, v197 mul:4\n\t" \
  "v_mfma_f32_16x16x32_bf16 v[148:151], v[212:215], v[104:107], v[148:151]\n\t" \
  "ds_read_b64_tr_b16 v[210:211], v249 offset:7688\n\t" \
  "v_fma_f32 v196, |v196|, v224, v196 mul:4\n\t" \
  "v_fma_f32 v199, |v199|, v227, v199 mul:4\n\t" \
  "v_mfma_f32_16x16x32_bf16 v[156:159], v[212:215], v[116:119], v[156:159]\n\t" \
  "s_cmp_eq_u32 s37, 0\n\t" \
  "s_cbranch_scc1 .Lcnx_f9\n.Lcnx_g9:\n\t" \
  "ds_read_b32 v219, v219 offset:0\n\t" \
  "v_fma_f32 v198, |v198|, v226, v198 mul:4\n\t" \
  "v_fma_f32 v205, |v205|, v229, v205 mul:4\n\t" \
  "v_fma_f32 v204, |v204|, v228, v204 mul:4\n\t" \
  "v_mfma_f32_16x16x32_bf16 v[164:167], v[212:215], v[128:131], v[164:167]\n\t" \
  "ds_read_b32 v218, v218 offset:0\n\t" \
  "v_fma_f32 v207, |v207|, v231, v207 mul:4\n\t" \
  "v_fma_f32 v206, |v206|, v230, v206 mul:4\n\t" \
  "v_cvt_pk_bf16_f32 v196, v196, v197\n\t" \
  "v_mfma_f32_16x16x32_bf16 v[172:175], v[212:215], v[140:143], v[172:175]\n\t" \
  "ds_read_b64_tr_b16 v[212:213], v249 offset:6160\n\t" \
  "v_cvt_pk_bf16_f32 v197, v198, v199\n\t" \
  "v_cvt_pk_bf16_f32 v204, v204, v205\n\t" \
  "v_cvt_pk_bf16_f32 v205, v206, v207\n\t" \
  "s_cmp_lg_u32 s58, 1\n\t" \
  "s_cbranch_scc1 .Lcnx_c19\n\t" \
  "s_mov_b64 vcc, -1\n\t" \
  "s_setprio 0\n\t" \
  "s_cmp_ge_u32 s41, 2048\n\t" \
  "s_cbranch_scc1 .Lcnx_c19\n\t" \
  "s_setprio 3\n\t" \
  ".Lcnx_c19:\n\t" \
  "s_cbranch_vccnz .Lcnx_p9_0\n\ts_setprio 0\n.Lcnx_p9_0:\n\t" \
  "v_exp_f16 v232, v233\n\t" \
  "v_cvt_pk_f16_f32 v224, |v162|, |v163| clamp\n\t" \
  "v_mfma_f32_16x16x32_bf16 v[56:59], v[242:245], v[236:239], v[56:59]\n\t" \
  "ds_read_b64_tr_b16 v[214:215], v249 offset:7696\n\t" \
  "v_cvt_pk_f16_f32 v234, v160, v161\n\t" \
  "v_cvt_pk_f16_f32 v226, |v170|, |v171| clamp\n\t" \
  "v_mfma_f32_16x16x32_bf16 v[60:63], v[240:243], v[236:239], v[60:63]\n\t" \
  "buffer_load_dwordx4 v[236:239], v250, s[8:11], s29 offen offset:128\n\t" \
  "s_cmp_lg_u32 s58, 0\n\t" \
  "s_cbranch_scc1 .Lcnx_c31\n\t" \
  "s_add_u32 m0, s25, 57400\n\t" \
  "s_add_u32 s24, s24, s26\n\t" \
  "buffer_load_dwordx4 v254, s[8:11], s24 offen lds\n\t" \
  "s_cmp_lg_u32 s38, 5\n\t" \
  "s_cbranch_scc1 .Lcnx_c31\n\t" \
  "s_mov_b32 m0, 163632\n\t" \
  "s_add_u32 s30, s30, s31\n\t" \
  "buffer_load_dword v252, s[8:11], s30 offen lds\n\t" \
  ".Lcnx_c31:\n\t" \
  "ds_read_b32 v221, v221 offset:0\n\t" \
  "v_cvt_pk_f16_f32 v228, |v168|, |v169| clamp\n\t" \
  "v_exp_f16_sdwa v232, v233 dst_sel:WORD_1 dst_unused:UNUSED_PRESERVE src0_sel:WORD_1\n\t" \
  "v_pk_mul_f16 v235, v234, v234\n\t" \
  "s_waitcnt lgkmcnt(5)\n\t" \
  "v_mfma_f32_16x16x32_bf16 v[0:3], v[208:211], v[178:181], v[0:3]\n\t" \
  "ds_read_b32 v220, v220 offset:0\n\t" \
  "v_and_b32_sdwa v225, s56, v224 dst_sel:DWORD dst_unused:UNUSED_PAD src0_sel:DWORD src1_sel:WORD_1\n\t" \
  "v_and_b32 v224, s56, v224\n\t" \
  "v_mfma_f32_16x16x32_bf16 v[24:27], v[208:211], v[186:189], v[24:27]\n\t" \
  "ds_read_b32 v225, v225 offset:0\n\t" \
  "v_and_b32_sdwa v227, s56, v226 dst_sel:DWORD dst_unused:UNUSED_PAD src0_sel:DWORD src1_sel:WORD_1\n\t" \
  "v_mfma_f32_16x16x32_bf16 v[48:51], v[208:211], v[194:197], v[48:51]\n\t" \
  "ds_read_b32 v224, v224 offset:0\n\t" \
  "v_and_b32 v226, s56, v226\n\t" \
  "v_and_b32_sdwa v229, s56, v228 dst_sel:DWORD dst_unused:UNUSED_PAD src0_sel:DWORD src1_sel:WORD_1\n\t" \
  "v_mfma_f32_16x16x32_bf16 v[72:75], v[208:211], v[202:205], v[72:75]\n\t" \
  "ds_read_b64_tr_b16 v[208:209], v249 offset:6216\n\t" \
  "v_and_b32 v228, s56, v228\n\t" \
  "s_waitcnt lgkmcnt(5)\n\t" \
  "v_mfma_f32_16x16x32_bf16 v[4:7], v[212:215], v[178:181], v[4:7]\n\t" \
  "ds_read_b64_tr_b16 v[210:211], v249 offset:7752\n\t" \
  "v_pk_fma_f16 v235, v235, v253, s54\n\t" \
  "v_mfma_f32_16x16x32_bf16 v[28:31], v[212:215], v[186:189], v[28:31]\n\t" \
  "ds_read_b32 v227, v227 offset:0\n\t" \
  "v_pk_mul_f16 v235, v234, v235\n\t" \
  "v_fma_f32 v147, |v147|, v217, v147 mul:4\n\t" \
  "v_fma_f32 v146, |v146|, v216, v146 mul:4\n\t" \
  "v_mfma_f32_16x16x32_bf16 v[52:55], v[212:215], v[194:197], v[52:55]\n\t" \
  "ds_read_b32 v226, v226 offset:0\n\t" \
  "v_fma_f32 v155, |v155|, v219, v155 mul:4\n\t" \
  "s_waitcnt lgkmcnt(6)\n\t" \
  "v_fma_f32 v154, |v154|, v218, v154 mul:4\n\t" \
  "v_fma_f32 v153, |v153|, v221, v153 mul:4\n\t" \
  "v_mfma_f32_16x16x32_bf16 v[76:79], v[212:215], v[202:205], v[76:79]\n\t" \
  "ds_read_b64_tr_b16 v[212:213], v249 offset:6224\n\t" \
  "v_fma_f32 v152, |v152|, v220, v152 mul:4\n\t" \
  "v_pk_fma_f16 v232, v232, s55, s55\n\t" \
  "s_waitcnt lgkmcnt(3)\n\t" \
  "v_mfma_f32_16x16x32_bf16 v[8:11], v[208:211], v[178:181], v[8:11]\n\t" \
  "ds_read_b64_tr_b16 v[214:215], v249 offset:7760\n\t" \
  "v_cvt_pk_bf16_f32 v147, v146, v147\n\t" \
  "v_cvt_pk_bf16_f32 v155, v154, v155\n\t" \
  "v_cvt_pk_bf16_f32 v154, v152, v153\n\t" \
  "v_mfma_f32_16x16x32_bf16 v[32:35], v[208:211], v[186:189], v[32:35]\n\t" \
  "ds_read_b32 v229, v229 offset:0\n\t" \
  "v_rcp_f16 v233, v232\n\t" \
  "v_exp_f16 v234, v235\n\t" \
  "v_cvt_pk_f16_f32 v216, |v148|, |v149| clamp\n\t" \
  "v_mfma_f32_16x16x32_bf16 v[56:59], v[208:211], v[194:197], v[56:59]\n\t" \
  "ds_read_b32 v228, v228 offset:0\n\t" \
  "s_cbranch_vccnz .Lcnx_p9_1\n\ts_setprio 1\n.Lcnx_p9_1:\n\t" \
  "v_cvt_pk_f16_f32 v218, |v150|, |v151| clamp\n\t" \
  "v_cvt_pk_f16_f32 v220, |v156|, |v157| clamp\n\t" \
  "v_mfma_f32_16x16x32_bf16 v[80:83], v[208:211], v[202:205], v[80:83]\n\t" \
  "ds_read_b64_tr_b16 v[208:209], v249 offset:6280\n\t" \
  "v_cvt_pk_f16_f32 v222, |v158|, |v159| clamp\n\t" \
  "v_rcp_f16_sdwa v232, v232 dst_sel:DWORD dst_unused:UNUSED_PAD src0_sel:WORD_1\n\t" \
  "v_exp_f16_sdwa v234, v235 dst_sel:WORD_1 dst_unused:UNUSED_PRESERVE src0_sel:WORD_1\n\t" \
  "s_waitcnt lgkmcnt(3)\n\t" \
  "v_mfma_f32_16x16x32_bf16 v[12:15], v[212:215], v[178:181], v[12:15]\n\t" \
  "ds_read_b64_tr_b16 v[210:211], v249 offset:7816\n\t" \
  "v_and_b32_sdwa v217, s56, v216 dst_sel:DWORD dst_unused:UNUSED_PAD src0_sel:DWORD src1_sel:WORD_1\n\t" \
  "v_and_b32 v216, s56, v216\n\t" \
  "v_mfma_f32_16x16x32_bf16 v[36:39], v[212:215], v[186:189], v[36:39]\n\t" \
  "ds_read_b32 v217, v217 offset:0\n\t" \
  "v_and_b32_sdwa v219, s56, v218 dst_sel:DWORD dst_unused:UNUSED_PAD src0_sel:DWORD src1_sel:WORD_1\n\t" \
  "v_mfma_f32_16x16x32_bf16 v[60:63], v[212:215], v[194:197], v[60:63]\n\t" \
  "ds_read_b32 v216, v216 offset:0\n\t" \
  "v_and_b32 v218, s56, v218\n\t" \
  "v_and_b32_sdwa v221, s56, v220 dst_sel:DWORD dst_unused:UNUSED_PAD src0_sel:DWORD src1_sel:WORD_1\n\t" \
  "v_mfma_f32_16x16x32_bf16 v[84:87], v[212:215], v[202:205], v[84:87]\n\t" \
  "ds_read_b64_tr_b16 v[212:213], v249 offset:6288\n\t" \
  "v_and_b32 v220, s56, v220\n\t" \
  "s_waitcnt lgkmcnt(3)\n\t" \
  "v_mfma_f32_16x16x32_bf16 v[16:19], v[208:211], v[178:181], v[16:19]\n\t" \
  "ds_read_b64_tr_b16 v[214:215], v249 offset:7824\n\t" \
  "v_and_b32_sdwa v223, s56, v222 dst_sel:DWORD dst_unused:UNUSED_PAD src0_sel:DWORD src1_sel:WORD_1\n\t" \
  "v_mfma_f32_16x16x32_bf16 v[40:43], v[208:211], v[186:189], v[40:43]\n\t" \
  "ds_read_b32 v219, v219 offset:0\n\t" \
  "v_and_b32 v222, s56, v222\n\t" \
  "v_fma_f32 v163, |v163|, v225, v163 mul:4\n\t" \
  "v_mfma_f32_16x16x32_bf16 v[64:67], v[208:211], v[194:197], v[64:67]\n\t" \
  "ds_read_b32 v218, v218 offset:0\n\t" \
  "v_fma_f32 v162, |v162|, v224, v162 mul:4\n\t" \
  "v_fma_f32 v171, |v171|, v227, v171 mul:4\n\t" \
  "v_mfma_f32_16x16x32_bf16 v[88:91], v[208:211], v[202:205], v[88:91]\n\t" \
  "ds_read_b64_tr_b16 v[208:209], v248 offset:49192\n\t" \
  "ds_read_b64_tr_b16 v[210:211], v248 offset:50216\n\t" \
  "v_fma_f32 v170, |v170|, v226, v170 mul:4\n\t" \
  "v_fma_f32 v169, |v169|, v229, v169 mul:4\n\t" \
  "s_waitcnt lgkmcnt(4)\n\t" \
  "v_mfma_f32_16x16x32_bf16 v[20:23], v[212:215], v[178:181], v[20:23]\n\t" \
  "ds_read_b32 v221, v221 offset:0\n\t" \
  "v_fma_f32 v168, |v168|, v228, v168 mul:4\n\t" \
  "v_fma_mix_f32 v144, v144, v233, 0 op_sel_hi:[0,1,0]\n\t" \
  "v_fma_mix_f32 v145, v145, v232, 0 op_sel_hi:[0,1,0]\n\t" \
  "v_mfma_f32_16x16x32_bf16 v[44:47], v[212:215], v[186:189], v[44:47]\n\t" \
  "ds_read_b32 v220, v220 offset:0\n\t" \
  "v_pk_fma_f16 v234, v234, s55, s55\n\t" \
  "v_cvt_pk_bf16_f32 v146, v144, v145\n\t" \
  "v_cvt_pk_bf16_f32 v163, v162, v163\n\t" \
  "v_mfma_f32_16x16x32_bf16 v[68:71], v[212:215], v[194:197], v[68:71]\n\t" \
  "ds_read_b32 v223, v223 offset:0\n\t" \
  "v_cvt_pk_bf16_f32 v171, v170, v171\n\t" \
  "v_cvt_pk_bf16_f32 v170, v168, v169\n\t" \
  "v_rcp_f16 v235, v234\n\t" \
  "v_mfma_f32_16x16x32_bf16 v[92:95], v[212:215], v[202:205], v[92:95]\n\t" \
  "ds_read_b64_tr_b16 v[212:213], v248 offset:52264\n\t" \
  "v_cvt_pk_f16_f32 v224, |v164|, |v165| clamp\n\t" \
  "s_cbranch_vccnz .Lcnx_p9_2\n\ts_setprio 2\n.Lcnx_p9_2:\n\t" \
  "v_cvt_pk_f16_f32 v226, |v166|, |v167| clamp\n\t" \
  "s_waitcnt lgkmcnt(4)\n\t" \
  "v_mfma_f32_16x16x32_bf16 v[176:179], v[208:211], v[96:99], 0\n\t" \
  "ds_read_b64_tr_b16 v[214:215], v248 offset:53288\n\t" \
  "v_cvt_pk_f16_f32 v228, |v172|, |v173| clamp\n\t" \
  "v_cvt_pk_f16_f32 v230, |v174|, |v175| clamp\n\t" \
  "v_rcp_f16_sdwa v234, v234 dst_sel:DWORD dst_unused:UNUSED_PAD src0_sel:WORD_1\n\t" \
  "v_mfma_f32_16x16x32_bf16 v[184:187], v[208:211], v[108:111], 0\n\t" \
  "ds_read_b32 v222, v222 offset:0\n\t" \
  "v_and_b32_sdwa v225, s56, v224 dst_sel:DWORD dst_unused:UNUSED_PAD src0_sel:DWORD src1_sel:WORD_1\n\t" \
  "v_and_b32 v224, s56, v224\n\t" \
  "v_mfma_f32_16x16x32_bf16 v[192:195], v[208:211], v[120:123], 0\n\t" \
  "ds_read_b32 v225, v225 offset:0\n\t" \
  "v_and_b32_sdwa v227, s56, v226 dst_sel:DWORD dst_unused:UNUSED_PAD src0_sel:DWORD src1_sel:WORD_1\n\t" \
  "v_mfma_f32_16x16x32_bf16 v[200:203], v[208:211], v[132:135], 0\n\t" \
  "ds_read_b64_tr_b16 v[208:209], v248 offset:51240\n\t" \
  "v_and_b32 v226, s56, v226\n\t" \
  "v_and_b32_sdwa v229, s56, v228 dst_sel:DWORD dst_unused:UNUSED_PAD src0_sel:DWORD src1_sel:WORD_1\n\t" \
  "s_waitcnt lgkmcnt(3)\n\t" \
  "v_mfma_f32_16x16x32_bf16 v[176:179], v[212:215], v[100:103], v[176:179]\n\t" \
  "ds_read_b64_tr_b16 v[210:211], v248 offset:54312\n\t" \
  "v_and_b32 v228, s56, v228\n\t" \
  "v_mfma_f32_16x16x32_bf16 v[184:187], v[212:215], v[112:115], v[184:187]\n\t" \
  "ds_read_b32 v224, v224 offset:0\n\t" \
  "v_and_b32_sdwa v231, s56, v230 dst_sel:DWORD dst_unused:UNUSED_PAD src0_sel:DWORD src1_sel:WORD_1\n\t" \
  "v_mfma_f32_16x16x32_bf16 v[192:195], v[212:215], v[124:127], v[192:195]\n\t" \
  "ds_read_b32 v227, v227 offset:0\n\t" \
  "v_and_b32 v230, s56, v230\n\t" \
  "v_fma_f32 v149, |v149|, v217, v149 mul:4\n\t" \
  "v_mfma_f32_16x16x32_bf16 v[200:203], v[212:215], v[136:139], v[200:203]\n\t" \
  "ds_read_b64_tr_b16 v[212:213], v248 offset:49704\n\t" \
  "ds_read_b64_tr_b16 v[214:215], v248 offset:50728\n\t" \
  "v_fma_f32 v148, |v148|, v216, v148 mul:4\n\t" \
  "v_fma_f32 v151, |v151|, v219, v151 mul:4\n\t" \
  "s_waitcnt lgkmcnt(4)\n\t" \
  "v_mfma_f32_16x16x32_bf16 v[176:179], v[208:211], v[104:107], v[176:179]\n\t" \
  "ds_read_b32 v226, v226 offset:0\n\t" \
  "v_fma_f32 v150, |v150|, v218, v150 mul:4\n\t" \
  "v_fma_f32 v157, |v157|, v221, v157 mul:4\n\t" \
  "v_mfma_f32_16x16x32_bf16 v[184:187], v[208:211], v[116:119], v[184:187]\n\t" \
  "ds_read_b32 v229, v229 offset:0\n\t" \
  "v_fma_f32 v156, |v156|, v220, v156 mul:4\n\t" \
  "v_fma_f32 v159, |v159|, v223, v159 mul:4\n\t" \
  "v_fma_f32 v158, |v158|, v222, v158 mul:4\n\t" \
  "v_mfma_f32_16x16x32_bf16 v[192:195], v[208:211], v[128:131], v[192:195]\n\t" \
  "ds_read_b32 v228, v228 offset:0\n\t" \
  "v_fma_mix_f32 v160, v160, v235, 0 op_sel_hi:[0,1,0]\n\t" \
  "v_fma_mix_f32 v161, v161, v234, 0 op_sel_hi:[0,1,0]\n\t" \
  "v_cvt_pk_bf16_f32 v162, v160, v161\n\t" \
  "v_mfma_f32_16x16x32_bf16 v[200:203], v[208:211], v[140:143], v[200:203]\n\t" \
  "ds_read_b64_tr_b16 v[208:209], v248 offset:52776\n\t" \
  "v_cvt_pk_bf16_f32 v148, v148, v149\n\t" \
  "v_cvt_pk_bf16_f32 v149, v150, v151\n\t" \
  "s_waitcnt lgkmcnt(4)\n\t" \
  "v_mfma_f32_16x16x32_bf16 v[180:183], v[212:215], v[96:99], 0\n\t" \
  "ds_read_b64_tr_b16 v[210:211], v248 offset:53800\n\t" \
  "v_cvt_pk_bf16_f32 v156, v156, v157\n\t" \
  "v_cvt_pk_bf16_f32 v157, v158, v159\n\t" \
  "s_cbranch_vccnz .Lcnx_p9_3\n\ts_setprio 3\n.Lcnx_p9_3:\n\t" \
  "v_cvt_pk_f16_f32 v216, |v178|, |v179| clamp\n\t" \
  "v_mfma_f32_16x16x32_bf16 v[188:191], v[212:215], v[108:111], 0\n\t" \
  "ds_read_b32 v231, v231 offset:0\n\t" \
  "v_cvt_pk_f16_f32 v232, v176, v177\n\t" \
  "v_cvt_pk_f16_f32 v218, |v186|, |v187| clamp\n\t" \
  "v_cvt_pk_f16_f32 v220, |v184|, |v185| clamp\n\t" \
  "v_mfma_f32_16x16x32_bf16 v[196:199], v[212:215], v[120:123], 0\n\t" \
  "ds_read_b32 v230, v230 offset:0\n\t" \
  "v_pk_mul_f16 v233, v232, v232\n\t" \
  "v_and_b32_sdwa v217, s56, v216 dst_sel:DWORD dst_unused:UNUSED_PAD src0_sel:DWORD src1_sel:WORD_1\n\t" \
  "v_mfma_f32_16x16x32_bf16 v[204:207], v[212:215], v[132:135], 0\n\t" \
  "ds_read_b64_tr_b16 v[212:213], v248 offset:51752\n\t" \
  "v_and_b32 v216, s56, v216\n\t" \
  "v_and_b32_sdwa v219, s56, v218 dst_sel:DWORD dst_unused:UNUSED_PAD src0_sel:DWORD src1_sel:WORD_1\n\t" \
  "s_waitcnt lgkmcnt(3)\n\t" \
  "v_mfma_f32_16x16x32_bf16 v[180:183], v[208:211], v[100:103], v[180:183]\n\t" \
  "ds_read_b64_tr_b16 v[214:215], v248 offset:54824\n\t" \
  "v_and_b32 v218, s56, v218\n\t" \
  "v_mfma_f32_16x16x32_bf16 v[188:191], v[208:211], v[112:115], v[188:191]\n\t" \
  "s_waitcnt vmcnt(1)\n\t" \
  "s_cmp_lg_u32 s58, 1\n\t" \
  "s_cbranch_scc1 .Lcnx_c20\n\t" \
  "s_waitcnt vmcnt(0)\n\t" \
  ".Lcnx_c20:\n\t" \
  "s_mov_b64 exec, 3\n\t" \
  "ds_add_rtn_u32 v246, v251, v250 offset:47728\n\t" \
  "s_mov_b64 exec, -1\n\t" \
  "ds_read_b32 v217, v217 offset:0\n\t" \
  "v_and_b32_sdwa v221, s56, v220 dst_sel:DWORD dst_unused:UNUSED_PAD src0_sel:DWORD src1_sel:WORD_1\n\t" \
  "v_mfma_f32_16x16x32_bf16 v[196:199], v[208:211], v[124:127], v[196:199]\n\t" \
  "ds_read_b32 v216, v216 offset:0\n\t" \
  "v_and_b32 v220, s56, v220\n\t" \
  "v_pk_fma_f16 v233, v233, v253, s54\n\t" \
  "v_mfma_f32_16x16x32_bf16 v[204:207], v[208:211], v[136:139], v[204:207]\n\t" \
  "s_waitcnt lgkmcnt(2)\n\t" \
  "v_readfirstlane_b32 s4, v246\n\t" \
  "s_cmp_lt_u32 s4, s57\n\t" \
  "s_cbranch_scc1 .Lcnx_w108\n.Lcnx_b108:\n\t" \
  "v_add_u32 v248, s59, v248\n\t" \
  "v_add_u32 v249, s60, v249\n\t" \
  "ds_read_b64_tr_b16 v[208:209], v249 offset:24600\n\t" \
  "v_pk_mul_f16 v233, v232, v233\n\t" \
  "v_fma_f32 v165, |v165|, v225, v165 mul:4\n\t" \
  "v_mfma_f32_16x16x32_bf16 v[180:183], v[212:215], v[104:107], v[180:183]\n\t" \
  "ds_read_b64_tr_b16 v[210:211], v249 offset:26136\n\t" \
  "v_fma_f32 v164, |v164|, v224, v164 mul:4\n\t" \
  "v_fma_f32 v167, |v167|, v227, v167 mul:4\n\t" \
  "v_mfma_f32_16x16x32_bf16 v[188:191], v[212:215], v[116:119], v[188:191]\n\t" \
  "s_cmp_eq_u32 s37, 0\n\t" \
  "s_cbranch_scc1 .Lcnx_f10\n.Lcnx_g10:\n\t" \
  "ds_read_b32 v219, v219 offset:0\n\t" \
  "v_fma_f32 v166, |v166|, v226, v166 mul:4\n\t" \
  "v_fma_f32 v173, |v173|, v229, v173 mul:4\n\t" \
  "v_fma_f32 v172, |v172|, v228, v172 mul:4\n\t" \
  "v_mfma_f32_16x16x32_bf16 v[196:199], v[212:215], v[128:131], v[196:199]\n\t" \
  "ds_read_b32 v218, v218 offset:0\n\t" \
  "v_fma_f32 v175, |v175|, v231, v175 mul:4\n\t" \
  "v_fma_f32 v174, |v174|, v230, v174 mul:4\n\t" \
  "v_cvt_pk_bf16_f32 v164, v164, v165\n\t" \
  "v_mfma_f32_16x16x32_bf16 v[204:207], v[212:215], v[140:143], v[204:207]\n\t" \
  "ds_read_b64_tr_b16 v[212:213], v249 offset:24608\n\t" \
  "v_cvt_pk_bf16_f32 v165, v166, v167\n\t" \
  "v_cvt_pk_bf16_f32 v172, v172, v173\n\t" \
  "v_cvt_pk_bf16_f32 v173, v174, v175\n\t" \
  "s_cbranch_vccnz .Lcnx_p10_0\n\ts_setprio 0\n.Lcnx_p10_0:\n\t" \
  "v_exp_f16 v232, v233\n\t" \
  "v_cvt_pk_f16_f32 v224, |v194|, |v195| clamp\n\t" \
  "v_mfma_f32_16x16x32_bf16 v[64:67], v[242:245], v[236:239], v[64:67]\n\t" \
  "ds_read_b64_tr_b16 v[214:215], v249 offset:26144\n\t" \
  "v_cvt_pk_f16_f32 v234, v192, v193\n\t" \
  "v_cvt_pk_f16_f32 v226, |v202|, |v203| clamp\n\t" \
  "v_mfma_f32_16x16x32_bf16 v[68:71], v[240:243], v[236:239], v[68:71]\n\t" \
  "buffer_load_dwordx4 v[236:239], v250, s[8:11], s29 offen offset:192\n\t" \
  "s_cmp_lg_u32 s58, 0\n\t" \
  "s_cbranch_scc1 .Lcnx_c32\n\t" \
  "s_add_u32 m0, s25, 65600\n\t" \
  "s_add_u32 s24, s24, s26\n\t" \
  "buffer_load_dwordx4 v254, s[8:11], s24 offen lds\n\t" \
  "s_cmp_lg_u32 s38, 6\n\t" \
  "s_cbranch_scc1 .Lcnx_c32\n\t" \
  "s_mov_b32 m0, 163632\n\t" \
  "s_add_u32 s30, s30, s31\n\t" \
  "buffer_load_dword v252, s[8:11], s30 offen lds\n\t" \
  ".Lcnx_c32:\n\t" \
  "ds_read_b32 v221, v221 offset:0\n\t" \
  "v_cvt_pk_f16_f32 v228, |v200|, |v201| clamp\n\t" \
  "v_exp_f16_sdwa v232, v233 dst_sel:WORD_1 dst_unused:UNUSED_PRESERVE src0_sel:WORD_1\n\t" \
  "v_pk_mul_f16 v235, v234, v234\n\t" \
  "s_waitcnt lgkmcnt(5)\n\t" \
  "v_mfma_f32_16x16x32_bf16 v[0:3], v[208:211], v[146:149], v[0:3]\n\t" \
  "ds_read_b32 v220, v220 offset:0\n\t" \
  "v_and_b32_sdwa v225, s56, v224 dst_sel:DWORD dst_unused:UNUSED_PAD src0_sel:DWORD src1_sel:WORD_1\n\t" \
  "v_and_b32 v224, s56, v224\n\t" \
  "v_mfma_f32_16x16x32_bf16 v[24:27], v[208:211], v[154:157], v[24:27]\n\t" \
  "ds_read_b32 v225, v225 offset:0\n\t" \
  "v_and_b32_sdwa v227, s56, v226 dst_sel:DWORD dst_unused:UNUSED_PAD src0_sel:DWORD src1_sel:WORD_1\n\t" \
  "v_mfma_f32_16x16x32_bf16 v[48:51], v[208:211], v[162:165], v[48:51]\n\t" \
  "ds_read_b32 v224, v224 offset:0\n\t" \
  "v_and_b32 v226, s56, v226\n\t" \
  "v_and_b32_sdwa v229, s56, v228 dst_sel:DWORD dst_unused:UNUSED_PAD src0_sel:DWORD src1_sel:WORD_1\n\t" \
  "v_mfma_f32_16x16x32_bf16 v[72:75], v[208:211], v[170:173], v[72:75]\n\t" \
  "ds_read_b64_tr_b16 v[208:209], v249 offset:24664\n\t" \
  "v_and_b32 v228, s56, v228\n\t" \
  "s_waitcnt lgkmcnt(5)\n\t" \
  "v_mfma_f32_16x16x32_bf16 v[4:7], v[212:215], v[146:149], v[4:7]\n\t" \
  "ds_read_b64_tr_b16 v[210:211], v249 offset:26200\n\t" \
  "v_pk_fma_f16 v235, v235, v253, s54\n\t" \
  "v_mfma_f32_16x16x32_bf16 v[28:31], v[212:215], v[154:157], v[28:31]\n\t" \
  "ds_read_b32 v227, v227 offset:0\n\t" \
  "v_pk_mul_f16 v235, v234, v235\n\t" \
  "v_fma_f32 v179, |v179|, v217, v179 mul:4\n\t" \
  "v_fma_f32 v178, |v178|, v216, v178 mul:4\n\t" \
  "v_mfma_f32_16x16x32_bf16 v[52:55], v[212:215], v[162:165], v[52:55]\n\t" \
  "ds_read_b32 v226, v226 offset:0\n\t" \
  "v_fma_f32 v187, |v187|, v219, v187 mul:4\n\t" \
  "s_waitcnt lgkmcnt(6)\n\t" \
  "v_fma_f32 v186, |v186|, v218, v186 mul:4\n\t" \
  "v_fma_f32 v185, |v185|, v221, v185 mul:4\n\t" \
  "v_mfma_f32_16x16x32_bf16 v[76:79], v[212:215], v[170:173], v[76:79]\n\t" \
  "ds_read_b64_tr_b16 v[212:213], v249 offset:24672\n\t" \
  "v_fma_f32 v184, |v184|, v220, v184 mul:4\n\t" \
  "v_pk_fma_f16 v232, v232, s55, s55\n\t" \
  "s_waitcnt lgkmcnt(3)\n\t" \
  "v_mfma_f32_16x16x32_bf16 v[8:11], v[208:211], v[146:149], v[8:11]\n\t" \
  "ds_read_b64_tr_b16 v[214:215], v249 offset:26208\n\t" \
  "v_cvt_pk_bf16_f32 v179, v178, v179\n\t" \
  "v_cvt_pk_bf16_f32 v187, v186, v187\n\t" \
  "v_cvt_pk_bf16_f32 v186, v184, v185\n\t" \
  "v_mfma_f32_16x16x32_bf16 v[32:35], v[208:211], v[154:157], v[32:35]\n\t" \
  "ds_read_b32 v229, v229 offset:0\n\t" \
  "v_rcp_f16 v233, v232\n\t" \
  "v_exp_f16 v234, v235\n\t" \
  "v_cvt_pk_f16_f32 v216, |v180|, |v181| clamp\n\t" \
  "v_mfma_f32_16x16x32_bf16 v[56:59], v[208:211], v[162:165], v[56:59]\n\t" \
  "ds_read_b32 v228, v228 offset:0\n\t" \
  "s_cbranch_vccnz .Lcnx_p10_1\n\ts_setprio 1\n.Lcnx_p10_1:\n\t" \
  "v_cvt_pk_f16_f32 v218, |v182|, |v183| clamp\n\t" \
  "v_cvt_pk_f16_f32 v220, |v188|, |v189| clamp\n\t" \
  "v_mfma_f32_16x16x32_bf16 v[80:83], v[208:211], v[170:173], v[80:83]\n\t" \
  "ds_read_b64_tr_b16 v[208:209], v249 offset:24728\n\t" \
  "v_cvt_pk_f16_f32 v222, |v190|, |v191| clamp\n\t" \
  "v_rcp_f16_sdwa v232, v232 dst_sel:DWORD dst_unused:UNUSED_PAD src0_sel:WORD_1\n\t" \
  "v_exp_f16_sdwa v234, v235 dst_sel:WORD_1 dst_unused:UNUSED_PRESERVE src0_sel:WORD_1\n\t" \
  "s_waitcnt lgkmcnt(3)\n\t" \
  "v_mfma_f32_16x16x32_bf16 v[12:15], v[212:215], v[146:149], v[12:15]\n\t" \
  "ds_read_b64_tr_b16 v[210:211], v249 offset:26264\n\t" \
  "v_and_b32_sdwa v217, s56, v216 dst_sel:DWORD dst_unused:UNUSED_PAD src0_sel:DWORD src1_sel:WORD_1\n\t" \
  "v_and_b32 v216, s56, v216\n\t" \
  "v_mfma_f32_16x16x32_bf16 v[36:39], v[212:215], v[154:157], v[36:39]\n\t" \
  "ds_read_b32 v217, v217 offset:0\n\t" \
  "v_and_b32_sdwa v219, s56, v218 dst_sel:DWORD dst_unused:UNUSED_PAD src0_sel:DWORD src1_sel:WORD_1\n\t" \
  "v_mfma_f32_16x16x32_bf16 v[60:63], v[212:215], v[162:165], v[60:63]\n\t" \
  "ds_read_b32 v216, v216 offset:0\n\t" \
  "v_and_b32 v218, s56, v218\n\t" \
  "v_and_b32_sdwa v221, s56, v220 dst_sel:DWORD dst_unused:UNUSED_PAD src0_sel:DWORD src1_sel:WORD_1\n\t" \
  "v_mfma_f32_16x16x32_bf16 v[84:87], v[212:215], v[170:173], v[84:87]\n\t" \
  "ds_read_b64_tr_b16 v[212:213], v249 offset:24736\n\t" \
  "v_and_b32 v220, s56, v220\n\t" \
  "s_waitcnt lgkmcnt(3)\n\t" \
  "v_mfma_f32_16x16x32_bf16 v[16:19], v[208:211], v[146:149], v[16:19]\n\t" \
  "ds_read_b64_tr_b16 v[214:215], v249 offset:26272\n\t" \
  "v_and_b32_sdwa v223, s56, v222 dst_sel:DWORD dst_unused:UNUSED_PAD src0_sel:DWORD src1_sel:WORD_1\n\t" \
  "v_mfma_f32_16x16x32_bf16 v[40:43], v[208:211], v[154:157], v[40:43]\n\t" \
  "ds_read_b32 v219, v219 offset:0\n\t" \
  "v_and_b32 v222, s56, v222\n\t" \
  "v_fma_f32 v195, |v195|, v225, v195 mul:4\n\t" \
  "v_mfma_f32_16x16x32_bf16 v[64:67], v[208:211], v[162:165], v[64:67]\n\t" \
  "ds_read_b32 v218, v218 offset:0\n\t" \
  "v_fma_f32 v194, |v194|, v224, v194 mul:4\n\t" \
  "v_fma_f32 v203, |v203|, v227, v203 mul:4\n\t" \
  "v_mfma_f32_16x16x32_bf16 v[88:91], v[208:211], v[170:173], v[88:91]\n\t" \
  "ds_read_b64_tr_b16 v[208:209], v248 offset:36904\n\t" \
  "ds_read_b64_tr_b16 v[210:211], v248 offset:37928\n\t" \
  "v_fma_f32 v202, |v202|, v226, v202 mul:4\n\t" \
  "v_fma_f32 v201, |v201|, v229, v201 mul:4\n\t" \
  "s_waitcnt lgkmcnt(4)\n\t" \
  "v_mfma_f32_16x16x32_bf16 v[20:23], v[212:215], v[146:149], v[20:23]\n\t" \
  "ds_read_b32 v221, v221 offset:0\n\t" \
  "v_fma_f32 v200, |v200|, v228, v200 mul:4\n\t" \
  "v_fma_mix_f32 v176, v176, v233, 0 op_sel_hi:[0,1,0]\n\t" \
  "v_fma_mix_f32 v177, v177, v232, 0 op_sel_hi:[0,1,0]\n\t" \
  "v_mfma_f32_16x16x32_bf16 v[44:47], v[212:215], v[154:157], v[44:47]\n\t" \
  "ds_read_b32 v220, v220 offset:0\n\t" \
  "v_pk_fma_f16 v234, v234, s55, s55\n\t" \
  "v_cvt_pk_bf16_f32 v178, v176, v177\n\t" \
  "v_cvt_pk_bf16_f32 v195, v194, v195\n\t" \
  "v_mfma_f32_16x16x32_bf16 v[68:71], v[212:215], v[162:165], v[68:71]\n\t" \
  "ds_read_b32 v223, v223 offset:0\n\t" \
  "v_cvt_pk_bf16_f32 v203, v202, v203\n\t" \
  "v_cvt_pk_bf16_f32 v202, v200, v201\n\t" \
  "v_rcp_f16 v235, v234\n\t" \
  "v_mfma_f32_16x16x32_bf16 v[92:95], v[212:215], v[170:173], v[92:95]\n\t" \
  "ds_read_b64_tr_b16 v[212:213], v248 offset:39976\n\t" \
  "v_cvt_pk_f16_f32 v224, |v196|, |v197| clamp\n\t" \
  "s_cbranch_vccnz .Lcnx_p10_2\n\ts_setprio 2\n.Lcnx_p10_2:\n\t" \
  "v_cvt_pk_f16_f32 v226, |v198|, |v199| clamp\n\t" \
  "s_waitcnt lgkmcnt(4)\n\t" \
  "v_mfma_f32_16x16x32_bf16 v[144:147], v[208:211], v[96:99], 0\n\t" \
  "ds_read_b64_tr_b16 v[214:215], v248 offset:41000\n\t" \
  "v_cvt_pk_f16_f32 v228, |v204|, |v205| clamp\n\t" \
  "v_cvt_pk_f16_f32 v230, |v206|, |v207| clamp\n\t" \
  "v_rcp_f16_sdwa v234, v234 dst_sel:DWORD dst_unused:UNUSED_PAD src0_sel:WORD_1\n\t" \
  "v_mfma_f32_16x16x32_bf16 v[152:155], v[208:211], v[108:111], 0\n\t" \
  "ds_read_b32 v222, v222 offset:0\n\t" \
  "v_and_b32_sdwa v225, s56, v224 dst_sel:DWORD dst_unused:UNUSED_PAD src0_sel:DWORD src1_sel:WORD_1\n\t" \
  "v_and_b32 v224, s56, v224\n\t" \
  "v_mfma_f32_16x16x32_bf16 v[160:163], v[208:211], v[120:123], 0\n\t" \
  "ds_read_b32 v225, v225 offset:0\n\t" \
  "v_and_b32_sdwa v227, s56, v226 dst_sel:DWORD dst_unused:UNUSED_PAD src0_sel:DWORD src1_sel:WORD_1\n\t" \
  "v_mfma_f32_16x16x32_bf16 v[168:171], v[208:211], v[132:135], 0\n\t" \
  "ds_read_b64_tr_b16 v[208:209], v248 offset:38952\n\t" \
  "v_and_b32 v226, s56, v226\n\t" \
  "v_and_b32_sdwa v229, s56, v228 dst_sel:DWORD dst_unused:UNUSED_PAD src0_sel:DWORD src1_sel:WORD_1\n\t" \
  "s_waitcnt lgkmcnt(3)\n\t" \
  "v_mfma_f32_16x16x32_bf16 v[144:147], v[212:215], v[100:103], v[144:147]\n\t" \
  "ds_read_b64_tr_b16 v[210:211], v248 offset:42024\n\t" \
  "v_and_b32 v228, s56, v228\n\t" \
  "v_mfma_f32_16x16x32_bf16 v[152:155], v[212:215], v[112:115], v[152:155]\n\t" \
  "ds_read_b32 v224, v224 offset:0\n\t" \
  "v_and_b32_sdwa v231, s56, v230 dst_sel:DWORD dst_unused:UNUSED_PAD src0_sel:DWORD src1_sel:WORD_1\n\t" \
  "v_mfma_f32_16x16x32_bf16 v[160:163], v[212:215], v[124:127], v[160:163]\n\t" \
  "ds_read_b32 v227, v227 offset:0\n\t" \
  "v_and_b32 v230, s56, v230\n\t" \
  "v_fma_f32 v181, |v181|, v217, v181 mul:4\n\t" \
  "v_mfma_f32_16x16x32_bf16 v[168:171], v[212:215], v[136:139], v[168:171]\n\t" \
  "ds_read_b64_tr_b16 v[212:213], v248 offset:37416\n\t" \
  "ds_read_b64_tr_b16 v[214:215], v248 offset:38440\n\t" \
  "v_fma_f32 v180, |v180|, v216, v180 mul:4\n\t" \
  "v_fma_f32 v183, |v183|, v219, v183 mul:4\n\t" \
  "s_waitcnt lgkmcnt(4)\n\t" \
  "v_mfma_f32_16x16x32_bf16 v[144:147], v[208:211], v[104:107], v[144:147]\n\t" \
  "ds_read_b32 v226, v226 offset:0\n\t" \
  "v_fma_f32 v182, |v182|, v218, v182 mul:4\n\t" \
  "v_fma_f32 v189, |v189|, v221, v189 mul:4\n\t" \
  "v_mfma_f32_16x16x32_bf16 v[152:155], v[208:211], v[116:119], v[152:155]\n\t" \
  "ds_read_b32 v229, v229 offset:0\n\t" \
  "v_fma_f32 v188, |v188|, v220, v188 mul:4\n\t" \
  "v_fma_f32 v191, |v191|, v223, v191 mul:4\n\t" \
  "v_fma_f32 v190, |v190|, v222, v190 mul:4\n\t" \
  "v_mfma_f32_16x16x32_bf16 v[160:163], v[208:211], v[128:131], v[160:163]\n\t" \
  "ds_read_b32 v228, v228 offset:0\n\t" \
  "v_fma_mix_f32 v192, v192, v235, 0 op_sel_hi:[0,1,0]\n\t" \
  "v_fma_mix_f32 v193, v193, v234, 0 op_sel_hi:[0,1,0]\n\t" \
  "v_cvt_pk_bf16_f32 v194, v192, v193\n\t" \
  "v_mfma_f32_16x16x32_bf16 v[168:171], v[208:211], v[140:143], v[168:171]\n\t" \
  "ds_read_b64_tr_b16 v[208:209], v248 offset:40488\n\t" \
  "v_cvt_pk_bf16_f32 v180, v180, v181\n\t" \
  "v_cvt_pk_bf16_f32 v181, v182, v183\n\t" \
  "s_waitcnt lgkmcnt(4)\n\t" \
  "v_mfma_f32_16x16x32_bf16 v[148:151], v[212:215], v[96:99], 0\n\t" \
  "ds_read_b64_tr_b16 v[210:211], v248 offset:41512\n\t" \
  "v_cvt_pk_bf16_f32 v188, v188, v189\n\t" \
  "v_cvt_pk_bf16_f32 v189, v190, v191\n\t" \
  "s_cbranch_vccnz .Lcnx_p10_3\n\ts_setprio 3\n.Lcnx_p10_3:\n\t" \
  "v_cvt_pk_f16_f32 v216, |v146|, |v147| clamp\n\t" \
  "v_mfma_f32_16x16x32_bf16 v[156:159], v[212:215], v[108:111], 0\n\t" \
  "ds_read_b32 v231, v231 offset:0\n\t" \
  "v_cvt_pk_f16_f32 v232, v144, v145\n\t" \
  "v_cvt_pk_f16_f32 v218, |v154|, |v155| clamp\n\t" \
  "v_cvt_pk_f16_f32 v220, |v152|, |v153| clamp\n\t" \
  "v_mfma_f32_16x16x32_bf16 v[164:167], v[212:215], v[120:123], 0\n\t" \
  "ds_read_b32 v230, v230 offset:0\n\t" \
  "v_pk_mul_f16 v233, v232, v232\n\t" \
  "v_and_b32_sdwa v217, s56, v216 dst_sel:DWORD dst_unused:UNUSED_PAD src0_sel:DWORD src1_sel:WORD_1\n\t" \
  "v_mfma_f32_16x16x32_bf16 v[172:175], v[212:215], v[132:135], 0\n\t" \
  "ds_read_b64_tr_b16 v[212:213], v248 offset:39464\n\t" \
  "v_and_b32 v216, s56, v216\n\t" \
  "v_and_b32_sdwa v219, s56, v218 dst_sel:DWORD dst_unused:UNUSED_PAD src0_sel:DWORD src1_sel:WORD_1\n\t" \
  "s_waitcnt lgkmcnt(3)\n\t" \
  "v_mfma_f32_16x16x32_bf16 v[148:151], v[208:211], v[100:103], v[148:151]\n\t" \
  "ds_read_b64_tr_b16 v[214:215], v248 offset:42536\n\t" \
  "v_and_b32 v218, s56, v218\n\t" \
  "v_mfma_f32_16x16x32_bf16 v[156:159], v[208:211], v[112:115], v[156:159]\n\t" \
  "s_waitcnt vmcnt(1)\n\t" \
  "s_cmp_lg_u32 s58, 1\n\t" \
  "s_cbranch_scc1 .Lcnx_c21\n\t" \
  "s_waitcnt vmcnt(0)\n\t" \
  ".Lcnx_c21:\n\t" \
  "s_mov_b64 exec, 3\n\t" \
  "ds_add_rtn_u32 v246, v251, v250 offset:47736\n\t" \
  "s_mov_b64 exec, -1\n\t" \
  "ds_read_b32 v217, v217 offset:0\n\t" \
  "v_and_b32_sdwa v221, s56, v220 dst_sel:DWORD dst_unused:UNUSED_PAD src0_sel:DWORD src1_sel:WORD_1\n\t" \
  "v_mfma_f32_16x16x32_bf16 v[164:167], v[208:211], v[124:127], v[164:167]\n\t" \
  "ds_read_b32 v216, v216 offset:0\n\t" \
  "v_and_b32 v220, s56, v220\n\t" \
  "v_pk_fma_f16 v233, v233, v253, s54\n\t" \
  "v_mfma_f32_16x16x32_bf16 v[172:175], v[208:211], v[136:139], v[172:175]\n\t" \
  "s_waitcnt lgkmcnt(2)\n\t" \
  "v_readfirstlane_b32 s4, v246\n\t" \
  "s_cmp_lt_u32 s4, s57\n\t" \
  "s_cbranch_scc1 .Lcnx_w120\n.Lcnx_b120:\n\t" \
  "ds_read_b64_tr_b16 v[208:209], v249 offset:30752\n\t" \
  "v_pk_mul_f16 v233, v232, v233\n\t" \
  "v_fma_f32 v197, |v197|, v225, v197 mul:4\n\t" \
  "v_mfma_f32_16x16x32_bf16 v[148:151], v[212:215], v[104:107], v[148:151]\n\t" \
  "ds_read_b64_tr_b16 v[210:211], v249 offset:32288\n\t" \
  "v_fma_f32 v196, |v196|, v224, v196 mul:4\n\t" \
  "v_fma_f32 v199, |v199|, v227, v199 mul:4\n\t" \
  "v_mfma_f32_16x16x32_bf16 v[156:159], v[212:215], v[116:119], v[156:159]\n\t" \
  "s_cmp_eq_u32 s37, 0\n\t" \
  "s_cbranch_scc1 .Lcnx_f11\n.Lcnx_g11:\n\t" \
  "ds_read_b32 v219, v219 offset:0\n\t" \
  "v_fma_f32 v198, |v198|, v226, v198 mul:4\n\t" \
  "v_fma_f32 v205, |v205|, v229, v205 mul:4\n\t" \
  "v_fma_f32 v204, |v204|, v228, v204 mul:4\n\t" \
  "v_mfma_f32_16x16x32_bf16 v[164:167], v[212:215], v[128:131], v[164:167]\n\t" \
  "ds_read_b32 v218, v218 offset:0\n\t" \
  "v_fma_f32 v207, |v207|, v231, v207 mul:4\n\t" \
  "v_fma_f32 v206, |v206|, v230, v206 mul:4\n\t" \
  "v_cvt_pk_bf16_f32 v196, v196, v197\n\t" \
  "v_mfma_f32_16x16x32_bf16 v[172:175], v[212:215], v[140:143], v[172:175]\n\t" \
  "ds_read_b64_tr_b16 v[212:213], v249 offset:30760\n\t" \
  "v_cvt_pk_bf16_f32 v197, v198, v199\n\t" \
  "v_cvt_pk_bf16_f32 v204, v204, v205\n\t" \
  "v_cvt_pk_bf16_f32 v205, v206, v207\n\t" \
  "s_cbranch_vccnz .Lcnx_p11_0\n\ts_setprio 0\n.Lcnx_p11_0:\n\t" \
  "v_exp_f16 v232, v233\n\t" \
  "v_cvt_pk_f16_f32 v224, |v162|, |v163| clamp\n\t" \
  "v_mfma_f32_16x16x32_bf16 v[72:75], v[242:245], v[236:239], v[72:75]\n\t" \
  "ds_read_b64_tr_b16 v[214:215], v249 offset:32296\n\t" \
  "v_cvt_pk_f16_f32 v234, v160, v161\n\t" \
  "v_cvt_pk_f16_f32 v226, |v170|, |v171| clamp\n\t" \
  "v_mfma_f32_16x16x32_bf16 v[76:79], v[240:243], v[236:239], v[76:79]\n\t" \
  "buffer_load_dwordx4 v[236:239], v250, s[8:11], s29 offen offset:256\n\t" \
  "s_cmp_lg_u32 s38, 7\n\t" \
  "s_cbranch_scc1 .Lcnx_c33\n\t" \
  "s_mov_b32 m0, 163632\n\t" \
  "s_add_u32 s30, s30, s31\n\t" \
  "buffer_load_dword v252, s[8:11], s30 offen lds\n\t" \
  ".Lcnx_c33:\n\t" \
  "ds_read_b32 v221, v221 offset:0\n\t" \
  "v_cvt_pk_f16_f32 v228, |v168|, |v169| clamp\n\t" \
  "v_exp_f16_sdwa v232, v233 dst_sel:WORD_1 dst_unused:UNUSED_PRESERVE src0_sel:WORD_1\n\t" \
  "v_pk_mul_f16 v235, v234, v234\n\t" \
  "s_waitcnt lgkmcnt(5)\n\t" \
  "v_mfma_f32_16x16x32_bf16 v[0:3], v[208:211], v[178:181], v[0:3]\n\t" \
  "ds_read_b32 v220, v220 offset:0\n\t" \
  "v_and_b32_sdwa v225, s56, v224 dst_sel:DWORD dst_unused:UNUSED_PAD src0_sel:DWORD src1_sel:WORD_1\n\t" \
  "v_and_b32 v224, s56, v224\n\t" \
  "v_mfma_f32_16x16x32_bf16 v[24:27], v[208:211], v[186:189], v[24:27]\n\t" \
  "ds_read_b32 v225, v225 offset:0\n\t" \
  "v_and_b32_sdwa v227, s56, v226 dst_sel:DWORD dst_unused:UNUSED_PAD src0_sel:DWORD src1_sel:WORD_1\n\t" \
  "v_mfma_f32_16x16x32_bf16 v[48:51], v[208:211], v[194:197], v[48:51]\n\t" \
  "ds_read_b32 v224, v224 offset:0\n\t" \
  "v_and_b32 v226, s56, v226\n\t" \
  "v_and_b32_sdwa v229, s56, v228 dst_sel:DWORD dst_unused:UNUSED_PAD src0_sel:DWORD src1_sel:WORD_1\n\t" \
  "v_mfma_f32_16x16x32_bf16 v[72:75], v[208:211], v[202:205], v[72:75]\n\t" \
  "ds_read_b64_tr_b16 v[208:209], v249 offset:30816\n\t" \
  "v_and_b32 v228, s56, v228\n\t" \
  "s_waitcnt lgkmcnt(5)\n\t" \
  "v_mfma_f32_16x16x32_bf16 v[4:7], v[212:215], v[178:181], v[4:7]\n\t" \
  "ds_read_b64_tr_b16 v[210:211], v249 offset:32352\n\t" \
  "v_pk_fma_f16 v235, v235, v253, s54\n\t" \
  "v_mfma_f32_16x16x32_bf16 v[28:31], v[212:215], v[186:189], v[28:31]\n\t" \
  "ds_read_b32 v227, v227 offset:0\n\t" \
  "v_pk_mul_f16 v235, v234, v235\n\t" \
  "v_fma_f32 v147, |v147|, v217, v147 mul:4\n\t" \
  "v_fma_f32 v146, |v146|, v216, v146 mul:4\n\t" \
  "v_mfma_f32_16x16x32_bf16 v[52:55], v[212:215], v[194:197], v[52:55]\n\t" \
  "ds_read_b32 v226, v226 offset:0\n\t" \
  "v_fma_f32 v155, |v155|, v219, v155 mul:4\n\t" \
  "s_waitcnt lgkmcnt(6)\n\t" \
  "v_fma_f32 v154, |v154|, v218, v154 mul:4\n\t" \
  "v_fma_f32 v153, |v153|, v221, v153 mul:4\n\t" \
  "v_mfma_f32_16x16x32_bf16 v[76:79], v[212:215], v[202:205], v[76:79]\n\t" \
  "ds_read_b64_tr_b16 v[212:213], v249 offset:30824\n\t" \
  "v_fma_f32 v152, |v152|, v220, v152 mul:4\n\t" \
  "v_pk_fma_f16 v232, v232, s55, s55\n\t" \
  "s_waitcnt lgkmcnt(3)\n\t" \
  "v_mfma_f32_16x16x32_bf16 v[8:11], v[208:211], v[178:181], v[8:11]\n\t" \
  "ds_read_b64_tr_b16 v[214:215], v249 offset:32360\n\t" \
  "v_cvt_pk_bf16_f32 v147, v146, v147\n\t" \
  "v_cvt_pk_bf16_f32 v155, v154, v155\n\t" \
  "v_cvt_pk_bf16_f32 v154, v152, v153\n\t" \
  "v_mfma_f32_16x16x32_bf16 v[32:35], v[208:211], v[186:189], v[32:35]\n\t" \
  "ds_read_b32 v229, v229 offset:0\n\t" \
  "v_rcp_f16 v233, v232\n\t" \
  "v_exp_f16 v234, v235\n\t" \
  "v_cvt_pk_f16_f32 v216, |v148|, |v149| clamp\n\t" \
  "v_mfma_f32_16x16x32_bf16 v[56:59], v[208:211], v[194:197], v[56:59]\n\t" \
  "ds_read_b32 v228, v228 offset:0\n\t" \
  "s_cbranch_vccnz .Lcnx_p11_1\n\ts_setprio 1\n.Lcnx_p11_1:\n\t" \
  "v_cvt_pk_f16_f32 v218, |v150|, |v151| clamp\n\t" \
  "v_cvt_pk_f16_f32 v220, |v156|, |v157| clamp\n\t" \
  "v_mfma_f32_16x16x32_bf16 v[80:83], v[208:211], v[202:205], v[80:83]\n\t" \
  "ds_read_b64_tr_b16 v[208:209], v249 offset:30880\n\t" \
  "v_cvt_pk_f16_f32 v222, |v158|, |v159| clamp\n\t" \
  "v_rcp_f16_sdwa v232, v232 dst_sel:DWORD dst_unused:UNUSED_PAD src0_sel:WORD_1\n\t" \
  "v_exp_f16_sdwa v234, v235 dst_sel:WORD_1 dst_unused:UNUSED_PRESERVE src0_sel:WORD_1\n\t" \
  "s_waitcnt lgkmcnt(3)\n\t" \
  "v_mfma_f32_16x16x32_bf16 v[12:15], v[212:215], v[178:181], v[12:15]\n\t" \
  "ds_read_b64_tr_b16 v[210:211], v249 offset:32416\n\t" \
  "v_and_b32_sdwa v217, s56, v216 dst_sel:DWORD dst_unused:UNUSED_PAD src0_sel:DWORD src1_sel:WORD_1\n\t" \
  "v_and_b32 v216, s56, v216\n\t" \
  "v_mfma_f32_16x16x32_bf16 v[36:39], v[212:215], v[186:189], v[36:39]\n\t" \
  "ds_read_b32 v217, v217 offset:0\n\t" \
  "v_and_b32_sdwa v219, s56, v218 dst_sel:DWORD dst_unused:UNUSED_PAD src0_sel:DWORD src1_sel:WORD_1\n\t" \
  "v_mfma_f32_16x16x32_bf16 v[60:63], v[212:215], v[194:197], v[60:63]\n\t" \
  "ds_read_b32 v216, v216 offset:0\n\t" \
  "v_and_b32 v218, s56, v218\n\t" \
  "v_and_b32_sdwa v221, s56, v220 dst_sel:DWORD dst_unused:UNUSED_PAD src0_sel:DWORD src1_sel:WORD_1\n\t" \
  "v_mfma_f32_16x16x32_bf16 v[84:87], v[212:215], v[202:205], v[84:87]\n\t" \
  "ds_read_b64_tr_b16 v[212:213], v249 offset:30888\n\t" \
  "v_and_b32 v220, s56, v220\n\t" \
  "s_waitcnt lgkmcnt(3)\n\t" \
  "v_mfma_f32_16x16x32_bf16 v[16:19], v[208:211], v[178:181], v[16:19]\n\t" \
  "ds_read_b64_tr_b16 v[214:215], v249 offset:32424\n\t" \
  "v_and_b32_sdwa v223, s56, v222 dst_sel:DWORD dst_unused:UNUSED_PAD src0_sel:DWORD src1_sel:WORD_1\n\t" \
  "v_mfma_f32_16x16x32_bf16 v[40:43], v[208:211], v[186:189], v[40:43]\n\t" \
  "ds_read_b32 v219, v219 offset:0\n\t" \
  "v_and_b32 v222, s56, v222\n\t" \
  "v_fma_f32 v163, |v163|, v225, v163 mul:4\n\t" \
  "v_mfma_f32_16x16x32_bf16 v[64:67], v[208:211], v[194:197], v[64:67]\n\t" \
  "ds_read_b32 v218, v218 offset:0\n\t" \
  "v_fma_f32 v162, |v162|, v224, v162 mul:4\n\t" \
  "v_fma_f32 v171, |v171|, v227, v171 mul:4\n\t" \
  "v_mfma_f32_16x16x32_bf16 v[88:91], v[208:211], v[202:205], v[88:91]\n\t" \
  "ds_read_b64_tr_b16 v[208:209], v248 offset:12304\n\t" \
  "ds_read_b64_tr_b16 v[210:211], v248 offset:13328\n\t" \
  "v_fma_f32 v170, |v170|, v226, v170 mul:4\n\t" \
  "v_fma_f32 v169, |v169|, v229, v169 mul:4\n\t" \
  "s_waitcnt lgkmcnt(4)\n\t" \
  "v_mfma_f32_16x16x32_bf16 v[20:23], v[212:215], v[178:181], v[20:23]\n\t" \
  "ds_read_b32 v221, v221 offset:0\n\t" \
  "v_fma_f32 v168, |v168|, v228, v168 mul:4\n\t" \
  "v_fma_mix_f32 v144, v144, v233, 0 op_sel_hi:[0,1,0]\n\t" \
  "v_fma_mix_f32 v145, v145, v232, 0 op_sel_hi:[0,1,0]\n\t" \
  "v_mfma_f32_16x16x32_bf16 v[44:47], v[212:215], v[186:189], v[44:47]\n\t" \
  "ds_read_b32 v220, v220 offset:0\n\t" \
  "v_pk_fma_f16 v234, v234, s55, s55\n\t" \
  "v_cvt_pk_bf16_f32 v146, v144, v145\n\t" \
  "v_cvt_pk_bf16_f32 v163, v162, v163\n\t" \
  "v_mfma_f32_16x16x32_bf16 v[68:71], v[212:215], v[194:197], v[68:71]\n\t" \
  "ds_read_b32 v223, v223 offset:0\n\t" \
  "v_cvt_pk_bf16_f32 v171, v170, v171\n\t" \
  "v_cvt_pk_bf16_f32 v170, v168, v169\n\t" \
  "v_rcp_f16 v235, v234\n\t" \
  "v_mfma_f32_16x16x32_bf16 v[92:95], v[212:215], v[202:205], v[92:95]\n\t" \
  "ds_read_b64_tr_b16 v[212:213], v248 offset:15376\n\t" \
  "v_cvt_pk_f16_f32 v224, |v164|, |v165| clamp\n\t" \
  "s_cbranch_vccnz .Lcnx_p11_2\n\ts_setprio 2\n.Lcnx_p11_2:\n\t" \
  "v_cvt_pk_f16_f32 v226, |v166|, |v167| clamp\n\t" \
  "s_waitcnt lgkmcnt(4)\n\t" \
  "v_mfma_f32_16x16x32_bf16 v[176:179], v[208:211], v[96:99], 0\n\t" \
  "ds_read_b64_tr_b16 v[214:215], v248 offset:16400\n\t" \
  "v_cvt_pk_f16_f32 v228, |v172|, |v173| clamp\n\t" \
  "v_cvt_pk_f16_f32 v230, |v174|, |v175| clamp\n\t" \
  "v_rcp_f16_sdwa v234, v234 dst_sel:DWORD dst_unused:UNUSED_PAD src0_sel:WORD_1\n\t" \
  "v_mfma_f32_16x16x32_bf16 v[184:187], v[208:211], v[108:111], 0\n\t" \
  "ds_read_b32 v222, v222 offset:0\n\t" \
  "v_and_b32_sdwa v225, s56, v224 dst_sel:DWORD dst_unused:UNUSED_PAD src0_sel:DWORD src1_sel:WORD_1\n\t" \
  "v_and_b32 v224, s56, v224\n\t" \
  "v_mfma_f32_16x16x32_bf16 v[192:195], v[208:211], v[120:123], 0\n\t" \
  "ds_read_b32 v225, v225 offset:0\n\t" \
  "v_and_b32_sdwa v227, s56, v226 dst_sel:DWORD dst_unused:UNUSED_PAD src0_sel:DWORD src1_sel:WORD_1\n\t" \
  "v_mfma_f32_16x16x32_bf16 v[200:203], v[208:211], v[132:135], 0\n\t" \
  "ds_read_b64_tr_b16 v[208:209], v248 offset:14352\n\t" \
  "v_and_b32 v226, s56, v226\n\t" \
  "v_and_b32_sdwa v229, s56, v228 dst_sel:DWORD dst_unused:UNUSED_PAD src0_sel:DWORD src1_sel:WORD_1\n\t" \
  "s_waitcnt lgkmcnt(3)\n\t" \
  "v_mfma_f32_16x16x32_bf16 v[176:179], v[212:215], v[100:103], v[176:179]\n\t" \
  "ds_read_b64_tr_b16 v[210:211], v248 offset:17424\n\t" \
  "v_and_b32 v228, s56, v228\n\t" \
  "v_mfma_f32_16x16x32_bf16 v[184:187], v[212:215], v[112:115], v[184:187]\n\t" \
  "ds_read_b32 v224, v224 offset:0\n\t" \
  "v_and_b32_sdwa v231, s56, v230 dst_sel:DWORD dst_unused:UNUSED_PAD src0_sel:DWORD src1_sel:WORD_1\n\t" \
  "v_mfma_f32_16x16x32_bf16 v[192:195], v[212:215], v[124:127], v[192:195]\n\t" \
  "ds_read_b32 v227, v227 offset:0\n\t" \
  "v_and_b32 v230, s56, v230\n\t" \
  "v_fma_f32 v149, |v149|, v217, v149 mul:4\n\t" \
  "v_mfma_f32_16x16x32_bf16 v[200:203], v[212:215], v[136:139], v[200:203]\n\t" \
  "ds_read_b64_tr_b16 v[212:213], v248 offset:12816\n\t" \
  "ds_read_b64_tr_b16 v[214:215], v248 offset:13840\n\t" \
  "v_fma_f32 v148, |v148|, v216, v148 mul:4\n\t" \
  "v_fma_f32 v151, |v151|, v219, v151 mul:4\n\t" \
  "s_waitcnt lgkmcnt(4)\n\t" \
  "v_mfma_f32_16x16x32_bf16 v[176:179], v[208:211], v[104:107], v[176:179]\n\t" \
  "ds_read_b32 v226, v226 offset:0\n\t" \
  "v_fma_f32 v150, |v150|, v218, v150 mul:4\n\t" \
  "v_fma_f32 v157, |v157|, v221, v157 mul:4\n\t" \
  "v_mfma_f32_16x16x32_bf16 v[184:187], v[208:211], v[116:119], v[184:187]\n\t" \
  "ds_read_b32 v229, v229 offset:0\n\t" \
  "v_fma_f32 v156, |v156|, v220, v156 mul:4\n\t" \
  "v_fma_f32 v159, |v159|, v223, v159 mul:4\n\t" \
  "v_fma_f32 v158, |v158|, v222, v158 mul:4\n\t" \
  "v_mfma_f32_16x16x32_bf16 v[192:195], v[208:211], v[128:131], v[192:195]\n\t" \
  "ds_read_b32 v228, v228 offset:0\n\t" \
  "v_fma_mix_f32 v160, v160, v235, 0 op_sel_hi:[0,1,0]\n\t" \
  "v_fma_mix_f32 v161, v161, v234, 0 op_sel_hi:[0,1,0]\n\t" \
  "v_cvt_pk_bf16_f32 v162, v160, v161\n\t" \
  "v_mfma_f32_16x16x32_bf16 v[200:203], v[208:211], v[140:143], v[200:203]\n\t" \
  "ds_read_b64_tr_b16 v[208:209], v248 offset:15888\n\t" \
  "v_cvt_pk_bf16_f32 v148, v148, v149\n\t" \
  "v_cvt_pk_bf16_f32 v149, v150, v151\n\t" \
  "s_waitcnt lgkmcnt(4)\n\t" \
  "v_mfma_f32_16x16x32_bf16 v[180:183], v[212:215], v[96:99], 0\n\t" \
  "ds_read_b64_tr_b16 v[210:211], v248 offset:16912\n\t" \
  "v_cvt_pk_bf16_f32 v156, v156, v157\n\t" \
  "v_cvt_pk_bf16_f32 v157, v158, v159\n\t" \
  "s_cbranch_vccnz .Lcnx_p11_3\n\ts_setprio 3\n.Lcnx_p11_3:\n\t" \
  "v_cvt_pk_f16_f32 v216, |v178|, |v179| clamp\n\t" \
  "v_mfma_f32_16x16x32_bf16 v[188:191], v[212:215], v[108:111], 0\n\t" \
  "ds_read_b32 v231, v231 offset:0\n\t" \
  "v_cvt_pk_f16_f32 v232, v176, v177\n\t" \
  "v_cvt_pk_f16_f32 v218, |v186|, |v187| clamp\n\t" \
  "v_cvt_pk_f16_f32 v220, |v184|, |v185| clamp\n\t" \
  "v_mfma_f32_16x16x32_bf16 v[196:199], v[212:215], v[120:123], 0\n\t" \
  "ds_read_b32 v230, v230 offset:0\n\t" \
  "v_pk_mul_f16 v233, v232, v232\n\t" \
  "v_and_b32_sdwa v217, s56, v216 dst_sel:DWORD dst_unused:UNUSED_PAD src0_sel:DWORD src1_sel:WORD_1\n\t" \
  "v_mfma_f32_16x16x32_bf16 v[204:207], v[212:215], v[132:135], 0\n\t" \
  "ds_read_b64_tr_b16 v[212:213], v248 offset:14864\n\t" \
  "v_and_b32 v216, s56, v216\n\t" \
  "v_and_b32_sdwa v219, s56, v218 dst_sel:DWORD dst_unused:UNUSED_PAD src0_sel:DWORD src1_sel:WORD_1\n\t" \
  "s_waitcnt lgkmcnt(3)\n\t" \
  "v_mfma_f32_16x16x32_bf16 v[180:183], v[208:211], v[100:103], v[180:183]\n\t" \
  "ds_read_b64_tr_b16 v[214:215], v248 offset:17936\n\t" \
  "v_and_b32 v218, s56, v218\n\t" \
  "v_mfma_f32_16x16x32_bf16 v[188:191], v[208:211], v[112:115], v[188:191]\n\t" \
  "s_waitcnt vmcnt(0)\n\t" \
  "s_mov_b64 exec, 3\n\t" \
  "ds_add_rtn_u32 v246, v251, v250 offset:47744\n\t" \
  "s_mov_b64 exec, -1\n\t" \
  "ds_read_b32 v217, v217 offset:0\n\t" \
  "v_and_b32_sdwa v221, s56, v220 dst_sel:DWORD dst_unused:UNUSED_PAD src0_sel:DWORD src1_sel:WORD_1\n\t" \
  "v_mfma_f32_16x16x32_bf16 v[196:199], v[208:211], v[124:127], v[196:199]\n\t" \
  "ds_read_b32 v216, v216 offset:0\n\t" \
  "v_and_b32 v220, s56, v220\n\t" \
  "v_pk_fma_f16 v233, v233, v253, s54\n\t" \
  "v_mfma_f32_16x16x32_bf16 v[204:207], v[208:211], v[136:139], v[204:207]\n\t" \
  "s_waitcnt lgkmcnt(2)\n\t" \
  "v_readfirstlane_b32 s4, v246\n\t" \
  "s_cmp_lt_u32 s4, s57\n\t" \
  "s_cbranch_scc1 .Lcnx_w132\n.Lcnx_b132:\n\t" \
  "ds_read_b64_tr_b16 v[208:209], v249 offset:0\n\t" \
  "v_pk_mul_f16 v233, v232, v233\n\t" \
  "v_fma_f32 v165, |v165|, v225, v165 mul:4\n\t" \
  "v_mfma_f32_16x16x32_bf16 v[180:183], v[212:215], v[104:107], v[180:183]\n\t" \
  "ds_read_b64_tr_b16 v[210:211], v249 offset:1536\n\t" \
  "v_fma_f32 v164, |v164|, v224, v164 mul:4\n\t" \
  "v_fma_f32 v167, |v167|, v227, v167 mul:4\n\t" \
  "v_mfma_f32_16x16x32_bf16 v[188:191], v[212:215], v[116:119], v[188:191]\n\t" \
  "ds_read_b32 v219, v219 offset:0\n\t" \
  "v_fma_f32 v166, |v166|, v226, v166 mul:4\n\t" \
  "v_fma_f32 v173, |v173|, v229, v173 mul:4\n\t" \
  "v_fma_f32 v172, |v172|, v228, v172 mul:4\n\t" \
  "v_mfma_f32_16x16x32_bf16 v[196:199], v[212:215], v[128:131], v[196:199]\n\t" \
  "ds_read_b32 v218, v218 offset:0\n\t" \
  "v_fma_f32 v175, |v175|, v231, v175 mul:4\n\t" \
  "v_fma_f32 v174, |v174|, v230, v174 mul:4\n\t" \
  "v_cvt_pk_bf16_f32 v164, v164, v165\n\t" \
  "v_mfma_f32_16x16x32_bf16 v[204:207], v[212:215], v[140:143], v[204:207]\n\t" \
  "ds_read_b64_tr_b16 v[212:213], v249 offset:8\n\t" \
  "v_cvt_pk_bf16_f32 v165, v166, v167\n\t" \
  "v_cvt_pk_bf16_f32 v172, v172, v173\n\t" \
  "v_cvt_pk_bf16_f32 v173, v174, v175\n\t" \
  "s_cbranch_vccnz .Lcnx_p12_0\n\ts_setprio 0\n.Lcnx_p12_0:\n\t" \
  "v_exp_f16 v232, v233\n\t" \
  "v_cvt_pk_f16_f32 v224, |v194|, |v195| clamp\n\t" \
  "v_mfma_f32_16x16x32_bf16 v[80:83], v[242:245], v[236:239], v[80:83]\n\t" \
  "ds_read_b64_tr_b16 v[214:215], v249 offset:1544\n\t" \
  "v_cvt_pk_f16_f32 v234, v192, v193\n\t" \
  "v_cvt_pk_f16_f32 v226, |v202|, |v203| clamp\n\t" \
  "v_cvt_pk_f16_f32 v228, |v200|, |v201| clamp\n\t" \
  "v_exp_f16_sdwa v232, v233 dst_sel:WORD_1 dst_unused:UNUSED_PRESERVE src0_sel:WORD_1\n\t" \
  "v_mfma_f32_16x16x32_bf16 v[84:87], v[240:243], v[236:239], v[84:87]\n\t" \
  "buffer_load_dwordx4 v[236:239], v250, s[8:11], s29 offen offset:320\n\t" \
  "s_cmp_lg_u32 s38, 4\n\t" \
  "s_cbranch_scc1 .Lcnx_c34\n\t" \
  "s_mov_b32 m0, 163632\n\t" \
  "s_add_u32 s30, s30, s31\n\t" \
  "buffer_load_dword v252, s[8:11], s30 offen lds\n\t" \
  ".Lcnx_c34:\n\t" \
  "ds_read_b32 v221, v221 offset:0\n\t" \
  "v_pk_mul_f16 v235, v234, v234\n\t" \
  "v_and_b32_sdwa v225, s56, v224 dst_sel:DWORD dst_unused:UNUSED_PAD src0_sel:DWORD src1_sel:WORD_1\n\t" \
  "v_and_b32 v224, s56, v224\n\t" \
  "s_waitcnt lgkmcnt(5)\n\t" \
  "v_mfma_f32_16x16x32_bf16 v[0:3], v[208:211], v[146:149], v[0:3]\n\t" \
  "ds_read_b32 v220, v220 offset:0\n\t" \
  "v_and_b32_sdwa v227, s56, v226 dst_sel:DWORD dst_unused:UNUSED_PAD src0_sel:DWORD src1_sel:WORD_1\n\t" \
  "v_and_b32 v226, s56, v226\n\t" \
  "v_and_b32_sdwa v229, s56, v228 dst_sel:DWORD dst_unused:UNUSED_PAD src0_sel:DWORD src1_sel:WORD_1\n\t" \
  "v_mfma_f32_16x16x32_bf16 v[24:27], v[208:211], v[154:157], v[24:27]\n\t" \
  "ds_read_b32 v225, v225 offset:0\n\t" \
  "v_and_b32 v228, s56, v228\n\t" \
  "v_pk_fma_f16 v235, v235, v253, s54\n\t" \
  "v_mfma_f32_16x16x32_bf16 v[48:51], v[208:211], v[162:165], v[48:51]\n\t" \
  "ds_read_b32 v224, v224 offset:0\n\t" \
  "v_pk_mul_f16 v235, v234, v235\n\t" \
  "s_waitcnt lgkmcnt(7)\n\t" \
  "v_fma_f32 v179, |v179|, v217, v179 mul:4\n\t" \
  "v_fma_f32 v178, |v178|, v216, v178 mul:4\n\t" \
  "v_fma_f32 v187, |v187|, v219, v187 mul:4\n\t" \
  "v_mfma_f32_16x16x32_bf16 v[72:75], v[208:211], v[170:173], v[72:75]\n\t" \
  "ds_read_b64_tr_b16 v[208:209], v249 offset:64\n\t" \
  "ds_read_b64_tr_b16 v[210:211], v249 offset:1600\n\t" \
  "s_waitcnt lgkmcnt(4)\n\t" \
  "v_fma_f32 v186, |v186|, v218, v186 mul:4\n\t" \
  "v_fma_f32 v185, |v185|, v221, v185 mul:4\n\t" \
  "v_fma_f32 v184, |v184|, v220, v184 mul:4\n\t" \
  "v_pk_fma_f16 v232, v232, s55, s55\n\t" \
  "v_mfma_f32_16x16x32_bf16 v[4:7], v[212:215], v[146:149], v[4:7]\n\t" \
  "ds_read_b32 v227, v227 offset:0\n\t" \
  "v_cvt_pk_bf16_f32 v179, v178, v179\n\t" \
  "v_cvt_pk_bf16_f32 v187, v186, v187\n\t" \
  "v_cvt_pk_bf16_f32 v186, v184, v185\n\t" \
  "v_rcp_f16 v233, v232\n\t" \
  "s_cbranch_vccnz .Lcnx_p12_1\n\ts_setprio 1\n.Lcnx_p12_1:\n\t" \
  "v_mfma_f32_16x16x32_bf16 v[28:31], v[212:215], v[154:157], v[28:31]\n\t" \
  "ds_read_b32 v226, v226 offset:0\n\t" \
  "v_exp_f16 v234, v235\n\t" \
  "v_cvt_pk_f16_f32 v216, |v180|, |v181| clamp\n\t" \
  "v_cvt_pk_f16_f32 v218, |v182|, |v183| clamp\n\t" \
  "v_cvt_pk_f16_f32 v220, |v188|, |v189| clamp\n\t" \
  "v_cvt_pk_f16_f32 v222, |v190|, |v191| clamp\n\t" \
  "v_mfma_f32_16x16x32_bf16 v[52:55], v[212:215], v[162:165], v[52:55]\n\t" \
  "ds_read_b32 v229, v229 offset:0\n\t" \
  "v_rcp_f16_sdwa v232, v232 dst_sel:DWORD dst_unused:UNUSED_PAD src0_sel:WORD_1\n\t" \
  "v_exp_f16_sdwa v234, v235 dst_sel:WORD_1 dst_unused:UNUSED_PRESERVE src0_sel:WORD_1\n\t" \
  "v_and_b32_sdwa v217, s56, v216 dst_sel:DWORD dst_unused:UNUSED_PAD src0_sel:DWORD src1_sel:WORD_1\n\t" \
  "v_and_b32 v216, s56, v216\n\t" \
  "v_mfma_f32_16x16x32_bf16 v[76:79], v[212:215], v[170:173], v[76:79]\n\t" \
  "ds_read_b64_tr_b16 v[212:213], v249 offset:72\n\t" \
  "v_and_b32_sdwa v219, s56, v218 dst_sel:DWORD dst_unused:UNUSED_PAD src0_sel:DWORD src1_sel:WORD_1\n\t" \
  "v_and_b32 v218, s56, v218\n\t" \
  "s_waitcnt lgkmcnt(4)\n\t" \
  "v_mfma_f32_16x16x32_bf16 v[8:11], v[208:211], v[146:149], v[8:11]\n\t" \
  "ds_read_b64_tr_b16 v[214:215], v249 offset:1608\n\t" \
  "v_and_b32_sdwa v221, s56, v220 dst_sel:DWORD dst_unused:UNUSED_PAD src0_sel:DWORD src1_sel:WORD_1\n\t" \
  "v_and_b32 v220, s56, v220\n\t" \
  "v_mfma_f32_16x16x32_bf16 v[32:35], v[208:211], v[154:157], v[32:35]\n\t" \
  "ds_read_b32 v228, v228 offset:0\n\t" \
  "ds_read_b32 v217, v217 offset:0\n\t" \
  "v_and_b32_sdwa v223, s56, v222 dst_sel:DWORD dst_unused:UNUSED_PAD src0_sel:DWORD src1_sel:WORD_1\n\t" \
  "v_and_b32 v222, s56, v222\n\t" \
  "s_waitcnt lgkmcnt(6)\n\t" \
  "v_fma_f32 v195, |v195|, v225, v195 mul:4\n\t" \
  "v_mfma_f32_16x16x32_bf16 v[56:59], v[208:211], v[162:165], v[56:59]\n\t" \
  "ds_read_b32 v216, v216 offset:0\n\t" \
  "ds_read_b32 v219, v219 offset:0\n\t" \
  "v_fma_f32 v194, |v194|, v224, v194 mul:4\n\t" \
  "v_fma_f32 v203, |v203|, v227, v203 mul:4\n\t" \
  "s_waitcnt lgkmcnt(3)\n\t" \
  "v_fma_f32 v202, |v202|, v226, v202 mul:4\n\t" \
  "v_mfma_f32_16x16x32_bf16 v[80:83], v[208:211], v[170:173], v[80:83]\n\t" \
  "ds_read_b64_tr_b16 v[208:209], v249 offset:128\n\t" \
  "ds_read_b64_tr_b16 v[210:211], v249 offset:1664\n\t" \
  "v_fma_f32 v201, |v201|, v229, v201 mul:4\n\t" \
  "v_fma_f32 v200, |v200|, v228, v200 mul:4\n\t" \
  "v_fma_mix_f32 v176, v176, v233, 0 op_sel_hi:[0,1,0]\n\t" \
  "v_fma_mix_f32 v177, v177, v232, 0 op_sel_hi:[0,1,0]\n\t" \
  "v_mfma_f32_16x16x32_bf16 v[12:15], v[212:215], v[146:149], v[12:15]\n\t" \
  "ds_read_b32 v218, v218 offset:0\n\t" \
  "s_cbranch_vccnz .Lcnx_p12_2\n\ts_setprio 2\n.Lcnx_p12_2:\n\t" \
  "v_pk_fma_f16 v234, v234, s55, s55\n\t" \
  "v_cvt_pk_bf16_f32 v178, v176, v177\n\t" \
  "v_cvt_pk_bf16_f32 v195, v194, v195\n\t" \
  "v_cvt_pk_bf16_f32 v203, v202, v203\n\t" \
  "v_cvt_pk_bf16_f32 v202, v200, v201\n\t" \
  "v_mfma_f32_16x16x32_bf16 v[36:39], v[212:215], v[154:157], v[36:39]\n\t" \
  "ds_read_b32 v221, v221 offset:0\n\t" \
  "v_rcp_f16 v235, v234\n\t" \
  "v_cvt_pk_f16_f32 v224, |v196|, |v197| clamp\n\t" \
  "v_cvt_pk_f16_f32 v226, |v198|, |v199| clamp\n\t" \
  "v_cvt_pk_f16_f32 v228, |v204|, |v205| clamp\n\t" \
  "v_mfma_f32_16x16x32_bf16 v[60:63], v[212:215], v[162:165], v[60:63]\n\t" \
  "ds_read_b32 v220, v220 offset:0\n\t" \
  "v_cvt_pk_f16_f32 v230, |v206|, |v207| clamp\n\t" \
  "v_rcp_f16_sdwa v234, v234 dst_sel:DWORD dst_unused:UNUSED_PAD src0_sel:WORD_1\n\t" \
  "v_and_b32_sdwa v225, s56, v224 dst_sel:DWORD dst_unused:UNUSED_PAD src0_sel:DWORD src1_sel:WORD_1\n\t" \
  "v_and_b32 v224, s56, v224\n\t" \
  "v_mfma_f32_16x16x32_bf16 v[84:87], v[212:215], v[170:173], v[84:87]\n\t" \
  "ds_read_b64_tr_b16 v[212:213], v249 offset:136\n\t" \
  "v_and_b32_sdwa v227, s56, v226 dst_sel:DWORD dst_unused:UNUSED_PAD src0_sel:DWORD src1_sel:WORD_1\n\t" \
  "v_and_b32 v226, s56, v226\n\t" \
  "s_waitcnt lgkmcnt(4)\n\t" \
  "v_mfma_f32_16x16x32_bf16 v[16:19], v[208:211], v[146:149], v[16:19]\n\t" \
  "ds_read_b64_tr_b16 v[214:215], v249 offset:1672\n\t" \
  "ds_read_b32 v223, v223 offset:0\n\t" \
  "v_and_b32_sdwa v229, s56, v228 dst_sel:DWORD dst_unused:UNUSED_PAD src0_sel:DWORD src1_sel:WORD_1\n\t" \
  "v_and_b32 v228, s56, v228\n\t" \
  "v_and_b32_sdwa v231, s56, v230 dst_sel:DWORD dst_unused:UNUSED_PAD src0_sel:DWORD src1_sel:WORD_1\n\t" \
  "v_mfma_f32_16x16x32_bf16 v[40:43], v[208:211], v[154:157], v[40:43]\n\t" \
  "s_waitcnt vmcnt(0)\n\t" \
  "s_mov_b64 exec, 3\n\t" \
  "ds_add_rtn_u32 v246, v251, v250 offset:47752\n\t" \
  "s_mov_b64 exec, -1\n\t" \
  "ds_read_b32 v222, v222 offset:0\n\t" \
  "ds_read_b32 v225, v225 offset:0\n\t" \
  "v_and_b32 v230, s56, v230\n\t" \
  "s_waitcnt lgkmcnt(8)\n\t" \
  "v_fma_f32 v181, |v181|, v217, v181 mul:4\n\t" \
  "v_mfma_f32_16x16x32_bf16 v[64:67], v[208:211], v[162:165], v[64:67]\n\t" \
  "ds_read_b32 v224, v224 offset:0\n\t" \
  "ds_read_b32 v227, v227 offset:0\n\t" \
  "v_fma_f32 v180, |v180|, v216, v180 mul:4\n\t" \
  "v_fma_f32 v183, |v183|, v219, v183 mul:4\n\t" \
  "v_fma_f32 v182, |v182|, v218, v182 mul:4\n\t" \
  "s_cbranch_vccnz .Lcnx_p12_3\n\ts_setprio 3\n.Lcnx_p12_3:\n\t" \
  "v_mfma_f32_16x16x32_bf16 v[88:91], v[208:211], v[170:173], v[88:91]\n\t" \
  "s_waitcnt lgkmcnt(4)\n\t" \
  "v_readfirstlane_b32 s4, v246\n\t" \
  "s_cmp_lt_u32 s4, s57\n\t" \
  "s_cbranch_scc1 .Lcnx_w138\n.Lcnx_b138:\n\t" \
  "ds_read_b64_tr_b16 v[208:209], v249 offset:6152\n\t" \
  "ds_read_b64_tr_b16 v[210:211], v249 offset:7688\n\t" \
  "s_waitcnt lgkmcnt(5)\n\t" \
  "v_fma_f32 v189, |v189|, v221, v189 mul:4\n\t" \
  "v_fma_f32 v188, |v188|, v220, v188 mul:4\n\t" \
  "v_fma_f32 v191, |v191|, v223, v191 mul:4\n\t" \
  "v_fma_f32 v190, |v190|, v222, v190 mul:4\n\t" \
  "v_fma_mix_f32 v192, v192, v235, 0 op_sel_hi:[0,1,0]\n\t" \
  "v_mfma_f32_16x16x32_bf16 v[20:23], v[212:215], v[146:149], v[20:23]\n\t" \
  "ds_read_b32 v226, v226 offset:0\n\t" \
  "v_fma_mix_f32 v193, v193, v234, 0 op_sel_hi:[0,1,0]\n\t" \
  "v_cvt_pk_bf16_f32 v194, v192, v193\n\t" \
  "v_cvt_pk_bf16_f32 v180, v180, v181\n\t" \
  "v_cvt_pk_bf16_f32 v181, v182, v183\n\t" \
  "v_mfma_f32_16x16x32_bf16 v[44:47], v[212:215], v[154:157], v[44:47]\n\t" \
  "ds_read_b32 v229, v229 offset:0\n\t" \
  "v_cvt_pk_bf16_f32 v188, v188, v189\n\t" \
  "v_cvt_pk_bf16_f32 v189, v190, v191\n\t" \
  "ds_read_b32 v228, v228 offset:0\n\t" \
  "ds_read_b32 v231, v231 offset:0\n\t" \
  "ds_read_b32 v230, v230 offset:0\n\t" \
  "s_waitcnt lgkmcnt(4)\n\t" \
  "v_fma_f32 v197, |v197|, v225, v197 mul:4\n\t" \
  "v_fma_f32 v196, |v196|, v224, v196 mul:4\n\t" \
  "v_fma_f32 v199, |v199|, v227, v199 mul:4\n\t" \
  "v_mfma_f32_16x16x32_bf16 v[68:71], v[212:215], v[162:165], v[68:71]\n\t" \
  "v_fma_f32 v198, |v198|, v226, v198 mul:4\n\t" \
  "s_waitcnt lgkmcnt(0)\n\t" \
  "v_fma_f32 v205, |v205|, v229, v205 mul:4\n\t" \
  "v_fma_f32 v204, |v204|, v228, v204 mul:4\n\t" \
  "v_fma_f32 v207, |v207|, v231, v207 mul:4\n\t" \
  "v_mfma_f32_16x16x32_bf16 v[92:95], v[212:215], v[170:173], v[92:95]\n\t" \
  "ds_read_b64_tr_b16 v[212:213], v249 offset:6160\n\t" \
  "v_fma_f32 v206, |v206|, v230, v206 mul:4\n\t" \
  "v_cvt_pk_bf16_f32 v196, v196, v197\n\t" \
  "v_cvt_pk_bf16_f32 v197, v198, v199\n\t" \
  "v_cvt_pk_bf16_f32 v204, v204, v205\n\t" \
  "v_cvt_pk_bf16_f32 v205, v206, v207\n\t" \
  "s_cmp_lg_u32 s58, 0\n\t" \
  "s_cbranch_scc1 .Lcnx_fin\n\t" \
  "v_mfma_f32_32x32x16_bf16 v[144:159], v[242:245], v[240:243], 0\n\t" \
  "v_mfma_f32_16x16x32_bf16 v[88:91], v[242:245], v[236:239], v[88:91]\n\t" \
  "ds_read_b64_tr_b16 v[214:215], v249 offset:7696\n\t" \
  "v_mfma_f32_16x16x32_bf16 v[92:95], v[240:243], v[236:239], v[92:95]\n\t" \
  "ds_read_b64 v[246:247], v251 offset:47136\n\t" \
  "v_mfma_f32_32x32x16_bf16 v[160:175], v[242:245], v[240:243], 0\n\t" \
  "v_mfma_f32_16x16x32_bf16 v[0:3], v[208:211], v[178:181], v[0:3]\n\t" \
  "s_waitcnt lgkmcnt(0)\n\t" \
  "v_add_u32 v246, s52, v246\n\t" \
  "v_mfma_f32_16x16x32_bf16 v[24:27], v[208:211], v[186:189], v[24:27]\n\t" \
  "v_mfma_f32_16x16x32_bf16 v[48:51], v[208:211], v[194:197], v[48:51]\n\t" \
  "v_add_u32 v247, s53, v247\n\t" \
  "v_mfma_f32_16x16x32_bf16 v[72:75], v[208:211], v[202:205], v[72:75]\n\t" \
  "ds_read_b64_tr_b16 v[208:209], v249 offset:6216\n\t" \
  "v_add_u32 v232, 1536, v247\n\t" \
  "v_mfma_f32_16x16x32_bf16 v[4:7], v[212:215], v[178:181], v[4:7]\n\t" \
  "ds_read_b64_tr_b16 v[210:211], v249 offset:7752\n\t" \
  "v_mfma_f32_16x16x32_bf16 v[28:31], v[212:215], v[186:189], v[28:31]\n\t" \
  "v_mfma_f32_16x16x32_bf16 v[52:55], v[212:215], v[194:197], v[52:55]\n\t" \
  "ds_read2_b32 v[98:99], v247 offset0:0 offset1:6\n\t" \
  "v_mfma_f32_16x16x32_bf16 v[76:79], v[212:215], v[202:205], v[76:79]\n\t" \
  "ds_read_b64_tr_b16 v[212:213], v249 offset:6224\n\t" \
  "s_waitcnt lgkmcnt(2)\n\t" \
  "v_mfma_f32_16x16x32_bf16 v[8:11], v[208:211], v[178:181], v[8:11]\n\t" \
  "ds_read_b64_tr_b16 v[214:215], v249 offset:7760\n\t" \
  "v_mfma_f32_16x16x32_bf16 v[32:35], v[208:211], v[186:189], v[32:35]\n\t" \
  "ds_read2_b32 v[114:115], v247 offset0:192 offset1:198\n\t" \
  "v_mfma_f32_16x16x32_bf16 v[56:59], v[208:211], v[194:197], v[56:59]\n\t" \
  "ds_read2_b32 v[130:131], v232 offset0:0 offset1:6\n\t" \
  "v_mfma_f32_16x16x32_bf16 v[80:83], v[208:211], v[202:205], v[80:83]\n\t" \
  "ds_read_b64_tr_b16 v[208:209], v249 offset:6280\n\t" \
  "s_waitcnt lgkmcnt(3)\n\t" \
  "v_mfma_f32_16x16x32_bf16 v[12:15], v[212:215], v[178:181], v[12:15]\n\t" \
  "ds_read_b64_tr_b16 v[210:211], v249 offset:7816\n\t" \
  "v_mfma_f32_16x16x32_bf16 v[36:39], v[212:215], v[186:189], v[36:39]\n\t" \
  "ds_read2_b32 v[102:103], v247 offset0:12 offset1:18\n\t" \
  "v_mfma_f32_16x16x32_bf16 v[60:63], v[212:215], v[194:197], v[60:63]\n\t" \
  "ds_read2_b32 v[118:119], v247 offset0:204 offset1:210\n\t" \
  "v_mfma_f32_16x16x32_bf16 v[84:87], v[212:215], v[202:205], v[84:87]\n\t" \
  "ds_read_b64_tr_b16 v[212:213], v249 offset:6288\n\t" \
  "s_waitcnt lgkmcnt(3)\n\t" \
  "v_mfma_f32_16x16x32_bf16 v[16:19], v[208:211], v[178:181], v[16:19]\n\t" \
  "ds_read_b64_tr_b16 v[214:215], v249 offset:7824\n\t" \
  "v_mfma_f32_16x16x32_bf16 v[40:43], v[208:211], v[186:189], v[40:43]\n\t" \
  "ds_read2_b32 v[134:135], v232 offset0:12 offset1:18\n\t" \
  "v_mfma_f32_16x16x32_bf16 v[64:67], v[208:211], v[194:197], v[64:67]\n\t" \
  "ds_read2_b32 v[106:107], v247 offset0:24 offset1:30\n\t" \
  "v_mfma_f32_16x16x32_bf16 v[88:91], v[208:211], v[202:205], v[88:91]\n\t" \
  "ds_read2_b32 v[122:123], v247 offset0:216 offset1:222\n\t" \
  "s_waitcnt lgkmcnt(3)\n\t" \
  "v_mfma_f32_16x16x32_bf16 v[20:23], v[212:215], v[178:181], v[20:23]\n\t" \
  "ds_read2_b32 v[138:139], v232 offset0:24 offset1:30\n\t" \
  "v_mfma_f32_16x16x32_bf16 v[44:47], v[212:215], v[186:189], v[44:47]\n\t" \
  "ds_read2_b32 v[110:111], v247 offset0:36 offset1:42\n\t" \
  "v_mfma_f32_16x16x32_bf16 v[68:71], v[212:215], v[194:197], v[68:71]\n\t" \
  "ds_read2_b32 v[126:127], v247 offset0:228 offset1:234\n\t" \
  "v_bfe_u32 v236, v251, 3, 2\n\t" \
  "v_mfma_f32_16x16x32_bf16 v[92:95], v[212:215], v[202:205], v[92:95]\n\t" \
  "ds_read2_b32 v[142:143], v232 offset0:36 offset1:42\n\t" \
  "v_mul_u32_u24 v236, 0x5555, v236\n\t" \
  "s_nop 7\n\t" \
  "s_mov_b32 s57, 6144\n\t" \
  "s_mov_b32 s58, 1\n\t" \
  "s_mov_b32 s38, 0\n\t" \
  "s_mov_b32 s59, -86104\n\t" \
  "s_mov_b32 s60, -36904\n\t" \
  "s_mov_b32 s36, 2\n\t" \
  "s_mov_b32 s61, s28\n\t" \
  "s_mov_b32 s62, s29\n\t" \
  "s_add_u32 s28, s28, s65\n\t" \
  "s_add_u32 s29, s29, s65\n\t" \
  "s_branch .Lcnx_round\n\t" \
  ".Lcnx_w9:\n\t" \
  "ds_read_b32 v246, v251 offset:47656\n\t" \
  "s_waitcnt lgkmcnt(0)\n\t" \
  "v_readfirstlane_b32 s4, v246\n\t" \
  "s_cmp_lt_u32 s4, s57\n\t" \
  "s_cbranch_scc1 .Lcnx_w9\n\t" \
  "s_branch .Lcnx_b9\n\t" \
  ".Lcnx_w12:\n\t" \
  "ds_read_b32 v246, v251 offset:47664\n\t" \
  "s_waitcnt lgkmcnt(0)\n\t" \
  "v_readfirstlane_b32 s4, v246\n\t" \
  "s_cmp_lt_u32 s4, s57\n\t" \
  "s_cbranch_scc1 .Lcnx_w12\n\t" \
  "s_branch .Lcnx_b12\n\t" \
  ".Lcnx_w24:\n\t" \
  "ds_read_b32 v246, v251 offset:47672\n\t" \
  "s_waitcnt lgkmcnt(0)\n\t" \
  "v_readfirstlane_b32 s4, v246\n\t" \
  "s_cmp_lt_u32 s4, s57\n\t" \
  "s_cbranch_scc1 .Lcnx_w24\n\t" \
  "s_branch .Lcnx_b24\n\t" \
  ".Lcnx_w36:\n\t" \
  "ds_read_b32 v246, v251 offset:47680\n\t" \
  "s_waitcnt lgkmcnt(0)\n\t" \
  "v_readfirstlane_b32 s4, v246\n\t" \
  "s_cmp_lt_u32 s4, s57\n\t" \
  "s_cbranch_scc1 .Lcnx_w36\n\t" \
  "s_branch .Lcnx_b36\n\t" \
  ".Lcnx_w48:\n\t" \
  "ds_read_b32 v246, v251 offset:47688\n\t" \
  "s_waitcnt lgkmcnt(0)\n\t" \
  "v_readfirstlane_b32 s4, v246\n\t" \
  "s_cmp_lt_u32 s4, s57\n\t" \
  "s_cbranch_scc1 .Lcnx_w48\n\t" \
  "s_branch .Lcnx_b48\n\t" \
  ".Lcnx_w60:\n\t" \
  "ds_read_b32 v246, v251 offset:47696\n\t" \
  "s_waitcnt lgkmcnt(0)\n\t" \
  "v_readfirstlane_b32 s4, v246\n\t" \
  "s_cmp_lt_u32 s4, s57\n\t" \
  "s_cbranch_scc1 .Lcnx_w60\n\t" \
  "s_branch .Lcnx_b60\n\t" \
  ".Lcnx_w72:\n\t" \
  "ds_read_b32 v246, v251 offset:47704\n\t" \
  "s_waitcnt lgkmcnt(0)\n\t" \
  "v_readfirstlane_b32 s4, v246\n\t" \
  "s_cmp_lt_u32 s4, s57\n\t" \
  "s_cbranch_scc1 .Lcnx_w72\n\t" \
  "s_branch .Lcnx_b72\n\t" \
  ".Lcnx_w84:\n\t" \
  "ds_read_b32 v246, v251 offset:47712\n\t" \
  "s_waitcnt lgkmcnt(0)\n\t" \
  "v_readfirstlane_b32 s4, v246\n\t" \
  "s_cmp_lt_u32 s4, s57\n\t" \
  "s_cbranch_scc1 .Lcnx_w84\n\t" \
  "s_branch .Lcnx_b84\n\t" \
  ".Lcnx_w96:\n\t" \
  "ds_read_b32 v246, v251 offset:47720\n\t" \
  "s_waitcnt lgkmcnt(0)\n\t" \
  "v_readfirstlane_b32 s4, v246\n\t" \
  "s_cmp_lt_u32 s4, s57\n\t" \
  "s_cbranch_scc1 .Lcnx_w96\n\t" \
  "s_branch .Lcnx_b96\n\t" \
  ".Lcnx_w108:\n\t" \
  "ds_read_b32 v246, v251 offset:47728\n\t" \
  "s_waitcnt lgkmcnt(0)\n\t" \
  "v_readfirstlane_b32 s4, v246\n\t" \
  "s_cmp_lt_u32 s4, s57\n\t" \
  "s_cbranch_scc1 .Lcnx_w108\n\t" \
  "s_branch .Lcnx_b108\n\t" \
  ".Lcnx_w120:\n\t" \
  "ds_read_b32 v246, v251 offset:47736\n\t" \
  "s_waitcnt lgkmcnt(0)\n\t" \
  "v_readfirstlane_b32 s4, v246\n\t" \
  "s_cmp_lt_u32 s4, s57\n\t" \
  "s_cbranch_scc1 .Lcnx_w120\n\t" \
  "s_branch .Lcnx_b120\n\t" \
  ".Lcnx_w132:\n\t" \
  "ds_read_b32 v246, v251 offset:47744\n\t" \
  "s_waitcnt lgkmcnt(0)\n\t" \
  "v_readfirstlane_b32 s4, v246\n\t" \
  "s_cmp_lt_u32 s4, s57\n\t" \
  "s_cbranch_scc1 .Lcnx_w132\n\t" \
  "s_branch .Lcnx_b132\n\t" \
  ".Lcnx_w138:\n\t" \
  "ds_read_b32 v246, v251 offset:47752\n\t" \
  "s_waitcnt lgkmcnt(0)\n\t" \
  "v_readfirstlane_b32 s4, v246\n\t" \
  "s_cmp_lt_u32 s4, s57\n\t" \
  "s_cbranch_scc1 .Lcnx_w138\n\t" \
  "s_branch .Lcnx_b138\n\t" \
  ".Lcnx_f0:\n\t" \
  "s_mov_b32 m0, s44\n\t" \
  "s_nop 0\n\t" \
  "buffer_load_dwordx4 v252, s[16:19], s33 offen lds\n\t" \
  "buffer_load_dwordx4 v252, s[16:19], s34 offen offset:1024 lds\n\t" \
  "buffer_load_dwordx4 v252, s[16:19], s35 offen offset:2048 lds\n\t" \
  "s_add_u32 s33, s33, s32\n\t" \
  "s_add_u32 s34, s34, s32\n\t" \
  "s_add_u32 s35, s35, s32\n\t" \
  "s_branch .Lcnx_g0\n\t" \
  ".Lcnx_f1:\n\t" \
  "s_mov_b32 m0, s45\n\t" \
  "s_nop 0\n\t" \
  "buffer_load_dwordx4 v252, s[16:19], s33 offen lds\n\t" \
  "buffer_load_dwordx4 v252, s[16:19], s34 offen offset:1024 lds\n\t" \
  "buffer_load_dwordx4 v252, s[16:19], s35 offen offset:2048 lds\n\t" \
  "s_add_u32 s33, s33, s32\n\t" \
  "s_add_u32 s34, s34, s32\n\t" \
  "s_add_u32 s35, s35, s32\n\t" \
  "s_branch .Lcnx_g1\n\t" \
  ".Lcnx_f2:\n\t" \
  "s_mov_b32 m0, s46\n\t" \
  "s_nop 0\n\t" \
  "buffer_load_dwordx4 v252, s[16:19], s33 offen lds\n\t" \
  "buffer_load_dwordx4 v252, s[16:19], s34 offen offset:1024 lds\n\t" \
  "buffer_load_dwordx4 v252, s[16:19], s35 offen offset:2048 lds\n\t" \
  "s_add_u32 s33, s33, s32\n\t" \
  "s_add_u32 s34, s34, s32\n\t" \
  "s_add_u32 s35, s35, s32\n\t" \
  "s_branch .Lcnx_g2\n\t" \
  ".Lcnx_f3:\n\t" \
  "s_mov_b32 m0, s47\n\t" \
  "s_nop 0\n\t" \
  "buffer_load_dwordx4 v252, s[16:19], s33 offen lds\n\t" \
  "buffer_load_dwordx4 v252, s[16:19], s34 offen offset:1024 lds\n\t" \
  "buffer_load_dwordx4 v252, s[16:19], s35 offen offset:2048 lds\n\t" \
  "s_add_u32 s33, s33, s32\n\t" \
  "s_add_u32 s34, s34, s32\n\t" \
  "s_add_u32 s35, s35, s32\n\t" \
  "s_branch .Lcnx_g3\n\t" \
  ".Lcnx_f4:\n\t" \
  "s_mov_b32 m0, s44\n\t" \
  "s_nop 0\n\t" \
  "buffer_load_dwordx4 v252, s[16:19], s33 offen lds\n\t" \
  "buffer_load_dwordx4 v252, s[16:19], s34 offen offset:1024 lds\n\t" \
  "buffer_load_dwordx4 v252, s[16:19], s35 offen offset:2048 lds\n\t" \
  "s_add_u32 s33, s33, s32\n\t" \
  "s_add_u32 s34, s34, s32\n\t" \
  "s_add_u32 s35, s35, s32\n\t" \
  "s_cmp_lg_u32 s58, 1\n\t" \
  "s_cbranch_scc1 .Lcnx_g4\n\t" \
  "s_add_u32 m0, s44, s50\n\t" \
  "s_add_u32 s4, s33, s51\n\t" \
  "buffer_load_dwordx4 v252, s[16:19], s4 offen offset:0 lds\n\t" \
  "s_add_u32 s4, s34, s51\n\t" \
  "buffer_load_dwordx4 v252, s[16:19], s4 offen offset:1024 lds\n\t" \
  "s_add_u32 s4, s35, s51\n\t" \
  "buffer_load_dwordx4 v252, s[16:19], s4 offen offset:2048 lds\n\t" \
  "s_branch .Lcnx_g4\n\t" \
  ".Lcnx_f5:\n\t" \
  "s_mov_b32 m0, s45\n\t" \
  "s_nop 0\n\t" \
  "buffer_load_dwordx4 v252, s[16:19], s33 offen lds\n\t" \
  "buffer_load_dwordx4 v252, s[16:19], s34 offen offset:1024 lds\n\t" \
  "buffer_load_dwordx4 v252, s[16:19], s35 offen offset:2048 lds\n\t" \
  "s_add_u32 s33, s33, s32\n\t" \
  "s_add_u32 s34, s34, s32\n\t" \
  "s_add_u32 s35, s35, s32\n\t" \
  "s_cmp_lg_u32 s58, 1\n\t" \
  "s_cbranch_scc1 .Lcnx_g5\n\t" \
  "s_add_u32 m0, s45, s50\n\t" \
  "s_add_u32 s4, s33, s51\n\t" \
  "buffer_load_dwordx4 v252, s[16:19], s4 offen offset:0 lds\n\t" \
  "s_add_u32 s4, s34, s51\n\t" \
  "buffer_load_dwordx4 v252, s[16:19], s4 offen offset:1024 lds\n\t" \
  "s_add_u32 s4, s35, s51\n\t" \
  "buffer_load_dwordx4 v252, s[16:19], s4 offen offset:2048 lds\n\t" \
  "s_branch .Lcnx_g5\n\t" \
  ".Lcnx_f6:\n\t" \
  "s_mov_b32 m0, s46\n\t" \
  "s_nop 0\n\t" \
  "buffer_load_dwordx4 v252, s[16:19], s33 offen lds\n\t" \
  "buffer_load_dwordx4 v252, s[16:19], s34 offen offset:1024 lds\n\t" \
  "buffer_load_dwordx4 v252, s[16:19], s35 offen offset:2048 lds\n\t" \
  "s_add_u32 s33, s33, s32\n\t" \
  "s_add_u32 s34, s34, s32\n\t" \
  "s_add_u32 s35, s35, s32\n\t" \
  "s_cmp_lg_u32 s58, 1\n\t" \
  "s_cbranch_scc1 .Lcnx_g6\n\t" \
  "s_cmp_lt_u32 s41, 1024\n\t" \
  "s_cbranch_scc1 .Lcnx_g6\n\t" \
  "s_add_u32 m0, s46, s50\n\t" \
  "s_add_u32 s4, s33, s51\n\t" \
  "buffer_load_dwordx4 v252, s[16:19], s4 offen offset:0 lds\n\t" \
  "s_add_u32 s4, s34, s51\n\t" \
  "buffer_load_dwordx4 v252, s[16:19], s4 offen offset:1024 lds\n\t" \
  "s_add_u32 s4, s35, s51\n\t" \
  "buffer_load_dwordx4 v252, s[16:19], s4 offen offset:2048 lds\n\t" \
  "s_branch .Lcnx_g6\n\t" \
  ".Lcnx_f7:\n\t" \
  "s_mov_b32 m0, s47\n\t" \
  "s_nop 0\n\t" \
  "buffer_load_dwordx4 v252, s[16:19], s33 offen lds\n\t" \
  "buffer_load_dwordx4 v252, s[16:19], s34 offen offset:1024 lds\n\t" \
  "buffer_load_dwordx4 v252, s[16:19], s35 offen offset:2048 lds\n\t" \
  "s_add_u32 s33, s33, s32\n\t" \
  "s_add_u32 s34, s34, s32\n\t" \
  "s_add_u32 s35, s35, s32\n\t" \
  "s_cmp_lg_u32 s58, 1\n\t" \
  "s_cbranch_scc1 .Lcnx_g7\n\t" \
  "s_cmp_lt_u32 s41, 1024\n\t" \
  "s_cbranch_scc1 .Lcnx_g7\n\t" \
  "s_add_u32 m0, s47, s50\n\t" \
  "s_add_u32 s4, s33, s51\n\t" \
  "buffer_load_dwordx4 v252, s[16:19], s4 offen offset:0 lds\n\t" \
  "s_add_u32 s4, s34, s51\n\t" \
  "buffer_load_dwordx4 v252, s[16:19], s4 offen offset:1024 lds\n\t" \
  "s_add_u32 s4, s35, s51\n\t" \
  "buffer_load_dwordx4 v252, s[16:19], s4 offen offset:2048 lds\n\t" \
  "s_branch .Lcnx_g7\n\t" \
  ".Lcnx_f8:\n\t" \
  "s_mov_b32 m0, s44\n\t" \
  "s_nop 0\n\t" \
  "buffer_load_dwordx4 v252, s[16:19], s33 offen lds\n\t" \
  "buffer_load_dwordx4 v252, s[16:19], s34 offen offset:1024 lds\n\t" \
  "buffer_load_dwordx4 v252, s[16:19], s35 offen offset:2048 lds\n\t" \
  "s_add_u32 s33, s33, s32\n\t" \
  "s_add_u32 s34, s34, s32\n\t" \
  "s_add_u32 s35, s35, s32\n\t" \
  "s_branch .Lcnx_g8\n\t" \
  ".Lcnx_f9:\n\t" \
  "s_mov_b32 m0, s45\n\t" \
  "s_nop 0\n\t" \
  "buffer_load_dwordx4 v252, s[16:19], s33 offen lds\n\t" \
  "buffer_load_dwordx4 v252, s[16:19], s34 offen offset:1024 lds\n\t" \
  "buffer_load_dwordx4 v252, s[16:19], s35 offen offset:2048 lds\n\t" \
  "s_add_u32 s33, s33, s32\n\t" \
  "s_add_u32 s34, s34, s32\n\t" \
  "s_add_u32 s35, s35, s32\n\t" \
  "s_sub_u32 s33, s33, s48\n\t" \
  "s_sub_u32 s34, s34, s48\n\t" \
  "s_sub_u32 s35, s35, s48\n\t" \
  "s_branch .Lcnx_g9\n\t" \
  ".Lcnx_f10:\n\t" \
  "s_mov_b32 m0, s46\n\t" \
  "s_nop 0\n\t" \
  "buffer_load_dwordx4 v252, s[16:19], s33 offen lds\n\t" \
  "buffer_load_dwordx4 v252, s[16:19], s34 offen offset:1024 lds\n\t" \
  "buffer_load_dwordx4 v252, s[16:19], s35 offen offset:2048 lds\n\t" \
  "s_add_u32 s33, s33, s32\n\t" \
  "s_add_u32 s34, s34, s32\n\t" \
  "s_add_u32 s35, s35, s32\n\t" \
  "s_branch .Lcnx_g10\n\t" \
  ".Lcnx_f11:\n\t" \
  "s_mov_b32 m0, s47\n\t" \
  "s_nop 0\n\t" \
  "buffer_load_dwordx4 v252, s[16:19], s33 offen lds\n\t" \
  "buffer_load_dwordx4 v252, s[16:19], s34 offen offset:1024 lds\n\t" \
  "buffer_load_dwordx4 v252, s[16:19], s35 offen offset:2048 lds\n\t" \
  "s_add_u32 s33, s33, s32\n\t" \
  "s_add_u32 s34, s34, s32\n\t" \
  "s_add_u32 s35, s35, s32\n\t" \
  "s_sub_u32 s33, s33, s49\n\t" \
  "s_sub_u32 s34, s34, s49\n\t" \
  "s_sub_u32 s35, s35, s49\n\t" \
  "s_branch .Lcnx_g11\n\t" \
  ".Lcnx_fin:\n\t" \
  "v_mfma_f32_16x16x32_bf16 v[88:91], v[242:245], v[236:239], v[88:91]\n\t" \
  "ds_read_b64_tr_b16 v[214:215], v249 offset:7696\n\t" \
  "v_mfma_f32_16x16x32_bf16 v[92:95], v[240:243], v[236:239], v[92:95]\n\t" \
  "v_mfma_f32_16x16x32_bf16 v[0:3], v[208:211], v[178:181], v[0:3]\n\t" \
  "v_mfma_f32_16x16x32_bf16 v[24:27], v[208:211], v[186:189], v[24:27]\n\t" \
  "v_mfma_f32_16x16x32_bf16 v[48:51], v[208:211], v[194:197], v[48:51]\n\t" \
  "v_mfma_f32_16x16x32_bf16 v[72:75], v[208:211], v[202:205], v[72:75]\n\t" \
  "ds_read_b64_tr_b16 v[208:209], v249 offset:6216\n\t" \
  "s_waitcnt lgkmcnt(1)\n\t" \
  "v_mfma_f32_16x16x32_bf16 v[4:7], v[212:215], v[178:181], v[4:7]\n\t" \
  "ds_read_b64_tr_b16 v[210:211], v249 offset:7752\n\t" \
  "v_mfma_f32_16x16x32_bf16 v[28:31], v[212:215], v[186:189], v[28:31]\n\t" \
  "v_mfma_f32_16x16x32_bf16 v[52:55], v[212:215], v[194:197], v[52:55]\n\t" \
  "v_mfma_f32_16x16x32_bf16 v[76:79], v[212:215], v[202:205], v[76:79]\n\t" \
  "ds_read_b64_tr_b16 v[212:213], v249 offset:6224\n\t" \
  "s_waitcnt lgkmcnt(1)\n\t" \
  "v_mfma_f32_16x16x32_bf16 v[8:11], v[208:211], v[178:181], v[8:11]\n\t" \
  "ds_read_b64_tr_b16 v[214:215], v249 offset:7760\n\t" \
  "v_mfma_f32_16x16x32_bf16 v[32:35], v[208:211], v[186:189], v[32:35]\n\t" \
  "v_mfma_f32_16x16x32_bf16 v[56:59], v[208:211], v[194:197], v[56:59]\n\t" \
  "v_mfma_f32_16x16x32_bf16 v[80:83], v[208:211], v[202:205], v[80:83]\n\t" \
  "ds_read_b64_tr_b16 v[208:209], v249 offset:6280\n\t" \
  "v_cvt_pk_bf16_f32 v0, v0, v1\n\t" \
  "v_cvt_pk_bf16_f32 v1, v2, v3\n\t" \
  "v_cvt_pk_bf16_f32 v2, v4, v5\n\t" \
  "v_cvt_pk_bf16_f32 v3, v6, v7\n\t" \
  "buffer_store_dwordx4 v[0:3], v250, s[20:23], s28 offen offset:0 sc1\n\t" \
  "v_cvt_pk_bf16_f32 v24, v24, v25\n\t" \
  "v_cvt_pk_bf16_f32 v25, v26, v27\n\t" \
  "v_cvt_pk_bf16_f32 v26, v28, v29\n\t" \
  "v_cvt_pk_bf16_f32 v27, v30, v31\n\t" \
  "buffer_store_dwordx4 v[24:27], v250, s[20:23], s28 offen offset:192 sc1\n\t" \
  "v_cvt_pk_bf16_f32 v48, v48, v49\n\t" \
  "v_cvt_pk_bf16_f32 v49, v50, v51\n\t" \
  "v_cvt_pk_bf16_f32 v50, v52, v53\n\t" \
  "v_cvt_pk_bf16_f32 v51, v54, v55\n\t" \
  "buffer_store_dwordx4 v[48:51], v250, s[20:23], s29 offen offset:0 sc1\n\t" \
  "v_cvt_pk_bf16_f32 v72, v72, v73\n\t" \
  "v_cvt_pk_bf16_f32 v73, v74, v75\n\t" \
  "v_cvt_pk_bf16_f32 v74, v76, v77\n\t" \
  "v_cvt_pk_bf16_f32 v75, v78, v79\n\t" \
  "buffer_store_dwordx4 v[72:75], v250, s[20:23], s29 offen offset:192 sc1\n\t" \
  "s_waitcnt lgkmcnt(1)\n\t" \
  "v_mfma_f32_16x16x32_bf16 v[12:15], v[212:215], v[178:181], v[12:15]\n\t" \
  "ds_read_b64_tr_b16 v[210:211], v249 offset:7816\n\t" \
  "v_mfma_f32_16x16x32_bf16 v[36:39], v[212:215], v[186:189], v[36:39]\n\t" \
  "v_mfma_f32_16x16x32_bf16 v[60:63], v[212:215], v[194:197], v[60:63]\n\t" \
  "v_mfma_f32_16x16x32_bf16 v[84:87], v[212:215], v[202:205], v[84:87]\n\t" \
  "ds_read_b64_tr_b16 v[212:213], v249 offset:6288\n\t" \
  "s_waitcnt lgkmcnt(1)\n\t" \
  "v_mfma_f32_16x16x32_bf16 v[16:19], v[208:211], v[178:181], v[16:19]\n\t" \
  "ds_read_b64_tr_b16 v[214:215], v249 offset:7824\n\t" \
  "v_mfma_f32_16x16x32_bf16 v[40:43], v[208:211], v[186:189], v[40:43]\n\t" \
  "v_mfma_f32_16x16x32_bf16 v[64:67], v[208:211], v[194:197], v[64:67]\n\t" \
  "v_mfma_f32_16x16x32_bf16 v[88:91], v[208:211], v[202:205], v[88:91]\n\t" \
  "v_cvt_pk_bf16_f32 v8, v8, v9\n\t" \
  "v_cvt_pk_bf16_f32 v9, v10, v11\n\t" \
  "v_cvt_pk_bf16_f32 v10, v12, v13\n\t" \
  "v_cvt_pk_bf16_f32 v11, v14, v15\n\t" \
  "buffer_store_dwordx4 v[8:11], v250, s[20:23], s28 offen offset:64 sc1\n\t" \
  "v_cvt_pk_bf16_f32 v32, v32, v33\n\t" \
  "v_cvt_pk_bf16_f32 v33, v34, v35\n\t" \
  "v_cvt_pk_bf16_f32 v34, v36, v37\n\t" \
  "v_cvt_pk_bf16_f32 v35, v38, v39\n\t" \
  "buffer_store_dwordx4 v[32:35], v250, s[20:23], s28 offen offset:256 sc1\n\t" \
  "v_cvt_pk_bf16_f32 v56, v56, v57\n\t" \
  "v_cvt_pk_bf16_f32 v57, v58, v59\n\t" \
  "v_cvt_pk_bf16_f32 v58, v60, v61\n\t" \
  "v_cvt_pk_bf16_f32 v59, v62, v63\n\t" \
  "buffer_store_dwordx4 v[56:59], v250, s[20:23], s29 offen offset:64 sc1\n\t" \
  "v_cvt_pk_bf16_f32 v80, v80, v81\n\t" \
  "v_cvt_pk_bf16_f32 v81, v82, v83\n\t" \
  "v_cvt_pk_bf16_f32 v82, v84, v85\n\t" \
  "v_cvt_pk_bf16_f32 v83, v86, v87\n\t" \
  "buffer_store_dwordx4 v[80:83], v250, s[20:23], s29 offen offset:256 sc1\n\t" \
  "s_waitcnt lgkmcnt(0)\n\t" \
  "v_mfma_f32_16x16x32_bf16 v[20:23], v[212:215], v[178:181], v[20:23]\n\t" \
  "v_mfma_f32_16x16x32_bf16 v[44:47], v[212:215], v[186:189], v[44:47]\n\t" \
  "v_mfma_f32_16x16x32_bf16 v[68:71], v[212:215], v[194:197], v[68:71]\n\t" \
  "v_mfma_f32_16x16x32_bf16 v[92:95], v[212:215], v[202:205], v[92:95]\n\t" \
  "v_cvt_pk_bf16_f32 v16, v16, v17\n\t" \
  "v_cvt_pk_bf16_f32 v17, v18, v19\n\t" \
  "s_nop 2\n\t" \
  "v_cvt_pk_bf16_f32 v18, v20, v21\n\t" \
  "v_cvt_pk_bf16_f32 v19, v22, v23\n\t" \
  "buffer_store_dwordx4 v[16:19], v250, s[20:23], s28 offen offset:128 sc1\n\t" \
  "v_cvt_pk_bf16_f32 v40, v40, v41\n\t" \
  "v_cvt_pk_bf16_f32 v41, v42, v43\n\t" \
  "v_cvt_pk_bf16_f32 v42, v44, v45\n\t" \
  "v_cvt_pk_bf16_f32 v43, v46, v47\n\t" \
  "buffer_store_dwordx4 v[40:43], v250, s[20:23], s28 offen offset:320 sc1\n\t" \
  "v_cvt_pk_bf16_f32 v64, v64, v65\n\t" \
  "v_cvt_pk_bf16_f32 v65, v66, v67\n\t" \
  "v_cvt_pk_bf16_f32 v66, v68, v69\n\t" \
  "v_cvt_pk_bf16_f32 v67, v70, v71\n\t" \
  "buffer_store_dwordx4 v[64:67], v250, s[20:23], s29 offen offset:128 sc1\n\t" \
  "v_cvt_pk_bf16_f32 v88, v88, v89\n\t" \
  "v_cvt_pk_bf16_f32 v89, v90, v91\n\t" \
  "v_cvt_pk_bf16_f32 v90, v92, v93\n\t" \
  "v_cvt_pk_bf16_f32 v91, v94, v95\n\t" \
  "buffer_store_dwordx4 v[88:91], v250, s[20:23], s29 offen offset:320 sc1\n\t" \
  "s_waitcnt vmcnt(0)\n\t" \
  ".Lcnx_end:\n\t" \
  ""


__device__ __forceinline__ unsigned lds_addr(const char* p) { return (unsigned)(uintptr_t)(lds_ptr_t)p; }
__device__ __forceinline__ unsigned sg(unsigned v) { return __builtin_amdgcn_readfirstlane(v); }

struct TanhTab { float v[1024]; };
constexpr TanhTab tanh_tab(int k0) {
    TanhTab t = {};
    for (int k = k0; k < k0 + 1024; ++k) {
        double y = ((k & 255) + 0.375 + (k >= 256 ? 256 : 0)) * 0x1p-22;
        for (int e = 1; e < (k >> 8); ++e) y *= 2;
        const double v = 8 * y, a = 2 * 0.79788456080286536 * (v + 0.044715 * v * v * v) / 1024;
        double m = 1;
        for (int i = 12; i > 1; --i) m = 1 + m * a / i;
        m *= a;
        for (int i = 0; i < 10; ++i) m *= m + 2;
        t.v[k - k0] = (float)(m / (m + 2));
    }
    return t;
}
struct WaveTab { unsigned v244[64][4], v16[64][4], s32[32]; };
struct RegTab { unsigned v240[64][4], v248[64][4]; WaveTab w[8]; };
constexpr RegTab reg_tab() {
    RegTab t = {};
    for (unsigned l = 0; l < 64; ++l) {
        const unsigned G = l >> 4, n = l & 15, q = n >> 2, p = l & 3;
        t.v240[l][2 + (p >> 1)] = G == q ? (p & 1 ? 0x3f800000u : 0x3f80u) : 0u;
        t.v248[l][0] = WB + (l >> 2) * 32 + (p & 1) * 16 + (p >> 1) * 8;
        t.v248[l][1] = WB + (G & 1) * 3080 + ((G >> 1) * 4 + q) * 192 + p * 16;
        t.v248[l][2] = n * 384 + G * 16;
        t.v248[l][3] = PB + l * 8;
        for (unsigned w = 0; w < 8; ++w) {
            t.w[w].v244[l][2] = XOFF + n * 400 + (G & 1) * XROW + (G >> 1) * 192 + w * 24;
            t.w[w].v244[l][3] = DWOFF + p * 192 + (1 - (q >> 1)) * 24 + (1 + (G & 1) - (q & 1)) * 8 + ((G ^ p) >> 1) * 4 + w * 2304;
            t.w[w].v16[l][0] = w >= 4 ? (l < 48 ? l * 128 : OOB)
                             : w < 2  ? (12 * ((l >> 3) & 3) + ((l >> 1) & 3)) * (HID * 2) + (2 * (l & 1) + (l >> 5)) * 16 : l * 16;
            const unsigned d = DWOFF + (w * 8 + (l >> 5) * 4 + p) * 768 + ((l >> 2) & 7) * 24;
            t.w[w].v16[l][1] = w < 3 ? d + (l & 1) * 192 : XOFF - 32 + p * 8;
            t.w[w].v16[l][2] = d + (~l & 1) * 192;
            t.w[w].v16[l][3] = l & 1 ? 0x07060302u : 0x05040100u;
        }
    }
    for (unsigned w = 0; w < 8; ++w) {
        const bool w1 = w < 2;
        const unsigned wpd = w1 ? 4 * HID * 2 - 1024 : 0, wl = WB + (w & 1) * (w1 ? 3072 : 3080), wc = w1 ? 64 : 6144;
        const unsigned so = (w & 1) * (w1 ? 48 * HID * 2 : 3072);
        const unsigned s[32] = {wc, so, so + wpd, so + 2 * wpd, w, w >> 2, w, XOFF - 8 * PB + w * 12288,
                                (w & 3) * 4096 + (w < 4 ? 16400 : 0), w * 512, XOFF - 2 * PB + w * 1024,
                                XOFF - 2 * PB + (8 + (w & 3)) * 1024 + (w >> 2) * 8,
                                wl + (w1 ? WA + 12304 : WA), wl + (w1 ? 12304 : WA + 6152), wl + (w1 ? WA + 18448 : 0),
                                wl + (w1 ? WA + 18448 + 6144 : 6152), w1 ? 12 * wc : 0, w1 ? 0 : 12 * wc,
                                w1 ? 0u - 86104 : 0u - 36904, 3 * wc, (w - 3) * 24, (w - 3) * 2304, GH1, GHH, 0x3ffc, 3072, 0, 0, 0,
                                OOB, OOB, (unsigned)sizeof(RegTab) - 2 * PB};
        for (int i = 0; i < 32; ++i) t.w[w].s32[i] = s[i];
    }
    return t;
}
struct Tables { RegTab r; TanhTab t[4]; };
static_assert(sizeof(WaveTab) == 64 * 34 && sizeof(RegTab) == 19456, "table layout");
__device__ const __attribute__((aligned(128))) Tables TABLES = {reg_tab(), {tanh_tab(0), tanh_tab(1024), tanh_tab(2048), tanh_tab(3072)}};

#define R10(r, a) r #a "0", r #a "1", r #a "2", r #a "3", r #a "4", r #a "5", r #a "6", r #a "7", r #a "8", r #a "9"
#define V10(a) R10("v", a)
#define S10(a) R10("s", a)
#define MAIN_CLOB "memory", "scc", "vcc", "v1", "v2", "v3", "v4", "v5", "v6", "v7", "v8", "v9", V10(1), V10(2), V10(3), V10(4), V10(5),  \
    V10(6), V10(7), V10(8), V10(9), V10(10), V10(11), V10(12), V10(13), V10(14), V10(15), V10(16), V10(17), V10(18), V10(19),        \
    V10(20), V10(21), V10(22), V10(23), V10(24), "v250", "v251", "v252", "v253", "v254", "s4", "s5", "s8", "s9", S10(1),       \
    S10(2), S10(3), S10(4), S10(5), "s60", "s61", "s62", "s63", "s64", "s65", "s66", "s67"

__global__ __launch_bounds__(512, 2) void convnext_kernel(const __hip_bfloat16* __restrict__ x,
                                                          const __hip_bfloat16* __restrict__ dw,
                                                          const __hip_bfloat16* __restrict__ w1,
                                                          const __hip_bfloat16* __restrict__ w2,
                                                          __hip_bfloat16* __restrict__ out) {
    __shared__ __attribute__((aligned(16))) char smem[LDS_BYTES];

    unsigned tid = threadIdx.x, lds = lds_addr(smem);
    const unsigned bid = blockIdx.x;
    asm volatile(MAIN_ASM
                 : "+{v0}"(tid), "+{s3}"(lds)
                 : "{s[0:1]}"(__builtin_amdgcn_kernarg_segment_ptr()), "{s2}"(bid), "{s[6:7]}"(&TABLES)
                 : MAIN_CLOB);

}

}

extern "C" void kernel_launch(void* const* d_in, const int* in_sizes, int n_in, void* d_out, int out_size,
                              void* d_ws, size_t ws_size, hipStream_t stream) {
    (void)in_sizes; (void)n_in; (void)out_size; (void)d_ws; (void)ws_size;
    const __hip_bfloat16* x = (const __hip_bfloat16*)d_in[0];
    const __hip_bfloat16* dw = (const __hip_bfloat16*)d_in[1];
    const __hip_bfloat16* w1 = (const __hip_bfloat16*)d_in[2];
    const __hip_bfloat16* w2 = (const __hip_bfloat16*)d_in[3];
    __hip_bfloat16* out = (__hip_bfloat16*)d_out;
    convnext_kernel<<<256, 512, 0, stream>>>(x, dw, w1, w2, out);
}
```

```cpp
#include <hip/hip_runtime.h>
#include <hip/hip_bf16.h>
#include <stdint.h>

namespace {

typedef __attribute__((address_space(3))) void* lds_ptr_t;

constexpr int IMG = 512;
constexpr int CH = 96;
constexpr int HID = 384;
constexpr int ROWB = IMG * CH * 2;
constexpr int LUTN = 3841;
constexpr int DWOFF = 15376;
constexpr int XOFF = DWOFF + 96 * 192 + 32;
constexpr int XROW = 8200;
constexpr int PB = XOFF + 10 * XROW;
constexpr int WB = XOFF + 9 * XROW;
constexpr int WA = 3 * XROW;
constexpr int TABOFF = WB + WA + 18448 + 12288;
constexpr int CNTOFF = TABOFF + 512;
constexpr int PFOFF = CNTOFF + 144;
constexpr int LDS_BYTES = 160 * 1024;
static_assert(PB == 115840 && CNTOFF - PB == 47648 && LUTN * 4 <= DWOFF && XOFF % 16 == 0 && PFOFF + 48 * 4 <= LDS_BYTES, "LDS map");
constexpr unsigned OOB = 0x40000000u;
constexpr unsigned GH1 = 0xcc9bcc9bu, GH2 = 0xd297d297u, GHH = 0x30003000u;

#define MAIN_ASM \
  "s_load_dwordx2 s[8:9], s[0:1], 0x0\n\t" \
  "s_load_dwordx2 s[12:13], s[0:1], 0x8\n\t" \
  "s_load_dwordx2 s[20:21], s[0:1], 0x20\n\t" \
  "v_readfirstlane_b32 s5, v0\n\t" \
  "v_and_b32 v21, 63, v0\n\t" \
  "s_mov_b32 m0, 0\n\t" \
  "s_cmp_lt_u32 s5, 128\n\t" \
  "s_cselect_b32 s3, 16, 24\n\t" \
  "v_lshlrev_b32 v22, 4, v21\n\t" \
  "s_load_dwordx2 s[16:17], s[0:1], s3\n\t" \
  "s_mul_i32 s4, s5, 34\n\t" \
  "s_add_u32 s66, s6, s4\n\t" \
  "s_addc_u32 s67, s7, 0\n\t" \
  "global_load_dwordx4 v[244:247], v22, s[66:67] offset:2048\n\t" \
  "global_load_dwordx4 v[16:19], v22, s[66:67] offset:3072\n\t" \
  "global_load_dwordx4 v[240:243], v22, s[6:7]\n\t" \
  "global_load_dwordx4 v[248:251], v22, s[6:7] offset:1024\n\t" \
  "s_cmp_lg_u32 s5, 192\n\t" \
  "s_cbranch_scc1 .Lcnx_nc\n\t" \
  "s_getpc_b64 s[14:15]\n\t" \
  ".Lcnx_pc:\n\t" \
  "s_lshr_b32 s3, s2, 3\n\t" \
  "s_lshl_b32 s3, s3, 11\n\t" \
  "v_lshl_add_u32 v1, v21, 5, s3\n\t" \
  "v_min_u32 v1, .Lcnx_end-.Lcnx_pc, v1\n\t" \
  "global_load_lds_dword v1, s[14:15]\n\t" \
  ".Lcnx_nc:\n\t" \
  "v_mul_u32_u24 v254, 0x147b, v0\n\t" \
  "s_bfe_u32 s27, s2, 0x40003\n\t" \
  "v_bfe_u32 v1, v0, 2, 3\n\t" \
  "s_mul_i32 s27, s27, 6144\n\t" \
  "v_lshrrev_b32 v254, 17, v254\n\t" \
  "s_sub_u32 s3, s27, 576\n\t" \
  "v_and_b32 v20, 3, v0\n\t" \
  "s_add_u32 s4, s27, 6720\n\t" \
  "v_sub_u32 v254, v0, v254\n\t" \
  "s_min_u32 s4, s4, 0x18000\n\t" \
  "v_and_b32 v2, 0x1e0, v0\n\t" \
  "s_and_b32 s24, s2, 7\n\t" \
  "v_lshl_add_u32 v254, v254, 4, s3\n\t" \
  "s_lshl_b32 s24, s24, 6\n\t" \
  "v_lshl_or_b32 v20, v20, 3, v2\n\t" \
  "s_mov_b32 s26, 0x18000\n\t" \
  "v_cmp_gt_u32 vcc, s4, v254\n\t" \
  "s_cmp_ge_u32 s2, 128\n\t" \
  "s_cselect_b32 s3, 7, 0\n\t" \
  "s_cselect_b32 s4, 0, 7\n\t" \
  "v_add_u32 v1, s3, v1\n\t" \
  "s_cselect_b32 s3, 28, 35\n\t" \
  "v_cndmask_b32 v254, 2.0, v254, vcc\n\t" \
  "s_cselect_b32 s26, s26, 0xfffe8000\n\t" \
  "v_xor_b32 v1, s4, v1\n\t" \
  "s_add_u32 s24, s24, s3\n\t" \
  "v_and_b32 v1, 7, v1\n\t" \
  "s_movk_i32 s3, 1344\n\t" \
  "s_mul_i32 s24, s24, 0x18000\n\t" \
  "v_mad_u32_u24 v1, v1, s3, v20\n\t" \
  "s_lshl4_add_u32 s25, s5, 33840\n\t" \
  "s_mov_b32 s10, 0x3000000\n\t" \
  "s_mov_b32 s11, 0x20000\n\t" \
  "s_mov_b32 s14, 9408\n\t" \
  "s_mov_b32 s15, 0x20000\n\t" \
  "s_mov_b32 s18, 73728\n\t" \
  "s_mov_b32 s19, 0x20000\n\t" \
  "s_mov_b32 s22, 0x3000000\n\t" \
  "s_mov_b32 s23, 0x20000\n\t" \
  "s_setreg_imm32_b32 hwreg(HW_REG_MODE, 4, 2), 0\n\t" \
  "s_setreg_imm32_b32 hwreg(HW_REG_MODE, 9, 1), 0\n\t" \
  "s_lshl_b32 s65, s26, 4\n\t" \
  "s_lshr_b32 s3, s5, 5\n\t" \
  "s_add_u32 s3, s3, 4\n\t" \
  "s_mul_i32 s3, s3, s26\n\t" \
  "s_add_u32 s28, s24, s27\n\t" \
  "s_add_u32 s28, s28, s3\n\t" \
  "s_add_u32 s29, s28, s26\n\t" \
  "s_bfe_u32 s3, s5, 0x20006\n\t" \
  "s_add_u32 s3, s3, 6\n\t" \
  "s_mul_i32 s3, s3, s26\n\t" \
  "s_add_u32 s30, s24, s65\n\t" \
  "s_add_u32 s30, s30, s3\n\t" \
  "s_lshl_b32 s31, s26, 2\n\t" \
  "s_mul_i32 s64, s26, 22\n\t" \
  "s_sub_u32 s64, s64, s65\n\t" \
  "v_mov_b32 v253, 0xd297d297\n\t" \
  "s_cmp_ge_u32 s5, 256\n\t" \
  "s_cselect_b32 s27, s27, 0\n\t" \
  "s_mov_b64 vcc, 0\n\t" \
  "s_waitcnt lgkmcnt(0)\n\t" \
  "s_and_b32 s13, s13, 0xffff\n\t" \
  "s_and_b32 s9, s9, 0xffff\n\t" \
  "s_and_b32 s17, s17, 0xffff\n\t" \
  "s_cmp_ge_u32 s5, 192\n\t" \
  "s_cbranch_scc1 .Lcnx_nd\n\t" \
  "buffer_load_dwordx2 v[2:3], v1, s[12:15], 0 offen offset:0\n\t" \
  "buffer_load_dwordx2 v[4:5], v1, s[12:15], 0 offen offset:192\n\t" \
  "buffer_load_dwordx2 v[6:7], v1, s[12:15], 0 offen offset:384\n\t" \
  "buffer_load_dwordx2 v[8:9], v1, s[12:15], 0 offen offset:576\n\t" \
  "buffer_load_dwordx2 v[10:11], v1, s[12:15], 0 offen offset:768\n\t" \
  "buffer_load_dwordx2 v[12:13], v1, s[12:15], 0 offen offset:960\n\t" \
  "buffer_load_dwordx2 v[14:15], v1, s[12:15], 0 offen offset:1152\n\t" \
  ".Lcnx_nd:\n\t" \
  "s_and_b32 s3, s5, 0x140\n\t" \
  "s_cmp_lg_u32 s3, 64\n\t" \
  "s_cbranch_scc1 .Lcnx_nw\n\t" \
  "s_lshr_b32 s3, s2, 3\n\t" \
  "s_mul_i32 s3, s3, 2304\n\t" \
  "s_mov_b32 m0, 0\n\t" \
  "v_mul_u32_u24 v21, 36, v21\n\t" \
  "buffer_load_dword v21, s[16:19], s3 offen lds\n\t" \
  ".Lcnx_nw:\n\t" \
  "s_add_u32 m0, s25, 0\n\t" \
  "s_add_u32 s24, s24, s26\n\t" \
  "buffer_load_dwordx4 v254, s[8:11], s24 offen nt lds\n\t" \
  "s_add_u32 m0, s25, 8200\n\t" \
  "s_add_u32 s24, s24, s26\n\t" \
  "buffer_load_dwordx4 v254, s[8:11], s24 offen nt lds\n\t" \
  "s_add_u32 m0, s25, 16400\n\t" \
  "s_add_u32 s24, s24, s26\n\t" \
  "buffer_load_dwordx4 v254, s[8:11], s24 offen nt lds\n\t" \
  "s_add_u32 m0, s25, 24600\n\t" \
  "s_add_u32 s24, s24, s26\n\t" \
  "buffer_load_dwordx4 v254, s[8:11], s24 offen nt lds\n\t" \
  "s_load_dwordx16 s[32:47], s[66:67], 0x1000\n\t" \
  "s_load_dwordx16 s[48:63], s[66:67], 0x1040\n\t" \
  "s_and_b32 s21, s21, 0xffff\n\t" \
  "s_waitcnt vmcnt(4)\n\t" \
  "v_mfma_f32_32x32x16_bf16 v[144:159], v[242:245], v[240:243], 0\n\t" \
  "v_mfma_f32_32x32x16_bf16 v[160:175], v[242:245], v[240:243], 0\n\t" \
  "v_bfe_u32 v236, v251, 3, 2\n\t" \
  "v_mul_u32_u24 v236, 0x5555, v236\n\t" \
  "v_add_u32 v252, s27, v16\n\t" \
  "s_cmp_gt_u32 s5, 192\n\t" \
  "s_cbranch_scc1 .Lcnx_e2\n\t" \
  "s_cmp_eq_u32 s5, 192\n\t" \
  "s_cbranch_scc1 .Lcnx_e1\n\t" \
  "v_xor_b32 v23, 0x02020202, v19\n\t" \
  "v_perm_b32 v24, v4, 0, v19\n\t" \
  "v_perm_b32 v25, v12, v8, v19\n\t" \
  "v_perm_b32 v26, v6, v2, v19\n\t" \
  "v_perm_b32 v27, v14, v10, v19\n\t" \
  "v_perm_b32 v28, v8, v4, v19\n\t" \
  "v_perm_b32 v29, 0, v12, v19\n\t" \
  "ds_write_b64 v17, v[24:25] offset:0\n\t" \
  "ds_write_b64 v17, v[26:27] offset:8\n\t" \
  "ds_write_b64 v17, v[28:29] offset:16\n\t" \
  "v_perm_b32 v30, v4, 0, v23\n\t" \
  "v_perm_b32 v31, v12, v8, v23\n\t" \
  "v_perm_b32 v32, v6, v2, v23\n\t" \
  "v_perm_b32 v33, v14, v10, v23\n\t" \
  "v_perm_b32 v34, v8, v4, v23\n\t" \
  "v_perm_b32 v35, 0, v12, v23\n\t" \
  "ds_write_b64 v18, v[30:31] offset:0\n\t" \
  "ds_write_b64 v18, v[32:33] offset:8\n\t" \
  "ds_write_b64 v18, v[34:35] offset:16\n\t" \
  "v_perm_b32 v37, v5, 0, v19\n\t" \
  "v_perm_b32 v36, v13, v9, v19\n\t" \
  "v_perm_b32 v39, v7, v3, v19\n\t" \
  "v_perm_b32 v38, v15, v11, v19\n\t" \
  "v_perm_b32 v41, v9, v5, v19\n\t" \
  "v_perm_b32 v40, 0, v13, v19\n\t" \
  "ds_write_b64 v17, v[36:37] offset:384\n\t" \
  "ds_write_b64 v17, v[38:39] offset:392\n\t" \
  "ds_write_b64 v17, v[40:41] offset:400\n\t" \
  "v_perm_b32 v43, v5, 0, v23\n\t" \
  "v_perm_b32 v42, v13, v9, v23\n\t" \
  "v_perm_b32 v45, v7, v3, v23\n\t" \
  "v_perm_b32 v44, v15, v11, v23\n\t" \
  "v_perm_b32 v47, v9, v5, v23\n\t" \
  "v_perm_b32 v46, 0, v13, v23\n\t" \
  "ds_write_b64 v18, v[42:43] offset:384\n\t" \
  "ds_write_b64 v18, v[44:45] offset:392\n\t" \
  "ds_write_b64 v18, v[46:47] offset:400\n\t" \
  "s_branch .Lcnx_e3\n\t" \
  ".Lcnx_e1:\n\t" \
  "ds_write_b64 v251, v[246:247] offset:47136\n\t" \
  "s_mov_b64 exec, 0x3ffff\n\t" \
  "ds_write_b64 v251, v[240:241] offset:47648\n\t" \
  "s_mov_b64 exec, 15\n\t" \
  "ds_write_b64 v17, v[240:241]\n\t" \
  "s_mov_b64 exec, -1\n\t" \
  "s_branch .Lcnx_e3\n\t" \
  ".Lcnx_e2:\n\t" \
  "s_setprio 1\n\t" \
  ".Lcnx_e3:\n\t" \
  "s_add_u32 m0, s25, 32800\n\t" \
  "s_add_u32 s24, s24, s26\n\t" \
  "buffer_load_dwordx4 v254, s[8:11], s24 offen nt lds\n\t" \
  "s_add_u32 m0, s25, 41000\n\t" \
  "s_add_u32 s24, s24, s26\n\t" \
  "buffer_load_dwordx4 v254, s[8:11], s24 offen nt lds\n\t" \
  "s_add_u32 m0, s25, 49200\n\t" \
  "s_add_u32 s24, s24, s26\n\t" \
  "buffer_load_dwordx4 v254, s[8:11], s24 offen nt lds\n\t" \
  "s_add_u32 m0, s25, 57400\n\t" \
  "s_add_u32 s24, s24, s26\n\t" \
  "buffer_load_dwordx4 v254, s[8:11], s24 offen nt lds\n\t" \
  "s_waitcnt vmcnt(6) lgkmcnt(0)\n\t" \
  "s_barrier\n\t" \
  "v_add_u32 v232, 1536, v247\n\t" \
  "ds_read2_b32 v[98:99], v247 offset0:0 offset1:6\n\t" \
  "ds_read2_b32 v[114:115], v247 offset0:192 offset1:198\n\t" \
  "ds_read2_b32 v[130:131], v232 offset0:0 offset1:6\n\t" \
  "ds_read2_b32 v[102:103], v247 offset0:12 offset1:18\n\t" \
  "ds_read2_b32 v[118:119], v247 offset0:204 offset1:210\n\t" \
  "ds_read2_b32 v[134:135], v232 offset0:12 offset1:18\n\t" \
  "ds_read2_b32 v[106:107], v247 offset0:24 offset1:30\n\t" \
  "ds_read2_b32 v[122:123], v247 offset0:216 offset1:222\n\t" \
  "ds_read2_b32 v[138:139], v232 offset0:24 offset1:30\n\t" \
  "ds_read2_b32 v[110:111], v247 offset0:36 offset1:42\n\t" \
  "ds_read2_b32 v[126:127], v247 offset0:228 offset1:234\n\t" \
  "ds_read2_b32 v[142:143], v232 offset0:36 offset1:42\n\t" \
  ".Lcnx_round:\n\t" \
  "v_add_u32 v232, 49200, v246\n\t" \
  "v_add_u32 v233, s41, v251\n\t" \
  "v_mfma_f32_32x32x16_bf16 v[176:191], v[242:245], v[240:243], 0\n\t" \
  "ds_read_b64 v[208:209], v246 offset:0\n\t" \
  "ds_read_b64 v[210:211], v246 offset:400\n\t" \
  "ds_read_b64 v[212:213], v246 offset:800\n\t" \
  "ds_read_b64 v[214:215], v246 offset:1200\n\t" \
  "ds_read_b64 v[216:217], v246 offset:8\n\t" \
  "ds_read_b64 v[218:219], v246 offset:408\n\t" \
  "ds_read_b64 v[220:221], v246 offset:808\n\t" \
  "ds_read_b64 v[222:223], v246 offset:1208\n\t" \
  "ds_read_b64 v[224:225], v246 offset:16\n\t" \
  "ds_read_b64 v[226:227], v246 offset:416\n\t" \
  "ds_read_b64 v[228:229], v246 offset:816\n\t" \
  "ds_read_b64 v[230:231], v246 offset:1216\n\t" \
  "s_cmp_lg_u32 s58, 0\n\t" \
  "s_cbranch_scc1 .Lcnx_c1\n\t" \
  "s_add_u32 m0, s25, 65600\n\t" \
  "s_add_u32 s24, s24, s26\n\t" \
  "buffer_load_dwordx4 v254, s[8:11], s24 offen nt lds\n\t" \
  "s_add_u32 m0, s25, 73800\n\t" \
  "s_add_u32 s24, s24, s26\n\t" \
  "buffer_load_dwordx4 v254, s[8:11], s24 offen nt lds\n\t" \
  "s_add_u32 m0, s25, 82000\n\t" \
  "s_add_u32 s24, s24, s26\n\t" \
  "buffer_load_dwordx4 v254, s[8:11], s24 offen nt lds\n\t" \
  "s_add_u32 m0, s25, 90200\n\t" \
  "s_add_u32 s24, s24, s26\n\t" \
  "buffer_load_dwordx4 v254, s[8:11], s24 offen nt lds\n\t" \
  ".Lcnx_c1:\n\t" \
  "s_waitcnt lgkmcnt(15)\n\t" \
  "v_and_b32 v96, 0xffff, v98\n\t" \
  "v_and_b32 v97, 0xffff0000, v98\n\t" \
  "v_and_b32 v98, 0xffff, v99\n\t" \
  "v_and_b32 v99, 0xffff0000, v99\n\t" \
  "v_and_b32 v112, 0xffff, v114\n\t" \
  "v_and_b32 v113, 0xffff0000, v114\n\t" \
  "v_and_b32 v114, 0xffff, v115\n\t" \
  "v_and_b32 v115, 0xffff0000, v115\n\t" \
  "v_and_b32 v128, 0xffff, v130\n\t" \
  "v_and_b32 v129, 0xffff0000, v130\n\t" \
  "v_and_b32 v130, 0xffff, v131\n\t" \
  "v_and_b32 v131, 0xffff0000, v131\n\t" \
  "s_waitcnt lgkmcnt(8)\n\t" \
  "v_smfmac_f32_16x16x64_bf16 v[144:147], v[96:99], v[208:215], v236\n\t" \
  "v_and_b32 v100, 0xffff, v102\n\t" \
  "v_and_b32 v101, 0xffff0000, v102\n\t" \
  "v_and_b32 v102, 0xffff, v103\n\t" \
  "v_and_b32 v103, 0xffff0000, v103\n\t" \
  "s_waitcnt lgkmcnt(4)\n\t" \
  "v_smfmac_f32_16x16x64_bf16 v[148:151], v[112:115], v[216:223], v236\n\t" \
  "v_and_b32 v116, 0xffff, v118\n\t" \
  "v_and_b32 v117, 0xffff0000, v118\n\t" \
  "v_and_b32 v118, 0xffff, v119\n\t" \
  "v_and_b32 v119, 0xffff0000, v119\n\t" \
  "s_cmp_lg_u32 s58, 1\n\t" \
  "s_cbranch_scc1 .Lcnx_c2\n\t" \
  "s_waitcnt vmcnt(5)\n\t" \
  ".Lcnx_c2:\n\t" \
  "s_waitcnt vmcnt(8) lgkmcnt(0)\n\t" \
  "s_barrier\n\t" \
  "ds_read_b64 v[208:209], v246 offset:16400\n\t" \
  "ds_read_b64 v[210:211], v246 offset:16800\n\t" \
  "ds_read_b64 v[212:213], v246 offset:17200\n\t" \
  "ds_read_b64 v[214:215], v246 offset:17600\n\t" \
  "ds_read_b64 v[216:217], v246 offset:16408\n\t" \
  "ds_read_b64 v[218:219], v246 offset:16808\n\t" \
  "ds_read_b64 v[220:221], v246 offset:17208\n\t" \
  "ds_read_b64 v[222:223], v246 offset:17608\n\t" \
  "v_smfmac_f32_16x16x64_bf16 v[152:155], v[128:131], v[224:231], v236\n\t" \
  "s_cmp_lg_u32 s58, 1\n\t" \
  "s_cbranch_scc1 .Lcnx_c3\n\t" \
  "s_add_u32 m0, s25, 73800\n\t" \
  "s_add_u32 s24, s24, s26\n\t" \
  "buffer_load_dwordx4 v254, s[8:11], s24 offen lds\n\t" \
  ".Lcnx_c3:\n\t" \
  "ds_read_b64 v[224:225], v246 offset:16416\n\t" \
  "ds_read_b64 v[226:227], v246 offset:16816\n\t" \
  "ds_read_b64 v[228:229], v246 offset:17216\n\t" \
  "v_and_b32 v132, 0xffff, v134\n\t" \
  "v_and_b32 v133, 0xffff0000, v134\n\t" \
  "v_and_b32 v134, 0xffff, v135\n\t" \
  "v_and_b32 v135, 0xffff0000, v135\n\t" \
  "s_waitcnt lgkmcnt(7)\n\t" \
  "v_smfmac_f32_16x16x64_bf16 v[156:159], v[96:99], v[208:215], v236\n\t" \
  "ds_read_b64 v[230:231], v246 offset:17616\n\t" \
  "v_and_b32 v104, 0xffff, v106\n\t" \
  "v_and_b32 v105, 0xffff0000, v106\n\t" \
  "v_and_b32 v106, 0xffff, v107\n\t" \
  "v_and_b32 v107, 0xffff0000, v107\n\t" \
  "v_smfmac_f32_16x16x64_bf16 v[144:147], v[100:103], v[208:215], v236\n\t" \
  "s_cmp_lg_u32 s58, 1\n\t" \
  "s_cbranch_scc1 .Lcnx_c4\n\t" \
  "s_add_u32 m0, s25, 82000\n\t" \
  "s_add_u32 s24, s24, s26\n\t" \
  "buffer_load_dwordx4 v254, s[8:11], s24 offen lds\n\t" \
  ".Lcnx_c4:\n\t" \
  "s_waitcnt lgkmcnt(4)\n\t" \
  "v_smfmac_f32_16x16x64_bf16 v[160:163], v[112:115], v[216:223], v236\n\t" \
  "v_and_b32 v120, 0xffff, v122\n\t" \
  "v_and_b32 v121, 0xffff0000, v122\n\t" \
  "v_and_b32 v122, 0xffff, v123\n\t" \
  "v_and_b32 v123, 0xffff0000, v123\n\t" \
  "v_smfmac_f32_16x16x64_bf16 v[148:151], v[116:119], v[216:223], v236\n\t" \
  "s_cmp_lg_u32 s58, 1\n\t" \
  "s_cbranch_scc1 .Lcnx_c5\n\t" \
  "s_add_u32 m0, s25, 90200\n\t" \
  "s_add_u32 s24, s24, s26\n\t" \
  "buffer_load_dwordx4 v254, s[8:11], s24 offen lds\n\t" \
  ".Lcnx_c5:\n\t" \
  "s_waitcnt vmcnt(6) lgkmcnt(0)\n\t" \
  "s_barrier\n\t" \
  "ds_read_b64 v[208:209], v246 offset:32800\n\t" \
  "ds_read_b64 v[210:211], v246 offset:33200\n\t" \
  "ds_read_b64 v[212:213], v246 offset:33600\n\t" \
  "ds_read_b64 v[214:215], v246 offset:34000\n\t" \
  "ds_read_b64 v[216:217], v246 offset:32808\n\t" \
  "ds_read_b64 v[218:219], v246 offset:33208\n\t" \
  "ds_read_b64 v[220:221], v246 offset:33608\n\t" \
  "ds_read_b64 v[222:223], v246 offset:34008\n\t" \
  "v_smfmac_f32_16x16x64_bf16 v[164:167], v[128:131], v[224:231], v236\n\t" \
  "v_and_b32 v136, 0xffff, v138\n\t" \
  "v_and_b32 v137, 0xffff0000, v138\n\t" \
  "v_and_b32 v138, 0xffff, v139\n\t" \
  "v_and_b32 v139, 0xffff0000, v139\n\t" \
  "v_cvt_pk_bf16_f32 v48, v48, v49\n\t" \
  "v_cvt_pk_bf16_f32 v49, v50, v51\n\t" \
  "v_cvt_pk_bf16_f32 v50, v52, v53\n\t" \
  "v_cvt_pk_bf16_f32 v51, v54, v55\n\t" \
  "v_cvt_pk_bf16_f32 v52, v0, v1\n\t" \
  "v_cvt_pk_bf16_f32 v53, v2, v3\n\t" \
  "s_add_u32 m0, s25, 0\n\t" \
  "s_add_u32 s24, s24, s26\n\t" \
  "v_smfmac_f32_16x16x64_bf16 v[152:155], v[132:135], v[224:231], v236\n\t" \
  "buffer_load_dwordx4 v254, s[8:11], s24 offen lds\n\t" \
  "ds_read_b64 v[224:225], v246 offset:32816\n\t" \
  "ds_read_b64 v[226:227], v246 offset:33216\n\t" \
  "ds_read_b64 v[228:229], v246 offset:33616\n\t" \
  "v_cvt_pk_bf16_f32 v54, v4, v5\n\t" \
  "v_cvt_pk_bf16_f32 v55, v6, v7\n\t" \
  "v_cvt_pk_bf16_f32 v56, v56, v57\n\t" \
  "s_waitcnt lgkmcnt(7)\n\t" \
  "v_smfmac_f32_16x16x64_bf16 v[168:171], v[96:99], v[208:215], v236\n\t" \
  "ds_read_b64 v[230:231], v246 offset:34016\n\t" \
  "v_and_b32 v108, 0xffff, v110\n\t" \
  "v_and_b32 v109, 0xffff0000, v110\n\t" \
  "v_and_b32 v110, 0xffff, v111\n\t" \
  "v_and_b32 v111, 0xffff0000, v111\n\t" \
  "v_cvt_pk_bf16_f32 v57, v58, v59\n\t" \
  "v_cvt_pk_bf16_f32 v58, v60, v61\n\t" \
  "v_cvt_pk_bf16_f32 v59, v62, v63\n\t" \
  "v_smfmac_f32_16x16x64_bf16 v[156:159], v[100:103], v[208:215], v236\n\t" \
  "v_cvt_pk_bf16_f32 v60, v8, v9\n\t" \
  "v_cvt_pk_bf16_f32 v61, v10, v11\n\t" \
  "v_cvt_pk_bf16_f32 v62, v12, v13\n\t" \
  "s_add_u32 m0, s25, 8200\n\t" \
  "s_add_u32 s24, s24, s26\n\t" \
  "v_smfmac_f32_16x16x64_bf16 v[144:147], v[104:107], v[208:215], v236\n\t" \
  "buffer_load_dwordx4 v254, s[8:11], s24 offen lds\n\t" \
  "v_cvt_pk_bf16_f32 v63, v14, v15\n\t" \
  "v_mfma_f32_32x32x16_bf16 v[0:15], v[242:245], v[240:243], 0\n\t" \
  "s_waitcnt lgkmcnt(4)\n\t" \
  "v_smfmac_f32_16x16x64_bf16 v[172:175], v[112:115], v[216:223], v236\n\t" \
  "v_and_b32 v124, 0xffff, v126\n\t" \
  "v_and_b32 v125, 0xffff0000, v126\n\t" \
  "v_and_b32 v126, 0xffff, v127\n\t" \
  "v_and_b32 v127, 0xffff0000, v127\n\t" \
  "s_add_u32 m0, s25, 16400\n\t" \
  "s_add_u32 s24, s24, s26\n\t" \
  "v_smfmac_f32_16x16x64_bf16 v[160:163], v[116:119], v[216:223], v236\n\t" \
  "buffer_load_dwordx4 v254, s[8:11], s24 offen lds\n\t" \
  "v_smfmac_f32_16x16x64_bf16 v[148:151], v[120:123], v[216:223], v236\n\t" \
  "s_waitcnt vmcnt(7) lgkmcnt(0)\n\t" \
  "s_barrier\n\t" \
  "ds_read_b64 v[208:209], v232 offset:0\n\t" \
  "ds_read_b64 v[210:211], v232 offset:400\n\t" \
  "ds_read_b64 v[212:213], v232 offset:800\n\t" \
  "ds_read_b64 v[214:215], v232 offset:1200\n\t" \
  "ds_read_b64 v[216:217], v232 offset:8\n\t" \
  "ds_read_b64 v[218:219], v232 offset:408\n\t" \
  "ds_read_b64 v[220:221], v232 offset:808\n\t" \
  "ds_read_b64 v[222:223], v232 offset:1208\n\t" \
  "v_smfmac_f32_16x16x64_bf16 v[176:179], v[128:131], v[224:231], v236\n\t" \
  "v_and_b32 v140, 0xffff, v142\n\t" \
  "v_and_b32 v141, 0xffff0000, v142\n\t" \
  "v_and_b32 v142, 0xffff, v143\n\t" \
  "v_and_b32 v143, 0xffff0000, v143\n\t" \
  "s_add_u32 m0, s25, 24600\n\t" \
  "s_add_u32 s24, s24, s26\n\t" \
  "v_smfmac_f32_16x16x64_bf16 v[164:167], v[132:135], v[224:231], v236\n\t" \
  "buffer_load_dwordx4 v254, s[8:11], s24 offen lds\n\t" \
  "v_smfmac_f32_16x16x64_bf16 v[152:155], v[136:139], v[224:231], v236\n\t" \
  "ds_read_b64 v[224:225], v232 offset:16\n\t" \
  "s_waitcnt lgkmcnt(5)\n\t" \
  "v_smfmac_f32_16x16x64_bf16 v[180:183], v[96:99], v[208:215], v236\n\t" \
  "ds_read_b64 v[226:227], v232 offset:416\n\t" \
  "v_smfmac_f32_16x16x64_bf16 v[168:171], v[100:103], v[208:215], v236\n\t" \
  "ds_read_b64 v[228:229], v232 offset:816\n\t" \
  "v_cvt_pk_bf16_f32 v64, v64, v65\n\t" \
  "v_cvt_pk_bf16_f32 v65, v66, v67\n\t" \
  "v_cvt_pk_bf16_f32 v66, v68, v69\n\t" \
  "v_cvt_pk_bf16_f32 v67, v70, v71\n\t" \
  "v_cvt_pk_bf16_f32 v68, v16, v17\n\t" \
  "v_cvt_pk_bf16_f32 v69, v18, v19\n\t" \
  "s_add_u32 m0, s25, 32800\n\t" \
  "s_add_u32 s24, s24, s26\n\t" \
  "v_smfmac_f32_16x16x64_bf16 v[156:159], v[104:107], v[208:215], v236\n\t" \
  "buffer_load_dwordx4 v254, s[8:11], s24 offen lds\n\t" \
  "ds_read_b64 v[230:231], v232 offset:1216\n\t" \
  "v_cvt_pk_bf16_f32 v70, v20, v21\n\t" \
  "v_cvt_pk_bf16_f32 v71, v22, v23\n\t" \
  "v_cvt_pk_bf16_f32 v72, v72, v73\n\t" \
  "v_smfmac_f32_16x16x64_bf16 v[144:147], v[108:111], v[208:215], v236\n\t" \
  "v_cvt_pk_bf16_f32 v73, v74, v75\n\t" \
  "v_cvt_pk_bf16_f32 v74, v76, v77\n\t" \
  "v_cvt_pk_bf16_f32 v75, v78, v79\n\t" \
  "s_waitcnt lgkmcnt(4)\n\t" \
  "v_smfmac_f32_16x16x64_bf16 v[184:187], v[112:115], v[216:223], v236\n\t" \
  "v_cvt_pk_bf16_f32 v76, v24, v25\n\t" \
  "v_cvt_pk_bf16_f32 v77, v26, v27\n\t" \
  "v_cvt_pk_bf16_f32 v78, v28, v29\n\t" \
  "v_smfmac_f32_16x16x64_bf16 v[172:175], v[116:119], v[216:223], v236\n\t" \
  "v_cvt_pk_bf16_f32 v79, v30, v31\n\t" \
  "v_mfma_f32_32x32x16_bf16 v[16:31], v[242:245], v[240:243], 0\n\t" \
  "s_add_u32 m0, s25, 41000\n\t" \
  "s_add_u32 s24, s24, s26\n\t" \
  "v_smfmac_f32_16x16x64_bf16 v[160:163], v[120:123], v[216:223], v236\n\t" \
  "buffer_load_dwordx4 v254, s[8:11], s24 offen lds\n\t" \
  "v_mul_f32 v193, v144, v144\n\t" \
  "v_add_f32 v192, v144, v145\n\t" \
  "v_fmac_f32 v193, v145, v145\n\t" \
  "v_add_f32 v192, v192, v146\n\t" \
  "v_smfmac_f32_16x16x64_bf16 v[148:151], v[124:127], v[216:223], v236\n\t" \
  "v_fmac_f32 v193, v146, v146\n\t" \
  "v_add_f32 v192, v192, v147\n\t" \
  "v_fmac_f32 v193, v147, v147\n\t" \
  "s_waitcnt vmcnt(8) lgkmcnt(0)\n\t" \
  "s_barrier\n\t" \
  "ds_read_b64 v[208:209], v232 offset:16400\n\t" \
  "ds_read_b64 v[210:211], v232 offset:16800\n\t" \
  "ds_read_b64 v[212:213], v232 offset:17200\n\t" \
  "ds_read_b64 v[214:215], v232 offset:17600\n\t" \
  "ds_read_b64 v[216:217], v232 offset:16408\n\t" \
  "ds_read_b64 v[218:219], v232 offset:16808\n\t" \
  "ds_read_b64 v[220:221], v232 offset:17208\n\t" \
  "ds_read_b64 v[222:223], v232 offset:17608\n\t" \
  "v_smfmac_f32_16x16x64_bf16 v[188:191], v[128:131], v[224:231], v236\n\t" \
  "s_add_u32 m0, s25, 49200\n\t" \
  "s_add_u32 s24, s24, s26\n\t" \
  "v_smfmac_f32_16x16x64_bf16 v[176:179], v[132:135], v[224:231], v236\n\t" \
  "buffer_load_dwordx4 v254, s[8:11], s24 offen lds\n\t" \
  "v_smfmac_f32_16x16x64_bf16 v[164:167], v[136:139], v[224:231], v236\n\t" \
  "v_add_f32 v192, v192, v148\n\t" \
  "v_fmac_f32 v193, v148, v148\n\t" \
  "v_add_f32 v192, v192, v149\n\t" \
  "v_fmac_f32 v193, v149, v149\n\t" \
  "v_add_f32 v192, v192, v150\n\t" \
  "v_fmac_f32 v193, v150, v150\n\t" \
  "v_smfmac_f32_16x16x64_bf16 v[152:155], v[140:143], v[224:231], v236\n\t" \
  "ds_read_b64 v[224:225], v232 offset:16416\n\t" \
  "v_add_f32 v192, v192, v151\n\t" \
  "v_fmac_f32 v193, v151, v151\n\t" \
  "s_waitcnt lgkmcnt(5)\n\t" \
  "v_smfmac_f32_16x16x64_bf16 v[0:3], v[96:99], v[208:215], v236\n\t" \
  "ds_read_b64 v[226:227], v232 offset:16816\n\t" \
  "v_smfmac_f32_16x16x64_bf16 v[180:183], v[100:103], v[208:215], v236\n\t" \
  "ds_read_b64 v[228:229], v232 offset:17216\n\t" \
  "v_cvt_pk_bf16_f32 v80, v80, v81\n\t" \
  "v_cvt_pk_bf16_f32 v81, v82, v83\n\t" \
  "v_cvt_pk_bf16_f32 v82, v84, v85\n\t" \
  "v_cvt_pk_bf16_f32 v83, v86, v87\n\t" \
  "v_cvt_pk_bf16_f32 v84, v32, v33\n\t" \
  "v_cvt_pk_bf16_f32 v85, v34, v35\n\t" \
  "s_add_u32 m0, s25, 57400\n\t" \
  "s_add_u32 s24, s24, s26\n\t" \
  "v_smfmac_f32_16x16x64_bf16 v[168:171], v[104:107], v[208:215], v236\n\t" \
  "buffer_load_dwordx4 v254, s[8:11], s24 offen lds\n\t" \
  "ds_read_b64 v[230:231], v232 offset:17616\n\t" \
  "v_add_f32 v192, v192, v152\n\t" \
  "v_cvt_pk_bf16_f32 v86, v36, v37\n\t" \
  "v_fmac_f32 v193, v152, v152\n\t" \
  "v_smfmac_f32_16x16x64_bf16 v[156:159], v[108:111], v[208:215], v236\n\t" \
  "v_add_f32 v192, v192, v153\n\t" \
  "v_cvt_pk_bf16_f32 v87, v38, v39\n\t" \
  "v_fmac_f32 v193, v153, v153\n\t" \
  "s_waitcnt lgkmcnt(4)\n\t" \
  "v_smfmac_f32_16x16x64_bf16 v[4:7], v[112:115], v[216:223], v236\n\t" \
  "v_cvt_pk_bf16_f32 v88, v88, v89\n\t" \
  "v_add_f32 v192, v192, v154\n\t" \
  "v_cvt_pk_bf16_f32 v89, v90, v91\n\t" \
  "v_smfmac_f32_16x16x64_bf16 v[184:187], v[116:119], v[216:223], v236\n\t" \
  "v_cvt_pk_bf16_f32 v90, v92, v93\n\t" \
  "v_fmac_f32 v193, v154, v154\n\t" \
  "v_cvt_pk_bf16_f32 v91, v94, v95\n\t" \
  "v_smfmac_f32_16x16x64_bf16 v[172:175], v[120:123], v[216:223], v236\n\t" \
  "v_cvt_pk_bf16_f32 v92, v40, v41\n\t" \
  "v_add_f32 v192, v192, v155\n\t" \
  "v_cvt_pk_bf16_f32 v93, v42, v43\n\t" \
  "v_smfmac_f32_16x16x64_bf16 v[160:163], v[124:127], v[216:223], v236\n\t" \
  "v_cvt_pk_bf16_f32 v94, v44, v45\n\t" \
  "v_fmac_f32 v193, v155, v155\n\t" \
  "v_cvt_pk_bf16_f32 v95, v46, v47\n\t" \
  "s_waitcnt vmcnt(8) lgkmcnt(0)\n\t" \
  "s_barrier\n\t" \
  "ds_read_b64 v[208:209], v232 offset:32800\n\t" \
  "ds_read_b64 v[210:211], v232 offset:33200\n\t" \
  "ds_read_b64 v[212:213], v232 offset:33600\n\t" \
  "ds_read_b64 v[214:215], v232 offset:34000\n\t" \
  "ds_read_b64 v[216:217], v232 offset:32808\n\t" \
  "ds_read_b64 v[218:219], v232 offset:33208\n\t" \
  "ds_read_b64 v[220:221], v232 offset:33608\n\t" \
  "ds_read_b64 v[222:223], v232 offset:34008\n\t" \
  "s_add_u32 m0, s25, 65600\n\t" \
  "s_add_u32 s24, s24, s26\n\t" \
  "v_smfmac_f32_16x16x64_bf16 v[8:11], v[128:131], v[224:231], v236\n\t" \
  "buffer_load_dwordx4 v254, s[8:11], s24 offen lds\n\t" \
  "v_mul_f32 v195, v156, v156\n\t" \
  "ds_write_b64 v233, v[192:193] offset:16400\n\t" \
  "v_mfma_f32_32x32x16_bf16 v[32:47], v[242:245], v[240:243], 0\n\t" \
  "v_smfmac_f32_16x16x64_bf16 v[188:191], v[132:135], v[224:231], v236\n\t" \
  "v_add_f32 v194, v156, v157\n\t" \
  "v_fmac_f32 v195, v157, v157\n\t" \
  "v_add_f32 v194, v194, v158\n\t" \
  "v_smfmac_f32_16x16x64_bf16 v[176:179], v[136:139], v[224:231], v236\n\t" \
  "v_fmac_f32 v195, v158, v158\n\t" \
  "v_add_f32 v194, v194, v159\n\t" \
  "v_fmac_f32 v195, v159, v159\n\t" \
  "v_smfmac_f32_16x16x64_bf16 v[164:167], v[140:143], v[224:231], v236\n\t" \
  "ds_read_b64 v[224:225], v232 offset:32816\n\t" \
  "v_add_f32 v194, v194, v160\n\t" \
  "v_fmac_f32 v195, v160, v160\n\t" \
  "v_add_f32 v194, v194, v161\n\t" \
  "s_waitcnt lgkmcnt(6)\n\t" \
  "s_add_u32 m0, s25, 73800\n\t" \
  "s_add_u32 s24, s24, s26\n\t" \
  "v_smfmac_f32_16x16x64_bf16 v[12:15], v[96:99], v[208:215], v236\n\t" \
  "buffer_load_dwordx4 v254, s[8:11], s24 offen lds\n\t" \
  "ds_read_b64 v[226:227], v232 offset:33216\n\t" \
  "v_fmac_f32 v195, v161, v161\n\t" \
  "v_add_f32 v194, v194, v162\n\t" \
  "v_fmac_f32 v195, v162, v162\n\t" \
  "v_smfmac_f32_16x16x64_bf16 v[0:3], v[100:103], v[208:215], v236\n\t" \
  "ds_read_b64 v[228:229], v232 offset:33616\n\t" \
  "v_add_f32 v194, v194, v163\n\t" \
  "v_fmac_f32 v195, v163, v163\n\t" \
  "v_smfmac_f32_16x16x64_bf16 v[180:183], v[104:107], v[208:215], v236\n\t" \
  "buffer_store_dwordx4 v[52:55], v250, s[20:23], s61 offen offset:0 sc1\n\t" \
  "ds_read_b64 v[230:231], v232 offset:34016\n\t" \
  "v_add_f32 v194, v194, v164\n\t" \
  "v_fmac_f32 v195, v164, v164\n\t" \
  "v_add_f32 v194, v194, v165\n\t" \
  "v_smfmac_f32_16x16x64_bf16 v[168:171], v[108:111], v[208:215], v236\n\t" \
  "v_fmac_f32 v195, v165, v165\n\t" \
  "v_add_f32 v194, v194, v166\n\t" \
  "v_fmac_f32 v195, v166, v166\n\t" \
  "s_waitcnt lgkmcnt(5)\n\t" \
  "v_smfmac_f32_16x16x64_bf16 v[16:19], v[112:115], v[216:223], v236\n\t" \
  "v_add_f32 v194, v194, v167\n\t" \
  "v_fmac_f32 v195, v167, v167\n\t" \
  "v_smfmac_f32_16x16x64_bf16 v[4:7], v[116:119], v[216:223], v236\n\t" \
  "ds_write_b64 v233, v[194:195] offset:20496\n\t" \
  "v_smfmac_f32_16x16x64_bf16 v[184:187], v[120:123], v[216:223], v236\n\t" \
  "v_mul_f32 v197, v168, v168\n\t" \
  "v_add_f32 v196, v168, v169\n\t" \
  "v_fmac_f32 v197, v169, v169\n\t" \
  "v_add_f32 v196, v196, v170\n\t" \
  "v_fmac_f32 v197, v170, v170\n\t" \
  "v_add_f32 v196, v196, v171\n\t" \
  "v_smfmac_f32_16x16x64_bf16 v[172:175], v[124:127], v[216:223], v236\n\t" \
  "v_fmac_f32 v197, v171, v171\n\t" \
  "s_waitcnt vmcnt(9) lgkmcnt(0)\n\t" \
  "s_barrier\n\t" \
  "ds_read_b64 v[208:209], v246 offset:0\n\t" \
  "ds_read_b64 v[210:211], v246 offset:400\n\t" \
  "ds_read_b64 v[212:213], v246 offset:800\n\t" \
  "ds_read_b64 v[214:215], v246 offset:1200\n\t" \
  "ds_read_b64 v[216:217], v246 offset:8\n\t" \
  "ds_read_b64 v[218:219], v246 offset:408\n\t" \
  "ds_read_b64 v[220:221], v246 offset:808\n\t" \
  "ds_read_b64 v[222:223], v246 offset:1208\n\t" \
  "v_smfmac_f32_16x16x64_bf16 v[20:23], v[128:131], v[224:231], v236\n\t" \
  "v_smfmac_f32_16x16x64_bf16 v[8:11], v[132:135], v[224:231], v236\n\t" \
  "buffer_store_dwordx4 v[60:63], v250, s[20:23], s61 offen offset:64 sc1\n\t" \
  "v_smfmac_f32_16x16x64_bf16 v[188:191], v[136:139], v[224:231], v236\n\t" \
  "v_add_f32 v196, v196, v172\n\t" \
  "v_fmac_f32 v197, v172, v172\n\t" \
  "v_add_f32 v196, v196, v173\n\t" \
  "v_fmac_f32 v197, v173, v173\n\t" \
  "v_add_f32 v196, v196, v174\n\t" \
  "v_fmac_f32 v197, v174, v174\n\t" \
  "v_smfmac_f32_16x16x64_bf16 v[176:179], v[140:143], v[224:231], v236\n\t" \
  "ds_read_b64 v[224:225], v246 offset:16\n\t" \
  "v_add_f32 v196, v196, v175\n\t" \
  "v_fmac_f32 v197, v175, v175\n\t" \
  "s_waitcnt lgkmcnt(5)\n\t" \
  "v_smfmac_f32_16x16x64_bf16 v[24:27], v[96:99], v[208:215], v236\n\t" \
  "ds_read_b64 v[226:227], v246 offset:416\n\t" \
  "v_smfmac_f32_16x16x64_bf16 v[12:15], v[100:103], v[208:215], v236\n\t" \
  "ds_read_b64 v[228:229], v246 offset:816\n\t" \
  "v_smfmac_f32_16x16x64_bf16 v[0:3], v[104:107], v[208:215], v236\n\t" \
  "ds_read_b64 v[230:231], v246 offset:1216\n\t" \
  "v_add_f32 v196, v196, v176\n\t" \
  "v_fmac_f32 v197, v176, v176\n\t" \
  "v_add_f32 v196, v196, v177\n\t" \
  "v_fmac_f32 v197, v177, v177\n\t" \
  "v_add_f32 v196, v196, v178\n\t" \
  "v_fmac_f32 v197, v178, v178\n\t" \
  "v_smfmac_f32_16x16x64_bf16 v[180:183], v[108:111], v[208:215], v236\n\t" \
  "v_add_f32 v196, v196, v179\n\t" \
  "v_fmac_f32 v197, v179, v179\n\t" \
  "s_waitcnt lgkmcnt(4)\n\t" \
  "v_smfmac_f32_16x16x64_bf16 v[28:31], v[112:115], v[216:223], v236\n\t" \
  "buffer_store_dwordx4 v[68:71], v250, s[20:23], s61 offen offset:128 sc1\n\t" \
  "v_smfmac_f32_16x16x64_bf16 v[16:19], v[116:119], v[216:223], v236\n\t" \
  "ds_write_b64 v233, v[196:197] offset:24592\n\t" \
  "v_smfmac_f32_16x16x64_bf16 v[4:7], v[120:123], v[216:223], v236\n\t" \
  "v_mul_f32 v199, v180, v180\n\t" \
  "v_add_f32 v198, v180, v181\n\t" \
  "v_fmac_f32 v199, v181, v181\n\t" \
  "v_add_f32 v198, v198, v182\n\t" \
  "v_fmac_f32 v199, v182, v182\n\t" \
  "v_add_f32 v198, v198, v183\n\t" \
  "v_smfmac_f32_16x16x64_bf16 v[184:187], v[124:127], v[216:223], v236\n\t" \
  "v_fmac_f32 v199, v183, v183\n\t" \
  "s_waitcnt vmcnt(9) lgkmcnt(0)\n\t" \
  "s_barrier\n\t" \
  "ds_read_b64 v[208:209], v246 offset:16400\n\t" \
  "ds_read_b64 v[210:211], v246 offset:16800\n\t" \
  "ds_read_b64 v[212:213], v246 offset:17200\n\t" \
  "ds_read_b64 v[214:215], v246 offset:17600\n\t" \
  "ds_read_b64 v[216:217], v246 offset:16408\n\t" \
  "ds_read_b64 v[218:219], v246 offset:16808\n\t" \
  "ds_read_b64 v[220:221], v246 offset:17208\n\t" \
  "ds_read_b64 v[222:223], v246 offset:17608\n\t" \
  "v_smfmac_f32_16x16x64_bf16 v[32:35], v[128:131], v[224:231], v236\n\t" \
  "v_smfmac_f32_16x16x64_bf16 v[20:23], v[132:135], v[224:231], v236\n\t" \
  "v_smfmac_f32_16x16x64_bf16 v[8:11], v[136:139], v[224:231], v236\n\t" \
  "v_add_f32 v198, v198, v184\n\t" \
  "v_fmac_f32 v199, v184, v184\n\t" \
  "v_add_f32 v198, v198, v185\n\t" \
  "v_fmac_f32 v199, v185, v185\n\t" \
  "v_add_f32 v198, v198, v186\n\t" \
  "v_smfmac_f32_16x16x64_bf16 v[188:191], v[140:143], v[224:231], v236\n\t" \
  "buffer_store_dwordx4 v[76:79], v250, s[20:23], s61 offen offset:192 sc1\n\t" \
  "ds_read_b64 v[224:225], v246 offset:16416\n\t" \
  "v_fmac_f32 v199, v186, v186\n\t" \
  "v_add_f32 v198, v198, v187\n\t" \
  "v_fmac_f32 v199, v187, v187\n\t" \
  "s_waitcnt lgkmcnt(5)\n\t" \
  "v_smfmac_f32_16x16x64_bf16 v[36:39], v[96:99], v[208:215], v236\n\t" \
  "ds_read_b64 v[226:227], v246 offset:16816\n\t" \
  "v_smfmac_f32_16x16x64_bf16 v[24:27], v[100:103], v[208:215], v236\n\t" \
  "ds_read_b64 v[228:229], v246 offset:17216\n\t" \
  "v_smfmac_f32_16x16x64_bf16 v[12:15], v[104:107], v[208:215], v236\n\t" \
  "ds_read_b64 v[230:231], v246 offset:17616\n\t" \
  "v_add_f32 v198, v198, v188\n\t" \
  "v_fmac_f32 v199, v188, v188\n\t" \
  "v_add_f32 v198, v198, v189\n\t" \
  "v_fmac_f32 v199, v189, v189\n\t" \
  "v_add_f32 v198, v198, v190\n\t" \
  "v_fmac_f32 v199, v190, v190\n\t" \
  "v_smfmac_f32_16x16x64_bf16 v[0:3], v[108:111], v[208:215], v236\n\t" \
  "v_add_f32 v198, v198, v191\n\t" \
  "v_fmac_f32 v199, v191, v191\n\t" \
  "s_waitcnt lgkmcnt(4)\n\t" \
  "v_smfmac_f32_16x16x64_bf16 v[40:43], v[112:115], v[216:223], v236\n\t" \
  "ds_write_b64 v233, v[198:199] offset:28688\n\t" \
  "v_smfmac_f32_16x16x64_bf16 v[28:31], v[116:119], v[216:223], v236\n\t" \
  "v_smfmac_f32_16x16x64_bf16 v[16:19], v[120:123], v[216:223], v236\n\t" \
  "buffer_store_dwordx4 v[84:87], v250, s[20:23], s61 offen offset:256 sc1\n\t" \
  "v_mul_f32 v201, v0, v0\n\t" \
  "v_add_f32 v200, v0, v1\n\t" \
  "v_fmac_f32 v201, v1, v1\n\t" \
  "v_add_f32 v200, v200, v2\n\t" \
  "v_fmac_f32 v201, v2, v2\n\t" \
  "v_add_f32 v200, v200, v3\n\t" \
  "v_smfmac_f32_16x16x64_bf16 v[4:7], v[124:127], v[216:223], v236\n\t" \
  "v_fmac_f32 v201, v3, v3\n\t" \
  "s_waitcnt vmcnt(9) lgkmcnt(0)\n\t" \
  "s_barrier\n\t" \
  "ds_read_b64 v[208:209], v246 offset:32800\n\t" \
  "ds_read_b64 v[210:211], v246 offset:33200\n\t" \
  "ds_read_b64 v[212:213], v246 offset:33600\n\t" \
  "ds_read_b64 v[214:215], v246 offset:34000\n\t" \
  "ds_read_b64 v[216:217], v246 offset:32808\n\t" \
  "ds_read_b64 v[218:219], v246 offset:33208\n\t" \
  "ds_read_b64 v[220:221], v246 offset:33608\n\t" \
  "ds_read_b64 v[222:223], v246 offset:34008\n\t" \
  "v_smfmac_f32_16x16x64_bf16 v[44:47], v[128:131], v[224:231], v236\n\t" \
  "v_smfmac_f32_16x16x64_bf16 v[32:35], v[132:135], v[224:231], v236\n\t" \
  "v_smfmac_f32_16x16x64_bf16 v[20:23], v[136:139], v[224:231], v236\n\t" \
  "v_add_f32 v200, v200, v4\n\t" \
  "v_fmac_f32 v201, v4, v4\n\t" \
  "v_add_f32 v200, v200, v5\n\t" \
  "v_fmac_f32 v201, v5, v5\n\t" \
  "v_add_f32 v200, v200, v6\n\t" \
  "v_fmac_f32 v201, v6, v6\n\t" \
  "v_smfmac_f32_16x16x64_bf16 v[8:11], v[140:143], v[224:231], v236\n\t" \
  "ds_read_b64 v[224:225], v246 offset:32816\n\t" \
  "v_add_f32 v200, v200, v7\n\t" \
  "v_fmac_f32 v201, v7, v7\n\t" \
  "s_waitcnt lgkmcnt(5)\n\t" \
  "v_smfmac_f32_16x16x64_bf16 v[36:39], v[100:103], v[208:215], v236\n\t" \
  "ds_read_b64 v[226:227], v246 offset:33216\n\t" \
  "v_smfmac_f32_16x16x64_bf16 v[24:27], v[104:107], v[208:215], v236\n\t" \
  "buffer_store_dwordx4 v[92:95], v250, s[20:23], s61 offen offset:320 sc1\n\t" \
  "ds_read_b64 v[228:229], v246 offset:33616\n\t" \
  "v_smfmac_f32_16x16x64_bf16 v[12:15], v[108:111], v[208:215], v236\n\t" \
  "ds_read_b64 v[230:231], v246 offset:34016\n\t" \
  "v_add_f32 v200, v200, v8\n\t" \
  "v_fmac_f32 v201, v8, v8\n\t" \
  "v_add_f32 v200, v200, v9\n\t" \
  "v_fmac_f32 v201, v9, v9\n\t" \
  "v_add_f32 v200, v200, v10\n\t" \
  "v_fmac_f32 v201, v10, v10\n\t" \
  "s_waitcnt lgkmcnt(4)\n\t" \
  "v_smfmac_f32_16x16x64_bf16 v[40:43], v[116:119], v[216:223], v236\n\t" \
  "v_add_f32 v200, v200, v11\n\t" \
  "v_fmac_f32 v201, v11, v11\n\t" \
  "v_smfmac_f32_16x16x64_bf16 v[28:31], v[120:123], v[216:223], v236\n\t" \
  "ds_write_b64 v233, v[200:201] offset:0\n\t" \
  "v_smfmac_f32_16x16x64_bf16 v[16:19], v[124:127], v[216:223], v236\n\t" \
  "v_mul_f32 v203, v12, v12\n\t" \
  "v_add_f32 v202, v12, v13\n\t" \
  "v_fmac_f32 v203, v13, v13\n\t" \
  "v_add_f32 v202, v202, v14\n\t" \
  "v_fmac_f32 v203, v14, v14\n\t" \
  "v_add_f32 v202, v202, v15\n\t" \
  "s_waitcnt vmcnt(8) lgkmcnt(0)\n\t" \
  "s_barrier\n\t" \
  "ds_read_b64 v[208:209], v232 offset:0\n\t" \
  "ds_read_b64 v[210:211], v232 offset:400\n\t" \
  "ds_read_b64 v[212:213], v232 offset:800\n\t" \
  "ds_read_b64 v[214:215], v232 offset:1200\n\t" \
  "ds_read_b64 v[216:217], v232 offset:8\n\t" \
  "ds_read_b64 v[218:219], v232 offset:408\n\t" \
  "ds_read_b64 v[220:221], v232 offset:808\n\t" \
  "ds_read_b64 v[222:223], v232 offset:1208\n\t" \
  "s_cmp_lg_u32 s58, 0\n\t" \
  "s_cbranch_scc1 .Lcnx_c6\n\t" \
  "v_lshl_add_u32 v48, v233, 1, s63\n\t" \
  "s_lshl_b32 m0, s41, 1\n\t" \
  "v_add_u32 v49, 0x2000, v48\n\t" \
  "global_load_lds_dwordx4 v48, s[6:7]\n\t" \
  "s_add_u32 m0, m0, 0x2000\n\t" \
  "s_cmp_eq_u32 s41, 3584\n\t" \
  "s_cselect_b64 exec, 1, -1\n\t" \
  "global_load_lds_dwordx4 v49, s[6:7]\n\t" \
  "s_mov_b64 exec, -1\n\t" \
  ".Lcnx_c6:\n\t" \
  "s_cmp_lg_u32 s37, 0\n\t" \
  "s_cbranch_scc1 .Lcnx_c7\n\t" \
  "v_add_u32 v128, s40, v251\n\t" \
  "ds_read_b64 v[96:97], v128 offset:0\n\t" \
  "ds_read_b64 v[98:99], v128 offset:512\n\t" \
  "ds_read_b64 v[100:101], v128 offset:1024\n\t" \
  "ds_read_b64 v[102:103], v128 offset:1536\n\t" \
  "ds_read_b64 v[112:113], v128 offset:2048\n\t" \
  "ds_read_b64 v[114:115], v128 offset:2560\n\t" \
  "ds_read_b64 v[116:117], v128 offset:3072\n\t" \
  "ds_read_b64 v[118:119], v128 offset:3584\n\t" \
  ".Lcnx_c7:\n\t" \
  "v_smfmac_f32_16x16x64_bf16 v[44:47], v[132:135], v[224:231], v236\n\t" \
  "v_fmac_f32 v203, v15, v15\n\t" \
  "v_smfmac_f32_16x16x64_bf16 v[32:35], v[136:139], v[224:231], v236\n\t" \
  "v_smfmac_f32_16x16x64_bf16 v[20:23], v[140:143], v[224:231], v236\n\t" \
  "buffer_store_dwordx4 v[48:51], v250, s[20:23], s62 offen offset:0 sc1\n\t" \
  "ds_read_b64 v[224:225], v232 offset:16\n\t" \
  "v_add_f32 v202, v202, v16\n\t" \
  "v_fmac_f32 v203, v16, v16\n\t" \
  "v_add_f32 v202, v202, v17\n\t" \
  "v_fmac_f32 v203, v17, v17\n\t" \
  "v_add_f32 v202, v202, v18\n\t" \
  "v_fmac_f32 v203, v18, v18\n\t" \
  "s_waitcnt lgkmcnt(5)\n\t" \
  "v_smfmac_f32_16x16x64_bf16 v[36:39], v[104:107], v[208:215], v236\n\t" \
  "ds_read_b64 v[226:227], v232 offset:416\n\t" \
  "ds_read_b64 v[228:229], v232 offset:816\n\t" \
  "ds_read_b64 v[230:231], v232 offset:1216\n\t" \
  "v_add_f32 v202, v202, v19\n\t" \
  "v_fmac_f32 v203, v19, v19\n\t" \
  "v_smfmac_f32_16x16x64_bf16 v[24:27], v[108:111], v[208:215], v236\n\t" \
  "s_waitcnt lgkmcnt(4)\n\t" \
  "v_smfmac_f32_16x16x64_bf16 v[40:43], v[120:123], v[216:223], v236\n\t" \
  "v_add_f32 v202, v202, v20\n\t" \
  "v_fmac_f32 v203, v20, v20\n\t" \
  "v_add_f32 v202, v202, v21\n\t" \
  "v_fmac_f32 v203, v21, v21\n\t" \
  "v_add_f32 v202, v202, v22\n\t" \
  "v_fmac_f32 v203, v22, v22\n\t" \
  "v_smfmac_f32_16x16x64_bf16 v[28:31], v[124:127], v[216:223], v236\n\t" \
  "v_add_f32 v202, v202, v23\n\t" \
  "v_fmac_f32 v203, v23, v23\n\t" \
  "ds_write_b64 v233, v[202:203] offset:4096\n\t" \
  "s_waitcnt vmcnt(7) lgkmcnt(0)\n\t" \
  "s_barrier\n\t" \
  "ds_read_b64 v[208:209], v232 offset:16400\n\t" \
  "ds_read_b64 v[210:211], v232 offset:16800\n\t" \
  "ds_read_b64 v[212:213], v232 offset:17200\n\t" \
  "ds_read_b64 v[214:215], v232 offset:17600\n\t" \
  "ds_read_b64 v[216:217], v232 offset:16408\n\t" \
  "ds_read_b64 v[218:219], v232 offset:16808\n\t" \
  "ds_read_b64 v[220:221], v232 offset:17208\n\t" \
  "ds_read_b64 v[222:223], v232 offset:17608\n\t" \
  "s_cmp_ge_u32 s36, 2\n\t" \
  "s_cbranch_scc1 .Lcnx_c8\n\t" \
  "s_mov_b32 m0, s46\n\t" \
  "s_nop 0\n\t" \
  "buffer_load_dwordx4 v252, s[16:19], s33 offen lds\n\t" \
  "buffer_load_dwordx4 v252, s[16:19], s34 offen offset:1024 lds\n\t" \
  "buffer_load_dwordx4 v252, s[16:19], s35 offen offset:2048 lds\n\t" \
  "s_add_u32 s33, s33, s32\n\t" \
  "s_add_u32 s34, s34, s32\n\t" \
  "s_add_u32 s35, s35, s32\n\t" \
  "s_mov_b32 m0, s47\n\t" \
  "s_nop 0\n\t" \
  "buffer_load_dwordx4 v252, s[16:19], s33 offen lds\n\t" \
  "buffer_load_dwordx4 v252, s[16:19], s34 offen offset:1024 lds\n\t" \
  "buffer_load_dwordx4 v252, s[16:19], s35 offen offset:2048 lds\n\t" \
  "s_add_u32 s33, s33, s32\n\t" \
  "s_add_u32 s34, s34, s32\n\t" \
  "s_add_u32 s35, s35, s32\n\t" \
  ".Lcnx_c8:\n\t" \
  "v_smfmac_f32_16x16x64_bf16 v[44:47], v[136:139], v[224:231], v236\n\t" \
  "v_mul_f32 v205, v24, v24\n\t" \
  "v_add_f32 v204, v24, v25\n\t" \
  "v_smfmac_f32_16x16x64_bf16 v[32:35], v[140:143], v[224:231], v236\n\t" \
  "buffer_store_dwordx4 v[56:59], v250, s[20:23], s62 offen offset:64 sc1\n\t" \
  "ds_read_b64 v[224:225], v232 offset:16416\n\t" \
  "ds_read_b64 v[226:227], v232 offset:16816\n\t" \
  "v_fmac_f32 v205, v25, v25\n\t" \
  "v_add_f32 v204, v204, v26\n\t" \
  "v_fmac_f32 v205, v26, v26\n\t" \
  "s_cmp_lg_u32 s37, 0\n\t" \
  "s_cbranch_scc1 .Lcnx_c9\n\t" \
  "s_waitcnt lgkmcnt(15)\n\t" \
  "v_add_f32 v96, v96, v98\n\t" \
  "v_add_f32 v97, v97, v99\n\t" \
  "v_add_f32 v100, v100, v102\n\t" \
  "v_add_f32 v101, v101, v103\n\t" \
  "v_add_f32 v112, v112, v114\n\t" \
  "v_add_f32 v113, v113, v115\n\t" \
  "v_add_f32 v116, v116, v118\n\t" \
  "v_add_f32 v117, v117, v119\n\t" \
  "v_add_f32 v96, v96, v100\n\t" \
  "v_add_f32 v97, v97, v101\n\t" \
  "v_add_f32 v112, v112, v116\n\t" \
  "v_add_f32 v113, v113, v117\n\t" \
  "v_add_f32 v96, v96, v112\n\t" \
  "v_add_f32 v97, v97, v113\n\t" \
  "v_mul_f32 v96, 0x3c2aaaab, v96\n\t" \
  "v_mul_f32 v97, 0x3c2aaaab, v97\n\t" \
  "v_fma_f32 v97, -v96, v96, v97\n\t" \
  "v_max_f32 v97, 0, v97\n\t" \
  "v_add_f32 v97, 0x3727c5ac, v97\n\t" \
  "v_rsq_f32 v97, v97\n\t" \
  "s_nop 0\n\t" \
  "v_mul_f32 v98, 0x3e000000, v97\n\t" \
  "v_mul_f32 v99, -v96, v98\n\t" \
  "ds_write_b64 v233, v[98:99] offset:32784\n\t" \
  ".Lcnx_c9:\n\t" \
  "s_waitcnt lgkmcnt(6)\n\t" \
  "v_smfmac_f32_16x16x64_bf16 v[36:39], v[108:111], v[208:215], v236\n\t" \
  "ds_read_b64 v[228:229], v232 offset:17216\n\t" \
  "ds_read_b64 v[230:231], v232 offset:17616\n\t" \
  "v_add_f32 v204, v204, v27\n\t" \
  "v_fmac_f32 v205, v27, v27\n\t" \
  "v_add_f32 v204, v204, v28\n\t" \
  "s_waitcnt lgkmcnt(4)\n\t" \
  "v_smfmac_f32_16x16x64_bf16 v[40:43], v[124:127], v[216:223], v236\n\t" \
  "v_fmac_f32 v205, v28, v28\n\t" \
  "v_add_f32 v204, v204, v29\n\t" \
  "v_fmac_f32 v205, v29, v29\n\t" \
  "s_waitcnt lgkmcnt(0)\n\t" \
  "v_smfmac_f32_16x16x64_bf16 v[44:47], v[140:143], v[224:231], v236\n\t" \
  "v_add_f32 v204, v204, v30\n\t" \
  "v_fmac_f32 v205, v30, v30\n\t" \
  "v_add_f32 v204, v204, v31\n\t" \
  "v_fmac_f32 v205, v31, v31\n\t" \
  "v_add_f32 v204, v204, v32\n\t" \
  "v_fmac_f32 v205, v32, v32\n\t" \
  "v_add_f32 v204, v204, v33\n\t" \
  "v_fmac_f32 v205, v33, v33\n\t" \
  "v_add_f32 v204, v204, v34\n\t" \
  "v_fmac_f32 v205, v34, v34\n\t" \
  "v_add_f32 v204, v204, v35\n\t" \
  "v_fmac_f32 v205, v35, v35\n\t" \
  "v_mul_f32 v207, v36, v36\n\t" \
  "v_add_f32 v206, v36, v37\n\t" \
  "v_fmac_f32 v207, v37, v37\n\t" \
  "v_add_f32 v206, v206, v38\n\t" \
  "v_fmac_f32 v207, v38, v38\n\t" \
  "v_add_f32 v206, v206, v39\n\t" \
  "v_fmac_f32 v207, v39, v39\n\t" \
  "v_add_f32 v206, v206, v40\n\t" \
  "v_fmac_f32 v207, v40, v40\n\t" \
  "v_add_f32 v206, v206, v41\n\t" \
  "v_fmac_f32 v207, v41, v41\n\t" \
  "v_add_f32 v206, v206, v42\n\t" \
  "v_fmac_f32 v207, v42, v42\n\t" \
  "v_add_f32 v206, v206, v43\n\t" \
  "v_fmac_f32 v207, v43, v43\n\t" \
  "v_add_f32 v206, v206, v44\n\t" \
  "v_fmac_f32 v207, v44, v44\n\t" \
  "v_add_f32 v206, v206, v45\n\t" \
  "v_fmac_f32 v207, v45, v45\n\t" \
  "v_add_f32 v206, v206, v46\n\t" \
  "v_fmac_f32 v207, v46, v46\n\t" \
  "v_add_f32 v206, v206, v47\n\t" \
  "v_fmac_f32 v207, v47, v47\n\t" \
  "ds_write_b64 v233, v[204:205] offset:8192\n\t" \
  "ds_write_b64 v233, v[206:207] offset:12288\n\t" \
  "s_waitcnt lgkmcnt(0)\n\t" \
  "s_barrier\n\t" \
  "ds_read_b64 v[192:193], v251 offset:32784\n\t" \
  "ds_read_b64 v[194:195], v251 offset:33296\n\t" \
  "ds_read_b64 v[196:197], v251 offset:33808\n\t" \
  "ds_read_b64 v[198:199], v251 offset:34320\n\t" \
  "s_cmp_lg_u32 s37, 1\n\t" \
  "s_cbranch_scc1 .Lcnx_c10\n\t" \
  "v_add_u32 v128, s40, v251\n\t" \
  "ds_read_b64 v[96:97], v128 offset:0\n\t" \
  "ds_read_b64 v[98:99], v128 offset:512\n\t" \
  "ds_read_b64 v[100:101], v128 offset:1024\n\t" \
  "ds_read_b64 v[102:103], v128 offset:1536\n\t" \
  "ds_read_b64 v[112:113], v128 offset:2048\n\t" \
  "ds_read_b64 v[114:115], v128 offset:2560\n\t" \
  "ds_read_b64 v[116:117], v128 offset:3072\n\t" \
  "ds_read_b64 v[118:119], v128 offset:3584\n\t" \
  ".Lcnx_c10:\n\t" \
  "v_lshl_add_u32 v208, v251, 1, s42\n\t" \
  "v_lshl_add_u32 v209, v251, 1, s43\n\t" \
  "s_waitcnt lgkmcnt(3)\n\t" \
  ".p2align 3\n\t" \
  "v_fma_f32 v144, v144, v192, v193\n\t" \
  "v_fma_f32 v145, v145, v192, v193\n\t" \
  "v_fma_f32 v146, v146, v192, v193\n\t" \
  "v_fma_f32 v147, v147, v192, v193\n\t" \
  "v_fma_f32 v148, v148, v192, v193\n\t" \
  "v_fma_f32 v149, v149, v192, v193\n\t" \
  "v_fma_f32 v150, v150, v192, v193\n\t" \
  "v_fma_f32 v151, v151, v192, v193\n\t" \
  "v_fma_f32 v152, v152, v192, v193\n\t" \
  "v_fma_f32 v153, v153, v192, v193\n\t" \
  "v_fma_f32 v154, v154, v192, v193\n\t" \
  "v_fma_f32 v155, v155, v192, v193\n\t" \
  "v_cvt_pk_bf16_f32 v144, v144, v145\n\t" \
  "v_cvt_pk_bf16_f32 v145, v146, v147\n\t" \
  "v_cvt_pk_bf16_f32 v146, v148, v149\n\t" \
  "v_cvt_pk_bf16_f32 v147, v150, v151\n\t" \
  "v_cvt_pk_bf16_f32 v148, v152, v153\n\t" \
  "v_cvt_pk_bf16_f32 v149, v154, v155\n\t" \
  "ds_write_b128 v208, v[144:147] offset:0\n\t" \
  "ds_write_b64 v209, v[148:149] offset:0\n\t" \
  "buffer_store_dwordx4 v[64:67], v250, s[20:23], s62 offen offset:128 sc1\n\t" \
  "s_cmp_lg_u32 s37, 1\n\t" \
  "s_cbranch_scc1 .Lcnx_c11\n\t" \
  "s_waitcnt lgkmcnt(2)\n\t" \
  "v_add_f32 v96, v96, v98\n\t" \
  "v_add_f32 v97, v97, v99\n\t" \
  "v_add_f32 v100, v100, v102\n\t" \
  "v_add_f32 v101, v101, v103\n\t" \
  "v_add_f32 v112, v112, v114\n\t" \
  "v_add_f32 v113, v113, v115\n\t" \
  "v_add_f32 v116, v116, v118\n\t" \
  "v_add_f32 v117, v117, v119\n\t" \
  "v_add_f32 v96, v96, v100\n\t" \
  "v_add_f32 v97, v97, v101\n\t" \
  "v_add_f32 v112, v112, v116\n\t" \
  "v_add_f32 v113, v113, v117\n\t" \
  "v_add_f32 v96, v96, v112\n\t" \
  "v_add_f32 v97, v97, v113\n\t" \
  "v_mul_f32 v96, 0x3c2aaaab, v96\n\t" \
  "v_mul_f32 v97, 0x3c2aaaab, v97\n\t" \
  "v_fma_f32 v97, -v96, v96, v97\n\t" \
  "v_max_f32 v97, 0, v97\n\t" \
  "v_add_f32 v97, 0x3727c5ac, v97\n\t" \
  "v_rsq_f32 v97, v97\n\t" \
  "s_nop 0\n\t" \
  "v_mul_f32 v98, 0x3e000000, v97\n\t" \
  "v_mul_f32 v99, -v96, v98\n\t" \
  "ds_write_b64 v233, v[98:99] offset:14352\n\t" \
  ".Lcnx_c11:\n\t" \
  "s_waitcnt lgkmcnt(4)\n\t" \
  ".p2align 3\n\t" \
  "v_fma_f32 v156, v156, v194, v195\n\t" \
  "v_fma_f32 v157, v157, v194, v195\n\t" \
  "v_fma_f32 v158, v158, v194, v195\n\t" \
  "v_fma_f32 v159, v159, v194, v195\n\t" \
  "v_fma_f32 v160, v160, v194, v195\n\t" \
  "v_fma_f32 v161, v161, v194, v195\n\t" \
  "v_fma_f32 v162, v162, v194, v195\n\t" \
  "v_fma_f32 v163, v163, v194, v195\n\t" \
  "v_fma_f32 v164, v164, v194, v195\n\t" \
  "v_fma_f32 v165, v165, v194, v195\n\t" \
  "v_fma_f32 v166, v166, v194, v195\n\t" \
  "v_fma_f32 v167, v167, v194, v195\n\t" \
  "v_cvt_pk_bf16_f32 v156, v156, v157\n\t" \
  "v_cvt_pk_bf16_f32 v157, v158, v159\n\t" \
  "v_cvt_pk_bf16_f32 v158, v160, v161\n\t" \
  "v_cvt_pk_bf16_f32 v159, v162, v163\n\t" \
  "v_cvt_pk_bf16_f32 v160, v164, v165\n\t" \
  "v_cvt_pk_bf16_f32 v161, v166, v167\n\t" \
  "ds_write_b128 v208, v[156:159] offset:12288\n\t" \
  "ds_write_b64 v209, v[160:161] offset:12288\n\t" \
  "s_waitcnt lgkmcnt(5)\n\t" \
  ".p2align 3\n\t" \
  "v_fma_f32 v168, v168, v196, v197\n\t" \
  "v_fma_f32 v169, v169, v196, v197\n\t" \
  "v_fma_f32 v170, v170, v196, v197\n\t" \
  "v_fma_f32 v171, v171, v196, v197\n\t" \
  "v_fma_f32 v172, v172, v196, v197\n\t" \
  "v_fma_f32 v173, v173, v196, v197\n\t" \
  "v_fma_f32 v174, v174, v196, v197\n\t" \
  "v_fma_f32 v175, v175, v196, v197\n\t" \
  "v_fma_f32 v176, v176, v196, v197\n\t" \
  "v_fma_f32 v177, v177, v196, v197\n\t" \
  "v_fma_f32 v178, v178, v196, v197\n\t" \
  "v_fma_f32 v179, v179, v196, v197\n\t" \
  "v_cvt_pk_bf16_f32 v168, v168, v169\n\t" \
  "v_cvt_pk_bf16_f32 v169, v170, v171\n\t" \
  "v_cvt_pk_bf16_f32 v170, v172, v173\n\t" \
  "v_cvt_pk_bf16_f32 v171, v174, v175\n\t" \
  "v_cvt_pk_bf16_f32 v172, v176, v177\n\t" \
  "v_cvt_pk_bf16_f32 v173, v178, v179\n\t" \
  "ds_write_b128 v208, v[168:171] offset:24576\n\t" \
  "ds_write_b64 v209, v[172:173] offset:24576\n\t" \
  "s_waitcnt lgkmcnt(6)\n\t" \
  ".p2align 3\n\t" \
  "v_fma_f32 v180, v180, v198, v199\n\t" \
  "v_fma_f32 v181, v181, v198, v199\n\t" \
  "v_fma_f32 v182, v182, v198, v199\n\t" \
  "v_fma_f32 v183, v183, v198, v199\n\t" \
  "v_fma_f32 v184, v184, v198, v199\n\t" \
  "v_fma_f32 v185, v185, v198, v199\n\t" \
  "v_fma_f32 v186, v186, v198, v199\n\t" \
  "v_fma_f32 v187, v187, v198, v199\n\t" \
  "v_fma_f32 v188, v188, v198, v199\n\t" \
  "v_fma_f32 v189, v189, v198, v199\n\t" \
  "v_fma_f32 v190, v190, v198, v199\n\t" \
  "v_fma_f32 v191, v191, v198, v199\n\t" \
  "v_cvt_pk_bf16_f32 v180, v180, v181\n\t" \
  "v_cvt_pk_bf16_f32 v181, v182, v183\n\t" \
  "v_cvt_pk_bf16_f32 v182, v184, v185\n\t" \
  "v_cvt_pk_bf16_f32 v183, v186, v187\n\t" \
  "v_cvt_pk_bf16_f32 v184, v188, v189\n\t" \
  "v_cvt_pk_bf16_f32 v185, v190, v191\n\t" \
  "ds_write_b128 v208, v[180:183] offset:36864\n\t" \
  "ds_write_b64 v209, v[184:185] offset:36864\n\t" \
  "v_add_u32 v208, 49152, v208\n\t" \
  "v_add_u32 v209, 49152, v209\n\t" \
  "s_waitcnt lgkmcnt(0)\n\t" \
  "s_barrier\n\t" \
  "ds_read_b64 v[200:201], v251 offset:16400\n\t" \
  "ds_read_b64 v[202:203], v251 offset:16912\n\t" \
  "ds_read_b64 v[204:205], v251 offset:17424\n\t" \
  "ds_read_b64 v[206:207], v251 offset:17936\n\t" \
  "s_waitcnt lgkmcnt(3)\n\t" \
  ".p2align 3\n\t" \
  "v_fma_f32 v0, v0, v200, v201\n\t" \
  "v_fma_f32 v1, v1, v200, v201\n\t" \
  "v_fma_f32 v2, v2, v200, v201\n\t" \
  "v_fma_f32 v3, v3, v200, v201\n\t" \
  "v_fma_f32 v4, v4, v200, v201\n\t" \
  "v_fma_f32 v5, v5, v200, v201\n\t" \
  "v_fma_f32 v6, v6, v200, v201\n\t" \
  "v_fma_f32 v7, v7, v200, v201\n\t" \
  "v_fma_f32 v8, v8, v200, v201\n\t" \
  "v_fma_f32 v9, v9, v200, v201\n\t" \
  "v_fma_f32 v10, v10, v200, v201\n\t" \
  "v_fma_f32 v11, v11, v200, v201\n\t" \
  "v_cvt_pk_bf16_f32 v0, v0, v1\n\t" \
  "v_cvt_pk_bf16_f32 v1, v2, v3\n\t" \
  "v_cvt_pk_bf16_f32 v2, v4, v5\n\t" \
  "v_cvt_pk_bf16_f32 v3, v6, v7\n\t" \
  "v_cvt_pk_bf16_f32 v4, v8, v9\n\t" \
  "v_cvt_pk_bf16_f32 v5, v10, v11\n\t" \
  "ds_write_b128 v208, v[0:3] offset:0\n\t" \
  "ds_write_b64 v209, v[4:5] offset:0\n\t" \
  "buffer_store_dwordx4 v[72:75], v250, s[20:23], s62 offen offset:192 sc1\n\t" \
  "s_waitcnt lgkmcnt(4)\n\t" \
  ".p2align 3\n\t" \
  "v_fma_f32 v12, v12, v202, v203\n\t" \
  "v_fma_f32 v13, v13, v202, v203\n\t" \
  "v_fma_f32 v14, v14, v202, v203\n\t" \
  "v_fma_f32 v15, v15, v202, v203\n\t" \
  "v_fma_f32 v16, v16, v202, v203\n\t" \
  "v_fma_f32 v17, v17, v202, v203\n\t" \
  "v_fma_f32 v18, v18, v202, v203\n\t" \
  "v_fma_f32 v19, v19, v202, v203\n\t" \
  "v_fma_f32 v20, v20, v202, v203\n\t" \
  "v_fma_f32 v21, v21, v202, v203\n\t" \
  "v_fma_f32 v22, v22, v202, v203\n\t" \
  "v_fma_f32 v23, v23, v202, v203\n\t" \
  "v_cvt_pk_bf16_f32 v12, v12, v13\n\t" \
  "v_cvt_pk_bf16_f32 v13, v14, v15\n\t" \
  "v_cvt_pk_bf16_f32 v14, v16, v17\n\t" \
  "v_cvt_pk_bf16_f32 v15, v18, v19\n\t" \
  "v_cvt_pk_bf16_f32 v16, v20, v21\n\t" \
  "v_cvt_pk_bf16_f32 v17, v22, v23\n\t" \
  "ds_write_b128 v208, v[12:15] offset:12288\n\t" \
  "ds_write_b64 v209, v[16:17] offset:12288\n\t" \
  "s_waitcnt lgkmcnt(5)\n\t" \
  ".p2align 3\n\t" \
  "v_fma_f32 v24, v24, v204, v205\n\t" \
  "v_fma_f32 v25, v25, v204, v205\n\t" \
  "v_fma_f32 v26, v26, v204, v205\n\t" \
  "v_fma_f32 v27, v27, v204, v205\n\t" \
  "v_fma_f32 v28, v28, v204, v205\n\t" \
  "v_fma_f32 v29, v29, v204, v205\n\t" \
  "v_fma_f32 v30, v30, v204, v205\n\t" \
  "v_fma_f32 v31, v31, v204, v205\n\t" \
  "v_fma_f32 v32, v32, v204, v205\n\t" \
  "v_fma_f32 v33, v33, v204, v205\n\t" \
  "v_fma_f32 v34, v34, v204, v205\n\t" \
  "v_fma_f32 v35, v35, v204, v205\n\t" \
  "v_cvt_pk_bf16_f32 v24, v24, v25\n\t" \
  "v_cvt_pk_bf16_f32 v25, v26, v27\n\t" \
  "v_cvt_pk_bf16_f32 v26, v28, v29\n\t" \
  "v_cvt_pk_bf16_f32 v27, v30, v31\n\t" \
  "v_cvt_pk_bf16_f32 v28, v32, v33\n\t" \
  "v_cvt_pk_bf16_f32 v29, v34, v35\n\t" \
  "ds_write_b128 v208, v[24:27] offset:24576\n\t" \
  "ds_write_b64 v209, v[28:29] offset:24576\n\t" \
  "s_waitcnt lgkmcnt(6)\n\t" \
  ".p2align 3\n\t" \
  "v_fma_f32 v36, v36, v206, v207\n\t" \
  "v_fma_f32 v37, v37, v206, v207\n\t" \
  "v_fma_f32 v38, v38, v206, v207\n\t" \
  "v_fma_f32 v39, v39, v206, v207\n\t" \
  "v_fma_f32 v40, v40, v206, v207\n\t" \
  "v_fma_f32 v41, v41, v206, v207\n\t" \
  "v_fma_f32 v42, v42, v206, v207\n\t" \
  "v_fma_f32 v43, v43, v206, v207\n\t" \
  "v_fma_f32 v44, v44, v206, v207\n\t" \
  "v_fma_f32 v45, v45, v206, v207\n\t" \
  "v_fma_f32 v46, v46, v206, v207\n\t" \
  "v_fma_f32 v47, v47, v206, v207\n\t" \
  "v_cvt_pk_bf16_f32 v36, v36, v37\n\t" \
  "v_cvt_pk_bf16_f32 v37, v38, v39\n\t" \
  "v_cvt_pk_bf16_f32 v38, v40, v41\n\t" \
  "v_cvt_pk_bf16_f32 v39, v42, v43\n\t" \
  "v_cvt_pk_bf16_f32 v40, v44, v45\n\t" \
  "v_cvt_pk_bf16_f32 v41, v46, v47\n\t" \
  "ds_write_b128 v208, v[36:39] offset:36864\n\t" \
  "ds_write_b64 v209, v[40:41] offset:36864\n\t" \
  "v_lshlrev_b32 v232, 1, v251\n\t" \
  "v_lshl_add_u32 v237, v251, 3, s39\n\t" \
  "v_and_b32 v232, 0xf0, v232\n\t" \
  "v_mad_i32_i24 v237, v232, -3, v237\n\t" \
  "s_cmp_lg_u32 s58, 0\n\t" \
  "s_cbranch_scc1 .Lcnx_c12\n\t" \
  "s_waitcnt vmcnt(0)\n\t" \
  ".Lcnx_c12:\n\t" \
  "s_waitcnt lgkmcnt(0)\n\t" \
  "s_barrier\n\t" \
  "ds_read_b64_tr_b16 v[208:209], v248 offset:43048\n\t" \
  "ds_read_b64_tr_b16 v[210:211], v248 offset:44072\n\t" \
  "ds_read_b128 v[96:99], v237 offset:0\n\t" \
  "ds_read_b128 v[108:111], v237 offset:256\n\t" \
  "ds_read_b128 v[120:123], v237 offset:512\n\t" \
  "ds_read_b128 v[132:135], v237 offset:768\n\t" \
  "ds_read_b64_tr_b16 v[212:213], v248 offset:46120\n\t" \
  "ds_read_b64_tr_b16 v[214:215], v248 offset:47144\n\t" \
  "ds_read_b128 v[100:103], v237 offset:4096\n\t" \
  "ds_read_b128 v[112:115], v237 offset:4352\n\t" \
  "ds_read_b128 v[124:127], v237 offset:4608\n\t" \
  "ds_read_b128 v[136:139], v237 offset:4864\n\t" \
  "ds_read_b128 v[104:107], v237 offset:8192\n\t" \
  "ds_read_b128 v[116:119], v237 offset:8448\n\t" \
  "ds_read_b128 v[128:131], v237 offset:8704\n\t" \
  "ds_read_b128 v[140:143], v237 offset:8960\n\t" \
  "s_waitcnt lgkmcnt(13)\n\t" \
  "v_mfma_f32_16x16x32_bf16 v[144:147], v[208:211], v[96:99], 0\n\t" \
  "s_waitcnt lgkmcnt(12)\n\t" \
  "v_mfma_f32_16x16x32_bf16 v[152:155], v[208:211], v[108:111], 0\n\t" \
  "s_cmp_eq_u32 s37, 0\n\t" \
  "s_cbranch_scc1 .Lcnx_f0\n.Lcnx_g0:\n\t" \
  "s_waitcnt lgkmcnt(11)\n\t" \
  "v_mfma_f32_16x16x32_bf16 v[160:163], v[208:211], v[120:123], 0\n\t" \
  "buffer_store_dwordx4 v[80:83], v250, s[20:23], s62 offen offset:256 sc1\n\t" \
  "s_waitcnt lgkmcnt(10)\n\t" \
  "v_mfma_f32_16x16x32_bf16 v[168:171], v[208:211], v[132:135], 0\n\t" \
  "ds_read_b64_tr_b16 v[208:209], v248 offset:45096\n\t" \
  "s_waitcnt lgkmcnt(8)\n\t" \
  "v_mfma_f32_16x16x32_bf16 v[144:147], v[212:215], v[100:103], v[144:147]\n\t" \
  "ds_read_b64_tr_b16 v[210:211], v248 offset:48168\n\t" \
  "s_waitcnt lgkmcnt(8)\n\t" \
  "v_mfma_f32_16x16x32_bf16 v[152:155], v[212:215], v[112:115], v[152:155]\n\t" \
  "s_waitcnt lgkmcnt(7)\n\t" \
  "v_mfma_f32_16x16x32_bf16 v[160:163], v[212:215], v[124:127], v[160:163]\n\t" \
  "s_waitcnt lgkmcnt(6)\n\t" \
  "v_mfma_f32_16x16x32_bf16 v[168:171], v[212:215], v[136:139], v[168:171]\n\t" \
  "ds_read_b64_tr_b16 v[212:213], v248 offset:43560\n\t" \
  "s_waitcnt lgkmcnt(1)\n\t" \
  "v_mfma_f32_16x16x32_bf16 v[144:147], v[208:211], v[104:107], v[144:147]\n\t" \
  "ds_read_b64_tr_b16 v[214:215], v248 offset:44584\n\t" \
  "v_mfma_f32_16x16x32_bf16 v[152:155], v[208:211], v[116:119], v[152:155]\n\t" \
  "v_mfma_f32_16x16x32_bf16 v[160:163], v[208:211], v[128:131], v[160:163]\n\t" \
  "v_mfma_f32_16x16x32_bf16 v[168:171], v[208:211], v[140:143], v[168:171]\n\t" \
  "ds_read_b64_tr_b16 v[208:209], v248 offset:46632\n\t" \
  "s_waitcnt lgkmcnt(1)\n\t" \
  "v_mfma_f32_16x16x32_bf16 v[148:151], v[212:215], v[96:99], 0\n\t" \
  "buffer_store_dwordx4 v[88:91], v250, s[20:23], s62 offen offset:320 sc1\n\t" \
  "ds_read_b64_tr_b16 v[210:211], v248 offset:47656\n\t" \
  "v_mfma_f32_16x16x32_bf16 v[156:159], v[212:215], v[108:111], 0\n\t" \
  "v_mfma_f32_16x16x32_bf16 v[164:167], v[212:215], v[120:123], 0\n\t" \
  "v_mfma_f32_16x16x32_bf16 v[172:175], v[212:215], v[132:135], 0\n\t" \
  "ds_read_b64_tr_b16 v[212:213], v248 offset:45608\n\t" \
  "s_waitcnt lgkmcnt(1)\n\t" \
  "v_mfma_f32_16x16x32_bf16 v[148:151], v[208:211], v[100:103], v[148:151]\n\t" \
  "ds_read_b64_tr_b16 v[214:215], v248 offset:48680\n\t" \
  "v_mfma_f32_16x16x32_bf16 v[156:159], v[208:211], v[112:115], v[156:159]\n\t" \
  "s_mov_b64 exec, 3\n\t" \
  "ds_add_rtn_u32 v246, v251, v250 offset:47648\n\t" \
  "s_mov_b64 exec, -1\n\t" \
  "v_cvt_pk_f16_f32 v216, |v146|, |v147| clamp\n\t" \
  "v_cvt_pk_f16_f32 v232, v144, v145\n\t" \
  "v_mfma_f32_16x16x32_bf16 v[164:167], v[208:211], v[124:127], v[164:167]\n\t" \
  "v_cvt_pk_f16_f32 v218, |v154|, |v155| clamp\n\t" \
  "v_cvt_pk_f16_f32 v220, |v152|, |v153| clamp\n\t" \
  "v_pk_mul_f16 v233, v232, v232\n\t" \
  "v_mfma_f32_16x16x32_bf16 v[172:175], v[208:211], v[136:139], v[172:175]\n\t" \
  "ds_read_b64_tr_b16 v[208:209], v248 offset:49192\n\t" \
  "v_and_b32_sdwa v217, s56, v216 dst_sel:DWORD dst_unused:UNUSED_PAD src0_sel:DWORD src1_sel:WORD_1\n\t" \
  "v_and_b32 v216, s56, v216\n\t" \
  "s_waitcnt lgkmcnt(2)\n\t" \
  "v_mfma_f32_16x16x32_bf16 v[148:151], v[212:215], v[104:107], v[148:151]\n\t" \
  "ds_read_b64_tr_b16 v[210:211], v248 offset:50216\n\t" \
  "v_and_b32_sdwa v219, s56, v218 dst_sel:DWORD dst_unused:UNUSED_PAD src0_sel:DWORD src1_sel:WORD_1\n\t" \
  "v_mfma_f32_16x16x32_bf16 v[156:159], v[212:215], v[116:119], v[156:159]\n\t" \
  "ds_read_b32 v217, v217 offset:0\n\t" \
  "v_and_b32 v218, s56, v218\n\t" \
  "v_and_b32_sdwa v221, s56, v220 dst_sel:DWORD dst_unused:UNUSED_PAD src0_sel:DWORD src1_sel:WORD_1\n\t" \
  "v_mfma_f32_16x16x32_bf16 v[164:167], v[212:215], v[128:131], v[164:167]\n\t" \
  "ds_read_b32 v216, v216 offset:0\n\t" \
  "v_and_b32 v220, s56, v220\n\t" \
  "v_mfma_f32_16x16x32_bf16 v[172:175], v[212:215], v[140:143], v[172:175]\n\t" \
  "ds_read_b64_tr_b16 v[212:213], v248 offset:52264\n\t" \
  "v_pk_fma_f16 v233, v233, v253, s54\n\t" \
  "v_pk_mul_f16 v233, v232, v233\n\t" \
  "s_cmp_lg_u32 s38, 4\n\t" \
  "s_cbranch_scc1 .Lcnx_c24\n\t" \
  "s_mov_b32 m0, 163632\n\t" \
  "s_add_u32 s30, s30, s31\n\t" \
  "buffer_load_dword v252, s[8:11], s30 offen lds\n\t" \
  ".Lcnx_c24:\n\t" \
  "s_setprio 0\n\t" \
  "v_exp_f16 v232, v233\n\t" \
  "v_cvt_pk_f16_f32 v224, |v162|, |v163| clamp\n\t" \
  "s_waitcnt lgkmcnt(3)\n\t" \
  "v_mfma_f32_16x16x32_bf16 v[176:179], v[208:211], v[96:99], 0\n\t" \
  "ds_read_b64_tr_b16 v[214:215], v248 offset:53288\n\t" \
  "v_cvt_pk_f16_f32 v234, v160, v161\n\t" \
  "v_cvt_pk_f16_f32 v226, |v170|, |v171| clamp\n\t" \
  "v_cvt_pk_f16_f32 v228, |v168|, |v169| clamp\n\t" \
  "v_exp_f16_sdwa v232, v233 dst_sel:WORD_1 dst_unused:UNUSED_PRESERVE src0_sel:WORD_1\n\t" \
  "v_pk_mul_f16 v235, v234, v234\n\t" \
  "v_mfma_f32_16x16x32_bf16 v[184:187], v[208:211], v[108:111], 0\n\t" \
  "buffer_load_dwordx4 v[236:239], v250, s[8:11], s28 offen offset:0\n\t" \
  "s_cmp_lg_u32 s38, 5\n\t" \
  "s_cbranch_scc1 .Lcnx_c25\n\t" \
  "s_mov_b32 m0, 163632\n\t" \
  "s_add_u32 s30, s30, s31\n\t" \
  "buffer_load_dword v252, s[8:11], s30 offen lds\n\t" \
  ".Lcnx_c25:\n\t" \
  "ds_read_b32 v219, v219 offset:0\n\t" \
  "v_and_b32_sdwa v225, s56, v224 dst_sel:DWORD dst_unused:UNUSED_PAD src0_sel:DWORD src1_sel:WORD_1\n\t" \
  "v_and_b32 v224, s56, v224\n\t" \
  "v_and_b32_sdwa v227, s56, v226 dst_sel:DWORD dst_unused:UNUSED_PAD src0_sel:DWORD src1_sel:WORD_1\n\t" \
  "v_and_b32 v226, s56, v226\n\t" \
  "v_mfma_f32_16x16x32_bf16 v[192:195], v[208:211], v[120:123], 0\n\t" \
  "ds_read_b32 v218, v218 offset:0\n\t" \
  "v_and_b32_sdwa v229, s56, v228 dst_sel:DWORD dst_unused:UNUSED_PAD src0_sel:DWORD src1_sel:WORD_1\n\t" \
  "v_and_b32 v228, s56, v228\n\t" \
  "v_mfma_f32_16x16x32_bf16 v[200:203], v[208:211], v[132:135], 0\n\t" \
  "ds_read_b64_tr_b16 v[208:209], v248 offset:51240\n\t" \
  "ds_read_b64_tr_b16 v[210:211], v248 offset:54312\n\t" \
  "v_pk_fma_f16 v235, v235, v253, s54\n\t" \
  "v_pk_mul_f16 v235, v234, v235\n\t" \
  "ds_read_b32 v221, v221 offset:0\n\t" \
  "ds_read_b32 v220, v220 offset:0\n\t" \
  "ds_read_b32 v225, v225 offset:0\n\t" \
  "ds_read_b32 v224, v224 offset:0\n\t" \
  "ds_read_b32 v227, v227 offset:0\n\t" \
  "ds_read_b32 v226, v226 offset:0\n\t" \
  "ds_read_b32 v229, v229 offset:0\n\t" \
  "ds_read_b32 v228, v228 offset:0\n\t" \
  "s_waitcnt lgkmcnt(11)\n\t" \
  "v_fma_f32 v147, |v147|, v217, v147 mul:4\n\t" \
  "v_fma_f32 v146, |v146|, v216, v146 mul:4\n\t" \
  "v_fma_f32 v155, |v155|, v219, v155 mul:4\n\t" \
  "v_mfma_f32_16x16x32_bf16 v[176:179], v[212:215], v[100:103], v[176:179]\n\t" \
  "s_waitcnt lgkmcnt(6)\n\t" \
  "v_fma_f32 v154, |v154|, v218, v154 mul:4\n\t" \
  "v_fma_f32 v153, |v153|, v221, v153 mul:4\n\t" \
  "v_fma_f32 v152, |v152|, v220, v152 mul:4\n\t" \
  "v_pk_fma_f16 v232, v232, s55, s55\n\t" \
  "v_cvt_pk_bf16_f32 v147, v146, v147\n\t" \
  "v_mfma_f32_16x16x32_bf16 v[184:187], v[212:215], v[112:115], v[184:187]\n\t" \
  "s_cmp_lg_u32 s37, 0\n\t" \
  "s_cbranch_scc1 .Lcnx_c13\n\t" \
  "s_waitcnt vmcnt(3)\n\t" \
  ".Lcnx_c13:\n\t" \
  "s_mov_b64 exec, 3\n\t" \
  "ds_add_rtn_u32 v246, v251, v250 offset:47656\n\t" \
  "s_mov_b64 exec, -1\n\t" \
  "v_cvt_pk_bf16_f32 v155, v154, v155\n\t" \
  "s_setprio 1\n\t" \
  "v_cvt_pk_bf16_f32 v154, v152, v153\n\t" \
  "v_rcp_f16 v233, v232\n\t" \
  "v_exp_f16 v234, v235\n\t" \
  "v_cvt_pk_f16_f32 v216, |v148|, |v149| clamp\n\t" \
  "v_cvt_pk_f16_f32 v218, |v150|, |v151| clamp\n\t" \
  "v_mfma_f32_16x16x32_bf16 v[192:195], v[212:215], v[124:127], v[192:195]\n\t" \
  "v_cvt_pk_f16_f32 v220, |v156|, |v157| clamp\n\t" \
  "v_cvt_pk_f16_f32 v222, |v158|, |v159| clamp\n\t" \
  "v_rcp_f16_sdwa v232, v232 dst_sel:DWORD dst_unused:UNUSED_PAD src0_sel:WORD_1\n\t" \
  "v_exp_f16_sdwa v234, v235 dst_sel:WORD_1 dst_unused:UNUSED_PRESERVE src0_sel:WORD_1\n\t" \
  "v_and_b32_sdwa v217, s56, v216 dst_sel:DWORD dst_unused:UNUSED_PAD src0_sel:DWORD src1_sel:WORD_1\n\t" \
  "v_and_b32 v216, s56, v216\n\t" \
  "v_mfma_f32_16x16x32_bf16 v[200:203], v[212:215], v[136:139], v[200:203]\n\t" \
  "s_waitcnt lgkmcnt(0)\n\t" \
  "v_readfirstlane_b32 s4, v246\n\t" \
  "s_cmp_lt_u32 s4, s57\n\t" \
  "s_cbranch_scc1 .Lcnx_w9\n.Lcnx_b9:\n\t" \
  "ds_read_b64_tr_b16 v[212:213], v248 offset:49704\n\t" \
  "v_and_b32_sdwa v219, s56, v218 dst_sel:DWORD dst_unused:UNUSED_PAD src0_sel:DWORD src1_sel:WORD_1\n\t" \
  "v_and_b32 v218, s56, v218\n\t" \
  "v_mfma_f32_16x16x32_bf16 v[176:179], v[208:211], v[104:107], v[176:179]\n\t" \
  "ds_read_b64_tr_b16 v[214:215], v248 offset:50728\n\t" \
  "v_and_b32_sdwa v221, s56, v220 dst_sel:DWORD dst_unused:UNUSED_PAD src0_sel:DWORD src1_sel:WORD_1\n\t" \
  "v_and_b32 v220, s56, v220\n\t" \
  "v_and_b32_sdwa v223, s56, v222 dst_sel:DWORD dst_unused:UNUSED_PAD src0_sel:DWORD src1_sel:WORD_1\n\t" \
  "v_mfma_f32_16x16x32_bf16 v[184:187], v[208:211], v[116:119], v[184:187]\n\t" \
  "s_cmp_eq_u32 s37, 0\n\t" \
  "s_cbranch_scc1 .Lcnx_f1\n.Lcnx_g1:\n\t" \
  "ds_read_b32 v217, v217 offset:0\n\t" \
  "ds_read_b32 v216, v216 offset:0\n\t" \
  "v_and_b32 v222, s56, v222\n\t" \
  "v_fma_f32 v163, |v163|, v225, v163 mul:4\n\t" \
  "v_fma_f32 v162, |v162|, v224, v162 mul:4\n\t" \
  "v_mfma_f32_16x16x32_bf16 v[192:195], v[208:211], v[128:131], v[192:195]\n\t" \
  "ds_read_b32 v219, v219 offset:0\n\t" \
  "ds_read_b32 v218, v218 offset:0\n\t" \
  "v_fma_f32 v171, |v171|, v227, v171 mul:4\n\t" \
  "v_fma_f32 v170, |v170|, v226, v170 mul:4\n\t" \
  "v_fma_f32 v169, |v169|, v229, v169 mul:4\n\t" \
  "v_fma_f32 v168, |v168|, v228, v168 mul:4\n\t" \
  "v_fma_mix_f32 v144, v144, v233, 0 op_sel_hi:[0,1,0]\n\t" \
  "v_fma_mix_f32 v145, v145, v232, 0 op_sel_hi:[0,1,0]\n\t" \
  "v_mfma_f32_16x16x32_bf16 v[200:203], v[208:211], v[140:143], v[200:203]\n\t" \
  "ds_read_b64_tr_b16 v[208:209], v248 offset:52776\n\t" \
  "ds_read_b64_tr_b16 v[210:211], v248 offset:53800\n\t" \
  "v_pk_fma_f16 v234, v234, s55, s55\n\t" \
  "v_cvt_pk_bf16_f32 v146, v144, v145\n\t" \
  "v_cvt_pk_bf16_f32 v163, v162, v163\n\t" \
  "v_cvt_pk_bf16_f32 v171, v170, v171\n\t" \
  "v_cvt_pk_bf16_f32 v170, v168, v169\n\t" \
  "s_setprio 2\n\t" \
  "v_rcp_f16 v235, v234\n\t" \
  "s_waitcnt lgkmcnt(6)\n\t" \
  "v_mfma_f32_16x16x32_bf16 v[180:183], v[212:215], v[96:99], 0\n\t" \
  "ds_read_b32 v221, v221 offset:0\n\t" \
  "v_cvt_pk_f16_f32 v224, |v164|, |v165| clamp\n\t" \
  "v_cvt_pk_f16_f32 v226, |v166|, |v167| clamp\n\t" \
  "v_cvt_pk_f16_f32 v228, |v172|, |v173| clamp\n\t" \
  "v_cvt_pk_f16_f32 v230, |v174|, |v175| clamp\n\t" \
  "v_rcp_f16_sdwa v234, v234 dst_sel:DWORD dst_unused:UNUSED_PAD src0_sel:WORD_1\n\t" \
  "v_mfma_f32_16x16x32_bf16 v[188:191], v[212:215], v[108:111], 0\n\t" \
  "ds_read_b32 v220, v220 offset:0\n\t" \
  "v_and_b32_sdwa v225, s56, v224 dst_sel:DWORD dst_unused:UNUSED_PAD src0_sel:DWORD src1_sel:WORD_1\n\t" \
  "v_and_b32 v224, s56, v224\n\t" \
  "v_and_b32_sdwa v227, s56, v226 dst_sel:DWORD dst_unused:UNUSED_PAD src0_sel:DWORD src1_sel:WORD_1\n\t" \
  "v_and_b32 v226, s56, v226\n\t" \
  "v_mfma_f32_16x16x32_bf16 v[196:199], v[212:215], v[120:123], 0\n\t" \
  "ds_read_b32 v223, v223 offset:0\n\t" \
  "v_and_b32_sdwa v229, s56, v228 dst_sel:DWORD dst_unused:UNUSED_PAD src0_sel:DWORD src1_sel:WORD_1\n\t" \
  "v_and_b32 v228, s56, v228\n\t" \
  "v_mfma_f32_16x16x32_bf16 v[204:207], v[212:215], v[132:135], 0\n\t" \
  "ds_read_b64_tr_b16 v[212:213], v248 offset:51752\n\t" \
  "ds_read_b64_tr_b16 v[214:215], v248 offset:54824\n\t" \
  "v_and_b32_sdwa v231, s56, v230 dst_sel:DWORD dst_unused:UNUSED_PAD src0_sel:DWORD src1_sel:WORD_1\n\t" \
  "v_and_b32 v230, s56, v230\n\t" \
  "ds_read_b32 v222, v222 offset:0\n\t" \
  "ds_read_b32 v225, v225 offset:0\n\t" \
  "ds_read_b32 v224, v224 offset:0\n\t" \
  "ds_read_b32 v227, v227 offset:0\n\t" \
  "ds_read_b32 v226, v226 offset:0\n\t" \
  "ds_read_b32 v229, v229 offset:0\n\t" \
  "ds_read_b32 v228, v228 offset:0\n\t" \
  "ds_read_b32 v231, v231 offset:0\n\t" \
  "s_waitcnt lgkmcnt(15)\n\t" \
  "v_fma_f32 v149, |v149|, v217, v149 mul:4\n\t" \
  "s_waitcnt lgkmcnt(13)\n\t" \
  "v_mfma_f32_16x16x32_bf16 v[180:183], v[208:211], v[100:103], v[180:183]\n\t" \
  "ds_read_b32 v230, v230 offset:0\n\t" \
  "v_fma_f32 v148, |v148|, v216, v148 mul:4\n\t" \
  "v_fma_f32 v151, |v151|, v219, v151 mul:4\n\t" \
  "v_fma_f32 v150, |v150|, v218, v150 mul:4\n\t" \
  "s_waitcnt lgkmcnt(8)\n\t" \
  "v_fma_f32 v157, |v157|, v221, v157 mul:4\n\t" \
  "v_fma_f32 v156, |v156|, v220, v156 mul:4\n\t" \
  "v_mfma_f32_16x16x32_bf16 v[188:191], v[208:211], v[112:115], v[188:191]\n\t" \
  "s_waitcnt vmcnt(0)\n\t" \
  "s_mov_b64 exec, 3\n\t" \
  "ds_add_rtn_u32 v246, v251, v250 offset:47664\n\t" \
  "s_mov_b64 exec, -1\n\t" \
  "v_fma_f32 v159, |v159|, v223, v159 mul:4\n\t" \
  "v_fma_f32 v158, |v158|, v222, v158 mul:4\n\t" \
  "s_setprio 3\n\t" \
  "v_fma_mix_f32 v160, v160, v235, 0 op_sel_hi:[0,1,0]\n\t" \
  "v_fma_mix_f32 v161, v161, v234, 0 op_sel_hi:[0,1,0]\n\t" \
  "v_cvt_pk_bf16_f32 v162, v160, v161\n\t" \
  "v_cvt_pk_bf16_f32 v148, v148, v149\n\t" \
  "v_mfma_f32_16x16x32_bf16 v[196:199], v[208:211], v[124:127], v[196:199]\n\t" \
  "v_cvt_pk_bf16_f32 v149, v150, v151\n\t" \
  "v_cvt_pk_bf16_f32 v156, v156, v157\n\t" \
  "v_cvt_pk_bf16_f32 v157, v158, v159\n\t" \
  "v_cvt_pk_f16_f32 v216, |v178|, |v179| clamp\n\t" \
  "v_cvt_pk_f16_f32 v232, v176, v177\n\t" \
  "v_cvt_pk_f16_f32 v218, |v186|, |v187| clamp\n\t" \
  "v_mfma_f32_16x16x32_bf16 v[204:207], v[208:211], v[136:139], v[204:207]\n\t" \
  "s_waitcnt lgkmcnt(0)\n\t" \
  "v_readfirstlane_b32 s4, v246\n\t" \
  "s_cmp_lt_u32 s4, s57\n\t" \
  "s_cbranch_scc1 .Lcnx_w12\n.Lcnx_b12:\n\t" \
  "ds_read_b64_tr_b16 v[208:209], v249 offset:24600\n\t" \
  "v_cvt_pk_f16_f32 v220, |v184|, |v185| clamp\n\t" \
  "v_pk_mul_f16 v233, v232, v232\n\t" \
  "v_and_b32_sdwa v217, s56, v216 dst_sel:DWORD dst_unused:UNUSED_PAD src0_sel:DWORD src1_sel:WORD_1\n\t" \
  "v_and_b32 v216, s56, v216\n\t" \
  "v_mfma_f32_16x16x32_bf16 v[180:183], v[212:215], v[104:107], v[180:183]\n\t" \
  "ds_read_b64_tr_b16 v[210:211], v249 offset:26136\n\t" \
  "v_and_b32_sdwa v219, s56, v218 dst_sel:DWORD dst_unused:UNUSED_PAD src0_sel:DWORD src1_sel:WORD_1\n\t" \
  "v_and_b32 v218, s56, v218\n\t" \
  "v_and_b32_sdwa v221, s56, v220 dst_sel:DWORD dst_unused:UNUSED_PAD src0_sel:DWORD src1_sel:WORD_1\n\t" \
  "v_mfma_f32_16x16x32_bf16 v[188:191], v[212:215], v[116:119], v[188:191]\n\t" \
  "s_cmp_eq_u32 s37, 0\n\t" \
  "s_cbranch_scc1 .Lcnx_f2\n.Lcnx_g2:\n\t" \
  "ds_read_b32 v217, v217 offset:0\n\t" \
  "v_and_b32 v220, s56, v220\n\t" \
  "v_pk_fma_f16 v233, v233, v253, s54\n\t" \
  "v_pk_mul_f16 v233, v232, v233\n\t" \
  "v_mfma_f32_16x16x32_bf16 v[196:199], v[212:215], v[128:131], v[196:199]\n\t" \
  "ds_read_b32 v216, v216 offset:0\n\t" \
  "v_fma_f32 v165, |v165|, v225, v165 mul:4\n\t" \
  "v_fma_f32 v164, |v164|, v224, v164 mul:4\n\t" \
  "v_fma_f32 v167, |v167|, v227, v167 mul:4\n\t" \
  "v_fma_f32 v166, |v166|, v226, v166 mul:4\n\t" \
  "v_fma_f32 v173, |v173|, v229, v173 mul:4\n\t" \
  "v_fma_f32 v172, |v172|, v228, v172 mul:4\n\t" \
  "v_mfma_f32_16x16x32_bf16 v[204:207], v[212:215], v[140:143], v[204:207]\n\t" \
  "ds_read_b64_tr_b16 v[212:213], v249 offset:24608\n\t" \
  "ds_read_b64_tr_b16 v[214:215], v249 offset:26144\n\t" \
  "v_fma_f32 v175, |v175|, v231, v175 mul:4\n\t" \
  "v_fma_f32 v174, |v174|, v230, v174 mul:4\n\t" \
  "v_cvt_pk_bf16_f32 v164, v164, v165\n\t" \
  "v_cvt_pk_bf16_f32 v165, v166, v167\n\t" \
  "v_cvt_pk_bf16_f32 v172, v172, v173\n\t" \
  "v_cvt_pk_bf16_f32 v173, v174, v175\n\t" \
  "s_setprio 0\n\t" \
  "v_exp_f16 v232, v233\n\t" \
  "v_cvt_pk_f16_f32 v224, |v194|, |v195| clamp\n\t" \
  "v_mfma_f32_16x16x32_bf16 v[0:3], v[242:245], v[236:239], 0\n\t" \
  "ds_read_b32 v219, v219 offset:0\n\t" \
  "v_cvt_pk_f16_f32 v234, v192, v193\n\t" \
  "v_cvt_pk_f16_f32 v226, |v202|, |v203| clamp\n\t" \
  "v_mfma_f32_16x16x32_bf16 v[4:7], v[240:243], v[236:239], 0\n\t" \
  "buffer_load_dwordx4 v[236:239], v250, s[8:11], s28 offen offset:64\n\t" \
  "s_cmp_lg_u32 s58, 0\n\t" \
  "s_cbranch_scc1 .Lcnx_c26\n\t" \
  "s_sub_u32 s24, s24, s64\n\t" \
  "s_add_u32 m0, s25, 0\n\t" \
  "s_add_u32 s24, s24, s26\n\t" \
  "buffer_load_dwordx4 v254, s[8:11], s24 offen lds\n\t" \
  "s_cmp_lg_u32 s38, 6\n\t" \
  "s_cbranch_scc1 .Lcnx_c26\n\t" \
  "s_mov_b32 m0, 163632\n\t" \
  "s_add_u32 s30, s30, s31\n\t" \
  "buffer_load_dword v252, s[8:11], s30 offen lds\n\t" \
  ".Lcnx_c26:\n\t" \
  "ds_read_b32 v218, v218 offset:0\n\t" \
  "v_cvt_pk_f16_f32 v228, |v200|, |v201| clamp\n\t" \
  "v_exp_f16_sdwa v232, v233 dst_sel:WORD_1 dst_unused:UNUSED_PRESERVE src0_sel:WORD_1\n\t" \
  "v_pk_mul_f16 v235, v234, v234\n\t" \
  "s_waitcnt lgkmcnt(6)\n\t" \
  "v_mfma_f32_16x16x32_bf16 v[0:3], v[208:211], v[146:149], v[0:3]\n\t" \
  "ds_read_b32 v221, v221 offset:0\n\t" \
  "v_and_b32_sdwa v225, s56, v224 dst_sel:DWORD dst_unused:UNUSED_PAD src0_sel:DWORD src1_sel:WORD_1\n\t" \
  "v_and_b32 v224, s56, v224\n\t" \
  "v_mfma_f32_16x16x32_bf16 v[24:27], v[208:211], v[154:157], 0\n\t" \
  "ds_read_b32 v220, v220 offset:0\n\t" \
  "v_and_b32_sdwa v227, s56, v226 dst_sel:DWORD dst_unused:UNUSED_PAD src0_sel:DWORD src1_sel:WORD_1\n\t" \
  "v_mfma_f32_16x16x32_bf16 v[48:51], v[208:211], v[162:165], 0\n\t" \
  "ds_read_b32 v225, v225 offset:0\n\t" \
  "v_and_b32 v226, s56, v226\n\t" \
  "v_and_b32_sdwa v229, s56, v228 dst_sel:DWORD dst_unused:UNUSED_PAD src0_sel:DWORD src1_sel:WORD_1\n\t" \
  "v_mfma_f32_16x16x32_bf16 v[72:75], v[208:211], v[170:173], 0\n\t" \
  "ds_read_b64_tr_b16 v[208:209], v249 offset:24664\n\t" \
  "v_and_b32 v228, s56, v228\n\t" \
  "s_waitcnt lgkmcnt(6)\n\t" \
  "v_mfma_f32_16x16x32_bf16 v[4:7], v[212:215], v[146:149], v[4:7]\n\t" \
  "ds_read_b64_tr_b16 v[210:211], v249 offset:26200\n\t" \
  "v_pk_fma_f16 v235, v235, v253, s54\n\t" \
  "v_mfma_f32_16x16x32_bf16 v[28:31], v[212:215], v[154:157], 0\n\t" \
  "ds_read_b32 v224, v224 offset:0\n\t" \
  "v_pk_mul_f16 v235, v234, v235\n\t" \
  "s_waitcnt lgkmcnt(7)\n\t" \
  "v_fma_f32 v179, |v179|, v217, v179 mul:4\n\t" \
  "v_fma_f32 v178, |v178|, v216, v178 mul:4\n\t" \
  "v_mfma_f32_16x16x32_bf16 v[52:55], v[212:215], v[162:165], 0\n\t" \
  "ds_read_b32 v227, v227 offset:0\n\t" \
  "v_fma_f32 v187, |v187|, v219, v187 mul:4\n\t" \
  "s_waitcnt lgkmcnt(5)\n\t" \
  "v_fma_f32 v186, |v186|, v218, v186 mul:4\n\t" \
  "v_fma_f32 v185, |v185|, v221, v185 mul:4\n\t" \
  "v_mfma_f32_16x16x32_bf16 v[76:79], v[212:215], v[170:173], 0\n\t" \
  "ds_read_b64_tr_b16 v[212:213], v249 offset:24672\n\t" \
  "v_fma_f32 v184, |v184|, v220, v184 mul:4\n\t" \
  "v_pk_fma_f16 v232, v232, s55, s55\n\t" \
  "s_waitcnt lgkmcnt(3)\n\t" \
  "v_mfma_f32_16x16x32_bf16 v[8:11], v[208:211], v[146:149], 0\n\t" \
  "ds_read_b64_tr_b16 v[214:215], v249 offset:26208\n\t" \
  "v_cvt_pk_bf16_f32 v179, v178, v179\n\t" \
  "v_cvt_pk_bf16_f32 v187, v186, v187\n\t" \
  "v_cvt_pk_bf16_f32 v186, v184, v185\n\t" \
  "v_mfma_f32_16x16x32_bf16 v[32:35], v[208:211], v[154:157], 0\n\t" \
  "ds_read_b32 v226, v226 offset:0\n\t" \
  "v_rcp_f16 v233, v232\n\t" \
  "v_exp_f16 v234, v235\n\t" \
  "v_cvt_pk_f16_f32 v216, |v180|, |v181| clamp\n\t" \
  "v_mfma_f32_16x16x32_bf16 v[56:59], v[208:211], v[162:165], 0\n\t" \
  "s_setprio 1\n\t" \
  "ds_read_b32 v229, v229 offset:0\n\t" \
  "v_cvt_pk_f16_f32 v218, |v182|, |v183| clamp\n\t" \
  "v_cvt_pk_f16_f32 v220, |v188|, |v189| clamp\n\t" \
  "v_mfma_f32_16x16x32_bf16 v[80:83], v[208:211], v[170:173], 0\n\t" \
  "ds_read_b64_tr_b16 v[208:209], v249 offset:24728\n\t" \
  "v_cvt_pk_f16_f32 v222, |v190|, |v191| clamp\n\t" \
  "v_rcp_f16_sdwa v232, v232 dst_sel:DWORD dst_unused:UNUSED_PAD src0_sel:WORD_1\n\t" \
  "v_exp_f16_sdwa v234, v235 dst_sel:WORD_1 dst_unused:UNUSED_PRESERVE src0_sel:WORD_1\n\t" \
  "s_waitcnt lgkmcnt(3)\n\t" \
  "v_mfma_f32_16x16x32_bf16 v[12:15], v[212:215], v[146:149], 0\n\t" \
  "ds_read_b64_tr_b16 v[210:211], v249 offset:26264\n\t" \
  "v_and_b32_sdwa v217, s56, v216 dst_sel:DWORD dst_unused:UNUSED_PAD src0_sel:DWORD src1_sel:WORD_1\n\t" \
  "v_and_b32 v216, s56, v216\n\t" \
  "v_mfma_f32_16x16x32_bf16 v[36:39], v[212:215], v[154:157], 0\n\t" \
  "ds_read_b32 v228, v228 offset:0\n\t" \
  "v_and_b32_sdwa v219, s56, v218 dst_sel:DWORD dst_unused:UNUSED_PAD src0_sel:DWORD src1_sel:WORD_1\n\t" \
  "v_mfma_f32_16x16x32_bf16 v[60:63], v[212:215], v[162:165], 0\n\t" \
  "ds_read_b32 v217, v217 offset:0\n\t" \
  "v_and_b32 v218, s56, v218\n\t" \
  "v_and_b32_sdwa v221, s56, v220 dst_sel:DWORD dst_unused:UNUSED_PAD src0_sel:DWORD src1_sel:WORD_1\n\t" \
  "v_mfma_f32_16x16x32_bf16 v[84:87], v[212:215], v[170:173], 0\n\t" \
  "ds_read_b64_tr_b16 v[212:213], v249 offset:24736\n\t" \
  "v_and_b32 v220, s56, v220\n\t" \
  "s_waitcnt lgkmcnt(3)\n\t" \
  "v_mfma_f32_16x16x32_bf16 v[16:19], v[208:211], v[146:149], 0\n\t" \
  "ds_read_b64_tr_b16 v[214:215], v249 offset:26272\n\t" \
  "v_and_b32_sdwa v223, s56, v222 dst_sel:DWORD dst_unused:UNUSED_PAD src0_sel:DWORD src1_sel:WORD_1\n\t" \
  "v_mfma_f32_16x16x32_bf16 v[40:43], v[208:211], v[154:157], 0\n\t" \
  "ds_read_b32 v216, v216 offset:0\n\t" \
  "v_and_b32 v222, s56, v222\n\t" \
  "v_fma_f32 v195, |v195|, v225, v195 mul:4\n\t" \
  "v_mfma_f32_16x16x32_bf16 v[64:67], v[208:211], v[162:165], 0\n\t" \
  "ds_read_b32 v219, v219 offset:0\n\t" \
  "v_fma_f32 v194, |v194|, v224, v194 mul:4\n\t" \
  "v_fma_f32 v203, |v203|, v227, v203 mul:4\n\t" \
  "v_mfma_f32_16x16x32_bf16 v[88:91], v[208:211], v[170:173], 0\n\t" \
  "ds_read_b64_tr_b16 v[208:209], v248 offset:36904\n\t" \
  "ds_read_b64_tr_b16 v[210:211], v248 offset:37928\n\t" \
  "s_waitcnt lgkmcnt(7)\n\t" \
  "v_fma_f32 v202, |v202|, v226, v202 mul:4\n\t" \
  "v_fma_f32 v201, |v201|, v229, v201 mul:4\n\t" \
  "s_waitcnt lgkmcnt(4)\n\t" \
  "v_mfma_f32_16x16x32_bf16 v[20:23], v[212:215], v[146:149], 0\n\t" \
  "ds_read_b32 v218, v218 offset:0\n\t" \
  "v_fma_f32 v200, |v200|, v228, v200 mul:4\n\t" \
  "v_fma_mix_f32 v176, v176, v233, 0 op_sel_hi:[0,1,0]\n\t" \
  "v_fma_mix_f32 v177, v177, v232, 0 op_sel_hi:[0,1,0]\n\t" \
  "v_mfma_f32_16x16x32_bf16 v[44:47], v[212:215], v[154:157], 0\n\t" \
  "ds_read_b32 v221, v221 offset:0\n\t" \
  "v_pk_fma_f16 v234, v234, s55, s55\n\t" \
  "v_cvt_pk_bf16_f32 v178, v176, v177\n\t" \
  "v_cvt_pk_bf16_f32 v195, v194, v195\n\t" \
  "v_mfma_f32_16x16x32_bf16 v[68:71], v[212:215], v[162:165], 0\n\t" \
  "ds_read_b32 v220, v220 offset:0\n\t" \
  "v_cvt_pk_bf16_f32 v203, v202, v203\n\t" \
  "v_cvt_pk_bf16_f32 v202, v200, v201\n\t" \
  "v_rcp_f16 v235, v234\n\t" \
  "v_mfma_f32_16x16x32_bf16 v[92:95], v[212:215], v[170:173], 0\n\t" \
  "ds_read_b64_tr_b16 v[212:213], v248 offset:39976\n\t" \
  "s_setprio 2\n\t" \
  "v_cvt_pk_f16_f32 v224, |v196|, |v197| clamp\n\t" \
  "v_cvt_pk_f16_f32 v226, |v198|, |v199| clamp\n\t" \
  "s_waitcnt lgkmcnt(4)\n\t" \
  "v_mfma_f32_16x16x32_bf16 v[144:147], v[208:211], v[96:99], 0\n\t" \
  "ds_read_b64_tr_b16 v[214:215], v248 offset:41000\n\t" \
  "v_cvt_pk_f16_f32 v228, |v204|, |v205| clamp\n\t" \
  "v_cvt_pk_f16_f32 v230, |v206|, |v207| clamp\n\t" \
  "v_rcp_f16_sdwa v234, v234 dst_sel:DWORD dst_unused:UNUSED_PAD src0_sel:WORD_1\n\t" \
  "v_mfma_f32_16x16x32_bf16 v[152:155], v[208:211], v[108:111], 0\n\t" \
  "ds_read_b32 v223, v223 offset:0\n\t" \
  "v_and_b32_sdwa v225, s56, v224 dst_sel:DWORD dst_unused:UNUSED_PAD src0_sel:DWORD src1_sel:WORD_1\n\t" \
  "v_and_b32 v224, s56, v224\n\t" \
  "v_mfma_f32_16x16x32_bf16 v[160:163], v[208:211], v[120:123], 0\n\t" \
  "ds_read_b32 v222, v222 offset:0\n\t" \
  "v_and_b32_sdwa v227, s56, v226 dst_sel:DWORD dst_unused:UNUSED_PAD src0_sel:DWORD src1_sel:WORD_1\n\t" \
  "v_mfma_f32_16x16x32_bf16 v[168:171], v[208:211], v[132:135], 0\n\t" \
  "ds_read_b64_tr_b16 v[208:209], v248 offset:38952\n\t" \
  "v_and_b32 v226, s56, v226\n\t" \
  "v_and_b32_sdwa v229, s56, v228 dst_sel:DWORD dst_unused:UNUSED_PAD src0_sel:DWORD src1_sel:WORD_1\n\t" \
  "s_waitcnt lgkmcnt(3)\n\t" \
  "v_mfma_f32_16x16x32_bf16 v[144:147], v[212:215], v[100:103], v[144:147]\n\t" \
  "ds_read_b64_tr_b16 v[210:211], v248 offset:42024\n\t" \
  "v_and_b32 v228, s56, v228\n\t" \
  "v_mfma_f32_16x16x32_bf16 v[152:155], v[212:215], v[112:115], v[152:155]\n\t" \
  "ds_read_b32 v225, v225 offset:0\n\t" \
  "v_and_b32_sdwa v231, s56, v230 dst_sel:DWORD dst_unused:UNUSED_PAD src0_sel:DWORD src1_sel:WORD_1\n\t" \
  "v_mfma_f32_16x16x32_bf16 v[160:163], v[212:215], v[124:127], v[160:163]\n\t" \
  "ds_read_b32 v224, v224 offset:0\n\t" \
  "v_and_b32 v230, s56, v230\n\t" \
  "v_fma_f32 v181, |v181|, v217, v181 mul:4\n\t" \
  "v_mfma_f32_16x16x32_bf16 v[168:171], v[212:215], v[136:139], v[168:171]\n\t" \
  "ds_read_b64_tr_b16 v[212:213], v248 offset:37416\n\t" \
  "ds_read_b64_tr_b16 v[214:215], v248 offset:38440\n\t" \
  "v_fma_f32 v180, |v180|, v216, v180 mul:4\n\t" \
  "v_fma_f32 v183, |v183|, v219, v183 mul:4\n\t" \
  "s_waitcnt lgkmcnt(4)\n\t" \
  "v_mfma_f32_16x16x32_bf16 v[144:147], v[208:211], v[104:107], v[144:147]\n\t" \
  "ds_read_b32 v227, v227 offset:0\n\t" \
  "ds_read_b32 v226, v226 offset:0\n\t" \
  "v_fma_f32 v182, |v182|, v218, v182 mul:4\n\t" \
  "v_fma_f32 v189, |v189|, v221, v189 mul:4\n\t" \
  "v_mfma_f32_16x16x32_bf16 v[152:155], v[208:211], v[116:119], v[152:155]\n\t" \
  "ds_read_b32 v229, v229 offset:0\n\t" \
  "v_fma_f32 v188, |v188|, v220, v188 mul:4\n\t" \
  "v_fma_f32 v191, |v191|, v223, v191 mul:4\n\t" \
  "v_fma_f32 v190, |v190|, v222, v190 mul:4\n\t" \
  "v_mfma_f32_16x16x32_bf16 v[160:163], v[208:211], v[128:131], v[160:163]\n\t" \
  "ds_read_b32 v228, v228 offset:0\n\t" \
  "v_fma_mix_f32 v192, v192, v235, 0 op_sel_hi:[0,1,0]\n\t" \
  "v_fma_mix_f32 v193, v193, v234, 0 op_sel_hi:[0,1,0]\n\t" \
  "v_cvt_pk_bf16_f32 v194, v192, v193\n\t" \
  "v_mfma_f32_16x16x32_bf16 v[168:171], v[208:211], v[140:143], v[168:171]\n\t" \
  "ds_read_b64_tr_b16 v[208:209], v248 offset:40488\n\t" \
  "v_cvt_pk_bf16_f32 v180, v180, v181\n\t" \
  "v_cvt_pk_bf16_f32 v181, v182, v183\n\t" \
  "s_waitcnt lgkmcnt(5)\n\t" \
  "v_mfma_f32_16x16x32_bf16 v[148:151], v[212:215], v[96:99], 0\n\t" \
  "ds_read_b64_tr_b16 v[210:211], v248 offset:41512\n\t" \
  "v_cvt_pk_bf16_f32 v188, v188, v189\n\t" \
  "s_setprio 3\n\t" \
  "v_cvt_pk_bf16_f32 v189, v190, v191\n\t" \
  "v_cvt_pk_f16_f32 v216, |v146|, |v147| clamp\n\t" \
  "v_mfma_f32_16x16x32_bf16 v[156:159], v[212:215], v[108:111], 0\n\t" \
  "ds_read_b32 v231, v231 offset:0\n\t" \
  "v_cvt_pk_f16_f32 v232, v144, v145\n\t" \
  "v_cvt_pk_f16_f32 v218, |v154|, |v155| clamp\n\t" \
  "v_cvt_pk_f16_f32 v220, |v152|, |v153| clamp\n\t" \
  "v_mfma_f32_16x16x32_bf16 v[164:167], v[212:215], v[120:123], 0\n\t" \
  "ds_read_b32 v230, v230 offset:0\n\t" \
  "v_pk_mul_f16 v233, v232, v232\n\t" \
  "v_and_b32_sdwa v217, s56, v216 dst_sel:DWORD dst_unused:UNUSED_PAD src0_sel:DWORD src1_sel:WORD_1\n\t" \
  "v_mfma_f32_16x16x32_bf16 v[172:175], v[212:215], v[132:135], 0\n\t" \
  "ds_read_b64_tr_b16 v[212:213], v248 offset:39464\n\t" \
  "v_and_b32 v216, s56, v216\n\t" \
  "v_and_b32_sdwa v219, s56, v218 dst_sel:DWORD dst_unused:UNUSED_PAD src0_sel:DWORD src1_sel:WORD_1\n\t" \
  "s_waitcnt lgkmcnt(3)\n\t" \
  "v_mfma_f32_16x16x32_bf16 v[148:151], v[208:211], v[100:103], v[148:151]\n\t" \
  "ds_read_b64_tr_b16 v[214:215], v248 offset:42536\n\t" \
  "v_and_b32 v218, s56, v218\n\t" \
  "v_mfma_f32_16x16x32_bf16 v[156:159], v[208:211], v[112:115], v[156:159]\n\t" \
  "s_waitcnt vmcnt(1)\n\t" \
  "s_cmp_lg_u32 s58, 1\n\t" \
  "s_cbranch_scc1 .Lcnx_c14\n\t" \
  "s_waitcnt vmcnt(0)\n\t" \
  ".Lcnx_c14:\n\t" \
  "s_mov_b64 exec, 3\n\t" \
  "ds_add_rtn_u32 v246, v251, v250 offset:47672\n\t" \
  "s_mov_b64 exec, -1\n\t" \
  "ds_read_b32 v217, v217 offset:0\n\t" \
  "v_and_b32_sdwa v221, s56, v220 dst_sel:DWORD dst_unused:UNUSED_PAD src0_sel:DWORD src1_sel:WORD_1\n\t" \
  "v_mfma_f32_16x16x32_bf16 v[164:167], v[208:211], v[124:127], v[164:167]\n\t" \
  "ds_read_b32 v216, v216 offset:0\n\t" \
  "v_and_b32 v220, s56, v220\n\t" \
  "v_pk_fma_f16 v233, v233, v253, s54\n\t" \
  "v_mfma_f32_16x16x32_bf16 v[172:175], v[208:211], v[136:139], v[172:175]\n\t" \
  "s_waitcnt lgkmcnt(2)\n\t" \
  "v_readfirstlane_b32 s4, v246\n\t" \
  "s_cmp_lt_u32 s4, s57\n\t" \
  "s_cbranch_scc1 .Lcnx_w24\n.Lcnx_b24:\n\t" \
  "ds_read_b64_tr_b16 v[208:209], v249 offset:30752\n\t" \
  "v_pk_mul_f16 v233, v232, v233\n\t" \
  "v_fma_f32 v197, |v197|, v225, v197 mul:4\n\t" \
  "v_mfma_f32_16x16x32_bf16 v[148:151], v[212:215], v[104:107], v[148:151]\n\t" \
  "ds_read_b64_tr_b16 v[210:211], v249 offset:32288\n\t" \
  "v_fma_f32 v196, |v196|, v224, v196 mul:4\n\t" \
  "v_fma_f32 v199, |v199|, v227, v199 mul:4\n\t" \
  "v_mfma_f32_16x16x32_bf16 v[156:159], v[212:215], v[116:119], v[156:159]\n\t" \
  "s_cmp_eq_u32 s37, 0\n\t" \
  "s_cbranch_scc1 .Lcnx_f3\n.Lcnx_g3:\n\t" \
  "ds_read_b32 v219, v219 offset:0\n\t" \
  "v_fma_f32 v198, |v198|, v226, v198 mul:4\n\t" \
  "v_fma_f32 v205, |v205|, v229, v205 mul:4\n\t" \
  "v_fma_f32 v204, |v204|, v228, v204 mul:4\n\t" \
  "v_mfma_f32_16x16x32_bf16 v[164:167], v[212:215], v[128:131], v[164:167]\n\t" \
  "ds_read_b32 v218, v218 offset:0\n\t" \
  "v_fma_f32 v207, |v207|, v231, v207 mul:4\n\t" \
  "v_fma_f32 v206, |v206|, v230, v206 mul:4\n\t" \
  "v_cvt_pk_bf16_f32 v196, v196, v197\n\t" \
  "v_mfma_f32_16x16x32_bf16 v[172:175], v[212:215], v[140:143], v[172:175]\n\t" \
  "ds_read_b64_tr_b16 v[212:213], v249 offset:30760\n\t" \
  "v_cvt_pk_bf16_f32 v197, v198, v199\n\t" \
  "v_cvt_pk_bf16_f32 v204, v204, v205\n\t" \
  "v_cvt_pk_bf16_f32 v205, v206, v207\n\t" \
  "s_setprio 0\n\t" \
  "v_exp_f16 v232, v233\n\t" \
  "v_cvt_pk_f16_f32 v224, |v162|, |v163| clamp\n\t" \
  "v_mfma_f32_16x16x32_bf16 v[8:11], v[242:245], v[236:239], v[8:11]\n\t" \
  "ds_read_b64_tr_b16 v[214:215], v249 offset:32296\n\t" \
  "v_cvt_pk_f16_f32 v234, v160, v161\n\t" \
  "v_cvt_pk_f16_f32 v226, |v170|, |v171| clamp\n\t" \
  "v_mfma_f32_16x16x32_bf16 v[12:15], v[240:243], v[236:239], v[12:15]\n\t" \
  "buffer_load_dwordx4 v[236:239], v250, s[8:11], s28 offen offset:128\n\t" \
  "s_cmp_lg_u32 s58, 0\n\t" \
  "s_cbranch_scc1 .Lcnx_c27\n\t" \
  "s_add_u32 m0, s25, 8200\n\t" \
  "s_add_u32 s24, s24, s26\n\t" \
  "buffer_load_dwordx4 v254, s[8:11], s24 offen lds\n\t" \
  "s_cmp_lg_u32 s38, 7\n\t" \
  "s_cbranch_scc1 .Lcnx_c27\n\t" \
  "s_mov_b32 m0, 163632\n\t" \
  "s_add_u32 s30, s30, s31\n\t" \
  "buffer_load_dword v252, s[8:11], s30 offen lds\n\t" \
  ".Lcnx_c27:\n\t" \
  "ds_read_b32 v221, v221 offset:0\n\t" \
  "v_cvt_pk_f16_f32 v228, |v168|, |v169| clamp\n\t" \
  "v_exp_f16_sdwa v232, v233 dst_sel:WORD_1 dst_unused:UNUSED_PRESERVE src0_sel:WORD_1\n\t" \
  "v_pk_mul_f16 v235, v234, v234\n\t" \
  "s_waitcnt lgkmcnt(5)\n\t" \
  "v_mfma_f32_16x16x32_bf16 v[0:3], v[208:211], v[178:181], v[0:3]\n\t" \
  "ds_read_b32 v220, v220 offset:0\n\t" \
  "v_and_b32_sdwa v225, s56, v224 dst_sel:DWORD dst_unused:UNUSED_PAD src0_sel:DWORD src1_sel:WORD_1\n\t" \
  "v_and_b32 v224, s56, v224\n\t" \
  "v_mfma_f32_16x16x32_bf16 v[24:27], v[208:211], v[186:189], v[24:27]\n\t" \
  "ds_read_b32 v225, v225 offset:0\n\t" \
  "v_and_b32_sdwa v227, s56, v226 dst_sel:DWORD dst_unused:UNUSED_PAD src0_sel:DWORD src1_sel:WORD_1\n\t" \
  "v_mfma_f32_16x16x32_bf16 v[48:51], v[208:211], v[194:197], v[48:51]\n\t" \
  "ds_read_b32 v224, v224 offset:0\n\t" \
  "v_and_b32 v226, s56, v226\n\t" \
  "v_and_b32_sdwa v229, s56, v228 dst_sel:DWORD dst_unused:UNUSED_PAD src0_sel:DWORD src1_sel:WORD_1\n\t" \
  "v_mfma_f32_16x16x32_bf16 v[72:75], v[208:211], v[202:205], v[72:75]\n\t" \
  "ds_read_b64_tr_b16 v[208:209], v249 offset:30816\n\t" \
  "v_and_b32 v228, s56, v228\n\t" \
  "s_waitcnt lgkmcnt(5)\n\t" \
  "v_mfma_f32_16x16x32_bf16 v[4:7], v[212:215], v[178:181], v[4:7]\n\t" \
  "ds_read_b64_tr_b16 v[210:211], v249 offset:32352\n\t" \
  "v_pk_fma_f16 v235, v235, v253, s54\n\t" \
  "v_mfma_f32_16x16x32_bf16 v[28:31], v[212:215], v[186:189], v[28:31]\n\t" \
  "ds_read_b32 v227, v227 offset:0\n\t" \
  "v_pk_mul_f16 v235, v234, v235\n\t" \
  "v_fma_f32 v147, |v147|, v217, v147 mul:4\n\t" \
  "v_fma_f32 v146, |v146|, v216, v146 mul:4\n\t" \
  "v_mfma_f32_16x16x32_bf16 v[52:55], v[212:215], v[194:197], v[52:55]\n\t" \
  "ds_read_b32 v226, v226 offset:0\n\t" \
  "v_fma_f32 v155, |v155|, v219, v155 mul:4\n\t" \
  "s_waitcnt lgkmcnt(6)\n\t" \
  "v_fma_f32 v154, |v154|, v218, v154 mul:4\n\t" \
  "v_fma_f32 v153, |v153|, v221, v153 mul:4\n\t" \
  "v_mfma_f32_16x16x32_bf16 v[76:79], v[212:215], v[202:205], v[76:79]\n\t" \
  "ds_read_b64_tr_b16 v[212:213], v249 offset:30824\n\t" \
  "v_fma_f32 v152, |v152|, v220, v152 mul:4\n\t" \
  "v_pk_fma_f16 v232, v232, s55, s55\n\t" \
  "s_waitcnt lgkmcnt(3)\n\t" \
  "v_mfma_f32_16x16x32_bf16 v[8:11], v[208:211], v[178:181], v[8:11]\n\t" \
  "ds_read_b64_tr_b16 v[214:215], v249 offset:32360\n\t" \
  "v_cvt_pk_bf16_f32 v147, v146, v147\n\t" \
  "v_cvt_pk_bf16_f32 v155, v154, v155\n\t" \
  "v_cvt_pk_bf16_f32 v154, v152, v153\n\t" \
  "v_mfma_f32_16x16x32_bf16 v[32:35], v[208:211], v[186:189], v[32:35]\n\t" \
  "ds_read_b32 v229, v229 offset:0\n\t" \
  "v_rcp_f16 v233, v232\n\t" \
  "v_exp_f16 v234, v235\n\t" \
  "v_cvt_pk_f16_f32 v216, |v148|, |v149| clamp\n\t" \
  "v_mfma_f32_16x16x32_bf16 v[56:59], v[208:211], v[194:197], v[56:59]\n\t" \
  "ds_read_b32 v228, v228 offset:0\n\t" \
  "s_setprio 1\n\t" \
  "v_cvt_pk_f16_f32 v218, |v150|, |v151| clamp\n\t" \
  "v_cvt_pk_f16_f32 v220, |v156|, |v157| clamp\n\t" \
  "v_mfma_f32_16x16x32_bf16 v[80:83], v[208:211], v[202:205], v[80:83]\n\t" \
  "ds_read_b64_tr_b16 v[208:209], v249 offset:30880\n\t" \
  "v_cvt_pk_f16_f32 v222, |v158|, |v159| clamp\n\t" \
  "v_rcp_f16_sdwa v232, v232 dst_sel:DWORD dst_unused:UNUSED_PAD src0_sel:WORD_1\n\t" \
  "v_exp_f16_sdwa v234, v235 dst_sel:WORD_1 dst_unused:UNUSED_PRESERVE src0_sel:WORD_1\n\t" \
  "s_waitcnt lgkmcnt(3)\n\t" \
  "v_mfma_f32_16x16x32_bf16 v[12:15], v[212:215], v[178:181], v[12:15]\n\t" \
  "ds_read_b64_tr_b16 v[210:211], v249 offset:32416\n\t" \
  "v_and_b32_sdwa v217, s56, v216 dst_sel:DWORD dst_unused:UNUSED_PAD src0_sel:DWORD src1_sel:WORD_1\n\t" \
  "v_and_b32 v216, s56, v216\n\t" \
  "v_mfma_f32_16x16x32_bf16 v[36:39], v[212:215], v[186:189], v[36:39]\n\t" \
  "ds_read_b32 v217, v217 offset:0\n\t" \
  "v_and_b32_sdwa v219, s56, v218 dst_sel:DWORD dst_unused:UNUSED_PAD src0_sel:DWORD src1_sel:WORD_1\n\t" \
  "v_mfma_f32_16x16x32_bf16 v[60:63], v[212:215], v[194:197], v[60:63]\n\t" \
  "ds_read_b32 v216, v216 offset:0\n\t" \
  "v_and_b32 v218, s56, v218\n\t" \
  "v_and_b32_sdwa v221, s56, v220 dst_sel:DWORD dst_unused:UNUSED_PAD src0_sel:DWORD src1_sel:WORD_1\n\t" \
  "v_mfma_f32_16x16x32_bf16 v[84:87], v[212:215], v[202:205], v[84:87]\n\t" \
  "ds_read_b64_tr_b16 v[212:213], v249 offset:30888\n\t" \
  "v_and_b32 v220, s56, v220\n\t" \
  "s_waitcnt lgkmcnt(3)\n\t" \
  "v_mfma_f32_16x16x32_bf16 v[16:19], v[208:211], v[178:181], v[16:19]\n\t" \
  "ds_read_b64_tr_b16 v[214:215], v249 offset:32424\n\t" \
  "v_and_b32_sdwa v223, s56, v222 dst_sel:DWORD dst_unused:UNUSED_PAD src0_sel:DWORD src1_sel:WORD_1\n\t" \
  "v_mfma_f32_16x16x32_bf16 v[40:43], v[208:211], v[186:189], v[40:43]\n\t" \
  "ds_read_b32 v219, v219 offset:0\n\t" \
  "v_and_b32 v222, s56, v222\n\t" \
  "v_fma_f32 v163, |v163|, v225, v163 mul:4\n\t" \
  "v_mfma_f32_16x16x32_bf16 v[64:67], v[208:211], v[194:197], v[64:67]\n\t" \
  "ds_read_b32 v218, v218 offset:0\n\t" \
  "v_fma_f32 v162, |v162|, v224, v162 mul:4\n\t" \
  "v_fma_f32 v171, |v171|, v227, v171 mul:4\n\t" \
  "v_mfma_f32_16x16x32_bf16 v[88:91], v[208:211], v[202:205], v[88:91]\n\t" \
  "ds_read_b64_tr_b16 v[208:209], v248 offset:12304\n\t" \
  "ds_read_b64_tr_b16 v[210:211], v248 offset:13328\n\t" \
  "v_fma_f32 v170, |v170|, v226, v170 mul:4\n\t" \
  "v_fma_f32 v169, |v169|, v229, v169 mul:4\n\t" \
  "s_waitcnt lgkmcnt(4)\n\t" \
  "v_mfma_f32_16x16x32_bf16 v[20:23], v[212:215], v[178:181], v[20:23]\n\t" \
  "ds_read_b32 v221, v221 offset:0\n\t" \
  "v_fma_f32 v168, |v168|, v228, v168 mul:4\n\t" \
  "v_fma_mix_f32 v144, v144, v233, 0 op_sel_hi:[0,1,0]\n\t" \
  "v_fma_mix_f32 v145, v145, v232, 0 op_sel_hi:[0,1,0]\n\t" \
  "v_mfma_f32_16x16x32_bf16 v[44:47], v[212:215], v[186:189], v[44:47]\n\t" \
  "ds_read_b32 v220, v220 offset:0\n\t" \
  "v_pk_fma_f16 v234, v234, s55, s55\n\t" \
  "v_cvt_pk_bf16_f32 v146, v144, v145\n\t" \
  "v_cvt_pk_bf16_f32 v163, v162, v163\n\t" \
  "v_mfma_f32_16x16x32_bf16 v[68:71], v[212:215], v[194:197], v[68:71]\n\t" \
  "ds_read_b32 v223, v223 offset:0\n\t" \
  "v_cvt_pk_bf16_f32 v171, v170, v171\n\t" \
  "v_cvt_pk_bf16_f32 v170, v168, v169\n\t" \
  "v_rcp_f16 v235, v234\n\t" \
  "v_mfma_f32_16x16x32_bf16 v[92:95], v[212:215], v[202:205], v[92:95]\n\t" \
  "ds_read_b64_tr_b16 v[212:213], v248 offset:15376\n\t" \
  "v_cvt_pk_f16_f32 v224, |v164|, |v165| clamp\n\t" \
  "s_setprio 2\n\t" \
  "v_cvt_pk_f16_f32 v226, |v166|, |v167| clamp\n\t" \
  "s_waitcnt lgkmcnt(4)\n\t" \
  "v_mfma_f32_16x16x32_bf16 v[176:179], v[208:211], v[96:99], 0\n\t" \
  "ds_read_b64_tr_b16 v[214:215], v248 offset:16400\n\t" \
  "v_cvt_pk_f16_f32 v228, |v172|, |v173| clamp\n\t" \
  "v_cvt_pk_f16_f32 v230, |v174|, |v175| clamp\n\t" \
  "v_rcp_f16_sdwa v234, v234 dst_sel:DWORD dst_unused:UNUSED_PAD src0_sel:WORD_1\n\t" \
  "v_mfma_f32_16x16x32_bf16 v[184:187], v[208:211], v[108:111], 0\n\t" \
  "ds_read_b32 v222, v222 offset:0\n\t" \
  "v_and_b32_sdwa v225, s56, v224 dst_sel:DWORD dst_unused:UNUSED_PAD src0_sel:DWORD src1_sel:WORD_1\n\t" \
  "v_and_b32 v224, s56, v224\n\t" \
  "v_mfma_f32_16x16x32_bf16 v[192:195], v[208:211], v[120:123], 0\n\t" \
  "ds_read_b32 v225, v225 offset:0\n\t" \
  "v_and_b32_sdwa v227, s56, v226 dst_sel:DWORD dst_unused:UNUSED_PAD src0_sel:DWORD src1_sel:WORD_1\n\t" \
  "v_mfma_f32_16x16x32_bf16 v[200:203], v[208:211], v[132:135], 0\n\t" \
  "ds_read_b64_tr_b16 v[208:209], v248 offset:14352\n\t" \
  "v_and_b32 v226, s56, v226\n\t" \
  "v_and_b32_sdwa v229, s56, v228 dst_sel:DWORD dst_unused:UNUSED_PAD src0_sel:DWORD src1_sel:WORD_1\n\t" \
  "s_waitcnt lgkmcnt(3)\n\t" \
  "v_mfma_f32_16x16x32_bf16 v[176:179], v[212:215], v[100:103], v[176:179]\n\t" \
  "ds_read_b64_tr_b16 v[210:211], v248 offset:17424\n\t" \
  "v_and_b32 v228, s56, v228\n\t" \
  "v_mfma_f32_16x16x32_bf16 v[184:187], v[212:215], v[112:115], v[184:187]\n\t" \
  "ds_read_b32 v224, v224 offset:0\n\t" \
  "v_and_b32_sdwa v231, s56, v230 dst_sel:DWORD dst_unused:UNUSED_PAD src0_sel:DWORD src1_sel:WORD_1\n\t" \
  "v_mfma_f32_16x16x32_bf16 v[192:195], v[212:215], v[124:127], v[192:195]\n\t" \
  "ds_read_b32 v227, v227 offset:0\n\t" \
  "v_and_b32 v230, s56, v230\n\t" \
  "v_fma_f32 v149, |v149|, v217, v149 mul:4\n\t" \
  "v_mfma_f32_16x16x32_bf16 v[200:203], v[212:215], v[136:139], v[200:203]\n\t" \
  "ds_read_b64_tr_b16 v[212:213], v248 offset:12816\n\t" \
  "ds_read_b64_tr_b16 v[214:215], v248 offset:13840\n\t" \
  "v_fma_f32 v148, |v148|, v216, v148 mul:4\n\t" \
  "v_fma_f32 v151, |v151|, v219, v151 mul:4\n\t" \
  "s_waitcnt lgkmcnt(4)\n\t" \
  "v_mfma_f32_16x16x32_bf16 v[176:179], v[208:211], v[104:107], v[176:179]\n\t" \
  "ds_read_b32 v226, v226 offset:0\n\t" \
  "v_fma_f32 v150, |v150|, v218, v150 mul:4\n\t" \
  "v_fma_f32 v157, |v157|, v221, v157 mul:4\n\t" \
  "v_mfma_f32_16x16x32_bf16 v[184:187], v[208:211], v[116:119], v[184:187]\n\t" \
  "ds_read_b32 v229, v229 offset:0\n\t" \
  "v_fma_f32 v156, |v156|, v220, v156 mul:4\n\t" \
  "v_fma_f32 v159, |v159|, v223, v159 mul:4\n\t" \
  "v_fma_f32 v158, |v158|, v222, v158 mul:4\n\t" \
  "v_mfma_f32_16x16x32_bf16 v[192:195], v[208:211], v[128:131], v[192:195]\n\t" \
  "ds_read_b32 v228, v228 offset:0\n\t" \
  "v_fma_mix_f32 v160, v160, v235, 0 op_sel_hi:[0,1,0]\n\t" \
  "v_fma_mix_f32 v161, v161, v234, 0 op_sel_hi:[0,1,0]\n\t" \
  "v_cvt_pk_bf16_f32 v162, v160, v161\n\t" \
  "v_mfma_f32_16x16x32_bf16 v[200:203], v[208:211], v[140:143], v[200:203]\n\t" \
  "ds_read_b64_tr_b16 v[208:209], v248 offset:15888\n\t" \
  "v_cvt_pk_bf16_f32 v148, v148, v149\n\t" \
  "v_cvt_pk_bf16_f32 v149, v150, v151\n\t" \
  "s_waitcnt lgkmcnt(4)\n\t" \
  "v_mfma_f32_16x16x32_bf16 v[180:183], v[212:215], v[96:99], 0\n\t" \
  "ds_read_b64_tr_b16 v[210:211], v248 offset:16912\n\t" \
  "v_cvt_pk_bf16_f32 v156, v156, v157\n\t" \
  "v_cvt_pk_bf16_f32 v157, v158, v159\n\t" \
  "s_setprio 3\n\t" \
  "v_cvt_pk_f16_f32 v216, |v178|, |v179| clamp\n\t" \
  "v_mfma_f32_16x16x32_bf16 v[188:191], v[212:215], v[108:111], 0\n\t" \
  "ds_read_b32 v231, v231 offset:0\n\t" \
  "v_cvt_pk_f16_f32 v232, v176, v177\n\t" \
  "v_cvt_pk_f16_f32 v218, |v186|, |v187| clamp\n\t" \
  "v_cvt_pk_f16_f32 v220, |v184|, |v185| clamp\n\t" \
  "v_mfma_f32_16x16x32_bf16 v[196:199], v[212:215], v[120:123], 0\n\t" \
  "ds_read_b32 v230, v230 offset:0\n\t" \
  "v_pk_mul_f16 v233, v232, v232\n\t" \
  "v_and_b32_sdwa v217, s56, v216 dst_sel:DWORD dst_unused:UNUSED_PAD src0_sel:DWORD src1_sel:WORD_1\n\t" \
  "v_mfma_f32_16x16x32_bf16 v[204:207], v[212:215], v[132:135], 0\n\t" \
  "ds_read_b64_tr_b16 v[212:213], v248 offset:14864\n\t" \
  "v_and_b32 v216, s56, v216\n\t" \
  "v_and_b32_sdwa v219, s56, v218 dst_sel:DWORD dst_unused:UNUSED_PAD src0_sel:DWORD src1_sel:WORD_1\n\t" \
  "s_waitcnt lgkmcnt(3)\n\t" \
  "v_mfma_f32_16x16x32_bf16 v[180:183], v[208:211], v[100:103], v[180:183]\n\t" \
  "ds_read_b64_tr_b16 v[214:215], v248 offset:17936\n\t" \
  "v_and_b32 v218, s56, v218\n\t" \
  "v_mfma_f32_16x16x32_bf16 v[188:191], v[208:211], v[112:115], v[188:191]\n\t" \
  "s_waitcnt vmcnt(1)\n\t" \
  "s_cmp_lg_u32 s58, 1\n\t" \
  "s_cbranch_scc1 .Lcnx_c15\n\t" \
  "s_waitcnt vmcnt(0)\n\t" \
  ".Lcnx_c15:\n\t" \
  "s_mov_b64 exec, 3\n\t" \
  "ds_add_rtn_u32 v246, v251, v250 offset:47680\n\t" \
  "s_mov_b64 exec, -1\n\t" \
  "ds_read_b32 v217, v217 offset:0\n\t" \
  "v_and_b32_sdwa v221, s56, v220 dst_sel:DWORD dst_unused:UNUSED_PAD src0_sel:DWORD src1_sel:WORD_1\n\t" \
  "v_mfma_f32_16x16x32_bf16 v[196:199], v[208:211], v[124:127], v[196:199]\n\t" \
  "ds_read_b32 v216, v216 offset:0\n\t" \
  "v_and_b32 v220, s56, v220\n\t" \
  "v_pk_fma_f16 v233, v233, v253, s54\n\t" \
  "v_mfma_f32_16x16x32_bf16 v[204:207], v[208:211], v[136:139], v[204:207]\n\t" \
  "s_waitcnt lgkmcnt(2)\n\t" \
  "v_readfirstlane_b32 s4, v246\n\t" \
  "s_cmp_lt_u32 s4, s57\n\t" \
  "s_cbranch_scc1 .Lcnx_w36\n.Lcnx_b36:\n\t" \
  "ds_read_b64_tr_b16 v[208:209], v249 offset:0\n\t" \
  "v_pk_mul_f16 v233, v232, v233\n\t" \
  "v_fma_f32 v165, |v165|, v225, v165 mul:4\n\t" \
  "v_mfma_f32_16x16x32_bf16 v[180:183], v[212:215], v[104:107], v[180:183]\n\t" \
  "ds_read_b64_tr_b16 v[210:211], v249 offset:1536\n\t" \
  "v_fma_f32 v164, |v164|, v224, v164 mul:4\n\t" \
  "v_fma_f32 v167, |v167|, v227, v167 mul:4\n\t" \
  "v_mfma_f32_16x16x32_bf16 v[188:191], v[212:215], v[116:119], v[188:191]\n\t" \
  "s_cmp_eq_u32 s37, 0\n\t" \
  "s_cbranch_scc1 .Lcnx_f4\n.Lcnx_g4:\n\t" \
  "ds_read_b32 v219, v219 offset:0\n\t" \
  "v_fma_f32 v166, |v166|, v226, v166 mul:4\n\t" \
  "v_fma_f32 v173, |v173|, v229, v173 mul:4\n\t" \
  "v_fma_f32 v172, |v172|, v228, v172 mul:4\n\t" \
  "v_mfma_f32_16x16x32_bf16 v[196:199], v[212:215], v[128:131], v[196:199]\n\t" \
  "ds_read_b32 v218, v218 offset:0\n\t" \
  "v_fma_f32 v175, |v175|, v231, v175 mul:4\n\t" \
  "v_fma_f32 v174, |v174|, v230, v174 mul:4\n\t" \
  "v_cvt_pk_bf16_f32 v164, v164, v165\n\t" \
  "v_mfma_f32_16x16x32_bf16 v[204:207], v[212:215], v[140:143], v[204:207]\n\t" \
  "ds_read_b64_tr_b16 v[212:213], v249 offset:8\n\t" \
  "v_cvt_pk_bf16_f32 v165, v166, v167\n\t" \
  "v_cvt_pk_bf16_f32 v172, v172, v173\n\t" \
  "v_cvt_pk_bf16_f32 v173, v174, v175\n\t" \
  "s_setprio 0\n\t" \
  "v_exp_f16 v232, v233\n\t" \
  "v_cvt_pk_f16_f32 v224, |v194|, |v195| clamp\n\t" \
  "v_mfma_f32_16x16x32_bf16 v[16:19], v[242:245], v[236:239], v[16:19]\n\t" \
  "ds_read_b64_tr_b16 v[214:215], v249 offset:1544\n\t" \
  "v_cvt_pk_f16_f32 v234, v192, v193\n\t" \
  "v_cvt_pk_f16_f32 v226, |v202|, |v203| clamp\n\t" \
  "v_mfma_f32_16x16x32_bf16 v[20:23], v[240:243], v[236:239], v[20:23]\n\t" \
  "buffer_load_dwordx4 v[236:239], v250, s[8:11], s28 offen offset:192\n\t" \
  "s_cmp_lg_u32 s58, 0\n\t" \
  "s_cbranch_scc1 .Lcnx_c28\n\t" \
  "s_add_u32 m0, s25, 16400\n\t" \
  "s_add_u32 s24, s24, s26\n\t" \
  "buffer_load_dwordx4 v254, s[8:11], s24 offen lds\n\t" \
  "s_cmp_lg_u32 s38, 4\n\t" \
  "s_cbranch_scc1 .Lcnx_c28\n\t" \
  "s_mov_b32 m0, 163632\n\t" \
  "s_add_u32 s30, s30, s31\n\t" \
  "buffer_load_dword v252, s[8:11], s30 offen lds\n\t" \
  ".Lcnx_c28:\n\t" \
  "ds_read_b32 v221, v221 offset:0\n\t" \
  "v_cvt_pk_f16_f32 v228, |v200|, |v201| clamp\n\t" \
  "v_exp_f16_sdwa v232, v233 dst_sel:WORD_1 dst_unused:UNUSED_PRESERVE src0_sel:WORD_1\n\t" \
  "v_pk_mul_f16 v235, v234, v234\n\t" \
  "s_waitcnt lgkmcnt(5)\n\t" \
  "v_mfma_f32_16x16x32_bf16 v[0:3], v[208:211], v[146:149], v[0:3]\n\t" \
  "ds_read_b32 v220, v220 offset:0\n\t" \
  "v_and_b32_sdwa v225, s56, v224 dst_sel:DWORD dst_unused:UNUSED_PAD src0_sel:DWORD src1_sel:WORD_1\n\t" \
  "v_and_b32 v224, s56, v224\n\t" \
  "v_mfma_f32_16x16x32_bf16 v[24:27], v[208:211], v[154:157], v[24:27]\n\t" \
  "ds_read_b32 v225, v225 offset:0\n\t" \
  "v_and_b32_sdwa v227, s56, v226 dst_sel:DWORD dst_unused:UNUSED_PAD src0_sel:DWORD src1_sel:WORD_1\n\t" \
  "v_mfma_f32_16x16x32_bf16 v[48:51], v[208:211], v[162:165], v[48:51]\n\t" \
  "ds_read_b32 v224, v224 offset:0\n\t" \
  "v_and_b32 v226, s56, v226\n\t" \
  "v_and_b32_sdwa v229, s56, v228 dst_sel:DWORD dst_unused:UNUSED_PAD src0_sel:DWORD src1_sel:WORD_1\n\t" \
  "v_mfma_f32_16x16x32_bf16 v[72:75], v[208:211], v[170:173], v[72:75]\n\t" \
  "ds_read_b64_tr_b16 v[208:209], v249 offset:64\n\t" \
  "v_and_b32 v228, s56, v228\n\t" \
  "s_waitcnt lgkmcnt(5)\n\t" \
  "v_mfma_f32_16x16x32_bf16 v[4:7], v[212:215], v[146:149], v[4:7]\n\t" \
  "ds_read_b64_tr_b16 v[210:211], v249 offset:1600\n\t" \
  "v_pk_fma_f16 v235, v235, v253, s54\n\t" \
  "v_mfma_f32_16x16x32_bf16 v[28:31], v[212:215], v[154:157], v[28:31]\n\t" \
  "ds_read_b32 v227, v227 offset:0\n\t" \
  "v_pk_mul_f16 v235, v234, v235\n\t" \
  "v_fma_f32 v179, |v179|, v217, v179 mul:4\n\t" \
  "v_fma_f32 v178, |v178|, v216, v178 mul:4\n\t" \
  "v_mfma_f32_16x16x32_bf16 v[52:55], v[212:215], v[162:165], v[52:55]\n\t" \
  "ds_read_b32 v226, v226 offset:0\n\t" \
  "v_fma_f32 v187, |v187|, v219, v187 mul:4\n\t" \
  "s_waitcnt lgkmcnt(6)\n\t" \
  "v_fma_f32 v186, |v186|, v218, v186 mul:4\n\t" \
  "v_fma_f32 v185, |v185|, v221, v185 mul:4\n\t" \
  "v_mfma_f32_16x16x32_bf16 v[76:79], v[212:215], v[170:173], v[76:79]\n\t" \
  "ds_read_b64_tr_b16 v[212:213], v249 offset:72\n\t" \
  "v_fma_f32 v184, |v184|, v220, v184 mul:4\n\t" \
  "v_pk_fma_f16 v232, v232, s55, s55\n\t" \
  "s_waitcnt lgkmcnt(3)\n\t" \
  "v_mfma_f32_16x16x32_bf16 v[8:11], v[208:211], v[146:149], v[8:11]\n\t" \
  "ds_read_b64_tr_b16 v[214:215], v249 offset:1608\n\t" \
  "v_cvt_pk_bf16_f32 v179, v178, v179\n\t" \
  "v_cvt_pk_bf16_f32 v187, v186, v187\n\t" \
  "v_cvt_pk_bf16_f32 v186, v184, v185\n\t" \
  "v_mfma_f32_16x16x32_bf16 v[32:35], v[208:211], v[154:157], v[32:35]\n\t" \
  "ds_read_b32 v229, v229 offset:0\n\t" \
  "v_rcp_f16 v233, v232\n\t" \
  "v_exp_f16 v234, v235\n\t" \
  "v_cvt_pk_f16_f32 v216, |v180|, |v181| clamp\n\t" \
  "v_mfma_f32_16x16x32_bf16 v[56:59], v[208:211], v[162:165], v[56:59]\n\t" \
  "ds_read_b32 v228, v228 offset:0\n\t" \
  "s_setprio 1\n\t" \
  "v_cvt_pk_f16_f32 v218, |v182|, |v183| clamp\n\t" \
  "v_cvt_pk_f16_f32 v220, |v188|, |v189| clamp\n\t" \
  "v_mfma_f32_16x16x32_bf16 v[80:83], v[208:211], v[170:173], v[80:83]\n\t" \
  "ds_read_b64_tr_b16 v[208:209], v249 offset:128\n\t" \
  "v_cvt_pk_f16_f32 v222, |v190|, |v191| clamp\n\t" \
  "v_rcp_f16_sdwa v232, v232 dst_sel:DWORD dst_unused:UNUSED_PAD src0_sel:WORD_1\n\t" \
  "v_exp_f16_sdwa v234, v235 dst_sel:WORD_1 dst_unused:UNUSED_PRESERVE src0_sel:WORD_1\n\t" \
  "s_waitcnt lgkmcnt(3)\n\t" \
  "v_mfma_f32_16x16x32_bf16 v[12:15], v[212:215], v[146:149], v[12:15]\n\t" \
  "ds_read_b64_tr_b16 v[210:211], v249 offset:1664\n\t" \
  "v_and_b32_sdwa v217, s56, v216 dst_sel:DWORD dst_unused:UNUSED_PAD src0_sel:DWORD src1_sel:WORD_1\n\t" \
  "v_and_b32 v216, s56, v216\n\t" \
  "v_mfma_f32_16x16x32_bf16 v[36:39], v[212:215], v[154:157], v[36:39]\n\t" \
  "ds_read_b32 v217, v217 offset:0\n\t" \
  "v_and_b32_sdwa v219, s56, v218 dst_sel:DWORD dst_unused:UNUSED_PAD src0_sel:DWORD src1_sel:WORD_1\n\t" \
  "v_mfma_f32_16x16x32_bf16 v[60:63], v[212:215], v[162:165], v[60:63]\n\t" \
  "ds_read_b32 v216, v216 offset:0\n\t" \
  "v_and_b32 v218, s56, v218\n\t" \
  "v_and_b32_sdwa v221, s56, v220 dst_sel:DWORD dst_unused:UNUSED_PAD src0_sel:DWORD src1_sel:WORD_1\n\t" \
  "v_mfma_f32_16x16x32_bf16 v[84:87], v[212:215], v[170:173], v[84:87]\n\t" \
  "ds_read_b64_tr_b16 v[212:213], v249 offset:136\n\t" \
  "v_and_b32 v220, s56, v220\n\t" \
  "s_waitcnt lgkmcnt(3)\n\t" \
  "v_mfma_f32_16x16x32_bf16 v[16:19], v[208:211], v[146:149], v[16:19]\n\t" \
  "ds_read_b64_tr_b16 v[214:215], v249 offset:1672\n\t" \
  "v_and_b32_sdwa v223, s56, v222 dst_sel:DWORD dst_unused:UNUSED_PAD src0_sel:DWORD src1_sel:WORD_1\n\t" \
  "v_mfma_f32_16x16x32_bf16 v[40:43], v[208:211], v[154:157], v[40:43]\n\t" \
  "ds_read_b32 v219, v219 offset:0\n\t" \
  "v_and_b32 v222, s56, v222\n\t" \
  "v_fma_f32 v195, |v195|, v225, v195 mul:4\n\t" \
  "v_mfma_f32_16x16x32_bf16 v[64:67], v[208:211], v[162:165], v[64:67]\n\t" \
  "ds_read_b32 v218, v218 offset:0\n\t" \
  "v_fma_f32 v194, |v194|, v224, v194 mul:4\n\t" \
  "v_fma_f32 v203, |v203|, v227, v203 mul:4\n\t" \
  "v_mfma_f32_16x16x32_bf16 v[88:91], v[208:211], v[170:173], v[88:91]\n\t" \
  "ds_read_b64_tr_b16 v[208:209], v248 offset:43048\n\t" \
  "ds_read_b64_tr_b16 v[210:211], v248 offset:44072\n\t" \
  "v_fma_f32 v202, |v202|, v226, v202 mul:4\n\t" \
  "v_fma_f32 v201, |v201|, v229, v201 mul:4\n\t" \
  "s_waitcnt lgkmcnt(4)\n\t" \
  "v_mfma_f32_16x16x32_bf16 v[20:23], v[212:215], v[146:149], v[20:23]\n\t" \
  "ds_read_b32 v221, v221 offset:0\n\t" \
  "v_fma_f32 v200, |v200|, v228, v200 mul:4\n\t" \
  "v_fma_mix_f32 v176, v176, v233, 0 op_sel_hi:[0,1,0]\n\t" \
  "v_fma_mix_f32 v177, v177, v232, 0 op_sel_hi:[0,1,0]\n\t" \
  "v_mfma_f32_16x16x32_bf16 v[44:47], v[212:215], v[154:157], v[44:47]\n\t" \
  "ds_read_b32 v220, v220 offset:0\n\t" \
  "v_pk_fma_f16 v234, v234, s55, s55\n\t" \
  "v_cvt_pk_bf16_f32 v178, v176, v177\n\t" \
  "v_cvt_pk_bf16_f32 v195, v194, v195\n\t" \
  "v_mfma_f32_16x16x32_bf16 v[68:71], v[212:215], v[162:165], v[68:71]\n\t" \
  "ds_read_b32 v223, v223 offset:0\n\t" \
  "v_cvt_pk_bf16_f32 v203, v202, v203\n\t" \
  "v_cvt_pk_bf16_f32 v202, v200, v201\n\t" \
  "v_rcp_f16 v235, v234\n\t" \
  "v_mfma_f32_16x16x32_bf16 v[92:95], v[212:215], v[170:173], v[92:95]\n\t" \
  "ds_read_b64_tr_b16 v[212:213], v248 offset:46120\n\t" \
  "v_cvt_pk_f16_f32 v224, |v196|, |v197| clamp\n\t" \
  "s_setprio 2\n\t" \
  "v_cvt_pk_f16_f32 v226, |v198|, |v199| clamp\n\t" \
  "s_waitcnt lgkmcnt(4)\n\t" \
  "v_mfma_f32_16x16x32_bf16 v[144:147], v[208:211], v[96:99], 0\n\t" \
  "ds_read_b64_tr_b16 v[214:215], v248 offset:47144\n\t" \
  "v_cvt_pk_f16_f32 v228, |v204|, |v205| clamp\n\t" \
  "v_cvt_pk_f16_f32 v230, |v206|, |v207| clamp\n\t" \
  "v_rcp_f16_sdwa v234, v234 dst_sel:DWORD dst_unused:UNUSED_PAD src0_sel:WORD_1\n\t" \
  "v_mfma_f32_16x16x32_bf16 v[152:155], v[208:211], v[108:111], 0\n\t" \
  "ds_read_b32 v222, v222 offset:0\n\t" \
  "v_and_b32_sdwa v225, s56, v224 dst_sel:DWORD dst_unused:UNUSED_PAD src0_sel:DWORD src1_sel:WORD_1\n\t" \
  "v_and_b32 v224, s56, v224\n\t" \
  "v_mfma_f32_16x16x32_bf16 v[160:163], v[208:211], v[120:123], 0\n\t" \
  "ds_read_b32 v225, v225 offset:0\n\t" \
  "v_and_b32_sdwa v227, s56, v226 dst_sel:DWORD dst_unused:UNUSED_PAD src0_sel:DWORD src1_sel:WORD_1\n\t" \
  "v_mfma_f32_16x16x32_bf16 v[168:171], v[208:211], v[132:135], 0\n\t" \
  "ds_read_b64_tr_b16 v[208:209], v248 offset:45096\n\t" \
  "v_and_b32 v226, s56, v226\n\t" \
  "v_and_b32_sdwa v229, s56, v228 dst_sel:DWORD dst_unused:UNUSED_PAD src0_sel:DWORD src1_sel:WORD_1\n\t" \
  "s_waitcnt lgkmcnt(3)\n\t" \
  "v_mfma_f32_16x16x32_bf16 v[144:147], v[212:215], v[100:103], v[144:147]\n\t" \
  "ds_read_b64_tr_b16 v[210:211], v248 offset:48168\n\t" \
  "v_and_b32 v228, s56, v228\n\t" \
  "v_mfma_f32_16x16x32_bf16 v[152:155], v[212:215], v[112:115], v[152:155]\n\t" \
  "ds_read_b32 v224, v224 offset:0\n\t" \
  "v_and_b32_sdwa v231, s56, v230 dst_sel:DWORD dst_unused:UNUSED_PAD src0_sel:DWORD src1_sel:WORD_1\n\t" \
  "v_mfma_f32_16x16x32_bf16 v[160:163], v[212:215], v[124:127], v[160:163]\n\t" \
  "ds_read_b32 v227, v227 offset:0\n\t" \
  "v_and_b32 v230, s56, v230\n\t" \
  "v_fma_f32 v181, |v181|, v217, v181 mul:4\n\t" \
  "v_mfma_f32_16x16x32_bf16 v[168:171], v[212:215], v[136:139], v[168:171]\n\t" \
  "ds_read_b64_tr_b16 v[212:213], v248 offset:43560\n\t" \
  "ds_read_b64_tr_b16 v[214:215], v248 offset:44584\n\t" \
  "v_fma_f32 v180, |v180|, v216, v180 mul:4\n\t" \
  "v_fma_f32 v183, |v183|, v219, v183 mul:4\n\t" \
  "s_waitcnt lgkmcnt(4)\n\t" \
  "v_mfma_f32_16x16x32_bf16 v[144:147], v[208:211], v[104:107], v[144:147]\n\t" \
  "ds_read_b32 v226, v226 offset:0\n\t" \
  "v_fma_f32 v182, |v182|, v218, v182 mul:4\n\t" \
  "v_fma_f32 v189, |v189|, v221, v189 mul:4\n\t" \
  "v_mfma_f32_16x16x32_bf16 v[152:155], v[208:211], v[116:119], v[152:155]\n\t" \
  "ds_read_b32 v229, v229 offset:0\n\t" \
  "v_fma_f32 v188, |v188|, v220, v188 mul:4\n\t" \
  "v_fma_f32 v191, |v191|, v223, v191 mul:4\n\t" \
  "v_fma_f32 v190, |v190|, v222, v190 mul:4\n\t" \
  "v_mfma_f32_16x16x32_bf16 v[160:163], v[208:211], v[128:131], v[160:163]\n\t" \
  "ds_read_b32 v228, v228 offset:0\n\t" \
  "v_fma_mix_f32 v192, v192, v235, 0 op_sel_hi:[0,1,0]\n\t" \
  "v_fma_mix_f32 v193, v193, v234, 0 op_sel_hi:[0,1,0]\n\t" \
  "v_cvt_pk_bf16_f32 v194, v192, v193\n\t" \
  "v_mfma_f32_16x16x32_bf16 v[168:171], v[208:211], v[140:143], v[168:171]\n\t" \
  "ds_read_b64_tr_b16 v[208:209], v248 offset:46632\n\t" \
  "v_cvt_pk_bf16_f32 v180, v180, v181\n\t" \
  "v_cvt_pk_bf16_f32 v181, v182, v183\n\t" \
  "s_waitcnt lgkmcnt(4)\n\t" \
  "v_mfma_f32_16x16x32_bf16 v[148:151], v[212:215], v[96:99], 0\n\t" \
  "ds_read_b64_tr_b16 v[210:211], v248 offset:47656\n\t" \
  "v_cvt_pk_bf16_f32 v188, v188, v189\n\t" \
  "v_cvt_pk_bf16_f32 v189, v190, v191\n\t" \
  "s_setprio 3\n\t" \
  "v_cvt_pk_f16_f32 v216, |v146|, |v147| clamp\n\t" \
  "v_mfma_f32_16x16x32_bf16 v[156:159], v[212:215], v[108:111], 0\n\t" \
  "ds_read_b32 v231, v231 offset:0\n\t" \
  "v_cvt_pk_f16_f32 v232, v144, v145\n\t" \
  "v_cvt_pk_f16_f32 v218, |v154|, |v155| clamp\n\t" \
  "v_cvt_pk_f16_f32 v220, |v152|, |v153| clamp\n\t" \
  "v_mfma_f32_16x16x32_bf16 v[164:167], v[212:215], v[120:123], 0\n\t" \
  "ds_read_b32 v230, v230 offset:0\n\t" \
  "v_pk_mul_f16 v233, v232, v232\n\t" \
  "v_and_b32_sdwa v217, s56, v216 dst_sel:DWORD dst_unused:UNUSED_PAD src0_sel:DWORD src1_sel:WORD_1\n\t" \
  "v_mfma_f32_16x16x32_bf16 v[172:175], v[212:215], v[132:135], 0\n\t" \
  "ds_read_b64_tr_b16 v[212:213], v248 offset:45608\n\t" \
  "v_and_b32 v216, s56, v216\n\t" \
  "v_and_b32_sdwa v219, s56, v218 dst_sel:DWORD dst_unused:UNUSED_PAD src0_sel:DWORD src1_sel:WORD_1\n\t" \
  "s_waitcnt lgkmcnt(3)\n\t" \
  "v_mfma_f32_16x16x32_bf16 v[148:151], v[208:211], v[100:103], v[148:151]\n\t" \
  "ds_read_b64_tr_b16 v[214:215], v248 offset:48680\n\t" \
  "v_and_b32 v218, s56, v218\n\t" \
  "v_mfma_f32_16x16x32_bf16 v[156:159], v[208:211], v[112:115], v[156:159]\n\t" \
  "s_waitcnt vmcnt(1)\n\t" \
  "s_cmp_lg_u32 s58, 1\n\t" \
  "s_cbranch_scc1 .Lcnx_c16\n\t" \
  "s_waitcnt vmcnt(0)\n\t" \
  ".Lcnx_c16:\n\t" \
  "s_mov_b64 exec, 3\n\t" \
  "ds_add_rtn_u32 v246, v251, v250 offset:47688\n\t" \
  "s_mov_b64 exec, -1\n\t" \
  "ds_read_b32 v217, v217 offset:0\n\t" \
  "v_and_b32_sdwa v221, s56, v220 dst_sel:DWORD dst_unused:UNUSED_PAD src0_sel:DWORD src1_sel:WORD_1\n\t" \
  "v_mfma_f32_16x16x32_bf16 v[164:167], v[208:211], v[124:127], v[164:167]\n\t" \
  "ds_read_b32 v216, v216 offset:0\n\t" \
  "v_and_b32 v220, s56, v220\n\t" \
  "v_pk_fma_f16 v233, v233, v253, s54\n\t" \
  "v_mfma_f32_16x16x32_bf16 v[172:175], v[208:211], v[136:139], v[172:175]\n\t" \
  "s_waitcnt lgkmcnt(2)\n\t" \
  "v_readfirstlane_b32 s4, v246\n\t" \
  "s_cmp_lt_u32 s4, s57\n\t" \
  "s_cbranch_scc1 .Lcnx_w48\n.Lcnx_b48:\n\t" \
  "ds_read_b64_tr_b16 v[208:209], v249 offset:6152\n\t" \
  "v_pk_mul_f16 v233, v232, v233\n\t" \
  "v_fma_f32 v197, |v197|, v225, v197 mul:4\n\t" \
  "v_mfma_f32_16x16x32_bf16 v[148:151], v[212:215], v[104:107], v[148:151]\n\t" \
  "ds_read_b64_tr_b16 v[210:211], v249 offset:7688\n\t" \
  "v_fma_f32 v196, |v196|, v224, v196 mul:4\n\t" \
  "v_fma_f32 v199, |v199|, v227, v199 mul:4\n\t" \
  "v_mfma_f32_16x16x32_bf16 v[156:159], v[212:215], v[116:119], v[156:159]\n\t" \
  "s_cmp_eq_u32 s37, 0\n\t" \
  "s_cbranch_scc1 .Lcnx_f5\n.Lcnx_g5:\n\t" \
  "ds_read_b32 v219, v219 offset:0\n\t" \
  "v_fma_f32 v198, |v198|, v226, v198 mul:4\n\t" \
  "v_fma_f32 v205, |v205|, v229, v205 mul:4\n\t" \
  "v_fma_f32 v204, |v204|, v228, v204 mul:4\n\t" \
  "v_mfma_f32_16x16x32_bf16 v[164:167], v[212:215], v[128:131], v[164:167]\n\t" \
  "ds_read_b32 v218, v218 offset:0\n\t" \
  "v_fma_f32 v207, |v207|, v231, v207 mul:4\n\t" \
  "v_fma_f32 v206, |v206|, v230, v206 mul:4\n\t" \
  "v_cvt_pk_bf16_f32 v196, v196, v197\n\t" \
  "v_mfma_f32_16x16x32_bf16 v[172:175], v[212:215], v[140:143], v[172:175]\n\t" \
  "ds_read_b64_tr_b16 v[212:213], v249 offset:6160\n\t" \
  "v_cvt_pk_bf16_f32 v197, v198, v199\n\t" \
  "v_cvt_pk_bf16_f32 v204, v204, v205\n\t" \
  "v_cvt_pk_bf16_f32 v205, v206, v207\n\t" \
  "s_setprio 0\n\t" \
  "v_exp_f16 v232, v233\n\t" \
  "v_cvt_pk_f16_f32 v224, |v162|, |v163| clamp\n\t" \
  "v_mfma_f32_16x16x32_bf16 v[24:27], v[242:245], v[236:239], v[24:27]\n\t" \
  "ds_read_b64_tr_b16 v[214:215], v249 offset:7696\n\t" \
  "v_cvt_pk_f16_f32 v234, v160, v161\n\t" \
  "v_cvt_pk_f16_f32 v226, |v170|, |v171| clamp\n\t" \
  "v_mfma_f32_16x16x32_bf16 v[28:31], v[240:243], v[236:239], v[28:31]\n\t" \
  "buffer_load_dwordx4 v[236:239], v250, s[8:11], s28 offen offset:256\n\t" \
  "s_cmp_lg_u32 s58, 0\n\t" \
  "s_cbranch_scc1 .Lcnx_c29\n\t" \
  "s_add_u32 m0, s25, 24600\n\t" \
  "s_add_u32 s24, s24, s26\n\t" \
  "buffer_load_dwordx4 v254, s[8:11], s24 offen lds\n\t" \
  "s_cmp_lg_u32 s38, 5\n\t" \
  "s_cbranch_scc1 .Lcnx_c29\n\t" \
  "s_mov_b32 m0, 163632\n\t" \
  "s_add_u32 s30, s30, s31\n\t" \
  "buffer_load_dword v252, s[8:11], s30 offen lds\n\t" \
  ".Lcnx_c29:\n\t" \
  "ds_read_b32 v221, v221 offset:0\n\t" \
  "v_cvt_pk_f16_f32 v228, |v168|, |v169| clamp\n\t" \
  "v_exp_f16_sdwa v232, v233 dst_sel:WORD_1 dst_unused:UNUSED_PRESERVE src0_sel:WORD_1\n\t" \
  "v_pk_mul_f16 v235, v234, v234\n\t" \
  "s_waitcnt lgkmcnt(5)\n\t" \
  "v_mfma_f32_16x16x32_bf16 v[0:3], v[208:211], v[178:181], v[0:3]\n\t" \
  "ds_read_b32 v220, v220 offset:0\n\t" \
  "v_and_b32_sdwa v225, s56, v224 dst_sel:DWORD dst_unused:UNUSED_PAD src0_sel:DWORD src1_sel:WORD_1\n\t" \
  "v_and_b32 v224, s56, v224\n\t" \
  "v_mfma_f32_16x16x32_bf16 v[24:27], v[208:211], v[186:189], v[24:27]\n\t" \
  "ds_read_b32 v225, v225 offset:0\n\t" \
  "v_and_b32_sdwa v227, s56, v226 dst_sel:DWORD dst_unused:UNUSED_PAD src0_sel:DWORD src1_sel:WORD_1\n\t" \
  "v_mfma_f32_16x16x32_bf16 v[48:51], v[208:211], v[194:197], v[48:51]\n\t" \
  "ds_read_b32 v224, v224 offset:0\n\t" \
  "v_and_b32 v226, s56, v226\n\t" \
  "v_and_b32_sdwa v229, s56, v228 dst_sel:DWORD dst_unused:UNUSED_PAD src0_sel:DWORD src1_sel:WORD_1\n\t" \
  "v_mfma_f32_16x16x32_bf16 v[72:75], v[208:211], v[202:205], v[72:75]\n\t" \
  "ds_read_b64_tr_b16 v[208:209], v249 offset:6216\n\t" \
  "v_and_b32 v228, s56, v228\n\t" \
  "s_waitcnt lgkmcnt(5)\n\t" \
  "v_mfma_f32_16x16x32_bf16 v[4:7], v[212:215], v[178:181], v[4:7]\n\t" \
  "ds_read_b64_tr_b16 v[210:211], v249 offset:7752\n\t" \
  "v_pk_fma_f16 v235, v235, v253, s54\n\t" \
  "v_mfma_f32_16x16x32_bf16 v[28:31], v[212:215], v[186:189], v[28:31]\n\t" \
  "ds_read_b32 v227, v227 offset:0\n\t" \
  "v_pk_mul_f16 v235, v234, v235\n\t" \
  "v_fma_f32 v147, |v147|, v217, v147 mul:4\n\t" \
  "v_fma_f32 v146, |v146|, v216, v146 mul:4\n\t" \
  "v_mfma_f32_16x16x32_bf16 v[52:55], v[212:215], v[194:197], v[52:55]\n\t" \
  "ds_read_b32 v226, v226 offset:0\n\t" \
  "v_fma_f32 v155, |v155|, v219, v155 mul:4\n\t" \
  "s_waitcnt lgkmcnt(6)\n\t" \
  "v_fma_f32 v154, |v154|, v218, v154 mul:4\n\t" \
  "v_fma_f32 v153, |v153|, v221, v153 mul:4\n\t" \
  "v_mfma_f32_16x16x32_bf16 v[76:79], v[212:215], v[202:205], v[76:79]\n\t" \
  "ds_read_b64_tr_b16 v[212:213], v249 offset:6224\n\t" \
  "v_fma_f32 v152, |v152|, v220, v152 mul:4\n\t" \
  "v_pk_fma_f16 v232, v232, s55, s55\n\t" \
  "s_waitcnt lgkmcnt(3)\n\t" \
  "v_mfma_f32_16x16x32_bf16 v[8:11], v[208:211], v[178:181], v[8:11]\n\t" \
  "ds_read_b64_tr_b16 v[214:215], v249 offset:7760\n\t" \
  "v_cvt_pk_bf16_f32 v147, v146, v147\n\t" \
  "v_cvt_pk_bf16_f32 v155, v154, v155\n\t" \
  "v_cvt_pk_bf16_f32 v154, v152, v153\n\t" \
  "v_mfma_f32_16x16x32_bf16 v[32:35], v[208:211], v[186:189], v[32:35]\n\t" \
  "ds_read_b32 v229, v229 offset:0\n\t" \
  "v_rcp_f16 v233, v232\n\t" \
  "v_exp_f16 v234, v235\n\t" \
  "v_cvt_pk_f16_f32 v216, |v148|, |v149| clamp\n\t" \
  "v_mfma_f32_16x16x32_bf16 v[56:59], v[208:211], v[194:197], v[56:59]\n\t" \
  "ds_read_b32 v228, v228 offset:0\n\t" \
  "s_setprio 1\n\t" \
  "v_cvt_pk_f16_f32 v218, |v150|, |v151| clamp\n\t" \
  "v_cvt_pk_f16_f32 v220, |v156|, |v157| clamp\n\t" \
  "v_mfma_f32_16x16x32_bf16 v[80:83], v[208:211], v[202:205], v[80:83]\n\t" \
  "ds_read_b64_tr_b16 v[208:209], v249 offset:6280\n\t" \
  "v_cvt_pk_f16_f32 v222, |v158|, |v159| clamp\n\t" \
  "v_rcp_f16_sdwa v232, v232 dst_sel:DWORD dst_unused:UNUSED_PAD src0_sel:WORD_1\n\t" \
  "v_exp_f16_sdwa v234, v235 dst_sel:WORD_1 dst_unused:UNUSED_PRESERVE src0_sel:WORD_1\n\t" \
  "s_waitcnt lgkmcnt(3)\n\t" \
  "v_mfma_f32_16x16x32_bf16 v[12:15], v[212:215], v[178:181], v[12:15]\n\t" \
  "ds_read_b64_tr_b16 v[210:211], v249 offset:7816\n\t" \
  "v_and_b32_sdwa v217, s56, v216 dst_sel:DWORD dst_unused:UNUSED_PAD src0_sel:DWORD src1_sel:WORD_1\n\t" \
  "v_and_b32 v216, s56, v216\n\t" \
  "v_mfma_f32_16x16x32_bf16 v[36:39], v[212:215], v[186:189], v[36:39]\n\t" \
  "ds_read_b32 v217, v217 offset:0\n\t" \
  "v_and_b32_sdwa v219, s56, v218 dst_sel:DWORD dst_unused:UNUSED_PAD src0_sel:DWORD src1_sel:WORD_1\n\t" \
  "v_mfma_f32_16x16x32_bf16 v[60:63], v[212:215], v[194:197], v[60:63]\n\t" \
  "ds_read_b32 v216, v216 offset:0\n\t" \
  "v_and_b32 v218, s56, v218\n\t" \
  "v_and_b32_sdwa v221, s56, v220 dst_sel:DWORD dst_unused:UNUSED_PAD src0_sel:DWORD src1_sel:WORD_1\n\t" \
  "v_mfma_f32_16x16x32_bf16 v[84:87], v[212:215], v[202:205], v[84:87]\n\t" \
  "ds_read_b64_tr_b16 v[212:213], v249 offset:6288\n\t" \
  "v_and_b32 v220, s56, v220\n\t" \
  "s_waitcnt lgkmcnt(3)\n\t" \
  "v_mfma_f32_16x16x32_bf16 v[16:19], v[208:211], v[178:181], v[16:19]\n\t" \
  "ds_read_b64_tr_b16 v[214:215], v249 offset:7824\n\t" \
  "v_and_b32_sdwa v223, s56, v222 dst_sel:DWORD dst_unused:UNUSED_PAD src0_sel:DWORD src1_sel:WORD_1\n\t" \
  "v_mfma_f32_16x16x32_bf16 v[40:43], v[208:211], v[186:189], v[40:43]\n\t" \
  "ds_read_b32 v219, v219 offset:0\n\t" \
  "v_and_b32 v222, s56, v222\n\t" \
  "v_fma_f32 v163, |v163|, v225, v163 mul:4\n\t" \
  "v_mfma_f32_16x16x32_bf16 v[64:67], v[208:211], v[194:197], v[64:67]\n\t" \
  "ds_read_b32 v218, v218 offset:0\n\t" \
  "v_fma_f32 v162, |v162|, v224, v162 mul:4\n\t" \
  "v_fma_f32 v171, |v171|, v227, v171 mul:4\n\t" \
  "v_mfma_f32_16x16x32_bf16 v[88:91], v[208:211], v[202:205], v[88:91]\n\t" \
  "ds_read_b64_tr_b16 v[208:209], v248 offset:49192\n\t" \
  "ds_read_b64_tr_b16 v[210:211], v248 offset:50216\n\t" \
  "v_fma_f32 v170, |v170|, v226, v170 mul:4\n\t" \
  "v_fma_f32 v169, |v169|, v229, v169 mul:4\n\t" \
  "s_waitcnt lgkmcnt(4)\n\t" \
  "v_mfma_f32_16x16x32_bf16 v[20:23], v[212:215], v[178:181], v[20:23]\n\t" \
  "ds_read_b32 v221, v221 offset:0\n\t" \
  "v_fma_f32 v168, |v168|, v228, v168 mul:4\n\t" \
  "v_fma_mix_f32 v144, v144, v233, 0 op_sel_hi:[0,1,0]\n\t" \
  "v_fma_mix_f32 v145, v145, v232, 0 op_sel_hi:[0,1,0]\n\t" \
  "v_mfma_f32_16x16x32_bf16 v[44:47], v[212:215], v[186:189], v[44:47]\n\t" \
  "ds_read_b32 v220, v220 offset:0\n\t" \
  "v_pk_fma_f16 v234, v234, s55, s55\n\t" \
  "v_cvt_pk_bf16_f32 v146, v144, v145\n\t" \
  "v_cvt_pk_bf16_f32 v163, v162, v163\n\t" \
  "v_mfma_f32_16x16x32_bf16 v[68:71], v[212:215], v[194:197], v[68:71]\n\t" \
  "ds_read_b32 v223, v223 offset:0\n\t" \
  "v_cvt_pk_bf16_f32 v171, v170, v171\n\t" \
  "v_cvt_pk_bf16_f32 v170, v168, v169\n\t" \
  "v_rcp_f16 v235, v234\n\t" \
  "v_mfma_f32_16x16x32_bf16 v[92:95], v[212:215], v[202:205], v[92:95]\n\t" \
  "ds_read_b64_tr_b16 v[212:213], v248 offset:52264\n\t" \
  "v_cvt_pk_f16_f32 v224, |v164|, |v165| clamp\n\t" \
  "s_setprio 2\n\t" \
  "v_cvt_pk_f16_f32 v226, |v166|, |v167| clamp\n\t" \
  "s_waitcnt lgkmcnt(4)\n\t" \
  "v_mfma_f32_16x16x32_bf16 v[176:179], v[208:211], v[96:99], 0\n\t" \
  "ds_read_b64_tr_b16 v[214:215], v248 offset:53288\n\t" \
  "v_cvt_pk_f16_f32 v228, |v172|, |v173| clamp\n\t" \
  "v_cvt_pk_f16_f32 v230, |v174|, |v175| clamp\n\t" \
  "v_rcp_f16_sdwa v234, v234 dst_sel:DWORD dst_unused:UNUSED_PAD src0_sel:WORD_1\n\t" \
  "v_mfma_f32_16x16x32_bf16 v[184:187], v[208:211], v[108:111], 0\n\t" \
  "ds_read_b32 v222, v222 offset:0\n\t" \
  "v_and_b32_sdwa v225, s56, v224 dst_sel:DWORD dst_unused:UNUSED_PAD src0_sel:DWORD src1_sel:WORD_1\n\t" \
  "v_and_b32 v224, s56, v224\n\t" \
  "v_mfma_f32_16x16x32_bf16 v[192:195], v[208:211], v[120:123], 0\n\t" \
  "ds_read_b32 v225, v225 offset:0\n\t" \
  "v_and_b32_sdwa v227, s56, v226 dst_sel:DWORD dst_unused:UNUSED_PAD src0_sel:DWORD src1_sel:WORD_1\n\t" \
  "v_mfma_f32_16x16x32_bf16 v[200:203], v[208:211], v[132:135], 0\n\t" \
  "ds_read_b64_tr_b16 v[208:209], v248 offset:51240\n\t" \
  "v_and_b32 v226, s56, v226\n\t" \
  "v_and_b32_sdwa v229, s56, v228 dst_sel:DWORD dst_unused:UNUSED_PAD src0_sel:DWORD src1_sel:WORD_1\n\t" \
  "s_waitcnt lgkmcnt(3)\n\t" \
  "v_mfma_f32_16x16x32_bf16 v[176:179], v[212:215], v[100:103], v[176:179]\n\t" \
  "ds_read_b64_tr_b16 v[210:211], v248 offset:54312\n\t" \
  "v_and_b32 v228, s56, v228\n\t" \
  "v_mfma_f32_16x16x32_bf16 v[184:187], v[212:215], v[112:115], v[184:187]\n\t" \
  "ds_read_b32 v224, v224 offset:0\n\t" \
  "v_and_b32_sdwa v231, s56, v230 dst_sel:DWORD dst_unused:UNUSED_PAD src0_sel:DWORD src1_sel:WORD_1\n\t" \
  "v_mfma_f32_16x16x32_bf16 v[192:195], v[212:215], v[124:127], v[192:195]\n\t" \
  "ds_read_b32 v227, v227 offset:0\n\t" \
  "v_and_b32 v230, s56, v230\n\t" \
  "v_fma_f32 v149, |v149|, v217, v149 mul:4\n\t" \
  "v_mfma_f32_16x16x32_bf16 v[200:203], v[212:215], v[136:139], v[200:203]\n\t" \
  "ds_read_b64_tr_b16 v[212:213], v248 offset:49704\n\t" \
  "ds_read_b64_tr_b16 v[214:215], v248 offset:50728\n\t" \
  "v_fma_f32 v148, |v148|, v216, v148 mul:4\n\t" \
  "v_fma_f32 v151, |v151|, v219, v151 mul:4\n\t" \
  "s_waitcnt lgkmcnt(4)\n\t" \
  "v_mfma_f32_16x16x32_bf16 v[176:179], v[208:211], v[104:107], v[176:179]\n\t" \
  "ds_read_b32 v226, v226 offset:0\n\t" \
  "v_fma_f32 v150, |v150|, v218, v150 mul:4\n\t" \
  "v_fma_f32 v157, |v157|, v221, v157 mul:4\n\t" \
  "v_mfma_f32_16x16x32_bf16 v[184:187], v[208:211], v[116:119], v[184:187]\n\t" \
  "ds_read_b32 v229, v229 offset:0\n\t" \
  "v_fma_f32 v156, |v156|, v220, v156 mul:4\n\t" \
  "v_fma_f32 v159, |v159|, v223, v159 mul:4\n\t" \
  "v_fma_f32 v158, |v158|, v222, v158 mul:4\n\t" \
  "v_mfma_f32_16x16x32_bf16 v[192:195], v[208:211], v[128:131], v[192:195]\n\t" \
  "ds_read_b32 v228, v228 offset:0\n\t" \
  "v_fma_mix_f32 v160, v160, v235, 0 op_sel_hi:[0,1,0]\n\t" \
  "v_fma_mix_f32 v161, v161, v234, 0 op_sel_hi:[0,1,0]\n\t" \
  "v_cvt_pk_bf16_f32 v162, v160, v161\n\t" \
  "v_mfma_f32_16x16x32_bf16 v[200:203], v[208:211], v[140:143], v[200:203]\n\t" \
  "ds_read_b64_tr_b16 v[208:209], v248 offset:52776\n\t" \
  "v_cvt_pk_bf16_f32 v148, v148, v149\n\t" \
  "v_cvt_pk_bf16_f32 v149, v150, v151\n\t" \
  "s_waitcnt lgkmcnt(4)\n\t" \
  "v_mfma_f32_16x16x32_bf16 v[180:183], v[212:215], v[96:99], 0\n\t" \
  "ds_read_b64_tr_b16 v[210:211], v248 offset:53800\n\t" \
  "v_cvt_pk_bf16_f32 v156, v156, v157\n\t" \
  "v_cvt_pk_bf16_f32 v157, v158, v159\n\t" \
  "s_setprio 3\n\t" \
  "v_cvt_pk_f16_f32 v216, |v178|, |v179| clamp\n\t" \
  "v_mfma_f32_16x16x32_bf16 v[188:191], v[212:215], v[108:111], 0\n\t" \
  "ds_read_b32 v231, v231 offset:0\n\t" \
  "v_cvt_pk_f16_f32 v232, v176, v177\n\t" \
  "v_cvt_pk_f16_f32 v218, |v186|, |v187| clamp\n\t" \
  "v_cvt_pk_f16_f32 v220, |v184|, |v185| clamp\n\t" \
  "v_mfma_f32_16x16x32_bf16 v[196:199], v[212:215], v[120:123], 0\n\t" \
  "ds_read_b32 v230, v230 offset:0\n\t" \
  "v_pk_mul_f16 v233, v232, v232\n\t" \
  "v_and_b32_sdwa v217, s56, v216 dst_sel:DWORD dst_unused:UNUSED_PAD src0_sel:DWORD src1_sel:WORD_1\n\t" \
  "v_mfma_f32_16x16x32_bf16 v[204:207], v[212:215], v[132:135], 0\n\t" \
  "ds_read_b64_tr_b16 v[212:213], v248 offset:51752\n\t" \
  "v_and_b32 v216, s56, v216\n\t" \
  "v_and_b32_sdwa v219, s56, v218 dst_sel:DWORD dst_unused:UNUSED_PAD src0_sel:DWORD src1_sel:WORD_1\n\t" \
  "s_waitcnt lgkmcnt(3)\n\t" \
  "v_mfma_f32_16x16x32_bf16 v[180:183], v[208:211], v[100:103], v[180:183]\n\t" \
  "ds_read_b64_tr_b16 v[214:215], v248 offset:54824\n\t" \
  "v_and_b32 v218, s56, v218\n\t" \
  "v_mfma_f32_16x16x32_bf16 v[188:191], v[208:211], v[112:115], v[188:191]\n\t" \
  "s_waitcnt vmcnt(1)\n\t" \
  "s_cmp_lg_u32 s58, 1\n\t" \
  "s_cbranch_scc1 .Lcnx_c17\n\t" \
  "s_waitcnt vmcnt(0)\n\t" \
  ".Lcnx_c17:\n\t" \
  "s_mov_b64 exec, 3\n\t" \
  "ds_add_rtn_u32 v246, v251, v250 offset:47696\n\t" \
  "s_mov_b64 exec, -1\n\t" \
  "ds_read_b32 v217, v217 offset:0\n\t" \
  "v_and_b32_sdwa v221, s56, v220 dst_sel:DWORD dst_unused:UNUSED_PAD src0_sel:DWORD src1_sel:WORD_1\n\t" \
  "v_mfma_f32_16x16x32_bf16 v[196:199], v[208:211], v[124:127], v[196:199]\n\t" \
  "ds_read_b32 v216, v216 offset:0\n\t" \
  "v_and_b32 v220, s56, v220\n\t" \
  "v_pk_fma_f16 v233, v233, v253, s54\n\t" \
  "v_mfma_f32_16x16x32_bf16 v[204:207], v[208:211], v[136:139], v[204:207]\n\t" \
  "s_waitcnt lgkmcnt(2)\n\t" \
  "v_readfirstlane_b32 s4, v246\n\t" \
  "s_cmp_lt_u32 s4, s57\n\t" \
  "s_cbranch_scc1 .Lcnx_w60\n.Lcnx_b60:\n\t" \
  "ds_read_b64_tr_b16 v[208:209], v249 offset:24600\n\t" \
  "v_pk_mul_f16 v233, v232, v233\n\t" \
  "v_fma_f32 v165, |v165|, v225, v165 mul:4\n\t" \
  "v_mfma_f32_16x16x32_bf16 v[180:183], v[212:215], v[104:107], v[180:183]\n\t" \
  "ds_read_b64_tr_b16 v[210:211], v249 offset:26136\n\t" \
  "v_fma_f32 v164, |v164|, v224, v164 mul:4\n\t" \
  "v_fma_f32 v167, |v167|, v227, v167 mul:4\n\t" \
  "v_mfma_f32_16x16x32_bf16 v[188:191], v[212:215], v[116:119], v[188:191]\n\t" \
  "s_cmp_eq_u32 s37, 0\n\t" \
  "s_cbranch_scc1 .Lcnx_f6\n.Lcnx_g6:\n\t" \
  "ds_read_b32 v219, v219 offset:0\n\t" \
  "v_fma_f32 v166, |v166|, v226, v166 mul:4\n\t" \
  "v_fma_f32 v173, |v173|, v229, v173 mul:4\n\t" \
  "v_fma_f32 v172, |v172|, v228, v172 mul:4\n\t" \
  "v_mfma_f32_16x16x32_bf16 v[196:199], v[212:215], v[128:131], v[196:199]\n\t" \
  "ds_read_b32 v218, v218 offset:0\n\t" \
  "v_fma_f32 v175, |v175|, v231, v175 mul:4\n\t" \
  "v_fma_f32 v174, |v174|, v230, v174 mul:4\n\t" \
  "v_cvt_pk_bf16_f32 v164, v164, v165\n\t" \
  "v_mfma_f32_16x16x32_bf16 v[204:207], v[212:215], v[140:143], v[204:207]\n\t" \
  "ds_read_b64_tr_b16 v[212:213], v249 offset:24608\n\t" \
  "v_cvt_pk_bf16_f32 v165, v166, v167\n\t" \
  "v_cvt_pk_bf16_f32 v172, v172, v173\n\t" \
  "v_cvt_pk_bf16_f32 v173, v174, v175\n\t" \
  "s_setprio 0\n\t" \
  "v_exp_f16 v232, v233\n\t" \
  "v_cvt_pk_f16_f32 v224, |v194|, |v195| clamp\n\t" \
  "v_mfma_f32_16x16x32_bf16 v[32:35], v[242:245], v[236:239], v[32:35]\n\t" \
  "ds_read_b64_tr_b16 v[214:215], v249 offset:26144\n\t" \
  "v_cvt_pk_f16_f32 v234, v192, v193\n\t" \
  "v_cvt_pk_f16_f32 v226, |v202|, |v203| clamp\n\t" \
  "v_mfma_f32_16x16x32_bf16 v[36:39], v[240:243], v[236:239], v[36:39]\n\t" \
  "buffer_load_dwordx4 v[236:239], v250, s[8:11], s28 offen offset:320\n\t" \
  "s_cmp_lg_u32 s58, 0\n\t" \
  "s_cbranch_scc1 .Lcnx_c30\n\t" \
  "s_add_u32 m0, s25, 32800\n\t" \
  "s_add_u32 s24, s24, s26\n\t" \
  "buffer_load_dwordx4 v254, s[8:11], s24 offen lds\n\t" \
  "s_cmp_lg_u32 s38, 6\n\t" \
  "s_cbranch_scc1 .Lcnx_c30\n\t" \
  "s_mov_b32 m0, 163632\n\t" \
  "s_add_u32 s30, s30, s31\n\t" \
  "buffer_load_dword v252, s[8:11], s30 offen lds\n\t" \
  ".Lcnx_c30:\n\t" \
  "ds_read_b32 v221, v221 offset:0\n\t" \
  "v_cvt_pk_f16_f32 v228, |v200|, |v201| clamp\n\t" \
  "v_exp_f16_sdwa v232, v233 dst_sel:WORD_1 dst_unused:UNUSED_PRESERVE src0_sel:WORD_1\n\t" \
  "v_pk_mul_f16 v235, v234, v234\n\t" \
  "s_waitcnt lgkmcnt(5)\n\t" \
  "v_mfma_f32_16x16x32_bf16 v[0:3], v[208:211], v[146:149], v[0:3]\n\t" \
  "ds_read_b32 v220, v220 offset:0\n\t" \
  "v_and_b32_sdwa v225, s56, v224 dst_sel:DWORD dst_unused:UNUSED_PAD src0_sel:DWORD src1_sel:WORD_1\n\t" \
  "v_and_b32 v224, s56, v224\n\t" \
  "v_mfma_f32_16x16x32_bf16 v[24:27], v[208:211], v[154:157], v[24:27]\n\t" \
  "ds_read_b32 v225, v225 offset:0\n\t" \
  "v_and_b32_sdwa v227, s56, v226 dst_sel:DWORD dst_unused:UNUSED_PAD src0_sel:DWORD src1_sel:WORD_1\n\t" \
  "v_mfma_f32_16x16x32_bf16 v[48:51], v[208:211], v[162:165], v[48:51]\n\t" \
  "ds_read_b32 v224, v224 offset:0\n\t" \
  "v_and_b32 v226, s56, v226\n\t" \
  "v_and_b32_sdwa v229, s56, v228 dst_sel:DWORD dst_unused:UNUSED_PAD src0_sel:DWORD src1_sel:WORD_1\n\t" \
  "v_mfma_f32_16x16x32_bf16 v[72:75], v[208:211], v[170:173], v[72:75]\n\t" \
  "ds_read_b64_tr_b16 v[208:209], v249 offset:24664\n\t" \
  "v_and_b32 v228, s56, v228\n\t" \
  "s_waitcnt lgkmcnt(5)\n\t" \
  "v_mfma_f32_16x16x32_bf16 v[4:7], v[212:215], v[146:149], v[4:7]\n\t" \
  "ds_read_b64_tr_b16 v[210:211], v249 offset:26200\n\t" \
  "v_pk_fma_f16 v235, v235, v253, s54\n\t" \
  "v_mfma_f32_16x16x32_bf16 v[28:31], v[212:215], v[154:157], v[28:31]\n\t" \
  "ds_read_b32 v227, v227 offset:0\n\t" \
  "v_pk_mul_f16 v235, v234, v235\n\t" \
  "v_fma_f32 v179, |v179|, v217, v179 mul:4\n\t" \
  "v_fma_f32 v178, |v178|, v216, v178 mul:4\n\t" \
  "v_mfma_f32_16x16x32_bf16 v[52:55], v[212:215], v[162:165], v[52:55]\n\t" \
  "ds_read_b32 v226, v226 offset:0\n\t" \
  "v_fma_f32 v187, |v187|, v219, v187 mul:4\n\t" \
  "s_waitcnt lgkmcnt(6)\n\t" \
  "v_fma_f32 v186, |v186|, v218, v186 mul:4\n\t" \
  "v_fma_f32 v185, |v185|, v221, v185 mul:4\n\t" \
  "v_mfma_f32_16x16x32_bf16 v[76:79], v[212:215], v[170:173], v[76:79]\n\t" \
  "ds_read_b64_tr_b16 v[212:213], v249 offset:24672\n\t" \
  "v_fma_f32 v184, |v184|, v220, v184 mul:4\n\t" \
  "v_pk_fma_f16 v232, v232, s55, s55\n\t" \
  "s_waitcnt lgkmcnt(3)\n\t" \
  "v_mfma_f32_16x16x32_bf16 v[8:11], v[208:211], v[146:149], v[8:11]\n\t" \
  "ds_read_b64_tr_b16 v[214:215], v249 offset:26208\n\t" \
  "v_cvt_pk_bf16_f32 v179, v178, v179\n\t" \
  "v_cvt_pk_bf16_f32 v187, v186, v187\n\t" \
  "v_cvt_pk_bf16_f32 v186, v184, v185\n\t" \
  "v_mfma_f32_16x16x32_bf16 v[32:35], v[208:211], v[154:157], v[32:35]\n\t" \
  "ds_read_b32 v229, v229 offset:0\n\t" \
  "v_rcp_f16 v233, v232\n\t" \
  "v_exp_f16 v234, v235\n\t" \
  "v_cvt_pk_f16_f32 v216, |v180|, |v181| clamp\n\t" \
  "v_mfma_f32_16x16x32_bf16 v[56:59], v[208:211], v[162:165], v[56:59]\n\t" \
  "ds_read_b32 v228, v228 offset:0\n\t" \
  "s_setprio 1\n\t" \
  "v_cvt_pk_f16_f32 v218, |v182|, |v183| clamp\n\t" \
  "v_cvt_pk_f16_f32 v220, |v188|, |v189| clamp\n\t" \
  "v_mfma_f32_16x16x32_bf16 v[80:83], v[208:211], v[170:173], v[80:83]\n\t" \
  "ds_read_b64_tr_b16 v[208:209], v249 offset:24728\n\t" \
  "v_cvt_pk_f16_f32 v222, |v190|, |v191| clamp\n\t" \
  "v_rcp_f16_sdwa v232, v232 dst_sel:DWORD dst_unused:UNUSED_PAD src0_sel:WORD_1\n\t" \
  "v_exp_f16_sdwa v234, v235 dst_sel:WORD_1 dst_unused:UNUSED_PRESERVE src0_sel:WORD_1\n\t" \
  "s_waitcnt lgkmcnt(3)\n\t" \
  "v_mfma_f32_16x16x32_bf16 v[12:15], v[212:215], v[146:149], v[12:15]\n\t" \
  "ds_read_b64_tr_b16 v[210:211], v249 offset:26264\n\t" \
  "v_and_b32_sdwa v217, s56, v216 dst_sel:DWORD dst_unused:UNUSED_PAD src0_sel:DWORD src1_sel:WORD_1\n\t" \
  "v_and_b32 v216, s56, v216\n\t" \
  "v_mfma_f32_16x16x32_bf16 v[36:39], v[212:215], v[154:157], v[36:39]\n\t" \
  "ds_read_b32 v217, v217 offset:0\n\t" \
  "v_and_b32_sdwa v219, s56, v218 dst_sel:DWORD dst_unused:UNUSED_PAD src0_sel:DWORD src1_sel:WORD_1\n\t" \
  "v_mfma_f32_16x16x32_bf16 v[60:63], v[212:215], v[162:165], v[60:63]\n\t" \
  "ds_read_b32 v216, v216 offset:0\n\t" \
  "v_and_b32 v218, s56, v218\n\t" \
  "v_and_b32_sdwa v221, s56, v220 dst_sel:DWORD dst_unused:UNUSED_PAD src0_sel:DWORD src1_sel:WORD_1\n\t" \
  "v_mfma_f32_16x16x32_bf16 v[84:87], v[212:215], v[170:173], v[84:87]\n\t" \
  "ds_read_b64_tr_b16 v[212:213], v249 offset:24736\n\t" \
  "v_and_b32 v220, s56, v220\n\t" \
  "s_waitcnt lgkmcnt(3)\n\t" \
  "v_mfma_f32_16x16x32_bf16 v[16:19], v[208:211], v[146:149], v[16:19]\n\t" \
  "ds_read_b64_tr_b16 v[214:215], v249 offset:26272\n\t" \
  "v_and_b32_sdwa v223, s56, v222 dst_sel:DWORD dst_unused:UNUSED_PAD src0_sel:DWORD src1_sel:WORD_1\n\t" \
  "v_mfma_f32_16x16x32_bf16 v[40:43], v[208:211], v[154:157], v[40:43]\n\t" \
  "ds_read_b32 v219, v219 offset:0\n\t" \
  "v_and_b32 v222, s56, v222\n\t" \
  "v_fma_f32 v195, |v195|, v225, v195 mul:4\n\t" \
  "v_mfma_f32_16x16x32_bf16 v[64:67], v[208:211], v[162:165], v[64:67]\n\t" \
  "ds_read_b32 v218, v218 offset:0\n\t" \
  "v_fma_f32 v194, |v194|, v224, v194 mul:4\n\t" \
  "v_fma_f32 v203, |v203|, v227, v203 mul:4\n\t" \
  "v_mfma_f32_16x16x32_bf16 v[88:91], v[208:211], v[170:173], v[88:91]\n\t" \
  "ds_read_b64_tr_b16 v[208:209], v248 offset:36904\n\t" \
  "ds_read_b64_tr_b16 v[210:211], v248 offset:37928\n\t" \
  "v_fma_f32 v202, |v202|, v226, v202 mul:4\n\t" \
  "v_fma_f32 v201, |v201|, v229, v201 mul:4\n\t" \
  "s_waitcnt lgkmcnt(4)\n\t" \
  "v_mfma_f32_16x16x32_bf16 v[20:23], v[212:215], v[146:149], v[20:23]\n\t" \
  "ds_read_b32 v221, v221 offset:0\n\t" \
  "v_fma_f32 v200, |v200|, v228, v200 mul:4\n\t" \
  "v_fma_mix_f32 v176, v176, v233, 0 op_sel_hi:[0,1,0]\n\t" \
  "v_fma_mix_f32 v177, v177, v232, 0 op_sel_hi:[0,1,0]\n\t" \
  "v_mfma_f32_16x16x32_bf16 v[44:47], v[212:215], v[154:157], v[44:47]\n\t" \
  "ds_read_b32 v220, v220 offset:0\n\t" \
  "v_pk_fma_f16 v234, v234, s55, s55\n\t" \
  "v_cvt_pk_bf16_f32 v178, v176, v177\n\t" \
  "v_cvt_pk_bf16_f32 v195, v194, v195\n\t" \
  "v_mfma_f32_16x16x32_bf16 v[68:71], v[212:215], v[162:165], v[68:71]\n\t" \
  "ds_read_b32 v223, v223 offset:0\n\t" \
  "v_cvt_pk_bf16_f32 v203, v202, v203\n\t" \
  "v_cvt_pk_bf16_f32 v202, v200, v201\n\t" \
  "v_rcp_f16 v235, v234\n\t" \
  "v_mfma_f32_16x16x32_bf16 v[92:95], v[212:215], v[170:173], v[92:95]\n\t" \
  "ds_read_b64_tr_b16 v[212:213], v248 offset:39976\n\t" \
  "v_cvt_pk_f16_f32 v224, |v196|, |v197| clamp\n\t" \
  "s_setprio 2\n\t" \
  "v_cvt_pk_f16_f32 v226, |v198|, |v199| clamp\n\t" \
  "s_waitcnt lgkmcnt(4)\n\t" \
  "v_mfma_f32_16x16x32_bf16 v[144:147], v[208:211], v[96:99], 0\n\t" \
  "ds_read_b64_tr_b16 v[214:215], v248 offset:41000\n\t" \
  "v_cvt_pk_f16_f32 v228, |v204|, |v205| clamp\n\t" \
  "v_cvt_pk_f16_f32 v230, |v206|, |v207| clamp\n\t" \
  "v_rcp_f16_sdwa v234, v234 dst_sel:DWORD dst_unused:UNUSED_PAD src0_sel:WORD_1\n\t" \
  "v_mfma_f32_16x16x32_bf16 v[152:155], v[208:211], v[108:111], 0\n\t" \
  "ds_read_b32 v222, v222 offset:0\n\t" \
  "v_and_b32_sdwa v225, s56, v224 dst_sel:DWORD dst_unused:UNUSED_PAD src0_sel:DWORD src1_sel:WORD_1\n\t" \
  "v_and_b32 v224, s56, v224\n\t" \
  "v_mfma_f32_16x16x32_bf16 v[160:163], v[208:211], v[120:123], 0\n\t" \
  "ds_read_b32 v225, v225 offset:0\n\t" \
  "v_and_b32_sdwa v227, s56, v226 dst_sel:DWORD dst_unused:UNUSED_PAD src0_sel:DWORD src1_sel:WORD_1\n\t" \
  "v_mfma_f32_16x16x32_bf16 v[168:171], v[208:211], v[132:135], 0\n\t" \
  "ds_read_b64_tr_b16 v[208:209], v248 offset:38952\n\t" \
  "v_and_b32 v226, s56, v226\n\t" \
  "v_and_b32_sdwa v229, s56, v228 dst_sel:DWORD dst_unused:UNUSED_PAD src0_sel:DWORD src1_sel:WORD_1\n\t" \
  "s_waitcnt lgkmcnt(3)\n\t" \
  "v_mfma_f32_16x16x32_bf16 v[144:147], v[212:215], v[100:103], v[144:147]\n\t" \
  "ds_read_b64_tr_b16 v[210:211], v248 offset:42024\n\t" \
  "v_and_b32 v228, s56, v228\n\t" \
  "v_mfma_f32_16x16x32_bf16 v[152:155], v[212:215], v[112:115], v[152:155]\n\t" \
  "ds_read_b32 v224, v224 offset:0\n\t" \
  "v_and_b32_sdwa v231, s56, v230 dst_sel:DWORD dst_unused:UNUSED_PAD src0_sel:DWORD src1_sel:WORD_1\n\t" \
  "v_mfma_f32_16x16x32_bf16 v[160:163], v[212:215], v[124:127], v[160:163]\n\t" \
  "ds_read_b32 v227, v227 offset:0\n\t" \
  "v_and_b32 v230, s56, v230\n\t" \
  "v_fma_f32 v181, |v181|, v217, v181 mul:4\n\t" \
  "v_mfma_f32_16x16x32_bf16 v[168:171], v[212:215], v[136:139], v[168:171]\n\t" \
  "ds_read_b64_tr_b16 v[212:213], v248 offset:37416\n\t" \
  "ds_read_b64_tr_b16 v[214:215], v248 offset:38440\n\t" \
  "v_fma_f32 v180, |v180|, v216, v180 mul:4\n\t" \
  "v_fma_f32 v183, |v183|, v219, v183 mul:4\n\t" \
  "s_waitcnt lgkmcnt(4)\n\t" \
  "v_mfma_f32_16x16x32_bf16 v[144:147], v[208:211], v[104:107], v[144:147]\n\t" \
  "ds_read_b32 v226, v226 offset:0\n\t" \
  "v_fma_f32 v182, |v182|, v218, v182 mul:4\n\t" \
  "v_fma_f32 v189, |v189|, v221, v189 mul:4\n\t" \
  "v_mfma_f32_16x16x32_bf16 v[152:155], v[208:211], v[116:119], v[152:155]\n\t" \
  "ds_read_b32 v229, v229 offset:0\n\t" \
  "v_fma_f32 v188, |v188|, v220, v188 mul:4\n\t" \
  "v_fma_f32 v191, |v191|, v223, v191 mul:4\n\t" \
  "v_fma_f32 v190, |v190|, v222, v190 mul:4\n\t" \
  "v_mfma_f32_16x16x32_bf16 v[160:163], v[208:211], v[128:131], v[160:163]\n\t" \
  "ds_read_b32 v228, v228 offset:0\n\t" \
  "v_fma_mix_f32 v192, v192, v235, 0 op_sel_hi:[0,1,0]\n\t" \
  "v_fma_mix_f32 v193, v193, v234, 0 op_sel_hi:[0,1,0]\n\t" \
  "v_cvt_pk_bf16_f32 v194, v192, v193\n\t" \
  "v_mfma_f32_16x16x32_bf16 v[168:171], v[208:211], v[140:143], v[168:171]\n\t" \
  "ds_read_b64_tr_b16 v[208:209], v248 offset:40488\n\t" \
  "v_cvt_pk_bf16_f32 v180, v180, v181\n\t" \
  "v_cvt_pk_bf16_f32 v181, v182, v183\n\t" \
  "s_waitcnt lgkmcnt(4)\n\t" \
  "v_mfma_f32_16x16x32_bf16 v[148:151], v[212:215], v[96:99], 0\n\t" \
  "ds_read_b64_tr_b16 v[210:211], v248 offset:41512\n\t" \
  "v_cvt_pk_bf16_f32 v188, v188, v189\n\t" \
  "v_cvt_pk_bf16_f32 v189, v190, v191\n\t" \
  "s_setprio 3\n\t" \
  "v_cvt_pk_f16_f32 v216, |v146|, |v147| clamp\n\t" \
  "v_mfma_f32_16x16x32_bf16 v[156:159], v[212:215], v[108:111], 0\n\t" \
  "ds_read_b32 v231, v231 offset:0\n\t" \
  "v_cvt_pk_f16_f32 v232, v144, v145\n\t" \
  "v_cvt_pk_f16_f32 v218, |v154|, |v155| clamp\n\t" \
  "v_cvt_pk_f16_f32 v220, |v152|, |v153| clamp\n\t" \
  "v_mfma_f32_16x16x32_bf16 v[164:167], v[212:215], v[120:123], 0\n\t" \
  "ds_read_b32 v230, v230 offset:0\n\t" \
  "v_pk_mul_f16 v233, v232, v232\n\t" \
  "v_and_b32_sdwa v217, s56, v216 dst_sel:DWORD dst_unused:UNUSED_PAD src0_sel:DWORD src1_sel:WORD_1\n\t" \
  "v_mfma_f32_16x16x32_bf16 v[172:175], v[212:215], v[132:135], 0\n\t" \
  "ds_read_b64_tr_b16 v[212:213], v248 offset:39464\n\t" \
  "v_and_b32 v216, s56, v216\n\t" \
  "v_and_b32_sdwa v219, s56, v218 dst_sel:DWORD dst_unused:UNUSED_PAD src0_sel:DWORD src1_sel:WORD_1\n\t" \
  "s_waitcnt lgkmcnt(3)\n\t" \
  "v_mfma_f32_16x16x32_bf16 v[148:151], v[208:211], v[100:103], v[148:151]\n\t" \
  "ds_read_b64_tr_b16 v[214:215], v248 offset:42536\n\t" \
  "v_and_b32 v218, s56, v218\n\t" \
  "v_mfma_f32_16x16x32_bf16 v[156:159], v[208:211], v[112:115], v[156:159]\n\t" \
  "s_waitcnt vmcnt(1)\n\t" \
  "s_cmp_lg_u32 s58, 1\n\t" \
  "s_cbranch_scc1 .Lcnx_c18\n\t" \
  "s_waitcnt vmcnt(0)\n\t" \
  ".Lcnx_c18:\n\t" \
  "s_mov_b64 exec, 3\n\t" \
  "ds_add_rtn_u32 v246, v251, v250 offset:47704\n\t" \
  "s_mov_b64 exec, -1\n\t" \
  "ds_read_b32 v217, v217 offset:0\n\t" \
  "v_and_b32_sdwa v221, s56, v220 dst_sel:DWORD dst_unused:UNUSED_PAD src0_sel:DWORD src1_sel:WORD_1\n\t" \
  "v_mfma_f32_16x16x32_bf16 v[164:167], v[208:211], v[124:127], v[164:167]\n\t" \
  "ds_read_b32 v216, v216 offset:0\n\t" \
  "v_and_b32 v220, s56, v220\n\t" \
  "v_pk_fma_f16 v233, v233, v253, s54\n\t" \
  "v_mfma_f32_16x16x32_bf16 v[172:175], v[208:211], v[136:139], v[172:175]\n\t" \
  "s_waitcnt lgkmcnt(2)\n\t" \
  "v_readfirstlane_b32 s4, v246\n\t" \
  "s_cmp_lt_u32 s4, s57\n\t" \
  "s_cbranch_scc1 .Lcnx_w72\n.Lcnx_b72:\n\t" \
  "ds_read_b64_tr_b16 v[208:209], v249 offset:30752\n\t" \
  "v_pk_mul_f16 v233, v232, v233\n\t" \
  "v_fma_f32 v197, |v197|, v225, v197 mul:4\n\t" \
  "v_mfma_f32_16x16x32_bf16 v[148:151], v[212:215], v[104:107], v[148:151]\n\t" \
  "ds_read_b64_tr_b16 v[210:211], v249 offset:32288\n\t" \
  "v_fma_f32 v196, |v196|, v224, v196 mul:4\n\t" \
  "v_fma_f32 v199, |v199|, v227, v199 mul:4\n\t" \
  "v_mfma_f32_16x16x32_bf16 v[156:159], v[212:215], v[116:119], v[156:159]\n\t" \
  "s_cmp_eq_u32 s37, 0\n\t" \
  "s_cbranch_scc1 .Lcnx_f7\n.Lcnx_g7:\n\t" \
  "ds_read_b32 v219, v219 offset:0\n\t" \
  "v_fma_f32 v198, |v198|, v226, v198 mul:4\n\t" \
  "v_fma_f32 v205, |v205|, v229, v205 mul:4\n\t" \
  "v_fma_f32 v204, |v204|, v228, v204 mul:4\n\t" \
  "v_mfma_f32_16x16x32_bf16 v[164:167], v[212:215], v[128:131], v[164:167]\n\t" \
  "ds_read_b32 v218, v218 offset:0\n\t" \
  "v_fma_f32 v207, |v207|, v231, v207 mul:4\n\t" \
  "v_fma_f32 v206, |v206|, v230, v206 mul:4\n\t" \
  "v_cvt_pk_bf16_f32 v196, v196, v197\n\t" \
  "v_mfma_f32_16x16x32_bf16 v[172:175], v[212:215], v[140:143], v[172:175]\n\t" \
  "ds_read_b64_tr_b16 v[212:213], v249 offset:30760\n\t" \
  "v_cvt_pk_bf16_f32 v197, v198, v199\n\t" \
  "v_cvt_pk_bf16_f32 v204, v204, v205\n\t" \
  "v_cvt_pk_bf16_f32 v205, v206, v207\n\t" \
  "s_setprio 0\n\t" \
  "v_exp_f16 v232, v233\n\t" \
  "v_cvt_pk_f16_f32 v224, |v162|, |v163| clamp\n\t" \
  "v_mfma_f32_16x16x32_bf16 v[40:43], v[242:245], v[236:239], v[40:43]\n\t" \
  "ds_read_b64_tr_b16 v[214:215], v249 offset:32296\n\t" \
  "v_cvt_pk_f16_f32 v234, v160, v161\n\t" \
  "v_cvt_pk_f16_f32 v226, |v170|, |v171| clamp\n\t" \
  "v_mfma_f32_16x16x32_bf16 v[44:47], v[240:243], v[236:239], v[44:47]\n\t" \
  "buffer_load_dwordx4 v[236:239], v250, s[8:11], s29 offen offset:0\n\t" \
  "s_cmp_lg_u32 s58, 0\n\t" \
  "s_cbranch_scc1 .Lcnx_c31\n\t" \
  "s_add_u32 m0, s25, 41000\n\t" \
  "s_add_u32 s24, s24, s26\n\t" \
  "buffer_load_dwordx4 v254, s[8:11], s24 offen lds\n\t" \
  "s_cmp_lg_u32 s38, 7\n\t" \
  "s_cbranch_scc1 .Lcnx_c31\n\t" \
  "s_mov_b32 m0, 163632\n\t" \
  "s_add_u32 s30, s30, s31\n\t" \
  "buffer_load_dword v252, s[8:11], s30 offen lds\n\t" \
  ".Lcnx_c31:\n\t" \
  "ds_read_b32 v221, v221 offset:0\n\t" \
  "v_cvt_pk_f16_f32 v228, |v168|, |v169| clamp\n\t" \
  "v_exp_f16_sdwa v232, v233 dst_sel:WORD_1 dst_unused:UNUSED_PRESERVE src0_sel:WORD_1\n\t" \
  "v_pk_mul_f16 v235, v234, v234\n\t" \
  "s_waitcnt lgkmcnt(5)\n\t" \
  "v_mfma_f32_16x16x32_bf16 v[0:3], v[208:211], v[178:181], v[0:3]\n\t" \
  "ds_read_b32 v220, v220 offset:0\n\t" \
  "v_and_b32_sdwa v225, s56, v224 dst_sel:DWORD dst_unused:UNUSED_PAD src0_sel:DWORD src1_sel:WORD_1\n\t" \
  "v_and_b32 v224, s56, v224\n\t" \
  "v_mfma_f32_16x16x32_bf16 v[24:27], v[208:211], v[186:189], v[24:27]\n\t" \
  "ds_read_b32 v225, v225 offset:0\n\t" \
  "v_and_b32_sdwa v227, s56, v226 dst_sel:DWORD dst_unused:UNUSED_PAD src0_sel:DWORD src1_sel:WORD_1\n\t" \
  "v_mfma_f32_16x16x32_bf16 v[48:51], v[208:211], v[194:197], v[48:51]\n\t" \
  "ds_read_b32 v224, v224 offset:0\n\t" \
  "v_and_b32 v226, s56, v226\n\t" \
  "v_and_b32_sdwa v229, s56, v228 dst_sel:DWORD dst_unused:UNUSED_PAD src0_sel:DWORD src1_sel:WORD_1\n\t" \
  "v_mfma_f32_16x16x32_bf16 v[72:75], v[208:211], v[202:205], v[72:75]\n\t" \
  "ds_read_b64_tr_b16 v[208:209], v249 offset:30816\n\t" \
  "v_and_b32 v228, s56, v228\n\t" \
  "s_waitcnt lgkmcnt(5)\n\t" \
  "v_mfma_f32_16x16x32_bf16 v[4:7], v[212:215], v[178:181], v[4:7]\n\t" \
  "ds_read_b64_tr_b16 v[210:211], v249 offset:32352\n\t" \
  "v_pk_fma_f16 v235, v235, v253, s54\n\t" \
  "v_mfma_f32_16x16x32_bf16 v[28:31], v[212:215], v[186:189], v[28:31]\n\t" \
  "ds_read_b32 v227, v227 offset:0\n\t" \
  "v_pk_mul_f16 v235, v234, v235\n\t" \
  "v_fma_f32 v147, |v147|, v217, v147 mul:4\n\t" \
  "v_fma_f32 v146, |v146|, v216, v146 mul:4\n\t" \
  "v_mfma_f32_16x16x32_bf16 v[52:55], v[212:215], v[194:197], v[52:55]\n\t" \
  "ds_read_b32 v226, v226 offset:0\n\t" \
  "v_fma_f32 v155, |v155|, v219, v155 mul:4\n\t" \
  "s_waitcnt lgkmcnt(6)\n\t" \
  "v_fma_f32 v154, |v154|, v218, v154 mul:4\n\t" \
  "v_fma_f32 v153, |v153|, v221, v153 mul:4\n\t" \
  "v_mfma_f32_16x16x32_bf16 v[76:79], v[212:215], v[202:205], v[76:79]\n\t" \
  "ds_read_b64_tr_b16 v[212:213], v249 offset:30824\n\t" \
  "v_fma_f32 v152, |v152|, v220, v152 mul:4\n\t" \
  "v_pk_fma_f16 v232, v232, s55, s55\n\t" \
  "s_waitcnt lgkmcnt(3)\n\t" \
  "v_mfma_f32_16x16x32_bf16 v[8:11], v[208:211], v[178:181], v[8:11]\n\t" \
  "ds_read_b64_tr_b16 v[214:215], v249 offset:32360\n\t" \
  "v_cvt_pk_bf16_f32 v147, v146, v147\n\t" \
  "v_cvt_pk_bf16_f32 v155, v154, v155\n\t" \
  "v_cvt_pk_bf16_f32 v154, v152, v153\n\t" \
  "v_mfma_f32_16x16x32_bf16 v[32:35], v[208:211], v[186:189], v[32:35]\n\t" \
  "ds_read_b32 v229, v229 offset:0\n\t" \
  "v_rcp_f16 v233, v232\n\t" \
  "v_exp_f16 v234, v235\n\t" \
  "v_cvt_pk_f16_f32 v216, |v148|, |v149| clamp\n\t" \
  "v_mfma_f32_16x16x32_bf16 v[56:59], v[208:211], v[194:197], v[56:59]\n\t" \
  "ds_read_b32 v228, v228 offset:0\n\t" \
  "s_setprio 1\n\t" \
  "v_cvt_pk_f16_f32 v218, |v150|, |v151| clamp\n\t" \
  "v_cvt_pk_f16_f32 v220, |v156|, |v157| clamp\n\t" \
  "v_mfma_f32_16x16x32_bf16 v[80:83], v[208:211], v[202:205], v[80:83]\n\t" \
  "ds_read_b64_tr_b16 v[208:209], v249 offset:30880\n\t" \
  "v_cvt_pk_f16_f32 v222, |v158|, |v159| clamp\n\t" \
  "v_rcp_f16_sdwa v232, v232 dst_sel:DWORD dst_unused:UNUSED_PAD src0_sel:WORD_1\n\t" \
  "v_exp_f16_sdwa v234, v235 dst_sel:WORD_1 dst_unused:UNUSED_PRESERVE src0_sel:WORD_1\n\t" \
  "s_waitcnt lgkmcnt(3)\n\t" \
  "v_mfma_f32_16x16x32_bf16 v[12:15], v[212:215], v[178:181], v[12:15]\n\t" \
  "ds_read_b64_tr_b16 v[210:211], v249 offset:32416\n\t" \
  "v_and_b32_sdwa v217, s56, v216 dst_sel:DWORD dst_unused:UNUSED_PAD src0_sel:DWORD src1_sel:WORD_1\n\t" \
  "v_and_b32 v216, s56, v216\n\t" \
  "v_mfma_f32_16x16x32_bf16 v[36:39], v[212:215], v[186:189], v[36:39]\n\t" \
  "ds_read_b32 v217, v217 offset:0\n\t" \
  "v_and_b32_sdwa v219, s56, v218 dst_sel:DWORD dst_unused:UNUSED_PAD src0_sel:DWORD src1_sel:WORD_1\n\t" \
  "v_mfma_f32_16x16x32_bf16 v[60:63], v[212:215], v[194:197], v[60:63]\n\t" \
  "ds_read_b32 v216, v216 offset:0\n\t" \
  "v_and_b32 v218, s56, v218\n\t" \
  "v_and_b32_sdwa v221, s56, v220 dst_sel:DWORD dst_unused:UNUSED_PAD src0_sel:DWORD src1_sel:WORD_1\n\t" \
  "v_mfma_f32_16x16x32_bf16 v[84:87], v[212:215], v[202:205], v[84:87]\n\t" \
  "ds_read_b64_tr_b16 v[212:213], v249 offset:30888\n\t" \
  "v_and_b32 v220, s56, v220\n\t" \
  "s_waitcnt lgkmcnt(3)\n\t" \
  "v_mfma_f32_16x16x32_bf16 v[16:19], v[208:211], v[178:181], v[16:19]\n\t" \
  "ds_read_b64_tr_b16 v[214:215], v249 offset:32424\n\t" \
  "v_and_b32_sdwa v223, s56, v222 dst_sel:DWORD dst_unused:UNUSED_PAD src0_sel:DWORD src1_sel:WORD_1\n\t" \
  "v_mfma_f32_16x16x32_bf16 v[40:43], v[208:211], v[186:189], v[40:43]\n\t" \
  "ds_read_b32 v219, v219 offset:0\n\t" \
  "v_and_b32 v222, s56, v222\n\t" \
  "v_fma_f32 v163, |v163|, v225, v163 mul:4\n\t" \
  "v_mfma_f32_16x16x32_bf16 v[64:67], v[208:211], v[194:197], v[64:67]\n\t" \
  "ds_read_b32 v218, v218 offset:0\n\t" \
  "v_fma_f32 v162, |v162|, v224, v162 mul:4\n\t" \
  "v_fma_f32 v171, |v171|, v227, v171 mul:4\n\t" \
  "v_mfma_f32_16x16x32_bf16 v[88:91], v[208:211], v[202:205], v[88:91]\n\t" \
  "ds_read_b64_tr_b16 v[208:209], v248 offset:12304\n\t" \
  "ds_read_b64_tr_b16 v[210:211], v248 offset:13328\n\t" \
  "v_fma_f32 v170, |v170|, v226, v170 mul:4\n\t" \
  "v_fma_f32 v169, |v169|, v229, v169 mul:4\n\t" \
  "s_waitcnt lgkmcnt(4)\n\t" \
  "v_mfma_f32_16x16x32_bf16 v[20:23], v[212:215], v[178:181], v[20:23]\n\t" \
  "ds_read_b32 v221, v221 offset:0\n\t" \
  "v_fma_f32 v168, |v168|, v228, v168 mul:4\n\t" \
  "v_fma_mix_f32 v144, v144, v233, 0 op_sel_hi:[0,1,0]\n\t" \
  "v_fma_mix_f32 v145, v145, v232, 0 op_sel_hi:[0,1,0]\n\t" \
  "v_mfma_f32_16x16x32_bf16 v[44:47], v[212:215], v[186:189], v[44:47]\n\t" \
  "ds_read_b32 v220, v220 offset:0\n\t" \
  "v_pk_fma_f16 v234, v234, s55, s55\n\t" \
  "v_cvt_pk_bf16_f32 v146, v144, v145\n\t" \
  "v_cvt_pk_bf16_f32 v163, v162, v163\n\t" \
  "v_mfma_f32_16x16x32_bf16 v[68:71], v[212:215], v[194:197], v[68:71]\n\t" \
  "ds_read_b32 v223, v223 offset:0\n\t" \
  "v_cvt_pk_bf16_f32 v171, v170, v171\n\t" \
  "v_cvt_pk_bf16_f32 v170, v168, v169\n\t" \
  "v_rcp_f16 v235, v234\n\t" \
  "v_mfma_f32_16x16x32_bf16 v[92:95], v[212:215], v[202:205], v[92:95]\n\t" \
  "ds_read_b64_tr_b16 v[212:213], v248 offset:15376\n\t" \
  "v_cvt_pk_f16_f32 v224, |v164|, |v165| clamp\n\t" \
  "s_setprio 2\n\t" \
  "v_cvt_pk_f16_f32 v226, |v166|, |v167| clamp\n\t" \
  "s_waitcnt lgkmcnt(4)\n\t" \
  "v_mfma_f32_16x16x32_bf16 v[176:179], v[208:211], v[96:99], 0\n\t" \
  "ds_read_b64_tr_b16 v[214:215], v248 offset:16400\n\t" \
  "v_cvt_pk_f16_f32 v228, |v172|, |v173| clamp\n\t" \
  "v_cvt_pk_f16_f32 v230, |v174|, |v175| clamp\n\t" \
  "v_rcp_f16_sdwa v234, v234 dst_sel:DWORD dst_unused:UNUSED_PAD src0_sel:WORD_1\n\t" \
  "v_mfma_f32_16x16x32_bf16 v[184:187], v[208:211], v[108:111], 0\n\t" \
  "ds_read_b32 v222, v222 offset:0\n\t" \
  "v_and_b32_sdwa v225, s56, v224 dst_sel:DWORD dst_unused:UNUSED_PAD src0_sel:DWORD src1_sel:WORD_1\n\t" \
  "v_and_b32 v224, s56, v224\n\t" \
  "v_mfma_f32_16x16x32_bf16 v[192:195], v[208:211], v[120:123], 0\n\t" \
  "ds_read_b32 v225, v225 offset:0\n\t" \
  "v_and_b32_sdwa v227, s56, v226 dst_sel:DWORD dst_unused:UNUSED_PAD src0_sel:DWORD src1_sel:WORD_1\n\t" \
  "v_mfma_f32_16x16x32_bf16 v[200:203], v[208:211], v[132:135], 0\n\t" \
  "ds_read_b64_tr_b16 v[208:209], v248 offset:14352\n\t" \
  "v_and_b32 v226, s56, v226\n\t" \
  "v_and_b32_sdwa v229, s56, v228 dst_sel:DWORD dst_unused:UNUSED_PAD src0_sel:DWORD src1_sel:WORD_1\n\t" \
  "s_waitcnt lgkmcnt(3)\n\t" \
  "v_mfma_f32_16x16x32_bf16 v[176:179], v[212:215], v[100:103], v[176:179]\n\t" \
  "ds_read_b64_tr_b16 v[210:211], v248 offset:17424\n\t" \
  "v_and_b32 v228, s56, v228\n\t" \
  "v_mfma_f32_16x16x32_bf16 v[184:187], v[212:215], v[112:115], v[184:187]\n\t" \
  "ds_read_b32 v224, v224 offset:0\n\t" \
  "v_and_b32_sdwa v231, s56, v230 dst_sel:DWORD dst_unused:UNUSED_PAD src0_sel:DWORD src1_sel:WORD_1\n\t" \
  "v_mfma_f32_16x16x32_bf16 v[192:195], v[212:215], v[124:127], v[192:195]\n\t" \
  "ds_read_b32 v227, v227 offset:0\n\t" \
  "v_and_b32 v230, s56, v230\n\t" \
  "v_fma_f32 v149, |v149|, v217, v149 mul:4\n\t" \
  "v_mfma_f32_16x16x32_bf16 v[200:203], v[212:215], v[136:139], v[200:203]\n\t" \
  "ds_read_b64_tr_b16 v[212:213], v248 offset:12816\n\t" \
  "ds_read_b64_tr_b16 v[214:215], v248 offset:13840\n\t" \
  "v_fma_f32 v148, |v148|, v216, v148 mul:4\n\t" \
  "v_fma_f32 v151, |v151|, v219, v151 mul:4\n\t" \
  "s_waitcnt lgkmcnt(4)\n\t" \
  "v_mfma_f32_16x16x32_bf16 v[176:179], v[208:211], v[104:107], v[176:179]\n\t" \
  "ds_read_b32 v226, v226 offset:0\n\t" \
  "v_fma_f32 v150, |v150|, v218, v150 mul:4\n\t" \
  "v_fma_f32 v157, |v157|, v221, v157 mul:4\n\t" \
  "v_mfma_f32_16x16x32_bf16 v[184:187], v[208:211], v[116:119], v[184:187]\n\t" \
  "ds_read_b32 v229, v229 offset:0\n\t" \
  "v_fma_f32 v156, |v156|, v220, v156 mul:4\n\t" \
  "v_fma_f32 v159, |v159|, v223, v159 mul:4\n\t" \
  "v_fma_f32 v158, |v158|, v222, v158 mul:4\n\t" \
  "v_mfma_f32_16x16x32_bf16 v[192:195], v[208:211], v[128:131], v[192:195]\n\t" \
  "ds_read_b32 v228, v228 offset:0\n\t" \
  "v_fma_mix_f32 v160, v160, v235, 0 op_sel_hi:[0,1,0]\n\t" \
  "v_fma_mix_f32 v161, v161, v234, 0 op_sel_hi:[0,1,0]\n\t" \
  "v_cvt_pk_bf16_f32 v162, v160, v161\n\t" \
  "v_mfma_f32_16x16x32_bf16 v[200:203], v[208:211], v[140:143], v[200:203]\n\t" \
  "ds_read_b64_tr_b16 v[208:209], v248 offset:15888\n\t" \
  "v_cvt_pk_bf16_f32 v148, v148, v149\n\t" \
  "v_cvt_pk_bf16_f32 v149, v150, v151\n\t" \
  "s_waitcnt lgkmcnt(4)\n\t" \
  "v_mfma_f32_16x16x32_bf16 v[180:183], v[212:215], v[96:99], 0\n\t" \
  "ds_read_b64_tr_b16 v[210:211], v248 offset:16912\n\t" \
  "v_cvt_pk_bf16_f32 v156, v156, v157\n\t" \
  "v_cvt_pk_bf16_f32 v157, v158, v159\n\t" \
  "s_setprio 3\n\t" \
  "v_cvt_pk_f16_f32 v216, |v178|, |v179| clamp\n\t" \
  "v_mfma_f32_16x16x32_bf16 v[188:191], v[212:215], v[108:111], 0\n\t" \
  "ds_read_b32 v231, v231 offset:0\n\t" \
  "v_cvt_pk_f16_f32 v232, v176, v177\n\t" \
  "v_cvt_pk_f16_f32 v218, |v186|, |v187| clamp\n\t" \
  "v_cvt_pk_f16_f32 v220, |v184|, |v185| clamp\n\t" \
  "v_mfma_f32_16x16x32_bf16 v[196:199], v[212:215], v[120:123], 0\n\t" \
  "ds_read_b32 v230, v230 offset:0\n\t" \
  "v_pk_mul_f16 v233, v232, v232\n\t" \
  "v_and_b32_sdwa v217, s56, v216 dst_sel:DWORD dst_unused:UNUSED_PAD src0_sel:DWORD src1_sel:WORD_1\n\t" \
  "v_mfma_f32_16x16x32_bf16 v[204:207], v[212:215], v[132:135], 0\n\t" \
  "ds_read_b64_tr_b16 v[212:213], v248 offset:14864\n\t" \
  "v_and_b32 v216, s56, v216\n\t" \
  "v_and_b32_sdwa v219, s56, v218 dst_sel:DWORD dst_unused:UNUSED_PAD src0_sel:DWORD src1_sel:WORD_1\n\t" \
  "s_waitcnt lgkmcnt(3)\n\t" \
  "v_mfma_f32_16x16x32_bf16 v[180:183], v[208:211], v[100:103], v[180:183]\n\t" \
  "ds_read_b64_tr_b16 v[214:215], v248 offset:17936\n\t" \
  "v_and_b32 v218, s56, v218\n\t" \
  "v_mfma_f32_16x16x32_bf16 v[188:191], v[208:211], v[112:115], v[188:191]\n\t" \
  "s_waitcnt vmcnt(1)\n\t" \
  "s_cmp_lg_u32 s58, 1\n\t" \
  "s_cbranch_scc1 .Lcnx_c19\n\t" \
  "s_waitcnt vmcnt(0)\n\t" \
  ".Lcnx_c19:\n\t" \
  "s_mov_b64 exec, 3\n\t" \
  "ds_add_rtn_u32 v246, v251, v250 offset:47712\n\t" \
  "s_mov_b64 exec, -1\n\t" \
  "ds_read_b32 v217, v217 offset:0\n\t" \
  "v_and_b32_sdwa v221, s56, v220 dst_sel:DWORD dst_unused:UNUSED_PAD src0_sel:DWORD src1_sel:WORD_1\n\t" \
  "v_mfma_f32_16x16x32_bf16 v[196:199], v[208:211], v[124:127], v[196:199]\n\t" \
  "ds_read_b32 v216, v216 offset:0\n\t" \
  "v_and_b32 v220, s56, v220\n\t" \
  "v_pk_fma_f16 v233, v233, v253, s54\n\t" \
  "v_mfma_f32_16x16x32_bf16 v[204:207], v[208:211], v[136:139], v[204:207]\n\t" \
  "s_waitcnt lgkmcnt(2)\n\t" \
  "v_readfirstlane_b32 s4, v246\n\t" \
  "s_cmp_lt_u32 s4, s57\n\t" \
  "s_cbranch_scc1 .Lcnx_w84\n.Lcnx_b84:\n\t" \
  "s_or_b32 s37, s37, s58\n\t" \
  "ds_read_b64_tr_b16 v[208:209], v249 offset:0\n\t" \
  "v_pk_mul_f16 v233, v232, v233\n\t" \
  "v_fma_f32 v165, |v165|, v225, v165 mul:4\n\t" \
  "v_mfma_f32_16x16x32_bf16 v[180:183], v[212:215], v[104:107], v[180:183]\n\t" \
  "ds_read_b64_tr_b16 v[210:211], v249 offset:1536\n\t" \
  "v_fma_f32 v164, |v164|, v224, v164 mul:4\n\t" \
  "v_fma_f32 v167, |v167|, v227, v167 mul:4\n\t" \
  "v_mfma_f32_16x16x32_bf16 v[188:191], v[212:215], v[116:119], v[188:191]\n\t" \
  "s_cmp_eq_u32 s37, 0\n\t" \
  "s_cbranch_scc1 .Lcnx_f8\n.Lcnx_g8:\n\t" \
  "ds_read_b32 v219, v219 offset:0\n\t" \
  "v_fma_f32 v166, |v166|, v226, v166 mul:4\n\t" \
  "v_fma_f32 v173, |v173|, v229, v173 mul:4\n\t" \
  "v_fma_f32 v172, |v172|, v228, v172 mul:4\n\t" \
  "v_mfma_f32_16x16x32_bf16 v[196:199], v[212:215], v[128:131], v[196:199]\n\t" \
  "ds_read_b32 v218, v218 offset:0\n\t" \
  "v_fma_f32 v175, |v175|, v231, v175 mul:4\n\t" \
  "v_fma_f32 v174, |v174|, v230, v174 mul:4\n\t" \
  "v_cvt_pk_bf16_f32 v164, v164, v165\n\t" \
  "v_mfma_f32_16x16x32_bf16 v[204:207], v[212:215], v[140:143], v[204:207]\n\t" \
  "ds_read_b64_tr_b16 v[212:213], v249 offset:8\n\t" \
  "v_cvt_pk_bf16_f32 v165, v166, v167\n\t" \
  "v_cvt_pk_bf16_f32 v172, v172, v173\n\t" \
  "v_cvt_pk_bf16_f32 v173, v174, v175\n\t" \
  "s_setprio 0\n\t" \
  "v_exp_f16 v232, v233\n\t" \
  "v_cvt_pk_f16_f32 v224, |v194|, |v195| clamp\n\t" \
  "v_mfma_f32_16x16x32_bf16 v[48:51], v[242:245], v[236:239], v[48:51]\n\t" \
  "ds_read_b64_tr_b16 v[214:215], v249 offset:1544\n\t" \
  "v_cvt_pk_f16_f32 v234, v192, v193\n\t" \
  "v_cvt_pk_f16_f32 v226, |v202|, |v203| clamp\n\t" \
  "v_mfma_f32_16x16x32_bf16 v[52:55], v[240:243], v[236:239], v[52:55]\n\t" \
  "buffer_load_dwordx4 v[236:239], v250, s[8:11], s29 offen offset:64\n\t" \
  "s_cmp_lg_u32 s58, 0\n\t" \
  "s_cbranch_scc1 .Lcnx_c32\n\t" \
  "s_add_u32 m0, s25, 49200\n\t" \
  "s_add_u32 s24, s24, s26\n\t" \
  "buffer_load_dwordx4 v254, s[8:11], s24 offen lds\n\t" \
  "s_cmp_lg_u32 s38, 4\n\t" \
  "s_cbranch_scc1 .Lcnx_c32\n\t" \
  "s_mov_b32 m0, 163632\n\t" \
  "s_add_u32 s30, s30, s31\n\t" \
  "buffer_load_dword v252, s[8:11], s30 offen lds\n\t" \
  ".Lcnx_c32:\n\t" \
  "ds_read_b32 v221, v221 offset:0\n\t" \
  "v_cvt_pk_f16_f32 v228, |v200|, |v201| clamp\n\t" \
  "v_exp_f16_sdwa v232, v233 dst_sel:WORD_1 dst_unused:UNUSED_PRESERVE src0_sel:WORD_1\n\t" \
  "v_pk_mul_f16 v235, v234, v234\n\t" \
  "s_waitcnt lgkmcnt(5)\n\t" \
  "v_mfma_f32_16x16x32_bf16 v[0:3], v[208:211], v[146:149], v[0:3]\n\t" \
  "ds_read_b32 v220, v220 offset:0\n\t" \
  "v_and_b32_sdwa v225, s56, v224 dst_sel:DWORD dst_unused:UNUSED_PAD src0_sel:DWORD src1_sel:WORD_1\n\t" \
  "v_and_b32 v224, s56, v224\n\t" \
  "v_mfma_f32_16x16x32_bf16 v[24:27], v[208:211], v[154:157], v[24:27]\n\t" \
  "ds_read_b32 v225, v225 offset:0\n\t" \
  "v_and_b32_sdwa v227, s56, v226 dst_sel:DWORD dst_unused:UNUSED_PAD src0_sel:DWORD src1_sel:WORD_1\n\t" \
  "v_mfma_f32_16x16x32_bf16 v[48:51], v[208:211], v[162:165], v[48:51]\n\t" \
  "ds_read_b32 v224, v224 offset:0\n\t" \
  "v_and_b32 v226, s56, v226\n\t" \
  "v_and_b32_sdwa v229, s56, v228 dst_sel:DWORD dst_unused:UNUSED_PAD src0_sel:DWORD src1_sel:WORD_1\n\t" \
  "v_mfma_f32_16x16x32_bf16 v[72:75], v[208:211], v[170:173], v[72:75]\n\t" \
  "ds_read_b64_tr_b16 v[208:209], v249 offset:64\n\t" \
  "v_and_b32 v228, s56, v228\n\t" \
  "s_waitcnt lgkmcnt(5)\n\t" \
  "v_mfma_f32_16x16x32_bf16 v[4:7], v[212:215], v[146:149], v[4:7]\n\t" \
  "ds_read_b64_tr_b16 v[210:211], v249 offset:1600\n\t" \
  "v_pk_fma_f16 v235, v235, v253, s54\n\t" \
  "v_mfma_f32_16x16x32_bf16 v[28:31], v[212:215], v[154:157], v[28:31]\n\t" \
  "ds_read_b32 v227, v227 offset:0\n\t" \
  "v_pk_mul_f16 v235, v234, v235\n\t" \
  "v_fma_f32 v179, |v179|, v217, v179 mul:4\n\t" \
  "v_fma_f32 v178, |v178|, v216, v178 mul:4\n\t" \
  "v_mfma_f32_16x16x32_bf16 v[52:55], v[212:215], v[162:165], v[52:55]\n\t" \
  "ds_read_b32 v226, v226 offset:0\n\t" \
  "v_fma_f32 v187, |v187|, v219, v187 mul:4\n\t" \
  "s_waitcnt lgkmcnt(6)\n\t" \
  "v_fma_f32 v186, |v186|, v218, v186 mul:4\n\t" \
  "v_fma_f32 v185, |v185|, v221, v185 mul:4\n\t" \
  "v_mfma_f32_16x16x32_bf16 v[76:79], v[212:215], v[170:173], v[76:79]\n\t" \
  "ds_read_b64_tr_b16 v[212:213], v249 offset:72\n\t" \
  "v_fma_f32 v184, |v184|, v220, v184 mul:4\n\t" \
  "v_pk_fma_f16 v232, v232, s55, s55\n\t" \
  "s_waitcnt lgkmcnt(3)\n\t" \
  "v_mfma_f32_16x16x32_bf16 v[8:11], v[208:211], v[146:149], v[8:11]\n\t" \
  "ds_read_b64_tr_b16 v[214:215], v249 offset:1608\n\t" \
  "v_cvt_pk_bf16_f32 v179, v178, v179\n\t" \
  "v_cvt_pk_bf16_f32 v187, v186, v187\n\t" \
  "v_cvt_pk_bf16_f32 v186, v184, v185\n\t" \
  "v_mfma_f32_16x16x32_bf16 v[32:35], v[208:211], v[154:157], v[32:35]\n\t" \
  "ds_read_b32 v229, v229 offset:0\n\t" \
  "v_rcp_f16 v233, v232\n\t" \
  "v_exp_f16 v234, v235\n\t" \
  "v_cvt_pk_f16_f32 v216, |v180|, |v181| clamp\n\t" \
  "v_mfma_f32_16x16x32_bf16 v[56:59], v[208:211], v[162:165], v[56:59]\n\t" \
  "ds_read_b32 v228, v228 offset:0\n\t" \
  "s_setprio 1\n\t" \
  "v_cvt_pk_f16_f32 v218, |v182|, |v183| clamp\n\t" \
  "v_cvt_pk_f16_f32 v220, |v188|, |v189| clamp\n\t" \
  "v_mfma_f32_16x16x32_bf16 v[80:83], v[208:211], v[170:173], v[80:83]\n\t" \
  "ds_read_b64_tr_b16 v[208:209], v249 offset:128\n\t" \
  "v_cvt_pk_f16_f32 v222, |v190|, |v191| clamp\n\t" \
  "v_rcp_f16_sdwa v232, v232 dst_sel:DWORD dst_unused:UNUSED_PAD src0_sel:WORD_1\n\t" \
  "v_exp_f16_sdwa v234, v235 dst_sel:WORD_1 dst_unused:UNUSED_PRESERVE src0_sel:WORD_1\n\t" \
  "s_waitcnt lgkmcnt(3)\n\t" \
  "v_mfma_f32_16x16x32_bf16 v[12:15], v[212:215], v[146:149], v[12:15]\n\t" \
  "ds_read_b64_tr_b16 v[210:211], v249 offset:1664\n\t" \
  "v_and_b32_sdwa v217, s56, v216 dst_sel:DWORD dst_unused:UNUSED_PAD src0_sel:DWORD src1_sel:WORD_1\n\t" \
  "v_and_b32 v216, s56, v216\n\t" \
  "v_mfma_f32_16x16x32_bf16 v[36:39], v[212:215], v[154:157], v[36:39]\n\t" \
  "ds_read_b32 v217, v217 offset:0\n\t" \
  "v_and_b32_sdwa v219, s56, v218 dst_sel:DWORD dst_unused:UNUSED_PAD src0_sel:DWORD src1_sel:WORD_1\n\t" \
  "v_mfma_f32_16x16x32_bf16 v[60:63], v[212:215], v[162:165], v[60:63]\n\t" \
  "ds_read_b32 v216, v216 offset:0\n\t" \
  "v_and_b32 v218, s56, v218\n\t" \
  "v_and_b32_sdwa v221, s56, v220 dst_sel:DWORD dst_unused:UNUSED_PAD src0_sel:DWORD src1_sel:WORD_1\n\t" \
  "v_mfma_f32_16x16x32_bf16 v[84:87], v[212:215], v[170:173], v[84:87]\n\t" \
  "ds_read_b64_tr_b16 v[212:213], v249 offset:136\n\t" \
  "v_and_b32 v220, s56, v220\n\t" \
  "s_waitcnt lgkmcnt(3)\n\t" \
  "v_mfma_f32_16x16x32_bf16 v[16:19], v[208:211], v[146:149], v[16:19]\n\t" \
  "ds_read_b64_tr_b16 v[214:215], v249 offset:1672\n\t" \
  "v_and_b32_sdwa v223, s56, v222 dst_sel:DWORD dst_unused:UNUSED_PAD src0_sel:DWORD src1_sel:WORD_1\n\t" \
  "v_mfma_f32_16x16x32_bf16 v[40:43], v[208:211], v[154:157], v[40:43]\n\t" \
  "ds_read_b32 v219, v219 offset:0\n\t" \
  "v_and_b32 v222, s56, v222\n\t" \
  "v_fma_f32 v195, |v195|, v225, v195 mul:4\n\t" \
  "v_mfma_f32_16x16x32_bf16 v[64:67], v[208:211], v[162:165], v[64:67]\n\t" \
  "ds_read_b32 v218, v218 offset:0\n\t" \
  "v_fma_f32 v194, |v194|, v224, v194 mul:4\n\t" \
  "v_fma_f32 v203, |v203|, v227, v203 mul:4\n\t" \
  "v_mfma_f32_16x16x32_bf16 v[88:91], v[208:211], v[170:173], v[88:91]\n\t" \
  "ds_read_b64_tr_b16 v[208:209], v248 offset:43048\n\t" \
  "ds_read_b64_tr_b16 v[210:211], v248 offset:44072\n\t" \
  "v_fma_f32 v202, |v202|, v226, v202 mul:4\n\t" \
  "v_fma_f32 v201, |v201|, v229, v201 mul:4\n\t" \
  "s_waitcnt lgkmcnt(4)\n\t" \
  "v_mfma_f32_16x16x32_bf16 v[20:23], v[212:215], v[146:149], v[20:23]\n\t" \
  "ds_read_b32 v221, v221 offset:0\n\t" \
  "v_fma_f32 v200, |v200|, v228, v200 mul:4\n\t" \
  "v_fma_mix_f32 v176, v176, v233, 0 op_sel_hi:[0,1,0]\n\t" \
  "v_fma_mix_f32 v177, v177, v232, 0 op_sel_hi:[0,1,0]\n\t" \
  "v_mfma_f32_16x16x32_bf16 v[44:47], v[212:215], v[154:157], v[44:47]\n\t" \
  "ds_read_b32 v220, v220 offset:0\n\t" \
  "v_pk_fma_f16 v234, v234, s55, s55\n\t" \
  "v_cvt_pk_bf16_f32 v178, v176, v177\n\t" \
  "v_cvt_pk_bf16_f32 v195, v194, v195\n\t" \
  "v_mfma_f32_16x16x32_bf16 v[68:71], v[212:215], v[162:165], v[68:71]\n\t" \
  "ds_read_b32 v223, v223 offset:0\n\t" \
  "v_cvt_pk_bf16_f32 v203, v202, v203\n\t" \
  "v_cvt_pk_bf16_f32 v202, v200, v201\n\t" \
  "v_rcp_f16 v235, v234\n\t" \
  "v_mfma_f32_16x16x32_bf16 v[92:95], v[212:215], v[170:173], v[92:95]\n\t" \
  "ds_read_b64_tr_b16 v[212:213], v248 offset:46120\n\t" \
  "v_cvt_pk_f16_f32 v224, |v196|, |v197| clamp\n\t" \
  "s_setprio 2\n\t" \
  "v_cvt_pk_f16_f32 v226, |v198|, |v199| clamp\n\t" \
  "s_waitcnt lgkmcnt(4)\n\t" \
  "v_mfma_f32_16x16x32_bf16 v[144:147], v[208:211], v[96:99], 0\n\t" \
  "ds_read_b64_tr_b16 v[214:215], v248 offset:47144\n\t" \
  "v_cvt_pk_f16_f32 v228, |v204|, |v205| clamp\n\t" \
  "v_cvt_pk_f16_f32 v230, |v206|, |v207| clamp\n\t" \
  "v_rcp_f16_sdwa v234, v234 dst_sel:DWORD dst_unused:UNUSED_PAD src0_sel:WORD_1\n\t" \
  "v_mfma_f32_16x16x32_bf16 v[152:155], v[208:211], v[108:111], 0\n\t" \
  "ds_read_b32 v222, v222 offset:0\n\t" \
  "v_and_b32_sdwa v225, s56, v224 dst_sel:DWORD dst_unused:UNUSED_PAD src0_sel:DWORD src1_sel:WORD_1\n\t" \
  "v_and_b32 v224, s56, v224\n\t" \
  "v_mfma_f32_16x16x32_bf16 v[160:163], v[208:211], v[120:123], 0\n\t" \
  "ds_read_b32 v225, v225 offset:0\n\t" \
  "v_and_b32_sdwa v227, s56, v226 dst_sel:DWORD dst_unused:UNUSED_PAD src0_sel:DWORD src1_sel:WORD_1\n\t" \
  "v_mfma_f32_16x16x32_bf16 v[168:171], v[208:211], v[132:135], 0\n\t" \
  "ds_read_b64_tr_b16 v[208:209], v248 offset:45096\n\t" \
  "v_and_b32 v226, s56, v226\n\t" \
  "v_and_b32_sdwa v229, s56, v228 dst_sel:DWORD dst_unused:UNUSED_PAD src0_sel:DWORD src1_sel:WORD_1\n\t" \
  "s_waitcnt lgkmcnt(3)\n\t" \
  "v_mfma_f32_16x16x32_bf16 v[144:147], v[212:215], v[100:103], v[144:147]\n\t" \
  "ds_read_b64_tr_b16 v[210:211], v248 offset:48168\n\t" \
  "v_and_b32 v228, s56, v228\n\t" \
  "v_mfma_f32_16x16x32_bf16 v[152:155], v[212:215], v[112:115], v[152:155]\n\t" \
  "ds_read_b32 v224, v224 offset:0\n\t" \
  "v_and_b32_sdwa v231, s56, v230 dst_sel:DWORD dst_unused:UNUSED_PAD src0_sel:DWORD src1_sel:WORD_1\n\t" \
  "v_mfma_f32_16x16x32_bf16 v[160:163], v[212:215], v[124:127], v[160:163]\n\t" \
  "ds_read_b32 v227, v227 offset:0\n\t" \
  "v_and_b32 v230, s56, v230\n\t" \
  "v_fma_f32 v181, |v181|, v217, v181 mul:4\n\t" \
  "v_mfma_f32_16x16x32_bf16 v[168:171], v[212:215], v[136:139], v[168:171]\n\t" \
  "ds_read_b64_tr_b16 v[212:213], v248 offset:43560\n\t" \
  "ds_read_b64_tr_b16 v[214:215], v248 offset:44584\n\t" \
  "v_fma_f32 v180, |v180|, v216, v180 mul:4\n\t" \
  "v_fma_f32 v183, |v183|, v219, v183 mul:4\n\t" \
  "s_waitcnt lgkmcnt(4)\n\t" \
  "v_mfma_f32_16x16x32_bf16 v[144:147], v[208:211], v[104:107], v[144:147]\n\t" \
  "ds_read_b32 v226, v226 offset:0\n\t" \
  "v_fma_f32 v182, |v182|, v218, v182 mul:4\n\t" \
  "v_fma_f32 v189, |v189|, v221, v189 mul:4\n\t" \
  "v_mfma_f32_16x16x32_bf16 v[152:155], v[208:211], v[116:119], v[152:155]\n\t" \
  "ds_read_b32 v229, v229 offset:0\n\t" \
  "v_fma_f32 v188, |v188|, v220, v188 mul:4\n\t" \
  "v_fma_f32 v191, |v191|, v223, v191 mul:4\n\t" \
  "v_fma_f32 v190, |v190|, v222, v190 mul:4\n\t" \
  "v_mfma_f32_16x16x32_bf16 v[160:163], v[208:211], v[128:131], v[160:163]\n\t" \
  "ds_read_b32 v228, v228 offset:0\n\t" \
  "v_fma_mix_f32 v192, v192, v235, 0 op_sel_hi:[0,1,0]\n\t" \
  "v_fma_mix_f32 v193, v193, v234, 0 op_sel_hi:[0,1,0]\n\t" \
  "v_cvt_pk_bf16_f32 v194, v192, v193\n\t" \
  "v_mfma_f32_16x16x32_bf16 v[168:171], v[208:211], v[140:143], v[168:171]\n\t" \
  "ds_read_b64_tr_b16 v[208:209], v248 offset:46632\n\t" \
  "v_cvt_pk_bf16_f32 v180, v180, v181\n\t" \
  "v_cvt_pk_bf16_f32 v181, v182, v183\n\t" \
  "s_waitcnt lgkmcnt(4)\n\t" \
  "v_mfma_f32_16x16x32_bf16 v[148:151], v[212:215], v[96:99], 0\n\t" \
  "ds_read_b64_tr_b16 v[210:211], v248 offset:47656\n\t" \
  "v_cvt_pk_bf16_f32 v188, v188, v189\n\t" \
  "v_cvt_pk_bf16_f32 v189, v190, v191\n\t" \
  "s_setprio 3\n\t" \
  "v_cvt_pk_f16_f32 v216, |v146|, |v147| clamp\n\t" \
  "v_mfma_f32_16x16x32_bf16 v[156:159], v[212:215], v[108:111], 0\n\t" \
  "ds_read_b32 v231, v231 offset:0\n\t" \
  "v_cvt_pk_f16_f32 v232, v144, v145\n\t" \
  "v_cvt_pk_f16_f32 v218, |v154|, |v155| clamp\n\t" \
  "v_cvt_pk_f16_f32 v220, |v152|, |v153| clamp\n\t" \
  "v_mfma_f32_16x16x32_bf16 v[164:167], v[212:215], v[120:123], 0\n\t" \
  "ds_read_b32 v230, v230 offset:0\n\t" \
  "v_pk_mul_f16 v233, v232, v232\n\t" \
  "v_and_b32_sdwa v217, s56, v216 dst_sel:DWORD dst_unused:UNUSED_PAD src0_sel:DWORD src1_sel:WORD_1\n\t" \
  "v_mfma_f32_16x16x32_bf16 v[172:175], v[212:215], v[132:135], 0\n\t" \
  "ds_read_b64_tr_b16 v[212:213], v248 offset:45608\n\t" \
  "v_and_b32 v216, s56, v216\n\t" \
  "v_and_b32_sdwa v219, s56, v218 dst_sel:DWORD dst_unused:UNUSED_PAD src0_sel:DWORD src1_sel:WORD_1\n\t" \
  "s_waitcnt lgkmcnt(3)\n\t" \
  "v_mfma_f32_16x16x32_bf16 v[148:151], v[208:211], v[100:103], v[148:151]\n\t" \
  "ds_read_b64_tr_b16 v[214:215], v248 offset:48680\n\t" \
  "v_and_b32 v218, s56, v218\n\t" \
  "v_mfma_f32_16x16x32_bf16 v[156:159], v[208:211], v[112:115], v[156:159]\n\t" \
  "s_waitcnt vmcnt(1)\n\t" \
  "s_cmp_lg_u32 s58, 1\n\t" \
  "s_cbranch_scc1 .Lcnx_c20\n\t" \
  "s_waitcnt vmcnt(0)\n\t" \
  ".Lcnx_c20:\n\t" \
  "s_mov_b64 exec, 3\n\t" \
  "ds_add_rtn_u32 v246, v251, v250 offset:47720\n\t" \
  "s_mov_b64 exec, -1\n\t" \
  "ds_read_b32 v217, v217 offset:0\n\t" \
  "v_and_b32_sdwa v221, s56, v220 dst_sel:DWORD dst_unused:UNUSED_PAD src0_sel:DWORD src1_sel:WORD_1\n\t" \
  "v_mfma_f32_16x16x32_bf16 v[164:167], v[208:211], v[124:127], v[164:167]\n\t" \
  "ds_read_b32 v216, v216 offset:0\n\t" \
  "v_and_b32 v220, s56, v220\n\t" \
  "v_pk_fma_f16 v233, v233, v253, s54\n\t" \
  "v_mfma_f32_16x16x32_bf16 v[172:175], v[208:211], v[136:139], v[172:175]\n\t" \
  "s_waitcnt lgkmcnt(2)\n\t" \
  "v_readfirstlane_b32 s4, v246\n\t" \
  "s_cmp_lt_u32 s4, s57\n\t" \
  "s_cbranch_scc1 .Lcnx_w96\n.Lcnx_b96:\n\t" \
  "s_cmp_lg_u32 s58, 1\n\t" \
  "s_cselect_b32 s57, s57, 0\n\t" \
  "ds_read_b64_tr_b16 v[208:209], v249 offset:6152\n\t" \
  "v_pk_mul_f16 v233, v232, v233\n\t" \
  "v_fma_f32 v197, |v197|, v225, v197 mul:4\n\t" \
  "v_mfma_f32_16x16x32_bf16 v[148:151], v[212:215], v[104:107], v[148:151]\n\t" \
  "ds_read_b64_tr_b16 v[210:211], v249 offset:7688\n\t" \
  "v_fma_f32 v196, |v196|, v224, v196 mul:4\n\t" \
  "v_fma_f32 v199, |v199|, v227, v199 mul:4\n\t" \
  "v_mfma_f32_16x16x32_bf16 v[156:159], v[212:215], v[116:119], v[156:159]\n\t" \
  "s_cmp_eq_u32 s37, 0\n\t" \
  "s_cbranch_scc1 .Lcnx_f9\n.Lcnx_g9:\n\t" \
  "ds_read_b32 v219, v219 offset:0\n\t" \
  "v_fma_f32 v198, |v198|, v226, v198 mul:4\n\t" \
  "v_fma_f32 v205, |v205|, v229, v205 mul:4\n\t" \
  "v_fma_f32 v204, |v204|, v228, v204 mul:4\n\t" \
  "v_mfma_f32_16x16x32_bf16 v[164:167], v[212:215], v[128:131], v[164:167]\n\t" \
  "ds_read_b32 v218, v218 offset:0\n\t" \
  "v_fma_f32 v207, |v207|, v231, v207 mul:4\n\t" \
  "v_fma_f32 v206, |v206|, v230, v206 mul:4\n\t" \
  "v_cvt_pk_bf16_f32 v196, v196, v197\n\t" \
  "v_mfma_f32_16x16x32_bf16 v[172:175], v[212:215], v[140:143], v[172:175]\n\t" \
  "ds_read_b64_tr_b16 v[212:213], v249 offset:6160\n\t" \
  "v_cvt_pk_bf16_f32 v197, v198, v199\n\t" \
  "v_cvt_pk_bf16_f32 v204, v204, v205\n\t" \
  "v_cvt_pk_bf16_f32 v205, v206, v207\n\t" \
  "s_cmp_lg_u32 s58, 1\n\t" \
  "s_cbranch_scc1 .Lcnx_c21\n\t" \
  "s_mov_b64 vcc, -1\n\t" \
  "s_setprio 0\n\t" \
  "s_cmp_ge_u32 s41, 2048\n\t" \
  "s_cbranch_scc1 .Lcnx_c21\n\t" \
  "s_setprio 3\n\t" \
  ".Lcnx_c21:\n\t" \
  "s_cbranch_vccnz .Lcnx_p9_0\n\ts_setprio 0\n.Lcnx_p9_0:\n\t" \
  "v_exp_f16 v232, v233\n\t" \
  "v_cvt_pk_f16_f32 v224, |v162|, |v163| clamp\n\t" \
  "v_mfma_f32_16x16x32_bf16 v[56:59], v[242:245], v[236:239], v[56:59]\n\t" \
  "ds_read_b64_tr_b16 v[214:215], v249 offset:7696\n\t" \
  "v_cvt_pk_f16_f32 v234, v160, v161\n\t" \
  "v_cvt_pk_f16_f32 v226, |v170|, |v171| clamp\n\t" \
  "v_mfma_f32_16x16x32_bf16 v[60:63], v[240:243], v[236:239], v[60:63]\n\t" \
  "buffer_load_dwordx4 v[236:239], v250, s[8:11], s29 offen offset:128\n\t" \
  "s_cmp_lg_u32 s58, 0\n\t" \
  "s_cbranch_scc1 .Lcnx_c33\n\t" \
  "s_add_u32 m0, s25, 57400\n\t" \
  "s_add_u32 s24, s24, s26\n\t" \
  "buffer_load_dwordx4 v254, s[8:11], s24 offen lds\n\t" \
  "s_cmp_lg_u32 s38, 5\n\t" \
  "s_cbranch_scc1 .Lcnx_c33\n\t" \
  "s_mov_b32 m0, 163632\n\t" \
  "s_add_u32 s30, s30, s31\n\t" \
  "buffer_load_dword v252, s[8:11], s30 offen lds\n\t" \
  ".Lcnx_c33:\n\t" \
  "ds_read_b32 v221, v221 offset:0\n\t" \
  "v_cvt_pk_f16_f32 v228, |v168|, |v169| clamp\n\t" \
  "v_exp_f16_sdwa v232, v233 dst_sel:WORD_1 dst_unused:UNUSED_PRESERVE src0_sel:WORD_1\n\t" \
  "v_pk_mul_f16 v235, v234, v234\n\t" \
  "s_waitcnt lgkmcnt(5)\n\t" \
  "v_mfma_f32_16x16x32_bf16 v[0:3], v[208:211], v[178:181], v[0:3]\n\t" \
  "ds_read_b32 v220, v220 offset:0\n\t" \
  "v_and_b32_sdwa v225, s56, v224 dst_sel:DWORD dst_unused:UNUSED_PAD src0_sel:DWORD src1_sel:WORD_1\n\t" \
  "v_and_b32 v224, s56, v224\n\t" \
  "v_mfma_f32_16x16x32_bf16 v[24:27], v[208:211], v[186:189], v[24:27]\n\t" \
  "ds_read_b32 v225, v225 offset:0\n\t" \
  "v_and_b32_sdwa v227, s56, v226 dst_sel:DWORD dst_unused:UNUSED_PAD src0_sel:DWORD src1_sel:WORD_1\n\t" \
  "v_mfma_f32_16x16x32_bf16 v[48:51], v[208:211], v[194:197], v[48:51]\n\t" \
  "ds_read_b32 v224, v224 offset:0\n\t" \
  "v_and_b32 v226, s56, v226\n\t" \
  "v_and_b32_sdwa v229, s56, v228 dst_sel:DWORD dst_unused:UNUSED_PAD src0_sel:DWORD src1_sel:WORD_1\n\t" \
  "v_mfma_f32_16x16x32_bf16 v[72:75], v[208:211], v[202:205], v[72:75]\n\t" \
  "ds_read_b64_tr_b16 v[208:209], v249 offset:6216\n\t" \
  "v_and_b32 v228, s56, v228\n\t" \
  "s_waitcnt lgkmcnt(5)\n\t" \
  "v_mfma_f32_16x16x32_bf16 v[4:7], v[212:215], v[178:181], v[4:7]\n\t" \
  "ds_read_b64_tr_b16 v[210:211], v249 offset:7752\n\t" \
  "v_pk_fma_f16 v235, v235, v253, s54\n\t" \
  "v_mfma_f32_16x16x32_bf16 v[28:31], v[212:215], v[186:189], v[28:31]\n\t" \
  "ds_read_b32 v227, v227 offset:0\n\t" \
  "v_pk_mul_f16 v235, v234, v235\n\t" \
  "v_fma_f32 v147, |v147|, v217, v147 mul:4\n\t" \
  "v_fma_f32 v146, |v146|, v216, v146 mul:4\n\t" \
  "v_mfma_f32_16x16x32_bf16 v[52:55], v[212:215], v[194:197], v[52:55]\n\t" \
  "ds_read_b32 v226, v226 offset:0\n\t" \
  "v_fma_f32 v155, |v155|, v219, v155 mul:4\n\t" \
  "s_waitcnt lgkmcnt(6)\n\t" \
  "v_fma_f32 v154, |v154|, v218, v154 mul:4\n\t" \
  "v_fma_f32 v153, |v153|, v221, v153 mul:4\n\t" \
  "v_mfma_f32_16x16x32_bf16 v[76:79], v[212:215], v[202:205], v[76:79]\n\t" \
  "ds_read_b64_tr_b16 v[212:213], v249 offset:6224\n\t" \
  "v_fma_f32 v152, |v152|, v220, v152 mul:4\n\t" \
  "v_pk_fma_f16 v232, v232, s55, s55\n\t" \
  "s_waitcnt lgkmcnt(3)\n\t" \
  "v_mfma_f32_16x16x32_bf16 v[8:11], v[208:211], v[178:181], v[8:11]\n\t" \
  "ds_read_b64_tr_b16 v[214:215], v249 offset:7760\n\t" \
  "v_cvt_pk_bf16_f32 v147, v146, v147\n\t" \
  "v_cvt_pk_bf16_f32 v155, v154, v155\n\t" \
  "v_cvt_pk_bf16_f32 v154, v152, v153\n\t" \
  "v_mfma_f32_16x16x32_bf16 v[32:35], v[208:211], v[186:189], v[32:35]\n\t" \
  "ds_read_b32 v229, v229 offset:0\n\t" \
  "v_rcp_f16 v233, v232\n\t" \
  "v_exp_f16 v234, v235\n\t" \
  "v_cvt_pk_f16_f32 v216, |v148|, |v149| clamp\n\t" \
  "v_mfma_f32_16x16x32_bf16 v[56:59], v[208:211], v[194:197], v[56:59]\n\t" \
  "ds_read_b32 v228, v228 offset:0\n\t" \
  "s_cbranch_vccnz .Lcnx_p9_1\n\ts_setprio 1\n.Lcnx_p9_1:\n\t" \
  "v_cvt_pk_f16_f32 v218, |v150|, |v151| clamp\n\t" \
  "v_cvt_pk_f16_f32 v220, |v156|, |v157| clamp\n\t" \
  "v_mfma_f32_16x16x32_bf16 v[80:83], v[208:211], v[202:205], v[80:83]\n\t" \
  "ds_read_b64_tr_b16 v[208:209], v249 offset:6280\n\t" \
  "v_cvt_pk_f16_f32 v222, |v158|, |v159| clamp\n\t" \
  "v_rcp_f16_sdwa v232, v232 dst_sel:DWORD dst_unused:UNUSED_PAD src0_sel:WORD_1\n\t" \
  "v_exp_f16_sdwa v234, v235 dst_sel:WORD_1 dst_unused:UNUSED_PRESERVE src0_sel:WORD_1\n\t" \
  "s_waitcnt lgkmcnt(3)\n\t" \
  "v_mfma_f32_16x16x32_bf16 v[12:15], v[212:215], v[178:181], v[12:15]\n\t" \
  "ds_read_b64_tr_b16 v[210:211], v249 offset:7816\n\t" \
  "v_and_b32_sdwa v217, s56, v216 dst_sel:DWORD dst_unused:UNUSED_PAD src0_sel:DWORD src1_sel:WORD_1\n\t" \
  "v_and_b32 v216, s56, v216\n\t" \
  "v_mfma_f32_16x16x32_bf16 v[36:39], v[212:215], v[186:189], v[36:39]\n\t" \
  "ds_read_b32 v217, v217 offset:0\n\t" \
  "v_and_b32_sdwa v219, s56, v218 dst_sel:DWORD dst_unused:UNUSED_PAD src0_sel:DWORD src1_sel:WORD_1\n\t" \
  "v_mfma_f32_16x16x32_bf16 v[60:63], v[212:215], v[194:197], v[60:63]\n\t" \
  "ds_read_b32 v216, v216 offset:0\n\t" \
  "v_and_b32 v218, s56, v218\n\t" \
  "v_and_b32_sdwa v221, s56, v220 dst_sel:DWORD dst_unused:UNUSED_PAD src0_sel:DWORD src1_sel:WORD_1\n\t" \
  "v_mfma_f32_16x16x32_bf16 v[84:87], v[212:215], v[202:205], v[84:87]\n\t" \
  "ds_read_b64_tr_b16 v[212:213], v249 offset:6288\n\t" \
  "v_and_b32 v220, s56, v220\n\t" \
  "s_waitcnt lgkmcnt(3)\n\t" \
  "v_mfma_f32_16x16x32_bf16 v[16:19], v[208:211], v[178:181], v[16:19]\n\t" \
  "ds_read_b64_tr_b16 v[214:215], v249 offset:7824\n\t" \
  "v_and_b32_sdwa v223, s56, v222 dst_sel:DWORD dst_unused:UNUSED_PAD src0_sel:DWORD src1_sel:WORD_1\n\t" \
  "v_mfma_f32_16x16x32_bf16 v[40:43], v[208:211], v[186:189], v[40:43]\n\t" \
  "ds_read_b32 v219, v219 offset:0\n\t" \
  "v_and_b32 v222, s56, v222\n\t" \
  "v_fma_f32 v163, |v163|, v225, v163 mul:4\n\t" \
  "v_mfma_f32_16x16x32_bf16 v[64:67], v[208:211], v[194:197], v[64:67]\n\t" \
  "ds_read_b32 v218, v218 offset:0\n\t" \
  "v_fma_f32 v162, |v162|, v224, v162 mul:4\n\t" \
  "v_fma_f32 v171, |v171|, v227, v171 mul:4\n\t" \
  "v_mfma_f32_16x16x32_bf16 v[88:91], v[208:211], v[202:205], v[88:91]\n\t" \
  "ds_read_b64_tr_b16 v[208:209], v248 offset:49192\n\t" \
  "ds_read_b64_tr_b16 v[210:211], v248 offset:50216\n\t" \
  "v_fma_f32 v170, |v170|, v226, v170 mul:4\n\t" \
  "v_fma_f32 v169, |v169|, v229, v169 mul:4\n\t" \
  "s_waitcnt lgkmcnt(4)\n\t" \
  "v_mfma_f32_16x16x32_bf16 v[20:23], v[212:215], v[178:181], v[20:23]\n\t" \
  "ds_read_b32 v221, v221 offset:0\n\t" \
  "v_fma_f32 v168, |v168|, v228, v168 mul:4\n\t" \
  "v_fma_mix_f32 v144, v144, v233, 0 op_sel_hi:[0,1,0]\n\t" \
  "v_fma_mix_f32 v145, v145, v232, 0 op_sel_hi:[0,1,0]\n\t" \
  "v_mfma_f32_16x16x32_bf16 v[44:47], v[212:215], v[186:189], v[44:47]\n\t" \
  "ds_read_b32 v220, v220 offset:0\n\t" \
  "v_pk_fma_f16 v234, v234, s55, s55\n\t" \
  "v_cvt_pk_bf16_f32 v146, v144, v145\n\t" \
  "v_cvt_pk_bf16_f32 v163, v162, v163\n\t" \
  "v_mfma_f32_16x16x32_bf16 v[68:71], v[212:215], v[194:197], v[68:71]\n\t" \
  "ds_read_b32 v223, v223 offset:0\n\t" \
  "v_cvt_pk_bf16_f32 v171, v170, v171\n\t" \
  "v_cvt_pk_bf16_f32 v170, v168, v169\n\t" \
  "v_rcp_f16 v235, v234\n\t" \
  "v_mfma_f32_16x16x32_bf16 v[92:95], v[212:215], v[202:205], v[92:95]\n\t" \
  "ds_read_b64_tr_b16 v[212:213], v248 offset:52264\n\t" \
  "v_cvt_pk_f16_f32 v224, |v164|, |v165| clamp\n\t" \
  "s_cbranch_vccnz .Lcnx_p9_2\n\ts_setprio 2\n.Lcnx_p9_2:\n\t" \
  "v_cvt_pk_f16_f32 v226, |v166|, |v167| clamp\n\t" \
  "s_waitcnt lgkmcnt(4)\n\t" \
  "v_mfma_f32_16x16x32_bf16 v[176:179], v[208:211], v[96:99], 0\n\t" \
  "ds_read_b64_tr_b16 v[214:215], v248 offset:53288\n\t" \
  "v_cvt_pk_f16_f32 v228, |v172|, |v173| clamp\n\t" \
  "v_cvt_pk_f16_f32 v230, |v174|, |v175| clamp\n\t" \
  "v_rcp_f16_sdwa v234, v234 dst_sel:DWORD dst_unused:UNUSED_PAD src0_sel:WORD_1\n\t" \
  "v_mfma_f32_16x16x32_bf16 v[184:187], v[208:211], v[108:111], 0\n\t" \
  "ds_read_b32 v222, v222 offset:0\n\t" \
  "v_and_b32_sdwa v225, s56, v224 dst_sel:DWORD dst_unused:UNUSED_PAD src0_sel:DWORD src1_sel:WORD_1\n\t" \
  "v_and_b32 v224, s56, v224\n\t" \
  "v_mfma_f32_16x16x32_bf16 v[192:195], v[208:211], v[120:123], 0\n\t" \
  "ds_read_b32 v225, v225 offset:0\n\t" \
  "v_and_b32_sdwa v227, s56, v226 dst_sel:DWORD dst_unused:UNUSED_PAD src0_sel:DWORD src1_sel:WORD_1\n\t" \
  "v_mfma_f32_16x16x32_bf16 v[200:203], v[208:211], v[132:135], 0\n\t" \
  "ds_read_b64_tr_b16 v[208:209], v248 offset:51240\n\t" \
  "v_and_b32 v226, s56, v226\n\t" \
  "v_and_b32_sdwa v229, s56, v228 dst_sel:DWORD dst_unused:UNUSED_PAD src0_sel:DWORD src1_sel:WORD_1\n\t" \
  "s_waitcnt lgkmcnt(3)\n\t" \
  "v_mfma_f32_16x16x32_bf16 v[176:179], v[212:215], v[100:103], v[176:179]\n\t" \
  "ds_read_b64_tr_b16 v[210:211], v248 offset:54312\n\t" \
  "v_and_b32 v228, s56, v228\n\t" \
  "v_mfma_f32_16x16x32_bf16 v[184:187], v[212:215], v[112:115], v[184:187]\n\t" \
  "ds_read_b32 v224, v224 offset:0\n\t" \
  "v_and_b32_sdwa v231, s56, v230 dst_sel:DWORD dst_unused:UNUSED_PAD src0_sel:DWORD src1_sel:WORD_1\n\t" \
  "v_mfma_f32_16x16x32_bf16 v[192:195], v[212:215], v[124:127], v[192:195]\n\t" \
  "ds_read_b32 v227, v227 offset:0\n\t" \
  "v_and_b32 v230, s56, v230\n\t" \
  "v_fma_f32 v149, |v149|, v217, v149 mul:4\n\t" \
  "v_mfma_f32_16x16x32_bf16 v[200:203], v[212:215], v[136:139], v[200:203]\n\t" \
  "ds_read_b64_tr_b16 v[212:213], v248 offset:49704\n\t" \
  "ds_read_b64_tr_b16 v[214:215], v248 offset:50728\n\t" \
  "v_fma_f32 v148, |v148|, v216, v148 mul:4\n\t" \
  "v_fma_f32 v151, |v151|, v219, v151 mul:4\n\t" \
  "s_waitcnt lgkmcnt(4)\n\t" \
  "v_mfma_f32_16x16x32_bf16 v[176:179], v[208:211], v[104:107], v[176:179]\n\t" \
  "ds_read_b32 v226, v226 offset:0\n\t" \
  "v_fma_f32 v150, |v150|, v218, v150 mul:4\n\t" \
  "v_fma_f32 v157, |v157|, v221, v157 mul:4\n\t" \
  "v_mfma_f32_16x16x32_bf16 v[184:187], v[208:211], v[116:119], v[184:187]\n\t" \
  "ds_read_b32 v229, v229 offset:0\n\t" \
  "v_fma_f32 v156, |v156|, v220, v156 mul:4\n\t" \
  "v_fma_f32 v159, |v159|, v223, v159 mul:4\n\t" \
  "v_fma_f32 v158, |v158|, v222, v158 mul:4\n\t" \
  "v_mfma_f32_16x16x32_bf16 v[192:195], v[208:211], v[128:131], v[192:195]\n\t" \
  "ds_read_b32 v228, v228 offset:0\n\t" \
  "v_fma_mix_f32 v160, v160, v235, 0 op_sel_hi:[0,1,0]\n\t" \
  "v_fma_mix_f32 v161, v161, v234, 0 op_sel_hi:[0,1,0]\n\t" \
  "v_cvt_pk_bf16_f32 v162, v160, v161\n\t" \
  "v_mfma_f32_16x16x32_bf16 v[200:203], v[208:211], v[140:143], v[200:203]\n\t" \
  "ds_read_b64_tr_b16 v[208:209], v248 offset:52776\n\t" \
  "v_cvt_pk_bf16_f32 v148, v148, v149\n\t" \
  "v_cvt_pk_bf16_f32 v149, v150, v151\n\t" \
  "s_waitcnt lgkmcnt(4)\n\t" \
  "v_mfma_f32_16x16x32_bf16 v[180:183], v[212:215], v[96:99], 0\n\t" \
  "ds_read_b64_tr_b16 v[210:211], v248 offset:53800\n\t" \
  "v_cvt_pk_bf16_f32 v156, v156, v157\n\t" \
  "v_cvt_pk_bf16_f32 v157, v158, v159\n\t" \
  "s_cbranch_vccnz .Lcnx_p9_3\n\ts_setprio 3\n.Lcnx_p9_3:\n\t" \
  "v_cvt_pk_f16_f32 v216, |v178|, |v179| clamp\n\t" \
  "v_mfma_f32_16x16x32_bf16 v[188:191], v[212:215], v[108:111], 0\n\t" \
  "ds_read_b32 v231, v231 offset:0\n\t" \
  "v_cvt_pk_f16_f32 v232, v176, v177\n\t" \
  "v_cvt_pk_f16_f32 v218, |v186|, |v187| clamp\n\t" \
  "v_cvt_pk_f16_f32 v220, |v184|, |v185| clamp\n\t" \
  "v_mfma_f32_16x16x32_bf16 v[196:199], v[212:215], v[120:123], 0\n\t" \
  "ds_read_b32 v230, v230 offset:0\n\t" \
  "v_pk_mul_f16 v233, v232, v232\n\t" \
  "v_and_b32_sdwa v217, s56, v216 dst_sel:DWORD dst_unused:UNUSED_PAD src0_sel:DWORD src1_sel:WORD_1\n\t" \
  "v_mfma_f32_16x16x32_bf16 v[204:207], v[212:215], v[132:135], 0\n\t" \
  "ds_read_b64_tr_b16 v[212:213], v248 offset:51752\n\t" \
  "v_and_b32 v216, s56, v216\n\t" \
  "v_and_b32_sdwa v219, s56, v218 dst_sel:DWORD dst_unused:UNUSED_PAD src0_sel:DWORD src1_sel:WORD_1\n\t" \
  "s_waitcnt lgkmcnt(3)\n\t" \
  "v_mfma_f32_16x16x32_bf16 v[180:183], v[208:211], v[100:103], v[180:183]\n\t" \
  "ds_read_b64_tr_b16 v[214:215], v248 offset:54824\n\t" \
  "v_and_b32 v218, s56, v218\n\t" \
  "v_mfma_f32_16x16x32_bf16 v[188:191], v[208:211], v[112:115], v[188:191]\n\t" \
  "s_waitcnt vmcnt(1)\n\t" \
  "s_cmp_lg_u32 s58, 1\n\t" \
  "s_cbranch_scc1 .Lcnx_c22\n\t" \
  "s_waitcnt vmcnt(0)\n\t" \
  ".Lcnx_c22:\n\t" \
  "s_mov_b64 exec, 3\n\t" \
  "ds_add_rtn_u32 v246, v251, v250 offset:47728\n\t" \
  "s_mov_b64 exec, -1\n\t" \
  "ds_read_b32 v217, v217 offset:0\n\t" \
  "v_and_b32_sdwa v221, s56, v220 dst_sel:DWORD dst_unused:UNUSED_PAD src0_sel:DWORD src1_sel:WORD_1\n\t" \
  "v_mfma_f32_16x16x32_bf16 v[196:199], v[208:211], v[124:127], v[196:199]\n\t" \
  "ds_read_b32 v216, v216 offset:0\n\t" \
  "v_and_b32 v220, s56, v220\n\t" \
  "v_pk_fma_f16 v233, v233, v253, s54\n\t" \
  "v_mfma_f32_16x16x32_bf16 v[204:207], v[208:211], v[136:139], v[204:207]\n\t" \
  "s_waitcnt lgkmcnt(2)\n\t" \
  "v_readfirstlane_b32 s4, v246\n\t" \
  "s_cmp_lt_u32 s4, s57\n\t" \
  "s_cbranch_scc1 .Lcnx_w108\n.Lcnx_b108:\n\t" \
  "v_add_u32 v248, s59, v248\n\t" \
  "v_add_u32 v249, s60, v249\n\t" \
  "ds_read_b64_tr_b16 v[208:209], v249 offset:24600\n\t" \
  "v_pk_mul_f16 v233, v232, v233\n\t" \
  "v_fma_f32 v165, |v165|, v225, v165 mul:4\n\t" \
  "v_mfma_f32_16x16x32_bf16 v[180:183], v[212:215], v[104:107], v[180:183]\n\t" \
  "ds_read_b64_tr_b16 v[210:211], v249 offset:26136\n\t" \
  "v_fma_f32 v164, |v164|, v224, v164 mul:4\n\t" \
  "v_fma_f32 v167, |v167|, v227, v167 mul:4\n\t" \
  "v_mfma_f32_16x16x32_bf16 v[188:191], v[212:215], v[116:119], v[188:191]\n\t" \
  "s_cmp_eq_u32 s37, 0\n\t" \
  "s_cbranch_scc1 .Lcnx_f10\n.Lcnx_g10:\n\t" \
  "ds_read_b32 v219, v219 offset:0\n\t" \
  "v_fma_f32 v166, |v166|, v226, v166 mul:4\n\t" \
  "v_fma_f32 v173, |v173|, v229, v173 mul:4\n\t" \
  "v_fma_f32 v172, |v172|, v228, v172 mul:4\n\t" \
  "v_mfma_f32_16x16x32_bf16 v[196:199], v[212:215], v[128:131], v[196:199]\n\t" \
  "ds_read_b32 v218, v218 offset:0\n\t" \
  "v_fma_f32 v175, |v175|, v231, v175 mul:4\n\t" \
  "v_fma_f32 v174, |v174|, v230, v174 mul:4\n\t" \
  "v_cvt_pk_bf16_f32 v164, v164, v165\n\t" \
  "v_mfma_f32_16x16x32_bf16 v[204:207], v[212:215], v[140:143], v[204:207]\n\t" \
  "ds_read_b64_tr_b16 v[212:213], v249 offset:24608\n\t" \
  "v_cvt_pk_bf16_f32 v165, v166, v167\n\t" \
  "v_cvt_pk_bf16_f32 v172, v172, v173\n\t" \
  "v_cvt_pk_bf16_f32 v173, v174, v175\n\t" \
  "s_cbranch_vccnz .Lcnx_p10_0\n\ts_setprio 0\n.Lcnx_p10_0:\n\t" \
  "v_exp_f16 v232, v233\n\t" \
  "v_cvt_pk_f16_f32 v224, |v194|, |v195| clamp\n\t" \
  "v_mfma_f32_16x16x32_bf16 v[64:67], v[242:245], v[236:239], v[64:67]\n\t" \
  "ds_read_b64_tr_b16 v[214:215], v249 offset:26144\n\t" \
  "v_cvt_pk_f16_f32 v234, v192, v193\n\t" \
  "v_cvt_pk_f16_f32 v226, |v202|, |v203| clamp\n\t" \
  "v_mfma_f32_16x16x32_bf16 v[68:71], v[240:243], v[236:239], v[68:71]\n\t" \
  "buffer_load_dwordx4 v[236:239], v250, s[8:11], s29 offen offset:192\n\t" \
  "s_cmp_lg_u32 s58, 0\n\t" \
  "s_cbranch_scc1 .Lcnx_c34\n\t" \
  "s_add_u32 m0, s25, 65600\n\t" \
  "s_add_u32 s24, s24, s26\n\t" \
  "buffer_load_dwordx4 v254, s[8:11], s24 offen lds\n\t" \
  "s_cmp_lg_u32 s38, 6\n\t" \
  "s_cbranch_scc1 .Lcnx_c34\n\t" \
  "s_mov_b32 m0, 163632\n\t" \
  "s_add_u32 s30, s30, s31\n\t" \
  "buffer_load_dword v252, s[8:11], s30 offen lds\n\t" \
  ".Lcnx_c34:\n\t" \
  "ds_read_b32 v221, v221 offset:0\n\t" \
  "v_cvt_pk_f16_f32 v228, |v200|, |v201| clamp\n\t" \
  "v_exp_f16_sdwa v232, v233 dst_sel:WORD_1 dst_unused:UNUSED_PRESERVE src0_sel:WORD_1\n\t" \
  "v_pk_mul_f16 v235, v234, v234\n\t" \
  "s_waitcnt lgkmcnt(5)\n\t" \
  "v_mfma_f32_16x16x32_bf16 v[0:3], v[208:211], v[146:149], v[0:3]\n\t" \
  "ds_read_b32 v220, v220 offset:0\n\t" \
  "v_and_b32_sdwa v225, s56, v224 dst_sel:DWORD dst_unused:UNUSED_PAD src0_sel:DWORD src1_sel:WORD_1\n\t" \
  "v_and_b32 v224, s56, v224\n\t" \
  "v_mfma_f32_16x16x32_bf16 v[24:27], v[208:211], v[154:157], v[24:27]\n\t" \
  "ds_read_b32 v225, v225 offset:0\n\t" \
  "v_and_b32_sdwa v227, s56, v226 dst_sel:DWORD dst_unused:UNUSED_PAD src0_sel:DWORD src1_sel:WORD_1\n\t" \
  "v_mfma_f32_16x16x32_bf16 v[48:51], v[208:211], v[162:165], v[48:51]\n\t" \
  "ds_read_b32 v224, v224 offset:0\n\t" \
  "v_and_b32 v226, s56, v226\n\t" \
  "v_and_b32_sdwa v229, s56, v228 dst_sel:DWORD dst_unused:UNUSED_PAD src0_sel:DWORD src1_sel:WORD_1\n\t" \
  "v_mfma_f32_16x16x32_bf16 v[72:75], v[208:211], v[170:173], v[72:75]\n\t" \
  "ds_read_b64_tr_b16 v[208:209], v249 offset:24664\n\t" \
  "v_and_b32 v228, s56, v228\n\t" \
  "s_waitcnt lgkmcnt(5)\n\t" \
  "v_mfma_f32_16x16x32_bf16 v[4:7], v[212:215], v[146:149], v[4:7]\n\t" \
  "ds_read_b64_tr_b16 v[210:211], v249 offset:26200\n\t" \
  "v_pk_fma_f16 v235, v235, v253, s54\n\t" \
  "v_mfma_f32_16x16x32_bf16 v[28:31], v[212:215], v[154:157], v[28:31]\n\t" \
  "ds_read_b32 v227, v227 offset:0\n\t" \
  "v_pk_mul_f16 v235, v234, v235\n\t" \
  "v_fma_f32 v179, |v179|, v217, v179 mul:4\n\t" \
  "v_fma_f32 v178, |v178|, v216, v178 mul:4\n\t" \
  "v_mfma_f32_16x16x32_bf16 v[52:55], v[212:215], v[162:165], v[52:55]\n\t" \
  "ds_read_b32 v226, v226 offset:0\n\t" \
  "v_fma_f32 v187, |v187|, v219, v187 mul:4\n\t" \
  "s_waitcnt lgkmcnt(6)\n\t" \
  "v_fma_f32 v186, |v186|, v218, v186 mul:4\n\t" \
  "v_fma_f32 v185, |v185|, v221, v185 mul:4\n\t" \
  "v_mfma_f32_16x16x32_bf16 v[76:79], v[212:215], v[170:173], v[76:79]\n\t" \
  "ds_read_b64_tr_b16 v[212:213], v249 offset:24672\n\t" \
  "v_fma_f32 v184, |v184|, v220, v184 mul:4\n\t" \
  "v_pk_fma_f16 v232, v232, s55, s55\n\t" \
  "s_waitcnt lgkmcnt(3)\n\t" \
  "v_mfma_f32_16x16x32_bf16 v[8:11], v[208:211], v[146:149], v[8:11]\n\t" \
  "ds_read_b64_tr_b16 v[214:215], v249 offset:26208\n\t" \
  "v_cvt_pk_bf16_f32 v179, v178, v179\n\t" \
  "v_cvt_pk_bf16_f32 v187, v186, v187\n\t" \
  "v_cvt_pk_bf16_f32 v186, v184, v185\n\t" \
  "v_mfma_f32_16x16x32_bf16 v[32:35], v[208:211], v[154:157], v[32:35]\n\t" \
  "ds_read_b32 v229, v229 offset:0\n\t" \
  "v_rcp_f16 v233, v232\n\t" \
  "v_exp_f16 v234, v235\n\t" \
  "v_cvt_pk_f16_f32 v216, |v180|, |v181| clamp\n\t" \
  "v_mfma_f32_16x16x32_bf16 v[56:59], v[208:211], v[162:165], v[56:59]\n\t" \
  "ds_read_b32 v228, v228 offset:0\n\t" \
  "s_cbranch_vccnz .Lcnx_p10_1\n\ts_setprio 1\n.Lcnx_p10_1:\n\t" \
  "v_cvt_pk_f16_f32 v218, |v182|, |v183| clamp\n\t" \
  "v_cvt_pk_f16_f32 v220, |v188|, |v189| clamp\n\t" \
  "v_mfma_f32_16x16x32_bf16 v[80:83], v[208:211], v[170:173], v[80:83]\n\t" \
  "ds_read_b64_tr_b16 v[208:209], v249 offset:24728\n\t" \
  "v_cvt_pk_f16_f32 v222, |v190|, |v191| clamp\n\t" \
  "v_rcp_f16_sdwa v232, v232 dst_sel:DWORD dst_unused:UNUSED_PAD src0_sel:WORD_1\n\t" \
  "v_exp_f16_sdwa v234, v235 dst_sel:WORD_1 dst_unused:UNUSED_PRESERVE src0_sel:WORD_1\n\t" \
  "s_waitcnt lgkmcnt(3)\n\t" \
  "v_mfma_f32_16x16x32_bf16 v[12:15], v[212:215], v[146:149], v[12:15]\n\t" \
  "ds_read_b64_tr_b16 v[210:211], v249 offset:26264\n\t" \
  "v_and_b32_sdwa v217, s56, v216 dst_sel:DWORD dst_unused:UNUSED_PAD src0_sel:DWORD src1_sel:WORD_1\n\t" \
  "v_and_b32 v216, s56, v216\n\t" \
  "v_mfma_f32_16x16x32_bf16 v[36:39], v[212:215], v[154:157], v[36:39]\n\t" \
  "ds_read_b32 v217, v217 offset:0\n\t" \
  "v_and_b32_sdwa v219, s56, v218 dst_sel:DWORD dst_unused:UNUSED_PAD src0_sel:DWORD src1_sel:WORD_1\n\t" \
  "v_mfma_f32_16x16x32_bf16 v[60:63], v[212:215], v[162:165], v[60:63]\n\t" \
  "ds_read_b32 v216, v216 offset:0\n\t" \
  "v_and_b32 v218, s56, v218\n\t" \
  "v_and_b32_sdwa v221, s56, v220 dst_sel:DWORD dst_unused:UNUSED_PAD src0_sel:DWORD src1_sel:WORD_1\n\t" \
  "v_mfma_f32_16x16x32_bf16 v[84:87], v[212:215], v[170:173], v[84:87]\n\t" \
  "ds_read_b64_tr_b16 v[212:213], v249 offset:24736\n\t" \
  "v_and_b32 v220, s56, v220\n\t" \
  "s_waitcnt lgkmcnt(3)\n\t" \
  "v_mfma_f32_16x16x32_bf16 v[16:19], v[208:211], v[146:149], v[16:19]\n\t" \
  "ds_read_b64_tr_b16 v[214:215], v249 offset:26272\n\t" \
  "v_and_b32_sdwa v223, s56, v222 dst_sel:DWORD dst_unused:UNUSED_PAD src0_sel:DWORD src1_sel:WORD_1\n\t" \
  "v_mfma_f32_16x16x32_bf16 v[40:43], v[208:211], v[154:157], v[40:43]\n\t" \
  "ds_read_b32 v219, v219 offset:0\n\t" \
  "v_and_b32 v222, s56, v222\n\t" \
  "v_fma_f32 v195, |v195|, v225, v195 mul:4\n\t" \
  "v_mfma_f32_16x16x32_bf16 v[64:67], v[208:211], v[162:165], v[64:67]\n\t" \
  "ds_read_b32 v218, v218 offset:0\n\t" \
  "v_fma_f32 v194, |v194|, v224, v194 mul:4\n\t" \
  "v_fma_f32 v203, |v203|, v227, v203 mul:4\n\t" \
  "v_mfma_f32_16x16x32_bf16 v[88:91], v[208:211], v[170:173], v[88:91]\n\t" \
  "ds_read_b64_tr_b16 v[208:209], v248 offset:36904\n\t" \
  "ds_read_b64_tr_b16 v[210:211], v248 offset:37928\n\t" \
  "v_fma_f32 v202, |v202|, v226, v202 mul:4\n\t" \
  "v_fma_f32 v201, |v201|, v229, v201 mul:4\n\t" \
  "s_waitcnt lgkmcnt(4)\n\t" \
  "v_mfma_f32_16x16x32_bf16 v[20:23], v[212:215], v[146:149], v[20:23]\n\t" \
  "ds_read_b32 v221, v221 offset:0\n\t" \
  "v_fma_f32 v200, |v200|, v228, v200 mul:4\n\t" \
  "v_fma_mix_f32 v176, v176, v233, 0 op_sel_hi:[0,1,0]\n\t" \
  "v_fma_mix_f32 v177, v177, v232, 0 op_sel_hi:[0,1,0]\n\t" \
  "v_mfma_f32_16x16x32_bf16 v[44:47], v[212:215], v[154:157], v[44:47]\n\t" \
  "ds_read_b32 v220, v220 offset:0\n\t" \
  "v_pk_fma_f16 v234, v234, s55, s55\n\t" \
  "v_cvt_pk_bf16_f32 v178, v176, v177\n\t" \
  "v_cvt_pk_bf16_f32 v195, v194, v195\n\t" \
  "v_mfma_f32_16x16x32_bf16 v[68:71], v[212:215], v[162:165], v[68:71]\n\t" \
  "ds_read_b32 v223, v223 offset:0\n\t" \
  "v_cvt_pk_bf16_f32 v203, v202, v203\n\t" \
  "v_cvt_pk_bf16_f32 v202, v200, v201\n\t" \
  "v_rcp_f16 v235, v234\n\t" \
  "v_mfma_f32_16x16x32_bf16 v[92:95], v[212:215], v[170:173], v[92:95]\n\t" \
  "ds_read_b64_tr_b16 v[212:213], v248 offset:39976\n\t" \
  "v_cvt_pk_f16_f32 v224, |v196|, |v197| clamp\n\t" \
  "s_cbranch_vccnz .Lcnx_p10_2\n\ts_setprio 2\n.Lcnx_p10_2:\n\t" \
  "v_cvt_pk_f16_f32 v226, |v198|, |v199| clamp\n\t" \
  "s_waitcnt lgkmcnt(4)\n\t" \
  "v_mfma_f32_16x16x32_bf16 v[144:147], v[208:211], v[96:99], 0\n\t" \
  "ds_read_b64_tr_b16 v[214:215], v248 offset:41000\n\t" \
  "v_cvt_pk_f16_f32 v228, |v204|, |v205| clamp\n\t" \
  "v_cvt_pk_f16_f32 v230, |v206|, |v207| clamp\n\t" \
  "v_rcp_f16_sdwa v234, v234 dst_sel:DWORD dst_unused:UNUSED_PAD src0_sel:WORD_1\n\t" \
  "v_mfma_f32_16x16x32_bf16 v[152:155], v[208:211], v[108:111], 0\n\t" \
  "ds_read_b32 v222, v222 offset:0\n\t" \
  "v_and_b32_sdwa v225, s56, v224 dst_sel:DWORD dst_unused:UNUSED_PAD src0_sel:DWORD src1_sel:WORD_1\n\t" \
  "v_and_b32 v224, s56, v224\n\t" \
  "v_mfma_f32_16x16x32_bf16 v[160:163], v[208:211], v[120:123], 0\n\t" \
  "ds_read_b32 v225, v225 offset:0\n\t" \
  "v_and_b32_sdwa v227, s56, v226 dst_sel:DWORD dst_unused:UNUSED_PAD src0_sel:DWORD src1_sel:WORD_1\n\t" \
  "v_mfma_f32_16x16x32_bf16 v[168:171], v[208:211], v[132:135], 0\n\t" \
  "ds_read_b64_tr_b16 v[208:209], v248 offset:38952\n\t" \
  "v_and_b32 v226, s56, v226\n\t" \
  "v_and_b32_sdwa v229, s56, v228 dst_sel:DWORD dst_unused:UNUSED_PAD src0_sel:DWORD src1_sel:WORD_1\n\t" \
  "s_waitcnt lgkmcnt(3)\n\t" \
  "v_mfma_f32_16x16x32_bf16 v[144:147], v[212:215], v[100:103], v[144:147]\n\t" \
  "ds_read_b64_tr_b16 v[210:211], v248 offset:42024\n\t" \
  "v_and_b32 v228, s56, v228\n\t" \
  "v_mfma_f32_16x16x32_bf16 v[152:155], v[212:215], v[112:115], v[152:155]\n\t" \
  "ds_read_b32 v224, v224 offset:0\n\t" \
  "v_and_b32_sdwa v231, s56, v230 dst_sel:DWORD dst_unused:UNUSED_PAD src0_sel:DWORD src1_sel:WORD_1\n\t" \
  "v_mfma_f32_16x16x32_bf16 v[160:163], v[212:215], v[124:127], v[160:163]\n\t" \
  "ds_read_b32 v227, v227 offset:0\n\t" \
  "v_and_b32 v230, s56, v230\n\t" \
  "v_fma_f32 v181, |v181|, v217, v181 mul:4\n\t" \
  "v_mfma_f32_16x16x32_bf16 v[168:171], v[212:215], v[136:139], v[168:171]\n\t" \
  "ds_read_b64_tr_b16 v[212:213], v248 offset:37416\n\t" \
  "ds_read_b64_tr_b16 v[214:215], v248 offset:38440\n\t" \
  "v_fma_f32 v180, |v180|, v216, v180 mul:4\n\t" \
  "v_fma_f32 v183, |v183|, v219, v183 mul:4\n\t" \
  "s_waitcnt lgkmcnt(4)\n\t" \
  "v_mfma_f32_16x16x32_bf16 v[144:147], v[208:211], v[104:107], v[144:147]\n\t" \
  "ds_read_b32 v226, v226 offset:0\n\t" \
  "v_fma_f32 v182, |v182|, v218, v182 mul:4\n\t" \
  "v_fma_f32 v189, |v189|, v221, v189 mul:4\n\t" \
  "v_mfma_f32_16x16x32_bf16 v[152:155], v[208:211], v[116:119], v[152:155]\n\t" \
  "ds_read_b32 v229, v229 offset:0\n\t" \
  "v_fma_f32 v188, |v188|, v220, v188 mul:4\n\t" \
  "v_fma_f32 v191, |v191|, v223, v191 mul:4\n\t" \
  "v_fma_f32 v190, |v190|, v222, v190 mul:4\n\t" \
  "v_mfma_f32_16x16x32_bf16 v[160:163], v[208:211], v[128:131], v[160:163]\n\t" \
  "ds_read_b32 v228, v228 offset:0\n\t" \
  "v_fma_mix_f32 v192, v192, v235, 0 op_sel_hi:[0,1,0]\n\t" \
  "v_fma_mix_f32 v193, v193, v234, 0 op_sel_hi:[0,1,0]\n\t" \
  "v_cvt_pk_bf16_f32 v194, v192, v193\n\t" \
  "v_mfma_f32_16x16x32_bf16 v[168:171], v[208:211], v[140:143], v[168:171]\n\t" \
  "ds_read_b64_tr_b16 v[208:209], v248 offset:40488\n\t" \
  "v_cvt_pk_bf16_f32 v180, v180, v181\n\t" \
  "v_cvt_pk_bf16_f32 v181, v182, v183\n\t" \
  "s_waitcnt lgkmcnt(4)\n\t" \
  "v_mfma_f32_16x16x32_bf16 v[148:151], v[212:215], v[96:99], 0\n\t" \
  "ds_read_b64_tr_b16 v[210:211], v248 offset:41512\n\t" \
  "v_cvt_pk_bf16_f32 v188, v188, v189\n\t" \
  "v_cvt_pk_bf16_f32 v189, v190, v191\n\t" \
  "s_cbranch_vccnz .Lcnx_p10_3\n\ts_setprio 3\n.Lcnx_p10_3:\n\t" \
  "v_cvt_pk_f16_f32 v216, |v146|, |v147| clamp\n\t" \
  "v_mfma_f32_16x16x32_bf16 v[156:159], v[212:215], v[108:111], 0\n\t" \
  "ds_read_b32 v231, v231 offset:0\n\t" \
  "v_cvt_pk_f16_f32 v232, v144, v145\n\t" \
  "v_cvt_pk_f16_f32 v218, |v154|, |v155| clamp\n\t" \
  "v_cvt_pk_f16_f32 v220, |v152|, |v153| clamp\n\t" \
  "v_mfma_f32_16x16x32_bf16 v[164:167], v[212:215], v[120:123], 0\n\t" \
  "ds_read_b32 v230, v230 offset:0\n\t" \
  "v_pk_mul_f16 v233, v232, v232\n\t" \
  "v_and_b32_sdwa v217, s56, v216 dst_sel:DWORD dst_unused:UNUSED_PAD src0_sel:DWORD src1_sel:WORD_1\n\t" \
  "v_mfma_f32_16x16x32_bf16 v[172:175], v[212:215], v[132:135], 0\n\t" \
  "ds_read_b64_tr_b16 v[212:213], v248 offset:39464\n\t" \
  "v_and_b32 v216, s56, v216\n\t" \
  "v_and_b32_sdwa v219, s56, v218 dst_sel:DWORD dst_unused:UNUSED_PAD src0_sel:DWORD src1_sel:WORD_1\n\t" \
  "s_waitcnt lgkmcnt(3)\n\t" \
  "v_mfma_f32_16x16x32_bf16 v[148:151], v[208:211], v[100:103], v[148:151]\n\t" \
  "ds_read_b64_tr_b16 v[214:215], v248 offset:42536\n\t" \
  "v_and_b32 v218, s56, v218\n\t" \
  "v_mfma_f32_16x16x32_bf16 v[156:159], v[208:211], v[112:115], v[156:159]\n\t" \
  "s_waitcnt vmcnt(1)\n\t" \
  "s_cmp_lg_u32 s58, 1\n\t" \
  "s_cbranch_scc1 .Lcnx_c23\n\t" \
  "s_waitcnt vmcnt(0)\n\t" \
  ".Lcnx_c23:\n\t" \
  "s_mov_b64 exec, 3\n\t" \
  "ds_add_rtn_u32 v246, v251, v250 offset:47736\n\t" \
  "s_mov_b64 exec, -1\n\t" \
  "ds_read_b32 v217, v217 offset:0\n\t" \
  "v_and_b32_sdwa v221, s56, v220 dst_sel:DWORD dst_unused:UNUSED_PAD src0_sel:DWORD src1_sel:WORD_1\n\t" \
  "v_mfma_f32_16x16x32_bf16 v[164:167], v[208:211], v[124:127], v[164:167]\n\t" \
  "ds_read_b32 v216, v216 offset:0\n\t" \
  "v_and_b32 v220, s56, v220\n\t" \
  "v_pk_fma_f16 v233, v233, v253, s54\n\t" \
  "v_mfma_f32_16x16x32_bf16 v[172:175], v[208:211], v[136:139], v[172:175]\n\t" \
  "s_waitcnt lgkmcnt(2)\n\t" \
  "v_readfirstlane_b32 s4, v246\n\t" \
  "s_cmp_lt_u32 s4, s57\n\t" \
  "s_cbranch_scc1 .Lcnx_w120\n.Lcnx_b120:\n\t" \
  "ds_read_b64_tr_b16 v[208:209], v249 offset:30752\n\t" \
  "v_pk_mul_f16 v233, v232, v233\n\t" \
  "v_fma_f32 v197, |v197|, v225, v197 mul:4\n\t" \
  "v_mfma_f32_16x16x32_bf16 v[148:151], v[212:215], v[104:107], v[148:151]\n\t" \
  "ds_read_b64_tr_b16 v[210:211], v249 offset:32288\n\t" \
  "v_fma_f32 v196, |v196|, v224, v196 mul:4\n\t" \
  "v_fma_f32 v199, |v199|, v227, v199 mul:4\n\t" \
  "v_mfma_f32_16x16x32_bf16 v[156:159], v[212:215], v[116:119], v[156:159]\n\t" \
  "s_cmp_eq_u32 s37, 0\n\t" \
  "s_cbranch_scc1 .Lcnx_f11\n.Lcnx_g11:\n\t" \
  "ds_read_b32 v219, v219 offset:0\n\t" \
  "v_fma_f32 v198, |v198|, v226, v198 mul:4\n\t" \
  "v_fma_f32 v205, |v205|, v229, v205 mul:4\n\t" \
  "v_fma_f32 v204, |v204|, v228, v204 mul:4\n\t" \
  "v_mfma_f32_16x16x32_bf16 v[164:167], v[212:215], v[128:131], v[164:167]\n\t" \
  "ds_read_b32 v218, v218 offset:0\n\t" \
  "v_fma_f32 v207, |v207|, v231, v207 mul:4\n\t" \
  "v_fma_f32 v206, |v206|, v230, v206 mul:4\n\t" \
  "v_cvt_pk_bf16_f32 v196, v196, v197\n\t" \
  "v_mfma_f32_16x16x32_bf16 v[172:175], v[212:215], v[140:143], v[172:175]\n\t" \
  "ds_read_b64_tr_b16 v[212:213], v249 offset:30760\n\t" \
  "v_cvt_pk_bf16_f32 v197, v198, v199\n\t" \
  "v_cvt_pk_bf16_f32 v204, v204, v205\n\t" \
  "v_cvt_pk_bf16_f32 v205, v206, v207\n\t" \
  "s_cbranch_vccnz .Lcnx_p11_0\n\ts_setprio 0\n.Lcnx_p11_0:\n\t" \
  "v_exp_f16 v232, v233\n\t" \
  "v_cvt_pk_f16_f32 v224, |v162|, |v163| clamp\n\t" \
  "v_mfma_f32_16x16x32_bf16 v[72:75], v[242:245], v[236:239], v[72:75]\n\t" \
  "ds_read_b64_tr_b16 v[214:215], v249 offset:32296\n\t" \
  "v_cvt_pk_f16_f32 v234, v160, v161\n\t" \
  "v_cvt_pk_f16_f32 v226, |v170|, |v171| clamp\n\t" \
  "v_mfma_f32_16x16x32_bf16 v[76:79], v[240:243], v[236:239], v[76:79]\n\t" \
  "buffer_load_dwordx4 v[236:239], v250, s[8:11], s29 offen offset:256\n\t" \
  "s_cmp_lg_u32 s38, 7\n\t" \
  "s_cbranch_scc1 .Lcnx_c35\n\t" \
  "s_mov_b32 m0, 163632\n\t" \
  "s_add_u32 s30, s30, s31\n\t" \
  "buffer_load_dword v252, s[8:11], s30 offen lds\n\t" \
  ".Lcnx_c35:\n\t" \
  "ds_read_b32 v221, v221 offset:0\n\t" \
  "v_cvt_pk_f16_f32 v228, |v168|, |v169| clamp\n\t" \
  "v_exp_f16_sdwa v232, v233 dst_sel:WORD_1 dst_unused:UNUSED_PRESERVE src0_sel:WORD_1\n\t" \
  "v_pk_mul_f16 v235, v234, v234\n\t" \
  "s_waitcnt lgkmcnt(5)\n\t" \
  "v_mfma_f32_16x16x32_bf16 v[0:3], v[208:211], v[178:181], v[0:3]\n\t" \
  "ds_read_b32 v220, v220 offset:0\n\t" \
  "v_and_b32_sdwa v225, s56, v224 dst_sel:DWORD dst_unused:UNUSED_PAD src0_sel:DWORD src1_sel:WORD_1\n\t" \
  "v_and_b32 v224, s56, v224\n\t" \
  "v_mfma_f32_16x16x32_bf16 v[24:27], v[208:211], v[186:189], v[24:27]\n\t" \
  "ds_read_b32 v225, v225 offset:0\n\t" \
  "v_and_b32_sdwa v227, s56, v226 dst_sel:DWORD dst_unused:UNUSED_PAD src0_sel:DWORD src1_sel:WORD_1\n\t" \
  "v_mfma_f32_16x16x32_bf16 v[48:51], v[208:211], v[194:197], v[48:51]\n\t" \
  "ds_read_b32 v224, v224 offset:0\n\t" \
  "v_and_b32 v226, s56, v226\n\t" \
  "v_and_b32_sdwa v229, s56, v228 dst_sel:DWORD dst_unused:UNUSED_PAD src0_sel:DWORD src1_sel:WORD_1\n\t" \
  "v_mfma_f32_16x16x32_bf16 v[72:75], v[208:211], v[202:205], v[72:75]\n\t" \
  "ds_read_b64_tr_b16 v[208:209], v249 offset:30816\n\t" \
  "v_and_b32 v228, s56, v228\n\t" \
  "s_waitcnt lgkmcnt(5)\n\t" \
  "v_mfma_f32_16x16x32_bf16 v[4:7], v[212:215], v[178:181], v[4:7]\n\t" \
  "ds_read_b64_tr_b16 v[210:211], v249 offset:32352\n\t" \
  "v_pk_fma_f16 v235, v235, v253, s54\n\t" \
  "v_mfma_f32_16x16x32_bf16 v[28:31], v[212:215], v[186:189], v[28:31]\n\t" \
  "ds_read_b32 v227, v227 offset:0\n\t" \
  "v_pk_mul_f16 v235, v234, v235\n\t" \
  "v_fma_f32 v147, |v147|, v217, v147 mul:4\n\t" \
  "v_fma_f32 v146, |v146|, v216, v146 mul:4\n\t" \
  "v_mfma_f32_16x16x32_bf16 v[52:55], v[212:215], v[194:197], v[52:55]\n\t" \
  "ds_read_b32 v226, v226 offset:0\n\t" \
  "v_fma_f32 v155, |v155|, v219, v155 mul:4\n\t" \
  "s_waitcnt lgkmcnt(6)\n\t" \
  "v_fma_f32 v154, |v154|, v218, v154 mul:4\n\t" \
  "v_fma_f32 v153, |v153|, v221, v153 mul:4\n\t" \
  "v_mfma_f32_16x16x32_bf16 v[76:79], v[212:215], v[202:205], v[76:79]\n\t" \
  "ds_read_b64_tr_b16 v[212:213], v249 offset:30824\n\t" \
  "v_fma_f32 v152, |v152|, v220, v152 mul:4\n\t" \
  "v_pk_fma_f16 v232, v232, s55, s55\n\t" \
  "s_waitcnt lgkmcnt(3)\n\t" \
  "v_mfma_f32_16x16x32_bf16 v[8:11], v[208:211], v[178:181], v[8:11]\n\t" \
  "ds_read_b64_tr_b16 v[214:215], v249 offset:32360\n\t" \
  "v_cvt_pk_bf16_f32 v147, v146, v147\n\t" \
  "v_cvt_pk_bf16_f32 v155, v154, v155\n\t" \
  "v_cvt_pk_bf16_f32 v154, v152, v153\n\t" \
  "v_mfma_f32_16x16x32_bf16 v[32:35], v[208:211], v[186:189], v[32:35]\n\t" \
  "ds_read_b32 v229, v229 offset:0\n\t" \
  "v_rcp_f16 v233, v232\n\t" \
  "v_exp_f16 v234, v235\n\t" \
  "v_cvt_pk_f16_f32 v216, |v148|, |v149| clamp\n\t" \
  "v_mfma_f32_16x16x32_bf16 v[56:59], v[208:211], v[194:197], v[56:59]\n\t" \
  "ds_read_b32 v228, v228 offset:0\n\t" \
  "s_cbranch_vccnz .Lcnx_p11_1\n\ts_setprio 1\n.Lcnx_p11_1:\n\t" \
  "v_cvt_pk_f16_f32 v218, |v150|, |v151| clamp\n\t" \
  "v_cvt_pk_f16_f32 v220, |v156|, |v157| clamp\n\t" \
  "v_mfma_f32_16x16x32_bf16 v[80:83], v[208:211], v[202:205], v[80:83]\n\t" \
  "ds_read_b64_tr_b16 v[208:209], v249 offset:30880\n\t" \
  "v_cvt_pk_f16_f32 v222, |v158|, |v159| clamp\n\t" \
  "v_rcp_f16_sdwa v232, v232 dst_sel:DWORD dst_unused:UNUSED_PAD src0_sel:WORD_1\n\t" \
  "v_exp_f16_sdwa v234, v235 dst_sel:WORD_1 dst_unused:UNUSED_PRESERVE src0_sel:WORD_1\n\t" \
  "s_waitcnt lgkmcnt(3)\n\t" \
  "v_mfma_f32_16x16x32_bf16 v[12:15], v[212:215], v[178:181], v[12:15]\n\t" \
  "ds_read_b64_tr_b16 v[210:211], v249 offset:32416\n\t" \
  "v_and_b32_sdwa v217, s56, v216 dst_sel:DWORD dst_unused:UNUSED_PAD src0_sel:DWORD src1_sel:WORD_1\n\t" \
  "v_and_b32 v216, s56, v216\n\t" \
  "v_mfma_f32_16x16x32_bf16 v[36:39], v[212:215], v[186:189], v[36:39]\n\t" \
  "ds_read_b32 v217, v217 offset:0\n\t" \
  "v_and_b32_sdwa v219, s56, v218 dst_sel:DWORD dst_unused:UNUSED_PAD src0_sel:DWORD src1_sel:WORD_1\n\t" \
  "v_mfma_f32_16x16x32_bf16 v[60:63], v[212:215], v[194:197], v[60:63]\n\t" \
  "ds_read_b32 v216, v216 offset:0\n\t" \
  "v_and_b32 v218, s56, v218\n\t" \
  "v_and_b32_sdwa v221, s56, v220 dst_sel:DWORD dst_unused:UNUSED_PAD src0_sel:DWORD src1_sel:WORD_1\n\t" \
  "v_mfma_f32_16x16x32_bf16 v[84:87], v[212:215], v[202:205], v[84:87]\n\t" \
  "ds_read_b64_tr_b16 v[212:213], v249 offset:30888\n\t" \
  "v_and_b32 v220, s56, v220\n\t" \
  "s_waitcnt lgkmcnt(3)\n\t" \
  "v_mfma_f32_16x16x32_bf16 v[16:19], v[208:211], v[178:181], v[16:19]\n\t" \
  "ds_read_b64_tr_b16 v[214:215], v249 offset:32424\n\t" \
  "v_and_b32_sdwa v223, s56, v222 dst_sel:DWORD dst_unused:UNUSED_PAD src0_sel:DWORD src1_sel:WORD_1\n\t" \
  "v_mfma_f32_16x16x32_bf16 v[40:43], v[208:211], v[186:189], v[40:43]\n\t" \
  "ds_read_b32 v219, v219 offset:0\n\t" \
  "v_and_b32 v222, s56, v222\n\t" \
  "v_fma_f32 v163, |v163|, v225, v163 mul:4\n\t" \
  "v_mfma_f32_16x16x32_bf16 v[64:67], v[208:211], v[194:197], v[64:67]\n\t" \
  "ds_read_b32 v218, v218 offset:0\n\t" \
  "v_fma_f32 v162, |v162|, v224, v162 mul:4\n\t" \
  "v_fma_f32 v171, |v171|, v227, v171 mul:4\n\t" \
  "v_mfma_f32_16x16x32_bf16 v[88:91], v[208:211], v[202:205], v[88:91]\n\t" \
  "ds_read_b64_tr_b16 v[208:209], v248 offset:12304\n\t" \
  "ds_read_b64_tr_b16 v[210:211], v248 offset:13328\n\t" \
  "v_fma_f32 v170, |v170|, v226, v170 mul:4\n\t" \
  "v_fma_f32 v169, |v169|, v229, v169 mul:4\n\t" \
  "s_waitcnt lgkmcnt(4)\n\t" \
  "v_mfma_f32_16x16x32_bf16 v[20:23], v[212:215], v[178:181], v[20:23]\n\t" \
  "ds_read_b32 v221, v221 offset:0\n\t" \
  "v_fma_f32 v168, |v168|, v228, v168 mul:4\n\t" \
  "v_fma_mix_f32 v144, v144, v233, 0 op_sel_hi:[0,1,0]\n\t" \
  "v_fma_mix_f32 v145, v145, v232, 0 op_sel_hi:[0,1,0]\n\t" \
  "v_mfma_f32_16x16x32_bf16 v[44:47], v[212:215], v[186:189], v[44:47]\n\t" \
  "ds_read_b32 v220, v220 offset:0\n\t" \
  "v_pk_fma_f16 v234, v234, s55, s55\n\t" \
  "v_cvt_pk_bf16_f32 v146, v144, v145\n\t" \
  "v_cvt_pk_bf16_f32 v163, v162, v163\n\t" \
  "v_mfma_f32_16x16x32_bf16 v[68:71], v[212:215], v[194:197], v[68:71]\n\t" \
  "ds_read_b32 v223, v223 offset:0\n\t" \
  "v_cvt_pk_bf16_f32 v171, v170, v171\n\t" \
  "v_cvt_pk_bf16_f32 v170, v168, v169\n\t" \
  "v_rcp_f16 v235, v234\n\t" \
  "v_mfma_f32_16x16x32_bf16 v[92:95], v[212:215], v[202:205], v[92:95]\n\t" \
  "ds_read_b64_tr_b16 v[212:213], v248 offset:15376\n\t" \
  "v_cvt_pk_f16_f32 v224, |v164|, |v165| clamp\n\t" \
  "s_cbranch_vccnz .Lcnx_p11_2\n\ts_setprio 2\n.Lcnx_p11_2:\n\t" \
  "v_cvt_pk_f16_f32 v226, |v166|, |v167| clamp\n\t" \
  "s_waitcnt lgkmcnt(4)\n\t" \
  "v_mfma_f32_16x16x32_bf16 v[176:179], v[208:211], v[96:99], 0\n\t" \
  "ds_read_b64_tr_b16 v[214:215], v248 offset:16400\n\t" \
  "v_cvt_pk_f16_f32 v228, |v172|, |v173| clamp\n\t" \
  "v_cvt_pk_f16_f32 v230, |v174|, |v175| clamp\n\t" \
  "v_rcp_f16_sdwa v234, v234 dst_sel:DWORD dst_unused:UNUSED_PAD src0_sel:WORD_1\n\t" \
  "v_mfma_f32_16x16x32_bf16 v[184:187], v[208:211], v[108:111], 0\n\t" \
  "ds_read_b32 v222, v222 offset:0\n\t" \
  "v_and_b32_sdwa v225, s56, v224 dst_sel:DWORD dst_unused:UNUSED_PAD src0_sel:DWORD src1_sel:WORD_1\n\t" \
  "v_and_b32 v224, s56, v224\n\t" \
  "v_mfma_f32_16x16x32_bf16 v[192:195], v[208:211], v[120:123], 0\n\t" \
  "ds_read_b32 v225, v225 offset:0\n\t" \
  "v_and_b32_sdwa v227, s56, v226 dst_sel:DWORD dst_unused:UNUSED_PAD src0_sel:DWORD src1_sel:WORD_1\n\t" \
  "v_mfma_f32_16x16x32_bf16 v[200:203], v[208:211], v[132:135], 0\n\t" \
  "ds_read_b64_tr_b16 v[208:209], v248 offset:14352\n\t" \
  "v_and_b32 v226, s56, v226\n\t" \
  "v_and_b32_sdwa v229, s56, v228 dst_sel:DWORD dst_unused:UNUSED_PAD src0_sel:DWORD src1_sel:WORD_1\n\t" \
  "s_waitcnt lgkmcnt(3)\n\t" \
  "v_mfma_f32_16x16x32_bf16 v[176:179], v[212:215], v[100:103], v[176:179]\n\t" \
  "ds_read_b64_tr_b16 v[210:211], v248 offset:17424\n\t" \
  "v_and_b32 v228, s56, v228\n\t" \
  "v_mfma_f32_16x16x32_bf16 v[184:187], v[212:215], v[112:115], v[184:187]\n\t" \
  "ds_read_b32 v224, v224 offset:0\n\t" \
  "v_and_b32_sdwa v231, s56, v230 dst_sel:DWORD dst_unused:UNUSED_PAD src0_sel:DWORD src1_sel:WORD_1\n\t" \
  "v_mfma_f32_16x16x32_bf16 v[192:195], v[212:215], v[124:127], v[192:195]\n\t" \
  "ds_read_b32 v227, v227 offset:0\n\t" \
  "v_and_b32 v230, s56, v230\n\t" \
  "v_fma_f32 v149, |v149|, v217, v149 mul:4\n\t" \
  "v_mfma_f32_16x16x32_bf16 v[200:203], v[212:215], v[136:139], v[200:203]\n\t" \
  "ds_read_b64_tr_b16 v[212:213], v248 offset:12816\n\t" \
  "ds_read_b64_tr_b16 v[214:215], v248 offset:13840\n\t" \
  "v_fma_f32 v148, |v148|, v216, v148 mul:4\n\t" \
  "v_fma_f32 v151, |v151|, v219, v151 mul:4\n\t" \
  "s_waitcnt lgkmcnt(4)\n\t" \
  "v_mfma_f32_16x16x32_bf16 v[176:179], v[208:211], v[104:107], v[176:179]\n\t" \
  "ds_read_b32 v226, v226 offset:0\n\t" \
  "v_fma_f32 v150, |v150|, v218, v150 mul:4\n\t" \
  "v_fma_f32 v157, |v157|, v221, v157 mul:4\n\t" \
  "v_mfma_f32_16x16x32_bf16 v[184:187], v[208:211], v[116:119], v[184:187]\n\t" \
  "ds_read_b32 v229, v229 offset:0\n\t" \
  "v_fma_f32 v156, |v156|, v220, v156 mul:4\n\t" \
  "v_fma_f32 v159, |v159|, v223, v159 mul:4\n\t" \
  "v_fma_f32 v158, |v158|, v222, v158 mul:4\n\t" \
  "v_mfma_f32_16x16x32_bf16 v[192:195], v[208:211], v[128:131], v[192:195]\n\t" \
  "ds_read_b32 v228, v228 offset:0\n\t" \
  "v_fma_mix_f32 v160, v160, v235, 0 op_sel_hi:[0,1,0]\n\t" \
  "v_fma_mix_f32 v161, v161, v234, 0 op_sel_hi:[0,1,0]\n\t" \
  "v_cvt_pk_bf16_f32 v162, v160, v161\n\t" \
  "v_mfma_f32_16x16x32_bf16 v[200:203], v[208:211], v[140:143], v[200:203]\n\t" \
  "ds_read_b64_tr_b16 v[208:209], v248 offset:15888\n\t" \
  "v_cvt_pk_bf16_f32 v148, v148, v149\n\t" \
  "v_cvt_pk_bf16_f32 v149, v150, v151\n\t" \
  "s_waitcnt lgkmcnt(4)\n\t" \
  "v_mfma_f32_16x16x32_bf16 v[180:183], v[212:215], v[96:99], 0\n\t" \
  "ds_read_b64_tr_b16 v[210:211], v248 offset:16912\n\t" \
  "v_cvt_pk_bf16_f32 v156, v156, v157\n\t" \
  "v_cvt_pk_bf16_f32 v157, v158, v159\n\t" \
  "s_cbranch_vccnz .Lcnx_p11_3\n\ts_setprio 3\n.Lcnx_p11_3:\n\t" \
  "v_cvt_pk_f16_f32 v216, |v178|, |v179| clamp\n\t" \
  "v_mfma_f32_16x16x32_bf16 v[188:191], v[212:215], v[108:111], 0\n\t" \
  "ds_read_b32 v231, v231 offset:0\n\t" \
  "v_cvt_pk_f16_f32 v232, v176, v177\n\t" \
  "v_cvt_pk_f16_f32 v218, |v186|, |v187| clamp\n\t" \
  "v_cvt_pk_f16_f32 v220, |v184|, |v185| clamp\n\t" \
  "v_mfma_f32_16x16x32_bf16 v[196:199], v[212:215], v[120:123], 0\n\t" \
  "ds_read_b32 v230, v230 offset:0\n\t" \
  "v_pk_mul_f16 v233, v232, v232\n\t" \
  "v_and_b32_sdwa v217, s56, v216 dst_sel:DWORD dst_unused:UNUSED_PAD src0_sel:DWORD src1_sel:WORD_1\n\t" \
  "v_mfma_f32_16x16x32_bf16 v[204:207], v[212:215], v[132:135], 0\n\t" \
  "ds_read_b64_tr_b16 v[212:213], v248 offset:14864\n\t" \
  "v_and_b32 v216, s56, v216\n\t" \
  "v_and_b32_sdwa v219, s56, v218 dst_sel:DWORD dst_unused:UNUSED_PAD src0_sel:DWORD src1_sel:WORD_1\n\t" \
  "s_waitcnt lgkmcnt(3)\n\t" \
  "v_mfma_f32_16x16x32_bf16 v[180:183], v[208:211], v[100:103], v[180:183]\n\t" \
  "ds_read_b64_tr_b16 v[214:215], v248 offset:17936\n\t" \
  "v_and_b32 v218, s56, v218\n\t" \
  "v_mfma_f32_16x16x32_bf16 v[188:191], v[208:211], v[112:115], v[188:191]\n\t" \
  "s_waitcnt vmcnt(0)\n\t" \
  "s_mov_b64 exec, 3\n\t" \
  "ds_add_rtn_u32 v246, v251, v250 offset:47744\n\t" \
  "s_mov_b64 exec, -1\n\t" \
  "ds_read_b32 v217, v217 offset:0\n\t" \
  "v_and_b32_sdwa v221, s56, v220 dst_sel:DWORD dst_unused:UNUSED_PAD src0_sel:DWORD src1_sel:WORD_1\n\t" \
  "v_mfma_f32_16x16x32_bf16 v[196:199], v[208:211], v[124:127], v[196:199]\n\t" \
  "ds_read_b32 v216, v216 offset:0\n\t" \
  "v_and_b32 v220, s56, v220\n\t" \
  "v_pk_fma_f16 v233, v233, v253, s54\n\t" \
  "v_mfma_f32_16x16x32_bf16 v[204:207], v[208:211], v[136:139], v[204:207]\n\t" \
  "s_waitcnt lgkmcnt(2)\n\t" \
  "v_readfirstlane_b32 s4, v246\n\t" \
  "s_cmp_lt_u32 s4, s57\n\t" \
  "s_cbranch_scc1 .Lcnx_w132\n.Lcnx_b132:\n\t" \
  "ds_read_b64_tr_b16 v[208:209], v249 offset:0\n\t" \
  "v_pk_mul_f16 v233, v232, v233\n\t" \
  "v_fma_f32 v165, |v165|, v225, v165 mul:4\n\t" \
  "v_mfma_f32_16x16x32_bf16 v[180:183], v[212:215], v[104:107], v[180:183]\n\t" \
  "ds_read_b64_tr_b16 v[210:211], v249 offset:1536\n\t" \
  "v_fma_f32 v164, |v164|, v224, v164 mul:4\n\t" \
  "v_fma_f32 v167, |v167|, v227, v167 mul:4\n\t" \
  "v_mfma_f32_16x16x32_bf16 v[188:191], v[212:215], v[116:119], v[188:191]\n\t" \
  "ds_read_b32 v219, v219 offset:0\n\t" \
  "v_fma_f32 v166, |v166|, v226, v166 mul:4\n\t" \
  "v_fma_f32 v173, |v173|, v229, v173 mul:4\n\t" \
  "v_fma_f32 v172, |v172|, v228, v172 mul:4\n\t" \
  "v_mfma_f32_16x16x32_bf16 v[196:199], v[212:215], v[128:131], v[196:199]\n\t" \
  "ds_read_b32 v218, v218 offset:0\n\t" \
  "v_fma_f32 v175, |v175|, v231, v175 mul:4\n\t" \
  "v_fma_f32 v174, |v174|, v230, v174 mul:4\n\t" \
  "v_cvt_pk_bf16_f32 v164, v164, v165\n\t" \
  "v_mfma_f32_16x16x32_bf16 v[204:207], v[212:215], v[140:143], v[204:207]\n\t" \
  "ds_read_b64_tr_b16 v[212:213], v249 offset:8\n\t" \
  "v_cvt_pk_bf16_f32 v165, v166, v167\n\t" \
  "v_cvt_pk_bf16_f32 v172, v172, v173\n\t" \
  "v_cvt_pk_bf16_f32 v173, v174, v175\n\t" \
  "s_cbranch_vccnz .Lcnx_p12_0\n\ts_setprio 0\n.Lcnx_p12_0:\n\t" \
  "v_exp_f16 v232, v233\n\t" \
  "v_cvt_pk_f16_f32 v224, |v194|, |v195| clamp\n\t" \
  "v_mfma_f32_16x16x32_bf16 v[80:83], v[242:245], v[236:239], v[80:83]\n\t" \
  "ds_read_b64_tr_b16 v[214:215], v249 offset:1544\n\t" \
  "v_cvt_pk_f16_f32 v234, v192, v193\n\t" \
  "v_cvt_pk_f16_f32 v226, |v202|, |v203| clamp\n\t" \
  "v_cvt_pk_f16_f32 v228, |v200|, |v201| clamp\n\t" \
  "v_exp_f16_sdwa v232, v233 dst_sel:WORD_1 dst_unused:UNUSED_PRESERVE src0_sel:WORD_1\n\t" \
  "v_mfma_f32_16x16x32_bf16 v[84:87], v[240:243], v[236:239], v[84:87]\n\t" \
  "buffer_load_dwordx4 v[236:239], v250, s[8:11], s29 offen offset:320\n\t" \
  "s_cmp_lg_u32 s38, 4\n\t" \
  "s_cbranch_scc1 .Lcnx_c36\n\t" \
  "s_mov_b32 m0, 163632\n\t" \
  "s_add_u32 s30, s30, s31\n\t" \
  "buffer_load_dword v252, s[8:11], s30 offen lds\n\t" \
  ".Lcnx_c36:\n\t" \
  "ds_read_b32 v221, v221 offset:0\n\t" \
  "v_pk_mul_f16 v235, v234, v234\n\t" \
  "v_and_b32_sdwa v225, s56, v224 dst_sel:DWORD dst_unused:UNUSED_PAD src0_sel:DWORD src1_sel:WORD_1\n\t" \
  "v_and_b32 v224, s56, v224\n\t" \
  "s_waitcnt lgkmcnt(5)\n\t" \
  "v_mfma_f32_16x16x32_bf16 v[0:3], v[208:211], v[146:149], v[0:3]\n\t" \
  "ds_read_b32 v220, v220 offset:0\n\t" \
  "v_and_b32_sdwa v227, s56, v226 dst_sel:DWORD dst_unused:UNUSED_PAD src0_sel:DWORD src1_sel:WORD_1\n\t" \
  "v_and_b32 v226, s56, v226\n\t" \
  "v_and_b32_sdwa v229, s56, v228 dst_sel:DWORD dst_unused:UNUSED_PAD src0_sel:DWORD src1_sel:WORD_1\n\t" \
  "v_mfma_f32_16x16x32_bf16 v[24:27], v[208:211], v[154:157], v[24:27]\n\t" \
  "ds_read_b32 v225, v225 offset:0\n\t" \
  "v_and_b32 v228, s56, v228\n\t" \
  "v_pk_fma_f16 v235, v235, v253, s54\n\t" \
  "v_mfma_f32_16x16x32_bf16 v[48:51], v[208:211], v[162:165], v[48:51]\n\t" \
  "ds_read_b32 v224, v224 offset:0\n\t" \
  "v_pk_mul_f16 v235, v234, v235\n\t" \
  "s_waitcnt lgkmcnt(7)\n\t" \
  "v_fma_f32 v179, |v179|, v217, v179 mul:4\n\t" \
  "v_fma_f32 v178, |v178|, v216, v178 mul:4\n\t" \
  "v_fma_f32 v187, |v187|, v219, v187 mul:4\n\t" \
  "v_mfma_f32_16x16x32_bf16 v[72:75], v[208:211], v[170:173], v[72:75]\n\t" \
  "ds_read_b64_tr_b16 v[208:209], v249 offset:64\n\t" \
  "ds_read_b64_tr_b16 v[210:211], v249 offset:1600\n\t" \
  "s_waitcnt lgkmcnt(4)\n\t" \
  "v_fma_f32 v186, |v186|, v218, v186 mul:4\n\t" \
  "v_fma_f32 v185, |v185|, v221, v185 mul:4\n\t" \
  "v_fma_f32 v184, |v184|, v220, v184 mul:4\n\t" \
  "v_pk_fma_f16 v232, v232, s55, s55\n\t" \
  "v_mfma_f32_16x16x32_bf16 v[4:7], v[212:215], v[146:149], v[4:7]\n\t" \
  "ds_read_b32 v227, v227 offset:0\n\t" \
  "v_cvt_pk_bf16_f32 v179, v178, v179\n\t" \
  "v_cvt_pk_bf16_f32 v187, v186, v187\n\t" \
  "v_cvt_pk_bf16_f32 v186, v184, v185\n\t" \
  "v_rcp_f16 v233, v232\n\t" \
  "s_cbranch_vccnz .Lcnx_p12_1\n\ts_setprio 1\n.Lcnx_p12_1:\n\t" \
  "v_mfma_f32_16x16x32_bf16 v[28:31], v[212:215], v[154:157], v[28:31]\n\t" \
  "ds_read_b32 v226, v226 offset:0\n\t" \
  "v_exp_f16 v234, v235\n\t" \
  "v_cvt_pk_f16_f32 v216, |v180|, |v181| clamp\n\t" \
  "v_cvt_pk_f16_f32 v218, |v182|, |v183| clamp\n\t" \
  "v_cvt_pk_f16_f32 v220, |v188|, |v189| clamp\n\t" \
  "v_cvt_pk_f16_f32 v222, |v190|, |v191| clamp\n\t" \
  "v_mfma_f32_16x16x32_bf16 v[52:55], v[212:215], v[162:165], v[52:55]\n\t" \
  "ds_read_b32 v229, v229 offset:0\n\t" \
  "v_rcp_f16_sdwa v232, v232 dst_sel:DWORD dst_unused:UNUSED_PAD src0_sel:WORD_1\n\t" \
  "v_exp_f16_sdwa v234, v235 dst_sel:WORD_1 dst_unused:UNUSED_PRESERVE src0_sel:WORD_1\n\t" \
  "v_and_b32_sdwa v217, s56, v216 dst_sel:DWORD dst_unused:UNUSED_PAD src0_sel:DWORD src1_sel:WORD_1\n\t" \
  "v_and_b32 v216, s56, v216\n\t" \
  "v_mfma_f32_16x16x32_bf16 v[76:79], v[212:215], v[170:173], v[76:79]\n\t" \
  "ds_read_b64_tr_b16 v[212:213], v249 offset:72\n\t" \
  "v_and_b32_sdwa v219, s56, v218 dst_sel:DWORD dst_unused:UNUSED_PAD src0_sel:DWORD src1_sel:WORD_1\n\t" \
  "v_and_b32 v218, s56, v218\n\t" \
  "s_waitcnt lgkmcnt(4)\n\t" \
  "v_mfma_f32_16x16x32_bf16 v[8:11], v[208:211], v[146:149], v[8:11]\n\t" \
  "ds_read_b64_tr_b16 v[214:215], v249 offset:1608\n\t" \
  "v_and_b32_sdwa v221, s56, v220 dst_sel:DWORD dst_unused:UNUSED_PAD src0_sel:DWORD src1_sel:WORD_1\n\t" \
  "v_and_b32 v220, s56, v220\n\t" \
  "v_mfma_f32_16x16x32_bf16 v[32:35], v[208:211], v[154:157], v[32:35]\n\t" \
  "ds_read_b32 v228, v228 offset:0\n\t" \
  "ds_read_b32 v217, v217 offset:0\n\t" \
  "v_and_b32_sdwa v223, s56, v222 dst_sel:DWORD dst_unused:UNUSED_PAD src0_sel:DWORD src1_sel:WORD_1\n\t" \
  "v_and_b32 v222, s56, v222\n\t" \
  "s_waitcnt lgkmcnt(6)\n\t" \
  "v_fma_f32 v195, |v195|, v225, v195 mul:4\n\t" \
  "v_mfma_f32_16x16x32_bf16 v[56:59], v[208:211], v[162:165], v[56:59]\n\t" \
  "ds_read_b32 v216, v216 offset:0\n\t" \
  "ds_read_b32 v219, v219 offset:0\n\t" \
  "v_fma_f32 v194, |v194|, v224, v194 mul:4\n\t" \
  "v_fma_f32 v203, |v203|, v227, v203 mul:4\n\t" \
  "s_waitcnt lgkmcnt(3)\n\t" \
  "v_fma_f32 v202, |v202|, v226, v202 mul:4\n\t" \
  "v_mfma_f32_16x16x32_bf16 v[80:83], v[208:211], v[170:173], v[80:83]\n\t" \
  "ds_read_b64_tr_b16 v[208:209], v249 offset:128\n\t" \
  "ds_read_b64_tr_b16 v[210:211], v249 offset:1664\n\t" \
  "v_fma_f32 v201, |v201|, v229, v201 mul:4\n\t" \
  "v_fma_f32 v200, |v200|, v228, v200 mul:4\n\t" \
  "v_fma_mix_f32 v176, v176, v233, 0 op_sel_hi:[0,1,0]\n\t" \
  "v_fma_mix_f32 v177, v177, v232, 0 op_sel_hi:[0,1,0]\n\t" \
  "v_mfma_f32_16x16x32_bf16 v[12:15], v[212:215], v[146:149], v[12:15]\n\t" \
  "ds_read_b32 v218, v218 offset:0\n\t" \
  "s_cbranch_vccnz .Lcnx_p12_2\n\ts_setprio 2\n.Lcnx_p12_2:\n\t" \
  "v_pk_fma_f16 v234, v234, s55, s55\n\t" \
  "v_cvt_pk_bf16_f32 v178, v176, v177\n\t" \
  "v_cvt_pk_bf16_f32 v195, v194, v195\n\t" \
  "v_cvt_pk_bf16_f32 v203, v202, v203\n\t" \
  "v_cvt_pk_bf16_f32 v202, v200, v201\n\t" \
  "v_mfma_f32_16x16x32_bf16 v[36:39], v[212:215], v[154:157], v[36:39]\n\t" \
  "ds_read_b32 v221, v221 offset:0\n\t" \
  "v_rcp_f16 v235, v234\n\t" \
  "v_cvt_pk_f16_f32 v224, |v196|, |v197| clamp\n\t" \
  "v_cvt_pk_f16_f32 v226, |v198|, |v199| clamp\n\t" \
  "v_cvt_pk_f16_f32 v228, |v204|, |v205| clamp\n\t" \
  "v_mfma_f32_16x16x32_bf16 v[60:63], v[212:215], v[162:165], v[60:63]\n\t" \
  "ds_read_b32 v220, v220 offset:0\n\t" \
  "v_cvt_pk_f16_f32 v230, |v206|, |v207| clamp\n\t" \
  "v_rcp_f16_sdwa v234, v234 dst_sel:DWORD dst_unused:UNUSED_PAD src0_sel:WORD_1\n\t" \
  "v_and_b32_sdwa v225, s56, v224 dst_sel:DWORD dst_unused:UNUSED_PAD src0_sel:DWORD src1_sel:WORD_1\n\t" \
  "v_and_b32 v224, s56, v224\n\t" \
  "v_mfma_f32_16x16x32_bf16 v[84:87], v[212:215], v[170:173], v[84:87]\n\t" \
  "ds_read_b64_tr_b16 v[212:213], v249 offset:136\n\t" \
  "v_and_b32_sdwa v227, s56, v226 dst_sel:DWORD dst_unused:UNUSED_PAD src0_sel:DWORD src1_sel:WORD_1\n\t" \
  "v_and_b32 v226, s56, v226\n\t" \
  "s_waitcnt lgkmcnt(4)\n\t" \
  "v_mfma_f32_16x16x32_bf16 v[16:19], v[208:211], v[146:149], v[16:19]\n\t" \
  "ds_read_b64_tr_b16 v[214:215], v249 offset:1672\n\t" \
  "ds_read_b32 v223, v223 offset:0\n\t" \
  "v_and_b32_sdwa v229, s56, v228 dst_sel:DWORD dst_unused:UNUSED_PAD src0_sel:DWORD src1_sel:WORD_1\n\t" \
  "v_and_b32 v228, s56, v228\n\t" \
  "v_and_b32_sdwa v231, s56, v230 dst_sel:DWORD dst_unused:UNUSED_PAD src0_sel:DWORD src1_sel:WORD_1\n\t" \
  "v_mfma_f32_16x16x32_bf16 v[40:43], v[208:211], v[154:157], v[40:43]\n\t" \
  "s_waitcnt vmcnt(0)\n\t" \
  "s_mov_b64 exec, 3\n\t" \
  "ds_add_rtn_u32 v246, v251, v250 offset:47752\n\t" \
  "s_mov_b64 exec, -1\n\t" \
  "ds_read_b32 v222, v222 offset:0\n\t" \
  "ds_read_b32 v225, v225 offset:0\n\t" \
  "v_and_b32 v230, s56, v230\n\t" \
  "s_waitcnt lgkmcnt(8)\n\t" \
  "v_fma_f32 v181, |v181|, v217, v181 mul:4\n\t" \
  "v_mfma_f32_16x16x32_bf16 v[64:67], v[208:211], v[162:165], v[64:67]\n\t" \
  "ds_read_b32 v224, v224 offset:0\n\t" \
  "ds_read_b32 v227, v227 offset:0\n\t" \
  "v_fma_f32 v180, |v180|, v216, v180 mul:4\n\t" \
  "v_fma_f32 v183, |v183|, v219, v183 mul:4\n\t" \
  "v_fma_f32 v182, |v182|, v218, v182 mul:4\n\t" \
  "s_cbranch_vccnz .Lcnx_p12_3\n\ts_setprio 3\n.Lcnx_p12_3:\n\t" \
  "v_mfma_f32_16x16x32_bf16 v[88:91], v[208:211], v[170:173], v[88:91]\n\t" \
  "s_waitcnt lgkmcnt(4)\n\t" \
  "v_readfirstlane_b32 s4, v246\n\t" \
  "s_cmp_lt_u32 s4, s57\n\t" \
  "s_cbranch_scc1 .Lcnx_w138\n.Lcnx_b138:\n\t" \
  "ds_read_b64_tr_b16 v[208:209], v249 offset:6152\n\t" \
  "ds_read_b64_tr_b16 v[210:211], v249 offset:7688\n\t" \
  "s_waitcnt lgkmcnt(5)\n\t" \
  "v_fma_f32 v189, |v189|, v221, v189 mul:4\n\t" \
  "v_fma_f32 v188, |v188|, v220, v188 mul:4\n\t" \
  "v_fma_f32 v191, |v191|, v223, v191 mul:4\n\t" \
  "v_fma_f32 v190, |v190|, v222, v190 mul:4\n\t" \
  "v_fma_mix_f32 v192, v192, v235, 0 op_sel_hi:[0,1,0]\n\t" \
  "v_mfma_f32_16x16x32_bf16 v[20:23], v[212:215], v[146:149], v[20:23]\n\t" \
  "ds_read_b32 v226, v226 offset:0\n\t" \
  "v_fma_mix_f32 v193, v193, v234, 0 op_sel_hi:[0,1,0]\n\t" \
  "v_cvt_pk_bf16_f32 v194, v192, v193\n\t" \
  "v_cvt_pk_bf16_f32 v180, v180, v181\n\t" \
  "v_cvt_pk_bf16_f32 v181, v182, v183\n\t" \
  "v_mfma_f32_16x16x32_bf16 v[44:47], v[212:215], v[154:157], v[44:47]\n\t" \
  "ds_read_b32 v229, v229 offset:0\n\t" \
  "v_cvt_pk_bf16_f32 v188, v188, v189\n\t" \
  "v_cvt_pk_bf16_f32 v189, v190, v191\n\t" \
  "ds_read_b32 v228, v228 offset:0\n\t" \
  "ds_read_b32 v231, v231 offset:0\n\t" \
  "ds_read_b32 v230, v230 offset:0\n\t" \
  "s_waitcnt lgkmcnt(4)\n\t" \
  "v_fma_f32 v197, |v197|, v225, v197 mul:4\n\t" \
  "v_fma_f32 v196, |v196|, v224, v196 mul:4\n\t" \
  "v_fma_f32 v199, |v199|, v227, v199 mul:4\n\t" \
  "v_mfma_f32_16x16x32_bf16 v[68:71], v[212:215], v[162:165], v[68:71]\n\t" \
  "v_fma_f32 v198, |v198|, v226, v198 mul:4\n\t" \
  "s_waitcnt lgkmcnt(0)\n\t" \
  "v_fma_f32 v205, |v205|, v229, v205 mul:4\n\t" \
  "v_fma_f32 v204, |v204|, v228, v204 mul:4\n\t" \
  "v_fma_f32 v207, |v207|, v231, v207 mul:4\n\t" \
  "v_mfma_f32_16x16x32_bf16 v[92:95], v[212:215], v[170:173], v[92:95]\n\t" \
  "ds_read_b64_tr_b16 v[212:213], v249 offset:6160\n\t" \
  "v_fma_f32 v206, |v206|, v230, v206 mul:4\n\t" \
  "v_cvt_pk_bf16_f32 v196, v196, v197\n\t" \
  "v_cvt_pk_bf16_f32 v197, v198, v199\n\t" \
  "v_cvt_pk_bf16_f32 v204, v204, v205\n\t" \
  "v_cvt_pk_bf16_f32 v205, v206, v207\n\t" \
  "s_cmp_lg_u32 s58, 0\n\t" \
  "s_cbranch_scc1 .Lcnx_fin\n\t" \
  "v_mfma_f32_32x32x16_bf16 v[144:159], v[242:245], v[240:243], 0\n\t" \
  "v_mfma_f32_16x16x32_bf16 v[88:91], v[242:245], v[236:239], v[88:91]\n\t" \
  "ds_read_b64_tr_b16 v[214:215], v249 offset:7696\n\t" \
  "v_mfma_f32_16x16x32_bf16 v[92:95], v[240:243], v[236:239], v[92:95]\n\t" \
  "ds_read_b64 v[246:247], v251 offset:47136\n\t" \
  "v_mfma_f32_32x32x16_bf16 v[160:175], v[242:245], v[240:243], 0\n\t" \
  "v_mfma_f32_16x16x32_bf16 v[0:3], v[208:211], v[178:181], v[0:3]\n\t" \
  "s_waitcnt lgkmcnt(0)\n\t" \
  "v_add_u32 v246, s52, v246\n\t" \
  "v_mfma_f32_16x16x32_bf16 v[24:27], v[208:211], v[186:189], v[24:27]\n\t" \
  "v_mfma_f32_16x16x32_bf16 v[48:51], v[208:211], v[194:197], v[48:51]\n\t" \
  "v_add_u32 v247, s53, v247\n\t" \
  "v_mfma_f32_16x16x32_bf16 v[72:75], v[208:211], v[202:205], v[72:75]\n\t" \
  "ds_read_b64_tr_b16 v[208:209], v249 offset:6216\n\t" \
  "v_add_u32 v232, 1536, v247\n\t" \
  "v_mfma_f32_16x16x32_bf16 v[4:7], v[212:215], v[178:181], v[4:7]\n\t" \
  "ds_read_b64_tr_b16 v[210:211], v249 offset:7752\n\t" \
  "v_mfma_f32_16x16x32_bf16 v[28:31], v[212:215], v[186:189], v[28:31]\n\t" \
  "v_mfma_f32_16x16x32_bf16 v[52:55], v[212:215], v[194:197], v[52:55]\n\t" \
  "ds_read2_b32 v[98:99], v247 offset0:0 offset1:6\n\t" \
  "v_mfma_f32_16x16x32_bf16 v[76:79], v[212:215], v[202:205], v[76:79]\n\t" \
  "ds_read_b64_tr_b16 v[212:213], v249 offset:6224\n\t" \
  "s_waitcnt lgkmcnt(2)\n\t" \
  "v_mfma_f32_16x16x32_bf16 v[8:11], v[208:211], v[178:181], v[8:11]\n\t" \
  "ds_read_b64_tr_b16 v[214:215], v249 offset:7760\n\t" \
  "v_mfma_f32_16x16x32_bf16 v[32:35], v[208:211], v[186:189], v[32:35]\n\t" \
  "ds_read2_b32 v[114:115], v247 offset0:192 offset1:198\n\t" \
  "v_mfma_f32_16x16x32_bf16 v[56:59], v[208:211], v[194:197], v[56:59]\n\t" \
  "ds_read2_b32 v[130:131], v232 offset0:0 offset1:6\n\t" \
  "v_mfma_f32_16x16x32_bf16 v[80:83], v[208:211], v[202:205], v[80:83]\n\t" \
  "ds_read_b64_tr_b16 v[208:209], v249 offset:6280\n\t" \
  "s_waitcnt lgkmcnt(3)\n\t" \
  "v_mfma_f32_16x16x32_bf16 v[12:15], v[212:215], v[178:181], v[12:15]\n\t" \
  "ds_read_b64_tr_b16 v[210:211], v249 offset:7816\n\t" \
  "v_mfma_f32_16x16x32_bf16 v[36:39], v[212:215], v[186:189], v[36:39]\n\t" \
  "ds_read2_b32 v[102:103], v247 offset0:12 offset1:18\n\t" \
  "v_mfma_f32_16x16x32_bf16 v[60:63], v[212:215], v[194:197], v[60:63]\n\t" \
  "ds_read2_b32 v[118:119], v247 offset0:204 offset1:210\n\t" \
  "v_mfma_f32_16x16x32_bf16 v[84:87], v[212:215], v[202:205], v[84:87]\n\t" \
  "ds_read_b64_tr_b16 v[212:213], v249 offset:6288\n\t" \
  "s_waitcnt lgkmcnt(3)\n\t" \
  "v_mfma_f32_16x16x32_bf16 v[16:19], v[208:211], v[178:181], v[16:19]\n\t" \
  "ds_read_b64_tr_b16 v[214:215], v249 offset:7824\n\t" \
  "v_mfma_f32_16x16x32_bf16 v[40:43], v[208:211], v[186:189], v[40:43]\n\t" \
  "ds_read2_b32 v[134:135], v232 offset0:12 offset1:18\n\t" \
  "v_mfma_f32_16x16x32_bf16 v[64:67], v[208:211], v[194:197], v[64:67]\n\t" \
  "ds_read2_b32 v[106:107], v247 offset0:24 offset1:30\n\t" \
  "v_mfma_f32_16x16x32_bf16 v[88:91], v[208:211], v[202:205], v[88:91]\n\t" \
  "ds_read2_b32 v[122:123], v247 offset0:216 offset1:222\n\t" \
  "s_waitcnt lgkmcnt(3)\n\t" \
  "v_mfma_f32_16x16x32_bf16 v[20:23], v[212:215], v[178:181], v[20:23]\n\t" \
  "ds_read2_b32 v[138:139], v232 offset0:24 offset1:30\n\t" \
  "v_mfma_f32_16x16x32_bf16 v[44:47], v[212:215], v[186:189], v[44:47]\n\t" \
  "ds_read2_b32 v[110:111], v247 offset0:36 offset1:42\n\t" \
  "v_mfma_f32_16x16x32_bf16 v[68:71], v[212:215], v[194:197], v[68:71]\n\t" \
  "ds_read2_b32 v[126:127], v247 offset0:228 offset1:234\n\t" \
  "v_bfe_u32 v236, v251, 3, 2\n\t" \
  "v_mfma_f32_16x16x32_bf16 v[92:95], v[212:215], v[202:205], v[92:95]\n\t" \
  "ds_read2_b32 v[142:143], v232 offset0:36 offset1:42\n\t" \
  "v_mul_u32_u24 v236, 0x5555, v236\n\t" \
  "s_nop 7\n\t" \
  "s_mov_b32 s57, 6144\n\t" \
  "s_mov_b32 s58, 1\n\t" \
  "s_mov_b32 s38, 0\n\t" \
  "s_mov_b32 s59, -86104\n\t" \
  "s_mov_b32 s60, -36904\n\t" \
  "s_mov_b32 s36, 2\n\t" \
  "s_mov_b32 s61, s28\n\t" \
  "s_mov_b32 s62, s29\n\t" \
  "s_add_u32 s28, s28, s65\n\t" \
  "s_add_u32 s29, s29, s65\n\t" \
  "s_branch .Lcnx_round\n\t" \
  ".Lcnx_w9:\n\t" \
  "ds_read_b32 v246, v251 offset:47656\n\t" \
  "s_waitcnt lgkmcnt(0)\n\t" \
  "v_readfirstlane_b32 s4, v246\n\t" \
  "s_cmp_lt_u32 s4, s57\n\t" \
  "s_cbranch_scc1 .Lcnx_w9\n\t" \
  "s_branch .Lcnx_b9\n\t" \
  ".Lcnx_w12:\n\t" \
  "ds_read_b32 v246, v251 offset:47664\n\t" \
  "s_waitcnt lgkmcnt(0)\n\t" \
  "v_readfirstlane_b32 s4, v246\n\t" \
  "s_cmp_lt_u32 s4, s57\n\t" \
  "s_cbranch_scc1 .Lcnx_w12\n\t" \
  "s_branch .Lcnx_b12\n\t" \
  ".Lcnx_w24:\n\t" \
  "ds_read_b32 v246, v251 offset:47672\n\t" \
  "s_waitcnt lgkmcnt(0)\n\t" \
  "v_readfirstlane_b32 s4, v246\n\t" \
  "s_cmp_lt_u32 s4, s57\n\t" \
  "s_cbranch_scc1 .Lcnx_w24\n\t" \
  "s_branch .Lcnx_b24\n\t" \
  ".Lcnx_w36:\n\t" \
  "ds_read_b32 v246, v251 offset:47680\n\t" \
  "s_waitcnt lgkmcnt(0)\n\t" \
  "v_readfirstlane_b32 s4, v246\n\t" \
  "s_cmp_lt_u32 s4, s57\n\t" \
  "s_cbranch_scc1 .Lcnx_w36\n\t" \
  "s_branch .Lcnx_b36\n\t" \
  ".Lcnx_w48:\n\t" \
  "ds_read_b32 v246, v251 offset:47688\n\t" \
  "s_waitcnt lgkmcnt(0)\n\t" \
  "v_readfirstlane_b32 s4, v246\n\t" \
  "s_cmp_lt_u32 s4, s57\n\t" \
  "s_cbranch_scc1 .Lcnx_w48\n\t" \
  "s_branch .Lcnx_b48\n\t" \
  ".Lcnx_w60:\n\t" \
  "ds_read_b32 v246, v251 offset:47696\n\t" \
  "s_waitcnt lgkmcnt(0)\n\t" \
  "v_readfirstlane_b32 s4, v246\n\t" \
  "s_cmp_lt_u32 s4, s57\n\t" \
  "s_cbranch_scc1 .Lcnx_w60\n\t" \
  "s_branch .Lcnx_b60\n\t" \
  ".Lcnx_w72:\n\t" \
  "ds_read_b32 v246, v251 offset:47704\n\t" \
  "s_waitcnt lgkmcnt(0)\n\t" \
  "v_readfirstlane_b32 s4, v246\n\t" \
  "s_cmp_lt_u32 s4, s57\n\t" \
  "s_cbranch_scc1 .Lcnx_w72\n\t" \
  "s_branch .Lcnx_b72\n\t" \
  ".Lcnx_w84:\n\t" \
  "ds_read_b32 v246, v251 offset:47712\n\t" \
  "s_waitcnt lgkmcnt(0)\n\t" \
  "v_readfirstlane_b32 s4, v246\n\t" \
  "s_cmp_lt_u32 s4, s57\n\t" \
  "s_cbranch_scc1 .Lcnx_w84\n\t" \
  "s_branch .Lcnx_b84\n\t" \
  ".Lcnx_w96:\n\t" \
  "ds_read_b32 v246, v251 offset:47720\n\t" \
  "s_waitcnt lgkmcnt(0)\n\t" \
  "v_readfirstlane_b32 s4, v246\n\t" \
  "s_cmp_lt_u32 s4, s57\n\t" \
  "s_cbranch_scc1 .Lcnx_w96\n\t" \
  "s_branch .Lcnx_b96\n\t" \
  ".Lcnx_w108:\n\t" \
  "ds_read_b32 v246, v251 offset:47728\n\t" \
  "s_waitcnt lgkmcnt(0)\n\t" \
  "v_readfirstlane_b32 s4, v246\n\t" \
  "s_cmp_lt_u32 s4, s57\n\t" \
  "s_cbranch_scc1 .Lcnx_w108\n\t" \
  "s_branch .Lcnx_b108\n\t" \
  ".Lcnx_w120:\n\t" \
  "ds_read_b32 v246, v251 offset:47736\n\t" \
  "s_waitcnt lgkmcnt(0)\n\t" \
  "v_readfirstlane_b32 s4, v246\n\t" \
  "s_cmp_lt_u32 s4, s57\n\t" \
  "s_cbranch_scc1 .Lcnx_w120\n\t" \
  "s_branch .Lcnx_b120\n\t" \
  ".Lcnx_w132:\n\t" \
  "ds_read_b32 v246, v251 offset:47744\n\t" \
  "s_waitcnt lgkmcnt(0)\n\t" \
  "v_readfirstlane_b32 s4, v246\n\t" \
  "s_cmp_lt_u32 s4, s57\n\t" \
  "s_cbranch_scc1 .Lcnx_w132\n\t" \
  "s_branch .Lcnx_b132\n\t" \
  ".Lcnx_w138:\n\t" \
  "ds_read_b32 v246, v251 offset:47752\n\t" \
  "s_waitcnt lgkmcnt(0)\n\t" \
  "v_readfirstlane_b32 s4, v246\n\t" \
  "s_cmp_lt_u32 s4, s57\n\t" \
  "s_cbranch_scc1 .Lcnx_w138\n\t" \
  "s_branch .Lcnx_b138\n\t" \
  ".Lcnx_f0:\n\t" \
  "s_mov_b32 m0, s44\n\t" \
  "s_nop 0\n\t" \
  "buffer_load_dwordx4 v252, s[16:19], s33 offen lds\n\t" \
  "buffer_load_dwordx4 v252, s[16:19], s34 offen offset:1024 lds\n\t" \
  "buffer_load_dwordx4 v252, s[16:19], s35 offen offset:2048 lds\n\t" \
  "s_add_u32 s33, s33, s32\n\t" \
  "s_add_u32 s34, s34, s32\n\t" \
  "s_add_u32 s35, s35, s32\n\t" \
  "s_branch .Lcnx_g0\n\t" \
  ".Lcnx_f1:\n\t" \
  "s_mov_b32 m0, s45\n\t" \
  "s_nop 0\n\t" \
  "buffer_load_dwordx4 v252, s[16:19], s33 offen lds\n\t" \
  "buffer_load_dwordx4 v252, s[16:19], s34 offen offset:1024 lds\n\t" \
  "buffer_load_dwordx4 v252, s[16:19], s35 offen offset:2048 lds\n\t" \
  "s_add_u32 s33, s33, s32\n\t" \
  "s_add_u32 s34, s34, s32\n\t" \
  "s_add_u32 s35, s35, s32\n\t" \
  "s_branch .Lcnx_g1\n\t" \
  ".Lcnx_f2:\n\t" \
  "s_mov_b32 m0, s46\n\t" \
  "s_nop 0\n\t" \
  "buffer_load_dwordx4 v252, s[16:19], s33 offen lds\n\t" \
  "buffer_load_dwordx4 v252, s[16:19], s34 offen offset:1024 lds\n\t" \
  "buffer_load_dwordx4 v252, s[16:19], s35 offen offset:2048 lds\n\t" \
  "s_add_u32 s33, s33, s32\n\t" \
  "s_add_u32 s34, s34, s32\n\t" \
  "s_add_u32 s35, s35, s32\n\t" \
  "s_branch .Lcnx_g2\n\t" \
  ".Lcnx_f3:\n\t" \
  "s_mov_b32 m0, s47\n\t" \
  "s_nop 0\n\t" \
  "buffer_load_dwordx4 v252, s[16:19], s33 offen lds\n\t" \
  "buffer_load_dwordx4 v252, s[16:19], s34 offen offset:1024 lds\n\t" \
  "buffer_load_dwordx4 v252, s[16:19], s35 offen offset:2048 lds\n\t" \
  "s_add_u32 s33, s33, s32\n\t" \
  "s_add_u32 s34, s34, s32\n\t" \
  "s_add_u32 s35, s35, s32\n\t" \
  "s_branch .Lcnx_g3\n\t" \
  ".Lcnx_f4:\n\t" \
  "s_mov_b32 m0, s44\n\t" \
  "s_nop 0\n\t" \
  "buffer_load_dwordx4 v252, s[16:19], s33 offen lds\n\t" \
  "buffer_load_dwordx4 v252, s[16:19], s34 offen offset:1024 lds\n\t" \
  "buffer_load_dwordx4 v252, s[16:19], s35 offen offset:2048 lds\n\t" \
  "s_add_u32 s33, s33, s32\n\t" \
  "s_add_u32 s34, s34, s32\n\t" \
  "s_add_u32 s35, s35, s32\n\t" \
  "s_cmp_lg_u32 s58, 1\n\t" \
  "s_cbranch_scc1 .Lcnx_g4\n\t" \
  "s_add_u32 m0, s44, s50\n\t" \
  "s_add_u32 s4, s33, s51\n\t" \
  "buffer_load_dwordx4 v252, s[16:19], s4 offen offset:0 lds\n\t" \
  "s_add_u32 s4, s34, s51\n\t" \
  "buffer_load_dwordx4 v252, s[16:19], s4 offen offset:1024 lds\n\t" \
  "s_add_u32 s4, s35, s51\n\t" \
  "buffer_load_dwordx4 v252, s[16:19], s4 offen offset:2048 lds\n\t" \
  "s_branch .Lcnx_g4\n\t" \
  ".Lcnx_f5:\n\t" \
  "s_mov_b32 m0, s45\n\t" \
  "s_nop 0\n\t" \
  "buffer_load_dwordx4 v252, s[16:19], s33 offen lds\n\t" \
  "buffer_load_dwordx4 v252, s[16:19], s34 offen offset:1024 lds\n\t" \
  "buffer_load_dwordx4 v252, s[16:19], s35 offen offset:2048 lds\n\t" \
  "s_add_u32 s33, s33, s32\n\t" \
  "s_add_u32 s34, s34, s32\n\t" \
  "s_add_u32 s35, s35, s32\n\t" \
  "s_cmp_lg_u32 s58, 1\n\t" \
  "s_cbranch_scc1 .Lcnx_g5\n\t" \
  "s_add_u32 m0, s45, s50\n\t" \
  "s_add_u32 s4, s33, s51\n\t" \
  "buffer_load_dwordx4 v252, s[16:19], s4 offen offset:0 lds\n\t" \
  "s_add_u32 s4, s34, s51\n\t" \
  "buffer_load_dwordx4 v252, s[16:19], s4 offen offset:1024 lds\n\t" \
  "s_add_u32 s4, s35, s51\n\t" \
  "buffer_load_dwordx4 v252, s[16:19], s4 offen offset:2048 lds\n\t" \
  "s_branch .Lcnx_g5\n\t" \
  ".Lcnx_f6:\n\t" \
  "s_mov_b32 m0, s46\n\t" \
  "s_nop 0\n\t" \
  "buffer_load_dwordx4 v252, s[16:19], s33 offen lds\n\t" \
  "buffer_load_dwordx4 v252, s[16:19], s34 offen offset:1024 lds\n\t" \
  "buffer_load_dwordx4 v252, s[16:19], s35 offen offset:2048 lds\n\t" \
  "s_add_u32 s33, s33, s32\n\t" \
  "s_add_u32 s34, s34, s32\n\t" \
  "s_add_u32 s35, s35, s32\n\t" \
  "s_cmp_lg_u32 s58, 1\n\t" \
  "s_cbranch_scc1 .Lcnx_g6\n\t" \
  "s_cmp_lt_u32 s41, 1024\n\t" \
  "s_cbranch_scc1 .Lcnx_g6\n\t" \
  "s_add_u32 m0, s46, s50\n\t" \
  "s_add_u32 s4, s33, s51\n\t" \
  "buffer_load_dwordx4 v252, s[16:19], s4 offen offset:0 lds\n\t" \
  "s_add_u32 s4, s34, s51\n\t" \
  "buffer_load_dwordx4 v252, s[16:19], s4 offen offset:1024 lds\n\t" \
  "s_add_u32 s4, s35, s51\n\t" \
  "buffer_load_dwordx4 v252, s[16:19], s4 offen offset:2048 lds\n\t" \
  "s_branch .Lcnx_g6\n\t" \
  ".Lcnx_f7:\n\t" \
  "s_mov_b32 m0, s47\n\t" \
  "s_nop 0\n\t" \
  "buffer_load_dwordx4 v252, s[16:19], s33 offen lds\n\t" \
  "buffer_load_dwordx4 v252, s[16:19], s34 offen offset:1024 lds\n\t" \
  "buffer_load_dwordx4 v252, s[16:19], s35 offen offset:2048 lds\n\t" \
  "s_add_u32 s33, s33, s32\n\t" \
  "s_add_u32 s34, s34, s32\n\t" \
  "s_add_u32 s35, s35, s32\n\t" \
  "s_cmp_lg_u32 s58, 1\n\t" \
  "s_cbranch_scc1 .Lcnx_g7\n\t" \
  "s_cmp_lt_u32 s41, 1024\n\t" \
  "s_cbranch_scc1 .Lcnx_g7\n\t" \
  "s_add_u32 m0, s47, s50\n\t" \
  "s_add_u32 s4, s33, s51\n\t" \
  "buffer_load_dwordx4 v252, s[16:19], s4 offen offset:0 lds\n\t" \
  "s_add_u32 s4, s34, s51\n\t" \
  "buffer_load_dwordx4 v252, s[16:19], s4 offen offset:1024 lds\n\t" \
  "s_add_u32 s4, s35, s51\n\t" \
  "buffer_load_dwordx4 v252, s[16:19], s4 offen offset:2048 lds\n\t" \
  "s_branch .Lcnx_g7\n\t" \
  ".Lcnx_f8:\n\t" \
  "s_mov_b32 m0, s44\n\t" \
  "s_nop 0\n\t" \
  "buffer_load_dwordx4 v252, s[16:19], s33 offen lds\n\t" \
  "buffer_load_dwordx4 v252, s[16:19], s34 offen offset:1024 lds\n\t" \
  "buffer_load_dwordx4 v252, s[16:19], s35 offen offset:2048 lds\n\t" \
  "s_add_u32 s33, s33, s32\n\t" \
  "s_add_u32 s34, s34, s32\n\t" \
  "s_add_u32 s35, s35, s32\n\t" \
  "s_branch .Lcnx_g8\n\t" \
  ".Lcnx_f9:\n\t" \
  "s_mov_b32 m0, s45\n\t" \
  "s_nop 0\n\t" \
  "buffer_load_dwordx4 v252, s[16:19], s33 offen lds\n\t" \
  "buffer_load_dwordx4 v252, s[16:19], s34 offen offset:1024 lds\n\t" \
  "buffer_load_dwordx4 v252, s[16:19], s35 offen offset:2048 lds\n\t" \
  "s_add_u32 s33, s33, s32\n\t" \
  "s_add_u32 s34, s34, s32\n\t" \
  "s_add_u32 s35, s35, s32\n\t" \
  "s_sub_u32 s33, s33, s48\n\t" \
  "s_sub_u32 s34, s34, s48\n\t" \
  "s_sub_u32 s35, s35, s48\n\t" \
  "s_branch .Lcnx_g9\n\t" \
  ".Lcnx_f10:\n\t" \
  "s_mov_b32 m0, s46\n\t" \
  "s_nop 0\n\t" \
  "buffer_load_dwordx4 v252, s[16:19], s33 offen lds\n\t" \
  "buffer_load_dwordx4 v252, s[16:19], s34 offen offset:1024 lds\n\t" \
  "buffer_load_dwordx4 v252, s[16:19], s35 offen offset:2048 lds\n\t" \
  "s_add_u32 s33, s33, s32\n\t" \
  "s_add_u32 s34, s34, s32\n\t" \
  "s_add_u32 s35, s35, s32\n\t" \
  "s_branch .Lcnx_g10\n\t" \
  ".Lcnx_f11:\n\t" \
  "s_mov_b32 m0, s47\n\t" \
  "s_nop 0\n\t" \
  "buffer_load_dwordx4 v252, s[16:19], s33 offen lds\n\t" \
  "buffer_load_dwordx4 v252, s[16:19], s34 offen offset:1024 lds\n\t" \
  "buffer_load_dwordx4 v252, s[16:19], s35 offen offset:2048 lds\n\t" \
  "s_add_u32 s33, s33, s32\n\t" \
  "s_add_u32 s34, s34, s32\n\t" \
  "s_add_u32 s35, s35, s32\n\t" \
  "s_sub_u32 s33, s33, s49\n\t" \
  "s_sub_u32 s34, s34, s49\n\t" \
  "s_sub_u32 s35, s35, s49\n\t" \
  "s_branch .Lcnx_g11\n\t" \
  ".Lcnx_fin:\n\t" \
  "v_mfma_f32_16x16x32_bf16 v[88:91], v[242:245], v[236:239], v[88:91]\n\t" \
  "ds_read_b64_tr_b16 v[214:215], v249 offset:7696\n\t" \
  "v_mfma_f32_16x16x32_bf16 v[92:95], v[240:243], v[236:239], v[92:95]\n\t" \
  "v_mfma_f32_16x16x32_bf16 v[0:3], v[208:211], v[178:181], v[0:3]\n\t" \
  "v_mfma_f32_16x16x32_bf16 v[24:27], v[208:211], v[186:189], v[24:27]\n\t" \
  "v_mfma_f32_16x16x32_bf16 v[48:51], v[208:211], v[194:197], v[48:51]\n\t" \
  "v_mfma_f32_16x16x32_bf16 v[72:75], v[208:211], v[202:205], v[72:75]\n\t" \
  "ds_read_b64_tr_b16 v[208:209], v249 offset:6216\n\t" \
  "s_waitcnt lgkmcnt(1)\n\t" \
  "v_mfma_f32_16x16x32_bf16 v[4:7], v[212:215], v[178:181], v[4:7]\n\t" \
  "ds_read_b64_tr_b16 v[210:211], v249 offset:7752\n\t" \
  "v_mfma_f32_16x16x32_bf16 v[28:31], v[212:215], v[186:189], v[28:31]\n\t" \
  "v_mfma_f32_16x16x32_bf16 v[52:55], v[212:215], v[194:197], v[52:55]\n\t" \
  "v_mfma_f32_16x16x32_bf16 v[76:79], v[212:215], v[202:205], v[76:79]\n\t" \
  "ds_read_b64_tr_b16 v[212:213], v249 offset:6224\n\t" \
  "s_waitcnt lgkmcnt(1)\n\t" \
  "v_mfma_f32_16x16x32_bf16 v[8:11], v[208:211], v[178:181], v[8:11]\n\t" \
  "ds_read_b64_tr_b16 v[214:215], v249 offset:7760\n\t" \
  "v_mfma_f32_16x16x32_bf16 v[32:35], v[208:211], v[186:189], v[32:35]\n\t" \
  "v_mfma_f32_16x16x32_bf16 v[56:59], v[208:211], v[194:197], v[56:59]\n\t" \
  "v_mfma_f32_16x16x32_bf16 v[80:83], v[208:211], v[202:205], v[80:83]\n\t" \
  "ds_read_b64_tr_b16 v[208:209], v249 offset:6280\n\t" \
  "v_cvt_pk_bf16_f32 v0, v0, v1\n\t" \
  "v_cvt_pk_bf16_f32 v1, v2, v3\n\t" \
  "v_cvt_pk_bf16_f32 v2, v4, v5\n\t" \
  "v_cvt_pk_bf16_f32 v3, v6, v7\n\t" \
  "buffer_store_dwordx4 v[0:3], v250, s[20:23], s28 offen offset:0 sc1\n\t" \
  "v_cvt_pk_bf16_f32 v24, v24, v25\n\t" \
  "v_cvt_pk_bf16_f32 v25, v26, v27\n\t" \
  "v_cvt_pk_bf16_f32 v26, v28, v29\n\t" \
  "v_cvt_pk_bf16_f32 v27, v30, v31\n\t" \
  "buffer_store_dwordx4 v[24:27], v250, s[20:23], s28 offen offset:192 sc1\n\t" \
  "v_cvt_pk_bf16_f32 v48, v48, v49\n\t" \
  "v_cvt_pk_bf16_f32 v49, v50, v51\n\t" \
  "v_cvt_pk_bf16_f32 v50, v52, v53\n\t" \
  "v_cvt_pk_bf16_f32 v51, v54, v55\n\t" \
  "buffer_store_dwordx4 v[48:51], v250, s[20:23], s29 offen offset:0 sc1\n\t" \
  "v_cvt_pk_bf16_f32 v72, v72, v73\n\t" \
  "v_cvt_pk_bf16_f32 v73, v74, v75\n\t" \
  "v_cvt_pk_bf16_f32 v74, v76, v77\n\t" \
  "v_cvt_pk_bf16_f32 v75, v78, v79\n\t" \
  "buffer_store_dwordx4 v[72:75], v250, s[20:23], s29 offen offset:192 sc1\n\t" \
  "s_waitcnt lgkmcnt(1)\n\t" \
  "v_mfma_f32_16x16x32_bf16 v[12:15], v[212:215], v[178:181], v[12:15]\n\t" \
  "ds_read_b64_tr_b16 v[210:211], v249 offset:7816\n\t" \
  "v_mfma_f32_16x16x32_bf16 v[36:39], v[212:215], v[186:189], v[36:39]\n\t" \
  "v_mfma_f32_16x16x32_bf16 v[60:63], v[212:215], v[194:197], v[60:63]\n\t" \
  "v_mfma_f32_16x16x32_bf16 v[84:87], v[212:215], v[202:205], v[84:87]\n\t" \
  "ds_read_b64_tr_b16 v[212:213], v249 offset:6288\n\t" \
  "s_waitcnt lgkmcnt(1)\n\t" \
  "v_mfma_f32_16x16x32_bf16 v[16:19], v[208:211], v[178:181], v[16:19]\n\t" \
  "ds_read_b64_tr_b16 v[214:215], v249 offset:7824\n\t" \
  "v_mfma_f32_16x16x32_bf16 v[40:43], v[208:211], v[186:189], v[40:43]\n\t" \
  "v_mfma_f32_16x16x32_bf16 v[64:67], v[208:211], v[194:197], v[64:67]\n\t" \
  "v_mfma_f32_16x16x32_bf16 v[88:91], v[208:211], v[202:205], v[88:91]\n\t" \
  "v_cvt_pk_bf16_f32 v8, v8, v9\n\t" \
  "v_cvt_pk_bf16_f32 v9, v10, v11\n\t" \
  "v_cvt_pk_bf16_f32 v10, v12, v13\n\t" \
  "v_cvt_pk_bf16_f32 v11, v14, v15\n\t" \
  "buffer_store_dwordx4 v[8:11], v250, s[20:23], s28 offen offset:64 sc1\n\t" \
  "v_cvt_pk_bf16_f32 v32, v32, v33\n\t" \
  "v_cvt_pk_bf16_f32 v33, v34, v35\n\t" \
  "v_cvt_pk_bf16_f32 v34, v36, v37\n\t" \
  "v_cvt_pk_bf16_f32 v35, v38, v39\n\t" \
  "buffer_store_dwordx4 v[32:35], v250, s[20:23], s28 offen offset:256 sc1\n\t" \
  "v_cvt_pk_bf16_f32 v56, v56, v57\n\t" \
  "v_cvt_pk_bf16_f32 v57, v58, v59\n\t" \
  "v_cvt_pk_bf16_f32 v58, v60, v61\n\t" \
  "v_cvt_pk_bf16_f32 v59, v62, v63\n\t" \
  "buffer_store_dwordx4 v[56:59], v250, s[20:23], s29 offen offset:64 sc1\n\t" \
  "v_cvt_pk_bf16_f32 v80, v80, v81\n\t" \
  "v_cvt_pk_bf16_f32 v81, v82, v83\n\t" \
  "v_cvt_pk_bf16_f32 v82, v84, v85\n\t" \
  "v_cvt_pk_bf16_f32 v83, v86, v87\n\t" \
  "buffer_store_dwordx4 v[80:83], v250, s[20:23], s29 offen offset:256 sc1\n\t" \
  "s_waitcnt lgkmcnt(0)\n\t" \
  "v_mfma_f32_16x16x32_bf16 v[20:23], v[212:215], v[178:181], v[20:23]\n\t" \
  "v_mfma_f32_16x16x32_bf16 v[44:47], v[212:215], v[186:189], v[44:47]\n\t" \
  "v_mfma_f32_16x16x32_bf16 v[68:71], v[212:215], v[194:197], v[68:71]\n\t" \
  "v_mfma_f32_16x16x32_bf16 v[92:95], v[212:215], v[202:205], v[92:95]\n\t" \
  "v_cvt_pk_bf16_f32 v16, v16, v17\n\t" \
  "v_cvt_pk_bf16_f32 v17, v18, v19\n\t" \
  "s_nop 2\n\t" \
  "v_cvt_pk_bf16_f32 v18, v20, v21\n\t" \
  "v_cvt_pk_bf16_f32 v19, v22, v23\n\t" \
  "buffer_store_dwordx4 v[16:19], v250, s[20:23], s28 offen offset:128 sc1\n\t" \
  "v_cvt_pk_bf16_f32 v40, v40, v41\n\t" \
  "v_cvt_pk_bf16_f32 v41, v42, v43\n\t" \
  "v_cvt_pk_bf16_f32 v42, v44, v45\n\t" \
  "v_cvt_pk_bf16_f32 v43, v46, v47\n\t" \
  "buffer_store_dwordx4 v[40:43], v250, s[20:23], s28 offen offset:320 sc1\n\t" \
  "v_cvt_pk_bf16_f32 v64, v64, v65\n\t" \
  "v_cvt_pk_bf16_f32 v65, v66, v67\n\t" \
  "v_cvt_pk_bf16_f32 v66, v68, v69\n\t" \
  "v_cvt_pk_bf16_f32 v67, v70, v71\n\t" \
  "buffer_store_dwordx4 v[64:67], v250, s[20:23], s29 offen offset:128 sc1\n\t" \
  "v_cvt_pk_bf16_f32 v88, v88, v89\n\t" \
  "v_cvt_pk_bf16_f32 v89, v90, v91\n\t" \
  "v_cvt_pk_bf16_f32 v90, v92, v93\n\t" \
  "v_cvt_pk_bf16_f32 v91, v94, v95\n\t" \
  "buffer_store_dwordx4 v[88:91], v250, s[20:23], s29 offen offset:320 sc1\n\t" \
  "s_waitcnt vmcnt(0)\n\t" \
  ".Lcnx_end:\n\t" \
  ""


__device__ __forceinline__ unsigned lds_addr(const char* p) { return (unsigned)(uintptr_t)(lds_ptr_t)p; }
__device__ __forceinline__ unsigned sg(unsigned v) { return __builtin_amdgcn_readfirstlane(v); }

struct TanhTab { float v[1024]; };
constexpr TanhTab tanh_tab(int k0) {
    TanhTab t = {};
    for (int k = k0; k < k0 + 1024; ++k) {
        double y = ((k & 255) + 0.375 + (k >= 256 ? 256 : 0)) * 0x1p-22;
        for (int e = 1; e < (k >> 8); ++e) y *= 2;
        const double v = 8 * y, a = 2 * 0.79788456080286536 * (v + 0.044715 * v * v * v) / 1024;
        double m = 1;
        for (int i = 12; i > 1; --i) m = 1 + m * a / i;
        m *= a;
        for (int i = 0; i < 10; ++i) m *= m + 2;
        t.v[k - k0] = (float)(m / (m + 2));
    }
    return t;
}
struct WaveTab { unsigned v244[64][4], v16[64][4], s32[32]; };
struct RegTab { unsigned v240[64][4], v248[64][4]; WaveTab w[8]; };
constexpr RegTab reg_tab() {
    RegTab t = {};
    for (unsigned l = 0; l < 64; ++l) {
        const unsigned G = l >> 4, n = l & 15, q = n >> 2, p = l & 3;
        t.v240[l][2 + (p >> 1)] = G == q ? (p & 1 ? 0x3f800000u : 0x3f80u) : 0u;
        t.v248[l][0] = WB + (l >> 2) * 32 + (p & 1) * 16 + (p >> 1) * 8;
        t.v248[l][1] = WB + (G & 1) * 3080 + ((G >> 1) * 4 + q) * 192 + p * 16;
        t.v248[l][2] = n * 384 + G * 16;
        t.v248[l][3] = PB + l * 8;
        for (unsigned w = 0; w < 8; ++w) {
            t.w[w].v244[l][2] = XOFF + n * 400 + (G & 1) * XROW + (G >> 1) * 192 + w * 24;
            t.w[w].v244[l][3] = DWOFF + p * 192 + (1 - (q >> 1)) * 24 + (1 + (G & 1) - (q & 1)) * 8 + ((G ^ p) >> 1) * 4 + w * 2304;
            t.w[w].v16[l][0] = w >= 4 ? (l < 48 ? l * 128 : OOB)
                             : w < 2  ? (12 * ((l >> 3) & 3) + ((l >> 1) & 3)) * (HID * 2) + (2 * (l & 1) + (l >> 5)) * 16 : l * 16;
            const unsigned d = DWOFF + (w * 8 + (l >> 5) * 4 + p) * 768 + ((l >> 2) & 7) * 24;
            t.w[w].v16[l][1] = w < 3 ? d + (l & 1) * 192 : XOFF - 32 + p * 8;
            t.w[w].v16[l][2] = d + (~l & 1) * 192;
            t.w[w].v16[l][3] = l & 1 ? 0x07060302u : 0x05040100u;
        }
    }
    for (unsigned w = 0; w < 8; ++w) {
        const bool w1 = w < 2;
        const unsigned wpd = w1 ? 4 * HID * 2 - 1024 : 0, wl = WB + (w & 1) * (w1 ? 3072 : 3080), wc = w1 ? 64 : 6144;
        const unsigned so = (w & 1) * (w1 ? 48 * HID * 2 : 3072);
        const unsigned s[32] = {wc, so, so + wpd, so + 2 * wpd, w, w >> 2, w, XOFF - 8 * PB + w * 12288,
                                (w & 3) * 4096 + (w < 4 ? 16400 : 0), w * 512, XOFF - 2 * PB + w * 1024,
                                XOFF - 2 * PB + (8 + (w & 3)) * 1024 + (w >> 2) * 8,
                                wl + (w1 ? WA + 12304 : WA), wl + (w1 ? 12304 : WA + 6152), wl + (w1 ? WA + 18448 : 0),
                                wl + (w1 ? WA + 18448 + 6144 : 6152), w1 ? 12 * wc : 0, w1 ? 0 : 12 * wc,
                                w1 ? 0u - 86104 : 0u - 36904, 3 * wc, (w - 3) * 24, (w - 3) * 2304, GH1, GHH, 0x3ffc, 3072, 0, 0, 0,
                                OOB, OOB, (unsigned)sizeof(RegTab) - 2 * PB};
        for (int i = 0; i < 32; ++i) t.w[w].s32[i] = s[i];
    }
    return t;
}
struct Tables { RegTab r; TanhTab t[4]; };
static_assert(sizeof(WaveTab) == 64 * 34 && sizeof(RegTab) == 19456, "table layout");
__device__ const __attribute__((aligned(128))) Tables TABLES = {reg_tab(), {tanh_tab(0), tanh_tab(1024), tanh_tab(2048), tanh_tab(3072)}};

#define R10(r, a) r #a "0", r #a "1", r #a "2", r #a "3", r #a "4", r #a "5", r #a "6", r #a "7", r #a "8", r #a "9"
#define V10(a) R10("v", a)
#define S10(a) R10("s", a)
#define MAIN_CLOB "memory", "scc", "vcc", "v1", "v2", "v3", "v4", "v5", "v6", "v7", "v8", "v9", V10(1), V10(2), V10(3), V10(4), V10(5),  \
    V10(6), V10(7), V10(8), V10(9), V10(10), V10(11), V10(12), V10(13), V10(14), V10(15), V10(16), V10(17), V10(18), V10(19),        \
    V10(20), V10(21), V10(22), V10(23), V10(24), "v250", "v251", "v252", "v253", "v254", "s4", "s5", "s8", "s9", S10(1),       \
    S10(2), S10(3), S10(4), S10(5), "s60", "s61", "s62", "s63", "s64", "s65", "s66", "s67"

__global__ __launch_bounds__(512, 2) void convnext_kernel(const __hip_bfloat16* __restrict__ x,
                                                          const __hip_bfloat16* __restrict__ dw,
                                                          const __hip_bfloat16* __restrict__ w1,
                                                          const __hip_bfloat16* __restrict__ w2,
                                                          __hip_bfloat16* __restrict__ out) {
    __shared__ __attribute__((aligned(16))) char smem[LDS_BYTES];

    unsigned tid = threadIdx.x, lds = lds_addr(smem);
    const unsigned bid = blockIdx.x;
    asm volatile(MAIN_ASM
                 : "+{v0}"(tid), "+{s3}"(lds)
                 : "{s[0:1]}"(__builtin_amdgcn_kernarg_segment_ptr()), "{s2}"(bid), "{s[6:7]}"(&TABLES)
                 : MAIN_CLOB);

}

}

extern "C" void kernel_launch(void* const* d_in, const int* in_sizes, int n_in, void* d_out, int out_size,
                              void* d_ws, size_t ws_size, hipStream_t stream) {
    (void)in_sizes; (void)n_in; (void)out_size; (void)d_ws; (void)ws_size;
    const __hip_bfloat16* x = (const __hip_bfloat16*)d_in[0];
    const __hip_bfloat16* dw = (const __hip_bfloat16*)d_in[1];
    const __hip_bfloat16* w1 = (const __hip_bfloat16*)d_in[2];
    const __hip_bfloat16* w2 = (const __hip_bfloat16*)d_in[3];
    __hip_bfloat16* out = (__hip_bfloat16*)d_out;
    convnext_kernel<<<256, 512, 0, stream>>>(x, dw, w1, w2, out);
}
```

```cpp
#include <hip/hip_runtime.h>
#include <hip/hip_bf16.h>
#include <stdint.h>

namespace {

typedef __attribute__((address_space(3))) void* lds_ptr_t;

constexpr int IMG = 512;
constexpr int CH = 96;
constexpr int HID = 384;
constexpr int ROWB = IMG * CH * 2;
constexpr int LUTN = 3841;
constexpr int IPOFF = 15376;
constexpr int DWOFF = IPOFF + 512;
constexpr int XOFF = DWOFF + 96 * 192 + 32;
constexpr int XROW = 8200;
constexpr int PB = XOFF + 10 * XROW;
constexpr int WB = XOFF + 9 * XROW;
constexpr int WA = 3 * XROW;
constexpr int CNTOFF = WB + WA + 18448 + 12288;
constexpr int PFOFF = CNTOFF + 144;
constexpr int LDS_BYTES = 160 * 1024;
static_assert(PB == 116352 && CNTOFF - PB == 47136 && LUTN * 4 <= IPOFF && XOFF % 16 == 0 && PFOFF + 48 * 4 <= LDS_BYTES, "LDS map");
constexpr unsigned OOB = 0x40000000u;

#define MAIN_ASM \
  "s_load_dwordx2 s[8:9], s[0:1], 0x0\n\t" \
  "s_load_dwordx2 s[12:13], s[0:1], 0x8\n\t" \
  "s_load_dwordx2 s[20:21], s[0:1], 0x20\n\t" \
  "v_readfirstlane_b32 s5, v0\n\t" \
  "v_and_b32 v21, 63, v0\n\t" \
  "s_mov_b32 m0, 0\n\t" \
  "s_cmp_lt_u32 s5, 128\n\t" \
  "s_cselect_b32 s3, 16, 24\n\t" \
  "v_lshlrev_b32 v22, 4, v21\n\t" \
  "s_load_dwordx2 s[16:17], s[0:1], s3\n\t" \
  "s_mul_i32 s4, s5, 34\n\t" \
  "s_add_u32 s66, s6, s4\n\t" \
  "s_addc_u32 s67, s7, 0\n\t" \
  "global_load_dwordx4 v[244:247], v22, s[66:67] offset:2048\n\t" \
  "global_load_dwordx4 v[16:19], v22, s[66:67] offset:3072\n\t" \
  "global_load_dwordx4 v[240:243], v22, s[6:7]\n\t" \
  "global_load_dwordx4 v[248:251], v22, s[6:7] offset:1024\n\t" \
  "s_cmp_lg_u32 s5, 192\n\t" \
  "s_cbranch_scc1 .Lcnx_nc\n\t" \
  "s_getpc_b64 s[14:15]\n\t" \
  ".Lcnx_pc:\n\t" \
  "s_lshr_b32 s3, s2, 3\n\t" \
  "s_lshl_b32 s3, s3, 11\n\t" \
  "v_lshl_add_u32 v1, v21, 5, s3\n\t" \
  "v_min_u32 v1, .Lcnx_end-.Lcnx_pc, v1\n\t" \
  "global_load_lds_dword v1, s[14:15]\n\t" \
  ".Lcnx_nc:\n\t" \
  "v_mul_u32_u24 v254, 0x147b, v0\n\t" \
  "s_bfe_u32 s27, s2, 0x40003\n\t" \
  "v_bfe_u32 v1, v0, 2, 3\n\t" \
  "s_mul_i32 s27, s27, 6144\n\t" \
  "v_lshrrev_b32 v254, 17, v254\n\t" \
  "s_sub_u32 s3, s27, 576\n\t" \
  "v_and_b32 v20, 3, v0\n\t" \
  "s_add_u32 s4, s27, 6720\n\t" \
  "v_sub_u32 v254, v0, v254\n\t" \
  "s_min_u32 s4, s4, 0x18000\n\t" \
  "v_and_b32 v2, 0x1e0, v0\n\t" \
  "s_and_b32 s24, s2, 7\n\t" \
  "v_lshl_add_u32 v254, v254, 4, s3\n\t" \
  "s_lshl_b32 s24, s24, 6\n\t" \
  "v_lshl_or_b32 v20, v20, 3, v2\n\t" \
  "s_mov_b32 s26, 0x18000\n\t" \
  "v_cmp_gt_u32 vcc, s4, v254\n\t" \
  "s_cmp_ge_u32 s2, 128\n\t" \
  "s_cselect_b32 s3, 7, 0\n\t" \
  "s_cselect_b32 s4, 0, 7\n\t" \
  "v_add_u32 v1, s3, v1\n\t" \
  "s_cselect_b32 s3, 28, 35\n\t" \
  "v_cndmask_b32 v254, 2.0, v254, vcc\n\t" \
  "s_cselect_b32 s26, s26, 0xfffe8000\n\t" \
  "v_xor_b32 v1, s4, v1\n\t" \
  "s_add_u32 s24, s24, s3\n\t" \
  "v_and_b32 v1, 7, v1\n\t" \
  "s_movk_i32 s3, 1344\n\t" \
  "s_mul_i32 s24, s24, 0x18000\n\t" \
  "v_mad_u32_u24 v1, v1, s3, v20\n\t" \
  "s_lshl4_add_u32 s25, s5, 34352\n\t" \
  "s_mov_b32 s10, 0x3000000\n\t" \
  "s_mov_b32 s11, 0x20000\n\t" \
  "s_mov_b32 s14, 9408\n\t" \
  "s_mov_b32 s15, 0x20000\n\t" \
  "s_mov_b32 s18, 73728\n\t" \
  "s_mov_b32 s19, 0x20000\n\t" \
  "s_mov_b32 s22, 0x3000000\n\t" \
  "s_mov_b32 s23, 0x20000\n\t" \
  "s_setreg_imm32_b32 hwreg(HW_REG_MODE, 4, 2), 0\n\t" \
  "s_setreg_imm32_b32 hwreg(HW_REG_MODE, 9, 1), 0\n\t" \
  "s_lshl_b32 s65, s26, 4\n\t" \
  "s_lshr_b32 s3, s5, 5\n\t" \
  "s_add_u32 s3, s3, 4\n\t" \
  "s_mul_i32 s3, s3, s26\n\t" \
  "s_add_u32 s28, s24, s27\n\t" \
  "s_add_u32 s28, s28, s3\n\t" \
  "s_add_u32 s29, s28, s26\n\t" \
  "s_bfe_u32 s3, s5, 0x20006\n\t" \
  "s_add_u32 s3, s3, 6\n\t" \
  "s_mul_i32 s3, s3, s26\n\t" \
  "s_add_u32 s30, s24, s65\n\t" \
  "s_add_u32 s30, s30, s3\n\t" \
  "s_lshl_b32 s31, s26, 2\n\t" \
  "s_mul_i32 s64, s26, 22\n\t" \
  "s_sub_u32 s64, s64, s65\n\t" \
  "s_cmp_ge_u32 s5, 256\n\t" \
  "s_cselect_b32 s27, s27, 0\n\t" \
  "s_mov_b64 vcc, 0\n\t" \
  "s_waitcnt lgkmcnt(0)\n\t" \
  "s_and_b32 s13, s13, 0xffff\n\t" \
  "s_and_b32 s9, s9, 0xffff\n\t" \
  "s_and_b32 s17, s17, 0xffff\n\t" \
  "s_cmp_ge_u32 s5, 192\n\t" \
  "s_cbranch_scc1 .Lcnx_nd\n\t" \
  "buffer_load_dwordx2 v[2:3], v1, s[12:15], 0 offen offset:0\n\t" \
  "buffer_load_dwordx2 v[4:5], v1, s[12:15], 0 offen offset:192\n\t" \
  "buffer_load_dwordx2 v[6:7], v1, s[12:15], 0 offen offset:384\n\t" \
  "buffer_load_dwordx2 v[8:9], v1, s[12:15], 0 offen offset:576\n\t" \
  "buffer_load_dwordx2 v[10:11], v1, s[12:15], 0 offen offset:768\n\t" \
  "buffer_load_dwordx2 v[12:13], v1, s[12:15], 0 offen offset:960\n\t" \
  "buffer_load_dwordx2 v[14:15], v1, s[12:15], 0 offen offset:1152\n\t" \
  ".Lcnx_nd:\n\t" \
  "s_and_b32 s3, s5, 0x140\n\t" \
  "s_cmp_lg_u32 s3, 64\n\t" \
  "s_cbranch_scc1 .Lcnx_nw\n\t" \
  "s_lshr_b32 s3, s2, 3\n\t" \
  "s_mul_i32 s3, s3, 2304\n\t" \
  "s_mov_b32 m0, 0\n\t" \
  "v_mul_u32_u24 v21, 36, v21\n\t" \
  "buffer_load_dword v21, s[16:19], s3 offen lds\n\t" \
  ".Lcnx_nw:\n\t" \
  "s_add_u32 m0, s25, 0\n\t" \
  "s_add_u32 s24, s24, s26\n\t" \
  "buffer_load_dwordx4 v254, s[8:11], s24 offen nt lds\n\t" \
  "s_add_u32 m0, s25, 8200\n\t" \
  "s_add_u32 s24, s24, s26\n\t" \
  "buffer_load_dwordx4 v254, s[8:11], s24 offen nt lds\n\t" \
  "s_add_u32 m0, s25, 16400\n\t" \
  "s_add_u32 s24, s24, s26\n\t" \
  "buffer_load_dwordx4 v254, s[8:11], s24 offen nt lds\n\t" \
  "s_add_u32 m0, s25, 24600\n\t" \
  "s_add_u32 s24, s24, s26\n\t" \
  "buffer_load_dwordx4 v254, s[8:11], s24 offen nt lds\n\t" \
  "s_load_dwordx16 s[32:47], s[66:67], 0x1000\n\t" \
  "s_load_dwordx16 s[48:63], s[66:67], 0x1040\n\t" \
  "s_and_b32 s21, s21, 0xffff\n\t" \
  "s_waitcnt vmcnt(4)\n\t" \
  "v_mfma_f32_32x32x16_bf16 v[144:159], v[242:245], v[240:243], 0\n\t" \
  "v_mfma_f32_32x32x16_bf16 v[160:175], v[242:245], v[240:243], 0\n\t" \
  "v_bfe_u32 v236, v251, 3, 2\n\t" \
  "v_mul_u32_u24 v236, 0x5555, v236\n\t" \
  "v_add_u32 v252, s27, v16\n\t" \
  "v_mov_b32 v253, v246\n\t" \
  "s_cmp_gt_u32 s5, 192\n\t" \
  "s_cbranch_scc1 .Lcnx_e2\n\t" \
  "s_cmp_eq_u32 s5, 192\n\t" \
  "s_cbranch_scc1 .Lcnx_e1\n\t" \
  "v_xor_b32 v23, 0x02020202, v19\n\t" \
  "v_perm_b32 v24, v4, 0, v19\n\t" \
  "v_perm_b32 v25, v12, v8, v19\n\t" \
  "v_perm_b32 v26, v6, v2, v19\n\t" \
  "v_perm_b32 v27, v14, v10, v19\n\t" \
  "v_perm_b32 v28, v8, v4, v19\n\t" \
  "v_perm_b32 v29, 0, v12, v19\n\t" \
  "ds_write_b64 v17, v[24:25] offset:0\n\t" \
  "ds_write_b64 v17, v[26:27] offset:8\n\t" \
  "ds_write_b64 v17, v[28:29] offset:16\n\t" \
  "v_perm_b32 v30, v4, 0, v23\n\t" \
  "v_perm_b32 v31, v12, v8, v23\n\t" \
  "v_perm_b32 v32, v6, v2, v23\n\t" \
  "v_perm_b32 v33, v14, v10, v23\n\t" \
  "v_perm_b32 v34, v8, v4, v23\n\t" \
  "v_perm_b32 v35, 0, v12, v23\n\t" \
  "ds_write_b64 v18, v[30:31] offset:0\n\t" \
  "ds_write_b64 v18, v[32:33] offset:8\n\t" \
  "ds_write_b64 v18, v[34:35] offset:16\n\t" \
  "v_perm_b32 v37, v5, 0, v19\n\t" \
  "v_perm_b32 v36, v13, v9, v19\n\t" \
  "v_perm_b32 v39, v7, v3, v19\n\t" \
  "v_perm_b32 v38, v15, v11, v19\n\t" \
  "v_perm_b32 v41, v9, v5, v19\n\t" \
  "v_perm_b32 v40, 0, v13, v19\n\t" \
  "ds_write_b64 v17, v[36:37] offset:384\n\t" \
  "ds_write_b64 v17, v[38:39] offset:392\n\t" \
  "ds_write_b64 v17, v[40:41] offset:400\n\t" \
  "v_perm_b32 v43, v5, 0, v23\n\t" \
  "v_perm_b32 v42, v13, v9, v23\n\t" \
  "v_perm_b32 v45, v7, v3, v23\n\t" \
  "v_perm_b32 v44, v15, v11, v23\n\t" \
  "v_perm_b32 v47, v9, v5, v23\n\t" \
  "v_perm_b32 v46, 0, v13, v23\n\t" \
  "ds_write_b64 v18, v[42:43] offset:384\n\t" \
  "ds_write_b64 v18, v[44:45] offset:392\n\t" \
  "ds_write_b64 v18, v[46:47] offset:400\n\t" \
  "s_branch .Lcnx_e3\n\t" \
  ".Lcnx_e1:\n\t" \
  "s_mov_b64 exec, 0x3ffff\n\t" \
  "ds_write_b64 v251, v[240:241] offset:47136\n\t" \
  "s_mov_b64 exec, 15\n\t" \
  "ds_write_b64 v17, v[240:241]\n\t" \
  "s_mov_b64 exec, -1\n\t" \
  "s_branch .Lcnx_e3\n\t" \
  ".Lcnx_e2:\n\t" \
  "s_setprio 1\n\t" \
  ".Lcnx_e3:\n\t" \
  "s_add_u32 m0, s25, 32800\n\t" \
  "s_add_u32 s24, s24, s26\n\t" \
  "buffer_load_dwordx4 v254, s[8:11], s24 offen nt lds\n\t" \
  "s_add_u32 m0, s25, 41000\n\t" \
  "s_add_u32 s24, s24, s26\n\t" \
  "buffer_load_dwordx4 v254, s[8:11], s24 offen nt lds\n\t" \
  "s_add_u32 m0, s25, 49200\n\t" \
  "s_add_u32 s24, s24, s26\n\t" \
  "buffer_load_dwordx4 v254, s[8:11], s24 offen nt lds\n\t" \
  "s_add_u32 m0, s25, 57400\n\t" \
  "s_add_u32 s24, s24, s26\n\t" \
  "buffer_load_dwordx4 v254, s[8:11], s24 offen nt lds\n\t" \
  "s_waitcnt vmcnt(6) lgkmcnt(0)\n\t" \
  "s_barrier\n\t" \
  "v_add_u32 v232, 1536, v247\n\t" \
  "ds_read2_b32 v[98:99], v247 offset0:0 offset1:6\n\t" \
  "ds_read2_b32 v[114:115], v247 offset0:192 offset1:198\n\t" \
  "ds_read2_b32 v[130:131], v232 offset0:0 offset1:6\n\t" \
  "ds_read2_b32 v[102:103], v247 offset0:12 offset1:18\n\t" \
  "ds_read2_b32 v[118:119], v247 offset0:204 offset1:210\n\t" \
  "ds_read2_b32 v[134:135], v232 offset0:12 offset1:18\n\t" \
  "ds_read2_b32 v[106:107], v247 offset0:24 offset1:30\n\t" \
  "ds_read2_b32 v[122:123], v247 offset0:216 offset1:222\n\t" \
  "ds_read2_b32 v[138:139], v232 offset0:24 offset1:30\n\t" \
  "ds_read2_b32 v[110:111], v247 offset0:36 offset1:42\n\t" \
  "ds_read2_b32 v[126:127], v247 offset0:228 offset1:234\n\t" \
  "ds_read2_b32 v[142:143], v232 offset0:36 offset1:42\n\t" \
  ".Lcnx_round:\n\t" \
  "v_add_u32 v232, 49200, v253\n\t" \
  "v_add_u32 v233, s41, v251\n\t" \
  "v_mfma_f32_32x32x16_bf16 v[176:191], v[242:245], v[240:243], 0\n\t" \
  "ds_read_b64 v[208:209], v253 offset:0\n\t" \
  "ds_read_b64 v[210:211], v253 offset:400\n\t" \
  "ds_read_b64 v[212:213], v253 offset:800\n\t" \
  "ds_read_b64 v[214:215], v253 offset:1200\n\t" \
  "ds_read_b64 v[216:217], v253 offset:8\n\t" \
  "ds_read_b64 v[218:219], v253 offset:408\n\t" \
  "ds_read_b64 v[220:221], v253 offset:808\n\t" \
  "ds_read_b64 v[222:223], v253 offset:1208\n\t" \
  "ds_read_b64 v[224:225], v253 offset:16\n\t" \
  "ds_read_b64 v[226:227], v253 offset:416\n\t" \
  "ds_read_b64 v[228:229], v253 offset:816\n\t" \
  "ds_read_b64 v[230:231], v253 offset:1216\n\t" \
  "s_cmp_lg_u32 s58, 0\n\t" \
  "s_cbranch_scc1 .Lcnx_c1\n\t" \
  "s_add_u32 m0, s25, 65600\n\t" \
  "s_add_u32 s24, s24, s26\n\t" \
  "buffer_load_dwordx4 v254, s[8:11], s24 offen nt lds\n\t" \
  "s_add_u32 m0, s25, 73800\n\t" \
  "s_add_u32 s24, s24, s26\n\t" \
  "buffer_load_dwordx4 v254, s[8:11], s24 offen nt lds\n\t" \
  "s_add_u32 m0, s25, 82000\n\t" \
  "s_add_u32 s24, s24, s26\n\t" \
  "buffer_load_dwordx4 v254, s[8:11], s24 offen nt lds\n\t" \
  "s_add_u32 m0, s25, 90200\n\t" \
  "s_add_u32 s24, s24, s26\n\t" \
  "buffer_load_dwordx4 v254, s[8:11], s24 offen nt lds\n\t" \
  ".Lcnx_c1:\n\t" \
  "s_waitcnt lgkmcnt(15)\n\t" \
  "v_and_b32 v96, 0xffff, v98\n\t" \
  "v_and_b32 v97, 0xffff0000, v98\n\t" \
  "v_and_b32 v98, 0xffff, v99\n\t" \
  "v_and_b32 v99, 0xffff0000, v99\n\t" \
  "v_and_b32 v112, 0xffff, v114\n\t" \
  "v_and_b32 v113, 0xffff0000, v114\n\t" \
  "v_and_b32 v114, 0xffff, v115\n\t" \
  "v_and_b32 v115, 0xffff0000, v115\n\t" \
  "v_and_b32 v128, 0xffff, v130\n\t" \
  "v_and_b32 v129, 0xffff0000, v130\n\t" \
  "v_and_b32 v130, 0xffff, v131\n\t" \
  "v_and_b32 v131, 0xffff0000, v131\n\t" \
  "s_waitcnt lgkmcnt(8)\n\t" \
  "v_smfmac_f32_16x16x64_bf16 v[144:147], v[96:99], v[208:215], v236\n\t" \
  "v_and_b32 v100, 0xffff, v102\n\t" \
  "v_and_b32 v101, 0xffff0000, v102\n\t" \
  "v_and_b32 v102, 0xffff, v103\n\t" \
  "v_and_b32 v103, 0xffff0000, v103\n\t" \
  "s_waitcnt lgkmcnt(4)\n\t" \
  "v_smfmac_f32_16x16x64_bf16 v[148:151], v[112:115], v[216:223], v236\n\t" \
  "v_and_b32 v116, 0xffff, v118\n\t" \
  "v_and_b32 v117, 0xffff0000, v118\n\t" \
  "v_and_b32 v118, 0xffff, v119\n\t" \
  "v_and_b32 v119, 0xffff0000, v119\n\t" \
  "s_cmp_lg_u32 s58, 1\n\t" \
  "s_cbranch_scc1 .Lcnx_c2\n\t" \
  "s_waitcnt vmcnt(5)\n\t" \
  ".Lcnx_c2:\n\t" \
  "s_waitcnt vmcnt(8) lgkmcnt(0)\n\t" \
  "s_barrier\n\t" \
  "ds_read_b64 v[208:209], v253 offset:16400\n\t" \
  "ds_read_b64 v[210:211], v253 offset:16800\n\t" \
  "ds_read_b64 v[212:213], v253 offset:17200\n\t" \
  "ds_read_b64 v[214:215], v253 offset:17600\n\t" \
  "ds_read_b64 v[216:217], v253 offset:16408\n\t" \
  "ds_read_b64 v[218:219], v253 offset:16808\n\t" \
  "ds_read_b64 v[220:221], v253 offset:17208\n\t" \
  "ds_read_b64 v[222:223], v253 offset:17608\n\t" \
  "v_smfmac_f32_16x16x64_bf16 v[152:155], v[128:131], v[224:231], v236\n\t" \
  "s_cmp_lg_u32 s58, 1\n\t" \
  "s_cbranch_scc1 .Lcnx_c3\n\t" \
  "s_add_u32 m0, s25, 73800\n\t" \
  "s_add_u32 s24, s24, s26\n\t" \
  "buffer_load_dwordx4 v254, s[8:11], s24 offen lds\n\t" \
  ".Lcnx_c3:\n\t" \
  "ds_read_b64 v[224:225], v253 offset:16416\n\t" \
  "ds_read_b64 v[226:227], v253 offset:16816\n\t" \
  "ds_read_b64 v[228:229], v253 offset:17216\n\t" \
  "v_and_b32 v132, 0xffff, v134\n\t" \
  "v_and_b32 v133, 0xffff0000, v134\n\t" \
  "v_and_b32 v134, 0xffff, v135\n\t" \
  "v_and_b32 v135, 0xffff0000, v135\n\t" \
  "s_waitcnt lgkmcnt(7)\n\t" \
  "v_smfmac_f32_16x16x64_bf16 v[156:159], v[96:99], v[208:215], v236\n\t" \
  "ds_read_b64 v[230:231], v253 offset:17616\n\t" \
  "v_and_b32 v104, 0xffff, v106\n\t" \
  "v_and_b32 v105, 0xffff0000, v106\n\t" \
  "v_and_b32 v106, 0xffff, v107\n\t" \
  "v_and_b32 v107, 0xffff0000, v107\n\t" \
  "v_smfmac_f32_16x16x64_bf16 v[144:147], v[100:103], v[208:215], v236\n\t" \
  "s_cmp_lg_u32 s58, 1\n\t" \
  "s_cbranch_scc1 .Lcnx_c4\n\t" \
  "s_add_u32 m0, s25, 82000\n\t" \
  "s_add_u32 s24, s24, s26\n\t" \
  "buffer_load_dwordx4 v254, s[8:11], s24 offen lds\n\t" \
  ".Lcnx_c4:\n\t" \
  "s_waitcnt lgkmcnt(4)\n\t" \
  "v_smfmac_f32_16x16x64_bf16 v[160:163], v[112:115], v[216:223], v236\n\t" \
  "v_and_b32 v120, 0xffff, v122\n\t" \
  "v_and_b32 v121, 0xffff0000, v122\n\t" \
  "v_and_b32 v122, 0xffff, v123\n\t" \
  "v_and_b32 v123, 0xffff0000, v123\n\t" \
  "v_smfmac_f32_16x16x64_bf16 v[148:151], v[116:119], v[216:223], v236\n\t" \
  "s_cmp_lg_u32 s58, 1\n\t" \
  "s_cbranch_scc1 .Lcnx_c5\n\t" \
  "s_add_u32 m0, s25, 90200\n\t" \
  "s_add_u32 s24, s24, s26\n\t" \
  "buffer_load_dwordx4 v254, s[8:11], s24 offen lds\n\t" \
  ".Lcnx_c5:\n\t" \
  "s_waitcnt vmcnt(6) lgkmcnt(0)\n\t" \
  "s_barrier\n\t" \
  "ds_read_b64 v[208:209], v253 offset:32800\n\t" \
  "ds_read_b64 v[210:211], v253 offset:33200\n\t" \
  "ds_read_b64 v[212:213], v253 offset:33600\n\t" \
  "ds_read_b64 v[214:215], v253 offset:34000\n\t" \
  "ds_read_b64 v[216:217], v253 offset:32808\n\t" \
  "ds_read_b64 v[218:219], v253 offset:33208\n\t" \
  "ds_read_b64 v[220:221], v253 offset:33608\n\t" \
  "ds_read_b64 v[222:223], v253 offset:34008\n\t" \
  "v_smfmac_f32_16x16x64_bf16 v[164:167], v[128:131], v[224:231], v236\n\t" \
  "v_and_b32 v136, 0xffff, v138\n\t" \
  "v_and_b32 v137, 0xffff0000, v138\n\t" \
  "v_and_b32 v138, 0xffff, v139\n\t" \
  "v_and_b32 v139, 0xffff0000, v139\n\t" \
  "v_cvt_pk_bf16_f32 v48, v48, v49\n\t" \
  "v_cvt_pk_bf16_f32 v49, v50, v51\n\t" \
  "v_cvt_pk_bf16_f32 v50, v52, v53\n\t" \
  "v_cvt_pk_bf16_f32 v51, v54, v55\n\t" \
  "v_cvt_pk_bf16_f32 v52, v0, v1\n\t" \
  "v_cvt_pk_bf16_f32 v53, v2, v3\n\t" \
  "s_add_u32 m0, s25, 0\n\t" \
  "s_add_u32 s24, s24, s26\n\t" \
  "v_smfmac_f32_16x16x64_bf16 v[152:155], v[132:135], v[224:231], v236\n\t" \
  "buffer_load_dwordx4 v254, s[8:11], s24 offen lds\n\t" \
  "ds_read_b64 v[224:225], v253 offset:32816\n\t" \
  "ds_read_b64 v[226:227], v253 offset:33216\n\t" \
  "ds_read_b64 v[228:229], v253 offset:33616\n\t" \
  "v_cvt_pk_bf16_f32 v54, v4, v5\n\t" \
  "v_cvt_pk_bf16_f32 v55, v6, v7\n\t" \
  "v_cvt_pk_bf16_f32 v56, v56, v57\n\t" \
  "s_waitcnt lgkmcnt(7)\n\t" \
  "v_smfmac_f32_16x16x64_bf16 v[168:171], v[96:99], v[208:215], v236\n\t" \
  "ds_read_b64 v[230:231], v253 offset:34016\n\t" \
  "v_and_b32 v108, 0xffff, v110\n\t" \
  "v_and_b32 v109, 0xffff0000, v110\n\t" \
  "v_and_b32 v110, 0xffff, v111\n\t" \
  "v_and_b32 v111, 0xffff0000, v111\n\t" \
  "v_cvt_pk_bf16_f32 v57, v58, v59\n\t" \
  "v_cvt_pk_bf16_f32 v58, v60, v61\n\t" \
  "v_cvt_pk_bf16_f32 v59, v62, v63\n\t" \
  "v_smfmac_f32_16x16x64_bf16 v[156:159], v[100:103], v[208:215], v236\n\t" \
  "v_cvt_pk_bf16_f32 v60, v8, v9\n\t" \
  "v_cvt_pk_bf16_f32 v61, v10, v11\n\t" \
  "v_cvt_pk_bf16_f32 v62, v12, v13\n\t" \
  "s_add_u32 m0, s25, 8200\n\t" \
  "s_add_u32 s24, s24, s26\n\t" \
  "v_smfmac_f32_16x16x64_bf16 v[144:147], v[104:107], v[208:215], v236\n\t" \
  "buffer_load_dwordx4 v254, s[8:11], s24 offen lds\n\t" \
  "v_cvt_pk_bf16_f32 v63, v14, v15\n\t" \
  "v_mfma_f32_32x32x16_bf16 v[0:15], v[242:245], v[240:243], 0\n\t" \
  "s_waitcnt lgkmcnt(4)\n\t" \
  "v_smfmac_f32_16x16x64_bf16 v[172:175], v[112:115], v[216:223], v236\n\t" \
  "v_and_b32 v124, 0xffff, v126\n\t" \
  "v_and_b32 v125, 0xffff0000, v126\n\t" \
  "v_and_b32 v126, 0xffff, v127\n\t" \
  "v_and_b32 v127, 0xffff0000, v127\n\t" \
  "s_add_u32 m0, s25, 16400\n\t" \
  "s_add_u32 s24, s24, s26\n\t" \
  "v_smfmac_f32_16x16x64_bf16 v[160:163], v[116:119], v[216:223], v236\n\t" \
  "buffer_load_dwordx4 v254, s[8:11], s24 offen lds\n\t" \
  "v_smfmac_f32_16x16x64_bf16 v[148:151], v[120:123], v[216:223], v236\n\t" \
  "s_waitcnt vmcnt(7) lgkmcnt(0)\n\t" \
  "s_barrier\n\t" \
  "ds_read_b64 v[208:209], v232 offset:0\n\t" \
  "ds_read_b64 v[210:211], v232 offset:400\n\t" \
  "ds_read_b64 v[212:213], v232 offset:800\n\t" \
  "ds_read_b64 v[214:215], v232 offset:1200\n\t" \
  "ds_read_b64 v[216:217], v232 offset:8\n\t" \
  "ds_read_b64 v[218:219], v232 offset:408\n\t" \
  "ds_read_b64 v[220:221], v232 offset:808\n\t" \
  "ds_read_b64 v[222:223], v232 offset:1208\n\t" \
  "v_smfmac_f32_16x16x64_bf16 v[176:179], v[128:131], v[224:231], v236\n\t" \
  "v_and_b32 v140, 0xffff, v142\n\t" \
  "v_and_b32 v141, 0xffff0000, v142\n\t" \
  "v_and_b32 v142, 0xffff, v143\n\t" \
  "v_and_b32 v143, 0xffff0000, v143\n\t" \
  "s_add_u32 m0, s25, 24600\n\t" \
  "s_add_u32 s24, s24, s26\n\t" \
  "v_smfmac_f32_16x16x64_bf16 v[164:167], v[132:135], v[224:231], v236\n\t" \
  "buffer_load_dwordx4 v254, s[8:11], s24 offen lds\n\t" \
  "v_smfmac_f32_16x16x64_bf16 v[152:155], v[136:139], v[224:231], v236\n\t" \
  "ds_read_b64 v[224:225], v232 offset:16\n\t" \
  "s_waitcnt lgkmcnt(5)\n\t" \
  "v_smfmac_f32_16x16x64_bf16 v[180:183], v[96:99], v[208:215], v236\n\t" \
  "ds_read_b64 v[226:227], v232 offset:416\n\t" \
  "v_smfmac_f32_16x16x64_bf16 v[168:171], v[100:103], v[208:215], v236\n\t" \
  "ds_read_b64 v[228:229], v232 offset:816\n\t" \
  "v_cvt_pk_bf16_f32 v64, v64, v65\n\t" \
  "v_cvt_pk_bf16_f32 v65, v66, v67\n\t" \
  "v_cvt_pk_bf16_f32 v66, v68, v69\n\t" \
  "v_cvt_pk_bf16_f32 v67, v70, v71\n\t" \
  "v_cvt_pk_bf16_f32 v68, v16, v17\n\t" \
  "v_cvt_pk_bf16_f32 v69, v18, v19\n\t" \
  "s_add_u32 m0, s25, 32800\n\t" \
  "s_add_u32 s24, s24, s26\n\t" \
  "v_smfmac_f32_16x16x64_bf16 v[156:159], v[104:107], v[208:215], v236\n\t" \
  "buffer_load_dwordx4 v254, s[8:11], s24 offen lds\n\t" \
  "ds_read_b64 v[230:231], v232 offset:1216\n\t" \
  "v_cvt_pk_bf16_f32 v70, v20, v21\n\t" \
  "v_cvt_pk_bf16_f32 v71, v22, v23\n\t" \
  "v_cvt_pk_bf16_f32 v72, v72, v73\n\t" \
  "v_smfmac_f32_16x16x64_bf16 v[144:147], v[108:111], v[208:215], v236\n\t" \
  "v_cvt_pk_bf16_f32 v73, v74, v75\n\t" \
  "v_cvt_pk_bf16_f32 v74, v76, v77\n\t" \
  "v_cvt_pk_bf16_f32 v75, v78, v79\n\t" \
  "s_waitcnt lgkmcnt(4)\n\t" \
  "v_smfmac_f32_16x16x64_bf16 v[184:187], v[112:115], v[216:223], v236\n\t" \
  "v_cvt_pk_bf16_f32 v76, v24, v25\n\t" \
  "v_cvt_pk_bf16_f32 v77, v26, v27\n\t" \
  "v_cvt_pk_bf16_f32 v78, v28, v29\n\t" \
  "v_smfmac_f32_16x16x64_bf16 v[172:175], v[116:119], v[216:223], v236\n\t" \
  "v_cvt_pk_bf16_f32 v79, v30, v31\n\t" \
  "v_mfma_f32_32x32x16_bf16 v[16:31], v[242:245], v[240:243], 0\n\t" \
  "s_add_u32 m0, s25, 41000\n\t" \
  "s_add_u32 s24, s24, s26\n\t" \
  "v_smfmac_f32_16x16x64_bf16 v[160:163], v[120:123], v[216:223], v236\n\t" \
  "buffer_load_dwordx4 v254, s[8:11], s24 offen lds\n\t" \
  "v_mul_f32 v193, v144, v144\n\t" \
  "v_add_f32 v192, v144, v145\n\t" \
  "v_fmac_f32 v193, v145, v145\n\t" \
  "v_add_f32 v192, v192, v146\n\t" \
  "v_smfmac_f32_16x16x64_bf16 v[148:151], v[124:127], v[216:223], v236\n\t" \
  "v_fmac_f32 v193, v146, v146\n\t" \
  "v_add_f32 v192, v192, v147\n\t" \
  "v_fmac_f32 v193, v147, v147\n\t" \
  "s_waitcnt vmcnt(8) lgkmcnt(0)\n\t" \
  "s_barrier\n\t" \
  "ds_read_b64 v[208:209], v232 offset:16400\n\t" \
  "ds_read_b64 v[210:211], v232 offset:16800\n\t" \
  "ds_read_b64 v[212:213], v232 offset:17200\n\t" \
  "ds_read_b64 v[214:215], v232 offset:17600\n\t" \
  "ds_read_b64 v[216:217], v232 offset:16408\n\t" \
  "ds_read_b64 v[218:219], v232 offset:16808\n\t" \
  "ds_read_b64 v[220:221], v232 offset:17208\n\t" \
  "ds_read_b64 v[222:223], v232 offset:17608\n\t" \
  "v_smfmac_f32_16x16x64_bf16 v[188:191], v[128:131], v[224:231], v236\n\t" \
  "s_add_u32 m0, s25, 49200\n\t" \
  "s_add_u32 s24, s24, s26\n\t" \
  "v_smfmac_f32_16x16x64_bf16 v[176:179], v[132:135], v[224:231], v236\n\t" \
  "buffer_load_dwordx4 v254, s[8:11], s24 offen lds\n\t" \
  "v_smfmac_f32_16x16x64_bf16 v[164:167], v[136:139], v[224:231], v236\n\t" \
  "v_add_f32 v192, v192, v148\n\t" \
  "v_fmac_f32 v193, v148, v148\n\t" \
  "v_add_f32 v192, v192, v149\n\t" \
  "v_fmac_f32 v193, v149, v149\n\t" \
  "v_add_f32 v192, v192, v150\n\t" \
  "v_fmac_f32 v193, v150, v150\n\t" \
  "v_smfmac_f32_16x16x64_bf16 v[152:155], v[140:143], v[224:231], v236\n\t" \
  "ds_read_b64 v[224:225], v232 offset:16416\n\t" \
  "v_add_f32 v192, v192, v151\n\t" \
  "v_fmac_f32 v193, v151, v151\n\t" \
  "s_waitcnt lgkmcnt(5)\n\t" \
  "v_smfmac_f32_16x16x64_bf16 v[0:3], v[96:99], v[208:215], v236\n\t" \
  "ds_read_b64 v[226:227], v232 offset:16816\n\t" \
  "v_smfmac_f32_16x16x64_bf16 v[180:183], v[100:103], v[208:215], v236\n\t" \
  "ds_read_b64 v[228:229], v232 offset:17216\n\t" \
  "v_cvt_pk_bf16_f32 v80, v80, v81\n\t" \
  "v_cvt_pk_bf16_f32 v81, v82, v83\n\t" \
  "v_cvt_pk_bf16_f32 v82, v84, v85\n\t" \
  "v_cvt_pk_bf16_f32 v83, v86, v87\n\t" \
  "v_cvt_pk_bf16_f32 v84, v32, v33\n\t" \
  "v_cvt_pk_bf16_f32 v85, v34, v35\n\t" \
  "s_add_u32 m0, s25, 57400\n\t" \
  "s_add_u32 s24, s24, s26\n\t" \
  "v_smfmac_f32_16x16x64_bf16 v[168:171], v[104:107], v[208:215], v236\n\t" \
  "buffer_load_dwordx4 v254, s[8:11], s24 offen lds\n\t" \
  "ds_read_b64 v[230:231], v232 offset:17616\n\t" \
  "v_add_f32 v192, v192, v152\n\t" \
  "v_cvt_pk_bf16_f32 v86, v36, v37\n\t" \
  "v_fmac_f32 v193, v152, v152\n\t" \
  "v_smfmac_f32_16x16x64_bf16 v[156:159], v[108:111], v[208:215], v236\n\t" \
  "v_add_f32 v192, v192, v153\n\t" \
  "v_cvt_pk_bf16_f32 v87, v38, v39\n\t" \
  "v_fmac_f32 v193, v153, v153\n\t" \
  "s_waitcnt lgkmcnt(4)\n\t" \
  "v_smfmac_f32_16x16x64_bf16 v[4:7], v[112:115], v[216:223], v236\n\t" \
  "v_cvt_pk_bf16_f32 v88, v88, v89\n\t" \
  "v_add_f32 v192, v192, v154\n\t" \
  "v_cvt_pk_bf16_f32 v89, v90, v91\n\t" \
  "v_smfmac_f32_16x16x64_bf16 v[184:187], v[116:119], v[216:223], v236\n\t" \
  "v_cvt_pk_bf16_f32 v90, v92, v93\n\t" \
  "v_fmac_f32 v193, v154, v154\n\t" \
  "v_cvt_pk_bf16_f32 v91, v94, v95\n\t" \
  "v_smfmac_f32_16x16x64_bf16 v[172:175], v[120:123], v[216:223], v236\n\t" \
  "v_cvt_pk_bf16_f32 v92, v40, v41\n\t" \
  "v_add_f32 v192, v192, v155\n\t" \
  "v_cvt_pk_bf16_f32 v93, v42, v43\n\t" \
  "v_smfmac_f32_16x16x64_bf16 v[160:163], v[124:127], v[216:223], v236\n\t" \
  "v_cvt_pk_bf16_f32 v94, v44, v45\n\t" \
  "v_fmac_f32 v193, v155, v155\n\t" \
  "v_cvt_pk_bf16_f32 v95, v46, v47\n\t" \
  "s_waitcnt vmcnt(8) lgkmcnt(0)\n\t" \
  "s_barrier\n\t" \
  "ds_read_b64 v[208:209], v232 offset:32800\n\t" \
  "ds_read_b64 v[210:211], v232 offset:33200\n\t" \
  "ds_read_b64 v[212:213], v232 offset:33600\n\t" \
  "ds_read_b64 v[214:215], v232 offset:34000\n\t" \
  "ds_read_b64 v[216:217], v232 offset:32808\n\t" \
  "ds_read_b64 v[218:219], v232 offset:33208\n\t" \
  "ds_read_b64 v[220:221], v232 offset:33608\n\t" \
  "ds_read_b64 v[222:223], v232 offset:34008\n\t" \
  "s_add_u32 m0, s25, 65600\n\t" \
  "s_add_u32 s24, s24, s26\n\t" \
  "v_smfmac_f32_16x16x64_bf16 v[8:11], v[128:131], v[224:231], v236\n\t" \
  "buffer_load_dwordx4 v254, s[8:11], s24 offen lds\n\t" \
  "v_mul_f32 v195, v156, v156\n\t" \
  "ds_write_b64 v233, v[192:193] offset:16400\n\t" \
  "v_mfma_f32_32x32x16_bf16 v[32:47], v[242:245], v[240:243], 0\n\t" \
  "v_smfmac_f32_16x16x64_bf16 v[188:191], v[132:135], v[224:231], v236\n\t" \
  "v_add_f32 v194, v156, v157\n\t" \
  "v_fmac_f32 v195, v157, v157\n\t" \
  "v_add_f32 v194, v194, v158\n\t" \
  "v_smfmac_f32_16x16x64_bf16 v[176:179], v[136:139], v[224:231], v236\n\t" \
  "v_fmac_f32 v195, v158, v158\n\t" \
  "v_add_f32 v194, v194, v159\n\t" \
  "v_fmac_f32 v195, v159, v159\n\t" \
  "v_smfmac_f32_16x16x64_bf16 v[164:167], v[140:143], v[224:231], v236\n\t" \
  "ds_read_b64 v[224:225], v232 offset:32816\n\t" \
  "v_add_f32 v194, v194, v160\n\t" \
  "v_fmac_f32 v195, v160, v160\n\t" \
  "v_add_f32 v194, v194, v161\n\t" \
  "s_waitcnt lgkmcnt(6)\n\t" \
  "s_add_u32 m0, s25, 73800\n\t" \
  "s_add_u32 s24, s24, s26\n\t" \
  "v_smfmac_f32_16x16x64_bf16 v[12:15], v[96:99], v[208:215], v236\n\t" \
  "buffer_load_dwordx4 v254, s[8:11], s24 offen lds\n\t" \
  "ds_read_b64 v[226:227], v232 offset:33216\n\t" \
  "v_fmac_f32 v195, v161, v161\n\t" \
  "v_add_f32 v194, v194, v162\n\t" \
  "v_fmac_f32 v195, v162, v162\n\t" \
  "v_smfmac_f32_16x16x64_bf16 v[0:3], v[100:103], v[208:215], v236\n\t" \
  "ds_read_b64 v[228:229], v232 offset:33616\n\t" \
  "v_add_f32 v194, v194, v163\n\t" \
  "v_fmac_f32 v195, v163, v163\n\t" \
  "v_smfmac_f32_16x16x64_bf16 v[180:183], v[104:107], v[208:215], v236\n\t" \
  "buffer_store_dwordx4 v[52:55], v250, s[20:23], s61 offen offset:0 sc1\n\t" \
  "ds_read_b64 v[230:231], v232 offset:34016\n\t" \
  "v_add_f32 v194, v194, v164\n\t" \
  "v_fmac_f32 v195, v164, v164\n\t" \
  "v_add_f32 v194, v194, v165\n\t" \
  "v_smfmac_f32_16x16x64_bf16 v[168:171], v[108:111], v[208:215], v236\n\t" \
  "v_fmac_f32 v195, v165, v165\n\t" \
  "v_add_f32 v194, v194, v166\n\t" \
  "v_fmac_f32 v195, v166, v166\n\t" \
  "s_waitcnt lgkmcnt(5)\n\t" \
  "v_smfmac_f32_16x16x64_bf16 v[16:19], v[112:115], v[216:223], v236\n\t" \
  "v_add_f32 v194, v194, v167\n\t" \
  "v_fmac_f32 v195, v167, v167\n\t" \
  "v_smfmac_f32_16x16x64_bf16 v[4:7], v[116:119], v[216:223], v236\n\t" \
  "ds_write_b64 v233, v[194:195] offset:20496\n\t" \
  "v_smfmac_f32_16x16x64_bf16 v[184:187], v[120:123], v[216:223], v236\n\t" \
  "v_mul_f32 v197, v168, v168\n\t" \
  "v_add_f32 v196, v168, v169\n\t" \
  "v_fmac_f32 v197, v169, v169\n\t" \
  "v_add_f32 v196, v196, v170\n\t" \
  "v_fmac_f32 v197, v170, v170\n\t" \
  "v_add_f32 v196, v196, v171\n\t" \
  "v_smfmac_f32_16x16x64_bf16 v[172:175], v[124:127], v[216:223], v236\n\t" \
  "v_fmac_f32 v197, v171, v171\n\t" \
  "s_waitcnt vmcnt(9) lgkmcnt(0)\n\t" \
  "s_barrier\n\t" \
  "ds_read_b64 v[208:209], v253 offset:0\n\t" \
  "ds_read_b64 v[210:211], v253 offset:400\n\t" \
  "ds_read_b64 v[212:213], v253 offset:800\n\t" \
  "ds_read_b64 v[214:215], v253 offset:1200\n\t" \
  "ds_read_b64 v[216:217], v253 offset:8\n\t" \
  "ds_read_b64 v[218:219], v253 offset:408\n\t" \
  "ds_read_b64 v[220:221], v253 offset:808\n\t" \
  "ds_read_b64 v[222:223], v253 offset:1208\n\t" \
  "v_smfmac_f32_16x16x64_bf16 v[20:23], v[128:131], v[224:231], v236\n\t" \
  "v_smfmac_f32_16x16x64_bf16 v[8:11], v[132:135], v[224:231], v236\n\t" \
  "buffer_store_dwordx4 v[60:63], v250, s[20:23], s61 offen offset:64 sc1\n\t" \
  "v_smfmac_f32_16x16x64_bf16 v[188:191], v[136:139], v[224:231], v236\n\t" \
  "v_add_f32 v196, v196, v172\n\t" \
  "v_fmac_f32 v197, v172, v172\n\t" \
  "v_add_f32 v196, v196, v173\n\t" \
  "v_fmac_f32 v197, v173, v173\n\t" \
  "v_add_f32 v196, v196, v174\n\t" \
  "v_fmac_f32 v197, v174, v174\n\t" \
  "v_smfmac_f32_16x16x64_bf16 v[176:179], v[140:143], v[224:231], v236\n\t" \
  "ds_read_b64 v[224:225], v253 offset:16\n\t" \
  "v_add_f32 v196, v196, v175\n\t" \
  "v_fmac_f32 v197, v175, v175\n\t" \
  "s_waitcnt lgkmcnt(5)\n\t" \
  "v_smfmac_f32_16x16x64_bf16 v[24:27], v[96:99], v[208:215], v236\n\t" \
  "ds_read_b64 v[226:227], v253 offset:416\n\t" \
  "v_smfmac_f32_16x16x64_bf16 v[12:15], v[100:103], v[208:215], v236\n\t" \
  "ds_read_b64 v[228:229], v253 offset:816\n\t" \
  "v_smfmac_f32_16x16x64_bf16 v[0:3], v[104:107], v[208:215], v236\n\t" \
  "ds_read_b64 v[230:231], v253 offset:1216\n\t" \
  "v_add_f32 v196, v196, v176\n\t" \
  "v_fmac_f32 v197, v176, v176\n\t" \
  "v_add_f32 v196, v196, v177\n\t" \
  "v_fmac_f32 v197, v177, v177\n\t" \
  "v_add_f32 v196, v196, v178\n\t" \
  "v_fmac_f32 v197, v178, v178\n\t" \
  "v_smfmac_f32_16x16x64_bf16 v[180:183], v[108:111], v[208:215], v236\n\t" \
  "v_add_f32 v196, v196, v179\n\t" \
  "v_fmac_f32 v197, v179, v179\n\t" \
  "s_waitcnt lgkmcnt(4)\n\t" \
  "v_smfmac_f32_16x16x64_bf16 v[28:31], v[112:115], v[216:223], v236\n\t" \
  "buffer_store_dwordx4 v[68:71], v250, s[20:23], s61 offen offset:128 sc1\n\t" \
  "v_smfmac_f32_16x16x64_bf16 v[16:19], v[116:119], v[216:223], v236\n\t" \
  "ds_write_b64 v233, v[196:197] offset:24592\n\t" \
  "v_smfmac_f32_16x16x64_bf16 v[4:7], v[120:123], v[216:223], v236\n\t" \
  "v_mul_f32 v199, v180, v180\n\t" \
  "v_add_f32 v198, v180, v181\n\t" \
  "v_fmac_f32 v199, v181, v181\n\t" \
  "v_add_f32 v198, v198, v182\n\t" \
  "v_fmac_f32 v199, v182, v182\n\t" \
  "v_add_f32 v198, v198, v183\n\t" \
  "v_smfmac_f32_16x16x64_bf16 v[184:187], v[124:127], v[216:223], v236\n\t" \
  "v_fmac_f32 v199, v183, v183\n\t" \
  "s_waitcnt vmcnt(9) lgkmcnt(0)\n\t" \
  "s_barrier\n\t" \
  "ds_read_b64 v[208:209], v253 offset:16400\n\t" \
  "ds_read_b64 v[210:211], v253 offset:16800\n\t" \
  "ds_read_b64 v[212:213], v253 offset:17200\n\t" \
  "ds_read_b64 v[214:215], v253 offset:17600\n\t" \
  "ds_read_b64 v[216:217], v253 offset:16408\n\t" \
  "ds_read_b64 v[218:219], v253 offset:16808\n\t" \
  "ds_read_b64 v[220:221], v253 offset:17208\n\t" \
  "ds_read_b64 v[222:223], v253 offset:17608\n\t" \
  "v_smfmac_f32_16x16x64_bf16 v[32:35], v[128:131], v[224:231], v236\n\t" \
  "v_smfmac_f32_16x16x64_bf16 v[20:23], v[132:135], v[224:231], v236\n\t" \
  "v_smfmac_f32_16x16x64_bf16 v[8:11], v[136:139], v[224:231], v236\n\t" \
  "v_add_f32 v198, v198, v184\n\t" \
  "v_fmac_f32 v199, v184, v184\n\t" \
  "v_add_f32 v198, v198, v185\n\t" \
  "v_fmac_f32 v199, v185, v185\n\t" \
  "v_add_f32 v198, v198, v186\n\t" \
  "v_smfmac_f32_16x16x64_bf16 v[188:191], v[140:143], v[224:231], v236\n\t" \
  "buffer_store_dwordx4 v[76:79], v250, s[20:23], s61 offen offset:192 sc1\n\t" \
  "ds_read_b64 v[224:225], v253 offset:16416\n\t" \
  "v_fmac_f32 v199, v186, v186\n\t" \
  "v_add_f32 v198, v198, v187\n\t" \
  "v_fmac_f32 v199, v187, v187\n\t" \
  "s_waitcnt lgkmcnt(5)\n\t" \
  "v_smfmac_f32_16x16x64_bf16 v[36:39], v[96:99], v[208:215], v236\n\t" \
  "ds_read_b64 v[226:227], v253 offset:16816\n\t" \
  "v_smfmac_f32_16x16x64_bf16 v[24:27], v[100:103], v[208:215], v236\n\t" \
  "ds_read_b64 v[228:229], v253 offset:17216\n\t" \
  "v_smfmac_f32_16x16x64_bf16 v[12:15], v[104:107], v[208:215], v236\n\t" \
  "ds_read_b64 v[230:231], v253 offset:17616\n\t" \
  "v_add_f32 v198, v198, v188\n\t" \
  "v_fmac_f32 v199, v188, v188\n\t" \
  "v_add_f32 v198, v198, v189\n\t" \
  "v_fmac_f32 v199, v189, v189\n\t" \
  "v_add_f32 v198, v198, v190\n\t" \
  "v_fmac_f32 v199, v190, v190\n\t" \
  "v_smfmac_f32_16x16x64_bf16 v[0:3], v[108:111], v[208:215], v236\n\t" \
  "v_add_f32 v198, v198, v191\n\t" \
  "v_fmac_f32 v199, v191, v191\n\t" \
  "s_waitcnt lgkmcnt(4)\n\t" \
  "v_smfmac_f32_16x16x64_bf16 v[40:43], v[112:115], v[216:223], v236\n\t" \
  "ds_write_b64 v233, v[198:199] offset:28688\n\t" \
  "v_smfmac_f32_16x16x64_bf16 v[28:31], v[116:119], v[216:223], v236\n\t" \
  "v_smfmac_f32_16x16x64_bf16 v[16:19], v[120:123], v[216:223], v236\n\t" \
  "buffer_store_dwordx4 v[84:87], v250, s[20:23], s61 offen offset:256 sc1\n\t" \
  "v_mul_f32 v201, v0, v0\n\t" \
  "v_add_f32 v200, v0, v1\n\t" \
  "v_fmac_f32 v201, v1, v1\n\t" \
  "v_add_f32 v200, v200, v2\n\t" \
  "v_fmac_f32 v201, v2, v2\n\t" \
  "v_add_f32 v200, v200, v3\n\t" \
  "v_smfmac_f32_16x16x64_bf16 v[4:7], v[124:127], v[216:223], v236\n\t" \
  "v_fmac_f32 v201, v3, v3\n\t" \
  "s_waitcnt vmcnt(9) lgkmcnt(0)\n\t" \
  "s_barrier\n\t" \
  "ds_read_b64 v[208:209], v253 offset:32800\n\t" \
  "ds_read_b64 v[210:211], v253 offset:33200\n\t" \
  "ds_read_b64 v[212:213], v253 offset:33600\n\t" \
  "ds_read_b64 v[214:215], v253 offset:34000\n\t" \
  "ds_read_b64 v[216:217], v253 offset:32808\n\t" \
  "ds_read_b64 v[218:219], v253 offset:33208\n\t" \
  "ds_read_b64 v[220:221], v253 offset:33608\n\t" \
  "ds_read_b64 v[222:223], v253 offset:34008\n\t" \
  "v_smfmac_f32_16x16x64_bf16 v[44:47], v[128:131], v[224:231], v236\n\t" \
  "v_smfmac_f32_16x16x64_bf16 v[32:35], v[132:135], v[224:231], v236\n\t" \
  "v_smfmac_f32_16x16x64_bf16 v[20:23], v[136:139], v[224:231], v236\n\t" \
  "v_add_f32 v200, v200, v4\n\t" \
  "v_fmac_f32 v201, v4, v4\n\t" \
  "v_add_f32 v200, v200, v5\n\t" \
  "v_fmac_f32 v201, v5, v5\n\t" \
  "v_add_f32 v200, v200, v6\n\t" \
  "v_fmac_f32 v201, v6, v6\n\t" \
  "v_smfmac_f32_16x16x64_bf16 v[8:11], v[140:143], v[224:231], v236\n\t" \
  "ds_read_b64 v[224:225], v253 offset:32816\n\t" \
  "v_add_f32 v200, v200, v7\n\t" \
  "v_fmac_f32 v201, v7, v7\n\t" \
  "s_waitcnt lgkmcnt(5)\n\t" \
  "v_smfmac_f32_16x16x64_bf16 v[36:39], v[100:103], v[208:215], v236\n\t" \
  "ds_read_b64 v[226:227], v253 offset:33216\n\t" \
  "v_smfmac_f32_16x16x64_bf16 v[24:27], v[104:107], v[208:215], v236\n\t" \
  "buffer_store_dwordx4 v[92:95], v250, s[20:23], s61 offen offset:320 sc1\n\t" \
  "ds_read_b64 v[228:229], v253 offset:33616\n\t" \
  "v_smfmac_f32_16x16x64_bf16 v[12:15], v[108:111], v[208:215], v236\n\t" \
  "ds_read_b64 v[230:231], v253 offset:34016\n\t" \
  "v_add_f32 v200, v200, v8\n\t" \
  "v_fmac_f32 v201, v8, v8\n\t" \
  "v_add_f32 v200, v200, v9\n\t" \
  "v_fmac_f32 v201, v9, v9\n\t" \
  "v_add_f32 v200, v200, v10\n\t" \
  "v_fmac_f32 v201, v10, v10\n\t" \
  "s_waitcnt lgkmcnt(4)\n\t" \
  "v_smfmac_f32_16x16x64_bf16 v[40:43], v[116:119], v[216:223], v236\n\t" \
  "v_add_f32 v200, v200, v11\n\t" \
  "v_fmac_f32 v201, v11, v11\n\t" \
  "v_smfmac_f32_16x16x64_bf16 v[28:31], v[120:123], v[216:223], v236\n\t" \
  "ds_write_b64 v233, v[200:201] offset:0\n\t" \
  "v_smfmac_f32_16x16x64_bf16 v[16:19], v[124:127], v[216:223], v236\n\t" \
  "v_mul_f32 v203, v12, v12\n\t" \
  "v_add_f32 v202, v12, v13\n\t" \
  "v_fmac_f32 v203, v13, v13\n\t" \
  "v_add_f32 v202, v202, v14\n\t" \
  "v_fmac_f32 v203, v14, v14\n\t" \
  "v_add_f32 v202, v202, v15\n\t" \
  "s_waitcnt vmcnt(8) lgkmcnt(0)\n\t" \
  "s_barrier\n\t" \
  "ds_read_b64 v[208:209], v232 offset:0\n\t" \
  "ds_read_b64 v[210:211], v232 offset:400\n\t" \
  "ds_read_b64 v[212:213], v232 offset:800\n\t" \
  "ds_read_b64 v[214:215], v232 offset:1200\n\t" \
  "ds_read_b64 v[216:217], v232 offset:8\n\t" \
  "ds_read_b64 v[218:219], v232 offset:408\n\t" \
  "ds_read_b64 v[220:221], v232 offset:808\n\t" \
  "ds_read_b64 v[222:223], v232 offset:1208\n\t" \
  "s_cmp_lg_u32 s58, 0\n\t" \
  "s_cbranch_scc1 .Lcnx_c6\n\t" \
  "v_lshl_add_u32 v48, v233, 1, s63\n\t" \
  "s_lshl_b32 m0, s41, 1\n\t" \
  "v_add_u32 v49, 0x2000, v48\n\t" \
  "global_load_lds_dwordx4 v48, s[6:7]\n\t" \
  "s_add_u32 m0, m0, 0x2000\n\t" \
  "s_cmp_eq_u32 s41, 3584\n\t" \
  "s_cselect_b32 exec_hi, 1, -1\n\t" \
  "global_load_lds_dwordx4 v49, s[6:7]\n\t" \
  "s_mov_b64 exec, -1\n\t" \
  ".Lcnx_c6:\n\t" \
  "s_cmp_lg_u32 s37, 0\n\t" \
  "s_cbranch_scc1 .Lcnx_c7\n\t" \
  "v_add_u32 v128, s40, v251\n\t" \
  "ds_read_b64 v[96:97], v128 offset:0\n\t" \
  "ds_read_b64 v[98:99], v128 offset:512\n\t" \
  "ds_read_b64 v[100:101], v128 offset:1024\n\t" \
  "ds_read_b64 v[102:103], v128 offset:1536\n\t" \
  "ds_read_b64 v[112:113], v128 offset:2048\n\t" \
  "ds_read_b64 v[114:115], v128 offset:2560\n\t" \
  "ds_read_b64 v[116:117], v128 offset:3072\n\t" \
  "ds_read_b64 v[118:119], v128 offset:3584\n\t" \
  ".Lcnx_c7:\n\t" \
  "v_smfmac_f32_16x16x64_bf16 v[44:47], v[132:135], v[224:231], v236\n\t" \
  "v_fmac_f32 v203, v15, v15\n\t" \
  "v_smfmac_f32_16x16x64_bf16 v[32:35], v[136:139], v[224:231], v236\n\t" \
  "v_smfmac_f32_16x16x64_bf16 v[20:23], v[140:143], v[224:231], v236\n\t" \
  "buffer_store_dwordx4 v[48:51], v250, s[20:23], s62 offen offset:0 sc1\n\t" \
  "ds_read_b64 v[224:225], v232 offset:16\n\t" \
  "v_add_f32 v202, v202, v16\n\t" \
  "v_fmac_f32 v203, v16, v16\n\t" \
  "v_add_f32 v202, v202, v17\n\t" \
  "v_fmac_f32 v203, v17, v17\n\t" \
  "v_add_f32 v202, v202, v18\n\t" \
  "v_fmac_f32 v203, v18, v18\n\t" \
  "s_waitcnt lgkmcnt(5)\n\t" \
  "v_smfmac_f32_16x16x64_bf16 v[36:39], v[104:107], v[208:215], v236\n\t" \
  "ds_read_b64 v[226:227], v232 offset:416\n\t" \
  "ds_read_b64 v[228:229], v232 offset:816\n\t" \
  "ds_read_b64 v[230:231], v232 offset:1216\n\t" \
  "v_add_f32 v202, v202, v19\n\t" \
  "v_fmac_f32 v203, v19, v19\n\t" \
  "v_smfmac_f32_16x16x64_bf16 v[24:27], v[108:111], v[208:215], v236\n\t" \
  "s_waitcnt lgkmcnt(4)\n\t" \
  "v_smfmac_f32_16x16x64_bf16 v[40:43], v[120:123], v[216:223], v236\n\t" \
  "v_add_f32 v202, v202, v20\n\t" \
  "v_fmac_f32 v203, v20, v20\n\t" \
  "v_add_f32 v202, v202, v21\n\t" \
  "v_fmac_f32 v203, v21, v21\n\t" \
  "v_add_f32 v202, v202, v22\n\t" \
  "v_fmac_f32 v203, v22, v22\n\t" \
  "v_smfmac_f32_16x16x64_bf16 v[28:31], v[124:127], v[216:223], v236\n\t" \
  "v_add_f32 v202, v202, v23\n\t" \
  "v_fmac_f32 v203, v23, v23\n\t" \
  "ds_write_b64 v233, v[202:203] offset:4096\n\t" \
  "s_waitcnt vmcnt(7) lgkmcnt(0)\n\t" \
  "s_barrier\n\t" \
  "ds_read_b64 v[208:209], v232 offset:16400\n\t" \
  "ds_read_b64 v[210:211], v232 offset:16800\n\t" \
  "ds_read_b64 v[212:213], v232 offset:17200\n\t" \
  "ds_read_b64 v[214:215], v232 offset:17600\n\t" \
  "ds_read_b64 v[216:217], v232 offset:16408\n\t" \
  "ds_read_b64 v[218:219], v232 offset:16808\n\t" \
  "ds_read_b64 v[220:221], v232 offset:17208\n\t" \
  "ds_read_b64 v[222:223], v232 offset:17608\n\t" \
  "s_cmp_ge_u32 s36, 2\n\t" \
  "s_cbranch_scc1 .Lcnx_c8\n\t" \
  "s_mov_b32 m0, s46\n\t" \
  "s_nop 0\n\t" \
  "buffer_load_dwordx4 v252, s[16:19], s33 offen lds\n\t" \
  "buffer_load_dwordx4 v252, s[16:19], s34 offen offset:1024 lds\n\t" \
  "buffer_load_dwordx4 v252, s[16:19], s35 offen offset:2048 lds\n\t" \
  "s_add_u32 s33, s33, s32\n\t" \
  "s_add_u32 s34, s34, s32\n\t" \
  "s_add_u32 s35, s35, s32\n\t" \
  "s_mov_b32 m0, s47\n\t" \
  "s_nop 0\n\t" \
  "buffer_load_dwordx4 v252, s[16:19], s33 offen lds\n\t" \
  "buffer_load_dwordx4 v252, s[16:19], s34 offen offset:1024 lds\n\t" \
  "buffer_load_dwordx4 v252, s[16:19], s35 offen offset:2048 lds\n\t" \
  "s_add_u32 s33, s33, s32\n\t" \
  "s_add_u32 s34, s34, s32\n\t" \
  "s_add_u32 s35, s35, s32\n\t" \
  ".Lcnx_c8:\n\t" \
  "v_smfmac_f32_16x16x64_bf16 v[44:47], v[136:139], v[224:231], v236\n\t" \
  "v_mul_f32 v205, v24, v24\n\t" \
  "v_add_f32 v204, v24, v25\n\t" \
  "v_smfmac_f32_16x16x64_bf16 v[32:35], v[140:143], v[224:231], v236\n\t" \
  "buffer_store_dwordx4 v[56:59], v250, s[20:23], s62 offen offset:64 sc1\n\t" \
  "ds_read_b64 v[224:225], v232 offset:16416\n\t" \
  "ds_read_b64 v[226:227], v232 offset:16816\n\t" \
  "v_fmac_f32 v205, v25, v25\n\t" \
  "v_add_f32 v204, v204, v26\n\t" \
  "v_fmac_f32 v205, v26, v26\n\t" \
  "s_cmp_lg_u32 s37, 0\n\t" \
  "s_cbranch_scc1 .Lcnx_c9\n\t" \
  "s_waitcnt lgkmcnt(15)\n\t" \
  "v_add_f32 v96, v96, v98\n\t" \
  "v_add_f32 v97, v97, v99\n\t" \
  "v_add_f32 v100, v100, v102\n\t" \
  "v_add_f32 v101, v101, v103\n\t" \
  "v_add_f32 v112, v112, v114\n\t" \
  "v_add_f32 v113, v113, v115\n\t" \
  "v_add_f32 v116, v116, v118\n\t" \
  "v_add_f32 v117, v117, v119\n\t" \
  "v_add_f32 v96, v96, v100\n\t" \
  "v_add_f32 v97, v97, v101\n\t" \
  "v_add_f32 v112, v112, v116\n\t" \
  "v_add_f32 v113, v113, v117\n\t" \
  "v_add_f32 v96, v96, v112\n\t" \
  "v_add_f32 v97, v97, v113\n\t" \
  "v_mul_f32 v96, 0x3c2aaaab, v96\n\t" \
  "v_mul_f32 v97, 0x3c2aaaab, v97\n\t" \
  "v_fma_f32 v97, -v96, v96, v97\n\t" \
  "v_max_f32 v97, 0, v97\n\t" \
  "v_add_f32 v97, 0x3727c5ac, v97\n\t" \
  "v_rsq_f32 v97, v97\n\t" \
  "s_nop 0\n\t" \
  "v_mul_f32 v98, 0x3e000000, v97\n\t" \
  "v_mul_f32 v99, -v96, v98\n\t" \
  "ds_write_b64 v233, v[98:99] offset:32784\n\t" \
  ".Lcnx_c9:\n\t" \
  "s_waitcnt lgkmcnt(6)\n\t" \
  "v_smfmac_f32_16x16x64_bf16 v[36:39], v[108:111], v[208:215], v236\n\t" \
  "ds_read_b64 v[228:229], v232 offset:17216\n\t" \
  "ds_read_b64 v[230:231], v232 offset:17616\n\t" \
  "v_add_f32 v204, v204, v27\n\t" \
  "v_fmac_f32 v205, v27, v27\n\t" \
  "v_add_f32 v204, v204, v28\n\t" \
  "s_waitcnt lgkmcnt(4)\n\t" \
  "v_smfmac_f32_16x16x64_bf16 v[40:43], v[124:127], v[216:223], v236\n\t" \
  "v_fmac_f32 v205, v28, v28\n\t" \
  "v_add_f32 v204, v204, v29\n\t" \
  "v_fmac_f32 v205, v29, v29\n\t" \
  "s_waitcnt lgkmcnt(0)\n\t" \
  "v_smfmac_f32_16x16x64_bf16 v[44:47], v[140:143], v[224:231], v236\n\t" \
  "v_add_f32 v204, v204, v30\n\t" \
  "v_fmac_f32 v205, v30, v30\n\t" \
  "v_add_f32 v204, v204, v31\n\t" \
  "v_fmac_f32 v205, v31, v31\n\t" \
  "v_add_f32 v204, v204, v32\n\t" \
  "v_fmac_f32 v205, v32, v32\n\t" \
  "v_add_f32 v204, v204, v33\n\t" \
  "v_fmac_f32 v205, v33, v33\n\t" \
  "v_add_f32 v204, v204, v34\n\t" \
  "v_fmac_f32 v205, v34, v34\n\t" \
  "v_add_f32 v204, v204, v35\n\t" \
  "v_fmac_f32 v205, v35, v35\n\t" \
  "v_mul_f32 v207, v36, v36\n\t" \
  "v_add_f32 v206, v36, v37\n\t" \
  "v_fmac_f32 v207, v37, v37\n\t" \
  "v_add_f32 v206, v206, v38\n\t" \
  "v_fmac_f32 v207, v38, v38\n\t" \
  "v_add_f32 v206, v206, v39\n\t" \
  "v_fmac_f32 v207, v39, v39\n\t" \
  "v_add_f32 v206, v206, v40\n\t" \
  "v_fmac_f32 v207, v40, v40\n\t" \
  "v_add_f32 v206, v206, v41\n\t" \
  "v_fmac_f32 v207, v41, v41\n\t" \
  "v_add_f32 v206, v206, v42\n\t" \
  "v_fmac_f32 v207, v42, v42\n\t" \
  "v_add_f32 v206, v206, v43\n\t" \
  "v_fmac_f32 v207, v43, v43\n\t" \
  "v_add_f32 v206, v206, v44\n\t" \
  "v_fmac_f32 v207, v44, v44\n\t" \
  "v_add_f32 v206, v206, v45\n\t" \
  "v_fmac_f32 v207, v45, v45\n\t" \
  "v_add_f32 v206, v206, v46\n\t" \
  "v_fmac_f32 v207, v46, v46\n\t" \
  "v_add_f32 v206, v206, v47\n\t" \
  "v_fmac_f32 v207, v47, v47\n\t" \
  "ds_write_b64 v233, v[204:205] offset:8192\n\t" \
  "ds_write_b64 v233, v[206:207] offset:12288\n\t" \
  "s_waitcnt lgkmcnt(0)\n\t" \
  "s_barrier\n\t" \
  "ds_read_b64 v[192:193], v251 offset:32784\n\t" \
  "ds_read_b64 v[194:195], v251 offset:33296\n\t" \
  "ds_read_b64 v[196:197], v251 offset:33808\n\t" \
  "ds_read_b64 v[198:199], v251 offset:34320\n\t" \
  "s_cmp_lg_u32 s37, 1\n\t" \
  "s_cbranch_scc1 .Lcnx_c10\n\t" \
  "v_add_u32 v128, s40, v251\n\t" \
  "ds_read_b64 v[96:97], v128 offset:0\n\t" \
  "ds_read_b64 v[98:99], v128 offset:512\n\t" \
  "ds_read_b64 v[100:101], v128 offset:1024\n\t" \
  "ds_read_b64 v[102:103], v128 offset:1536\n\t" \
  "ds_read_b64 v[112:113], v128 offset:2048\n\t" \
  "ds_read_b64 v[114:115], v128 offset:2560\n\t" \
  "ds_read_b64 v[116:117], v128 offset:3072\n\t" \
  "ds_read_b64 v[118:119], v128 offset:3584\n\t" \
  ".Lcnx_c10:\n\t" \
  "v_lshl_add_u32 v208, v251, 1, s42\n\t" \
  "v_lshl_add_u32 v209, v251, 1, s43\n\t" \
  "s_waitcnt lgkmcnt(3)\n\t" \
  ".p2align 3\n\t" \
  "v_fma_f32 v144, v144, v192, v193\n\t" \
  "v_fma_f32 v145, v145, v192, v193\n\t" \
  "v_fma_f32 v146, v146, v192, v193\n\t" \
  "v_fma_f32 v147, v147, v192, v193\n\t" \
  "v_fma_f32 v148, v148, v192, v193\n\t" \
  "v_fma_f32 v149, v149, v192, v193\n\t" \
  "v_fma_f32 v150, v150, v192, v193\n\t" \
  "v_fma_f32 v151, v151, v192, v193\n\t" \
  "v_fma_f32 v152, v152, v192, v193\n\t" \
  "v_fma_f32 v153, v153, v192, v193\n\t" \
  "v_fma_f32 v154, v154, v192, v193\n\t" \
  "v_fma_f32 v155, v155, v192, v193\n\t" \
  "v_cvt_pk_bf16_f32 v144, v144, v145\n\t" \
  "v_cvt_pk_bf16_f32 v145, v146, v147\n\t" \
  "v_cvt_pk_bf16_f32 v146, v148, v149\n\t" \
  "v_cvt_pk_bf16_f32 v147, v150, v151\n\t" \
  "v_cvt_pk_bf16_f32 v148, v152, v153\n\t" \
  "v_cvt_pk_bf16_f32 v149, v154, v155\n\t" \
  "ds_write_b128 v208, v[144:147] offset:0\n\t" \
  "ds_write_b64 v209, v[148:149] offset:0\n\t" \
  "buffer_store_dwordx4 v[64:67], v250, s[20:23], s62 offen offset:128 sc1\n\t" \
  "s_cmp_lg_u32 s37, 1\n\t" \
  "s_cbranch_scc1 .Lcnx_c11\n\t" \
  "s_waitcnt lgkmcnt(2)\n\t" \
  "v_add_f32 v96, v96, v98\n\t" \
  "v_add_f32 v97, v97, v99\n\t" \
  "v_add_f32 v100, v100, v102\n\t" \
  "v_add_f32 v101, v101, v103\n\t" \
  "v_add_f32 v112, v112, v114\n\t" \
  "v_add_f32 v113, v113, v115\n\t" \
  "v_add_f32 v116, v116, v118\n\t" \
  "v_add_f32 v117, v117, v119\n\t" \
  "v_add_f32 v96, v96, v100\n\t" \
  "v_add_f32 v97, v97, v101\n\t" \
  "v_add_f32 v112, v112, v116\n\t" \
  "v_add_f32 v113, v113, v117\n\t" \
  "v_add_f32 v96, v96, v112\n\t" \
  "v_add_f32 v97, v97, v113\n\t" \
  "v_mul_f32 v96, 0x3c2aaaab, v96\n\t" \
  "v_mul_f32 v97, 0x3c2aaaab, v97\n\t" \
  "v_fma_f32 v97, -v96, v96, v97\n\t" \
  "v_max_f32 v97, 0, v97\n\t" \
  "v_add_f32 v97, 0x3727c5ac, v97\n\t" \
  "v_rsq_f32 v97, v97\n\t" \
  "s_nop 0\n\t" \
  "v_mul_f32 v98, 0x3e000000, v97\n\t" \
  "v_mul_f32 v99, -v96, v98\n\t" \
  "ds_write_b64 v233, v[98:99] offset:14352\n\t" \
  ".Lcnx_c11:\n\t" \
  "s_waitcnt lgkmcnt(4)\n\t" \
  ".p2align 3\n\t" \
  "v_fma_f32 v156, v156, v194, v195\n\t" \
  "v_fma_f32 v157, v157, v194, v195\n\t" \
  "v_fma_f32 v158, v158, v194, v195\n\t" \
  "v_fma_f32 v159, v159, v194, v195\n\t" \
  "v_fma_f32 v160, v160, v194, v195\n\t" \
  "v_fma_f32 v161, v161, v194, v195\n\t" \
  "v_fma_f32 v162, v162, v194, v195\n\t" \
  "v_fma_f32 v163, v163, v194, v195\n\t" \
  "v_fma_f32 v164, v164, v194, v195\n\t" \
  "v_fma_f32 v165, v165, v194, v195\n\t" \
  "v_fma_f32 v166, v166, v194, v195\n\t" \
  "v_fma_f32 v167, v167, v194, v195\n\t" \
  "v_cvt_pk_bf16_f32 v156, v156, v157\n\t" \
  "v_cvt_pk_bf16_f32 v157, v158, v159\n\t" \
  "v_cvt_pk_bf16_f32 v158, v160, v161\n\t" \
  "v_cvt_pk_bf16_f32 v159, v162, v163\n\t" \
  "v_cvt_pk_bf16_f32 v160, v164, v165\n\t" \
  "v_cvt_pk_bf16_f32 v161, v166, v167\n\t" \
  "ds_write_b128 v208, v[156:159] offset:12288\n\t" \
  "ds_write_b64 v209, v[160:161] offset:12288\n\t" \
  "s_waitcnt lgkmcnt(5)\n\t" \
  ".p2align 3\n\t" \
  "v_fma_f32 v168, v168, v196, v197\n\t" \
  "v_fma_f32 v169, v169, v196, v197\n\t" \
  "v_fma_f32 v170, v170, v196, v197\n\t" \
  "v_fma_f32 v171, v171, v196, v197\n\t" \
  "v_fma_f32 v172, v172, v196, v197\n\t" \
  "v_fma_f32 v173, v173, v196, v197\n\t" \
  "v_fma_f32 v174, v174, v196, v197\n\t" \
  "v_fma_f32 v175, v175, v196, v197\n\t" \
  "v_fma_f32 v176, v176, v196, v197\n\t" \
  "v_fma_f32 v177, v177, v196, v197\n\t" \
  "v_fma_f32 v178, v178, v196, v197\n\t" \
  "v_fma_f32 v179, v179, v196, v197\n\t" \
  "v_cvt_pk_bf16_f32 v168, v168, v169\n\t" \
  "v_cvt_pk_bf16_f32 v169, v170, v171\n\t" \
  "v_cvt_pk_bf16_f32 v170, v172, v173\n\t" \
  "v_cvt_pk_bf16_f32 v171, v174, v175\n\t" \
  "v_cvt_pk_bf16_f32 v172, v176, v177\n\t" \
  "v_cvt_pk_bf16_f32 v173, v178, v179\n\t" \
  "ds_write_b128 v208, v[168:171] offset:24576\n\t" \
  "ds_write_b64 v209, v[172:173] offset:24576\n\t" \
  "s_waitcnt lgkmcnt(6)\n\t" \
  ".p2align 3\n\t" \
  "v_fma_f32 v180, v180, v198, v199\n\t" \
  "v_fma_f32 v181, v181, v198, v199\n\t" \
  "v_fma_f32 v182, v182, v198, v199\n\t" \
  "v_fma_f32 v183, v183, v198, v199\n\t" \
  "v_fma_f32 v184, v184, v198, v199\n\t" \
  "v_fma_f32 v185, v185, v198, v199\n\t" \
  "v_fma_f32 v186, v186, v198, v199\n\t" \
  "v_fma_f32 v187, v187, v198, v199\n\t" \
  "v_fma_f32 v188, v188, v198, v199\n\t" \
  "v_fma_f32 v189, v189, v198, v199\n\t" \
  "v_fma_f32 v190, v190, v198, v199\n\t" \
  "v_fma_f32 v191, v191, v198, v199\n\t" \
  "v_cvt_pk_bf16_f32 v180, v180, v181\n\t" \
  "v_cvt_pk_bf16_f32 v181, v182, v183\n\t" \
  "v_cvt_pk_bf16_f32 v182, v184, v185\n\t" \
  "v_cvt_pk_bf16_f32 v183, v186, v187\n\t" \
  "v_cvt_pk_bf16_f32 v184, v188, v189\n\t" \
  "v_cvt_pk_bf16_f32 v185, v190, v191\n\t" \
  "ds_write_b128 v208, v[180:183] offset:36864\n\t" \
  "ds_write_b64 v209, v[184:185] offset:36864\n\t" \
  "v_add_u32 v208, 49152, v208\n\t" \
  "v_add_u32 v209, 49152, v209\n\t" \
  "s_waitcnt lgkmcnt(0)\n\t" \
  "s_barrier\n\t" \
  "ds_read_b64 v[200:201], v251 offset:16400\n\t" \
  "ds_read_b64 v[202:203], v251 offset:16912\n\t" \
  "ds_read_b64 v[204:205], v251 offset:17424\n\t" \
  "ds_read_b64 v[206:207], v251 offset:17936\n\t" \
  "s_waitcnt lgkmcnt(3)\n\t" \
  ".p2align 3\n\t" \
  "v_fma_f32 v0, v0, v200, v201\n\t" \
  "v_fma_f32 v1, v1, v200, v201\n\t" \
  "v_fma_f32 v2, v2, v200, v201\n\t" \
  "v_fma_f32 v3, v3, v200, v201\n\t" \
  "v_fma_f32 v4, v4, v200, v201\n\t" \
  "v_fma_f32 v5, v5, v200, v201\n\t" \
  "v_fma_f32 v6, v6, v200, v201\n\t" \
  "v_fma_f32 v7, v7, v200, v201\n\t" \
  "v_fma_f32 v8, v8, v200, v201\n\t" \
  "v_fma_f32 v9, v9, v200, v201\n\t" \
  "v_fma_f32 v10, v10, v200, v201\n\t" \
  "v_fma_f32 v11, v11, v200, v201\n\t" \
  "v_cvt_pk_bf16_f32 v0, v0, v1\n\t" \
  "v_cvt_pk_bf16_f32 v1, v2, v3\n\t" \
  "v_cvt_pk_bf16_f32 v2, v4, v5\n\t" \
  "v_cvt_pk_bf16_f32 v3, v6, v7\n\t" \
  "v_cvt_pk_bf16_f32 v4, v8, v9\n\t" \
  "v_cvt_pk_bf16_f32 v5, v10, v11\n\t" \
  "ds_write_b128 v208, v[0:3] offset:0\n\t" \
  "ds_write_b64 v209, v[4:5] offset:0\n\t" \
  "buffer_store_dwordx4 v[72:75], v250, s[20:23], s62 offen offset:192 sc1\n\t" \
  "s_waitcnt lgkmcnt(4)\n\t" \
  ".p2align 3\n\t" \
  "v_fma_f32 v12, v12, v202, v203\n\t" \
  "v_fma_f32 v13, v13, v202, v203\n\t" \
  "v_fma_f32 v14, v14, v202, v203\n\t" \
  "v_fma_f32 v15, v15, v202, v203\n\t" \
  "v_fma_f32 v16, v16, v202, v203\n\t" \
  "v_fma_f32 v17, v17, v202, v203\n\t" \
  "v_fma_f32 v18, v18, v202, v203\n\t" \
  "v_fma_f32 v19, v19, v202, v203\n\t" \
  "v_fma_f32 v20, v20, v202, v203\n\t" \
  "v_fma_f32 v21, v21, v202, v203\n\t" \
  "v_fma_f32 v22, v22, v202, v203\n\t" \
  "v_fma_f32 v23, v23, v202, v203\n\t" \
  "v_cvt_pk_bf16_f32 v12, v12, v13\n\t" \
  "v_cvt_pk_bf16_f32 v13, v14, v15\n\t" \
  "v_cvt_pk_bf16_f32 v14, v16, v17\n\t" \
  "v_cvt_pk_bf16_f32 v15, v18, v19\n\t" \
  "v_cvt_pk_bf16_f32 v16, v20, v21\n\t" \
  "v_cvt_pk_bf16_f32 v17, v22, v23\n\t" \
  "ds_write_b128 v208, v[12:15] offset:12288\n\t" \
  "ds_write_b64 v209, v[16:17] offset:12288\n\t" \
  "s_waitcnt lgkmcnt(5)\n\t" \
  ".p2align 3\n\t" \
  "v_fma_f32 v24, v24, v204, v205\n\t" \
  "v_fma_f32 v25, v25, v204, v205\n\t" \
  "v_fma_f32 v26, v26, v204, v205\n\t" \
  "v_fma_f32 v27, v27, v204, v205\n\t" \
  "v_fma_f32 v28, v28, v204, v205\n\t" \
  "v_fma_f32 v29, v29, v204, v205\n\t" \
  "v_fma_f32 v30, v30, v204, v205\n\t" \
  "v_fma_f32 v31, v31, v204, v205\n\t" \
  "v_fma_f32 v32, v32, v204, v205\n\t" \
  "v_fma_f32 v33, v33, v204, v205\n\t" \
  "v_fma_f32 v34, v34, v204, v205\n\t" \
  "v_fma_f32 v35, v35, v204, v205\n\t" \
  "v_cvt_pk_bf16_f32 v24, v24, v25\n\t" \
  "v_cvt_pk_bf16_f32 v25, v26, v27\n\t" \
  "v_cvt_pk_bf16_f32 v26, v28, v29\n\t" \
  "v_cvt_pk_bf16_f32 v27, v30, v31\n\t" \
  "v_cvt_pk_bf16_f32 v28, v32, v33\n\t" \
  "v_cvt_pk_bf16_f32 v29, v34, v35\n\t" \
  "ds_write_b128 v208, v[24:27] offset:24576\n\t" \
  "ds_write_b64 v209, v[28:29] offset:24576\n\t" \
  "s_waitcnt lgkmcnt(6)\n\t" \
  ".p2align 3\n\t" \
  "v_fma_f32 v36, v36, v206, v207\n\t" \
  "v_fma_f32 v37, v37, v206, v207\n\t" \
  "v_fma_f32 v38, v38, v206, v207\n\t" \
  "v_fma_f32 v39, v39, v206, v207\n\t" \
  "v_fma_f32 v40, v40, v206, v207\n\t" \
  "v_fma_f32 v41, v41, v206, v207\n\t" \
  "v_fma_f32 v42, v42, v206, v207\n\t" \
  "v_fma_f32 v43, v43, v206, v207\n\t" \
  "v_fma_f32 v44, v44, v206, v207\n\t" \
  "v_fma_f32 v45, v45, v206, v207\n\t" \
  "v_fma_f32 v46, v46, v206, v207\n\t" \
  "v_fma_f32 v47, v47, v206, v207\n\t" \
  "v_cvt_pk_bf16_f32 v36, v36, v37\n\t" \
  "v_cvt_pk_bf16_f32 v37, v38, v39\n\t" \
  "v_cvt_pk_bf16_f32 v38, v40, v41\n\t" \
  "v_cvt_pk_bf16_f32 v39, v42, v43\n\t" \
  "v_cvt_pk_bf16_f32 v40, v44, v45\n\t" \
  "v_cvt_pk_bf16_f32 v41, v46, v47\n\t" \
  "ds_write_b128 v208, v[36:39] offset:36864\n\t" \
  "ds_write_b64 v209, v[40:41] offset:36864\n\t" \
  "v_lshlrev_b32 v232, 1, v251\n\t" \
  "v_lshl_add_u32 v237, v251, 3, s39\n\t" \
  "v_and_b32 v232, 0xf0, v232\n\t" \
  "v_mad_i32_i24 v237, v232, -3, v237\n\t" \
  "s_cmp_lg_u32 s58, 0\n\t" \
  "s_cbranch_scc1 .Lcnx_c12\n\t" \
  "s_waitcnt vmcnt(0)\n\t" \
  ".Lcnx_c12:\n\t" \
  "s_waitcnt lgkmcnt(0)\n\t" \
  "s_barrier\n\t" \
  "ds_read_b64_tr_b16 v[208:209], v248 offset:43048\n\t" \
  "ds_read_b64_tr_b16 v[210:211], v248 offset:44072\n\t" \
  "ds_read_b128 v[96:99], v237 offset:0\n\t" \
  "ds_read_b128 v[108:111], v237 offset:256\n\t" \
  "ds_read_b128 v[120:123], v237 offset:512\n\t" \
  "ds_read_b128 v[132:135], v237 offset:768\n\t" \
  "ds_read_b64_tr_b16 v[212:213], v248 offset:46120\n\t" \
  "ds_read_b64_tr_b16 v[214:215], v248 offset:47144\n\t" \
  "ds_read_b128 v[100:103], v237 offset:4096\n\t" \
  "ds_read_b128 v[112:115], v237 offset:4352\n\t" \
  "ds_read_b128 v[124:127], v237 offset:4608\n\t" \
  "ds_read_b128 v[136:139], v237 offset:4864\n\t" \
  "ds_read_b64_tr_b16 v[216:217], v248 offset:45096\n\t" \
  "ds_read_b64_tr_b16 v[218:219], v248 offset:48168\n\t" \
  "ds_read_b128 v[104:107], v237 offset:8192\n\t" \
  "ds_read_b128 v[116:119], v237 offset:8448\n\t" \
  "ds_read_b128 v[128:131], v237 offset:8704\n\t" \
  "ds_read_b128 v[140:143], v237 offset:8960\n\t" \
  "s_waitcnt lgkmcnt(15)\n\t" \
  "v_mfma_f32_16x16x32_bf16 v[144:147], v[208:211], v[96:99], 0\n\t" \
  "s_waitcnt lgkmcnt(14)\n\t" \
  "v_mfma_f32_16x16x32_bf16 v[152:155], v[208:211], v[108:111], 0\n\t" \
  "s_cmp_eq_u32 s37, 0\n\t" \
  "s_cbranch_scc1 .Lcnx_f0\n.Lcnx_g0:\n\t" \
  "s_waitcnt lgkmcnt(13)\n\t" \
  "v_mfma_f32_16x16x32_bf16 v[160:163], v[208:211], v[120:123], 0\n\t" \
  "buffer_store_dwordx4 v[80:83], v250, s[20:23], s62 offen offset:256 sc1\n\t" \
  "s_waitcnt lgkmcnt(12)\n\t" \
  "v_mfma_f32_16x16x32_bf16 v[168:171], v[208:211], v[132:135], 0\n\t" \
  "ds_read_b64_tr_b16 v[208:209], v248 offset:43560\n\t" \
  "s_waitcnt lgkmcnt(10)\n\t" \
  "v_mfma_f32_16x16x32_bf16 v[144:147], v[212:215], v[100:103], v[144:147]\n\t" \
  "ds_read_b64_tr_b16 v[210:211], v248 offset:44584\n\t" \
  "s_waitcnt lgkmcnt(10)\n\t" \
  "v_mfma_f32_16x16x32_bf16 v[152:155], v[212:215], v[112:115], v[152:155]\n\t" \
  "s_waitcnt lgkmcnt(9)\n\t" \
  "v_mfma_f32_16x16x32_bf16 v[160:163], v[212:215], v[124:127], v[160:163]\n\t" \
  "s_waitcnt lgkmcnt(8)\n\t" \
  "v_mfma_f32_16x16x32_bf16 v[168:171], v[212:215], v[136:139], v[168:171]\n\t" \
  "ds_read_b64_tr_b16 v[212:213], v248 offset:46632\n\t" \
  "s_waitcnt lgkmcnt(6)\n\t" \
  "v_mfma_f32_16x16x32_bf16 v[144:147], v[216:219], v[104:107], v[144:147]\n\t" \
  "ds_read_b64_tr_b16 v[214:215], v248 offset:47656\n\t" \
  "s_waitcnt lgkmcnt(6)\n\t" \
  "v_mfma_f32_16x16x32_bf16 v[152:155], v[216:219], v[116:119], v[152:155]\n\t" \
  "s_waitcnt lgkmcnt(5)\n\t" \
  "v_mfma_f32_16x16x32_bf16 v[160:163], v[216:219], v[128:131], v[160:163]\n\t" \
  "s_waitcnt lgkmcnt(4)\n\t" \
  "v_mfma_f32_16x16x32_bf16 v[168:171], v[216:219], v[140:143], v[168:171]\n\t" \
  "ds_read_b64_tr_b16 v[216:217], v248 offset:45608\n\t" \
  "s_waitcnt lgkmcnt(3)\n\t" \
  "v_mfma_f32_16x16x32_bf16 v[148:151], v[208:211], v[96:99], 0\n\t" \
  "buffer_store_dwordx4 v[88:91], v250, s[20:23], s62 offen offset:320 sc1\n\t" \
  "ds_read_b64_tr_b16 v[218:219], v248 offset:48680\n\t" \
  "v_mfma_f32_16x16x32_bf16 v[156:159], v[208:211], v[108:111], 0\n\t" \
  "s_mov_b64 exec, 3\n\t" \
  "ds_add_rtn_u32 v246, v251, v250 offset:47136\n\t" \
  "s_mov_b64 exec, -1\n\t" \
  "v_mfma_f32_16x16x32_bf16 v[164:167], v[208:211], v[120:123], 0\n\t" \
  "v_mfma_f32_16x16x32_bf16 v[172:175], v[208:211], v[132:135], 0\n\t" \
  "ds_read_b64_tr_b16 v[208:209], v248 offset:49192\n\t" \
  "s_waitcnt lgkmcnt(4)\n\t" \
  "v_mfma_f32_16x16x32_bf16 v[148:151], v[212:215], v[100:103], v[148:151]\n\t" \
  "ds_read_b64_tr_b16 v[210:211], v248 offset:50216\n\t" \
  "v_mfma_f32_16x16x32_bf16 v[156:159], v[212:215], v[112:115], v[156:159]\n\t" \
  "v_cvt_pk_f16_f32 v220, |v146|, |v147| clamp\n\t" \
  "v_cvt_pknorm_i16_f32 v222, |v144|, |v145|\n\t" \
  "v_mfma_f32_16x16x32_bf16 v[164:167], v[212:215], v[124:127], v[164:167]\n\t" \
  "v_cvt_pk_f16_f32 v224, |v154|, |v155| clamp\n\t" \
  "v_cvt_pknorm_i16_f32 v226, |v152|, |v153|\n\t" \
  "v_and_b32_sdwa v221, s56, v220 dst_sel:DWORD dst_unused:UNUSED_PAD src0_sel:DWORD src1_sel:WORD_1\n\t" \
  "v_mfma_f32_16x16x32_bf16 v[172:175], v[212:215], v[136:139], v[172:175]\n\t" \
  "ds_read_b64_tr_b16 v[212:213], v248 offset:52264\n\t" \
  "v_and_b32 v220, s56, v220\n\t" \
  "v_lshlrev_b32_sdwa v223, 2, v222 dst_sel:DWORD dst_unused:UNUSED_PAD src0_sel:DWORD src1_sel:BYTE_3\n\t" \
  "s_waitcnt lgkmcnt(4)\n\t" \
  "v_mfma_f32_16x16x32_bf16 v[148:151], v[216:219], v[104:107], v[148:151]\n\t" \
  "ds_read_b64_tr_b16 v[214:215], v248 offset:53288\n\t" \
  "v_lshlrev_b32_sdwa v222, 2, v222 dst_sel:DWORD dst_unused:UNUSED_PAD src0_sel:DWORD src1_sel:BYTE_1\n\t" \
  "v_mfma_f32_16x16x32_bf16 v[156:159], v[216:219], v[116:119], v[156:159]\n\t" \
  "ds_read_b32 v221, v221 offset:0\n\t" \
  "v_and_b32_sdwa v225, s56, v224 dst_sel:DWORD dst_unused:UNUSED_PAD src0_sel:DWORD src1_sel:WORD_1\n\t" \
  "v_and_b32 v224, s56, v224\n\t" \
  "v_mfma_f32_16x16x32_bf16 v[164:167], v[216:219], v[128:131], v[164:167]\n\t" \
  "ds_read_b32 v220, v220 offset:0\n\t" \
  "v_lshlrev_b32_sdwa v227, 2, v226 dst_sel:DWORD dst_unused:UNUSED_PAD src0_sel:DWORD src1_sel:BYTE_3\n\t" \
  "v_mfma_f32_16x16x32_bf16 v[172:175], v[216:219], v[140:143], v[172:175]\n\t" \
  "ds_read_b64_tr_b16 v[216:217], v248 offset:51240\n\t" \
  "ds_read_b64_tr_b16 v[218:219], v248 offset:54312\n\t" \
  "v_lshlrev_b32_sdwa v226, 2, v226 dst_sel:DWORD dst_unused:UNUSED_PAD src0_sel:DWORD src1_sel:BYTE_1\n\t" \
  "s_cmp_lg_u32 s38, 4\n\t" \
  "s_cbranch_scc1 .Lcnx_c24\n\t" \
  "s_mov_b32 m0, 163632\n\t" \
  "s_add_u32 s30, s30, s31\n\t" \
  "buffer_load_dword v252, s[8:11], s30 offen lds\n\t" \
  ".Lcnx_c24:\n\t" \
  "s_setprio 3\n\t" \
  "v_cvt_pk_f16_f32 v228, |v162|, |v163| clamp\n\t" \
  "v_cvt_pknorm_i16_f32 v230, |v160|, |v161|\n\t" \
  "s_waitcnt lgkmcnt(6)\n\t" \
  "v_mfma_f32_16x16x32_bf16 v[176:179], v[208:211], v[96:99], 0\n\t" \
  "ds_read_b32 v223, v223 offset:15376\n\t" \
  "ds_read_b32 v222, v222 offset:15376\n\t" \
  "v_cvt_pk_f16_f32 v232, |v170|, |v171| clamp\n\t" \
  "v_cvt_pk_f16_f32 v234, |v168|, |v169| clamp\n\t" \
  "v_and_b32_sdwa v229, s56, v228 dst_sel:DWORD dst_unused:UNUSED_PAD src0_sel:DWORD src1_sel:WORD_1\n\t" \
  "v_and_b32 v228, s56, v228\n\t" \
  "v_mfma_f32_16x16x32_bf16 v[184:187], v[208:211], v[108:111], 0\n\t" \
  "buffer_load_dwordx4 v[236:239], v250, s[8:11], s28 offen offset:0\n\t" \
  "s_cmp_lg_u32 s38, 5\n\t" \
  "s_cbranch_scc1 .Lcnx_c25\n\t" \
  "s_mov_b32 m0, 163632\n\t" \
  "s_add_u32 s30, s30, s31\n\t" \
  "buffer_load_dword v252, s[8:11], s30 offen lds\n\t" \
  ".Lcnx_c25:\n\t" \
  "s_cmp_lg_u32 s37, 0\n\t" \
  "s_cbranch_scc1 .Lcnx_c13\n\t" \
  "s_waitcnt vmcnt(3)\n\t" \
  ".Lcnx_c13:\n\t" \
  "s_mov_b64 exec, 3\n\t" \
  "ds_add_rtn_u32 v246, v251, v250 offset:47144\n\t" \
  "s_mov_b64 exec, -1\n\t" \
  "ds_read_b32 v225, v225 offset:0\n\t" \
  "v_lshlrev_b32_sdwa v231, 2, v230 dst_sel:DWORD dst_unused:UNUSED_PAD src0_sel:DWORD src1_sel:BYTE_3\n\t" \
  "v_lshlrev_b32_sdwa v230, 2, v230 dst_sel:DWORD dst_unused:UNUSED_PAD src0_sel:DWORD src1_sel:BYTE_1\n\t" \
  "v_and_b32_sdwa v233, s56, v232 dst_sel:DWORD dst_unused:UNUSED_PAD src0_sel:DWORD src1_sel:WORD_1\n\t" \
  "v_and_b32 v232, s56, v232\n\t" \
  "v_mfma_f32_16x16x32_bf16 v[192:195], v[208:211], v[120:123], 0\n\t" \
  "ds_read_b32 v224, v224 offset:0\n\t" \
  "ds_read_b32 v227, v227 offset:15376\n\t" \
  "v_and_b32_sdwa v235, s56, v234 dst_sel:DWORD dst_unused:UNUSED_PAD src0_sel:DWORD src1_sel:WORD_1\n\t" \
  "v_and_b32 v234, s56, v234\n\t" \
  "ds_read_b32 v226, v226 offset:15376\n\t" \
  "ds_read_b32 v229, v229 offset:0\n\t" \
  "ds_read_b32 v228, v228 offset:0\n\t" \
  "ds_read_b32 v231, v231 offset:15376\n\t" \
  "ds_read_b32 v230, v230 offset:15376\n\t" \
  "ds_read_b32 v233, v233 offset:0\n\t" \
  "ds_read_b32 v232, v232 offset:0\n\t" \
  "ds_read_b32 v235, v235 offset:0\n\t" \
  "s_waitcnt lgkmcnt(12)\n\t" \
  "v_fma_f32 v147, |v147|, v221, v147 mul:4\n\t" \
  "v_fma_f32 v146, |v146|, v220, v146 mul:4\n\t" \
  "v_fma_mix_f32 v223, |v145|, v223, v223 op_sel:[0,1,0] op_sel_hi:[0,1,1] clamp\n\t" \
  "v_mfma_f32_16x16x32_bf16 v[200:203], v[208:211], v[132:135], 0\n\t" \
  "s_waitcnt lgkmcnt(11)\n\t" \
  "v_readfirstlane_b32 s4, v246\n\t" \
  "s_cmp_lt_u32 s4, s57\n\t" \
  "s_cbranch_scc1 .Lcnx_w9\n.Lcnx_b9:\n\t" \
  "ds_read_b64_tr_b16 v[208:209], v248 offset:49704\n\t" \
  "v_fma_mix_f32 v222, |v144|, v222, v222 op_sel:[0,1,0] op_sel_hi:[0,1,1] clamp\n\t" \
  "s_waitcnt lgkmcnt(8)\n\t" \
  "v_fma_f32 v155, |v155|, v225, v155 mul:4\n\t" \
  "v_fma_f32 v154, |v154|, v224, v154 mul:4\n\t" \
  "v_fma_mix_f32 v227, |v153|, v227, v227 op_sel:[0,1,0] op_sel_hi:[0,1,1] clamp\n\t" \
  "v_mfma_f32_16x16x32_bf16 v[176:179], v[212:215], v[100:103], v[176:179]\n\t" \
  "ds_read_b64_tr_b16 v[210:211], v248 offset:50728\n\t" \
  "s_setprio 2\n\t" \
  "v_fma_mix_f32 v226, |v152|, v226, v226 op_sel:[0,1,0] op_sel_hi:[0,1,1] clamp\n\t" \
  "v_fma_f32 v145, |v145|, v223, v145 mul:4\n\t" \
  "v_fma_f32 v144, |v144|, v222, v144 mul:4\n\t" \
  "v_fma_f32 v153, |v153|, v227, v153 mul:4\n\t" \
  "v_fma_f32 v152, |v152|, v226, v152 mul:4\n\t" \
  "v_mfma_f32_16x16x32_bf16 v[184:187], v[212:215], v[112:115], v[184:187]\n\t" \
  "s_cmp_eq_u32 s37, 0\n\t" \
  "s_cbranch_scc1 .Lcnx_f1\n.Lcnx_g1:\n\t" \
  "ds_read_b32 v234, v234 offset:0\n\t" \
  "v_cvt_pk_bf16_f32 v147, v146, v147\n\t" \
  "v_cvt_pk_bf16_f32 v146, v144, v145\n\t" \
  "v_cvt_pk_bf16_f32 v155, v154, v155\n\t" \
  "v_cvt_pk_bf16_f32 v154, v152, v153\n\t" \
  "v_mfma_f32_16x16x32_bf16 v[192:195], v[212:215], v[124:127], v[192:195]\n\t" \
  "v_cvt_pk_f16_f32 v220, |v148|, |v149| clamp\n\t" \
  "v_cvt_pknorm_i16_f32 v222, |v150|, |v151|\n\t" \
  "v_cvt_pk_f16_f32 v224, |v156|, |v157| clamp\n\t" \
  "v_cvt_pknorm_i16_f32 v226, |v158|, |v159|\n\t" \
  "v_mfma_f32_16x16x32_bf16 v[200:203], v[212:215], v[136:139], v[200:203]\n\t" \
  "ds_read_b64_tr_b16 v[212:213], v248 offset:52776\n\t" \
  "v_and_b32_sdwa v221, s56, v220 dst_sel:DWORD dst_unused:UNUSED_PAD src0_sel:DWORD src1_sel:WORD_1\n\t" \
  "v_and_b32 v220, s56, v220\n\t" \
  "v_lshlrev_b32_sdwa v223, 2, v222 dst_sel:DWORD dst_unused:UNUSED_PAD src0_sel:DWORD src1_sel:BYTE_3\n\t" \
  "v_lshlrev_b32_sdwa v222, 2, v222 dst_sel:DWORD dst_unused:UNUSED_PAD src0_sel:DWORD src1_sel:BYTE_1\n\t" \
  "v_and_b32_sdwa v225, s56, v224 dst_sel:DWORD dst_unused:UNUSED_PAD src0_sel:DWORD src1_sel:WORD_1\n\t" \
  "v_mfma_f32_16x16x32_bf16 v[176:179], v[216:219], v[104:107], v[176:179]\n\t" \
  "ds_read_b64_tr_b16 v[214:215], v248 offset:53800\n\t" \
  "v_and_b32 v224, s56, v224\n\t" \
  "v_lshlrev_b32_sdwa v227, 2, v226 dst_sel:DWORD dst_unused:UNUSED_PAD src0_sel:DWORD src1_sel:BYTE_3\n\t" \
  "v_lshlrev_b32_sdwa v226, 2, v226 dst_sel:DWORD dst_unused:UNUSED_PAD src0_sel:DWORD src1_sel:BYTE_1\n\t" \
  "s_waitcnt lgkmcnt(8)\n\t" \
  "v_fma_f32 v163, |v163|, v229, v163 mul:4\n\t" \
  "v_mfma_f32_16x16x32_bf16 v[184:187], v[216:219], v[116:119], v[184:187]\n\t" \
  "ds_read_b32 v221, v221 offset:0\n\t" \
  "ds_read_b32 v220, v220 offset:0\n\t" \
  "v_fma_f32 v162, |v162|, v228, v162 mul:4\n\t" \
  "v_fma_mix_f32 v231, |v161|, v231, v231 op_sel:[0,1,0] op_sel_hi:[0,1,1] clamp\n\t" \
  "v_fma_mix_f32 v230, |v160|, v230, v230 op_sel:[0,1,0] op_sel_hi:[0,1,1] clamp\n\t" \
  "s_waitcnt lgkmcnt(4)\n\t" \
  "v_fma_f32 v171, |v171|, v233, v171 mul:4\n\t" \
  "v_fma_f32 v170, |v170|, v232, v170 mul:4\n\t" \
  "v_mfma_f32_16x16x32_bf16 v[192:195], v[216:219], v[128:131], v[192:195]\n\t" \
  "ds_read_b32 v223, v223 offset:15376\n\t" \
  "ds_read_b32 v222, v222 offset:15376\n\t" \
  "v_fma_f32 v169, |v169|, v235, v169 mul:4\n\t" \
  "v_fma_f32 v168, |v168|, v234, v168 mul:4\n\t" \
  "v_fma_f32 v161, |v161|, v231, v161 mul:4\n\t" \
  "v_fma_f32 v160, |v160|, v230, v160 mul:4\n\t" \
  "v_mfma_f32_16x16x32_bf16 v[200:203], v[216:219], v[140:143], v[200:203]\n\t" \
  "ds_read_b64_tr_b16 v[216:217], v248 offset:51752\n\t" \
  "ds_read_b64_tr_b16 v[218:219], v248 offset:54824\n\t" \
  "v_cvt_pk_bf16_f32 v163, v162, v163\n\t" \
  "s_setprio 1\n\t" \
  "v_cvt_pk_bf16_f32 v162, v160, v161\n\t" \
  "v_cvt_pk_bf16_f32 v171, v170, v171\n\t" \
  "v_cvt_pk_bf16_f32 v170, v168, v169\n\t" \
  "v_cvt_pk_f16_f32 v228, |v164|, |v165| clamp\n\t" \
  "v_mfma_f32_16x16x32_bf16 v[180:183], v[208:211], v[96:99], 0\n\t" \
  "ds_read_b32 v225, v225 offset:0\n\t" \
  "v_cvt_pknorm_i16_f32 v230, |v166|, |v167|\n\t" \
  "v_cvt_pk_f16_f32 v232, |v172|, |v173| clamp\n\t" \
  "v_cvt_pk_f16_f32 v234, |v174|, |v175| clamp\n\t" \
  "v_and_b32_sdwa v229, s56, v228 dst_sel:DWORD dst_unused:UNUSED_PAD src0_sel:DWORD src1_sel:WORD_1\n\t" \
  "v_mfma_f32_16x16x32_bf16 v[188:191], v[208:211], v[108:111], 0\n\t" \
  "s_waitcnt vmcnt(0)\n\t" \
  "s_mov_b64 exec, 3\n\t" \
  "ds_add_rtn_u32 v246, v251, v250 offset:47152\n\t" \
  "s_mov_b64 exec, -1\n\t" \
  "ds_read_b32 v224, v224 offset:0\n\t" \
  "v_and_b32 v228, s56, v228\n\t" \
  "v_lshlrev_b32_sdwa v231, 2, v230 dst_sel:DWORD dst_unused:UNUSED_PAD src0_sel:DWORD src1_sel:BYTE_3\n\t" \
  "v_lshlrev_b32_sdwa v230, 2, v230 dst_sel:DWORD dst_unused:UNUSED_PAD src0_sel:DWORD src1_sel:BYTE_1\n\t" \
  "v_and_b32_sdwa v233, s56, v232 dst_sel:DWORD dst_unused:UNUSED_PAD src0_sel:DWORD src1_sel:WORD_1\n\t" \
  "v_mfma_f32_16x16x32_bf16 v[196:199], v[208:211], v[120:123], 0\n\t" \
  "ds_read_b32 v227, v227 offset:15376\n\t" \
  "ds_read_b32 v226, v226 offset:15376\n\t" \
  "v_and_b32 v232, s56, v232\n\t" \
  "v_and_b32_sdwa v235, s56, v234 dst_sel:DWORD dst_unused:UNUSED_PAD src0_sel:DWORD src1_sel:WORD_1\n\t" \
  "v_and_b32 v234, s56, v234\n\t" \
  "s_waitcnt lgkmcnt(7)\n\t" \
  "v_fma_f32 v149, |v149|, v221, v149 mul:4\n\t" \
  "v_fma_f32 v148, |v148|, v220, v148 mul:4\n\t" \
  "v_mfma_f32_16x16x32_bf16 v[204:207], v[208:211], v[132:135], 0\n\t" \
  "s_waitcnt lgkmcnt(3)\n\t" \
  "v_readfirstlane_b32 s4, v246\n\t" \
  "s_cmp_lt_u32 s4, s57\n\t" \
  "s_cbranch_scc1 .Lcnx_w12\n.Lcnx_b12:\n\t" \
  "ds_read_b64_tr_b16 v[208:209], v249 offset:24600\n\t" \
  "ds_read_b64_tr_b16 v[210:211], v249 offset:26136\n\t" \
  "v_fma_mix_f32 v223, |v151|, v223, v223 op_sel:[0,1,0] op_sel_hi:[0,1,1] clamp\n\t" \
  "v_fma_mix_f32 v222, |v150|, v222, v222 op_sel:[0,1,0] op_sel_hi:[0,1,1] clamp\n\t" \
  "s_waitcnt lgkmcnt(2)\n\t" \
  "v_fma_f32 v157, |v157|, v225, v157 mul:4\n\t" \
  "v_fma_f32 v156, |v156|, v224, v156 mul:4\n\t" \
  "v_mfma_f32_16x16x32_bf16 v[180:183], v[212:215], v[100:103], v[180:183]\n\t" \
  "s_cmp_eq_u32 s37, 0\n\t" \
  "s_cbranch_scc1 .Lcnx_f2\n.Lcnx_g2:\n\t" \
  "ds_read_b32 v229, v229 offset:0\n\t" \
  "ds_read_b32 v228, v228 offset:0\n\t" \
  "v_fma_mix_f32 v227, |v159|, v227, v227 op_sel:[0,1,0] op_sel_hi:[0,1,1] clamp\n\t" \
  "v_fma_mix_f32 v226, |v158|, v226, v226 op_sel:[0,1,0] op_sel_hi:[0,1,1] clamp\n\t" \
  "v_fma_f32 v151, |v151|, v223, v151 mul:4\n\t" \
  "v_fma_f32 v150, |v150|, v222, v150 mul:4\n\t" \
  "v_fma_f32 v159, |v159|, v227, v159 mul:4\n\t" \
  "s_setprio 0\n\t" \
  "v_mfma_f32_16x16x32_bf16 v[188:191], v[212:215], v[112:115], v[188:191]\n\t" \
  "ds_read_b32 v231, v231 offset:15376\n\t" \
  "ds_read_b32 v230, v230 offset:15376\n\t" \
  "v_fma_f32 v158, |v158|, v226, v158 mul:4\n\t" \
  "v_cvt_pk_bf16_f32 v148, v148, v149\n\t" \
  "v_cvt_pk_bf16_f32 v149, v150, v151\n\t" \
  "v_cvt_pk_bf16_f32 v156, v156, v157\n\t" \
  "v_mfma_f32_16x16x32_bf16 v[196:199], v[212:215], v[124:127], v[196:199]\n\t" \
  "ds_read_b32 v233, v233 offset:0\n\t" \
  "v_cvt_pk_bf16_f32 v157, v158, v159\n\t" \
  "v_cvt_pk_f16_f32 v220, |v178|, |v179| clamp\n\t" \
  "v_cvt_pknorm_i16_f32 v222, |v176|, |v177|\n\t" \
  "v_cvt_pk_f16_f32 v224, |v186|, |v187| clamp\n\t" \
  "v_mfma_f32_16x16x32_bf16 v[204:207], v[212:215], v[136:139], v[204:207]\n\t" \
  "ds_read_b64_tr_b16 v[212:213], v249 offset:24608\n\t" \
  "v_cvt_pknorm_i16_f32 v226, |v184|, |v185|\n\t" \
  "v_and_b32_sdwa v221, s56, v220 dst_sel:DWORD dst_unused:UNUSED_PAD src0_sel:DWORD src1_sel:WORD_1\n\t" \
  "v_and_b32 v220, s56, v220\n\t" \
  "v_lshlrev_b32_sdwa v223, 2, v222 dst_sel:DWORD dst_unused:UNUSED_PAD src0_sel:DWORD src1_sel:BYTE_3\n\t" \
  "v_lshlrev_b32_sdwa v222, 2, v222 dst_sel:DWORD dst_unused:UNUSED_PAD src0_sel:DWORD src1_sel:BYTE_1\n\t" \
  "v_mfma_f32_16x16x32_bf16 v[180:183], v[216:219], v[104:107], v[180:183]\n\t" \
  "ds_read_b64_tr_b16 v[214:215], v249 offset:26144\n\t" \
  "ds_read_b32 v232, v232 offset:0\n\t" \
  "v_and_b32_sdwa v225, s56, v224 dst_sel:DWORD dst_unused:UNUSED_PAD src0_sel:DWORD src1_sel:WORD_1\n\t" \
  "v_and_b32 v224, s56, v224\n\t" \
  "v_lshlrev_b32_sdwa v227, 2, v226 dst_sel:DWORD dst_unused:UNUSED_PAD src0_sel:DWORD src1_sel:BYTE_3\n\t" \
  "v_lshlrev_b32_sdwa v226, 2, v226 dst_sel:DWORD dst_unused:UNUSED_PAD src0_sel:DWORD src1_sel:BYTE_1\n\t" \
  "v_mfma_f32_16x16x32_bf16 v[188:191], v[216:219], v[116:119], v[188:191]\n\t" \
  "ds_read_b32 v235, v235 offset:0\n\t" \
  "ds_read_b32 v234, v234 offset:0\n\t" \
  "s_waitcnt lgkmcnt(6)\n\t" \
  "v_fma_f32 v165, |v165|, v229, v165 mul:4\n\t" \
  "v_fma_f32 v164, |v164|, v228, v164 mul:4\n\t" \
  "v_fma_mix_f32 v231, |v167|, v231, v231 op_sel:[0,1,0] op_sel_hi:[0,1,1] clamp\n\t" \
  "v_fma_mix_f32 v230, |v166|, v230, v230 op_sel:[0,1,0] op_sel_hi:[0,1,1] clamp\n\t" \
  "s_waitcnt lgkmcnt(0)\n\t" \
  "v_fma_f32 v173, |v173|, v233, v173 mul:4\n\t" \
  "v_mfma_f32_16x16x32_bf16 v[196:199], v[216:219], v[128:131], v[196:199]\n\t" \
  "ds_read_b32 v221, v221 offset:0\n\t" \
  "ds_read_b32 v220, v220 offset:0\n\t" \
  "v_fma_f32 v172, |v172|, v232, v172 mul:4\n\t" \
  "v_fma_f32 v175, |v175|, v235, v175 mul:4\n\t" \
  "v_fma_f32 v174, |v174|, v234, v174 mul:4\n\t" \
  "v_fma_f32 v167, |v167|, v231, v167 mul:4\n\t" \
  "v_mfma_f32_16x16x32_bf16 v[204:207], v[216:219], v[140:143], v[204:207]\n\t" \
  "ds_read_b64_tr_b16 v[216:217], v249 offset:24664\n\t" \
  "ds_read_b64_tr_b16 v[218:219], v249 offset:26200\n\t" \
  "v_fma_f32 v166, |v166|, v230, v166 mul:4\n\t" \
  "v_cvt_pk_bf16_f32 v164, v164, v165\n\t" \
  "v_cvt_pk_bf16_f32 v165, v166, v167\n\t" \
  "v_cvt_pk_bf16_f32 v172, v172, v173\n\t" \
  "v_cvt_pk_bf16_f32 v173, v174, v175\n\t" \
  "s_setprio 0\n\t" \
  "v_cvt_pk_f16_f32 v228, |v194|, |v195| clamp\n\t" \
  "v_cvt_pknorm_i16_f32 v230, |v192|, |v193|\n\t" \
  "v_mfma_f32_16x16x32_bf16 v[0:3], v[242:245], v[236:239], 0\n\t" \
  "ds_read_b32 v223, v223 offset:15376\n\t" \
  "ds_read_b32 v222, v222 offset:15376\n\t" \
  "v_cvt_pk_f16_f32 v232, |v202|, |v203| clamp\n\t" \
  "v_cvt_pk_f16_f32 v234, |v200|, |v201| clamp\n\t" \
  "v_mfma_f32_16x16x32_bf16 v[4:7], v[240:243], v[236:239], 0\n\t" \
  "buffer_load_dwordx4 v[236:239], v250, s[8:11], s28 offen offset:64\n\t" \
  "s_cmp_lg_u32 s58, 0\n\t" \
  "s_cbranch_scc1 .Lcnx_c26\n\t" \
  "s_sub_u32 s24, s24, s64\n\t" \
  "s_add_u32 m0, s25, 0\n\t" \
  "s_add_u32 s24, s24, s26\n\t" \
  "buffer_load_dwordx4 v254, s[8:11], s24 offen lds\n\t" \
  "s_cmp_lg_u32 s38, 6\n\t" \
  "s_cbranch_scc1 .Lcnx_c26\n\t" \
  "s_mov_b32 m0, 163632\n\t" \
  "s_add_u32 s30, s30, s31\n\t" \
  "buffer_load_dword v252, s[8:11], s30 offen lds\n\t" \
  ".Lcnx_c26:\n\t" \
  "ds_read_b32 v225, v225 offset:0\n\t" \
  "v_and_b32_sdwa v229, s56, v228 dst_sel:DWORD dst_unused:UNUSED_PAD src0_sel:DWORD src1_sel:WORD_1\n\t" \
  "v_and_b32 v228, s56, v228\n\t" \
  "v_mfma_f32_16x16x32_bf16 v[0:3], v[208:211], v[146:149], v[0:3]\n\t" \
  "ds_read_b32 v224, v224 offset:0\n\t" \
  "v_lshlrev_b32_sdwa v231, 2, v230 dst_sel:DWORD dst_unused:UNUSED_PAD src0_sel:DWORD src1_sel:BYTE_3\n\t" \
  "v_lshlrev_b32_sdwa v230, 2, v230 dst_sel:DWORD dst_unused:UNUSED_PAD src0_sel:DWORD src1_sel:BYTE_1\n\t" \
  "v_mfma_f32_16x16x32_bf16 v[24:27], v[208:211], v[154:157], 0\n\t" \
  "ds_read_b32 v227, v227 offset:15376\n\t" \
  "v_and_b32_sdwa v233, s56, v232 dst_sel:DWORD dst_unused:UNUSED_PAD src0_sel:DWORD src1_sel:WORD_1\n\t" \
  "v_and_b32 v232, s56, v232\n\t" \
  "v_mfma_f32_16x16x32_bf16 v[48:51], v[208:211], v[162:165], 0\n\t" \
  "ds_read_b32 v226, v226 offset:15376\n\t" \
  "ds_read_b32 v229, v229 offset:0\n\t" \
  "v_and_b32_sdwa v235, s56, v234 dst_sel:DWORD dst_unused:UNUSED_PAD src0_sel:DWORD src1_sel:WORD_1\n\t" \
  "v_and_b32 v234, s56, v234\n\t" \
  "v_mfma_f32_16x16x32_bf16 v[72:75], v[208:211], v[170:173], 0\n\t" \
  "ds_read_b64_tr_b16 v[208:209], v249 offset:24672\n\t" \
  "ds_read_b64_tr_b16 v[210:211], v249 offset:26208\n\t" \
  "s_waitcnt lgkmcnt(7)\n\t" \
  "v_fma_f32 v179, |v179|, v221, v179 mul:4\n\t" \
  "v_fma_f32 v178, |v178|, v220, v178 mul:4\n\t" \
  "v_mfma_f32_16x16x32_bf16 v[4:7], v[212:215], v[146:149], v[4:7]\n\t" \
  "ds_read_b32 v228, v228 offset:0\n\t" \
  "ds_read_b32 v231, v231 offset:15376\n\t" \
  "v_fma_mix_f32 v223, |v177|, v223, v223 op_sel:[0,1,0] op_sel_hi:[0,1,1] clamp\n\t" \
  "v_fma_mix_f32 v222, |v176|, v222, v222 op_sel:[0,1,0] op_sel_hi:[0,1,1] clamp\n\t" \
  "v_mfma_f32_16x16x32_bf16 v[28:31], v[212:215], v[154:157], 0\n\t" \
  "ds_read_b32 v230, v230 offset:15376\n\t" \
  "s_waitcnt lgkmcnt(6)\n\t" \
  "v_fma_f32 v187, |v187|, v225, v187 mul:4\n\t" \
  "v_fma_f32 v186, |v186|, v224, v186 mul:4\n\t" \
  "v_mfma_f32_16x16x32_bf16 v[52:55], v[212:215], v[162:165], 0\n\t" \
  "ds_read_b32 v233, v233 offset:0\n\t" \
  "v_fma_mix_f32 v227, |v185|, v227, v227 op_sel:[0,1,0] op_sel_hi:[0,1,1] clamp\n\t" \
  "v_fma_mix_f32 v226, |v184|, v226, v226 op_sel:[0,1,0] op_sel_hi:[0,1,1] clamp\n\t" \
  "v_fma_f32 v177, |v177|, v223, v177 mul:4\n\t" \
  "v_mfma_f32_16x16x32_bf16 v[76:79], v[212:215], v[170:173], 0\n\t" \
  "ds_read_b64_tr_b16 v[212:213], v249 offset:24728\n\t" \
  "v_fma_f32 v176, |v176|, v222, v176 mul:4\n\t" \
  "v_fma_f32 v185, |v185|, v227, v185 mul:4\n\t" \
  "v_mfma_f32_16x16x32_bf16 v[8:11], v[216:219], v[146:149], 0\n\t" \
  "ds_read_b64_tr_b16 v[214:215], v249 offset:26264\n\t" \
  "v_fma_f32 v184, |v184|, v226, v184 mul:4\n\t" \
  "v_cvt_pk_bf16_f32 v179, v178, v179\n\t" \
  "v_mfma_f32_16x16x32_bf16 v[32:35], v[216:219], v[154:157], 0\n\t" \
  "ds_read_b32 v232, v232 offset:0\n\t" \
  "v_cvt_pk_bf16_f32 v178, v176, v177\n\t" \
  "v_cvt_pk_bf16_f32 v187, v186, v187\n\t" \
  "v_mfma_f32_16x16x32_bf16 v[56:59], v[216:219], v[162:165], 0\n\t" \
  "ds_read_b32 v235, v235 offset:0\n\t" \
  "s_setprio 1\n\t" \
  "v_cvt_pk_bf16_f32 v186, v184, v185\n\t" \
  "v_cvt_pk_f16_f32 v220, |v180|, |v181| clamp\n\t" \
  "v_mfma_f32_16x16x32_bf16 v[80:83], v[216:219], v[170:173], 0\n\t" \
  "ds_read_b64_tr_b16 v[216:217], v249 offset:24736\n\t" \
  "v_cvt_pknorm_i16_f32 v222, |v182|, |v183|\n\t" \
  "v_cvt_pk_f16_f32 v224, |v188|, |v189| clamp\n\t" \
  "s_waitcnt lgkmcnt(9)\n\t" \
  "v_mfma_f32_16x16x32_bf16 v[12:15], v[208:211], v[146:149], 0\n\t" \
  "ds_read_b64_tr_b16 v[218:219], v249 offset:26272\n\t" \
  "v_cvt_pknorm_i16_f32 v226, |v190|, |v191|\n\t" \
  "v_and_b32_sdwa v221, s56, v220 dst_sel:DWORD dst_unused:UNUSED_PAD src0_sel:DWORD src1_sel:WORD_1\n\t" \
  "v_mfma_f32_16x16x32_bf16 v[36:39], v[208:211], v[154:157], 0\n\t" \
  "ds_read_b32 v234, v234 offset:0\n\t" \
  "v_and_b32 v220, s56, v220\n\t" \
  "v_lshlrev_b32_sdwa v223, 2, v222 dst_sel:DWORD dst_unused:UNUSED_PAD src0_sel:DWORD src1_sel:BYTE_3\n\t" \
  "v_mfma_f32_16x16x32_bf16 v[60:63], v[208:211], v[162:165], 0\n\t" \
  "ds_read_b32 v221, v221 offset:0\n\t" \
  "v_lshlrev_b32_sdwa v222, 2, v222 dst_sel:DWORD dst_unused:UNUSED_PAD src0_sel:DWORD src1_sel:BYTE_1\n\t" \
  "v_and_b32_sdwa v225, s56, v224 dst_sel:DWORD dst_unused:UNUSED_PAD src0_sel:DWORD src1_sel:WORD_1\n\t" \
  "v_and_b32 v224, s56, v224\n\t" \
  "v_mfma_f32_16x16x32_bf16 v[84:87], v[208:211], v[170:173], 0\n\t" \
  "ds_read_b64_tr_b16 v[208:209], v248 offset:36904\n\t" \
  "ds_read_b64_tr_b16 v[210:211], v248 offset:37928\n\t" \
  "v_lshlrev_b32_sdwa v227, 2, v226 dst_sel:DWORD dst_unused:UNUSED_PAD src0_sel:DWORD src1_sel:BYTE_3\n\t" \
  "v_lshlrev_b32_sdwa v226, 2, v226 dst_sel:DWORD dst_unused:UNUSED_PAD src0_sel:DWORD src1_sel:BYTE_1\n\t" \
  "s_waitcnt lgkmcnt(8)\n\t" \
  "v_mfma_f32_16x16x32_bf16 v[16:19], v[212:215], v[146:149], 0\n\t" \
  "ds_read_b32 v220, v220 offset:0\n\t" \
  "ds_read_b32 v223, v223 offset:15376\n\t" \
  "v_fma_f32 v195, |v195|, v229, v195 mul:4\n\t" \
  "v_fma_f32 v194, |v194|, v228, v194 mul:4\n\t" \
  "v_mfma_f32_16x16x32_bf16 v[40:43], v[212:215], v[154:157], 0\n\t" \
  "ds_read_b32 v222, v222 offset:15376\n\t" \
  "v_fma_mix_f32 v231, |v193|, v231, v231 op_sel:[0,1,0] op_sel_hi:[0,1,1] clamp\n\t" \
  "v_fma_mix_f32 v230, |v192|, v230, v230 op_sel:[0,1,0] op_sel_hi:[0,1,1] clamp\n\t" \
  "v_mfma_f32_16x16x32_bf16 v[64:67], v[212:215], v[162:165], 0\n\t" \
  "ds_read_b32 v225, v225 offset:0\n\t" \
  "s_waitcnt lgkmcnt(7)\n\t" \
  "v_fma_f32 v203, |v203|, v233, v203 mul:4\n\t" \
  "v_fma_f32 v202, |v202|, v232, v202 mul:4\n\t" \
  "v_mfma_f32_16x16x32_bf16 v[88:91], v[212:215], v[170:173], 0\n\t" \
  "ds_read_b64_tr_b16 v[212:213], v248 offset:39976\n\t" \
  "v_fma_f32 v201, |v201|, v235, v201 mul:4\n\t" \
  "v_fma_f32 v200, |v200|, v234, v200 mul:4\n\t" \
  "v_mfma_f32_16x16x32_bf16 v[20:23], v[216:219], v[146:149], 0\n\t" \
  "ds_read_b64_tr_b16 v[214:215], v248 offset:41000\n\t" \
  "v_fma_f32 v193, |v193|, v231, v193 mul:4\n\t" \
  "v_fma_f32 v192, |v192|, v230, v192 mul:4\n\t" \
  "v_mfma_f32_16x16x32_bf16 v[44:47], v[216:219], v[154:157], 0\n\t" \
  "ds_read_b32 v224, v224 offset:0\n\t" \
  "v_cvt_pk_bf16_f32 v195, v194, v195\n\t" \
  "v_cvt_pk_bf16_f32 v194, v192, v193\n\t" \
  "v_mfma_f32_16x16x32_bf16 v[68:71], v[216:219], v[162:165], 0\n\t" \
  "ds_read_b32 v227, v227 offset:15376\n\t" \
  "v_cvt_pk_bf16_f32 v203, v202, v203\n\t" \
  "v_cvt_pk_bf16_f32 v202, v200, v201\n\t" \
  "v_cvt_pk_f16_f32 v228, |v196|, |v197| clamp\n\t" \
  "v_mfma_f32_16x16x32_bf16 v[92:95], v[216:219], v[170:173], 0\n\t" \
  "ds_read_b64_tr_b16 v[216:217], v248 offset:38952\n\t" \
  "v_cvt_pknorm_i16_f32 v230, |v198|, |v199|\n\t" \
  "s_setprio 2\n\t" \
  "v_cvt_pk_f16_f32 v232, |v204|, |v205| clamp\n\t" \
  "s_waitcnt lgkmcnt(9)\n\t" \
  "v_mfma_f32_16x16x32_bf16 v[144:147], v[208:211], v[96:99], 0\n\t" \
  "ds_read_b64_tr_b16 v[218:219], v248 offset:42024\n\t" \
  "v_cvt_pk_f16_f32 v234, |v206|, |v207| clamp\n\t" \
  "v_and_b32_sdwa v229, s56, v228 dst_sel:DWORD dst_unused:UNUSED_PAD src0_sel:DWORD src1_sel:WORD_1\n\t" \
  "v_mfma_f32_16x16x32_bf16 v[152:155], v[208:211], v[108:111], 0\n\t" \
  "ds_read_b32 v226, v226 offset:15376\n\t" \
  "v_and_b32 v228, s56, v228\n\t" \
  "v_lshlrev_b32_sdwa v231, 2, v230 dst_sel:DWORD dst_unused:UNUSED_PAD src0_sel:DWORD src1_sel:BYTE_3\n\t" \
  "v_mfma_f32_16x16x32_bf16 v[160:163], v[208:211], v[120:123], 0\n\t" \
  "ds_read_b32 v229, v229 offset:0\n\t" \
  "v_lshlrev_b32_sdwa v230, 2, v230 dst_sel:DWORD dst_unused:UNUSED_PAD src0_sel:DWORD src1_sel:BYTE_1\n\t" \
  "v_and_b32_sdwa v233, s56, v232 dst_sel:DWORD dst_unused:UNUSED_PAD src0_sel:DWORD src1_sel:WORD_1\n\t" \
  "v_mfma_f32_16x16x32_bf16 v[168:171], v[208:211], v[132:135], 0\n\t" \
  "ds_read_b64_tr_b16 v[208:209], v248 offset:37416\n\t" \
  "v_and_b32 v232, s56, v232\n\t" \
  "v_and_b32_sdwa v235, s56, v234 dst_sel:DWORD dst_unused:UNUSED_PAD src0_sel:DWORD src1_sel:WORD_1\n\t" \
  "s_waitcnt lgkmcnt(7)\n\t" \
  "v_mfma_f32_16x16x32_bf16 v[144:147], v[212:215], v[100:103], v[144:147]\n\t" \
  "ds_read_b64_tr_b16 v[210:211], v248 offset:38440\n\t" \
  "ds_read_b32 v228, v228 offset:0\n\t" \
  "v_and_b32 v234, s56, v234\n\t" \
  "v_fma_f32 v181, |v181|, v221, v181 mul:4\n\t" \
  "v_mfma_f32_16x16x32_bf16 v[152:155], v[212:215], v[112:115], v[152:155]\n\t" \
  "ds_read_b32 v231, v231 offset:15376\n\t" \
  "ds_read_b32 v230, v230 offset:15376\n\t" \
  "v_fma_f32 v180, |v180|, v220, v180 mul:4\n\t" \
  "v_fma_mix_f32 v223, |v183|, v223, v223 op_sel:[0,1,0] op_sel_hi:[0,1,1] clamp\n\t" \
  "v_mfma_f32_16x16x32_bf16 v[160:163], v[212:215], v[124:127], v[160:163]\n\t" \
  "ds_read_b32 v233, v233 offset:0\n\t" \
  "v_fma_mix_f32 v222, |v182|, v222, v222 op_sel:[0,1,0] op_sel_hi:[0,1,1] clamp\n\t" \
  "s_waitcnt lgkmcnt(7)\n\t" \
  "v_fma_f32 v189, |v189|, v225, v189 mul:4\n\t" \
  "v_mfma_f32_16x16x32_bf16 v[168:171], v[212:215], v[136:139], v[168:171]\n\t" \
  "ds_read_b64_tr_b16 v[212:213], v248 offset:40488\n\t" \
  "v_fma_f32 v188, |v188|, v224, v188 mul:4\n\t" \
  "v_fma_mix_f32 v227, |v191|, v227, v227 op_sel:[0,1,0] op_sel_hi:[0,1,1] clamp\n\t" \
  "v_fma_mix_f32 v226, |v190|, v226, v226 op_sel:[0,1,0] op_sel_hi:[0,1,1] clamp\n\t" \
  "v_mfma_f32_16x16x32_bf16 v[144:147], v[216:219], v[104:107], v[144:147]\n\t" \
  "ds_read_b64_tr_b16 v[214:215], v248 offset:41512\n\t" \
  "v_fma_f32 v183, |v183|, v223, v183 mul:4\n\t" \
  "v_fma_f32 v182, |v182|, v222, v182 mul:4\n\t" \
  "v_mfma_f32_16x16x32_bf16 v[152:155], v[216:219], v[116:119], v[152:155]\n\t" \
  "ds_read_b32 v232, v232 offset:0\n\t" \
  "v_fma_f32 v191, |v191|, v227, v191 mul:4\n\t" \
  "v_fma_f32 v190, |v190|, v226, v190 mul:4\n\t" \
  "v_mfma_f32_16x16x32_bf16 v[160:163], v[216:219], v[128:131], v[160:163]\n\t" \
  "ds_read_b32 v235, v235 offset:0\n\t" \
  "v_cvt_pk_bf16_f32 v180, v180, v181\n\t" \
  "v_cvt_pk_bf16_f32 v181, v182, v183\n\t" \
  "v_mfma_f32_16x16x32_bf16 v[168:171], v[216:219], v[140:143], v[168:171]\n\t" \
  "ds_read_b64_tr_b16 v[216:217], v248 offset:39464\n\t" \
  "v_cvt_pk_bf16_f32 v188, v188, v189\n\t" \
  "v_cvt_pk_bf16_f32 v189, v190, v191\n\t" \
  "s_waitcnt lgkmcnt(9)\n\t" \
  "v_mfma_f32_16x16x32_bf16 v[148:151], v[208:211], v[96:99], 0\n\t" \
  "ds_read_b64_tr_b16 v[218:219], v248 offset:42536\n\t" \
  "v_cvt_pk_f16_f32 v220, |v146|, |v147| clamp\n\t" \
  "s_setprio 3\n\t" \
  "v_cvt_pknorm_i16_f32 v222, |v144|, |v145|\n\t" \
  "v_mfma_f32_16x16x32_bf16 v[156:159], v[208:211], v[108:111], 0\n\t" \
  "s_waitcnt vmcnt(1)\n\t" \
  "s_cmp_lg_u32 s58, 1\n\t" \
  "s_cbranch_scc1 .Lcnx_c14\n\t" \
  "s_waitcnt vmcnt(0)\n\t" \
  ".Lcnx_c14:\n\t" \
  "s_mov_b64 exec, 3\n\t" \
  "ds_add_rtn_u32 v246, v251, v250 offset:47160\n\t" \
  "s_mov_b64 exec, -1\n\t" \
  "ds_read_b32 v234, v234 offset:0\n\t" \
  "v_cvt_pk_f16_f32 v224, |v154|, |v155| clamp\n\t" \
  "v_cvt_pknorm_i16_f32 v226, |v152|, |v153|\n\t" \
  "v_mfma_f32_16x16x32_bf16 v[164:167], v[208:211], v[120:123], 0\n\t" \
  "v_and_b32_sdwa v221, s56, v220 dst_sel:DWORD dst_unused:UNUSED_PAD src0_sel:DWORD src1_sel:WORD_1\n\t" \
  "v_and_b32 v220, s56, v220\n\t" \
  "v_mfma_f32_16x16x32_bf16 v[172:175], v[208:211], v[132:135], 0\n\t" \
  "s_waitcnt lgkmcnt(1)\n\t" \
  "v_readfirstlane_b32 s4, v246\n\t" \
  "s_cmp_lt_u32 s4, s57\n\t" \
  "s_cbranch_scc1 .Lcnx_w24\n.Lcnx_b24:\n\t" \
  "ds_read_b64_tr_b16 v[208:209], v249 offset:30752\n\t" \
  "v_lshlrev_b32_sdwa v223, 2, v222 dst_sel:DWORD dst_unused:UNUSED_PAD src0_sel:DWORD src1_sel:BYTE_3\n\t" \
  "v_lshlrev_b32_sdwa v222, 2, v222 dst_sel:DWORD dst_unused:UNUSED_PAD src0_sel:DWORD src1_sel:BYTE_1\n\t" \
  "v_and_b32_sdwa v225, s56, v224 dst_sel:DWORD dst_unused:UNUSED_PAD src0_sel:DWORD src1_sel:WORD_1\n\t" \
  "v_mfma_f32_16x16x32_bf16 v[148:151], v[212:215], v[100:103], v[148:151]\n\t" \
  "ds_read_b64_tr_b16 v[210:211], v249 offset:32288\n\t" \
  "v_and_b32 v224, s56, v224\n\t" \
  "v_lshlrev_b32_sdwa v227, 2, v226 dst_sel:DWORD dst_unused:UNUSED_PAD src0_sel:DWORD src1_sel:BYTE_3\n\t" \
  "v_mfma_f32_16x16x32_bf16 v[156:159], v[212:215], v[112:115], v[156:159]\n\t" \
  "s_cmp_eq_u32 s37, 0\n\t" \
  "s_cbranch_scc1 .Lcnx_f3\n.Lcnx_g3:\n\t" \
  "ds_read_b32 v221, v221 offset:0\n\t" \
  "ds_read_b32 v220, v220 offset:0\n\t" \
  "v_lshlrev_b32_sdwa v226, 2, v226 dst_sel:DWORD dst_unused:UNUSED_PAD src0_sel:DWORD src1_sel:BYTE_1\n\t" \
  "v_fma_f32 v197, |v197|, v229, v197 mul:4\n\t" \
  "v_mfma_f32_16x16x32_bf16 v[164:167], v[212:215], v[124:127], v[164:167]\n\t" \
  "ds_read_b32 v223, v223 offset:15376\n\t" \
  "ds_read_b32 v222, v222 offset:15376\n\t" \
  "v_fma_f32 v196, |v196|, v228, v196 mul:4\n\t" \
  "v_fma_mix_f32 v231, |v199|, v231, v231 op_sel:[0,1,0] op_sel_hi:[0,1,1] clamp\n\t" \
  "v_mfma_f32_16x16x32_bf16 v[172:175], v[212:215], v[136:139], v[172:175]\n\t" \
  "ds_read_b64_tr_b16 v[212:213], v249 offset:30760\n\t" \
  "ds_read_b64_tr_b16 v[214:215], v249 offset:32296\n\t" \
  "v_fma_mix_f32 v230, |v198|, v230, v230 op_sel:[0,1,0] op_sel_hi:[0,1,1] clamp\n\t" \
  "s_waitcnt lgkmcnt(8)\n\t" \
  "v_fma_f32 v205, |v205|, v233, v205 mul:4\n\t" \
  "v_mfma_f32_16x16x32_bf16 v[148:151], v[216:219], v[104:107], v[148:151]\n\t" \
  "ds_read_b32 v225, v225 offset:0\n\t" \
  "v_fma_f32 v204, |v204|, v232, v204 mul:4\n\t" \
  "v_fma_f32 v207, |v207|, v235, v207 mul:4\n\t" \
  "v_mfma_f32_16x16x32_bf16 v[156:159], v[216:219], v[116:119], v[156:159]\n\t" \
  "ds_read_b32 v224, v224 offset:0\n\t" \
  "v_fma_f32 v206, |v206|, v234, v206 mul:4\n\t" \
  "v_fma_f32 v199, |v199|, v231, v199 mul:4\n\t" \
  "v_mfma_f32_16x16x32_bf16 v[164:167], v[216:219], v[128:131], v[164:167]\n\t" \
  "ds_read_b32 v227, v227 offset:15376\n\t" \
  "v_fma_f32 v198, |v198|, v230, v198 mul:4\n\t" \
  "v_cvt_pk_bf16_f32 v196, v196, v197\n\t" \
  "v_mfma_f32_16x16x32_bf16 v[172:175], v[216:219], v[140:143], v[172:175]\n\t" \
  "ds_read_b64_tr_b16 v[216:217], v249 offset:30816\n\t" \
  "v_cvt_pk_bf16_f32 v197, v198, v199\n\t" \
  "v_cvt_pk_bf16_f32 v204, v204, v205\n\t" \
  "v_cvt_pk_bf16_f32 v205, v206, v207\n\t" \
  "s_setprio 0\n\t" \
  "v_cvt_pk_f16_f32 v228, |v162|, |v163| clamp\n\t" \
  "v_cvt_pknorm_i16_f32 v230, |v160|, |v161|\n\t" \
  "v_mfma_f32_16x16x32_bf16 v[8:11], v[242:245], v[236:239], v[8:11]\n\t" \
  "ds_read_b64_tr_b16 v[218:219], v249 offset:32352\n\t" \
  "v_cvt_pk_f16_f32 v232, |v170|, |v171| clamp\n\t" \
  "v_cvt_pk_f16_f32 v234, |v168|, |v169| clamp\n\t" \
  "v_mfma_f32_16x16x32_bf16 v[12:15], v[240:243], v[236:239], v[12:15]\n\t" \
  "buffer_load_dwordx4 v[236:239], v250, s[8:11], s28 offen offset:128\n\t" \
  "s_cmp_lg_u32 s58, 0\n\t" \
  "s_cbranch_scc1 .Lcnx_c27\n\t" \
  "s_add_u32 m0, s25, 8200\n\t" \
  "s_add_u32 s24, s24, s26\n\t" \
  "buffer_load_dwordx4 v254, s[8:11], s24 offen lds\n\t" \
  "s_cmp_lg_u32 s38, 7\n\t" \
  "s_cbranch_scc1 .Lcnx_c27\n\t" \
  "s_mov_b32 m0, 163632\n\t" \
  "s_add_u32 s30, s30, s31\n\t" \
  "buffer_load_dword v252, s[8:11], s30 offen lds\n\t" \
  ".Lcnx_c27:\n\t" \
  "ds_read_b32 v226, v226 offset:15376\n\t" \
  "v_and_b32_sdwa v229, s56, v228 dst_sel:DWORD dst_unused:UNUSED_PAD src0_sel:DWORD src1_sel:WORD_1\n\t" \
  "v_and_b32 v228, s56, v228\n\t" \
  "s_waitcnt lgkmcnt(12)\n\t" \
  "v_mfma_f32_16x16x32_bf16 v[0:3], v[208:211], v[178:181], v[0:3]\n\t" \
  "ds_read_b32 v229, v229 offset:0\n\t" \
  "v_lshlrev_b32_sdwa v231, 2, v230 dst_sel:DWORD dst_unused:UNUSED_PAD src0_sel:DWORD src1_sel:BYTE_3\n\t" \
  "v_lshlrev_b32_sdwa v230, 2, v230 dst_sel:DWORD dst_unused:UNUSED_PAD src0_sel:DWORD src1_sel:BYTE_1\n\t" \
  "v_mfma_f32_16x16x32_bf16 v[24:27], v[208:211], v[186:189], v[24:27]\n\t" \
  "ds_read_b32 v228, v228 offset:0\n\t" \
  "v_and_b32_sdwa v233, s56, v232 dst_sel:DWORD dst_unused:UNUSED_PAD src0_sel:DWORD src1_sel:WORD_1\n\t" \
  "v_and_b32 v232, s56, v232\n\t" \
  "v_mfma_f32_16x16x32_bf16 v[48:51], v[208:211], v[194:197], v[48:51]\n\t" \
  "ds_read_b32 v231, v231 offset:15376\n\t" \
  "v_and_b32_sdwa v235, s56, v234 dst_sel:DWORD dst_unused:UNUSED_PAD src0_sel:DWORD src1_sel:WORD_1\n\t" \
  "v_and_b32 v234, s56, v234\n\t" \
  "v_mfma_f32_16x16x32_bf16 v[72:75], v[208:211], v[202:205], v[72:75]\n\t" \
  "ds_read_b64_tr_b16 v[208:209], v249 offset:30824\n\t" \
  "ds_read_b64_tr_b16 v[210:211], v249 offset:32360\n\t" \
  "s_waitcnt lgkmcnt(13)\n\t" \
  "v_fma_f32 v147, |v147|, v221, v147 mul:4\n\t" \
  "v_fma_f32 v146, |v146|, v220, v146 mul:4\n\t" \
  "s_waitcnt lgkmcnt(11)\n\t" \
  "v_mfma_f32_16x16x32_bf16 v[4:7], v[212:215], v[178:181], v[4:7]\n\t" \
  "ds_read_b32 v230, v230 offset:15376\n\t" \
  "v_fma_mix_f32 v223, |v145|, v223, v223 op_sel:[0,1,0] op_sel_hi:[0,1,1] clamp\n\t" \
  "v_fma_mix_f32 v222, |v144|, v222, v222 op_sel:[0,1,0] op_sel_hi:[0,1,1] clamp\n\t" \
  "v_mfma_f32_16x16x32_bf16 v[28:31], v[212:215], v[186:189], v[28:31]\n\t" \
  "ds_read_b32 v233, v233 offset:0\n\t" \
  "s_waitcnt lgkmcnt(7)\n\t" \
  "v_fma_f32 v155, |v155|, v225, v155 mul:4\n\t" \
  "v_fma_f32 v154, |v154|, v224, v154 mul:4\n\t" \
  "v_mfma_f32_16x16x32_bf16 v[52:55], v[212:215], v[194:197], v[52:55]\n\t" \
  "ds_read_b32 v232, v232 offset:0\n\t" \
  "v_fma_mix_f32 v227, |v153|, v227, v227 op_sel:[0,1,0] op_sel_hi:[0,1,1] clamp\n\t" \
  "v_fma_mix_f32 v226, |v152|, v226, v226 op_sel:[0,1,0] op_sel_hi:[0,1,1] clamp\n\t" \
  "v_fma_f32 v145, |v145|, v223, v145 mul:4\n\t" \
  "v_mfma_f32_16x16x32_bf16 v[76:79], v[212:215], v[202:205], v[76:79]\n\t" \
  "ds_read_b64_tr_b16 v[212:213], v249 offset:30880\n\t" \
  "v_fma_f32 v144, |v144|, v222, v144 mul:4\n\t" \
  "v_fma_f32 v153, |v153|, v227, v153 mul:4\n\t" \
  "v_mfma_f32_16x16x32_bf16 v[8:11], v[216:219], v[178:181], v[8:11]\n\t" \
  "ds_read_b64_tr_b16 v[214:215], v249 offset:32416\n\t" \
  "v_fma_f32 v152, |v152|, v226, v152 mul:4\n\t" \
  "v_cvt_pk_bf16_f32 v147, v146, v147\n\t" \
  "v_mfma_f32_16x16x32_bf16 v[32:35], v[216:219], v[186:189], v[32:35]\n\t" \
  "ds_read_b32 v235, v235 offset:0\n\t" \
  "v_cvt_pk_bf16_f32 v146, v144, v145\n\t" \
  "v_cvt_pk_bf16_f32 v155, v154, v155\n\t" \
  "v_mfma_f32_16x16x32_bf16 v[56:59], v[216:219], v[194:197], v[56:59]\n\t" \
  "ds_read_b32 v234, v234 offset:0\n\t" \
  "v_cvt_pk_bf16_f32 v154, v152, v153\n\t" \
  "s_setprio 1\n\t" \
  "v_cvt_pk_f16_f32 v220, |v148|, |v149| clamp\n\t" \
  "v_mfma_f32_16x16x32_bf16 v[80:83], v[216:219], v[202:205], v[80:83]\n\t" \
  "ds_read_b64_tr_b16 v[216:217], v249 offset:30888\n\t" \
  "v_cvt_pknorm_i16_f32 v222, |v150|, |v151|\n\t" \
  "v_cvt_pk_f16_f32 v224, |v156|, |v157| clamp\n\t" \
  "s_waitcnt lgkmcnt(8)\n\t" \
  "v_mfma_f32_16x16x32_bf16 v[12:15], v[208:211], v[178:181], v[12:15]\n\t" \
  "ds_read_b64_tr_b16 v[218:219], v249 offset:32424\n\t" \
  "v_cvt_pknorm_i16_f32 v226, |v158|, |v159|\n\t" \
  "v_and_b32_sdwa v221, s56, v220 dst_sel:DWORD dst_unused:UNUSED_PAD src0_sel:DWORD src1_sel:WORD_1\n\t" \
  "v_mfma_f32_16x16x32_bf16 v[36:39], v[208:211], v[186:189], v[36:39]\n\t" \
  "v_and_b32 v220, s56, v220\n\t" \
  "v_lshlrev_b32_sdwa v223, 2, v222 dst_sel:DWORD dst_unused:UNUSED_PAD src0_sel:DWORD src1_sel:BYTE_3\n\t" \
  "v_mfma_f32_16x16x32_bf16 v[60:63], v[208:211], v[194:197], v[60:63]\n\t" \
  "ds_read_b32 v221, v221 offset:0\n\t" \
  "v_lshlrev_b32_sdwa v222, 2, v222 dst_sel:DWORD dst_unused:UNUSED_PAD src0_sel:DWORD src1_sel:BYTE_1\n\t" \
  "v_and_b32_sdwa v225, s56, v224 dst_sel:DWORD dst_unused:UNUSED_PAD src0_sel:DWORD src1_sel:WORD_1\n\t" \
  "v_and_b32 v224, s56, v224\n\t" \
  "v_mfma_f32_16x16x32_bf16 v[84:87], v[208:211], v[202:205], v[84:87]\n\t" \
  "ds_read_b64_tr_b16 v[208:209], v248 offset:12304\n\t" \
  "ds_read_b64_tr_b16 v[210:211], v248 offset:13328\n\t" \
  "v_lshlrev_b32_sdwa v227, 2, v226 dst_sel:DWORD dst_unused:UNUSED_PAD src0_sel:DWORD src1_sel:BYTE_3\n\t" \
  "v_lshlrev_b32_sdwa v226, 2, v226 dst_sel:DWORD dst_unused:UNUSED_PAD src0_sel:DWORD src1_sel:BYTE_1\n\t" \
  "s_waitcnt lgkmcnt(7)\n\t" \
  "v_mfma_f32_16x16x32_bf16 v[16:19], v[212:215], v[178:181], v[16:19]\n\t" \
  "ds_read_b32 v220, v220 offset:0\n\t" \
  "ds_read_b32 v223, v223 offset:15376\n\t" \
  "v_fma_f32 v163, |v163|, v229, v163 mul:4\n\t" \
  "v_fma_f32 v162, |v162|, v228, v162 mul:4\n\t" \
  "v_mfma_f32_16x16x32_bf16 v[40:43], v[212:215], v[186:189], v[40:43]\n\t" \
  "ds_read_b32 v222, v222 offset:15376\n\t" \
  "v_fma_mix_f32 v231, |v161|, v231, v231 op_sel:[0,1,0] op_sel_hi:[0,1,1] clamp\n\t" \
  "v_fma_mix_f32 v230, |v160|, v230, v230 op_sel:[0,1,0] op_sel_hi:[0,1,1] clamp\n\t" \
  "v_mfma_f32_16x16x32_bf16 v[64:67], v[212:215], v[194:197], v[64:67]\n\t" \
  "ds_read_b32 v225, v225 offset:0\n\t" \
  "s_waitcnt lgkmcnt(9)\n\t" \
  "v_fma_f32 v171, |v171|, v233, v171 mul:4\n\t" \
  "v_fma_f32 v170, |v170|, v232, v170 mul:4\n\t" \
  "v_mfma_f32_16x16x32_bf16 v[88:91], v[212:215], v[202:205], v[88:91]\n\t" \
  "ds_read_b64_tr_b16 v[212:213], v248 offset:15376\n\t" \
  "v_fma_f32 v169, |v169|, v235, v169 mul:4\n\t" \
  "v_fma_f32 v168, |v168|, v234, v168 mul:4\n\t" \
  "s_waitcnt lgkmcnt(8)\n\t" \
  "v_mfma_f32_16x16x32_bf16 v[20:23], v[216:219], v[178:181], v[20:23]\n\t" \
  "ds_read_b64_tr_b16 v[214:215], v248 offset:16400\n\t" \
  "v_fma_f32 v161, |v161|, v231, v161 mul:4\n\t" \
  "v_fma_f32 v160, |v160|, v230, v160 mul:4\n\t" \
  "v_mfma_f32_16x16x32_bf16 v[44:47], v[216:219], v[186:189], v[44:47]\n\t" \
  "ds_read_b32 v224, v224 offset:0\n\t" \
  "v_cvt_pk_bf16_f32 v163, v162, v163\n\t" \
  "v_cvt_pk_bf16_f32 v162, v160, v161\n\t" \
  "v_mfma_f32_16x16x32_bf16 v[68:71], v[216:219], v[194:197], v[68:71]\n\t" \
  "ds_read_b32 v227, v227 offset:15376\n\t" \
  "v_cvt_pk_bf16_f32 v171, v170, v171\n\t" \
  "v_cvt_pk_bf16_f32 v170, v168, v169\n\t" \
  "v_cvt_pk_f16_f32 v228, |v164|, |v165| clamp\n\t" \
  "v_mfma_f32_16x16x32_bf16 v[92:95], v[216:219], v[202:205], v[92:95]\n\t" \
  "ds_read_b64_tr_b16 v[216:217], v248 offset:14352\n\t" \
  "v_cvt_pknorm_i16_f32 v230, |v166|, |v167|\n\t" \
  "s_setprio 2\n\t" \
  "v_cvt_pk_f16_f32 v232, |v172|, |v173| clamp\n\t" \
  "s_waitcnt lgkmcnt(9)\n\t" \
  "v_mfma_f32_16x16x32_bf16 v[176:179], v[208:211], v[96:99], 0\n\t" \
  "ds_read_b64_tr_b16 v[218:219], v248 offset:17424\n\t" \
  "v_cvt_pk_f16_f32 v234, |v174|, |v175| clamp\n\t" \
  "v_and_b32_sdwa v229, s56, v228 dst_sel:DWORD dst_unused:UNUSED_PAD src0_sel:DWORD src1_sel:WORD_1\n\t" \
  "v_mfma_f32_16x16x32_bf16 v[184:187], v[208:211], v[108:111], 0\n\t" \
  "ds_read_b32 v226, v226 offset:15376\n\t" \
  "v_and_b32 v228, s56, v228\n\t" \
  "v_lshlrev_b32_sdwa v231, 2, v230 dst_sel:DWORD dst_unused:UNUSED_PAD src0_sel:DWORD src1_sel:BYTE_3\n\t" \
  "v_mfma_f32_16x16x32_bf16 v[192:195], v[208:211], v[120:123], 0\n\t" \
  "ds_read_b32 v229, v229 offset:0\n\t" \
  "v_lshlrev_b32_sdwa v230, 2, v230 dst_sel:DWORD dst_unused:UNUSED_PAD src0_sel:DWORD src1_sel:BYTE_1\n\t" \
  "v_and_b32_sdwa v233, s56, v232 dst_sel:DWORD dst_unused:UNUSED_PAD src0_sel:DWORD src1_sel:WORD_1\n\t" \
  "v_mfma_f32_16x16x32_bf16 v[200:203], v[208:211], v[132:135], 0\n\t" \
  "ds_read_b64_tr_b16 v[208:209], v248 offset:12816\n\t" \
  "v_and_b32 v232, s56, v232\n\t" \
  "v_and_b32_sdwa v235, s56, v234 dst_sel:DWORD dst_unused:UNUSED_PAD src0_sel:DWORD src1_sel:WORD_1\n\t" \
  "s_waitcnt lgkmcnt(7)\n\t" \
  "v_mfma_f32_16x16x32_bf16 v[176:179], v[212:215], v[100:103], v[176:179]\n\t" \
  "ds_read_b64_tr_b16 v[210:211], v248 offset:13840\n\t" \
  "ds_read_b32 v228, v228 offset:0\n\t" \
  "v_and_b32 v234, s56, v234\n\t" \
  "v_fma_f32 v149, |v149|, v221, v149 mul:4\n\t" \
  "v_mfma_f32_16x16x32_bf16 v[184:187], v[212:215], v[112:115], v[184:187]\n\t" \
  "ds_read_b32 v231, v231 offset:15376\n\t" \
  "ds_read_b32 v230, v230 offset:15376\n\t" \
  "v_fma_f32 v148, |v148|, v220, v148 mul:4\n\t" \
  "v_fma_mix_f32 v223, |v151|, v223, v223 op_sel:[0,1,0] op_sel_hi:[0,1,1] clamp\n\t" \
  "v_mfma_f32_16x16x32_bf16 v[192:195], v[212:215], v[124:127], v[192:195]\n\t" \
  "ds_read_b32 v233, v233 offset:0\n\t" \
  "v_fma_mix_f32 v222, |v150|, v222, v222 op_sel:[0,1,0] op_sel_hi:[0,1,1] clamp\n\t" \
  "s_waitcnt lgkmcnt(7)\n\t" \
  "v_fma_f32 v157, |v157|, v225, v157 mul:4\n\t" \
  "v_mfma_f32_16x16x32_bf16 v[200:203], v[212:215], v[136:139], v[200:203]\n\t" \
  "ds_read_b64_tr_b16 v[212:213], v248 offset:15888\n\t" \
  "v_fma_f32 v156, |v156|, v224, v156 mul:4\n\t" \
  "v_fma_mix_f32 v227, |v159|, v227, v227 op_sel:[0,1,0] op_sel_hi:[0,1,1] clamp\n\t" \
  "v_fma_mix_f32 v226, |v158|, v226, v226 op_sel:[0,1,0] op_sel_hi:[0,1,1] clamp\n\t" \
  "v_mfma_f32_16x16x32_bf16 v[176:179], v[216:219], v[104:107], v[176:179]\n\t" \
  "ds_read_b64_tr_b16 v[214:215], v248 offset:16912\n\t" \
  "v_fma_f32 v151, |v151|, v223, v151 mul:4\n\t" \
  "v_fma_f32 v150, |v150|, v222, v150 mul:4\n\t" \
  "v_mfma_f32_16x16x32_bf16 v[184:187], v[216:219], v[116:119], v[184:187]\n\t" \
  "ds_read_b32 v232, v232 offset:0\n\t" \
  "v_fma_f32 v159, |v159|, v227, v159 mul:4\n\t" \
  "v_fma_f32 v158, |v158|, v226, v158 mul:4\n\t" \
  "v_mfma_f32_16x16x32_bf16 v[192:195], v[216:219], v[128:131], v[192:195]\n\t" \
  "ds_read_b32 v235, v235 offset:0\n\t" \
  "v_cvt_pk_bf16_f32 v148, v148, v149\n\t" \
  "v_cvt_pk_bf16_f32 v149, v150, v151\n\t" \
  "v_mfma_f32_16x16x32_bf16 v[200:203], v[216:219], v[140:143], v[200:203]\n\t" \
  "ds_read_b64_tr_b16 v[216:217], v248 offset:14864\n\t" \
  "v_cvt_pk_bf16_f32 v156, v156, v157\n\t" \
  "v_cvt_pk_bf16_f32 v157, v158, v159\n\t" \
  "s_waitcnt lgkmcnt(9)\n\t" \
  "v_mfma_f32_16x16x32_bf16 v[180:183], v[208:211], v[96:99], 0\n\t" \
  "ds_read_b64_tr_b16 v[218:219], v248 offset:17936\n\t" \
  "v_cvt_pk_f16_f32 v220, |v178|, |v179| clamp\n\t" \
  "s_setprio 3\n\t" \
  "v_cvt_pknorm_i16_f32 v222, |v176|, |v177|\n\t" \
  "v_mfma_f32_16x16x32_bf16 v[188:191], v[208:211], v[108:111], 0\n\t" \
  "s_waitcnt vmcnt(1)\n\t" \
  "s_cmp_lg_u32 s58, 1\n\t" \
  "s_cbranch_scc1 .Lcnx_c15\n\t" \
  "s_waitcnt vmcnt(0)\n\t" \
  ".Lcnx_c15:\n\t" \
  "s_mov_b64 exec, 3\n\t" \
  "ds_add_rtn_u32 v246, v251, v250 offset:47168\n\t" \
  "s_mov_b64 exec, -1\n\t" \
  "ds_read_b32 v234, v234 offset:0\n\t" \
  "v_cvt_pk_f16_f32 v224, |v186|, |v187| clamp\n\t" \
  "v_cvt_pknorm_i16_f32 v226, |v184|, |v185|\n\t" \
  "v_mfma_f32_16x16x32_bf16 v[196:199], v[208:211], v[120:123], 0\n\t" \
  "v_and_b32_sdwa v221, s56, v220 dst_sel:DWORD dst_unused:UNUSED_PAD src0_sel:DWORD src1_sel:WORD_1\n\t" \
  "v_and_b32 v220, s56, v220\n\t" \
  "v_mfma_f32_16x16x32_bf16 v[204:207], v[208:211], v[132:135], 0\n\t" \
  "s_waitcnt lgkmcnt(1)\n\t" \
  "v_readfirstlane_b32 s4, v246\n\t" \
  "s_cmp_lt_u32 s4, s57\n\t" \
  "s_cbranch_scc1 .Lcnx_w36\n.Lcnx_b36:\n\t" \
  "ds_read_b64_tr_b16 v[208:209], v249 offset:0\n\t" \
  "v_lshlrev_b32_sdwa v223, 2, v222 dst_sel:DWORD dst_unused:UNUSED_PAD src0_sel:DWORD src1_sel:BYTE_3\n\t" \
  "v_lshlrev_b32_sdwa v222, 2, v222 dst_sel:DWORD dst_unused:UNUSED_PAD src0_sel:DWORD src1_sel:BYTE_1\n\t" \
  "v_and_b32_sdwa v225, s56, v224 dst_sel:DWORD dst_unused:UNUSED_PAD src0_sel:DWORD src1_sel:WORD_1\n\t" \
  "v_mfma_f32_16x16x32_bf16 v[180:183], v[212:215], v[100:103], v[180:183]\n\t" \
  "ds_read_b64_tr_b16 v[210:211], v249 offset:1536\n\t" \
  "v_and_b32 v224, s56, v224\n\t" \
  "v_lshlrev_b32_sdwa v227, 2, v226 dst_sel:DWORD dst_unused:UNUSED_PAD src0_sel:DWORD src1_sel:BYTE_3\n\t" \
  "v_mfma_f32_16x16x32_bf16 v[188:191], v[212:215], v[112:115], v[188:191]\n\t" \
  "s_cmp_eq_u32 s37, 0\n\t" \
  "s_cbranch_scc1 .Lcnx_f4\n.Lcnx_g4:\n\t" \
  "ds_read_b32 v221, v221 offset:0\n\t" \
  "ds_read_b32 v220, v220 offset:0\n\t" \
  "v_lshlrev_b32_sdwa v226, 2, v226 dst_sel:DWORD dst_unused:UNUSED_PAD src0_sel:DWORD src1_sel:BYTE_1\n\t" \
  "v_fma_f32 v165, |v165|, v229, v165 mul:4\n\t" \
  "v_mfma_f32_16x16x32_bf16 v[196:199], v[212:215], v[124:127], v[196:199]\n\t" \
  "ds_read_b32 v223, v223 offset:15376\n\t" \
  "ds_read_b32 v222, v222 offset:15376\n\t" \
  "v_fma_f32 v164, |v164|, v228, v164 mul:4\n\t" \
  "v_fma_mix_f32 v231, |v167|, v231, v231 op_sel:[0,1,0] op_sel_hi:[0,1,1] clamp\n\t" \
  "v_mfma_f32_16x16x32_bf16 v[204:207], v[212:215], v[136:139], v[204:207]\n\t" \
  "ds_read_b64_tr_b16 v[212:213], v249 offset:8\n\t" \
  "ds_read_b64_tr_b16 v[214:215], v249 offset:1544\n\t" \
  "v_fma_mix_f32 v230, |v166|, v230, v230 op_sel:[0,1,0] op_sel_hi:[0,1,1] clamp\n\t" \
  "s_waitcnt lgkmcnt(8)\n\t" \
  "v_fma_f32 v173, |v173|, v233, v173 mul:4\n\t" \
  "v_mfma_f32_16x16x32_bf16 v[180:183], v[216:219], v[104:107], v[180:183]\n\t" \
  "ds_read_b32 v225, v225 offset:0\n\t" \
  "v_fma_f32 v172, |v172|, v232, v172 mul:4\n\t" \
  "v_fma_f32 v175, |v175|, v235, v175 mul:4\n\t" \
  "v_mfma_f32_16x16x32_bf16 v[188:191], v[216:219], v[116:119], v[188:191]\n\t" \
  "ds_read_b32 v224, v224 offset:0\n\t" \
  "v_fma_f32 v174, |v174|, v234, v174 mul:4\n\t" \
  "v_fma_f32 v167, |v167|, v231, v167 mul:4\n\t" \
  "v_mfma_f32_16x16x32_bf16 v[196:199], v[216:219], v[128:131], v[196:199]\n\t" \
  "ds_read_b32 v227, v227 offset:15376\n\t" \
  "v_fma_f32 v166, |v166|, v230, v166 mul:4\n\t" \
  "v_cvt_pk_bf16_f32 v164, v164, v165\n\t" \
  "v_mfma_f32_16x16x32_bf16 v[204:207], v[216:219], v[140:143], v[204:207]\n\t" \
  "ds_read_b64_tr_b16 v[216:217], v249 offset:64\n\t" \
  "v_cvt_pk_bf16_f32 v165, v166, v167\n\t" \
  "v_cvt_pk_bf16_f32 v172, v172, v173\n\t" \
  "v_cvt_pk_bf16_f32 v173, v174, v175\n\t" \
  "s_setprio 0\n\t" \
  "v_cvt_pk_f16_f32 v228, |v194|, |v195| clamp\n\t" \
  "v_cvt_pknorm_i16_f32 v230, |v192|, |v193|\n\t" \
  "v_mfma_f32_16x16x32_bf16 v[16:19], v[242:245], v[236:239], v[16:19]\n\t" \
  "ds_read_b64_tr_b16 v[218:219], v249 offset:1600\n\t" \
  "v_cvt_pk_f16_f32 v232, |v202|, |v203| clamp\n\t" \
  "v_cvt_pk_f16_f32 v234, |v200|, |v201| clamp\n\t" \
  "v_mfma_f32_16x16x32_bf16 v[20:23], v[240:243], v[236:239], v[20:23]\n\t" \
  "buffer_load_dwordx4 v[236:239], v250, s[8:11], s28 offen offset:192\n\t" \
  "s_cmp_lg_u32 s58, 0\n\t" \
  "s_cbranch_scc1 .Lcnx_c28\n\t" \
  "s_add_u32 m0, s25, 16400\n\t" \
  "s_add_u32 s24, s24, s26\n\t" \
  "buffer_load_dwordx4 v254, s[8:11], s24 offen lds\n\t" \
  "s_cmp_lg_u32 s38, 4\n\t" \
  "s_cbranch_scc1 .Lcnx_c28\n\t" \
  "s_mov_b32 m0, 163632\n\t" \
  "s_add_u32 s30, s30, s31\n\t" \
  "buffer_load_dword v252, s[8:11], s30 offen lds\n\t" \
  ".Lcnx_c28:\n\t" \
  "ds_read_b32 v226, v226 offset:15376\n\t" \
  "v_and_b32_sdwa v229, s56, v228 dst_sel:DWORD dst_unused:UNUSED_PAD src0_sel:DWORD src1_sel:WORD_1\n\t" \
  "v_and_b32 v228, s56, v228\n\t" \
  "s_waitcnt lgkmcnt(12)\n\t" \
  "v_mfma_f32_16x16x32_bf16 v[0:3], v[208:211], v[146:149], v[0:3]\n\t" \
  "ds_read_b32 v229, v229 offset:0\n\t" \
  "v_lshlrev_b32_sdwa v231, 2, v230 dst_sel:DWORD dst_unused:UNUSED_PAD src0_sel:DWORD src1_sel:BYTE_3\n\t" \
  "v_lshlrev_b32_sdwa v230, 2, v230 dst_sel:DWORD dst_unused:UNUSED_PAD src0_sel:DWORD src1_sel:BYTE_1\n\t" \
  "v_mfma_f32_16x16x32_bf16 v[24:27], v[208:211], v[154:157], v[24:27]\n\t" \
  "ds_read_b32 v228, v228 offset:0\n\t" \
  "v_and_b32_sdwa v233, s56, v232 dst_sel:DWORD dst_unused:UNUSED_PAD src0_sel:DWORD src1_sel:WORD_1\n\t" \
  "v_and_b32 v232, s56, v232\n\t" \
  "v_mfma_f32_16x16x32_bf16 v[48:51], v[208:211], v[162:165], v[48:51]\n\t" \
  "ds_read_b32 v231, v231 offset:15376\n\t" \
  "v_and_b32_sdwa v235, s56, v234 dst_sel:DWORD dst_unused:UNUSED_PAD src0_sel:DWORD src1_sel:WORD_1\n\t" \
  "v_and_b32 v234, s56, v234\n\t" \
  "v_mfma_f32_16x16x32_bf16 v[72:75], v[208:211], v[170:173], v[72:75]\n\t" \
  "ds_read_b64_tr_b16 v[208:209], v249 offset:72\n\t" \
  "ds_read_b64_tr_b16 v[210:211], v249 offset:1608\n\t" \
  "s_waitcnt lgkmcnt(13)\n\t" \
  "v_fma_f32 v179, |v179|, v221, v179 mul:4\n\t" \
  "v_fma_f32 v178, |v178|, v220, v178 mul:4\n\t" \
  "s_waitcnt lgkmcnt(11)\n\t" \
  "v_mfma_f32_16x16x32_bf16 v[4:7], v[212:215], v[146:149], v[4:7]\n\t" \
  "ds_read_b32 v230, v230 offset:15376\n\t" \
  "v_fma_mix_f32 v223, |v177|, v223, v223 op_sel:[0,1,0] op_sel_hi:[0,1,1] clamp\n\t" \
  "v_fma_mix_f32 v222, |v176|, v222, v222 op_sel:[0,1,0] op_sel_hi:[0,1,1] clamp\n\t" \
  "v_mfma_f32_16x16x32_bf16 v[28:31], v[212:215], v[154:157], v[28:31]\n\t" \
  "ds_read_b32 v233, v233 offset:0\n\t" \
  "s_waitcnt lgkmcnt(7)\n\t" \
  "v_fma_f32 v187, |v187|, v225, v187 mul:4\n\t" \
  "v_fma_f32 v186, |v186|, v224, v186 mul:4\n\t" \
  "v_mfma_f32_16x16x32_bf16 v[52:55], v[212:215], v[162:165], v[52:55]\n\t" \
  "ds_read_b32 v232, v232 offset:0\n\t" \
  "v_fma_mix_f32 v227, |v185|, v227, v227 op_sel:[0,1,0] op_sel_hi:[0,1,1] clamp\n\t" \
  "v_fma_mix_f32 v226, |v184|, v226, v226 op_sel:[0,1,0] op_sel_hi:[0,1,1] clamp\n\t" \
  "v_fma_f32 v177, |v177|, v223, v177 mul:4\n\t" \
  "v_mfma_f32_16x16x32_bf16 v[76:79], v[212:215], v[170:173], v[76:79]\n\t" \
  "ds_read_b64_tr_b16 v[212:213], v249 offset:128\n\t" \
  "v_fma_f32 v176, |v176|, v222, v176 mul:4\n\t" \
  "v_fma_f32 v185, |v185|, v227, v185 mul:4\n\t" \
  "v_mfma_f32_16x16x32_bf16 v[8:11], v[216:219], v[146:149], v[8:11]\n\t" \
  "ds_read_b64_tr_b16 v[214:215], v249 offset:1664\n\t" \
  "v_fma_f32 v184, |v184|, v226, v184 mul:4\n\t" \
  "v_cvt_pk_bf16_f32 v179, v178, v179\n\t" \
  "v_mfma_f32_16x16x32_bf16 v[32:35], v[216:219], v[154:157], v[32:35]\n\t" \
  "ds_read_b32 v235, v235 offset:0\n\t" \
  "v_cvt_pk_bf16_f32 v178, v176, v177\n\t" \
  "v_cvt_pk_bf16_f32 v187, v186, v187\n\t" \
  "v_mfma_f32_16x16x32_bf16 v[56:59], v[216:219], v[162:165], v[56:59]\n\t" \
  "ds_read_b32 v234, v234 offset:0\n\t" \
  "v_cvt_pk_bf16_f32 v186, v184, v185\n\t" \
  "s_setprio 1\n\t" \
  "v_cvt_pk_f16_f32 v220, |v180|, |v181| clamp\n\t" \
  "v_mfma_f32_16x16x32_bf16 v[80:83], v[216:219], v[170:173], v[80:83]\n\t" \
  "ds_read_b64_tr_b16 v[216:217], v249 offset:136\n\t" \
  "v_cvt_pknorm_i16_f32 v222, |v182|, |v183|\n\t" \
  "v_cvt_pk_f16_f32 v224, |v188|, |v189| clamp\n\t" \
  "s_waitcnt lgkmcnt(8)\n\t" \
  "v_mfma_f32_16x16x32_bf16 v[12:15], v[208:211], v[146:149], v[12:15]\n\t" \
  "ds_read_b64_tr_b16 v[218:219], v249 offset:1672\n\t" \
  "v_cvt_pknorm_i16_f32 v226, |v190|, |v191|\n\t" \
  "v_and_b32_sdwa v221, s56, v220 dst_sel:DWORD dst_unused:UNUSED_PAD src0_sel:DWORD src1_sel:WORD_1\n\t" \
  "v_mfma_f32_16x16x32_bf16 v[36:39], v[208:211], v[154:157], v[36:39]\n\t" \
  "v_and_b32 v220, s56, v220\n\t" \
  "v_lshlrev_b32_sdwa v223, 2, v222 dst_sel:DWORD dst_unused:UNUSED_PAD src0_sel:DWORD src1_sel:BYTE_3\n\t" \
  "v_mfma_f32_16x16x32_bf16 v[60:63], v[208:211], v[162:165], v[60:63]\n\t" \
  "ds_read_b32 v221, v221 offset:0\n\t" \
  "v_lshlrev_b32_sdwa v222, 2, v222 dst_sel:DWORD dst_unused:UNUSED_PAD src0_sel:DWORD src1_sel:BYTE_1\n\t" \
  "v_and_b32_sdwa v225, s56, v224 dst_sel:DWORD dst_unused:UNUSED_PAD src0_sel:DWORD src1_sel:WORD_1\n\t" \
  "v_and_b32 v224, s56, v224\n\t" \
  "v_mfma_f32_16x16x32_bf16 v[84:87], v[208:211], v[170:173], v[84:87]\n\t" \
  "ds_read_b64_tr_b16 v[208:209], v248 offset:43048\n\t" \
  "ds_read_b64_tr_b16 v[210:211], v248 offset:44072\n\t" \
  "v_lshlrev_b32_sdwa v227, 2, v226 dst_sel:DWORD dst_unused:UNUSED_PAD src0_sel:DWORD src1_sel:BYTE_3\n\t" \
  "v_lshlrev_b32_sdwa v226, 2, v226 dst_sel:DWORD dst_unused:UNUSED_PAD src0_sel:DWORD src1_sel:BYTE_1\n\t" \
  "s_waitcnt lgkmcnt(7)\n\t" \
  "v_mfma_f32_16x16x32_bf16 v[16:19], v[212:215], v[146:149], v[16:19]\n\t" \
  "ds_read_b32 v220, v220 offset:0\n\t" \
  "ds_read_b32 v223, v223 offset:15376\n\t" \
  "v_fma_f32 v195, |v195|, v229, v195 mul:4\n\t" \
  "v_fma_f32 v194, |v194|, v228, v194 mul:4\n\t" \
  "v_mfma_f32_16x16x32_bf16 v[40:43], v[212:215], v[154:157], v[40:43]\n\t" \
  "ds_read_b32 v222, v222 offset:15376\n\t" \
  "v_fma_mix_f32 v231, |v193|, v231, v231 op_sel:[0,1,0] op_sel_hi:[0,1,1] clamp\n\t" \
  "v_fma_mix_f32 v230, |v192|, v230, v230 op_sel:[0,1,0] op_sel_hi:[0,1,1] clamp\n\t" \
  "v_mfma_f32_16x16x32_bf16 v[64:67], v[212:215], v[162:165], v[64:67]\n\t" \
  "ds_read_b32 v225, v225 offset:0\n\t" \
  "s_waitcnt lgkmcnt(9)\n\t" \
  "v_fma_f32 v203, |v203|, v233, v203 mul:4\n\t" \
  "v_fma_f32 v202, |v202|, v232, v202 mul:4\n\t" \
  "v_mfma_f32_16x16x32_bf16 v[88:91], v[212:215], v[170:173], v[88:91]\n\t" \
  "ds_read_b64_tr_b16 v[212:213], v248 offset:46120\n\t" \
  "v_fma_f32 v201, |v201|, v235, v201 mul:4\n\t" \
  "v_fma_f32 v200, |v200|, v234, v200 mul:4\n\t" \
  "s_waitcnt lgkmcnt(8)\n\t" \
  "v_mfma_f32_16x16x32_bf16 v[20:23], v[216:219], v[146:149], v[20:23]\n\t" \
  "ds_read_b64_tr_b16 v[214:215], v248 offset:47144\n\t" \
  "v_fma_f32 v193, |v193|, v231, v193 mul:4\n\t" \
  "v_fma_f32 v192, |v192|, v230, v192 mul:4\n\t" \
  "v_mfma_f32_16x16x32_bf16 v[44:47], v[216:219], v[154:157], v[44:47]\n\t" \
  "ds_read_b32 v224, v224 offset:0\n\t" \
  "v_cvt_pk_bf16_f32 v195, v194, v195\n\t" \
  "v_cvt_pk_bf16_f32 v194, v192, v193\n\t" \
  "v_mfma_f32_16x16x32_bf16 v[68:71], v[216:219], v[162:165], v[68:71]\n\t" \
  "ds_read_b32 v227, v227 offset:15376\n\t" \
  "v_cvt_pk_bf16_f32 v203, v202, v203\n\t" \
  "v_cvt_pk_bf16_f32 v202, v200, v201\n\t" \
  "v_cvt_pk_f16_f32 v228, |v196|, |v197| clamp\n\t" \
  "v_mfma_f32_16x16x32_bf16 v[92:95], v[216:219], v[170:173], v[92:95]\n\t" \
  "ds_read_b64_tr_b16 v[216:217], v248 offset:45096\n\t" \
  "v_cvt_pknorm_i16_f32 v230, |v198|, |v199|\n\t" \
  "s_setprio 2\n\t" \
  "v_cvt_pk_f16_f32 v232, |v204|, |v205| clamp\n\t" \
  "s_waitcnt lgkmcnt(9)\n\t" \
  "v_mfma_f32_16x16x32_bf16 v[144:147], v[208:211], v[96:99], 0\n\t" \
  "ds_read_b64_tr_b16 v[218:219], v248 offset:48168\n\t" \
  "v_cvt_pk_f16_f32 v234, |v206|, |v207| clamp\n\t" \
  "v_and_b32_sdwa v229, s56, v228 dst_sel:DWORD dst_unused:UNUSED_PAD src0_sel:DWORD src1_sel:WORD_1\n\t" \
  "v_mfma_f32_16x16x32_bf16 v[152:155], v[208:211], v[108:111], 0\n\t" \
  "ds_read_b32 v226, v226 offset:15376\n\t" \
  "v_and_b32 v228, s56, v228\n\t" \
  "v_lshlrev_b32_sdwa v231, 2, v230 dst_sel:DWORD dst_unused:UNUSED_PAD src0_sel:DWORD src1_sel:BYTE_3\n\t" \
  "v_mfma_f32_16x16x32_bf16 v[160:163], v[208:211], v[120:123], 0\n\t" \
  "ds_read_b32 v229, v229 offset:0\n\t" \
  "v_lshlrev_b32_sdwa v230, 2, v230 dst_sel:DWORD dst_unused:UNUSED_PAD src0_sel:DWORD src1_sel:BYTE_1\n\t" \
  "v_and_b32_sdwa v233, s56, v232 dst_sel:DWORD dst_unused:UNUSED_PAD src0_sel:DWORD src1_sel:WORD_1\n\t" \
  "v_mfma_f32_16x16x32_bf16 v[168:171], v[208:211], v[132:135], 0\n\t" \
  "ds_read_b64_tr_b16 v[208:209], v248 offset:43560\n\t" \
  "v_and_b32 v232, s56, v232\n\t" \
  "v_and_b32_sdwa v235, s56, v234 dst_sel:DWORD dst_unused:UNUSED_PAD src0_sel:DWORD src1_sel:WORD_1\n\t" \
  "s_waitcnt lgkmcnt(7)\n\t" \
  "v_mfma_f32_16x16x32_bf16 v[144:147], v[212:215], v[100:103], v[144:147]\n\t" \
  "ds_read_b64_tr_b16 v[210:211], v248 offset:44584\n\t" \
  "ds_read_b32 v228, v228 offset:0\n\t" \
  "v_and_b32 v234, s56, v234\n\t" \
  "v_fma_f32 v181, |v181|, v221, v181 mul:4\n\t" \
  "v_mfma_f32_16x16x32_bf16 v[152:155], v[212:215], v[112:115], v[152:155]\n\t" \
  "ds_read_b32 v231, v231 offset:15376\n\t" \
  "ds_read_b32 v230, v230 offset:15376\n\t" \
  "v_fma_f32 v180, |v180|, v220, v180 mul:4\n\t" \
  "v_fma_mix_f32 v223, |v183|, v223, v223 op_sel:[0,1,0] op_sel_hi:[0,1,1] clamp\n\t" \
  "v_mfma_f32_16x16x32_bf16 v[160:163], v[212:215], v[124:127], v[160:163]\n\t" \
  "ds_read_b32 v233, v233 offset:0\n\t" \
  "v_fma_mix_f32 v222, |v182|, v222, v222 op_sel:[0,1,0] op_sel_hi:[0,1,1] clamp\n\t" \
  "s_waitcnt lgkmcnt(7)\n\t" \
  "v_fma_f32 v189, |v189|, v225, v189 mul:4\n\t" \
  "v_mfma_f32_16x16x32_bf16 v[168:171], v[212:215], v[136:139], v[168:171]\n\t" \
  "ds_read_b64_tr_b16 v[212:213], v248 offset:46632\n\t" \
  "v_fma_f32 v188, |v188|, v224, v188 mul:4\n\t" \
  "v_fma_mix_f32 v227, |v191|, v227, v227 op_sel:[0,1,0] op_sel_hi:[0,1,1] clamp\n\t" \
  "v_fma_mix_f32 v226, |v190|, v226, v226 op_sel:[0,1,0] op_sel_hi:[0,1,1] clamp\n\t" \
  "v_mfma_f32_16x16x32_bf16 v[144:147], v[216:219], v[104:107], v[144:147]\n\t" \
  "ds_read_b64_tr_b16 v[214:215], v248 offset:47656\n\t" \
  "v_fma_f32 v183, |v183|, v223, v183 mul:4\n\t" \
  "v_fma_f32 v182, |v182|, v222, v182 mul:4\n\t" \
  "v_mfma_f32_16x16x32_bf16 v[152:155], v[216:219], v[116:119], v[152:155]\n\t" \
  "ds_read_b32 v232, v232 offset:0\n\t" \
  "v_fma_f32 v191, |v191|, v227, v191 mul:4\n\t" \
  "v_fma_f32 v190, |v190|, v226, v190 mul:4\n\t" \
  "v_mfma_f32_16x16x32_bf16 v[160:163], v[216:219], v[128:131], v[160:163]\n\t" \
  "ds_read_b32 v235, v235 offset:0\n\t" \
  "v_cvt_pk_bf16_f32 v180, v180, v181\n\t" \
  "v_cvt_pk_bf16_f32 v181, v182, v183\n\t" \
  "v_mfma_f32_16x16x32_bf16 v[168:171], v[216:219], v[140:143], v[168:171]\n\t" \
  "ds_read_b64_tr_b16 v[216:217], v248 offset:45608\n\t" \
  "v_cvt_pk_bf16_f32 v188, v188, v189\n\t" \
  "v_cvt_pk_bf16_f32 v189, v190, v191\n\t" \
  "s_waitcnt lgkmcnt(9)\n\t" \
  "v_mfma_f32_16x16x32_bf16 v[148:151], v[208:211], v[96:99], 0\n\t" \
  "ds_read_b64_tr_b16 v[218:219], v248 offset:48680\n\t" \
  "v_cvt_pk_f16_f32 v220, |v146|, |v147| clamp\n\t" \
  "s_setprio 3\n\t" \
  "v_cvt_pknorm_i16_f32 v222, |v144|, |v145|\n\t" \
  "v_mfma_f32_16x16x32_bf16 v[156:159], v[208:211], v[108:111], 0\n\t" \
  "s_waitcnt vmcnt(1)\n\t" \
  "s_cmp_lg_u32 s58, 1\n\t" \
  "s_cbranch_scc1 .Lcnx_c16\n\t" \
  "s_waitcnt vmcnt(0)\n\t" \
  ".Lcnx_c16:\n\t" \
  "s_mov_b64 exec, 3\n\t" \
  "ds_add_rtn_u32 v246, v251, v250 offset:47176\n\t" \
  "s_mov_b64 exec, -1\n\t" \
  "ds_read_b32 v234, v234 offset:0\n\t" \
  "v_cvt_pk_f16_f32 v224, |v154|, |v155| clamp\n\t" \
  "v_cvt_pknorm_i16_f32 v226, |v152|, |v153|\n\t" \
  "v_mfma_f32_16x16x32_bf16 v[164:167], v[208:211], v[120:123], 0\n\t" \
  "v_and_b32_sdwa v221, s56, v220 dst_sel:DWORD dst_unused:UNUSED_PAD src0_sel:DWORD src1_sel:WORD_1\n\t" \
  "v_and_b32 v220, s56, v220\n\t" \
  "v_mfma_f32_16x16x32_bf16 v[172:175], v[208:211], v[132:135], 0\n\t" \
  "s_waitcnt lgkmcnt(1)\n\t" \
  "v_readfirstlane_b32 s4, v246\n\t" \
  "s_cmp_lt_u32 s4, s57\n\t" \
  "s_cbranch_scc1 .Lcnx_w48\n.Lcnx_b48:\n\t" \
  "ds_read_b64_tr_b16 v[208:209], v249 offset:6152\n\t" \
  "v_lshlrev_b32_sdwa v223, 2, v222 dst_sel:DWORD dst_unused:UNUSED_PAD src0_sel:DWORD src1_sel:BYTE_3\n\t" \
  "v_lshlrev_b32_sdwa v222, 2, v222 dst_sel:DWORD dst_unused:UNUSED_PAD src0_sel:DWORD src1_sel:BYTE_1\n\t" \
  "v_and_b32_sdwa v225, s56, v224 dst_sel:DWORD dst_unused:UNUSED_PAD src0_sel:DWORD src1_sel:WORD_1\n\t" \
  "v_mfma_f32_16x16x32_bf16 v[148:151], v[212:215], v[100:103], v[148:151]\n\t" \
  "ds_read_b64_tr_b16 v[210:211], v249 offset:7688\n\t" \
  "v_and_b32 v224, s56, v224\n\t" \
  "v_lshlrev_b32_sdwa v227, 2, v226 dst_sel:DWORD dst_unused:UNUSED_PAD src0_sel:DWORD src1_sel:BYTE_3\n\t" \
  "v_mfma_f32_16x16x32_bf16 v[156:159], v[212:215], v[112:115], v[156:159]\n\t" \
  "s_cmp_eq_u32 s37, 0\n\t" \
  "s_cbranch_scc1 .Lcnx_f5\n.Lcnx_g5:\n\t" \
  "ds_read_b32 v221, v221 offset:0\n\t" \
  "ds_read_b32 v220, v220 offset:0\n\t" \
  "v_lshlrev_b32_sdwa v226, 2, v226 dst_sel:DWORD dst_unused:UNUSED_PAD src0_sel:DWORD src1_sel:BYTE_1\n\t" \
  "v_fma_f32 v197, |v197|, v229, v197 mul:4\n\t" \
  "v_mfma_f32_16x16x32_bf16 v[164:167], v[212:215], v[124:127], v[164:167]\n\t" \
  "ds_read_b32 v223, v223 offset:15376\n\t" \
  "ds_read_b32 v222, v222 offset:15376\n\t" \
  "v_fma_f32 v196, |v196|, v228, v196 mul:4\n\t" \
  "v_fma_mix_f32 v231, |v199|, v231, v231 op_sel:[0,1,0] op_sel_hi:[0,1,1] clamp\n\t" \
  "v_mfma_f32_16x16x32_bf16 v[172:175], v[212:215], v[136:139], v[172:175]\n\t" \
  "ds_read_b64_tr_b16 v[212:213], v249 offset:6160\n\t" \
  "ds_read_b64_tr_b16 v[214:215], v249 offset:7696\n\t" \
  "v_fma_mix_f32 v230, |v198|, v230, v230 op_sel:[0,1,0] op_sel_hi:[0,1,1] clamp\n\t" \
  "s_waitcnt lgkmcnt(8)\n\t" \
  "v_fma_f32 v205, |v205|, v233, v205 mul:4\n\t" \
  "v_mfma_f32_16x16x32_bf16 v[148:151], v[216:219], v[104:107], v[148:151]\n\t" \
  "ds_read_b32 v225, v225 offset:0\n\t" \
  "v_fma_f32 v204, |v204|, v232, v204 mul:4\n\t" \
  "v_fma_f32 v207, |v207|, v235, v207 mul:4\n\t" \
  "v_mfma_f32_16x16x32_bf16 v[156:159], v[216:219], v[116:119], v[156:159]\n\t" \
  "ds_read_b32 v224, v224 offset:0\n\t" \
  "v_fma_f32 v206, |v206|, v234, v206 mul:4\n\t" \
  "v_fma_f32 v199, |v199|, v231, v199 mul:4\n\t" \
  "v_mfma_f32_16x16x32_bf16 v[164:167], v[216:219], v[128:131], v[164:167]\n\t" \
  "ds_read_b32 v227, v227 offset:15376\n\t" \
  "v_fma_f32 v198, |v198|, v230, v198 mul:4\n\t" \
  "v_cvt_pk_bf16_f32 v196, v196, v197\n\t" \
  "v_mfma_f32_16x16x32_bf16 v[172:175], v[216:219], v[140:143], v[172:175]\n\t" \
  "ds_read_b64_tr_b16 v[216:217], v249 offset:6216\n\t" \
  "v_cvt_pk_bf16_f32 v197, v198, v199\n\t" \
  "v_cvt_pk_bf16_f32 v204, v204, v205\n\t" \
  "v_cvt_pk_bf16_f32 v205, v206, v207\n\t" \
  "s_setprio 0\n\t" \
  "v_cvt_pk_f16_f32 v228, |v162|, |v163| clamp\n\t" \
  "v_cvt_pknorm_i16_f32 v230, |v160|, |v161|\n\t" \
  "v_mfma_f32_16x16x32_bf16 v[24:27], v[242:245], v[236:239], v[24:27]\n\t" \
  "ds_read_b64_tr_b16 v[218:219], v249 offset:7752\n\t" \
  "v_cvt_pk_f16_f32 v232, |v170|, |v171| clamp\n\t" \
  "v_cvt_pk_f16_f32 v234, |v168|, |v169| clamp\n\t" \
  "v_mfma_f32_16x16x32_bf16 v[28:31], v[240:243], v[236:239], v[28:31]\n\t" \
  "buffer_load_dwordx4 v[236:239], v250, s[8:11], s28 offen offset:256\n\t" \
  "s_cmp_lg_u32 s58, 0\n\t" \
  "s_cbranch_scc1 .Lcnx_c29\n\t" \
  "s_add_u32 m0, s25, 24600\n\t" \
  "s_add_u32 s24, s24, s26\n\t" \
  "buffer_load_dwordx4 v254, s[8:11], s24 offen lds\n\t" \
  "s_cmp_lg_u32 s38, 5\n\t" \
  "s_cbranch_scc1 .Lcnx_c29\n\t" \
  "s_mov_b32 m0, 163632\n\t" \
  "s_add_u32 s30, s30, s31\n\t" \
  "buffer_load_dword v252, s[8:11], s30 offen lds\n\t" \
  ".Lcnx_c29:\n\t" \
  "ds_read_b32 v226, v226 offset:15376\n\t" \
  "v_and_b32_sdwa v229, s56, v228 dst_sel:DWORD dst_unused:UNUSED_PAD src0_sel:DWORD src1_sel:WORD_1\n\t" \
  "v_and_b32 v228, s56, v228\n\t" \
  "s_waitcnt lgkmcnt(12)\n\t" \
  "v_mfma_f32_16x16x32_bf16 v[0:3], v[208:211], v[178:181], v[0:3]\n\t" \
  "ds_read_b32 v229, v229 offset:0\n\t" \
  "v_lshlrev_b32_sdwa v231, 2, v230 dst_sel:DWORD dst_unused:UNUSED_PAD src0_sel:DWORD src1_sel:BYTE_3\n\t" \
  "v_lshlrev_b32_sdwa v230, 2, v230 dst_sel:DWORD dst_unused:UNUSED_PAD src0_sel:DWORD src1_sel:BYTE_1\n\t" \
  "v_mfma_f32_16x16x32_bf16 v[24:27], v[208:211], v[186:189], v[24:27]\n\t" \
  "ds_read_b32 v228, v228 offset:0\n\t" \
  "v_and_b32_sdwa v233, s56, v232 dst_sel:DWORD dst_unused:UNUSED_PAD src0_sel:DWORD src1_sel:WORD_1\n\t" \
  "v_and_b32 v232, s56, v232\n\t" \
  "v_mfma_f32_16x16x32_bf16 v[48:51], v[208:211], v[194:197], v[48:51]\n\t" \
  "ds_read_b32 v231, v231 offset:15376\n\t" \
  "v_and_b32_sdwa v235, s56, v234 dst_sel:DWORD dst_unused:UNUSED_PAD src0_sel:DWORD src1_sel:WORD_1\n\t" \
  "v_and_b32 v234, s56, v234\n\t" \
  "v_mfma_f32_16x16x32_bf16 v[72:75], v[208:211], v[202:205], v[72:75]\n\t" \
  "ds_read_b64_tr_b16 v[208:209], v249 offset:6224\n\t" \
  "ds_read_b64_tr_b16 v[210:211], v249 offset:7760\n\t" \
  "s_waitcnt lgkmcnt(13)\n\t" \
  "v_fma_f32 v147, |v147|, v221, v147 mul:4\n\t" \
  "v_fma_f32 v146, |v146|, v220, v146 mul:4\n\t" \
  "s_waitcnt lgkmcnt(11)\n\t" \
  "v_mfma_f32_16x16x32_bf16 v[4:7], v[212:215], v[178:181], v[4:7]\n\t" \
  "ds_read_b32 v230, v230 offset:15376\n\t" \
  "v_fma_mix_f32 v223, |v145|, v223, v223 op_sel:[0,1,0] op_sel_hi:[0,1,1] clamp\n\t" \
  "v_fma_mix_f32 v222, |v144|, v222, v222 op_sel:[0,1,0] op_sel_hi:[0,1,1] clamp\n\t" \
  "v_mfma_f32_16x16x32_bf16 v[28:31], v[212:215], v[186:189], v[28:31]\n\t" \
  "ds_read_b32 v233, v233 offset:0\n\t" \
  "s_waitcnt lgkmcnt(7)\n\t" \
  "v_fma_f32 v155, |v155|, v225, v155 mul:4\n\t" \
  "v_fma_f32 v154, |v154|, v224, v154 mul:4\n\t" \
  "v_mfma_f32_16x16x32_bf16 v[52:55], v[212:215], v[194:197], v[52:55]\n\t" \
  "ds_read_b32 v232, v232 offset:0\n\t" \
  "v_fma_mix_f32 v227, |v153|, v227, v227 op_sel:[0,1,0] op_sel_hi:[0,1,1] clamp\n\t" \
  "v_fma_mix_f32 v226, |v152|, v226, v226 op_sel:[0,1,0] op_sel_hi:[0,1,1] clamp\n\t" \
  "v_fma_f32 v145, |v145|, v223, v145 mul:4\n\t" \
  "v_mfma_f32_16x16x32_bf16 v[76:79], v[212:215], v[202:205], v[76:79]\n\t" \
  "ds_read_b64_tr_b16 v[212:213], v249 offset:6280\n\t" \
  "v_fma_f32 v144, |v144|, v222, v144 mul:4\n\t" \
  "v_fma_f32 v153, |v153|, v227, v153 mul:4\n\t" \
  "v_mfma_f32_16x16x32_bf16 v[8:11], v[216:219], v[178:181], v[8:11]\n\t" \
  "ds_read_b64_tr_b16 v[214:215], v249 offset:7816\n\t" \
  "v_fma_f32 v152, |v152|, v226, v152 mul:4\n\t" \
  "v_cvt_pk_bf16_f32 v147, v146, v147\n\t" \
  "v_mfma_f32_16x16x32_bf16 v[32:35], v[216:219], v[186:189], v[32:35]\n\t" \
  "ds_read_b32 v235, v235 offset:0\n\t" \
  "v_cvt_pk_bf16_f32 v146, v144, v145\n\t" \
  "v_cvt_pk_bf16_f32 v155, v154, v155\n\t" \
  "v_mfma_f32_16x16x32_bf16 v[56:59], v[216:219], v[194:197], v[56:59]\n\t" \
  "ds_read_b32 v234, v234 offset:0\n\t" \
  "v_cvt_pk_bf16_f32 v154, v152, v153\n\t" \
  "s_setprio 1\n\t" \
  "v_cvt_pk_f16_f32 v220, |v148|, |v149| clamp\n\t" \
  "v_mfma_f32_16x16x32_bf16 v[80:83], v[216:219], v[202:205], v[80:83]\n\t" \
  "ds_read_b64_tr_b16 v[216:217], v249 offset:6288\n\t" \
  "v_cvt_pknorm_i16_f32 v222, |v150|, |v151|\n\t" \
  "v_cvt_pk_f16_f32 v224, |v156|, |v157| clamp\n\t" \
  "s_waitcnt lgkmcnt(8)\n\t" \
  "v_mfma_f32_16x16x32_bf16 v[12:15], v[208:211], v[178:181], v[12:15]\n\t" \
  "ds_read_b64_tr_b16 v[218:219], v249 offset:7824\n\t" \
  "v_cvt_pknorm_i16_f32 v226, |v158|, |v159|\n\t" \
  "v_and_b32_sdwa v221, s56, v220 dst_sel:DWORD dst_unused:UNUSED_PAD src0_sel:DWORD src1_sel:WORD_1\n\t" \
  "v_mfma_f32_16x16x32_bf16 v[36:39], v[208:211], v[186:189], v[36:39]\n\t" \
  "v_and_b32 v220, s56, v220\n\t" \
  "v_lshlrev_b32_sdwa v223, 2, v222 dst_sel:DWORD dst_unused:UNUSED_PAD src0_sel:DWORD src1_sel:BYTE_3\n\t" \
  "v_mfma_f32_16x16x32_bf16 v[60:63], v[208:211], v[194:197], v[60:63]\n\t" \
  "ds_read_b32 v221, v221 offset:0\n\t" \
  "v_lshlrev_b32_sdwa v222, 2, v222 dst_sel:DWORD dst_unused:UNUSED_PAD src0_sel:DWORD src1_sel:BYTE_1\n\t" \
  "v_and_b32_sdwa v225, s56, v224 dst_sel:DWORD dst_unused:UNUSED_PAD src0_sel:DWORD src1_sel:WORD_1\n\t" \
  "v_and_b32 v224, s56, v224\n\t" \
  "v_mfma_f32_16x16x32_bf16 v[84:87], v[208:211], v[202:205], v[84:87]\n\t" \
  "ds_read_b64_tr_b16 v[208:209], v248 offset:49192\n\t" \
  "ds_read_b64_tr_b16 v[210:211], v248 offset:50216\n\t" \
  "v_lshlrev_b32_sdwa v227, 2, v226 dst_sel:DWORD dst_unused:UNUSED_PAD src0_sel:DWORD src1_sel:BYTE_3\n\t" \
  "v_lshlrev_b32_sdwa v226, 2, v226 dst_sel:DWORD dst_unused:UNUSED_PAD src0_sel:DWORD src1_sel:BYTE_1\n\t" \
  "s_waitcnt lgkmcnt(7)\n\t" \
  "v_mfma_f32_16x16x32_bf16 v[16:19], v[212:215], v[178:181], v[16:19]\n\t" \
  "ds_read_b32 v220, v220 offset:0\n\t" \
  "ds_read_b32 v223, v223 offset:15376\n\t" \
  "v_fma_f32 v163, |v163|, v229, v163 mul:4\n\t" \
  "v_fma_f32 v162, |v162|, v228, v162 mul:4\n\t" \
  "v_mfma_f32_16x16x32_bf16 v[40:43], v[212:215], v[186:189], v[40:43]\n\t" \
  "ds_read_b32 v222, v222 offset:15376\n\t" \
  "v_fma_mix_f32 v231, |v161|, v231, v231 op_sel:[0,1,0] op_sel_hi:[0,1,1] clamp\n\t" \
  "v_fma_mix_f32 v230, |v160|, v230, v230 op_sel:[0,1,0] op_sel_hi:[0,1,1] clamp\n\t" \
  "v_mfma_f32_16x16x32_bf16 v[64:67], v[212:215], v[194:197], v[64:67]\n\t" \
  "ds_read_b32 v225, v225 offset:0\n\t" \
  "s_waitcnt lgkmcnt(9)\n\t" \
  "v_fma_f32 v171, |v171|, v233, v171 mul:4\n\t" \
  "v_fma_f32 v170, |v170|, v232, v170 mul:4\n\t" \
  "v_mfma_f32_16x16x32_bf16 v[88:91], v[212:215], v[202:205], v[88:91]\n\t" \
  "ds_read_b64_tr_b16 v[212:213], v248 offset:52264\n\t" \
  "v_fma_f32 v169, |v169|, v235, v169 mul:4\n\t" \
  "v_fma_f32 v168, |v168|, v234, v168 mul:4\n\t" \
  "s_waitcnt lgkmcnt(8)\n\t" \
  "v_mfma_f32_16x16x32_bf16 v[20:23], v[216:219], v[178:181], v[20:23]\n\t" \
  "ds_read_b64_tr_b16 v[214:215], v248 offset:53288\n\t" \
  "v_fma_f32 v161, |v161|, v231, v161 mul:4\n\t" \
  "v_fma_f32 v160, |v160|, v230, v160 mul:4\n\t" \
  "v_mfma_f32_16x16x32_bf16 v[44:47], v[216:219], v[186:189], v[44:47]\n\t" \
  "ds_read_b32 v224, v224 offset:0\n\t" \
  "v_cvt_pk_bf16_f32 v163, v162, v163\n\t" \
  "v_cvt_pk_bf16_f32 v162, v160, v161\n\t" \
  "v_mfma_f32_16x16x32_bf16 v[68:71], v[216:219], v[194:197], v[68:71]\n\t" \
  "ds_read_b32 v227, v227 offset:15376\n\t" \
  "v_cvt_pk_bf16_f32 v171, v170, v171\n\t" \
  "v_cvt_pk_bf16_f32 v170, v168, v169\n\t" \
  "v_cvt_pk_f16_f32 v228, |v164|, |v165| clamp\n\t" \
  "v_mfma_f32_16x16x32_bf16 v[92:95], v[216:219], v[202:205], v[92:95]\n\t" \
  "ds_read_b64_tr_b16 v[216:217], v248 offset:51240\n\t" \
  "v_cvt_pknorm_i16_f32 v230, |v166|, |v167|\n\t" \
  "s_setprio 2\n\t" \
  "v_cvt_pk_f16_f32 v232, |v172|, |v173| clamp\n\t" \
  "s_waitcnt lgkmcnt(9)\n\t" \
  "v_mfma_f32_16x16x32_bf16 v[176:179], v[208:211], v[96:99], 0\n\t" \
  "ds_read_b64_tr_b16 v[218:219], v248 offset:54312\n\t" \
  "v_cvt_pk_f16_f32 v234, |v174|, |v175| clamp\n\t" \
  "v_and_b32_sdwa v229, s56, v228 dst_sel:DWORD dst_unused:UNUSED_PAD src0_sel:DWORD src1_sel:WORD_1\n\t" \
  "v_mfma_f32_16x16x32_bf16 v[184:187], v[208:211], v[108:111], 0\n\t" \
  "ds_read_b32 v226, v226 offset:15376\n\t" \
  "v_and_b32 v228, s56, v228\n\t" \
  "v_lshlrev_b32_sdwa v231, 2, v230 dst_sel:DWORD dst_unused:UNUSED_PAD src0_sel:DWORD src1_sel:BYTE_3\n\t" \
  "v_mfma_f32_16x16x32_bf16 v[192:195], v[208:211], v[120:123], 0\n\t" \
  "ds_read_b32 v229, v229 offset:0\n\t" \
  "v_lshlrev_b32_sdwa v230, 2, v230 dst_sel:DWORD dst_unused:UNUSED_PAD src0_sel:DWORD src1_sel:BYTE_1\n\t" \
  "v_and_b32_sdwa v233, s56, v232 dst_sel:DWORD dst_unused:UNUSED_PAD src0_sel:DWORD src1_sel:WORD_1\n\t" \
  "v_mfma_f32_16x16x32_bf16 v[200:203], v[208:211], v[132:135], 0\n\t" \
  "ds_read_b64_tr_b16 v[208:209], v248 offset:49704\n\t" \
  "v_and_b32 v232, s56, v232\n\t" \
  "v_and_b32_sdwa v235, s56, v234 dst_sel:DWORD dst_unused:UNUSED_PAD src0_sel:DWORD src1_sel:WORD_1\n\t" \
  "s_waitcnt lgkmcnt(7)\n\t" \
  "v_mfma_f32_16x16x32_bf16 v[176:179], v[212:215], v[100:103], v[176:179]\n\t" \
  "ds_read_b64_tr_b16 v[210:211], v248 offset:50728\n\t" \
  "ds_read_b32 v228, v228 offset:0\n\t" \
  "v_and_b32 v234, s56, v234\n\t" \
  "v_fma_f32 v149, |v149|, v221, v149 mul:4\n\t" \
  "v_mfma_f32_16x16x32_bf16 v[184:187], v[212:215], v[112:115], v[184:187]\n\t" \
  "ds_read_b32 v231, v231 offset:15376\n\t" \
  "ds_read_b32 v230, v230 offset:15376\n\t" \
  "v_fma_f32 v148, |v148|, v220, v148 mul:4\n\t" \
  "v_fma_mix_f32 v223, |v151|, v223, v223 op_sel:[0,1,0] op_sel_hi:[0,1,1] clamp\n\t" \
  "v_mfma_f32_16x16x32_bf16 v[192:195], v[212:215], v[124:127], v[192:195]\n\t" \
  "ds_read_b32 v233, v233 offset:0\n\t" \
  "v_fma_mix_f32 v222, |v150|, v222, v222 op_sel:[0,1,0] op_sel_hi:[0,1,1] clamp\n\t" \
  "s_waitcnt lgkmcnt(7)\n\t" \
  "v_fma_f32 v157, |v157|, v225, v157 mul:4\n\t" \
  "v_mfma_f32_16x16x32_bf16 v[200:203], v[212:215], v[136:139], v[200:203]\n\t" \
  "ds_read_b64_tr_b16 v[212:213], v248 offset:52776\n\t" \
  "v_fma_f32 v156, |v156|, v224, v156 mul:4\n\t" \
  "v_fma_mix_f32 v227, |v159|, v227, v227 op_sel:[0,1,0] op_sel_hi:[0,1,1] clamp\n\t" \
  "v_fma_mix_f32 v226, |v158|, v226, v226 op_sel:[0,1,0] op_sel_hi:[0,1,1] clamp\n\t" \
  "v_mfma_f32_16x16x32_bf16 v[176:179], v[216:219], v[104:107], v[176:179]\n\t" \
  "ds_read_b64_tr_b16 v[214:215], v248 offset:53800\n\t" \
  "v_fma_f32 v151, |v151|, v223, v151 mul:4\n\t" \
  "v_fma_f32 v150, |v150|, v222, v150 mul:4\n\t" \
  "v_mfma_f32_16x16x32_bf16 v[184:187], v[216:219], v[116:119], v[184:187]\n\t" \
  "ds_read_b32 v232, v232 offset:0\n\t" \
  "v_fma_f32 v159, |v159|, v227, v159 mul:4\n\t" \
  "v_fma_f32 v158, |v158|, v226, v158 mul:4\n\t" \
  "v_mfma_f32_16x16x32_bf16 v[192:195], v[216:219], v[128:131], v[192:195]\n\t" \
  "ds_read_b32 v235, v235 offset:0\n\t" \
  "v_cvt_pk_bf16_f32 v148, v148, v149\n\t" \
  "v_cvt_pk_bf16_f32 v149, v150, v151\n\t" \
  "v_mfma_f32_16x16x32_bf16 v[200:203], v[216:219], v[140:143], v[200:203]\n\t" \
  "ds_read_b64_tr_b16 v[216:217], v248 offset:51752\n\t" \
  "v_cvt_pk_bf16_f32 v156, v156, v157\n\t" \
  "v_cvt_pk_bf16_f32 v157, v158, v159\n\t" \
  "s_waitcnt lgkmcnt(9)\n\t" \
  "v_mfma_f32_16x16x32_bf16 v[180:183], v[208:211], v[96:99], 0\n\t" \
  "ds_read_b64_tr_b16 v[218:219], v248 offset:54824\n\t" \
  "v_cvt_pk_f16_f32 v220, |v178|, |v179| clamp\n\t" \
  "s_setprio 3\n\t" \
  "v_cvt_pknorm_i16_f32 v222, |v176|, |v177|\n\t" \
  "v_mfma_f32_16x16x32_bf16 v[188:191], v[208:211], v[108:111], 0\n\t" \
  "s_waitcnt vmcnt(1)\n\t" \
  "s_cmp_lg_u32 s58, 1\n\t" \
  "s_cbranch_scc1 .Lcnx_c17\n\t" \
  "s_waitcnt vmcnt(0)\n\t" \
  ".Lcnx_c17:\n\t" \
  "s_mov_b64 exec, 3\n\t" \
  "ds_add_rtn_u32 v246, v251, v250 offset:47184\n\t" \
  "s_mov_b64 exec, -1\n\t" \
  "ds_read_b32 v234, v234 offset:0\n\t" \
  "v_cvt_pk_f16_f32 v224, |v186|, |v187| clamp\n\t" \
  "v_cvt_pknorm_i16_f32 v226, |v184|, |v185|\n\t" \
  "v_mfma_f32_16x16x32_bf16 v[196:199], v[208:211], v[120:123], 0\n\t" \
  "v_and_b32_sdwa v221, s56, v220 dst_sel:DWORD dst_unused:UNUSED_PAD src0_sel:DWORD src1_sel:WORD_1\n\t" \
  "v_and_b32 v220, s56, v220\n\t" \
  "v_mfma_f32_16x16x32_bf16 v[204:207], v[208:211], v[132:135], 0\n\t" \
  "s_waitcnt lgkmcnt(1)\n\t" \
  "v_readfirstlane_b32 s4, v246\n\t" \
  "s_cmp_lt_u32 s4, s57\n\t" \
  "s_cbranch_scc1 .Lcnx_w60\n.Lcnx_b60:\n\t" \
  "ds_read_b64_tr_b16 v[208:209], v249 offset:24600\n\t" \
  "v_lshlrev_b32_sdwa v223, 2, v222 dst_sel:DWORD dst_unused:UNUSED_PAD src0_sel:DWORD src1_sel:BYTE_3\n\t" \
  "v_lshlrev_b32_sdwa v222, 2, v222 dst_sel:DWORD dst_unused:UNUSED_PAD src0_sel:DWORD src1_sel:BYTE_1\n\t" \
  "v_and_b32_sdwa v225, s56, v224 dst_sel:DWORD dst_unused:UNUSED_PAD src0_sel:DWORD src1_sel:WORD_1\n\t" \
  "v_mfma_f32_16x16x32_bf16 v[180:183], v[212:215], v[100:103], v[180:183]\n\t" \
  "ds_read_b64_tr_b16 v[210:211], v249 offset:26136\n\t" \
  "v_and_b32 v224, s56, v224\n\t" \
  "v_lshlrev_b32_sdwa v227, 2, v226 dst_sel:DWORD dst_unused:UNUSED_PAD src0_sel:DWORD src1_sel:BYTE_3\n\t" \
  "v_mfma_f32_16x16x32_bf16 v[188:191], v[212:215], v[112:115], v[188:191]\n\t" \
  "s_cmp_eq_u32 s37, 0\n\t" \
  "s_cbranch_scc1 .Lcnx_f6\n.Lcnx_g6:\n\t" \
  "ds_read_b32 v221, v221 offset:0\n\t" \
  "ds_read_b32 v220, v220 offset:0\n\t" \
  "v_lshlrev_b32_sdwa v226, 2, v226 dst_sel:DWORD dst_unused:UNUSED_PAD src0_sel:DWORD src1_sel:BYTE_1\n\t" \
  "v_fma_f32 v165, |v165|, v229, v165 mul:4\n\t" \
  "v_mfma_f32_16x16x32_bf16 v[196:199], v[212:215], v[124:127], v[196:199]\n\t" \
  "ds_read_b32 v223, v223 offset:15376\n\t" \
  "ds_read_b32 v222, v222 offset:15376\n\t" \
  "v_fma_f32 v164, |v164|, v228, v164 mul:4\n\t" \
  "v_fma_mix_f32 v231, |v167|, v231, v231 op_sel:[0,1,0] op_sel_hi:[0,1,1] clamp\n\t" \
  "v_mfma_f32_16x16x32_bf16 v[204:207], v[212:215], v[136:139], v[204:207]\n\t" \
  "ds_read_b64_tr_b16 v[212:213], v249 offset:24608\n\t" \
  "ds_read_b64_tr_b16 v[214:215], v249 offset:26144\n\t" \
  "v_fma_mix_f32 v230, |v166|, v230, v230 op_sel:[0,1,0] op_sel_hi:[0,1,1] clamp\n\t" \
  "s_waitcnt lgkmcnt(8)\n\t" \
  "v_fma_f32 v173, |v173|, v233, v173 mul:4\n\t" \
  "v_mfma_f32_16x16x32_bf16 v[180:183], v[216:219], v[104:107], v[180:183]\n\t" \
  "ds_read_b32 v225, v225 offset:0\n\t" \
  "v_fma_f32 v172, |v172|, v232, v172 mul:4\n\t" \
  "v_fma_f32 v175, |v175|, v235, v175 mul:4\n\t" \
  "v_mfma_f32_16x16x32_bf16 v[188:191], v[216:219], v[116:119], v[188:191]\n\t" \
  "ds_read_b32 v224, v224 offset:0\n\t" \
  "v_fma_f32 v174, |v174|, v234, v174 mul:4\n\t" \
  "v_fma_f32 v167, |v167|, v231, v167 mul:4\n\t" \
  "v_mfma_f32_16x16x32_bf16 v[196:199], v[216:219], v[128:131], v[196:199]\n\t" \
  "ds_read_b32 v227, v227 offset:15376\n\t" \
  "v_fma_f32 v166, |v166|, v230, v166 mul:4\n\t" \
  "v_cvt_pk_bf16_f32 v164, v164, v165\n\t" \
  "v_mfma_f32_16x16x32_bf16 v[204:207], v[216:219], v[140:143], v[204:207]\n\t" \
  "ds_read_b64_tr_b16 v[216:217], v249 offset:24664\n\t" \
  "v_cvt_pk_bf16_f32 v165, v166, v167\n\t" \
  "v_cvt_pk_bf16_f32 v172, v172, v173\n\t" \
  "v_cvt_pk_bf16_f32 v173, v174, v175\n\t" \
  "s_setprio 0\n\t" \
  "v_cvt_pk_f16_f32 v228, |v194|, |v195| clamp\n\t" \
  "v_cvt_pknorm_i16_f32 v230, |v192|, |v193|\n\t" \
  "v_mfma_f32_16x16x32_bf16 v[32:35], v[242:245], v[236:239], v[32:35]\n\t" \
  "ds_read_b64_tr_b16 v[218:219], v249 offset:26200\n\t" \
  "v_cvt_pk_f16_f32 v232, |v202|, |v203| clamp\n\t" \
  "v_cvt_pk_f16_f32 v234, |v200|, |v201| clamp\n\t" \
  "v_mfma_f32_16x16x32_bf16 v[36:39], v[240:243], v[236:239], v[36:39]\n\t" \
  "buffer_load_dwordx4 v[236:239], v250, s[8:11], s28 offen offset:320\n\t" \
  "s_cmp_lg_u32 s58, 0\n\t" \
  "s_cbranch_scc1 .Lcnx_c30\n\t" \
  "s_add_u32 m0, s25, 32800\n\t" \
  "s_add_u32 s24, s24, s26\n\t" \
  "buffer_load_dwordx4 v254, s[8:11], s24 offen lds\n\t" \
  "s_cmp_lg_u32 s38, 6\n\t" \
  "s_cbranch_scc1 .Lcnx_c30\n\t" \
  "s_mov_b32 m0, 163632\n\t" \
  "s_add_u32 s30, s30, s31\n\t" \
  "buffer_load_dword v252, s[8:11], s30 offen lds\n\t" \
  ".Lcnx_c30:\n\t" \
  "ds_read_b32 v226, v226 offset:15376\n\t" \
  "v_and_b32_sdwa v229, s56, v228 dst_sel:DWORD dst_unused:UNUSED_PAD src0_sel:DWORD src1_sel:WORD_1\n\t" \
  "v_and_b32 v228, s56, v228\n\t" \
  "s_waitcnt lgkmcnt(12)\n\t" \
  "v_mfma_f32_16x16x32_bf16 v[0:3], v[208:211], v[146:149], v[0:3]\n\t" \
  "ds_read_b32 v229, v229 offset:0\n\t" \
  "v_lshlrev_b32_sdwa v231, 2, v230 dst_sel:DWORD dst_unused:UNUSED_PAD src0_sel:DWORD src1_sel:BYTE_3\n\t" \
  "v_lshlrev_b32_sdwa v230, 2, v230 dst_sel:DWORD dst_unused:UNUSED_PAD src0_sel:DWORD src1_sel:BYTE_1\n\t" \
  "v_mfma_f32_16x16x32_bf16 v[24:27], v[208:211], v[154:157], v[24:27]\n\t" \
  "ds_read_b32 v228, v228 offset:0\n\t" \
  "v_and_b32_sdwa v233, s56, v232 dst_sel:DWORD dst_unused:UNUSED_PAD src0_sel:DWORD src1_sel:WORD_1\n\t" \
  "v_and_b32 v232, s56, v232\n\t" \
  "v_mfma_f32_16x16x32_bf16 v[48:51], v[208:211], v[162:165], v[48:51]\n\t" \
  "ds_read_b32 v231, v231 offset:15376\n\t" \
  "v_and_b32_sdwa v235, s56, v234 dst_sel:DWORD dst_unused:UNUSED_PAD src0_sel:DWORD src1_sel:WORD_1\n\t" \
  "v_and_b32 v234, s56, v234\n\t" \
  "v_mfma_f32_16x16x32_bf16 v[72:75], v[208:211], v[170:173], v[72:75]\n\t" \
  "ds_read_b64_tr_b16 v[208:209], v249 offset:24672\n\t" \
  "ds_read_b64_tr_b16 v[210:211], v249 offset:26208\n\t" \
  "s_waitcnt lgkmcnt(13)\n\t" \
  "v_fma_f32 v179, |v179|, v221, v179 mul:4\n\t" \
  "v_fma_f32 v178, |v178|, v220, v178 mul:4\n\t" \
  "s_waitcnt lgkmcnt(11)\n\t" \
  "v_mfma_f32_16x16x32_bf16 v[4:7], v[212:215], v[146:149], v[4:7]\n\t" \
  "ds_read_b32 v230, v230 offset:15376\n\t" \
  "v_fma_mix_f32 v223, |v177|, v223, v223 op_sel:[0,1,0] op_sel_hi:[0,1,1] clamp\n\t" \
  "v_fma_mix_f32 v222, |v176|, v222, v222 op_sel:[0,1,0] op_sel_hi:[0,1,1] clamp\n\t" \
  "v_mfma_f32_16x16x32_bf16 v[28:31], v[212:215], v[154:157], v[28:31]\n\t" \
  "ds_read_b32 v233, v233 offset:0\n\t" \
  "s_waitcnt lgkmcnt(7)\n\t" \
  "v_fma_f32 v187, |v187|, v225, v187 mul:4\n\t" \
  "v_fma_f32 v186, |v186|, v224, v186 mul:4\n\t" \
  "v_mfma_f32_16x16x32_bf16 v[52:55], v[212:215], v[162:165], v[52:55]\n\t" \
  "ds_read_b32 v232, v232 offset:0\n\t" \
  "v_fma_mix_f32 v227, |v185|, v227, v227 op_sel:[0,1,0] op_sel_hi:[0,1,1] clamp\n\t" \
  "v_fma_mix_f32 v226, |v184|, v226, v226 op_sel:[0,1,0] op_sel_hi:[0,1,1] clamp\n\t" \
  "v_fma_f32 v177, |v177|, v223, v177 mul:4\n\t" \
  "v_mfma_f32_16x16x32_bf16 v[76:79], v[212:215], v[170:173], v[76:79]\n\t" \
  "ds_read_b64_tr_b16 v[212:213], v249 offset:24728\n\t" \
  "v_fma_f32 v176, |v176|, v222, v176 mul:4\n\t" \
  "v_fma_f32 v185, |v185|, v227, v185 mul:4\n\t" \
  "v_mfma_f32_16x16x32_bf16 v[8:11], v[216:219], v[146:149], v[8:11]\n\t" \
  "ds_read_b64_tr_b16 v[214:215], v249 offset:26264\n\t" \
  "v_fma_f32 v184, |v184|, v226, v184 mul:4\n\t" \
  "v_cvt_pk_bf16_f32 v179, v178, v179\n\t" \
  "v_mfma_f32_16x16x32_bf16 v[32:35], v[216:219], v[154:157], v[32:35]\n\t" \
  "ds_read_b32 v235, v235 offset:0\n\t" \
  "v_cvt_pk_bf16_f32 v178, v176, v177\n\t" \
  "v_cvt_pk_bf16_f32 v187, v186, v187\n\t" \
  "v_mfma_f32_16x16x32_bf16 v[56:59], v[216:219], v[162:165], v[56:59]\n\t" \
  "ds_read_b32 v234, v234 offset:0\n\t" \
  "v_cvt_pk_bf16_f32 v186, v184, v185\n\t" \
  "s_setprio 1\n\t" \
  "v_cvt_pk_f16_f32 v220, |v180|, |v181| clamp\n\t" \
  "v_mfma_f32_16x16x32_bf16 v[80:83], v[216:219], v[170:173], v[80:83]\n\t" \
  "ds_read_b64_tr_b16 v[216:217], v249 offset:24736\n\t" \
  "v_cvt_pknorm_i16_f32 v222, |v182|, |v183|\n\t" \
  "v_cvt_pk_f16_f32 v224, |v188|, |v189| clamp\n\t" \
  "s_waitcnt lgkmcnt(8)\n\t" \
  "v_mfma_f32_16x16x32_bf16 v[12:15], v[208:211], v[146:149], v[12:15]\n\t" \
  "ds_read_b64_tr_b16 v[218:219], v249 offset:26272\n\t" \
  "v_cvt_pknorm_i16_f32 v226, |v190|, |v191|\n\t" \
  "v_and_b32_sdwa v221, s56, v220 dst_sel:DWORD dst_unused:UNUSED_PAD src0_sel:DWORD src1_sel:WORD_1\n\t" \
  "v_mfma_f32_16x16x32_bf16 v[36:39], v[208:211], v[154:157], v[36:39]\n\t" \
  "v_and_b32 v220, s56, v220\n\t" \
  "v_lshlrev_b32_sdwa v223, 2, v222 dst_sel:DWORD dst_unused:UNUSED_PAD src0_sel:DWORD src1_sel:BYTE_3\n\t" \
  "v_mfma_f32_16x16x32_bf16 v[60:63], v[208:211], v[162:165], v[60:63]\n\t" \
  "ds_read_b32 v221, v221 offset:0\n\t" \
  "v_lshlrev_b32_sdwa v222, 2, v222 dst_sel:DWORD dst_unused:UNUSED_PAD src0_sel:DWORD src1_sel:BYTE_1\n\t" \
  "v_and_b32_sdwa v225, s56, v224 dst_sel:DWORD dst_unused:UNUSED_PAD src0_sel:DWORD src1_sel:WORD_1\n\t" \
  "v_and_b32 v224, s56, v224\n\t" \
  "v_mfma_f32_16x16x32_bf16 v[84:87], v[208:211], v[170:173], v[84:87]\n\t" \
  "ds_read_b64_tr_b16 v[208:209], v248 offset:36904\n\t" \
  "ds_read_b64_tr_b16 v[210:211], v248 offset:37928\n\t" \
  "v_lshlrev_b32_sdwa v227, 2, v226 dst_sel:DWORD dst_unused:UNUSED_PAD src0_sel:DWORD src1_sel:BYTE_3\n\t" \
  "v_lshlrev_b32_sdwa v226, 2, v226 dst_sel:DWORD dst_unused:UNUSED_PAD src0_sel:DWORD src1_sel:BYTE_1\n\t" \
  "s_waitcnt lgkmcnt(7)\n\t" \
  "v_mfma_f32_16x16x32_bf16 v[16:19], v[212:215], v[146:149], v[16:19]\n\t" \
  "ds_read_b32 v220, v220 offset:0\n\t" \
  "ds_read_b32 v223, v223 offset:15376\n\t" \
  "v_fma_f32 v195, |v195|, v229, v195 mul:4\n\t" \
  "v_fma_f32 v194, |v194|, v228, v194 mul:4\n\t" \
  "v_mfma_f32_16x16x32_bf16 v[40:43], v[212:215], v[154:157], v[40:43]\n\t" \
  "ds_read_b32 v222, v222 offset:15376\n\t" \
  "v_fma_mix_f32 v231, |v193|, v231, v231 op_sel:[0,1,0] op_sel_hi:[0,1,1] clamp\n\t" \
  "v_fma_mix_f32 v230, |v192|, v230, v230 op_sel:[0,1,0] op_sel_hi:[0,1,1] clamp\n\t" \
  "v_mfma_f32_16x16x32_bf16 v[64:67], v[212:215], v[162:165], v[64:67]\n\t" \
  "ds_read_b32 v225, v225 offset:0\n\t" \
  "s_waitcnt lgkmcnt(9)\n\t" \
  "v_fma_f32 v203, |v203|, v233, v203 mul:4\n\t" \
  "v_fma_f32 v202, |v202|, v232, v202 mul:4\n\t" \
  "v_mfma_f32_16x16x32_bf16 v[88:91], v[212:215], v[170:173], v[88:91]\n\t" \
  "ds_read_b64_tr_b16 v[212:213], v248 offset:39976\n\t" \
  "v_fma_f32 v201, |v201|, v235, v201 mul:4\n\t" \
  "v_fma_f32 v200, |v200|, v234, v200 mul:4\n\t" \
  "s_waitcnt lgkmcnt(8)\n\t" \
  "v_mfma_f32_16x16x32_bf16 v[20:23], v[216:219], v[146:149], v[20:23]\n\t" \
  "ds_read_b64_tr_b16 v[214:215], v248 offset:41000\n\t" \
  "v_fma_f32 v193, |v193|, v231, v193 mul:4\n\t" \
  "v_fma_f32 v192, |v192|, v230, v192 mul:4\n\t" \
  "v_mfma_f32_16x16x32_bf16 v[44:47], v[216:219], v[154:157], v[44:47]\n\t" \
  "ds_read_b32 v224, v224 offset:0\n\t" \
  "v_cvt_pk_bf16_f32 v195, v194, v195\n\t" \
  "v_cvt_pk_bf16_f32 v194, v192, v193\n\t" \
  "v_mfma_f32_16x16x32_bf16 v[68:71], v[216:219], v[162:165], v[68:71]\n\t" \
  "ds_read_b32 v227, v227 offset:15376\n\t" \
  "v_cvt_pk_bf16_f32 v203, v202, v203\n\t" \
  "v_cvt_pk_bf16_f32 v202, v200, v201\n\t" \
  "v_cvt_pk_f16_f32 v228, |v196|, |v197| clamp\n\t" \
  "v_mfma_f32_16x16x32_bf16 v[92:95], v[216:219], v[170:173], v[92:95]\n\t" \
  "ds_read_b64_tr_b16 v[216:217], v248 offset:38952\n\t" \
  "v_cvt_pknorm_i16_f32 v230, |v198|, |v199|\n\t" \
  "s_setprio 2\n\t" \
  "v_cvt_pk_f16_f32 v232, |v204|, |v205| clamp\n\t" \
  "s_waitcnt lgkmcnt(9)\n\t" \
  "v_mfma_f32_16x16x32_bf16 v[144:147], v[208:211], v[96:99], 0\n\t" \
  "ds_read_b64_tr_b16 v[218:219], v248 offset:42024\n\t" \
  "v_cvt_pk_f16_f32 v234, |v206|, |v207| clamp\n\t" \
  "v_and_b32_sdwa v229, s56, v228 dst_sel:DWORD dst_unused:UNUSED_PAD src0_sel:DWORD src1_sel:WORD_1\n\t" \
  "v_mfma_f32_16x16x32_bf16 v[152:155], v[208:211], v[108:111], 0\n\t" \
  "ds_read_b32 v226, v226 offset:15376\n\t" \
  "v_and_b32 v228, s56, v228\n\t" \
  "v_lshlrev_b32_sdwa v231, 2, v230 dst_sel:DWORD dst_unused:UNUSED_PAD src0_sel:DWORD src1_sel:BYTE_3\n\t" \
  "v_mfma_f32_16x16x32_bf16 v[160:163], v[208:211], v[120:123], 0\n\t" \
  "ds_read_b32 v229, v229 offset:0\n\t" \
  "v_lshlrev_b32_sdwa v230, 2, v230 dst_sel:DWORD dst_unused:UNUSED_PAD src0_sel:DWORD src1_sel:BYTE_1\n\t" \
  "v_and_b32_sdwa v233, s56, v232 dst_sel:DWORD dst_unused:UNUSED_PAD src0_sel:DWORD src1_sel:WORD_1\n\t" \
  "v_mfma_f32_16x16x32_bf16 v[168:171], v[208:211], v[132:135], 0\n\t" \
  "ds_read_b64_tr_b16 v[208:209], v248 offset:37416\n\t" \
  "v_and_b32 v232, s56, v232\n\t" \
  "v_and_b32_sdwa v235, s56, v234 dst_sel:DWORD dst_unused:UNUSED_PAD src0_sel:DWORD src1_sel:WORD_1\n\t" \
  "s_waitcnt lgkmcnt(7)\n\t" \
  "v_mfma_f32_16x16x32_bf16 v[144:147], v[212:215], v[100:103], v[144:147]\n\t" \
  "ds_read_b64_tr_b16 v[210:211], v248 offset:38440\n\t" \
  "ds_read_b32 v228, v228 offset:0\n\t" \
  "v_and_b32 v234, s56, v234\n\t" \
  "v_fma_f32 v181, |v181|, v221, v181 mul:4\n\t" \
  "v_mfma_f32_16x16x32_bf16 v[152:155], v[212:215], v[112:115], v[152:155]\n\t" \
  "ds_read_b32 v231, v231 offset:15376\n\t" \
  "ds_read_b32 v230, v230 offset:15376\n\t" \
  "v_fma_f32 v180, |v180|, v220, v180 mul:4\n\t" \
  "v_fma_mix_f32 v223, |v183|, v223, v223 op_sel:[0,1,0] op_sel_hi:[0,1,1] clamp\n\t" \
  "v_mfma_f32_16x16x32_bf16 v[160:163], v[212:215], v[124:127], v[160:163]\n\t" \
  "ds_read_b32 v233, v233 offset:0\n\t" \
  "v_fma_mix_f32 v222, |v182|, v222, v222 op_sel:[0,1,0] op_sel_hi:[0,1,1] clamp\n\t" \
  "s_waitcnt lgkmcnt(7)\n\t" \
  "v_fma_f32 v189, |v189|, v225, v189 mul:4\n\t" \
  "v_mfma_f32_16x16x32_bf16 v[168:171], v[212:215], v[136:139], v[168:171]\n\t" \
  "ds_read_b64_tr_b16 v[212:213], v248 offset:40488\n\t" \
  "v_fma_f32 v188, |v188|, v224, v188 mul:4\n\t" \
  "v_fma_mix_f32 v227, |v191|, v227, v227 op_sel:[0,1,0] op_sel_hi:[0,1,1] clamp\n\t" \
  "v_fma_mix_f32 v226, |v190|, v226, v226 op_sel:[0,1,0] op_sel_hi:[0,1,1] clamp\n\t" \
  "v_mfma_f32_16x16x32_bf16 v[144:147], v[216:219], v[104:107], v[144:147]\n\t" \
  "ds_read_b64_tr_b16 v[214:215], v248 offset:41512\n\t" \
  "v_fma_f32 v183, |v183|, v223, v183 mul:4\n\t" \
  "v_fma_f32 v182, |v182|, v222, v182 mul:4\n\t" \
  "v_mfma_f32_16x16x32_bf16 v[152:155], v[216:219], v[116:119], v[152:155]\n\t" \
  "ds_read_b32 v232, v232 offset:0\n\t" \
  "v_fma_f32 v191, |v191|, v227, v191 mul:4\n\t" \
  "v_fma_f32 v190, |v190|, v226, v190 mul:4\n\t" \
  "v_mfma_f32_16x16x32_bf16 v[160:163], v[216:219], v[128:131], v[160:163]\n\t" \
  "ds_read_b32 v235, v235 offset:0\n\t" \
  "v_cvt_pk_bf16_f32 v180, v180, v181\n\t" \
  "v_cvt_pk_bf16_f32 v181, v182, v183\n\t" \
  "v_mfma_f32_16x16x32_bf16 v[168:171], v[216:219], v[140:143], v[168:171]\n\t" \
  "ds_read_b64_tr_b16 v[216:217], v248 offset:39464\n\t" \
  "v_cvt_pk_bf16_f32 v188, v188, v189\n\t" \
  "v_cvt_pk_bf16_f32 v189, v190, v191\n\t" \
  "s_waitcnt lgkmcnt(9)\n\t" \
  "v_mfma_f32_16x16x32_bf16 v[148:151], v[208:211], v[96:99], 0\n\t" \
  "ds_read_b64_tr_b16 v[218:219], v248 offset:42536\n\t" \
  "v_cvt_pk_f16_f32 v220, |v146|, |v147| clamp\n\t" \
  "s_setprio 3\n\t" \
  "v_cvt_pknorm_i16_f32 v222, |v144|, |v145|\n\t" \
  "v_mfma_f32_16x16x32_bf16 v[156:159], v[208:211], v[108:111], 0\n\t" \
  "s_waitcnt vmcnt(1)\n\t" \
  "s_cmp_lg_u32 s58, 1\n\t" \
  "s_cbranch_scc1 .Lcnx_c18\n\t" \
  "s_waitcnt vmcnt(0)\n\t" \
  ".Lcnx_c18:\n\t" \
  "s_mov_b64 exec, 3\n\t" \
  "ds_add_rtn_u32 v246, v251, v250 offset:47192\n\t" \
  "s_mov_b64 exec, -1\n\t" \
  "ds_read_b32 v234, v234 offset:0\n\t" \
  "v_cvt_pk_f16_f32 v224, |v154|, |v155| clamp\n\t" \
  "v_cvt_pknorm_i16_f32 v226, |v152|, |v153|\n\t" \
  "v_mfma_f32_16x16x32_bf16 v[164:167], v[208:211], v[120:123], 0\n\t" \
  "v_and_b32_sdwa v221, s56, v220 dst_sel:DWORD dst_unused:UNUSED_PAD src0_sel:DWORD src1_sel:WORD_1\n\t" \
  "v_and_b32 v220, s56, v220\n\t" \
  "v_mfma_f32_16x16x32_bf16 v[172:175], v[208:211], v[132:135], 0\n\t" \
  "s_waitcnt lgkmcnt(1)\n\t" \
  "v_readfirstlane_b32 s4, v246\n\t" \
  "s_cmp_lt_u32 s4, s57\n\t" \
  "s_cbranch_scc1 .Lcnx_w72\n.Lcnx_b72:\n\t" \
  "ds_read_b64_tr_b16 v[208:209], v249 offset:30752\n\t" \
  "v_lshlrev_b32_sdwa v223, 2, v222 dst_sel:DWORD dst_unused:UNUSED_PAD src0_sel:DWORD src1_sel:BYTE_3\n\t" \
  "v_lshlrev_b32_sdwa v222, 2, v222 dst_sel:DWORD dst_unused:UNUSED_PAD src0_sel:DWORD src1_sel:BYTE_1\n\t" \
  "v_and_b32_sdwa v225, s56, v224 dst_sel:DWORD dst_unused:UNUSED_PAD src0_sel:DWORD src1_sel:WORD_1\n\t" \
  "v_mfma_f32_16x16x32_bf16 v[148:151], v[212:215], v[100:103], v[148:151]\n\t" \
  "ds_read_b64_tr_b16 v[210:211], v249 offset:32288\n\t" \
  "v_and_b32 v224, s56, v224\n\t" \
  "v_lshlrev_b32_sdwa v227, 2, v226 dst_sel:DWORD dst_unused:UNUSED_PAD src0_sel:DWORD src1_sel:BYTE_3\n\t" \
  "v_mfma_f32_16x16x32_bf16 v[156:159], v[212:215], v[112:115], v[156:159]\n\t" \
  "s_cmp_eq_u32 s37, 0\n\t" \
  "s_cbranch_scc1 .Lcnx_f7\n.Lcnx_g7:\n\t" \
  "ds_read_b32 v221, v221 offset:0\n\t" \
  "ds_read_b32 v220, v220 offset:0\n\t" \
  "v_lshlrev_b32_sdwa v226, 2, v226 dst_sel:DWORD dst_unused:UNUSED_PAD src0_sel:DWORD src1_sel:BYTE_1\n\t" \
  "v_fma_f32 v197, |v197|, v229, v197 mul:4\n\t" \
  "v_mfma_f32_16x16x32_bf16 v[164:167], v[212:215], v[124:127], v[164:167]\n\t" \
  "ds_read_b32 v223, v223 offset:15376\n\t" \
  "ds_read_b32 v222, v222 offset:15376\n\t" \
  "v_fma_f32 v196, |v196|, v228, v196 mul:4\n\t" \
  "v_fma_mix_f32 v231, |v199|, v231, v231 op_sel:[0,1,0] op_sel_hi:[0,1,1] clamp\n\t" \
  "v_mfma_f32_16x16x32_bf16 v[172:175], v[212:215], v[136:139], v[172:175]\n\t" \
  "ds_read_b64_tr_b16 v[212:213], v249 offset:30760\n\t" \
  "ds_read_b64_tr_b16 v[214:215], v249 offset:32296\n\t" \
  "v_fma_mix_f32 v230, |v198|, v230, v230 op_sel:[0,1,0] op_sel_hi:[0,1,1] clamp\n\t" \
  "s_waitcnt lgkmcnt(8)\n\t" \
  "v_fma_f32 v205, |v205|, v233, v205 mul:4\n\t" \
  "v_mfma_f32_16x16x32_bf16 v[148:151], v[216:219], v[104:107], v[148:151]\n\t" \
  "ds_read_b32 v225, v225 offset:0\n\t" \
  "v_fma_f32 v204, |v204|, v232, v204 mul:4\n\t" \
  "v_fma_f32 v207, |v207|, v235, v207 mul:4\n\t" \
  "v_mfma_f32_16x16x32_bf16 v[156:159], v[216:219], v[116:119], v[156:159]\n\t" \
  "ds_read_b32 v224, v224 offset:0\n\t" \
  "v_fma_f32 v206, |v206|, v234, v206 mul:4\n\t" \
  "v_fma_f32 v199, |v199|, v231, v199 mul:4\n\t" \
  "v_mfma_f32_16x16x32_bf16 v[164:167], v[216:219], v[128:131], v[164:167]\n\t" \
  "ds_read_b32 v227, v227 offset:15376\n\t" \
  "v_fma_f32 v198, |v198|, v230, v198 mul:4\n\t" \
  "v_cvt_pk_bf16_f32 v196, v196, v197\n\t" \
  "v_mfma_f32_16x16x32_bf16 v[172:175], v[216:219], v[140:143], v[172:175]\n\t" \
  "ds_read_b64_tr_b16 v[216:217], v249 offset:30816\n\t" \
  "v_cvt_pk_bf16_f32 v197, v198, v199\n\t" \
  "v_cvt_pk_bf16_f32 v204, v204, v205\n\t" \
  "v_cvt_pk_bf16_f32 v205, v206, v207\n\t" \
  "s_setprio 0\n\t" \
  "v_cvt_pk_f16_f32 v228, |v162|, |v163| clamp\n\t" \
  "v_cvt_pknorm_i16_f32 v230, |v160|, |v161|\n\t" \
  "v_mfma_f32_16x16x32_bf16 v[40:43], v[242:245], v[236:239], v[40:43]\n\t" \
  "ds_read_b64_tr_b16 v[218:219], v249 offset:32352\n\t" \
  "v_cvt_pk_f16_f32 v232, |v170|, |v171| clamp\n\t" \
  "v_cvt_pk_f16_f32 v234, |v168|, |v169| clamp\n\t" \
  "v_mfma_f32_16x16x32_bf16 v[44:47], v[240:243], v[236:239], v[44:47]\n\t" \
  "buffer_load_dwordx4 v[236:239], v250, s[8:11], s29 offen offset:0\n\t" \
  "s_cmp_lg_u32 s58, 0\n\t" \
  "s_cbranch_scc1 .Lcnx_c31\n\t" \
  "s_add_u32 m0, s25, 41000\n\t" \
  "s_add_u32 s24, s24, s26\n\t" \
  "buffer_load_dwordx4 v254, s[8:11], s24 offen lds\n\t" \
  "s_cmp_lg_u32 s38, 7\n\t" \
  "s_cbranch_scc1 .Lcnx_c31\n\t" \
  "s_mov_b32 m0, 163632\n\t" \
  "s_add_u32 s30, s30, s31\n\t" \
  "buffer_load_dword v252, s[8:11], s30 offen lds\n\t" \
  ".Lcnx_c31:\n\t" \
  "ds_read_b32 v226, v226 offset:15376\n\t" \
  "v_and_b32_sdwa v229, s56, v228 dst_sel:DWORD dst_unused:UNUSED_PAD src0_sel:DWORD src1_sel:WORD_1\n\t" \
  "v_and_b32 v228, s56, v228\n\t" \
  "s_waitcnt lgkmcnt(12)\n\t" \
  "v_mfma_f32_16x16x32_bf16 v[0:3], v[208:211], v[178:181], v[0:3]\n\t" \
  "ds_read_b32 v229, v229 offset:0\n\t" \
  "v_lshlrev_b32_sdwa v231, 2, v230 dst_sel:DWORD dst_unused:UNUSED_PAD src0_sel:DWORD src1_sel:BYTE_3\n\t" \
  "v_lshlrev_b32_sdwa v230, 2, v230 dst_sel:DWORD dst_unused:UNUSED_PAD src0_sel:DWORD src1_sel:BYTE_1\n\t" \
  "v_mfma_f32_16x16x32_bf16 v[24:27], v[208:211], v[186:189], v[24:27]\n\t" \
  "ds_read_b32 v228, v228 offset:0\n\t" \
  "v_and_b32_sdwa v233, s56, v232 dst_sel:DWORD dst_unused:UNUSED_PAD src0_sel:DWORD src1_sel:WORD_1\n\t" \
  "v_and_b32 v232, s56, v232\n\t" \
  "v_mfma_f32_16x16x32_bf16 v[48:51], v[208:211], v[194:197], v[48:51]\n\t" \
  "ds_read_b32 v231, v231 offset:15376\n\t" \
  "v_and_b32_sdwa v235, s56, v234 dst_sel:DWORD dst_unused:UNUSED_PAD src0_sel:DWORD src1_sel:WORD_1\n\t" \
  "v_and_b32 v234, s56, v234\n\t" \
  "v_mfma_f32_16x16x32_bf16 v[72:75], v[208:211], v[202:205], v[72:75]\n\t" \
  "ds_read_b64_tr_b16 v[208:209], v249 offset:30824\n\t" \
  "ds_read_b64_tr_b16 v[210:211], v249 offset:32360\n\t" \
  "s_waitcnt lgkmcnt(13)\n\t" \
  "v_fma_f32 v147, |v147|, v221, v147 mul:4\n\t" \
  "v_fma_f32 v146, |v146|, v220, v146 mul:4\n\t" \
  "s_waitcnt lgkmcnt(11)\n\t" \
  "v_mfma_f32_16x16x32_bf16 v[4:7], v[212:215], v[178:181], v[4:7]\n\t" \
  "ds_read_b32 v230, v230 offset:15376\n\t" \
  "v_fma_mix_f32 v223, |v145|, v223, v223 op_sel:[0,1,0] op_sel_hi:[0,1,1] clamp\n\t" \
  "v_fma_mix_f32 v222, |v144|, v222, v222 op_sel:[0,1,0] op_sel_hi:[0,1,1] clamp\n\t" \
  "v_mfma_f32_16x16x32_bf16 v[28:31], v[212:215], v[186:189], v[28:31]\n\t" \
  "ds_read_b32 v233, v233 offset:0\n\t" \
  "s_waitcnt lgkmcnt(7)\n\t" \
  "v_fma_f32 v155, |v155|, v225, v155 mul:4\n\t" \
  "v_fma_f32 v154, |v154|, v224, v154 mul:4\n\t" \
  "v_mfma_f32_16x16x32_bf16 v[52:55], v[212:215], v[194:197], v[52:55]\n\t" \
  "ds_read_b32 v232, v232 offset:0\n\t" \
  "v_fma_mix_f32 v227, |v153|, v227, v227 op_sel:[0,1,0] op_sel_hi:[0,1,1] clamp\n\t" \
  "v_fma_mix_f32 v226, |v152|, v226, v226 op_sel:[0,1,0] op_sel_hi:[0,1,1] clamp\n\t" \
  "v_fma_f32 v145, |v145|, v223, v145 mul:4\n\t" \
  "v_mfma_f32_16x16x32_bf16 v[76:79], v[212:215], v[202:205], v[76:79]\n\t" \
  "ds_read_b64_tr_b16 v[212:213], v249 offset:30880\n\t" \
  "v_fma_f32 v144, |v144|, v222, v144 mul:4\n\t" \
  "v_fma_f32 v153, |v153|, v227, v153 mul:4\n\t" \
  "v_mfma_f32_16x16x32_bf16 v[8:11], v[216:219], v[178:181], v[8:11]\n\t" \
  "ds_read_b64_tr_b16 v[214:215], v249 offset:32416\n\t" \
  "v_fma_f32 v152, |v152|, v226, v152 mul:4\n\t" \
  "v_cvt_pk_bf16_f32 v147, v146, v147\n\t" \
  "v_mfma_f32_16x16x32_bf16 v[32:35], v[216:219], v[186:189], v[32:35]\n\t" \
  "ds_read_b32 v235, v235 offset:0\n\t" \
  "v_cvt_pk_bf16_f32 v146, v144, v145\n\t" \
  "v_cvt_pk_bf16_f32 v155, v154, v155\n\t" \
  "v_mfma_f32_16x16x32_bf16 v[56:59], v[216:219], v[194:197], v[56:59]\n\t" \
  "ds_read_b32 v234, v234 offset:0\n\t" \
  "v_cvt_pk_bf16_f32 v154, v152, v153\n\t" \
  "s_setprio 1\n\t" \
  "v_cvt_pk_f16_f32 v220, |v148|, |v149| clamp\n\t" \
  "v_mfma_f32_16x16x32_bf16 v[80:83], v[216:219], v[202:205], v[80:83]\n\t" \
  "ds_read_b64_tr_b16 v[216:217], v249 offset:30888\n\t" \
  "v_cvt_pknorm_i16_f32 v222, |v150|, |v151|\n\t" \
  "v_cvt_pk_f16_f32 v224, |v156|, |v157| clamp\n\t" \
  "s_waitcnt lgkmcnt(8)\n\t" \
  "v_mfma_f32_16x16x32_bf16 v[12:15], v[208:211], v[178:181], v[12:15]\n\t" \
  "ds_read_b64_tr_b16 v[218:219], v249 offset:32424\n\t" \
  "v_cvt_pknorm_i16_f32 v226, |v158|, |v159|\n\t" \
  "v_and_b32_sdwa v221, s56, v220 dst_sel:DWORD dst_unused:UNUSED_PAD src0_sel:DWORD src1_sel:WORD_1\n\t" \
  "v_mfma_f32_16x16x32_bf16 v[36:39], v[208:211], v[186:189], v[36:39]\n\t" \
  "v_and_b32 v220, s56, v220\n\t" \
  "v_lshlrev_b32_sdwa v223, 2, v222 dst_sel:DWORD dst_unused:UNUSED_PAD src0_sel:DWORD src1_sel:BYTE_3\n\t" \
  "v_mfma_f32_16x16x32_bf16 v[60:63], v[208:211], v[194:197], v[60:63]\n\t" \
  "ds_read_b32 v221, v221 offset:0\n\t" \
  "v_lshlrev_b32_sdwa v222, 2, v222 dst_sel:DWORD dst_unused:UNUSED_PAD src0_sel:DWORD src1_sel:BYTE_1\n\t" \
  "v_and_b32_sdwa v225, s56, v224 dst_sel:DWORD dst_unused:UNUSED_PAD src0_sel:DWORD src1_sel:WORD_1\n\t" \
  "v_and_b32 v224, s56, v224\n\t" \
  "v_mfma_f32_16x16x32_bf16 v[84:87], v[208:211], v[202:205], v[84:87]\n\t" \
  "ds_read_b64_tr_b16 v[208:209], v248 offset:12304\n\t" \
  "ds_read_b64_tr_b16 v[210:211], v248 offset:13328\n\t" \
  "v_lshlrev_b32_sdwa v227, 2, v226 dst_sel:DWORD dst_unused:UNUSED_PAD src0_sel:DWORD src1_sel:BYTE_3\n\t" \
  "v_lshlrev_b32_sdwa v226, 2, v226 dst_sel:DWORD dst_unused:UNUSED_PAD src0_sel:DWORD src1_sel:BYTE_1\n\t" \
  "s_waitcnt lgkmcnt(7)\n\t" \
  "v_mfma_f32_16x16x32_bf16 v[16:19], v[212:215], v[178:181], v[16:19]\n\t" \
  "ds_read_b32 v220, v220 offset:0\n\t" \
  "ds_read_b32 v223, v223 offset:15376\n\t" \
  "v_fma_f32 v163, |v163|, v229, v163 mul:4\n\t" \
  "v_fma_f32 v162, |v162|, v228, v162 mul:4\n\t" \
  "v_mfma_f32_16x16x32_bf16 v[40:43], v[212:215], v[186:189], v[40:43]\n\t" \
  "ds_read_b32 v222, v222 offset:15376\n\t" \
  "v_fma_mix_f32 v231, |v161|, v231, v231 op_sel:[0,1,0] op_sel_hi:[0,1,1] clamp\n\t" \
  "v_fma_mix_f32 v230, |v160|, v230, v230 op_sel:[0,1,0] op_sel_hi:[0,1,1] clamp\n\t" \
  "v_mfma_f32_16x16x32_bf16 v[64:67], v[212:215], v[194:197], v[64:67]\n\t" \
  "ds_read_b32 v225, v225 offset:0\n\t" \
  "s_waitcnt lgkmcnt(9)\n\t" \
  "v_fma_f32 v171, |v171|, v233, v171 mul:4\n\t" \
  "v_fma_f32 v170, |v170|, v232, v170 mul:4\n\t" \
  "v_mfma_f32_16x16x32_bf16 v[88:91], v[212:215], v[202:205], v[88:91]\n\t" \
  "ds_read_b64_tr_b16 v[212:213], v248 offset:15376\n\t" \
  "v_fma_f32 v169, |v169|, v235, v169 mul:4\n\t" \
  "v_fma_f32 v168, |v168|, v234, v168 mul:4\n\t" \
  "s_waitcnt lgkmcnt(8)\n\t" \
  "v_mfma_f32_16x16x32_bf16 v[20:23], v[216:219], v[178:181], v[20:23]\n\t" \
  "ds_read_b64_tr_b16 v[214:215], v248 offset:16400\n\t" \
  "v_fma_f32 v161, |v161|, v231, v161 mul:4\n\t" \
  "v_fma_f32 v160, |v160|, v230, v160 mul:4\n\t" \
  "v_mfma_f32_16x16x32_bf16 v[44:47], v[216:219], v[186:189], v[44:47]\n\t" \
  "ds_read_b32 v224, v224 offset:0\n\t" \
  "v_cvt_pk_bf16_f32 v163, v162, v163\n\t" \
  "v_cvt_pk_bf16_f32 v162, v160, v161\n\t" \
  "v_mfma_f32_16x16x32_bf16 v[68:71], v[216:219], v[194:197], v[68:71]\n\t" \
  "ds_read_b32 v227, v227 offset:15376\n\t" \
  "v_cvt_pk_bf16_f32 v171, v170, v171\n\t" \
  "v_cvt_pk_bf16_f32 v170, v168, v169\n\t" \
  "v_cvt_pk_f16_f32 v228, |v164|, |v165| clamp\n\t" \
  "v_mfma_f32_16x16x32_bf16 v[92:95], v[216:219], v[202:205], v[92:95]\n\t" \
  "ds_read_b64_tr_b16 v[216:217], v248 offset:14352\n\t" \
  "v_cvt_pknorm_i16_f32 v230, |v166|, |v167|\n\t" \
  "s_setprio 2\n\t" \
  "v_cvt_pk_f16_f32 v232, |v172|, |v173| clamp\n\t" \
  "s_waitcnt lgkmcnt(9)\n\t" \
  "v_mfma_f32_16x16x32_bf16 v[176:179], v[208:211], v[96:99], 0\n\t" \
  "ds_read_b64_tr_b16 v[218:219], v248 offset:17424\n\t" \
  "v_cvt_pk_f16_f32 v234, |v174|, |v175| clamp\n\t" \
  "v_and_b32_sdwa v229, s56, v228 dst_sel:DWORD dst_unused:UNUSED_PAD src0_sel:DWORD src1_sel:WORD_1\n\t" \
  "v_mfma_f32_16x16x32_bf16 v[184:187], v[208:211], v[108:111], 0\n\t" \
  "ds_read_b32 v226, v226 offset:15376\n\t" \
  "v_and_b32 v228, s56, v228\n\t" \
  "v_lshlrev_b32_sdwa v231, 2, v230 dst_sel:DWORD dst_unused:UNUSED_PAD src0_sel:DWORD src1_sel:BYTE_3\n\t" \
  "v_mfma_f32_16x16x32_bf16 v[192:195], v[208:211], v[120:123], 0\n\t" \
  "ds_read_b32 v229, v229 offset:0\n\t" \
  "v_lshlrev_b32_sdwa v230, 2, v230 dst_sel:DWORD dst_unused:UNUSED_PAD src0_sel:DWORD src1_sel:BYTE_1\n\t" \
  "v_and_b32_sdwa v233, s56, v232 dst_sel:DWORD dst_unused:UNUSED_PAD src0_sel:DWORD src1_sel:WORD_1\n\t" \
  "v_mfma_f32_16x16x32_bf16 v[200:203], v[208:211], v[132:135], 0\n\t" \
  "ds_read_b64_tr_b16 v[208:209], v248 offset:12816\n\t" \
  "v_and_b32 v232, s56, v232\n\t" \
  "v_and_b32_sdwa v235, s56, v234 dst_sel:DWORD dst_unused:UNUSED_PAD src0_sel:DWORD src1_sel:WORD_1\n\t" \
  "s_waitcnt lgkmcnt(7)\n\t" \
  "v_mfma_f32_16x16x32_bf16 v[176:179], v[212:215], v[100:103], v[176:179]\n\t" \
  "ds_read_b64_tr_b16 v[210:211], v248 offset:13840\n\t" \
  "ds_read_b32 v228, v228 offset:0\n\t" \
  "v_and_b32 v234, s56, v234\n\t" \
  "v_fma_f32 v149, |v149|, v221, v149 mul:4\n\t" \
  "v_mfma_f32_16x16x32_bf16 v[184:187], v[212:215], v[112:115], v[184:187]\n\t" \
  "ds_read_b32 v231, v231 offset:15376\n\t" \
  "ds_read_b32 v230, v230 offset:15376\n\t" \
  "v_fma_f32 v148, |v148|, v220, v148 mul:4\n\t" \
  "v_fma_mix_f32 v223, |v151|, v223, v223 op_sel:[0,1,0] op_sel_hi:[0,1,1] clamp\n\t" \
  "v_mfma_f32_16x16x32_bf16 v[192:195], v[212:215], v[124:127], v[192:195]\n\t" \
  "ds_read_b32 v233, v233 offset:0\n\t" \
  "v_fma_mix_f32 v222, |v150|, v222, v222 op_sel:[0,1,0] op_sel_hi:[0,1,1] clamp\n\t" \
  "s_waitcnt lgkmcnt(7)\n\t" \
  "v_fma_f32 v157, |v157|, v225, v157 mul:4\n\t" \
  "v_mfma_f32_16x16x32_bf16 v[200:203], v[212:215], v[136:139], v[200:203]\n\t" \
  "ds_read_b64_tr_b16 v[212:213], v248 offset:15888\n\t" \
  "v_fma_f32 v156, |v156|, v224, v156 mul:4\n\t" \
  "v_fma_mix_f32 v227, |v159|, v227, v227 op_sel:[0,1,0] op_sel_hi:[0,1,1] clamp\n\t" \
  "v_fma_mix_f32 v226, |v158|, v226, v226 op_sel:[0,1,0] op_sel_hi:[0,1,1] clamp\n\t" \
  "v_mfma_f32_16x16x32_bf16 v[176:179], v[216:219], v[104:107], v[176:179]\n\t" \
  "ds_read_b64_tr_b16 v[214:215], v248 offset:16912\n\t" \
  "v_fma_f32 v151, |v151|, v223, v151 mul:4\n\t" \
  "v_fma_f32 v150, |v150|, v222, v150 mul:4\n\t" \
  "v_mfma_f32_16x16x32_bf16 v[184:187], v[216:219], v[116:119], v[184:187]\n\t" \
  "ds_read_b32 v232, v232 offset:0\n\t" \
  "v_fma_f32 v159, |v159|, v227, v159 mul:4\n\t" \
  "v_fma_f32 v158, |v158|, v226, v158 mul:4\n\t" \
  "v_mfma_f32_16x16x32_bf16 v[192:195], v[216:219], v[128:131], v[192:195]\n\t" \
  "ds_read_b32 v235, v235 offset:0\n\t" \
  "v_cvt_pk_bf16_f32 v148, v148, v149\n\t" \
  "v_cvt_pk_bf16_f32 v149, v150, v151\n\t" \
  "v_mfma_f32_16x16x32_bf16 v[200:203], v[216:219], v[140:143], v[200:203]\n\t" \
  "ds_read_b64_tr_b16 v[216:217], v248 offset:14864\n\t" \
  "v_cvt_pk_bf16_f32 v156, v156, v157\n\t" \
  "v_cvt_pk_bf16_f32 v157, v158, v159\n\t" \
  "s_waitcnt lgkmcnt(9)\n\t" \
  "v_mfma_f32_16x16x32_bf16 v[180:183], v[208:211], v[96:99], 0\n\t" \
  "ds_read_b64_tr_b16 v[218:219], v248 offset:17936\n\t" \
  "v_cvt_pk_f16_f32 v220, |v178|, |v179| clamp\n\t" \
  "s_setprio 3\n\t" \
  "v_cvt_pknorm_i16_f32 v222, |v176|, |v177|\n\t" \
  "v_mfma_f32_16x16x32_bf16 v[188:191], v[208:211], v[108:111], 0\n\t" \
  "s_waitcnt vmcnt(1)\n\t" \
  "s_cmp_lg_u32 s58, 1\n\t" \
  "s_cbranch_scc1 .Lcnx_c19\n\t" \
  "s_waitcnt vmcnt(0)\n\t" \
  ".Lcnx_c19:\n\t" \
  "s_mov_b64 exec, 3\n\t" \
  "ds_add_rtn_u32 v246, v251, v250 offset:47200\n\t" \
  "s_mov_b64 exec, -1\n\t" \
  "ds_read_b32 v234, v234 offset:0\n\t" \
  "v_cvt_pk_f16_f32 v224, |v186|, |v187| clamp\n\t" \
  "v_cvt_pknorm_i16_f32 v226, |v184|, |v185|\n\t" \
  "v_mfma_f32_16x16x32_bf16 v[196:199], v[208:211], v[120:123], 0\n\t" \
  "v_and_b32_sdwa v221, s56, v220 dst_sel:DWORD dst_unused:UNUSED_PAD src0_sel:DWORD src1_sel:WORD_1\n\t" \
  "v_and_b32 v220, s56, v220\n\t" \
  "v_mfma_f32_16x16x32_bf16 v[204:207], v[208:211], v[132:135], 0\n\t" \
  "s_waitcnt lgkmcnt(1)\n\t" \
  "v_readfirstlane_b32 s4, v246\n\t" \
  "s_cmp_lt_u32 s4, s57\n\t" \
  "s_cbranch_scc1 .Lcnx_w84\n.Lcnx_b84:\n\t" \
  "s_or_b32 s37, s37, s58\n\t" \
  "ds_read_b64_tr_b16 v[208:209], v249 offset:0\n\t" \
  "v_lshlrev_b32_sdwa v223, 2, v222 dst_sel:DWORD dst_unused:UNUSED_PAD src0_sel:DWORD src1_sel:BYTE_3\n\t" \
  "v_lshlrev_b32_sdwa v222, 2, v222 dst_sel:DWORD dst_unused:UNUSED_PAD src0_sel:DWORD src1_sel:BYTE_1\n\t" \
  "v_and_b32_sdwa v225, s56, v224 dst_sel:DWORD dst_unused:UNUSED_PAD src0_sel:DWORD src1_sel:WORD_1\n\t" \
  "v_mfma_f32_16x16x32_bf16 v[180:183], v[212:215], v[100:103], v[180:183]\n\t" \
  "ds_read_b64_tr_b16 v[210:211], v249 offset:1536\n\t" \
  "v_and_b32 v224, s56, v224\n\t" \
  "v_lshlrev_b32_sdwa v227, 2, v226 dst_sel:DWORD dst_unused:UNUSED_PAD src0_sel:DWORD src1_sel:BYTE_3\n\t" \
  "v_mfma_f32_16x16x32_bf16 v[188:191], v[212:215], v[112:115], v[188:191]\n\t" \
  "s_cmp_eq_u32 s37, 0\n\t" \
  "s_cbranch_scc1 .Lcnx_f8\n.Lcnx_g8:\n\t" \
  "ds_read_b32 v221, v221 offset:0\n\t" \
  "ds_read_b32 v220, v220 offset:0\n\t" \
  "v_lshlrev_b32_sdwa v226, 2, v226 dst_sel:DWORD dst_unused:UNUSED_PAD src0_sel:DWORD src1_sel:BYTE_1\n\t" \
  "v_fma_f32 v165, |v165|, v229, v165 mul:4\n\t" \
  "v_mfma_f32_16x16x32_bf16 v[196:199], v[212:215], v[124:127], v[196:199]\n\t" \
  "ds_read_b32 v223, v223 offset:15376\n\t" \
  "ds_read_b32 v222, v222 offset:15376\n\t" \
  "v_fma_f32 v164, |v164|, v228, v164 mul:4\n\t" \
  "v_fma_mix_f32 v231, |v167|, v231, v231 op_sel:[0,1,0] op_sel_hi:[0,1,1] clamp\n\t" \
  "v_mfma_f32_16x16x32_bf16 v[204:207], v[212:215], v[136:139], v[204:207]\n\t" \
  "ds_read_b64_tr_b16 v[212:213], v249 offset:8\n\t" \
  "ds_read_b64_tr_b16 v[214:215], v249 offset:1544\n\t" \
  "v_fma_mix_f32 v230, |v166|, v230, v230 op_sel:[0,1,0] op_sel_hi:[0,1,1] clamp\n\t" \
  "s_waitcnt lgkmcnt(8)\n\t" \
  "v_fma_f32 v173, |v173|, v233, v173 mul:4\n\t" \
  "v_mfma_f32_16x16x32_bf16 v[180:183], v[216:219], v[104:107], v[180:183]\n\t" \
  "ds_read_b32 v225, v225 offset:0\n\t" \
  "v_fma_f32 v172, |v172|, v232, v172 mul:4\n\t" \
  "v_fma_f32 v175, |v175|, v235, v175 mul:4\n\t" \
  "v_mfma_f32_16x16x32_bf16 v[188:191], v[216:219], v[116:119], v[188:191]\n\t" \
  "ds_read_b32 v224, v224 offset:0\n\t" \
  "v_fma_f32 v174, |v174|, v234, v174 mul:4\n\t" \
  "v_fma_f32 v167, |v167|, v231, v167 mul:4\n\t" \
  "v_mfma_f32_16x16x32_bf16 v[196:199], v[216:219], v[128:131], v[196:199]\n\t" \
  "ds_read_b32 v227, v227 offset:15376\n\t" \
  "v_fma_f32 v166, |v166|, v230, v166 mul:4\n\t" \
  "v_cvt_pk_bf16_f32 v164, v164, v165\n\t" \
  "v_mfma_f32_16x16x32_bf16 v[204:207], v[216:219], v[140:143], v[204:207]\n\t" \
  "ds_read_b64_tr_b16 v[216:217], v249 offset:64\n\t" \
  "v_cvt_pk_bf16_f32 v165, v166, v167\n\t" \
  "v_cvt_pk_bf16_f32 v172, v172, v173\n\t" \
  "v_cvt_pk_bf16_f32 v173, v174, v175\n\t" \
  "s_setprio 0\n\t" \
  "v_cvt_pk_f16_f32 v228, |v194|, |v195| clamp\n\t" \
  "v_cvt_pknorm_i16_f32 v230, |v192|, |v193|\n\t" \
  "v_mfma_f32_16x16x32_bf16 v[48:51], v[242:245], v[236:239], v[48:51]\n\t" \
  "ds_read_b64_tr_b16 v[218:219], v249 offset:1600\n\t" \
  "v_cvt_pk_f16_f32 v232, |v202|, |v203| clamp\n\t" \
  "v_cvt_pk_f16_f32 v234, |v200|, |v201| clamp\n\t" \
  "v_mfma_f32_16x16x32_bf16 v[52:55], v[240:243], v[236:239], v[52:55]\n\t" \
  "buffer_load_dwordx4 v[236:239], v250, s[8:11], s29 offen offset:64\n\t" \
  "s_cmp_lg_u32 s58, 0\n\t" \
  "s_cbranch_scc1 .Lcnx_c32\n\t" \
  "s_add_u32 m0, s25, 49200\n\t" \
  "s_add_u32 s24, s24, s26\n\t" \
  "buffer_load_dwordx4 v254, s[8:11], s24 offen lds\n\t" \
  "s_cmp_lg_u32 s38, 4\n\t" \
  "s_cbranch_scc1 .Lcnx_c32\n\t" \
  "s_mov_b32 m0, 163632\n\t" \
  "s_add_u32 s30, s30, s31\n\t" \
  "buffer_load_dword v252, s[8:11], s30 offen lds\n\t" \
  ".Lcnx_c32:\n\t" \
  "ds_read_b32 v226, v226 offset:15376\n\t" \
  "v_and_b32_sdwa v229, s56, v228 dst_sel:DWORD dst_unused:UNUSED_PAD src0_sel:DWORD src1_sel:WORD_1\n\t" \
  "v_and_b32 v228, s56, v228\n\t" \
  "s_waitcnt lgkmcnt(12)\n\t" \
  "v_mfma_f32_16x16x32_bf16 v[0:3], v[208:211], v[146:149], v[0:3]\n\t" \
  "ds_read_b32 v229, v229 offset:0\n\t" \
  "v_lshlrev_b32_sdwa v231, 2, v230 dst_sel:DWORD dst_unused:UNUSED_PAD src0_sel:DWORD src1_sel:BYTE_3\n\t" \
  "v_lshlrev_b32_sdwa v230, 2, v230 dst_sel:DWORD dst_unused:UNUSED_PAD src0_sel:DWORD src1_sel:BYTE_1\n\t" \
  "v_mfma_f32_16x16x32_bf16 v[24:27], v[208:211], v[154:157], v[24:27]\n\t" \
  "ds_read_b32 v228, v228 offset:0\n\t" \
  "v_and_b32_sdwa v233, s56, v232 dst_sel:DWORD dst_unused:UNUSED_PAD src0_sel:DWORD src1_sel:WORD_1\n\t" \
  "v_and_b32 v232, s56, v232\n\t" \
  "v_mfma_f32_16x16x32_bf16 v[48:51], v[208:211], v[162:165], v[48:51]\n\t" \
  "ds_read_b32 v231, v231 offset:15376\n\t" \
  "v_and_b32_sdwa v235, s56, v234 dst_sel:DWORD dst_unused:UNUSED_PAD src0_sel:DWORD src1_sel:WORD_1\n\t" \
  "v_and_b32 v234, s56, v234\n\t" \
  "v_mfma_f32_16x16x32_bf16 v[72:75], v[208:211], v[170:173], v[72:75]\n\t" \
  "ds_read_b64_tr_b16 v[208:209], v249 offset:72\n\t" \
  "ds_read_b64_tr_b16 v[210:211], v249 offset:1608\n\t" \
  "s_waitcnt lgkmcnt(13)\n\t" \
  "v_fma_f32 v179, |v179|, v221, v179 mul:4\n\t" \
  "v_fma_f32 v178, |v178|, v220, v178 mul:4\n\t" \
  "s_waitcnt lgkmcnt(11)\n\t" \
  "v_mfma_f32_16x16x32_bf16 v[4:7], v[212:215], v[146:149], v[4:7]\n\t" \
  "ds_read_b32 v230, v230 offset:15376\n\t" \
  "v_fma_mix_f32 v223, |v177|, v223, v223 op_sel:[0,1,0] op_sel_hi:[0,1,1] clamp\n\t" \
  "v_fma_mix_f32 v222, |v176|, v222, v222 op_sel:[0,1,0] op_sel_hi:[0,1,1] clamp\n\t" \
  "v_mfma_f32_16x16x32_bf16 v[28:31], v[212:215], v[154:157], v[28:31]\n\t" \
  "ds_read_b32 v233, v233 offset:0\n\t" \
  "s_waitcnt lgkmcnt(7)\n\t" \
  "v_fma_f32 v187, |v187|, v225, v187 mul:4\n\t" \
  "v_fma_f32 v186, |v186|, v224, v186 mul:4\n\t" \
  "v_mfma_f32_16x16x32_bf16 v[52:55], v[212:215], v[162:165], v[52:55]\n\t" \
  "ds_read_b32 v232, v232 offset:0\n\t" \
  "v_fma_mix_f32 v227, |v185|, v227, v227 op_sel:[0,1,0] op_sel_hi:[0,1,1] clamp\n\t" \
  "v_fma_mix_f32 v226, |v184|, v226, v226 op_sel:[0,1,0] op_sel_hi:[0,1,1] clamp\n\t" \
  "v_fma_f32 v177, |v177|, v223, v177 mul:4\n\t" \
  "v_mfma_f32_16x16x32_bf16 v[76:79], v[212:215], v[170:173], v[76:79]\n\t" \
  "ds_read_b64_tr_b16 v[212:213], v249 offset:128\n\t" \
  "v_fma_f32 v176, |v176|, v222, v176 mul:4\n\t" \
  "v_fma_f32 v185, |v185|, v227, v185 mul:4\n\t" \
  "v_mfma_f32_16x16x32_bf16 v[8:11], v[216:219], v[146:149], v[8:11]\n\t" \
  "ds_read_b64_tr_b16 v[214:215], v249 offset:1664\n\t" \
  "v_fma_f32 v184, |v184|, v226, v184 mul:4\n\t" \
  "v_cvt_pk_bf16_f32 v179, v178, v179\n\t" \
  "v_mfma_f32_16x16x32_bf16 v[32:35], v[216:219], v[154:157], v[32:35]\n\t" \
  "ds_read_b32 v235, v235 offset:0\n\t" \
  "v_cvt_pk_bf16_f32 v178, v176, v177\n\t" \
  "v_cvt_pk_bf16_f32 v187, v186, v187\n\t" \
  "v_mfma_f32_16x16x32_bf16 v[56:59], v[216:219], v[162:165], v[56:59]\n\t" \
  "ds_read_b32 v234, v234 offset:0\n\t" \
  "v_cvt_pk_bf16_f32 v186, v184, v185\n\t" \
  "s_setprio 1\n\t" \
  "v_cvt_pk_f16_f32 v220, |v180|, |v181| clamp\n\t" \
  "v_mfma_f32_16x16x32_bf16 v[80:83], v[216:219], v[170:173], v[80:83]\n\t" \
  "ds_read_b64_tr_b16 v[216:217], v249 offset:136\n\t" \
  "v_cvt_pknorm_i16_f32 v222, |v182|, |v183|\n\t" \
  "v_cvt_pk_f16_f32 v224, |v188|, |v189| clamp\n\t" \
  "s_waitcnt lgkmcnt(8)\n\t" \
  "v_mfma_f32_16x16x32_bf16 v[12:15], v[208:211], v[146:149], v[12:15]\n\t" \
  "ds_read_b64_tr_b16 v[218:219], v249 offset:1672\n\t" \
  "v_cvt_pknorm_i16_f32 v226, |v190|, |v191|\n\t" \
  "v_and_b32_sdwa v221, s56, v220 dst_sel:DWORD dst_unused:UNUSED_PAD src0_sel:DWORD src1_sel:WORD_1\n\t" \
  "v_mfma_f32_16x16x32_bf16 v[36:39], v[208:211], v[154:157], v[36:39]\n\t" \
  "v_and_b32 v220, s56, v220\n\t" \
  "v_lshlrev_b32_sdwa v223, 2, v222 dst_sel:DWORD dst_unused:UNUSED_PAD src0_sel:DWORD src1_sel:BYTE_3\n\t" \
  "v_mfma_f32_16x16x32_bf16 v[60:63], v[208:211], v[162:165], v[60:63]\n\t" \
  "ds_read_b32 v221, v221 offset:0\n\t" \
  "v_lshlrev_b32_sdwa v222, 2, v222 dst_sel:DWORD dst_unused:UNUSED_PAD src0_sel:DWORD src1_sel:BYTE_1\n\t" \
  "v_and_b32_sdwa v225, s56, v224 dst_sel:DWORD dst_unused:UNUSED_PAD src0_sel:DWORD src1_sel:WORD_1\n\t" \
  "v_and_b32 v224, s56, v224\n\t" \
  "v_mfma_f32_16x16x32_bf16 v[84:87], v[208:211], v[170:173], v[84:87]\n\t" \
  "ds_read_b64_tr_b16 v[208:209], v248 offset:43048\n\t" \
  "ds_read_b64_tr_b16 v[210:211], v248 offset:44072\n\t" \
  "v_lshlrev_b32_sdwa v227, 2, v226 dst_sel:DWORD dst_unused:UNUSED_PAD src0_sel:DWORD src1_sel:BYTE_3\n\t" \
  "v_lshlrev_b32_sdwa v226, 2, v226 dst_sel:DWORD dst_unused:UNUSED_PAD src0_sel:DWORD src1_sel:BYTE_1\n\t" \
  "s_waitcnt lgkmcnt(7)\n\t" \
  "v_mfma_f32_16x16x32_bf16 v[16:19], v[212:215], v[146:149], v[16:19]\n\t" \
  "ds_read_b32 v220, v220 offset:0\n\t" \
  "ds_read_b32 v223, v223 offset:15376\n\t" \
  "v_fma_f32 v195, |v195|, v229, v195 mul:4\n\t" \
  "v_fma_f32 v194, |v194|, v228, v194 mul:4\n\t" \
  "v_mfma_f32_16x16x32_bf16 v[40:43], v[212:215], v[154:157], v[40:43]\n\t" \
  "ds_read_b32 v222, v222 offset:15376\n\t" \
  "v_fma_mix_f32 v231, |v193|, v231, v231 op_sel:[0,1,0] op_sel_hi:[0,1,1] clamp\n\t" \
  "v_fma_mix_f32 v230, |v192|, v230, v230 op_sel:[0,1,0] op_sel_hi:[0,1,1] clamp\n\t" \
  "v_mfma_f32_16x16x32_bf16 v[64:67], v[212:215], v[162:165], v[64:67]\n\t" \
  "ds_read_b32 v225, v225 offset:0\n\t" \
  "s_waitcnt lgkmcnt(9)\n\t" \
  "v_fma_f32 v203, |v203|, v233, v203 mul:4\n\t" \
  "v_fma_f32 v202, |v202|, v232, v202 mul:4\n\t" \
  "v_mfma_f32_16x16x32_bf16 v[88:91], v[212:215], v[170:173], v[88:91]\n\t" \
  "ds_read_b64_tr_b16 v[212:213], v248 offset:46120\n\t" \
  "v_fma_f32 v201, |v201|, v235, v201 mul:4\n\t" \
  "v_fma_f32 v200, |v200|, v234, v200 mul:4\n\t" \
  "s_waitcnt lgkmcnt(8)\n\t" \
  "v_mfma_f32_16x16x32_bf16 v[20:23], v[216:219], v[146:149], v[20:23]\n\t" \
  "ds_read_b64_tr_b16 v[214:215], v248 offset:47144\n\t" \
  "v_fma_f32 v193, |v193|, v231, v193 mul:4\n\t" \
  "v_fma_f32 v192, |v192|, v230, v192 mul:4\n\t" \
  "v_mfma_f32_16x16x32_bf16 v[44:47], v[216:219], v[154:157], v[44:47]\n\t" \
  "ds_read_b32 v224, v224 offset:0\n\t" \
  "v_cvt_pk_bf16_f32 v195, v194, v195\n\t" \
  "v_cvt_pk_bf16_f32 v194, v192, v193\n\t" \
  "v_mfma_f32_16x16x32_bf16 v[68:71], v[216:219], v[162:165], v[68:71]\n\t" \
  "ds_read_b32 v227, v227 offset:15376\n\t" \
  "v_cvt_pk_bf16_f32 v203, v202, v203\n\t" \
  "v_cvt_pk_bf16_f32 v202, v200, v201\n\t" \
  "v_cvt_pk_f16_f32 v228, |v196|, |v197| clamp\n\t" \
  "v_mfma_f32_16x16x32_bf16 v[92:95], v[216:219], v[170:173], v[92:95]\n\t" \
  "ds_read_b64_tr_b16 v[216:217], v248 offset:45096\n\t" \
  "v_cvt_pknorm_i16_f32 v230, |v198|, |v199|\n\t" \
  "s_setprio 2\n\t" \
  "v_cvt_pk_f16_f32 v232, |v204|, |v205| clamp\n\t" \
  "s_waitcnt lgkmcnt(9)\n\t" \
  "v_mfma_f32_16x16x32_bf16 v[144:147], v[208:211], v[96:99], 0\n\t" \
  "ds_read_b64_tr_b16 v[218:219], v248 offset:48168\n\t" \
  "v_cvt_pk_f16_f32 v234, |v206|, |v207| clamp\n\t" \
  "v_and_b32_sdwa v229, s56, v228 dst_sel:DWORD dst_unused:UNUSED_PAD src0_sel:DWORD src1_sel:WORD_1\n\t" \
  "v_mfma_f32_16x16x32_bf16 v[152:155], v[208:211], v[108:111], 0\n\t" \
  "ds_read_b32 v226, v226 offset:15376\n\t" \
  "v_and_b32 v228, s56, v228\n\t" \
  "v_lshlrev_b32_sdwa v231, 2, v230 dst_sel:DWORD dst_unused:UNUSED_PAD src0_sel:DWORD src1_sel:BYTE_3\n\t" \
  "v_mfma_f32_16x16x32_bf16 v[160:163], v[208:211], v[120:123], 0\n\t" \
  "ds_read_b32 v229, v229 offset:0\n\t" \
  "v_lshlrev_b32_sdwa v230, 2, v230 dst_sel:DWORD dst_unused:UNUSED_PAD src0_sel:DWORD src1_sel:BYTE_1\n\t" \
  "v_and_b32_sdwa v233, s56, v232 dst_sel:DWORD dst_unused:UNUSED_PAD src0_sel:DWORD src1_sel:WORD_1\n\t" \
  "v_mfma_f32_16x16x32_bf16 v[168:171], v[208:211], v[132:135], 0\n\t" \
  "ds_read_b64_tr_b16 v[208:209], v248 offset:43560\n\t" \
  "v_and_b32 v232, s56, v232\n\t" \
  "v_and_b32_sdwa v235, s56, v234 dst_sel:DWORD dst_unused:UNUSED_PAD src0_sel:DWORD src1_sel:WORD_1\n\t" \
  "s_waitcnt lgkmcnt(7)\n\t" \
  "v_mfma_f32_16x16x32_bf16 v[144:147], v[212:215], v[100:103], v[144:147]\n\t" \
  "ds_read_b64_tr_b16 v[210:211], v248 offset:44584\n\t" \
  "ds_read_b32 v228, v228 offset:0\n\t" \
  "v_and_b32 v234, s56, v234\n\t" \
  "v_fma_f32 v181, |v181|, v221, v181 mul:4\n\t" \
  "v_mfma_f32_16x16x32_bf16 v[152:155], v[212:215], v[112:115], v[152:155]\n\t" \
  "ds_read_b32 v231, v231 offset:15376\n\t" \
  "ds_read_b32 v230, v230 offset:15376\n\t" \
  "v_fma_f32 v180, |v180|, v220, v180 mul:4\n\t" \
  "v_fma_mix_f32 v223, |v183|, v223, v223 op_sel:[0,1,0] op_sel_hi:[0,1,1] clamp\n\t" \
  "v_mfma_f32_16x16x32_bf16 v[160:163], v[212:215], v[124:127], v[160:163]\n\t" \
  "ds_read_b32 v233, v233 offset:0\n\t" \
  "v_fma_mix_f32 v222, |v182|, v222, v222 op_sel:[0,1,0] op_sel_hi:[0,1,1] clamp\n\t" \
  "s_waitcnt lgkmcnt(7)\n\t" \
  "v_fma_f32 v189, |v189|, v225, v189 mul:4\n\t" \
  "v_mfma_f32_16x16x32_bf16 v[168:171], v[212:215], v[136:139], v[168:171]\n\t" \
  "ds_read_b64_tr_b16 v[212:213], v248 offset:46632\n\t" \
  "v_fma_f32 v188, |v188|, v224, v188 mul:4\n\t" \
  "v_fma_mix_f32 v227, |v191|, v227, v227 op_sel:[0,1,0] op_sel_hi:[0,1,1] clamp\n\t" \
  "v_fma_mix_f32 v226, |v190|, v226, v226 op_sel:[0,1,0] op_sel_hi:[0,1,1] clamp\n\t" \
  "v_mfma_f32_16x16x32_bf16 v[144:147], v[216:219], v[104:107], v[144:147]\n\t" \
  "ds_read_b64_tr_b16 v[214:215], v248 offset:47656\n\t" \
  "v_fma_f32 v183, |v183|, v223, v183 mul:4\n\t" \
  "v_fma_f32 v182, |v182|, v222, v182 mul:4\n\t" \
  "v_mfma_f32_16x16x32_bf16 v[152:155], v[216:219], v[116:119], v[152:155]\n\t" \
  "ds_read_b32 v232, v232 offset:0\n\t" \
  "v_fma_f32 v191, |v191|, v227, v191 mul:4\n\t" \
  "v_fma_f32 v190, |v190|, v226, v190 mul:4\n\t" \
  "v_mfma_f32_16x16x32_bf16 v[160:163], v[216:219], v[128:131], v[160:163]\n\t" \
  "ds_read_b32 v235, v235 offset:0\n\t" \
  "v_cvt_pk_bf16_f32 v180, v180, v181\n\t" \
  "v_cvt_pk_bf16_f32 v181, v182, v183\n\t" \
  "v_mfma_f32_16x16x32_bf16 v[168:171], v[216:219], v[140:143], v[168:171]\n\t" \
  "ds_read_b64_tr_b16 v[216:217], v248 offset:45608\n\t" \
  "v_cvt_pk_bf16_f32 v188, v188, v189\n\t" \
  "v_cvt_pk_bf16_f32 v189, v190, v191\n\t" \
  "s_waitcnt lgkmcnt(9)\n\t" \
  "v_mfma_f32_16x16x32_bf16 v[148:151], v[208:211], v[96:99], 0\n\t" \
  "ds_read_b64_tr_b16 v[218:219], v248 offset:48680\n\t" \
  "v_cvt_pk_f16_f32 v220, |v146|, |v147| clamp\n\t" \
  "s_setprio 3\n\t" \
  "v_cvt_pknorm_i16_f32 v222, |v144|, |v145|\n\t" \
  "v_mfma_f32_16x16x32_bf16 v[156:159], v[208:211], v[108:111], 0\n\t" \
  "s_waitcnt vmcnt(1)\n\t" \
  "s_cmp_lg_u32 s58, 1\n\t" \
  "s_cbranch_scc1 .Lcnx_c20\n\t" \
  "s_waitcnt vmcnt(0)\n\t" \
  ".Lcnx_c20:\n\t" \
  "s_mov_b64 exec, 3\n\t" \
  "ds_add_rtn_u32 v246, v251, v250 offset:47208\n\t" \
  "s_mov_b64 exec, -1\n\t" \
  "ds_read_b32 v234, v234 offset:0\n\t" \
  "v_cvt_pk_f16_f32 v224, |v154|, |v155| clamp\n\t" \
  "v_cvt_pknorm_i16_f32 v226, |v152|, |v153|\n\t" \
  "v_mfma_f32_16x16x32_bf16 v[164:167], v[208:211], v[120:123], 0\n\t" \
  "v_and_b32_sdwa v221, s56, v220 dst_sel:DWORD dst_unused:UNUSED_PAD src0_sel:DWORD src1_sel:WORD_1\n\t" \
  "v_and_b32 v220, s56, v220\n\t" \
  "v_mfma_f32_16x16x32_bf16 v[172:175], v[208:211], v[132:135], 0\n\t" \
  "s_waitcnt lgkmcnt(1)\n\t" \
  "v_readfirstlane_b32 s4, v246\n\t" \
  "s_cmp_lt_u32 s4, s57\n\t" \
  "s_cbranch_scc1 .Lcnx_w96\n.Lcnx_b96:\n\t" \
  "s_cmp_lg_u32 s58, 1\n\t" \
  "s_cselect_b32 s57, s57, 0\n\t" \
  "ds_read_b64_tr_b16 v[208:209], v249 offset:6152\n\t" \
  "v_lshlrev_b32_sdwa v223, 2, v222 dst_sel:DWORD dst_unused:UNUSED_PAD src0_sel:DWORD src1_sel:BYTE_3\n\t" \
  "v_lshlrev_b32_sdwa v222, 2, v222 dst_sel:DWORD dst_unused:UNUSED_PAD src0_sel:DWORD src1_sel:BYTE_1\n\t" \
  "v_and_b32_sdwa v225, s56, v224 dst_sel:DWORD dst_unused:UNUSED_PAD src0_sel:DWORD src1_sel:WORD_1\n\t" \
  "v_mfma_f32_16x16x32_bf16 v[148:151], v[212:215], v[100:103], v[148:151]\n\t" \
  "ds_read_b64_tr_b16 v[210:211], v249 offset:7688\n\t" \
  "v_and_b32 v224, s56, v224\n\t" \
  "v_lshlrev_b32_sdwa v227, 2, v226 dst_sel:DWORD dst_unused:UNUSED_PAD src0_sel:DWORD src1_sel:BYTE_3\n\t" \
  "v_mfma_f32_16x16x32_bf16 v[156:159], v[212:215], v[112:115], v[156:159]\n\t" \
  "s_cmp_eq_u32 s37, 0\n\t" \
  "s_cbranch_scc1 .Lcnx_f9\n.Lcnx_g9:\n\t" \
  "ds_read_b32 v221, v221 offset:0\n\t" \
  "ds_read_b32 v220, v220 offset:0\n\t" \
  "v_lshlrev_b32_sdwa v226, 2, v226 dst_sel:DWORD dst_unused:UNUSED_PAD src0_sel:DWORD src1_sel:BYTE_1\n\t" \
  "v_fma_f32 v197, |v197|, v229, v197 mul:4\n\t" \
  "v_mfma_f32_16x16x32_bf16 v[164:167], v[212:215], v[124:127], v[164:167]\n\t" \
  "ds_read_b32 v223, v223 offset:15376\n\t" \
  "ds_read_b32 v222, v222 offset:15376\n\t" \
  "v_fma_f32 v196, |v196|, v228, v196 mul:4\n\t" \
  "v_fma_mix_f32 v231, |v199|, v231, v231 op_sel:[0,1,0] op_sel_hi:[0,1,1] clamp\n\t" \
  "v_mfma_f32_16x16x32_bf16 v[172:175], v[212:215], v[136:139], v[172:175]\n\t" \
  "ds_read_b64_tr_b16 v[212:213], v249 offset:6160\n\t" \
  "ds_read_b64_tr_b16 v[214:215], v249 offset:7696\n\t" \
  "v_fma_mix_f32 v230, |v198|, v230, v230 op_sel:[0,1,0] op_sel_hi:[0,1,1] clamp\n\t" \
  "s_waitcnt lgkmcnt(8)\n\t" \
  "v_fma_f32 v205, |v205|, v233, v205 mul:4\n\t" \
  "v_mfma_f32_16x16x32_bf16 v[148:151], v[216:219], v[104:107], v[148:151]\n\t" \
  "ds_read_b32 v225, v225 offset:0\n\t" \
  "v_fma_f32 v204, |v204|, v232, v204 mul:4\n\t" \
  "v_fma_f32 v207, |v207|, v235, v207 mul:4\n\t" \
  "v_mfma_f32_16x16x32_bf16 v[156:159], v[216:219], v[116:119], v[156:159]\n\t" \
  "ds_read_b32 v224, v224 offset:0\n\t" \
  "v_fma_f32 v206, |v206|, v234, v206 mul:4\n\t" \
  "v_fma_f32 v199, |v199|, v231, v199 mul:4\n\t" \
  "v_mfma_f32_16x16x32_bf16 v[164:167], v[216:219], v[128:131], v[164:167]\n\t" \
  "ds_read_b32 v227, v227 offset:15376\n\t" \
  "v_fma_f32 v198, |v198|, v230, v198 mul:4\n\t" \
  "v_cvt_pk_bf16_f32 v196, v196, v197\n\t" \
  "v_mfma_f32_16x16x32_bf16 v[172:175], v[216:219], v[140:143], v[172:175]\n\t" \
  "ds_read_b64_tr_b16 v[216:217], v249 offset:6216\n\t" \
  "v_cvt_pk_bf16_f32 v197, v198, v199\n\t" \
  "v_cvt_pk_bf16_f32 v204, v204, v205\n\t" \
  "v_cvt_pk_bf16_f32 v205, v206, v207\n\t" \
  "s_cmp_lg_u32 s58, 1\n\t" \
  "s_cbranch_scc1 .Lcnx_c21\n\t" \
  "s_mov_b64 vcc, -1\n\t" \
  "s_setprio 0\n\t" \
  "s_cmp_ge_u32 s41, 2048\n\t" \
  "s_cbranch_scc1 .Lcnx_c21\n\t" \
  "s_setprio 3\n\t" \
  ".Lcnx_c21:\n\t" \
  "s_cbranch_vccnz .Lcnx_p9_0\n\ts_setprio 0\n.Lcnx_p9_0:\n\t" \
  "v_cvt_pk_f16_f32 v228, |v162|, |v163| clamp\n\t" \
  "v_cvt_pknorm_i16_f32 v230, |v160|, |v161|\n\t" \
  "v_mfma_f32_16x16x32_bf16 v[56:59], v[242:245], v[236:239], v[56:59]\n\t" \
  "ds_read_b64_tr_b16 v[218:219], v249 offset:7752\n\t" \
  "v_cvt_pk_f16_f32 v232, |v170|, |v171| clamp\n\t" \
  "v_cvt_pk_f16_f32 v234, |v168|, |v169| clamp\n\t" \
  "v_mfma_f32_16x16x32_bf16 v[60:63], v[240:243], v[236:239], v[60:63]\n\t" \
  "buffer_load_dwordx4 v[236:239], v250, s[8:11], s29 offen offset:128\n\t" \
  "s_cmp_lg_u32 s58, 0\n\t" \
  "s_cbranch_scc1 .Lcnx_c33\n\t" \
  "s_add_u32 m0, s25, 57400\n\t" \
  "s_add_u32 s24, s24, s26\n\t" \
  "buffer_load_dwordx4 v254, s[8:11], s24 offen lds\n\t" \
  "s_cmp_lg_u32 s38, 5\n\t" \
  "s_cbranch_scc1 .Lcnx_c33\n\t" \
  "s_mov_b32 m0, 163632\n\t" \
  "s_add_u32 s30, s30, s31\n\t" \
  "buffer_load_dword v252, s[8:11], s30 offen lds\n\t" \
  ".Lcnx_c33:\n\t" \
  "ds_read_b32 v226, v226 offset:15376\n\t" \
  "v_and_b32_sdwa v229, s56, v228 dst_sel:DWORD dst_unused:UNUSED_PAD src0_sel:DWORD src1_sel:WORD_1\n\t" \
  "v_and_b32 v228, s56, v228\n\t" \
  "s_waitcnt lgkmcnt(12)\n\t" \
  "v_mfma_f32_16x16x32_bf16 v[0:3], v[208:211], v[178:181], v[0:3]\n\t" \
  "ds_read_b32 v229, v229 offset:0\n\t" \
  "v_lshlrev_b32_sdwa v231, 2, v230 dst_sel:DWORD dst_unused:UNUSED_PAD src0_sel:DWORD src1_sel:BYTE_3\n\t" \
  "v_lshlrev_b32_sdwa v230, 2, v230 dst_sel:DWORD dst_unused:UNUSED_PAD src0_sel:DWORD src1_sel:BYTE_1\n\t" \
  "v_mfma_f32_16x16x32_bf16 v[24:27], v[208:211], v[186:189], v[24:27]\n\t" \
  "ds_read_b32 v228, v228 offset:0\n\t" \
  "v_and_b32_sdwa v233, s56, v232 dst_sel:DWORD dst_unused:UNUSED_PAD src0_sel:DWORD src1_sel:WORD_1\n\t" \
  "v_and_b32 v232, s56, v232\n\t" \
  "v_mfma_f32_16x16x32_bf16 v[48:51], v[208:211], v[194:197], v[48:51]\n\t" \
  "ds_read_b32 v231, v231 offset:15376\n\t" \
  "v_and_b32_sdwa v235, s56, v234 dst_sel:DWORD dst_unused:UNUSED_PAD src0_sel:DWORD src1_sel:WORD_1\n\t" \
  "v_and_b32 v234, s56, v234\n\t" \
  "v_mfma_f32_16x16x32_bf16 v[72:75], v[208:211], v[202:205], v[72:75]\n\t" \
  "ds_read_b64_tr_b16 v[208:209], v249 offset:6224\n\t" \
  "ds_read_b64_tr_b16 v[210:211], v249 offset:7760\n\t" \
  "s_waitcnt lgkmcnt(13)\n\t" \
  "v_fma_f32 v147, |v147|, v221, v147 mul:4\n\t" \
  "v_fma_f32 v146, |v146|, v220, v146 mul:4\n\t" \
  "s_waitcnt lgkmcnt(11)\n\t" \
  "v_mfma_f32_16x16x32_bf16 v[4:7], v[212:215], v[178:181], v[4:7]\n\t" \
  "ds_read_b32 v230, v230 offset:15376\n\t" \
  "v_fma_mix_f32 v223, |v145|, v223, v223 op_sel:[0,1,0] op_sel_hi:[0,1,1] clamp\n\t" \
  "v_fma_mix_f32 v222, |v144|, v222, v222 op_sel:[0,1,0] op_sel_hi:[0,1,1] clamp\n\t" \
  "v_mfma_f32_16x16x32_bf16 v[28:31], v[212:215], v[186:189], v[28:31]\n\t" \
  "ds_read_b32 v233, v233 offset:0\n\t" \
  "s_waitcnt lgkmcnt(7)\n\t" \
  "v_fma_f32 v155, |v155|, v225, v155 mul:4\n\t" \
  "v_fma_f32 v154, |v154|, v224, v154 mul:4\n\t" \
  "v_mfma_f32_16x16x32_bf16 v[52:55], v[212:215], v[194:197], v[52:55]\n\t" \
  "ds_read_b32 v232, v232 offset:0\n\t" \
  "v_fma_mix_f32 v227, |v153|, v227, v227 op_sel:[0,1,0] op_sel_hi:[0,1,1] clamp\n\t" \
  "v_fma_mix_f32 v226, |v152|, v226, v226 op_sel:[0,1,0] op_sel_hi:[0,1,1] clamp\n\t" \
  "v_fma_f32 v145, |v145|, v223, v145 mul:4\n\t" \
  "v_mfma_f32_16x16x32_bf16 v[76:79], v[212:215], v[202:205], v[76:79]\n\t" \
  "ds_read_b64_tr_b16 v[212:213], v249 offset:6280\n\t" \
  "v_fma_f32 v144, |v144|, v222, v144 mul:4\n\t" \
  "v_fma_f32 v153, |v153|, v227, v153 mul:4\n\t" \
  "v_mfma_f32_16x16x32_bf16 v[8:11], v[216:219], v[178:181], v[8:11]\n\t" \
  "ds_read_b64_tr_b16 v[214:215], v249 offset:7816\n\t" \
  "v_fma_f32 v152, |v152|, v226, v152 mul:4\n\t" \
  "v_cvt_pk_bf16_f32 v147, v146, v147\n\t" \
  "v_mfma_f32_16x16x32_bf16 v[32:35], v[216:219], v[186:189], v[32:35]\n\t" \
  "ds_read_b32 v235, v235 offset:0\n\t" \
  "v_cvt_pk_bf16_f32 v146, v144, v145\n\t" \
  "v_cvt_pk_bf16_f32 v155, v154, v155\n\t" \
  "v_mfma_f32_16x16x32_bf16 v[56:59], v[216:219], v[194:197], v[56:59]\n\t" \
  "ds_read_b32 v234, v234 offset:0\n\t" \
  "v_cvt_pk_bf16_f32 v154, v152, v153\n\t" \
  "s_cbranch_vccnz .Lcnx_p9_1\n\ts_setprio 1\n.Lcnx_p9_1:\n\t" \
  "v_cvt_pk_f16_f32 v220, |v148|, |v149| clamp\n\t" \
  "v_mfma_f32_16x16x32_bf16 v[80:83], v[216:219], v[202:205], v[80:83]\n\t" \
  "ds_read_b64_tr_b16 v[216:217], v249 offset:6288\n\t" \
  "v_cvt_pknorm_i16_f32 v222, |v150|, |v151|\n\t" \
  "v_cvt_pk_f16_f32 v224, |v156|, |v157| clamp\n\t" \
  "s_waitcnt lgkmcnt(8)\n\t" \
  "v_mfma_f32_16x16x32_bf16 v[12:15], v[208:211], v[178:181], v[12:15]\n\t" \
  "ds_read_b64_tr_b16 v[218:219], v249 offset:7824\n\t" \
  "v_cvt_pknorm_i16_f32 v226, |v158|, |v159|\n\t" \
  "v_and_b32_sdwa v221, s56, v220 dst_sel:DWORD dst_unused:UNUSED_PAD src0_sel:DWORD src1_sel:WORD_1\n\t" \
  "v_mfma_f32_16x16x32_bf16 v[36:39], v[208:211], v[186:189], v[36:39]\n\t" \
  "v_and_b32 v220, s56, v220\n\t" \
  "v_lshlrev_b32_sdwa v223, 2, v222 dst_sel:DWORD dst_unused:UNUSED_PAD src0_sel:DWORD src1_sel:BYTE_3\n\t" \
  "v_mfma_f32_16x16x32_bf16 v[60:63], v[208:211], v[194:197], v[60:63]\n\t" \
  "ds_read_b32 v221, v221 offset:0\n\t" \
  "v_lshlrev_b32_sdwa v222, 2, v222 dst_sel:DWORD dst_unused:UNUSED_PAD src0_sel:DWORD src1_sel:BYTE_1\n\t" \
  "v_and_b32_sdwa v225, s56, v224 dst_sel:DWORD dst_unused:UNUSED_PAD src0_sel:DWORD src1_sel:WORD_1\n\t" \
  "v_and_b32 v224, s56, v224\n\t" \
  "v_mfma_f32_16x16x32_bf16 v[84:87], v[208:211], v[202:205], v[84:87]\n\t" \
  "ds_read_b64_tr_b16 v[208:209], v248 offset:49192\n\t" \
  "ds_read_b64_tr_b16 v[210:211], v248 offset:50216\n\t" \
  "v_lshlrev_b32_sdwa v227, 2, v226 dst_sel:DWORD dst_unused:UNUSED_PAD src0_sel:DWORD src1_sel:BYTE_3\n\t" \
  "v_lshlrev_b32_sdwa v226, 2, v226 dst_sel:DWORD dst_unused:UNUSED_PAD src0_sel:DWORD src1_sel:BYTE_1\n\t" \
  "s_waitcnt lgkmcnt(7)\n\t" \
  "v_mfma_f32_16x16x32_bf16 v[16:19], v[212:215], v[178:181], v[16:19]\n\t" \
  "ds_read_b32 v220, v220 offset:0\n\t" \
  "ds_read_b32 v223, v223 offset:15376\n\t" \
  "v_fma_f32 v163, |v163|, v229, v163 mul:4\n\t" \
  "v_fma_f32 v162, |v162|, v228, v162 mul:4\n\t" \
  "v_mfma_f32_16x16x32_bf16 v[40:43], v[212:215], v[186:189], v[40:43]\n\t" \
  "ds_read_b32 v222, v222 offset:15376\n\t" \
  "v_fma_mix_f32 v231, |v161|, v231, v231 op_sel:[0,1,0] op_sel_hi:[0,1,1] clamp\n\t" \
  "v_fma_mix_f32 v230, |v160|, v230, v230 op_sel:[0,1,0] op_sel_hi:[0,1,1] clamp\n\t" \
  "v_mfma_f32_16x16x32_bf16 v[64:67], v[212:215], v[194:197], v[64:67]\n\t" \
  "ds_read_b32 v225, v225 offset:0\n\t" \
  "s_waitcnt lgkmcnt(9)\n\t" \
  "v_fma_f32 v171, |v171|, v233, v171 mul:4\n\t" \
  "v_fma_f32 v170, |v170|, v232, v170 mul:4\n\t" \
  "v_mfma_f32_16x16x32_bf16 v[88:91], v[212:215], v[202:205], v[88:91]\n\t" \
  "ds_read_b64_tr_b16 v[212:213], v248 offset:52264\n\t" \
  "v_fma_f32 v169, |v169|, v235, v169 mul:4\n\t" \
  "v_fma_f32 v168, |v168|, v234, v168 mul:4\n\t" \
  "s_waitcnt lgkmcnt(8)\n\t" \
  "v_mfma_f32_16x16x32_bf16 v[20:23], v[216:219], v[178:181], v[20:23]\n\t" \
  "ds_read_b64_tr_b16 v[214:215], v248 offset:53288\n\t" \
  "v_fma_f32 v161, |v161|, v231, v161 mul:4\n\t" \
  "v_fma_f32 v160, |v160|, v230, v160 mul:4\n\t" \
  "v_mfma_f32_16x16x32_bf16 v[44:47], v[216:219], v[186:189], v[44:47]\n\t" \
  "ds_read_b32 v224, v224 offset:0\n\t" \
  "v_cvt_pk_bf16_f32 v163, v162, v163\n\t" \
  "v_cvt_pk_bf16_f32 v162, v160, v161\n\t" \
  "v_mfma_f32_16x16x32_bf16 v[68:71], v[216:219], v[194:197], v[68:71]\n\t" \
  "ds_read_b32 v227, v227 offset:15376\n\t" \
  "v_cvt_pk_bf16_f32 v171, v170, v171\n\t" \
  "v_cvt_pk_bf16_f32 v170, v168, v169\n\t" \
  "v_cvt_pk_f16_f32 v228, |v164|, |v165| clamp\n\t" \
  "v_mfma_f32_16x16x32_bf16 v[92:95], v[216:219], v[202:205], v[92:95]\n\t" \
  "ds_read_b64_tr_b16 v[216:217], v248 offset:51240\n\t" \
  "v_cvt_pknorm_i16_f32 v230, |v166|, |v167|\n\t" \
  "s_cbranch_vccnz .Lcnx_p9_2\n\ts_setprio 2\n.Lcnx_p9_2:\n\t" \
  "v_cvt_pk_f16_f32 v232, |v172|, |v173| clamp\n\t" \
  "s_waitcnt lgkmcnt(9)\n\t" \
  "v_mfma_f32_16x16x32_bf16 v[176:179], v[208:211], v[96:99], 0\n\t" \
  "ds_read_b64_tr_b16 v[218:219], v248 offset:54312\n\t" \
  "v_cvt_pk_f16_f32 v234, |v174|, |v175| clamp\n\t" \
  "v_and_b32_sdwa v229, s56, v228 dst_sel:DWORD dst_unused:UNUSED_PAD src0_sel:DWORD src1_sel:WORD_1\n\t" \
  "v_mfma_f32_16x16x32_bf16 v[184:187], v[208:211], v[108:111], 0\n\t" \
  "ds_read_b32 v226, v226 offset:15376\n\t" \
  "v_and_b32 v228, s56, v228\n\t" \
  "v_lshlrev_b32_sdwa v231, 2, v230 dst_sel:DWORD dst_unused:UNUSED_PAD src0_sel:DWORD src1_sel:BYTE_3\n\t" \
  "v_mfma_f32_16x16x32_bf16 v[192:195], v[208:211], v[120:123], 0\n\t" \
  "ds_read_b32 v229, v229 offset:0\n\t" \
  "v_lshlrev_b32_sdwa v230, 2, v230 dst_sel:DWORD dst_unused:UNUSED_PAD src0_sel:DWORD src1_sel:BYTE_1\n\t" \
  "v_and_b32_sdwa v233, s56, v232 dst_sel:DWORD dst_unused:UNUSED_PAD src0_sel:DWORD src1_sel:WORD_1\n\t" \
  "v_mfma_f32_16x16x32_bf16 v[200:203], v[208:211], v[132:135], 0\n\t" \
  "ds_read_b64_tr_b16 v[208:209], v248 offset:49704\n\t" \
  "v_and_b32 v232, s56, v232\n\t" \
  "v_and_b32_sdwa v235, s56, v234 dst_sel:DWORD dst_unused:UNUSED_PAD src0_sel:DWORD src1_sel:WORD_1\n\t" \
  "s_waitcnt lgkmcnt(7)\n\t" \
  "v_mfma_f32_16x16x32_bf16 v[176:179], v[212:215], v[100:103], v[176:179]\n\t" \
  "ds_read_b64_tr_b16 v[210:211], v248 offset:50728\n\t" \
  "ds_read_b32 v228, v228 offset:0\n\t" \
  "v_and_b32 v234, s56, v234\n\t" \
  "v_fma_f32 v149, |v149|, v221, v149 mul:4\n\t" \
  "v_mfma_f32_16x16x32_bf16 v[184:187], v[212:215], v[112:115], v[184:187]\n\t" \
  "ds_read_b32 v231, v231 offset:15376\n\t" \
  "ds_read_b32 v230, v230 offset:15376\n\t" \
  "v_fma_f32 v148, |v148|, v220, v148 mul:4\n\t" \
  "v_fma_mix_f32 v223, |v151|, v223, v223 op_sel:[0,1,0] op_sel_hi:[0,1,1] clamp\n\t" \
  "v_mfma_f32_16x16x32_bf16 v[192:195], v[212:215], v[124:127], v[192:195]\n\t" \
  "ds_read_b32 v233, v233 offset:0\n\t" \
  "v_fma_mix_f32 v222, |v150|, v222, v222 op_sel:[0,1,0] op_sel_hi:[0,1,1] clamp\n\t" \
  "s_waitcnt lgkmcnt(7)\n\t" \
  "v_fma_f32 v157, |v157|, v225, v157 mul:4\n\t" \
  "v_mfma_f32_16x16x32_bf16 v[200:203], v[212:215], v[136:139], v[200:203]\n\t" \
  "ds_read_b64_tr_b16 v[212:213], v248 offset:52776\n\t" \
  "v_fma_f32 v156, |v156|, v224, v156 mul:4\n\t" \
  "v_fma_mix_f32 v227, |v159|, v227, v227 op_sel:[0,1,0] op_sel_hi:[0,1,1] clamp\n\t" \
  "v_fma_mix_f32 v226, |v158|, v226, v226 op_sel:[0,1,0] op_sel_hi:[0,1,1] clamp\n\t" \
  "v_mfma_f32_16x16x32_bf16 v[176:179], v[216:219], v[104:107], v[176:179]\n\t" \
  "ds_read_b64_tr_b16 v[214:215], v248 offset:53800\n\t" \
  "v_fma_f32 v151, |v151|, v223, v151 mul:4\n\t" \
  "v_fma_f32 v150, |v150|, v222, v150 mul:4\n\t" \
  "v_mfma_f32_16x16x32_bf16 v[184:187], v[216:219], v[116:119], v[184:187]\n\t" \
  "ds_read_b32 v232, v232 offset:0\n\t" \
  "v_fma_f32 v159, |v159|, v227, v159 mul:4\n\t" \
  "v_fma_f32 v158, |v158|, v226, v158 mul:4\n\t" \
  "v_mfma_f32_16x16x32_bf16 v[192:195], v[216:219], v[128:131], v[192:195]\n\t" \
  "ds_read_b32 v235, v235 offset:0\n\t" \
  "v_cvt_pk_bf16_f32 v148, v148, v149\n\t" \
  "v_cvt_pk_bf16_f32 v149, v150, v151\n\t" \
  "v_mfma_f32_16x16x32_bf16 v[200:203], v[216:219], v[140:143], v[200:203]\n\t" \
  "ds_read_b64_tr_b16 v[216:217], v248 offset:51752\n\t" \
  "v_cvt_pk_bf16_f32 v156, v156, v157\n\t" \
  "v_cvt_pk_bf16_f32 v157, v158, v159\n\t" \
  "s_waitcnt lgkmcnt(9)\n\t" \
  "v_mfma_f32_16x16x32_bf16 v[180:183], v[208:211], v[96:99], 0\n\t" \
  "ds_read_b64_tr_b16 v[218:219], v248 offset:54824\n\t" \
  "v_cvt_pk_f16_f32 v220, |v178|, |v179| clamp\n\t" \
  "s_cbranch_vccnz .Lcnx_p9_3\n\ts_setprio 3\n.Lcnx_p9_3:\n\t" \
  "v_cvt_pknorm_i16_f32 v222, |v176|, |v177|\n\t" \
  "v_mfma_f32_16x16x32_bf16 v[188:191], v[208:211], v[108:111], 0\n\t" \
  "s_waitcnt vmcnt(1)\n\t" \
  "s_cmp_lg_u32 s58, 1\n\t" \
  "s_cbranch_scc1 .Lcnx_c22\n\t" \
  "s_waitcnt vmcnt(0)\n\t" \
  ".Lcnx_c22:\n\t" \
  "s_mov_b64 exec, 3\n\t" \
  "ds_add_rtn_u32 v246, v251, v250 offset:47216\n\t" \
  "s_mov_b64 exec, -1\n\t" \
  "ds_read_b32 v234, v234 offset:0\n\t" \
  "v_cvt_pk_f16_f32 v224, |v186|, |v187| clamp\n\t" \
  "v_cvt_pknorm_i16_f32 v226, |v184|, |v185|\n\t" \
  "v_mfma_f32_16x16x32_bf16 v[196:199], v[208:211], v[120:123], 0\n\t" \
  "v_and_b32_sdwa v221, s56, v220 dst_sel:DWORD dst_unused:UNUSED_PAD src0_sel:DWORD src1_sel:WORD_1\n\t" \
  "v_and_b32 v220, s56, v220\n\t" \
  "v_mfma_f32_16x16x32_bf16 v[204:207], v[208:211], v[132:135], 0\n\t" \
  "s_waitcnt lgkmcnt(1)\n\t" \
  "v_readfirstlane_b32 s4, v246\n\t" \
  "s_cmp_lt_u32 s4, s57\n\t" \
  "s_cbranch_scc1 .Lcnx_w108\n.Lcnx_b108:\n\t" \
  "v_add_u32 v248, s59, v248\n\t" \
  "v_add_u32 v249, s60, v249\n\t" \
  "ds_read_b64_tr_b16 v[208:209], v249 offset:24600\n\t" \
  "v_lshlrev_b32_sdwa v223, 2, v222 dst_sel:DWORD dst_unused:UNUSED_PAD src0_sel:DWORD src1_sel:BYTE_3\n\t" \
  "v_lshlrev_b32_sdwa v222, 2, v222 dst_sel:DWORD dst_unused:UNUSED_PAD src0_sel:DWORD src1_sel:BYTE_1\n\t" \
  "v_and_b32_sdwa v225, s56, v224 dst_sel:DWORD dst_unused:UNUSED_PAD src0_sel:DWORD src1_sel:WORD_1\n\t" \
  "v_mfma_f32_16x16x32_bf16 v[180:183], v[212:215], v[100:103], v[180:183]\n\t" \
  "ds_read_b64_tr_b16 v[210:211], v249 offset:26136\n\t" \
  "v_and_b32 v224, s56, v224\n\t" \
  "v_lshlrev_b32_sdwa v227, 2, v226 dst_sel:DWORD dst_unused:UNUSED_PAD src0_sel:DWORD src1_sel:BYTE_3\n\t" \
  "v_mfma_f32_16x16x32_bf16 v[188:191], v[212:215], v[112:115], v[188:191]\n\t" \
  "s_cmp_eq_u32 s37, 0\n\t" \
  "s_cbranch_scc1 .Lcnx_f10\n.Lcnx_g10:\n\t" \
  "ds_read_b32 v221, v221 offset:0\n\t" \
  "ds_read_b32 v220, v220 offset:0\n\t" \
  "v_lshlrev_b32_sdwa v226, 2, v226 dst_sel:DWORD dst_unused:UNUSED_PAD src0_sel:DWORD src1_sel:BYTE_1\n\t" \
  "v_fma_f32 v165, |v165|, v229, v165 mul:4\n\t" \
  "v_mfma_f32_16x16x32_bf16 v[196:199], v[212:215], v[124:127], v[196:199]\n\t" \
  "ds_read_b32 v223, v223 offset:15376\n\t" \
  "ds_read_b32 v222, v222 offset:15376\n\t" \
  "v_fma_f32 v164, |v164|, v228, v164 mul:4\n\t" \
  "v_fma_mix_f32 v231, |v167|, v231, v231 op_sel:[0,1,0] op_sel_hi:[0,1,1] clamp\n\t" \
  "v_mfma_f32_16x16x32_bf16 v[204:207], v[212:215], v[136:139], v[204:207]\n\t" \
  "ds_read_b64_tr_b16 v[212:213], v249 offset:24608\n\t" \
  "ds_read_b64_tr_b16 v[214:215], v249 offset:26144\n\t" \
  "v_fma_mix_f32 v230, |v166|, v230, v230 op_sel:[0,1,0] op_sel_hi:[0,1,1] clamp\n\t" \
  "s_waitcnt lgkmcnt(8)\n\t" \
  "v_fma_f32 v173, |v173|, v233, v173 mul:4\n\t" \
  "v_mfma_f32_16x16x32_bf16 v[180:183], v[216:219], v[104:107], v[180:183]\n\t" \
  "ds_read_b32 v225, v225 offset:0\n\t" \
  "v_fma_f32 v172, |v172|, v232, v172 mul:4\n\t" \
  "v_fma_f32 v175, |v175|, v235, v175 mul:4\n\t" \
  "v_mfma_f32_16x16x32_bf16 v[188:191], v[216:219], v[116:119], v[188:191]\n\t" \
  "ds_read_b32 v224, v224 offset:0\n\t" \
  "v_fma_f32 v174, |v174|, v234, v174 mul:4\n\t" \
  "v_fma_f32 v167, |v167|, v231, v167 mul:4\n\t" \
  "v_mfma_f32_16x16x32_bf16 v[196:199], v[216:219], v[128:131], v[196:199]\n\t" \
  "ds_read_b32 v227, v227 offset:15376\n\t" \
  "v_fma_f32 v166, |v166|, v230, v166 mul:4\n\t" \
  "v_cvt_pk_bf16_f32 v164, v164, v165\n\t" \
  "v_mfma_f32_16x16x32_bf16 v[204:207], v[216:219], v[140:143], v[204:207]\n\t" \
  "ds_read_b64_tr_b16 v[216:217], v249 offset:24664\n\t" \
  "v_cvt_pk_bf16_f32 v165, v166, v167\n\t" \
  "v_cvt_pk_bf16_f32 v172, v172, v173\n\t" \
  "v_cvt_pk_bf16_f32 v173, v174, v175\n\t" \
  "s_cbranch_vccnz .Lcnx_p10_0\n\ts_setprio 0\n.Lcnx_p10_0:\n\t" \
  "v_cvt_pk_f16_f32 v228, |v194|, |v195| clamp\n\t" \
  "v_cvt_pknorm_i16_f32 v230, |v192|, |v193|\n\t" \
  "v_mfma_f32_16x16x32_bf16 v[64:67], v[242:245], v[236:239], v[64:67]\n\t" \
  "ds_read_b64_tr_b16 v[218:219], v249 offset:26200\n\t" \
  "v_cvt_pk_f16_f32 v232, |v202|, |v203| clamp\n\t" \
  "v_cvt_pk_f16_f32 v234, |v200|, |v201| clamp\n\t" \
  "v_mfma_f32_16x16x32_bf16 v[68:71], v[240:243], v[236:239], v[68:71]\n\t" \
  "buffer_load_dwordx4 v[236:239], v250, s[8:11], s29 offen offset:192\n\t" \
  "s_cmp_lg_u32 s58, 0\n\t" \
  "s_cbranch_scc1 .Lcnx_c34\n\t" \
  "s_add_u32 m0, s25, 65600\n\t" \
  "s_add_u32 s24, s24, s26\n\t" \
  "buffer_load_dwordx4 v254, s[8:11], s24 offen lds\n\t" \
  "s_cmp_lg_u32 s38, 6\n\t" \
  "s_cbranch_scc1 .Lcnx_c34\n\t" \
  "s_mov_b32 m0, 163632\n\t" \
  "s_add_u32 s30, s30, s31\n\t" \
  "buffer_load_dword v252, s[8:11], s30 offen lds\n\t" \
  ".Lcnx_c34:\n\t" \
  "ds_read_b32 v226, v226 offset:15376\n\t" \
  "v_and_b32_sdwa v229, s56, v228 dst_sel:DWORD dst_unused:UNUSED_PAD src0_sel:DWORD src1_sel:WORD_1\n\t" \
  "v_and_b32 v228, s56, v228\n\t" \
  "s_waitcnt lgkmcnt(12)\n\t" \
  "v_mfma_f32_16x16x32_bf16 v[0:3], v[208:211], v[146:149], v[0:3]\n\t" \
  "ds_read_b32 v229, v229 offset:0\n\t" \
  "v_lshlrev_b32_sdwa v231, 2, v230 dst_sel:DWORD dst_unused:UNUSED_PAD src0_sel:DWORD src1_sel:BYTE_3\n\t" \
  "v_lshlrev_b32_sdwa v230, 2, v230 dst_sel:DWORD dst_unused:UNUSED_PAD src0_sel:DWORD src1_sel:BYTE_1\n\t" \
  "v_mfma_f32_16x16x32_bf16 v[24:27], v[208:211], v[154:157], v[24:27]\n\t" \
  "ds_read_b32 v228, v228 offset:0\n\t" \
  "v_and_b32_sdwa v233, s56, v232 dst_sel:DWORD dst_unused:UNUSED_PAD src0_sel:DWORD src1_sel:WORD_1\n\t" \
  "v_and_b32 v232, s56, v232\n\t" \
  "v_mfma_f32_16x16x32_bf16 v[48:51], v[208:211], v[162:165], v[48:51]\n\t" \
  "ds_read_b32 v231, v231 offset:15376\n\t" \
  "v_and_b32_sdwa v235, s56, v234 dst_sel:DWORD dst_unused:UNUSED_PAD src0_sel:DWORD src1_sel:WORD_1\n\t" \
  "v_and_b32 v234, s56, v234\n\t" \
  "v_mfma_f32_16x16x32_bf16 v[72:75], v[208:211], v[170:173], v[72:75]\n\t" \
  "ds_read_b64_tr_b16 v[208:209], v249 offset:24672\n\t" \
  "ds_read_b64_tr_b16 v[210:211], v249 offset:26208\n\t" \
  "s_waitcnt lgkmcnt(13)\n\t" \
  "v_fma_f32 v179, |v179|, v221, v179 mul:4\n\t" \
  "v_fma_f32 v178, |v178|, v220, v178 mul:4\n\t" \
  "s_waitcnt lgkmcnt(11)\n\t" \
  "v_mfma_f32_16x16x32_bf16 v[4:7], v[212:215], v[146:149], v[4:7]\n\t" \
  "ds_read_b32 v230, v230 offset:15376\n\t" \
  "v_fma_mix_f32 v223, |v177|, v223, v223 op_sel:[0,1,0] op_sel_hi:[0,1,1] clamp\n\t" \
  "v_fma_mix_f32 v222, |v176|, v222, v222 op_sel:[0,1,0] op_sel_hi:[0,1,1] clamp\n\t" \
  "v_mfma_f32_16x16x32_bf16 v[28:31], v[212:215], v[154:157], v[28:31]\n\t" \
  "ds_read_b32 v233, v233 offset:0\n\t" \
  "s_waitcnt lgkmcnt(7)\n\t" \
  "v_fma_f32 v187, |v187|, v225, v187 mul:4\n\t" \
  "v_fma_f32 v186, |v186|, v224, v186 mul:4\n\t" \
  "v_mfma_f32_16x16x32_bf16 v[52:55], v[212:215], v[162:165], v[52:55]\n\t" \
  "ds_read_b32 v232, v232 offset:0\n\t" \
  "v_fma_mix_f32 v227, |v185|, v227, v227 op_sel:[0,1,0] op_sel_hi:[0,1,1] clamp\n\t" \
  "v_fma_mix_f32 v226, |v184|, v226, v226 op_sel:[0,1,0] op_sel_hi:[0,1,1] clamp\n\t" \
  "v_fma_f32 v177, |v177|, v223, v177 mul:4\n\t" \
  "v_mfma_f32_16x16x32_bf16 v[76:79], v[212:215], v[170:173], v[76:79]\n\t" \
  "ds_read_b64_tr_b16 v[212:213], v249 offset:24728\n\t" \
  "v_fma_f32 v176, |v176|, v222, v176 mul:4\n\t" \
  "v_fma_f32 v185, |v185|, v227, v185 mul:4\n\t" \
  "v_mfma_f32_16x16x32_bf16 v[8:11], v[216:219], v[146:149], v[8:11]\n\t" \
  "ds_read_b64_tr_b16 v[214:215], v249 offset:26264\n\t" \
  "v_fma_f32 v184, |v184|, v226, v184 mul:4\n\t" \
  "v_cvt_pk_bf16_f32 v179, v178, v179\n\t" \
  "v_mfma_f32_16x16x32_bf16 v[32:35], v[216:219], v[154:157], v[32:35]\n\t" \
  "ds_read_b32 v235, v235 offset:0\n\t" \
  "v_cvt_pk_bf16_f32 v178, v176, v177\n\t" \
  "v_cvt_pk_bf16_f32 v187, v186, v187\n\t" \
  "v_mfma_f32_16x16x32_bf16 v[56:59], v[216:219], v[162:165], v[56:59]\n\t" \
  "ds_read_b32 v234, v234 offset:0\n\t" \
  "v_cvt_pk_bf16_f32 v186, v184, v185\n\t" \
  "s_cbranch_vccnz .Lcnx_p10_1\n\ts_setprio 1\n.Lcnx_p10_1:\n\t" \
  "v_cvt_pk_f16_f32 v220, |v180|, |v181| clamp\n\t" \
  "v_mfma_f32_16x16x32_bf16 v[80:83], v[216:219], v[170:173], v[80:83]\n\t" \
  "ds_read_b64_tr_b16 v[216:217], v249 offset:24736\n\t" \
  "v_cvt_pknorm_i16_f32 v222, |v182|, |v183|\n\t" \
  "v_cvt_pk_f16_f32 v224, |v188|, |v189| clamp\n\t" \
  "s_waitcnt lgkmcnt(8)\n\t" \
  "v_mfma_f32_16x16x32_bf16 v[12:15], v[208:211], v[146:149], v[12:15]\n\t" \
  "ds_read_b64_tr_b16 v[218:219], v249 offset:26272\n\t" \
  "v_cvt_pknorm_i16_f32 v226, |v190|, |v191|\n\t" \
  "v_and_b32_sdwa v221, s56, v220 dst_sel:DWORD dst_unused:UNUSED_PAD src0_sel:DWORD src1_sel:WORD_1\n\t" \
  "v_mfma_f32_16x16x32_bf16 v[36:39], v[208:211], v[154:157], v[36:39]\n\t" \
  "v_and_b32 v220, s56, v220\n\t" \
  "v_lshlrev_b32_sdwa v223, 2, v222 dst_sel:DWORD dst_unused:UNUSED_PAD src0_sel:DWORD src1_sel:BYTE_3\n\t" \
  "v_mfma_f32_16x16x32_bf16 v[60:63], v[208:211], v[162:165], v[60:63]\n\t" \
  "ds_read_b32 v221, v221 offset:0\n\t" \
  "v_lshlrev_b32_sdwa v222, 2, v222 dst_sel:DWORD dst_unused:UNUSED_PAD src0_sel:DWORD src1_sel:BYTE_1\n\t" \
  "v_and_b32_sdwa v225, s56, v224 dst_sel:DWORD dst_unused:UNUSED_PAD src0_sel:DWORD src1_sel:WORD_1\n\t" \
  "v_and_b32 v224, s56, v224\n\t" \
  "v_mfma_f32_16x16x32_bf16 v[84:87], v[208:211], v[170:173], v[84:87]\n\t" \
  "ds_read_b64_tr_b16 v[208:209], v248 offset:36904\n\t" \
  "ds_read_b64_tr_b16 v[210:211], v248 offset:37928\n\t" \
  "v_lshlrev_b32_sdwa v227, 2, v226 dst_sel:DWORD dst_unused:UNUSED_PAD src0_sel:DWORD src1_sel:BYTE_3\n\t" \
  "v_lshlrev_b32_sdwa v226, 2, v226 dst_sel:DWORD dst_unused:UNUSED_PAD src0_sel:DWORD src1_sel:BYTE_1\n\t" \
  "s_waitcnt lgkmcnt(7)\n\t" \
  "v_mfma_f32_16x16x32_bf16 v[16:19], v[212:215], v[146:149], v[16:19]\n\t" \
  "ds_read_b32 v220, v220 offset:0\n\t" \
  "ds_read_b32 v223, v223 offset:15376\n\t" \
  "v_fma_f32 v195, |v195|, v229, v195 mul:4\n\t" \
  "v_fma_f32 v194, |v194|, v228, v194 mul:4\n\t" \
  "v_mfma_f32_16x16x32_bf16 v[40:43], v[212:215], v[154:157], v[40:43]\n\t" \
  "ds_read_b32 v222, v222 offset:15376\n\t" \
  "v_fma_mix_f32 v231, |v193|, v231, v231 op_sel:[0,1,0] op_sel_hi:[0,1,1] clamp\n\t" \
  "v_fma_mix_f32 v230, |v192|, v230, v230 op_sel:[0,1,0] op_sel_hi:[0,1,1] clamp\n\t" \
  "v_mfma_f32_16x16x32_bf16 v[64:67], v[212:215], v[162:165], v[64:67]\n\t" \
  "ds_read_b32 v225, v225 offset:0\n\t" \
  "s_waitcnt lgkmcnt(9)\n\t" \
  "v_fma_f32 v203, |v203|, v233, v203 mul:4\n\t" \
  "v_fma_f32 v202, |v202|, v232, v202 mul:4\n\t" \
  "v_mfma_f32_16x16x32_bf16 v[88:91], v[212:215], v[170:173], v[88:91]\n\t" \
  "ds_read_b64_tr_b16 v[212:213], v248 offset:39976\n\t" \
  "v_fma_f32 v201, |v201|, v235, v201 mul:4\n\t" \
  "v_fma_f32 v200, |v200|, v234, v200 mul:4\n\t" \
  "s_waitcnt lgkmcnt(8)\n\t" \
  "v_mfma_f32_16x16x32_bf16 v[20:23], v[216:219], v[146:149], v[20:23]\n\t" \
  "ds_read_b64_tr_b16 v[214:215], v248 offset:41000\n\t" \
  "v_fma_f32 v193, |v193|, v231, v193 mul:4\n\t" \
  "v_fma_f32 v192, |v192|, v230, v192 mul:4\n\t" \
  "v_mfma_f32_16x16x32_bf16 v[44:47], v[216:219], v[154:157], v[44:47]\n\t" \
  "ds_read_b32 v224, v224 offset:0\n\t" \
  "v_cvt_pk_bf16_f32 v195, v194, v195\n\t" \
  "v_cvt_pk_bf16_f32 v194, v192, v193\n\t" \
  "v_mfma_f32_16x16x32_bf16 v[68:71], v[216:219], v[162:165], v[68:71]\n\t" \
  "ds_read_b32 v227, v227 offset:15376\n\t" \
  "v_cvt_pk_bf16_f32 v203, v202, v203\n\t" \
  "v_cvt_pk_bf16_f32 v202, v200, v201\n\t" \
  "v_cvt_pk_f16_f32 v228, |v196|, |v197| clamp\n\t" \
  "v_mfma_f32_16x16x32_bf16 v[92:95], v[216:219], v[170:173], v[92:95]\n\t" \
  "ds_read_b64_tr_b16 v[216:217], v248 offset:38952\n\t" \
  "v_cvt_pknorm_i16_f32 v230, |v198|, |v199|\n\t" \
  "s_cbranch_vccnz .Lcnx_p10_2\n\ts_setprio 2\n.Lcnx_p10_2:\n\t" \
  "v_cvt_pk_f16_f32 v232, |v204|, |v205| clamp\n\t" \
  "s_waitcnt lgkmcnt(9)\n\t" \
  "v_mfma_f32_16x16x32_bf16 v[144:147], v[208:211], v[96:99], 0\n\t" \
  "ds_read_b64_tr_b16 v[218:219], v248 offset:42024\n\t" \
  "v_cvt_pk_f16_f32 v234, |v206|, |v207| clamp\n\t" \
  "v_and_b32_sdwa v229, s56, v228 dst_sel:DWORD dst_unused:UNUSED_PAD src0_sel:DWORD src1_sel:WORD_1\n\t" \
  "v_mfma_f32_16x16x32_bf16 v[152:155], v[208:211], v[108:111], 0\n\t" \
  "ds_read_b32 v226, v226 offset:15376\n\t" \
  "v_and_b32 v228, s56, v228\n\t" \
  "v_lshlrev_b32_sdwa v231, 2, v230 dst_sel:DWORD dst_unused:UNUSED_PAD src0_sel:DWORD src1_sel:BYTE_3\n\t" \
  "v_mfma_f32_16x16x32_bf16 v[160:163], v[208:211], v[120:123], 0\n\t" \
  "ds_read_b32 v229, v229 offset:0\n\t" \
  "v_lshlrev_b32_sdwa v230, 2, v230 dst_sel:DWORD dst_unused:UNUSED_PAD src0_sel:DWORD src1_sel:BYTE_1\n\t" \
  "v_and_b32_sdwa v233, s56, v232 dst_sel:DWORD dst_unused:UNUSED_PAD src0_sel:DWORD src1_sel:WORD_1\n\t" \
  "v_mfma_f32_16x16x32_bf16 v[168:171], v[208:211], v[132:135], 0\n\t" \
  "ds_read_b64_tr_b16 v[208:209], v248 offset:37416\n\t" \
  "v_and_b32 v232, s56, v232\n\t" \
  "v_and_b32_sdwa v235, s56, v234 dst_sel:DWORD dst_unused:UNUSED_PAD src0_sel:DWORD src1_sel:WORD_1\n\t" \
  "s_waitcnt lgkmcnt(7)\n\t" \
  "v_mfma_f32_16x16x32_bf16 v[144:147], v[212:215], v[100:103], v[144:147]\n\t" \
  "ds_read_b64_tr_b16 v[210:211], v248 offset:38440\n\t" \
  "ds_read_b32 v228, v228 offset:0\n\t" \
  "v_and_b32 v234, s56, v234\n\t" \
  "v_fma_f32 v181, |v181|, v221, v181 mul:4\n\t" \
  "v_mfma_f32_16x16x32_bf16 v[152:155], v[212:215], v[112:115], v[152:155]\n\t" \
  "ds_read_b32 v231, v231 offset:15376\n\t" \
  "ds_read_b32 v230, v230 offset:15376\n\t" \
  "v_fma_f32 v180, |v180|, v220, v180 mul:4\n\t" \
  "v_fma_mix_f32 v223, |v183|, v223, v223 op_sel:[0,1,0] op_sel_hi:[0,1,1] clamp\n\t" \
  "v_mfma_f32_16x16x32_bf16 v[160:163], v[212:215], v[124:127], v[160:163]\n\t" \
  "ds_read_b32 v233, v233 offset:0\n\t" \
  "v_fma_mix_f32 v222, |v182|, v222, v222 op_sel:[0,1,0] op_sel_hi:[0,1,1] clamp\n\t" \
  "s_waitcnt lgkmcnt(7)\n\t" \
  "v_fma_f32 v189, |v189|, v225, v189 mul:4\n\t" \
  "v_mfma_f32_16x16x32_bf16 v[168:171], v[212:215], v[136:139], v[168:171]\n\t" \
  "ds_read_b64_tr_b16 v[212:213], v248 offset:40488\n\t" \
  "v_fma_f32 v188, |v188|, v224, v188 mul:4\n\t" \
  "v_fma_mix_f32 v227, |v191|, v227, v227 op_sel:[0,1,0] op_sel_hi:[0,1,1] clamp\n\t" \
  "v_fma_mix_f32 v226, |v190|, v226, v226 op_sel:[0,1,0] op_sel_hi:[0,1,1] clamp\n\t" \
  "v_mfma_f32_16x16x32_bf16 v[144:147], v[216:219], v[104:107], v[144:147]\n\t" \
  "ds_read_b64_tr_b16 v[214:215], v248 offset:41512\n\t" \
  "v_fma_f32 v183, |v183|, v223, v183 mul:4\n\t" \
  "v_fma_f32 v182, |v182|, v222, v182 mul:4\n\t" \
  "v_mfma_f32_16x16x32_bf16 v[152:155], v[216:219], v[116:119], v[152:155]\n\t" \
  "ds_read_b32 v232, v232 offset:0\n\t" \
  "v_fma_f32 v191, |v191|, v227, v191 mul:4\n\t" \
  "v_fma_f32 v190, |v190|, v226, v190 mul:4\n\t" \
  "v_mfma_f32_16x16x32_bf16 v[160:163], v[216:219], v[128:131], v[160:163]\n\t" \
  "ds_read_b32 v235, v235 offset:0\n\t" \
  "v_cvt_pk_bf16_f32 v180, v180, v181\n\t" \
  "v_cvt_pk_bf16_f32 v181, v182, v183\n\t" \
  "v_mfma_f32_16x16x32_bf16 v[168:171], v[216:219], v[140:143], v[168:171]\n\t" \
  "ds_read_b64_tr_b16 v[216:217], v248 offset:39464\n\t" \
  "v_cvt_pk_bf16_f32 v188, v188, v189\n\t" \
  "v_cvt_pk_bf16_f32 v189, v190, v191\n\t" \
  "s_waitcnt lgkmcnt(9)\n\t" \
  "v_mfma_f32_16x16x32_bf16 v[148:151], v[208:211], v[96:99], 0\n\t" \
  "ds_read_b64_tr_b16 v[218:219], v248 offset:42536\n\t" \
  "v_cvt_pk_f16_f32 v220, |v146|, |v147| clamp\n\t" \
  "s_cbranch_vccnz .Lcnx_p10_3\n\ts_setprio 3\n.Lcnx_p10_3:\n\t" \
  "v_cvt_pknorm_i16_f32 v222, |v144|, |v145|\n\t" \
  "v_mfma_f32_16x16x32_bf16 v[156:159], v[208:211], v[108:111], 0\n\t" \
  "s_waitcnt vmcnt(1)\n\t" \
  "s_cmp_lg_u32 s58, 1\n\t" \
  "s_cbranch_scc1 .Lcnx_c23\n\t" \
  "s_waitcnt vmcnt(0)\n\t" \
  ".Lcnx_c23:\n\t" \
  "s_mov_b64 exec, 3\n\t" \
  "ds_add_rtn_u32 v246, v251, v250 offset:47224\n\t" \
  "s_mov_b64 exec, -1\n\t" \
  "ds_read_b32 v234, v234 offset:0\n\t" \
  "v_cvt_pk_f16_f32 v224, |v154|, |v155| clamp\n\t" \
  "v_cvt_pknorm_i16_f32 v226, |v152|, |v153|\n\t" \
  "v_mfma_f32_16x16x32_bf16 v[164:167], v[208:211], v[120:123], 0\n\t" \
  "v_and_b32_sdwa v221, s56, v220 dst_sel:DWORD dst_unused:UNUSED_PAD src0_sel:DWORD src1_sel:WORD_1\n\t" \
  "v_and_b32 v220, s56, v220\n\t" \
  "v_mfma_f32_16x16x32_bf16 v[172:175], v[208:211], v[132:135], 0\n\t" \
  "s_waitcnt lgkmcnt(1)\n\t" \
  "v_readfirstlane_b32 s4, v246\n\t" \
  "s_cmp_lt_u32 s4, s57\n\t" \
  "s_cbranch_scc1 .Lcnx_w120\n.Lcnx_b120:\n\t" \
  "ds_read_b64_tr_b16 v[208:209], v249 offset:30752\n\t" \
  "v_lshlrev_b32_sdwa v223, 2, v222 dst_sel:DWORD dst_unused:UNUSED_PAD src0_sel:DWORD src1_sel:BYTE_3\n\t" \
  "v_lshlrev_b32_sdwa v222, 2, v222 dst_sel:DWORD dst_unused:UNUSED_PAD src0_sel:DWORD src1_sel:BYTE_1\n\t" \
  "v_and_b32_sdwa v225, s56, v224 dst_sel:DWORD dst_unused:UNUSED_PAD src0_sel:DWORD src1_sel:WORD_1\n\t" \
  "v_mfma_f32_16x16x32_bf16 v[148:151], v[212:215], v[100:103], v[148:151]\n\t" \
  "ds_read_b64_tr_b16 v[210:211], v249 offset:32288\n\t" \
  "v_and_b32 v224, s56, v224\n\t" \
  "v_lshlrev_b32_sdwa v227, 2, v226 dst_sel:DWORD dst_unused:UNUSED_PAD src0_sel:DWORD src1_sel:BYTE_3\n\t" \
  "v_mfma_f32_16x16x32_bf16 v[156:159], v[212:215], v[112:115], v[156:159]\n\t" \
  "s_cmp_eq_u32 s37, 0\n\t" \
  "s_cbranch_scc1 .Lcnx_f11\n.Lcnx_g11:\n\t" \
  "ds_read_b32 v221, v221 offset:0\n\t" \
  "ds_read_b32 v220, v220 offset:0\n\t" \
  "v_lshlrev_b32_sdwa v226, 2, v226 dst_sel:DWORD dst_unused:UNUSED_PAD src0_sel:DWORD src1_sel:BYTE_1\n\t" \
  "v_fma_f32 v197, |v197|, v229, v197 mul:4\n\t" \
  "v_mfma_f32_16x16x32_bf16 v[164:167], v[212:215], v[124:127], v[164:167]\n\t" \
  "ds_read_b32 v223, v223 offset:15376\n\t" \
  "ds_read_b32 v222, v222 offset:15376\n\t" \
  "v_fma_f32 v196, |v196|, v228, v196 mul:4\n\t" \
  "v_fma_mix_f32 v231, |v199|, v231, v231 op_sel:[0,1,0] op_sel_hi:[0,1,1] clamp\n\t" \
  "v_mfma_f32_16x16x32_bf16 v[172:175], v[212:215], v[136:139], v[172:175]\n\t" \
  "ds_read_b64_tr_b16 v[212:213], v249 offset:30760\n\t" \
  "ds_read_b64_tr_b16 v[214:215], v249 offset:32296\n\t" \
  "v_fma_mix_f32 v230, |v198|, v230, v230 op_sel:[0,1,0] op_sel_hi:[0,1,1] clamp\n\t" \
  "s_waitcnt lgkmcnt(8)\n\t" \
  "v_fma_f32 v205, |v205|, v233, v205 mul:4\n\t" \
  "v_mfma_f32_16x16x32_bf16 v[148:151], v[216:219], v[104:107], v[148:151]\n\t" \
  "ds_read_b32 v225, v225 offset:0\n\t" \
  "v_fma_f32 v204, |v204|, v232, v204 mul:4\n\t" \
  "v_fma_f32 v207, |v207|, v235, v207 mul:4\n\t" \
  "v_mfma_f32_16x16x32_bf16 v[156:159], v[216:219], v[116:119], v[156:159]\n\t" \
  "ds_read_b32 v224, v224 offset:0\n\t" \
  "v_fma_f32 v206, |v206|, v234, v206 mul:4\n\t" \
  "v_fma_f32 v199, |v199|, v231, v199 mul:4\n\t" \
  "v_mfma_f32_16x16x32_bf16 v[164:167], v[216:219], v[128:131], v[164:167]\n\t" \
  "ds_read_b32 v227, v227 offset:15376\n\t" \
  "v_fma_f32 v198, |v198|, v230, v198 mul:4\n\t" \
  "v_cvt_pk_bf16_f32 v196, v196, v197\n\t" \
  "v_mfma_f32_16x16x32_bf16 v[172:175], v[216:219], v[140:143], v[172:175]\n\t" \
  "ds_read_b64_tr_b16 v[216:217], v249 offset:30816\n\t" \
  "v_cvt_pk_bf16_f32 v197, v198, v199\n\t" \
  "v_cvt_pk_bf16_f32 v204, v204, v205\n\t" \
  "v_cvt_pk_bf16_f32 v205, v206, v207\n\t" \
  "s_cbranch_vccnz .Lcnx_p11_0\n\ts_setprio 0\n.Lcnx_p11_0:\n\t" \
  "v_cvt_pk_f16_f32 v228, |v162|, |v163| clamp\n\t" \
  "v_cvt_pknorm_i16_f32 v230, |v160|, |v161|\n\t" \
  "v_mfma_f32_16x16x32_bf16 v[72:75], v[242:245], v[236:239], v[72:75]\n\t" \
  "ds_read_b64_tr_b16 v[218:219], v249 offset:32352\n\t" \
  "v_cvt_pk_f16_f32 v232, |v170|, |v171| clamp\n\t" \
  "v_cvt_pk_f16_f32 v234, |v168|, |v169| clamp\n\t" \
  "v_mfma_f32_16x16x32_bf16 v[76:79], v[240:243], v[236:239], v[76:79]\n\t" \
  "buffer_load_dwordx4 v[236:239], v250, s[8:11], s29 offen offset:256\n\t" \
  "s_cmp_lg_u32 s38, 7\n\t" \
  "s_cbranch_scc1 .Lcnx_c35\n\t" \
  "s_mov_b32 m0, 163632\n\t" \
  "s_add_u32 s30, s30, s31\n\t" \
  "buffer_load_dword v252, s[8:11], s30 offen lds\n\t" \
  ".Lcnx_c35:\n\t" \
  "ds_read_b32 v226, v226 offset:15376\n\t" \
  "v_and_b32_sdwa v229, s56, v228 dst_sel:DWORD dst_unused:UNUSED_PAD src0_sel:DWORD src1_sel:WORD_1\n\t" \
  "v_and_b32 v228, s56, v228\n\t" \
  "s_waitcnt lgkmcnt(12)\n\t" \
  "v_mfma_f32_16x16x32_bf16 v[0:3], v[208:211], v[178:181], v[0:3]\n\t" \
  "ds_read_b32 v229, v229 offset:0\n\t" \
  "v_lshlrev_b32_sdwa v231, 2, v230 dst_sel:DWORD dst_unused:UNUSED_PAD src0_sel:DWORD src1_sel:BYTE_3\n\t" \
  "v_lshlrev_b32_sdwa v230, 2, v230 dst_sel:DWORD dst_unused:UNUSED_PAD src0_sel:DWORD src1_sel:BYTE_1\n\t" \
  "v_mfma_f32_16x16x32_bf16 v[24:27], v[208:211], v[186:189], v[24:27]\n\t" \
  "ds_read_b32 v228, v228 offset:0\n\t" \
  "v_and_b32_sdwa v233, s56, v232 dst_sel:DWORD dst_unused:UNUSED_PAD src0_sel:DWORD src1_sel:WORD_1\n\t" \
  "v_and_b32 v232, s56, v232\n\t" \
  "v_mfma_f32_16x16x32_bf16 v[48:51], v[208:211], v[194:197], v[48:51]\n\t" \
  "ds_read_b32 v231, v231 offset:15376\n\t" \
  "v_and_b32_sdwa v235, s56, v234 dst_sel:DWORD dst_unused:UNUSED_PAD src0_sel:DWORD src1_sel:WORD_1\n\t" \
  "v_and_b32 v234, s56, v234\n\t" \
  "v_mfma_f32_16x16x32_bf16 v[72:75], v[208:211], v[202:205], v[72:75]\n\t" \
  "ds_read_b64_tr_b16 v[208:209], v249 offset:30824\n\t" \
  "ds_read_b64_tr_b16 v[210:211], v249 offset:32360\n\t" \
  "s_waitcnt lgkmcnt(13)\n\t" \
  "v_fma_f32 v147, |v147|, v221, v147 mul:4\n\t" \
  "v_fma_f32 v146, |v146|, v220, v146 mul:4\n\t" \
  "s_waitcnt lgkmcnt(11)\n\t" \
  "v_mfma_f32_16x16x32_bf16 v[4:7], v[212:215], v[178:181], v[4:7]\n\t" \
  "ds_read_b32 v230, v230 offset:15376\n\t" \
  "v_fma_mix_f32 v223, |v145|, v223, v223 op_sel:[0,1,0] op_sel_hi:[0,1,1] clamp\n\t" \
  "v_fma_mix_f32 v222, |v144|, v222, v222 op_sel:[0,1,0] op_sel_hi:[0,1,1] clamp\n\t" \
  "v_mfma_f32_16x16x32_bf16 v[28:31], v[212:215], v[186:189], v[28:31]\n\t" \
  "ds_read_b32 v233, v233 offset:0\n\t" \
  "s_waitcnt lgkmcnt(7)\n\t" \
  "v_fma_f32 v155, |v155|, v225, v155 mul:4\n\t" \
  "v_fma_f32 v154, |v154|, v224, v154 mul:4\n\t" \
  "v_mfma_f32_16x16x32_bf16 v[52:55], v[212:215], v[194:197], v[52:55]\n\t" \
  "ds_read_b32 v232, v232 offset:0\n\t" \
  "v_fma_mix_f32 v227, |v153|, v227, v227 op_sel:[0,1,0] op_sel_hi:[0,1,1] clamp\n\t" \
  "v_fma_mix_f32 v226, |v152|, v226, v226 op_sel:[0,1,0] op_sel_hi:[0,1,1] clamp\n\t" \
  "v_fma_f32 v145, |v145|, v223, v145 mul:4\n\t" \
  "v_mfma_f32_16x16x32_bf16 v[76:79], v[212:215], v[202:205], v[76:79]\n\t" \
  "ds_read_b64_tr_b16 v[212:213], v249 offset:30880\n\t" \
  "v_fma_f32 v144, |v144|, v222, v144 mul:4\n\t" \
  "v_fma_f32 v153, |v153|, v227, v153 mul:4\n\t" \
  "v_mfma_f32_16x16x32_bf16 v[8:11], v[216:219], v[178:181], v[8:11]\n\t" \
  "ds_read_b64_tr_b16 v[214:215], v249 offset:32416\n\t" \
  "v_fma_f32 v152, |v152|, v226, v152 mul:4\n\t" \
  "v_cvt_pk_bf16_f32 v147, v146, v147\n\t" \
  "v_mfma_f32_16x16x32_bf16 v[32:35], v[216:219], v[186:189], v[32:35]\n\t" \
  "ds_read_b32 v235, v235 offset:0\n\t" \
  "v_cvt_pk_bf16_f32 v146, v144, v145\n\t" \
  "v_cvt_pk_bf16_f32 v155, v154, v155\n\t" \
  "v_mfma_f32_16x16x32_bf16 v[56:59], v[216:219], v[194:197], v[56:59]\n\t" \
  "ds_read_b32 v234, v234 offset:0\n\t" \
  "v_cvt_pk_bf16_f32 v154, v152, v153\n\t" \
  "s_cbranch_vccnz .Lcnx_p11_1\n\ts_setprio 1\n.Lcnx_p11_1:\n\t" \
  "v_cvt_pk_f16_f32 v220, |v148|, |v149| clamp\n\t" \
  "v_mfma_f32_16x16x32_bf16 v[80:83], v[216:219], v[202:205], v[80:83]\n\t" \
  "ds_read_b64_tr_b16 v[216:217], v249 offset:30888\n\t" \
  "v_cvt_pknorm_i16_f32 v222, |v150|, |v151|\n\t" \
  "v_cvt_pk_f16_f32 v224, |v156|, |v157| clamp\n\t" \
  "s_waitcnt lgkmcnt(8)\n\t" \
  "v_mfma_f32_16x16x32_bf16 v[12:15], v[208:211], v[178:181], v[12:15]\n\t" \
  "ds_read_b64_tr_b16 v[218:219], v249 offset:32424\n\t" \
  "v_cvt_pknorm_i16_f32 v226, |v158|, |v159|\n\t" \
  "v_and_b32_sdwa v221, s56, v220 dst_sel:DWORD dst_unused:UNUSED_PAD src0_sel:DWORD src1_sel:WORD_1\n\t" \
  "v_mfma_f32_16x16x32_bf16 v[36:39], v[208:211], v[186:189], v[36:39]\n\t" \
  "v_and_b32 v220, s56, v220\n\t" \
  "v_lshlrev_b32_sdwa v223, 2, v222 dst_sel:DWORD dst_unused:UNUSED_PAD src0_sel:DWORD src1_sel:BYTE_3\n\t" \
  "v_mfma_f32_16x16x32_bf16 v[60:63], v[208:211], v[194:197], v[60:63]\n\t" \
  "ds_read_b32 v221, v221 offset:0\n\t" \
  "v_lshlrev_b32_sdwa v222, 2, v222 dst_sel:DWORD dst_unused:UNUSED_PAD src0_sel:DWORD src1_sel:BYTE_1\n\t" \
  "v_and_b32_sdwa v225, s56, v224 dst_sel:DWORD dst_unused:UNUSED_PAD src0_sel:DWORD src1_sel:WORD_1\n\t" \
  "v_and_b32 v224, s56, v224\n\t" \
  "v_mfma_f32_16x16x32_bf16 v[84:87], v[208:211], v[202:205], v[84:87]\n\t" \
  "ds_read_b64_tr_b16 v[208:209], v248 offset:12304\n\t" \
  "ds_read_b64_tr_b16 v[210:211], v248 offset:13328\n\t" \
  "v_lshlrev_b32_sdwa v227, 2, v226 dst_sel:DWORD dst_unused:UNUSED_PAD src0_sel:DWORD src1_sel:BYTE_3\n\t" \
  "v_lshlrev_b32_sdwa v226, 2, v226 dst_sel:DWORD dst_unused:UNUSED_PAD src0_sel:DWORD src1_sel:BYTE_1\n\t" \
  "s_waitcnt lgkmcnt(7)\n\t" \
  "v_mfma_f32_16x16x32_bf16 v[16:19], v[212:215], v[178:181], v[16:19]\n\t" \
  "ds_read_b32 v220, v220 offset:0\n\t" \
  "ds_read_b32 v223, v223 offset:15376\n\t" \
  "v_fma_f32 v163, |v163|, v229, v163 mul:4\n\t" \
  "v_fma_f32 v162, |v162|, v228, v162 mul:4\n\t" \
  "v_mfma_f32_16x16x32_bf16 v[40:43], v[212:215], v[186:189], v[40:43]\n\t" \
  "ds_read_b32 v222, v222 offset:15376\n\t" \
  "v_fma_mix_f32 v231, |v161|, v231, v231 op_sel:[0,1,0] op_sel_hi:[0,1,1] clamp\n\t" \
  "v_fma_mix_f32 v230, |v160|, v230, v230 op_sel:[0,1,0] op_sel_hi:[0,1,1] clamp\n\t" \
  "v_mfma_f32_16x16x32_bf16 v[64:67], v[212:215], v[194:197], v[64:67]\n\t" \
  "ds_read_b32 v225, v225 offset:0\n\t" \
  "s_waitcnt lgkmcnt(9)\n\t" \
  "v_fma_f32 v171, |v171|, v233, v171 mul:4\n\t" \
  "v_fma_f32 v170, |v170|, v232, v170 mul:4\n\t" \
  "v_mfma_f32_16x16x32_bf16 v[88:91], v[212:215], v[202:205], v[88:91]\n\t" \
  "ds_read_b64_tr_b16 v[212:213], v248 offset:15376\n\t" \
  "v_fma_f32 v169, |v169|, v235, v169 mul:4\n\t" \
  "v_fma_f32 v168, |v168|, v234, v168 mul:4\n\t" \
  "s_waitcnt lgkmcnt(8)\n\t" \
  "v_mfma_f32_16x16x32_bf16 v[20:23], v[216:219], v[178:181], v[20:23]\n\t" \
  "ds_read_b64_tr_b16 v[214:215], v248 offset:16400\n\t" \
  "v_fma_f32 v161, |v161|, v231, v161 mul:4\n\t" \
  "v_fma_f32 v160, |v160|, v230, v160 mul:4\n\t" \
  "v_mfma_f32_16x16x32_bf16 v[44:47], v[216:219], v[186:189], v[44:47]\n\t" \
  "ds_read_b32 v224, v224 offset:0\n\t" \
  "v_cvt_pk_bf16_f32 v163, v162, v163\n\t" \
  "v_cvt_pk_bf16_f32 v162, v160, v161\n\t" \
  "v_mfma_f32_16x16x32_bf16 v[68:71], v[216:219], v[194:197], v[68:71]\n\t" \
  "ds_read_b32 v227, v227 offset:15376\n\t" \
  "v_cvt_pk_bf16_f32 v171, v170, v171\n\t" \
  "v_cvt_pk_bf16_f32 v170, v168, v169\n\t" \
  "v_cvt_pk_f16_f32 v228, |v164|, |v165| clamp\n\t" \
  "v_mfma_f32_16x16x32_bf16 v[92:95], v[216:219], v[202:205], v[92:95]\n\t" \
  "ds_read_b64_tr_b16 v[216:217], v248 offset:14352\n\t" \
  "v_cvt_pknorm_i16_f32 v230, |v166|, |v167|\n\t" \
  "s_cbranch_vccnz .Lcnx_p11_2\n\ts_setprio 2\n.Lcnx_p11_2:\n\t" \
  "v_cvt_pk_f16_f32 v232, |v172|, |v173| clamp\n\t" \
  "s_waitcnt lgkmcnt(9)\n\t" \
  "v_mfma_f32_16x16x32_bf16 v[176:179], v[208:211], v[96:99], 0\n\t" \
  "ds_read_b64_tr_b16 v[218:219], v248 offset:17424\n\t" \
  "v_cvt_pk_f16_f32 v234, |v174|, |v175| clamp\n\t" \
  "v_and_b32_sdwa v229, s56, v228 dst_sel:DWORD dst_unused:UNUSED_PAD src0_sel:DWORD src1_sel:WORD_1\n\t" \
  "v_mfma_f32_16x16x32_bf16 v[184:187], v[208:211], v[108:111], 0\n\t" \
  "ds_read_b32 v226, v226 offset:15376\n\t" \
  "v_and_b32 v228, s56, v228\n\t" \
  "v_lshlrev_b32_sdwa v231, 2, v230 dst_sel:DWORD dst_unused:UNUSED_PAD src0_sel:DWORD src1_sel:BYTE_3\n\t" \
  "v_mfma_f32_16x16x32_bf16 v[192:195], v[208:211], v[120:123], 0\n\t" \
  "ds_read_b32 v229, v229 offset:0\n\t" \
  "v_lshlrev_b32_sdwa v230, 2, v230 dst_sel:DWORD dst_unused:UNUSED_PAD src0_sel:DWORD src1_sel:BYTE_1\n\t" \
  "v_and_b32_sdwa v233, s56, v232 dst_sel:DWORD dst_unused:UNUSED_PAD src0_sel:DWORD src1_sel:WORD_1\n\t" \
  "v_mfma_f32_16x16x32_bf16 v[200:203], v[208:211], v[132:135], 0\n\t" \
  "ds_read_b64_tr_b16 v[208:209], v248 offset:12816\n\t" \
  "v_and_b32 v232, s56, v232\n\t" \
  "v_and_b32_sdwa v235, s56, v234 dst_sel:DWORD dst_unused:UNUSED_PAD src0_sel:DWORD src1_sel:WORD_1\n\t" \
  "s_waitcnt lgkmcnt(7)\n\t" \
  "v_mfma_f32_16x16x32_bf16 v[176:179], v[212:215], v[100:103], v[176:179]\n\t" \
  "ds_read_b64_tr_b16 v[210:211], v248 offset:13840\n\t" \
  "ds_read_b32 v228, v228 offset:0\n\t" \
  "v_and_b32 v234, s56, v234\n\t" \
  "v_fma_f32 v149, |v149|, v221, v149 mul:4\n\t" \
  "v_mfma_f32_16x16x32_bf16 v[184:187], v[212:215], v[112:115], v[184:187]\n\t" \
  "ds_read_b32 v231, v231 offset:15376\n\t" \
  "ds_read_b32 v230, v230 offset:15376\n\t" \
  "v_fma_f32 v148, |v148|, v220, v148 mul:4\n\t" \
  "v_fma_mix_f32 v223, |v151|, v223, v223 op_sel:[0,1,0] op_sel_hi:[0,1,1] clamp\n\t" \
  "v_mfma_f32_16x16x32_bf16 v[192:195], v[212:215], v[124:127], v[192:195]\n\t" \
  "ds_read_b32 v233, v233 offset:0\n\t" \
  "v_fma_mix_f32 v222, |v150|, v222, v222 op_sel:[0,1,0] op_sel_hi:[0,1,1] clamp\n\t" \
  "s_waitcnt lgkmcnt(7)\n\t" \
  "v_fma_f32 v157, |v157|, v225, v157 mul:4\n\t" \
  "v_mfma_f32_16x16x32_bf16 v[200:203], v[212:215], v[136:139], v[200:203]\n\t" \
  "ds_read_b64_tr_b16 v[212:213], v248 offset:15888\n\t" \
  "v_fma_f32 v156, |v156|, v224, v156 mul:4\n\t" \
  "v_fma_mix_f32 v227, |v159|, v227, v227 op_sel:[0,1,0] op_sel_hi:[0,1,1] clamp\n\t" \
  "v_fma_mix_f32 v226, |v158|, v226, v226 op_sel:[0,1,0] op_sel_hi:[0,1,1] clamp\n\t" \
  "v_mfma_f32_16x16x32_bf16 v[176:179], v[216:219], v[104:107], v[176:179]\n\t" \
  "ds_read_b64_tr_b16 v[214:215], v248 offset:16912\n\t" \
  "v_fma_f32 v151, |v151|, v223, v151 mul:4\n\t" \
  "v_fma_f32 v150, |v150|, v222, v150 mul:4\n\t" \
  "v_mfma_f32_16x16x32_bf16 v[184:187], v[216:219], v[116:119], v[184:187]\n\t" \
  "ds_read_b32 v232, v232 offset:0\n\t" \
  "v_fma_f32 v159, |v159|, v227, v159 mul:4\n\t" \
  "v_fma_f32 v158, |v158|, v226, v158 mul:4\n\t" \
  "v_mfma_f32_16x16x32_bf16 v[192:195], v[216:219], v[128:131], v[192:195]\n\t" \
  "ds_read_b32 v235, v235 offset:0\n\t" \
  "v_cvt_pk_bf16_f32 v148, v148, v149\n\t" \
  "v_cvt_pk_bf16_f32 v149, v150, v151\n\t" \
  "v_mfma_f32_16x16x32_bf16 v[200:203], v[216:219], v[140:143], v[200:203]\n\t" \
  "ds_read_b64_tr_b16 v[216:217], v248 offset:14864\n\t" \
  "v_cvt_pk_bf16_f32 v156, v156, v157\n\t" \
  "v_cvt_pk_bf16_f32 v157, v158, v159\n\t" \
  "s_waitcnt lgkmcnt(9)\n\t" \
  "v_mfma_f32_16x16x32_bf16 v[180:183], v[208:211], v[96:99], 0\n\t" \
  "ds_read_b64_tr_b16 v[218:219], v248 offset:17936\n\t" \
  "v_cvt_pk_f16_f32 v220, |v178|, |v179| clamp\n\t" \
  "s_cbranch_vccnz .Lcnx_p11_3\n\ts_setprio 3\n.Lcnx_p11_3:\n\t" \
  "v_cvt_pknorm_i16_f32 v222, |v176|, |v177|\n\t" \
  "v_mfma_f32_16x16x32_bf16 v[188:191], v[208:211], v[108:111], 0\n\t" \
  "s_waitcnt vmcnt(0)\n\t" \
  "s_mov_b64 exec, 3\n\t" \
  "ds_add_rtn_u32 v246, v251, v250 offset:47232\n\t" \
  "s_mov_b64 exec, -1\n\t" \
  "ds_read_b32 v234, v234 offset:0\n\t" \
  "v_cvt_pk_f16_f32 v224, |v186|, |v187| clamp\n\t" \
  "v_cvt_pknorm_i16_f32 v226, |v184|, |v185|\n\t" \
  "v_mfma_f32_16x16x32_bf16 v[196:199], v[208:211], v[120:123], 0\n\t" \
  "v_and_b32_sdwa v221, s56, v220 dst_sel:DWORD dst_unused:UNUSED_PAD src0_sel:DWORD src1_sel:WORD_1\n\t" \
  "v_and_b32 v220, s56, v220\n\t" \
  "v_mfma_f32_16x16x32_bf16 v[204:207], v[208:211], v[132:135], 0\n\t" \
  "s_waitcnt lgkmcnt(1)\n\t" \
  "v_readfirstlane_b32 s4, v246\n\t" \
  "s_cmp_lt_u32 s4, s57\n\t" \
  "s_cbranch_scc1 .Lcnx_w132\n.Lcnx_b132:\n\t" \
  "ds_read_b64_tr_b16 v[208:209], v249 offset:0\n\t" \
  "v_lshlrev_b32_sdwa v223, 2, v222 dst_sel:DWORD dst_unused:UNUSED_PAD src0_sel:DWORD src1_sel:BYTE_3\n\t" \
  "v_lshlrev_b32_sdwa v222, 2, v222 dst_sel:DWORD dst_unused:UNUSED_PAD src0_sel:DWORD src1_sel:BYTE_1\n\t" \
  "v_and_b32_sdwa v225, s56, v224 dst_sel:DWORD dst_unused:UNUSED_PAD src0_sel:DWORD src1_sel:WORD_1\n\t" \
  "v_mfma_f32_16x16x32_bf16 v[180:183], v[212:215], v[100:103], v[180:183]\n\t" \
  "ds_read_b64_tr_b16 v[210:211], v249 offset:1536\n\t" \
  "v_and_b32 v224, s56, v224\n\t" \
  "v_lshlrev_b32_sdwa v227, 2, v226 dst_sel:DWORD dst_unused:UNUSED_PAD src0_sel:DWORD src1_sel:BYTE_3\n\t" \
  "v_mfma_f32_16x16x32_bf16 v[188:191], v[212:215], v[112:115], v[188:191]\n\t" \
  "ds_read_b32 v221, v221 offset:0\n\t" \
  "ds_read_b32 v220, v220 offset:0\n\t" \
  "v_lshlrev_b32_sdwa v226, 2, v226 dst_sel:DWORD dst_unused:UNUSED_PAD src0_sel:DWORD src1_sel:BYTE_1\n\t" \
  "v_fma_f32 v165, |v165|, v229, v165 mul:4\n\t" \
  "v_mfma_f32_16x16x32_bf16 v[196:199], v[212:215], v[124:127], v[196:199]\n\t" \
  "ds_read_b32 v223, v223 offset:15376\n\t" \
  "ds_read_b32 v222, v222 offset:15376\n\t" \
  "v_fma_f32 v164, |v164|, v228, v164 mul:4\n\t" \
  "v_fma_mix_f32 v231, |v167|, v231, v231 op_sel:[0,1,0] op_sel_hi:[0,1,1] clamp\n\t" \
  "v_mfma_f32_16x16x32_bf16 v[204:207], v[212:215], v[136:139], v[204:207]\n\t" \
  "ds_read_b64_tr_b16 v[212:213], v249 offset:8\n\t" \
  "ds_read_b64_tr_b16 v[214:215], v249 offset:1544\n\t" \
  "v_fma_mix_f32 v230, |v166|, v230, v230 op_sel:[0,1,0] op_sel_hi:[0,1,1] clamp\n\t" \
  "s_waitcnt lgkmcnt(8)\n\t" \
  "v_fma_f32 v173, |v173|, v233, v173 mul:4\n\t" \
  "v_mfma_f32_16x16x32_bf16 v[180:183], v[216:219], v[104:107], v[180:183]\n\t" \
  "ds_read_b32 v225, v225 offset:0\n\t" \
  "v_fma_f32 v172, |v172|, v232, v172 mul:4\n\t" \
  "v_fma_f32 v175, |v175|, v235, v175 mul:4\n\t" \
  "v_mfma_f32_16x16x32_bf16 v[188:191], v[216:219], v[116:119], v[188:191]\n\t" \
  "ds_read_b32 v224, v224 offset:0\n\t" \
  "v_fma_f32 v174, |v174|, v234, v174 mul:4\n\t" \
  "v_fma_f32 v167, |v167|, v231, v167 mul:4\n\t" \
  "v_mfma_f32_16x16x32_bf16 v[196:199], v[216:219], v[128:131], v[196:199]\n\t" \
  "ds_read_b32 v227, v227 offset:15376\n\t" \
  "v_fma_f32 v166, |v166|, v230, v166 mul:4\n\t" \
  "v_cvt_pk_bf16_f32 v164, v164, v165\n\t" \
  "v_mfma_f32_16x16x32_bf16 v[204:207], v[216:219], v[140:143], v[204:207]\n\t" \
  "ds_read_b64_tr_b16 v[216:217], v249 offset:64\n\t" \
  "v_cvt_pk_bf16_f32 v165, v166, v167\n\t" \
  "v_cvt_pk_bf16_f32 v172, v172, v173\n\t" \
  "v_cvt_pk_bf16_f32 v173, v174, v175\n\t" \
  "s_cbranch_vccnz .Lcnx_p12_0\n\ts_setprio 0\n.Lcnx_p12_0:\n\t" \
  "v_cvt_pk_f16_f32 v228, |v194|, |v195| clamp\n\t" \
  "v_cvt_pknorm_i16_f32 v230, |v192|, |v193|\n\t" \
  "v_mfma_f32_16x16x32_bf16 v[80:83], v[242:245], v[236:239], v[80:83]\n\t" \
  "ds_read_b64_tr_b16 v[218:219], v249 offset:1600\n\t" \
  "v_cvt_pk_f16_f32 v232, |v202|, |v203| clamp\n\t" \
  "v_cvt_pk_f16_f32 v234, |v200|, |v201| clamp\n\t" \
  "v_and_b32_sdwa v229, s56, v228 dst_sel:DWORD dst_unused:UNUSED_PAD src0_sel:DWORD src1_sel:WORD_1\n\t" \
  "v_mfma_f32_16x16x32_bf16 v[84:87], v[240:243], v[236:239], v[84:87]\n\t" \
  "buffer_load_dwordx4 v[236:239], v250, s[8:11], s29 offen offset:320\n\t" \
  "s_cmp_lg_u32 s38, 4\n\t" \
  "s_cbranch_scc1 .Lcnx_c36\n\t" \
  "s_mov_b32 m0, 163632\n\t" \
  "s_add_u32 s30, s30, s31\n\t" \
  "buffer_load_dword v252, s[8:11], s30 offen lds\n\t" \
  ".Lcnx_c36:\n\t" \
  "ds_read_b32 v226, v226 offset:15376\n\t" \
  "v_and_b32 v228, s56, v228\n\t" \
  "v_lshlrev_b32_sdwa v231, 2, v230 dst_sel:DWORD dst_unused:UNUSED_PAD src0_sel:DWORD src1_sel:BYTE_3\n\t" \
  "v_lshlrev_b32_sdwa v230, 2, v230 dst_sel:DWORD dst_unused:UNUSED_PAD src0_sel:DWORD src1_sel:BYTE_1\n\t" \
  "v_and_b32_sdwa v233, s56, v232 dst_sel:DWORD dst_unused:UNUSED_PAD src0_sel:DWORD src1_sel:WORD_1\n\t" \
  "s_waitcnt lgkmcnt(12)\n\t" \
  "v_mfma_f32_16x16x32_bf16 v[0:3], v[208:211], v[146:149], v[0:3]\n\t" \
  "ds_read_b32 v229, v229 offset:0\n\t" \
  "v_and_b32 v232, s56, v232\n\t" \
  "v_and_b32_sdwa v235, s56, v234 dst_sel:DWORD dst_unused:UNUSED_PAD src0_sel:DWORD src1_sel:WORD_1\n\t" \
  "v_and_b32 v234, s56, v234\n\t" \
  "v_mfma_f32_16x16x32_bf16 v[24:27], v[208:211], v[154:157], v[24:27]\n\t" \
  "ds_read_b32 v228, v228 offset:0\n\t" \
  "ds_read_b32 v231, v231 offset:15376\n\t" \
  "s_waitcnt lgkmcnt(11)\n\t" \
  "v_fma_f32 v179, |v179|, v221, v179 mul:4\n\t" \
  "v_fma_f32 v178, |v178|, v220, v178 mul:4\n\t" \
  "v_fma_mix_f32 v223, |v177|, v223, v223 op_sel:[0,1,0] op_sel_hi:[0,1,1] clamp\n\t" \
  "v_fma_mix_f32 v222, |v176|, v222, v222 op_sel:[0,1,0] op_sel_hi:[0,1,1] clamp\n\t" \
  "v_mfma_f32_16x16x32_bf16 v[48:51], v[208:211], v[162:165], v[48:51]\n\t" \
  "ds_read_b32 v230, v230 offset:15376\n\t" \
  "s_waitcnt lgkmcnt(4)\n\t" \
  "v_fma_f32 v187, |v187|, v225, v187 mul:4\n\t" \
  "v_fma_f32 v186, |v186|, v224, v186 mul:4\n\t" \
  "v_fma_mix_f32 v227, |v185|, v227, v227 op_sel:[0,1,0] op_sel_hi:[0,1,1] clamp\n\t" \
  "v_fma_mix_f32 v226, |v184|, v226, v226 op_sel:[0,1,0] op_sel_hi:[0,1,1] clamp\n\t" \
  "v_mfma_f32_16x16x32_bf16 v[72:75], v[208:211], v[170:173], v[72:75]\n\t" \
  "ds_read_b64_tr_b16 v[208:209], v249 offset:72\n\t" \
  "ds_read_b64_tr_b16 v[210:211], v249 offset:1608\n\t" \
  "v_fma_f32 v177, |v177|, v223, v177 mul:4\n\t" \
  "v_fma_f32 v176, |v176|, v222, v176 mul:4\n\t" \
  "v_fma_f32 v185, |v185|, v227, v185 mul:4\n\t" \
  "v_mfma_f32_16x16x32_bf16 v[4:7], v[212:215], v[146:149], v[4:7]\n\t" \
  "ds_read_b32 v233, v233 offset:0\n\t" \
  "v_fma_f32 v184, |v184|, v226, v184 mul:4\n\t" \
  "v_cvt_pk_bf16_f32 v179, v178, v179\n\t" \
  "s_cbranch_vccnz .Lcnx_p12_1\n\ts_setprio 1\n.Lcnx_p12_1:\n\t" \
  "v_cvt_pk_bf16_f32 v178, v176, v177\n\t" \
  "v_cvt_pk_bf16_f32 v187, v186, v187\n\t" \
  "v_mfma_f32_16x16x32_bf16 v[28:31], v[212:215], v[154:157], v[28:31]\n\t" \
  "ds_read_b32 v232, v232 offset:0\n\t" \
  "v_cvt_pk_bf16_f32 v186, v184, v185\n\t" \
  "v_cvt_pk_f16_f32 v220, |v180|, |v181| clamp\n\t" \
  "v_cvt_pknorm_i16_f32 v222, |v182|, |v183|\n\t" \
  "v_mfma_f32_16x16x32_bf16 v[52:55], v[212:215], v[162:165], v[52:55]\n\t" \
  "ds_read_b32 v235, v235 offset:0\n\t" \
  "v_cvt_pk_f16_f32 v224, |v188|, |v189| clamp\n\t" \
  "v_cvt_pknorm_i16_f32 v226, |v190|, |v191|\n\t" \
  "v_and_b32_sdwa v221, s56, v220 dst_sel:DWORD dst_unused:UNUSED_PAD src0_sel:DWORD src1_sel:WORD_1\n\t" \
  "v_and_b32 v220, s56, v220\n\t" \
  "v_mfma_f32_16x16x32_bf16 v[76:79], v[212:215], v[170:173], v[76:79]\n\t" \
  "ds_read_b64_tr_b16 v[212:213], v249 offset:128\n\t" \
  "v_lshlrev_b32_sdwa v223, 2, v222 dst_sel:DWORD dst_unused:UNUSED_PAD src0_sel:DWORD src1_sel:BYTE_3\n\t" \
  "v_lshlrev_b32_sdwa v222, 2, v222 dst_sel:DWORD dst_unused:UNUSED_PAD src0_sel:DWORD src1_sel:BYTE_1\n\t" \
  "v_and_b32_sdwa v225, s56, v224 dst_sel:DWORD dst_unused:UNUSED_PAD src0_sel:DWORD src1_sel:WORD_1\n\t" \
  "v_and_b32 v224, s56, v224\n\t" \
  "v_mfma_f32_16x16x32_bf16 v[8:11], v[216:219], v[146:149], v[8:11]\n\t" \
  "ds_read_b64_tr_b16 v[214:215], v249 offset:1664\n\t" \
  "ds_read_b32 v234, v234 offset:0\n\t" \
  "v_lshlrev_b32_sdwa v227, 2, v226 dst_sel:DWORD dst_unused:UNUSED_PAD src0_sel:DWORD src1_sel:BYTE_3\n\t" \
  "v_lshlrev_b32_sdwa v226, 2, v226 dst_sel:DWORD dst_unused:UNUSED_PAD src0_sel:DWORD src1_sel:BYTE_1\n\t" \
  "s_waitcnt lgkmcnt(8)\n\t" \
  "v_fma_f32 v195, |v195|, v229, v195 mul:4\n\t" \
  "v_mfma_f32_16x16x32_bf16 v[32:35], v[216:219], v[154:157], v[32:35]\n\t" \
  "ds_read_b32 v221, v221 offset:0\n\t" \
  "ds_read_b32 v220, v220 offset:0\n\t" \
  "v_fma_f32 v194, |v194|, v228, v194 mul:4\n\t" \
  "v_fma_mix_f32 v231, |v193|, v231, v231 op_sel:[0,1,0] op_sel_hi:[0,1,1] clamp\n\t" \
  "v_fma_mix_f32 v230, |v192|, v230, v230 op_sel:[0,1,0] op_sel_hi:[0,1,1] clamp\n\t" \
  "s_waitcnt lgkmcnt(2)\n\t" \
  "v_fma_f32 v203, |v203|, v233, v203 mul:4\n\t" \
  "v_mfma_f32_16x16x32_bf16 v[56:59], v[216:219], v[162:165], v[56:59]\n\t" \
  "ds_read_b32 v223, v223 offset:15376\n\t" \
  "ds_read_b32 v222, v222 offset:15376\n\t" \
  "v_fma_f32 v202, |v202|, v232, v202 mul:4\n\t" \
  "v_fma_f32 v201, |v201|, v235, v201 mul:4\n\t" \
  "v_fma_f32 v200, |v200|, v234, v200 mul:4\n\t" \
  "v_fma_f32 v193, |v193|, v231, v193 mul:4\n\t" \
  "v_mfma_f32_16x16x32_bf16 v[80:83], v[216:219], v[170:173], v[80:83]\n\t" \
  "ds_read_b64_tr_b16 v[216:217], v249 offset:136\n\t" \
  "ds_read_b64_tr_b16 v[218:219], v249 offset:1672\n\t" \
  "s_cbranch_vccnz .Lcnx_p12_2\n\ts_setprio 2\n.Lcnx_p12_2:\n\t" \
  "v_fma_f32 v192, |v192|, v230, v192 mul:4\n\t" \
  "v_cvt_pk_bf16_f32 v195, v194, v195\n\t" \
  "v_cvt_pk_bf16_f32 v194, v192, v193\n\t" \
  "v_mfma_f32_16x16x32_bf16 v[12:15], v[208:211], v[146:149], v[12:15]\n\t" \
  "ds_read_b32 v225, v225 offset:0\n\t" \
  "v_cvt_pk_bf16_f32 v203, v202, v203\n\t" \
  "v_cvt_pk_bf16_f32 v202, v200, v201\n\t" \
  "v_cvt_pk_f16_f32 v228, |v196|, |v197| clamp\n\t" \
  "v_cvt_pknorm_i16_f32 v230, |v198|, |v199|\n\t" \
  "v_mfma_f32_16x16x32_bf16 v[36:39], v[208:211], v[154:157], v[36:39]\n\t" \
  "s_waitcnt vmcnt(0)\n\t" \
  "s_mov_b64 exec, 3\n\t" \
  "ds_add_rtn_u32 v246, v251, v250 offset:47240\n\t" \
  "s_mov_b64 exec, -1\n\t" \
  "ds_read_b32 v224, v224 offset:0\n\t" \
  "v_cvt_pk_f16_f32 v232, |v204|, |v205| clamp\n\t" \
  "v_cvt_pk_f16_f32 v234, |v206|, |v207| clamp\n\t" \
  "v_and_b32_sdwa v229, s56, v228 dst_sel:DWORD dst_unused:UNUSED_PAD src0_sel:DWORD src1_sel:WORD_1\n\t" \
  "v_mfma_f32_16x16x32_bf16 v[60:63], v[208:211], v[162:165], v[60:63]\n\t" \
  "ds_read_b32 v227, v227 offset:15376\n\t" \
  "v_and_b32 v228, s56, v228\n\t" \
  "v_lshlrev_b32_sdwa v231, 2, v230 dst_sel:DWORD dst_unused:UNUSED_PAD src0_sel:DWORD src1_sel:BYTE_3\n\t" \
  "v_lshlrev_b32_sdwa v230, 2, v230 dst_sel:DWORD dst_unused:UNUSED_PAD src0_sel:DWORD src1_sel:BYTE_1\n\t" \
  "v_and_b32_sdwa v233, s56, v232 dst_sel:DWORD dst_unused:UNUSED_PAD src0_sel:DWORD src1_sel:WORD_1\n\t" \
  "v_mfma_f32_16x16x32_bf16 v[84:87], v[208:211], v[170:173], v[84:87]\n\t" \
  "s_waitcnt lgkmcnt(2)\n\t" \
  "v_readfirstlane_b32 s4, v246\n\t" \
  "s_cmp_lt_u32 s4, s57\n\t" \
  "s_cbranch_scc1 .Lcnx_w138\n.Lcnx_b138:\n\t" \
  "ds_read_b64_tr_b16 v[208:209], v249 offset:6152\n\t" \
  "ds_read_b64_tr_b16 v[210:211], v249 offset:7688\n\t" \
  "v_and_b32 v232, s56, v232\n\t" \
  "v_and_b32_sdwa v235, s56, v234 dst_sel:DWORD dst_unused:UNUSED_PAD src0_sel:DWORD src1_sel:WORD_1\n\t" \
  "v_and_b32 v234, s56, v234\n\t" \
  "ds_read_b32 v226, v226 offset:15376\n\t" \
  "ds_read_b32 v229, v229 offset:0\n\t" \
  "ds_read_b32 v228, v228 offset:0\n\t" \
  "ds_read_b32 v231, v231 offset:15376\n\t" \
  "ds_read_b32 v230, v230 offset:15376\n\t" \
  "ds_read_b32 v233, v233 offset:0\n\t" \
  "ds_read_b32 v232, v232 offset:0\n\t" \
  "ds_read_b32 v235, v235 offset:0\n\t" \
  "v_fma_f32 v181, |v181|, v221, v181 mul:4\n\t" \
  "s_cbranch_vccnz .Lcnx_p12_3\n\ts_setprio 3\n.Lcnx_p12_3:\n\t" \
  "v_mfma_f32_16x16x32_bf16 v[16:19], v[212:215], v[146:149], v[16:19]\n\t" \
  "ds_read_b32 v234, v234 offset:0\n\t" \
  "v_fma_f32 v180, |v180|, v220, v180 mul:4\n\t" \
  "v_fma_mix_f32 v223, |v183|, v223, v223 op_sel:[0,1,0] op_sel_hi:[0,1,1] clamp\n\t" \
  "v_fma_mix_f32 v222, |v182|, v222, v222 op_sel:[0,1,0] op_sel_hi:[0,1,1] clamp\n\t" \
  "v_mfma_f32_16x16x32_bf16 v[40:43], v[212:215], v[154:157], v[40:43]\n\t" \
  "s_waitcnt lgkmcnt(8)\n\t" \
  "v_fma_f32 v189, |v189|, v225, v189 mul:4\n\t" \
  "v_fma_f32 v188, |v188|, v224, v188 mul:4\n\t" \
  "v_fma_mix_f32 v227, |v191|, v227, v227 op_sel:[0,1,0] op_sel_hi:[0,1,1] clamp\n\t" \
  "v_fma_mix_f32 v226, |v190|, v226, v226 op_sel:[0,1,0] op_sel_hi:[0,1,1] clamp\n\t" \
  "v_mfma_f32_16x16x32_bf16 v[64:67], v[212:215], v[162:165], v[64:67]\n\t" \
  "v_fma_f32 v183, |v183|, v223, v183 mul:4\n\t" \
  "v_fma_f32 v182, |v182|, v222, v182 mul:4\n\t" \
  "v_fma_f32 v191, |v191|, v227, v191 mul:4\n\t" \
  "v_mfma_f32_16x16x32_bf16 v[88:91], v[212:215], v[170:173], v[88:91]\n\t" \
  "ds_read_b64_tr_b16 v[212:213], v249 offset:6160\n\t" \
  "v_fma_f32 v190, |v190|, v226, v190 mul:4\n\t" \
  "v_cvt_pk_bf16_f32 v180, v180, v181\n\t" \
  "v_cvt_pk_bf16_f32 v181, v182, v183\n\t" \
  "v_cvt_pk_bf16_f32 v188, v188, v189\n\t" \
  "v_mfma_f32_16x16x32_bf16 v[20:23], v[216:219], v[146:149], v[20:23]\n\t" \
  "ds_read_b64_tr_b16 v[214:215], v249 offset:7696\n\t" \
  "v_cvt_pk_bf16_f32 v189, v190, v191\n\t" \
  "s_waitcnt lgkmcnt(6)\n\t" \
  "v_fma_f32 v197, |v197|, v229, v197 mul:4\n\t" \
  "v_fma_f32 v196, |v196|, v228, v196 mul:4\n\t" \
  "v_fma_mix_f32 v231, |v199|, v231, v231 op_sel:[0,1,0] op_sel_hi:[0,1,1] clamp\n\t" \
  "v_mfma_f32_16x16x32_bf16 v[44:47], v[216:219], v[154:157], v[44:47]\n\t" \
  "v_fma_mix_f32 v230, |v198|, v230, v230 op_sel:[0,1,0] op_sel_hi:[0,1,1] clamp\n\t" \
  "s_waitcnt lgkmcnt(2)\n\t" \
  "v_fma_f32 v205, |v205|, v233, v205 mul:4\n\t" \
  "v_fma_f32 v204, |v204|, v232, v204 mul:4\n\t" \
  "v_mfma_f32_16x16x32_bf16 v[68:71], v[216:219], v[162:165], v[68:71]\n\t" \
  "v_fma_f32 v207, |v207|, v235, v207 mul:4\n\t" \
  "v_fma_f32 v206, |v206|, v234, v206 mul:4\n\t" \
  "v_fma_f32 v199, |v199|, v231, v199 mul:4\n\t" \
  "v_fma_f32 v198, |v198|, v230, v198 mul:4\n\t" \
  "v_mfma_f32_16x16x32_bf16 v[92:95], v[216:219], v[170:173], v[92:95]\n\t" \
  "ds_read_b64_tr_b16 v[216:217], v249 offset:6216\n\t" \
  "v_cvt_pk_bf16_f32 v196, v196, v197\n\t" \
  "v_cvt_pk_bf16_f32 v197, v198, v199\n\t" \
  "v_cvt_pk_bf16_f32 v204, v204, v205\n\t" \
  "v_cvt_pk_bf16_f32 v205, v206, v207\n\t" \
  "s_cmp_lg_u32 s58, 0\n\t" \
  "s_cbranch_scc1 .Lcnx_fin\n\t" \
  "v_mfma_f32_32x32x16_bf16 v[144:159], v[242:245], v[240:243], 0\n\t" \
  "v_mfma_f32_32x32x16_bf16 v[160:175], v[242:245], v[240:243], 0\n\t" \
  "v_mfma_f32_16x16x32_bf16 v[88:91], v[242:245], v[236:239], v[88:91]\n\t" \
  "ds_read_b64_tr_b16 v[218:219], v249 offset:7752\n\t" \
  "v_mfma_f32_16x16x32_bf16 v[92:95], v[240:243], v[236:239], v[92:95]\n\t" \
  "v_add_u32 v232, 1536, v247\n\t" \
  "v_mfma_f32_16x16x32_bf16 v[0:3], v[208:211], v[178:181], v[0:3]\n\t" \
  "v_mfma_f32_16x16x32_bf16 v[24:27], v[208:211], v[186:189], v[24:27]\n\t" \
  "v_mfma_f32_16x16x32_bf16 v[48:51], v[208:211], v[194:197], v[48:51]\n\t" \
  "ds_read2_b32 v[98:99], v247 offset0:0 offset1:6\n\t" \
  "v_mfma_f32_16x16x32_bf16 v[72:75], v[208:211], v[202:205], v[72:75]\n\t" \
  "ds_read_b64_tr_b16 v[208:209], v249 offset:6224\n\t" \
  "s_waitcnt lgkmcnt(4)\n\t" \
  "v_mfma_f32_16x16x32_bf16 v[4:7], v[212:215], v[178:181], v[4:7]\n\t" \
  "ds_read_b64_tr_b16 v[210:211], v249 offset:7760\n\t" \
  "v_mfma_f32_16x16x32_bf16 v[28:31], v[212:215], v[186:189], v[28:31]\n\t" \
  "ds_read2_b32 v[114:115], v247 offset0:192 offset1:198\n\t" \
  "v_mfma_f32_16x16x32_bf16 v[52:55], v[212:215], v[194:197], v[52:55]\n\t" \
  "ds_read2_b32 v[130:131], v232 offset0:0 offset1:6\n\t" \
  "v_mfma_f32_16x16x32_bf16 v[76:79], v[212:215], v[202:205], v[76:79]\n\t" \
  "ds_read_b64_tr_b16 v[212:213], v249 offset:6280\n\t" \
  "s_waitcnt lgkmcnt(6)\n\t" \
  "v_mfma_f32_16x16x32_bf16 v[8:11], v[216:219], v[178:181], v[8:11]\n\t" \
  "ds_read_b64_tr_b16 v[214:215], v249 offset:7816\n\t" \
  "v_mfma_f32_16x16x32_bf16 v[32:35], v[216:219], v[186:189], v[32:35]\n\t" \
  "ds_read2_b32 v[102:103], v247 offset0:12 offset1:18\n\t" \
  "v_mfma_f32_16x16x32_bf16 v[56:59], v[216:219], v[194:197], v[56:59]\n\t" \
  "ds_read2_b32 v[118:119], v247 offset0:204 offset1:210\n\t" \
  "v_mfma_f32_16x16x32_bf16 v[80:83], v[216:219], v[202:205], v[80:83]\n\t" \
  "ds_read_b64_tr_b16 v[216:217], v249 offset:6288\n\t" \
  "s_waitcnt lgkmcnt(7)\n\t" \
  "v_mfma_f32_16x16x32_bf16 v[12:15], v[208:211], v[178:181], v[12:15]\n\t" \
  "ds_read_b64_tr_b16 v[218:219], v249 offset:7824\n\t" \
  "v_mfma_f32_16x16x32_bf16 v[36:39], v[208:211], v[186:189], v[36:39]\n\t" \
  "ds_read2_b32 v[134:135], v232 offset0:12 offset1:18\n\t" \
  "v_mfma_f32_16x16x32_bf16 v[60:63], v[208:211], v[194:197], v[60:63]\n\t" \
  "ds_read2_b32 v[106:107], v247 offset0:24 offset1:30\n\t" \
  "v_mfma_f32_16x16x32_bf16 v[84:87], v[208:211], v[202:205], v[84:87]\n\t" \
  "ds_read2_b32 v[122:123], v247 offset0:216 offset1:222\n\t" \
  "s_waitcnt lgkmcnt(7)\n\t" \
  "v_mfma_f32_16x16x32_bf16 v[16:19], v[212:215], v[178:181], v[16:19]\n\t" \
  "ds_read2_b32 v[138:139], v232 offset0:24 offset1:30\n\t" \
  "v_mfma_f32_16x16x32_bf16 v[40:43], v[212:215], v[186:189], v[40:43]\n\t" \
  "v_mfma_f32_16x16x32_bf16 v[64:67], v[212:215], v[194:197], v[64:67]\n\t" \
  "ds_read2_b32 v[110:111], v247 offset0:36 offset1:42\n\t" \
  "v_mfma_f32_16x16x32_bf16 v[88:91], v[212:215], v[202:205], v[88:91]\n\t" \
  "ds_read2_b32 v[126:127], v247 offset0:228 offset1:234\n\t" \
  "s_waitcnt lgkmcnt(6)\n\t" \
  "v_mfma_f32_16x16x32_bf16 v[20:23], v[216:219], v[178:181], v[20:23]\n\t" \
  "v_mfma_f32_16x16x32_bf16 v[44:47], v[216:219], v[186:189], v[44:47]\n\t" \
  "ds_read2_b32 v[142:143], v232 offset0:36 offset1:42\n\t" \
  "v_mfma_f32_16x16x32_bf16 v[68:71], v[216:219], v[194:197], v[68:71]\n\t" \
  "v_bfe_u32 v236, v251, 3, 2\n\t" \
  "v_mfma_f32_16x16x32_bf16 v[92:95], v[216:219], v[202:205], v[92:95]\n\t" \
  "v_mul_u32_u24 v236, 0x5555, v236\n\t" \
  "s_nop 7\n\t" \
  "s_mov_b32 s57, 6144\n\t" \
  "s_mov_b32 s58, 1\n\t" \
  "s_mov_b32 s38, 0\n\t" \
  "s_mov_b32 s59, -86104\n\t" \
  "s_mov_b32 s60, -36904\n\t" \
  "s_mov_b32 s36, 2\n\t" \
  "s_mov_b32 s61, s28\n\t" \
  "s_mov_b32 s62, s29\n\t" \
  "s_add_u32 s28, s28, s65\n\t" \
  "s_add_u32 s29, s29, s65\n\t" \
  "s_branch .Lcnx_round\n\t" \
  ".Lcnx_w9:\n\t" \
  "ds_read_b32 v246, v251 offset:47144\n\t" \
  "s_waitcnt lgkmcnt(0)\n\t" \
  "v_readfirstlane_b32 s4, v246\n\t" \
  "s_cmp_lt_u32 s4, s57\n\t" \
  "s_cbranch_scc1 .Lcnx_w9\n\t" \
  "s_branch .Lcnx_b9\n\t" \
  ".Lcnx_w12:\n\t" \
  "ds_read_b32 v246, v251 offset:47152\n\t" \
  "s_waitcnt lgkmcnt(0)\n\t" \
  "v_readfirstlane_b32 s4, v246\n\t" \
  "s_cmp_lt_u32 s4, s57\n\t" \
  "s_cbranch_scc1 .Lcnx_w12\n\t" \
  "s_branch .Lcnx_b12\n\t" \
  ".Lcnx_w24:\n\t" \
  "ds_read_b32 v246, v251 offset:47160\n\t" \
  "s_waitcnt lgkmcnt(0)\n\t" \
  "v_readfirstlane_b32 s4, v246\n\t" \
  "s_cmp_lt_u32 s4, s57\n\t" \
  "s_cbranch_scc1 .Lcnx_w24\n\t" \
  "s_branch .Lcnx_b24\n\t" \
  ".Lcnx_w36:\n\t" \
  "ds_read_b32 v246, v251 offset:47168\n\t" \
  "s_waitcnt lgkmcnt(0)\n\t" \
  "v_readfirstlane_b32 s4, v246\n\t" \
  "s_cmp_lt_u32 s4, s57\n\t" \
  "s_cbranch_scc1 .Lcnx_w36\n\t" \
  "s_branch .Lcnx_b36\n\t" \
  ".Lcnx_w48:\n\t" \
  "ds_read_b32 v246, v251 offset:47176\n\t" \
  "s_waitcnt lgkmcnt(0)\n\t" \
  "v_readfirstlane_b32 s4, v246\n\t" \
  "s_cmp_lt_u32 s4, s57\n\t" \
  "s_cbranch_scc1 .Lcnx_w48\n\t" \
  "s_branch .Lcnx_b48\n\t" \
  ".Lcnx_w60:\n\t" \
  "ds_read_b32 v246, v251 offset:47184\n\t" \
  "s_waitcnt lgkmcnt(0)\n\t" \
  "v_readfirstlane_b32 s4, v246\n\t" \
  "s_cmp_lt_u32 s4, s57\n\t" \
  "s_cbranch_scc1 .Lcnx_w60\n\t" \
  "s_branch .Lcnx_b60\n\t" \
  ".Lcnx_w72:\n\t" \
  "ds_read_b32 v246, v251 offset:47192\n\t" \
  "s_waitcnt lgkmcnt(0)\n\t" \
  "v_readfirstlane_b32 s4, v246\n\t" \
  "s_cmp_lt_u32 s4, s57\n\t" \
  "s_cbranch_scc1 .Lcnx_w72\n\t" \
  "s_branch .Lcnx_b72\n\t" \
  ".Lcnx_w84:\n\t" \
  "ds_read_b32 v246, v251 offset:47200\n\t" \
  "s_waitcnt lgkmcnt(0)\n\t" \
  "v_readfirstlane_b32 s4, v246\n\t" \
  "s_cmp_lt_u32 s4, s57\n\t" \
  "s_cbranch_scc1 .Lcnx_w84\n\t" \
  "s_branch .Lcnx_b84\n\t" \
  ".Lcnx_w96:\n\t" \
  "ds_read_b32 v246, v251 offset:47208\n\t" \
  "s_waitcnt lgkmcnt(0)\n\t" \
  "v_readfirstlane_b32 s4, v246\n\t" \
  "s_cmp_lt_u32 s4, s57\n\t" \
  "s_cbranch_scc1 .Lcnx_w96\n\t" \
  "s_branch .Lcnx_b96\n\t" \
  ".Lcnx_w108:\n\t" \
  "ds_read_b32 v246, v251 offset:47216\n\t" \
  "s_waitcnt lgkmcnt(0)\n\t" \
  "v_readfirstlane_b32 s4, v246\n\t" \
  "s_cmp_lt_u32 s4, s57\n\t" \
  "s_cbranch_scc1 .Lcnx_w108\n\t" \
  "s_branch .Lcnx_b108\n\t" \
  ".Lcnx_w120:\n\t" \
  "ds_read_b32 v246, v251 offset:47224\n\t" \
  "s_waitcnt lgkmcnt(0)\n\t" \
  "v_readfirstlane_b32 s4, v246\n\t" \
  "s_cmp_lt_u32 s4, s57\n\t" \
  "s_cbranch_scc1 .Lcnx_w120\n\t" \
  "s_branch .Lcnx_b120\n\t" \
  ".Lcnx_w132:\n\t" \
  "ds_read_b32 v246, v251 offset:47232\n\t" \
  "s_waitcnt lgkmcnt(0)\n\t" \
  "v_readfirstlane_b32 s4, v246\n\t" \
  "s_cmp_lt_u32 s4, s57\n\t" \
  "s_cbranch_scc1 .Lcnx_w132\n\t" \
  "s_branch .Lcnx_b132\n\t" \
  ".Lcnx_w138:\n\t" \
  "ds_read_b32 v246, v251 offset:47240\n\t" \
  "s_waitcnt lgkmcnt(0)\n\t" \
  "v_readfirstlane_b32 s4, v246\n\t" \
  "s_cmp_lt_u32 s4, s57\n\t" \
  "s_cbranch_scc1 .Lcnx_w138\n\t" \
  "s_branch .Lcnx_b138\n\t" \
  ".Lcnx_f0:\n\t" \
  "s_mov_b32 m0, s44\n\t" \
  "s_nop 0\n\t" \
  "buffer_load_dwordx4 v252, s[16:19], s33 offen lds\n\t" \
  "buffer_load_dwordx4 v252, s[16:19], s34 offen offset:1024 lds\n\t" \
  "buffer_load_dwordx4 v252, s[16:19], s35 offen offset:2048 lds\n\t" \
  "s_add_u32 s33, s33, s32\n\t" \
  "s_add_u32 s34, s34, s32\n\t" \
  "s_add_u32 s35, s35, s32\n\t" \
  "s_branch .Lcnx_g0\n\t" \
  ".Lcnx_f1:\n\t" \
  "s_mov_b32 m0, s45\n\t" \
  "s_nop 0\n\t" \
  "buffer_load_dwordx4 v252, s[16:19], s33 offen lds\n\t" \
  "buffer_load_dwordx4 v252, s[16:19], s34 offen offset:1024 lds\n\t" \
  "buffer_load_dwordx4 v252, s[16:19], s35 offen offset:2048 lds\n\t" \
  "s_add_u32 s33, s33, s32\n\t" \
  "s_add_u32 s34, s34, s32\n\t" \
  "s_add_u32 s35, s35, s32\n\t" \
  "s_branch .Lcnx_g1\n\t" \
  ".Lcnx_f2:\n\t" \
  "s_mov_b32 m0, s46\n\t" \
  "s_nop 0\n\t" \
  "buffer_load_dwordx4 v252, s[16:19], s33 offen lds\n\t" \
  "buffer_load_dwordx4 v252, s[16:19], s34 offen offset:1024 lds\n\t" \
  "buffer_load_dwordx4 v252, s[16:19], s35 offen offset:2048 lds\n\t" \
  "s_add_u32 s33, s33, s32\n\t" \
  "s_add_u32 s34, s34, s32\n\t" \
  "s_add_u32 s35, s35, s32\n\t" \
  "s_branch .Lcnx_g2\n\t" \
  ".Lcnx_f3:\n\t" \
  "s_mov_b32 m0, s47\n\t" \
  "s_nop 0\n\t" \
  "buffer_load_dwordx4 v252, s[16:19], s33 offen lds\n\t" \
  "buffer_load_dwordx4 v252, s[16:19], s34 offen offset:1024 lds\n\t" \
  "buffer_load_dwordx4 v252, s[16:19], s35 offen offset:2048 lds\n\t" \
  "s_add_u32 s33, s33, s32\n\t" \
  "s_add_u32 s34, s34, s32\n\t" \
  "s_add_u32 s35, s35, s32\n\t" \
  "s_branch .Lcnx_g3\n\t" \
  ".Lcnx_f4:\n\t" \
  "s_mov_b32 m0, s44\n\t" \
  "s_nop 0\n\t" \
  "buffer_load_dwordx4 v252, s[16:19], s33 offen lds\n\t" \
  "buffer_load_dwordx4 v252, s[16:19], s34 offen offset:1024 lds\n\t" \
  "buffer_load_dwordx4 v252, s[16:19], s35 offen offset:2048 lds\n\t" \
  "s_add_u32 s33, s33, s32\n\t" \
  "s_add_u32 s34, s34, s32\n\t" \
  "s_add_u32 s35, s35, s32\n\t" \
  "s_cmp_lg_u32 s58, 1\n\t" \
  "s_cbranch_scc1 .Lcnx_g4\n\t" \
  "s_add_u32 m0, s44, s50\n\t" \
  "s_add_u32 s4, s33, s51\n\t" \
  "buffer_load_dwordx4 v252, s[16:19], s4 offen offset:0 lds\n\t" \
  "s_add_u32 s4, s34, s51\n\t" \
  "buffer_load_dwordx4 v252, s[16:19], s4 offen offset:1024 lds\n\t" \
  "s_add_u32 s4, s35, s51\n\t" \
  "buffer_load_dwordx4 v252, s[16:19], s4 offen offset:2048 lds\n\t" \
  "s_branch .Lcnx_g4\n\t" \
  ".Lcnx_f5:\n\t" \
  "s_mov_b32 m0, s45\n\t" \
  "s_nop 0\n\t" \
  "buffer_load_dwordx4 v252, s[16:19], s33 offen lds\n\t" \
  "buffer_load_dwordx4 v252, s[16:19], s34 offen offset:1024 lds\n\t" \
  "buffer_load_dwordx4 v252, s[16:19], s35 offen offset:2048 lds\n\t" \
  "s_add_u32 s33, s33, s32\n\t" \
  "s_add_u32 s34, s34, s32\n\t" \
  "s_add_u32 s35, s35, s32\n\t" \
  "s_cmp_lg_u32 s58, 1\n\t" \
  "s_cbranch_scc1 .Lcnx_g5\n\t" \
  "s_add_u32 m0, s45, s50\n\t" \
  "s_add_u32 s4, s33, s51\n\t" \
  "buffer_load_dwordx4 v252, s[16:19], s4 offen offset:0 lds\n\t" \
  "s_add_u32 s4, s34, s51\n\t" \
  "buffer_load_dwordx4 v252, s[16:19], s4 offen offset:1024 lds\n\t" \
  "s_add_u32 s4, s35, s51\n\t" \
  "buffer_load_dwordx4 v252, s[16:19], s4 offen offset:2048 lds\n\t" \
  "s_branch .Lcnx_g5\n\t" \
  ".Lcnx_f6:\n\t" \
  "s_mov_b32 m0, s46\n\t" \
  "s_nop 0\n\t" \
  "buffer_load_dwordx4 v252, s[16:19], s33 offen lds\n\t" \
  "buffer_load_dwordx4 v252, s[16:19], s34 offen offset:1024 lds\n\t" \
  "buffer_load_dwordx4 v252, s[16:19], s35 offen offset:2048 lds\n\t" \
  "s_add_u32 s33, s33, s32\n\t" \
  "s_add_u32 s34, s34, s32\n\t" \
  "s_add_u32 s35, s35, s32\n\t" \
  "s_cmp_lg_u32 s58, 1\n\t" \
  "s_cbranch_scc1 .Lcnx_g6\n\t" \
  "s_cmp_lt_u32 s41, 1024\n\t" \
  "s_cbranch_scc1 .Lcnx_g6\n\t" \
  "s_add_u32 m0, s46, s50\n\t" \
  "s_add_u32 s4, s33, s51\n\t" \
  "buffer_load_dwordx4 v252, s[16:19], s4 offen offset:0 lds\n\t" \
  "s_add_u32 s4, s34, s51\n\t" \
  "buffer_load_dwordx4 v252, s[16:19], s4 offen offset:1024 lds\n\t" \
  "s_add_u32 s4, s35, s51\n\t" \
  "buffer_load_dwordx4 v252, s[16:19], s4 offen offset:2048 lds\n\t" \
  "s_branch .Lcnx_g6\n\t" \
  ".Lcnx_f7:\n\t" \
  "s_mov_b32 m0, s47\n\t" \
  "s_nop 0\n\t" \
  "buffer_load_dwordx4 v252, s[16:19], s33 offen lds\n\t" \
  "buffer_load_dwordx4 v252, s[16:19], s34 offen offset:1024 lds\n\t" \
  "buffer_load_dwordx4 v252, s[16:19], s35 offen offset:2048 lds\n\t" \
  "s_add_u32 s33, s33, s32\n\t" \
  "s_add_u32 s34, s34, s32\n\t" \
  "s_add_u32 s35, s35, s32\n\t" \
  "s_cmp_lg_u32 s58, 1\n\t" \
  "s_cbranch_scc1 .Lcnx_g7\n\t" \
  "s_cmp_lt_u32 s41, 1024\n\t" \
  "s_cbranch_scc1 .Lcnx_g7\n\t" \
  "s_add_u32 m0, s47, s50\n\t" \
  "s_add_u32 s4, s33, s51\n\t" \
  "buffer_load_dwordx4 v252, s[16:19], s4 offen offset:0 lds\n\t" \
  "s_add_u32 s4, s34, s51\n\t" \
  "buffer_load_dwordx4 v252, s[16:19], s4 offen offset:1024 lds\n\t" \
  "s_add_u32 s4, s35, s51\n\t" \
  "buffer_load_dwordx4 v252, s[16:19], s4 offen offset:2048 lds\n\t" \
  "s_branch .Lcnx_g7\n\t" \
  ".Lcnx_f8:\n\t" \
  "s_mov_b32 m0, s44\n\t" \
  "s_nop 0\n\t" \
  "buffer_load_dwordx4 v252, s[16:19], s33 offen lds\n\t" \
  "buffer_load_dwordx4 v252, s[16:19], s34 offen offset:1024 lds\n\t" \
  "buffer_load_dwordx4 v252, s[16:19], s35 offen offset:2048 lds\n\t" \
  "s_add_u32 s33, s33, s32\n\t" \
  "s_add_u32 s34, s34, s32\n\t" \
  "s_add_u32 s35, s35, s32\n\t" \
  "s_branch .Lcnx_g8\n\t" \
  ".Lcnx_f9:\n\t" \
  "s_mov_b32 m0, s45\n\t" \
  "s_nop 0\n\t" \
  "buffer_load_dwordx4 v252, s[16:19], s33 offen lds\n\t" \
  "buffer_load_dwordx4 v252, s[16:19], s34 offen offset:1024 lds\n\t" \
  "buffer_load_dwordx4 v252, s[16:19], s35 offen offset:2048 lds\n\t" \
  "s_add_u32 s33, s33, s32\n\t" \
  "s_add_u32 s34, s34, s32\n\t" \
  "s_add_u32 s35, s35, s32\n\t" \
  "s_sub_u32 s33, s33, s48\n\t" \
  "s_sub_u32 s34, s34, s48\n\t" \
  "s_sub_u32 s35, s35, s48\n\t" \
  "s_branch .Lcnx_g9\n\t" \
  ".Lcnx_f10:\n\t" \
  "s_mov_b32 m0, s46\n\t" \
  "s_nop 0\n\t" \
  "buffer_load_dwordx4 v252, s[16:19], s33 offen lds\n\t" \
  "buffer_load_dwordx4 v252, s[16:19], s34 offen offset:1024 lds\n\t" \
  "buffer_load_dwordx4 v252, s[16:19], s35 offen offset:2048 lds\n\t" \
  "s_add_u32 s33, s33, s32\n\t" \
  "s_add_u32 s34, s34, s32\n\t" \
  "s_add_u32 s35, s35, s32\n\t" \
  "s_branch .Lcnx_g10\n\t" \
  ".Lcnx_f11:\n\t" \
  "s_mov_b32 m0, s47\n\t" \
  "s_nop 0\n\t" \
  "buffer_load_dwordx4 v252, s[16:19], s33 offen lds\n\t" \
  "buffer_load_dwordx4 v252, s[16:19], s34 offen offset:1024 lds\n\t" \
  "buffer_load_dwordx4 v252, s[16:19], s35 offen offset:2048 lds\n\t" \
  "s_add_u32 s33, s33, s32\n\t" \
  "s_add_u32 s34, s34, s32\n\t" \
  "s_add_u32 s35, s35, s32\n\t" \
  "s_sub_u32 s33, s33, s49\n\t" \
  "s_sub_u32 s34, s34, s49\n\t" \
  "s_sub_u32 s35, s35, s49\n\t" \
  "s_branch .Lcnx_g11\n\t" \
  ".Lcnx_fin:\n\t" \
  "v_mfma_f32_16x16x32_bf16 v[88:91], v[242:245], v[236:239], v[88:91]\n\t" \
  "ds_read_b64_tr_b16 v[218:219], v249 offset:7752\n\t" \
  "v_mfma_f32_16x16x32_bf16 v[92:95], v[240:243], v[236:239], v[92:95]\n\t" \
  "v_mfma_f32_16x16x32_bf16 v[0:3], v[208:211], v[178:181], v[0:3]\n\t" \
  "v_mfma_f32_16x16x32_bf16 v[24:27], v[208:211], v[186:189], v[24:27]\n\t" \
  "v_mfma_f32_16x16x32_bf16 v[48:51], v[208:211], v[194:197], v[48:51]\n\t" \
  "v_mfma_f32_16x16x32_bf16 v[72:75], v[208:211], v[202:205], v[72:75]\n\t" \
  "ds_read_b64_tr_b16 v[208:209], v249 offset:6224\n\t" \
  "s_waitcnt lgkmcnt(3)\n\t" \
  "v_mfma_f32_16x16x32_bf16 v[4:7], v[212:215], v[178:181], v[4:7]\n\t" \
  "ds_read_b64_tr_b16 v[210:211], v249 offset:7760\n\t" \
  "v_mfma_f32_16x16x32_bf16 v[28:31], v[212:215], v[186:189], v[28:31]\n\t" \
  "v_mfma_f32_16x16x32_bf16 v[52:55], v[212:215], v[194:197], v[52:55]\n\t" \
  "v_mfma_f32_16x16x32_bf16 v[76:79], v[212:215], v[202:205], v[76:79]\n\t" \
  "ds_read_b64_tr_b16 v[212:213], v249 offset:6280\n\t" \
  "s_waitcnt lgkmcnt(3)\n\t" \
  "v_mfma_f32_16x16x32_bf16 v[8:11], v[216:219], v[178:181], v[8:11]\n\t" \
  "ds_read_b64_tr_b16 v[214:215], v249 offset:7816\n\t" \
  "v_mfma_f32_16x16x32_bf16 v[32:35], v[216:219], v[186:189], v[32:35]\n\t" \
  "v_mfma_f32_16x16x32_bf16 v[56:59], v[216:219], v[194:197], v[56:59]\n\t" \
  "v_mfma_f32_16x16x32_bf16 v[80:83], v[216:219], v[202:205], v[80:83]\n\t" \
  "ds_read_b64_tr_b16 v[216:217], v249 offset:6288\n\t" \
  "v_cvt_pk_bf16_f32 v0, v0, v1\n\t" \
  "v_cvt_pk_bf16_f32 v1, v2, v3\n\t" \
  "v_cvt_pk_bf16_f32 v2, v4, v5\n\t" \
  "v_cvt_pk_bf16_f32 v3, v6, v7\n\t" \
  "buffer_store_dwordx4 v[0:3], v250, s[20:23], s28 offen offset:0 sc1\n\t" \
  "v_cvt_pk_bf16_f32 v24, v24, v25\n\t" \
  "v_cvt_pk_bf16_f32 v25, v26, v27\n\t" \
  "v_cvt_pk_bf16_f32 v26, v28, v29\n\t" \
  "v_cvt_pk_bf16_f32 v27, v30, v31\n\t" \
  "buffer_store_dwordx4 v[24:27], v250, s[20:23], s28 offen offset:192 sc1\n\t" \
  "v_cvt_pk_bf16_f32 v48, v48, v49\n\t" \
  "v_cvt_pk_bf16_f32 v49, v50, v51\n\t" \
  "v_cvt_pk_bf16_f32 v50, v52, v53\n\t" \
  "v_cvt_pk_bf16_f32 v51, v54, v55\n\t" \
  "buffer_store_dwordx4 v[48:51], v250, s[20:23], s29 offen offset:0 sc1\n\t" \
  "v_cvt_pk_bf16_f32 v72, v72, v73\n\t" \
  "v_cvt_pk_bf16_f32 v73, v74, v75\n\t" \
  "v_cvt_pk_bf16_f32 v74, v76, v77\n\t" \
  "v_cvt_pk_bf16_f32 v75, v78, v79\n\t" \
  "buffer_store_dwordx4 v[72:75], v250, s[20:23], s29 offen offset:192 sc1\n\t" \
  "s_waitcnt lgkmcnt(3)\n\t" \
  "v_mfma_f32_16x16x32_bf16 v[12:15], v[208:211], v[178:181], v[12:15]\n\t" \
  "ds_read_b64_tr_b16 v[218:219], v249 offset:7824\n\t" \
  "v_mfma_f32_16x16x32_bf16 v[36:39], v[208:211], v[186:189], v[36:39]\n\t" \
  "v_mfma_f32_16x16x32_bf16 v[60:63], v[208:211], v[194:197], v[60:63]\n\t" \
  "v_mfma_f32_16x16x32_bf16 v[84:87], v[208:211], v[202:205], v[84:87]\n\t" \
  "s_waitcnt lgkmcnt(2)\n\t" \
  "v_mfma_f32_16x16x32_bf16 v[16:19], v[212:215], v[178:181], v[16:19]\n\t" \
  "v_mfma_f32_16x16x32_bf16 v[40:43], v[212:215], v[186:189], v[40:43]\n\t" \
  "v_mfma_f32_16x16x32_bf16 v[64:67], v[212:215], v[194:197], v[64:67]\n\t" \
  "v_mfma_f32_16x16x32_bf16 v[88:91], v[212:215], v[202:205], v[88:91]\n\t" \
  "v_cvt_pk_bf16_f32 v8, v8, v9\n\t" \
  "v_cvt_pk_bf16_f32 v9, v10, v11\n\t" \
  "v_cvt_pk_bf16_f32 v10, v12, v13\n\t" \
  "v_cvt_pk_bf16_f32 v11, v14, v15\n\t" \
  "buffer_store_dwordx4 v[8:11], v250, s[20:23], s28 offen offset:64 sc1\n\t" \
  "v_cvt_pk_bf16_f32 v32, v32, v33\n\t" \
  "v_cvt_pk_bf16_f32 v33, v34, v35\n\t" \
  "v_cvt_pk_bf16_f32 v34, v36, v37\n\t" \
  "v_cvt_pk_bf16_f32 v35, v38, v39\n\t" \
  "buffer_store_dwordx4 v[32:35], v250, s[20:23], s28 offen offset:256 sc1\n\t" \
  "v_cvt_pk_bf16_f32 v56, v56, v57\n\t" \
  "v_cvt_pk_bf16_f32 v57, v58, v59\n\t" \
  "v_cvt_pk_bf16_f32 v58, v60, v61\n\t" \
  "v_cvt_pk_bf16_f32 v59, v62, v63\n\t" \
  "buffer_store_dwordx4 v[56:59], v250, s[20:23], s29 offen offset:64 sc1\n\t" \
  "v_cvt_pk_bf16_f32 v80, v80, v81\n\t" \
  "v_cvt_pk_bf16_f32 v81, v82, v83\n\t" \
  "v_cvt_pk_bf16_f32 v82, v84, v85\n\t" \
  "v_cvt_pk_bf16_f32 v83, v86, v87\n\t" \
  "buffer_store_dwordx4 v[80:83], v250, s[20:23], s29 offen offset:256 sc1\n\t" \
  "s_waitcnt lgkmcnt(0)\n\t" \
  "v_mfma_f32_16x16x32_bf16 v[20:23], v[216:219], v[178:181], v[20:23]\n\t" \
  "v_mfma_f32_16x16x32_bf16 v[44:47], v[216:219], v[186:189], v[44:47]\n\t" \
  "v_mfma_f32_16x16x32_bf16 v[68:71], v[216:219], v[194:197], v[68:71]\n\t" \
  "v_mfma_f32_16x16x32_bf16 v[92:95], v[216:219], v[202:205], v[92:95]\n\t" \
  "v_cvt_pk_bf16_f32 v16, v16, v17\n\t" \
  "v_cvt_pk_bf16_f32 v17, v18, v19\n\t" \
  "s_nop 2\n\t" \
  "v_cvt_pk_bf16_f32 v18, v20, v21\n\t" \
  "v_cvt_pk_bf16_f32 v19, v22, v23\n\t" \
  "buffer_store_dwordx4 v[16:19], v250, s[20:23], s28 offen offset:128 sc1\n\t" \
  "v_cvt_pk_bf16_f32 v40, v40, v41\n\t" \
  "v_cvt_pk_bf16_f32 v41, v42, v43\n\t" \
  "v_cvt_pk_bf16_f32 v42, v44, v45\n\t" \
  "v_cvt_pk_bf16_f32 v43, v46, v47\n\t" \
  "buffer_store_dwordx4 v[40:43], v250, s[20:23], s28 offen offset:320 sc1\n\t" \
  "v_cvt_pk_bf16_f32 v64, v64, v65\n\t" \
  "v_cvt_pk_bf16_f32 v65, v66, v67\n\t" \
  "v_cvt_pk_bf16_f32 v66, v68, v69\n\t" \
  "v_cvt_pk_bf16_f32 v67, v70, v71\n\t" \
  "buffer_store_dwordx4 v[64:67], v250, s[20:23], s29 offen offset:128 sc1\n\t" \
  "v_cvt_pk_bf16_f32 v88, v88, v89\n\t" \
  "v_cvt_pk_bf16_f32 v89, v90, v91\n\t" \
  "v_cvt_pk_bf16_f32 v90, v92, v93\n\t" \
  "v_cvt_pk_bf16_f32 v91, v94, v95\n\t" \
  "buffer_store_dwordx4 v[88:91], v250, s[20:23], s29 offen offset:320 sc1\n\t" \
  "s_waitcnt vmcnt(0)\n\t" \
  ".Lcnx_end:\n\t" \
  ""


__device__ __forceinline__ unsigned lds_addr(const char* p) { return (unsigned)(uintptr_t)(lds_ptr_t)p; }
__device__ __forceinline__ unsigned sg(unsigned v) { return __builtin_amdgcn_readfirstlane(v); }

constexpr double tanh_u(double y) {
    const double v = 8 * y, a = 2 * 0.79788456080286536 * (v + 0.044715 * v * v * v) / 1024;
    double m = 1;
    for (int i = 12; i > 1; --i) m = 1 + m * a / i;
    m *= a;
    for (int i = 0; i < 10; ++i) m *= m + 2;
    return m / (m + 2);
}
constexpr double h2d(int h) {
    if (h < 1024) return 0;
    double x = (1024 + (h & 1023)) * 0x1p-24;
    for (int e = 1; e < (h >> 10); ++e) x *= 2;
    return x;
}
constexpr int d2h(double x) {
    int h = 0;
    for (int s = 1 << 14; s; s >>= 1) if (h2d(h + s) <= x) h += s;
    return h;
}
constexpr unsigned ip_entry(int k) {
    if (k == 127) return 0x3c003c00u;
    const double y0 = k ? (256 * k - 0.5) / 32767 : 0, dy = ((256 * k + 255.5) / 32767 - y0) / 8;
    double t[9] = {};
    for (int i = 0; i < 9; ++i) t[i] = tanh_u(y0 + i * dy);
    const double b0 = (t[8] - t[0]) / (8 * dy), ym = y0 + 4 * dy, tm = (t[4] + (t[0] + t[8]) / 2) / 2;
    unsigned best = 0;
    double err = 1;
    for (int ha = d2h(tm - b0 * ym) - 1, i = 0; i < 4; ++i, ++ha)
        for (int hb = d2h((tm - h2d(ha)) / ym), j = 0; j < 2; ++j, ++hb) {
            double e = 0;
            for (int n = 0; n < 9; ++n) {
                const double s = h2d(ha) + h2d(hb) * (y0 + n * dy), d = (s < 1 ? s : 1) - t[n];
                e = d > e ? d : -d > e ? -d : e;
            }
            if (e < err) err = e, best = (ha < 1024 ? 0 : ha) | (hb < 1024 ? 0 : hb) << 16;
        }
    return best;
}
struct TanhTab { unsigned v[1024]; };
constexpr TanhTab tanh_tab(int k0) {
    TanhTab t = {};
    for (int k = k0; k < k0 + 1024 && k < DWOFF / 4; ++k) {
        double y = ((k & 255) + 0.375 + (k >= 256 ? 256 : 0)) * 0x1p-22;
        for (int e = 1; e < (k >> 8); ++e) y *= 2;
        t.v[k - k0] = k < IPOFF / 4 ? __builtin_bit_cast(unsigned, (float)tanh_u(y)) : ip_entry(k - IPOFF / 4);
    }
    return t;
}
struct WaveTab { unsigned v244[64][4], v16[64][4], s32[32]; };
struct RegTab { unsigned v240[64][4], v248[64][4]; WaveTab w[8]; };
constexpr RegTab reg_tab() {
    RegTab t = {};
    for (unsigned l = 0; l < 64; ++l) {
        const unsigned G = l >> 4, n = l & 15, q = n >> 2, p = l & 3;
        t.v240[l][2 + (p >> 1)] = G == q ? (p & 1 ? 0x3f800000u : 0x3f80u) : 0u;
        t.v248[l][0] = WB + (l >> 2) * 32 + (p & 1) * 16 + (p >> 1) * 8;
        t.v248[l][1] = WB + (G & 1) * 3080 + ((G >> 1) * 4 + q) * 192 + p * 16;
        t.v248[l][2] = n * 384 + G * 16;
        t.v248[l][3] = PB + l * 8;
        for (unsigned w = 0; w < 8; ++w) {
            t.w[w].v244[l][2] = XOFF + n * 400 + (G & 1) * XROW + (G >> 1) * 192 + w * 24;
            t.w[w].v244[l][3] = DWOFF + p * 192 + (1 - (q >> 1)) * 24 + (1 + (G & 1) - (q & 1)) * 8 + ((G ^ p) >> 1) * 4 + w * 2304;
            t.w[w].v16[l][0] = w >= 4 ? (l < 48 ? l * 128 : OOB)
                             : w < 2  ? (12 * ((l >> 3) & 3) + ((l >> 1) & 3)) * (HID * 2) + (2 * (l & 1) + (l >> 5)) * 16 : l * 16;
            const unsigned d = DWOFF + (w * 8 + (l >> 5) * 4 + p) * 768 + ((l >> 2) & 7) * 24;
            t.w[w].v16[l][1] = w < 3 ? d + (l & 1) * 192 : XOFF - 32 + p * 8;
            t.w[w].v16[l][2] = d + (~l & 1) * 192;
            t.w[w].v16[l][3] = l & 1 ? 0x07060302u : 0x05040100u;
        }
    }
    for (unsigned w = 0; w < 8; ++w) {
        const bool w1 = w < 2;
        const unsigned wpd = w1 ? 4 * HID * 2 - 1024 : 0, wl = WB + (w & 1) * (w1 ? 3072 : 3080), wc = w1 ? 64 : 6144;
        const unsigned so = (w & 1) * (w1 ? 48 * HID * 2 : 3072);
        const unsigned s[32] = {wc, so, so + wpd, so + 2 * wpd, w, w >> 2, w, XOFF - 8 * PB + w * 12288,
                                (w & 3) * 4096 + (w < 4 ? 16400 : 0), w * 512, XOFF - 2 * PB + w * 1024,
                                XOFF - 2 * PB + (8 + (w & 3)) * 1024 + (w >> 2) * 8,
                                wl + (w1 ? WA + 12304 : WA), wl + (w1 ? 12304 : WA + 6152), wl + (w1 ? WA + 18448 : 0),
                                wl + (w1 ? WA + 18448 + 6144 : 6152), w1 ? 12 * wc : 0, w1 ? 0 : 12 * wc,
                                w1 ? 0u - 86104 : 0u - 36904, 3 * wc, 0, 0, 0, 0, 0x3ffc, 3072, 0, 0, 0,
                                OOB, OOB, (unsigned)sizeof(RegTab) - 2 * PB};
        for (int i = 0; i < 32; ++i) t.w[w].s32[i] = s[i];
    }
    return t;
}
struct Tables { RegTab r; TanhTab t[4]; };
static_assert(sizeof(WaveTab) == 64 * 34 && sizeof(RegTab) == 19456, "table layout");
__device__ const __attribute__((aligned(128))) Tables TABLES = {reg_tab(), {tanh_tab(0), tanh_tab(1024), tanh_tab(2048), tanh_tab(3072)}};

#define R10(r, a) r #a "0", r #a "1", r #a "2", r #a "3", r #a "4", r #a "5", r #a "6", r #a "7", r #a "8", r #a "9"
#define V10(a) R10("v", a)
#define S10(a) R10("s", a)
#define MAIN_CLOB "memory", "scc", "vcc", "v1", "v2", "v3", "v4", "v5", "v6", "v7", "v8", "v9", V10(1), V10(2), V10(3), V10(4), V10(5),  \
    V10(6), V10(7), V10(8), V10(9), V10(10), V10(11), V10(12), V10(13), V10(14), V10(15), V10(16), V10(17), V10(18), V10(19),        \
    V10(20), V10(21), V10(22), V10(23), V10(24), "v250", "v251", "v252", "v253", "v254", "s4", "s5", "s8", "s9", S10(1),       \
    S10(2), S10(3), S10(4), S10(5), "s60", "s61", "s62", "s63", "s64", "s65", "s66", "s67"

__global__ __launch_bounds__(512, 2) void convnext_kernel(const __hip_bfloat16* __restrict__ x,
                                                          const __hip_bfloat16* __restrict__ dw,
                                                          const __hip_bfloat16* __restrict__ w1,
                                                          const __hip_bfloat16* __restrict__ w2,
                                                          __hip_bfloat16* __restrict__ out) {
    __shared__ __attribute__((aligned(16))) char smem[LDS_BYTES];

    unsigned tid = threadIdx.x, lds = lds_addr(smem);
    const unsigned bid = blockIdx.x;
    asm volatile(MAIN_ASM
                 : "+{v0}"(tid), "+{s3}"(lds)
                 : "{s[0:1]}"(__builtin_amdgcn_kernarg_segment_ptr()), "{s2}"(bid), "{s[6:7]}"(&TABLES)
                 : MAIN_CLOB);

}

}

extern "C" void kernel_launch(void* const* d_in, const int* in_sizes, int n_in, void* d_out, int out_size,
                              void* d_ws, size_t ws_size, hipStream_t stream) {
    (void)in_sizes; (void)n_in; (void)out_size; (void)d_ws; (void)ws_size;
    const __hip_bfloat16* x = (const __hip_bfloat16*)d_in[0];
    const __hip_bfloat16* dw = (const __hip_bfloat16*)d_in[1];
    const __hip_bfloat16* w1 = (const __hip_bfloat16*)d_in[2];
    const __hip_bfloat16* w2 = (const __hip_bfloat16*)d_in[3];
    __hip_bfloat16* out = (__hip_bfloat16*)d_out;
    convnext_kernel<<<256, 512, 0, stream>>>(x, dw, w1, w2, out);
}
```

```cpp
#include <hip/hip_runtime.h>
#include <hip/hip_bf16.h>
#include <stdint.h>

namespace {

typedef __attribute__((address_space(3))) void* lds_ptr_t;

constexpr int IMG = 512;
constexpr int CH = 96;
constexpr int HID = 384;
constexpr int ROWB = IMG * CH * 2;
constexpr int LUTN = 3841;
constexpr int IPOFF = 15376;
constexpr int DWOFF = IPOFF + 512;
constexpr int XOFF = DWOFF + 96 * 192 + 32;
constexpr int XROW = 8200;
constexpr int PB = XOFF + 10 * XROW;
constexpr int WB = XOFF + 9 * XROW;
constexpr int WA = 3 * XROW;
constexpr int PTOP = XOFF + 12 * XROW - 12288;
constexpr int CNTOFF = WB + WA + 18448 + 12288;
constexpr int PFOFF = CNTOFF + 144;
constexpr int LDS_BYTES = 160 * 1024;
static_assert(PB == 116352 && CNTOFF - PB == 47136 && LUTN * 4 <= IPOFF && XOFF % 16 == 0 && PFOFF + 48 * 4 <= LDS_BYTES, "LDS map");
constexpr unsigned OOB = 0x40000000u;

#define MAIN_ASM \
  "s_load_dwordx2 s[8:9], s[0:1], 0x0\n\t" \
  "s_load_dwordx2 s[12:13], s[0:1], 0x8\n\t" \
  "s_load_dwordx2 s[20:21], s[0:1], 0x20\n\t" \
  "v_readfirstlane_b32 s5, v0\n\t" \
  "v_and_b32 v21, 63, v0\n\t" \
  "s_mov_b32 m0, 0\n\t" \
  "s_cmp_lt_u32 s5, 128\n\t" \
  "s_cselect_b32 s3, 16, 24\n\t" \
  "v_lshlrev_b32 v22, 4, v21\n\t" \
  "s_load_dwordx2 s[16:17], s[0:1], s3\n\t" \
  "s_mul_i32 s4, s5, 50\n\t" \
  "s_add_u32 s66, s6, s4\n\t" \
  "s_addc_u32 s67, s7, 0\n\t" \
  "global_load_dwordx4 v[244:247], v22, s[66:67] offset:1024\n\t" \
  "global_load_dwordx4 v[16:19], v22, s[66:67] offset:2048\n\t" \
  "global_load_dwordx4 v[240:243], v22, s[6:7] offset:-2048\n\t" \
  "global_load_dwordx4 v[248:251], v22, s[6:7] offset:-1024\n\t" \
  "global_load_dwordx4 v[192:195], v22, s[66:67]\n\t" \
  "s_cmp_lg_u32 s5, 192\n\t" \
  "s_cbranch_scc1 .Lcnx_nc\n\t" \
  "s_getpc_b64 s[14:15]\n\t" \
  ".Lcnx_pc:\n\t" \
  "s_lshr_b32 s3, s2, 3\n\t" \
  "s_lshl_b32 s3, s3, 11\n\t" \
  "v_lshl_add_u32 v1, v21, 5, s3\n\t" \
  "v_min_u32 v1, .Lcnx_end-.Lcnx_pc, v1\n\t" \
  "global_load_lds_dword v1, s[14:15]\n\t" \
  ".Lcnx_nc:\n\t" \
  "v_mul_u32_u24 v254, 0x147b, v0\n\t" \
  "s_bfe_u32 s27, s2, 0x40003\n\t" \
  "v_bfe_u32 v1, v0, 2, 3\n\t" \
  "s_mul_i32 s27, s27, 6144\n\t" \
  "v_lshrrev_b32 v254, 17, v254\n\t" \
  "s_sub_u32 s3, s27, 576\n\t" \
  "v_and_b32 v20, 3, v0\n\t" \
  "s_add_u32 s4, s27, 6720\n\t" \
  "v_sub_u32 v254, v0, v254\n\t" \
  "s_min_u32 s4, s4, 0x18000\n\t" \
  "v_and_b32 v2, 0x1e0, v0\n\t" \
  "s_and_b32 s24, s2, 7\n\t" \
  "v_lshl_add_u32 v254, v254, 4, s3\n\t" \
  "s_lshl_b32 s24, s24, 6\n\t" \
  "v_lshl_or_b32 v20, v20, 3, v2\n\t" \
  "s_mov_b32 s26, 0x18000\n\t" \
  "v_cmp_gt_u32 vcc, s4, v254\n\t" \
  "s_cmp_ge_u32 s2, 128\n\t" \
  "s_cselect_b32 s3, 7, 0\n\t" \
  "s_cselect_b32 s4, 0, 7\n\t" \
  "v_add_u32 v1, s3, v1\n\t" \
  "s_cselect_b32 s3, 28, 35\n\t" \
  "v_cndmask_b32 v254, 2.0, v254, vcc\n\t" \
  "s_cselect_b32 s26, s26, 0xfffe8000\n\t" \
  "v_xor_b32 v1, s4, v1\n\t" \
  "s_add_u32 s24, s24, s3\n\t" \
  "v_and_b32 v1, 7, v1\n\t" \
  "s_movk_i32 s3, 1344\n\t" \
  "s_mul_i32 s24, s24, 0x18000\n\t" \
  "v_mad_u32_u24 v1, v1, s3, v20\n\t" \
  "s_lshl4_add_u32 s25, s5, 34352\n\t" \
  "s_mov_b32 s10, 0x3000000\n\t" \
  "s_mov_b32 s11, 0x20000\n\t" \
  "s_mov_b32 s14, 9408\n\t" \
  "s_mov_b32 s15, 0x20000\n\t" \
  "s_mov_b32 s18, 73728\n\t" \
  "s_mov_b32 s19, 0x20000\n\t" \
  "s_mov_b32 s22, 0x3000000\n\t" \
  "s_mov_b32 s23, 0x20000\n\t" \
  "s_setreg_imm32_b32 hwreg(HW_REG_MODE, 4, 2), 0\n\t" \
  "s_setreg_imm32_b32 hwreg(HW_REG_MODE, 9, 1), 0\n\t" \
  "s_lshl_b32 s65, s26, 4\n\t" \
  "s_lshr_b32 s3, s5, 7\n\t" \
  "s_add_u32 s3, s3, 8\n\t" \
  "s_mul_i32 s3, s3, s26\n\t" \
  "s_add_u32 s29, s24, s27\n\t" \
  "s_add_u32 s29, s29, s3\n\t" \
  "s_and_b32 s3, s5, 64\n\t" \
  "s_mul_i32 s3, s3, 48\n\t" \
  "s_add_u32 s29, s29, s3\n\t" \
  "s_bfe_u32 s3, s5, 0x20006\n\t" \
  "s_add_u32 s3, s3, 6\n\t" \
  "s_mul_i32 s3, s3, s26\n\t" \
  "s_add_u32 s30, s24, s65\n\t" \
  "s_add_u32 s30, s30, s3\n\t" \
  "s_lshl_b32 s31, s26, 2\n\t" \
  "s_mul_i32 s64, s26, 22\n\t" \
  "s_sub_u32 s64, s64, s65\n\t" \
  "s_cmp_ge_u32 s5, 256\n\t" \
  "s_cselect_b32 s27, s27, 0\n\t" \
  "s_cselect_b32 s3, -6, -2\n\t" \
  "s_mul_i32 s3, s3, s26\n\t" \
  "s_add_u32 s28, s29, s3\n\t" \
  "s_mov_b64 vcc, 0\n\t" \
  "s_waitcnt lgkmcnt(0)\n\t" \
  "s_and_b32 s13, s13, 0xffff\n\t" \
  "s_and_b32 s9, s9, 0xffff\n\t" \
  "s_and_b32 s17, s17, 0xffff\n\t" \
  "s_cmp_ge_u32 s5, 192\n\t" \
  "s_cbranch_scc1 .Lcnx_nd\n\t" \
  "buffer_load_dwordx2 v[2:3], v1, s[12:15], 0 offen offset:0\n\t" \
  "buffer_load_dwordx2 v[4:5], v1, s[12:15], 0 offen offset:192\n\t" \
  "buffer_load_dwordx2 v[6:7], v1, s[12:15], 0 offen offset:384\n\t" \
  "buffer_load_dwordx2 v[8:9], v1, s[12:15], 0 offen offset:576\n\t" \
  "buffer_load_dwordx2 v[10:11], v1, s[12:15], 0 offen offset:768\n\t" \
  "buffer_load_dwordx2 v[12:13], v1, s[12:15], 0 offen offset:960\n\t" \
  "buffer_load_dwordx2 v[14:15], v1, s[12:15], 0 offen offset:1152\n\t" \
  ".Lcnx_nd:\n\t" \
  "s_and_b32 s3, s5, 0x140\n\t" \
  "s_cmp_lg_u32 s3, 64\n\t" \
  "s_cbranch_scc1 .Lcnx_nw\n\t" \
  "s_lshr_b32 s3, s2, 3\n\t" \
  "s_mul_i32 s3, s3, 2304\n\t" \
  "s_mov_b32 m0, 0\n\t" \
  "v_mul_u32_u24 v21, 36, v21\n\t" \
  "buffer_load_dword v21, s[16:19], s3 offen lds\n\t" \
  ".Lcnx_nw:\n\t" \
  "s_add_u32 m0, s25, 0\n\t" \
  "s_add_u32 s24, s24, s26\n\t" \
  "buffer_load_dwordx4 v254, s[8:11], s24 offen nt lds\n\t" \
  "s_add_u32 m0, s25, 8200\n\t" \
  "s_add_u32 s24, s24, s26\n\t" \
  "buffer_load_dwordx4 v254, s[8:11], s24 offen nt lds\n\t" \
  "s_add_u32 m0, s25, 16400\n\t" \
  "s_add_u32 s24, s24, s26\n\t" \
  "buffer_load_dwordx4 v254, s[8:11], s24 offen nt lds\n\t" \
  "s_add_u32 m0, s25, 24600\n\t" \
  "s_add_u32 s24, s24, s26\n\t" \
  "buffer_load_dwordx4 v254, s[8:11], s24 offen nt lds\n\t" \
  "s_load_dwordx16 s[32:47], s[66:67], 0xc00\n\t" \
  "s_load_dwordx16 s[48:63], s[66:67], 0xc40\n\t" \
  "s_and_b32 s21, s21, 0xffff\n\t" \
  "s_waitcnt vmcnt(4)\n\t" \
  "v_mfma_f32_32x32x16_bf16 v[144:159], v[242:245], v[240:243], 0\n\t" \
  "v_mfma_f32_32x32x16_bf16 v[160:175], v[242:245], v[240:243], 0\n\t" \
  "v_bfe_u32 v236, v251, 3, 2\n\t" \
  "v_mul_u32_u24 v236, 0x5555, v236\n\t" \
  "v_add_u32 v252, s27, v16\n\t" \
  "s_lshl2_add_u32 s12, s26, s29\n\t" \
  "s_lshl3_add_u32 s13, s26, s29\n\t" \
  "v_mov_b32 v253, v246\n\t" \
  "s_mov_b32 s14, 2.0\n\t" \
  "s_mov_b32 s15, 2.0\n\t" \
  "s_cmp_gt_u32 s5, 192\n\t" \
  "s_cbranch_scc1 .Lcnx_e2\n\t" \
  "s_cmp_eq_u32 s5, 192\n\t" \
  "s_cbranch_scc1 .Lcnx_e1\n\t" \
  "v_xor_b32 v23, 0x02020202, v19\n\t" \
  "v_perm_b32 v24, v4, 0, v19\n\t" \
  "v_perm_b32 v25, v12, v8, v19\n\t" \
  "v_perm_b32 v26, v6, v2, v19\n\t" \
  "v_perm_b32 v27, v14, v10, v19\n\t" \
  "v_perm_b32 v28, v8, v4, v19\n\t" \
  "v_perm_b32 v29, 0, v12, v19\n\t" \
  "ds_write_b64 v17, v[24:25] offset:0\n\t" \
  "ds_write_b64 v17, v[26:27] offset:8\n\t" \
  "ds_write_b64 v17, v[28:29] offset:16\n\t" \
  "v_perm_b32 v30, v4, 0, v23\n\t" \
  "v_perm_b32 v31, v12, v8, v23\n\t" \
  "v_perm_b32 v32, v6, v2, v23\n\t" \
  "v_perm_b32 v33, v14, v10, v23\n\t" \
  "v_perm_b32 v34, v8, v4, v23\n\t" \
  "v_perm_b32 v35, 0, v12, v23\n\t" \
  "ds_write_b64 v18, v[30:31] offset:0\n\t" \
  "ds_write_b64 v18, v[32:33] offset:8\n\t" \
  "ds_write_b64 v18, v[34:35] offset:16\n\t" \
  "v_perm_b32 v37, v5, 0, v19\n\t" \
  "v_perm_b32 v36, v13, v9, v19\n\t" \
  "v_perm_b32 v39, v7, v3, v19\n\t" \
  "v_perm_b32 v38, v15, v11, v19\n\t" \
  "v_perm_b32 v41, v9, v5, v19\n\t" \
  "v_perm_b32 v40, 0, v13, v19\n\t" \
  "ds_write_b64 v17, v[36:37] offset:384\n\t" \
  "ds_write_b64 v17, v[38:39] offset:392\n\t" \
  "ds_write_b64 v17, v[40:41] offset:400\n\t" \
  "v_perm_b32 v43, v5, 0, v23\n\t" \
  "v_perm_b32 v42, v13, v9, v23\n\t" \
  "v_perm_b32 v45, v7, v3, v23\n\t" \
  "v_perm_b32 v44, v15, v11, v23\n\t" \
  "v_perm_b32 v47, v9, v5, v23\n\t" \
  "v_perm_b32 v46, 0, v13, v23\n\t" \
  "ds_write_b64 v18, v[42:43] offset:384\n\t" \
  "ds_write_b64 v18, v[44:45] offset:392\n\t" \
  "ds_write_b64 v18, v[46:47] offset:400\n\t" \
  "s_branch .Lcnx_e3\n\t" \
  ".Lcnx_e1:\n\t" \
  "s_mov_b64 exec, 0x3ffff\n\t" \
  "ds_write_b64 v251, v[240:241] offset:47136\n\t" \
  "s_mov_b64 exec, 15\n\t" \
  "ds_write_b64 v17, v[240:241]\n\t" \
  "s_mov_b64 exec, -1\n\t" \
  "s_branch .Lcnx_e3\n\t" \
  ".Lcnx_e2:\n\t" \
  "s_setprio 1\n\t" \
  ".Lcnx_e3:\n\t" \
  "s_add_u32 m0, s25, 32800\n\t" \
  "s_add_u32 s24, s24, s26\n\t" \
  "buffer_load_dwordx4 v254, s[8:11], s24 offen nt lds\n\t" \
  "s_add_u32 m0, s25, 41000\n\t" \
  "s_add_u32 s24, s24, s26\n\t" \
  "buffer_load_dwordx4 v254, s[8:11], s24 offen nt lds\n\t" \
  "s_add_u32 m0, s25, 49200\n\t" \
  "s_add_u32 s24, s24, s26\n\t" \
  "buffer_load_dwordx4 v254, s[8:11], s24 offen nt lds\n\t" \
  "s_add_u32 m0, s25, 57400\n\t" \
  "s_add_u32 s24, s24, s26\n\t" \
  "buffer_load_dwordx4 v254, s[8:11], s24 offen nt lds\n\t" \
  "s_waitcnt vmcnt(6) lgkmcnt(0)\n\t" \
  "s_barrier\n\t" \
  "v_add_u32 v232, 1536, v247\n\t" \
  "ds_read2_b32 v[98:99], v247 offset0:0 offset1:6\n\t" \
  "ds_read2_b32 v[114:115], v247 offset0:192 offset1:198\n\t" \
  "ds_read2_b32 v[130:131], v232 offset0:0 offset1:6\n\t" \
  "ds_read2_b32 v[102:103], v247 offset0:12 offset1:18\n\t" \
  "ds_read2_b32 v[118:119], v247 offset0:204 offset1:210\n\t" \
  "ds_read2_b32 v[134:135], v232 offset0:12 offset1:18\n\t" \
  "ds_read2_b32 v[106:107], v247 offset0:24 offset1:30\n\t" \
  "ds_read2_b32 v[122:123], v247 offset0:216 offset1:222\n\t" \
  "ds_read2_b32 v[138:139], v232 offset0:24 offset1:30\n\t" \
  "ds_read2_b32 v[110:111], v247 offset0:36 offset1:42\n\t" \
  "ds_read2_b32 v[126:127], v247 offset0:228 offset1:234\n\t" \
  "ds_read2_b32 v[142:143], v232 offset0:36 offset1:42\n\t" \
  ".Lcnx_round:\n\t" \
  "v_add_u32 v232, 49200, v253\n\t" \
  "v_add_u32 v233, s39, v251\n\t" \
  "v_mfma_f32_32x32x16_bf16 v[176:191], v[242:245], v[240:243], 0\n\t" \
  "ds_read_b64 v[200:201], v253 offset:0\n\t" \
  "ds_read_b64 v[202:203], v253 offset:400\n\t" \
  "ds_read_b64 v[204:205], v253 offset:800\n\t" \
  "ds_read_b64 v[206:207], v253 offset:1200\n\t" \
  "ds_read_b64 v[208:209], v253 offset:8\n\t" \
  "ds_read_b64 v[210:211], v253 offset:408\n\t" \
  "ds_read_b64 v[212:213], v253 offset:808\n\t" \
  "ds_read_b64 v[214:215], v253 offset:1208\n\t" \
  "ds_read_b64 v[216:217], v253 offset:16\n\t" \
  "ds_read_b64 v[218:219], v253 offset:416\n\t" \
  "ds_read_b64 v[220:221], v253 offset:816\n\t" \
  "ds_read_b64 v[222:223], v253 offset:1216\n\t" \
  "s_cmp_lg_u32 s58, 0\n\t" \
  "s_cbranch_scc1 .Lcnx_c1\n\t" \
  "s_add_u32 m0, s25, 65600\n\t" \
  "s_add_u32 s24, s24, s26\n\t" \
  "buffer_load_dwordx4 v254, s[8:11], s24 offen nt lds\n\t" \
  "s_add_u32 m0, s25, 73800\n\t" \
  "s_add_u32 s24, s24, s26\n\t" \
  "buffer_load_dwordx4 v254, s[8:11], s24 offen nt lds\n\t" \
  "s_add_u32 m0, s25, 82000\n\t" \
  "s_add_u32 s24, s24, s26\n\t" \
  "buffer_load_dwordx4 v254, s[8:11], s24 offen nt lds\n\t" \
  "s_add_u32 m0, s25, 90200\n\t" \
  "s_add_u32 s24, s24, s26\n\t" \
  "buffer_load_dwordx4 v254, s[8:11], s24 offen nt lds\n\t" \
  ".Lcnx_c1:\n\t" \
  "s_waitcnt lgkmcnt(15)\n\t" \
  "v_and_b32 v96, 0xffff, v98\n\t" \
  "v_and_b32 v97, 0xffff0000, v98\n\t" \
  "v_and_b32 v98, 0xffff, v99\n\t" \
  "v_and_b32 v99, 0xffff0000, v99\n\t" \
  "v_and_b32 v112, 0xffff, v114\n\t" \
  "v_and_b32 v113, 0xffff0000, v114\n\t" \
  "v_and_b32 v114, 0xffff, v115\n\t" \
  "v_and_b32 v115, 0xffff0000, v115\n\t" \
  "v_and_b32 v128, 0xffff, v130\n\t" \
  "v_and_b32 v129, 0xffff0000, v130\n\t" \
  "v_and_b32 v130, 0xffff, v131\n\t" \
  "v_and_b32 v131, 0xffff0000, v131\n\t" \
  "s_waitcnt lgkmcnt(8)\n\t" \
  "v_smfmac_f32_16x16x64_bf16 v[144:147], v[96:99], v[200:207], v236\n\t" \
  "v_and_b32 v100, 0xffff, v102\n\t" \
  "v_and_b32 v101, 0xffff0000, v102\n\t" \
  "v_and_b32 v102, 0xffff, v103\n\t" \
  "v_and_b32 v103, 0xffff0000, v103\n\t" \
  "s_cmp_lg_u32 s58, 1\n\t" \
  "s_cbranch_scc1 .Lcnx_c2\n\t" \
  "s_waitcnt vmcnt(5)\n\t" \
  ".Lcnx_c2:\n\t" \
  "s_waitcnt vmcnt(8) lgkmcnt(0)\n\t" \
  "s_barrier\n\t" \
  "ds_read_b64 v[224:225], v253 offset:16400\n\t" \
  "ds_read_b64 v[226:227], v253 offset:16800\n\t" \
  "ds_read_b64 v[228:229], v253 offset:17200\n\t" \
  "ds_read_b64 v[230:231], v253 offset:17600\n\t" \
  "v_smfmac_f32_16x16x64_bf16 v[148:151], v[112:115], v[208:215], v236\n\t" \
  "s_cmp_lg_u32 s58, 1\n\t" \
  "s_cbranch_scc1 .Lcnx_c3\n\t" \
  "s_add_u32 m0, s25, 73800\n\t" \
  "s_add_u32 s24, s24, s26\n\t" \
  "buffer_load_dwordx4 v254, s[8:11], s24 offen lds\n\t" \
  ".Lcnx_c3:\n\t" \
  "ds_read_b64 v[200:201], v253 offset:16408\n\t" \
  "ds_read_b64 v[202:203], v253 offset:16808\n\t" \
  "ds_read_b64 v[204:205], v253 offset:17208\n\t" \
  "ds_read_b64 v[206:207], v253 offset:17608\n\t" \
  "v_and_b32 v116, 0xffff, v118\n\t" \
  "v_and_b32 v117, 0xffff0000, v118\n\t" \
  "v_and_b32 v118, 0xffff, v119\n\t" \
  "v_and_b32 v119, 0xffff0000, v119\n\t" \
  "v_smfmac_f32_16x16x64_bf16 v[152:155], v[128:131], v[216:223], v236\n\t" \
  "s_cmp_lg_u32 s58, 1\n\t" \
  "s_cbranch_scc1 .Lcnx_c4\n\t" \
  "s_add_u32 m0, s25, 82000\n\t" \
  "s_add_u32 s24, s24, s26\n\t" \
  "buffer_load_dwordx4 v254, s[8:11], s24 offen lds\n\t" \
  ".Lcnx_c4:\n\t" \
  "ds_read_b64 v[208:209], v253 offset:16416\n\t" \
  "ds_read_b64 v[210:211], v253 offset:16816\n\t" \
  "ds_read_b64 v[212:213], v253 offset:17216\n\t" \
  "ds_read_b64 v[214:215], v253 offset:17616\n\t" \
  "v_and_b32 v132, 0xffff, v134\n\t" \
  "v_and_b32 v133, 0xffff0000, v134\n\t" \
  "v_and_b32 v134, 0xffff, v135\n\t" \
  "v_and_b32 v135, 0xffff0000, v135\n\t" \
  "s_waitcnt lgkmcnt(8)\n\t" \
  "v_smfmac_f32_16x16x64_bf16 v[156:159], v[96:99], v[224:231], v236\n\t" \
  "s_cmp_lg_u32 s58, 1\n\t" \
  "s_cbranch_scc1 .Lcnx_c5\n\t" \
  "s_add_u32 m0, s25, 90200\n\t" \
  "s_add_u32 s24, s24, s26\n\t" \
  "buffer_load_dwordx4 v254, s[8:11], s24 offen lds\n\t" \
  ".Lcnx_c5:\n\t" \
  "v_and_b32 v104, 0xffff, v106\n\t" \
  "v_and_b32 v105, 0xffff0000, v106\n\t" \
  "v_and_b32 v106, 0xffff, v107\n\t" \
  "v_and_b32 v107, 0xffff0000, v107\n\t" \
  "v_smfmac_f32_16x16x64_bf16 v[144:147], v[100:103], v[224:231], v236\n\t" \
  "s_waitcnt vmcnt(6) lgkmcnt(0)\n\t" \
  "s_barrier\n\t" \
  "ds_read_b64 v[216:217], v253 offset:32800\n\t" \
  "ds_read_b64 v[218:219], v253 offset:33200\n\t" \
  "ds_read_b64 v[220:221], v253 offset:33600\n\t" \
  "ds_read_b64 v[222:223], v253 offset:34000\n\t" \
  "s_add_u32 m0, s25, 0\n\t" \
  "s_add_u32 s24, s24, s26\n\t" \
  "v_smfmac_f32_16x16x64_bf16 v[160:163], v[112:115], v[200:207], v236\n\t" \
  "buffer_load_dwordx4 v254, s[8:11], s24 offen lds\n\t" \
  "ds_read_b64 v[224:225], v253 offset:32808\n\t" \
  "ds_read_b64 v[226:227], v253 offset:33208\n\t" \
  "v_and_b32 v120, 0xffff, v122\n\t" \
  "v_and_b32 v121, 0xffff0000, v122\n\t" \
  "v_and_b32 v122, 0xffff, v123\n\t" \
  "v_and_b32 v123, 0xffff0000, v123\n\t" \
  "v_smfmac_f32_16x16x64_bf16 v[148:151], v[116:119], v[200:207], v236\n\t" \
  "ds_read_b64 v[228:229], v253 offset:33608\n\t" \
  "ds_read_b64 v[230:231], v253 offset:34008\n\t" \
  "s_add_u32 m0, s25, 8200\n\t" \
  "s_add_u32 s24, s24, s26\n\t" \
  "v_smfmac_f32_16x16x64_bf16 v[164:167], v[128:131], v[208:215], v236\n\t" \
  "buffer_load_dwordx4 v254, s[8:11], s24 offen lds\n\t" \
  "ds_read_b64 v[200:201], v253 offset:32816\n\t" \
  "ds_read_b64 v[202:203], v253 offset:33216\n\t" \
  "v_and_b32 v136, 0xffff, v138\n\t" \
  "v_and_b32 v137, 0xffff0000, v138\n\t" \
  "v_and_b32 v138, 0xffff, v139\n\t" \
  "v_and_b32 v139, 0xffff0000, v139\n\t" \
  "v_mfma_f32_32x32x16_bf16 v[0:15], v[242:245], v[240:243], 0\n\t" \
  "v_smfmac_f32_16x16x64_bf16 v[152:155], v[132:135], v[208:215], v236\n\t" \
  "ds_read_b64 v[204:205], v253 offset:33616\n\t" \
  "s_waitcnt lgkmcnt(7)\n\t" \
  "v_smfmac_f32_16x16x64_bf16 v[168:171], v[96:99], v[216:223], v236\n\t" \
  "ds_read_b64 v[206:207], v253 offset:34016\n\t" \
  "v_and_b32 v108, 0xffff, v110\n\t" \
  "v_and_b32 v109, 0xffff0000, v110\n\t" \
  "v_and_b32 v110, 0xffff, v111\n\t" \
  "v_and_b32 v111, 0xffff0000, v111\n\t" \
  "s_add_u32 m0, s25, 16400\n\t" \
  "s_add_u32 s24, s24, s26\n\t" \
  "v_smfmac_f32_16x16x64_bf16 v[156:159], v[100:103], v[216:223], v236\n\t" \
  "buffer_load_dwordx4 v254, s[8:11], s24 offen lds\n\t" \
  "v_smfmac_f32_16x16x64_bf16 v[144:147], v[104:107], v[216:223], v236\n\t" \
  "s_waitcnt vmcnt(7) lgkmcnt(0)\n\t" \
  "s_barrier\n\t" \
  "v_smfmac_f32_16x16x64_bf16 v[172:175], v[112:115], v[224:231], v236\n\t" \
  "ds_read_b64 v[208:209], v232 offset:0\n\t" \
  "ds_read_b64 v[210:211], v232 offset:400\n\t" \
  "v_and_b32 v124, 0xffff, v126\n\t" \
  "v_and_b32 v125, 0xffff0000, v126\n\t" \
  "v_and_b32 v126, 0xffff, v127\n\t" \
  "v_and_b32 v127, 0xffff0000, v127\n\t" \
  "s_add_u32 m0, s25, 24600\n\t" \
  "s_add_u32 s24, s24, s26\n\t" \
  "v_smfmac_f32_16x16x64_bf16 v[160:163], v[116:119], v[224:231], v236\n\t" \
  "buffer_load_dwordx4 v254, s[8:11], s24 offen lds\n\t" \
  "ds_read_b64 v[212:213], v232 offset:800\n\t" \
  "ds_read_b64 v[214:215], v232 offset:1200\n\t" \
  "v_smfmac_f32_16x16x64_bf16 v[148:151], v[120:123], v[224:231], v236\n\t" \
  "ds_read_b64 v[216:217], v232 offset:8\n\t" \
  "ds_read_b64 v[218:219], v232 offset:408\n\t" \
  "v_smfmac_f32_16x16x64_bf16 v[176:179], v[128:131], v[200:207], v236\n\t" \
  "ds_read_b64 v[220:221], v232 offset:808\n\t" \
  "ds_read_b64 v[222:223], v232 offset:1208\n\t" \
  "v_and_b32 v140, 0xffff, v142\n\t" \
  "v_and_b32 v141, 0xffff0000, v142\n\t" \
  "v_and_b32 v142, 0xffff, v143\n\t" \
  "v_and_b32 v143, 0xffff0000, v143\n\t" \
  "v_smfmac_f32_16x16x64_bf16 v[164:167], v[132:135], v[200:207], v236\n\t" \
  "ds_read_b64 v[224:225], v232 offset:16\n\t" \
  "s_add_u32 m0, s25, 32800\n\t" \
  "s_add_u32 s24, s24, s26\n\t" \
  "v_smfmac_f32_16x16x64_bf16 v[152:155], v[136:139], v[200:207], v236\n\t" \
  "buffer_load_dwordx4 v254, s[8:11], s24 offen lds\n\t" \
  "ds_read_b64 v[226:227], v232 offset:416\n\t" \
  "s_waitcnt lgkmcnt(6)\n\t" \
  "v_smfmac_f32_16x16x64_bf16 v[180:183], v[96:99], v[208:215], v236\n\t" \
  "ds_read_b64 v[228:229], v232 offset:816\n\t" \
  "v_smfmac_f32_16x16x64_bf16 v[168:171], v[100:103], v[208:215], v236\n\t" \
  "ds_read_b64 v[230:231], v232 offset:1216\n\t" \
  "v_mfma_f32_32x32x16_bf16 v[16:31], v[242:245], v[240:243], 0\n\t" \
  "v_mov_b32 v196, 0x3f803f80\n\t" \
  "v_mov_b32 v197, 0x3f803f80\n\t" \
  "v_smfmac_f32_16x16x64_bf16 v[156:159], v[104:107], v[208:215], v236\n\t" \
  "v_mov_b32 v198, 0x3f803f80\n\t" \
  "v_mov_b32 v199, 0x3f803f80\n\t" \
  "s_add_u32 m0, s25, 41000\n\t" \
  "s_add_u32 s24, s24, s26\n\t" \
  "v_smfmac_f32_16x16x64_bf16 v[144:147], v[108:111], v[208:215], v236\n\t" \
  "buffer_load_dwordx4 v254, s[8:11], s24 offen lds\n\t" \
  "s_waitcnt vmcnt(8) lgkmcnt(0)\n\t" \
  "s_barrier\n\t" \
  "v_smfmac_f32_16x16x64_bf16 v[184:187], v[112:115], v[216:223], v236\n\t" \
  "ds_read_b64 v[200:201], v232 offset:16400\n\t" \
  "ds_read_b64 v[202:203], v232 offset:16800\n\t" \
  "v_smfmac_f32_16x16x64_bf16 v[172:175], v[116:119], v[216:223], v236\n\t" \
  "ds_read_b64 v[204:205], v232 offset:17200\n\t" \
  "ds_read_b64 v[206:207], v232 offset:17600\n\t" \
  "v_smfmac_f32_16x16x64_bf16 v[160:163], v[120:123], v[216:223], v236\n\t" \
  "ds_read_b64 v[208:209], v232 offset:16408\n\t" \
  "v_cvt_pk_bf16_f32 v144, v144, v145\n\t" \
  "v_cvt_pk_bf16_f32 v145, v146, v147\n\t" \
  "s_add_u32 m0, s25, 49200\n\t" \
  "s_add_u32 s24, s24, s26\n\t" \
  "v_smfmac_f32_16x16x64_bf16 v[148:151], v[124:127], v[216:223], v236\n\t" \
  "buffer_load_dwordx4 v254, s[8:11], s24 offen lds\n\t" \
  "ds_read_b64 v[210:211], v232 offset:16808\n\t" \
  "v_smfmac_f32_16x16x64_bf16 v[188:191], v[128:131], v[224:231], v236\n\t" \
  "ds_read_b64 v[212:213], v232 offset:17208\n\t" \
  "v_smfmac_f32_16x16x64_bf16 v[176:179], v[132:135], v[224:231], v236\n\t" \
  "ds_read_b64 v[214:215], v232 offset:17608\n\t" \
  "v_smfmac_f32_16x16x64_bf16 v[164:167], v[136:139], v[224:231], v236\n\t" \
  "ds_read_b64 v[216:217], v232 offset:16416\n\t" \
  "v_cvt_pk_bf16_f32 v148, v148, v149\n\t" \
  "v_cvt_pk_bf16_f32 v149, v150, v151\n\t" \
  "s_add_u32 m0, s25, 57400\n\t" \
  "s_add_u32 s24, s24, s26\n\t" \
  "v_smfmac_f32_16x16x64_bf16 v[152:155], v[140:143], v[224:231], v236\n\t" \
  "buffer_load_dwordx4 v254, s[8:11], s24 offen lds\n\t" \
  "ds_read_b64 v[218:219], v232 offset:16816\n\t" \
  "s_waitcnt lgkmcnt(6)\n\t" \
  "v_smfmac_f32_16x16x64_bf16 v[0:3], v[96:99], v[200:207], v236\n\t" \
  "ds_read_b64 v[220:221], v232 offset:17216\n\t" \
  "v_smfmac_f32_16x16x64_bf16 v[180:183], v[100:103], v[200:207], v236\n\t" \
  "ds_read_b64 v[222:223], v232 offset:17616\n\t" \
  "v_mfma_f32_32x32x16_bf16 v[32:47], v[242:245], v[240:243], 0\n\t" \
  "v_smfmac_f32_16x16x64_bf16 v[168:171], v[104:107], v[200:207], v236\n\t" \
  "v_cvt_pk_bf16_f32 v152, v152, v153\n\t" \
  "v_cvt_pk_bf16_f32 v153, v154, v155\n\t" \
  "v_smfmac_f32_16x16x64_bf16 v[156:159], v[108:111], v[200:207], v236\n\t" \
  "s_waitcnt vmcnt(8) lgkmcnt(0)\n\t" \
  "s_barrier\n\t" \
  "s_add_u32 m0, s25, 65600\n\t" \
  "s_add_u32 s24, s24, s26\n\t" \
  "v_smfmac_f32_16x16x64_bf16 v[4:7], v[112:115], v[208:215], v236\n\t" \
  "buffer_load_dwordx4 v254, s[8:11], s24 offen lds\n\t" \
  "ds_read_b64 v[224:225], v232 offset:32800\n\t" \
  "ds_read_b64 v[226:227], v232 offset:33200\n\t" \
  "v_smfmac_f32_16x16x64_bf16 v[184:187], v[116:119], v[208:215], v236\n\t" \
  "ds_read_b64 v[228:229], v232 offset:33600\n\t" \
  "ds_read_b64 v[230:231], v232 offset:34000\n\t" \
  "s_add_u32 m0, s25, 73800\n\t" \
  "s_add_u32 s24, s24, s26\n\t" \
  "v_smfmac_f32_16x16x64_bf16 v[172:175], v[120:123], v[208:215], v236\n\t" \
  "buffer_load_dwordx4 v254, s[8:11], s24 offen lds\n\t" \
  "ds_read_b64 v[200:201], v232 offset:32808\n\t" \
  "v_cvt_pk_bf16_f32 v156, v156, v157\n\t" \
  "v_cvt_pk_bf16_f32 v157, v158, v159\n\t" \
  "v_smfmac_f32_16x16x64_bf16 v[160:163], v[124:127], v[208:215], v236\n\t" \
  "ds_read_b64 v[202:203], v232 offset:33208\n\t" \
  "v_smfmac_f32_16x16x64_bf16 v[8:11], v[128:131], v[216:223], v236\n\t" \
  "ds_read_b64 v[204:205], v232 offset:33608\n\t" \
  "v_smfmac_f32_16x16x64_bf16 v[188:191], v[132:135], v[216:223], v236\n\t" \
  "ds_read_b64 v[206:207], v232 offset:34008\n\t" \
  "v_smfmac_f32_16x16x64_bf16 v[176:179], v[136:139], v[216:223], v236\n\t" \
  "buffer_store_dwordx4 v[52:55], v250, s[20:23], s61 offen offset:0 sc1\n\t" \
  "ds_read_b64 v[208:209], v232 offset:32816\n\t" \
  "v_cvt_pk_bf16_f32 v160, v160, v161\n\t" \
  "v_cvt_pk_bf16_f32 v161, v162, v163\n\t" \
  "v_smfmac_f32_16x16x64_bf16 v[164:167], v[140:143], v[216:223], v236\n\t" \
  "ds_read_b64 v[210:211], v232 offset:33216\n\t" \
  "s_waitcnt lgkmcnt(6)\n\t" \
  "v_smfmac_f32_16x16x64_bf16 v[12:15], v[96:99], v[224:231], v236\n\t" \
  "ds_read_b64 v[212:213], v232 offset:33616\n\t" \
  "v_smfmac_f32_16x16x64_bf16 v[0:3], v[100:103], v[224:231], v236\n\t" \
  "ds_read_b64 v[214:215], v232 offset:34016\n\t" \
  "v_smfmac_f32_16x16x64_bf16 v[180:183], v[104:107], v[224:231], v236\n\t" \
  "s_nop 0\n\t" \
  "v_cvt_pk_bf16_f32 v164, v164, v165\n\t" \
  "v_cvt_pk_bf16_f32 v165, v166, v167\n\t" \
  "v_smfmac_f32_16x16x64_bf16 v[168:171], v[108:111], v[224:231], v236\n\t" \
  "s_waitcnt vmcnt(9) lgkmcnt(0)\n\t" \
  "s_barrier\n\t" \
  "v_smfmac_f32_16x16x64_bf16 v[16:19], v[112:115], v[200:207], v236\n\t" \
  "ds_write_b64 v193, v[144:145] offset:12288\n\t" \
  "ds_read_b64 v[216:217], v253 offset:0\n\t" \
  "v_smfmac_f32_16x16x64_bf16 v[4:7], v[116:119], v[200:207], v236\n\t" \
  "ds_write_b64 v193, v[148:149] offset:16384\n\t" \
  "ds_read_b64 v[218:219], v253 offset:400\n\t" \
  "v_smfmac_f32_16x16x64_bf16 v[184:187], v[120:123], v[200:207], v236\n\t" \
  "ds_write_b64 v193, v[152:153] offset:20480\n\t" \
  "ds_read_b64 v[220:221], v253 offset:800\n\t" \
  "v_cvt_pk_bf16_f32 v168, v168, v169\n\t" \
  "v_cvt_pk_bf16_f32 v169, v170, v171\n\t" \
  "v_smfmac_f32_16x16x64_bf16 v[172:175], v[124:127], v[200:207], v236\n\t" \
  "ds_write_b64 v193, v[156:157] offset:0\n\t" \
  "ds_read_b64 v[222:223], v253 offset:1200\n\t" \
  "v_smfmac_f32_16x16x64_bf16 v[20:23], v[128:131], v[208:215], v236\n\t" \
  "buffer_store_dwordx4 v[60:63], v250, s[20:23], s61 offen offset:64 sc1\n\t" \
  "ds_read_b64 v[224:225], v253 offset:8\n\t" \
  "ds_read_b64 v[226:227], v253 offset:408\n\t" \
  "v_smfmac_f32_16x16x64_bf16 v[8:11], v[132:135], v[208:215], v236\n\t" \
  "ds_write_b64 v193, v[160:161] offset:4096\n\t" \
  "ds_read_b64 v[228:229], v253 offset:808\n\t" \
  "v_smfmac_f32_16x16x64_bf16 v[188:191], v[136:139], v[208:215], v236\n\t" \
  "ds_write_b64 v193, v[164:165] offset:8192\n\t" \
  "ds_read_b64 v[230:231], v253 offset:1208\n\t" \
  "v_cvt_pk_bf16_f32 v172, v172, v173\n\t" \
  "v_cvt_pk_bf16_f32 v173, v174, v175\n\t" \
  "v_smfmac_f32_16x16x64_bf16 v[176:179], v[140:143], v[208:215], v236\n\t" \
  "ds_read_b64 v[200:201], v253 offset:16\n\t" \
  "ds_read_b64 v[202:203], v253 offset:416\n\t" \
  "s_waitcnt lgkmcnt(8)\n\t" \
  "v_smfmac_f32_16x16x64_bf16 v[24:27], v[96:99], v[216:223], v236\n\t" \
  "ds_read_b64 v[204:205], v253 offset:816\n\t" \
  "v_smfmac_f32_16x16x64_bf16 v[12:15], v[100:103], v[216:223], v236\n\t" \
  "ds_read_b64 v[206:207], v253 offset:1216\n\t" \
  "v_smfmac_f32_16x16x64_bf16 v[0:3], v[104:107], v[216:223], v236\n\t" \
  "v_cvt_pk_bf16_f32 v176, v176, v177\n\t" \
  "v_cvt_pk_bf16_f32 v177, v178, v179\n\t" \
  "v_smfmac_f32_16x16x64_bf16 v[180:183], v[108:111], v[216:223], v236\n\t" \
  "s_waitcnt vmcnt(8) lgkmcnt(0)\n\t" \
  "s_barrier\n\t" \
  "v_smfmac_f32_16x16x64_bf16 v[28:31], v[112:115], v[224:231], v236\n\t" \
  "ds_write_b64 v192, v[168:169] offset:0\n\t" \
  "ds_read_b64 v[208:209], v253 offset:16400\n\t" \
  "ds_read_b128 v[144:147], v194 offset:0\n\t" \
  "v_smfmac_f32_16x16x64_bf16 v[16:19], v[116:119], v[224:231], v236\n\t" \
  "ds_write_b64 v192, v[172:173] offset:4096\n\t" \
  "ds_read_b64 v[210:211], v253 offset:16800\n\t" \
  "ds_read_b128 v[148:151], v194 offset:4096\n\t" \
  "v_smfmac_f32_16x16x64_bf16 v[4:7], v[120:123], v[224:231], v236\n\t" \
  "buffer_store_dwordx4 v[68:71], v250, s[20:23], s61 offen offset:128 sc1\n\t" \
  "ds_read_b64 v[212:213], v253 offset:17200\n\t" \
  "ds_read_b64 v[214:215], v253 offset:17600\n\t" \
  "v_cvt_pk_bf16_f32 v180, v180, v181\n\t" \
  "v_cvt_pk_bf16_f32 v181, v182, v183\n\t" \
  "ds_read_b128 v[152:155], v194 offset:8192\n\t" \
  "v_smfmac_f32_16x16x64_bf16 v[184:187], v[124:127], v[224:231], v236\n\t" \
  "ds_write_b64 v192, v[176:177] offset:8192\n\t" \
  "ds_read_b64 v[216:217], v253 offset:16408\n\t" \
  "v_add_u32 v194, -86112, v194\n\t" \
  "v_mfma_f32_16x16x32_bf16 v[160:163], v[242:245], v[240:243], 0.5\n\t" \
  "ds_read_b64 v[218:219], v253 offset:16808\n\t" \
  "v_smfmac_f32_16x16x64_bf16 v[32:35], v[128:131], v[200:207], v236\n\t" \
  "ds_read_b64 v[220:221], v253 offset:17208\n\t" \
  "v_mfma_f32_16x16x32_bf16 v[164:167], v[242:245], v[240:243], 0.5\n\t" \
  "ds_read_b64 v[222:223], v253 offset:17608\n\t" \
  "v_smfmac_f32_16x16x64_bf16 v[20:23], v[132:135], v[200:207], v236\n\t" \
  "ds_read_b64 v[224:225], v253 offset:16416\n\t" \
  "s_waitcnt lgkmcnt(12)\n\t" \
  "v_mfma_f32_16x16x32_bf16 v[168:171], v[144:147], v[144:147], 0\n\t" \
  "ds_read_b64 v[226:227], v253 offset:16816\n\t" \
  "v_smfmac_f32_16x16x64_bf16 v[8:11], v[136:139], v[200:207], v236\n\t" \
  "ds_read_b64 v[228:229], v253 offset:17216\n\t" \
  "v_cvt_pk_bf16_f32 v184, v184, v185\n\t" \
  "v_cvt_pk_bf16_f32 v185, v186, v187\n\t" \
  "v_mfma_f32_16x16x32_bf16 v[172:175], v[196:199], v[144:147], 0\n\t" \
  "ds_read_b64 v[230:231], v253 offset:17616\n\t" \
  "v_smfmac_f32_16x16x64_bf16 v[188:191], v[140:143], v[200:207], v236\n\t" \
  "s_waitcnt lgkmcnt(12)\n\t" \
  "v_mfma_f32_16x16x32_bf16 v[168:171], v[148:151], v[148:151], v[168:171]\n\t" \
  "s_waitcnt lgkmcnt(10)\n\t" \
  "v_smfmac_f32_16x16x64_bf16 v[36:39], v[96:99], v[208:215], v236\n\t" \
  "v_mfma_f32_16x16x32_bf16 v[172:175], v[196:199], v[148:151], v[172:175]\n\t" \
  "v_smfmac_f32_16x16x64_bf16 v[24:27], v[100:103], v[208:215], v236\n\t" \
  "s_waitcnt lgkmcnt(9)\n\t" \
  "v_mfma_f32_16x16x32_bf16 v[168:171], v[152:155], v[152:155], v[168:171]\n\t" \
  "v_smfmac_f32_16x16x64_bf16 v[12:15], v[104:107], v[208:215], v236\n\t" \
  "v_cvt_pk_bf16_f32 v188, v188, v189\n\t" \
  "v_cvt_pk_bf16_f32 v189, v190, v191\n\t" \
  "v_mfma_f32_16x16x32_bf16 v[172:175], v[196:199], v[152:155], v[172:175]\n\t" \
  "v_smfmac_f32_16x16x64_bf16 v[0:3], v[108:111], v[208:215], v236\n\t" \
  "v_mfma_f32_32x32x16_bf16 v[144:159], v[242:245], v[240:243], 0.5\n\t" \
  "buffer_store_dwordx4 v[76:79], v250, s[20:23], s62 offen offset:0 sc1\n\t" \
  "s_waitcnt vmcnt(8) lgkmcnt(0)\n\t" \
  "s_barrier\n\t" \
  "s_cmp_lg_u32 s58, 0\n\t" \
  "s_cbranch_scc1 .Lcnx_c6\n\t" \
  "v_lshl_add_u32 v48, v233, 1, s63\n\t" \
  "s_lshl_b32 m0, s39, 1\n\t" \
  "v_add_u32 v49, 0x2000, v48\n\t" \
  "global_load_lds_dwordx4 v48, s[6:7]\n\t" \
  "s_add_u32 m0, m0, 0x2000\n\t" \
  "s_cmp_eq_u32 s39, 3584\n\t" \
  "s_cselect_b32 exec_hi, 1, -1\n\t" \
  "global_load_lds_dwordx4 v49, s[6:7]\n\t" \
  "s_mov_b64 exec, -1\n\t" \
  ".Lcnx_c6:\n\t" \
  "v_smfmac_f32_16x16x64_bf16 v[40:43], v[112:115], v[216:223], v236\n\t" \
  "ds_write_b64 v192, v[180:181] offset:12288\n\t" \
  "ds_read_b64 v[200:201], v253 offset:32800\n\t" \
  "v_smfmac_f32_16x16x64_bf16 v[28:31], v[116:119], v[216:223], v236\n\t" \
  "ds_write_b64 v192, v[184:185] offset:16384\n\t" \
  "ds_read_b64 v[202:203], v253 offset:33200\n\t" \
  "v_cndmask_b32 v168, v168, v169, s[40:41]\n\t" \
  "v_cndmask_b32 v170, v170, v171, s[40:41]\n\t" \
  "v_smfmac_f32_16x16x64_bf16 v[16:19], v[120:123], v[216:223], v236\n\t" \
  "ds_write_b64 v192, v[188:189] offset:20480\n\t" \
  "ds_read_b64 v[204:205], v253 offset:33600\n\t" \
  "v_cvt_pk_bf16_f32 v0, v0, v1\n\t" \
  "v_cvt_pk_bf16_f32 v1, v2, v3\n\t" \
  "v_cndmask_b32 v173, v168, v170, s[42:43]\n\t" \
  "v_mov_b32 v174, 0x3927c5ac\n\t" \
  "ds_read_b64_tr_b16 v[144:145], v195 offset:0\n\t" \
  "v_smfmac_f32_16x16x64_bf16 v[4:7], v[124:127], v[216:223], v236\n\t" \
  "ds_read_b64 v[206:207], v253 offset:34000\n\t" \
  "ds_read_b64 v[208:209], v253 offset:32808\n\t" \
  "s_not_b64 vcc, s[52:53]\n\t" \
  "ds_read_b64_tr_b16 v[148:149], v195 offset:4096\n\t" \
  "v_smfmac_f32_16x16x64_bf16 v[44:47], v[128:131], v[224:231], v236\n\t" \
  "ds_read_b64 v[210:211], v253 offset:33208\n\t" \
  "ds_read_b64 v[212:213], v253 offset:33608\n\t" \
  "ds_read_b64_tr_b16 v[152:153], v195 offset:2048\n\t" \
  "v_smfmac_f32_16x16x64_bf16 v[32:35], v[132:135], v[224:231], v236\n\t" \
  "ds_read_b64 v[214:215], v253 offset:34008\n\t" \
  "ds_read_b64 v[216:217], v253 offset:32816\n\t" \
  "ds_read_b64_tr_b16 v[156:157], v195 offset:6144\n\t" \
  "v_smfmac_f32_16x16x64_bf16 v[20:23], v[136:139], v[224:231], v236\n\t" \
  "ds_read_b64 v[218:219], v253 offset:33216\n\t" \
  "v_cvt_pk_bf16_f32 v4, v4, v5\n\t" \
  "v_cvt_pk_bf16_f32 v5, v6, v7\n\t" \
  "ds_read_b64_tr_b16 v[160:161], v195 offset:8192\n\t" \
  "v_smfmac_f32_16x16x64_bf16 v[8:11], v[140:143], v[224:231], v236\n\t" \
  "ds_read_b64 v[220:221], v253 offset:33616\n\t" \
  "ds_read_b64_tr_b16 v[164:165], v195 offset:10240\n\t" \
  "s_waitcnt lgkmcnt(12)\n\t" \
  "v_smfmac_f32_16x16x64_bf16 v[36:39], v[100:103], v[200:207], v236\n\t" \
  "ds_read_b64 v[222:223], v253 offset:34016\n\t" \
  "v_add_u32 v195, -86112, v195\n\t" \
  "v_smfmac_f32_16x16x64_bf16 v[24:27], v[104:107], v[200:207], v236\n\t" \
  "v_smfmac_f32_16x16x64_bf16 v[12:15], v[108:111], v[200:207], v236\n\t" \
  "buffer_store_dwordx4 v[84:87], v250, s[20:23], s62 offen offset:64 sc1\n\t" \
  "v_cvt_pk_bf16_f32 v8, v8, v9\n\t" \
  "v_cvt_pk_bf16_f32 v9, v10, v11\n\t" \
  "s_waitcnt vmcnt(7) lgkmcnt(0)\n\t" \
  "s_barrier\n\t" \
  "s_cmp_ge_u32 s36, 2\n\t" \
  "s_cbranch_scc1 .Lcnx_c7\n\t" \
  "s_mov_b32 m0, s46\n\t" \
  "s_nop 0\n\t" \
  "buffer_load_dwordx4 v252, s[16:19], s33 offen lds\n\t" \
  "buffer_load_dwordx4 v252, s[16:19], s34 offen offset:1024 lds\n\t" \
  "buffer_load_dwordx4 v252, s[16:19], s35 offen offset:2048 lds\n\t" \
  "s_add_u32 s33, s33, s32\n\t" \
  "s_add_u32 s34, s34, s32\n\t" \
  "s_add_u32 s35, s35, s32\n\t" \
  "s_mov_b32 m0, s47\n\t" \
  "s_nop 0\n\t" \
  "buffer_load_dwordx4 v252, s[16:19], s33 offen lds\n\t" \
  "buffer_load_dwordx4 v252, s[16:19], s34 offen offset:1024 lds\n\t" \
  "buffer_load_dwordx4 v252, s[16:19], s35 offen offset:2048 lds\n\t" \
  "s_add_u32 s33, s33, s32\n\t" \
  "s_add_u32 s34, s34, s32\n\t" \
  "s_add_u32 s35, s35, s32\n\t" \
  ".Lcnx_c7:\n\t" \
  "v_smfmac_f32_16x16x64_bf16 v[40:43], v[116:119], v[208:215], v236\n\t" \
  "ds_write_b64 v192, v[0:1] offset:24576\n\t" \
  "ds_read_b64 v[224:225], v232 offset:0\n\t" \
  "ds_read_b128 v[180:183], v194 offset:0\n\t" \
  "ds_read_b128 v[184:187], v194 offset:4096\n\t" \
  "ds_read_b128 v[188:191], v194 offset:8192\n\t" \
  "v_smfmac_f32_16x16x64_bf16 v[28:31], v[120:123], v[208:215], v236\n\t" \
  "ds_write_b64 v192, v[4:5] offset:28672\n\t" \
  "ds_read_b64 v[226:227], v232 offset:400\n\t" \
  "ds_read_b64 v[228:229], v232 offset:800\n\t" \
  "v_smfmac_f32_16x16x64_bf16 v[16:19], v[124:127], v[208:215], v236\n\t" \
  "ds_write_b64 v192, v[8:9] offset:32768\n\t" \
  "ds_read_b64 v[230:231], v232 offset:1200\n\t" \
  "ds_read_b64 v[200:201], v232 offset:8\n\t" \
  "v_cvt_pk_bf16_f32 v12, v12, v13\n\t" \
  "v_cvt_pk_bf16_f32 v13, v14, v15\n\t" \
  "v_smfmac_f32_16x16x64_bf16 v[44:47], v[132:135], v[216:223], v236\n\t" \
  "ds_write_b64 v192, v[12:13] offset:36864\n\t" \
  "v_mfma_f32_32x32x16_bf16 v[0:15], v[242:245], v[240:243], 0.5\n\t" \
  "ds_read_b64 v[202:203], v232 offset:408\n\t" \
  "ds_read_b64 v[204:205], v232 offset:808\n\t" \
  "s_waitcnt lgkmcnt(11)\n\t" \
  "v_mfma_f32_16x16x32_bf16 v[176:179], v[180:183], v[180:183], 0\n\t" \
  "ds_read_b64 v[206:207], v232 offset:1208\n\t" \
  "v_mfma_f32_16x16x32_bf16 v[168:171], v[196:199], v[180:183], 0\n\t" \
  "ds_read_b64 v[208:209], v232 offset:16\n\t" \
  "v_smfmac_f32_16x16x64_bf16 v[32:35], v[136:139], v[216:223], v236\n\t" \
  "ds_read_b64 v[210:211], v232 offset:416\n\t" \
  "ds_read_b64 v[212:213], v232 offset:816\n\t" \
  "s_waitcnt lgkmcnt(14)\n\t" \
  "v_mfma_f32_16x16x32_bf16 v[176:179], v[184:187], v[184:187], v[176:179]\n\t" \
  "ds_read_b64 v[214:215], v232 offset:1216\n\t" \
  "v_mfma_f32_16x16x32_bf16 v[168:171], v[196:199], v[184:187], v[168:171]\n\t" \
  "v_smfmac_f32_16x16x64_bf16 v[20:23], v[140:143], v[216:223], v236\n\t" \
  "v_cvt_pk_bf16_f32 v16, v16, v17\n\t" \
  "v_cvt_pk_bf16_f32 v17, v18, v19\n\t" \
  "s_waitcnt lgkmcnt(14)\n\t" \
  "v_mfma_f32_16x16x32_bf16 v[176:179], v[188:191], v[188:191], v[176:179]\n\t" \
  "ds_write_b64 v192, v[16:17] offset:40960\n\t" \
  "v_mfma_f32_16x16x32_bf16 v[16:19], v[242:245], v[240:243], 0.5\n\t" \
  "v_mfma_f32_16x16x32_bf16 v[168:171], v[196:199], v[188:191], v[168:171]\n\t" \
  "s_waitcnt lgkmcnt(10)\n\t" \
  "v_smfmac_f32_16x16x64_bf16 v[36:39], v[104:107], v[224:231], v236\n\t" \
  "v_smfmac_f32_16x16x64_bf16 v[24:27], v[108:111], v[224:231], v236\n\t" \
  "s_nop 1\n\t" \
  "v_cndmask_b32 v176, v176, v177, s[40:41]\n\t" \
  "v_cndmask_b32 v178, v178, v179, s[40:41]\n\t" \
  "v_cndmask_b32 v176, v176, v178, s[42:43]\n\t" \
  "v_cndmask_b32_dpp v172, v168, v172, vcc row_ror:8 row_mask:0xf bank_mask:0xf\n\t" \
  "v_mul_f32 v172, 0x3d2aaaab, v172\n\t" \
  "s_waitcnt vmcnt(5) lgkmcnt(0)\n\t" \
  "s_barrier\n\t" \
  "ds_read_b64 v[216:217], v232 offset:16400\n\t" \
  "ds_read_b64 v[218:219], v232 offset:16800\n\t" \
  "ds_read_b64 v[220:221], v232 offset:17200\n\t" \
  "ds_read_b64 v[222:223], v232 offset:17600\n\t" \
  "v_smfmac_f32_16x16x64_bf16 v[40:43], v[120:123], v[200:207], v236\n\t" \
  "ds_read_b64 v[224:225], v232 offset:16408\n\t" \
  "ds_read_b64 v[226:227], v232 offset:16808\n\t" \
  "v_cvt_pk_bf16_f32 v20, v20, v21\n\t" \
  "v_cvt_pk_bf16_f32 v21, v22, v23\n\t" \
  "v_cndmask_b32_dpp v173, v176, v173, vcc row_ror:8 row_mask:0xf bank_mask:0xf\n\t" \
  "v_fmamk_f32 v173, v173, 0x3e2aaaab, v174\n\t" \
  "v_fma_f32 v173, -v172, v172, v173\n\t" \
  "v_max_f32 v173, v174, v173\n\t" \
  "v_rsq_f32 v173, v173 div:2\n\t" \
  "v_smfmac_f32_16x16x64_bf16 v[28:31], v[124:127], v[200:207], v236\n\t" \
  "buffer_store_dwordx4 v[92:95], v250, s[20:23], s62 offen offset:128 sc1\n\t" \
  "ds_read_b64 v[228:229], v232 offset:17208\n\t" \
  "ds_read_b64 v[230:231], v232 offset:17608\n\t" \
  "s_mov_b64 vcc, s[40:41]\n\t" \
  "v_cvt_pk_bf16_f32 v175, v173, v173\n\t" \
  "v_and_b32 v173, 0xffff0000, v175\n\t" \
  "v_mul_f32 v174, -v172, v173 div:2\n\t" \
  "v_smfmac_f32_16x16x64_bf16 v[44:47], v[136:139], v[208:215], v236\n\t" \
  "ds_write_b64 v192, v[20:21] offset:45056\n\t" \
  "v_mfma_f32_16x16x32_bf16 v[20:23], v[242:245], v[240:243], 0.5\n\t" \
  "ds_read_b64 v[200:201], v232 offset:16416\n\t" \
  "v_cvt_pk_bf16_f32 v24, v24, v25\n\t" \
  "v_cvt_pk_bf16_f32 v25, v26, v27\n\t" \
  "v_cndmask_b32_sdwa v175, v175, v173, vcc dst_sel:DWORD dst_unused:UNUSED_PAD src0_sel:WORD_1 src1_sel:DWORD\n\t" \
  "v_and_b32 v172, 0xffff0000, v174\n\t" \
  "s_or_b64 vcc, s[52:53], s[42:43]\n\t" \
  "v_sub_f32 v174, v174, v172\n\t" \
  "v_cndmask_b32 v180, v175, v240, vcc\n\t" \
  "v_smfmac_f32_16x16x64_bf16 v[32:35], v[140:143], v[208:215], v236\n\t" \
  "ds_write_b64 v192, v[24:25] offset:49152\n\t" \
  "ds_read_b64 v[202:203], v232 offset:16816\n\t" \
  "v_cndmask_b32_dpp v184, v175, v240, vcc row_ror:8 row_mask:0xf bank_mask:0xf\n\t" \
  "s_orn2_b64 vcc, s[52:53], s[42:43]\n\t" \
  "v_cndmask_b32 v181, v175, v240, vcc\n\t" \
  "v_cndmask_b32_dpp v185, v175, v240, vcc row_ror:8 row_mask:0xf bank_mask:0xf\n\t" \
  "s_mov_b64 vcc, s[52:53]\n\t" \
  "ds_read_b64_tr_b16 v[0:1], v195 offset:0\n\t" \
  "ds_read_b64_tr_b16 v[4:5], v195 offset:4096\n\t" \
  "s_waitcnt lgkmcnt(10)\n\t" \
  "v_smfmac_f32_16x16x64_bf16 v[36:39], v[108:111], v[216:223], v236\n\t" \
  "ds_read_b64 v[204:205], v232 offset:17216\n\t" \
  "ds_read_b64 v[206:207], v232 offset:17616\n\t" \
  "v_cvt_pk_bf16_f32 v28, v28, v29\n\t" \
  "v_cvt_pk_bf16_f32 v29, v30, v31\n\t" \
  "v_cndmask_b32 v182, v172, v240, vcc\n\t" \
  "v_cndmask_b32_dpp v186, v172, v240, vcc row_ror:8 row_mask:0xf bank_mask:0xf\n\t" \
  "v_cndmask_b32 v183, v174, v240, vcc\n\t" \
  "v_cndmask_b32_dpp v187, v174, v240, vcc row_ror:8 row_mask:0xf bank_mask:0xf\n\t" \
  "ds_read_b64_tr_b16 v[8:9], v195 offset:2048\n\t" \
  "ds_read_b64_tr_b16 v[12:13], v195 offset:6144\n\t" \
  "s_waitcnt lgkmcnt(10)\n\t" \
  "v_smfmac_f32_16x16x64_bf16 v[40:43], v[124:127], v[224:231], v236\n\t" \
  "ds_write_b64 v192, v[28:29] offset:53248\n\t" \
  "ds_read_b64_tr_b16 v[16:17], v195 offset:8192\n\t" \
  "ds_read_b64_tr_b16 v[20:21], v195 offset:10240\n\t" \
  "v_mfma_f32_16x16x32_bf16 v[188:191], v[144:147], v[180:183], 0\n\t" \
  "s_waitcnt lgkmcnt(5)\n\t" \
  "v_smfmac_f32_16x16x64_bf16 v[44:47], v[140:143], v[200:207], v236\n\t" \
  "v_cvt_pk_bf16_f32 v32, v32, v33\n\t" \
  "v_cvt_pk_bf16_f32 v33, v34, v35\n\t" \
  "v_mfma_f32_16x16x32_bf16 v[176:179], v[148:151], v[180:183], 0\n\t" \
  "ds_write_b64 v192, v[32:33] offset:57344\n\t" \
  "v_cvt_pk_bf16_f32 v36, v36, v37\n\t" \
  "v_cvt_pk_bf16_f32 v37, v38, v39\n\t" \
  "ds_write_b64 v193, v[36:37] offset:0\n\t" \
  "v_mfma_f32_32x32x16_bf16 v[24:39], v[242:245], v[240:243], 0.5\n\t" \
  "v_cvt_pk_bf16_f32 v40, v40, v41\n\t" \
  "v_cvt_pk_bf16_f32 v41, v42, v43\n\t" \
  "ds_write_b64 v193, v[40:41] offset:4096\n\t" \
  "v_mfma_f32_16x16x32_bf16 v[40:43], v[242:245], v[240:243], 0.5\n\t" \
  "v_mfma_f32_16x16x32_bf16 v[168:171], v[152:155], v[180:183], 0\n\t" \
  "v_cvt_pk_bf16_f32 v96, v188, v189\n\t" \
  "v_cvt_pk_bf16_f32 v97, v190, v191\n\t" \
  "v_mfma_f32_16x16x32_bf16 v[172:175], v[156:159], v[180:183], 0\n\t" \
  "v_cvt_pk_bf16_f32 v44, v44, v45\n\t" \
  "v_cvt_pk_bf16_f32 v45, v46, v47\n\t" \
  "ds_write_b64 v193, v[44:45] offset:8192\n\t" \
  "v_mfma_f32_16x16x32_bf16 v[44:47], v[242:245], v[240:243], 0.5\n\t" \
  "v_cvt_pk_bf16_f32 v98, v176, v177\n\t" \
  "v_cvt_pk_bf16_f32 v99, v178, v179\n\t" \
  "v_mfma_f32_16x16x32_bf16 v[188:191], v[160:163], v[180:183], 0\n\t" \
  "v_cvt_pk_bf16_f32 v112, v168, v169\n\t" \
  "v_cvt_pk_bf16_f32 v113, v170, v171\n\t" \
  "v_mfma_f32_16x16x32_bf16 v[176:179], v[164:167], v[180:183], 0\n\t" \
  "v_cvt_pk_bf16_f32 v114, v172, v173\n\t" \
  "v_cvt_pk_bf16_f32 v115, v174, v175\n\t" \
  "s_nop 2\n\t" \
  "v_cvt_pk_bf16_f32 v128, v188, v189\n\t" \
  "v_cvt_pk_bf16_f32 v129, v190, v191\n\t" \
  "s_nop 0\n\t" \
  "v_cvt_pk_bf16_f32 v130, v176, v177\n\t" \
  "v_cvt_pk_bf16_f32 v131, v178, v179\n\t" \
  "s_cmp_lg_u32 s58, 0\n\t" \
  "s_cbranch_scc1 .Lcnx_c8\n\t" \
  "s_waitcnt vmcnt(0)\n\t" \
  ".Lcnx_c8:\n\t" \
  "s_waitcnt lgkmcnt(0)\n\t" \
  "s_barrier\n\t" \
  "ds_read_b128 v[104:107], v194 offset:24576\n\t" \
  "ds_read_b128 v[120:123], v194 offset:28672\n\t" \
  "ds_read_b128 v[136:139], v194 offset:32768\n\t" \
  "v_add_u32 v194, s54, v194\n\t" \
  "ds_read_b128 v[108:111], v194 offset:49152\n\t" \
  "ds_read_b128 v[124:127], v194 offset:53248\n\t" \
  "ds_read_b128 v[140:143], v194 offset:57344\n\t" \
  "v_mfma_f32_16x16x32_bf16 v[168:171], v[0:3], v[184:187], 0\n\t" \
  "ds_read_b64_tr_b16 v[208:209], v248 offset:43048\n\t" \
  "ds_read_b64_tr_b16 v[210:211], v248 offset:44072\n\t" \
  "v_mfma_f32_16x16x32_bf16 v[172:175], v[4:7], v[184:187], 0\n\t" \
  "ds_read_b64_tr_b16 v[212:213], v248 offset:46120\n\t" \
  "ds_read_b64_tr_b16 v[214:215], v248 offset:47144\n\t" \
  "s_cmp_eq_u32 s37, 0\n\t" \
  "s_cbranch_scc1 .Lcnx_f0\n.Lcnx_g0:\n\t" \
  "v_mfma_f32_16x16x32_bf16 v[188:191], v[8:11], v[184:187], 0\n\t" \
  "ds_read_b64_tr_b16 v[216:217], v248 offset:45096\n\t" \
  "ds_read_b64_tr_b16 v[218:219], v248 offset:48168\n\t" \
  "v_cvt_pk_bf16_f32 v100, v168, v169\n\t" \
  "v_cvt_pk_bf16_f32 v101, v170, v171\n\t" \
  "v_mfma_f32_16x16x32_bf16 v[176:179], v[12:15], v[184:187], 0\n\t" \
  "ds_read_b64_tr_b16 v[24:25], v195 offset:24576\n\t" \
  "ds_read_b64_tr_b16 v[28:29], v195 offset:28672\n\t" \
  "v_cvt_pk_bf16_f32 v102, v172, v173\n\t" \
  "v_cvt_pk_bf16_f32 v103, v174, v175\n\t" \
  "v_mfma_f32_16x16x32_bf16 v[168:171], v[16:19], v[184:187], 0\n\t" \
  "ds_read_b64_tr_b16 v[32:33], v195 offset:26624\n\t" \
  "v_cvt_pk_bf16_f32 v116, v188, v189\n\t" \
  "v_cvt_pk_bf16_f32 v117, v190, v191\n\t" \
  "v_mfma_f32_16x16x32_bf16 v[172:175], v[20:23], v[184:187], 0\n\t" \
  "ds_read_b64_tr_b16 v[36:37], v195 offset:30720\n\t" \
  "v_cvt_pk_bf16_f32 v118, v176, v177\n\t" \
  "v_cvt_pk_bf16_f32 v119, v178, v179\n\t" \
  "s_waitcnt lgkmcnt(15)\n\t" \
  "v_mfma_f32_16x16x32_bf16 v[200:203], v[104:107], v[104:107], 0\n\t" \
  "buffer_store_dwordx4 v[48:51], v250, s[20:23], s14 offen offset:0 sc1\n\t" \
  "ds_read_b64_tr_b16 v[40:41], v195 offset:32768\n\t" \
  "ds_read_b64_tr_b16 v[44:45], v195 offset:34816\n\t" \
  "v_cvt_pk_bf16_f32 v132, v168, v169\n\t" \
  "v_cvt_pk_bf16_f32 v133, v170, v171\n\t" \
  "v_mfma_f32_16x16x32_bf16 v[204:207], v[196:199], v[104:107], 0\n\t" \
  "v_add_u32 v195, s54, v195\n\t" \
  "ds_read_b64_tr_b16 v[160:161], v248 offset:43560\n\t" \
  "ds_read_b64_tr_b16 v[162:163], v248 offset:44584\n\t" \
  "v_cvt_pk_bf16_f32 v134, v172, v173\n\t" \
  "v_cvt_pk_bf16_f32 v135, v174, v175\n\t" \
  "s_waitcnt lgkmcnt(15)\n\t" \
  "v_mfma_f32_16x16x32_bf16 v[200:203], v[120:123], v[120:123], v[200:203]\n\t" \
  "ds_read_b64_tr_b16 v[164:165], v248 offset:46632\n\t" \
  "ds_read_b64_tr_b16 v[166:167], v248 offset:47656\n\t" \
  "v_mfma_f32_16x16x32_bf16 v[204:207], v[196:199], v[120:123], v[204:207]\n\t" \
  "ds_read_b64_tr_b16 v[232:233], v248 offset:45608\n\t" \
  "ds_read_b64_tr_b16 v[234:235], v248 offset:48680\n\t" \
  "v_mfma_f32_16x16x32_bf16 v[200:203], v[136:139], v[136:139], v[200:203]\n\t" \
  "ds_read_b64_tr_b16 v[0:1], v195 offset:49152\n\t" \
  "ds_read_b64_tr_b16 v[4:5], v195 offset:53248\n\t" \
  "v_mfma_f32_16x16x32_bf16 v[204:207], v[196:199], v[136:139], v[204:207]\n\t" \
  "ds_read_b64_tr_b16 v[8:9], v195 offset:51200\n\t" \
  "v_mfma_f32_16x16x32_bf16 v[220:223], v[108:111], v[108:111], 0\n\t" \
  "ds_read_b64_tr_b16 v[12:13], v195 offset:55296\n\t" \
  "v_mfma_f32_16x16x32_bf16 v[224:227], v[196:199], v[108:111], 0\n\t" \
  "ds_read_b64_tr_b16 v[16:17], v195 offset:57344\n\t" \
  "v_mfma_f32_16x16x32_bf16 v[220:223], v[124:127], v[124:127], v[220:223]\n\t" \
  "ds_read_b64_tr_b16 v[20:21], v195 offset:59392\n\t" \
  "v_cndmask_b32 v200, v200, v201, s[40:41]\n\t" \
  "v_cndmask_b32 v202, v202, v203, s[40:41]\n\t" \
  "v_mfma_f32_16x16x32_bf16 v[224:227], v[196:199], v[124:127], v[224:227]\n\t" \
  "v_cndmask_b32 v205, v200, v202, s[42:43]\n\t" \
  "v_mov_b32 v206, 0x3927c5ac\n\t" \
  "s_waitcnt lgkmcnt(15)\n\t" \
  "v_mfma_f32_16x16x32_bf16 v[220:223], v[140:143], v[140:143], v[220:223]\n\t" \
  "s_not_b64 vcc, s[52:53]\n\t" \
  "v_mfma_f32_16x16x32_bf16 v[224:227], v[196:199], v[140:143], v[224:227]\n\t" \
  "v_mfma_f32_16x16x32_bf16 v[144:147], v[208:211], v[96:99], 0\n\t" \
  "v_mfma_f32_16x16x32_bf16 v[152:155], v[208:211], v[100:103], 0\n\t" \
  "s_nop 3\n\t" \
  "v_cndmask_b32 v220, v220, v221, s[40:41]\n\t" \
  "v_cndmask_b32 v222, v222, v223, s[40:41]\n\t" \
  "v_cndmask_b32 v220, v220, v222, s[42:43]\n\t" \
  "v_mfma_f32_16x16x32_bf16 v[144:147], v[212:215], v[112:115], v[144:147]\n\t" \
  "v_cndmask_b32_dpp v204, v224, v204, vcc row_ror:8 row_mask:0xf bank_mask:0xf\n\t" \
  "v_mul_f32 v204, 0x3d2aaaab, v204\n\t" \
  "v_cndmask_b32_dpp v205, v220, v205, vcc row_ror:8 row_mask:0xf bank_mask:0xf\n\t" \
  "v_mfma_f32_16x16x32_bf16 v[152:155], v[212:215], v[116:119], v[152:155]\n\t" \
  "v_fmamk_f32 v205, v205, 0x3e2aaaab, v206\n\t" \
  "v_fma_f32 v205, -v204, v204, v205\n\t" \
  "v_max_f32 v205, v206, v205\n\t" \
  "v_mfma_f32_16x16x32_bf16 v[144:147], v[216:219], v[128:131], v[144:147]\n\t" \
  "buffer_store_dwordx4 v[56:59], v250, s[20:23], s14 offen offset:64 sc1\n\t" \
  "v_rsq_f32 v205, v205 div:2\n\t" \
  "s_mov_b64 vcc, s[40:41]\n\t" \
  "v_cvt_pk_bf16_f32 v207, v205, v205\n\t" \
  "v_mfma_f32_16x16x32_bf16 v[152:155], v[216:219], v[132:135], v[152:155]\n\t" \
  "v_and_b32 v205, 0xffff0000, v207\n\t" \
  "v_mul_f32 v206, -v204, v205 div:2\n\t" \
  "v_cndmask_b32_sdwa v207, v207, v205, vcc dst_sel:DWORD dst_unused:UNUSED_PAD src0_sel:WORD_1 src1_sel:DWORD\n\t" \
  "s_waitcnt lgkmcnt(10)\n\t" \
  "v_mfma_f32_16x16x32_bf16 v[148:151], v[160:163], v[96:99], 0\n\t" \
  "v_and_b32 v204, 0xffff0000, v206\n\t" \
  "s_or_b64 vcc, s[52:53], s[42:43]\n\t" \
  "v_sub_f32 v206, v206, v204\n\t" \
  "v_mfma_f32_16x16x32_bf16 v[156:159], v[160:163], v[100:103], 0\n\t" \
  "v_cndmask_b32 v180, v207, v240, vcc\n\t" \
  "v_cndmask_b32_dpp v184, v207, v240, vcc row_ror:8 row_mask:0xf bank_mask:0xf\n\t" \
  "s_orn2_b64 vcc, s[52:53], s[42:43]\n\t" \
  "s_waitcnt lgkmcnt(8)\n\t" \
  "v_mfma_f32_16x16x32_bf16 v[148:151], v[164:167], v[112:115], v[148:151]\n\t" \
  "v_cndmask_b32 v181, v207, v240, vcc\n\t" \
  "v_cndmask_b32_dpp v185, v207, v240, vcc row_ror:8 row_mask:0xf bank_mask:0xf\n\t" \
  "v_mfma_f32_16x16x32_bf16 v[156:159], v[164:167], v[116:119], v[156:159]\n\t" \
  "s_mov_b64 vcc, s[52:53]\n\t" \
  "v_cndmask_b32 v182, v204, v240, vcc\n\t" \
  "s_waitcnt lgkmcnt(6)\n\t" \
  "v_mfma_f32_16x16x32_bf16 v[148:151], v[232:235], v[128:131], v[148:151]\n\t" \
  "v_cndmask_b32_dpp v186, v204, v240, vcc row_ror:8 row_mask:0xf bank_mask:0xf\n\t" \
  "v_cndmask_b32 v183, v206, v240, vcc\n\t" \
  "v_mfma_f32_16x16x32_bf16 v[156:159], v[232:235], v[132:135], v[156:159]\n\t" \
  "v_cndmask_b32_dpp v187, v206, v240, vcc row_ror:8 row_mask:0xf bank_mask:0xf\n\t" \
  "s_mov_b64 vcc, 0\n\t" \
  "s_waitcnt lgkmcnt(0)\n\t" \
  "v_mfma_f32_16x16x32_bf16 v[188:191], v[24:27], v[180:183], 0\n\t" \
  "v_mfma_f32_16x16x32_bf16 v[176:179], v[28:31], v[180:183], 0\n\t" \
  "v_mfma_f32_16x16x32_bf16 v[200:203], v[0:3], v[184:187], 0\n\t" \
  "s_nop 5\n\t" \
  "v_cvt_pk_bf16_f32 v104, v188, v189\n\t" \
  "v_cvt_pk_bf16_f32 v105, v190, v191\n\t" \
  "v_mfma_f32_16x16x32_bf16 v[204:207], v[4:7], v[184:187], 0\n\t" \
  "v_cvt_pk_bf16_f32 v106, v176, v177\n\t" \
  "v_cvt_pk_bf16_f32 v107, v178, v179\n\t" \
  "v_mfma_f32_16x16x32_bf16 v[196:199], v[32:35], v[180:183], 0\n\t" \
  "v_cvt_pk_bf16_f32 v108, v200, v201\n\t" \
  "v_cvt_pk_bf16_f32 v109, v202, v203\n\t" \
  "v_mfma_f32_16x16x32_bf16 v[188:191], v[36:39], v[180:183], 0\n\t" \
  "s_nop 1\n\t" \
  "v_cvt_pk_bf16_f32 v110, v204, v205\n\t" \
  "v_mfma_f32_16x16x32_bf16 v[160:163], v[208:211], v[104:107], 0\n\t" \
  "v_cvt_pk_bf16_f32 v111, v206, v207\n\t" \
  "v_mfma_f32_16x16x32_bf16 v[176:179], v[8:11], v[184:187], 0\n\t" \
  "v_cvt_pk_bf16_f32 v120, v196, v197\n\t" \
  "v_cvt_pk_bf16_f32 v121, v198, v199\n\t" \
  "v_mfma_f32_16x16x32_bf16 v[200:203], v[12:15], v[184:187], 0\n\t" \
  "buffer_store_dwordx4 v[64:67], v250, s[20:23], s14 offen offset:128 sc1\n\t" \
  "v_cvt_pk_bf16_f32 v122, v188, v189\n\t" \
  "v_mfma_f32_16x16x32_bf16 v[168:171], v[208:211], v[108:111], 0\n\t" \
  "ds_read_b64_tr_b16 v[208:209], v248 offset:43560\n\t" \
  "v_cvt_pk_bf16_f32 v123, v190, v191\n\t" \
  "v_mfma_f32_16x16x32_bf16 v[204:207], v[40:43], v[180:183], 0\n\t" \
  "ds_read_b64_tr_b16 v[210:211], v248 offset:44584\n\t" \
  "v_cvt_pk_bf16_f32 v124, v176, v177\n\t" \
  "v_cvt_pk_bf16_f32 v125, v178, v179\n\t" \
  "v_mfma_f32_16x16x32_bf16 v[196:199], v[44:47], v[180:183], 0\n\t" \
  "v_cvt_pk_bf16_f32 v126, v200, v201\n\t" \
  "v_mfma_f32_16x16x32_bf16 v[160:163], v[212:215], v[120:123], v[160:163]\n\t" \
  "v_cvt_pk_bf16_f32 v127, v202, v203\n\t" \
  "v_mfma_f32_16x16x32_bf16 v[188:191], v[16:19], v[184:187], 0\n\t" \
  "v_cvt_pk_bf16_f32 v136, v204, v205\n\t" \
  "v_cvt_pk_bf16_f32 v137, v206, v207\n\t" \
  "v_mfma_f32_16x16x32_bf16 v[176:179], v[20:23], v[184:187], 0\n\t" \
  "s_nop 0\n\t" \
  "v_cvt_pk_bf16_f32 v138, v196, v197\n\t" \
  "v_mfma_f32_16x16x32_bf16 v[168:171], v[212:215], v[124:127], v[168:171]\n\t" \
  "ds_read_b64_tr_b16 v[212:213], v248 offset:46632\n\t" \
  "v_cvt_pk_bf16_f32 v139, v198, v199\n\t" \
  "v_cvt_pk_bf16_f32 v140, v188, v189\n\t" \
  "v_cvt_pk_bf16_f32 v141, v190, v191\n\t" \
  "s_nop 0\n\t" \
  "v_cvt_pk_bf16_f32 v142, v176, v177\n\t" \
  "v_mfma_f32_16x16x32_bf16 v[160:163], v[216:219], v[136:139], v[160:163]\n\t" \
  "ds_read_b64_tr_b16 v[214:215], v248 offset:47656\n\t" \
  "v_cvt_pk_bf16_f32 v143, v178, v179\n\t" \
  "s_nop 1\n\t" \
  "v_mfma_f32_16x16x32_bf16 v[168:171], v[216:219], v[140:143], v[168:171]\n\t" \
  "ds_read_b64_tr_b16 v[216:217], v248 offset:45608\n\t" \
  "s_waitcnt lgkmcnt(3)\n\t" \
  "v_cvt_pk_f16_f32 v220, |v146|, |v147| clamp\n\t" \
  "ds_read_b64_tr_b16 v[218:219], v248 offset:48680\n\t" \
  "s_mov_b64 exec, 3\n\t" \
  "ds_add_rtn_u32 v246, v251, v250 offset:47136\n\t" \
  "s_mov_b64 exec, -1\n\t" \
  "v_cvt_pknorm_i16_f32 v222, |v144|, |v145|\n\t" \
  "v_cvt_pk_f16_f32 v224, |v154|, |v155| clamp\n\t" \
  "v_mfma_f32_16x16x32_bf16 v[164:167], v[208:211], v[104:107], 0\n\t" \
  "v_cvt_pknorm_i16_f32 v226, |v152|, |v153|\n\t" \
  "v_and_b32_sdwa v221, s56, v220 dst_sel:DWORD dst_unused:UNUSED_PAD src0_sel:DWORD src1_sel:WORD_1\n\t" \
  "v_mfma_f32_16x16x32_bf16 v[172:175], v[208:211], v[108:111], 0\n\t" \
  "ds_read_b64_tr_b16 v[208:209], v248 offset:49192\n\t" \
  "v_and_b32 v220, s56, v220\n\t" \
  "s_waitcnt lgkmcnt(4)\n\t" \
  "v_lshlrev_b32_sdwa v223, 2, v222 dst_sel:DWORD dst_unused:UNUSED_PAD src0_sel:DWORD src1_sel:BYTE_3\n\t" \
  "v_lshlrev_b32_sdwa v222, 2, v222 dst_sel:DWORD dst_unused:UNUSED_PAD src0_sel:DWORD src1_sel:BYTE_1\n\t" \
  "v_mfma_f32_16x16x32_bf16 v[164:167], v[212:215], v[120:123], v[164:167]\n\t" \
  "ds_read_b64_tr_b16 v[210:211], v248 offset:50216\n\t" \
  "v_mfma_f32_16x16x32_bf16 v[172:175], v[212:215], v[124:127], v[172:175]\n\t" \
  "ds_read_b64_tr_b16 v[212:213], v248 offset:52264\n\t" \
  "v_and_b32_sdwa v225, s56, v224 dst_sel:DWORD dst_unused:UNUSED_PAD src0_sel:DWORD src1_sel:WORD_1\n\t" \
  "s_waitcnt lgkmcnt(4)\n\t" \
  "v_and_b32 v224, s56, v224\n\t" \
  "v_lshlrev_b32_sdwa v227, 2, v226 dst_sel:DWORD dst_unused:UNUSED_PAD src0_sel:DWORD src1_sel:BYTE_3\n\t" \
  "v_mfma_f32_16x16x32_bf16 v[164:167], v[216:219], v[136:139], v[164:167]\n\t" \
  "ds_read_b64_tr_b16 v[214:215], v248 offset:53288\n\t" \
  "ds_read_b32 v221, v221 offset:0\n\t" \
  "v_lshlrev_b32_sdwa v226, 2, v226 dst_sel:DWORD dst_unused:UNUSED_PAD src0_sel:DWORD src1_sel:BYTE_1\n\t" \
  "v_mfma_f32_16x16x32_bf16 v[172:175], v[216:219], v[140:143], v[172:175]\n\t" \
  "ds_read_b64_tr_b16 v[216:217], v248 offset:51240\n\t" \
  "ds_read_b64_tr_b16 v[218:219], v248 offset:54312\n\t" \
  "s_cmp_lg_u32 s38, 4\n\t" \
  "s_cbranch_scc1 .Lcnx_c20\n\t" \
  "s_mov_b32 m0, 163632\n\t" \
  "s_add_u32 s30, s30, s31\n\t" \
  "buffer_load_dword v252, s[8:11], s30 offen lds\n\t" \
  ".Lcnx_c20:\n\t" \
  "s_setprio 3\n\t" \
  "v_cvt_pk_f16_f32 v228, |v162|, |v163| clamp\n\t" \
  "v_cvt_pknorm_i16_f32 v230, |v160|, |v161|\n\t" \
  "s_waitcnt lgkmcnt(5)\n\t" \
  "v_mfma_f32_16x16x32_bf16 v[176:179], v[208:211], v[96:99], 0\n\t" \
  "ds_read_b32 v220, v220 offset:0\n\t" \
  "ds_read_b32 v223, v223 offset:15376\n\t" \
  "v_cvt_pk_f16_f32 v232, |v170|, |v171| clamp\n\t" \
  "v_cvt_pk_f16_f32 v234, |v168|, |v169| clamp\n\t" \
  "v_and_b32_sdwa v229, s56, v228 dst_sel:DWORD dst_unused:UNUSED_PAD src0_sel:DWORD src1_sel:WORD_1\n\t" \
  "v_and_b32 v228, s56, v228\n\t" \
  "v_mfma_f32_16x16x32_bf16 v[184:187], v[208:211], v[100:103], 0\n\t" \
  "buffer_load_dwordx4 v[236:239], v250, s[8:11], s28 offen offset:0\n\t" \
  "s_cmp_lg_u32 s38, 5\n\t" \
  "s_cbranch_scc1 .Lcnx_c21\n\t" \
  "s_mov_b32 m0, 163632\n\t" \
  "s_add_u32 s30, s30, s31\n\t" \
  "buffer_load_dword v252, s[8:11], s30 offen lds\n\t" \
  ".Lcnx_c21:\n\t" \
  "s_cmp_lg_u32 s37, 0\n\t" \
  "s_cbranch_scc1 .Lcnx_c9\n\t" \
  "s_waitcnt vmcnt(4)\n\t" \
  ".Lcnx_c9:\n\t" \
  "s_mov_b64 exec, 3\n\t" \
  "ds_add_rtn_u32 v246, v251, v250 offset:47144\n\t" \
  "s_mov_b64 exec, -1\n\t" \
  "ds_read_b32 v222, v222 offset:15376\n\t" \
  "ds_read_b32 v225, v225 offset:0\n\t" \
  "v_lshlrev_b32_sdwa v231, 2, v230 dst_sel:DWORD dst_unused:UNUSED_PAD src0_sel:DWORD src1_sel:BYTE_3\n\t" \
  "v_lshlrev_b32_sdwa v230, 2, v230 dst_sel:DWORD dst_unused:UNUSED_PAD src0_sel:DWORD src1_sel:BYTE_1\n\t" \
  "v_and_b32_sdwa v233, s56, v232 dst_sel:DWORD dst_unused:UNUSED_PAD src0_sel:DWORD src1_sel:WORD_1\n\t" \
  "v_and_b32 v232, s56, v232\n\t" \
  "v_mfma_f32_16x16x32_bf16 v[192:195], v[208:211], v[104:107], 0\n\t" \
  "ds_read_b32 v224, v224 offset:0\n\t" \
  "ds_read_b32 v227, v227 offset:15376\n\t" \
  "v_and_b32_sdwa v235, s56, v234 dst_sel:DWORD dst_unused:UNUSED_PAD src0_sel:DWORD src1_sel:WORD_1\n\t" \
  "v_and_b32 v234, s56, v234\n\t" \
  "ds_read_b32 v226, v226 offset:15376\n\t" \
  "ds_read_b32 v229, v229 offset:0\n\t" \
  "ds_read_b32 v228, v228 offset:0\n\t" \
  "ds_read_b32 v231, v231 offset:15376\n\t" \
  "ds_read_b32 v230, v230 offset:15376\n\t" \
  "ds_read_b32 v233, v233 offset:0\n\t" \
  "ds_read_b32 v232, v232 offset:0\n\t" \
  "ds_read_b32 v235, v235 offset:0\n\t" \
  "s_waitcnt lgkmcnt(11)\n\t" \
  "v_fma_f32 v147, |v147|, v221, v147 mul:4\n\t" \
  "v_fma_f32 v146, |v146|, v220, v146 mul:4\n\t" \
  "v_fma_mix_f32 v223, |v145|, v223, v223 op_sel:[0,1,0] op_sel_hi:[0,1,1] clamp\n\t" \
  "v_mfma_f32_16x16x32_bf16 v[200:203], v[208:211], v[108:111], 0\n\t" \
  "v_readfirstlane_b32 s4, v246\n\t" \
  "s_cmp_lt_u32 s4, s57\n\t" \
  "s_cbranch_scc1 .Lcnx_w9\n.Lcnx_b9:\n\t" \
  "ds_read_b64_tr_b16 v[208:209], v248 offset:49704\n\t" \
  "v_fma_mix_f32 v222, |v144|, v222, v222 op_sel:[0,1,0] op_sel_hi:[0,1,1] clamp\n\t" \
  "s_waitcnt lgkmcnt(8)\n\t" \
  "v_fma_f32 v155, |v155|, v225, v155 mul:4\n\t" \
  "v_fma_f32 v154, |v154|, v224, v154 mul:4\n\t" \
  "v_fma_mix_f32 v227, |v153|, v227, v227 op_sel:[0,1,0] op_sel_hi:[0,1,1] clamp\n\t" \
  "v_mfma_f32_16x16x32_bf16 v[176:179], v[212:215], v[112:115], v[176:179]\n\t" \
  "ds_read_b64_tr_b16 v[210:211], v248 offset:50728\n\t" \
  "s_setprio 2\n\t" \
  "v_fma_mix_f32 v226, |v152|, v226, v226 op_sel:[0,1,0] op_sel_hi:[0,1,1] clamp\n\t" \
  "v_fma_f32 v145, |v145|, v223, v145 mul:4\n\t" \
  "v_fma_f32 v144, |v144|, v222, v144 mul:4\n\t" \
  "v_fma_f32 v153, |v153|, v227, v153 mul:4\n\t" \
  "v_fma_f32 v152, |v152|, v226, v152 mul:4\n\t" \
  "v_mfma_f32_16x16x32_bf16 v[184:187], v[212:215], v[116:119], v[184:187]\n\t" \
  "s_cmp_eq_u32 s37, 0\n\t" \
  "s_cbranch_scc1 .Lcnx_f1\n.Lcnx_g1:\n\t" \
  "ds_read_b32 v234, v234 offset:0\n\t" \
  "v_cvt_pk_bf16_f32 v147, v146, v147\n\t" \
  "v_cvt_pk_bf16_f32 v146, v144, v145\n\t" \
  "v_cvt_pk_bf16_f32 v155, v154, v155\n\t" \
  "v_cvt_pk_bf16_f32 v154, v152, v153\n\t" \
  "v_mfma_f32_16x16x32_bf16 v[192:195], v[212:215], v[120:123], v[192:195]\n\t" \
  "buffer_store_dwordx4 v[72:75], v250, s[20:23], s15 offen offset:0 sc1\n\t" \
  "v_cvt_pk_f16_f32 v220, |v148|, |v149| clamp\n\t" \
  "v_cvt_pknorm_i16_f32 v222, |v150|, |v151|\n\t" \
  "v_cvt_pk_f16_f32 v224, |v156|, |v157| clamp\n\t" \
  "v_cvt_pknorm_i16_f32 v226, |v158|, |v159|\n\t" \
  "v_mfma_f32_16x16x32_bf16 v[200:203], v[212:215], v[124:127], v[200:203]\n\t" \
  "ds_read_b64_tr_b16 v[212:213], v248 offset:52776\n\t" \
  "v_and_b32_sdwa v221, s56, v220 dst_sel:DWORD dst_unused:UNUSED_PAD src0_sel:DWORD src1_sel:WORD_1\n\t" \
  "v_and_b32 v220, s56, v220\n\t" \
  "v_lshlrev_b32_sdwa v223, 2, v222 dst_sel:DWORD dst_unused:UNUSED_PAD src0_sel:DWORD src1_sel:BYTE_3\n\t" \
  "v_lshlrev_b32_sdwa v222, 2, v222 dst_sel:DWORD dst_unused:UNUSED_PAD src0_sel:DWORD src1_sel:BYTE_1\n\t" \
  "v_and_b32_sdwa v225, s56, v224 dst_sel:DWORD dst_unused:UNUSED_PAD src0_sel:DWORD src1_sel:WORD_1\n\t" \
  "v_mfma_f32_16x16x32_bf16 v[176:179], v[216:219], v[128:131], v[176:179]\n\t" \
  "buffer_store_dwordx4 v[80:83], v250, s[20:23], s15 offen offset:64 sc1\n\t" \
  "ds_read_b64_tr_b16 v[214:215], v248 offset:53800\n\t" \
  "v_and_b32 v224, s56, v224\n\t" \
  "v_lshlrev_b32_sdwa v227, 2, v226 dst_sel:DWORD dst_unused:UNUSED_PAD src0_sel:DWORD src1_sel:BYTE_3\n\t" \
  "v_lshlrev_b32_sdwa v226, 2, v226 dst_sel:DWORD dst_unused:UNUSED_PAD src0_sel:DWORD src1_sel:BYTE_1\n\t" \
  "s_waitcnt lgkmcnt(8)\n\t" \
  "v_fma_f32 v163, |v163|, v229, v163 mul:4\n\t" \
  "v_mfma_f32_16x16x32_bf16 v[184:187], v[216:219], v[132:135], v[184:187]\n\t" \
  "ds_read_b32 v221, v221 offset:0\n\t" \
  "ds_read_b32 v220, v220 offset:0\n\t" \
  "v_fma_f32 v162, |v162|, v228, v162 mul:4\n\t" \
  "v_fma_mix_f32 v231, |v161|, v231, v231 op_sel:[0,1,0] op_sel_hi:[0,1,1] clamp\n\t" \
  "v_fma_mix_f32 v230, |v160|, v230, v230 op_sel:[0,1,0] op_sel_hi:[0,1,1] clamp\n\t" \
  "s_waitcnt lgkmcnt(4)\n\t" \
  "v_fma_f32 v171, |v171|, v233, v171 mul:4\n\t" \
  "v_fma_f32 v170, |v170|, v232, v170 mul:4\n\t" \
  "v_mfma_f32_16x16x32_bf16 v[192:195], v[216:219], v[136:139], v[192:195]\n\t" \
  "ds_read_b32 v223, v223 offset:15376\n\t" \
  "ds_read_b32 v222, v222 offset:15376\n\t" \
  "v_fma_f32 v169, |v169|, v235, v169 mul:4\n\t" \
  "v_fma_f32 v168, |v168|, v234, v168 mul:4\n\t" \
  "v_fma_f32 v161, |v161|, v231, v161 mul:4\n\t" \
  "v_fma_f32 v160, |v160|, v230, v160 mul:4\n\t" \
  "v_mfma_f32_16x16x32_bf16 v[200:203], v[216:219], v[140:143], v[200:203]\n\t" \
  "buffer_store_dwordx4 v[88:91], v250, s[20:23], s15 offen offset:128 sc1\n\t" \
  "ds_read_b64_tr_b16 v[216:217], v248 offset:51752\n\t" \
  "ds_read_b64_tr_b16 v[218:219], v248 offset:54824\n\t" \
  "v_cvt_pk_bf16_f32 v163, v162, v163\n\t" \
  "s_setprio 1\n\t" \
  "v_cvt_pk_bf16_f32 v162, v160, v161\n\t" \
  "v_cvt_pk_bf16_f32 v171, v170, v171\n\t" \
  "v_cvt_pk_bf16_f32 v170, v168, v169\n\t" \
  "v_cvt_pk_f16_f32 v228, |v164|, |v165| clamp\n\t" \
  "v_mfma_f32_16x16x32_bf16 v[180:183], v[208:211], v[96:99], 0\n\t" \
  "ds_read_b32 v225, v225 offset:0\n\t" \
  "v_cvt_pknorm_i16_f32 v230, |v166|, |v167|\n\t" \
  "v_cvt_pk_f16_f32 v232, |v172|, |v173| clamp\n\t" \
  "v_cvt_pk_f16_f32 v234, |v174|, |v175| clamp\n\t" \
  "v_and_b32_sdwa v229, s56, v228 dst_sel:DWORD dst_unused:UNUSED_PAD src0_sel:DWORD src1_sel:WORD_1\n\t" \
  "v_mfma_f32_16x16x32_bf16 v[188:191], v[208:211], v[100:103], 0\n\t" \
  "s_waitcnt vmcnt(3)\n\t" \
  "s_mov_b64 exec, 3\n\t" \
  "ds_add_rtn_u32 v246, v251, v250 offset:47152\n\t" \
  "s_mov_b64 exec, -1\n\t" \
  "ds_read_b32 v224, v224 offset:0\n\t" \
  "v_and_b32 v228, s56, v228\n\t" \
  "v_lshlrev_b32_sdwa v231, 2, v230 dst_sel:DWORD dst_unused:UNUSED_PAD src0_sel:DWORD src1_sel:BYTE_3\n\t" \
  "v_lshlrev_b32_sdwa v230, 2, v230 dst_sel:DWORD dst_unused:UNUSED_PAD src0_sel:DWORD src1_sel:BYTE_1\n\t" \
  "v_and_b32_sdwa v233, s56, v232 dst_sel:DWORD dst_unused:UNUSED_PAD src0_sel:DWORD src1_sel:WORD_1\n\t" \
  "v_mfma_f32_16x16x32_bf16 v[196:199], v[208:211], v[104:107], 0\n\t" \
  "ds_read_b32 v227, v227 offset:15376\n\t" \
  "ds_read_b32 v226, v226 offset:15376\n\t" \
  "v_and_b32 v232, s56, v232\n\t" \
  "v_and_b32_sdwa v235, s56, v234 dst_sel:DWORD dst_unused:UNUSED_PAD src0_sel:DWORD src1_sel:WORD_1\n\t" \
  "v_and_b32 v234, s56, v234\n\t" \
  "s_waitcnt lgkmcnt(7)\n\t" \
  "v_fma_f32 v149, |v149|, v221, v149 mul:4\n\t" \
  "v_fma_f32 v148, |v148|, v220, v148 mul:4\n\t" \
  "v_mfma_f32_16x16x32_bf16 v[204:207], v[208:211], v[108:111], 0\n\t" \
  "s_waitcnt lgkmcnt(3)\n\t" \
  "v_readfirstlane_b32 s4, v246\n\t" \
  "s_cmp_lt_u32 s4, s57\n\t" \
  "s_cbranch_scc1 .Lcnx_w12\n.Lcnx_b12:\n\t" \
  "ds_read_b64_tr_b16 v[208:209], v249 offset:24600\n\t" \
  "ds_read_b64_tr_b16 v[210:211], v249 offset:26136\n\t" \
  "v_fma_mix_f32 v223, |v151|, v223, v223 op_sel:[0,1,0] op_sel_hi:[0,1,1] clamp\n\t" \
  "v_fma_mix_f32 v222, |v150|, v222, v222 op_sel:[0,1,0] op_sel_hi:[0,1,1] clamp\n\t" \
  "s_waitcnt lgkmcnt(2)\n\t" \
  "v_fma_f32 v157, |v157|, v225, v157 mul:4\n\t" \
  "v_fma_f32 v156, |v156|, v224, v156 mul:4\n\t" \
  "v_mfma_f32_16x16x32_bf16 v[180:183], v[212:215], v[112:115], v[180:183]\n\t" \
  "s_cmp_eq_u32 s37, 0\n\t" \
  "s_cbranch_scc1 .Lcnx_f2\n.Lcnx_g2:\n\t" \
  "ds_read_b32 v229, v229 offset:0\n\t" \
  "ds_read_b32 v228, v228 offset:0\n\t" \
  "v_fma_mix_f32 v227, |v159|, v227, v227 op_sel:[0,1,0] op_sel_hi:[0,1,1] clamp\n\t" \
  "v_fma_mix_f32 v226, |v158|, v226, v226 op_sel:[0,1,0] op_sel_hi:[0,1,1] clamp\n\t" \
  "v_fma_f32 v151, |v151|, v223, v151 mul:4\n\t" \
  "v_fma_f32 v150, |v150|, v222, v150 mul:4\n\t" \
  "v_fma_f32 v159, |v159|, v227, v159 mul:4\n\t" \
  "s_setprio 0\n\t" \
  "v_mfma_f32_16x16x32_bf16 v[188:191], v[212:215], v[116:119], v[188:191]\n\t" \
  "ds_read_b32 v231, v231 offset:15376\n\t" \
  "ds_read_b32 v230, v230 offset:15376\n\t" \
  "v_fma_f32 v158, |v158|, v226, v158 mul:4\n\t" \
  "v_cvt_pk_bf16_f32 v148, v148, v149\n\t" \
  "v_cvt_pk_bf16_f32 v149, v150, v151\n\t" \
  "v_cvt_pk_bf16_f32 v156, v156, v157\n\t" \
  "v_mfma_f32_16x16x32_bf16 v[196:199], v[212:215], v[120:123], v[196:199]\n\t" \
  "ds_read_b32 v233, v233 offset:0\n\t" \
  "v_cvt_pk_bf16_f32 v157, v158, v159\n\t" \
  "v_cvt_pk_f16_f32 v220, |v178|, |v179| clamp\n\t" \
  "v_cvt_pknorm_i16_f32 v222, |v176|, |v177|\n\t" \
  "v_cvt_pk_f16_f32 v224, |v186|, |v187| clamp\n\t" \
  "v_mfma_f32_16x16x32_bf16 v[204:207], v[212:215], v[124:127], v[204:207]\n\t" \
  "ds_read_b64_tr_b16 v[212:213], v249 offset:24608\n\t" \
  "v_cvt_pknorm_i16_f32 v226, |v184|, |v185|\n\t" \
  "v_and_b32_sdwa v221, s56, v220 dst_sel:DWORD dst_unused:UNUSED_PAD src0_sel:DWORD src1_sel:WORD_1\n\t" \
  "v_and_b32 v220, s56, v220\n\t" \
  "v_lshlrev_b32_sdwa v223, 2, v222 dst_sel:DWORD dst_unused:UNUSED_PAD src0_sel:DWORD src1_sel:BYTE_3\n\t" \
  "v_lshlrev_b32_sdwa v222, 2, v222 dst_sel:DWORD dst_unused:UNUSED_PAD src0_sel:DWORD src1_sel:BYTE_1\n\t" \
  "v_mfma_f32_16x16x32_bf16 v[180:183], v[216:219], v[128:131], v[180:183]\n\t" \
  "ds_read_b64_tr_b16 v[214:215], v249 offset:26144\n\t" \
  "ds_read_b32 v232, v232 offset:0\n\t" \
  "v_and_b32_sdwa v225, s56, v224 dst_sel:DWORD dst_unused:UNUSED_PAD src0_sel:DWORD src1_sel:WORD_1\n\t" \
  "v_and_b32 v224, s56, v224\n\t" \
  "v_lshlrev_b32_sdwa v227, 2, v226 dst_sel:DWORD dst_unused:UNUSED_PAD src0_sel:DWORD src1_sel:BYTE_3\n\t" \
  "v_lshlrev_b32_sdwa v226, 2, v226 dst_sel:DWORD dst_unused:UNUSED_PAD src0_sel:DWORD src1_sel:BYTE_1\n\t" \
  "v_mfma_f32_16x16x32_bf16 v[188:191], v[216:219], v[132:135], v[188:191]\n\t" \
  "ds_read_b32 v235, v235 offset:0\n\t" \
  "ds_read_b32 v234, v234 offset:0\n\t" \
  "s_waitcnt lgkmcnt(6)\n\t" \
  "v_fma_f32 v165, |v165|, v229, v165 mul:4\n\t" \
  "v_fma_f32 v164, |v164|, v228, v164 mul:4\n\t" \
  "v_fma_mix_f32 v231, |v167|, v231, v231 op_sel:[0,1,0] op_sel_hi:[0,1,1] clamp\n\t" \
  "v_fma_mix_f32 v230, |v166|, v230, v230 op_sel:[0,1,0] op_sel_hi:[0,1,1] clamp\n\t" \
  "s_waitcnt lgkmcnt(0)\n\t" \
  "v_fma_f32 v173, |v173|, v233, v173 mul:4\n\t" \
  "v_mfma_f32_16x16x32_bf16 v[196:199], v[216:219], v[136:139], v[196:199]\n\t" \
  "ds_read_b32 v221, v221 offset:0\n\t" \
  "ds_read_b32 v220, v220 offset:0\n\t" \
  "v_fma_f32 v172, |v172|, v232, v172 mul:4\n\t" \
  "v_fma_f32 v175, |v175|, v235, v175 mul:4\n\t" \
  "v_fma_f32 v174, |v174|, v234, v174 mul:4\n\t" \
  "v_fma_f32 v167, |v167|, v231, v167 mul:4\n\t" \
  "v_mfma_f32_16x16x32_bf16 v[204:207], v[216:219], v[140:143], v[204:207]\n\t" \
  "ds_read_b64_tr_b16 v[216:217], v249 offset:24664\n\t" \
  "ds_read_b64_tr_b16 v[218:219], v249 offset:26200\n\t" \
  "v_fma_f32 v166, |v166|, v230, v166 mul:4\n\t" \
  "v_cvt_pk_bf16_f32 v164, v164, v165\n\t" \
  "v_cvt_pk_bf16_f32 v165, v166, v167\n\t" \
  "v_cvt_pk_bf16_f32 v172, v172, v173\n\t" \
  "v_cvt_pk_bf16_f32 v173, v174, v175\n\t" \
  "s_setprio 0\n\t" \
  "v_cvt_pk_f16_f32 v228, |v194|, |v195| clamp\n\t" \
  "v_cvt_pknorm_i16_f32 v230, |v192|, |v193|\n\t" \
  "v_mfma_f32_16x16x32_bf16 v[0:3], v[242:245], v[236:239], 0\n\t" \
  "ds_read_b32 v223, v223 offset:15376\n\t" \
  "ds_read_b32 v222, v222 offset:15376\n\t" \
  "v_cvt_pk_f16_f32 v232, |v202|, |v203| clamp\n\t" \
  "v_cvt_pk_f16_f32 v234, |v200|, |v201| clamp\n\t" \
  "v_mfma_f32_16x16x32_bf16 v[4:7], v[240:243], v[236:239], 0\n\t" \
  "buffer_load_dwordx4 v[236:239], v250, s[8:11], s28 offen offset:64\n\t" \
  "s_cmp_lg_u32 s58, 0\n\t" \
  "s_cbranch_scc1 .Lcnx_c22\n\t" \
  "s_sub_u32 s24, s24, s64\n\t" \
  "s_add_u32 m0, s25, 0\n\t" \
  "s_add_u32 s24, s24, s26\n\t" \
  "buffer_load_dwordx4 v254, s[8:11], s24 offen lds\n\t" \
  "s_cmp_lg_u32 s38, 6\n\t" \
  "s_cbranch_scc1 .Lcnx_c22\n\t" \
  "s_mov_b32 m0, 163632\n\t" \
  "s_add_u32 s30, s30, s31\n\t" \
  "buffer_load_dword v252, s[8:11], s30 offen lds\n\t" \
  ".Lcnx_c22:\n\t" \
  "ds_read_b32 v225, v225 offset:0\n\t" \
  "v_and_b32_sdwa v229, s56, v228 dst_sel:DWORD dst_unused:UNUSED_PAD src0_sel:DWORD src1_sel:WORD_1\n\t" \
  "v_and_b32 v228, s56, v228\n\t" \
  "v_mfma_f32_16x16x32_bf16 v[0:3], v[208:211], v[146:149], v[0:3]\n\t" \
  "ds_read_b32 v224, v224 offset:0\n\t" \
  "v_lshlrev_b32_sdwa v231, 2, v230 dst_sel:DWORD dst_unused:UNUSED_PAD src0_sel:DWORD src1_sel:BYTE_3\n\t" \
  "v_lshlrev_b32_sdwa v230, 2, v230 dst_sel:DWORD dst_unused:UNUSED_PAD src0_sel:DWORD src1_sel:BYTE_1\n\t" \
  "v_mfma_f32_16x16x32_bf16 v[24:27], v[208:211], v[154:157], 0\n\t" \
  "ds_read_b32 v227, v227 offset:15376\n\t" \
  "v_and_b32_sdwa v233, s56, v232 dst_sel:DWORD dst_unused:UNUSED_PAD src0_sel:DWORD src1_sel:WORD_1\n\t" \
  "v_and_b32 v232, s56, v232\n\t" \
  "v_mfma_f32_16x16x32_bf16 v[48:51], v[208:211], v[162:165], 0\n\t" \
  "ds_read_b32 v226, v226 offset:15376\n\t" \
  "ds_read_b32 v229, v229 offset:0\n\t" \
  "v_and_b32_sdwa v235, s56, v234 dst_sel:DWORD dst_unused:UNUSED_PAD src0_sel:DWORD src1_sel:WORD_1\n\t" \
  "v_and_b32 v234, s56, v234\n\t" \
  "v_mfma_f32_16x16x32_bf16 v[72:75], v[208:211], v[170:173], 0\n\t" \
  "ds_read_b64_tr_b16 v[208:209], v249 offset:24672\n\t" \
  "ds_read_b64_tr_b16 v[210:211], v249 offset:26208\n\t" \
  "s_waitcnt lgkmcnt(7)\n\t" \
  "v_fma_f32 v179, |v179|, v221, v179 mul:4\n\t" \
  "v_fma_f32 v178, |v178|, v220, v178 mul:4\n\t" \
  "v_mfma_f32_16x16x32_bf16 v[4:7], v[212:215], v[146:149], v[4:7]\n\t" \
  "ds_read_b32 v228, v228 offset:0\n\t" \
  "ds_read_b32 v231, v231 offset:15376\n\t" \
  "v_fma_mix_f32 v223, |v177|, v223, v223 op_sel:[0,1,0] op_sel_hi:[0,1,1] clamp\n\t" \
  "v_fma_mix_f32 v222, |v176|, v222, v222 op_sel:[0,1,0] op_sel_hi:[0,1,1] clamp\n\t" \
  "v_mfma_f32_16x16x32_bf16 v[28:31], v[212:215], v[154:157], 0\n\t" \
  "ds_read_b32 v230, v230 offset:15376\n\t" \
  "s_waitcnt lgkmcnt(6)\n\t" \
  "v_fma_f32 v187, |v187|, v225, v187 mul:4\n\t" \
  "v_fma_f32 v186, |v186|, v224, v186 mul:4\n\t" \
  "v_mfma_f32_16x16x32_bf16 v[52:55], v[212:215], v[162:165], 0\n\t" \
  "ds_read_b32 v233, v233 offset:0\n\t" \
  "v_fma_mix_f32 v227, |v185|, v227, v227 op_sel:[0,1,0] op_sel_hi:[0,1,1] clamp\n\t" \
  "v_fma_mix_f32 v226, |v184|, v226, v226 op_sel:[0,1,0] op_sel_hi:[0,1,1] clamp\n\t" \
  "v_fma_f32 v177, |v177|, v223, v177 mul:4\n\t" \
  "v_mfma_f32_16x16x32_bf16 v[76:79], v[212:215], v[170:173], 0\n\t" \
  "ds_read_b64_tr_b16 v[212:213], v249 offset:24728\n\t" \
  "v_fma_f32 v176, |v176|, v222, v176 mul:4\n\t" \
  "v_fma_f32 v185, |v185|, v227, v185 mul:4\n\t" \
  "v_mfma_f32_16x16x32_bf16 v[8:11], v[216:219], v[146:149], 0\n\t" \
  "ds_read_b64_tr_b16 v[214:215], v249 offset:26264\n\t" \
  "v_fma_f32 v184, |v184|, v226, v184 mul:4\n\t" \
  "v_cvt_pk_bf16_f32 v179, v178, v179\n\t" \
  "v_mfma_f32_16x16x32_bf16 v[32:35], v[216:219], v[154:157], 0\n\t" \
  "ds_read_b32 v232, v232 offset:0\n\t" \
  "v_cvt_pk_bf16_f32 v178, v176, v177\n\t" \
  "v_cvt_pk_bf16_f32 v187, v186, v187\n\t" \
  "v_mfma_f32_16x16x32_bf16 v[56:59], v[216:219], v[162:165], 0\n\t" \
  "ds_read_b32 v235, v235 offset:0\n\t" \
  "s_setprio 1\n\t" \
  "v_cvt_pk_bf16_f32 v186, v184, v185\n\t" \
  "v_cvt_pk_f16_f32 v220, |v180|, |v181| clamp\n\t" \
  "v_mfma_f32_16x16x32_bf16 v[80:83], v[216:219], v[170:173], 0\n\t" \
  "ds_read_b64_tr_b16 v[216:217], v249 offset:24736\n\t" \
  "v_cvt_pknorm_i16_f32 v222, |v182|, |v183|\n\t" \
  "v_cvt_pk_f16_f32 v224, |v188|, |v189| clamp\n\t" \
  "s_waitcnt lgkmcnt(9)\n\t" \
  "v_mfma_f32_16x16x32_bf16 v[12:15], v[208:211], v[146:149], 0\n\t" \
  "ds_read_b64_tr_b16 v[218:219], v249 offset:26272\n\t" \
  "v_cvt_pknorm_i16_f32 v226, |v190|, |v191|\n\t" \
  "v_and_b32_sdwa v221, s56, v220 dst_sel:DWORD dst_unused:UNUSED_PAD src0_sel:DWORD src1_sel:WORD_1\n\t" \
  "v_mfma_f32_16x16x32_bf16 v[36:39], v[208:211], v[154:157], 0\n\t" \
  "ds_read_b32 v234, v234 offset:0\n\t" \
  "v_and_b32 v220, s56, v220\n\t" \
  "v_lshlrev_b32_sdwa v223, 2, v222 dst_sel:DWORD dst_unused:UNUSED_PAD src0_sel:DWORD src1_sel:BYTE_3\n\t" \
  "v_mfma_f32_16x16x32_bf16 v[60:63], v[208:211], v[162:165], 0\n\t" \
  "ds_read_b32 v221, v221 offset:0\n\t" \
  "v_lshlrev_b32_sdwa v222, 2, v222 dst_sel:DWORD dst_unused:UNUSED_PAD src0_sel:DWORD src1_sel:BYTE_1\n\t" \
  "v_and_b32_sdwa v225, s56, v224 dst_sel:DWORD dst_unused:UNUSED_PAD src0_sel:DWORD src1_sel:WORD_1\n\t" \
  "v_and_b32 v224, s56, v224\n\t" \
  "v_mfma_f32_16x16x32_bf16 v[84:87], v[208:211], v[170:173], 0\n\t" \
  "ds_read_b64_tr_b16 v[208:209], v248 offset:36904\n\t" \
  "ds_read_b64_tr_b16 v[210:211], v248 offset:37928\n\t" \
  "v_lshlrev_b32_sdwa v227, 2, v226 dst_sel:DWORD dst_unused:UNUSED_PAD src0_sel:DWORD src1_sel:BYTE_3\n\t" \
  "v_lshlrev_b32_sdwa v226, 2, v226 dst_sel:DWORD dst_unused:UNUSED_PAD src0_sel:DWORD src1_sel:BYTE_1\n\t" \
  "s_waitcnt lgkmcnt(8)\n\t" \
  "v_mfma_f32_16x16x32_bf16 v[16:19], v[212:215], v[146:149], 0\n\t" \
  "ds_read_b32 v220, v220 offset:0\n\t" \
  "ds_read_b32 v223, v223 offset:15376\n\t" \
  "v_fma_f32 v195, |v195|, v229, v195 mul:4\n\t" \
  "v_fma_f32 v194, |v194|, v228, v194 mul:4\n\t" \
  "v_mfma_f32_16x16x32_bf16 v[40:43], v[212:215], v[154:157], 0\n\t" \
  "ds_read_b32 v222, v222 offset:15376\n\t" \
  "v_fma_mix_f32 v231, |v193|, v231, v231 op_sel:[0,1,0] op_sel_hi:[0,1,1] clamp\n\t" \
  "v_fma_mix_f32 v230, |v192|, v230, v230 op_sel:[0,1,0] op_sel_hi:[0,1,1] clamp\n\t" \
  "v_mfma_f32_16x16x32_bf16 v[64:67], v[212:215], v[162:165], 0\n\t" \
  "ds_read_b32 v225, v225 offset:0\n\t" \
  "s_waitcnt lgkmcnt(7)\n\t" \
  "v_fma_f32 v203, |v203|, v233, v203 mul:4\n\t" \
  "v_fma_f32 v202, |v202|, v232, v202 mul:4\n\t" \
  "v_mfma_f32_16x16x32_bf16 v[88:91], v[212:215], v[170:173], 0\n\t" \
  "ds_read_b64_tr_b16 v[212:213], v248 offset:39976\n\t" \
  "v_fma_f32 v201, |v201|, v235, v201 mul:4\n\t" \
  "v_fma_f32 v200, |v200|, v234, v200 mul:4\n\t" \
  "v_mfma_f32_16x16x32_bf16 v[20:23], v[216:219], v[146:149], 0\n\t" \
  "ds_read_b64_tr_b16 v[214:215], v248 offset:41000\n\t" \
  "v_fma_f32 v193, |v193|, v231, v193 mul:4\n\t" \
  "v_fma_f32 v192, |v192|, v230, v192 mul:4\n\t" \
  "v_mfma_f32_16x16x32_bf16 v[44:47], v[216:219], v[154:157], 0\n\t" \
  "ds_read_b32 v224, v224 offset:0\n\t" \
  "v_cvt_pk_bf16_f32 v195, v194, v195\n\t" \
  "v_cvt_pk_bf16_f32 v194, v192, v193\n\t" \
  "v_mfma_f32_16x16x32_bf16 v[68:71], v[216:219], v[162:165], 0\n\t" \
  "ds_read_b32 v227, v227 offset:15376\n\t" \
  "v_cvt_pk_bf16_f32 v203, v202, v203\n\t" \
  "v_cvt_pk_bf16_f32 v202, v200, v201\n\t" \
  "v_cvt_pk_f16_f32 v228, |v196|, |v197| clamp\n\t" \
  "v_mfma_f32_16x16x32_bf16 v[92:95], v[216:219], v[170:173], 0\n\t" \
  "ds_read_b64_tr_b16 v[216:217], v248 offset:38952\n\t" \
  "v_cvt_pknorm_i16_f32 v230, |v198|, |v199|\n\t" \
  "s_setprio 2\n\t" \
  "v_cvt_pk_f16_f32 v232, |v204|, |v205| clamp\n\t" \
  "s_waitcnt lgkmcnt(9)\n\t" \
  "v_mfma_f32_16x16x32_bf16 v[144:147], v[208:211], v[96:99], 0\n\t" \
  "ds_read_b64_tr_b16 v[218:219], v248 offset:42024\n\t" \
  "v_cvt_pk_f16_f32 v234, |v206|, |v207| clamp\n\t" \
  "v_and_b32_sdwa v229, s56, v228 dst_sel:DWORD dst_unused:UNUSED_PAD src0_sel:DWORD src1_sel:WORD_1\n\t" \
  "v_mfma_f32_16x16x32_bf16 v[152:155], v[208:211], v[100:103], 0\n\t" \
  "ds_read_b32 v226, v226 offset:15376\n\t" \
  "v_and_b32 v228, s56, v228\n\t" \
  "v_lshlrev_b32_sdwa v231, 2, v230 dst_sel:DWORD dst_unused:UNUSED_PAD src0_sel:DWORD src1_sel:BYTE_3\n\t" \
  "v_mfma_f32_16x16x32_bf16 v[160:163], v[208:211], v[104:107], 0\n\t" \
  "ds_read_b32 v229, v229 offset:0\n\t" \
  "v_lshlrev_b32_sdwa v230, 2, v230 dst_sel:DWORD dst_unused:UNUSED_PAD src0_sel:DWORD src1_sel:BYTE_1\n\t" \
  "v_and_b32_sdwa v233, s56, v232 dst_sel:DWORD dst_unused:UNUSED_PAD src0_sel:DWORD src1_sel:WORD_1\n\t" \
  "v_mfma_f32_16x16x32_bf16 v[168:171], v[208:211], v[108:111], 0\n\t" \
  "ds_read_b64_tr_b16 v[208:209], v248 offset:37416\n\t" \
  "v_and_b32 v232, s56, v232\n\t" \
  "v_and_b32_sdwa v235, s56, v234 dst_sel:DWORD dst_unused:UNUSED_PAD src0_sel:DWORD src1_sel:WORD_1\n\t" \
  "s_waitcnt lgkmcnt(7)\n\t" \
  "v_mfma_f32_16x16x32_bf16 v[144:147], v[212:215], v[112:115], v[144:147]\n\t" \
  "ds_read_b64_tr_b16 v[210:211], v248 offset:38440\n\t" \
  "ds_read_b32 v228, v228 offset:0\n\t" \
  "v_and_b32 v234, s56, v234\n\t" \
  "v_fma_f32 v181, |v181|, v221, v181 mul:4\n\t" \
  "v_mfma_f32_16x16x32_bf16 v[152:155], v[212:215], v[116:119], v[152:155]\n\t" \
  "ds_read_b32 v231, v231 offset:15376\n\t" \
  "ds_read_b32 v230, v230 offset:15376\n\t" \
  "v_fma_f32 v180, |v180|, v220, v180 mul:4\n\t" \
  "v_fma_mix_f32 v223, |v183|, v223, v223 op_sel:[0,1,0] op_sel_hi:[0,1,1] clamp\n\t" \
  "v_mfma_f32_16x16x32_bf16 v[160:163], v[212:215], v[120:123], v[160:163]\n\t" \
  "ds_read_b32 v233, v233 offset:0\n\t" \
  "v_fma_mix_f32 v222, |v182|, v222, v222 op_sel:[0,1,0] op_sel_hi:[0,1,1] clamp\n\t" \
  "s_waitcnt lgkmcnt(7)\n\t" \
  "v_fma_f32 v189, |v189|, v225, v189 mul:4\n\t" \
  "v_mfma_f32_16x16x32_bf16 v[168:171], v[212:215], v[124:127], v[168:171]\n\t" \
  "ds_read_b64_tr_b16 v[212:213], v248 offset:40488\n\t" \
  "v_fma_f32 v188, |v188|, v224, v188 mul:4\n\t" \
  "v_fma_mix_f32 v227, |v191|, v227, v227 op_sel:[0,1,0] op_sel_hi:[0,1,1] clamp\n\t" \
  "v_fma_mix_f32 v226, |v190|, v226, v226 op_sel:[0,1,0] op_sel_hi:[0,1,1] clamp\n\t" \
  "v_mfma_f32_16x16x32_bf16 v[144:147], v[216:219], v[128:131], v[144:147]\n\t" \
  "ds_read_b64_tr_b16 v[214:215], v248 offset:41512\n\t" \
  "v_fma_f32 v183, |v183|, v223, v183 mul:4\n\t" \
  "v_fma_f32 v182, |v182|, v222, v182 mul:4\n\t" \
  "v_mfma_f32_16x16x32_bf16 v[152:155], v[216:219], v[132:135], v[152:155]\n\t" \
  "ds_read_b32 v232, v232 offset:0\n\t" \
  "v_fma_f32 v191, |v191|, v227, v191 mul:4\n\t" \
  "v_fma_f32 v190, |v190|, v226, v190 mul:4\n\t" \
  "v_mfma_f32_16x16x32_bf16 v[160:163], v[216:219], v[136:139], v[160:163]\n\t" \
  "ds_read_b32 v235, v235 offset:0\n\t" \
  "v_cvt_pk_bf16_f32 v180, v180, v181\n\t" \
  "v_cvt_pk_bf16_f32 v181, v182, v183\n\t" \
  "v_mfma_f32_16x16x32_bf16 v[168:171], v[216:219], v[140:143], v[168:171]\n\t" \
  "ds_read_b64_tr_b16 v[216:217], v248 offset:39464\n\t" \
  "v_cvt_pk_bf16_f32 v188, v188, v189\n\t" \
  "v_cvt_pk_bf16_f32 v189, v190, v191\n\t" \
  "s_waitcnt lgkmcnt(9)\n\t" \
  "v_mfma_f32_16x16x32_bf16 v[148:151], v[208:211], v[96:99], 0\n\t" \
  "ds_read_b64_tr_b16 v[218:219], v248 offset:42536\n\t" \
  "v_cvt_pk_f16_f32 v220, |v146|, |v147| clamp\n\t" \
  "s_setprio 3\n\t" \
  "v_cvt_pknorm_i16_f32 v222, |v144|, |v145|\n\t" \
  "v_mfma_f32_16x16x32_bf16 v[156:159], v[208:211], v[100:103], 0\n\t" \
  "s_waitcnt vmcnt(1)\n\t" \
  "s_cmp_lg_u32 s58, 1\n\t" \
  "s_cbranch_scc1 .Lcnx_c10\n\t" \
  "s_waitcnt vmcnt(0)\n\t" \
  ".Lcnx_c10:\n\t" \
  "s_mov_b64 exec, 3\n\t" \
  "ds_add_rtn_u32 v246, v251, v250 offset:47160\n\t" \
  "s_mov_b64 exec, -1\n\t" \
  "ds_read_b32 v234, v234 offset:0\n\t" \
  "v_cvt_pk_f16_f32 v224, |v154|, |v155| clamp\n\t" \
  "v_cvt_pknorm_i16_f32 v226, |v152|, |v153|\n\t" \
  "v_mfma_f32_16x16x32_bf16 v[164:167], v[208:211], v[104:107], 0\n\t" \
  "v_and_b32_sdwa v221, s56, v220 dst_sel:DWORD dst_unused:UNUSED_PAD src0_sel:DWORD src1_sel:WORD_1\n\t" \
  "v_and_b32 v220, s56, v220\n\t" \
  "v_mfma_f32_16x16x32_bf16 v[172:175], v[208:211], v[108:111], 0\n\t" \
  "s_waitcnt lgkmcnt(1)\n\t" \
  "v_readfirstlane_b32 s4, v246\n\t" \
  "s_cmp_lt_u32 s4, s57\n\t" \
  "s_cbranch_scc1 .Lcnx_w24\n.Lcnx_b24:\n\t" \
  "ds_read_b64_tr_b16 v[208:209], v249 offset:30752\n\t" \
  "v_lshlrev_b32_sdwa v223, 2, v222 dst_sel:DWORD dst_unused:UNUSED_PAD src0_sel:DWORD src1_sel:BYTE_3\n\t" \
  "v_lshlrev_b32_sdwa v222, 2, v222 dst_sel:DWORD dst_unused:UNUSED_PAD src0_sel:DWORD src1_sel:BYTE_1\n\t" \
  "v_and_b32_sdwa v225, s56, v224 dst_sel:DWORD dst_unused:UNUSED_PAD src0_sel:DWORD src1_sel:WORD_1\n\t" \
  "v_mfma_f32_16x16x32_bf16 v[148:151], v[212:215], v[112:115], v[148:151]\n\t" \
  "ds_read_b64_tr_b16 v[210:211], v249 offset:32288\n\t" \
  "v_and_b32 v224, s56, v224\n\t" \
  "v_lshlrev_b32_sdwa v227, 2, v226 dst_sel:DWORD dst_unused:UNUSED_PAD src0_sel:DWORD src1_sel:BYTE_3\n\t" \
  "v_mfma_f32_16x16x32_bf16 v[156:159], v[212:215], v[116:119], v[156:159]\n\t" \
  "s_cmp_eq_u32 s37, 0\n\t" \
  "s_cbranch_scc1 .Lcnx_f3\n.Lcnx_g3:\n\t" \
  "ds_read_b32 v221, v221 offset:0\n\t" \
  "ds_read_b32 v220, v220 offset:0\n\t" \
  "v_lshlrev_b32_sdwa v226, 2, v226 dst_sel:DWORD dst_unused:UNUSED_PAD src0_sel:DWORD src1_sel:BYTE_1\n\t" \
  "v_fma_f32 v197, |v197|, v229, v197 mul:4\n\t" \
  "v_mfma_f32_16x16x32_bf16 v[164:167], v[212:215], v[120:123], v[164:167]\n\t" \
  "ds_read_b32 v223, v223 offset:15376\n\t" \
  "ds_read_b32 v222, v222 offset:15376\n\t" \
  "v_fma_f32 v196, |v196|, v228, v196 mul:4\n\t" \
  "v_fma_mix_f32 v231, |v199|, v231, v231 op_sel:[0,1,0] op_sel_hi:[0,1,1] clamp\n\t" \
  "v_mfma_f32_16x16x32_bf16 v[172:175], v[212:215], v[124:127], v[172:175]\n\t" \
  "ds_read_b64_tr_b16 v[212:213], v249 offset:30760\n\t" \
  "ds_read_b64_tr_b16 v[214:215], v249 offset:32296\n\t" \
  "v_fma_mix_f32 v230, |v198|, v230, v230 op_sel:[0,1,0] op_sel_hi:[0,1,1] clamp\n\t" \
  "s_waitcnt lgkmcnt(8)\n\t" \
  "v_fma_f32 v205, |v205|, v233, v205 mul:4\n\t" \
  "v_mfma_f32_16x16x32_bf16 v[148:151], v[216:219], v[128:131], v[148:151]\n\t" \
  "ds_read_b32 v225, v225 offset:0\n\t" \
  "v_fma_f32 v204, |v204|, v232, v204 mul:4\n\t" \
  "v_fma_f32 v207, |v207|, v235, v207 mul:4\n\t" \
  "v_mfma_f32_16x16x32_bf16 v[156:159], v[216:219], v[132:135], v[156:159]\n\t" \
  "ds_read_b32 v224, v224 offset:0\n\t" \
  "v_fma_f32 v206, |v206|, v234, v206 mul:4\n\t" \
  "v_fma_f32 v199, |v199|, v231, v199 mul:4\n\t" \
  "v_mfma_f32_16x16x32_bf16 v[164:167], v[216:219], v[136:139], v[164:167]\n\t" \
  "ds_read_b32 v227, v227 offset:15376\n\t" \
  "v_fma_f32 v198, |v198|, v230, v198 mul:4\n\t" \
  "v_cvt_pk_bf16_f32 v196, v196, v197\n\t" \
  "v_mfma_f32_16x16x32_bf16 v[172:175], v[216:219], v[140:143], v[172:175]\n\t" \
  "ds_read_b64_tr_b16 v[216:217], v249 offset:30816\n\t" \
  "v_cvt_pk_bf16_f32 v197, v198, v199\n\t" \
  "v_cvt_pk_bf16_f32 v204, v204, v205\n\t" \
  "v_cvt_pk_bf16_f32 v205, v206, v207\n\t" \
  "s_setprio 0\n\t" \
  "v_cvt_pk_f16_f32 v228, |v162|, |v163| clamp\n\t" \
  "v_cvt_pknorm_i16_f32 v230, |v160|, |v161|\n\t" \
  "v_mfma_f32_16x16x32_bf16 v[8:11], v[242:245], v[236:239], v[8:11]\n\t" \
  "ds_read_b64_tr_b16 v[218:219], v249 offset:32352\n\t" \
  "v_cvt_pk_f16_f32 v232, |v170|, |v171| clamp\n\t" \
  "v_cvt_pk_f16_f32 v234, |v168|, |v169| clamp\n\t" \
  "v_mfma_f32_16x16x32_bf16 v[12:15], v[240:243], v[236:239], v[12:15]\n\t" \
  "buffer_load_dwordx4 v[236:239], v250, s[8:11], s28 offen offset:128\n\t" \
  "s_cmp_lg_u32 s58, 0\n\t" \
  "s_cbranch_scc1 .Lcnx_c23\n\t" \
  "s_add_u32 m0, s25, 8200\n\t" \
  "s_add_u32 s24, s24, s26\n\t" \
  "buffer_load_dwordx4 v254, s[8:11], s24 offen lds\n\t" \
  "s_cmp_lg_u32 s38, 7\n\t" \
  "s_cbranch_scc1 .Lcnx_c23\n\t" \
  "s_mov_b32 m0, 163632\n\t" \
  "s_add_u32 s30, s30, s31\n\t" \
  "buffer_load_dword v252, s[8:11], s30 offen lds\n\t" \
  ".Lcnx_c23:\n\t" \
  "ds_read_b32 v226, v226 offset:15376\n\t" \
  "v_and_b32_sdwa v229, s56, v228 dst_sel:DWORD dst_unused:UNUSED_PAD src0_sel:DWORD src1_sel:WORD_1\n\t" \
  "v_and_b32 v228, s56, v228\n\t" \
  "s_waitcnt lgkmcnt(12)\n\t" \
  "v_mfma_f32_16x16x32_bf16 v[0:3], v[208:211], v[178:181], v[0:3]\n\t" \
  "ds_read_b32 v229, v229 offset:0\n\t" \
  "v_lshlrev_b32_sdwa v231, 2, v230 dst_sel:DWORD dst_unused:UNUSED_PAD src0_sel:DWORD src1_sel:BYTE_3\n\t" \
  "v_lshlrev_b32_sdwa v230, 2, v230 dst_sel:DWORD dst_unused:UNUSED_PAD src0_sel:DWORD src1_sel:BYTE_1\n\t" \
  "v_mfma_f32_16x16x32_bf16 v[24:27], v[208:211], v[186:189], v[24:27]\n\t" \
  "ds_read_b32 v228, v228 offset:0\n\t" \
  "v_and_b32_sdwa v233, s56, v232 dst_sel:DWORD dst_unused:UNUSED_PAD src0_sel:DWORD src1_sel:WORD_1\n\t" \
  "v_and_b32 v232, s56, v232\n\t" \
  "v_mfma_f32_16x16x32_bf16 v[48:51], v[208:211], v[194:197], v[48:51]\n\t" \
  "ds_read_b32 v231, v231 offset:15376\n\t" \
  "v_and_b32_sdwa v235, s56, v234 dst_sel:DWORD dst_unused:UNUSED_PAD src0_sel:DWORD src1_sel:WORD_1\n\t" \
  "v_and_b32 v234, s56, v234\n\t" \
  "v_mfma_f32_16x16x32_bf16 v[72:75], v[208:211], v[202:205], v[72:75]\n\t" \
  "ds_read_b64_tr_b16 v[208:209], v249 offset:30824\n\t" \
  "ds_read_b64_tr_b16 v[210:211], v249 offset:32360\n\t" \
  "s_waitcnt lgkmcnt(13)\n\t" \
  "v_fma_f32 v147, |v147|, v221, v147 mul:4\n\t" \
  "v_fma_f32 v146, |v146|, v220, v146 mul:4\n\t" \
  "s_waitcnt lgkmcnt(11)\n\t" \
  "v_mfma_f32_16x16x32_bf16 v[4:7], v[212:215], v[178:181], v[4:7]\n\t" \
  "ds_read_b32 v230, v230 offset:15376\n\t" \
  "v_fma_mix_f32 v223, |v145|, v223, v223 op_sel:[0,1,0] op_sel_hi:[0,1,1] clamp\n\t" \
  "v_fma_mix_f32 v222, |v144|, v222, v222 op_sel:[0,1,0] op_sel_hi:[0,1,1] clamp\n\t" \
  "v_mfma_f32_16x16x32_bf16 v[28:31], v[212:215], v[186:189], v[28:31]\n\t" \
  "ds_read_b32 v233, v233 offset:0\n\t" \
  "s_waitcnt lgkmcnt(7)\n\t" \
  "v_fma_f32 v155, |v155|, v225, v155 mul:4\n\t" \
  "v_fma_f32 v154, |v154|, v224, v154 mul:4\n\t" \
  "v_mfma_f32_16x16x32_bf16 v[52:55], v[212:215], v[194:197], v[52:55]\n\t" \
  "ds_read_b32 v232, v232 offset:0\n\t" \
  "v_fma_mix_f32 v227, |v153|, v227, v227 op_sel:[0,1,0] op_sel_hi:[0,1,1] clamp\n\t" \
  "v_fma_mix_f32 v226, |v152|, v226, v226 op_sel:[0,1,0] op_sel_hi:[0,1,1] clamp\n\t" \
  "v_fma_f32 v145, |v145|, v223, v145 mul:4\n\t" \
  "v_mfma_f32_16x16x32_bf16 v[76:79], v[212:215], v[202:205], v[76:79]\n\t" \
  "ds_read_b64_tr_b16 v[212:213], v249 offset:30880\n\t" \
  "v_fma_f32 v144, |v144|, v222, v144 mul:4\n\t" \
  "v_fma_f32 v153, |v153|, v227, v153 mul:4\n\t" \
  "v_mfma_f32_16x16x32_bf16 v[8:11], v[216:219], v[178:181], v[8:11]\n\t" \
  "ds_read_b64_tr_b16 v[214:215], v249 offset:32416\n\t" \
  "v_fma_f32 v152, |v152|, v226, v152 mul:4\n\t" \
  "v_cvt_pk_bf16_f32 v147, v146, v147\n\t" \
  "v_mfma_f32_16x16x32_bf16 v[32:35], v[216:219], v[186:189], v[32:35]\n\t" \
  "ds_read_b32 v235, v235 offset:0\n\t" \
  "v_cvt_pk_bf16_f32 v146, v144, v145\n\t" \
  "v_cvt_pk_bf16_f32 v155, v154, v155\n\t" \
  "v_mfma_f32_16x16x32_bf16 v[56:59], v[216:219], v[194:197], v[56:59]\n\t" \
  "ds_read_b32 v234, v234 offset:0\n\t" \
  "v_cvt_pk_bf16_f32 v154, v152, v153\n\t" \
  "s_setprio 1\n\t" \
  "v_cvt_pk_f16_f32 v220, |v148|, |v149| clamp\n\t" \
  "v_mfma_f32_16x16x32_bf16 v[80:83], v[216:219], v[202:205], v[80:83]\n\t" \
  "ds_read_b64_tr_b16 v[216:217], v249 offset:30888\n\t" \
  "v_cvt_pknorm_i16_f32 v222, |v150|, |v151|\n\t" \
  "v_cvt_pk_f16_f32 v224, |v156|, |v157| clamp\n\t" \
  "s_waitcnt lgkmcnt(8)\n\t" \
  "v_mfma_f32_16x16x32_bf16 v[12:15], v[208:211], v[178:181], v[12:15]\n\t" \
  "ds_read_b64_tr_b16 v[218:219], v249 offset:32424\n\t" \
  "v_cvt_pknorm_i16_f32 v226, |v158|, |v159|\n\t" \
  "v_and_b32_sdwa v221, s56, v220 dst_sel:DWORD dst_unused:UNUSED_PAD src0_sel:DWORD src1_sel:WORD_1\n\t" \
  "v_mfma_f32_16x16x32_bf16 v[36:39], v[208:211], v[186:189], v[36:39]\n\t" \
  "v_and_b32 v220, s56, v220\n\t" \
  "v_lshlrev_b32_sdwa v223, 2, v222 dst_sel:DWORD dst_unused:UNUSED_PAD src0_sel:DWORD src1_sel:BYTE_3\n\t" \
  "v_mfma_f32_16x16x32_bf16 v[60:63], v[208:211], v[194:197], v[60:63]\n\t" \
  "ds_read_b32 v221, v221 offset:0\n\t" \
  "v_lshlrev_b32_sdwa v222, 2, v222 dst_sel:DWORD dst_unused:UNUSED_PAD src0_sel:DWORD src1_sel:BYTE_1\n\t" \
  "v_and_b32_sdwa v225, s56, v224 dst_sel:DWORD dst_unused:UNUSED_PAD src0_sel:DWORD src1_sel:WORD_1\n\t" \
  "v_and_b32 v224, s56, v224\n\t" \
  "v_mfma_f32_16x16x32_bf16 v[84:87], v[208:211], v[202:205], v[84:87]\n\t" \
  "ds_read_b64_tr_b16 v[208:209], v248 offset:12304\n\t" \
  "ds_read_b64_tr_b16 v[210:211], v248 offset:13328\n\t" \
  "v_lshlrev_b32_sdwa v227, 2, v226 dst_sel:DWORD dst_unused:UNUSED_PAD src0_sel:DWORD src1_sel:BYTE_3\n\t" \
  "v_lshlrev_b32_sdwa v226, 2, v226 dst_sel:DWORD dst_unused:UNUSED_PAD src0_sel:DWORD src1_sel:BYTE_1\n\t" \
  "s_waitcnt lgkmcnt(7)\n\t" \
  "v_mfma_f32_16x16x32_bf16 v[16:19], v[212:215], v[178:181], v[16:19]\n\t" \
  "ds_read_b32 v220, v220 offset:0\n\t" \
  "ds_read_b32 v223, v223 offset:15376\n\t" \
  "v_fma_f32 v163, |v163|, v229, v163 mul:4\n\t" \
  "v_fma_f32 v162, |v162|, v228, v162 mul:4\n\t" \
  "v_mfma_f32_16x16x32_bf16 v[40:43], v[212:215], v[186:189], v[40:43]\n\t" \
  "ds_read_b32 v222, v222 offset:15376\n\t" \
  "v_fma_mix_f32 v231, |v161|, v231, v231 op_sel:[0,1,0] op_sel_hi:[0,1,1] clamp\n\t" \
  "v_fma_mix_f32 v230, |v160|, v230, v230 op_sel:[0,1,0] op_sel_hi:[0,1,1] clamp\n\t" \
  "v_mfma_f32_16x16x32_bf16 v[64:67], v[212:215], v[194:197], v[64:67]\n\t" \
  "ds_read_b32 v225, v225 offset:0\n\t" \
  "s_waitcnt lgkmcnt(9)\n\t" \
  "v_fma_f32 v171, |v171|, v233, v171 mul:4\n\t" \
  "v_fma_f32 v170, |v170|, v232, v170 mul:4\n\t" \
  "v_mfma_f32_16x16x32_bf16 v[88:91], v[212:215], v[202:205], v[88:91]\n\t" \
  "ds_read_b64_tr_b16 v[212:213], v248 offset:15376\n\t" \
  "v_fma_f32 v169, |v169|, v235, v169 mul:4\n\t" \
  "v_fma_f32 v168, |v168|, v234, v168 mul:4\n\t" \
  "s_waitcnt lgkmcnt(8)\n\t" \
  "v_mfma_f32_16x16x32_bf16 v[20:23], v[216:219], v[178:181], v[20:23]\n\t" \
  "ds_read_b64_tr_b16 v[214:215], v248 offset:16400\n\t" \
  "v_fma_f32 v161, |v161|, v231, v161 mul:4\n\t" \
  "v_fma_f32 v160, |v160|, v230, v160 mul:4\n\t" \
  "v_mfma_f32_16x16x32_bf16 v[44:47], v[216:219], v[186:189], v[44:47]\n\t" \
  "ds_read_b32 v224, v224 offset:0\n\t" \
  "v_cvt_pk_bf16_f32 v163, v162, v163\n\t" \
  "v_cvt_pk_bf16_f32 v162, v160, v161\n\t" \
  "v_mfma_f32_16x16x32_bf16 v[68:71], v[216:219], v[194:197], v[68:71]\n\t" \
  "ds_read_b32 v227, v227 offset:15376\n\t" \
  "v_cvt_pk_bf16_f32 v171, v170, v171\n\t" \
  "v_cvt_pk_bf16_f32 v170, v168, v169\n\t" \
  "v_cvt_pk_f16_f32 v228, |v164|, |v165| clamp\n\t" \
  "v_mfma_f32_16x16x32_bf16 v[92:95], v[216:219], v[202:205], v[92:95]\n\t" \
  "ds_read_b64_tr_b16 v[216:217], v248 offset:14352\n\t" \
  "v_cvt_pknorm_i16_f32 v230, |v166|, |v167|\n\t" \
  "s_setprio 2\n\t" \
  "v_cvt_pk_f16_f32 v232, |v172|, |v173| clamp\n\t" \
  "s_waitcnt lgkmcnt(9)\n\t" \
  "v_mfma_f32_16x16x32_bf16 v[176:179], v[208:211], v[96:99], 0\n\t" \
  "ds_read_b64_tr_b16 v[218:219], v248 offset:17424\n\t" \
  "v_cvt_pk_f16_f32 v234, |v174|, |v175| clamp\n\t" \
  "v_and_b32_sdwa v229, s56, v228 dst_sel:DWORD dst_unused:UNUSED_PAD src0_sel:DWORD src1_sel:WORD_1\n\t" \
  "v_mfma_f32_16x16x32_bf16 v[184:187], v[208:211], v[100:103], 0\n\t" \
  "ds_read_b32 v226, v226 offset:15376\n\t" \
  "v_and_b32 v228, s56, v228\n\t" \
  "v_lshlrev_b32_sdwa v231, 2, v230 dst_sel:DWORD dst_unused:UNUSED_PAD src0_sel:DWORD src1_sel:BYTE_3\n\t" \
  "v_mfma_f32_16x16x32_bf16 v[192:195], v[208:211], v[104:107], 0\n\t" \
  "ds_read_b32 v229, v229 offset:0\n\t" \
  "v_lshlrev_b32_sdwa v230, 2, v230 dst_sel:DWORD dst_unused:UNUSED_PAD src0_sel:DWORD src1_sel:BYTE_1\n\t" \
  "v_and_b32_sdwa v233, s56, v232 dst_sel:DWORD dst_unused:UNUSED_PAD src0_sel:DWORD src1_sel:WORD_1\n\t" \
  "v_mfma_f32_16x16x32_bf16 v[200:203], v[208:211], v[108:111], 0\n\t" \
  "ds_read_b64_tr_b16 v[208:209], v248 offset:12816\n\t" \
  "v_and_b32 v232, s56, v232\n\t" \
  "v_and_b32_sdwa v235, s56, v234 dst_sel:DWORD dst_unused:UNUSED_PAD src0_sel:DWORD src1_sel:WORD_1\n\t" \
  "s_waitcnt lgkmcnt(7)\n\t" \
  "v_mfma_f32_16x16x32_bf16 v[176:179], v[212:215], v[112:115], v[176:179]\n\t" \
  "ds_read_b64_tr_b16 v[210:211], v248 offset:13840\n\t" \
  "ds_read_b32 v228, v228 offset:0\n\t" \
  "v_and_b32 v234, s56, v234\n\t" \
  "v_fma_f32 v149, |v149|, v221, v149 mul:4\n\t" \
  "v_mfma_f32_16x16x32_bf16 v[184:187], v[212:215], v[116:119], v[184:187]\n\t" \
  "ds_read_b32 v231, v231 offset:15376\n\t" \
  "ds_read_b32 v230, v230 offset:15376\n\t" \
  "v_fma_f32 v148, |v148|, v220, v148 mul:4\n\t" \
  "v_fma_mix_f32 v223, |v151|, v223, v223 op_sel:[0,1,0] op_sel_hi:[0,1,1] clamp\n\t" \
  "v_mfma_f32_16x16x32_bf16 v[192:195], v[212:215], v[120:123], v[192:195]\n\t" \
  "ds_read_b32 v233, v233 offset:0\n\t" \
  "v_fma_mix_f32 v222, |v150|, v222, v222 op_sel:[0,1,0] op_sel_hi:[0,1,1] clamp\n\t" \
  "s_waitcnt lgkmcnt(7)\n\t" \
  "v_fma_f32 v157, |v157|, v225, v157 mul:4\n\t" \
  "v_mfma_f32_16x16x32_bf16 v[200:203], v[212:215], v[124:127], v[200:203]\n\t" \
  "ds_read_b64_tr_b16 v[212:213], v248 offset:15888\n\t" \
  "v_fma_f32 v156, |v156|, v224, v156 mul:4\n\t" \
  "v_fma_mix_f32 v227, |v159|, v227, v227 op_sel:[0,1,0] op_sel_hi:[0,1,1] clamp\n\t" \
  "v_fma_mix_f32 v226, |v158|, v226, v226 op_sel:[0,1,0] op_sel_hi:[0,1,1] clamp\n\t" \
  "v_mfma_f32_16x16x32_bf16 v[176:179], v[216:219], v[128:131], v[176:179]\n\t" \
  "ds_read_b64_tr_b16 v[214:215], v248 offset:16912\n\t" \
  "v_fma_f32 v151, |v151|, v223, v151 mul:4\n\t" \
  "v_fma_f32 v150, |v150|, v222, v150 mul:4\n\t" \
  "v_mfma_f32_16x16x32_bf16 v[184:187], v[216:219], v[132:135], v[184:187]\n\t" \
  "ds_read_b32 v232, v232 offset:0\n\t" \
  "v_fma_f32 v159, |v159|, v227, v159 mul:4\n\t" \
  "v_fma_f32 v158, |v158|, v226, v158 mul:4\n\t" \
  "v_mfma_f32_16x16x32_bf16 v[192:195], v[216:219], v[136:139], v[192:195]\n\t" \
  "ds_read_b32 v235, v235 offset:0\n\t" \
  "v_cvt_pk_bf16_f32 v148, v148, v149\n\t" \
  "v_cvt_pk_bf16_f32 v149, v150, v151\n\t" \
  "v_mfma_f32_16x16x32_bf16 v[200:203], v[216:219], v[140:143], v[200:203]\n\t" \
  "ds_read_b64_tr_b16 v[216:217], v248 offset:14864\n\t" \
  "v_cvt_pk_bf16_f32 v156, v156, v157\n\t" \
  "v_cvt_pk_bf16_f32 v157, v158, v159\n\t" \
  "s_waitcnt lgkmcnt(9)\n\t" \
  "v_mfma_f32_16x16x32_bf16 v[180:183], v[208:211], v[96:99], 0\n\t" \
  "ds_read_b64_tr_b16 v[218:219], v248 offset:17936\n\t" \
  "v_cvt_pk_f16_f32 v220, |v178|, |v179| clamp\n\t" \
  "s_setprio 3\n\t" \
  "v_cvt_pknorm_i16_f32 v222, |v176|, |v177|\n\t" \
  "v_mfma_f32_16x16x32_bf16 v[188:191], v[208:211], v[100:103], 0\n\t" \
  "s_waitcnt vmcnt(1)\n\t" \
  "s_cmp_lg_u32 s58, 1\n\t" \
  "s_cbranch_scc1 .Lcnx_c11\n\t" \
  "s_waitcnt vmcnt(0)\n\t" \
  ".Lcnx_c11:\n\t" \
  "s_mov_b64 exec, 3\n\t" \
  "ds_add_rtn_u32 v246, v251, v250 offset:47168\n\t" \
  "s_mov_b64 exec, -1\n\t" \
  "ds_read_b32 v234, v234 offset:0\n\t" \
  "v_cvt_pk_f16_f32 v224, |v186|, |v187| clamp\n\t" \
  "v_cvt_pknorm_i16_f32 v226, |v184|, |v185|\n\t" \
  "v_mfma_f32_16x16x32_bf16 v[196:199], v[208:211], v[104:107], 0\n\t" \
  "v_and_b32_sdwa v221, s56, v220 dst_sel:DWORD dst_unused:UNUSED_PAD src0_sel:DWORD src1_sel:WORD_1\n\t" \
  "v_and_b32 v220, s56, v220\n\t" \
  "v_mfma_f32_16x16x32_bf16 v[204:207], v[208:211], v[108:111], 0\n\t" \
  "s_waitcnt lgkmcnt(1)\n\t" \
  "v_readfirstlane_b32 s4, v246\n\t" \
  "s_cmp_lt_u32 s4, s57\n\t" \
  "s_cbranch_scc1 .Lcnx_w36\n.Lcnx_b36:\n\t" \
  "ds_read_b64_tr_b16 v[208:209], v249 offset:0\n\t" \
  "v_lshlrev_b32_sdwa v223, 2, v222 dst_sel:DWORD dst_unused:UNUSED_PAD src0_sel:DWORD src1_sel:BYTE_3\n\t" \
  "v_lshlrev_b32_sdwa v222, 2, v222 dst_sel:DWORD dst_unused:UNUSED_PAD src0_sel:DWORD src1_sel:BYTE_1\n\t" \
  "v_and_b32_sdwa v225, s56, v224 dst_sel:DWORD dst_unused:UNUSED_PAD src0_sel:DWORD src1_sel:WORD_1\n\t" \
  "v_mfma_f32_16x16x32_bf16 v[180:183], v[212:215], v[112:115], v[180:183]\n\t" \
  "ds_read_b64_tr_b16 v[210:211], v249 offset:1536\n\t" \
  "v_and_b32 v224, s56, v224\n\t" \
  "v_lshlrev_b32_sdwa v227, 2, v226 dst_sel:DWORD dst_unused:UNUSED_PAD src0_sel:DWORD src1_sel:BYTE_3\n\t" \
  "v_mfma_f32_16x16x32_bf16 v[188:191], v[212:215], v[116:119], v[188:191]\n\t" \
  "s_cmp_eq_u32 s37, 0\n\t" \
  "s_cbranch_scc1 .Lcnx_f4\n.Lcnx_g4:\n\t" \
  "ds_read_b32 v221, v221 offset:0\n\t" \
  "ds_read_b32 v220, v220 offset:0\n\t" \
  "v_lshlrev_b32_sdwa v226, 2, v226 dst_sel:DWORD dst_unused:UNUSED_PAD src0_sel:DWORD src1_sel:BYTE_1\n\t" \
  "v_fma_f32 v165, |v165|, v229, v165 mul:4\n\t" \
  "v_mfma_f32_16x16x32_bf16 v[196:199], v[212:215], v[120:123], v[196:199]\n\t" \
  "ds_read_b32 v223, v223 offset:15376\n\t" \
  "ds_read_b32 v222, v222 offset:15376\n\t" \
  "v_fma_f32 v164, |v164|, v228, v164 mul:4\n\t" \
  "v_fma_mix_f32 v231, |v167|, v231, v231 op_sel:[0,1,0] op_sel_hi:[0,1,1] clamp\n\t" \
  "v_mfma_f32_16x16x32_bf16 v[204:207], v[212:215], v[124:127], v[204:207]\n\t" \
  "ds_read_b64_tr_b16 v[212:213], v249 offset:8\n\t" \
  "ds_read_b64_tr_b16 v[214:215], v249 offset:1544\n\t" \
  "v_fma_mix_f32 v230, |v166|, v230, v230 op_sel:[0,1,0] op_sel_hi:[0,1,1] clamp\n\t" \
  "s_waitcnt lgkmcnt(8)\n\t" \
  "v_fma_f32 v173, |v173|, v233, v173 mul:4\n\t" \
  "v_mfma_f32_16x16x32_bf16 v[180:183], v[216:219], v[128:131], v[180:183]\n\t" \
  "ds_read_b32 v225, v225 offset:0\n\t" \
  "v_fma_f32 v172, |v172|, v232, v172 mul:4\n\t" \
  "v_fma_f32 v175, |v175|, v235, v175 mul:4\n\t" \
  "v_mfma_f32_16x16x32_bf16 v[188:191], v[216:219], v[132:135], v[188:191]\n\t" \
  "ds_read_b32 v224, v224 offset:0\n\t" \
  "v_fma_f32 v174, |v174|, v234, v174 mul:4\n\t" \
  "v_fma_f32 v167, |v167|, v231, v167 mul:4\n\t" \
  "v_mfma_f32_16x16x32_bf16 v[196:199], v[216:219], v[136:139], v[196:199]\n\t" \
  "ds_read_b32 v227, v227 offset:15376\n\t" \
  "v_fma_f32 v166, |v166|, v230, v166 mul:4\n\t" \
  "v_cvt_pk_bf16_f32 v164, v164, v165\n\t" \
  "v_mfma_f32_16x16x32_bf16 v[204:207], v[216:219], v[140:143], v[204:207]\n\t" \
  "ds_read_b64_tr_b16 v[216:217], v249 offset:64\n\t" \
  "v_cvt_pk_bf16_f32 v165, v166, v167\n\t" \
  "v_cvt_pk_bf16_f32 v172, v172, v173\n\t" \
  "v_cvt_pk_bf16_f32 v173, v174, v175\n\t" \
  "s_setprio 0\n\t" \
  "v_cvt_pk_f16_f32 v228, |v194|, |v195| clamp\n\t" \
  "v_cvt_pknorm_i16_f32 v230, |v192|, |v193|\n\t" \
  "v_mfma_f32_16x16x32_bf16 v[16:19], v[242:245], v[236:239], v[16:19]\n\t" \
  "ds_read_b64_tr_b16 v[218:219], v249 offset:1600\n\t" \
  "v_cvt_pk_f16_f32 v232, |v202|, |v203| clamp\n\t" \
  "v_cvt_pk_f16_f32 v234, |v200|, |v201| clamp\n\t" \
  "v_mfma_f32_16x16x32_bf16 v[20:23], v[240:243], v[236:239], v[20:23]\n\t" \
  "buffer_load_dwordx4 v[236:239], v250, s[8:11], s29 offen offset:0\n\t" \
  "s_cmp_lg_u32 s58, 0\n\t" \
  "s_cbranch_scc1 .Lcnx_c24\n\t" \
  "s_add_u32 m0, s25, 16400\n\t" \
  "s_add_u32 s24, s24, s26\n\t" \
  "buffer_load_dwordx4 v254, s[8:11], s24 offen lds\n\t" \
  "s_cmp_lg_u32 s38, 4\n\t" \
  "s_cbranch_scc1 .Lcnx_c24\n\t" \
  "s_mov_b32 m0, 163632\n\t" \
  "s_add_u32 s30, s30, s31\n\t" \
  "buffer_load_dword v252, s[8:11], s30 offen lds\n\t" \
  ".Lcnx_c24:\n\t" \
  "ds_read_b32 v226, v226 offset:15376\n\t" \
  "v_and_b32_sdwa v229, s56, v228 dst_sel:DWORD dst_unused:UNUSED_PAD src0_sel:DWORD src1_sel:WORD_1\n\t" \
  "v_and_b32 v228, s56, v228\n\t" \
  "s_waitcnt lgkmcnt(12)\n\t" \
  "v_mfma_f32_16x16x32_bf16 v[0:3], v[208:211], v[146:149], v[0:3]\n\t" \
  "ds_read_b32 v229, v229 offset:0\n\t" \
  "v_lshlrev_b32_sdwa v231, 2, v230 dst_sel:DWORD dst_unused:UNUSED_PAD src0_sel:DWORD src1_sel:BYTE_3\n\t" \
  "v_lshlrev_b32_sdwa v230, 2, v230 dst_sel:DWORD dst_unused:UNUSED_PAD src0_sel:DWORD src1_sel:BYTE_1\n\t" \
  "v_mfma_f32_16x16x32_bf16 v[24:27], v[208:211], v[154:157], v[24:27]\n\t" \
  "ds_read_b32 v228, v228 offset:0\n\t" \
  "v_and_b32_sdwa v233, s56, v232 dst_sel:DWORD dst_unused:UNUSED_PAD src0_sel:DWORD src1_sel:WORD_1\n\t" \
  "v_and_b32 v232, s56, v232\n\t" \
  "v_mfma_f32_16x16x32_bf16 v[48:51], v[208:211], v[162:165], v[48:51]\n\t" \
  "ds_read_b32 v231, v231 offset:15376\n\t" \
  "v_and_b32_sdwa v235, s56, v234 dst_sel:DWORD dst_unused:UNUSED_PAD src0_sel:DWORD src1_sel:WORD_1\n\t" \
  "v_and_b32 v234, s56, v234\n\t" \
  "v_mfma_f32_16x16x32_bf16 v[72:75], v[208:211], v[170:173], v[72:75]\n\t" \
  "ds_read_b64_tr_b16 v[208:209], v249 offset:72\n\t" \
  "ds_read_b64_tr_b16 v[210:211], v249 offset:1608\n\t" \
  "s_waitcnt lgkmcnt(13)\n\t" \
  "v_fma_f32 v179, |v179|, v221, v179 mul:4\n\t" \
  "v_fma_f32 v178, |v178|, v220, v178 mul:4\n\t" \
  "s_waitcnt lgkmcnt(11)\n\t" \
  "v_mfma_f32_16x16x32_bf16 v[4:7], v[212:215], v[146:149], v[4:7]\n\t" \
  "ds_read_b32 v230, v230 offset:15376\n\t" \
  "v_fma_mix_f32 v223, |v177|, v223, v223 op_sel:[0,1,0] op_sel_hi:[0,1,1] clamp\n\t" \
  "v_fma_mix_f32 v222, |v176|, v222, v222 op_sel:[0,1,0] op_sel_hi:[0,1,1] clamp\n\t" \
  "v_mfma_f32_16x16x32_bf16 v[28:31], v[212:215], v[154:157], v[28:31]\n\t" \
  "ds_read_b32 v233, v233 offset:0\n\t" \
  "s_waitcnt lgkmcnt(7)\n\t" \
  "v_fma_f32 v187, |v187|, v225, v187 mul:4\n\t" \
  "v_fma_f32 v186, |v186|, v224, v186 mul:4\n\t" \
  "v_mfma_f32_16x16x32_bf16 v[52:55], v[212:215], v[162:165], v[52:55]\n\t" \
  "ds_read_b32 v232, v232 offset:0\n\t" \
  "v_fma_mix_f32 v227, |v185|, v227, v227 op_sel:[0,1,0] op_sel_hi:[0,1,1] clamp\n\t" \
  "v_fma_mix_f32 v226, |v184|, v226, v226 op_sel:[0,1,0] op_sel_hi:[0,1,1] clamp\n\t" \
  "v_fma_f32 v177, |v177|, v223, v177 mul:4\n\t" \
  "v_mfma_f32_16x16x32_bf16 v[76:79], v[212:215], v[170:173], v[76:79]\n\t" \
  "ds_read_b64_tr_b16 v[212:213], v249 offset:128\n\t" \
  "v_fma_f32 v176, |v176|, v222, v176 mul:4\n\t" \
  "v_fma_f32 v185, |v185|, v227, v185 mul:4\n\t" \
  "v_mfma_f32_16x16x32_bf16 v[8:11], v[216:219], v[146:149], v[8:11]\n\t" \
  "ds_read_b64_tr_b16 v[214:215], v249 offset:1664\n\t" \
  "v_fma_f32 v184, |v184|, v226, v184 mul:4\n\t" \
  "v_cvt_pk_bf16_f32 v179, v178, v179\n\t" \
  "v_mfma_f32_16x16x32_bf16 v[32:35], v[216:219], v[154:157], v[32:35]\n\t" \
  "ds_read_b32 v235, v235 offset:0\n\t" \
  "v_cvt_pk_bf16_f32 v178, v176, v177\n\t" \
  "v_cvt_pk_bf16_f32 v187, v186, v187\n\t" \
  "v_mfma_f32_16x16x32_bf16 v[56:59], v[216:219], v[162:165], v[56:59]\n\t" \
  "ds_read_b32 v234, v234 offset:0\n\t" \
  "v_cvt_pk_bf16_f32 v186, v184, v185\n\t" \
  "s_setprio 1\n\t" \
  "v_cvt_pk_f16_f32 v220, |v180|, |v181| clamp\n\t" \
  "v_mfma_f32_16x16x32_bf16 v[80:83], v[216:219], v[170:173], v[80:83]\n\t" \
  "ds_read_b64_tr_b16 v[216:217], v249 offset:136\n\t" \
  "v_cvt_pknorm_i16_f32 v222, |v182|, |v183|\n\t" \
  "v_cvt_pk_f16_f32 v224, |v188|, |v189| clamp\n\t" \
  "s_waitcnt lgkmcnt(8)\n\t" \
  "v_mfma_f32_16x16x32_bf16 v[12:15], v[208:211], v[146:149], v[12:15]\n\t" \
  "ds_read_b64_tr_b16 v[218:219], v249 offset:1672\n\t" \
  "v_cvt_pknorm_i16_f32 v226, |v190|, |v191|\n\t" \
  "v_and_b32_sdwa v221, s56, v220 dst_sel:DWORD dst_unused:UNUSED_PAD src0_sel:DWORD src1_sel:WORD_1\n\t" \
  "v_mfma_f32_16x16x32_bf16 v[36:39], v[208:211], v[154:157], v[36:39]\n\t" \
  "v_and_b32 v220, s56, v220\n\t" \
  "v_lshlrev_b32_sdwa v223, 2, v222 dst_sel:DWORD dst_unused:UNUSED_PAD src0_sel:DWORD src1_sel:BYTE_3\n\t" \
  "v_mfma_f32_16x16x32_bf16 v[60:63], v[208:211], v[162:165], v[60:63]\n\t" \
  "ds_read_b32 v221, v221 offset:0\n\t" \
  "v_lshlrev_b32_sdwa v222, 2, v222 dst_sel:DWORD dst_unused:UNUSED_PAD src0_sel:DWORD src1_sel:BYTE_1\n\t" \
  "v_and_b32_sdwa v225, s56, v224 dst_sel:DWORD dst_unused:UNUSED_PAD src0_sel:DWORD src1_sel:WORD_1\n\t" \
  "v_and_b32 v224, s56, v224\n\t" \
  "v_mfma_f32_16x16x32_bf16 v[84:87], v[208:211], v[170:173], v[84:87]\n\t" \
  "ds_read_b64_tr_b16 v[208:209], v248 offset:43048\n\t" \
  "ds_read_b64_tr_b16 v[210:211], v248 offset:44072\n\t" \
  "v_lshlrev_b32_sdwa v227, 2, v226 dst_sel:DWORD dst_unused:UNUSED_PAD src0_sel:DWORD src1_sel:BYTE_3\n\t" \
  "v_lshlrev_b32_sdwa v226, 2, v226 dst_sel:DWORD dst_unused:UNUSED_PAD src0_sel:DWORD src1_sel:BYTE_1\n\t" \
  "s_waitcnt lgkmcnt(7)\n\t" \
  "v_mfma_f32_16x16x32_bf16 v[16:19], v[212:215], v[146:149], v[16:19]\n\t" \
  "ds_read_b32 v220, v220 offset:0\n\t" \
  "ds_read_b32 v223, v223 offset:15376\n\t" \
  "v_fma_f32 v195, |v195|, v229, v195 mul:4\n\t" \
  "v_fma_f32 v194, |v194|, v228, v194 mul:4\n\t" \
  "v_mfma_f32_16x16x32_bf16 v[40:43], v[212:215], v[154:157], v[40:43]\n\t" \
  "ds_read_b32 v222, v222 offset:15376\n\t" \
  "v_fma_mix_f32 v231, |v193|, v231, v231 op_sel:[0,1,0] op_sel_hi:[0,1,1] clamp\n\t" \
  "v_fma_mix_f32 v230, |v192|, v230, v230 op_sel:[0,1,0] op_sel_hi:[0,1,1] clamp\n\t" \
  "v_mfma_f32_16x16x32_bf16 v[64:67], v[212:215], v[162:165], v[64:67]\n\t" \
  "ds_read_b32 v225, v225 offset:0\n\t" \
  "s_waitcnt lgkmcnt(9)\n\t" \
  "v_fma_f32 v203, |v203|, v233, v203 mul:4\n\t" \
  "v_fma_f32 v202, |v202|, v232, v202 mul:4\n\t" \
  "v_mfma_f32_16x16x32_bf16 v[88:91], v[212:215], v[170:173], v[88:91]\n\t" \
  "ds_read_b64_tr_b16 v[212:213], v248 offset:46120\n\t" \
  "v_fma_f32 v201, |v201|, v235, v201 mul:4\n\t" \
  "v_fma_f32 v200, |v200|, v234, v200 mul:4\n\t" \
  "s_waitcnt lgkmcnt(8)\n\t" \
  "v_mfma_f32_16x16x32_bf16 v[20:23], v[216:219], v[146:149], v[20:23]\n\t" \
  "ds_read_b64_tr_b16 v[214:215], v248 offset:47144\n\t" \
  "v_fma_f32 v193, |v193|, v231, v193 mul:4\n\t" \
  "v_fma_f32 v192, |v192|, v230, v192 mul:4\n\t" \
  "v_mfma_f32_16x16x32_bf16 v[44:47], v[216:219], v[154:157], v[44:47]\n\t" \
  "ds_read_b32 v224, v224 offset:0\n\t" \
  "v_cvt_pk_bf16_f32 v195, v194, v195\n\t" \
  "v_cvt_pk_bf16_f32 v194, v192, v193\n\t" \
  "v_mfma_f32_16x16x32_bf16 v[68:71], v[216:219], v[162:165], v[68:71]\n\t" \
  "ds_read_b32 v227, v227 offset:15376\n\t" \
  "v_cvt_pk_bf16_f32 v203, v202, v203\n\t" \
  "v_cvt_pk_bf16_f32 v202, v200, v201\n\t" \
  "v_cvt_pk_f16_f32 v228, |v196|, |v197| clamp\n\t" \
  "v_mfma_f32_16x16x32_bf16 v[92:95], v[216:219], v[170:173], v[92:95]\n\t" \
  "ds_read_b64_tr_b16 v[216:217], v248 offset:45096\n\t" \
  "v_cvt_pknorm_i16_f32 v230, |v198|, |v199|\n\t" \
  "s_setprio 2\n\t" \
  "v_cvt_pk_f16_f32 v232, |v204|, |v205| clamp\n\t" \
  "s_waitcnt lgkmcnt(9)\n\t" \
  "v_mfma_f32_16x16x32_bf16 v[144:147], v[208:211], v[96:99], 0\n\t" \
  "ds_read_b64_tr_b16 v[218:219], v248 offset:48168\n\t" \
  "v_cvt_pk_f16_f32 v234, |v206|, |v207| clamp\n\t" \
  "v_and_b32_sdwa v229, s56, v228 dst_sel:DWORD dst_unused:UNUSED_PAD src0_sel:DWORD src1_sel:WORD_1\n\t" \
  "v_mfma_f32_16x16x32_bf16 v[152:155], v[208:211], v[100:103], 0\n\t" \
  "ds_read_b32 v226, v226 offset:15376\n\t" \
  "v_and_b32 v228, s56, v228\n\t" \
  "v_lshlrev_b32_sdwa v231, 2, v230 dst_sel:DWORD dst_unused:UNUSED_PAD src0_sel:DWORD src1_sel:BYTE_3\n\t" \
  "v_mfma_f32_16x16x32_bf16 v[160:163], v[208:211], v[104:107], 0\n\t" \
  "ds_read_b32 v229, v229 offset:0\n\t" \
  "v_lshlrev_b32_sdwa v230, 2, v230 dst_sel:DWORD dst_unused:UNUSED_PAD src0_sel:DWORD src1_sel:BYTE_1\n\t" \
  "v_and_b32_sdwa v233, s56, v232 dst_sel:DWORD dst_unused:UNUSED_PAD src0_sel:DWORD src1_sel:WORD_1\n\t" \
  "v_mfma_f32_16x16x32_bf16 v[168:171], v[208:211], v[108:111], 0\n\t" \
  "ds_read_b64_tr_b16 v[208:209], v248 offset:43560\n\t" \
  "v_and_b32 v232, s56, v232\n\t" \
  "v_and_b32_sdwa v235, s56, v234 dst_sel:DWORD dst_unused:UNUSED_PAD src0_sel:DWORD src1_sel:WORD_1\n\t" \
  "s_waitcnt lgkmcnt(7)\n\t" \
  "v_mfma_f32_16x16x32_bf16 v[144:147], v[212:215], v[112:115], v[144:147]\n\t" \
  "ds_read_b64_tr_b16 v[210:211], v248 offset:44584\n\t" \
  "ds_read_b32 v228, v228 offset:0\n\t" \
  "v_and_b32 v234, s56, v234\n\t" \
  "v_fma_f32 v181, |v181|, v221, v181 mul:4\n\t" \
  "v_mfma_f32_16x16x32_bf16 v[152:155], v[212:215], v[116:119], v[152:155]\n\t" \
  "ds_read_b32 v231, v231 offset:15376\n\t" \
  "ds_read_b32 v230, v230 offset:15376\n\t" \
  "v_fma_f32 v180, |v180|, v220, v180 mul:4\n\t" \
  "v_fma_mix_f32 v223, |v183|, v223, v223 op_sel:[0,1,0] op_sel_hi:[0,1,1] clamp\n\t" \
  "v_mfma_f32_16x16x32_bf16 v[160:163], v[212:215], v[120:123], v[160:163]\n\t" \
  "ds_read_b32 v233, v233 offset:0\n\t" \
  "v_fma_mix_f32 v222, |v182|, v222, v222 op_sel:[0,1,0] op_sel_hi:[0,1,1] clamp\n\t" \
  "s_waitcnt lgkmcnt(7)\n\t" \
  "v_fma_f32 v189, |v189|, v225, v189 mul:4\n\t" \
  "v_mfma_f32_16x16x32_bf16 v[168:171], v[212:215], v[124:127], v[168:171]\n\t" \
  "ds_read_b64_tr_b16 v[212:213], v248 offset:46632\n\t" \
  "v_fma_f32 v188, |v188|, v224, v188 mul:4\n\t" \
  "v_fma_mix_f32 v227, |v191|, v227, v227 op_sel:[0,1,0] op_sel_hi:[0,1,1] clamp\n\t" \
  "v_fma_mix_f32 v226, |v190|, v226, v226 op_sel:[0,1,0] op_sel_hi:[0,1,1] clamp\n\t" \
  "v_mfma_f32_16x16x32_bf16 v[144:147], v[216:219], v[128:131], v[144:147]\n\t" \
  "ds_read_b64_tr_b16 v[214:215], v248 offset:47656\n\t" \
  "v_fma_f32 v183, |v183|, v223, v183 mul:4\n\t" \
  "v_fma_f32 v182, |v182|, v222, v182 mul:4\n\t" \
  "v_mfma_f32_16x16x32_bf16 v[152:155], v[216:219], v[132:135], v[152:155]\n\t" \
  "ds_read_b32 v232, v232 offset:0\n\t" \
  "v_fma_f32 v191, |v191|, v227, v191 mul:4\n\t" \
  "v_fma_f32 v190, |v190|, v226, v190 mul:4\n\t" \
  "v_mfma_f32_16x16x32_bf16 v[160:163], v[216:219], v[136:139], v[160:163]\n\t" \
  "ds_read_b32 v235, v235 offset:0\n\t" \
  "v_cvt_pk_bf16_f32 v180, v180, v181\n\t" \
  "v_cvt_pk_bf16_f32 v181, v182, v183\n\t" \
  "v_mfma_f32_16x16x32_bf16 v[168:171], v[216:219], v[140:143], v[168:171]\n\t" \
  "ds_read_b64_tr_b16 v[216:217], v248 offset:45608\n\t" \
  "v_cvt_pk_bf16_f32 v188, v188, v189\n\t" \
  "v_cvt_pk_bf16_f32 v189, v190, v191\n\t" \
  "s_waitcnt lgkmcnt(9)\n\t" \
  "v_mfma_f32_16x16x32_bf16 v[148:151], v[208:211], v[96:99], 0\n\t" \
  "ds_read_b64_tr_b16 v[218:219], v248 offset:48680\n\t" \
  "v_cvt_pk_f16_f32 v220, |v146|, |v147| clamp\n\t" \
  "s_setprio 3\n\t" \
  "v_cvt_pknorm_i16_f32 v222, |v144|, |v145|\n\t" \
  "v_mfma_f32_16x16x32_bf16 v[156:159], v[208:211], v[100:103], 0\n\t" \
  "s_waitcnt vmcnt(1)\n\t" \
  "s_cmp_lg_u32 s58, 1\n\t" \
  "s_cbranch_scc1 .Lcnx_c12\n\t" \
  "s_waitcnt vmcnt(0)\n\t" \
  ".Lcnx_c12:\n\t" \
  "s_mov_b64 exec, 3\n\t" \
  "ds_add_rtn_u32 v246, v251, v250 offset:47176\n\t" \
  "s_mov_b64 exec, -1\n\t" \
  "ds_read_b32 v234, v234 offset:0\n\t" \
  "v_cvt_pk_f16_f32 v224, |v154|, |v155| clamp\n\t" \
  "v_cvt_pknorm_i16_f32 v226, |v152|, |v153|\n\t" \
  "v_mfma_f32_16x16x32_bf16 v[164:167], v[208:211], v[104:107], 0\n\t" \
  "v_and_b32_sdwa v221, s56, v220 dst_sel:DWORD dst_unused:UNUSED_PAD src0_sel:DWORD src1_sel:WORD_1\n\t" \
  "v_and_b32 v220, s56, v220\n\t" \
  "v_mfma_f32_16x16x32_bf16 v[172:175], v[208:211], v[108:111], 0\n\t" \
  "s_waitcnt lgkmcnt(1)\n\t" \
  "v_readfirstlane_b32 s4, v246\n\t" \
  "s_cmp_lt_u32 s4, s57\n\t" \
  "s_cbranch_scc1 .Lcnx_w48\n.Lcnx_b48:\n\t" \
  "ds_read_b64_tr_b16 v[208:209], v249 offset:6152\n\t" \
  "v_lshlrev_b32_sdwa v223, 2, v222 dst_sel:DWORD dst_unused:UNUSED_PAD src0_sel:DWORD src1_sel:BYTE_3\n\t" \
  "v_lshlrev_b32_sdwa v222, 2, v222 dst_sel:DWORD dst_unused:UNUSED_PAD src0_sel:DWORD src1_sel:BYTE_1\n\t" \
  "v_and_b32_sdwa v225, s56, v224 dst_sel:DWORD dst_unused:UNUSED_PAD src0_sel:DWORD src1_sel:WORD_1\n\t" \
  "v_mfma_f32_16x16x32_bf16 v[148:151], v[212:215], v[112:115], v[148:151]\n\t" \
  "ds_read_b64_tr_b16 v[210:211], v249 offset:7688\n\t" \
  "v_and_b32 v224, s56, v224\n\t" \
  "v_lshlrev_b32_sdwa v227, 2, v226 dst_sel:DWORD dst_unused:UNUSED_PAD src0_sel:DWORD src1_sel:BYTE_3\n\t" \
  "v_mfma_f32_16x16x32_bf16 v[156:159], v[212:215], v[116:119], v[156:159]\n\t" \
  "s_cmp_eq_u32 s37, 0\n\t" \
  "s_cbranch_scc1 .Lcnx_f5\n.Lcnx_g5:\n\t" \
  "ds_read_b32 v221, v221 offset:0\n\t" \
  "ds_read_b32 v220, v220 offset:0\n\t" \
  "v_lshlrev_b32_sdwa v226, 2, v226 dst_sel:DWORD dst_unused:UNUSED_PAD src0_sel:DWORD src1_sel:BYTE_1\n\t" \
  "v_fma_f32 v197, |v197|, v229, v197 mul:4\n\t" \
  "v_mfma_f32_16x16x32_bf16 v[164:167], v[212:215], v[120:123], v[164:167]\n\t" \
  "ds_read_b32 v223, v223 offset:15376\n\t" \
  "ds_read_b32 v222, v222 offset:15376\n\t" \
  "v_fma_f32 v196, |v196|, v228, v196 mul:4\n\t" \
  "v_fma_mix_f32 v231, |v199|, v231, v231 op_sel:[0,1,0] op_sel_hi:[0,1,1] clamp\n\t" \
  "v_mfma_f32_16x16x32_bf16 v[172:175], v[212:215], v[124:127], v[172:175]\n\t" \
  "ds_read_b64_tr_b16 v[212:213], v249 offset:6160\n\t" \
  "ds_read_b64_tr_b16 v[214:215], v249 offset:7696\n\t" \
  "v_fma_mix_f32 v230, |v198|, v230, v230 op_sel:[0,1,0] op_sel_hi:[0,1,1] clamp\n\t" \
  "s_waitcnt lgkmcnt(8)\n\t" \
  "v_fma_f32 v205, |v205|, v233, v205 mul:4\n\t" \
  "v_mfma_f32_16x16x32_bf16 v[148:151], v[216:219], v[128:131], v[148:151]\n\t" \
  "ds_read_b32 v225, v225 offset:0\n\t" \
  "v_fma_f32 v204, |v204|, v232, v204 mul:4\n\t" \
  "v_fma_f32 v207, |v207|, v235, v207 mul:4\n\t" \
  "v_mfma_f32_16x16x32_bf16 v[156:159], v[216:219], v[132:135], v[156:159]\n\t" \
  "ds_read_b32 v224, v224 offset:0\n\t" \
  "v_fma_f32 v206, |v206|, v234, v206 mul:4\n\t" \
  "v_fma_f32 v199, |v199|, v231, v199 mul:4\n\t" \
  "v_mfma_f32_16x16x32_bf16 v[164:167], v[216:219], v[136:139], v[164:167]\n\t" \
  "ds_read_b32 v227, v227 offset:15376\n\t" \
  "v_fma_f32 v198, |v198|, v230, v198 mul:4\n\t" \
  "v_cvt_pk_bf16_f32 v196, v196, v197\n\t" \
  "v_mfma_f32_16x16x32_bf16 v[172:175], v[216:219], v[140:143], v[172:175]\n\t" \
  "ds_read_b64_tr_b16 v[216:217], v249 offset:6216\n\t" \
  "v_cvt_pk_bf16_f32 v197, v198, v199\n\t" \
  "v_cvt_pk_bf16_f32 v204, v204, v205\n\t" \
  "v_cvt_pk_bf16_f32 v205, v206, v207\n\t" \
  "s_setprio 0\n\t" \
  "v_cvt_pk_f16_f32 v228, |v162|, |v163| clamp\n\t" \
  "v_cvt_pknorm_i16_f32 v230, |v160|, |v161|\n\t" \
  "v_mfma_f32_16x16x32_bf16 v[24:27], v[242:245], v[236:239], v[24:27]\n\t" \
  "ds_read_b64_tr_b16 v[218:219], v249 offset:7752\n\t" \
  "v_cvt_pk_f16_f32 v232, |v170|, |v171| clamp\n\t" \
  "v_cvt_pk_f16_f32 v234, |v168|, |v169| clamp\n\t" \
  "v_mfma_f32_16x16x32_bf16 v[28:31], v[240:243], v[236:239], v[28:31]\n\t" \
  "buffer_load_dwordx4 v[236:239], v250, s[8:11], s29 offen offset:64\n\t" \
  "s_cmp_lg_u32 s58, 0\n\t" \
  "s_cbranch_scc1 .Lcnx_c25\n\t" \
  "s_add_u32 m0, s25, 24600\n\t" \
  "s_add_u32 s24, s24, s26\n\t" \
  "buffer_load_dwordx4 v254, s[8:11], s24 offen lds\n\t" \
  "s_cmp_lg_u32 s38, 5\n\t" \
  "s_cbranch_scc1 .Lcnx_c25\n\t" \
  "s_mov_b32 m0, 163632\n\t" \
  "s_add_u32 s30, s30, s31\n\t" \
  "buffer_load_dword v252, s[8:11], s30 offen lds\n\t" \
  ".Lcnx_c25:\n\t" \
  "ds_read_b32 v226, v226 offset:15376\n\t" \
  "v_and_b32_sdwa v229, s56, v228 dst_sel:DWORD dst_unused:UNUSED_PAD src0_sel:DWORD src1_sel:WORD_1\n\t" \
  "v_and_b32 v228, s56, v228\n\t" \
  "s_waitcnt lgkmcnt(12)\n\t" \
  "v_mfma_f32_16x16x32_bf16 v[0:3], v[208:211], v[178:181], v[0:3]\n\t" \
  "ds_read_b32 v229, v229 offset:0\n\t" \
  "v_lshlrev_b32_sdwa v231, 2, v230 dst_sel:DWORD dst_unused:UNUSED_PAD src0_sel:DWORD src1_sel:BYTE_3\n\t" \
  "v_lshlrev_b32_sdwa v230, 2, v230 dst_sel:DWORD dst_unused:UNUSED_PAD src0_sel:DWORD src1_sel:BYTE_1\n\t" \
  "v_mfma_f32_16x16x32_bf16 v[24:27], v[208:211], v[186:189], v[24:27]\n\t" \
  "ds_read_b32 v228, v228 offset:0\n\t" \
  "v_and_b32_sdwa v233, s56, v232 dst_sel:DWORD dst_unused:UNUSED_PAD src0_sel:DWORD src1_sel:WORD_1\n\t" \
  "v_and_b32 v232, s56, v232\n\t" \
  "v_mfma_f32_16x16x32_bf16 v[48:51], v[208:211], v[194:197], v[48:51]\n\t" \
  "ds_read_b32 v231, v231 offset:15376\n\t" \
  "v_and_b32_sdwa v235, s56, v234 dst_sel:DWORD dst_unused:UNUSED_PAD src0_sel:DWORD src1_sel:WORD_1\n\t" \
  "v_and_b32 v234, s56, v234\n\t" \
  "v_mfma_f32_16x16x32_bf16 v[72:75], v[208:211], v[202:205], v[72:75]\n\t" \
  "ds_read_b64_tr_b16 v[208:209], v249 offset:6224\n\t" \
  "ds_read_b64_tr_b16 v[210:211], v249 offset:7760\n\t" \
  "s_waitcnt lgkmcnt(13)\n\t" \
  "v_fma_f32 v147, |v147|, v221, v147 mul:4\n\t" \
  "v_fma_f32 v146, |v146|, v220, v146 mul:4\n\t" \
  "s_waitcnt lgkmcnt(11)\n\t" \
  "v_mfma_f32_16x16x32_bf16 v[4:7], v[212:215], v[178:181], v[4:7]\n\t" \
  "ds_read_b32 v230, v230 offset:15376\n\t" \
  "v_fma_mix_f32 v223, |v145|, v223, v223 op_sel:[0,1,0] op_sel_hi:[0,1,1] clamp\n\t" \
  "v_fma_mix_f32 v222, |v144|, v222, v222 op_sel:[0,1,0] op_sel_hi:[0,1,1] clamp\n\t" \
  "v_mfma_f32_16x16x32_bf16 v[28:31], v[212:215], v[186:189], v[28:31]\n\t" \
  "ds_read_b32 v233, v233 offset:0\n\t" \
  "s_waitcnt lgkmcnt(7)\n\t" \
  "v_fma_f32 v155, |v155|, v225, v155 mul:4\n\t" \
  "v_fma_f32 v154, |v154|, v224, v154 mul:4\n\t" \
  "v_mfma_f32_16x16x32_bf16 v[52:55], v[212:215], v[194:197], v[52:55]\n\t" \
  "ds_read_b32 v232, v232 offset:0\n\t" \
  "v_fma_mix_f32 v227, |v153|, v227, v227 op_sel:[0,1,0] op_sel_hi:[0,1,1] clamp\n\t" \
  "v_fma_mix_f32 v226, |v152|, v226, v226 op_sel:[0,1,0] op_sel_hi:[0,1,1] clamp\n\t" \
  "v_fma_f32 v145, |v145|, v223, v145 mul:4\n\t" \
  "v_mfma_f32_16x16x32_bf16 v[76:79], v[212:215], v[202:205], v[76:79]\n\t" \
  "ds_read_b64_tr_b16 v[212:213], v249 offset:6280\n\t" \
  "v_fma_f32 v144, |v144|, v222, v144 mul:4\n\t" \
  "v_fma_f32 v153, |v153|, v227, v153 mul:4\n\t" \
  "v_mfma_f32_16x16x32_bf16 v[8:11], v[216:219], v[178:181], v[8:11]\n\t" \
  "ds_read_b64_tr_b16 v[214:215], v249 offset:7816\n\t" \
  "v_fma_f32 v152, |v152|, v226, v152 mul:4\n\t" \
  "v_cvt_pk_bf16_f32 v147, v146, v147\n\t" \
  "v_mfma_f32_16x16x32_bf16 v[32:35], v[216:219], v[186:189], v[32:35]\n\t" \
  "ds_read_b32 v235, v235 offset:0\n\t" \
  "v_cvt_pk_bf16_f32 v146, v144, v145\n\t" \
  "v_cvt_pk_bf16_f32 v155, v154, v155\n\t" \
  "v_mfma_f32_16x16x32_bf16 v[56:59], v[216:219], v[194:197], v[56:59]\n\t" \
  "ds_read_b32 v234, v234 offset:0\n\t" \
  "v_cvt_pk_bf16_f32 v154, v152, v153\n\t" \
  "s_setprio 1\n\t" \
  "v_cvt_pk_f16_f32 v220, |v148|, |v149| clamp\n\t" \
  "v_mfma_f32_16x16x32_bf16 v[80:83], v[216:219], v[202:205], v[80:83]\n\t" \
  "ds_read_b64_tr_b16 v[216:217], v249 offset:6288\n\t" \
  "v_cvt_pknorm_i16_f32 v222, |v150|, |v151|\n\t" \
  "v_cvt_pk_f16_f32 v224, |v156|, |v157| clamp\n\t" \
  "s_waitcnt lgkmcnt(8)\n\t" \
  "v_mfma_f32_16x16x32_bf16 v[12:15], v[208:211], v[178:181], v[12:15]\n\t" \
  "ds_read_b64_tr_b16 v[218:219], v249 offset:7824\n\t" \
  "v_cvt_pknorm_i16_f32 v226, |v158|, |v159|\n\t" \
  "v_and_b32_sdwa v221, s56, v220 dst_sel:DWORD dst_unused:UNUSED_PAD src0_sel:DWORD src1_sel:WORD_1\n\t" \
  "v_mfma_f32_16x16x32_bf16 v[36:39], v[208:211], v[186:189], v[36:39]\n\t" \
  "v_and_b32 v220, s56, v220\n\t" \
  "v_lshlrev_b32_sdwa v223, 2, v222 dst_sel:DWORD dst_unused:UNUSED_PAD src0_sel:DWORD src1_sel:BYTE_3\n\t" \
  "v_mfma_f32_16x16x32_bf16 v[60:63], v[208:211], v[194:197], v[60:63]\n\t" \
  "ds_read_b32 v221, v221 offset:0\n\t" \
  "v_lshlrev_b32_sdwa v222, 2, v222 dst_sel:DWORD dst_unused:UNUSED_PAD src0_sel:DWORD src1_sel:BYTE_1\n\t" \
  "v_and_b32_sdwa v225, s56, v224 dst_sel:DWORD dst_unused:UNUSED_PAD src0_sel:DWORD src1_sel:WORD_1\n\t" \
  "v_and_b32 v224, s56, v224\n\t" \
  "v_mfma_f32_16x16x32_bf16 v[84:87], v[208:211], v[202:205], v[84:87]\n\t" \
  "ds_read_b64_tr_b16 v[208:209], v248 offset:49192\n\t" \
  "ds_read_b64_tr_b16 v[210:211], v248 offset:50216\n\t" \
  "v_lshlrev_b32_sdwa v227, 2, v226 dst_sel:DWORD dst_unused:UNUSED_PAD src0_sel:DWORD src1_sel:BYTE_3\n\t" \
  "v_lshlrev_b32_sdwa v226, 2, v226 dst_sel:DWORD dst_unused:UNUSED_PAD src0_sel:DWORD src1_sel:BYTE_1\n\t" \
  "s_waitcnt lgkmcnt(7)\n\t" \
  "v_mfma_f32_16x16x32_bf16 v[16:19], v[212:215], v[178:181], v[16:19]\n\t" \
  "ds_read_b32 v220, v220 offset:0\n\t" \
  "ds_read_b32 v223, v223 offset:15376\n\t" \
  "v_fma_f32 v163, |v163|, v229, v163 mul:4\n\t" \
  "v_fma_f32 v162, |v162|, v228, v162 mul:4\n\t" \
  "v_mfma_f32_16x16x32_bf16 v[40:43], v[212:215], v[186:189], v[40:43]\n\t" \
  "ds_read_b32 v222, v222 offset:15376\n\t" \
  "v_fma_mix_f32 v231, |v161|, v231, v231 op_sel:[0,1,0] op_sel_hi:[0,1,1] clamp\n\t" \
  "v_fma_mix_f32 v230, |v160|, v230, v230 op_sel:[0,1,0] op_sel_hi:[0,1,1] clamp\n\t" \
  "v_mfma_f32_16x16x32_bf16 v[64:67], v[212:215], v[194:197], v[64:67]\n\t" \
  "ds_read_b32 v225, v225 offset:0\n\t" \
  "s_waitcnt lgkmcnt(9)\n\t" \
  "v_fma_f32 v171, |v171|, v233, v171 mul:4\n\t" \
  "v_fma_f32 v170, |v170|, v232, v170 mul:4\n\t" \
  "v_mfma_f32_16x16x32_bf16 v[88:91], v[212:215], v[202:205], v[88:91]\n\t" \
  "ds_read_b64_tr_b16 v[212:213], v248 offset:52264\n\t" \
  "v_fma_f32 v169, |v169|, v235, v169 mul:4\n\t" \
  "v_fma_f32 v168, |v168|, v234, v168 mul:4\n\t" \
  "s_waitcnt lgkmcnt(8)\n\t" \
  "v_mfma_f32_16x16x32_bf16 v[20:23], v[216:219], v[178:181], v[20:23]\n\t" \
  "ds_read_b64_tr_b16 v[214:215], v248 offset:53288\n\t" \
  "v_fma_f32 v161, |v161|, v231, v161 mul:4\n\t" \
  "v_fma_f32 v160, |v160|, v230, v160 mul:4\n\t" \
  "v_mfma_f32_16x16x32_bf16 v[44:47], v[216:219], v[186:189], v[44:47]\n\t" \
  "ds_read_b32 v224, v224 offset:0\n\t" \
  "v_cvt_pk_bf16_f32 v163, v162, v163\n\t" \
  "v_cvt_pk_bf16_f32 v162, v160, v161\n\t" \
  "v_mfma_f32_16x16x32_bf16 v[68:71], v[216:219], v[194:197], v[68:71]\n\t" \
  "ds_read_b32 v227, v227 offset:15376\n\t" \
  "v_cvt_pk_bf16_f32 v171, v170, v171\n\t" \
  "v_cvt_pk_bf16_f32 v170, v168, v169\n\t" \
  "v_cvt_pk_f16_f32 v228, |v164|, |v165| clamp\n\t" \
  "v_mfma_f32_16x16x32_bf16 v[92:95], v[216:219], v[202:205], v[92:95]\n\t" \
  "ds_read_b64_tr_b16 v[216:217], v248 offset:51240\n\t" \
  "v_cvt_pknorm_i16_f32 v230, |v166|, |v167|\n\t" \
  "s_setprio 2\n\t" \
  "v_cvt_pk_f16_f32 v232, |v172|, |v173| clamp\n\t" \
  "s_waitcnt lgkmcnt(9)\n\t" \
  "v_mfma_f32_16x16x32_bf16 v[176:179], v[208:211], v[96:99], 0\n\t" \
  "ds_read_b64_tr_b16 v[218:219], v248 offset:54312\n\t" \
  "v_cvt_pk_f16_f32 v234, |v174|, |v175| clamp\n\t" \
  "v_and_b32_sdwa v229, s56, v228 dst_sel:DWORD dst_unused:UNUSED_PAD src0_sel:DWORD src1_sel:WORD_1\n\t" \
  "v_mfma_f32_16x16x32_bf16 v[184:187], v[208:211], v[100:103], 0\n\t" \
  "ds_read_b32 v226, v226 offset:15376\n\t" \
  "v_and_b32 v228, s56, v228\n\t" \
  "v_lshlrev_b32_sdwa v231, 2, v230 dst_sel:DWORD dst_unused:UNUSED_PAD src0_sel:DWORD src1_sel:BYTE_3\n\t" \
  "v_mfma_f32_16x16x32_bf16 v[192:195], v[208:211], v[104:107], 0\n\t" \
  "ds_read_b32 v229, v229 offset:0\n\t" \
  "v_lshlrev_b32_sdwa v230, 2, v230 dst_sel:DWORD dst_unused:UNUSED_PAD src0_sel:DWORD src1_sel:BYTE_1\n\t" \
  "v_and_b32_sdwa v233, s56, v232 dst_sel:DWORD dst_unused:UNUSED_PAD src0_sel:DWORD src1_sel:WORD_1\n\t" \
  "v_mfma_f32_16x16x32_bf16 v[200:203], v[208:211], v[108:111], 0\n\t" \
  "ds_read_b64_tr_b16 v[208:209], v248 offset:49704\n\t" \
  "v_and_b32 v232, s56, v232\n\t" \
  "v_and_b32_sdwa v235, s56, v234 dst_sel:DWORD dst_unused:UNUSED_PAD src0_sel:DWORD src1_sel:WORD_1\n\t" \
  "s_waitcnt lgkmcnt(7)\n\t" \
  "v_mfma_f32_16x16x32_bf16 v[176:179], v[212:215], v[112:115], v[176:179]\n\t" \
  "ds_read_b64_tr_b16 v[210:211], v248 offset:50728\n\t" \
  "ds_read_b32 v228, v228 offset:0\n\t" \
  "v_and_b32 v234, s56, v234\n\t" \
  "v_fma_f32 v149, |v149|, v221, v149 mul:4\n\t" \
  "v_mfma_f32_16x16x32_bf16 v[184:187], v[212:215], v[116:119], v[184:187]\n\t" \
  "ds_read_b32 v231, v231 offset:15376\n\t" \
  "ds_read_b32 v230, v230 offset:15376\n\t" \
  "v_fma_f32 v148, |v148|, v220, v148 mul:4\n\t" \
  "v_fma_mix_f32 v223, |v151|, v223, v223 op_sel:[0,1,0] op_sel_hi:[0,1,1] clamp\n\t" \
  "v_mfma_f32_16x16x32_bf16 v[192:195], v[212:215], v[120:123], v[192:195]\n\t" \
  "ds_read_b32 v233, v233 offset:0\n\t" \
  "v_fma_mix_f32 v222, |v150|, v222, v222 op_sel:[0,1,0] op_sel_hi:[0,1,1] clamp\n\t" \
  "s_waitcnt lgkmcnt(7)\n\t" \
  "v_fma_f32 v157, |v157|, v225, v157 mul:4\n\t" \
  "v_mfma_f32_16x16x32_bf16 v[200:203], v[212:215], v[124:127], v[200:203]\n\t" \
  "ds_read_b64_tr_b16 v[212:213], v248 offset:52776\n\t" \
  "v_fma_f32 v156, |v156|, v224, v156 mul:4\n\t" \
  "v_fma_mix_f32 v227, |v159|, v227, v227 op_sel:[0,1,0] op_sel_hi:[0,1,1] clamp\n\t" \
  "v_fma_mix_f32 v226, |v158|, v226, v226 op_sel:[0,1,0] op_sel_hi:[0,1,1] clamp\n\t" \
  "v_mfma_f32_16x16x32_bf16 v[176:179], v[216:219], v[128:131], v[176:179]\n\t" \
  "ds_read_b64_tr_b16 v[214:215], v248 offset:53800\n\t" \
  "v_fma_f32 v151, |v151|, v223, v151 mul:4\n\t" \
  "v_fma_f32 v150, |v150|, v222, v150 mul:4\n\t" \
  "v_mfma_f32_16x16x32_bf16 v[184:187], v[216:219], v[132:135], v[184:187]\n\t" \
  "ds_read_b32 v232, v232 offset:0\n\t" \
  "v_fma_f32 v159, |v159|, v227, v159 mul:4\n\t" \
  "v_fma_f32 v158, |v158|, v226, v158 mul:4\n\t" \
  "v_mfma_f32_16x16x32_bf16 v[192:195], v[216:219], v[136:139], v[192:195]\n\t" \
  "ds_read_b32 v235, v235 offset:0\n\t" \
  "v_cvt_pk_bf16_f32 v148, v148, v149\n\t" \
  "v_cvt_pk_bf16_f32 v149, v150, v151\n\t" \
  "v_mfma_f32_16x16x32_bf16 v[200:203], v[216:219], v[140:143], v[200:203]\n\t" \
  "ds_read_b64_tr_b16 v[216:217], v248 offset:51752\n\t" \
  "v_cvt_pk_bf16_f32 v156, v156, v157\n\t" \
  "v_cvt_pk_bf16_f32 v157, v158, v159\n\t" \
  "s_waitcnt lgkmcnt(9)\n\t" \
  "v_mfma_f32_16x16x32_bf16 v[180:183], v[208:211], v[96:99], 0\n\t" \
  "ds_read_b64_tr_b16 v[218:219], v248 offset:54824\n\t" \
  "v_cvt_pk_f16_f32 v220, |v178|, |v179| clamp\n\t" \
  "s_setprio 3\n\t" \
  "v_cvt_pknorm_i16_f32 v222, |v176|, |v177|\n\t" \
  "v_mfma_f32_16x16x32_bf16 v[188:191], v[208:211], v[100:103], 0\n\t" \
  "s_waitcnt vmcnt(1)\n\t" \
  "s_cmp_lg_u32 s58, 1\n\t" \
  "s_cbranch_scc1 .Lcnx_c13\n\t" \
  "s_waitcnt vmcnt(0)\n\t" \
  ".Lcnx_c13:\n\t" \
  "s_mov_b64 exec, 3\n\t" \
  "ds_add_rtn_u32 v246, v251, v250 offset:47184\n\t" \
  "s_mov_b64 exec, -1\n\t" \
  "ds_read_b32 v234, v234 offset:0\n\t" \
  "v_cvt_pk_f16_f32 v224, |v186|, |v187| clamp\n\t" \
  "v_cvt_pknorm_i16_f32 v226, |v184|, |v185|\n\t" \
  "v_mfma_f32_16x16x32_bf16 v[196:199], v[208:211], v[104:107], 0\n\t" \
  "v_and_b32_sdwa v221, s56, v220 dst_sel:DWORD dst_unused:UNUSED_PAD src0_sel:DWORD src1_sel:WORD_1\n\t" \
  "v_and_b32 v220, s56, v220\n\t" \
  "v_mfma_f32_16x16x32_bf16 v[204:207], v[208:211], v[108:111], 0\n\t" \
  "s_waitcnt lgkmcnt(1)\n\t" \
  "v_readfirstlane_b32 s4, v246\n\t" \
  "s_cmp_lt_u32 s4, s57\n\t" \
  "s_cbranch_scc1 .Lcnx_w60\n.Lcnx_b60:\n\t" \
  "ds_read_b64_tr_b16 v[208:209], v249 offset:24600\n\t" \
  "v_lshlrev_b32_sdwa v223, 2, v222 dst_sel:DWORD dst_unused:UNUSED_PAD src0_sel:DWORD src1_sel:BYTE_3\n\t" \
  "v_lshlrev_b32_sdwa v222, 2, v222 dst_sel:DWORD dst_unused:UNUSED_PAD src0_sel:DWORD src1_sel:BYTE_1\n\t" \
  "v_and_b32_sdwa v225, s56, v224 dst_sel:DWORD dst_unused:UNUSED_PAD src0_sel:DWORD src1_sel:WORD_1\n\t" \
  "v_mfma_f32_16x16x32_bf16 v[180:183], v[212:215], v[112:115], v[180:183]\n\t" \
  "ds_read_b64_tr_b16 v[210:211], v249 offset:26136\n\t" \
  "v_and_b32 v224, s56, v224\n\t" \
  "v_lshlrev_b32_sdwa v227, 2, v226 dst_sel:DWORD dst_unused:UNUSED_PAD src0_sel:DWORD src1_sel:BYTE_3\n\t" \
  "v_mfma_f32_16x16x32_bf16 v[188:191], v[212:215], v[116:119], v[188:191]\n\t" \
  "s_cmp_eq_u32 s37, 0\n\t" \
  "s_cbranch_scc1 .Lcnx_f6\n.Lcnx_g6:\n\t" \
  "ds_read_b32 v221, v221 offset:0\n\t" \
  "ds_read_b32 v220, v220 offset:0\n\t" \
  "v_lshlrev_b32_sdwa v226, 2, v226 dst_sel:DWORD dst_unused:UNUSED_PAD src0_sel:DWORD src1_sel:BYTE_1\n\t" \
  "v_fma_f32 v165, |v165|, v229, v165 mul:4\n\t" \
  "v_mfma_f32_16x16x32_bf16 v[196:199], v[212:215], v[120:123], v[196:199]\n\t" \
  "ds_read_b32 v223, v223 offset:15376\n\t" \
  "ds_read_b32 v222, v222 offset:15376\n\t" \
  "v_fma_f32 v164, |v164|, v228, v164 mul:4\n\t" \
  "v_fma_mix_f32 v231, |v167|, v231, v231 op_sel:[0,1,0] op_sel_hi:[0,1,1] clamp\n\t" \
  "v_mfma_f32_16x16x32_bf16 v[204:207], v[212:215], v[124:127], v[204:207]\n\t" \
  "ds_read_b64_tr_b16 v[212:213], v249 offset:24608\n\t" \
  "ds_read_b64_tr_b16 v[214:215], v249 offset:26144\n\t" \
  "v_fma_mix_f32 v230, |v166|, v230, v230 op_sel:[0,1,0] op_sel_hi:[0,1,1] clamp\n\t" \
  "s_waitcnt lgkmcnt(8)\n\t" \
  "v_fma_f32 v173, |v173|, v233, v173 mul:4\n\t" \
  "v_mfma_f32_16x16x32_bf16 v[180:183], v[216:219], v[128:131], v[180:183]\n\t" \
  "ds_read_b32 v225, v225 offset:0\n\t" \
  "v_fma_f32 v172, |v172|, v232, v172 mul:4\n\t" \
  "v_fma_f32 v175, |v175|, v235, v175 mul:4\n\t" \
  "v_mfma_f32_16x16x32_bf16 v[188:191], v[216:219], v[132:135], v[188:191]\n\t" \
  "ds_read_b32 v224, v224 offset:0\n\t" \
  "v_fma_f32 v174, |v174|, v234, v174 mul:4\n\t" \
  "v_fma_f32 v167, |v167|, v231, v167 mul:4\n\t" \
  "v_mfma_f32_16x16x32_bf16 v[196:199], v[216:219], v[136:139], v[196:199]\n\t" \
  "ds_read_b32 v227, v227 offset:15376\n\t" \
  "v_fma_f32 v166, |v166|, v230, v166 mul:4\n\t" \
  "v_cvt_pk_bf16_f32 v164, v164, v165\n\t" \
  "v_mfma_f32_16x16x32_bf16 v[204:207], v[216:219], v[140:143], v[204:207]\n\t" \
  "ds_read_b64_tr_b16 v[216:217], v249 offset:24664\n\t" \
  "v_cvt_pk_bf16_f32 v165, v166, v167\n\t" \
  "v_cvt_pk_bf16_f32 v172, v172, v173\n\t" \
  "v_cvt_pk_bf16_f32 v173, v174, v175\n\t" \
  "s_setprio 0\n\t" \
  "v_cvt_pk_f16_f32 v228, |v194|, |v195| clamp\n\t" \
  "v_cvt_pknorm_i16_f32 v230, |v192|, |v193|\n\t" \
  "v_mfma_f32_16x16x32_bf16 v[32:35], v[242:245], v[236:239], v[32:35]\n\t" \
  "ds_read_b64_tr_b16 v[218:219], v249 offset:26200\n\t" \
  "v_cvt_pk_f16_f32 v232, |v202|, |v203| clamp\n\t" \
  "v_cvt_pk_f16_f32 v234, |v200|, |v201| clamp\n\t" \
  "v_mfma_f32_16x16x32_bf16 v[36:39], v[240:243], v[236:239], v[36:39]\n\t" \
  "buffer_load_dwordx4 v[236:239], v250, s[8:11], s29 offen offset:128\n\t" \
  "s_cmp_lg_u32 s58, 0\n\t" \
  "s_cbranch_scc1 .Lcnx_c26\n\t" \
  "s_add_u32 m0, s25, 32800\n\t" \
  "s_add_u32 s24, s24, s26\n\t" \
  "buffer_load_dwordx4 v254, s[8:11], s24 offen lds\n\t" \
  "s_cmp_lg_u32 s38, 6\n\t" \
  "s_cbranch_scc1 .Lcnx_c26\n\t" \
  "s_mov_b32 m0, 163632\n\t" \
  "s_add_u32 s30, s30, s31\n\t" \
  "buffer_load_dword v252, s[8:11], s30 offen lds\n\t" \
  ".Lcnx_c26:\n\t" \
  "ds_read_b32 v226, v226 offset:15376\n\t" \
  "v_and_b32_sdwa v229, s56, v228 dst_sel:DWORD dst_unused:UNUSED_PAD src0_sel:DWORD src1_sel:WORD_1\n\t" \
  "v_and_b32 v228, s56, v228\n\t" \
  "s_waitcnt lgkmcnt(12)\n\t" \
  "v_mfma_f32_16x16x32_bf16 v[0:3], v[208:211], v[146:149], v[0:3]\n\t" \
  "ds_read_b32 v229, v229 offset:0\n\t" \
  "v_lshlrev_b32_sdwa v231, 2, v230 dst_sel:DWORD dst_unused:UNUSED_PAD src0_sel:DWORD src1_sel:BYTE_3\n\t" \
  "v_lshlrev_b32_sdwa v230, 2, v230 dst_sel:DWORD dst_unused:UNUSED_PAD src0_sel:DWORD src1_sel:BYTE_1\n\t" \
  "v_mfma_f32_16x16x32_bf16 v[24:27], v[208:211], v[154:157], v[24:27]\n\t" \
  "ds_read_b32 v228, v228 offset:0\n\t" \
  "v_and_b32_sdwa v233, s56, v232 dst_sel:DWORD dst_unused:UNUSED_PAD src0_sel:DWORD src1_sel:WORD_1\n\t" \
  "v_and_b32 v232, s56, v232\n\t" \
  "v_mfma_f32_16x16x32_bf16 v[48:51], v[208:211], v[162:165], v[48:51]\n\t" \
  "ds_read_b32 v231, v231 offset:15376\n\t" \
  "v_and_b32_sdwa v235, s56, v234 dst_sel:DWORD dst_unused:UNUSED_PAD src0_sel:DWORD src1_sel:WORD_1\n\t" \
  "v_and_b32 v234, s56, v234\n\t" \
  "v_mfma_f32_16x16x32_bf16 v[72:75], v[208:211], v[170:173], v[72:75]\n\t" \
  "ds_read_b64_tr_b16 v[208:209], v249 offset:24672\n\t" \
  "ds_read_b64_tr_b16 v[210:211], v249 offset:26208\n\t" \
  "s_waitcnt lgkmcnt(13)\n\t" \
  "v_fma_f32 v179, |v179|, v221, v179 mul:4\n\t" \
  "v_fma_f32 v178, |v178|, v220, v178 mul:4\n\t" \
  "s_waitcnt lgkmcnt(11)\n\t" \
  "v_mfma_f32_16x16x32_bf16 v[4:7], v[212:215], v[146:149], v[4:7]\n\t" \
  "ds_read_b32 v230, v230 offset:15376\n\t" \
  "v_fma_mix_f32 v223, |v177|, v223, v223 op_sel:[0,1,0] op_sel_hi:[0,1,1] clamp\n\t" \
  "v_fma_mix_f32 v222, |v176|, v222, v222 op_sel:[0,1,0] op_sel_hi:[0,1,1] clamp\n\t" \
  "v_mfma_f32_16x16x32_bf16 v[28:31], v[212:215], v[154:157], v[28:31]\n\t" \
  "ds_read_b32 v233, v233 offset:0\n\t" \
  "s_waitcnt lgkmcnt(7)\n\t" \
  "v_fma_f32 v187, |v187|, v225, v187 mul:4\n\t" \
  "v_fma_f32 v186, |v186|, v224, v186 mul:4\n\t" \
  "v_mfma_f32_16x16x32_bf16 v[52:55], v[212:215], v[162:165], v[52:55]\n\t" \
  "ds_read_b32 v232, v232 offset:0\n\t" \
  "v_fma_mix_f32 v227, |v185|, v227, v227 op_sel:[0,1,0] op_sel_hi:[0,1,1] clamp\n\t" \
  "v_fma_mix_f32 v226, |v184|, v226, v226 op_sel:[0,1,0] op_sel_hi:[0,1,1] clamp\n\t" \
  "v_fma_f32 v177, |v177|, v223, v177 mul:4\n\t" \
  "v_mfma_f32_16x16x32_bf16 v[76:79], v[212:215], v[170:173], v[76:79]\n\t" \
  "ds_read_b64_tr_b16 v[212:213], v249 offset:24728\n\t" \
  "v_fma_f32 v176, |v176|, v222, v176 mul:4\n\t" \
  "v_fma_f32 v185, |v185|, v227, v185 mul:4\n\t" \
  "v_mfma_f32_16x16x32_bf16 v[8:11], v[216:219], v[146:149], v[8:11]\n\t" \
  "ds_read_b64_tr_b16 v[214:215], v249 offset:26264\n\t" \
  "v_fma_f32 v184, |v184|, v226, v184 mul:4\n\t" \
  "v_cvt_pk_bf16_f32 v179, v178, v179\n\t" \
  "v_mfma_f32_16x16x32_bf16 v[32:35], v[216:219], v[154:157], v[32:35]\n\t" \
  "ds_read_b32 v235, v235 offset:0\n\t" \
  "v_cvt_pk_bf16_f32 v178, v176, v177\n\t" \
  "v_cvt_pk_bf16_f32 v187, v186, v187\n\t" \
  "v_mfma_f32_16x16x32_bf16 v[56:59], v[216:219], v[162:165], v[56:59]\n\t" \
  "ds_read_b32 v234, v234 offset:0\n\t" \
  "v_cvt_pk_bf16_f32 v186, v184, v185\n\t" \
  "s_setprio 1\n\t" \
  "v_cvt_pk_f16_f32 v220, |v180|, |v181| clamp\n\t" \
  "v_mfma_f32_16x16x32_bf16 v[80:83], v[216:219], v[170:173], v[80:83]\n\t" \
  "ds_read_b64_tr_b16 v[216:217], v249 offset:24736\n\t" \
  "v_cvt_pknorm_i16_f32 v222, |v182|, |v183|\n\t" \
  "v_cvt_pk_f16_f32 v224, |v188|, |v189| clamp\n\t" \
  "s_waitcnt lgkmcnt(8)\n\t" \
  "v_mfma_f32_16x16x32_bf16 v[12:15], v[208:211], v[146:149], v[12:15]\n\t" \
  "ds_read_b64_tr_b16 v[218:219], v249 offset:26272\n\t" \
  "v_cvt_pknorm_i16_f32 v226, |v190|, |v191|\n\t" \
  "v_and_b32_sdwa v221, s56, v220 dst_sel:DWORD dst_unused:UNUSED_PAD src0_sel:DWORD src1_sel:WORD_1\n\t" \
  "v_mfma_f32_16x16x32_bf16 v[36:39], v[208:211], v[154:157], v[36:39]\n\t" \
  "v_and_b32 v220, s56, v220\n\t" \
  "v_lshlrev_b32_sdwa v223, 2, v222 dst_sel:DWORD dst_unused:UNUSED_PAD src0_sel:DWORD src1_sel:BYTE_3\n\t" \
  "v_mfma_f32_16x16x32_bf16 v[60:63], v[208:211], v[162:165], v[60:63]\n\t" \
  "ds_read_b32 v221, v221 offset:0\n\t" \
  "v_lshlrev_b32_sdwa v222, 2, v222 dst_sel:DWORD dst_unused:UNUSED_PAD src0_sel:DWORD src1_sel:BYTE_1\n\t" \
  "v_and_b32_sdwa v225, s56, v224 dst_sel:DWORD dst_unused:UNUSED_PAD src0_sel:DWORD src1_sel:WORD_1\n\t" \
  "v_and_b32 v224, s56, v224\n\t" \
  "v_mfma_f32_16x16x32_bf16 v[84:87], v[208:211], v[170:173], v[84:87]\n\t" \
  "ds_read_b64_tr_b16 v[208:209], v248 offset:36904\n\t" \
  "ds_read_b64_tr_b16 v[210:211], v248 offset:37928\n\t" \
  "v_lshlrev_b32_sdwa v227, 2, v226 dst_sel:DWORD dst_unused:UNUSED_PAD src0_sel:DWORD src1_sel:BYTE_3\n\t" \
  "v_lshlrev_b32_sdwa v226, 2, v226 dst_sel:DWORD dst_unused:UNUSED_PAD src0_sel:DWORD src1_sel:BYTE_1\n\t" \
  "s_waitcnt lgkmcnt(7)\n\t" \
  "v_mfma_f32_16x16x32_bf16 v[16:19], v[212:215], v[146:149], v[16:19]\n\t" \
  "ds_read_b32 v220, v220 offset:0\n\t" \
  "ds_read_b32 v223, v223 offset:15376\n\t" \
  "v_fma_f32 v195, |v195|, v229, v195 mul:4\n\t" \
  "v_fma_f32 v194, |v194|, v228, v194 mul:4\n\t" \
  "v_mfma_f32_16x16x32_bf16 v[40:43], v[212:215], v[154:157], v[40:43]\n\t" \
  "ds_read_b32 v222, v222 offset:15376\n\t" \
  "v_fma_mix_f32 v231, |v193|, v231, v231 op_sel:[0,1,0] op_sel_hi:[0,1,1] clamp\n\t" \
  "v_fma_mix_f32 v230, |v192|, v230, v230 op_sel:[0,1,0] op_sel_hi:[0,1,1] clamp\n\t" \
  "v_mfma_f32_16x16x32_bf16 v[64:67], v[212:215], v[162:165], v[64:67]\n\t" \
  "ds_read_b32 v225, v225 offset:0\n\t" \
  "s_waitcnt lgkmcnt(9)\n\t" \
  "v_fma_f32 v203, |v203|, v233, v203 mul:4\n\t" \
  "v_fma_f32 v202, |v202|, v232, v202 mul:4\n\t" \
  "v_mfma_f32_16x16x32_bf16 v[88:91], v[212:215], v[170:173], v[88:91]\n\t" \
  "ds_read_b64_tr_b16 v[212:213], v248 offset:39976\n\t" \
  "v_fma_f32 v201, |v201|, v235, v201 mul:4\n\t" \
  "v_fma_f32 v200, |v200|, v234, v200 mul:4\n\t" \
  "s_waitcnt lgkmcnt(8)\n\t" \
  "v_mfma_f32_16x16x32_bf16 v[20:23], v[216:219], v[146:149], v[20:23]\n\t" \
  "ds_read_b64_tr_b16 v[214:215], v248 offset:41000\n\t" \
  "v_fma_f32 v193, |v193|, v231, v193 mul:4\n\t" \
  "v_fma_f32 v192, |v192|, v230, v192 mul:4\n\t" \
  "v_mfma_f32_16x16x32_bf16 v[44:47], v[216:219], v[154:157], v[44:47]\n\t" \
  "ds_read_b32 v224, v224 offset:0\n\t" \
  "v_cvt_pk_bf16_f32 v195, v194, v195\n\t" \
  "v_cvt_pk_bf16_f32 v194, v192, v193\n\t" \
  "v_mfma_f32_16x16x32_bf16 v[68:71], v[216:219], v[162:165], v[68:71]\n\t" \
  "ds_read_b32 v227, v227 offset:15376\n\t" \
  "v_cvt_pk_bf16_f32 v203, v202, v203\n\t" \
  "v_cvt_pk_bf16_f32 v202, v200, v201\n\t" \
  "v_cvt_pk_f16_f32 v228, |v196|, |v197| clamp\n\t" \
  "v_mfma_f32_16x16x32_bf16 v[92:95], v[216:219], v[170:173], v[92:95]\n\t" \
  "ds_read_b64_tr_b16 v[216:217], v248 offset:38952\n\t" \
  "v_cvt_pknorm_i16_f32 v230, |v198|, |v199|\n\t" \
  "s_setprio 2\n\t" \
  "v_cvt_pk_f16_f32 v232, |v204|, |v205| clamp\n\t" \
  "s_waitcnt lgkmcnt(9)\n\t" \
  "v_mfma_f32_16x16x32_bf16 v[144:147], v[208:211], v[96:99], 0\n\t" \
  "ds_read_b64_tr_b16 v[218:219], v248 offset:42024\n\t" \
  "v_cvt_pk_f16_f32 v234, |v206|, |v207| clamp\n\t" \
  "v_and_b32_sdwa v229, s56, v228 dst_sel:DWORD dst_unused:UNUSED_PAD src0_sel:DWORD src1_sel:WORD_1\n\t" \
  "v_mfma_f32_16x16x32_bf16 v[152:155], v[208:211], v[100:103], 0\n\t" \
  "ds_read_b32 v226, v226 offset:15376\n\t" \
  "v_and_b32 v228, s56, v228\n\t" \
  "v_lshlrev_b32_sdwa v231, 2, v230 dst_sel:DWORD dst_unused:UNUSED_PAD src0_sel:DWORD src1_sel:BYTE_3\n\t" \
  "v_mfma_f32_16x16x32_bf16 v[160:163], v[208:211], v[104:107], 0\n\t" \
  "ds_read_b32 v229, v229 offset:0\n\t" \
  "v_lshlrev_b32_sdwa v230, 2, v230 dst_sel:DWORD dst_unused:UNUSED_PAD src0_sel:DWORD src1_sel:BYTE_1\n\t" \
  "v_and_b32_sdwa v233, s56, v232 dst_sel:DWORD dst_unused:UNUSED_PAD src0_sel:DWORD src1_sel:WORD_1\n\t" \
  "v_mfma_f32_16x16x32_bf16 v[168:171], v[208:211], v[108:111], 0\n\t" \
  "ds_read_b64_tr_b16 v[208:209], v248 offset:37416\n\t" \
  "v_and_b32 v232, s56, v232\n\t" \
  "v_and_b32_sdwa v235, s56, v234 dst_sel:DWORD dst_unused:UNUSED_PAD src0_sel:DWORD src1_sel:WORD_1\n\t" \
  "s_waitcnt lgkmcnt(7)\n\t" \
  "v_mfma_f32_16x16x32_bf16 v[144:147], v[212:215], v[112:115], v[144:147]\n\t" \
  "ds_read_b64_tr_b16 v[210:211], v248 offset:38440\n\t" \
  "ds_read_b32 v228, v228 offset:0\n\t" \
  "v_and_b32 v234, s56, v234\n\t" \
  "v_fma_f32 v181, |v181|, v221, v181 mul:4\n\t" \
  "v_mfma_f32_16x16x32_bf16 v[152:155], v[212:215], v[116:119], v[152:155]\n\t" \
  "ds_read_b32 v231, v231 offset:15376\n\t" \
  "ds_read_b32 v230, v230 offset:15376\n\t" \
  "v_fma_f32 v180, |v180|, v220, v180 mul:4\n\t" \
  "v_fma_mix_f32 v223, |v183|, v223, v223 op_sel:[0,1,0] op_sel_hi:[0,1,1] clamp\n\t" \
  "v_mfma_f32_16x16x32_bf16 v[160:163], v[212:215], v[120:123], v[160:163]\n\t" \
  "ds_read_b32 v233, v233 offset:0\n\t" \
  "v_fma_mix_f32 v222, |v182|, v222, v222 op_sel:[0,1,0] op_sel_hi:[0,1,1] clamp\n\t" \
  "s_waitcnt lgkmcnt(7)\n\t" \
  "v_fma_f32 v189, |v189|, v225, v189 mul:4\n\t" \
  "v_mfma_f32_16x16x32_bf16 v[168:171], v[212:215], v[124:127], v[168:171]\n\t" \
  "ds_read_b64_tr_b16 v[212:213], v248 offset:40488\n\t" \
  "v_fma_f32 v188, |v188|, v224, v188 mul:4\n\t" \
  "v_fma_mix_f32 v227, |v191|, v227, v227 op_sel:[0,1,0] op_sel_hi:[0,1,1] clamp\n\t" \
  "v_fma_mix_f32 v226, |v190|, v226, v226 op_sel:[0,1,0] op_sel_hi:[0,1,1] clamp\n\t" \
  "v_mfma_f32_16x16x32_bf16 v[144:147], v[216:219], v[128:131], v[144:147]\n\t" \
  "ds_read_b64_tr_b16 v[214:215], v248 offset:41512\n\t" \
  "v_fma_f32 v183, |v183|, v223, v183 mul:4\n\t" \
  "v_fma_f32 v182, |v182|, v222, v182 mul:4\n\t" \
  "v_mfma_f32_16x16x32_bf16 v[152:155], v[216:219], v[132:135], v[152:155]\n\t" \
  "ds_read_b32 v232, v232 offset:0\n\t" \
  "v_fma_f32 v191, |v191|, v227, v191 mul:4\n\t" \
  "v_fma_f32 v190, |v190|, v226, v190 mul:4\n\t" \
  "v_mfma_f32_16x16x32_bf16 v[160:163], v[216:219], v[136:139], v[160:163]\n\t" \
  "ds_read_b32 v235, v235 offset:0\n\t" \
  "v_cvt_pk_bf16_f32 v180, v180, v181\n\t" \
  "v_cvt_pk_bf16_f32 v181, v182, v183\n\t" \
  "v_mfma_f32_16x16x32_bf16 v[168:171], v[216:219], v[140:143], v[168:171]\n\t" \
  "ds_read_b64_tr_b16 v[216:217], v248 offset:39464\n\t" \
  "v_cvt_pk_bf16_f32 v188, v188, v189\n\t" \
  "v_cvt_pk_bf16_f32 v189, v190, v191\n\t" \
  "s_waitcnt lgkmcnt(9)\n\t" \
  "v_mfma_f32_16x16x32_bf16 v[148:151], v[208:211], v[96:99], 0\n\t" \
  "ds_read_b64_tr_b16 v[218:219], v248 offset:42536\n\t" \
  "v_cvt_pk_f16_f32 v220, |v146|, |v147| clamp\n\t" \
  "s_setprio 3\n\t" \
  "v_cvt_pknorm_i16_f32 v222, |v144|, |v145|\n\t" \
  "v_mfma_f32_16x16x32_bf16 v[156:159], v[208:211], v[100:103], 0\n\t" \
  "s_waitcnt vmcnt(1)\n\t" \
  "s_cmp_lg_u32 s58, 1\n\t" \
  "s_cbranch_scc1 .Lcnx_c14\n\t" \
  "s_waitcnt vmcnt(0)\n\t" \
  ".Lcnx_c14:\n\t" \
  "s_mov_b64 exec, 3\n\t" \
  "ds_add_rtn_u32 v246, v251, v250 offset:47192\n\t" \
  "s_mov_b64 exec, -1\n\t" \
  "ds_read_b32 v234, v234 offset:0\n\t" \
  "v_cvt_pk_f16_f32 v224, |v154|, |v155| clamp\n\t" \
  "v_cvt_pknorm_i16_f32 v226, |v152|, |v153|\n\t" \
  "v_mfma_f32_16x16x32_bf16 v[164:167], v[208:211], v[104:107], 0\n\t" \
  "v_and_b32_sdwa v221, s56, v220 dst_sel:DWORD dst_unused:UNUSED_PAD src0_sel:DWORD src1_sel:WORD_1\n\t" \
  "v_and_b32 v220, s56, v220\n\t" \
  "v_mfma_f32_16x16x32_bf16 v[172:175], v[208:211], v[108:111], 0\n\t" \
  "s_waitcnt lgkmcnt(1)\n\t" \
  "v_readfirstlane_b32 s4, v246\n\t" \
  "s_cmp_lt_u32 s4, s57\n\t" \
  "s_cbranch_scc1 .Lcnx_w72\n.Lcnx_b72:\n\t" \
  "ds_read_b64_tr_b16 v[208:209], v249 offset:30752\n\t" \
  "v_lshlrev_b32_sdwa v223, 2, v222 dst_sel:DWORD dst_unused:UNUSED_PAD src0_sel:DWORD src1_sel:BYTE_3\n\t" \
  "v_lshlrev_b32_sdwa v222, 2, v222 dst_sel:DWORD dst_unused:UNUSED_PAD src0_sel:DWORD src1_sel:BYTE_1\n\t" \
  "v_and_b32_sdwa v225, s56, v224 dst_sel:DWORD dst_unused:UNUSED_PAD src0_sel:DWORD src1_sel:WORD_1\n\t" \
  "v_mfma_f32_16x16x32_bf16 v[148:151], v[212:215], v[112:115], v[148:151]\n\t" \
  "ds_read_b64_tr_b16 v[210:211], v249 offset:32288\n\t" \
  "v_and_b32 v224, s56, v224\n\t" \
  "v_lshlrev_b32_sdwa v227, 2, v226 dst_sel:DWORD dst_unused:UNUSED_PAD src0_sel:DWORD src1_sel:BYTE_3\n\t" \
  "v_mfma_f32_16x16x32_bf16 v[156:159], v[212:215], v[116:119], v[156:159]\n\t" \
  "s_cmp_eq_u32 s37, 0\n\t" \
  "s_cbranch_scc1 .Lcnx_f7\n.Lcnx_g7:\n\t" \
  "ds_read_b32 v221, v221 offset:0\n\t" \
  "ds_read_b32 v220, v220 offset:0\n\t" \
  "v_lshlrev_b32_sdwa v226, 2, v226 dst_sel:DWORD dst_unused:UNUSED_PAD src0_sel:DWORD src1_sel:BYTE_1\n\t" \
  "v_fma_f32 v197, |v197|, v229, v197 mul:4\n\t" \
  "v_mfma_f32_16x16x32_bf16 v[164:167], v[212:215], v[120:123], v[164:167]\n\t" \
  "ds_read_b32 v223, v223 offset:15376\n\t" \
  "ds_read_b32 v222, v222 offset:15376\n\t" \
  "v_fma_f32 v196, |v196|, v228, v196 mul:4\n\t" \
  "v_fma_mix_f32 v231, |v199|, v231, v231 op_sel:[0,1,0] op_sel_hi:[0,1,1] clamp\n\t" \
  "v_mfma_f32_16x16x32_bf16 v[172:175], v[212:215], v[124:127], v[172:175]\n\t" \
  "ds_read_b64_tr_b16 v[212:213], v249 offset:30760\n\t" \
  "ds_read_b64_tr_b16 v[214:215], v249 offset:32296\n\t" \
  "v_fma_mix_f32 v230, |v198|, v230, v230 op_sel:[0,1,0] op_sel_hi:[0,1,1] clamp\n\t" \
  "s_waitcnt lgkmcnt(8)\n\t" \
  "v_fma_f32 v205, |v205|, v233, v205 mul:4\n\t" \
  "v_mfma_f32_16x16x32_bf16 v[148:151], v[216:219], v[128:131], v[148:151]\n\t" \
  "ds_read_b32 v225, v225 offset:0\n\t" \
  "v_fma_f32 v204, |v204|, v232, v204 mul:4\n\t" \
  "v_fma_f32 v207, |v207|, v235, v207 mul:4\n\t" \
  "v_mfma_f32_16x16x32_bf16 v[156:159], v[216:219], v[132:135], v[156:159]\n\t" \
  "ds_read_b32 v224, v224 offset:0\n\t" \
  "v_fma_f32 v206, |v206|, v234, v206 mul:4\n\t" \
  "v_fma_f32 v199, |v199|, v231, v199 mul:4\n\t" \
  "v_mfma_f32_16x16x32_bf16 v[164:167], v[216:219], v[136:139], v[164:167]\n\t" \
  "ds_read_b32 v227, v227 offset:15376\n\t" \
  "v_fma_f32 v198, |v198|, v230, v198 mul:4\n\t" \
  "v_cvt_pk_bf16_f32 v196, v196, v197\n\t" \
  "v_mfma_f32_16x16x32_bf16 v[172:175], v[216:219], v[140:143], v[172:175]\n\t" \
  "ds_read_b64_tr_b16 v[216:217], v249 offset:30816\n\t" \
  "v_cvt_pk_bf16_f32 v197, v198, v199\n\t" \
  "v_cvt_pk_bf16_f32 v204, v204, v205\n\t" \
  "v_cvt_pk_bf16_f32 v205, v206, v207\n\t" \
  "s_setprio 0\n\t" \
  "v_cvt_pk_f16_f32 v228, |v162|, |v163| clamp\n\t" \
  "v_cvt_pknorm_i16_f32 v230, |v160|, |v161|\n\t" \
  "v_mfma_f32_16x16x32_bf16 v[40:43], v[242:245], v[236:239], v[40:43]\n\t" \
  "ds_read_b64_tr_b16 v[218:219], v249 offset:32352\n\t" \
  "v_cvt_pk_f16_f32 v232, |v170|, |v171| clamp\n\t" \
  "v_cvt_pk_f16_f32 v234, |v168|, |v169| clamp\n\t" \
  "v_mfma_f32_16x16x32_bf16 v[44:47], v[240:243], v[236:239], v[44:47]\n\t" \
  "buffer_load_dwordx4 v[236:239], v250, s[8:11], s12 offen offset:0\n\t" \
  "s_cmp_lg_u32 s58, 0\n\t" \
  "s_cbranch_scc1 .Lcnx_c27\n\t" \
  "s_add_u32 m0, s25, 41000\n\t" \
  "s_add_u32 s24, s24, s26\n\t" \
  "buffer_load_dwordx4 v254, s[8:11], s24 offen lds\n\t" \
  "s_cmp_lg_u32 s38, 7\n\t" \
  "s_cbranch_scc1 .Lcnx_c27\n\t" \
  "s_mov_b32 m0, 163632\n\t" \
  "s_add_u32 s30, s30, s31\n\t" \
  "buffer_load_dword v252, s[8:11], s30 offen lds\n\t" \
  ".Lcnx_c27:\n\t" \
  "ds_read_b32 v226, v226 offset:15376\n\t" \
  "v_and_b32_sdwa v229, s56, v228 dst_sel:DWORD dst_unused:UNUSED_PAD src0_sel:DWORD src1_sel:WORD_1\n\t" \
  "v_and_b32 v228, s56, v228\n\t" \
  "s_waitcnt lgkmcnt(12)\n\t" \
  "v_mfma_f32_16x16x32_bf16 v[0:3], v[208:211], v[178:181], v[0:3]\n\t" \
  "ds_read_b32 v229, v229 offset:0\n\t" \
  "v_lshlrev_b32_sdwa v231, 2, v230 dst_sel:DWORD dst_unused:UNUSED_PAD src0_sel:DWORD src1_sel:BYTE_3\n\t" \
  "v_lshlrev_b32_sdwa v230, 2, v230 dst_sel:DWORD dst_unused:UNUSED_PAD src0_sel:DWORD src1_sel:BYTE_1\n\t" \
  "v_mfma_f32_16x16x32_bf16 v[24:27], v[208:211], v[186:189], v[24:27]\n\t" \
  "ds_read_b32 v228, v228 offset:0\n\t" \
  "v_and_b32_sdwa v233, s56, v232 dst_sel:DWORD dst_unused:UNUSED_PAD src0_sel:DWORD src1_sel:WORD_1\n\t" \
  "v_and_b32 v232, s56, v232\n\t" \
  "v_mfma_f32_16x16x32_bf16 v[48:51], v[208:211], v[194:197], v[48:51]\n\t" \
  "ds_read_b32 v231, v231 offset:15376\n\t" \
  "v_and_b32_sdwa v235, s56, v234 dst_sel:DWORD dst_unused:UNUSED_PAD src0_sel:DWORD src1_sel:WORD_1\n\t" \
  "v_and_b32 v234, s56, v234\n\t" \
  "v_mfma_f32_16x16x32_bf16 v[72:75], v[208:211], v[202:205], v[72:75]\n\t" \
  "ds_read_b64_tr_b16 v[208:209], v249 offset:30824\n\t" \
  "ds_read_b64_tr_b16 v[210:211], v249 offset:32360\n\t" \
  "s_waitcnt lgkmcnt(13)\n\t" \
  "v_fma_f32 v147, |v147|, v221, v147 mul:4\n\t" \
  "v_fma_f32 v146, |v146|, v220, v146 mul:4\n\t" \
  "s_waitcnt lgkmcnt(11)\n\t" \
  "v_mfma_f32_16x16x32_bf16 v[4:7], v[212:215], v[178:181], v[4:7]\n\t" \
  "ds_read_b32 v230, v230 offset:15376\n\t" \
  "v_fma_mix_f32 v223, |v145|, v223, v223 op_sel:[0,1,0] op_sel_hi:[0,1,1] clamp\n\t" \
  "v_fma_mix_f32 v222, |v144|, v222, v222 op_sel:[0,1,0] op_sel_hi:[0,1,1] clamp\n\t" \
  "v_mfma_f32_16x16x32_bf16 v[28:31], v[212:215], v[186:189], v[28:31]\n\t" \
  "ds_read_b32 v233, v233 offset:0\n\t" \
  "s_waitcnt lgkmcnt(7)\n\t" \
  "v_fma_f32 v155, |v155|, v225, v155 mul:4\n\t" \
  "v_fma_f32 v154, |v154|, v224, v154 mul:4\n\t" \
  "v_mfma_f32_16x16x32_bf16 v[52:55], v[212:215], v[194:197], v[52:55]\n\t" \
  "ds_read_b32 v232, v232 offset:0\n\t" \
  "v_fma_mix_f32 v227, |v153|, v227, v227 op_sel:[0,1,0] op_sel_hi:[0,1,1] clamp\n\t" \
  "v_fma_mix_f32 v226, |v152|, v226, v226 op_sel:[0,1,0] op_sel_hi:[0,1,1] clamp\n\t" \
  "v_fma_f32 v145, |v145|, v223, v145 mul:4\n\t" \
  "v_mfma_f32_16x16x32_bf16 v[76:79], v[212:215], v[202:205], v[76:79]\n\t" \
  "ds_read_b64_tr_b16 v[212:213], v249 offset:30880\n\t" \
  "v_fma_f32 v144, |v144|, v222, v144 mul:4\n\t" \
  "v_fma_f32 v153, |v153|, v227, v153 mul:4\n\t" \
  "v_mfma_f32_16x16x32_bf16 v[8:11], v[216:219], v[178:181], v[8:11]\n\t" \
  "ds_read_b64_tr_b16 v[214:215], v249 offset:32416\n\t" \
  "v_fma_f32 v152, |v152|, v226, v152 mul:4\n\t" \
  "v_cvt_pk_bf16_f32 v147, v146, v147\n\t" \
  "v_mfma_f32_16x16x32_bf16 v[32:35], v[216:219], v[186:189], v[32:35]\n\t" \
  "ds_read_b32 v235, v235 offset:0\n\t" \
  "v_cvt_pk_bf16_f32 v146, v144, v145\n\t" \
  "v_cvt_pk_bf16_f32 v155, v154, v155\n\t" \
  "v_mfma_f32_16x16x32_bf16 v[56:59], v[216:219], v[194:197], v[56:59]\n\t" \
  "ds_read_b32 v234, v234 offset:0\n\t" \
  "v_cvt_pk_bf16_f32 v154, v152, v153\n\t" \
  "s_setprio 1\n\t" \
  "v_cvt_pk_f16_f32 v220, |v148|, |v149| clamp\n\t" \
  "v_mfma_f32_16x16x32_bf16 v[80:83], v[216:219], v[202:205], v[80:83]\n\t" \
  "ds_read_b64_tr_b16 v[216:217], v249 offset:30888\n\t" \
  "v_cvt_pknorm_i16_f32 v222, |v150|, |v151|\n\t" \
  "v_cvt_pk_f16_f32 v224, |v156|, |v157| clamp\n\t" \
  "s_waitcnt lgkmcnt(8)\n\t" \
  "v_mfma_f32_16x16x32_bf16 v[12:15], v[208:211], v[178:181], v[12:15]\n\t" \
  "ds_read_b64_tr_b16 v[218:219], v249 offset:32424\n\t" \
  "v_cvt_pknorm_i16_f32 v226, |v158|, |v159|\n\t" \
  "v_and_b32_sdwa v221, s56, v220 dst_sel:DWORD dst_unused:UNUSED_PAD src0_sel:DWORD src1_sel:WORD_1\n\t" \
  "v_mfma_f32_16x16x32_bf16 v[36:39], v[208:211], v[186:189], v[36:39]\n\t" \
  "v_and_b32 v220, s56, v220\n\t" \
  "v_lshlrev_b32_sdwa v223, 2, v222 dst_sel:DWORD dst_unused:UNUSED_PAD src0_sel:DWORD src1_sel:BYTE_3\n\t" \
  "v_mfma_f32_16x16x32_bf16 v[60:63], v[208:211], v[194:197], v[60:63]\n\t" \
  "ds_read_b32 v221, v221 offset:0\n\t" \
  "v_lshlrev_b32_sdwa v222, 2, v222 dst_sel:DWORD dst_unused:UNUSED_PAD src0_sel:DWORD src1_sel:BYTE_1\n\t" \
  "v_and_b32_sdwa v225, s56, v224 dst_sel:DWORD dst_unused:UNUSED_PAD src0_sel:DWORD src1_sel:WORD_1\n\t" \
  "v_and_b32 v224, s56, v224\n\t" \
  "v_mfma_f32_16x16x32_bf16 v[84:87], v[208:211], v[202:205], v[84:87]\n\t" \
  "ds_read_b64_tr_b16 v[208:209], v248 offset:12304\n\t" \
  "ds_read_b64_tr_b16 v[210:211], v248 offset:13328\n\t" \
  "v_lshlrev_b32_sdwa v227, 2, v226 dst_sel:DWORD dst_unused:UNUSED_PAD src0_sel:DWORD src1_sel:BYTE_3\n\t" \
  "v_lshlrev_b32_sdwa v226, 2, v226 dst_sel:DWORD dst_unused:UNUSED_PAD src0_sel:DWORD src1_sel:BYTE_1\n\t" \
  "s_waitcnt lgkmcnt(7)\n\t" \
  "v_mfma_f32_16x16x32_bf16 v[16:19], v[212:215], v[178:181], v[16:19]\n\t" \
  "ds_read_b32 v220, v220 offset:0\n\t" \
  "ds_read_b32 v223, v223 offset:15376\n\t" \
  "v_fma_f32 v163, |v163|, v229, v163 mul:4\n\t" \
  "v_fma_f32 v162, |v162|, v228, v162 mul:4\n\t" \
  "v_mfma_f32_16x16x32_bf16 v[40:43], v[212:215], v[186:189], v[40:43]\n\t" \
  "ds_read_b32 v222, v222 offset:15376\n\t" \
  "v_fma_mix_f32 v231, |v161|, v231, v231 op_sel:[0,1,0] op_sel_hi:[0,1,1] clamp\n\t" \
  "v_fma_mix_f32 v230, |v160|, v230, v230 op_sel:[0,1,0] op_sel_hi:[0,1,1] clamp\n\t" \
  "v_mfma_f32_16x16x32_bf16 v[64:67], v[212:215], v[194:197], v[64:67]\n\t" \
  "ds_read_b32 v225, v225 offset:0\n\t" \
  "s_waitcnt lgkmcnt(9)\n\t" \
  "v_fma_f32 v171, |v171|, v233, v171 mul:4\n\t" \
  "v_fma_f32 v170, |v170|, v232, v170 mul:4\n\t" \
  "v_mfma_f32_16x16x32_bf16 v[88:91], v[212:215], v[202:205], v[88:91]\n\t" \
  "ds_read_b64_tr_b16 v[212:213], v248 offset:15376\n\t" \
  "v_fma_f32 v169, |v169|, v235, v169 mul:4\n\t" \
  "v_fma_f32 v168, |v168|, v234, v168 mul:4\n\t" \
  "s_waitcnt lgkmcnt(8)\n\t" \
  "v_mfma_f32_16x16x32_bf16 v[20:23], v[216:219], v[178:181], v[20:23]\n\t" \
  "ds_read_b64_tr_b16 v[214:215], v248 offset:16400\n\t" \
  "v_fma_f32 v161, |v161|, v231, v161 mul:4\n\t" \
  "v_fma_f32 v160, |v160|, v230, v160 mul:4\n\t" \
  "v_mfma_f32_16x16x32_bf16 v[44:47], v[216:219], v[186:189], v[44:47]\n\t" \
  "ds_read_b32 v224, v224 offset:0\n\t" \
  "v_cvt_pk_bf16_f32 v163, v162, v163\n\t" \
  "v_cvt_pk_bf16_f32 v162, v160, v161\n\t" \
  "v_mfma_f32_16x16x32_bf16 v[68:71], v[216:219], v[194:197], v[68:71]\n\t" \
  "ds_read_b32 v227, v227 offset:15376\n\t" \
  "v_cvt_pk_bf16_f32 v171, v170, v171\n\t" \
  "v_cvt_pk_bf16_f32 v170, v168, v169\n\t" \
  "v_cvt_pk_f16_f32 v228, |v164|, |v165| clamp\n\t" \
  "v_mfma_f32_16x16x32_bf16 v[92:95], v[216:219], v[202:205], v[92:95]\n\t" \
  "ds_read_b64_tr_b16 v[216:217], v248 offset:14352\n\t" \
  "v_cvt_pknorm_i16_f32 v230, |v166|, |v167|\n\t" \
  "s_setprio 2\n\t" \
  "v_cvt_pk_f16_f32 v232, |v172|, |v173| clamp\n\t" \
  "s_waitcnt lgkmcnt(9)\n\t" \
  "v_mfma_f32_16x16x32_bf16 v[176:179], v[208:211], v[96:99], 0\n\t" \
  "ds_read_b64_tr_b16 v[218:219], v248 offset:17424\n\t" \
  "v_cvt_pk_f16_f32 v234, |v174|, |v175| clamp\n\t" \
  "v_and_b32_sdwa v229, s56, v228 dst_sel:DWORD dst_unused:UNUSED_PAD src0_sel:DWORD src1_sel:WORD_1\n\t" \
  "v_mfma_f32_16x16x32_bf16 v[184:187], v[208:211], v[100:103], 0\n\t" \
  "ds_read_b32 v226, v226 offset:15376\n\t" \
  "v_and_b32 v228, s56, v228\n\t" \
  "v_lshlrev_b32_sdwa v231, 2, v230 dst_sel:DWORD dst_unused:UNUSED_PAD src0_sel:DWORD src1_sel:BYTE_3\n\t" \
  "v_mfma_f32_16x16x32_bf16 v[192:195], v[208:211], v[104:107], 0\n\t" \
  "ds_read_b32 v229, v229 offset:0\n\t" \
  "v_lshlrev_b32_sdwa v230, 2, v230 dst_sel:DWORD dst_unused:UNUSED_PAD src0_sel:DWORD src1_sel:BYTE_1\n\t" \
  "v_and_b32_sdwa v233, s56, v232 dst_sel:DWORD dst_unused:UNUSED_PAD src0_sel:DWORD src1_sel:WORD_1\n\t" \
  "v_mfma_f32_16x16x32_bf16 v[200:203], v[208:211], v[108:111], 0\n\t" \
  "ds_read_b64_tr_b16 v[208:209], v248 offset:12816\n\t" \
  "v_and_b32 v232, s56, v232\n\t" \
  "v_and_b32_sdwa v235, s56, v234 dst_sel:DWORD dst_unused:UNUSED_PAD src0_sel:DWORD src1_sel:WORD_1\n\t" \
  "s_waitcnt lgkmcnt(7)\n\t" \
  "v_mfma_f32_16x16x32_bf16 v[176:179], v[212:215], v[112:115], v[176:179]\n\t" \
  "ds_read_b64_tr_b16 v[210:211], v248 offset:13840\n\t" \
  "ds_read_b32 v228, v228 offset:0\n\t" \
  "v_and_b32 v234, s56, v234\n\t" \
  "v_fma_f32 v149, |v149|, v221, v149 mul:4\n\t" \
  "v_mfma_f32_16x16x32_bf16 v[184:187], v[212:215], v[116:119], v[184:187]\n\t" \
  "ds_read_b32 v231, v231 offset:15376\n\t" \
  "ds_read_b32 v230, v230 offset:15376\n\t" \
  "v_fma_f32 v148, |v148|, v220, v148 mul:4\n\t" \
  "v_fma_mix_f32 v223, |v151|, v223, v223 op_sel:[0,1,0] op_sel_hi:[0,1,1] clamp\n\t" \
  "v_mfma_f32_16x16x32_bf16 v[192:195], v[212:215], v[120:123], v[192:195]\n\t" \
  "ds_read_b32 v233, v233 offset:0\n\t" \
  "v_fma_mix_f32 v222, |v150|, v222, v222 op_sel:[0,1,0] op_sel_hi:[0,1,1] clamp\n\t" \
  "s_waitcnt lgkmcnt(7)\n\t" \
  "v_fma_f32 v157, |v157|, v225, v157 mul:4\n\t" \
  "v_mfma_f32_16x16x32_bf16 v[200:203], v[212:215], v[124:127], v[200:203]\n\t" \
  "ds_read_b64_tr_b16 v[212:213], v248 offset:15888\n\t" \
  "v_fma_f32 v156, |v156|, v224, v156 mul:4\n\t" \
  "v_fma_mix_f32 v227, |v159|, v227, v227 op_sel:[0,1,0] op_sel_hi:[0,1,1] clamp\n\t" \
  "v_fma_mix_f32 v226, |v158|, v226, v226 op_sel:[0,1,0] op_sel_hi:[0,1,1] clamp\n\t" \
  "v_mfma_f32_16x16x32_bf16 v[176:179], v[216:219], v[128:131], v[176:179]\n\t" \
  "ds_read_b64_tr_b16 v[214:215], v248 offset:16912\n\t" \
  "v_fma_f32 v151, |v151|, v223, v151 mul:4\n\t" \
  "v_fma_f32 v150, |v150|, v222, v150 mul:4\n\t" \
  "v_mfma_f32_16x16x32_bf16 v[184:187], v[216:219], v[132:135], v[184:187]\n\t" \
  "ds_read_b32 v232, v232 offset:0\n\t" \
  "v_fma_f32 v159, |v159|, v227, v159 mul:4\n\t" \
  "v_fma_f32 v158, |v158|, v226, v158 mul:4\n\t" \
  "v_mfma_f32_16x16x32_bf16 v[192:195], v[216:219], v[136:139], v[192:195]\n\t" \
  "ds_read_b32 v235, v235 offset:0\n\t" \
  "v_cvt_pk_bf16_f32 v148, v148, v149\n\t" \
  "v_cvt_pk_bf16_f32 v149, v150, v151\n\t" \
  "v_mfma_f32_16x16x32_bf16 v[200:203], v[216:219], v[140:143], v[200:203]\n\t" \
  "ds_read_b64_tr_b16 v[216:217], v248 offset:14864\n\t" \
  "v_cvt_pk_bf16_f32 v156, v156, v157\n\t" \
  "v_cvt_pk_bf16_f32 v157, v158, v159\n\t" \
  "s_waitcnt lgkmcnt(9)\n\t" \
  "v_mfma_f32_16x16x32_bf16 v[180:183], v[208:211], v[96:99], 0\n\t" \
  "ds_read_b64_tr_b16 v[218:219], v248 offset:17936\n\t" \
  "v_cvt_pk_f16_f32 v220, |v178|, |v179| clamp\n\t" \
  "s_setprio 3\n\t" \
  "v_cvt_pknorm_i16_f32 v222, |v176|, |v177|\n\t" \
  "v_mfma_f32_16x16x32_bf16 v[188:191], v[208:211], v[100:103], 0\n\t" \
  "s_waitcnt vmcnt(1)\n\t" \
  "s_cmp_lg_u32 s58, 1\n\t" \
  "s_cbranch_scc1 .Lcnx_c15\n\t" \
  "s_waitcnt vmcnt(0)\n\t" \
  ".Lcnx_c15:\n\t" \
  "s_mov_b64 exec, 3\n\t" \
  "ds_add_rtn_u32 v246, v251, v250 offset:47200\n\t" \
  "s_mov_b64 exec, -1\n\t" \
  "ds_read_b32 v234, v234 offset:0\n\t" \
  "v_cvt_pk_f16_f32 v224, |v186|, |v187| clamp\n\t" \
  "v_cvt_pknorm_i16_f32 v226, |v184|, |v185|\n\t" \
  "v_mfma_f32_16x16x32_bf16 v[196:199], v[208:211], v[104:107], 0\n\t" \
  "v_and_b32_sdwa v221, s56, v220 dst_sel:DWORD dst_unused:UNUSED_PAD src0_sel:DWORD src1_sel:WORD_1\n\t" \
  "v_and_b32 v220, s56, v220\n\t" \
  "v_mfma_f32_16x16x32_bf16 v[204:207], v[208:211], v[108:111], 0\n\t" \
  "s_waitcnt lgkmcnt(1)\n\t" \
  "v_readfirstlane_b32 s4, v246\n\t" \
  "s_cmp_lt_u32 s4, s57\n\t" \
  "s_cbranch_scc1 .Lcnx_w84\n.Lcnx_b84:\n\t" \
  "s_or_b32 s37, s37, s58\n\t" \
  "ds_read_b64_tr_b16 v[208:209], v249 offset:0\n\t" \
  "v_lshlrev_b32_sdwa v223, 2, v222 dst_sel:DWORD dst_unused:UNUSED_PAD src0_sel:DWORD src1_sel:BYTE_3\n\t" \
  "v_lshlrev_b32_sdwa v222, 2, v222 dst_sel:DWORD dst_unused:UNUSED_PAD src0_sel:DWORD src1_sel:BYTE_1\n\t" \
  "v_and_b32_sdwa v225, s56, v224 dst_sel:DWORD dst_unused:UNUSED_PAD src0_sel:DWORD src1_sel:WORD_1\n\t" \
  "v_mfma_f32_16x16x32_bf16 v[180:183], v[212:215], v[112:115], v[180:183]\n\t" \
  "ds_read_b64_tr_b16 v[210:211], v249 offset:1536\n\t" \
  "v_and_b32 v224, s56, v224\n\t" \
  "v_lshlrev_b32_sdwa v227, 2, v226 dst_sel:DWORD dst_unused:UNUSED_PAD src0_sel:DWORD src1_sel:BYTE_3\n\t" \
  "v_mfma_f32_16x16x32_bf16 v[188:191], v[212:215], v[116:119], v[188:191]\n\t" \
  "s_cmp_eq_u32 s37, 0\n\t" \
  "s_cbranch_scc1 .Lcnx_f8\n.Lcnx_g8:\n\t" \
  "ds_read_b32 v221, v221 offset:0\n\t" \
  "ds_read_b32 v220, v220 offset:0\n\t" \
  "v_lshlrev_b32_sdwa v226, 2, v226 dst_sel:DWORD dst_unused:UNUSED_PAD src0_sel:DWORD src1_sel:BYTE_1\n\t" \
  "v_fma_f32 v165, |v165|, v229, v165 mul:4\n\t" \
  "v_mfma_f32_16x16x32_bf16 v[196:199], v[212:215], v[120:123], v[196:199]\n\t" \
  "ds_read_b32 v223, v223 offset:15376\n\t" \
  "ds_read_b32 v222, v222 offset:15376\n\t" \
  "v_fma_f32 v164, |v164|, v228, v164 mul:4\n\t" \
  "v_fma_mix_f32 v231, |v167|, v231, v231 op_sel:[0,1,0] op_sel_hi:[0,1,1] clamp\n\t" \
  "v_mfma_f32_16x16x32_bf16 v[204:207], v[212:215], v[124:127], v[204:207]\n\t" \
  "ds_read_b64_tr_b16 v[212:213], v249 offset:8\n\t" \
  "ds_read_b64_tr_b16 v[214:215], v249 offset:1544\n\t" \
  "v_fma_mix_f32 v230, |v166|, v230, v230 op_sel:[0,1,0] op_sel_hi:[0,1,1] clamp\n\t" \
  "s_waitcnt lgkmcnt(8)\n\t" \
  "v_fma_f32 v173, |v173|, v233, v173 mul:4\n\t" \
  "v_mfma_f32_16x16x32_bf16 v[180:183], v[216:219], v[128:131], v[180:183]\n\t" \
  "ds_read_b32 v225, v225 offset:0\n\t" \
  "v_fma_f32 v172, |v172|, v232, v172 mul:4\n\t" \
  "v_fma_f32 v175, |v175|, v235, v175 mul:4\n\t" \
  "v_mfma_f32_16x16x32_bf16 v[188:191], v[216:219], v[132:135], v[188:191]\n\t" \
  "ds_read_b32 v224, v224 offset:0\n\t" \
  "v_fma_f32 v174, |v174|, v234, v174 mul:4\n\t" \
  "v_fma_f32 v167, |v167|, v231, v167 mul:4\n\t" \
  "v_mfma_f32_16x16x32_bf16 v[196:199], v[216:219], v[136:139], v[196:199]\n\t" \
  "ds_read_b32 v227, v227 offset:15376\n\t" \
  "v_fma_f32 v166, |v166|, v230, v166 mul:4\n\t" \
  "v_cvt_pk_bf16_f32 v164, v164, v165\n\t" \
  "v_mfma_f32_16x16x32_bf16 v[204:207], v[216:219], v[140:143], v[204:207]\n\t" \
  "ds_read_b64_tr_b16 v[216:217], v249 offset:64\n\t" \
  "v_cvt_pk_bf16_f32 v165, v166, v167\n\t" \
  "v_cvt_pk_bf16_f32 v172, v172, v173\n\t" \
  "v_cvt_pk_bf16_f32 v173, v174, v175\n\t" \
  "s_setprio 0\n\t" \
  "v_cvt_pk_f16_f32 v228, |v194|, |v195| clamp\n\t" \
  "v_cvt_pknorm_i16_f32 v230, |v192|, |v193|\n\t" \
  "v_mfma_f32_16x16x32_bf16 v[48:51], v[242:245], v[236:239], v[48:51]\n\t" \
  "ds_read_b64_tr_b16 v[218:219], v249 offset:1600\n\t" \
  "v_cvt_pk_f16_f32 v232, |v202|, |v203| clamp\n\t" \
  "v_cvt_pk_f16_f32 v234, |v200|, |v201| clamp\n\t" \
  "v_mfma_f32_16x16x32_bf16 v[52:55], v[240:243], v[236:239], v[52:55]\n\t" \
  "buffer_load_dwordx4 v[236:239], v250, s[8:11], s12 offen offset:64\n\t" \
  "s_cmp_lg_u32 s58, 0\n\t" \
  "s_cbranch_scc1 .Lcnx_c28\n\t" \
  "s_add_u32 m0, s25, 49200\n\t" \
  "s_add_u32 s24, s24, s26\n\t" \
  "buffer_load_dwordx4 v254, s[8:11], s24 offen lds\n\t" \
  "s_cmp_lg_u32 s38, 4\n\t" \
  "s_cbranch_scc1 .Lcnx_c28\n\t" \
  "s_mov_b32 m0, 163632\n\t" \
  "s_add_u32 s30, s30, s31\n\t" \
  "buffer_load_dword v252, s[8:11], s30 offen lds\n\t" \
  ".Lcnx_c28:\n\t" \
  "ds_read_b32 v226, v226 offset:15376\n\t" \
  "v_and_b32_sdwa v229, s56, v228 dst_sel:DWORD dst_unused:UNUSED_PAD src0_sel:DWORD src1_sel:WORD_1\n\t" \
  "v_and_b32 v228, s56, v228\n\t" \
  "s_waitcnt lgkmcnt(12)\n\t" \
  "v_mfma_f32_16x16x32_bf16 v[0:3], v[208:211], v[146:149], v[0:3]\n\t" \
  "ds_read_b32 v229, v229 offset:0\n\t" \
  "v_lshlrev_b32_sdwa v231, 2, v230 dst_sel:DWORD dst_unused:UNUSED_PAD src0_sel:DWORD src1_sel:BYTE_3\n\t" \
  "v_lshlrev_b32_sdwa v230, 2, v230 dst_sel:DWORD dst_unused:UNUSED_PAD src0_sel:DWORD src1_sel:BYTE_1\n\t" \
  "v_mfma_f32_16x16x32_bf16 v[24:27], v[208:211], v[154:157], v[24:27]\n\t" \
  "ds_read_b32 v228, v228 offset:0\n\t" \
  "v_and_b32_sdwa v233, s56, v232 dst_sel:DWORD dst_unused:UNUSED_PAD src0_sel:DWORD src1_sel:WORD_1\n\t" \
  "v_and_b32 v232, s56, v232\n\t" \
  "v_mfma_f32_16x16x32_bf16 v[48:51], v[208:211], v[162:165], v[48:51]\n\t" \
  "ds_read_b32 v231, v231 offset:15376\n\t" \
  "v_and_b32_sdwa v235, s56, v234 dst_sel:DWORD dst_unused:UNUSED_PAD src0_sel:DWORD src1_sel:WORD_1\n\t" \
  "v_and_b32 v234, s56, v234\n\t" \
  "v_mfma_f32_16x16x32_bf16 v[72:75], v[208:211], v[170:173], v[72:75]\n\t" \
  "ds_read_b64_tr_b16 v[208:209], v249 offset:72\n\t" \
  "ds_read_b64_tr_b16 v[210:211], v249 offset:1608\n\t" \
  "s_waitcnt lgkmcnt(13)\n\t" \
  "v_fma_f32 v179, |v179|, v221, v179 mul:4\n\t" \
  "v_fma_f32 v178, |v178|, v220, v178 mul:4\n\t" \
  "s_waitcnt lgkmcnt(11)\n\t" \
  "v_mfma_f32_16x16x32_bf16 v[4:7], v[212:215], v[146:149], v[4:7]\n\t" \
  "ds_read_b32 v230, v230 offset:15376\n\t" \
  "v_fma_mix_f32 v223, |v177|, v223, v223 op_sel:[0,1,0] op_sel_hi:[0,1,1] clamp\n\t" \
  "v_fma_mix_f32 v222, |v176|, v222, v222 op_sel:[0,1,0] op_sel_hi:[0,1,1] clamp\n\t" \
  "v_mfma_f32_16x16x32_bf16 v[28:31], v[212:215], v[154:157], v[28:31]\n\t" \
  "ds_read_b32 v233, v233 offset:0\n\t" \
  "s_waitcnt lgkmcnt(7)\n\t" \
  "v_fma_f32 v187, |v187|, v225, v187 mul:4\n\t" \
  "v_fma_f32 v186, |v186|, v224, v186 mul:4\n\t" \
  "v_mfma_f32_16x16x32_bf16 v[52:55], v[212:215], v[162:165], v[52:55]\n\t" \
  "ds_read_b32 v232, v232 offset:0\n\t" \
  "v_fma_mix_f32 v227, |v185|, v227, v227 op_sel:[0,1,0] op_sel_hi:[0,1,1] clamp\n\t" \
  "v_fma_mix_f32 v226, |v184|, v226, v226 op_sel:[0,1,0] op_sel_hi:[0,1,1] clamp\n\t" \
  "v_fma_f32 v177, |v177|, v223, v177 mul:4\n\t" \
  "v_mfma_f32_16x16x32_bf16 v[76:79], v[212:215], v[170:173], v[76:79]\n\t" \
  "ds_read_b64_tr_b16 v[212:213], v249 offset:128\n\t" \
  "v_fma_f32 v176, |v176|, v222, v176 mul:4\n\t" \
  "v_fma_f32 v185, |v185|, v227, v185 mul:4\n\t" \
  "v_mfma_f32_16x16x32_bf16 v[8:11], v[216:219], v[146:149], v[8:11]\n\t" \
  "ds_read_b64_tr_b16 v[214:215], v249 offset:1664\n\t" \
  "v_fma_f32 v184, |v184|, v226, v184 mul:4\n\t" \
  "v_cvt_pk_bf16_f32 v179, v178, v179\n\t" \
  "v_mfma_f32_16x16x32_bf16 v[32:35], v[216:219], v[154:157], v[32:35]\n\t" \
  "ds_read_b32 v235, v235 offset:0\n\t" \
  "v_cvt_pk_bf16_f32 v178, v176, v177\n\t" \
  "v_cvt_pk_bf16_f32 v187, v186, v187\n\t" \
  "v_mfma_f32_16x16x32_bf16 v[56:59], v[216:219], v[162:165], v[56:59]\n\t" \
  "ds_read_b32 v234, v234 offset:0\n\t" \
  "v_cvt_pk_bf16_f32 v186, v184, v185\n\t" \
  "s_setprio 1\n\t" \
  "v_cvt_pk_f16_f32 v220, |v180|, |v181| clamp\n\t" \
  "v_mfma_f32_16x16x32_bf16 v[80:83], v[216:219], v[170:173], v[80:83]\n\t" \
  "ds_read_b64_tr_b16 v[216:217], v249 offset:136\n\t" \
  "v_cvt_pknorm_i16_f32 v222, |v182|, |v183|\n\t" \
  "v_cvt_pk_f16_f32 v224, |v188|, |v189| clamp\n\t" \
  "s_waitcnt lgkmcnt(8)\n\t" \
  "v_mfma_f32_16x16x32_bf16 v[12:15], v[208:211], v[146:149], v[12:15]\n\t" \
  "ds_read_b64_tr_b16 v[218:219], v249 offset:1672\n\t" \
  "v_cvt_pknorm_i16_f32 v226, |v190|, |v191|\n\t" \
  "v_and_b32_sdwa v221, s56, v220 dst_sel:DWORD dst_unused:UNUSED_PAD src0_sel:DWORD src1_sel:WORD_1\n\t" \
  "v_mfma_f32_16x16x32_bf16 v[36:39], v[208:211], v[154:157], v[36:39]\n\t" \
  "v_and_b32 v220, s56, v220\n\t" \
  "v_lshlrev_b32_sdwa v223, 2, v222 dst_sel:DWORD dst_unused:UNUSED_PAD src0_sel:DWORD src1_sel:BYTE_3\n\t" \
  "v_mfma_f32_16x16x32_bf16 v[60:63], v[208:211], v[162:165], v[60:63]\n\t" \
  "ds_read_b32 v221, v221 offset:0\n\t" \
  "v_lshlrev_b32_sdwa v222, 2, v222 dst_sel:DWORD dst_unused:UNUSED_PAD src0_sel:DWORD src1_sel:BYTE_1\n\t" \
  "v_and_b32_sdwa v225, s56, v224 dst_sel:DWORD dst_unused:UNUSED_PAD src0_sel:DWORD src1_sel:WORD_1\n\t" \
  "v_and_b32 v224, s56, v224\n\t" \
  "v_mfma_f32_16x16x32_bf16 v[84:87], v[208:211], v[170:173], v[84:87]\n\t" \
  "ds_read_b64_tr_b16 v[208:209], v248 offset:43048\n\t" \
  "ds_read_b64_tr_b16 v[210:211], v248 offset:44072\n\t" \
  "v_lshlrev_b32_sdwa v227, 2, v226 dst_sel:DWORD dst_unused:UNUSED_PAD src0_sel:DWORD src1_sel:BYTE_3\n\t" \
  "v_lshlrev_b32_sdwa v226, 2, v226 dst_sel:DWORD dst_unused:UNUSED_PAD src0_sel:DWORD src1_sel:BYTE_1\n\t" \
  "s_waitcnt lgkmcnt(7)\n\t" \
  "v_mfma_f32_16x16x32_bf16 v[16:19], v[212:215], v[146:149], v[16:19]\n\t" \
  "ds_read_b32 v220, v220 offset:0\n\t" \
  "ds_read_b32 v223, v223 offset:15376\n\t" \
  "v_fma_f32 v195, |v195|, v229, v195 mul:4\n\t" \
  "v_fma_f32 v194, |v194|, v228, v194 mul:4\n\t" \
  "v_mfma_f32_16x16x32_bf16 v[40:43], v[212:215], v[154:157], v[40:43]\n\t" \
  "ds_read_b32 v222, v222 offset:15376\n\t" \
  "v_fma_mix_f32 v231, |v193|, v231, v231 op_sel:[0,1,0] op_sel_hi:[0,1,1] clamp\n\t" \
  "v_fma_mix_f32 v230, |v192|, v230, v230 op_sel:[0,1,0] op_sel_hi:[0,1,1] clamp\n\t" \
  "v_mfma_f32_16x16x32_bf16 v[64:67], v[212:215], v[162:165], v[64:67]\n\t" \
  "ds_read_b32 v225, v225 offset:0\n\t" \
  "s_waitcnt lgkmcnt(9)\n\t" \
  "v_fma_f32 v203, |v203|, v233, v203 mul:4\n\t" \
  "v_fma_f32 v202, |v202|, v232, v202 mul:4\n\t" \
  "v_mfma_f32_16x16x32_bf16 v[88:91], v[212:215], v[170:173], v[88:91]\n\t" \
  "ds_read_b64_tr_b16 v[212:213], v248 offset:46120\n\t" \
  "v_fma_f32 v201, |v201|, v235, v201 mul:4\n\t" \
  "v_fma_f32 v200, |v200|, v234, v200 mul:4\n\t" \
  "s_waitcnt lgkmcnt(8)\n\t" \
  "v_mfma_f32_16x16x32_bf16 v[20:23], v[216:219], v[146:149], v[20:23]\n\t" \
  "ds_read_b64_tr_b16 v[214:215], v248 offset:47144\n\t" \
  "v_fma_f32 v193, |v193|, v231, v193 mul:4\n\t" \
  "v_fma_f32 v192, |v192|, v230, v192 mul:4\n\t" \
  "v_mfma_f32_16x16x32_bf16 v[44:47], v[216:219], v[154:157], v[44:47]\n\t" \
  "ds_read_b32 v224, v224 offset:0\n\t" \
  "v_cvt_pk_bf16_f32 v195, v194, v195\n\t" \
  "v_cvt_pk_bf16_f32 v194, v192, v193\n\t" \
  "v_mfma_f32_16x16x32_bf16 v[68:71], v[216:219], v[162:165], v[68:71]\n\t" \
  "ds_read_b32 v227, v227 offset:15376\n\t" \
  "v_cvt_pk_bf16_f32 v203, v202, v203\n\t" \
  "v_cvt_pk_bf16_f32 v202, v200, v201\n\t" \
  "v_cvt_pk_f16_f32 v228, |v196|, |v197| clamp\n\t" \
  "v_mfma_f32_16x16x32_bf16 v[92:95], v[216:219], v[170:173], v[92:95]\n\t" \
  "ds_read_b64_tr_b16 v[216:217], v248 offset:45096\n\t" \
  "v_cvt_pknorm_i16_f32 v230, |v198|, |v199|\n\t" \
  "s_setprio 2\n\t" \
  "v_cvt_pk_f16_f32 v232, |v204|, |v205| clamp\n\t" \
  "s_waitcnt lgkmcnt(9)\n\t" \
  "v_mfma_f32_16x16x32_bf16 v[144:147], v[208:211], v[96:99], 0\n\t" \
  "ds_read_b64_tr_b16 v[218:219], v248 offset:48168\n\t" \
  "v_cvt_pk_f16_f32 v234, |v206|, |v207| clamp\n\t" \
  "v_and_b32_sdwa v229, s56, v228 dst_sel:DWORD dst_unused:UNUSED_PAD src0_sel:DWORD src1_sel:WORD_1\n\t" \
  "v_mfma_f32_16x16x32_bf16 v[152:155], v[208:211], v[100:103], 0\n\t" \
  "ds_read_b32 v226, v226 offset:15376\n\t" \
  "v_and_b32 v228, s56, v228\n\t" \
  "v_lshlrev_b32_sdwa v231, 2, v230 dst_sel:DWORD dst_unused:UNUSED_PAD src0_sel:DWORD src1_sel:BYTE_3\n\t" \
  "v_mfma_f32_16x16x32_bf16 v[160:163], v[208:211], v[104:107], 0\n\t" \
  "ds_read_b32 v229, v229 offset:0\n\t" \
  "v_lshlrev_b32_sdwa v230, 2, v230 dst_sel:DWORD dst_unused:UNUSED_PAD src0_sel:DWORD src1_sel:BYTE_1\n\t" \
  "v_and_b32_sdwa v233, s56, v232 dst_sel:DWORD dst_unused:UNUSED_PAD src0_sel:DWORD src1_sel:WORD_1\n\t" \
  "v_mfma_f32_16x16x32_bf16 v[168:171], v[208:211], v[108:111], 0\n\t" \
  "ds_read_b64_tr_b16 v[208:209], v248 offset:43560\n\t" \
  "v_and_b32 v232, s56, v232\n\t" \
  "v_and_b32_sdwa v235, s56, v234 dst_sel:DWORD dst_unused:UNUSED_PAD src0_sel:DWORD src1_sel:WORD_1\n\t" \
  "s_waitcnt lgkmcnt(7)\n\t" \
  "v_mfma_f32_16x16x32_bf16 v[144:147], v[212:215], v[112:115], v[144:147]\n\t" \
  "ds_read_b64_tr_b16 v[210:211], v248 offset:44584\n\t" \
  "ds_read_b32 v228, v228 offset:0\n\t" \
  "v_and_b32 v234, s56, v234\n\t" \
  "v_fma_f32 v181, |v181|, v221, v181 mul:4\n\t" \
  "v_mfma_f32_16x16x32_bf16 v[152:155], v[212:215], v[116:119], v[152:155]\n\t" \
  "ds_read_b32 v231, v231 offset:15376\n\t" \
  "ds_read_b32 v230, v230 offset:15376\n\t" \
  "v_fma_f32 v180, |v180|, v220, v180 mul:4\n\t" \
  "v_fma_mix_f32 v223, |v183|, v223, v223 op_sel:[0,1,0] op_sel_hi:[0,1,1] clamp\n\t" \
  "v_mfma_f32_16x16x32_bf16 v[160:163], v[212:215], v[120:123], v[160:163]\n\t" \
  "ds_read_b32 v233, v233 offset:0\n\t" \
  "v_fma_mix_f32 v222, |v182|, v222, v222 op_sel:[0,1,0] op_sel_hi:[0,1,1] clamp\n\t" \
  "s_waitcnt lgkmcnt(7)\n\t" \
  "v_fma_f32 v189, |v189|, v225, v189 mul:4\n\t" \
  "v_mfma_f32_16x16x32_bf16 v[168:171], v[212:215], v[124:127], v[168:171]\n\t" \
  "ds_read_b64_tr_b16 v[212:213], v248 offset:46632\n\t" \
  "v_fma_f32 v188, |v188|, v224, v188 mul:4\n\t" \
  "v_fma_mix_f32 v227, |v191|, v227, v227 op_sel:[0,1,0] op_sel_hi:[0,1,1] clamp\n\t" \
  "v_fma_mix_f32 v226, |v190|, v226, v226 op_sel:[0,1,0] op_sel_hi:[0,1,1] clamp\n\t" \
  "v_mfma_f32_16x16x32_bf16 v[144:147], v[216:219], v[128:131], v[144:147]\n\t" \
  "ds_read_b64_tr_b16 v[214:215], v248 offset:47656\n\t" \
  "v_fma_f32 v183, |v183|, v223, v183 mul:4\n\t" \
  "v_fma_f32 v182, |v182|, v222, v182 mul:4\n\t" \
  "v_mfma_f32_16x16x32_bf16 v[152:155], v[216:219], v[132:135], v[152:155]\n\t" \
  "ds_read_b32 v232, v232 offset:0\n\t" \
  "v_fma_f32 v191, |v191|, v227, v191 mul:4\n\t" \
  "v_fma_f32 v190, |v190|, v226, v190 mul:4\n\t" \
  "v_mfma_f32_16x16x32_bf16 v[160:163], v[216:219], v[136:139], v[160:163]\n\t" \
  "ds_read_b32 v235, v235 offset:0\n\t" \
  "v_cvt_pk_bf16_f32 v180, v180, v181\n\t" \
  "v_cvt_pk_bf16_f32 v181, v182, v183\n\t" \
  "v_mfma_f32_16x16x32_bf16 v[168:171], v[216:219], v[140:143], v[168:171]\n\t" \
  "ds_read_b64_tr_b16 v[216:217], v248 offset:45608\n\t" \
  "v_cvt_pk_bf16_f32 v188, v188, v189\n\t" \
  "v_cvt_pk_bf16_f32 v189, v190, v191\n\t" \
  "s_waitcnt lgkmcnt(9)\n\t" \
  "v_mfma_f32_16x16x32_bf16 v[148:151], v[208:211], v[96:99], 0\n\t" \
  "ds_read_b64_tr_b16 v[218:219], v248 offset:48680\n\t" \
  "v_cvt_pk_f16_f32 v220, |v146|, |v147| clamp\n\t" \
  "s_setprio 3\n\t" \
  "v_cvt_pknorm_i16_f32 v222, |v144|, |v145|\n\t" \
  "v_mfma_f32_16x16x32_bf16 v[156:159], v[208:211], v[100:103], 0\n\t" \
  "s_waitcnt vmcnt(1)\n\t" \
  "s_cmp_lg_u32 s58, 1\n\t" \
  "s_cbranch_scc1 .Lcnx_c16\n\t" \
  "s_waitcnt vmcnt(0)\n\t" \
  ".Lcnx_c16:\n\t" \
  "s_mov_b64 exec, 3\n\t" \
  "ds_add_rtn_u32 v246, v251, v250 offset:47208\n\t" \
  "s_mov_b64 exec, -1\n\t" \
  "ds_read_b32 v234, v234 offset:0\n\t" \
  "v_cvt_pk_f16_f32 v224, |v154|, |v155| clamp\n\t" \
  "v_cvt_pknorm_i16_f32 v226, |v152|, |v153|\n\t" \
  "v_mfma_f32_16x16x32_bf16 v[164:167], v[208:211], v[104:107], 0\n\t" \
  "v_and_b32_sdwa v221, s56, v220 dst_sel:DWORD dst_unused:UNUSED_PAD src0_sel:DWORD src1_sel:WORD_1\n\t" \
  "v_and_b32 v220, s56, v220\n\t" \
  "v_mfma_f32_16x16x32_bf16 v[172:175], v[208:211], v[108:111], 0\n\t" \
  "s_waitcnt lgkmcnt(1)\n\t" \
  "v_readfirstlane_b32 s4, v246\n\t" \
  "s_cmp_lt_u32 s4, s57\n\t" \
  "s_cbranch_scc1 .Lcnx_w96\n.Lcnx_b96:\n\t" \
  "s_cmp_lg_u32 s58, 1\n\t" \
  "s_cselect_b32 s57, s57, 0\n\t" \
  "ds_read_b64_tr_b16 v[208:209], v249 offset:6152\n\t" \
  "v_lshlrev_b32_sdwa v223, 2, v222 dst_sel:DWORD dst_unused:UNUSED_PAD src0_sel:DWORD src1_sel:BYTE_3\n\t" \
  "v_lshlrev_b32_sdwa v222, 2, v222 dst_sel:DWORD dst_unused:UNUSED_PAD src0_sel:DWORD src1_sel:BYTE_1\n\t" \
  "v_and_b32_sdwa v225, s56, v224 dst_sel:DWORD dst_unused:UNUSED_PAD src0_sel:DWORD src1_sel:WORD_1\n\t" \
  "v_mfma_f32_16x16x32_bf16 v[148:151], v[212:215], v[112:115], v[148:151]\n\t" \
  "ds_read_b64_tr_b16 v[210:211], v249 offset:7688\n\t" \
  "v_and_b32 v224, s56, v224\n\t" \
  "v_lshlrev_b32_sdwa v227, 2, v226 dst_sel:DWORD dst_unused:UNUSED_PAD src0_sel:DWORD src1_sel:BYTE_3\n\t" \
  "v_mfma_f32_16x16x32_bf16 v[156:159], v[212:215], v[116:119], v[156:159]\n\t" \
  "s_cmp_eq_u32 s37, 0\n\t" \
  "s_cbranch_scc1 .Lcnx_f9\n.Lcnx_g9:\n\t" \
  "ds_read_b32 v221, v221 offset:0\n\t" \
  "ds_read_b32 v220, v220 offset:0\n\t" \
  "v_lshlrev_b32_sdwa v226, 2, v226 dst_sel:DWORD dst_unused:UNUSED_PAD src0_sel:DWORD src1_sel:BYTE_1\n\t" \
  "v_fma_f32 v197, |v197|, v229, v197 mul:4\n\t" \
  "v_mfma_f32_16x16x32_bf16 v[164:167], v[212:215], v[120:123], v[164:167]\n\t" \
  "ds_read_b32 v223, v223 offset:15376\n\t" \
  "ds_read_b32 v222, v222 offset:15376\n\t" \
  "v_fma_f32 v196, |v196|, v228, v196 mul:4\n\t" \
  "v_fma_mix_f32 v231, |v199|, v231, v231 op_sel:[0,1,0] op_sel_hi:[0,1,1] clamp\n\t" \
  "v_mfma_f32_16x16x32_bf16 v[172:175], v[212:215], v[124:127], v[172:175]\n\t" \
  "ds_read_b64_tr_b16 v[212:213], v249 offset:6160\n\t" \
  "ds_read_b64_tr_b16 v[214:215], v249 offset:7696\n\t" \
  "v_fma_mix_f32 v230, |v198|, v230, v230 op_sel:[0,1,0] op_sel_hi:[0,1,1] clamp\n\t" \
  "s_waitcnt lgkmcnt(8)\n\t" \
  "v_fma_f32 v205, |v205|, v233, v205 mul:4\n\t" \
  "v_mfma_f32_16x16x32_bf16 v[148:151], v[216:219], v[128:131], v[148:151]\n\t" \
  "ds_read_b32 v225, v225 offset:0\n\t" \
  "v_fma_f32 v204, |v204|, v232, v204 mul:4\n\t" \
  "v_fma_f32 v207, |v207|, v235, v207 mul:4\n\t" \
  "v_mfma_f32_16x16x32_bf16 v[156:159], v[216:219], v[132:135], v[156:159]\n\t" \
  "ds_read_b32 v224, v224 offset:0\n\t" \
  "v_fma_f32 v206, |v206|, v234, v206 mul:4\n\t" \
  "v_fma_f32 v199, |v199|, v231, v199 mul:4\n\t" \
  "v_mfma_f32_16x16x32_bf16 v[164:167], v[216:219], v[136:139], v[164:167]\n\t" \
  "ds_read_b32 v227, v227 offset:15376\n\t" \
  "v_fma_f32 v198, |v198|, v230, v198 mul:4\n\t" \
  "v_cvt_pk_bf16_f32 v196, v196, v197\n\t" \
  "v_mfma_f32_16x16x32_bf16 v[172:175], v[216:219], v[140:143], v[172:175]\n\t" \
  "ds_read_b64_tr_b16 v[216:217], v249 offset:6216\n\t" \
  "v_cvt_pk_bf16_f32 v197, v198, v199\n\t" \
  "v_cvt_pk_bf16_f32 v204, v204, v205\n\t" \
  "v_cvt_pk_bf16_f32 v205, v206, v207\n\t" \
  "s_cmp_lg_u32 s58, 1\n\t" \
  "s_cbranch_scc1 .Lcnx_c17\n\t" \
  "s_mov_b64 vcc, -1\n\t" \
  "s_setprio 0\n\t" \
  "s_cmp_ge_u32 s39, 2048\n\t" \
  "s_cbranch_scc1 .Lcnx_c17\n\t" \
  "s_setprio 3\n\t" \
  ".Lcnx_c17:\n\t" \
  "s_cbranch_vccnz .Lcnx_p9_0\n\ts_setprio 0\n.Lcnx_p9_0:\n\t" \
  "v_cvt_pk_f16_f32 v228, |v162|, |v163| clamp\n\t" \
  "v_cvt_pknorm_i16_f32 v230, |v160|, |v161|\n\t" \
  "v_mfma_f32_16x16x32_bf16 v[56:59], v[242:245], v[236:239], v[56:59]\n\t" \
  "ds_read_b64_tr_b16 v[218:219], v249 offset:7752\n\t" \
  "v_cvt_pk_f16_f32 v232, |v170|, |v171| clamp\n\t" \
  "v_cvt_pk_f16_f32 v234, |v168|, |v169| clamp\n\t" \
  "v_mfma_f32_16x16x32_bf16 v[60:63], v[240:243], v[236:239], v[60:63]\n\t" \
  "buffer_load_dwordx4 v[236:239], v250, s[8:11], s12 offen offset:128\n\t" \
  "s_cmp_lg_u32 s58, 0\n\t" \
  "s_cbranch_scc1 .Lcnx_c29\n\t" \
  "s_add_u32 m0, s25, 57400\n\t" \
  "s_add_u32 s24, s24, s26\n\t" \
  "buffer_load_dwordx4 v254, s[8:11], s24 offen lds\n\t" \
  "s_cmp_lg_u32 s38, 5\n\t" \
  "s_cbranch_scc1 .Lcnx_c29\n\t" \
  "s_mov_b32 m0, 163632\n\t" \
  "s_add_u32 s30, s30, s31\n\t" \
  "buffer_load_dword v252, s[8:11], s30 offen lds\n\t" \
  ".Lcnx_c29:\n\t" \
  "ds_read_b32 v226, v226 offset:15376\n\t" \
  "v_and_b32_sdwa v229, s56, v228 dst_sel:DWORD dst_unused:UNUSED_PAD src0_sel:DWORD src1_sel:WORD_1\n\t" \
  "v_and_b32 v228, s56, v228\n\t" \
  "s_waitcnt lgkmcnt(12)\n\t" \
  "v_mfma_f32_16x16x32_bf16 v[0:3], v[208:211], v[178:181], v[0:3]\n\t" \
  "ds_read_b32 v229, v229 offset:0\n\t" \
  "v_lshlrev_b32_sdwa v231, 2, v230 dst_sel:DWORD dst_unused:UNUSED_PAD src0_sel:DWORD src1_sel:BYTE_3\n\t" \
  "v_lshlrev_b32_sdwa v230, 2, v230 dst_sel:DWORD dst_unused:UNUSED_PAD src0_sel:DWORD src1_sel:BYTE_1\n\t" \
  "v_mfma_f32_16x16x32_bf16 v[24:27], v[208:211], v[186:189], v[24:27]\n\t" \
  "ds_read_b32 v228, v228 offset:0\n\t" \
  "v_and_b32_sdwa v233, s56, v232 dst_sel:DWORD dst_unused:UNUSED_PAD src0_sel:DWORD src1_sel:WORD_1\n\t" \
  "v_and_b32 v232, s56, v232\n\t" \
  "v_mfma_f32_16x16x32_bf16 v[48:51], v[208:211], v[194:197], v[48:51]\n\t" \
  "ds_read_b32 v231, v231 offset:15376\n\t" \
  "v_and_b32_sdwa v235, s56, v234 dst_sel:DWORD dst_unused:UNUSED_PAD src0_sel:DWORD src1_sel:WORD_1\n\t" \
  "v_and_b32 v234, s56, v234\n\t" \
  "v_mfma_f32_16x16x32_bf16 v[72:75], v[208:211], v[202:205], v[72:75]\n\t" \
  "ds_read_b64_tr_b16 v[208:209], v249 offset:6224\n\t" \
  "ds_read_b64_tr_b16 v[210:211], v249 offset:7760\n\t" \
  "s_waitcnt lgkmcnt(13)\n\t" \
  "v_fma_f32 v147, |v147|, v221, v147 mul:4\n\t" \
  "v_fma_f32 v146, |v146|, v220, v146 mul:4\n\t" \
  "s_waitcnt lgkmcnt(11)\n\t" \
  "v_mfma_f32_16x16x32_bf16 v[4:7], v[212:215], v[178:181], v[4:7]\n\t" \
  "ds_read_b32 v230, v230 offset:15376\n\t" \
  "v_fma_mix_f32 v223, |v145|, v223, v223 op_sel:[0,1,0] op_sel_hi:[0,1,1] clamp\n\t" \
  "v_fma_mix_f32 v222, |v144|, v222, v222 op_sel:[0,1,0] op_sel_hi:[0,1,1] clamp\n\t" \
  "v_mfma_f32_16x16x32_bf16 v[28:31], v[212:215], v[186:189], v[28:31]\n\t" \
  "ds_read_b32 v233, v233 offset:0\n\t" \
  "s_waitcnt lgkmcnt(7)\n\t" \
  "v_fma_f32 v155, |v155|, v225, v155 mul:4\n\t" \
  "v_fma_f32 v154, |v154|, v224, v154 mul:4\n\t" \
  "v_mfma_f32_16x16x32_bf16 v[52:55], v[212:215], v[194:197], v[52:55]\n\t" \
  "ds_read_b32 v232, v232 offset:0\n\t" \
  "v_fma_mix_f32 v227, |v153|, v227, v227 op_sel:[0,1,0] op_sel_hi:[0,1,1] clamp\n\t" \
  "v_fma_mix_f32 v226, |v152|, v226, v226 op_sel:[0,1,0] op_sel_hi:[0,1,1] clamp\n\t" \
  "v_fma_f32 v145, |v145|, v223, v145 mul:4\n\t" \
  "v_mfma_f32_16x16x32_bf16 v[76:79], v[212:215], v[202:205], v[76:79]\n\t" \
  "ds_read_b64_tr_b16 v[212:213], v249 offset:6280\n\t" \
  "v_fma_f32 v144, |v144|, v222, v144 mul:4\n\t" \
  "v_fma_f32 v153, |v153|, v227, v153 mul:4\n\t" \
  "v_mfma_f32_16x16x32_bf16 v[8:11], v[216:219], v[178:181], v[8:11]\n\t" \
  "ds_read_b64_tr_b16 v[214:215], v249 offset:7816\n\t" \
  "v_fma_f32 v152, |v152|, v226, v152 mul:4\n\t" \
  "v_cvt_pk_bf16_f32 v147, v146, v147\n\t" \
  "v_mfma_f32_16x16x32_bf16 v[32:35], v[216:219], v[186:189], v[32:35]\n\t" \
  "ds_read_b32 v235, v235 offset:0\n\t" \
  "v_cvt_pk_bf16_f32 v146, v144, v145\n\t" \
  "v_cvt_pk_bf16_f32 v155, v154, v155\n\t" \
  "v_mfma_f32_16x16x32_bf16 v[56:59], v[216:219], v[194:197], v[56:59]\n\t" \
  "ds_read_b32 v234, v234 offset:0\n\t" \
  "v_cvt_pk_bf16_f32 v154, v152, v153\n\t" \
  "s_cbranch_vccnz .Lcnx_p9_1\n\ts_setprio 1\n.Lcnx_p9_1:\n\t" \
  "v_cvt_pk_f16_f32 v220, |v148|, |v149| clamp\n\t" \
  "v_mfma_f32_16x16x32_bf16 v[80:83], v[216:219], v[202:205], v[80:83]\n\t" \
  "ds_read_b64_tr_b16 v[216:217], v249 offset:6288\n\t" \
  "v_cvt_pknorm_i16_f32 v222, |v150|, |v151|\n\t" \
  "v_cvt_pk_f16_f32 v224, |v156|, |v157| clamp\n\t" \
  "s_waitcnt lgkmcnt(8)\n\t" \
  "v_mfma_f32_16x16x32_bf16 v[12:15], v[208:211], v[178:181], v[12:15]\n\t" \
  "ds_read_b64_tr_b16 v[218:219], v249 offset:7824\n\t" \
  "v_cvt_pknorm_i16_f32 v226, |v158|, |v159|\n\t" \
  "v_and_b32_sdwa v221, s56, v220 dst_sel:DWORD dst_unused:UNUSED_PAD src0_sel:DWORD src1_sel:WORD_1\n\t" \
  "v_mfma_f32_16x16x32_bf16 v[36:39], v[208:211], v[186:189], v[36:39]\n\t" \
  "v_and_b32 v220, s56, v220\n\t" \
  "v_lshlrev_b32_sdwa v223, 2, v222 dst_sel:DWORD dst_unused:UNUSED_PAD src0_sel:DWORD src1_sel:BYTE_3\n\t" \
  "v_mfma_f32_16x16x32_bf16 v[60:63], v[208:211], v[194:197], v[60:63]\n\t" \
  "ds_read_b32 v221, v221 offset:0\n\t" \
  "v_lshlrev_b32_sdwa v222, 2, v222 dst_sel:DWORD dst_unused:UNUSED_PAD src0_sel:DWORD src1_sel:BYTE_1\n\t" \
  "v_and_b32_sdwa v225, s56, v224 dst_sel:DWORD dst_unused:UNUSED_PAD src0_sel:DWORD src1_sel:WORD_1\n\t" \
  "v_and_b32 v224, s56, v224\n\t" \
  "v_mfma_f32_16x16x32_bf16 v[84:87], v[208:211], v[202:205], v[84:87]\n\t" \
  "ds_read_b64_tr_b16 v[208:209], v248 offset:49192\n\t" \
  "ds_read_b64_tr_b16 v[210:211], v248 offset:50216\n\t" \
  "v_lshlrev_b32_sdwa v227, 2, v226 dst_sel:DWORD dst_unused:UNUSED_PAD src0_sel:DWORD src1_sel:BYTE_3\n\t" \
  "v_lshlrev_b32_sdwa v226, 2, v226 dst_sel:DWORD dst_unused:UNUSED_PAD src0_sel:DWORD src1_sel:BYTE_1\n\t" \
  "s_waitcnt lgkmcnt(7)\n\t" \
  "v_mfma_f32_16x16x32_bf16 v[16:19], v[212:215], v[178:181], v[16:19]\n\t" \
  "ds_read_b32 v220, v220 offset:0\n\t" \
  "ds_read_b32 v223, v223 offset:15376\n\t" \
  "v_fma_f32 v163, |v163|, v229, v163 mul:4\n\t" \
  "v_fma_f32 v162, |v162|, v228, v162 mul:4\n\t" \
  "v_mfma_f32_16x16x32_bf16 v[40:43], v[212:215], v[186:189], v[40:43]\n\t" \
  "ds_read_b32 v222, v222 offset:15376\n\t" \
  "v_fma_mix_f32 v231, |v161|, v231, v231 op_sel:[0,1,0] op_sel_hi:[0,1,1] clamp\n\t" \
  "v_fma_mix_f32 v230, |v160|, v230, v230 op_sel:[0,1,0] op_sel_hi:[0,1,1] clamp\n\t" \
  "v_mfma_f32_16x16x32_bf16 v[64:67], v[212:215], v[194:197], v[64:67]\n\t" \
  "ds_read_b32 v225, v225 offset:0\n\t" \
  "s_waitcnt lgkmcnt(9)\n\t" \
  "v_fma_f32 v171, |v171|, v233, v171 mul:4\n\t" \
  "v_fma_f32 v170, |v170|, v232, v170 mul:4\n\t" \
  "v_mfma_f32_16x16x32_bf16 v[88:91], v[212:215], v[202:205], v[88:91]\n\t" \
  "ds_read_b64_tr_b16 v[212:213], v248 offset:52264\n\t" \
  "v_fma_f32 v169, |v169|, v235, v169 mul:4\n\t" \
  "v_fma_f32 v168, |v168|, v234, v168 mul:4\n\t" \
  "s_waitcnt lgkmcnt(8)\n\t" \
  "v_mfma_f32_16x16x32_bf16 v[20:23], v[216:219], v[178:181], v[20:23]\n\t" \
  "ds_read_b64_tr_b16 v[214:215], v248 offset:53288\n\t" \
  "v_fma_f32 v161, |v161|, v231, v161 mul:4\n\t" \
  "v_fma_f32 v160, |v160|, v230, v160 mul:4\n\t" \
  "v_mfma_f32_16x16x32_bf16 v[44:47], v[216:219], v[186:189], v[44:47]\n\t" \
  "ds_read_b32 v224, v224 offset:0\n\t" \
  "v_cvt_pk_bf16_f32 v163, v162, v163\n\t" \
  "v_cvt_pk_bf16_f32 v162, v160, v161\n\t" \
  "v_mfma_f32_16x16x32_bf16 v[68:71], v[216:219], v[194:197], v[68:71]\n\t" \
  "ds_read_b32 v227, v227 offset:15376\n\t" \
  "v_cvt_pk_bf16_f32 v171, v170, v171\n\t" \
  "v_cvt_pk_bf16_f32 v170, v168, v169\n\t" \
  "v_cvt_pk_f16_f32 v228, |v164|, |v165| clamp\n\t" \
  "v_mfma_f32_16x16x32_bf16 v[92:95], v[216:219], v[202:205], v[92:95]\n\t" \
  "ds_read_b64_tr_b16 v[216:217], v248 offset:51240\n\t" \
  "v_cvt_pknorm_i16_f32 v230, |v166|, |v167|\n\t" \
  "s_cbranch_vccnz .Lcnx_p9_2\n\ts_setprio 2\n.Lcnx_p9_2:\n\t" \
  "v_cvt_pk_f16_f32 v232, |v172|, |v173| clamp\n\t" \
  "s_waitcnt lgkmcnt(9)\n\t" \
  "v_mfma_f32_16x16x32_bf16 v[176:179], v[208:211], v[96:99], 0\n\t" \
  "ds_read_b64_tr_b16 v[218:219], v248 offset:54312\n\t" \
  "v_cvt_pk_f16_f32 v234, |v174|, |v175| clamp\n\t" \
  "v_and_b32_sdwa v229, s56, v228 dst_sel:DWORD dst_unused:UNUSED_PAD src0_sel:DWORD src1_sel:WORD_1\n\t" \
  "v_mfma_f32_16x16x32_bf16 v[184:187], v[208:211], v[100:103], 0\n\t" \
  "ds_read_b32 v226, v226 offset:15376\n\t" \
  "v_and_b32 v228, s56, v228\n\t" \
  "v_lshlrev_b32_sdwa v231, 2, v230 dst_sel:DWORD dst_unused:UNUSED_PAD src0_sel:DWORD src1_sel:BYTE_3\n\t" \
  "v_mfma_f32_16x16x32_bf16 v[192:195], v[208:211], v[104:107], 0\n\t" \
  "ds_read_b32 v229, v229 offset:0\n\t" \
  "v_lshlrev_b32_sdwa v230, 2, v230 dst_sel:DWORD dst_unused:UNUSED_PAD src0_sel:DWORD src1_sel:BYTE_1\n\t" \
  "v_and_b32_sdwa v233, s56, v232 dst_sel:DWORD dst_unused:UNUSED_PAD src0_sel:DWORD src1_sel:WORD_1\n\t" \
  "v_mfma_f32_16x16x32_bf16 v[200:203], v[208:211], v[108:111], 0\n\t" \
  "ds_read_b64_tr_b16 v[208:209], v248 offset:49704\n\t" \
  "v_and_b32 v232, s56, v232\n\t" \
  "v_and_b32_sdwa v235, s56, v234 dst_sel:DWORD dst_unused:UNUSED_PAD src0_sel:DWORD src1_sel:WORD_1\n\t" \
  "s_waitcnt lgkmcnt(7)\n\t" \
  "v_mfma_f32_16x16x32_bf16 v[176:179], v[212:215], v[112:115], v[176:179]\n\t" \
  "ds_read_b64_tr_b16 v[210:211], v248 offset:50728\n\t" \
  "ds_read_b32 v228, v228 offset:0\n\t" \
  "v_and_b32 v234, s56, v234\n\t" \
  "v_fma_f32 v149, |v149|, v221, v149 mul:4\n\t" \
  "v_mfma_f32_16x16x32_bf16 v[184:187], v[212:215], v[116:119], v[184:187]\n\t" \
  "ds_read_b32 v231, v231 offset:15376\n\t" \
  "ds_read_b32 v230, v230 offset:15376\n\t" \
  "v_fma_f32 v148, |v148|, v220, v148 mul:4\n\t" \
  "v_fma_mix_f32 v223, |v151|, v223, v223 op_sel:[0,1,0] op_sel_hi:[0,1,1] clamp\n\t" \
  "v_mfma_f32_16x16x32_bf16 v[192:195], v[212:215], v[120:123], v[192:195]\n\t" \
  "ds_read_b32 v233, v233 offset:0\n\t" \
  "v_fma_mix_f32 v222, |v150|, v222, v222 op_sel:[0,1,0] op_sel_hi:[0,1,1] clamp\n\t" \
  "s_waitcnt lgkmcnt(7)\n\t" \
  "v_fma_f32 v157, |v157|, v225, v157 mul:4\n\t" \
  "v_mfma_f32_16x16x32_bf16 v[200:203], v[212:215], v[124:127], v[200:203]\n\t" \
  "ds_read_b64_tr_b16 v[212:213], v248 offset:52776\n\t" \
  "v_fma_f32 v156, |v156|, v224, v156 mul:4\n\t" \
  "v_fma_mix_f32 v227, |v159|, v227, v227 op_sel:[0,1,0] op_sel_hi:[0,1,1] clamp\n\t" \
  "v_fma_mix_f32 v226, |v158|, v226, v226 op_sel:[0,1,0] op_sel_hi:[0,1,1] clamp\n\t" \
  "v_mfma_f32_16x16x32_bf16 v[176:179], v[216:219], v[128:131], v[176:179]\n\t" \
  "ds_read_b64_tr_b16 v[214:215], v248 offset:53800\n\t" \
  "v_fma_f32 v151, |v151|, v223, v151 mul:4\n\t" \
  "v_fma_f32 v150, |v150|, v222, v150 mul:4\n\t" \
  "v_mfma_f32_16x16x32_bf16 v[184:187], v[216:219], v[132:135], v[184:187]\n\t" \
  "ds_read_b32 v232, v232 offset:0\n\t" \
  "v_fma_f32 v159, |v159|, v227, v159 mul:4\n\t" \
  "v_fma_f32 v158, |v158|, v226, v158 mul:4\n\t" \
  "v_mfma_f32_16x16x32_bf16 v[192:195], v[216:219], v[136:139], v[192:195]\n\t" \
  "ds_read_b32 v235, v235 offset:0\n\t" \
  "v_cvt_pk_bf16_f32 v148, v148, v149\n\t" \
  "v_cvt_pk_bf16_f32 v149, v150, v151\n\t" \
  "v_mfma_f32_16x16x32_bf16 v[200:203], v[216:219], v[140:143], v[200:203]\n\t" \
  "ds_read_b64_tr_b16 v[216:217], v248 offset:51752\n\t" \
  "v_cvt_pk_bf16_f32 v156, v156, v157\n\t" \
  "v_cvt_pk_bf16_f32 v157, v158, v159\n\t" \
  "s_waitcnt lgkmcnt(9)\n\t" \
  "v_mfma_f32_16x16x32_bf16 v[180:183], v[208:211], v[96:99], 0\n\t" \
  "ds_read_b64_tr_b16 v[218:219], v248 offset:54824\n\t" \
  "v_cvt_pk_f16_f32 v220, |v178|, |v179| clamp\n\t" \
  "s_cbranch_vccnz .Lcnx_p9_3\n\ts_setprio 3\n.Lcnx_p9_3:\n\t" \
  "v_cvt_pknorm_i16_f32 v222, |v176|, |v177|\n\t" \
  "v_mfma_f32_16x16x32_bf16 v[188:191], v[208:211], v[100:103], 0\n\t" \
  "s_waitcnt vmcnt(1)\n\t" \
  "s_cmp_lg_u32 s58, 1\n\t" \
  "s_cbranch_scc1 .Lcnx_c18\n\t" \
  "s_waitcnt vmcnt(0)\n\t" \
  ".Lcnx_c18:\n\t" \
  "s_mov_b64 exec, 3\n\t" \
  "ds_add_rtn_u32 v246, v251, v250 offset:47216\n\t" \
  "s_mov_b64 exec, -1\n\t" \
  "ds_read_b32 v234, v234 offset:0\n\t" \
  "v_cvt_pk_f16_f32 v224, |v186|, |v187| clamp\n\t" \
  "v_cvt_pknorm_i16_f32 v226, |v184|, |v185|\n\t" \
  "v_mfma_f32_16x16x32_bf16 v[196:199], v[208:211], v[104:107], 0\n\t" \
  "v_and_b32_sdwa v221, s56, v220 dst_sel:DWORD dst_unused:UNUSED_PAD src0_sel:DWORD src1_sel:WORD_1\n\t" \
  "v_and_b32 v220, s56, v220\n\t" \
  "v_mfma_f32_16x16x32_bf16 v[204:207], v[208:211], v[108:111], 0\n\t" \
  "s_waitcnt lgkmcnt(1)\n\t" \
  "v_readfirstlane_b32 s4, v246\n\t" \
  "s_cmp_lt_u32 s4, s57\n\t" \
  "s_cbranch_scc1 .Lcnx_w108\n.Lcnx_b108:\n\t" \
  "v_add_u32 v248, s59, v248\n\t" \
  "v_add_u32 v249, s60, v249\n\t" \
  "ds_read_b64_tr_b16 v[208:209], v249 offset:24600\n\t" \
  "v_lshlrev_b32_sdwa v223, 2, v222 dst_sel:DWORD dst_unused:UNUSED_PAD src0_sel:DWORD src1_sel:BYTE_3\n\t" \
  "v_lshlrev_b32_sdwa v222, 2, v222 dst_sel:DWORD dst_unused:UNUSED_PAD src0_sel:DWORD src1_sel:BYTE_1\n\t" \
  "v_and_b32_sdwa v225, s56, v224 dst_sel:DWORD dst_unused:UNUSED_PAD src0_sel:DWORD src1_sel:WORD_1\n\t" \
  "v_mfma_f32_16x16x32_bf16 v[180:183], v[212:215], v[112:115], v[180:183]\n\t" \
  "ds_read_b64_tr_b16 v[210:211], v249 offset:26136\n\t" \
  "v_and_b32 v224, s56, v224\n\t" \
  "v_lshlrev_b32_sdwa v227, 2, v226 dst_sel:DWORD dst_unused:UNUSED_PAD src0_sel:DWORD src1_sel:BYTE_3\n\t" \
  "v_mfma_f32_16x16x32_bf16 v[188:191], v[212:215], v[116:119], v[188:191]\n\t" \
  "s_cmp_eq_u32 s37, 0\n\t" \
  "s_cbranch_scc1 .Lcnx_f10\n.Lcnx_g10:\n\t" \
  "ds_read_b32 v221, v221 offset:0\n\t" \
  "ds_read_b32 v220, v220 offset:0\n\t" \
  "v_lshlrev_b32_sdwa v226, 2, v226 dst_sel:DWORD dst_unused:UNUSED_PAD src0_sel:DWORD src1_sel:BYTE_1\n\t" \
  "v_fma_f32 v165, |v165|, v229, v165 mul:4\n\t" \
  "v_mfma_f32_16x16x32_bf16 v[196:199], v[212:215], v[120:123], v[196:199]\n\t" \
  "ds_read_b32 v223, v223 offset:15376\n\t" \
  "ds_read_b32 v222, v222 offset:15376\n\t" \
  "v_fma_f32 v164, |v164|, v228, v164 mul:4\n\t" \
  "v_fma_mix_f32 v231, |v167|, v231, v231 op_sel:[0,1,0] op_sel_hi:[0,1,1] clamp\n\t" \
  "v_mfma_f32_16x16x32_bf16 v[204:207], v[212:215], v[124:127], v[204:207]\n\t" \
  "ds_read_b64_tr_b16 v[212:213], v249 offset:24608\n\t" \
  "ds_read_b64_tr_b16 v[214:215], v249 offset:26144\n\t" \
  "v_fma_mix_f32 v230, |v166|, v230, v230 op_sel:[0,1,0] op_sel_hi:[0,1,1] clamp\n\t" \
  "s_waitcnt lgkmcnt(8)\n\t" \
  "v_fma_f32 v173, |v173|, v233, v173 mul:4\n\t" \
  "v_mfma_f32_16x16x32_bf16 v[180:183], v[216:219], v[128:131], v[180:183]\n\t" \
  "ds_read_b32 v225, v225 offset:0\n\t" \
  "v_fma_f32 v172, |v172|, v232, v172 mul:4\n\t" \
  "v_fma_f32 v175, |v175|, v235, v175 mul:4\n\t" \
  "v_mfma_f32_16x16x32_bf16 v[188:191], v[216:219], v[132:135], v[188:191]\n\t" \
  "ds_read_b32 v224, v224 offset:0\n\t" \
  "v_fma_f32 v174, |v174|, v234, v174 mul:4\n\t" \
  "v_fma_f32 v167, |v167|, v231, v167 mul:4\n\t" \
  "v_mfma_f32_16x16x32_bf16 v[196:199], v[216:219], v[136:139], v[196:199]\n\t" \
  "ds_read_b32 v227, v227 offset:15376\n\t" \
  "v_fma_f32 v166, |v166|, v230, v166 mul:4\n\t" \
  "v_cvt_pk_bf16_f32 v164, v164, v165\n\t" \
  "v_mfma_f32_16x16x32_bf16 v[204:207], v[216:219], v[140:143], v[204:207]\n\t" \
  "ds_read_b64_tr_b16 v[216:217], v249 offset:24664\n\t" \
  "v_cvt_pk_bf16_f32 v165, v166, v167\n\t" \
  "v_cvt_pk_bf16_f32 v172, v172, v173\n\t" \
  "v_cvt_pk_bf16_f32 v173, v174, v175\n\t" \
  "s_cbranch_vccnz .Lcnx_p10_0\n\ts_setprio 0\n.Lcnx_p10_0:\n\t" \
  "v_cvt_pk_f16_f32 v228, |v194|, |v195| clamp\n\t" \
  "v_cvt_pknorm_i16_f32 v230, |v192|, |v193|\n\t" \
  "v_mfma_f32_16x16x32_bf16 v[64:67], v[242:245], v[236:239], v[64:67]\n\t" \
  "ds_read_b64_tr_b16 v[218:219], v249 offset:26200\n\t" \
  "v_cvt_pk_f16_f32 v232, |v202|, |v203| clamp\n\t" \
  "v_cvt_pk_f16_f32 v234, |v200|, |v201| clamp\n\t" \
  "v_mfma_f32_16x16x32_bf16 v[68:71], v[240:243], v[236:239], v[68:71]\n\t" \
  "buffer_load_dwordx4 v[236:239], v250, s[8:11], s13 offen offset:0\n\t" \
  "s_cmp_lg_u32 s58, 0\n\t" \
  "s_cbranch_scc1 .Lcnx_c30\n\t" \
  "s_add_u32 m0, s25, 65600\n\t" \
  "s_add_u32 s24, s24, s26\n\t" \
  "buffer_load_dwordx4 v254, s[8:11], s24 offen lds\n\t" \
  "s_cmp_lg_u32 s38, 6\n\t" \
  "s_cbranch_scc1 .Lcnx_c30\n\t" \
  "s_mov_b32 m0, 163632\n\t" \
  "s_add_u32 s30, s30, s31\n\t" \
  "buffer_load_dword v252, s[8:11], s30 offen lds\n\t" \
  ".Lcnx_c30:\n\t" \
  "ds_read_b32 v226, v226 offset:15376\n\t" \
  "v_and_b32_sdwa v229, s56, v228 dst_sel:DWORD dst_unused:UNUSED_PAD src0_sel:DWORD src1_sel:WORD_1\n\t" \
  "v_and_b32 v228, s56, v228\n\t" \
  "s_waitcnt lgkmcnt(12)\n\t" \
  "v_mfma_f32_16x16x32_bf16 v[0:3], v[208:211], v[146:149], v[0:3]\n\t" \
  "ds_read_b32 v229, v229 offset:0\n\t" \
  "v_lshlrev_b32_sdwa v231, 2, v230 dst_sel:DWORD dst_unused:UNUSED_PAD src0_sel:DWORD src1_sel:BYTE_3\n\t" \
  "v_lshlrev_b32_sdwa v230, 2, v230 dst_sel:DWORD dst_unused:UNUSED_PAD src0_sel:DWORD src1_sel:BYTE_1\n\t" \
  "v_mfma_f32_16x16x32_bf16 v[24:27], v[208:211], v[154:157], v[24:27]\n\t" \
  "ds_read_b32 v228, v228 offset:0\n\t" \
  "v_and_b32_sdwa v233, s56, v232 dst_sel:DWORD dst_unused:UNUSED_PAD src0_sel:DWORD src1_sel:WORD_1\n\t" \
  "v_and_b32 v232, s56, v232\n\t" \
  "v_mfma_f32_16x16x32_bf16 v[48:51], v[208:211], v[162:165], v[48:51]\n\t" \
  "ds_read_b32 v231, v231 offset:15376\n\t" \
  "v_and_b32_sdwa v235, s56, v234 dst_sel:DWORD dst_unused:UNUSED_PAD src0_sel:DWORD src1_sel:WORD_1\n\t" \
  "v_and_b32 v234, s56, v234\n\t" \
  "v_mfma_f32_16x16x32_bf16 v[72:75], v[208:211], v[170:173], v[72:75]\n\t" \
  "ds_read_b64_tr_b16 v[208:209], v249 offset:24672\n\t" \
  "ds_read_b64_tr_b16 v[210:211], v249 offset:26208\n\t" \
  "s_waitcnt lgkmcnt(13)\n\t" \
  "v_fma_f32 v179, |v179|, v221, v179 mul:4\n\t" \
  "v_fma_f32 v178, |v178|, v220, v178 mul:4\n\t" \
  "s_waitcnt lgkmcnt(11)\n\t" \
  "v_mfma_f32_16x16x32_bf16 v[4:7], v[212:215], v[146:149], v[4:7]\n\t" \
  "ds_read_b32 v230, v230 offset:15376\n\t" \
  "v_fma_mix_f32 v223, |v177|, v223, v223 op_sel:[0,1,0] op_sel_hi:[0,1,1] clamp\n\t" \
  "v_fma_mix_f32 v222, |v176|, v222, v222 op_sel:[0,1,0] op_sel_hi:[0,1,1] clamp\n\t" \
  "v_mfma_f32_16x16x32_bf16 v[28:31], v[212:215], v[154:157], v[28:31]\n\t" \
  "ds_read_b32 v233, v233 offset:0\n\t" \
  "s_waitcnt lgkmcnt(7)\n\t" \
  "v_fma_f32 v187, |v187|, v225, v187 mul:4\n\t" \
  "v_fma_f32 v186, |v186|, v224, v186 mul:4\n\t" \
  "v_mfma_f32_16x16x32_bf16 v[52:55], v[212:215], v[162:165], v[52:55]\n\t" \
  "ds_read_b32 v232, v232 offset:0\n\t" \
  "v_fma_mix_f32 v227, |v185|, v227, v227 op_sel:[0,1,0] op_sel_hi:[0,1,1] clamp\n\t" \
  "v_fma_mix_f32 v226, |v184|, v226, v226 op_sel:[0,1,0] op_sel_hi:[0,1,1] clamp\n\t" \
  "v_fma_f32 v177, |v177|, v223, v177 mul:4\n\t" \
  "v_mfma_f32_16x16x32_bf16 v[76:79], v[212:215], v[170:173], v[76:79]\n\t" \
  "ds_read_b64_tr_b16 v[212:213], v249 offset:24728\n\t" \
  "v_fma_f32 v176, |v176|, v222, v176 mul:4\n\t" \
  "v_fma_f32 v185, |v185|, v227, v185 mul:4\n\t" \
  "v_mfma_f32_16x16x32_bf16 v[8:11], v[216:219], v[146:149], v[8:11]\n\t" \
  "ds_read_b64_tr_b16 v[214:215], v249 offset:26264\n\t" \
  "v_fma_f32 v184, |v184|, v226, v184 mul:4\n\t" \
  "v_cvt_pk_bf16_f32 v179, v178, v179\n\t" \
  "v_mfma_f32_16x16x32_bf16 v[32:35], v[216:219], v[154:157], v[32:35]\n\t" \
  "ds_read_b32 v235, v235 offset:0\n\t" \
  "v_cvt_pk_bf16_f32 v178, v176, v177\n\t" \
  "v_cvt_pk_bf16_f32 v187, v186, v187\n\t" \
  "v_mfma_f32_16x16x32_bf16 v[56:59], v[216:219], v[162:165], v[56:59]\n\t" \
  "ds_read_b32 v234, v234 offset:0\n\t" \
  "v_cvt_pk_bf16_f32 v186, v184, v185\n\t" \
  "s_cbranch_vccnz .Lcnx_p10_1\n\ts_setprio 1\n.Lcnx_p10_1:\n\t" \
  "v_cvt_pk_f16_f32 v220, |v180|, |v181| clamp\n\t" \
  "v_mfma_f32_16x16x32_bf16 v[80:83], v[216:219], v[170:173], v[80:83]\n\t" \
  "ds_read_b64_tr_b16 v[216:217], v249 offset:24736\n\t" \
  "v_cvt_pknorm_i16_f32 v222, |v182|, |v183|\n\t" \
  "v_cvt_pk_f16_f32 v224, |v188|, |v189| clamp\n\t" \
  "s_waitcnt lgkmcnt(8)\n\t" \
  "v_mfma_f32_16x16x32_bf16 v[12:15], v[208:211], v[146:149], v[12:15]\n\t" \
  "ds_read_b64_tr_b16 v[218:219], v249 offset:26272\n\t" \
  "v_cvt_pknorm_i16_f32 v226, |v190|, |v191|\n\t" \
  "v_and_b32_sdwa v221, s56, v220 dst_sel:DWORD dst_unused:UNUSED_PAD src0_sel:DWORD src1_sel:WORD_1\n\t" \
  "v_mfma_f32_16x16x32_bf16 v[36:39], v[208:211], v[154:157], v[36:39]\n\t" \
  "v_and_b32 v220, s56, v220\n\t" \
  "v_lshlrev_b32_sdwa v223, 2, v222 dst_sel:DWORD dst_unused:UNUSED_PAD src0_sel:DWORD src1_sel:BYTE_3\n\t" \
  "v_mfma_f32_16x16x32_bf16 v[60:63], v[208:211], v[162:165], v[60:63]\n\t" \
  "ds_read_b32 v221, v221 offset:0\n\t" \
  "v_lshlrev_b32_sdwa v222, 2, v222 dst_sel:DWORD dst_unused:UNUSED_PAD src0_sel:DWORD src1_sel:BYTE_1\n\t" \
  "v_and_b32_sdwa v225, s56, v224 dst_sel:DWORD dst_unused:UNUSED_PAD src0_sel:DWORD src1_sel:WORD_1\n\t" \
  "v_and_b32 v224, s56, v224\n\t" \
  "v_mfma_f32_16x16x32_bf16 v[84:87], v[208:211], v[170:173], v[84:87]\n\t" \
  "ds_read_b64_tr_b16 v[208:209], v248 offset:36904\n\t" \
  "ds_read_b64_tr_b16 v[210:211], v248 offset:37928\n\t" \
  "v_lshlrev_b32_sdwa v227, 2, v226 dst_sel:DWORD dst_unused:UNUSED_PAD src0_sel:DWORD src1_sel:BYTE_3\n\t" \
  "v_lshlrev_b32_sdwa v226, 2, v226 dst_sel:DWORD dst_unused:UNUSED_PAD src0_sel:DWORD src1_sel:BYTE_1\n\t" \
  "s_waitcnt lgkmcnt(7)\n\t" \
  "v_mfma_f32_16x16x32_bf16 v[16:19], v[212:215], v[146:149], v[16:19]\n\t" \
  "ds_read_b32 v220, v220 offset:0\n\t" \
  "ds_read_b32 v223, v223 offset:15376\n\t" \
  "v_fma_f32 v195, |v195|, v229, v195 mul:4\n\t" \
  "v_fma_f32 v194, |v194|, v228, v194 mul:4\n\t" \
  "v_mfma_f32_16x16x32_bf16 v[40:43], v[212:215], v[154:157], v[40:43]\n\t" \
  "ds_read_b32 v222, v222 offset:15376\n\t" \
  "v_fma_mix_f32 v231, |v193|, v231, v231 op_sel:[0,1,0] op_sel_hi:[0,1,1] clamp\n\t" \
  "v_fma_mix_f32 v230, |v192|, v230, v230 op_sel:[0,1,0] op_sel_hi:[0,1,1] clamp\n\t" \
  "v_mfma_f32_16x16x32_bf16 v[64:67], v[212:215], v[162:165], v[64:67]\n\t" \
  "ds_read_b32 v225, v225 offset:0\n\t" \
  "s_waitcnt lgkmcnt(9)\n\t" \
  "v_fma_f32 v203, |v203|, v233, v203 mul:4\n\t" \
  "v_fma_f32 v202, |v202|, v232, v202 mul:4\n\t" \
  "v_mfma_f32_16x16x32_bf16 v[88:91], v[212:215], v[170:173], v[88:91]\n\t" \
  "ds_read_b64_tr_b16 v[212:213], v248 offset:39976\n\t" \
  "v_fma_f32 v201, |v201|, v235, v201 mul:4\n\t" \
  "v_fma_f32 v200, |v200|, v234, v200 mul:4\n\t" \
  "s_waitcnt lgkmcnt(8)\n\t" \
  "v_mfma_f32_16x16x32_bf16 v[20:23], v[216:219], v[146:149], v[20:23]\n\t" \
  "ds_read_b64_tr_b16 v[214:215], v248 offset:41000\n\t" \
  "v_fma_f32 v193, |v193|, v231, v193 mul:4\n\t" \
  "v_fma_f32 v192, |v192|, v230, v192 mul:4\n\t" \
  "v_mfma_f32_16x16x32_bf16 v[44:47], v[216:219], v[154:157], v[44:47]\n\t" \
  "ds_read_b32 v224, v224 offset:0\n\t" \
  "v_cvt_pk_bf16_f32 v195, v194, v195\n\t" \
  "v_cvt_pk_bf16_f32 v194, v192, v193\n\t" \
  "v_mfma_f32_16x16x32_bf16 v[68:71], v[216:219], v[162:165], v[68:71]\n\t" \
  "ds_read_b32 v227, v227 offset:15376\n\t" \
  "v_cvt_pk_bf16_f32 v203, v202, v203\n\t" \
  "v_cvt_pk_bf16_f32 v202, v200, v201\n\t" \
  "v_cvt_pk_f16_f32 v228, |v196|, |v197| clamp\n\t" \
  "v_mfma_f32_16x16x32_bf16 v[92:95], v[216:219], v[170:173], v[92:95]\n\t" \
  "ds_read_b64_tr_b16 v[216:217], v248 offset:38952\n\t" \
  "v_cvt_pknorm_i16_f32 v230, |v198|, |v199|\n\t" \
  "s_cbranch_vccnz .Lcnx_p10_2\n\ts_setprio 2\n.Lcnx_p10_2:\n\t" \
  "v_cvt_pk_f16_f32 v232, |v204|, |v205| clamp\n\t" \
  "s_waitcnt lgkmcnt(9)\n\t" \
  "v_mfma_f32_16x16x32_bf16 v[144:147], v[208:211], v[96:99], 0\n\t" \
  "ds_read_b64_tr_b16 v[218:219], v248 offset:42024\n\t" \
  "v_cvt_pk_f16_f32 v234, |v206|, |v207| clamp\n\t" \
  "v_and_b32_sdwa v229, s56, v228 dst_sel:DWORD dst_unused:UNUSED_PAD src0_sel:DWORD src1_sel:WORD_1\n\t" \
  "v_mfma_f32_16x16x32_bf16 v[152:155], v[208:211], v[100:103], 0\n\t" \
  "ds_read_b32 v226, v226 offset:15376\n\t" \
  "v_and_b32 v228, s56, v228\n\t" \
  "v_lshlrev_b32_sdwa v231, 2, v230 dst_sel:DWORD dst_unused:UNUSED_PAD src0_sel:DWORD src1_sel:BYTE_3\n\t" \
  "v_mfma_f32_16x16x32_bf16 v[160:163], v[208:211], v[104:107], 0\n\t" \
  "ds_read_b32 v229, v229 offset:0\n\t" \
  "v_lshlrev_b32_sdwa v230, 2, v230 dst_sel:DWORD dst_unused:UNUSED_PAD src0_sel:DWORD src1_sel:BYTE_1\n\t" \
  "v_and_b32_sdwa v233, s56, v232 dst_sel:DWORD dst_unused:UNUSED_PAD src0_sel:DWORD src1_sel:WORD_1\n\t" \
  "v_mfma_f32_16x16x32_bf16 v[168:171], v[208:211], v[108:111], 0\n\t" \
  "ds_read_b64_tr_b16 v[208:209], v248 offset:37416\n\t" \
  "v_and_b32 v232, s56, v232\n\t" \
  "v_and_b32_sdwa v235, s56, v234 dst_sel:DWORD dst_unused:UNUSED_PAD src0_sel:DWORD src1_sel:WORD_1\n\t" \
  "s_waitcnt lgkmcnt(7)\n\t" \
  "v_mfma_f32_16x16x32_bf16 v[144:147], v[212:215], v[112:115], v[144:147]\n\t" \
  "ds_read_b64_tr_b16 v[210:211], v248 offset:38440\n\t" \
  "ds_read_b32 v228, v228 offset:0\n\t" \
  "v_and_b32 v234, s56, v234\n\t" \
  "v_fma_f32 v181, |v181|, v221, v181 mul:4\n\t" \
  "v_mfma_f32_16x16x32_bf16 v[152:155], v[212:215], v[116:119], v[152:155]\n\t" \
  "ds_read_b32 v231, v231 offset:15376\n\t" \
  "ds_read_b32 v230, v230 offset:15376\n\t" \
  "v_fma_f32 v180, |v180|, v220, v180 mul:4\n\t" \
  "v_fma_mix_f32 v223, |v183|, v223, v223 op_sel:[0,1,0] op_sel_hi:[0,1,1] clamp\n\t" \
  "v_mfma_f32_16x16x32_bf16 v[160:163], v[212:215], v[120:123], v[160:163]\n\t" \
  "ds_read_b32 v233, v233 offset:0\n\t" \
  "v_fma_mix_f32 v222, |v182|, v222, v222 op_sel:[0,1,0] op_sel_hi:[0,1,1] clamp\n\t" \
  "s_waitcnt lgkmcnt(7)\n\t" \
  "v_fma_f32 v189, |v189|, v225, v189 mul:4\n\t" \
  "v_mfma_f32_16x16x32_bf16 v[168:171], v[212:215], v[124:127], v[168:171]\n\t" \
  "ds_read_b64_tr_b16 v[212:213], v248 offset:40488\n\t" \
  "v_fma_f32 v188, |v188|, v224, v188 mul:4\n\t" \
  "v_fma_mix_f32 v227, |v191|, v227, v227 op_sel:[0,1,0] op_sel_hi:[0,1,1] clamp\n\t" \
  "v_fma_mix_f32 v226, |v190|, v226, v226 op_sel:[0,1,0] op_sel_hi:[0,1,1] clamp\n\t" \
  "v_mfma_f32_16x16x32_bf16 v[144:147], v[216:219], v[128:131], v[144:147]\n\t" \
  "ds_read_b64_tr_b16 v[214:215], v248 offset:41512\n\t" \
  "v_fma_f32 v183, |v183|, v223, v183 mul:4\n\t" \
  "v_fma_f32 v182, |v182|, v222, v182 mul:4\n\t" \
  "v_mfma_f32_16x16x32_bf16 v[152:155], v[216:219], v[132:135], v[152:155]\n\t" \
  "ds_read_b32 v232, v232 offset:0\n\t" \
  "v_fma_f32 v191, |v191|, v227, v191 mul:4\n\t" \
  "v_fma_f32 v190, |v190|, v226, v190 mul:4\n\t" \
  "v_mfma_f32_16x16x32_bf16 v[160:163], v[216:219], v[136:139], v[160:163]\n\t" \
  "ds_read_b32 v235, v235 offset:0\n\t" \
  "v_cvt_pk_bf16_f32 v180, v180, v181\n\t" \
  "v_cvt_pk_bf16_f32 v181, v182, v183\n\t" \
  "v_mfma_f32_16x16x32_bf16 v[168:171], v[216:219], v[140:143], v[168:171]\n\t" \
  "ds_read_b64_tr_b16 v[216:217], v248 offset:39464\n\t" \
  "v_cvt_pk_bf16_f32 v188, v188, v189\n\t" \
  "v_cvt_pk_bf16_f32 v189, v190, v191\n\t" \
  "s_waitcnt lgkmcnt(9)\n\t" \
  "v_mfma_f32_16x16x32_bf16 v[148:151], v[208:211], v[96:99], 0\n\t" \
  "ds_read_b64_tr_b16 v[218:219], v248 offset:42536\n\t" \
  "v_cvt_pk_f16_f32 v220, |v146|, |v147| clamp\n\t" \
  "s_cbranch_vccnz .Lcnx_p10_3\n\ts_setprio 3\n.Lcnx_p10_3:\n\t" \
  "v_cvt_pknorm_i16_f32 v222, |v144|, |v145|\n\t" \
  "v_mfma_f32_16x16x32_bf16 v[156:159], v[208:211], v[100:103], 0\n\t" \
  "s_waitcnt vmcnt(1)\n\t" \
  "s_cmp_lg_u32 s58, 1\n\t" \
  "s_cbranch_scc1 .Lcnx_c19\n\t" \
  "s_waitcnt vmcnt(0)\n\t" \
  ".Lcnx_c19:\n\t" \
  "s_mov_b64 exec, 3\n\t" \
  "ds_add_rtn_u32 v246, v251, v250 offset:47224\n\t" \
  "s_mov_b64 exec, -1\n\t" \
  "ds_read_b32 v234, v234 offset:0\n\t" \
  "v_cvt_pk_f16_f32 v224, |v154|, |v155| clamp\n\t" \
  "v_cvt_pknorm_i16_f32 v226, |v152|, |v153|\n\t" \
  "v_mfma_f32_16x16x32_bf16 v[164:167], v[208:211], v[104:107], 0\n\t" \
  "v_and_b32_sdwa v221, s56, v220 dst_sel:DWORD dst_unused:UNUSED_PAD src0_sel:DWORD src1_sel:WORD_1\n\t" \
  "v_and_b32 v220, s56, v220\n\t" \
  "v_mfma_f32_16x16x32_bf16 v[172:175], v[208:211], v[108:111], 0\n\t" \
  "s_waitcnt lgkmcnt(1)\n\t" \
  "v_readfirstlane_b32 s4, v246\n\t" \
  "s_cmp_lt_u32 s4, s57\n\t" \
  "s_cbranch_scc1 .Lcnx_w120\n.Lcnx_b120:\n\t" \
  "ds_read_b64_tr_b16 v[208:209], v249 offset:30752\n\t" \
  "v_lshlrev_b32_sdwa v223, 2, v222 dst_sel:DWORD dst_unused:UNUSED_PAD src0_sel:DWORD src1_sel:BYTE_3\n\t" \
  "v_lshlrev_b32_sdwa v222, 2, v222 dst_sel:DWORD dst_unused:UNUSED_PAD src0_sel:DWORD src1_sel:BYTE_1\n\t" \
  "v_and_b32_sdwa v225, s56, v224 dst_sel:DWORD dst_unused:UNUSED_PAD src0_sel:DWORD src1_sel:WORD_1\n\t" \
  "v_mfma_f32_16x16x32_bf16 v[148:151], v[212:215], v[112:115], v[148:151]\n\t" \
  "ds_read_b64_tr_b16 v[210:211], v249 offset:32288\n\t" \
  "v_and_b32 v224, s56, v224\n\t" \
  "v_lshlrev_b32_sdwa v227, 2, v226 dst_sel:DWORD dst_unused:UNUSED_PAD src0_sel:DWORD src1_sel:BYTE_3\n\t" \
  "v_mfma_f32_16x16x32_bf16 v[156:159], v[212:215], v[116:119], v[156:159]\n\t" \
  "s_cmp_eq_u32 s37, 0\n\t" \
  "s_cbranch_scc1 .Lcnx_f11\n.Lcnx_g11:\n\t" \
  "ds_read_b32 v221, v221 offset:0\n\t" \
  "ds_read_b32 v220, v220 offset:0\n\t" \
  "v_lshlrev_b32_sdwa v226, 2, v226 dst_sel:DWORD dst_unused:UNUSED_PAD src0_sel:DWORD src1_sel:BYTE_1\n\t" \
  "v_fma_f32 v197, |v197|, v229, v197 mul:4\n\t" \
  "v_mfma_f32_16x16x32_bf16 v[164:167], v[212:215], v[120:123], v[164:167]\n\t" \
  "ds_read_b32 v223, v223 offset:15376\n\t" \
  "ds_read_b32 v222, v222 offset:15376\n\t" \
  "v_fma_f32 v196, |v196|, v228, v196 mul:4\n\t" \
  "v_fma_mix_f32 v231, |v199|, v231, v231 op_sel:[0,1,0] op_sel_hi:[0,1,1] clamp\n\t" \
  "v_mfma_f32_16x16x32_bf16 v[172:175], v[212:215], v[124:127], v[172:175]\n\t" \
  "ds_read_b64_tr_b16 v[212:213], v249 offset:30760\n\t" \
  "ds_read_b64_tr_b16 v[214:215], v249 offset:32296\n\t" \
  "v_fma_mix_f32 v230, |v198|, v230, v230 op_sel:[0,1,0] op_sel_hi:[0,1,1] clamp\n\t" \
  "s_waitcnt lgkmcnt(8)\n\t" \
  "v_fma_f32 v205, |v205|, v233, v205 mul:4\n\t" \
  "v_mfma_f32_16x16x32_bf16 v[148:151], v[216:219], v[128:131], v[148:151]\n\t" \
  "ds_read_b32 v225, v225 offset:0\n\t" \
  "v_fma_f32 v204, |v204|, v232, v204 mul:4\n\t" \
  "v_fma_f32 v207, |v207|, v235, v207 mul:4\n\t" \
  "v_mfma_f32_16x16x32_bf16 v[156:159], v[216:219], v[132:135], v[156:159]\n\t" \
  "ds_read_b32 v224, v224 offset:0\n\t" \
  "v_fma_f32 v206, |v206|, v234, v206 mul:4\n\t" \
  "v_fma_f32 v199, |v199|, v231, v199 mul:4\n\t" \
  "v_mfma_f32_16x16x32_bf16 v[164:167], v[216:219], v[136:139], v[164:167]\n\t" \
  "ds_read_b32 v227, v227 offset:15376\n\t" \
  "v_fma_f32 v198, |v198|, v230, v198 mul:4\n\t" \
  "v_cvt_pk_bf16_f32 v196, v196, v197\n\t" \
  "v_mfma_f32_16x16x32_bf16 v[172:175], v[216:219], v[140:143], v[172:175]\n\t" \
  "ds_read_b64_tr_b16 v[216:217], v249 offset:30816\n\t" \
  "v_cvt_pk_bf16_f32 v197, v198, v199\n\t" \
  "v_cvt_pk_bf16_f32 v204, v204, v205\n\t" \
  "v_cvt_pk_bf16_f32 v205, v206, v207\n\t" \
  "s_cbranch_vccnz .Lcnx_p11_0\n\ts_setprio 0\n.Lcnx_p11_0:\n\t" \
  "v_cvt_pk_f16_f32 v228, |v162|, |v163| clamp\n\t" \
  "v_cvt_pknorm_i16_f32 v230, |v160|, |v161|\n\t" \
  "v_mfma_f32_16x16x32_bf16 v[72:75], v[242:245], v[236:239], v[72:75]\n\t" \
  "ds_read_b64_tr_b16 v[218:219], v249 offset:32352\n\t" \
  "v_cvt_pk_f16_f32 v232, |v170|, |v171| clamp\n\t" \
  "v_cvt_pk_f16_f32 v234, |v168|, |v169| clamp\n\t" \
  "v_mfma_f32_16x16x32_bf16 v[76:79], v[240:243], v[236:239], v[76:79]\n\t" \
  "buffer_load_dwordx4 v[236:239], v250, s[8:11], s13 offen offset:64\n\t" \
  "s_cmp_lg_u32 s38, 7\n\t" \
  "s_cbranch_scc1 .Lcnx_c31\n\t" \
  "s_mov_b32 m0, 163632\n\t" \
  "s_add_u32 s30, s30, s31\n\t" \
  "buffer_load_dword v252, s[8:11], s30 offen lds\n\t" \
  ".Lcnx_c31:\n\t" \
  "ds_read_b32 v226, v226 offset:15376\n\t" \
  "v_and_b32_sdwa v229, s56, v228 dst_sel:DWORD dst_unused:UNUSED_PAD src0_sel:DWORD src1_sel:WORD_1\n\t" \
  "v_and_b32 v228, s56, v228\n\t" \
  "s_waitcnt lgkmcnt(12)\n\t" \
  "v_mfma_f32_16x16x32_bf16 v[0:3], v[208:211], v[178:181], v[0:3]\n\t" \
  "ds_read_b32 v229, v229 offset:0\n\t" \
  "v_lshlrev_b32_sdwa v231, 2, v230 dst_sel:DWORD dst_unused:UNUSED_PAD src0_sel:DWORD src1_sel:BYTE_3\n\t" \
  "v_lshlrev_b32_sdwa v230, 2, v230 dst_sel:DWORD dst_unused:UNUSED_PAD src0_sel:DWORD src1_sel:BYTE_1\n\t" \
  "v_mfma_f32_16x16x32_bf16 v[24:27], v[208:211], v[186:189], v[24:27]\n\t" \
  "ds_read_b32 v228, v228 offset:0\n\t" \
  "v_and_b32_sdwa v233, s56, v232 dst_sel:DWORD dst_unused:UNUSED_PAD src0_sel:DWORD src1_sel:WORD_1\n\t" \
  "v_and_b32 v232, s56, v232\n\t" \
  "v_mfma_f32_16x16x32_bf16 v[48:51], v[208:211], v[194:197], v[48:51]\n\t" \
  "ds_read_b32 v231, v231 offset:15376\n\t" \
  "v_and_b32_sdwa v235, s56, v234 dst_sel:DWORD dst_unused:UNUSED_PAD src0_sel:DWORD src1_sel:WORD_1\n\t" \
  "v_and_b32 v234, s56, v234\n\t" \
  "v_mfma_f32_16x16x32_bf16 v[72:75], v[208:211], v[202:205], v[72:75]\n\t" \
  "ds_read_b64_tr_b16 v[208:209], v249 offset:30824\n\t" \
  "ds_read_b64_tr_b16 v[210:211], v249 offset:32360\n\t" \
  "s_waitcnt lgkmcnt(13)\n\t" \
  "v_fma_f32 v147, |v147|, v221, v147 mul:4\n\t" \
  "v_fma_f32 v146, |v146|, v220, v146 mul:4\n\t" \
  "s_waitcnt lgkmcnt(11)\n\t" \
  "v_mfma_f32_16x16x32_bf16 v[4:7], v[212:215], v[178:181], v[4:7]\n\t" \
  "ds_read_b32 v230, v230 offset:15376\n\t" \
  "v_fma_mix_f32 v223, |v145|, v223, v223 op_sel:[0,1,0] op_sel_hi:[0,1,1] clamp\n\t" \
  "v_fma_mix_f32 v222, |v144|, v222, v222 op_sel:[0,1,0] op_sel_hi:[0,1,1] clamp\n\t" \
  "v_mfma_f32_16x16x32_bf16 v[28:31], v[212:215], v[186:189], v[28:31]\n\t" \
  "ds_read_b32 v233, v233 offset:0\n\t" \
  "s_waitcnt lgkmcnt(7)\n\t" \
  "v_fma_f32 v155, |v155|, v225, v155 mul:4\n\t" \
  "v_fma_f32 v154, |v154|, v224, v154 mul:4\n\t" \
  "v_mfma_f32_16x16x32_bf16 v[52:55], v[212:215], v[194:197], v[52:55]\n\t" \
  "ds_read_b32 v232, v232 offset:0\n\t" \
  "v_fma_mix_f32 v227, |v153|, v227, v227 op_sel:[0,1,0] op_sel_hi:[0,1,1] clamp\n\t" \
  "v_fma_mix_f32 v226, |v152|, v226, v226 op_sel:[0,1,0] op_sel_hi:[0,1,1] clamp\n\t" \
  "v_fma_f32 v145, |v145|, v223, v145 mul:4\n\t" \
  "v_mfma_f32_16x16x32_bf16 v[76:79], v[212:215], v[202:205], v[76:79]\n\t" \
  "ds_read_b64_tr_b16 v[212:213], v249 offset:30880\n\t" \
  "v_fma_f32 v144, |v144|, v222, v144 mul:4\n\t" \
  "v_fma_f32 v153, |v153|, v227, v153 mul:4\n\t" \
  "v_mfma_f32_16x16x32_bf16 v[8:11], v[216:219], v[178:181], v[8:11]\n\t" \
  "ds_read_b64_tr_b16 v[214:215], v249 offset:32416\n\t" \
  "v_fma_f32 v152, |v152|, v226, v152 mul:4\n\t" \
  "v_cvt_pk_bf16_f32 v147, v146, v147\n\t" \
  "v_mfma_f32_16x16x32_bf16 v[32:35], v[216:219], v[186:189], v[32:35]\n\t" \
  "ds_read_b32 v235, v235 offset:0\n\t" \
  "v_cvt_pk_bf16_f32 v146, v144, v145\n\t" \
  "v_cvt_pk_bf16_f32 v155, v154, v155\n\t" \
  "v_mfma_f32_16x16x32_bf16 v[56:59], v[216:219], v[194:197], v[56:59]\n\t" \
  "ds_read_b32 v234, v234 offset:0\n\t" \
  "v_cvt_pk_bf16_f32 v154, v152, v153\n\t" \
  "s_cbranch_vccnz .Lcnx_p11_1\n\ts_setprio 1\n.Lcnx_p11_1:\n\t" \
  "v_cvt_pk_f16_f32 v220, |v148|, |v149| clamp\n\t" \
  "v_mfma_f32_16x16x32_bf16 v[80:83], v[216:219], v[202:205], v[80:83]\n\t" \
  "ds_read_b64_tr_b16 v[216:217], v249 offset:30888\n\t" \
  "v_cvt_pknorm_i16_f32 v222, |v150|, |v151|\n\t" \
  "v_cvt_pk_f16_f32 v224, |v156|, |v157| clamp\n\t" \
  "s_waitcnt lgkmcnt(8)\n\t" \
  "v_mfma_f32_16x16x32_bf16 v[12:15], v[208:211], v[178:181], v[12:15]\n\t" \
  "ds_read_b64_tr_b16 v[218:219], v249 offset:32424\n\t" \
  "v_cvt_pknorm_i16_f32 v226, |v158|, |v159|\n\t" \
  "v_and_b32_sdwa v221, s56, v220 dst_sel:DWORD dst_unused:UNUSED_PAD src0_sel:DWORD src1_sel:WORD_1\n\t" \
  "v_mfma_f32_16x16x32_bf16 v[36:39], v[208:211], v[186:189], v[36:39]\n\t" \
  "v_and_b32 v220, s56, v220\n\t" \
  "v_lshlrev_b32_sdwa v223, 2, v222 dst_sel:DWORD dst_unused:UNUSED_PAD src0_sel:DWORD src1_sel:BYTE_3\n\t" \
  "v_mfma_f32_16x16x32_bf16 v[60:63], v[208:211], v[194:197], v[60:63]\n\t" \
  "ds_read_b32 v221, v221 offset:0\n\t" \
  "v_lshlrev_b32_sdwa v222, 2, v222 dst_sel:DWORD dst_unused:UNUSED_PAD src0_sel:DWORD src1_sel:BYTE_1\n\t" \
  "v_and_b32_sdwa v225, s56, v224 dst_sel:DWORD dst_unused:UNUSED_PAD src0_sel:DWORD src1_sel:WORD_1\n\t" \
  "v_and_b32 v224, s56, v224\n\t" \
  "v_mfma_f32_16x16x32_bf16 v[84:87], v[208:211], v[202:205], v[84:87]\n\t" \
  "ds_read_b64_tr_b16 v[208:209], v248 offset:12304\n\t" \
  "ds_read_b64_tr_b16 v[210:211], v248 offset:13328\n\t" \
  "v_lshlrev_b32_sdwa v227, 2, v226 dst_sel:DWORD dst_unused:UNUSED_PAD src0_sel:DWORD src1_sel:BYTE_3\n\t" \
  "v_lshlrev_b32_sdwa v226, 2, v226 dst_sel:DWORD dst_unused:UNUSED_PAD src0_sel:DWORD src1_sel:BYTE_1\n\t" \
  "s_waitcnt lgkmcnt(7)\n\t" \
  "v_mfma_f32_16x16x32_bf16 v[16:19], v[212:215], v[178:181], v[16:19]\n\t" \
  "ds_read_b32 v220, v220 offset:0\n\t" \
  "ds_read_b32 v223, v223 offset:15376\n\t" \
  "v_fma_f32 v163, |v163|, v229, v163 mul:4\n\t" \
  "v_fma_f32 v162, |v162|, v228, v162 mul:4\n\t" \
  "v_mfma_f32_16x16x32_bf16 v[40:43], v[212:215], v[186:189], v[40:43]\n\t" \
  "ds_read_b32 v222, v222 offset:15376\n\t" \
  "v_fma_mix_f32 v231, |v161|, v231, v231 op_sel:[0,1,0] op_sel_hi:[0,1,1] clamp\n\t" \
  "v_fma_mix_f32 v230, |v160|, v230, v230 op_sel:[0,1,0] op_sel_hi:[0,1,1] clamp\n\t" \
  "v_mfma_f32_16x16x32_bf16 v[64:67], v[212:215], v[194:197], v[64:67]\n\t" \
  "ds_read_b32 v225, v225 offset:0\n\t" \
  "s_waitcnt lgkmcnt(9)\n\t" \
  "v_fma_f32 v171, |v171|, v233, v171 mul:4\n\t" \
  "v_fma_f32 v170, |v170|, v232, v170 mul:4\n\t" \
  "v_mfma_f32_16x16x32_bf16 v[88:91], v[212:215], v[202:205], v[88:91]\n\t" \
  "ds_read_b64_tr_b16 v[212:213], v248 offset:15376\n\t" \
  "v_fma_f32 v169, |v169|, v235, v169 mul:4\n\t" \
  "v_fma_f32 v168, |v168|, v234, v168 mul:4\n\t" \
  "s_waitcnt lgkmcnt(8)\n\t" \
  "v_mfma_f32_16x16x32_bf16 v[20:23], v[216:219], v[178:181], v[20:23]\n\t" \
  "ds_read_b64_tr_b16 v[214:215], v248 offset:16400\n\t" \
  "v_fma_f32 v161, |v161|, v231, v161 mul:4\n\t" \
  "v_fma_f32 v160, |v160|, v230, v160 mul:4\n\t" \
  "v_mfma_f32_16x16x32_bf16 v[44:47], v[216:219], v[186:189], v[44:47]\n\t" \
  "ds_read_b32 v224, v224 offset:0\n\t" \
  "v_cvt_pk_bf16_f32 v163, v162, v163\n\t" \
  "v_cvt_pk_bf16_f32 v162, v160, v161\n\t" \
  "v_mfma_f32_16x16x32_bf16 v[68:71], v[216:219], v[194:197], v[68:71]\n\t" \
  "ds_read_b32 v227, v227 offset:15376\n\t" \
  "v_cvt_pk_bf16_f32 v171, v170, v171\n\t" \
  "v_cvt_pk_bf16_f32 v170, v168, v169\n\t" \
  "v_cvt_pk_f16_f32 v228, |v164|, |v165| clamp\n\t" \
  "v_mfma_f32_16x16x32_bf16 v[92:95], v[216:219], v[202:205], v[92:95]\n\t" \
  "ds_read_b64_tr_b16 v[216:217], v248 offset:14352\n\t" \
  "v_cvt_pknorm_i16_f32 v230, |v166|, |v167|\n\t" \
  "s_cbranch_vccnz .Lcnx_p11_2\n\ts_setprio 2\n.Lcnx_p11_2:\n\t" \
  "v_cvt_pk_f16_f32 v232, |v172|, |v173| clamp\n\t" \
  "s_waitcnt lgkmcnt(9)\n\t" \
  "v_mfma_f32_16x16x32_bf16 v[176:179], v[208:211], v[96:99], 0\n\t" \
  "ds_read_b64_tr_b16 v[218:219], v248 offset:17424\n\t" \
  "v_cvt_pk_f16_f32 v234, |v174|, |v175| clamp\n\t" \
  "v_and_b32_sdwa v229, s56, v228 dst_sel:DWORD dst_unused:UNUSED_PAD src0_sel:DWORD src1_sel:WORD_1\n\t" \
  "v_mfma_f32_16x16x32_bf16 v[184:187], v[208:211], v[100:103], 0\n\t" \
  "ds_read_b32 v226, v226 offset:15376\n\t" \
  "v_and_b32 v228, s56, v228\n\t" \
  "v_lshlrev_b32_sdwa v231, 2, v230 dst_sel:DWORD dst_unused:UNUSED_PAD src0_sel:DWORD src1_sel:BYTE_3\n\t" \
  "v_mfma_f32_16x16x32_bf16 v[192:195], v[208:211], v[104:107], 0\n\t" \
  "ds_read_b32 v229, v229 offset:0\n\t" \
  "v_lshlrev_b32_sdwa v230, 2, v230 dst_sel:DWORD dst_unused:UNUSED_PAD src0_sel:DWORD src1_sel:BYTE_1\n\t" \
  "v_and_b32_sdwa v233, s56, v232 dst_sel:DWORD dst_unused:UNUSED_PAD src0_sel:DWORD src1_sel:WORD_1\n\t" \
  "v_mfma_f32_16x16x32_bf16 v[200:203], v[208:211], v[108:111], 0\n\t" \
  "ds_read_b64_tr_b16 v[208:209], v248 offset:12816\n\t" \
  "v_and_b32 v232, s56, v232\n\t" \
  "v_and_b32_sdwa v235, s56, v234 dst_sel:DWORD dst_unused:UNUSED_PAD src0_sel:DWORD src1_sel:WORD_1\n\t" \
  "s_waitcnt lgkmcnt(7)\n\t" \
  "v_mfma_f32_16x16x32_bf16 v[176:179], v[212:215], v[112:115], v[176:179]\n\t" \
  "ds_read_b64_tr_b16 v[210:211], v248 offset:13840\n\t" \
  "ds_read_b32 v228, v228 offset:0\n\t" \
  "v_and_b32 v234, s56, v234\n\t" \
  "v_fma_f32 v149, |v149|, v221, v149 mul:4\n\t" \
  "v_mfma_f32_16x16x32_bf16 v[184:187], v[212:215], v[116:119], v[184:187]\n\t" \
  "ds_read_b32 v231, v231 offset:15376\n\t" \
  "ds_read_b32 v230, v230 offset:15376\n\t" \
  "v_fma_f32 v148, |v148|, v220, v148 mul:4\n\t" \
  "v_fma_mix_f32 v223, |v151|, v223, v223 op_sel:[0,1,0] op_sel_hi:[0,1,1] clamp\n\t" \
  "v_mfma_f32_16x16x32_bf16 v[192:195], v[212:215], v[120:123], v[192:195]\n\t" \
  "ds_read_b32 v233, v233 offset:0\n\t" \
  "v_fma_mix_f32 v222, |v150|, v222, v222 op_sel:[0,1,0] op_sel_hi:[0,1,1] clamp\n\t" \
  "s_waitcnt lgkmcnt(7)\n\t" \
  "v_fma_f32 v157, |v157|, v225, v157 mul:4\n\t" \
  "v_mfma_f32_16x16x32_bf16 v[200:203], v[212:215], v[124:127], v[200:203]\n\t" \
  "ds_read_b64_tr_b16 v[212:213], v248 offset:15888\n\t" \
  "v_fma_f32 v156, |v156|, v224, v156 mul:4\n\t" \
  "v_fma_mix_f32 v227, |v159|, v227, v227 op_sel:[0,1,0] op_sel_hi:[0,1,1] clamp\n\t" \
  "v_fma_mix_f32 v226, |v158|, v226, v226 op_sel:[0,1,0] op_sel_hi:[0,1,1] clamp\n\t" \
  "v_mfma_f32_16x16x32_bf16 v[176:179], v[216:219], v[128:131], v[176:179]\n\t" \
  "ds_read_b64_tr_b16 v[214:215], v248 offset:16912\n\t" \
  "v_fma_f32 v151, |v151|, v223, v151 mul:4\n\t" \
  "v_fma_f32 v150, |v150|, v222, v150 mul:4\n\t" \
  "v_mfma_f32_16x16x32_bf16 v[184:187], v[216:219], v[132:135], v[184:187]\n\t" \
  "ds_read_b32 v232, v232 offset:0\n\t" \
  "v_fma_f32 v159, |v159|, v227, v159 mul:4\n\t" \
  "v_fma_f32 v158, |v158|, v226, v158 mul:4\n\t" \
  "v_mfma_f32_16x16x32_bf16 v[192:195], v[216:219], v[136:139], v[192:195]\n\t" \
  "ds_read_b32 v235, v235 offset:0\n\t" \
  "v_cvt_pk_bf16_f32 v148, v148, v149\n\t" \
  "v_cvt_pk_bf16_f32 v149, v150, v151\n\t" \
  "v_mfma_f32_16x16x32_bf16 v[200:203], v[216:219], v[140:143], v[200:203]\n\t" \
  "ds_read_b64_tr_b16 v[216:217], v248 offset:14864\n\t" \
  "v_cvt_pk_bf16_f32 v156, v156, v157\n\t" \
  "v_cvt_pk_bf16_f32 v157, v158, v159\n\t" \
  "s_waitcnt lgkmcnt(9)\n\t" \
  "v_mfma_f32_16x16x32_bf16 v[180:183], v[208:211], v[96:99], 0\n\t" \
  "ds_read_b64_tr_b16 v[218:219], v248 offset:17936\n\t" \
  "v_cvt_pk_f16_f32 v220, |v178|, |v179| clamp\n\t" \
  "s_cbranch_vccnz .Lcnx_p11_3\n\ts_setprio 3\n.Lcnx_p11_3:\n\t" \
  "v_cvt_pknorm_i16_f32 v222, |v176|, |v177|\n\t" \
  "v_mfma_f32_16x16x32_bf16 v[188:191], v[208:211], v[100:103], 0\n\t" \
  "s_waitcnt vmcnt(0)\n\t" \
  "s_mov_b64 exec, 3\n\t" \
  "ds_add_rtn_u32 v246, v251, v250 offset:47232\n\t" \
  "s_mov_b64 exec, -1\n\t" \
  "ds_read_b32 v234, v234 offset:0\n\t" \
  "v_cvt_pk_f16_f32 v224, |v186|, |v187| clamp\n\t" \
  "v_cvt_pknorm_i16_f32 v226, |v184|, |v185|\n\t" \
  "v_mfma_f32_16x16x32_bf16 v[196:199], v[208:211], v[104:107], 0\n\t" \
  "v_and_b32_sdwa v221, s56, v220 dst_sel:DWORD dst_unused:UNUSED_PAD src0_sel:DWORD src1_sel:WORD_1\n\t" \
  "v_and_b32 v220, s56, v220\n\t" \
  "v_mfma_f32_16x16x32_bf16 v[204:207], v[208:211], v[108:111], 0\n\t" \
  "s_waitcnt lgkmcnt(1)\n\t" \
  "v_readfirstlane_b32 s4, v246\n\t" \
  "s_cmp_lt_u32 s4, s57\n\t" \
  "s_cbranch_scc1 .Lcnx_w132\n.Lcnx_b132:\n\t" \
  "ds_read_b64_tr_b16 v[208:209], v249 offset:0\n\t" \
  "v_lshlrev_b32_sdwa v223, 2, v222 dst_sel:DWORD dst_unused:UNUSED_PAD src0_sel:DWORD src1_sel:BYTE_3\n\t" \
  "v_lshlrev_b32_sdwa v222, 2, v222 dst_sel:DWORD dst_unused:UNUSED_PAD src0_sel:DWORD src1_sel:BYTE_1\n\t" \
  "v_and_b32_sdwa v225, s56, v224 dst_sel:DWORD dst_unused:UNUSED_PAD src0_sel:DWORD src1_sel:WORD_1\n\t" \
  "v_mfma_f32_16x16x32_bf16 v[180:183], v[212:215], v[112:115], v[180:183]\n\t" \
  "ds_read_b64_tr_b16 v[210:211], v249 offset:1536\n\t" \
  "v_and_b32 v224, s56, v224\n\t" \
  "v_lshlrev_b32_sdwa v227, 2, v226 dst_sel:DWORD dst_unused:UNUSED_PAD src0_sel:DWORD src1_sel:BYTE_3\n\t" \
  "v_mfma_f32_16x16x32_bf16 v[188:191], v[212:215], v[116:119], v[188:191]\n\t" \
  "ds_read_b32 v221, v221 offset:0\n\t" \
  "ds_read_b32 v220, v220 offset:0\n\t" \
  "v_lshlrev_b32_sdwa v226, 2, v226 dst_sel:DWORD dst_unused:UNUSED_PAD src0_sel:DWORD src1_sel:BYTE_1\n\t" \
  "v_fma_f32 v165, |v165|, v229, v165 mul:4\n\t" \
  "v_mfma_f32_16x16x32_bf16 v[196:199], v[212:215], v[120:123], v[196:199]\n\t" \
  "ds_read_b32 v223, v223 offset:15376\n\t" \
  "ds_read_b32 v222, v222 offset:15376\n\t" \
  "v_fma_f32 v164, |v164|, v228, v164 mul:4\n\t" \
  "v_fma_mix_f32 v231, |v167|, v231, v231 op_sel:[0,1,0] op_sel_hi:[0,1,1] clamp\n\t" \
  "v_mfma_f32_16x16x32_bf16 v[204:207], v[212:215], v[124:127], v[204:207]\n\t" \
  "ds_read_b64_tr_b16 v[212:213], v249 offset:8\n\t" \
  "ds_read_b64_tr_b16 v[214:215], v249 offset:1544\n\t" \
  "v_fma_mix_f32 v230, |v166|, v230, v230 op_sel:[0,1,0] op_sel_hi:[0,1,1] clamp\n\t" \
  "s_waitcnt lgkmcnt(8)\n\t" \
  "v_fma_f32 v173, |v173|, v233, v173 mul:4\n\t" \
  "v_mfma_f32_16x16x32_bf16 v[180:183], v[216:219], v[128:131], v[180:183]\n\t" \
  "ds_read_b32 v225, v225 offset:0\n\t" \
  "v_fma_f32 v172, |v172|, v232, v172 mul:4\n\t" \
  "v_fma_f32 v175, |v175|, v235, v175 mul:4\n\t" \
  "v_mfma_f32_16x16x32_bf16 v[188:191], v[216:219], v[132:135], v[188:191]\n\t" \
  "ds_read_b32 v224, v224 offset:0\n\t" \
  "v_fma_f32 v174, |v174|, v234, v174 mul:4\n\t" \
  "v_fma_f32 v167, |v167|, v231, v167 mul:4\n\t" \
  "v_mfma_f32_16x16x32_bf16 v[196:199], v[216:219], v[136:139], v[196:199]\n\t" \
  "ds_read_b32 v227, v227 offset:15376\n\t" \
  "v_fma_f32 v166, |v166|, v230, v166 mul:4\n\t" \
  "v_cvt_pk_bf16_f32 v164, v164, v165\n\t" \
  "v_mfma_f32_16x16x32_bf16 v[204:207], v[216:219], v[140:143], v[204:207]\n\t" \
  "ds_read_b64_tr_b16 v[216:217], v249 offset:64\n\t" \
  "v_cvt_pk_bf16_f32 v165, v166, v167\n\t" \
  "v_cvt_pk_bf16_f32 v172, v172, v173\n\t" \
  "v_cvt_pk_bf16_f32 v173, v174, v175\n\t" \
  "s_cbranch_vccnz .Lcnx_p12_0\n\ts_setprio 3\n.Lcnx_p12_0:\n\t" \
  "v_cvt_pk_f16_f32 v228, |v194|, |v195| clamp\n\t" \
  "v_cvt_pknorm_i16_f32 v230, |v192|, |v193|\n\t" \
  "v_mfma_f32_16x16x32_bf16 v[80:83], v[242:245], v[236:239], v[80:83]\n\t" \
  "ds_read_b64_tr_b16 v[218:219], v249 offset:1600\n\t" \
  "v_cvt_pk_f16_f32 v232, |v202|, |v203| clamp\n\t" \
  "v_cvt_pk_f16_f32 v234, |v200|, |v201| clamp\n\t" \
  "v_and_b32_sdwa v229, s56, v228 dst_sel:DWORD dst_unused:UNUSED_PAD src0_sel:DWORD src1_sel:WORD_1\n\t" \
  "v_mfma_f32_16x16x32_bf16 v[84:87], v[240:243], v[236:239], v[84:87]\n\t" \
  "buffer_load_dwordx4 v[236:239], v250, s[8:11], s13 offen offset:128\n\t" \
  "s_cmp_lg_u32 s38, 4\n\t" \
  "s_cbranch_scc1 .Lcnx_c32\n\t" \
  "s_mov_b32 m0, 163632\n\t" \
  "s_add_u32 s30, s30, s31\n\t" \
  "buffer_load_dword v252, s[8:11], s30 offen lds\n\t" \
  ".Lcnx_c32:\n\t" \
  "ds_read_b32 v226, v226 offset:15376\n\t" \
  "v_and_b32 v228, s56, v228\n\t" \
  "v_lshlrev_b32_sdwa v231, 2, v230 dst_sel:DWORD dst_unused:UNUSED_PAD src0_sel:DWORD src1_sel:BYTE_3\n\t" \
  "v_lshlrev_b32_sdwa v230, 2, v230 dst_sel:DWORD dst_unused:UNUSED_PAD src0_sel:DWORD src1_sel:BYTE_1\n\t" \
  "v_and_b32_sdwa v233, s56, v232 dst_sel:DWORD dst_unused:UNUSED_PAD src0_sel:DWORD src1_sel:WORD_1\n\t" \
  "s_waitcnt lgkmcnt(12)\n\t" \
  "v_mfma_f32_16x16x32_bf16 v[0:3], v[208:211], v[146:149], v[0:3]\n\t" \
  "ds_read_b32 v229, v229 offset:0\n\t" \
  "v_and_b32 v232, s56, v232\n\t" \
  "v_and_b32_sdwa v235, s56, v234 dst_sel:DWORD dst_unused:UNUSED_PAD src0_sel:DWORD src1_sel:WORD_1\n\t" \
  "v_and_b32 v234, s56, v234\n\t" \
  "v_mfma_f32_16x16x32_bf16 v[24:27], v[208:211], v[154:157], v[24:27]\n\t" \
  "ds_read_b32 v228, v228 offset:0\n\t" \
  "ds_read_b32 v231, v231 offset:15376\n\t" \
  "s_waitcnt lgkmcnt(11)\n\t" \
  "v_fma_f32 v179, |v179|, v221, v179 mul:4\n\t" \
  "v_fma_f32 v178, |v178|, v220, v178 mul:4\n\t" \
  "v_fma_mix_f32 v223, |v177|, v223, v223 op_sel:[0,1,0] op_sel_hi:[0,1,1] clamp\n\t" \
  "v_fma_mix_f32 v222, |v176|, v222, v222 op_sel:[0,1,0] op_sel_hi:[0,1,1] clamp\n\t" \
  "v_mfma_f32_16x16x32_bf16 v[48:51], v[208:211], v[162:165], v[48:51]\n\t" \
  "ds_read_b32 v230, v230 offset:15376\n\t" \
  "s_waitcnt lgkmcnt(4)\n\t" \
  "v_fma_f32 v187, |v187|, v225, v187 mul:4\n\t" \
  "v_fma_f32 v186, |v186|, v224, v186 mul:4\n\t" \
  "v_fma_mix_f32 v227, |v185|, v227, v227 op_sel:[0,1,0] op_sel_hi:[0,1,1] clamp\n\t" \
  "v_fma_mix_f32 v226, |v184|, v226, v226 op_sel:[0,1,0] op_sel_hi:[0,1,1] clamp\n\t" \
  "v_mfma_f32_16x16x32_bf16 v[72:75], v[208:211], v[170:173], v[72:75]\n\t" \
  "ds_read_b64_tr_b16 v[208:209], v249 offset:72\n\t" \
  "ds_read_b64_tr_b16 v[210:211], v249 offset:1608\n\t" \
  "v_fma_f32 v177, |v177|, v223, v177 mul:4\n\t" \
  "v_fma_f32 v176, |v176|, v222, v176 mul:4\n\t" \
  "v_fma_f32 v185, |v185|, v227, v185 mul:4\n\t" \
  "v_mfma_f32_16x16x32_bf16 v[4:7], v[212:215], v[146:149], v[4:7]\n\t" \
  "ds_read_b32 v233, v233 offset:0\n\t" \
  "v_fma_f32 v184, |v184|, v226, v184 mul:4\n\t" \
  "v_cvt_pk_bf16_f32 v179, v178, v179\n\t" \
  "s_cbranch_vccnz .Lcnx_p12_1\n\ts_setprio 2\n.Lcnx_p12_1:\n\t" \
  "v_cvt_pk_bf16_f32 v178, v176, v177\n\t" \
  "v_cvt_pk_bf16_f32 v187, v186, v187\n\t" \
  "v_mfma_f32_16x16x32_bf16 v[28:31], v[212:215], v[154:157], v[28:31]\n\t" \
  "ds_read_b32 v232, v232 offset:0\n\t" \
  "v_cvt_pk_bf16_f32 v186, v184, v185\n\t" \
  "v_cvt_pk_f16_f32 v220, |v180|, |v181| clamp\n\t" \
  "v_cvt_pknorm_i16_f32 v222, |v182|, |v183|\n\t" \
  "v_mfma_f32_16x16x32_bf16 v[52:55], v[212:215], v[162:165], v[52:55]\n\t" \
  "ds_read_b32 v235, v235 offset:0\n\t" \
  "v_cvt_pk_f16_f32 v224, |v188|, |v189| clamp\n\t" \
  "v_cvt_pknorm_i16_f32 v226, |v190|, |v191|\n\t" \
  "v_and_b32_sdwa v221, s56, v220 dst_sel:DWORD dst_unused:UNUSED_PAD src0_sel:DWORD src1_sel:WORD_1\n\t" \
  "v_and_b32 v220, s56, v220\n\t" \
  "v_mfma_f32_16x16x32_bf16 v[76:79], v[212:215], v[170:173], v[76:79]\n\t" \
  "ds_read_b64_tr_b16 v[212:213], v249 offset:128\n\t" \
  "v_lshlrev_b32_sdwa v223, 2, v222 dst_sel:DWORD dst_unused:UNUSED_PAD src0_sel:DWORD src1_sel:BYTE_3\n\t" \
  "v_lshlrev_b32_sdwa v222, 2, v222 dst_sel:DWORD dst_unused:UNUSED_PAD src0_sel:DWORD src1_sel:BYTE_1\n\t" \
  "v_and_b32_sdwa v225, s56, v224 dst_sel:DWORD dst_unused:UNUSED_PAD src0_sel:DWORD src1_sel:WORD_1\n\t" \
  "v_and_b32 v224, s56, v224\n\t" \
  "v_mfma_f32_16x16x32_bf16 v[8:11], v[216:219], v[146:149], v[8:11]\n\t" \
  "ds_read_b64_tr_b16 v[214:215], v249 offset:1664\n\t" \
  "ds_read_b32 v234, v234 offset:0\n\t" \
  "v_lshlrev_b32_sdwa v227, 2, v226 dst_sel:DWORD dst_unused:UNUSED_PAD src0_sel:DWORD src1_sel:BYTE_3\n\t" \
  "v_lshlrev_b32_sdwa v226, 2, v226 dst_sel:DWORD dst_unused:UNUSED_PAD src0_sel:DWORD src1_sel:BYTE_1\n\t" \
  "s_waitcnt lgkmcnt(8)\n\t" \
  "v_fma_f32 v195, |v195|, v229, v195 mul:4\n\t" \
  "v_mfma_f32_16x16x32_bf16 v[32:35], v[216:219], v[154:157], v[32:35]\n\t" \
  "ds_read_b32 v221, v221 offset:0\n\t" \
  "ds_read_b32 v220, v220 offset:0\n\t" \
  "v_fma_f32 v194, |v194|, v228, v194 mul:4\n\t" \
  "v_fma_mix_f32 v231, |v193|, v231, v231 op_sel:[0,1,0] op_sel_hi:[0,1,1] clamp\n\t" \
  "v_fma_mix_f32 v230, |v192|, v230, v230 op_sel:[0,1,0] op_sel_hi:[0,1,1] clamp\n\t" \
  "s_waitcnt lgkmcnt(2)\n\t" \
  "v_fma_f32 v203, |v203|, v233, v203 mul:4\n\t" \
  "v_mfma_f32_16x16x32_bf16 v[56:59], v[216:219], v[162:165], v[56:59]\n\t" \
  "ds_read_b32 v223, v223 offset:15376\n\t" \
  "ds_read_b32 v222, v222 offset:15376\n\t" \
  "v_fma_f32 v202, |v202|, v232, v202 mul:4\n\t" \
  "v_fma_f32 v201, |v201|, v235, v201 mul:4\n\t" \
  "v_fma_f32 v200, |v200|, v234, v200 mul:4\n\t" \
  "v_fma_f32 v193, |v193|, v231, v193 mul:4\n\t" \
  "v_mfma_f32_16x16x32_bf16 v[80:83], v[216:219], v[170:173], v[80:83]\n\t" \
  "ds_read_b64_tr_b16 v[216:217], v249 offset:136\n\t" \
  "ds_read_b64_tr_b16 v[218:219], v249 offset:1672\n\t" \
  "s_cbranch_vccnz .Lcnx_p12_2\n\ts_setprio 1\n.Lcnx_p12_2:\n\t" \
  "v_fma_f32 v192, |v192|, v230, v192 mul:4\n\t" \
  "v_cvt_pk_bf16_f32 v195, v194, v195\n\t" \
  "v_cvt_pk_bf16_f32 v194, v192, v193\n\t" \
  "v_mfma_f32_16x16x32_bf16 v[12:15], v[208:211], v[146:149], v[12:15]\n\t" \
  "ds_read_b32 v225, v225 offset:0\n\t" \
  "v_cvt_pk_bf16_f32 v203, v202, v203\n\t" \
  "v_cvt_pk_bf16_f32 v202, v200, v201\n\t" \
  "v_cvt_pk_f16_f32 v228, |v196|, |v197| clamp\n\t" \
  "v_cvt_pknorm_i16_f32 v230, |v198|, |v199|\n\t" \
  "v_mfma_f32_16x16x32_bf16 v[36:39], v[208:211], v[154:157], v[36:39]\n\t" \
  "s_waitcnt vmcnt(0)\n\t" \
  "s_mov_b64 exec, 3\n\t" \
  "ds_add_rtn_u32 v246, v251, v250 offset:47240\n\t" \
  "s_mov_b64 exec, -1\n\t" \
  "ds_read_b32 v224, v224 offset:0\n\t" \
  "v_cvt_pk_f16_f32 v232, |v204|, |v205| clamp\n\t" \
  "v_cvt_pk_f16_f32 v234, |v206|, |v207| clamp\n\t" \
  "v_and_b32_sdwa v229, s56, v228 dst_sel:DWORD dst_unused:UNUSED_PAD src0_sel:DWORD src1_sel:WORD_1\n\t" \
  "v_mfma_f32_16x16x32_bf16 v[60:63], v[208:211], v[162:165], v[60:63]\n\t" \
  "ds_read_b32 v227, v227 offset:15376\n\t" \
  "v_and_b32 v228, s56, v228\n\t" \
  "v_lshlrev_b32_sdwa v231, 2, v230 dst_sel:DWORD dst_unused:UNUSED_PAD src0_sel:DWORD src1_sel:BYTE_3\n\t" \
  "v_lshlrev_b32_sdwa v230, 2, v230 dst_sel:DWORD dst_unused:UNUSED_PAD src0_sel:DWORD src1_sel:BYTE_1\n\t" \
  "v_and_b32_sdwa v233, s56, v232 dst_sel:DWORD dst_unused:UNUSED_PAD src0_sel:DWORD src1_sel:WORD_1\n\t" \
  "v_mfma_f32_16x16x32_bf16 v[84:87], v[208:211], v[170:173], v[84:87]\n\t" \
  "s_waitcnt lgkmcnt(2)\n\t" \
  "v_readfirstlane_b32 s4, v246\n\t" \
  "s_cmp_lt_u32 s4, s57\n\t" \
  "s_cbranch_scc1 .Lcnx_w138\n.Lcnx_b138:\n\t" \
  "ds_read_b64_tr_b16 v[208:209], v249 offset:6152\n\t" \
  "ds_read_b64_tr_b16 v[210:211], v249 offset:7688\n\t" \
  "v_and_b32 v232, s56, v232\n\t" \
  "v_and_b32_sdwa v235, s56, v234 dst_sel:DWORD dst_unused:UNUSED_PAD src0_sel:DWORD src1_sel:WORD_1\n\t" \
  "v_and_b32 v234, s56, v234\n\t" \
  "ds_read_b32 v226, v226 offset:15376\n\t" \
  "ds_read_b32 v229, v229 offset:0\n\t" \
  "ds_read_b32 v228, v228 offset:0\n\t" \
  "ds_read_b32 v231, v231 offset:15376\n\t" \
  "ds_read_b32 v230, v230 offset:15376\n\t" \
  "ds_read_b32 v233, v233 offset:0\n\t" \
  "ds_read_b32 v232, v232 offset:0\n\t" \
  "ds_read_b32 v235, v235 offset:0\n\t" \
  "v_fma_f32 v181, |v181|, v221, v181 mul:4\n\t" \
  "s_cbranch_vccnz .Lcnx_p12_3\n\ts_setprio 0\n.Lcnx_p12_3:\n\t" \
  "v_mfma_f32_16x16x32_bf16 v[16:19], v[212:215], v[146:149], v[16:19]\n\t" \
  "ds_read_b32 v234, v234 offset:0\n\t" \
  "v_fma_f32 v180, |v180|, v220, v180 mul:4\n\t" \
  "v_fma_mix_f32 v223, |v183|, v223, v223 op_sel:[0,1,0] op_sel_hi:[0,1,1] clamp\n\t" \
  "v_fma_mix_f32 v222, |v182|, v222, v222 op_sel:[0,1,0] op_sel_hi:[0,1,1] clamp\n\t" \
  "v_mfma_f32_16x16x32_bf16 v[40:43], v[212:215], v[154:157], v[40:43]\n\t" \
  "s_waitcnt lgkmcnt(8)\n\t" \
  "v_fma_f32 v189, |v189|, v225, v189 mul:4\n\t" \
  "v_fma_f32 v188, |v188|, v224, v188 mul:4\n\t" \
  "v_fma_mix_f32 v227, |v191|, v227, v227 op_sel:[0,1,0] op_sel_hi:[0,1,1] clamp\n\t" \
  "v_fma_mix_f32 v226, |v190|, v226, v226 op_sel:[0,1,0] op_sel_hi:[0,1,1] clamp\n\t" \
  "v_mfma_f32_16x16x32_bf16 v[64:67], v[212:215], v[162:165], v[64:67]\n\t" \
  "v_fma_f32 v183, |v183|, v223, v183 mul:4\n\t" \
  "v_fma_f32 v182, |v182|, v222, v182 mul:4\n\t" \
  "v_fma_f32 v191, |v191|, v227, v191 mul:4\n\t" \
  "v_mfma_f32_16x16x32_bf16 v[88:91], v[212:215], v[170:173], v[88:91]\n\t" \
  "ds_read_b64_tr_b16 v[212:213], v249 offset:6160\n\t" \
  "v_fma_f32 v190, |v190|, v226, v190 mul:4\n\t" \
  "v_cvt_pk_bf16_f32 v180, v180, v181\n\t" \
  "v_cvt_pk_bf16_f32 v181, v182, v183\n\t" \
  "v_cvt_pk_bf16_f32 v188, v188, v189\n\t" \
  "v_mfma_f32_16x16x32_bf16 v[20:23], v[216:219], v[146:149], v[20:23]\n\t" \
  "ds_read_b64_tr_b16 v[214:215], v249 offset:7696\n\t" \
  "v_cvt_pk_bf16_f32 v189, v190, v191\n\t" \
  "s_waitcnt lgkmcnt(6)\n\t" \
  "v_fma_f32 v197, |v197|, v229, v197 mul:4\n\t" \
  "v_fma_f32 v196, |v196|, v228, v196 mul:4\n\t" \
  "v_fma_mix_f32 v231, |v199|, v231, v231 op_sel:[0,1,0] op_sel_hi:[0,1,1] clamp\n\t" \
  "v_mfma_f32_16x16x32_bf16 v[44:47], v[216:219], v[154:157], v[44:47]\n\t" \
  "v_fma_mix_f32 v230, |v198|, v230, v230 op_sel:[0,1,0] op_sel_hi:[0,1,1] clamp\n\t" \
  "s_waitcnt lgkmcnt(2)\n\t" \
  "v_fma_f32 v205, |v205|, v233, v205 mul:4\n\t" \
  "v_fma_f32 v204, |v204|, v232, v204 mul:4\n\t" \
  "v_mfma_f32_16x16x32_bf16 v[68:71], v[216:219], v[162:165], v[68:71]\n\t" \
  "v_fma_f32 v207, |v207|, v235, v207 mul:4\n\t" \
  "v_fma_f32 v206, |v206|, v234, v206 mul:4\n\t" \
  "v_fma_f32 v199, |v199|, v231, v199 mul:4\n\t" \
  "v_fma_f32 v198, |v198|, v230, v198 mul:4\n\t" \
  "v_mfma_f32_16x16x32_bf16 v[92:95], v[216:219], v[170:173], v[92:95]\n\t" \
  "ds_read_b64_tr_b16 v[216:217], v249 offset:6216\n\t" \
  "v_cvt_pk_bf16_f32 v196, v196, v197\n\t" \
  "v_cvt_pk_bf16_f32 v197, v198, v199\n\t" \
  "v_cvt_pk_bf16_f32 v204, v204, v205\n\t" \
  "v_cvt_pk_bf16_f32 v205, v206, v207\n\t" \
  "s_cmp_lg_u32 s58, 0\n\t" \
  "s_cbranch_scc1 .Lcnx_fin\n\t" \
  "v_mfma_f32_32x32x16_bf16 v[144:159], v[242:245], v[240:243], 0\n\t" \
  "v_mfma_f32_32x32x16_bf16 v[160:175], v[242:245], v[240:243], 0\n\t" \
  "v_mfma_f32_16x16x32_bf16 v[88:91], v[242:245], v[236:239], v[88:91]\n\t" \
  "ds_read_b64_tr_b16 v[218:219], v249 offset:7752\n\t" \
  "v_add_u32 v232, 1536, v247\n\t" \
  "v_mfma_f32_16x16x32_bf16 v[92:95], v[240:243], v[236:239], v[92:95]\n\t" \
  "ds_read2_b32 v[98:99], v247 offset0:0 offset1:6\n\t" \
  "v_mfma_f32_16x16x32_bf16 v[0:3], v[208:211], v[178:181], v[0:3]\n\t" \
  "ds_read2_b32 v[114:115], v247 offset0:192 offset1:198\n\t" \
  "v_mfma_f32_16x16x32_bf16 v[24:27], v[208:211], v[186:189], v[24:27]\n\t" \
  "ds_read2_b32 v[130:131], v232 offset0:0 offset1:6\n\t" \
  "v_mfma_f32_16x16x32_bf16 v[48:51], v[208:211], v[194:197], v[48:51]\n\t" \
  "ds_read2_b32 v[102:103], v247 offset0:12 offset1:18\n\t" \
  "v_mfma_f32_16x16x32_bf16 v[72:75], v[208:211], v[202:205], v[72:75]\n\t" \
  "ds_read_b64_tr_b16 v[208:209], v249 offset:6224\n\t" \
  "ds_read_b64_tr_b16 v[210:211], v249 offset:7760\n\t" \
  "v_bfe_u32 v236, v251, 3, 2\n\t" \
  "s_waitcnt lgkmcnt(8)\n\t" \
  "v_mfma_f32_16x16x32_bf16 v[4:7], v[212:215], v[178:181], v[4:7]\n\t" \
  "ds_read2_b32 v[118:119], v247 offset0:204 offset1:210\n\t" \
  "ds_read2_b32 v[134:135], v232 offset0:12 offset1:18\n\t" \
  "v_mul_u32_u24 v236, 0x5555, v236\n\t" \
  "v_mfma_f32_16x16x32_bf16 v[28:31], v[212:215], v[186:189], v[28:31]\n\t" \
  "ds_read2_b32 v[106:107], v247 offset0:24 offset1:30\n\t" \
  "ds_read2_b32 v[122:123], v247 offset0:216 offset1:222\n\t" \
  "v_mfma_f32_16x16x32_bf16 v[52:55], v[212:215], v[194:197], v[52:55]\n\t" \
  "ds_read2_b32 v[138:139], v232 offset0:24 offset1:30\n\t" \
  "v_mfma_f32_16x16x32_bf16 v[76:79], v[212:215], v[202:205], v[76:79]\n\t" \
  "ds_read_b64_tr_b16 v[212:213], v249 offset:6280\n\t" \
  "s_waitcnt lgkmcnt(12)\n\t" \
  "v_mfma_f32_16x16x32_bf16 v[8:11], v[216:219], v[178:181], v[8:11]\n\t" \
  "ds_read_b64_tr_b16 v[214:215], v249 offset:7816\n\t" \
  "v_mfma_f32_16x16x32_bf16 v[32:35], v[216:219], v[186:189], v[32:35]\n\t" \
  "ds_read2_b32 v[110:111], v247 offset0:36 offset1:42\n\t" \
  "v_mfma_f32_16x16x32_bf16 v[56:59], v[216:219], v[194:197], v[56:59]\n\t" \
  "ds_read2_b32 v[126:127], v247 offset0:228 offset1:234\n\t" \
  "v_mfma_f32_16x16x32_bf16 v[80:83], v[216:219], v[202:205], v[80:83]\n\t" \
  "ds_read_b64_tr_b16 v[216:217], v249 offset:6288\n\t" \
  "v_cvt_pk_bf16_f32 v48, v48, v49\n\t" \
  "v_cvt_pk_bf16_f32 v49, v50, v51\n\t" \
  "v_cvt_pk_bf16_f32 v50, v52, v53\n\t" \
  "v_cvt_pk_bf16_f32 v51, v54, v55\n\t" \
  "v_cvt_pk_bf16_f32 v52, v0, v1\n\t" \
  "v_cvt_pk_bf16_f32 v53, v2, v3\n\t" \
  "v_cvt_pk_bf16_f32 v54, v4, v5\n\t" \
  "v_cvt_pk_bf16_f32 v55, v6, v7\n\t" \
  "v_cvt_pk_bf16_f32 v72, v72, v73\n\t" \
  "v_cvt_pk_bf16_f32 v73, v74, v75\n\t" \
  "v_cvt_pk_bf16_f32 v74, v76, v77\n\t" \
  "v_cvt_pk_bf16_f32 v75, v78, v79\n\t" \
  "v_cvt_pk_bf16_f32 v76, v24, v25\n\t" \
  "v_cvt_pk_bf16_f32 v77, v26, v27\n\t" \
  "v_cvt_pk_bf16_f32 v78, v28, v29\n\t" \
  "v_cvt_pk_bf16_f32 v79, v30, v31\n\t" \
  "s_waitcnt lgkmcnt(10)\n\t" \
  "v_mfma_f32_16x16x32_bf16 v[12:15], v[208:211], v[178:181], v[12:15]\n\t" \
  "ds_read_b64_tr_b16 v[218:219], v249 offset:7824\n\t" \
  "v_mfma_f32_16x16x32_bf16 v[36:39], v[208:211], v[186:189], v[36:39]\n\t" \
  "ds_read2_b32 v[142:143], v232 offset0:36 offset1:42\n\t" \
  "v_mfma_f32_16x16x32_bf16 v[60:63], v[208:211], v[194:197], v[60:63]\n\t" \
  "v_mfma_f32_16x16x32_bf16 v[84:87], v[208:211], v[202:205], v[84:87]\n\t" \
  "s_waitcnt lgkmcnt(5)\n\t" \
  "v_mfma_f32_16x16x32_bf16 v[16:19], v[212:215], v[178:181], v[16:19]\n\t" \
  "v_mfma_f32_16x16x32_bf16 v[40:43], v[212:215], v[186:189], v[40:43]\n\t" \
  "v_mfma_f32_16x16x32_bf16 v[64:67], v[212:215], v[194:197], v[64:67]\n\t" \
  "v_mfma_f32_16x16x32_bf16 v[88:91], v[212:215], v[202:205], v[88:91]\n\t" \
  "v_cvt_pk_bf16_f32 v56, v56, v57\n\t" \
  "v_cvt_pk_bf16_f32 v57, v58, v59\n\t" \
  "v_cvt_pk_bf16_f32 v58, v60, v61\n\t" \
  "v_cvt_pk_bf16_f32 v59, v62, v63\n\t" \
  "v_cvt_pk_bf16_f32 v60, v8, v9\n\t" \
  "v_cvt_pk_bf16_f32 v61, v10, v11\n\t" \
  "v_cvt_pk_bf16_f32 v62, v12, v13\n\t" \
  "v_cvt_pk_bf16_f32 v63, v14, v15\n\t" \
  "v_cvt_pk_bf16_f32 v80, v80, v81\n\t" \
  "v_cvt_pk_bf16_f32 v81, v82, v83\n\t" \
  "v_cvt_pk_bf16_f32 v82, v84, v85\n\t" \
  "v_cvt_pk_bf16_f32 v83, v86, v87\n\t" \
  "v_cvt_pk_bf16_f32 v84, v32, v33\n\t" \
  "v_cvt_pk_bf16_f32 v85, v34, v35\n\t" \
  "v_cvt_pk_bf16_f32 v86, v36, v37\n\t" \
  "v_cvt_pk_bf16_f32 v87, v38, v39\n\t" \
  "s_waitcnt lgkmcnt(1)\n\t" \
  "v_mfma_f32_16x16x32_bf16 v[20:23], v[216:219], v[178:181], v[20:23]\n\t" \
  "v_mfma_f32_16x16x32_bf16 v[44:47], v[216:219], v[186:189], v[44:47]\n\t" \
  "v_mfma_f32_16x16x32_bf16 v[68:71], v[216:219], v[194:197], v[68:71]\n\t" \
  "v_mfma_f32_16x16x32_bf16 v[92:95], v[216:219], v[202:205], v[92:95]\n\t" \
  "v_cvt_pk_bf16_f32 v64, v64, v65\n\t" \
  "v_cvt_pk_bf16_f32 v65, v66, v67\n\t" \
  "s_nop 4\n\t" \
  "v_cvt_pk_bf16_f32 v66, v68, v69\n\t" \
  "v_cvt_pk_bf16_f32 v67, v70, v71\n\t" \
  "v_cvt_pk_bf16_f32 v68, v16, v17\n\t" \
  "v_cvt_pk_bf16_f32 v69, v18, v19\n\t" \
  "v_cvt_pk_bf16_f32 v70, v20, v21\n\t" \
  "v_cvt_pk_bf16_f32 v71, v22, v23\n\t" \
  "v_cvt_pk_bf16_f32 v88, v88, v89\n\t" \
  "v_cvt_pk_bf16_f32 v89, v90, v91\n\t" \
  "v_cvt_pk_bf16_f32 v90, v92, v93\n\t" \
  "v_cvt_pk_bf16_f32 v91, v94, v95\n\t" \
  "v_cvt_pk_bf16_f32 v92, v40, v41\n\t" \
  "v_cvt_pk_bf16_f32 v93, v42, v43\n\t" \
  "v_cvt_pk_bf16_f32 v94, v44, v45\n\t" \
  "v_cvt_pk_bf16_f32 v95, v46, v47\n\t" \
  "s_nop 7\n\t" \
  "s_mov_b32 s57, 6144\n\t" \
  "s_mov_b32 s58, 1\n\t" \
  "s_mov_b32 s38, 0\n\t" \
  "s_mov_b32 s59, -86104\n\t" \
  "s_mov_b32 s60, -36904\n\t" \
  "s_mov_b32 s36, 2\n\t" \
  "s_mov_b32 s61, s28\n\t" \
  "s_mov_b32 s62, s29\n\t" \
  "s_mov_b32 s14, s12\n\t" \
  "s_mov_b32 s15, s13\n\t" \
  "s_add_u32 s12, s12, s65\n\t" \
  "s_add_u32 s13, s13, s65\n\t" \
  "s_add_u32 s28, s28, s65\n\t" \
  "s_add_u32 s29, s29, s65\n\t" \
  "s_sub_u32 s66, s66, 232704\n\t" \
  "s_subb_u32 s67, s67, 0\n\t" \
  "v_lshlrev_b32 v198, 1, v251\n\t" \
  "global_load_dwordx4 v[192:195], v198, s[66:67]\n\t" \
  "s_branch .Lcnx_round\n\t" \
  ".Lcnx_w9:\n\t" \
  "ds_read_b32 v246, v251 offset:47144\n\t" \
  "s_waitcnt lgkmcnt(0)\n\t" \
  "v_readfirstlane_b32 s4, v246\n\t" \
  "s_cmp_lt_u32 s4, s57\n\t" \
  "s_cbranch_scc1 .Lcnx_w9\n\t" \
  "s_branch .Lcnx_b9\n\t" \
  ".Lcnx_w12:\n\t" \
  "ds_read_b32 v246, v251 offset:47152\n\t" \
  "s_waitcnt lgkmcnt(0)\n\t" \
  "v_readfirstlane_b32 s4, v246\n\t" \
  "s_cmp_lt_u32 s4, s57\n\t" \
  "s_cbranch_scc1 .Lcnx_w12\n\t" \
  "s_branch .Lcnx_b12\n\t" \
  ".Lcnx_w24:\n\t" \
  "ds_read_b32 v246, v251 offset:47160\n\t" \
  "s_waitcnt lgkmcnt(0)\n\t" \
  "v_readfirstlane_b32 s4, v246\n\t" \
  "s_cmp_lt_u32 s4, s57\n\t" \
  "s_cbranch_scc1 .Lcnx_w24\n\t" \
  "s_branch .Lcnx_b24\n\t" \
  ".Lcnx_w36:\n\t" \
  "ds_read_b32 v246, v251 offset:47168\n\t" \
  "s_waitcnt lgkmcnt(0)\n\t" \
  "v_readfirstlane_b32 s4, v246\n\t" \
  "s_cmp_lt_u32 s4, s57\n\t" \
  "s_cbranch_scc1 .Lcnx_w36\n\t" \
  "s_branch .Lcnx_b36\n\t" \
  ".Lcnx_w48:\n\t" \
  "ds_read_b32 v246, v251 offset:47176\n\t" \
  "s_waitcnt lgkmcnt(0)\n\t" \
  "v_readfirstlane_b32 s4, v246\n\t" \
  "s_cmp_lt_u32 s4, s57\n\t" \
  "s_cbranch_scc1 .Lcnx_w48\n\t" \
  "s_branch .Lcnx_b48\n\t" \
  ".Lcnx_w60:\n\t" \
  "ds_read_b32 v246, v251 offset:47184\n\t" \
  "s_waitcnt lgkmcnt(0)\n\t" \
  "v_readfirstlane_b32 s4, v246\n\t" \
  "s_cmp_lt_u32 s4, s57\n\t" \
  "s_cbranch_scc1 .Lcnx_w60\n\t" \
  "s_branch .Lcnx_b60\n\t" \
  ".Lcnx_w72:\n\t" \
  "ds_read_b32 v246, v251 offset:47192\n\t" \
  "s_waitcnt lgkmcnt(0)\n\t" \
  "v_readfirstlane_b32 s4, v246\n\t" \
  "s_cmp_lt_u32 s4, s57\n\t" \
  "s_cbranch_scc1 .Lcnx_w72\n\t" \
  "s_branch .Lcnx_b72\n\t" \
  ".Lcnx_w84:\n\t" \
  "ds_read_b32 v246, v251 offset:47200\n\t" \
  "s_waitcnt lgkmcnt(0)\n\t" \
  "v_readfirstlane_b32 s4, v246\n\t" \
  "s_cmp_lt_u32 s4, s57\n\t" \
  "s_cbranch_scc1 .Lcnx_w84\n\t" \
  "s_branch .Lcnx_b84\n\t" \
  ".Lcnx_w96:\n\t" \
  "ds_read_b32 v246, v251 offset:47208\n\t" \
  "s_waitcnt lgkmcnt(0)\n\t" \
  "v_readfirstlane_b32 s4, v246\n\t" \
  "s_cmp_lt_u32 s4, s57\n\t" \
  "s_cbranch_scc1 .Lcnx_w96\n\t" \
  "s_branch .Lcnx_b96\n\t" \
  ".Lcnx_w108:\n\t" \
  "ds_read_b32 v246, v251 offset:47216\n\t" \
  "s_waitcnt lgkmcnt(0)\n\t" \
  "v_readfirstlane_b32 s4, v246\n\t" \
  "s_cmp_lt_u32 s4, s57\n\t" \
  "s_cbranch_scc1 .Lcnx_w108\n\t" \
  "s_branch .Lcnx_b108\n\t" \
  ".Lcnx_w120:\n\t" \
  "ds_read_b32 v246, v251 offset:47224\n\t" \
  "s_waitcnt lgkmcnt(0)\n\t" \
  "v_readfirstlane_b32 s4, v246\n\t" \
  "s_cmp_lt_u32 s4, s57\n\t" \
  "s_cbranch_scc1 .Lcnx_w120\n\t" \
  "s_branch .Lcnx_b120\n\t" \
  ".Lcnx_w132:\n\t" \
  "ds_read_b32 v246, v251 offset:47232\n\t" \
  "s_waitcnt lgkmcnt(0)\n\t" \
  "v_readfirstlane_b32 s4, v246\n\t" \
  "s_cmp_lt_u32 s4, s57\n\t" \
  "s_cbranch_scc1 .Lcnx_w132\n\t" \
  "s_branch .Lcnx_b132\n\t" \
  ".Lcnx_w138:\n\t" \
  "ds_read_b32 v246, v251 offset:47240\n\t" \
  "s_waitcnt lgkmcnt(0)\n\t" \
  "v_readfirstlane_b32 s4, v246\n\t" \
  "s_cmp_lt_u32 s4, s57\n\t" \
  "s_cbranch_scc1 .Lcnx_w138\n\t" \
  "s_branch .Lcnx_b138\n\t" \
  ".Lcnx_f0:\n\t" \
  "s_mov_b32 m0, s44\n\t" \
  "s_nop 0\n\t" \
  "buffer_load_dwordx4 v252, s[16:19], s33 offen lds\n\t" \
  "buffer_load_dwordx4 v252, s[16:19], s34 offen offset:1024 lds\n\t" \
  "buffer_load_dwordx4 v252, s[16:19], s35 offen offset:2048 lds\n\t" \
  "s_add_u32 s33, s33, s32\n\t" \
  "s_add_u32 s34, s34, s32\n\t" \
  "s_add_u32 s35, s35, s32\n\t" \
  "s_branch .Lcnx_g0\n\t" \
  ".Lcnx_f1:\n\t" \
  "s_mov_b32 m0, s45\n\t" \
  "s_nop 0\n\t" \
  "buffer_load_dwordx4 v252, s[16:19], s33 offen lds\n\t" \
  "buffer_load_dwordx4 v252, s[16:19], s34 offen offset:1024 lds\n\t" \
  "buffer_load_dwordx4 v252, s[16:19], s35 offen offset:2048 lds\n\t" \
  "s_add_u32 s33, s33, s32\n\t" \
  "s_add_u32 s34, s34, s32\n\t" \
  "s_add_u32 s35, s35, s32\n\t" \
  "s_branch .Lcnx_g1\n\t" \
  ".Lcnx_f2:\n\t" \
  "s_mov_b32 m0, s46\n\t" \
  "s_nop 0\n\t" \
  "buffer_load_dwordx4 v252, s[16:19], s33 offen lds\n\t" \
  "buffer_load_dwordx4 v252, s[16:19], s34 offen offset:1024 lds\n\t" \
  "buffer_load_dwordx4 v252, s[16:19], s35 offen offset:2048 lds\n\t" \
  "s_add_u32 s33, s33, s32\n\t" \
  "s_add_u32 s34, s34, s32\n\t" \
  "s_add_u32 s35, s35, s32\n\t" \
  "s_branch .Lcnx_g2\n\t" \
  ".Lcnx_f3:\n\t" \
  "s_mov_b32 m0, s47\n\t" \
  "s_nop 0\n\t" \
  "buffer_load_dwordx4 v252, s[16:19], s33 offen lds\n\t" \
  "buffer_load_dwordx4 v252, s[16:19], s34 offen offset:1024 lds\n\t" \
  "buffer_load_dwordx4 v252, s[16:19], s35 offen offset:2048 lds\n\t" \
  "s_add_u32 s33, s33, s32\n\t" \
  "s_add_u32 s34, s34, s32\n\t" \
  "s_add_u32 s35, s35, s32\n\t" \
  "s_branch .Lcnx_g3\n\t" \
  ".Lcnx_f4:\n\t" \
  "s_mov_b32 m0, s44\n\t" \
  "s_nop 0\n\t" \
  "buffer_load_dwordx4 v252, s[16:19], s33 offen lds\n\t" \
  "buffer_load_dwordx4 v252, s[16:19], s34 offen offset:1024 lds\n\t" \
  "buffer_load_dwordx4 v252, s[16:19], s35 offen offset:2048 lds\n\t" \
  "s_add_u32 s33, s33, s32\n\t" \
  "s_add_u32 s34, s34, s32\n\t" \
  "s_add_u32 s35, s35, s32\n\t" \
  "s_cmp_lg_u32 s58, 1\n\t" \
  "s_cbranch_scc1 .Lcnx_g4\n\t" \
  "s_add_u32 m0, s44, s50\n\t" \
  "s_add_u32 s4, s33, s51\n\t" \
  "buffer_load_dwordx4 v252, s[16:19], s4 offen offset:0 lds\n\t" \
  "s_add_u32 s4, s34, s51\n\t" \
  "buffer_load_dwordx4 v252, s[16:19], s4 offen offset:1024 lds\n\t" \
  "s_add_u32 s4, s35, s51\n\t" \
  "buffer_load_dwordx4 v252, s[16:19], s4 offen offset:2048 lds\n\t" \
  "s_branch .Lcnx_g4\n\t" \
  ".Lcnx_f5:\n\t" \
  "s_mov_b32 m0, s45\n\t" \
  "s_nop 0\n\t" \
  "buffer_load_dwordx4 v252, s[16:19], s33 offen lds\n\t" \
  "buffer_load_dwordx4 v252, s[16:19], s34 offen offset:1024 lds\n\t" \
  "buffer_load_dwordx4 v252, s[16:19], s35 offen offset:2048 lds\n\t" \
  "s_add_u32 s33, s33, s32\n\t" \
  "s_add_u32 s34, s34, s32\n\t" \
  "s_add_u32 s35, s35, s32\n\t" \
  "s_cmp_lg_u32 s58, 1\n\t" \
  "s_cbranch_scc1 .Lcnx_g5\n\t" \
  "s_add_u32 m0, s45, s50\n\t" \
  "s_add_u32 s4, s33, s51\n\t" \
  "buffer_load_dwordx4 v252, s[16:19], s4 offen offset:0 lds\n\t" \
  "s_add_u32 s4, s34, s51\n\t" \
  "buffer_load_dwordx4 v252, s[16:19], s4 offen offset:1024 lds\n\t" \
  "s_add_u32 s4, s35, s51\n\t" \
  "buffer_load_dwordx4 v252, s[16:19], s4 offen offset:2048 lds\n\t" \
  "s_branch .Lcnx_g5\n\t" \
  ".Lcnx_f6:\n\t" \
  "s_mov_b32 m0, s46\n\t" \
  "s_nop 0\n\t" \
  "buffer_load_dwordx4 v252, s[16:19], s33 offen lds\n\t" \
  "buffer_load_dwordx4 v252, s[16:19], s34 offen offset:1024 lds\n\t" \
  "buffer_load_dwordx4 v252, s[16:19], s35 offen offset:2048 lds\n\t" \
  "s_add_u32 s33, s33, s32\n\t" \
  "s_add_u32 s34, s34, s32\n\t" \
  "s_add_u32 s35, s35, s32\n\t" \
  "s_cmp_lg_u32 s58, 1\n\t" \
  "s_cbranch_scc1 .Lcnx_g6\n\t" \
  "s_cmp_lt_u32 s39, 1024\n\t" \
  "s_cbranch_scc1 .Lcnx_g6\n\t" \
  "s_add_u32 m0, s46, s50\n\t" \
  "s_add_u32 s4, s33, s51\n\t" \
  "buffer_load_dwordx4 v252, s[16:19], s4 offen offset:0 lds\n\t" \
  "s_add_u32 s4, s34, s51\n\t" \
  "buffer_load_dwordx4 v252, s[16:19], s4 offen offset:1024 lds\n\t" \
  "s_add_u32 s4, s35, s51\n\t" \
  "buffer_load_dwordx4 v252, s[16:19], s4 offen offset:2048 lds\n\t" \
  "s_branch .Lcnx_g6\n\t" \
  ".Lcnx_f7:\n\t" \
  "s_mov_b32 m0, s47\n\t" \
  "s_nop 0\n\t" \
  "buffer_load_dwordx4 v252, s[16:19], s33 offen lds\n\t" \
  "buffer_load_dwordx4 v252, s[16:19], s34 offen offset:1024 lds\n\t" \
  "buffer_load_dwordx4 v252, s[16:19], s35 offen offset:2048 lds\n\t" \
  "s_add_u32 s33, s33, s32\n\t" \
  "s_add_u32 s34, s34, s32\n\t" \
  "s_add_u32 s35, s35, s32\n\t" \
  "s_cmp_lg_u32 s58, 1\n\t" \
  "s_cbranch_scc1 .Lcnx_g7\n\t" \
  "s_cmp_lt_u32 s39, 1024\n\t" \
  "s_cbranch_scc1 .Lcnx_g7\n\t" \
  "s_add_u32 m0, s47, s50\n\t" \
  "s_add_u32 s4, s33, s51\n\t" \
  "buffer_load_dwordx4 v252, s[16:19], s4 offen offset:0 lds\n\t" \
  "s_add_u32 s4, s34, s51\n\t" \
  "buffer_load_dwordx4 v252, s[16:19], s4 offen offset:1024 lds\n\t" \
  "s_add_u32 s4, s35, s51\n\t" \
  "buffer_load_dwordx4 v252, s[16:19], s4 offen offset:2048 lds\n\t" \
  "s_branch .Lcnx_g7\n\t" \
  ".Lcnx_f8:\n\t" \
  "s_mov_b32 m0, s44\n\t" \
  "s_nop 0\n\t" \
  "buffer_load_dwordx4 v252, s[16:19], s33 offen lds\n\t" \
  "buffer_load_dwordx4 v252, s[16:19], s34 offen offset:1024 lds\n\t" \
  "buffer_load_dwordx4 v252, s[16:19], s35 offen offset:2048 lds\n\t" \
  "s_add_u32 s33, s33, s32\n\t" \
  "s_add_u32 s34, s34, s32\n\t" \
  "s_add_u32 s35, s35, s32\n\t" \
  "s_branch .Lcnx_g8\n\t" \
  ".Lcnx_f9:\n\t" \
  "s_mov_b32 m0, s45\n\t" \
  "s_nop 0\n\t" \
  "buffer_load_dwordx4 v252, s[16:19], s33 offen lds\n\t" \
  "buffer_load_dwordx4 v252, s[16:19], s34 offen offset:1024 lds\n\t" \
  "buffer_load_dwordx4 v252, s[16:19], s35 offen offset:2048 lds\n\t" \
  "s_add_u32 s33, s33, s32\n\t" \
  "s_add_u32 s34, s34, s32\n\t" \
  "s_add_u32 s35, s35, s32\n\t" \
  "s_sub_u32 s33, s33, s48\n\t" \
  "s_sub_u32 s34, s34, s48\n\t" \
  "s_sub_u32 s35, s35, s48\n\t" \
  "s_branch .Lcnx_g9\n\t" \
  ".Lcnx_f10:\n\t" \
  "s_mov_b32 m0, s46\n\t" \
  "s_nop 0\n\t" \
  "buffer_load_dwordx4 v252, s[16:19], s33 offen lds\n\t" \
  "buffer_load_dwordx4 v252, s[16:19], s34 offen offset:1024 lds\n\t" \
  "buffer_load_dwordx4 v252, s[16:19], s35 offen offset:2048 lds\n\t" \
  "s_add_u32 s33, s33, s32\n\t" \
  "s_add_u32 s34, s34, s32\n\t" \
  "s_add_u32 s35, s35, s32\n\t" \
  "s_branch .Lcnx_g10\n\t" \
  ".Lcnx_f11:\n\t" \
  "s_mov_b32 m0, s47\n\t" \
  "s_nop 0\n\t" \
  "buffer_load_dwordx4 v252, s[16:19], s33 offen lds\n\t" \
  "buffer_load_dwordx4 v252, s[16:19], s34 offen offset:1024 lds\n\t" \
  "buffer_load_dwordx4 v252, s[16:19], s35 offen offset:2048 lds\n\t" \
  "s_add_u32 s33, s33, s32\n\t" \
  "s_add_u32 s34, s34, s32\n\t" \
  "s_add_u32 s35, s35, s32\n\t" \
  "s_sub_u32 s33, s33, s49\n\t" \
  "s_sub_u32 s34, s34, s49\n\t" \
  "s_sub_u32 s35, s35, s49\n\t" \
  "s_branch .Lcnx_g11\n\t" \
  ".Lcnx_fin:\n\t" \
  "v_mfma_f32_16x16x32_bf16 v[88:91], v[242:245], v[236:239], v[88:91]\n\t" \
  "ds_read_b64_tr_b16 v[218:219], v249 offset:7752\n\t" \
  "v_mfma_f32_16x16x32_bf16 v[92:95], v[240:243], v[236:239], v[92:95]\n\t" \
  "v_mfma_f32_16x16x32_bf16 v[0:3], v[208:211], v[178:181], v[0:3]\n\t" \
  "v_mfma_f32_16x16x32_bf16 v[24:27], v[208:211], v[186:189], v[24:27]\n\t" \
  "v_mfma_f32_16x16x32_bf16 v[48:51], v[208:211], v[194:197], v[48:51]\n\t" \
  "v_mfma_f32_16x16x32_bf16 v[72:75], v[208:211], v[202:205], v[72:75]\n\t" \
  "ds_read_b64_tr_b16 v[208:209], v249 offset:6224\n\t" \
  "s_waitcnt lgkmcnt(3)\n\t" \
  "v_mfma_f32_16x16x32_bf16 v[4:7], v[212:215], v[178:181], v[4:7]\n\t" \
  "ds_read_b64_tr_b16 v[210:211], v249 offset:7760\n\t" \
  "v_mfma_f32_16x16x32_bf16 v[28:31], v[212:215], v[186:189], v[28:31]\n\t" \
  "v_mfma_f32_16x16x32_bf16 v[52:55], v[212:215], v[194:197], v[52:55]\n\t" \
  "v_mfma_f32_16x16x32_bf16 v[76:79], v[212:215], v[202:205], v[76:79]\n\t" \
  "ds_read_b64_tr_b16 v[212:213], v249 offset:6280\n\t" \
  "s_waitcnt lgkmcnt(3)\n\t" \
  "v_mfma_f32_16x16x32_bf16 v[8:11], v[216:219], v[178:181], v[8:11]\n\t" \
  "ds_read_b64_tr_b16 v[214:215], v249 offset:7816\n\t" \
  "v_mfma_f32_16x16x32_bf16 v[32:35], v[216:219], v[186:189], v[32:35]\n\t" \
  "v_mfma_f32_16x16x32_bf16 v[56:59], v[216:219], v[194:197], v[56:59]\n\t" \
  "v_mfma_f32_16x16x32_bf16 v[80:83], v[216:219], v[202:205], v[80:83]\n\t" \
  "ds_read_b64_tr_b16 v[216:217], v249 offset:6288\n\t" \
  "v_cvt_pk_bf16_f32 v0, v0, v1\n\t" \
  "v_cvt_pk_bf16_f32 v1, v2, v3\n\t" \
  "v_cvt_pk_bf16_f32 v2, v4, v5\n\t" \
  "v_cvt_pk_bf16_f32 v3, v6, v7\n\t" \
  "buffer_store_dwordx4 v[0:3], v250, s[20:23], s28 offen offset:0 sc1\n\t" \
  "v_cvt_pk_bf16_f32 v24, v24, v25\n\t" \
  "v_cvt_pk_bf16_f32 v25, v26, v27\n\t" \
  "v_cvt_pk_bf16_f32 v26, v28, v29\n\t" \
  "v_cvt_pk_bf16_f32 v27, v30, v31\n\t" \
  "buffer_store_dwordx4 v[24:27], v250, s[20:23], s29 offen offset:0 sc1\n\t" \
  "v_cvt_pk_bf16_f32 v48, v48, v49\n\t" \
  "v_cvt_pk_bf16_f32 v49, v50, v51\n\t" \
  "v_cvt_pk_bf16_f32 v50, v52, v53\n\t" \
  "v_cvt_pk_bf16_f32 v51, v54, v55\n\t" \
  "buffer_store_dwordx4 v[48:51], v250, s[20:23], s12 offen offset:0 sc1\n\t" \
  "v_cvt_pk_bf16_f32 v72, v72, v73\n\t" \
  "v_cvt_pk_bf16_f32 v73, v74, v75\n\t" \
  "v_cvt_pk_bf16_f32 v74, v76, v77\n\t" \
  "v_cvt_pk_bf16_f32 v75, v78, v79\n\t" \
  "buffer_store_dwordx4 v[72:75], v250, s[20:23], s13 offen offset:0 sc1\n\t" \
  "s_waitcnt lgkmcnt(3)\n\t" \
  "v_mfma_f32_16x16x32_bf16 v[12:15], v[208:211], v[178:181], v[12:15]\n\t" \
  "ds_read_b64_tr_b16 v[218:219], v249 offset:7824\n\t" \
  "v_mfma_f32_16x16x32_bf16 v[36:39], v[208:211], v[186:189], v[36:39]\n\t" \
  "v_mfma_f32_16x16x32_bf16 v[60:63], v[208:211], v[194:197], v[60:63]\n\t" \
  "v_mfma_f32_16x16x32_bf16 v[84:87], v[208:211], v[202:205], v[84:87]\n\t" \
  "s_waitcnt lgkmcnt(2)\n\t" \
  "v_mfma_f32_16x16x32_bf16 v[16:19], v[212:215], v[178:181], v[16:19]\n\t" \
  "v_mfma_f32_16x16x32_bf16 v[40:43], v[212:215], v[186:189], v[40:43]\n\t" \
  "v_mfma_f32_16x16x32_bf16 v[64:67], v[212:215], v[194:197], v[64:67]\n\t" \
  "v_mfma_f32_16x16x32_bf16 v[88:91], v[212:215], v[202:205], v[88:91]\n\t" \
  "v_cvt_pk_bf16_f32 v8, v8, v9\n\t" \
  "v_cvt_pk_bf16_f32 v9, v10, v11\n\t" \
  "v_cvt_pk_bf16_f32 v10, v12, v13\n\t" \
  "v_cvt_pk_bf16_f32 v11, v14, v15\n\t" \
  "buffer_store_dwordx4 v[8:11], v250, s[20:23], s28 offen offset:64 sc1\n\t" \
  "v_cvt_pk_bf16_f32 v32, v32, v33\n\t" \
  "v_cvt_pk_bf16_f32 v33, v34, v35\n\t" \
  "v_cvt_pk_bf16_f32 v34, v36, v37\n\t" \
  "v_cvt_pk_bf16_f32 v35, v38, v39\n\t" \
  "buffer_store_dwordx4 v[32:35], v250, s[20:23], s29 offen offset:64 sc1\n\t" \
  "v_cvt_pk_bf16_f32 v56, v56, v57\n\t" \
  "v_cvt_pk_bf16_f32 v57, v58, v59\n\t" \
  "v_cvt_pk_bf16_f32 v58, v60, v61\n\t" \
  "v_cvt_pk_bf16_f32 v59, v62, v63\n\t" \
  "buffer_store_dwordx4 v[56:59], v250, s[20:23], s12 offen offset:64 sc1\n\t" \
  "v_cvt_pk_bf16_f32 v80, v80, v81\n\t" \
  "v_cvt_pk_bf16_f32 v81, v82, v83\n\t" \
  "v_cvt_pk_bf16_f32 v82, v84, v85\n\t" \
  "v_cvt_pk_bf16_f32 v83, v86, v87\n\t" \
  "buffer_store_dwordx4 v[80:83], v250, s[20:23], s13 offen offset:64 sc1\n\t" \
  "s_waitcnt lgkmcnt(0)\n\t" \
  "v_mfma_f32_16x16x32_bf16 v[20:23], v[216:219], v[178:181], v[20:23]\n\t" \
  "v_mfma_f32_16x16x32_bf16 v[44:47], v[216:219], v[186:189], v[44:47]\n\t" \
  "v_mfma_f32_16x16x32_bf16 v[68:71], v[216:219], v[194:197], v[68:71]\n\t" \
  "v_mfma_f32_16x16x32_bf16 v[92:95], v[216:219], v[202:205], v[92:95]\n\t" \
  "v_cvt_pk_bf16_f32 v16, v16, v17\n\t" \
  "v_cvt_pk_bf16_f32 v17, v18, v19\n\t" \
  "s_nop 2\n\t" \
  "v_cvt_pk_bf16_f32 v18, v20, v21\n\t" \
  "v_cvt_pk_bf16_f32 v19, v22, v23\n\t" \
  "buffer_store_dwordx4 v[16:19], v250, s[20:23], s28 offen offset:128 sc1\n\t" \
  "v_cvt_pk_bf16_f32 v40, v40, v41\n\t" \
  "v_cvt_pk_bf16_f32 v41, v42, v43\n\t" \
  "v_cvt_pk_bf16_f32 v42, v44, v45\n\t" \
  "v_cvt_pk_bf16_f32 v43, v46, v47\n\t" \
  "buffer_store_dwordx4 v[40:43], v250, s[20:23], s29 offen offset:128 sc1\n\t" \
  "v_cvt_pk_bf16_f32 v64, v64, v65\n\t" \
  "v_cvt_pk_bf16_f32 v65, v66, v67\n\t" \
  "v_cvt_pk_bf16_f32 v66, v68, v69\n\t" \
  "v_cvt_pk_bf16_f32 v67, v70, v71\n\t" \
  "buffer_store_dwordx4 v[64:67], v250, s[20:23], s12 offen offset:128 sc1\n\t" \
  "v_cvt_pk_bf16_f32 v88, v88, v89\n\t" \
  "v_cvt_pk_bf16_f32 v89, v90, v91\n\t" \
  "v_cvt_pk_bf16_f32 v90, v92, v93\n\t" \
  "v_cvt_pk_bf16_f32 v91, v94, v95\n\t" \
  "buffer_store_dwordx4 v[88:91], v250, s[20:23], s13 offen offset:128 sc1\n\t" \
  "s_waitcnt vmcnt(0)\n\t" \
  ".Lcnx_end:\n\t" \
  ""


__device__ __forceinline__ unsigned lds_addr(const char* p) { return (unsigned)(uintptr_t)(lds_ptr_t)p; }
__device__ __forceinline__ unsigned sg(unsigned v) { return __builtin_amdgcn_readfirstlane(v); }

constexpr double tanh_u(double y) {
    const double v = 8 * y, a = 2 * 0.79788456080286536 * (v + 0.044715 * v * v * v) / 1024;
    double m = 1;
    for (int i = 12; i > 1; --i) m = 1 + m * a / i;
    m *= a;
    for (int i = 0; i < 10; ++i) m *= m + 2;
    return m / (m + 2);
}
constexpr double h2d(int h) {
    if (h < 1024) return 0;
    double x = (1024 + (h & 1023)) * 0x1p-24;
    for (int e = 1; e < (h >> 10); ++e) x *= 2;
    return x;
}
constexpr int d2h(double x) {
    int h = 0;
    for (int s = 1 << 14; s; s >>= 1) if (h2d(h + s) <= x) h += s;
    return h;
}
constexpr unsigned ip_entry(int k) {
    if (k == 127) return 0x3c003c00u;
    const double y0 = k ? (256 * k - 0.5) / 32767 : 0, dy = ((256 * k + 255.5) / 32767 - y0) / 8;
    double t[9] = {};
    for (int i = 0; i < 9; ++i) t[i] = tanh_u(y0 + i * dy);
    const double b0 = (t[8] - t[0]) / (8 * dy), ym = y0 + 4 * dy, tm = (t[4] + (t[0] + t[8]) / 2) / 2;
    unsigned best = 0;
    double err = 1;
    for (int ha = d2h(tm - b0 * ym) - 1, i = 0; i < 4; ++i, ++ha)
        for (int hb = d2h((tm - h2d(ha)) / ym), j = 0; j < 2; ++j, ++hb) {
            double e = 0;
            for (int n = 0; n < 9; ++n) {
                const double s = h2d(ha) + h2d(hb) * (y0 + n * dy), d = (s < 1 ? s : 1) - t[n];
                e = d > e ? d : -d > e ? -d : e;
            }
            if (e < err) err = e, best = (ha < 1024 ? 0 : ha) | (hb < 1024 ? 0 : hb) << 16;
        }
    return best;
}
struct TanhTab { unsigned v[1024]; };
constexpr TanhTab tanh_tab(int k0) {
    TanhTab t = {};
    for (int k = k0; k < k0 + 1024 && k < DWOFF / 4; ++k) {
        double y = ((k & 255) + 0.375 + (k >= 256 ? 256 : 0)) * 0x1p-22;
        for (int e = 1; e < (k >> 8); ++e) y *= 2;
        t.v[k - k0] = k < IPOFF / 4 ? __builtin_bit_cast(unsigned, (float)tanh_u(y)) : ip_entry(k - IPOFF / 4);
    }
    return t;
}
struct WaveTab { unsigned v192[64][4], v244[64][4], v16[64][4], s32[32]; };
struct RegTab { unsigned v240[64][4], v248[64][4]; WaveTab w[8]; };
constexpr unsigned cpos(unsigned w, unsigned px) { return (w >> 1) * 1024 + (px ^ (w & 2) << 2) * 16 + ((w ^ px >> 4) & 1) * 8; }
constexpr RegTab reg_tab() {
    RegTab t = {};
    for (unsigned l = 0; l < 64; ++l) {
        const unsigned G = l >> 4, n = l & 15, q = n >> 2, p = l & 3;
        t.v240[l][2 + (p >> 1)] = G == q ? (p & 1 ? 0x3f800000u : 0x3f80u) : 0u;
        t.v248[l][0] = WB + (l >> 2) * 32 + (p & 1) * 16 + (p >> 1) * 8;
        t.v248[l][1] = WB + (G & 1) * 3080 + ((G >> 1) * 4 + q) * 192 + p * 16;
        t.v248[l][2] = (n & 7) * 384 + (n >> 3) * 192 + G * 16;
        t.v248[l][3] = PB + l * 8;
        for (unsigned w = 0; w < 8; ++w) {
            t.w[w].v244[l][2] = XOFF + n * 400 + (G & 1) * XROW + (G >> 1) * 192 + w * 24;
            t.w[w].v244[l][3] = DWOFF + p * 192 + (1 - (q >> 1)) * 24 + (1 + (G & 1) - (q & 1)) * 8 + ((G ^ p) >> 1) * 4 + w * 2304;
            const unsigned pl = PTOP + (w >> 2) * 12288, blk = (w & 3) * 16;
            t.w[w].v192[l][0] = XOFF + cpos(w, (G & 2) * 16 + (n & 8) * 2 + (G & 1) * 8 + (n & 7));
            t.w[w].v192[l][1] = t.w[w].v192[l][0] + PTOP - XOFF;
            t.w[w].v192[l][2] = pl + (cpos((G & 1) * 4 + (G & 2), blk + n) & ~8u);
            t.w[w].v192[l][3] = pl + cpos(p, blk + (l >> 2));
            t.w[w].v16[l][0] = w >= 4 ? (l < 48 ? l * 128 : OOB)
                             : w < 2  ? (12 * ((l >> 3) & 3) + ((l >> 1) & 3)) * (HID * 2) + (2 * (l & 1) + (l >> 5)) * 16 : l * 16;
            const unsigned d = DWOFF + (w * 8 + (l >> 5) * 4 + p) * 768 + ((l >> 2) & 7) * 24;
            t.w[w].v16[l][1] = w < 3 ? d + (l & 1) * 192 : XOFF - 32 + p * 8;
            t.w[w].v16[l][2] = d + (~l & 1) * 192;
            t.w[w].v16[l][3] = l & 1 ? 0x07060302u : 0x05040100u;
        }
    }
    for (unsigned w = 0; w < 8; ++w) {
        const bool w1 = w < 2;
        const unsigned wpd = w1 ? 4 * HID * 2 - 1024 : 0, wl = WB + (w & 1) * (w1 ? 3072 : 3080), wc = w1 ? 64 : 6144;
        const unsigned so = (w & 1) * (w1 ? 48 * HID * 2 : 3072);
        const unsigned s[32] = {wc, so, so + wpd, so + 2 * wpd, w, w >> 2, w, w * 512,
                                0xaaaaaaaau, 0xaaaaaaaau, 0xccccccccu, 0xccccccccu,
                                wl + (w1 ? WA + 12304 : WA), wl + (w1 ? 12304 : WA + 6152), wl + (w1 ? WA + 18448 : 0),
                                wl + (w1 ? WA + 18448 + 6144 : 6152), w1 ? 12 * wc : 0, w1 ? 0 : 12 * wc,
                                w1 ? 0u - 86104 : 0u - 36904, 3 * wc, ~0x00f0000fu, ~0xf0000f00u, (w >> 2) * (PTOP - XOFF - 5 * 12288), 0, 0x3ffc, 3072, 0, 0, 0,
                                OOB, OOB, 8 * (unsigned)sizeof(WaveTab) - 2 * PB};
        for (int i = 0; i < 32; ++i) t.w[w].s32[i] = s[i];
    }
    return t;
}
struct Tables { RegTab r; TanhTab t[4]; };
static_assert(sizeof(WaveTab) == 64 * 50 && sizeof(RegTab) == 27648, "table layout");
__device__ const __attribute__((aligned(128))) Tables TABLES = {reg_tab(), {tanh_tab(0), tanh_tab(1024), tanh_tab(2048), tanh_tab(3072)}};

#define R10(r, a) r #a "0", r #a "1", r #a "2", r #a "3", r #a "4", r #a "5", r #a "6", r #a "7", r #a "8", r #a "9"
#define V10(a) R10("v", a)
#define S10(a) R10("s", a)
#define MAIN_CLOB "memory", "scc", "vcc", "v1", "v2", "v3", "v4", "v5", "v6", "v7", "v8", "v9", V10(1), V10(2), V10(3), V10(4), V10(5),  \
    V10(6), V10(7), V10(8), V10(9), V10(10), V10(11), V10(12), V10(13), V10(14), V10(15), V10(16), V10(17), V10(18), V10(19),        \
    V10(20), V10(21), V10(22), V10(23), V10(24), "v250", "v251", "v252", "v253", "v254", "s4", "s5", "s8", "s9", S10(1),       \
    S10(2), S10(3), S10(4), S10(5), "s60", "s61", "s62", "s63", "s64", "s65", "s66", "s67"

__global__ __launch_bounds__(512, 2) void convnext_kernel(const __hip_bfloat16* __restrict__ x,
                                                          const __hip_bfloat16* __restrict__ dw,
                                                          const __hip_bfloat16* __restrict__ w1,
                                                          const __hip_bfloat16* __restrict__ w2,
                                                          __hip_bfloat16* __restrict__ out) {
    __shared__ __attribute__((aligned(16))) char smem[LDS_BYTES];

    unsigned tid = threadIdx.x, lds = lds_addr(smem);
    const unsigned bid = blockIdx.x;
    asm volatile(MAIN_ASM
                 : "+{v0}"(tid), "+{s3}"(lds)
                 : "{s[0:1]}"(__builtin_amdgcn_kernarg_segment_ptr()), "{s2}"(bid), "{s[6:7]}"(TABLES.r.w)
                 : MAIN_CLOB);

}

}

extern "C" void kernel_launch(void* const* d_in, const int* in_sizes, int n_in, void* d_out, int out_size,
                              void* d_ws, size_t ws_size, hipStream_t stream) {
    (void)in_sizes; (void)n_in; (void)out_size; (void)d_ws; (void)ws_size;
    const __hip_bfloat16* x = (const __hip_bfloat16*)d_in[0];
    const __hip_bfloat16* dw = (const __hip_bfloat16*)d_in[1];
    const __hip_bfloat16* w1 = (const __hip_bfloat16*)d_in[2];
    const __hip_bfloat16* w2 = (const __hip_bfloat16*)d_in[3];
    __hip_bfloat16* out = (__hip_bfloat16*)d_out;
    convnext_kernel<<<256, 512, 0, stream>>>(x, dw, w1, w2, out);
}
```

```cpp
#include <hip/hip_runtime.h>
#include <hip/hip_bf16.h>
#include <stdint.h>

namespace {

typedef __attribute__((address_space(3))) void* lds_ptr_t;

constexpr int IMG = 512;
constexpr int CH = 96;
constexpr int HID = 384;
constexpr int ROWB = IMG * CH * 2;
constexpr int LUTN = 3841;
constexpr int IPOFF = 15376;
constexpr int DWOFF = IPOFF + 512;
constexpr int XOFF = DWOFF + 96 * 192 + 32;
constexpr int XROW = 8200;
constexpr int PB = XOFF + 10 * XROW;
constexpr int WB = XOFF + 9 * XROW;
constexpr int WA = 3 * XROW;
constexpr int PTOP = XOFF + 12 * XROW - 12288;
constexpr int CNTOFF = WB + WA + 18448 + 12288;
constexpr int PFOFF = CNTOFF + 144;
constexpr int LDS_BYTES = 160 * 1024;
static_assert(PB == 116352 && CNTOFF - PB == 47136 && LUTN * 4 <= IPOFF && XOFF % 16 == 0 && PFOFF + 48 * 4 <= LDS_BYTES, "LDS map");
constexpr unsigned OOB = 0x40000000u;

#define MAIN_ASM \
  "s_load_dwordx2 s[8:9], s[0:1], 0x0\n\t" \
  "s_load_dwordx2 s[12:13], s[0:1], 0x8\n\t" \
  "s_load_dwordx2 s[20:21], s[0:1], 0x20\n\t" \
  "v_readfirstlane_b32 s5, v0\n\t" \
  "v_and_b32 v21, 63, v0\n\t" \
  "s_mov_b32 m0, 0\n\t" \
  "s_cmp_lt_u32 s5, 128\n\t" \
  "s_cselect_b32 s3, 16, 24\n\t" \
  "v_lshlrev_b32 v22, 4, v21\n\t" \
  "s_load_dwordx2 s[16:17], s[0:1], s3\n\t" \
  "s_mul_i32 s4, s5, 50\n\t" \
  "s_add_u32 s66, s6, s4\n\t" \
  "s_addc_u32 s67, s7, 0\n\t" \
  "global_load_dwordx4 v[244:247], v22, s[66:67] offset:1024\n\t" \
  "global_load_dwordx4 v[16:19], v22, s[66:67] offset:2048\n\t" \
  "global_load_dwordx4 v[240:243], v22, s[6:7] offset:-2048\n\t" \
  "global_load_dwordx4 v[248:251], v22, s[6:7] offset:-1024\n\t" \
  "global_load_dwordx4 v[192:195], v22, s[66:67]\n\t" \
  "s_cmp_lg_u32 s5, 192\n\t" \
  "s_cbranch_scc1 .Lcnx_nc\n\t" \
  "s_getpc_b64 s[14:15]\n\t" \
  ".Lcnx_pc:\n\t" \
  "s_lshr_b32 s3, s2, 3\n\t" \
  "s_lshl_b32 s3, s3, 11\n\t" \
  "v_lshl_add_u32 v1, v21, 5, s3\n\t" \
  "v_min_u32 v1, .Lcnx_end-.Lcnx_pc, v1\n\t" \
  "global_load_lds_dword v1, s[14:15]\n\t" \
  ".Lcnx_nc:\n\t" \
  "v_mul_u32_u24 v254, 0x147b, v0\n\t" \
  "s_bfe_u32 s27, s2, 0x40003\n\t" \
  "v_bfe_u32 v1, v0, 2, 3\n\t" \
  "s_mul_i32 s27, s27, 6144\n\t" \
  "v_lshrrev_b32 v254, 17, v254\n\t" \
  "s_sub_u32 s3, s27, 576\n\t" \
  "v_and_b32 v20, 3, v0\n\t" \
  "s_add_u32 s4, s27, 6720\n\t" \
  "v_sub_u32 v254, v0, v254\n\t" \
  "s_min_u32 s4, s4, 0x18000\n\t" \
  "v_and_b32 v2, 0x1e0, v0\n\t" \
  "s_and_b32 s24, s2, 7\n\t" \
  "v_lshl_add_u32 v254, v254, 4, s3\n\t" \
  "s_lshl_b32 s24, s24, 6\n\t" \
  "v_lshl_or_b32 v20, v20, 3, v2\n\t" \
  "s_mov_b32 s26, 0x18000\n\t" \
  "v_cmp_gt_u32 vcc, s4, v254\n\t" \
  "s_cmp_ge_u32 s2, 128\n\t" \
  "s_cselect_b32 s3, 7, 0\n\t" \
  "s_cselect_b32 s4, 0, 7\n\t" \
  "v_add_u32 v1, s3, v1\n\t" \
  "s_cselect_b32 s3, 28, 35\n\t" \
  "v_cndmask_b32 v254, 2.0, v254, vcc\n\t" \
  "s_cselect_b32 s26, s26, 0xfffe8000\n\t" \
  "v_xor_b32 v1, s4, v1\n\t" \
  "s_add_u32 s24, s24, s3\n\t" \
  "v_and_b32 v1, 7, v1\n\t" \
  "s_movk_i32 s3, 1344\n\t" \
  "s_mul_i32 s24, s24, 0x18000\n\t" \
  "v_mad_u32_u24 v1, v1, s3, v20\n\t" \
  "s_lshl4_add_u32 s25, s5, 34352\n\t" \
  "s_mov_b32 s10, 0x3000000\n\t" \
  "s_mov_b32 s11, 0x20000\n\t" \
  "s_mov_b32 s14, 9408\n\t" \
  "s_mov_b32 s15, 0x20000\n\t" \
  "s_mov_b32 s18, 73728\n\t" \
  "s_mov_b32 s19, 0x20000\n\t" \
  "s_mov_b32 s22, 0x3000000\n\t" \
  "s_mov_b32 s23, 0x20000\n\t" \
  "s_setreg_imm32_b32 hwreg(HW_REG_MODE, 4, 2), 0\n\t" \
  "s_setreg_imm32_b32 hwreg(HW_REG_MODE, 9, 1), 0\n\t" \
  "s_lshl_b32 s65, s26, 4\n\t" \
  "s_lshr_b32 s3, s5, 7\n\t" \
  "s_add_u32 s3, s3, 8\n\t" \
  "s_mul_i32 s3, s3, s26\n\t" \
  "s_add_u32 s29, s24, s27\n\t" \
  "s_add_u32 s29, s29, s3\n\t" \
  "s_and_b32 s3, s5, 64\n\t" \
  "s_mul_i32 s3, s3, 48\n\t" \
  "s_add_u32 s29, s29, s3\n\t" \
  "s_bfe_u32 s3, s5, 0x20006\n\t" \
  "s_add_u32 s3, s3, 6\n\t" \
  "s_mul_i32 s3, s3, s26\n\t" \
  "s_add_u32 s30, s24, s65\n\t" \
  "s_add_u32 s30, s30, s3\n\t" \
  "s_lshl_b32 s31, s26, 2\n\t" \
  "s_mul_i32 s64, s26, 22\n\t" \
  "s_sub_u32 s64, s64, s65\n\t" \
  "s_cmp_ge_u32 s5, 256\n\t" \
  "s_cselect_b32 s27, s27, 0\n\t" \
  "s_cselect_b32 s3, -6, -2\n\t" \
  "s_mul_i32 s3, s3, s26\n\t" \
  "s_add_u32 s28, s29, s3\n\t" \
  "s_mov_b64 vcc, 0\n\t" \
  "s_waitcnt lgkmcnt(0)\n\t" \
  "s_and_b32 s13, s13, 0xffff\n\t" \
  "s_and_b32 s9, s9, 0xffff\n\t" \
  "s_and_b32 s17, s17, 0xffff\n\t" \
  "s_cmp_ge_u32 s5, 192\n\t" \
  "s_cbranch_scc1 .Lcnx_nd\n\t" \
  "buffer_load_dwordx2 v[2:3], v1, s[12:15], 0 offen offset:0\n\t" \
  "buffer_load_dwordx2 v[4:5], v1, s[12:15], 0 offen offset:192\n\t" \
  "buffer_load_dwordx2 v[6:7], v1, s[12:15], 0 offen offset:384\n\t" \
  "buffer_load_dwordx2 v[8:9], v1, s[12:15], 0 offen offset:576\n\t" \
  "buffer_load_dwordx2 v[10:11], v1, s[12:15], 0 offen offset:768\n\t" \
  "buffer_load_dwordx2 v[12:13], v1, s[12:15], 0 offen offset:960\n\t" \
  "buffer_load_dwordx2 v[14:15], v1, s[12:15], 0 offen offset:1152\n\t" \
  ".Lcnx_nd:\n\t" \
  "s_and_b32 s3, s5, 0x140\n\t" \
  "s_cmp_lg_u32 s3, 64\n\t" \
  "s_cbranch_scc1 .Lcnx_nw\n\t" \
  "s_lshr_b32 s3, s2, 3\n\t" \
  "s_mul_i32 s3, s3, 2304\n\t" \
  "s_mov_b32 m0, 0\n\t" \
  "v_mul_u32_u24 v21, 36, v21\n\t" \
  "buffer_load_dword v21, s[16:19], s3 offen lds\n\t" \
  ".Lcnx_nw:\n\t" \
  "s_add_u32 m0, s25, 0\n\t" \
  "s_add_u32 s24, s24, s26\n\t" \
  "buffer_load_dwordx4 v254, s[8:11], s24 offen nt lds\n\t" \
  "s_add_u32 m0, s25, 8200\n\t" \
  "s_add_u32 s24, s24, s26\n\t" \
  "buffer_load_dwordx4 v254, s[8:11], s24 offen nt lds\n\t" \
  "s_add_u32 m0, s25, 16400\n\t" \
  "s_add_u32 s24, s24, s26\n\t" \
  "buffer_load_dwordx4 v254, s[8:11], s24 offen nt lds\n\t" \
  "s_add_u32 m0, s25, 24600\n\t" \
  "s_add_u32 s24, s24, s26\n\t" \
  "buffer_load_dwordx4 v254, s[8:11], s24 offen nt lds\n\t" \
  "s_load_dwordx16 s[32:47], s[66:67], 0xc00\n\t" \
  "s_load_dwordx16 s[48:63], s[66:67], 0xc40\n\t" \
  "s_and_b32 s21, s21, 0xffff\n\t" \
  "s_waitcnt vmcnt(4)\n\t" \
  "v_mfma_f32_32x32x16_bf16 v[144:159], v[242:245], v[240:243], 0\n\t" \
  "v_mfma_f32_32x32x16_bf16 v[160:175], v[242:245], v[240:243], 0\n\t" \
  "v_bfe_u32 v236, v251, 3, 2\n\t" \
  "v_mul_u32_u24 v236, 0x5555, v236\n\t" \
  "v_add_u32 v252, s27, v16\n\t" \
  "s_lshl2_add_u32 s12, s26, s29\n\t" \
  "s_lshl3_add_u32 s13, s26, s29\n\t" \
  "v_mov_b32 v253, v246\n\t" \
  "s_mov_b32 s14, 2.0\n\t" \
  "s_mov_b32 s15, 2.0\n\t" \
  "s_cmp_gt_u32 s5, 192\n\t" \
  "s_cbranch_scc1 .Lcnx_e2\n\t" \
  "s_cmp_eq_u32 s5, 192\n\t" \
  "s_cbranch_scc1 .Lcnx_e1\n\t" \
  "v_xor_b32 v23, 0x02020202, v19\n\t" \
  "v_perm_b32 v24, v4, 0, v19\n\t" \
  "v_perm_b32 v25, v12, v8, v19\n\t" \
  "v_perm_b32 v26, v6, v2, v19\n\t" \
  "v_perm_b32 v27, v14, v10, v19\n\t" \
  "v_perm_b32 v28, v8, v4, v19\n\t" \
  "v_perm_b32 v29, 0, v12, v19\n\t" \
  "ds_write_b64 v17, v[24:25] offset:0\n\t" \
  "ds_write_b64 v17, v[26:27] offset:8\n\t" \
  "ds_write_b64 v17, v[28:29] offset:16\n\t" \
  "v_perm_b32 v30, v4, 0, v23\n\t" \
  "v_perm_b32 v31, v12, v8, v23\n\t" \
  "v_perm_b32 v32, v6, v2, v23\n\t" \
  "v_perm_b32 v33, v14, v10, v23\n\t" \
  "v_perm_b32 v34, v8, v4, v23\n\t" \
  "v_perm_b32 v35, 0, v12, v23\n\t" \
  "ds_write_b64 v18, v[30:31] offset:0\n\t" \
  "ds_write_b64 v18, v[32:33] offset:8\n\t" \
  "ds_write_b64 v18, v[34:35] offset:16\n\t" \
  "v_perm_b32 v37, v5, 0, v19\n\t" \
  "v_perm_b32 v36, v13, v9, v19\n\t" \
  "v_perm_b32 v39, v7, v3, v19\n\t" \
  "v_perm_b32 v38, v15, v11, v19\n\t" \
  "v_perm_b32 v41, v9, v5, v19\n\t" \
  "v_perm_b32 v40, 0, v13, v19\n\t" \
  "ds_write_b64 v17, v[36:37] offset:384\n\t" \
  "ds_write_b64 v17, v[38:39] offset:392\n\t" \
  "ds_write_b64 v17, v[40:41] offset:400\n\t" \
  "v_perm_b32 v43, v5, 0, v23\n\t" \
  "v_perm_b32 v42, v13, v9, v23\n\t" \
  "v_perm_b32 v45, v7, v3, v23\n\t" \
  "v_perm_b32 v44, v15, v11, v23\n\t" \
  "v_perm_b32 v47, v9, v5, v23\n\t" \
  "v_perm_b32 v46, 0, v13, v23\n\t" \
  "ds_write_b64 v18, v[42:43] offset:384\n\t" \
  "ds_write_b64 v18, v[44:45] offset:392\n\t" \
  "ds_write_b64 v18, v[46:47] offset:400\n\t" \
  "s_branch .Lcnx_e3\n\t" \
  ".Lcnx_e1:\n\t" \
  "s_mov_b64 exec, 0x3ffff\n\t" \
  "ds_write_b64 v251, v[240:241] offset:47136\n\t" \
  "s_mov_b64 exec, 15\n\t" \
  "ds_write_b64 v17, v[240:241]\n\t" \
  "s_mov_b64 exec, -1\n\t" \
  "s_branch .Lcnx_e3\n\t" \
  ".Lcnx_e2:\n\t" \
  "s_setprio 1\n\t" \
  ".Lcnx_e3:\n\t" \
  "s_add_u32 m0, s25, 32800\n\t" \
  "s_add_u32 s24, s24, s26\n\t" \
  "buffer_load_dwordx4 v254, s[8:11], s24 offen nt lds\n\t" \
  "s_add_u32 m0, s25, 41000\n\t" \
  "s_add_u32 s24, s24, s26\n\t" \
  "buffer_load_dwordx4 v254, s[8:11], s24 offen nt lds\n\t" \
  "s_add_u32 m0, s25, 49200\n\t" \
  "s_add_u32 s24, s24, s26\n\t" \
  "buffer_load_dwordx4 v254, s[8:11], s24 offen nt lds\n\t" \
  "s_add_u32 m0, s25, 57400\n\t" \
  "s_add_u32 s24, s24, s26\n\t" \
  "buffer_load_dwordx4 v254, s[8:11], s24 offen nt lds\n\t" \
  "s_waitcnt vmcnt(6) lgkmcnt(0)\n\t" \
  "s_barrier\n\t" \
  "v_add_u32 v232, 1536, v247\n\t" \
  "ds_read2_b32 v[98:99], v247 offset0:0 offset1:6\n\t" \
  "ds_read2_b32 v[114:115], v247 offset0:192 offset1:198\n\t" \
  "ds_read2_b32 v[130:131], v232 offset0:0 offset1:6\n\t" \
  "ds_read2_b32 v[102:103], v247 offset0:12 offset1:18\n\t" \
  "ds_read2_b32 v[118:119], v247 offset0:204 offset1:210\n\t" \
  "ds_read2_b32 v[134:135], v232 offset0:12 offset1:18\n\t" \
  "ds_read2_b32 v[106:107], v247 offset0:24 offset1:30\n\t" \
  "ds_read2_b32 v[122:123], v247 offset0:216 offset1:222\n\t" \
  "ds_read2_b32 v[138:139], v232 offset0:24 offset1:30\n\t" \
  "ds_read2_b32 v[110:111], v247 offset0:36 offset1:42\n\t" \
  "ds_read2_b32 v[126:127], v247 offset0:228 offset1:234\n\t" \
  "ds_read2_b32 v[142:143], v232 offset0:36 offset1:42\n\t" \
  ".Lcnx_round:\n\t" \
  "v_add_u32 v232, 49200, v253\n\t" \
  "v_add_u32 v233, s39, v251\n\t" \
  "v_mfma_f32_32x32x16_bf16 v[176:191], v[242:245], v[240:243], 0\n\t" \
  "ds_read_b64 v[200:201], v253 offset:0\n\t" \
  "ds_read_b64 v[202:203], v253 offset:400\n\t" \
  "ds_read_b64 v[204:205], v253 offset:800\n\t" \
  "ds_read_b64 v[206:207], v253 offset:1200\n\t" \
  "ds_read_b64 v[208:209], v253 offset:8\n\t" \
  "ds_read_b64 v[210:211], v253 offset:408\n\t" \
  "ds_read_b64 v[212:213], v253 offset:808\n\t" \
  "ds_read_b64 v[214:215], v253 offset:1208\n\t" \
  "ds_read_b64 v[216:217], v253 offset:16\n\t" \
  "ds_read_b64 v[218:219], v253 offset:416\n\t" \
  "ds_read_b64 v[220:221], v253 offset:816\n\t" \
  "ds_read_b64 v[222:223], v253 offset:1216\n\t" \
  "s_cmp_lg_u32 s58, 0\n\t" \
  "s_cbranch_scc1 .Lcnx_c1\n\t" \
  "s_add_u32 m0, s25, 65600\n\t" \
  "s_add_u32 s24, s24, s26\n\t" \
  "buffer_load_dwordx4 v254, s[8:11], s24 offen nt lds\n\t" \
  "s_add_u32 m0, s25, 73800\n\t" \
  "s_add_u32 s24, s24, s26\n\t" \
  "buffer_load_dwordx4 v254, s[8:11], s24 offen nt lds\n\t" \
  "s_add_u32 m0, s25, 82000\n\t" \
  "s_add_u32 s24, s24, s26\n\t" \
  "buffer_load_dwordx4 v254, s[8:11], s24 offen nt lds\n\t" \
  "s_add_u32 m0, s25, 90200\n\t" \
  "s_add_u32 s24, s24, s26\n\t" \
  "buffer_load_dwordx4 v254, s[8:11], s24 offen nt lds\n\t" \
  ".Lcnx_c1:\n\t" \
  "s_waitcnt lgkmcnt(15)\n\t" \
  "v_and_b32 v96, 0xffff, v98\n\t" \
  "v_and_b32 v97, 0xffff0000, v98\n\t" \
  "v_and_b32 v98, 0xffff, v99\n\t" \
  "v_and_b32 v99, 0xffff0000, v99\n\t" \
  "v_and_b32 v112, 0xffff, v114\n\t" \
  "v_and_b32 v113, 0xffff0000, v114\n\t" \
  "v_and_b32 v114, 0xffff, v115\n\t" \
  "v_and_b32 v115, 0xffff0000, v115\n\t" \
  "v_and_b32 v128, 0xffff, v130\n\t" \
  "v_and_b32 v129, 0xffff0000, v130\n\t" \
  "v_and_b32 v130, 0xffff, v131\n\t" \
  "v_and_b32 v131, 0xffff0000, v131\n\t" \
  "s_waitcnt lgkmcnt(8)\n\t" \
  "v_smfmac_f32_16x16x64_bf16 v[144:147], v[96:99], v[200:207], v236\n\t" \
  "v_and_b32 v100, 0xffff, v102\n\t" \
  "v_and_b32 v101, 0xffff0000, v102\n\t" \
  "v_and_b32 v102, 0xffff, v103\n\t" \
  "v_and_b32 v103, 0xffff0000, v103\n\t" \
  "s_cmp_lg_u32 s58, 1\n\t" \
  "s_cbranch_scc1 .Lcnx_c2\n\t" \
  "s_waitcnt vmcnt(5)\n\t" \
  ".Lcnx_c2:\n\t" \
  "s_waitcnt vmcnt(8) lgkmcnt(0)\n\t" \
  "s_barrier\n\t" \
  "ds_read_b64 v[224:225], v253 offset:16400\n\t" \
  "ds_read_b64 v[226:227], v253 offset:16800\n\t" \
  "ds_read_b64 v[228:229], v253 offset:17200\n\t" \
  "ds_read_b64 v[230:231], v253 offset:17600\n\t" \
  "v_smfmac_f32_16x16x64_bf16 v[148:151], v[112:115], v[208:215], v236\n\t" \
  "s_cmp_lg_u32 s58, 1\n\t" \
  "s_cbranch_scc1 .Lcnx_c3\n\t" \
  "s_add_u32 m0, s25, 73800\n\t" \
  "s_add_u32 s24, s24, s26\n\t" \
  "buffer_load_dwordx4 v254, s[8:11], s24 offen lds\n\t" \
  ".Lcnx_c3:\n\t" \
  "ds_read_b64 v[200:201], v253 offset:16408\n\t" \
  "ds_read_b64 v[202:203], v253 offset:16808\n\t" \
  "ds_read_b64 v[204:205], v253 offset:17208\n\t" \
  "ds_read_b64 v[206:207], v253 offset:17608\n\t" \
  "v_and_b32 v116, 0xffff, v118\n\t" \
  "v_and_b32 v117, 0xffff0000, v118\n\t" \
  "v_and_b32 v118, 0xffff, v119\n\t" \
  "v_and_b32 v119, 0xffff0000, v119\n\t" \
  "v_smfmac_f32_16x16x64_bf16 v[152:155], v[128:131], v[216:223], v236\n\t" \
  "s_cmp_lg_u32 s58, 1\n\t" \
  "s_cbranch_scc1 .Lcnx_c4\n\t" \
  "s_add_u32 m0, s25, 82000\n\t" \
  "s_add_u32 s24, s24, s26\n\t" \
  "buffer_load_dwordx4 v254, s[8:11], s24 offen lds\n\t" \
  ".Lcnx_c4:\n\t" \
  "ds_read_b64 v[208:209], v253 offset:16416\n\t" \
  "ds_read_b64 v[210:211], v253 offset:16816\n\t" \
  "ds_read_b64 v[212:213], v253 offset:17216\n\t" \
  "ds_read_b64 v[214:215], v253 offset:17616\n\t" \
  "v_and_b32 v132, 0xffff, v134\n\t" \
  "v_and_b32 v133, 0xffff0000, v134\n\t" \
  "v_and_b32 v134, 0xffff, v135\n\t" \
  "v_and_b32 v135, 0xffff0000, v135\n\t" \
  "s_waitcnt lgkmcnt(8)\n\t" \
  "v_smfmac_f32_16x16x64_bf16 v[156:159], v[96:99], v[224:231], v236\n\t" \
  "s_cmp_lg_u32 s58, 1\n\t" \
  "s_cbranch_scc1 .Lcnx_c5\n\t" \
  "s_add_u32 m0, s25, 90200\n\t" \
  "s_add_u32 s24, s24, s26\n\t" \
  "buffer_load_dwordx4 v254, s[8:11], s24 offen lds\n\t" \
  ".Lcnx_c5:\n\t" \
  "v_and_b32 v104, 0xffff, v106\n\t" \
  "v_and_b32 v105, 0xffff0000, v106\n\t" \
  "v_and_b32 v106, 0xffff, v107\n\t" \
  "v_and_b32 v107, 0xffff0000, v107\n\t" \
  "v_smfmac_f32_16x16x64_bf16 v[144:147], v[100:103], v[224:231], v236\n\t" \
  "s_waitcnt vmcnt(6) lgkmcnt(0)\n\t" \
  "s_barrier\n\t" \
  "ds_read_b64 v[216:217], v253 offset:32800\n\t" \
  "ds_read_b64 v[218:219], v253 offset:33200\n\t" \
  "ds_read_b64 v[220:221], v253 offset:33600\n\t" \
  "ds_read_b64 v[222:223], v253 offset:34000\n\t" \
  "s_add_u32 m0, s25, 0\n\t" \
  "s_add_u32 s24, s24, s26\n\t" \
  "v_smfmac_f32_16x16x64_bf16 v[160:163], v[112:115], v[200:207], v236\n\t" \
  "buffer_load_dwordx4 v254, s[8:11], s24 offen lds\n\t" \
  "ds_read_b64 v[224:225], v253 offset:32808\n\t" \
  "ds_read_b64 v[226:227], v253 offset:33208\n\t" \
  "v_and_b32 v120, 0xffff, v122\n\t" \
  "v_and_b32 v121, 0xffff0000, v122\n\t" \
  "v_and_b32 v122, 0xffff, v123\n\t" \
  "v_and_b32 v123, 0xffff0000, v123\n\t" \
  "v_smfmac_f32_16x16x64_bf16 v[148:151], v[116:119], v[200:207], v236\n\t" \
  "ds_read_b64 v[228:229], v253 offset:33608\n\t" \
  "ds_read_b64 v[230:231], v253 offset:34008\n\t" \
  "s_add_u32 m0, s25, 8200\n\t" \
  "s_add_u32 s24, s24, s26\n\t" \
  "v_smfmac_f32_16x16x64_bf16 v[164:167], v[128:131], v[208:215], v236\n\t" \
  "buffer_load_dwordx4 v254, s[8:11], s24 offen lds\n\t" \
  "ds_read_b64 v[200:201], v253 offset:32816\n\t" \
  "ds_read_b64 v[202:203], v253 offset:33216\n\t" \
  "v_and_b32 v136, 0xffff, v138\n\t" \
  "v_and_b32 v137, 0xffff0000, v138\n\t" \
  "v_and_b32 v138, 0xffff, v139\n\t" \
  "v_and_b32 v139, 0xffff0000, v139\n\t" \
  "v_mfma_f32_32x32x16_bf16 v[0:15], v[242:245], v[240:243], 0\n\t" \
  "v_smfmac_f32_16x16x64_bf16 v[152:155], v[132:135], v[208:215], v236\n\t" \
  "ds_read_b64 v[204:205], v253 offset:33616\n\t" \
  "s_waitcnt lgkmcnt(7)\n\t" \
  "v_smfmac_f32_16x16x64_bf16 v[168:171], v[96:99], v[216:223], v236\n\t" \
  "ds_read_b64 v[206:207], v253 offset:34016\n\t" \
  "v_and_b32 v108, 0xffff, v110\n\t" \
  "v_and_b32 v109, 0xffff0000, v110\n\t" \
  "v_and_b32 v110, 0xffff, v111\n\t" \
  "v_and_b32 v111, 0xffff0000, v111\n\t" \
  "s_add_u32 m0, s25, 16400\n\t" \
  "s_add_u32 s24, s24, s26\n\t" \
  "v_smfmac_f32_16x16x64_bf16 v[156:159], v[100:103], v[216:223], v236\n\t" \
  "buffer_load_dwordx4 v254, s[8:11], s24 offen lds\n\t" \
  "v_smfmac_f32_16x16x64_bf16 v[144:147], v[104:107], v[216:223], v236\n\t" \
  "s_waitcnt vmcnt(7) lgkmcnt(0)\n\t" \
  "s_barrier\n\t" \
  "v_smfmac_f32_16x16x64_bf16 v[172:175], v[112:115], v[224:231], v236\n\t" \
  "ds_read_b64 v[208:209], v232 offset:0\n\t" \
  "ds_read_b64 v[210:211], v232 offset:400\n\t" \
  "v_and_b32 v124, 0xffff, v126\n\t" \
  "v_and_b32 v125, 0xffff0000, v126\n\t" \
  "v_and_b32 v126, 0xffff, v127\n\t" \
  "v_and_b32 v127, 0xffff0000, v127\n\t" \
  "s_add_u32 m0, s25, 24600\n\t" \
  "s_add_u32 s24, s24, s26\n\t" \
  "v_smfmac_f32_16x16x64_bf16 v[160:163], v[116:119], v[224:231], v236\n\t" \
  "buffer_load_dwordx4 v254, s[8:11], s24 offen lds\n\t" \
  "ds_read_b64 v[212:213], v232 offset:800\n\t" \
  "ds_read_b64 v[214:215], v232 offset:1200\n\t" \
  "v_smfmac_f32_16x16x64_bf16 v[148:151], v[120:123], v[224:231], v236\n\t" \
  "ds_read_b64 v[216:217], v232 offset:8\n\t" \
  "ds_read_b64 v[218:219], v232 offset:408\n\t" \
  "v_smfmac_f32_16x16x64_bf16 v[176:179], v[128:131], v[200:207], v236\n\t" \
  "ds_read_b64 v[220:221], v232 offset:808\n\t" \
  "ds_read_b64 v[222:223], v232 offset:1208\n\t" \
  "v_and_b32 v140, 0xffff, v142\n\t" \
  "v_and_b32 v141, 0xffff0000, v142\n\t" \
  "v_and_b32 v142, 0xffff, v143\n\t" \
  "v_and_b32 v143, 0xffff0000, v143\n\t" \
  "v_smfmac_f32_16x16x64_bf16 v[164:167], v[132:135], v[200:207], v236\n\t" \
  "ds_read_b64 v[224:225], v232 offset:16\n\t" \
  "s_add_u32 m0, s25, 32800\n\t" \
  "s_add_u32 s24, s24, s26\n\t" \
  "v_smfmac_f32_16x16x64_bf16 v[152:155], v[136:139], v[200:207], v236\n\t" \
  "buffer_load_dwordx4 v254, s[8:11], s24 offen lds\n\t" \
  "ds_read_b64 v[226:227], v232 offset:416\n\t" \
  "s_waitcnt lgkmcnt(6)\n\t" \
  "v_smfmac_f32_16x16x64_bf16 v[180:183], v[96:99], v[208:215], v236\n\t" \
  "ds_read_b64 v[228:229], v232 offset:816\n\t" \
  "v_smfmac_f32_16x16x64_bf16 v[168:171], v[100:103], v[208:215], v236\n\t" \
  "ds_read_b64 v[230:231], v232 offset:1216\n\t" \
  "v_mfma_f32_32x32x16_bf16 v[16:31], v[242:245], v[240:243], 0\n\t" \
  "v_mov_b32 v196, 0x3f003f00\n\t" \
  "v_mov_b32 v197, 0x3f003f00\n\t" \
  "v_smfmac_f32_16x16x64_bf16 v[156:159], v[104:107], v[208:215], v236\n\t" \
  "v_mov_b32 v198, 0x3f003f00\n\t" \
  "v_mov_b32 v199, 0x3f003f00\n\t" \
  "s_add_u32 m0, s25, 41000\n\t" \
  "s_add_u32 s24, s24, s26\n\t" \
  "v_smfmac_f32_16x16x64_bf16 v[144:147], v[108:111], v[208:215], v236\n\t" \
  "buffer_load_dwordx4 v254, s[8:11], s24 offen lds\n\t" \
  "s_waitcnt vmcnt(8) lgkmcnt(0)\n\t" \
  "s_barrier\n\t" \
  "v_smfmac_f32_16x16x64_bf16 v[184:187], v[112:115], v[216:223], v236\n\t" \
  "ds_read_b64 v[200:201], v232 offset:16400\n\t" \
  "ds_read_b64 v[202:203], v232 offset:16800\n\t" \
  "v_smfmac_f32_16x16x64_bf16 v[172:175], v[116:119], v[216:223], v236\n\t" \
  "ds_read_b64 v[204:205], v232 offset:17200\n\t" \
  "ds_read_b64 v[206:207], v232 offset:17600\n\t" \
  "v_smfmac_f32_16x16x64_bf16 v[160:163], v[120:123], v[216:223], v236\n\t" \
  "ds_read_b64 v[208:209], v232 offset:16408\n\t" \
  "v_cvt_pk_bf16_f32 v144, v144, v145\n\t" \
  "v_cvt_pk_bf16_f32 v145, v146, v147\n\t" \
  "s_add_u32 m0, s25, 49200\n\t" \
  "s_add_u32 s24, s24, s26\n\t" \
  "v_smfmac_f32_16x16x64_bf16 v[148:151], v[124:127], v[216:223], v236\n\t" \
  "buffer_load_dwordx4 v254, s[8:11], s24 offen lds\n\t" \
  "ds_read_b64 v[210:211], v232 offset:16808\n\t" \
  "v_smfmac_f32_16x16x64_bf16 v[188:191], v[128:131], v[224:231], v236\n\t" \
  "ds_read_b64 v[212:213], v232 offset:17208\n\t" \
  "v_smfmac_f32_16x16x64_bf16 v[176:179], v[132:135], v[224:231], v236\n\t" \
  "ds_read_b64 v[214:215], v232 offset:17608\n\t" \
  "v_smfmac_f32_16x16x64_bf16 v[164:167], v[136:139], v[224:231], v236\n\t" \
  "ds_read_b64 v[216:217], v232 offset:16416\n\t" \
  "v_cvt_pk_bf16_f32 v148, v148, v149\n\t" \
  "v_cvt_pk_bf16_f32 v149, v150, v151\n\t" \
  "s_add_u32 m0, s25, 57400\n\t" \
  "s_add_u32 s24, s24, s26\n\t" \
  "v_smfmac_f32_16x16x64_bf16 v[152:155], v[140:143], v[224:231], v236\n\t" \
  "buffer_load_dwordx4 v254, s[8:11], s24 offen lds\n\t" \
  "ds_read_b64 v[218:219], v232 offset:16816\n\t" \
  "s_waitcnt lgkmcnt(6)\n\t" \
  "v_smfmac_f32_16x16x64_bf16 v[0:3], v[96:99], v[200:207], v236\n\t" \
  "ds_read_b64 v[220:221], v232 offset:17216\n\t" \
  "v_smfmac_f32_16x16x64_bf16 v[180:183], v[100:103], v[200:207], v236\n\t" \
  "ds_read_b64 v[222:223], v232 offset:17616\n\t" \
  "v_mfma_f32_32x32x16_bf16 v[32:47], v[242:245], v[240:243], 0\n\t" \
  "v_smfmac_f32_16x16x64_bf16 v[168:171], v[104:107], v[200:207], v236\n\t" \
  "v_cvt_pk_bf16_f32 v152, v152, v153\n\t" \
  "v_cvt_pk_bf16_f32 v153, v154, v155\n\t" \
  "v_smfmac_f32_16x16x64_bf16 v[156:159], v[108:111], v[200:207], v236\n\t" \
  "s_waitcnt vmcnt(8) lgkmcnt(0)\n\t" \
  "s_barrier\n\t" \
  "s_add_u32 m0, s25, 65600\n\t" \
  "s_add_u32 s24, s24, s26\n\t" \
  "v_smfmac_f32_16x16x64_bf16 v[4:7], v[112:115], v[208:215], v236\n\t" \
  "buffer_load_dwordx4 v254, s[8:11], s24 offen lds\n\t" \
  "ds_read_b64 v[224:225], v232 offset:32800\n\t" \
  "ds_read_b64 v[226:227], v232 offset:33200\n\t" \
  "v_smfmac_f32_16x16x64_bf16 v[184:187], v[116:119], v[208:215], v236\n\t" \
  "ds_read_b64 v[228:229], v232 offset:33600\n\t" \
  "ds_read_b64 v[230:231], v232 offset:34000\n\t" \
  "s_add_u32 m0, s25, 73800\n\t" \
  "s_add_u32 s24, s24, s26\n\t" \
  "v_smfmac_f32_16x16x64_bf16 v[172:175], v[120:123], v[208:215], v236\n\t" \
  "buffer_load_dwordx4 v254, s[8:11], s24 offen lds\n\t" \
  "ds_read_b64 v[200:201], v232 offset:32808\n\t" \
  "v_cvt_pk_bf16_f32 v156, v156, v157\n\t" \
  "v_cvt_pk_bf16_f32 v157, v158, v159\n\t" \
  "v_smfmac_f32_16x16x64_bf16 v[160:163], v[124:127], v[208:215], v236\n\t" \
  "ds_read_b64 v[202:203], v232 offset:33208\n\t" \
  "v_smfmac_f32_16x16x64_bf16 v[8:11], v[128:131], v[216:223], v236\n\t" \
  "ds_read_b64 v[204:205], v232 offset:33608\n\t" \
  "v_smfmac_f32_16x16x64_bf16 v[188:191], v[132:135], v[216:223], v236\n\t" \
  "ds_read_b64 v[206:207], v232 offset:34008\n\t" \
  "v_smfmac_f32_16x16x64_bf16 v[176:179], v[136:139], v[216:223], v236\n\t" \
  "buffer_store_dwordx4 v[52:55], v250, s[20:23], s61 offen offset:0 sc1\n\t" \
  "ds_read_b64 v[208:209], v232 offset:32816\n\t" \
  "v_cvt_pk_bf16_f32 v160, v160, v161\n\t" \
  "v_cvt_pk_bf16_f32 v161, v162, v163\n\t" \
  "v_smfmac_f32_16x16x64_bf16 v[164:167], v[140:143], v[216:223], v236\n\t" \
  "ds_read_b64 v[210:211], v232 offset:33216\n\t" \
  "s_waitcnt lgkmcnt(6)\n\t" \
  "v_smfmac_f32_16x16x64_bf16 v[12:15], v[96:99], v[224:231], v236\n\t" \
  "ds_read_b64 v[212:213], v232 offset:33616\n\t" \
  "v_smfmac_f32_16x16x64_bf16 v[0:3], v[100:103], v[224:231], v236\n\t" \
  "ds_read_b64 v[214:215], v232 offset:34016\n\t" \
  "v_smfmac_f32_16x16x64_bf16 v[180:183], v[104:107], v[224:231], v236\n\t" \
  "s_nop 0\n\t" \
  "v_cvt_pk_bf16_f32 v164, v164, v165\n\t" \
  "v_cvt_pk_bf16_f32 v165, v166, v167\n\t" \
  "v_smfmac_f32_16x16x64_bf16 v[168:171], v[108:111], v[224:231], v236\n\t" \
  "s_waitcnt vmcnt(9) lgkmcnt(0)\n\t" \
  "s_barrier\n\t" \
  "v_smfmac_f32_16x16x64_bf16 v[16:19], v[112:115], v[200:207], v236\n\t" \
  "ds_write_b64 v193, v[144:145] offset:12288\n\t" \
  "ds_read_b64 v[216:217], v253 offset:0\n\t" \
  "v_smfmac_f32_16x16x64_bf16 v[4:7], v[116:119], v[200:207], v236\n\t" \
  "ds_write_b64 v193, v[148:149] offset:16384\n\t" \
  "ds_read_b64 v[218:219], v253 offset:400\n\t" \
  "v_smfmac_f32_16x16x64_bf16 v[184:187], v[120:123], v[200:207], v236\n\t" \
  "ds_write_b64 v193, v[152:153] offset:20480\n\t" \
  "ds_read_b64 v[220:221], v253 offset:800\n\t" \
  "v_cvt_pk_bf16_f32 v168, v168, v169\n\t" \
  "v_cvt_pk_bf16_f32 v169, v170, v171\n\t" \
  "v_smfmac_f32_16x16x64_bf16 v[172:175], v[124:127], v[200:207], v236\n\t" \
  "ds_write_b64 v193, v[156:157] offset:0\n\t" \
  "ds_read_b64 v[222:223], v253 offset:1200\n\t" \
  "v_smfmac_f32_16x16x64_bf16 v[20:23], v[128:131], v[208:215], v236\n\t" \
  "buffer_store_dwordx4 v[60:63], v250, s[20:23], s61 offen offset:64 sc1\n\t" \
  "ds_read_b64 v[224:225], v253 offset:8\n\t" \
  "ds_read_b64 v[226:227], v253 offset:408\n\t" \
  "v_smfmac_f32_16x16x64_bf16 v[8:11], v[132:135], v[208:215], v236\n\t" \
  "ds_write_b64 v193, v[160:161] offset:4096\n\t" \
  "ds_read_b64 v[228:229], v253 offset:808\n\t" \
  "v_smfmac_f32_16x16x64_bf16 v[188:191], v[136:139], v[208:215], v236\n\t" \
  "ds_write_b64 v193, v[164:165] offset:8192\n\t" \
  "ds_read_b64 v[230:231], v253 offset:1208\n\t" \
  "v_cvt_pk_bf16_f32 v172, v172, v173\n\t" \
  "v_cvt_pk_bf16_f32 v173, v174, v175\n\t" \
  "v_smfmac_f32_16x16x64_bf16 v[176:179], v[140:143], v[208:215], v236\n\t" \
  "ds_read_b64 v[200:201], v253 offset:16\n\t" \
  "ds_read_b64 v[202:203], v253 offset:416\n\t" \
  "s_waitcnt lgkmcnt(8)\n\t" \
  "v_smfmac_f32_16x16x64_bf16 v[24:27], v[96:99], v[216:223], v236\n\t" \
  "ds_read_b64 v[204:205], v253 offset:816\n\t" \
  "v_smfmac_f32_16x16x64_bf16 v[12:15], v[100:103], v[216:223], v236\n\t" \
  "ds_read_b64 v[206:207], v253 offset:1216\n\t" \
  "v_smfmac_f32_16x16x64_bf16 v[0:3], v[104:107], v[216:223], v236\n\t" \
  "v_cvt_pk_bf16_f32 v176, v176, v177\n\t" \
  "v_cvt_pk_bf16_f32 v177, v178, v179\n\t" \
  "v_smfmac_f32_16x16x64_bf16 v[180:183], v[108:111], v[216:223], v236\n\t" \
  "s_waitcnt vmcnt(8) lgkmcnt(0)\n\t" \
  "s_barrier\n\t" \
  "v_smfmac_f32_16x16x64_bf16 v[28:31], v[112:115], v[224:231], v236\n\t" \
  "ds_write_b64 v192, v[168:169] offset:0\n\t" \
  "ds_read_b64 v[208:209], v253 offset:16400\n\t" \
  "ds_read_b128 v[144:147], v194 offset:0\n\t" \
  "v_smfmac_f32_16x16x64_bf16 v[16:19], v[116:119], v[224:231], v236\n\t" \
  "ds_write_b64 v192, v[172:173] offset:4096\n\t" \
  "ds_read_b64 v[210:211], v253 offset:16800\n\t" \
  "ds_read_b128 v[148:151], v194 offset:4096\n\t" \
  "v_smfmac_f32_16x16x64_bf16 v[4:7], v[120:123], v[224:231], v236\n\t" \
  "buffer_store_dwordx4 v[68:71], v250, s[20:23], s61 offen offset:128 sc1\n\t" \
  "ds_read_b64 v[212:213], v253 offset:17200\n\t" \
  "ds_read_b64 v[214:215], v253 offset:17600\n\t" \
  "v_cvt_pk_bf16_f32 v180, v180, v181\n\t" \
  "v_cvt_pk_bf16_f32 v181, v182, v183\n\t" \
  "ds_read_b128 v[152:155], v194 offset:8192\n\t" \
  "v_smfmac_f32_16x16x64_bf16 v[184:187], v[124:127], v[224:231], v236\n\t" \
  "ds_write_b64 v192, v[176:177] offset:8192\n\t" \
  "ds_read_b64 v[216:217], v253 offset:16408\n\t" \
  "v_add_u32 v194, -86112, v194\n\t" \
  "v_smfmac_f32_16x16x64_bf16 v[32:35], v[128:131], v[200:207], v236\n\t" \
  "ds_read_b64 v[218:219], v253 offset:16808\n\t" \
  "ds_read_b64 v[220:221], v253 offset:17208\n\t" \
  "v_smfmac_f32_16x16x64_bf16 v[20:23], v[132:135], v[200:207], v236\n\t" \
  "ds_read_b64 v[222:223], v253 offset:17608\n\t" \
  "s_waitcnt lgkmcnt(11)\n\t" \
  "v_mfma_f32_16x16x32_bf16 v[168:171], v[144:147], v[144:147], 0\n\t" \
  "ds_read_b64 v[224:225], v253 offset:16416\n\t" \
  "v_smfmac_f32_16x16x64_bf16 v[8:11], v[136:139], v[200:207], v236\n\t" \
  "ds_read_b64 v[226:227], v253 offset:16816\n\t" \
  "v_cvt_pk_bf16_f32 v184, v184, v185\n\t" \
  "v_cvt_pk_bf16_f32 v185, v186, v187\n\t" \
  "v_mfma_f32_16x16x32_bf16 v[172:175], v[196:199], v[144:147], 0\n\t" \
  "ds_read_b64 v[228:229], v253 offset:17216\n\t" \
  "v_smfmac_f32_16x16x64_bf16 v[188:191], v[140:143], v[200:207], v236\n\t" \
  "ds_read_b64 v[230:231], v253 offset:17616\n\t" \
  "s_waitcnt lgkmcnt(12)\n\t" \
  "v_mfma_f32_16x16x32_bf16 v[168:171], v[148:151], v[148:151], v[168:171]\n\t" \
  "s_waitcnt lgkmcnt(10)\n\t" \
  "v_smfmac_f32_16x16x64_bf16 v[36:39], v[96:99], v[208:215], v236\n\t" \
  "v_mfma_f32_16x16x32_bf16 v[172:175], v[196:199], v[148:151], v[172:175]\n\t" \
  "v_smfmac_f32_16x16x64_bf16 v[24:27], v[100:103], v[208:215], v236\n\t" \
  "s_waitcnt lgkmcnt(9)\n\t" \
  "v_mfma_f32_16x16x32_bf16 v[168:171], v[152:155], v[152:155], v[168:171]\n\t" \
  "v_smfmac_f32_16x16x64_bf16 v[12:15], v[104:107], v[208:215], v236\n\t" \
  "v_cvt_pk_bf16_f32 v188, v188, v189\n\t" \
  "v_cvt_pk_bf16_f32 v189, v190, v191\n\t" \
  "v_mfma_f32_16x16x32_bf16 v[172:175], v[196:199], v[152:155], v[172:175]\n\t" \
  "v_smfmac_f32_16x16x64_bf16 v[0:3], v[108:111], v[208:215], v236\n\t" \
  "buffer_store_dwordx4 v[76:79], v250, s[20:23], s62 offen offset:0 sc1\n\t" \
  "s_waitcnt vmcnt(8) lgkmcnt(0)\n\t" \
  "s_barrier\n\t" \
  "s_cmp_lg_u32 s58, 0\n\t" \
  "s_cbranch_scc1 .Lcnx_c6\n\t" \
  "v_lshl_add_u32 v48, v233, 1, s63\n\t" \
  "s_lshl_b32 m0, s39, 1\n\t" \
  "v_add_u32 v49, 0x2000, v48\n\t" \
  "global_load_lds_dwordx4 v48, s[6:7]\n\t" \
  "s_add_u32 m0, m0, 0x2000\n\t" \
  "s_cmp_eq_u32 s39, 3584\n\t" \
  "s_cselect_b32 exec_hi, 1, -1\n\t" \
  "global_load_lds_dwordx4 v49, s[6:7]\n\t" \
  "s_mov_b64 exec, -1\n\t" \
  ".Lcnx_c6:\n\t" \
  "v_smfmac_f32_16x16x64_bf16 v[40:43], v[112:115], v[216:223], v236\n\t" \
  "ds_write_b64 v192, v[180:181] offset:12288\n\t" \
  "ds_read_b64 v[200:201], v253 offset:32800\n\t" \
  "v_smfmac_f32_16x16x64_bf16 v[28:31], v[116:119], v[216:223], v236\n\t" \
  "ds_write_b64 v192, v[184:185] offset:16384\n\t" \
  "ds_read_b64 v[202:203], v253 offset:33200\n\t" \
  "v_cndmask_b32 v168, v168, v169, s[40:41]\n\t" \
  "v_cndmask_b32 v170, v170, v171, s[40:41]\n\t" \
  "v_smfmac_f32_16x16x64_bf16 v[16:19], v[120:123], v[216:223], v236\n\t" \
  "ds_write_b64 v192, v[188:189] offset:20480\n\t" \
  "ds_read_b64 v[204:205], v253 offset:33600\n\t" \
  "v_cvt_pk_bf16_f32 v0, v0, v1\n\t" \
  "v_cvt_pk_bf16_f32 v1, v2, v3\n\t" \
  "v_cndmask_b32 v173, v168, v170, s[42:43]\n\t" \
  "v_mov_b32 v174, 0x3927c5ac\n\t" \
  "ds_read_b64_tr_b16 v[156:157], v195 offset:0\n\t" \
  "v_smfmac_f32_16x16x64_bf16 v[4:7], v[124:127], v[216:223], v236\n\t" \
  "ds_read_b64 v[206:207], v253 offset:34000\n\t" \
  "ds_read_b64 v[208:209], v253 offset:32808\n\t" \
  "s_not_b64 vcc, s[52:53]\n\t" \
  "ds_read_b64_tr_b16 v[158:159], v195 offset:4096\n\t" \
  "v_smfmac_f32_16x16x64_bf16 v[44:47], v[128:131], v[224:231], v236\n\t" \
  "ds_read_b64 v[210:211], v253 offset:33208\n\t" \
  "ds_read_b64 v[212:213], v253 offset:33608\n\t" \
  "ds_read_b64_tr_b16 v[160:161], v195 offset:2048\n\t" \
  "v_smfmac_f32_16x16x64_bf16 v[32:35], v[132:135], v[224:231], v236\n\t" \
  "ds_read_b64 v[214:215], v253 offset:34008\n\t" \
  "ds_read_b64 v[216:217], v253 offset:32816\n\t" \
  "ds_read_b64_tr_b16 v[162:163], v195 offset:6144\n\t" \
  "v_smfmac_f32_16x16x64_bf16 v[20:23], v[136:139], v[224:231], v236\n\t" \
  "ds_read_b64 v[218:219], v253 offset:33216\n\t" \
  "v_cvt_pk_bf16_f32 v4, v4, v5\n\t" \
  "v_cvt_pk_bf16_f32 v5, v6, v7\n\t" \
  "ds_read_b64_tr_b16 v[164:165], v195 offset:8192\n\t" \
  "v_smfmac_f32_16x16x64_bf16 v[8:11], v[140:143], v[224:231], v236\n\t" \
  "ds_read_b64 v[220:221], v253 offset:33616\n\t" \
  "ds_read_b64_tr_b16 v[166:167], v195 offset:10240\n\t" \
  "s_waitcnt lgkmcnt(12)\n\t" \
  "v_smfmac_f32_16x16x64_bf16 v[36:39], v[100:103], v[200:207], v236\n\t" \
  "ds_read_b64 v[222:223], v253 offset:34016\n\t" \
  "v_add_u32 v195, -86112, v195\n\t" \
  "v_smfmac_f32_16x16x64_bf16 v[24:27], v[104:107], v[200:207], v236\n\t" \
  "v_smfmac_f32_16x16x64_bf16 v[12:15], v[108:111], v[200:207], v236\n\t" \
  "buffer_store_dwordx4 v[84:87], v250, s[20:23], s62 offen offset:64 sc1\n\t" \
  "v_cvt_pk_bf16_f32 v8, v8, v9\n\t" \
  "v_cvt_pk_bf16_f32 v9, v10, v11\n\t" \
  "s_waitcnt vmcnt(7) lgkmcnt(0)\n\t" \
  "s_barrier\n\t" \
  "s_cmp_ge_u32 s36, 2\n\t" \
  "s_cbranch_scc1 .Lcnx_c7\n\t" \
  "s_mov_b32 m0, s46\n\t" \
  "s_nop 0\n\t" \
  "buffer_load_dwordx4 v252, s[16:19], s33 offen lds\n\t" \
  "buffer_load_dwordx4 v252, s[16:19], s34 offen offset:1024 lds\n\t" \
  "buffer_load_dwordx4 v252, s[16:19], s35 offen offset:2048 lds\n\t" \
  "s_add_u32 s33, s33, s32\n\t" \
  "s_add_u32 s34, s34, s32\n\t" \
  "s_add_u32 s35, s35, s32\n\t" \
  "s_mov_b32 m0, s47\n\t" \
  "s_nop 0\n\t" \
  "buffer_load_dwordx4 v252, s[16:19], s33 offen lds\n\t" \
  "buffer_load_dwordx4 v252, s[16:19], s34 offen offset:1024 lds\n\t" \
  "buffer_load_dwordx4 v252, s[16:19], s35 offen offset:2048 lds\n\t" \
  "s_add_u32 s33, s33, s32\n\t" \
  "s_add_u32 s34, s34, s32\n\t" \
  "s_add_u32 s35, s35, s32\n\t" \
  ".Lcnx_c7:\n\t" \
  "v_smfmac_f32_16x16x64_bf16 v[40:43], v[116:119], v[208:215], v236\n\t" \
  "ds_write_b64 v192, v[0:1] offset:24576\n\t" \
  "ds_read_b64 v[224:225], v232 offset:0\n\t" \
  "ds_read_b128 v[180:183], v194 offset:0\n\t" \
  "ds_read_b128 v[184:187], v194 offset:4096\n\t" \
  "ds_read_b128 v[188:191], v194 offset:8192\n\t" \
  "v_smfmac_f32_16x16x64_bf16 v[28:31], v[120:123], v[208:215], v236\n\t" \
  "ds_write_b64 v192, v[4:5] offset:28672\n\t" \
  "ds_read_b64 v[226:227], v232 offset:400\n\t" \
  "ds_read_b64 v[228:229], v232 offset:800\n\t" \
  "v_smfmac_f32_16x16x64_bf16 v[16:19], v[124:127], v[208:215], v236\n\t" \
  "ds_write_b64 v192, v[8:9] offset:32768\n\t" \
  "ds_read_b64 v[230:231], v232 offset:1200\n\t" \
  "ds_read_b64 v[200:201], v232 offset:8\n\t" \
  "v_cvt_pk_bf16_f32 v12, v12, v13\n\t" \
  "v_cvt_pk_bf16_f32 v13, v14, v15\n\t" \
  "v_smfmac_f32_16x16x64_bf16 v[44:47], v[132:135], v[216:223], v236\n\t" \
  "ds_write_b64 v192, v[12:13] offset:36864\n\t" \
  "ds_read_b64 v[202:203], v232 offset:408\n\t" \
  "ds_read_b64 v[204:205], v232 offset:808\n\t" \
  "s_waitcnt lgkmcnt(11)\n\t" \
  "v_mfma_f32_16x16x32_bf16 v[176:179], v[180:183], v[180:183], 0\n\t" \
  "ds_read_b64 v[206:207], v232 offset:1208\n\t" \
  "v_mfma_f32_16x16x32_bf16 v[168:171], v[196:199], v[180:183], 0\n\t" \
  "ds_read_b64 v[208:209], v232 offset:16\n\t" \
  "v_smfmac_f32_16x16x64_bf16 v[32:35], v[136:139], v[216:223], v236\n\t" \
  "ds_read_b64 v[210:211], v232 offset:416\n\t" \
  "ds_read_b64 v[212:213], v232 offset:816\n\t" \
  "s_waitcnt lgkmcnt(14)\n\t" \
  "v_mfma_f32_16x16x32_bf16 v[176:179], v[184:187], v[184:187], v[176:179]\n\t" \
  "ds_read_b64 v[214:215], v232 offset:1216\n\t" \
  "v_mfma_f32_16x16x32_bf16 v[168:171], v[196:199], v[184:187], v[168:171]\n\t" \
  "v_smfmac_f32_16x16x64_bf16 v[20:23], v[140:143], v[216:223], v236\n\t" \
  "v_cvt_pk_bf16_f32 v16, v16, v17\n\t" \
  "v_cvt_pk_bf16_f32 v17, v18, v19\n\t" \
  "s_waitcnt lgkmcnt(14)\n\t" \
  "v_mfma_f32_16x16x32_bf16 v[176:179], v[188:191], v[188:191], v[176:179]\n\t" \
  "ds_write_b64 v192, v[16:17] offset:40960\n\t" \
  "v_mfma_f32_16x16x32_bf16 v[168:171], v[196:199], v[188:191], v[168:171]\n\t" \
  "s_waitcnt lgkmcnt(10)\n\t" \
  "v_smfmac_f32_16x16x64_bf16 v[36:39], v[104:107], v[224:231], v236\n\t" \
  "v_mov_b32 v183, 0\n\t" \
  "v_mov_b32 v187, 0\n\t" \
  "s_nop 1\n\t" \
  "v_cndmask_b32 v176, v176, v177, s[40:41]\n\t" \
  "v_cndmask_b32 v178, v178, v179, s[40:41]\n\t" \
  "v_cndmask_b32 v176, v176, v178, s[42:43]\n\t" \
  "v_smfmac_f32_16x16x64_bf16 v[24:27], v[108:111], v[224:231], v236\n\t" \
  "v_cndmask_b32_dpp v172, v168, v172, vcc row_ror:8 row_mask:0xf bank_mask:0xf\n\t" \
  "v_mul_f32 v172, 0x3daaaaab, v172\n\t" \
  "v_cndmask_b32_dpp v173, v176, v173, vcc row_ror:8 row_mask:0xf bank_mask:0xf\n\t" \
  "v_fmamk_f32 v173, v173, 0x3e2aaaab, v174\n\t" \
  "s_waitcnt vmcnt(5) lgkmcnt(0)\n\t" \
  "s_barrier\n\t" \
  "ds_read_b64 v[216:217], v232 offset:16400\n\t" \
  "ds_read_b64 v[218:219], v232 offset:16800\n\t" \
  "ds_read_b64 v[220:221], v232 offset:17200\n\t" \
  "ds_read_b64 v[222:223], v232 offset:17600\n\t" \
  "v_smfmac_f32_16x16x64_bf16 v[40:43], v[120:123], v[200:207], v236\n\t" \
  "ds_read_b64 v[224:225], v232 offset:16408\n\t" \
  "ds_read_b64 v[226:227], v232 offset:16808\n\t" \
  "v_cvt_pk_bf16_f32 v20, v20, v21\n\t" \
  "v_cvt_pk_bf16_f32 v21, v22, v23\n\t" \
  "v_fma_f32 v173, -v172, v172, v173\n\t" \
  "v_max_f32 v173, v174, v173\n\t" \
  "v_rsq_f32 v173, v173 div:2\n\t" \
  "s_mov_b64 vcc, s[40:41]\n\t" \
  "v_smfmac_f32_16x16x64_bf16 v[28:31], v[124:127], v[200:207], v236\n\t" \
  "buffer_store_dwordx4 v[92:95], v250, s[20:23], s62 offen offset:128 sc1\n\t" \
  "ds_read_b64 v[228:229], v232 offset:17208\n\t" \
  "ds_read_b64 v[230:231], v232 offset:17608\n\t" \
  "v_cvt_pk_bf16_f32 v175, v173, v173\n\t" \
  "v_and_b32 v173, 0xffff0000, v175\n\t" \
  "v_mul_f32 v174, -v172, v173 div:2\n\t" \
  "v_cndmask_b32_sdwa v175, v175, v173, vcc dst_sel:DWORD dst_unused:UNUSED_PAD src0_sel:WORD_1 src1_sel:DWORD\n\t" \
  "v_smfmac_f32_16x16x64_bf16 v[44:47], v[136:139], v[208:215], v236\n\t" \
  "ds_write_b64 v192, v[20:21] offset:45056\n\t" \
  "ds_read_b64 v[200:201], v232 offset:16416\n\t" \
  "v_cvt_pk_bf16_f32 v24, v24, v25\n\t" \
  "v_cvt_pk_bf16_f32 v25, v26, v27\n\t" \
  "v_and_b32 v172, 0xffff0000, v174\n\t" \
  "s_or_b64 vcc, s[52:53], s[42:43]\n\t" \
  "v_sub_f32 v174, v174, v172\n\t" \
  "v_cndmask_b32 v180, v175, v240, vcc\n\t" \
  "v_smfmac_f32_16x16x64_bf16 v[32:35], v[140:143], v[208:215], v236\n\t" \
  "ds_write_b64 v192, v[24:25] offset:49152\n\t" \
  "ds_read_b64 v[202:203], v232 offset:16816\n\t" \
  "v_perm_b32 v174, v172, v174, s55\n\t" \
  "v_cndmask_b32_dpp v184, v175, v240, vcc row_ror:8 row_mask:0xf bank_mask:0xf\n\t" \
  "s_mov_b64 vcc, s[52:53]\n\t" \
  "v_cndmask_b32 v182, v174, v240, vcc\n\t" \
  "ds_read_b64_tr_b16 v[144:145], v195 offset:0\n\t" \
  "ds_read_b64_tr_b16 v[146:147], v195 offset:4096\n\t" \
  "s_waitcnt lgkmcnt(10)\n\t" \
  "v_smfmac_f32_16x16x64_bf16 v[36:39], v[108:111], v[216:223], v236\n\t" \
  "ds_read_b64 v[204:205], v232 offset:17216\n\t" \
  "ds_read_b64 v[206:207], v232 offset:17616\n\t" \
  "v_cvt_pk_bf16_f32 v28, v28, v29\n\t" \
  "v_cvt_pk_bf16_f32 v29, v30, v31\n\t" \
  "v_cndmask_b32_dpp v186, v174, v240, vcc row_ror:8 row_mask:0xf bank_mask:0xf\n\t" \
  "s_orn2_b64 vcc, s[52:53], s[42:43]\n\t" \
  "v_cndmask_b32 v181, v175, v240, vcc\n\t" \
  "v_cndmask_b32_dpp v185, v175, v240, vcc row_ror:8 row_mask:0xf bank_mask:0xf\n\t" \
  "ds_read_b64_tr_b16 v[148:149], v195 offset:2048\n\t" \
  "ds_read_b64_tr_b16 v[150:151], v195 offset:6144\n\t" \
  "s_waitcnt lgkmcnt(10)\n\t" \
  "v_smfmac_f32_16x16x64_bf16 v[40:43], v[124:127], v[224:231], v236\n\t" \
  "ds_write_b64 v192, v[28:29] offset:53248\n\t" \
  "ds_read_b64_tr_b16 v[152:153], v195 offset:8192\n\t" \
  "ds_read_b64_tr_b16 v[154:155], v195 offset:10240\n\t" \
  "v_mfma_f32_16x16x16_bf16 v[0:3], v[196:197], v[182:183], 0\n\t" \
  "v_mfma_f32_16x16x16_bf16 v[4:7], v[196:197], v[186:187], 0\n\t" \
  "s_waitcnt lgkmcnt(5)\n\t" \
  "v_smfmac_f32_16x16x64_bf16 v[44:47], v[140:143], v[200:207], v236\n\t" \
  "v_cvt_pk_bf16_f32 v32, v32, v33\n\t" \
  "v_cvt_pk_bf16_f32 v33, v34, v35\n\t" \
  "v_mfma_f32_16x16x16_bf16 v[188:191], v[156:157], v[180:181], v[0:3]\n\t" \
  "ds_write_b64 v192, v[32:33] offset:57344\n\t" \
  "v_mfma_f32_16x16x16_bf16 v[176:179], v[158:159], v[180:181], v[0:3]\n\t" \
  "v_cvt_pk_bf16_f32 v36, v36, v37\n\t" \
  "v_cvt_pk_bf16_f32 v37, v38, v39\n\t" \
  "ds_write_b64 v193, v[36:37] offset:0\n\t" \
  "v_cvt_pk_bf16_f32 v40, v40, v41\n\t" \
  "v_cvt_pk_bf16_f32 v41, v42, v43\n\t" \
  "ds_write_b64 v193, v[40:41] offset:4096\n\t" \
  "v_mfma_f32_16x16x16_bf16 v[168:171], v[160:161], v[180:181], v[0:3]\n\t" \
  "v_cvt_pk_bf16_f32 v96, v188, v189\n\t" \
  "v_cvt_pk_bf16_f32 v97, v190, v191\n\t" \
  "v_mfma_f32_16x16x16_bf16 v[172:175], v[162:163], v[180:181], v[0:3]\n\t" \
  "v_cvt_pk_bf16_f32 v44, v44, v45\n\t" \
  "v_cvt_pk_bf16_f32 v45, v46, v47\n\t" \
  "ds_write_b64 v193, v[44:45] offset:8192\n\t" \
  "v_cvt_pk_bf16_f32 v98, v176, v177\n\t" \
  "v_cvt_pk_bf16_f32 v99, v178, v179\n\t" \
  "v_mfma_f32_16x16x16_bf16 v[188:191], v[164:165], v[180:181], v[0:3]\n\t" \
  "v_cvt_pk_bf16_f32 v112, v168, v169\n\t" \
  "v_cvt_pk_bf16_f32 v113, v170, v171\n\t" \
  "v_mfma_f32_16x16x16_bf16 v[176:179], v[166:167], v[180:181], v[0:3]\n\t" \
  "v_cvt_pk_bf16_f32 v114, v172, v173\n\t" \
  "v_cvt_pk_bf16_f32 v115, v174, v175\n\t" \
  "s_nop 2\n\t" \
  "v_cvt_pk_bf16_f32 v128, v188, v189\n\t" \
  "v_cvt_pk_bf16_f32 v129, v190, v191\n\t" \
  "s_nop 0\n\t" \
  "v_cvt_pk_bf16_f32 v130, v176, v177\n\t" \
  "v_cvt_pk_bf16_f32 v131, v178, v179\n\t" \
  "s_cmp_lg_u32 s58, 0\n\t" \
  "s_cbranch_scc1 .Lcnx_c8\n\t" \
  "s_waitcnt vmcnt(0)\n\t" \
  ".Lcnx_c8:\n\t" \
  "s_waitcnt lgkmcnt(0)\n\t" \
  "s_barrier\n\t" \
  "ds_read_b64_tr_b16 v[208:209], v248 offset:43048\n\t" \
  "ds_read_b64_tr_b16 v[210:211], v248 offset:44072\n\t" \
  "ds_read_b128 v[104:107], v194 offset:24576\n\t" \
  "ds_read_b128 v[120:123], v194 offset:28672\n\t" \
  "ds_read_b128 v[136:139], v194 offset:32768\n\t" \
  "v_add_u32 v194, s54, v194\n\t" \
  "ds_read_b128 v[108:111], v194 offset:49152\n\t" \
  "ds_read_b128 v[124:127], v194 offset:53248\n\t" \
  "ds_read_b128 v[140:143], v194 offset:57344\n\t" \
  "v_mfma_f32_16x16x16_bf16 v[168:171], v[144:145], v[184:185], v[4:7]\n\t" \
  "ds_read_b64_tr_b16 v[212:213], v248 offset:46120\n\t" \
  "ds_read_b64_tr_b16 v[214:215], v248 offset:47144\n\t" \
  "v_mfma_f32_16x16x16_bf16 v[172:175], v[146:147], v[184:185], v[4:7]\n\t" \
  "ds_read_b64_tr_b16 v[216:217], v248 offset:45096\n\t" \
  "ds_read_b64_tr_b16 v[218:219], v248 offset:48168\n\t" \
  "s_cmp_eq_u32 s37, 0\n\t" \
  "s_cbranch_scc1 .Lcnx_f0\n.Lcnx_g0:\n\t" \
  "v_mfma_f32_16x16x16_bf16 v[188:191], v[148:149], v[184:185], v[4:7]\n\t" \
  "ds_read_b64_tr_b16 v[160:161], v248 offset:43560\n\t" \
  "ds_read_b64_tr_b16 v[162:163], v248 offset:44584\n\t" \
  "v_cvt_pk_bf16_f32 v100, v168, v169\n\t" \
  "v_cvt_pk_bf16_f32 v101, v170, v171\n\t" \
  "v_mfma_f32_16x16x16_bf16 v[176:179], v[150:151], v[184:185], v[4:7]\n\t" \
  "ds_read_b64_tr_b16 v[164:165], v248 offset:46632\n\t" \
  "ds_read_b64_tr_b16 v[166:167], v248 offset:47656\n\t" \
  "s_cmp_eq_u32 s37, 0\n\t" \
  "s_cbranch_scc1 .Lcnx_f1\n.Lcnx_g1:\n\t" \
  "v_cvt_pk_bf16_f32 v102, v172, v173\n\t" \
  "v_cvt_pk_bf16_f32 v103, v174, v175\n\t" \
  "v_mfma_f32_16x16x16_bf16 v[168:171], v[152:153], v[184:185], v[4:7]\n\t" \
  "buffer_load_dwordx4 v[236:239], v250, s[8:11], s28 offen offset:0\n\t" \
  "ds_read_b64_tr_b16 v[232:233], v248 offset:45608\n\t" \
  "ds_read_b64_tr_b16 v[234:235], v248 offset:48680\n\t" \
  "v_cvt_pk_bf16_f32 v116, v188, v189\n\t" \
  "v_cvt_pk_bf16_f32 v117, v190, v191\n\t" \
  "v_mfma_f32_16x16x16_bf16 v[172:175], v[154:155], v[184:185], v[4:7]\n\t" \
  "ds_read_b64_tr_b16 v[24:25], v195 offset:24576\n\t" \
  "ds_read_b64_tr_b16 v[26:27], v195 offset:28672\n\t" \
  "v_cvt_pk_bf16_f32 v118, v176, v177\n\t" \
  "v_cvt_pk_bf16_f32 v119, v178, v179\n\t" \
  "s_waitcnt lgkmcnt(15)\n\t" \
  "v_mfma_f32_16x16x32_bf16 v[144:147], v[208:211], v[96:99], 0\n\t" \
  "ds_read_b64_tr_b16 v[28:29], v195 offset:26624\n\t" \
  "ds_read_b64_tr_b16 v[30:31], v195 offset:30720\n\t" \
  "v_cvt_pk_bf16_f32 v132, v168, v169\n\t" \
  "v_cvt_pk_bf16_f32 v133, v170, v171\n\t" \
  "s_waitcnt lgkmcnt(12)\n\t" \
  "v_mfma_f32_16x16x32_bf16 v[144:147], v[212:215], v[112:115], v[144:147]\n\t" \
  "buffer_store_dwordx4 v[48:51], v250, s[20:23], s14 offen offset:0 sc1\n\t" \
  "ds_read_b64_tr_b16 v[32:33], v195 offset:32768\n\t" \
  "ds_read_b64_tr_b16 v[34:35], v195 offset:34816\n\t" \
  "v_cvt_pk_bf16_f32 v134, v172, v173\n\t" \
  "v_cvt_pk_bf16_f32 v135, v174, v175\n\t" \
  "v_mfma_f32_16x16x32_bf16 v[152:155], v[208:211], v[100:103], 0\n\t" \
  "v_add_u32 v195, s54, v195\n\t" \
  "ds_read_b64_tr_b16 v[36:37], v195 offset:49152\n\t" \
  "ds_read_b64_tr_b16 v[38:39], v195 offset:53248\n\t" \
  "s_waitcnt lgkmcnt(14)\n\t" \
  "v_mfma_f32_16x16x32_bf16 v[144:147], v[216:219], v[128:131], v[144:147]\n\t" \
  "ds_read_b64_tr_b16 v[40:41], v195 offset:51200\n\t" \
  "v_mfma_f32_16x16x32_bf16 v[152:155], v[212:215], v[116:119], v[152:155]\n\t" \
  "ds_read_b64_tr_b16 v[42:43], v195 offset:55296\n\t" \
  "v_mfma_f32_16x16x32_bf16 v[152:155], v[216:219], v[132:135], v[152:155]\n\t" \
  "ds_read_b64_tr_b16 v[44:45], v195 offset:57344\n\t" \
  "v_mfma_f32_16x16x32_bf16 v[8:11], v[104:107], v[104:107], 0\n\t" \
  "ds_read_b64_tr_b16 v[46:47], v195 offset:59392\n\t" \
  "v_mfma_f32_16x16x32_bf16 v[12:15], v[196:199], v[104:107], 0\n\t" \
  "v_cvt_pk_f16_f32 v220, |v146|, |v147| clamp\n\t" \
  "v_cvt_pknorm_i16_f32 v222, |v144|, |v145|\n\t" \
  "v_mfma_f32_16x16x32_bf16 v[8:11], v[120:123], v[120:123], v[8:11]\n\t" \
  "s_nop 0\n\t" \
  "v_cvt_pk_f16_f32 v224, |v154|, |v155| clamp\n\t" \
  "v_cvt_pknorm_i16_f32 v226, |v152|, |v153|\n\t" \
  "v_mfma_f32_16x16x32_bf16 v[12:15], v[196:199], v[120:123], v[12:15]\n\t" \
  "v_and_b32_sdwa v221, s56, v220 dst_sel:DWORD dst_unused:UNUSED_PAD src0_sel:DWORD src1_sel:WORD_1\n\t" \
  "v_and_b32 v220, s56, v220\n\t" \
  "v_mfma_f32_16x16x32_bf16 v[8:11], v[136:139], v[136:139], v[8:11]\n\t" \
  "ds_read_b32 v221, v221 offset:0\n\t" \
  "v_lshlrev_b32_sdwa v223, 2, v222 dst_sel:DWORD dst_unused:UNUSED_PAD src0_sel:DWORD src1_sel:BYTE_3\n\t" \
  "v_lshlrev_b32_sdwa v222, 2, v222 dst_sel:DWORD dst_unused:UNUSED_PAD src0_sel:DWORD src1_sel:BYTE_1\n\t" \
  "v_mfma_f32_16x16x32_bf16 v[12:15], v[196:199], v[136:139], v[12:15]\n\t" \
  "ds_read_b32 v220, v220 offset:0\n\t" \
  "v_and_b32_sdwa v225, s56, v224 dst_sel:DWORD dst_unused:UNUSED_PAD src0_sel:DWORD src1_sel:WORD_1\n\t" \
  "v_and_b32 v224, s56, v224\n\t" \
  "v_mfma_f32_16x16x32_bf16 v[16:19], v[108:111], v[108:111], 0\n\t" \
  "ds_read_b32 v223, v223 offset:15376\n\t" \
  "v_lshlrev_b32_sdwa v227, 2, v226 dst_sel:DWORD dst_unused:UNUSED_PAD src0_sel:DWORD src1_sel:BYTE_3\n\t" \
  "v_lshlrev_b32_sdwa v226, 2, v226 dst_sel:DWORD dst_unused:UNUSED_PAD src0_sel:DWORD src1_sel:BYTE_1\n\t" \
  "v_mfma_f32_16x16x32_bf16 v[20:23], v[196:199], v[108:111], 0\n\t" \
  "ds_read_b32 v222, v222 offset:15376\n\t" \
  "v_cndmask_b32 v8, v8, v9, s[40:41]\n\t" \
  "v_cndmask_b32 v10, v10, v11, s[40:41]\n\t" \
  "v_mfma_f32_16x16x32_bf16 v[16:19], v[124:127], v[124:127], v[16:19]\n\t" \
  "ds_read_b32 v225, v225 offset:0\n\t" \
  "v_cndmask_b32 v13, v8, v10, s[42:43]\n\t" \
  "v_mov_b32 v14, 0x3927c5ac\n\t" \
  "v_mfma_f32_16x16x32_bf16 v[20:23], v[196:199], v[124:127], v[20:23]\n\t" \
  "buffer_store_dwordx4 v[56:59], v250, s[20:23], s14 offen offset:64 sc1\n\t" \
  "ds_read_b32 v224, v224 offset:0\n\t" \
  "s_not_b64 vcc, s[52:53]\n\t" \
  "v_mfma_f32_16x16x32_bf16 v[16:19], v[140:143], v[140:143], v[16:19]\n\t" \
  "ds_read_b32 v227, v227 offset:15376\n\t" \
  "ds_read_b32 v226, v226 offset:15376\n\t" \
  "s_waitcnt lgkmcnt(4)\n\t" \
  "v_fma_f32 v147, |v147|, v221, v147 mul:4\n\t" \
  "v_fma_f32 v146, |v146|, v220, v146 mul:4\n\t" \
  "v_mfma_f32_16x16x32_bf16 v[20:23], v[196:199], v[140:143], v[20:23]\n\t" \
  "v_fma_mix_f32 v223, |v145|, v223, v223 op_sel:[0,1,0] op_sel_hi:[0,1,1] clamp\n\t" \
  "v_fma_mix_f32 v222, |v144|, v222, v222 op_sel:[0,1,0] op_sel_hi:[0,1,1] clamp\n\t" \
  "v_mfma_f32_16x16x32_bf16 v[148:151], v[160:163], v[96:99], 0\n\t" \
  "s_waitcnt lgkmcnt(0)\n\t" \
  "v_fma_f32 v155, |v155|, v225, v155 mul:4\n\t" \
  "v_fma_f32 v154, |v154|, v224, v154 mul:4\n\t" \
  "v_mfma_f32_16x16x32_bf16 v[156:159], v[160:163], v[100:103], 0\n\t" \
  "v_cndmask_b32 v16, v16, v17, s[40:41]\n\t" \
  "v_cndmask_b32 v18, v18, v19, s[40:41]\n\t" \
  "v_cndmask_b32 v16, v16, v18, s[42:43]\n\t" \
  "v_cndmask_b32_dpp v12, v20, v12, vcc row_ror:8 row_mask:0xf bank_mask:0xf\n\t" \
  "v_mul_f32 v12, 0x3daaaaab, v12\n\t" \
  "v_mfma_f32_16x16x32_bf16 v[148:151], v[164:167], v[112:115], v[148:151]\n\t" \
  "v_cndmask_b32_dpp v13, v16, v13, vcc row_ror:8 row_mask:0xf bank_mask:0xf\n\t" \
  "v_fmamk_f32 v13, v13, 0x3e2aaaab, v14\n\t" \
  "v_fma_f32 v13, -v12, v12, v13\n\t" \
  "v_max_f32 v13, v14, v13\n\t" \
  "v_rsq_f32 v13, v13 div:2\n\t" \
  "v_mfma_f32_16x16x32_bf16 v[156:159], v[164:167], v[116:119], v[156:159]\n\t" \
  "s_mov_b64 vcc, s[40:41]\n\t" \
  "v_cvt_pk_bf16_f32 v15, v13, v13\n\t" \
  "v_and_b32 v13, 0xffff0000, v15\n\t" \
  "v_mul_f32 v14, -v12, v13 div:2\n\t" \
  "v_cndmask_b32_sdwa v15, v15, v13, vcc dst_sel:DWORD dst_unused:UNUSED_PAD src0_sel:WORD_1 src1_sel:DWORD\n\t" \
  "v_and_b32 v12, 0xffff0000, v14\n\t" \
  "v_mfma_f32_16x16x32_bf16 v[148:151], v[232:235], v[128:131], v[148:151]\n\t" \
  "s_or_b64 vcc, s[52:53], s[42:43]\n\t" \
  "v_sub_f32 v14, v14, v12\n\t" \
  "v_cndmask_b32 v180, v15, v240, vcc\n\t" \
  "v_perm_b32 v14, v12, v14, s55\n\t" \
  "v_cndmask_b32_dpp v184, v15, v240, vcc row_ror:8 row_mask:0xf bank_mask:0xf\n\t" \
  "v_mfma_f32_16x16x32_bf16 v[156:159], v[232:235], v[132:135], v[156:159]\n\t" \
  "s_mov_b64 vcc, s[52:53]\n\t" \
  "v_cndmask_b32 v182, v14, v240, vcc\n\t" \
  "v_cndmask_b32_dpp v186, v14, v240, vcc row_ror:8 row_mask:0xf bank_mask:0xf\n\t" \
  "s_orn2_b64 vcc, s[52:53], s[42:43]\n\t" \
  "v_cndmask_b32 v181, v15, v240, vcc\n\t" \
  "v_mfma_f32_16x16x16_bf16 v[0:3], v[196:197], v[182:183], 0\n\t" \
  "v_cndmask_b32_dpp v185, v15, v240, vcc row_ror:8 row_mask:0xf bank_mask:0xf\n\t" \
  "s_mov_b64 vcc, 0\n\t" \
  "v_fma_mix_f32 v227, |v153|, v227, v227 op_sel:[0,1,0] op_sel_hi:[0,1,1] clamp\n\t" \
  "v_mfma_f32_16x16x16_bf16 v[4:7], v[196:197], v[186:187], 0\n\t" \
  "v_fma_mix_f32 v226, |v152|, v226, v226 op_sel:[0,1,0] op_sel_hi:[0,1,1] clamp\n\t" \
  "v_fma_f32 v145, |v145|, v223, v145 mul:4\n\t" \
  "v_mfma_f32_16x16x16_bf16 v[188:191], v[24:25], v[180:181], v[0:3]\n\t" \
  "v_fma_f32 v144, |v144|, v222, v144 mul:4\n\t" \
  "v_fma_f32 v153, |v153|, v227, v153 mul:4\n\t" \
  "v_mfma_f32_16x16x16_bf16 v[176:179], v[26:27], v[180:181], v[0:3]\n\t" \
  "v_fma_f32 v152, |v152|, v226, v152 mul:4\n\t" \
  "v_cvt_pk_bf16_f32 v147, v146, v147\n\t" \
  "v_mfma_f32_16x16x16_bf16 v[200:203], v[36:37], v[184:185], v[4:7]\n\t" \
  "s_nop 1\n\t" \
  "v_cvt_pk_bf16_f32 v104, v188, v189\n\t" \
  "v_cvt_pk_bf16_f32 v105, v190, v191\n\t" \
  "v_mfma_f32_16x16x16_bf16 v[204:207], v[38:39], v[184:185], v[4:7]\n\t" \
  "buffer_store_dwordx4 v[64:67], v250, s[20:23], s14 offen offset:128 sc1\n\t" \
  "v_cvt_pk_bf16_f32 v106, v176, v177\n\t" \
  "v_cvt_pk_bf16_f32 v107, v178, v179\n\t" \
  "v_mfma_f32_16x16x16_bf16 v[196:199], v[28:29], v[180:181], v[0:3]\n\t" \
  "v_cvt_pk_bf16_f32 v108, v200, v201\n\t" \
  "v_cvt_pk_bf16_f32 v109, v202, v203\n\t" \
  "v_mfma_f32_16x16x16_bf16 v[188:191], v[30:31], v[180:181], v[0:3]\n\t" \
  "s_nop 0\n\t" \
  "v_cvt_pk_bf16_f32 v110, v204, v205\n\t" \
  "v_cvt_pk_bf16_f32 v111, v206, v207\n\t" \
  "v_mfma_f32_16x16x32_bf16 v[160:163], v[208:211], v[104:107], 0\n\t" \
  "s_nop 0\n\t" \
  "v_cvt_pk_bf16_f32 v120, v196, v197\n\t" \
  "v_cvt_pk_bf16_f32 v121, v198, v199\n\t" \
  "v_mfma_f32_16x16x16_bf16 v[176:179], v[40:41], v[184:185], v[4:7]\n\t" \
  "v_cvt_pk_bf16_f32 v122, v188, v189\n\t" \
  "v_cvt_pk_bf16_f32 v123, v190, v191\n\t" \
  "v_mfma_f32_16x16x16_bf16 v[200:203], v[42:43], v[184:185], v[4:7]\n\t" \
  "v_cvt_pk_bf16_f32 v146, v144, v145\n\t" \
  "v_cvt_pk_bf16_f32 v155, v154, v155\n\t" \
  "v_mfma_f32_16x16x32_bf16 v[168:171], v[208:211], v[108:111], 0\n\t" \
  "ds_read_b64_tr_b16 v[208:209], v248 offset:43560\n\t" \
  "s_nop 0\n\t" \
  "v_cvt_pk_bf16_f32 v124, v176, v177\n\t" \
  "v_cvt_pk_bf16_f32 v125, v178, v179\n\t" \
  "v_mfma_f32_16x16x16_bf16 v[204:207], v[32:33], v[180:181], v[0:3]\n\t" \
  "ds_read_b64_tr_b16 v[210:211], v248 offset:44584\n\t" \
  "v_cvt_pk_bf16_f32 v126, v200, v201\n\t" \
  "v_cvt_pk_bf16_f32 v127, v202, v203\n\t" \
  "v_mfma_f32_16x16x16_bf16 v[196:199], v[34:35], v[180:181], v[0:3]\n\t" \
  "v_cvt_pk_bf16_f32 v154, v152, v153\n\t" \
  "v_cvt_pk_f16_f32 v228, |v148|, |v149| clamp\n\t" \
  "v_mfma_f32_16x16x32_bf16 v[160:163], v[212:215], v[120:123], v[160:163]\n\t" \
  "s_nop 0\n\t" \
  "v_cvt_pk_bf16_f32 v136, v204, v205\n\t" \
  "v_cvt_pk_bf16_f32 v137, v206, v207\n\t" \
  "v_mfma_f32_16x16x16_bf16 v[188:191], v[44:45], v[184:185], v[4:7]\n\t" \
  "s_nop 0\n\t" \
  "v_cvt_pk_bf16_f32 v138, v196, v197\n\t" \
  "v_cvt_pk_bf16_f32 v139, v198, v199\n\t" \
  "v_mfma_f32_16x16x16_bf16 v[176:179], v[46:47], v[184:185], v[4:7]\n\t" \
  "v_cvt_pknorm_i16_f32 v230, |v150|, |v151|\n\t" \
  "v_cvt_pk_f16_f32 v232, |v156|, |v157| clamp\n\t" \
  "v_mfma_f32_16x16x32_bf16 v[168:171], v[212:215], v[124:127], v[168:171]\n\t" \
  "ds_read_b64_tr_b16 v[212:213], v248 offset:46632\n\t" \
  "v_cvt_pk_bf16_f32 v140, v188, v189\n\t" \
  "v_cvt_pk_bf16_f32 v141, v190, v191\n\t" \
  "v_mfma_f32_16x16x32_bf16 v[160:163], v[216:219], v[136:139], v[160:163]\n\t" \
  "ds_read_b64_tr_b16 v[214:215], v248 offset:47656\n\t" \
  "v_cvt_pk_bf16_f32 v142, v176, v177\n\t" \
  "v_cvt_pk_bf16_f32 v143, v178, v179\n\t" \
  "s_nop 1\n\t" \
  "v_mfma_f32_16x16x32_bf16 v[168:171], v[216:219], v[140:143], v[168:171]\n\t" \
  "ds_read_b64_tr_b16 v[216:217], v248 offset:45608\n\t" \
  "v_cvt_pknorm_i16_f32 v234, |v158|, |v159|\n\t" \
  "v_and_b32_sdwa v229, s56, v228 dst_sel:DWORD dst_unused:UNUSED_PAD src0_sel:DWORD src1_sel:WORD_1\n\t" \
  "s_waitcnt lgkmcnt(3)\n\t" \
  "ds_read_b64_tr_b16 v[218:219], v248 offset:48680\n\t" \
  "s_waitcnt vmcnt(4)\n\t" \
  "s_mov_b64 exec, 3\n\t" \
  "ds_add_rtn_u32 v246, v251, v250 offset:47136\n\t" \
  "s_mov_b64 exec, -1\n\t" \
  "v_mfma_f32_16x16x32_bf16 v[164:167], v[208:211], v[104:107], 0\n\t" \
  "v_and_b32 v228, s56, v228\n\t" \
  "v_lshlrev_b32_sdwa v231, 2, v230 dst_sel:DWORD dst_unused:UNUSED_PAD src0_sel:DWORD src1_sel:BYTE_3\n\t" \
  "v_mfma_f32_16x16x32_bf16 v[172:175], v[208:211], v[108:111], 0\n\t" \
  "ds_read_b64_tr_b16 v[208:209], v248 offset:49192\n\t" \
  "v_lshlrev_b32_sdwa v230, 2, v230 dst_sel:DWORD dst_unused:UNUSED_PAD src0_sel:DWORD src1_sel:BYTE_1\n\t" \
  "v_and_b32_sdwa v233, s56, v232 dst_sel:DWORD dst_unused:UNUSED_PAD src0_sel:DWORD src1_sel:WORD_1\n\t" \
  "s_waitcnt lgkmcnt(4)\n\t" \
  "v_mfma_f32_16x16x32_bf16 v[164:167], v[212:215], v[120:123], v[164:167]\n\t" \
  "ds_read_b64_tr_b16 v[210:211], v248 offset:50216\n\t" \
  "v_and_b32 v232, s56, v232\n\t" \
  "v_lshlrev_b32_sdwa v235, 2, v234 dst_sel:DWORD dst_unused:UNUSED_PAD src0_sel:DWORD src1_sel:BYTE_3\n\t" \
  "v_mfma_f32_16x16x32_bf16 v[172:175], v[212:215], v[124:127], v[172:175]\n\t" \
  "ds_read_b64_tr_b16 v[212:213], v248 offset:52264\n\t" \
  "ds_read_b64_tr_b16 v[214:215], v248 offset:53288\n\t" \
  "v_lshlrev_b32_sdwa v234, 2, v234 dst_sel:DWORD dst_unused:UNUSED_PAD src0_sel:DWORD src1_sel:BYTE_1\n\t" \
  "s_waitcnt lgkmcnt(5)\n\t" \
  "v_mfma_f32_16x16x32_bf16 v[164:167], v[216:219], v[136:139], v[164:167]\n\t" \
  "ds_read_b32 v229, v229 offset:0\n\t" \
  "ds_read_b32 v228, v228 offset:0\n\t" \
  "v_mfma_f32_16x16x32_bf16 v[172:175], v[216:219], v[140:143], v[172:175]\n\t" \
  "ds_read_b64_tr_b16 v[216:217], v248 offset:51240\n\t" \
  "ds_read_b64_tr_b16 v[218:219], v248 offset:54312\n\t" \
  "s_cmp_lg_u32 s38, 4\n\t" \
  "s_cbranch_scc1 .Lcnx_c19\n\t" \
  "s_mov_b32 m0, 163632\n\t" \
  "s_add_u32 s30, s30, s31\n\t" \
  "buffer_load_dword v252, s[8:11], s30 offen lds\n\t" \
  ".Lcnx_c19:\n\t" \
  "s_setprio 3\n\t" \
  "v_cvt_pk_f16_f32 v220, |v162|, |v163| clamp\n\t" \
  "v_cvt_pknorm_i16_f32 v222, |v160|, |v161|\n\t" \
  "s_waitcnt lgkmcnt(6)\n\t" \
  "v_mfma_f32_16x16x32_bf16 v[176:179], v[208:211], v[96:99], 0\n\t" \
  "buffer_store_dwordx4 v[72:75], v250, s[20:23], s15 offen offset:0 sc1\n\t" \
  "ds_read_b32 v231, v231 offset:15376\n\t" \
  "ds_read_b32 v230, v230 offset:15376\n\t" \
  "v_cvt_pk_f16_f32 v224, |v170|, |v171| clamp\n\t" \
  "v_cvt_pk_f16_f32 v226, |v168|, |v169| clamp\n\t" \
  "v_and_b32_sdwa v221, s56, v220 dst_sel:DWORD dst_unused:UNUSED_PAD src0_sel:DWORD src1_sel:WORD_1\n\t" \
  "v_mfma_f32_16x16x32_bf16 v[184:187], v[208:211], v[100:103], 0\n\t" \
  "s_cmp_lg_u32 s38, 5\n\t" \
  "s_cbranch_scc1 .Lcnx_c20\n\t" \
  "s_mov_b32 m0, 163632\n\t" \
  "s_add_u32 s30, s30, s31\n\t" \
  "buffer_load_dword v252, s[8:11], s30 offen lds\n\t" \
  ".Lcnx_c20:\n\t" \
  "ds_read_b32 v233, v233 offset:0\n\t" \
  "v_and_b32 v220, s56, v220\n\t" \
  "v_lshlrev_b32_sdwa v223, 2, v222 dst_sel:DWORD dst_unused:UNUSED_PAD src0_sel:DWORD src1_sel:BYTE_3\n\t" \
  "v_lshlrev_b32_sdwa v222, 2, v222 dst_sel:DWORD dst_unused:UNUSED_PAD src0_sel:DWORD src1_sel:BYTE_1\n\t" \
  "v_mfma_f32_16x16x32_bf16 v[192:195], v[208:211], v[104:107], 0\n\t" \
  "ds_read_b32 v232, v232 offset:0\n\t" \
  "ds_read_b32 v235, v235 offset:15376\n\t" \
  "v_and_b32_sdwa v225, s56, v224 dst_sel:DWORD dst_unused:UNUSED_PAD src0_sel:DWORD src1_sel:WORD_1\n\t" \
  "v_and_b32 v224, s56, v224\n\t" \
  "v_and_b32_sdwa v227, s56, v226 dst_sel:DWORD dst_unused:UNUSED_PAD src0_sel:DWORD src1_sel:WORD_1\n\t" \
  "v_mfma_f32_16x16x32_bf16 v[200:203], v[208:211], v[108:111], 0\n\t" \
  "ds_read_b64_tr_b16 v[208:209], v248 offset:49704\n\t" \
  "ds_read_b64_tr_b16 v[210:211], v248 offset:50728\n\t" \
  "v_and_b32 v226, s56, v226\n\t" \
  "ds_read_b32 v234, v234 offset:15376\n\t" \
  "ds_read_b32 v221, v221 offset:0\n\t" \
  "ds_read_b32 v220, v220 offset:0\n\t" \
  "ds_read_b32 v223, v223 offset:15376\n\t" \
  "ds_read_b32 v222, v222 offset:15376\n\t" \
  "ds_read_b32 v225, v225 offset:0\n\t" \
  "ds_read_b32 v224, v224 offset:0\n\t" \
  "ds_read_b32 v227, v227 offset:0\n\t" \
  "s_waitcnt lgkmcnt(13)\n\t" \
  "v_fma_f32 v149, |v149|, v229, v149 mul:4\n\t" \
  "v_fma_f32 v148, |v148|, v228, v148 mul:4\n\t" \
  "v_fma_mix_f32 v231, |v151|, v231, v231 op_sel:[0,1,0] op_sel_hi:[0,1,1] clamp\n\t" \
  "v_mfma_f32_16x16x32_bf16 v[176:179], v[212:215], v[112:115], v[176:179]\n\t" \
  "buffer_store_dwordx4 v[80:83], v250, s[20:23], s15 offen offset:64 sc1\n\t" \
  "ds_read_b32 v226, v226 offset:0\n\t" \
  "v_fma_mix_f32 v230, |v150|, v230, v230 op_sel:[0,1,0] op_sel_hi:[0,1,1] clamp\n\t" \
  "s_waitcnt lgkmcnt(8)\n\t" \
  "s_setprio 2\n\t" \
  "v_fma_f32 v157, |v157|, v233, v157 mul:4\n\t" \
  "v_fma_f32 v156, |v156|, v232, v156 mul:4\n\t" \
  "v_mfma_f32_16x16x32_bf16 v[184:187], v[212:215], v[116:119], v[184:187]\n\t" \
  "v_fma_mix_f32 v235, |v159|, v235, v235 op_sel:[0,1,0] op_sel_hi:[0,1,1] clamp\n\t" \
  "v_fma_mix_f32 v234, |v158|, v234, v234 op_sel:[0,1,0] op_sel_hi:[0,1,1] clamp\n\t" \
  "v_fma_f32 v151, |v151|, v231, v151 mul:4\n\t" \
  "v_mfma_f32_16x16x32_bf16 v[192:195], v[212:215], v[120:123], v[192:195]\n\t" \
  "v_fma_f32 v150, |v150|, v230, v150 mul:4\n\t" \
  "v_fma_f32 v159, |v159|, v235, v159 mul:4\n\t" \
  "v_fma_f32 v158, |v158|, v234, v158 mul:4\n\t" \
  "v_mfma_f32_16x16x32_bf16 v[200:203], v[212:215], v[124:127], v[200:203]\n\t" \
  "ds_read_b64_tr_b16 v[212:213], v248 offset:52776\n\t" \
  "v_cvt_pk_bf16_f32 v148, v148, v149\n\t" \
  "v_cvt_pk_bf16_f32 v149, v150, v151\n\t" \
  "v_cvt_pk_bf16_f32 v156, v156, v157\n\t" \
  "v_cvt_pk_bf16_f32 v157, v158, v159\n\t" \
  "v_mfma_f32_16x16x32_bf16 v[176:179], v[216:219], v[128:131], v[176:179]\n\t" \
  "ds_read_b64_tr_b16 v[214:215], v248 offset:53800\n\t" \
  "v_cvt_pk_f16_f32 v228, |v164|, |v165| clamp\n\t" \
  "v_cvt_pknorm_i16_f32 v230, |v166|, |v167|\n\t" \
  "v_cvt_pk_f16_f32 v232, |v172|, |v173| clamp\n\t" \
  "v_mfma_f32_16x16x32_bf16 v[184:187], v[216:219], v[132:135], v[184:187]\n\t" \
  "buffer_store_dwordx4 v[88:91], v250, s[20:23], s15 offen offset:128 sc1\n\t" \
  "v_cvt_pk_f16_f32 v234, |v174|, |v175| clamp\n\t" \
  "v_and_b32_sdwa v229, s56, v228 dst_sel:DWORD dst_unused:UNUSED_PAD src0_sel:DWORD src1_sel:WORD_1\n\t" \
  "v_and_b32 v228, s56, v228\n\t" \
  "v_mfma_f32_16x16x32_bf16 v[192:195], v[216:219], v[136:139], v[192:195]\n\t" \
  "ds_read_b32 v229, v229 offset:0\n\t" \
  "v_lshlrev_b32_sdwa v231, 2, v230 dst_sel:DWORD dst_unused:UNUSED_PAD src0_sel:DWORD src1_sel:BYTE_3\n\t" \
  "v_lshlrev_b32_sdwa v230, 2, v230 dst_sel:DWORD dst_unused:UNUSED_PAD src0_sel:DWORD src1_sel:BYTE_1\n\t" \
  "v_and_b32_sdwa v233, s56, v232 dst_sel:DWORD dst_unused:UNUSED_PAD src0_sel:DWORD src1_sel:WORD_1\n\t" \
  "v_mfma_f32_16x16x32_bf16 v[200:203], v[216:219], v[140:143], v[200:203]\n\t" \
  "ds_read_b64_tr_b16 v[216:217], v248 offset:51752\n\t" \
  "v_and_b32 v232, s56, v232\n\t" \
  "v_and_b32_sdwa v235, s56, v234 dst_sel:DWORD dst_unused:UNUSED_PAD src0_sel:DWORD src1_sel:WORD_1\n\t" \
  "v_and_b32 v234, s56, v234\n\t" \
  "s_waitcnt lgkmcnt(8)\n\t" \
  "v_fma_f32 v163, |v163|, v221, v163 mul:4\n\t" \
  "v_mfma_f32_16x16x32_bf16 v[180:183], v[208:211], v[96:99], 0\n\t" \
  "ds_read_b64_tr_b16 v[218:219], v248 offset:54824\n\t" \
  "ds_read_b32 v228, v228 offset:0\n\t" \
  "v_fma_f32 v162, |v162|, v220, v162 mul:4\n\t" \
  "s_setprio 1\n\t" \
  "v_fma_mix_f32 v223, |v161|, v223, v223 op_sel:[0,1,0] op_sel_hi:[0,1,1] clamp\n\t" \
  "v_fma_mix_f32 v222, |v160|, v222, v222 op_sel:[0,1,0] op_sel_hi:[0,1,1] clamp\n\t" \
  "v_mfma_f32_16x16x32_bf16 v[188:191], v[208:211], v[100:103], 0\n\t" \
  "s_waitcnt vmcnt(6)\n\t" \
  "s_mov_b64 exec, 3\n\t" \
  "ds_add_rtn_u32 v246, v251, v250 offset:47144\n\t" \
  "s_mov_b64 exec, -1\n\t" \
  "ds_read_b32 v231, v231 offset:15376\n\t" \
  "ds_read_b32 v230, v230 offset:15376\n\t" \
  "s_waitcnt lgkmcnt(9)\n\t" \
  "v_fma_f32 v171, |v171|, v225, v171 mul:4\n\t" \
  "v_fma_f32 v170, |v170|, v224, v170 mul:4\n\t" \
  "v_fma_f32 v169, |v169|, v227, v169 mul:4\n\t" \
  "v_mfma_f32_16x16x32_bf16 v[196:199], v[208:211], v[104:107], 0\n\t" \
  "ds_read_b32 v233, v233 offset:0\n\t" \
  "v_fma_f32 v168, |v168|, v226, v168 mul:4\n\t" \
  "v_fma_f32 v161, |v161|, v223, v161 mul:4\n\t" \
  "v_fma_f32 v160, |v160|, v222, v160 mul:4\n\t" \
  "v_mfma_f32_16x16x32_bf16 v[204:207], v[208:211], v[108:111], 0\n\t" \
  "s_waitcnt lgkmcnt(3)\n\t" \
  "v_readfirstlane_b32 s4, v246\n\t" \
  "s_cmp_lt_u32 s4, s57\n\t" \
  "s_cbranch_scc1 .Lcnx_w12\n.Lcnx_b12:\n\t" \
  "ds_read_b64_tr_b16 v[208:209], v249 offset:24600\n\t" \
  "v_cvt_pk_bf16_f32 v163, v162, v163\n\t" \
  "v_cvt_pk_bf16_f32 v162, v160, v161\n\t" \
  "v_cvt_pk_bf16_f32 v171, v170, v171\n\t" \
  "v_cvt_pk_bf16_f32 v170, v168, v169\n\t" \
  "v_mfma_f32_16x16x32_bf16 v[180:183], v[212:215], v[112:115], v[180:183]\n\t" \
  "ds_read_b64_tr_b16 v[210:211], v249 offset:26136\n\t" \
  "v_cvt_pk_f16_f32 v220, |v178|, |v179| clamp\n\t" \
  "v_cvt_pknorm_i16_f32 v222, |v176|, |v177|\n\t" \
  "v_cvt_pk_f16_f32 v224, |v186|, |v187| clamp\n\t" \
  "v_mfma_f32_16x16x32_bf16 v[188:191], v[212:215], v[116:119], v[188:191]\n\t" \
  "s_cmp_eq_u32 s37, 0\n\t" \
  "s_cbranch_scc1 .Lcnx_f2\n.Lcnx_g2:\n\t" \
  "ds_read_b32 v232, v232 offset:0\n\t" \
  "v_cvt_pknorm_i16_f32 v226, |v184|, |v185|\n\t" \
  "v_and_b32_sdwa v221, s56, v220 dst_sel:DWORD dst_unused:UNUSED_PAD src0_sel:DWORD src1_sel:WORD_1\n\t" \
  "v_and_b32 v220, s56, v220\n\t" \
  "s_setprio 0\n\t" \
  "v_mfma_f32_16x16x32_bf16 v[196:199], v[212:215], v[120:123], v[196:199]\n\t" \
  "ds_read_b32 v235, v235 offset:0\n\t" \
  "v_lshlrev_b32_sdwa v223, 2, v222 dst_sel:DWORD dst_unused:UNUSED_PAD src0_sel:DWORD src1_sel:BYTE_3\n\t" \
  "v_lshlrev_b32_sdwa v222, 2, v222 dst_sel:DWORD dst_unused:UNUSED_PAD src0_sel:DWORD src1_sel:BYTE_1\n\t" \
  "v_and_b32_sdwa v225, s56, v224 dst_sel:DWORD dst_unused:UNUSED_PAD src0_sel:DWORD src1_sel:WORD_1\n\t" \
  "v_mfma_f32_16x16x32_bf16 v[204:207], v[212:215], v[124:127], v[204:207]\n\t" \
  "ds_read_b64_tr_b16 v[212:213], v249 offset:24608\n\t" \
  "ds_read_b64_tr_b16 v[214:215], v249 offset:26144\n\t" \
  "v_and_b32 v224, s56, v224\n\t" \
  "v_lshlrev_b32_sdwa v227, 2, v226 dst_sel:DWORD dst_unused:UNUSED_PAD src0_sel:DWORD src1_sel:BYTE_3\n\t" \
  "v_lshlrev_b32_sdwa v226, 2, v226 dst_sel:DWORD dst_unused:UNUSED_PAD src0_sel:DWORD src1_sel:BYTE_1\n\t" \
  "ds_read_b32 v234, v234 offset:0\n\t" \
  "ds_read_b32 v221, v221 offset:0\n\t" \
  "ds_read_b32 v220, v220 offset:0\n\t" \
  "ds_read_b32 v223, v223 offset:15376\n\t" \
  "ds_read_b32 v222, v222 offset:15376\n\t" \
  "ds_read_b32 v225, v225 offset:0\n\t" \
  "ds_read_b32 v224, v224 offset:0\n\t" \
  "ds_read_b32 v227, v227 offset:15376\n\t" \
  "s_waitcnt lgkmcnt(15)\n\t" \
  "v_fma_f32 v165, |v165|, v229, v165 mul:4\n\t" \
  "v_mfma_f32_16x16x32_bf16 v[180:183], v[216:219], v[128:131], v[180:183]\n\t" \
  "ds_read_b32 v226, v226 offset:15376\n\t" \
  "v_fma_f32 v164, |v164|, v228, v164 mul:4\n\t" \
  "v_fma_mix_f32 v231, |v167|, v231, v231 op_sel:[0,1,0] op_sel_hi:[0,1,1] clamp\n\t" \
  "v_fma_mix_f32 v230, |v166|, v230, v230 op_sel:[0,1,0] op_sel_hi:[0,1,1] clamp\n\t" \
  "v_mfma_f32_16x16x32_bf16 v[188:191], v[216:219], v[132:135], v[188:191]\n\t" \
  "s_waitcnt lgkmcnt(8)\n\t" \
  "v_fma_f32 v173, |v173|, v233, v173 mul:4\n\t" \
  "v_fma_f32 v172, |v172|, v232, v172 mul:4\n\t" \
  "v_fma_f32 v175, |v175|, v235, v175 mul:4\n\t" \
  "v_mfma_f32_16x16x32_bf16 v[196:199], v[216:219], v[136:139], v[196:199]\n\t" \
  "v_fma_f32 v174, |v174|, v234, v174 mul:4\n\t" \
  "v_fma_f32 v167, |v167|, v231, v167 mul:4\n\t" \
  "v_fma_f32 v166, |v166|, v230, v166 mul:4\n\t" \
  "v_mfma_f32_16x16x32_bf16 v[204:207], v[216:219], v[140:143], v[204:207]\n\t" \
  "ds_read_b64_tr_b16 v[216:217], v249 offset:24664\n\t" \
  "v_cvt_pk_bf16_f32 v164, v164, v165\n\t" \
  "v_cvt_pk_bf16_f32 v165, v166, v167\n\t" \
  "v_cvt_pk_bf16_f32 v172, v172, v173\n\t" \
  "v_cvt_pk_bf16_f32 v173, v174, v175\n\t" \
  "s_setprio 0\n\t" \
  "v_cvt_pk_f16_f32 v228, |v194|, |v195| clamp\n\t" \
  "v_cvt_pknorm_i16_f32 v230, |v192|, |v193|\n\t" \
  "v_mfma_f32_16x16x32_bf16 v[0:3], v[242:245], v[236:239], 0\n\t" \
  "ds_read_b64_tr_b16 v[218:219], v249 offset:26200\n\t" \
  "v_cvt_pk_f16_f32 v232, |v202|, |v203| clamp\n\t" \
  "v_cvt_pk_f16_f32 v234, |v200|, |v201| clamp\n\t" \
  "v_mfma_f32_16x16x32_bf16 v[4:7], v[240:243], v[236:239], 0\n\t" \
  "buffer_load_dwordx4 v[236:239], v250, s[8:11], s28 offen offset:64\n\t" \
  "s_cmp_lg_u32 s58, 0\n\t" \
  "s_cbranch_scc1 .Lcnx_c21\n\t" \
  "s_sub_u32 s24, s24, s64\n\t" \
  "s_add_u32 m0, s25, 0\n\t" \
  "s_add_u32 s24, s24, s26\n\t" \
  "buffer_load_dwordx4 v254, s[8:11], s24 offen lds\n\t" \
  "s_cmp_lg_u32 s38, 6\n\t" \
  "s_cbranch_scc1 .Lcnx_c21\n\t" \
  "s_mov_b32 m0, 163632\n\t" \
  "s_add_u32 s30, s30, s31\n\t" \
  "buffer_load_dword v252, s[8:11], s30 offen lds\n\t" \
  ".Lcnx_c21:\n\t" \
  "v_and_b32_sdwa v229, s56, v228 dst_sel:DWORD dst_unused:UNUSED_PAD src0_sel:DWORD src1_sel:WORD_1\n\t" \
  "v_and_b32 v228, s56, v228\n\t" \
  "v_mfma_f32_16x16x32_bf16 v[0:3], v[208:211], v[146:149], v[0:3]\n\t" \
  "ds_read_b32 v229, v229 offset:0\n\t" \
  "v_lshlrev_b32_sdwa v231, 2, v230 dst_sel:DWORD dst_unused:UNUSED_PAD src0_sel:DWORD src1_sel:BYTE_3\n\t" \
  "v_lshlrev_b32_sdwa v230, 2, v230 dst_sel:DWORD dst_unused:UNUSED_PAD src0_sel:DWORD src1_sel:BYTE_1\n\t" \
  "v_mfma_f32_16x16x32_bf16 v[24:27], v[208:211], v[154:157], 0\n\t" \
  "ds_read_b32 v228, v228 offset:0\n\t" \
  "v_and_b32_sdwa v233, s56, v232 dst_sel:DWORD dst_unused:UNUSED_PAD src0_sel:DWORD src1_sel:WORD_1\n\t" \
  "v_and_b32 v232, s56, v232\n\t" \
  "v_mfma_f32_16x16x32_bf16 v[48:51], v[208:211], v[162:165], 0\n\t" \
  "ds_read_b32 v231, v231 offset:15376\n\t" \
  "v_and_b32_sdwa v235, s56, v234 dst_sel:DWORD dst_unused:UNUSED_PAD src0_sel:DWORD src1_sel:WORD_1\n\t" \
  "v_and_b32 v234, s56, v234\n\t" \
  "v_mfma_f32_16x16x32_bf16 v[72:75], v[208:211], v[170:173], 0\n\t" \
  "ds_read_b64_tr_b16 v[208:209], v249 offset:24672\n\t" \
  "ds_read_b64_tr_b16 v[210:211], v249 offset:26208\n\t" \
  "s_waitcnt lgkmcnt(11)\n\t" \
  "v_fma_f32 v179, |v179|, v221, v179 mul:4\n\t" \
  "v_fma_f32 v178, |v178|, v220, v178 mul:4\n\t" \
  "v_mfma_f32_16x16x32_bf16 v[4:7], v[212:215], v[146:149], v[4:7]\n\t" \
  "ds_read_b32 v230, v230 offset:15376\n\t" \
  "v_fma_mix_f32 v223, |v177|, v223, v223 op_sel:[0,1,0] op_sel_hi:[0,1,1] clamp\n\t" \
  "v_fma_mix_f32 v222, |v176|, v222, v222 op_sel:[0,1,0] op_sel_hi:[0,1,1] clamp\n\t" \
  "v_mfma_f32_16x16x32_bf16 v[28:31], v[212:215], v[154:157], 0\n\t" \
  "ds_read_b32 v233, v233 offset:0\n\t" \
  "s_waitcnt lgkmcnt(9)\n\t" \
  "v_fma_f32 v187, |v187|, v225, v187 mul:4\n\t" \
  "v_fma_f32 v186, |v186|, v224, v186 mul:4\n\t" \
  "v_mfma_f32_16x16x32_bf16 v[52:55], v[212:215], v[162:165], 0\n\t" \
  "ds_read_b32 v232, v232 offset:0\n\t" \
  "v_fma_mix_f32 v227, |v185|, v227, v227 op_sel:[0,1,0] op_sel_hi:[0,1,1] clamp\n\t" \
  "v_fma_mix_f32 v226, |v184|, v226, v226 op_sel:[0,1,0] op_sel_hi:[0,1,1] clamp\n\t" \
  "v_fma_f32 v177, |v177|, v223, v177 mul:4\n\t" \
  "v_mfma_f32_16x16x32_bf16 v[76:79], v[212:215], v[170:173], 0\n\t" \
  "ds_read_b64_tr_b16 v[212:213], v249 offset:24728\n\t" \
  "v_fma_f32 v176, |v176|, v222, v176 mul:4\n\t" \
  "v_fma_f32 v185, |v185|, v227, v185 mul:4\n\t" \
  "s_waitcnt lgkmcnt(9)\n\t" \
  "v_mfma_f32_16x16x32_bf16 v[8:11], v[216:219], v[146:149], 0\n\t" \
  "ds_read_b64_tr_b16 v[214:215], v249 offset:26264\n\t" \
  "v_fma_f32 v184, |v184|, v226, v184 mul:4\n\t" \
  "v_cvt_pk_bf16_f32 v179, v178, v179\n\t" \
  "v_mfma_f32_16x16x32_bf16 v[32:35], v[216:219], v[154:157], 0\n\t" \
  "ds_read_b32 v235, v235 offset:0\n\t" \
  "v_cvt_pk_bf16_f32 v178, v176, v177\n\t" \
  "v_cvt_pk_bf16_f32 v187, v186, v187\n\t" \
  "v_mfma_f32_16x16x32_bf16 v[56:59], v[216:219], v[162:165], 0\n\t" \
  "ds_read_b32 v234, v234 offset:0\n\t" \
  "v_cvt_pk_bf16_f32 v186, v184, v185\n\t" \
  "v_cvt_pk_f16_f32 v220, |v180|, |v181| clamp\n\t" \
  "s_setprio 1\n\t" \
  "v_mfma_f32_16x16x32_bf16 v[80:83], v[216:219], v[170:173], 0\n\t" \
  "ds_read_b64_tr_b16 v[216:217], v249 offset:24736\n\t" \
  "v_cvt_pknorm_i16_f32 v222, |v182|, |v183|\n\t" \
  "v_cvt_pk_f16_f32 v224, |v188|, |v189| clamp\n\t" \
  "s_waitcnt lgkmcnt(8)\n\t" \
  "v_mfma_f32_16x16x32_bf16 v[12:15], v[208:211], v[146:149], 0\n\t" \
  "ds_read_b64_tr_b16 v[218:219], v249 offset:26272\n\t" \
  "v_cvt_pknorm_i16_f32 v226, |v190|, |v191|\n\t" \
  "v_and_b32_sdwa v221, s56, v220 dst_sel:DWORD dst_unused:UNUSED_PAD src0_sel:DWORD src1_sel:WORD_1\n\t" \
  "v_mfma_f32_16x16x32_bf16 v[36:39], v[208:211], v[154:157], 0\n\t" \
  "v_and_b32 v220, s56, v220\n\t" \
  "v_lshlrev_b32_sdwa v223, 2, v222 dst_sel:DWORD dst_unused:UNUSED_PAD src0_sel:DWORD src1_sel:BYTE_3\n\t" \
  "v_mfma_f32_16x16x32_bf16 v[60:63], v[208:211], v[162:165], 0\n\t" \
  "ds_read_b32 v221, v221 offset:0\n\t" \
  "v_lshlrev_b32_sdwa v222, 2, v222 dst_sel:DWORD dst_unused:UNUSED_PAD src0_sel:DWORD src1_sel:BYTE_1\n\t" \
  "v_and_b32_sdwa v225, s56, v224 dst_sel:DWORD dst_unused:UNUSED_PAD src0_sel:DWORD src1_sel:WORD_1\n\t" \
  "v_and_b32 v224, s56, v224\n\t" \
  "v_mfma_f32_16x16x32_bf16 v[84:87], v[208:211], v[170:173], 0\n\t" \
  "ds_read_b64_tr_b16 v[208:209], v248 offset:36904\n\t" \
  "ds_read_b64_tr_b16 v[210:211], v248 offset:37928\n\t" \
  "v_lshlrev_b32_sdwa v227, 2, v226 dst_sel:DWORD dst_unused:UNUSED_PAD src0_sel:DWORD src1_sel:BYTE_3\n\t" \
  "v_lshlrev_b32_sdwa v226, 2, v226 dst_sel:DWORD dst_unused:UNUSED_PAD src0_sel:DWORD src1_sel:BYTE_1\n\t" \
  "s_waitcnt lgkmcnt(7)\n\t" \
  "v_mfma_f32_16x16x32_bf16 v[16:19], v[212:215], v[146:149], 0\n\t" \
  "ds_read_b32 v220, v220 offset:0\n\t" \
  "ds_read_b32 v223, v223 offset:15376\n\t" \
  "v_fma_f32 v195, |v195|, v229, v195 mul:4\n\t" \
  "v_fma_f32 v194, |v194|, v228, v194 mul:4\n\t" \
  "v_mfma_f32_16x16x32_bf16 v[40:43], v[212:215], v[154:157], 0\n\t" \
  "ds_read_b32 v222, v222 offset:15376\n\t" \
  "v_fma_mix_f32 v231, |v193|, v231, v231 op_sel:[0,1,0] op_sel_hi:[0,1,1] clamp\n\t" \
  "v_fma_mix_f32 v230, |v192|, v230, v230 op_sel:[0,1,0] op_sel_hi:[0,1,1] clamp\n\t" \
  "v_mfma_f32_16x16x32_bf16 v[64:67], v[212:215], v[162:165], 0\n\t" \
  "ds_read_b32 v225, v225 offset:0\n\t" \
  "s_waitcnt lgkmcnt(9)\n\t" \
  "v_fma_f32 v203, |v203|, v233, v203 mul:4\n\t" \
  "v_fma_f32 v202, |v202|, v232, v202 mul:4\n\t" \
  "v_mfma_f32_16x16x32_bf16 v[88:91], v[212:215], v[170:173], 0\n\t" \
  "ds_read_b64_tr_b16 v[212:213], v248 offset:39976\n\t" \
  "v_fma_f32 v201, |v201|, v235, v201 mul:4\n\t" \
  "v_fma_f32 v200, |v200|, v234, v200 mul:4\n\t" \
  "s_waitcnt lgkmcnt(8)\n\t" \
  "v_mfma_f32_16x16x32_bf16 v[20:23], v[216:219], v[146:149], 0\n\t" \
  "ds_read_b64_tr_b16 v[214:215], v248 offset:41000\n\t" \
  "v_fma_f32 v193, |v193|, v231, v193 mul:4\n\t" \
  "v_fma_f32 v192, |v192|, v230, v192 mul:4\n\t" \
  "v_mfma_f32_16x16x32_bf16 v[44:47], v[216:219], v[154:157], 0\n\t" \
  "ds_read_b32 v224, v224 offset:0\n\t" \
  "v_cvt_pk_bf16_f32 v195, v194, v195\n\t" \
  "v_cvt_pk_bf16_f32 v194, v192, v193\n\t" \
  "v_mfma_f32_16x16x32_bf16 v[68:71], v[216:219], v[162:165], 0\n\t" \
  "ds_read_b32 v227, v227 offset:15376\n\t" \
  "v_cvt_pk_bf16_f32 v203, v202, v203\n\t" \
  "v_cvt_pk_bf16_f32 v202, v200, v201\n\t" \
  "v_cvt_pk_f16_f32 v228, |v196|, |v197| clamp\n\t" \
  "v_mfma_f32_16x16x32_bf16 v[92:95], v[216:219], v[170:173], 0\n\t" \
  "ds_read_b64_tr_b16 v[216:217], v248 offset:38952\n\t" \
  "v_cvt_pknorm_i16_f32 v230, |v198|, |v199|\n\t" \
  "v_cvt_pk_f16_f32 v232, |v204|, |v205| clamp\n\t" \
  "s_setprio 2\n\t" \
  "s_waitcnt lgkmcnt(9)\n\t" \
  "v_mfma_f32_16x16x32_bf16 v[144:147], v[208:211], v[96:99], 0\n\t" \
  "ds_read_b64_tr_b16 v[218:219], v248 offset:42024\n\t" \
  "v_cvt_pk_f16_f32 v234, |v206|, |v207| clamp\n\t" \
  "v_and_b32_sdwa v229, s56, v228 dst_sel:DWORD dst_unused:UNUSED_PAD src0_sel:DWORD src1_sel:WORD_1\n\t" \
  "v_mfma_f32_16x16x32_bf16 v[152:155], v[208:211], v[100:103], 0\n\t" \
  "ds_read_b32 v226, v226 offset:15376\n\t" \
  "v_and_b32 v228, s56, v228\n\t" \
  "v_lshlrev_b32_sdwa v231, 2, v230 dst_sel:DWORD dst_unused:UNUSED_PAD src0_sel:DWORD src1_sel:BYTE_3\n\t" \
  "v_mfma_f32_16x16x32_bf16 v[160:163], v[208:211], v[104:107], 0\n\t" \
  "ds_read_b32 v229, v229 offset:0\n\t" \
  "v_lshlrev_b32_sdwa v230, 2, v230 dst_sel:DWORD dst_unused:UNUSED_PAD src0_sel:DWORD src1_sel:BYTE_1\n\t" \
  "v_and_b32_sdwa v233, s56, v232 dst_sel:DWORD dst_unused:UNUSED_PAD src0_sel:DWORD src1_sel:WORD_1\n\t" \
  "v_mfma_f32_16x16x32_bf16 v[168:171], v[208:211], v[108:111], 0\n\t" \
  "ds_read_b64_tr_b16 v[208:209], v248 offset:37416\n\t" \
  "v_and_b32 v232, s56, v232\n\t" \
  "v_and_b32_sdwa v235, s56, v234 dst_sel:DWORD dst_unused:UNUSED_PAD src0_sel:DWORD src1_sel:WORD_1\n\t" \
  "s_waitcnt lgkmcnt(7)\n\t" \
  "v_mfma_f32_16x16x32_bf16 v[144:147], v[212:215], v[112:115], v[144:147]\n\t" \
  "ds_read_b64_tr_b16 v[210:211], v248 offset:38440\n\t" \
  "ds_read_b32 v228, v228 offset:0\n\t" \
  "v_and_b32 v234, s56, v234\n\t" \
  "v_fma_f32 v181, |v181|, v221, v181 mul:4\n\t" \
  "v_mfma_f32_16x16x32_bf16 v[152:155], v[212:215], v[116:119], v[152:155]\n\t" \
  "ds_read_b32 v231, v231 offset:15376\n\t" \
  "ds_read_b32 v230, v230 offset:15376\n\t" \
  "v_fma_f32 v180, |v180|, v220, v180 mul:4\n\t" \
  "v_fma_mix_f32 v223, |v183|, v223, v223 op_sel:[0,1,0] op_sel_hi:[0,1,1] clamp\n\t" \
  "v_mfma_f32_16x16x32_bf16 v[160:163], v[212:215], v[120:123], v[160:163]\n\t" \
  "ds_read_b32 v233, v233 offset:0\n\t" \
  "v_fma_mix_f32 v222, |v182|, v222, v222 op_sel:[0,1,0] op_sel_hi:[0,1,1] clamp\n\t" \
  "s_waitcnt lgkmcnt(7)\n\t" \
  "v_fma_f32 v189, |v189|, v225, v189 mul:4\n\t" \
  "v_mfma_f32_16x16x32_bf16 v[168:171], v[212:215], v[124:127], v[168:171]\n\t" \
  "ds_read_b64_tr_b16 v[212:213], v248 offset:40488\n\t" \
  "v_fma_f32 v188, |v188|, v224, v188 mul:4\n\t" \
  "v_fma_mix_f32 v227, |v191|, v227, v227 op_sel:[0,1,0] op_sel_hi:[0,1,1] clamp\n\t" \
  "v_fma_mix_f32 v226, |v190|, v226, v226 op_sel:[0,1,0] op_sel_hi:[0,1,1] clamp\n\t" \
  "v_mfma_f32_16x16x32_bf16 v[144:147], v[216:219], v[128:131], v[144:147]\n\t" \
  "ds_read_b64_tr_b16 v[214:215], v248 offset:41512\n\t" \
  "v_fma_f32 v183, |v183|, v223, v183 mul:4\n\t" \
  "v_fma_f32 v182, |v182|, v222, v182 mul:4\n\t" \
  "v_mfma_f32_16x16x32_bf16 v[152:155], v[216:219], v[132:135], v[152:155]\n\t" \
  "ds_read_b32 v232, v232 offset:0\n\t" \
  "v_fma_f32 v191, |v191|, v227, v191 mul:4\n\t" \
  "v_fma_f32 v190, |v190|, v226, v190 mul:4\n\t" \
  "v_mfma_f32_16x16x32_bf16 v[160:163], v[216:219], v[136:139], v[160:163]\n\t" \
  "ds_read_b32 v235, v235 offset:0\n\t" \
  "v_cvt_pk_bf16_f32 v180, v180, v181\n\t" \
  "v_cvt_pk_bf16_f32 v181, v182, v183\n\t" \
  "v_mfma_f32_16x16x32_bf16 v[168:171], v[216:219], v[140:143], v[168:171]\n\t" \
  "ds_read_b64_tr_b16 v[216:217], v248 offset:39464\n\t" \
  "v_cvt_pk_bf16_f32 v188, v188, v189\n\t" \
  "v_cvt_pk_bf16_f32 v189, v190, v191\n\t" \
  "s_waitcnt lgkmcnt(9)\n\t" \
  "v_mfma_f32_16x16x32_bf16 v[148:151], v[208:211], v[96:99], 0\n\t" \
  "ds_read_b64_tr_b16 v[218:219], v248 offset:42536\n\t" \
  "v_cvt_pk_f16_f32 v220, |v146|, |v147| clamp\n\t" \
  "v_cvt_pknorm_i16_f32 v222, |v144|, |v145|\n\t" \
  "s_setprio 3\n\t" \
  "v_mfma_f32_16x16x32_bf16 v[156:159], v[208:211], v[100:103], 0\n\t" \
  "s_waitcnt vmcnt(1)\n\t" \
  "s_cmp_lg_u32 s58, 1\n\t" \
  "s_cbranch_scc1 .Lcnx_c9\n\t" \
  "s_waitcnt vmcnt(0)\n\t" \
  ".Lcnx_c9:\n\t" \
  "s_mov_b64 exec, 3\n\t" \
  "ds_add_rtn_u32 v246, v251, v250 offset:47152\n\t" \
  "s_mov_b64 exec, -1\n\t" \
  "ds_read_b32 v234, v234 offset:0\n\t" \
  "v_cvt_pk_f16_f32 v224, |v154|, |v155| clamp\n\t" \
  "v_cvt_pknorm_i16_f32 v226, |v152|, |v153|\n\t" \
  "v_mfma_f32_16x16x32_bf16 v[164:167], v[208:211], v[104:107], 0\n\t" \
  "v_and_b32_sdwa v221, s56, v220 dst_sel:DWORD dst_unused:UNUSED_PAD src0_sel:DWORD src1_sel:WORD_1\n\t" \
  "v_and_b32 v220, s56, v220\n\t" \
  "v_mfma_f32_16x16x32_bf16 v[172:175], v[208:211], v[108:111], 0\n\t" \
  "s_waitcnt lgkmcnt(1)\n\t" \
  "v_readfirstlane_b32 s4, v246\n\t" \
  "s_cmp_lt_u32 s4, s57\n\t" \
  "s_cbranch_scc1 .Lcnx_w24\n.Lcnx_b24:\n\t" \
  "ds_read_b64_tr_b16 v[208:209], v249 offset:30752\n\t" \
  "v_lshlrev_b32_sdwa v223, 2, v222 dst_sel:DWORD dst_unused:UNUSED_PAD src0_sel:DWORD src1_sel:BYTE_3\n\t" \
  "v_lshlrev_b32_sdwa v222, 2, v222 dst_sel:DWORD dst_unused:UNUSED_PAD src0_sel:DWORD src1_sel:BYTE_1\n\t" \
  "v_and_b32_sdwa v225, s56, v224 dst_sel:DWORD dst_unused:UNUSED_PAD src0_sel:DWORD src1_sel:WORD_1\n\t" \
  "v_mfma_f32_16x16x32_bf16 v[148:151], v[212:215], v[112:115], v[148:151]\n\t" \
  "ds_read_b64_tr_b16 v[210:211], v249 offset:32288\n\t" \
  "v_and_b32 v224, s56, v224\n\t" \
  "v_lshlrev_b32_sdwa v227, 2, v226 dst_sel:DWORD dst_unused:UNUSED_PAD src0_sel:DWORD src1_sel:BYTE_3\n\t" \
  "v_mfma_f32_16x16x32_bf16 v[156:159], v[212:215], v[116:119], v[156:159]\n\t" \
  "s_cmp_eq_u32 s37, 0\n\t" \
  "s_cbranch_scc1 .Lcnx_f3\n.Lcnx_g3:\n\t" \
  "ds_read_b32 v221, v221 offset:0\n\t" \
  "ds_read_b32 v220, v220 offset:0\n\t" \
  "v_lshlrev_b32_sdwa v226, 2, v226 dst_sel:DWORD dst_unused:UNUSED_PAD src0_sel:DWORD src1_sel:BYTE_1\n\t" \
  "v_fma_f32 v197, |v197|, v229, v197 mul:4\n\t" \
  "v_mfma_f32_16x16x32_bf16 v[164:167], v[212:215], v[120:123], v[164:167]\n\t" \
  "ds_read_b32 v223, v223 offset:15376\n\t" \
  "ds_read_b32 v222, v222 offset:15376\n\t" \
  "v_fma_f32 v196, |v196|, v228, v196 mul:4\n\t" \
  "v_fma_mix_f32 v231, |v199|, v231, v231 op_sel:[0,1,0] op_sel_hi:[0,1,1] clamp\n\t" \
  "v_mfma_f32_16x16x32_bf16 v[172:175], v[212:215], v[124:127], v[172:175]\n\t" \
  "ds_read_b64_tr_b16 v[212:213], v249 offset:30760\n\t" \
  "ds_read_b64_tr_b16 v[214:215], v249 offset:32296\n\t" \
  "v_fma_mix_f32 v230, |v198|, v230, v230 op_sel:[0,1,0] op_sel_hi:[0,1,1] clamp\n\t" \
  "s_waitcnt lgkmcnt(8)\n\t" \
  "v_fma_f32 v205, |v205|, v233, v205 mul:4\n\t" \
  "v_mfma_f32_16x16x32_bf16 v[148:151], v[216:219], v[128:131], v[148:151]\n\t" \
  "ds_read_b32 v225, v225 offset:0\n\t" \
  "v_fma_f32 v204, |v204|, v232, v204 mul:4\n\t" \
  "v_fma_f32 v207, |v207|, v235, v207 mul:4\n\t" \
  "v_mfma_f32_16x16x32_bf16 v[156:159], v[216:219], v[132:135], v[156:159]\n\t" \
  "ds_read_b32 v224, v224 offset:0\n\t" \
  "v_fma_f32 v206, |v206|, v234, v206 mul:4\n\t" \
  "v_fma_f32 v199, |v199|, v231, v199 mul:4\n\t" \
  "v_mfma_f32_16x16x32_bf16 v[164:167], v[216:219], v[136:139], v[164:167]\n\t" \
  "ds_read_b32 v227, v227 offset:15376\n\t" \
  "v_fma_f32 v198, |v198|, v230, v198 mul:4\n\t" \
  "v_cvt_pk_bf16_f32 v196, v196, v197\n\t" \
  "v_mfma_f32_16x16x32_bf16 v[172:175], v[216:219], v[140:143], v[172:175]\n\t" \
  "ds_read_b64_tr_b16 v[216:217], v249 offset:30816\n\t" \
  "v_cvt_pk_bf16_f32 v197, v198, v199\n\t" \
  "v_cvt_pk_bf16_f32 v204, v204, v205\n\t" \
  "v_cvt_pk_bf16_f32 v205, v206, v207\n\t" \
  "s_setprio 0\n\t" \
  "v_cvt_pk_f16_f32 v228, |v162|, |v163| clamp\n\t" \
  "v_cvt_pknorm_i16_f32 v230, |v160|, |v161|\n\t" \
  "v_mfma_f32_16x16x32_bf16 v[8:11], v[242:245], v[236:239], v[8:11]\n\t" \
  "ds_read_b64_tr_b16 v[218:219], v249 offset:32352\n\t" \
  "v_cvt_pk_f16_f32 v232, |v170|, |v171| clamp\n\t" \
  "v_cvt_pk_f16_f32 v234, |v168|, |v169| clamp\n\t" \
  "v_mfma_f32_16x16x32_bf16 v[12:15], v[240:243], v[236:239], v[12:15]\n\t" \
  "buffer_load_dwordx4 v[236:239], v250, s[8:11], s28 offen offset:128\n\t" \
  "s_cmp_lg_u32 s58, 0\n\t" \
  "s_cbranch_scc1 .Lcnx_c22\n\t" \
  "s_add_u32 m0, s25, 8200\n\t" \
  "s_add_u32 s24, s24, s26\n\t" \
  "buffer_load_dwordx4 v254, s[8:11], s24 offen lds\n\t" \
  "s_cmp_lg_u32 s38, 7\n\t" \
  "s_cbranch_scc1 .Lcnx_c22\n\t" \
  "s_mov_b32 m0, 163632\n\t" \
  "s_add_u32 s30, s30, s31\n\t" \
  "buffer_load_dword v252, s[8:11], s30 offen lds\n\t" \
  ".Lcnx_c22:\n\t" \
  "ds_read_b32 v226, v226 offset:15376\n\t" \
  "v_and_b32_sdwa v229, s56, v228 dst_sel:DWORD dst_unused:UNUSED_PAD src0_sel:DWORD src1_sel:WORD_1\n\t" \
  "v_and_b32 v228, s56, v228\n\t" \
  "s_waitcnt lgkmcnt(12)\n\t" \
  "v_mfma_f32_16x16x32_bf16 v[0:3], v[208:211], v[178:181], v[0:3]\n\t" \
  "ds_read_b32 v229, v229 offset:0\n\t" \
  "v_lshlrev_b32_sdwa v231, 2, v230 dst_sel:DWORD dst_unused:UNUSED_PAD src0_sel:DWORD src1_sel:BYTE_3\n\t" \
  "v_lshlrev_b32_sdwa v230, 2, v230 dst_sel:DWORD dst_unused:UNUSED_PAD src0_sel:DWORD src1_sel:BYTE_1\n\t" \
  "v_mfma_f32_16x16x32_bf16 v[24:27], v[208:211], v[186:189], v[24:27]\n\t" \
  "ds_read_b32 v228, v228 offset:0\n\t" \
  "v_and_b32_sdwa v233, s56, v232 dst_sel:DWORD dst_unused:UNUSED_PAD src0_sel:DWORD src1_sel:WORD_1\n\t" \
  "v_and_b32 v232, s56, v232\n\t" \
  "v_mfma_f32_16x16x32_bf16 v[48:51], v[208:211], v[194:197], v[48:51]\n\t" \
  "ds_read_b32 v231, v231 offset:15376\n\t" \
  "v_and_b32_sdwa v235, s56, v234 dst_sel:DWORD dst_unused:UNUSED_PAD src0_sel:DWORD src1_sel:WORD_1\n\t" \
  "v_and_b32 v234, s56, v234\n\t" \
  "v_mfma_f32_16x16x32_bf16 v[72:75], v[208:211], v[202:205], v[72:75]\n\t" \
  "ds_read_b64_tr_b16 v[208:209], v249 offset:30824\n\t" \
  "ds_read_b64_tr_b16 v[210:211], v249 offset:32360\n\t" \
  "s_waitcnt lgkmcnt(13)\n\t" \
  "v_fma_f32 v147, |v147|, v221, v147 mul:4\n\t" \
  "v_fma_f32 v146, |v146|, v220, v146 mul:4\n\t" \
  "s_waitcnt lgkmcnt(11)\n\t" \
  "v_mfma_f32_16x16x32_bf16 v[4:7], v[212:215], v[178:181], v[4:7]\n\t" \
  "ds_read_b32 v230, v230 offset:15376\n\t" \
  "v_fma_mix_f32 v223, |v145|, v223, v223 op_sel:[0,1,0] op_sel_hi:[0,1,1] clamp\n\t" \
  "v_fma_mix_f32 v222, |v144|, v222, v222 op_sel:[0,1,0] op_sel_hi:[0,1,1] clamp\n\t" \
  "v_mfma_f32_16x16x32_bf16 v[28:31], v[212:215], v[186:189], v[28:31]\n\t" \
  "ds_read_b32 v233, v233 offset:0\n\t" \
  "s_waitcnt lgkmcnt(7)\n\t" \
  "v_fma_f32 v155, |v155|, v225, v155 mul:4\n\t" \
  "v_fma_f32 v154, |v154|, v224, v154 mul:4\n\t" \
  "v_mfma_f32_16x16x32_bf16 v[52:55], v[212:215], v[194:197], v[52:55]\n\t" \
  "ds_read_b32 v232, v232 offset:0\n\t" \
  "v_fma_mix_f32 v227, |v153|, v227, v227 op_sel:[0,1,0] op_sel_hi:[0,1,1] clamp\n\t" \
  "v_fma_mix_f32 v226, |v152|, v226, v226 op_sel:[0,1,0] op_sel_hi:[0,1,1] clamp\n\t" \
  "v_fma_f32 v145, |v145|, v223, v145 mul:4\n\t" \
  "v_mfma_f32_16x16x32_bf16 v[76:79], v[212:215], v[202:205], v[76:79]\n\t" \
  "ds_read_b64_tr_b16 v[212:213], v249 offset:30880\n\t" \
  "v_fma_f32 v144, |v144|, v222, v144 mul:4\n\t" \
  "v_fma_f32 v153, |v153|, v227, v153 mul:4\n\t" \
  "v_mfma_f32_16x16x32_bf16 v[8:11], v[216:219], v[178:181], v[8:11]\n\t" \
  "ds_read_b64_tr_b16 v[214:215], v249 offset:32416\n\t" \
  "v_fma_f32 v152, |v152|, v226, v152 mul:4\n\t" \
  "v_cvt_pk_bf16_f32 v147, v146, v147\n\t" \
  "v_mfma_f32_16x16x32_bf16 v[32:35], v[216:219], v[186:189], v[32:35]\n\t" \
  "ds_read_b32 v235, v235 offset:0\n\t" \
  "v_cvt_pk_bf16_f32 v146, v144, v145\n\t" \
  "v_cvt_pk_bf16_f32 v155, v154, v155\n\t" \
  "v_mfma_f32_16x16x32_bf16 v[56:59], v[216:219], v[194:197], v[56:59]\n\t" \
  "ds_read_b32 v234, v234 offset:0\n\t" \
  "v_cvt_pk_bf16_f32 v154, v152, v153\n\t" \
  "s_setprio 1\n\t" \
  "v_cvt_pk_f16_f32 v220, |v148|, |v149| clamp\n\t" \
  "v_mfma_f32_16x16x32_bf16 v[80:83], v[216:219], v[202:205], v[80:83]\n\t" \
  "ds_read_b64_tr_b16 v[216:217], v249 offset:30888\n\t" \
  "v_cvt_pknorm_i16_f32 v222, |v150|, |v151|\n\t" \
  "v_cvt_pk_f16_f32 v224, |v156|, |v157| clamp\n\t" \
  "s_waitcnt lgkmcnt(8)\n\t" \
  "v_mfma_f32_16x16x32_bf16 v[12:15], v[208:211], v[178:181], v[12:15]\n\t" \
  "ds_read_b64_tr_b16 v[218:219], v249 offset:32424\n\t" \
  "v_cvt_pknorm_i16_f32 v226, |v158|, |v159|\n\t" \
  "v_and_b32_sdwa v221, s56, v220 dst_sel:DWORD dst_unused:UNUSED_PAD src0_sel:DWORD src1_sel:WORD_1\n\t" \
  "v_mfma_f32_16x16x32_bf16 v[36:39], v[208:211], v[186:189], v[36:39]\n\t" \
  "v_and_b32 v220, s56, v220\n\t" \
  "v_lshlrev_b32_sdwa v223, 2, v222 dst_sel:DWORD dst_unused:UNUSED_PAD src0_sel:DWORD src1_sel:BYTE_3\n\t" \
  "v_mfma_f32_16x16x32_bf16 v[60:63], v[208:211], v[194:197], v[60:63]\n\t" \
  "ds_read_b32 v221, v221 offset:0\n\t" \
  "v_lshlrev_b32_sdwa v222, 2, v222 dst_sel:DWORD dst_unused:UNUSED_PAD src0_sel:DWORD src1_sel:BYTE_1\n\t" \
  "v_and_b32_sdwa v225, s56, v224 dst_sel:DWORD dst_unused:UNUSED_PAD src0_sel:DWORD src1_sel:WORD_1\n\t" \
  "v_and_b32 v224, s56, v224\n\t" \
  "v_mfma_f32_16x16x32_bf16 v[84:87], v[208:211], v[202:205], v[84:87]\n\t" \
  "ds_read_b64_tr_b16 v[208:209], v248 offset:0\n\t" \
  "ds_read_b64_tr_b16 v[210:211], v248 offset:1024\n\t" \
  "v_lshlrev_b32_sdwa v227, 2, v226 dst_sel:DWORD dst_unused:UNUSED_PAD src0_sel:DWORD src1_sel:BYTE_3\n\t" \
  "v_lshlrev_b32_sdwa v226, 2, v226 dst_sel:DWORD dst_unused:UNUSED_PAD src0_sel:DWORD src1_sel:BYTE_1\n\t" \
  "s_waitcnt lgkmcnt(7)\n\t" \
  "v_mfma_f32_16x16x32_bf16 v[16:19], v[212:215], v[178:181], v[16:19]\n\t" \
  "ds_read_b32 v220, v220 offset:0\n\t" \
  "ds_read_b32 v223, v223 offset:15376\n\t" \
  "v_fma_f32 v163, |v163|, v229, v163 mul:4\n\t" \
  "v_fma_f32 v162, |v162|, v228, v162 mul:4\n\t" \
  "v_mfma_f32_16x16x32_bf16 v[40:43], v[212:215], v[186:189], v[40:43]\n\t" \
  "ds_read_b32 v222, v222 offset:15376\n\t" \
  "v_fma_mix_f32 v231, |v161|, v231, v231 op_sel:[0,1,0] op_sel_hi:[0,1,1] clamp\n\t" \
  "v_fma_mix_f32 v230, |v160|, v230, v230 op_sel:[0,1,0] op_sel_hi:[0,1,1] clamp\n\t" \
  "v_mfma_f32_16x16x32_bf16 v[64:67], v[212:215], v[194:197], v[64:67]\n\t" \
  "ds_read_b32 v225, v225 offset:0\n\t" \
  "s_waitcnt lgkmcnt(9)\n\t" \
  "v_fma_f32 v171, |v171|, v233, v171 mul:4\n\t" \
  "v_fma_f32 v170, |v170|, v232, v170 mul:4\n\t" \
  "v_mfma_f32_16x16x32_bf16 v[88:91], v[212:215], v[202:205], v[88:91]\n\t" \
  "ds_read_b64_tr_b16 v[212:213], v248 offset:3072\n\t" \
  "v_fma_f32 v169, |v169|, v235, v169 mul:4\n\t" \
  "v_fma_f32 v168, |v168|, v234, v168 mul:4\n\t" \
  "s_waitcnt lgkmcnt(8)\n\t" \
  "v_mfma_f32_16x16x32_bf16 v[20:23], v[216:219], v[178:181], v[20:23]\n\t" \
  "ds_read_b64_tr_b16 v[214:215], v248 offset:4096\n\t" \
  "v_fma_f32 v161, |v161|, v231, v161 mul:4\n\t" \
  "v_fma_f32 v160, |v160|, v230, v160 mul:4\n\t" \
  "v_mfma_f32_16x16x32_bf16 v[44:47], v[216:219], v[186:189], v[44:47]\n\t" \
  "ds_read_b32 v224, v224 offset:0\n\t" \
  "v_cvt_pk_bf16_f32 v163, v162, v163\n\t" \
  "v_cvt_pk_bf16_f32 v162, v160, v161\n\t" \
  "v_mfma_f32_16x16x32_bf16 v[68:71], v[216:219], v[194:197], v[68:71]\n\t" \
  "ds_read_b32 v227, v227 offset:15376\n\t" \
  "v_cvt_pk_bf16_f32 v171, v170, v171\n\t" \
  "v_cvt_pk_bf16_f32 v170, v168, v169\n\t" \
  "v_cvt_pk_f16_f32 v228, |v164|, |v165| clamp\n\t" \
  "v_mfma_f32_16x16x32_bf16 v[92:95], v[216:219], v[202:205], v[92:95]\n\t" \
  "ds_read_b64_tr_b16 v[216:217], v248 offset:2048\n\t" \
  "v_cvt_pknorm_i16_f32 v230, |v166|, |v167|\n\t" \
  "s_setprio 2\n\t" \
  "v_cvt_pk_f16_f32 v232, |v172|, |v173| clamp\n\t" \
  "s_waitcnt lgkmcnt(9)\n\t" \
  "v_mfma_f32_16x16x32_bf16 v[176:179], v[208:211], v[96:99], 0\n\t" \
  "ds_read_b64_tr_b16 v[218:219], v248 offset:5120\n\t" \
  "v_cvt_pk_f16_f32 v234, |v174|, |v175| clamp\n\t" \
  "v_and_b32_sdwa v229, s56, v228 dst_sel:DWORD dst_unused:UNUSED_PAD src0_sel:DWORD src1_sel:WORD_1\n\t" \
  "v_mfma_f32_16x16x32_bf16 v[184:187], v[208:211], v[100:103], 0\n\t" \
  "ds_read_b32 v226, v226 offset:15376\n\t" \
  "v_and_b32 v228, s56, v228\n\t" \
  "v_lshlrev_b32_sdwa v231, 2, v230 dst_sel:DWORD dst_unused:UNUSED_PAD src0_sel:DWORD src1_sel:BYTE_3\n\t" \
  "v_mfma_f32_16x16x32_bf16 v[192:195], v[208:211], v[104:107], 0\n\t" \
  "ds_read_b32 v229, v229 offset:0\n\t" \
  "v_lshlrev_b32_sdwa v230, 2, v230 dst_sel:DWORD dst_unused:UNUSED_PAD src0_sel:DWORD src1_sel:BYTE_1\n\t" \
  "v_and_b32_sdwa v233, s56, v232 dst_sel:DWORD dst_unused:UNUSED_PAD src0_sel:DWORD src1_sel:WORD_1\n\t" \
  "v_mfma_f32_16x16x32_bf16 v[200:203], v[208:211], v[108:111], 0\n\t" \
  "ds_read_b64_tr_b16 v[208:209], v248 offset:512\n\t" \
  "v_and_b32 v232, s56, v232\n\t" \
  "v_and_b32_sdwa v235, s56, v234 dst_sel:DWORD dst_unused:UNUSED_PAD src0_sel:DWORD src1_sel:WORD_1\n\t" \
  "s_waitcnt lgkmcnt(7)\n\t" \
  "v_mfma_f32_16x16x32_bf16 v[176:179], v[212:215], v[112:115], v[176:179]\n\t" \
  "ds_read_b64_tr_b16 v[210:211], v248 offset:1536\n\t" \
  "ds_read_b32 v228, v228 offset:0\n\t" \
  "v_and_b32 v234, s56, v234\n\t" \
  "v_fma_f32 v149, |v149|, v221, v149 mul:4\n\t" \
  "v_mfma_f32_16x16x32_bf16 v[184:187], v[212:215], v[116:119], v[184:187]\n\t" \
  "ds_read_b32 v231, v231 offset:15376\n\t" \
  "ds_read_b32 v230, v230 offset:15376\n\t" \
  "v_fma_f32 v148, |v148|, v220, v148 mul:4\n\t" \
  "v_fma_mix_f32 v223, |v151|, v223, v223 op_sel:[0,1,0] op_sel_hi:[0,1,1] clamp\n\t" \
  "v_mfma_f32_16x16x32_bf16 v[192:195], v[212:215], v[120:123], v[192:195]\n\t" \
  "ds_read_b32 v233, v233 offset:0\n\t" \
  "v_fma_mix_f32 v222, |v150|, v222, v222 op_sel:[0,1,0] op_sel_hi:[0,1,1] clamp\n\t" \
  "s_waitcnt lgkmcnt(7)\n\t" \
  "v_fma_f32 v157, |v157|, v225, v157 mul:4\n\t" \
  "v_mfma_f32_16x16x32_bf16 v[200:203], v[212:215], v[124:127], v[200:203]\n\t" \
  "ds_read_b64_tr_b16 v[212:213], v248 offset:3584\n\t" \
  "v_fma_f32 v156, |v156|, v224, v156 mul:4\n\t" \
  "v_fma_mix_f32 v227, |v159|, v227, v227 op_sel:[0,1,0] op_sel_hi:[0,1,1] clamp\n\t" \
  "v_fma_mix_f32 v226, |v158|, v226, v226 op_sel:[0,1,0] op_sel_hi:[0,1,1] clamp\n\t" \
  "v_mfma_f32_16x16x32_bf16 v[176:179], v[216:219], v[128:131], v[176:179]\n\t" \
  "ds_read_b64_tr_b16 v[214:215], v248 offset:4608\n\t" \
  "v_fma_f32 v151, |v151|, v223, v151 mul:4\n\t" \
  "v_fma_f32 v150, |v150|, v222, v150 mul:4\n\t" \
  "v_mfma_f32_16x16x32_bf16 v[184:187], v[216:219], v[132:135], v[184:187]\n\t" \
  "ds_read_b32 v232, v232 offset:0\n\t" \
  "v_fma_f32 v159, |v159|, v227, v159 mul:4\n\t" \
  "v_fma_f32 v158, |v158|, v226, v158 mul:4\n\t" \
  "v_mfma_f32_16x16x32_bf16 v[192:195], v[216:219], v[136:139], v[192:195]\n\t" \
  "ds_read_b32 v235, v235 offset:0\n\t" \
  "v_cvt_pk_bf16_f32 v148, v148, v149\n\t" \
  "v_cvt_pk_bf16_f32 v149, v150, v151\n\t" \
  "v_mfma_f32_16x16x32_bf16 v[200:203], v[216:219], v[140:143], v[200:203]\n\t" \
  "ds_read_b64_tr_b16 v[216:217], v248 offset:2560\n\t" \
  "v_cvt_pk_bf16_f32 v156, v156, v157\n\t" \
  "v_cvt_pk_bf16_f32 v157, v158, v159\n\t" \
  "s_waitcnt lgkmcnt(9)\n\t" \
  "v_mfma_f32_16x16x32_bf16 v[180:183], v[208:211], v[96:99], 0\n\t" \
  "ds_read_b64_tr_b16 v[218:219], v248 offset:5632\n\t" \
  "v_cvt_pk_f16_f32 v220, |v178|, |v179| clamp\n\t" \
  "s_setprio 3\n\t" \
  "v_cvt_pknorm_i16_f32 v222, |v176|, |v177|\n\t" \
  "v_mfma_f32_16x16x32_bf16 v[188:191], v[208:211], v[100:103], 0\n\t" \
  "s_waitcnt vmcnt(1)\n\t" \
  "s_cmp_lg_u32 s58, 1\n\t" \
  "s_cbranch_scc1 .Lcnx_c10\n\t" \
  "s_waitcnt vmcnt(0)\n\t" \
  ".Lcnx_c10:\n\t" \
  "s_mov_b64 exec, 3\n\t" \
  "ds_add_rtn_u32 v246, v251, v250 offset:47160\n\t" \
  "s_mov_b64 exec, -1\n\t" \
  "ds_read_b32 v234, v234 offset:0\n\t" \
  "v_cvt_pk_f16_f32 v224, |v186|, |v187| clamp\n\t" \
  "v_cvt_pknorm_i16_f32 v226, |v184|, |v185|\n\t" \
  "v_mfma_f32_16x16x32_bf16 v[196:199], v[208:211], v[104:107], 0\n\t" \
  "v_and_b32_sdwa v221, s56, v220 dst_sel:DWORD dst_unused:UNUSED_PAD src0_sel:DWORD src1_sel:WORD_1\n\t" \
  "v_and_b32 v220, s56, v220\n\t" \
  "v_mfma_f32_16x16x32_bf16 v[204:207], v[208:211], v[108:111], 0\n\t" \
  "s_waitcnt lgkmcnt(1)\n\t" \
  "v_readfirstlane_b32 s4, v246\n\t" \
  "s_cmp_lt_u32 s4, s57\n\t" \
  "s_cbranch_scc1 .Lcnx_w36\n.Lcnx_b36:\n\t" \
  "ds_read_b64_tr_b16 v[208:209], v249 offset:6152\n\t" \
  "v_lshlrev_b32_sdwa v223, 2, v222 dst_sel:DWORD dst_unused:UNUSED_PAD src0_sel:DWORD src1_sel:BYTE_3\n\t" \
  "v_lshlrev_b32_sdwa v222, 2, v222 dst_sel:DWORD dst_unused:UNUSED_PAD src0_sel:DWORD src1_sel:BYTE_1\n\t" \
  "v_and_b32_sdwa v225, s56, v224 dst_sel:DWORD dst_unused:UNUSED_PAD src0_sel:DWORD src1_sel:WORD_1\n\t" \
  "v_mfma_f32_16x16x32_bf16 v[180:183], v[212:215], v[112:115], v[180:183]\n\t" \
  "ds_read_b64_tr_b16 v[210:211], v249 offset:7688\n\t" \
  "v_and_b32 v224, s56, v224\n\t" \
  "v_lshlrev_b32_sdwa v227, 2, v226 dst_sel:DWORD dst_unused:UNUSED_PAD src0_sel:DWORD src1_sel:BYTE_3\n\t" \
  "v_mfma_f32_16x16x32_bf16 v[188:191], v[212:215], v[116:119], v[188:191]\n\t" \
  "s_cmp_eq_u32 s37, 0\n\t" \
  "s_cbranch_scc1 .Lcnx_f4\n.Lcnx_g4:\n\t" \
  "ds_read_b32 v221, v221 offset:0\n\t" \
  "ds_read_b32 v220, v220 offset:0\n\t" \
  "v_lshlrev_b32_sdwa v226, 2, v226 dst_sel:DWORD dst_unused:UNUSED_PAD src0_sel:DWORD src1_sel:BYTE_1\n\t" \
  "v_fma_f32 v165, |v165|, v229, v165 mul:4\n\t" \
  "v_mfma_f32_16x16x32_bf16 v[196:199], v[212:215], v[120:123], v[196:199]\n\t" \
  "ds_read_b32 v223, v223 offset:15376\n\t" \
  "ds_read_b32 v222, v222 offset:15376\n\t" \
  "v_fma_f32 v164, |v164|, v228, v164 mul:4\n\t" \
  "v_fma_mix_f32 v231, |v167|, v231, v231 op_sel:[0,1,0] op_sel_hi:[0,1,1] clamp\n\t" \
  "v_mfma_f32_16x16x32_bf16 v[204:207], v[212:215], v[124:127], v[204:207]\n\t" \
  "ds_read_b64_tr_b16 v[212:213], v249 offset:6160\n\t" \
  "ds_read_b64_tr_b16 v[214:215], v249 offset:7696\n\t" \
  "v_fma_mix_f32 v230, |v166|, v230, v230 op_sel:[0,1,0] op_sel_hi:[0,1,1] clamp\n\t" \
  "s_waitcnt lgkmcnt(8)\n\t" \
  "v_fma_f32 v173, |v173|, v233, v173 mul:4\n\t" \
  "v_mfma_f32_16x16x32_bf16 v[180:183], v[216:219], v[128:131], v[180:183]\n\t" \
  "ds_read_b32 v225, v225 offset:0\n\t" \
  "v_fma_f32 v172, |v172|, v232, v172 mul:4\n\t" \
  "v_fma_f32 v175, |v175|, v235, v175 mul:4\n\t" \
  "v_mfma_f32_16x16x32_bf16 v[188:191], v[216:219], v[132:135], v[188:191]\n\t" \
  "ds_read_b32 v224, v224 offset:0\n\t" \
  "v_fma_f32 v174, |v174|, v234, v174 mul:4\n\t" \
  "v_fma_f32 v167, |v167|, v231, v167 mul:4\n\t" \
  "v_mfma_f32_16x16x32_bf16 v[196:199], v[216:219], v[136:139], v[196:199]\n\t" \
  "ds_read_b32 v227, v227 offset:15376\n\t" \
  "v_fma_f32 v166, |v166|, v230, v166 mul:4\n\t" \
  "v_cvt_pk_bf16_f32 v164, v164, v165\n\t" \
  "v_mfma_f32_16x16x32_bf16 v[204:207], v[216:219], v[140:143], v[204:207]\n\t" \
  "ds_read_b64_tr_b16 v[216:217], v249 offset:6216\n\t" \
  "v_cvt_pk_bf16_f32 v165, v166, v167\n\t" \
  "v_cvt_pk_bf16_f32 v172, v172, v173\n\t" \
  "v_cvt_pk_bf16_f32 v173, v174, v175\n\t" \
  "s_setprio 0\n\t" \
  "v_cvt_pk_f16_f32 v228, |v194|, |v195| clamp\n\t" \
  "v_cvt_pknorm_i16_f32 v230, |v192|, |v193|\n\t" \
  "v_mfma_f32_16x16x32_bf16 v[16:19], v[242:245], v[236:239], v[16:19]\n\t" \
  "ds_read_b64_tr_b16 v[218:219], v249 offset:7752\n\t" \
  "v_cvt_pk_f16_f32 v232, |v202|, |v203| clamp\n\t" \
  "v_cvt_pk_f16_f32 v234, |v200|, |v201| clamp\n\t" \
  "v_mfma_f32_16x16x32_bf16 v[20:23], v[240:243], v[236:239], v[20:23]\n\t" \
  "buffer_load_dwordx4 v[236:239], v250, s[8:11], s29 offen offset:0\n\t" \
  "s_cmp_lg_u32 s58, 0\n\t" \
  "s_cbranch_scc1 .Lcnx_c23\n\t" \
  "s_add_u32 m0, s25, 16400\n\t" \
  "s_add_u32 s24, s24, s26\n\t" \
  "buffer_load_dwordx4 v254, s[8:11], s24 offen lds\n\t" \
  "s_cmp_lg_u32 s38, 4\n\t" \
  "s_cbranch_scc1 .Lcnx_c23\n\t" \
  "s_mov_b32 m0, 163632\n\t" \
  "s_add_u32 s30, s30, s31\n\t" \
  "buffer_load_dword v252, s[8:11], s30 offen lds\n\t" \
  ".Lcnx_c23:\n\t" \
  "ds_read_b32 v226, v226 offset:15376\n\t" \
  "v_and_b32_sdwa v229, s56, v228 dst_sel:DWORD dst_unused:UNUSED_PAD src0_sel:DWORD src1_sel:WORD_1\n\t" \
  "v_and_b32 v228, s56, v228\n\t" \
  "s_waitcnt lgkmcnt(12)\n\t" \
  "v_mfma_f32_16x16x32_bf16 v[0:3], v[208:211], v[146:149], v[0:3]\n\t" \
  "ds_read_b32 v229, v229 offset:0\n\t" \
  "v_lshlrev_b32_sdwa v231, 2, v230 dst_sel:DWORD dst_unused:UNUSED_PAD src0_sel:DWORD src1_sel:BYTE_3\n\t" \
  "v_lshlrev_b32_sdwa v230, 2, v230 dst_sel:DWORD dst_unused:UNUSED_PAD src0_sel:DWORD src1_sel:BYTE_1\n\t" \
  "v_mfma_f32_16x16x32_bf16 v[24:27], v[208:211], v[154:157], v[24:27]\n\t" \
  "ds_read_b32 v228, v228 offset:0\n\t" \
  "v_and_b32_sdwa v233, s56, v232 dst_sel:DWORD dst_unused:UNUSED_PAD src0_sel:DWORD src1_sel:WORD_1\n\t" \
  "v_and_b32 v232, s56, v232\n\t" \
  "v_mfma_f32_16x16x32_bf16 v[48:51], v[208:211], v[162:165], v[48:51]\n\t" \
  "ds_read_b32 v231, v231 offset:15376\n\t" \
  "v_and_b32_sdwa v235, s56, v234 dst_sel:DWORD dst_unused:UNUSED_PAD src0_sel:DWORD src1_sel:WORD_1\n\t" \
  "v_and_b32 v234, s56, v234\n\t" \
  "v_mfma_f32_16x16x32_bf16 v[72:75], v[208:211], v[170:173], v[72:75]\n\t" \
  "ds_read_b64_tr_b16 v[208:209], v249 offset:6224\n\t" \
  "ds_read_b64_tr_b16 v[210:211], v249 offset:7760\n\t" \
  "s_waitcnt lgkmcnt(13)\n\t" \
  "v_fma_f32 v179, |v179|, v221, v179 mul:4\n\t" \
  "v_fma_f32 v178, |v178|, v220, v178 mul:4\n\t" \
  "s_waitcnt lgkmcnt(11)\n\t" \
  "v_mfma_f32_16x16x32_bf16 v[4:7], v[212:215], v[146:149], v[4:7]\n\t" \
  "ds_read_b32 v230, v230 offset:15376\n\t" \
  "v_fma_mix_f32 v223, |v177|, v223, v223 op_sel:[0,1,0] op_sel_hi:[0,1,1] clamp\n\t" \
  "v_fma_mix_f32 v222, |v176|, v222, v222 op_sel:[0,1,0] op_sel_hi:[0,1,1] clamp\n\t" \
  "v_mfma_f32_16x16x32_bf16 v[28:31], v[212:215], v[154:157], v[28:31]\n\t" \
  "ds_read_b32 v233, v233 offset:0\n\t" \
  "s_waitcnt lgkmcnt(7)\n\t" \
  "v_fma_f32 v187, |v187|, v225, v187 mul:4\n\t" \
  "v_fma_f32 v186, |v186|, v224, v186 mul:4\n\t" \
  "v_mfma_f32_16x16x32_bf16 v[52:55], v[212:215], v[162:165], v[52:55]\n\t" \
  "ds_read_b32 v232, v232 offset:0\n\t" \
  "v_fma_mix_f32 v227, |v185|, v227, v227 op_sel:[0,1,0] op_sel_hi:[0,1,1] clamp\n\t" \
  "v_fma_mix_f32 v226, |v184|, v226, v226 op_sel:[0,1,0] op_sel_hi:[0,1,1] clamp\n\t" \
  "v_fma_f32 v177, |v177|, v223, v177 mul:4\n\t" \
  "v_mfma_f32_16x16x32_bf16 v[76:79], v[212:215], v[170:173], v[76:79]\n\t" \
  "ds_read_b64_tr_b16 v[212:213], v249 offset:6280\n\t" \
  "v_fma_f32 v176, |v176|, v222, v176 mul:4\n\t" \
  "v_fma_f32 v185, |v185|, v227, v185 mul:4\n\t" \
  "v_mfma_f32_16x16x32_bf16 v[8:11], v[216:219], v[146:149], v[8:11]\n\t" \
  "ds_read_b64_tr_b16 v[214:215], v249 offset:7816\n\t" \
  "v_fma_f32 v184, |v184|, v226, v184 mul:4\n\t" \
  "v_cvt_pk_bf16_f32 v179, v178, v179\n\t" \
  "v_mfma_f32_16x16x32_bf16 v[32:35], v[216:219], v[154:157], v[32:35]\n\t" \
  "ds_read_b32 v235, v235 offset:0\n\t" \
  "v_cvt_pk_bf16_f32 v178, v176, v177\n\t" \
  "v_cvt_pk_bf16_f32 v187, v186, v187\n\t" \
  "v_mfma_f32_16x16x32_bf16 v[56:59], v[216:219], v[162:165], v[56:59]\n\t" \
  "ds_read_b32 v234, v234 offset:0\n\t" \
  "v_cvt_pk_bf16_f32 v186, v184, v185\n\t" \
  "s_setprio 1\n\t" \
  "v_cvt_pk_f16_f32 v220, |v180|, |v181| clamp\n\t" \
  "v_mfma_f32_16x16x32_bf16 v[80:83], v[216:219], v[170:173], v[80:83]\n\t" \
  "ds_read_b64_tr_b16 v[216:217], v249 offset:6288\n\t" \
  "v_cvt_pknorm_i16_f32 v222, |v182|, |v183|\n\t" \
  "v_cvt_pk_f16_f32 v224, |v188|, |v189| clamp\n\t" \
  "s_waitcnt lgkmcnt(8)\n\t" \
  "v_mfma_f32_16x16x32_bf16 v[12:15], v[208:211], v[146:149], v[12:15]\n\t" \
  "ds_read_b64_tr_b16 v[218:219], v249 offset:7824\n\t" \
  "v_cvt_pknorm_i16_f32 v226, |v190|, |v191|\n\t" \
  "v_and_b32_sdwa v221, s56, v220 dst_sel:DWORD dst_unused:UNUSED_PAD src0_sel:DWORD src1_sel:WORD_1\n\t" \
  "v_mfma_f32_16x16x32_bf16 v[36:39], v[208:211], v[154:157], v[36:39]\n\t" \
  "v_and_b32 v220, s56, v220\n\t" \
  "v_lshlrev_b32_sdwa v223, 2, v222 dst_sel:DWORD dst_unused:UNUSED_PAD src0_sel:DWORD src1_sel:BYTE_3\n\t" \
  "v_mfma_f32_16x16x32_bf16 v[60:63], v[208:211], v[162:165], v[60:63]\n\t" \
  "ds_read_b32 v221, v221 offset:0\n\t" \
  "v_lshlrev_b32_sdwa v222, 2, v222 dst_sel:DWORD dst_unused:UNUSED_PAD src0_sel:DWORD src1_sel:BYTE_1\n\t" \
  "v_and_b32_sdwa v225, s56, v224 dst_sel:DWORD dst_unused:UNUSED_PAD src0_sel:DWORD src1_sel:WORD_1\n\t" \
  "v_and_b32 v224, s56, v224\n\t" \
  "v_mfma_f32_16x16x32_bf16 v[84:87], v[208:211], v[170:173], v[84:87]\n\t" \
  "ds_read_b64_tr_b16 v[208:209], v248 offset:43048\n\t" \
  "ds_read_b64_tr_b16 v[210:211], v248 offset:44072\n\t" \
  "v_lshlrev_b32_sdwa v227, 2, v226 dst_sel:DWORD dst_unused:UNUSED_PAD src0_sel:DWORD src1_sel:BYTE_3\n\t" \
  "v_lshlrev_b32_sdwa v226, 2, v226 dst_sel:DWORD dst_unused:UNUSED_PAD src0_sel:DWORD src1_sel:BYTE_1\n\t" \
  "s_waitcnt lgkmcnt(7)\n\t" \
  "v_mfma_f32_16x16x32_bf16 v[16:19], v[212:215], v[146:149], v[16:19]\n\t" \
  "ds_read_b32 v220, v220 offset:0\n\t" \
  "ds_read_b32 v223, v223 offset:15376\n\t" \
  "v_fma_f32 v195, |v195|, v229, v195 mul:4\n\t" \
  "v_fma_f32 v194, |v194|, v228, v194 mul:4\n\t" \
  "v_mfma_f32_16x16x32_bf16 v[40:43], v[212:215], v[154:157], v[40:43]\n\t" \
  "ds_read_b32 v222, v222 offset:15376\n\t" \
  "v_fma_mix_f32 v231, |v193|, v231, v231 op_sel:[0,1,0] op_sel_hi:[0,1,1] clamp\n\t" \
  "v_fma_mix_f32 v230, |v192|, v230, v230 op_sel:[0,1,0] op_sel_hi:[0,1,1] clamp\n\t" \
  "v_mfma_f32_16x16x32_bf16 v[64:67], v[212:215], v[162:165], v[64:67]\n\t" \
  "ds_read_b32 v225, v225 offset:0\n\t" \
  "s_waitcnt lgkmcnt(9)\n\t" \
  "v_fma_f32 v203, |v203|, v233, v203 mul:4\n\t" \
  "v_fma_f32 v202, |v202|, v232, v202 mul:4\n\t" \
  "v_mfma_f32_16x16x32_bf16 v[88:91], v[212:215], v[170:173], v[88:91]\n\t" \
  "ds_read_b64_tr_b16 v[212:213], v248 offset:46120\n\t" \
  "v_fma_f32 v201, |v201|, v235, v201 mul:4\n\t" \
  "v_fma_f32 v200, |v200|, v234, v200 mul:4\n\t" \
  "s_waitcnt lgkmcnt(8)\n\t" \
  "v_mfma_f32_16x16x32_bf16 v[20:23], v[216:219], v[146:149], v[20:23]\n\t" \
  "ds_read_b64_tr_b16 v[214:215], v248 offset:47144\n\t" \
  "v_fma_f32 v193, |v193|, v231, v193 mul:4\n\t" \
  "v_fma_f32 v192, |v192|, v230, v192 mul:4\n\t" \
  "v_mfma_f32_16x16x32_bf16 v[44:47], v[216:219], v[154:157], v[44:47]\n\t" \
  "ds_read_b32 v224, v224 offset:0\n\t" \
  "v_cvt_pk_bf16_f32 v195, v194, v195\n\t" \
  "v_cvt_pk_bf16_f32 v194, v192, v193\n\t" \
  "v_mfma_f32_16x16x32_bf16 v[68:71], v[216:219], v[162:165], v[68:71]\n\t" \
  "ds_read_b32 v227, v227 offset:15376\n\t" \
  "v_cvt_pk_bf16_f32 v203, v202, v203\n\t" \
  "v_cvt_pk_bf16_f32 v202, v200, v201\n\t" \
  "v_cvt_pk_f16_f32 v228, |v196|, |v197| clamp\n\t" \
  "v_mfma_f32_16x16x32_bf16 v[92:95], v[216:219], v[170:173], v[92:95]\n\t" \
  "ds_read_b64_tr_b16 v[216:217], v248 offset:45096\n\t" \
  "v_cvt_pknorm_i16_f32 v230, |v198|, |v199|\n\t" \
  "s_setprio 2\n\t" \
  "v_cvt_pk_f16_f32 v232, |v204|, |v205| clamp\n\t" \
  "s_waitcnt lgkmcnt(9)\n\t" \
  "v_mfma_f32_16x16x32_bf16 v[144:147], v[208:211], v[96:99], 0\n\t" \
  "ds_read_b64_tr_b16 v[218:219], v248 offset:48168\n\t" \
  "v_cvt_pk_f16_f32 v234, |v206|, |v207| clamp\n\t" \
  "v_and_b32_sdwa v229, s56, v228 dst_sel:DWORD dst_unused:UNUSED_PAD src0_sel:DWORD src1_sel:WORD_1\n\t" \
  "v_mfma_f32_16x16x32_bf16 v[152:155], v[208:211], v[100:103], 0\n\t" \
  "ds_read_b32 v226, v226 offset:15376\n\t" \
  "v_and_b32 v228, s56, v228\n\t" \
  "v_lshlrev_b32_sdwa v231, 2, v230 dst_sel:DWORD dst_unused:UNUSED_PAD src0_sel:DWORD src1_sel:BYTE_3\n\t" \
  "v_mfma_f32_16x16x32_bf16 v[160:163], v[208:211], v[104:107], 0\n\t" \
  "ds_read_b32 v229, v229 offset:0\n\t" \
  "v_lshlrev_b32_sdwa v230, 2, v230 dst_sel:DWORD dst_unused:UNUSED_PAD src0_sel:DWORD src1_sel:BYTE_1\n\t" \
  "v_and_b32_sdwa v233, s56, v232 dst_sel:DWORD dst_unused:UNUSED_PAD src0_sel:DWORD src1_sel:WORD_1\n\t" \
  "v_mfma_f32_16x16x32_bf16 v[168:171], v[208:211], v[108:111], 0\n\t" \
  "ds_read_b64_tr_b16 v[208:209], v248 offset:43560\n\t" \
  "v_and_b32 v232, s56, v232\n\t" \
  "v_and_b32_sdwa v235, s56, v234 dst_sel:DWORD dst_unused:UNUSED_PAD src0_sel:DWORD src1_sel:WORD_1\n\t" \
  "s_waitcnt lgkmcnt(7)\n\t" \
  "v_mfma_f32_16x16x32_bf16 v[144:147], v[212:215], v[112:115], v[144:147]\n\t" \
  "ds_read_b64_tr_b16 v[210:211], v248 offset:44584\n\t" \
  "ds_read_b32 v228, v228 offset:0\n\t" \
  "v_and_b32 v234, s56, v234\n\t" \
  "v_fma_f32 v181, |v181|, v221, v181 mul:4\n\t" \
  "v_mfma_f32_16x16x32_bf16 v[152:155], v[212:215], v[116:119], v[152:155]\n\t" \
  "ds_read_b32 v231, v231 offset:15376\n\t" \
  "ds_read_b32 v230, v230 offset:15376\n\t" \
  "v_fma_f32 v180, |v180|, v220, v180 mul:4\n\t" \
  "v_fma_mix_f32 v223, |v183|, v223, v223 op_sel:[0,1,0] op_sel_hi:[0,1,1] clamp\n\t" \
  "v_mfma_f32_16x16x32_bf16 v[160:163], v[212:215], v[120:123], v[160:163]\n\t" \
  "ds_read_b32 v233, v233 offset:0\n\t" \
  "v_fma_mix_f32 v222, |v182|, v222, v222 op_sel:[0,1,0] op_sel_hi:[0,1,1] clamp\n\t" \
  "s_waitcnt lgkmcnt(7)\n\t" \
  "v_fma_f32 v189, |v189|, v225, v189 mul:4\n\t" \
  "v_mfma_f32_16x16x32_bf16 v[168:171], v[212:215], v[124:127], v[168:171]\n\t" \
  "ds_read_b64_tr_b16 v[212:213], v248 offset:46632\n\t" \
  "v_fma_f32 v188, |v188|, v224, v188 mul:4\n\t" \
  "v_fma_mix_f32 v227, |v191|, v227, v227 op_sel:[0,1,0] op_sel_hi:[0,1,1] clamp\n\t" \
  "v_fma_mix_f32 v226, |v190|, v226, v226 op_sel:[0,1,0] op_sel_hi:[0,1,1] clamp\n\t" \
  "v_mfma_f32_16x16x32_bf16 v[144:147], v[216:219], v[128:131], v[144:147]\n\t" \
  "ds_read_b64_tr_b16 v[214:215], v248 offset:47656\n\t" \
  "v_fma_f32 v183, |v183|, v223, v183 mul:4\n\t" \
  "v_fma_f32 v182, |v182|, v222, v182 mul:4\n\t" \
  "v_mfma_f32_16x16x32_bf16 v[152:155], v[216:219], v[132:135], v[152:155]\n\t" \
  "ds_read_b32 v232, v232 offset:0\n\t" \
  "v_fma_f32 v191, |v191|, v227, v191 mul:4\n\t" \
  "v_fma_f32 v190, |v190|, v226, v190 mul:4\n\t" \
  "v_mfma_f32_16x16x32_bf16 v[160:163], v[216:219], v[136:139], v[160:163]\n\t" \
  "ds_read_b32 v235, v235 offset:0\n\t" \
  "v_cvt_pk_bf16_f32 v180, v180, v181\n\t" \
  "v_cvt_pk_bf16_f32 v181, v182, v183\n\t" \
  "v_mfma_f32_16x16x32_bf16 v[168:171], v[216:219], v[140:143], v[168:171]\n\t" \
  "ds_read_b64_tr_b16 v[216:217], v248 offset:45608\n\t" \
  "v_cvt_pk_bf16_f32 v188, v188, v189\n\t" \
  "v_cvt_pk_bf16_f32 v189, v190, v191\n\t" \
  "s_waitcnt lgkmcnt(9)\n\t" \
  "v_mfma_f32_16x16x32_bf16 v[148:151], v[208:211], v[96:99], 0\n\t" \
  "ds_read_b64_tr_b16 v[218:219], v248 offset:48680\n\t" \
  "v_cvt_pk_f16_f32 v220, |v146|, |v147| clamp\n\t" \
  "s_setprio 3\n\t" \
  "v_cvt_pknorm_i16_f32 v222, |v144|, |v145|\n\t" \
  "v_mfma_f32_16x16x32_bf16 v[156:159], v[208:211], v[100:103], 0\n\t" \
  "s_waitcnt vmcnt(1)\n\t" \
  "s_cmp_lg_u32 s58, 1\n\t" \
  "s_cbranch_scc1 .Lcnx_c11\n\t" \
  "s_waitcnt vmcnt(0)\n\t" \
  ".Lcnx_c11:\n\t" \
  "s_mov_b64 exec, 3\n\t" \
  "ds_add_rtn_u32 v246, v251, v250 offset:47168\n\t" \
  "s_mov_b64 exec, -1\n\t" \
  "ds_read_b32 v234, v234 offset:0\n\t" \
  "v_cvt_pk_f16_f32 v224, |v154|, |v155| clamp\n\t" \
  "v_cvt_pknorm_i16_f32 v226, |v152|, |v153|\n\t" \
  "v_mfma_f32_16x16x32_bf16 v[164:167], v[208:211], v[104:107], 0\n\t" \
  "v_and_b32_sdwa v221, s56, v220 dst_sel:DWORD dst_unused:UNUSED_PAD src0_sel:DWORD src1_sel:WORD_1\n\t" \
  "v_and_b32 v220, s56, v220\n\t" \
  "v_mfma_f32_16x16x32_bf16 v[172:175], v[208:211], v[108:111], 0\n\t" \
  "s_waitcnt lgkmcnt(1)\n\t" \
  "v_readfirstlane_b32 s4, v246\n\t" \
  "s_cmp_lt_u32 s4, s57\n\t" \
  "s_cbranch_scc1 .Lcnx_w48\n.Lcnx_b48:\n\t" \
  "ds_read_b64_tr_b16 v[208:209], v249 offset:12304\n\t" \
  "v_lshlrev_b32_sdwa v223, 2, v222 dst_sel:DWORD dst_unused:UNUSED_PAD src0_sel:DWORD src1_sel:BYTE_3\n\t" \
  "v_lshlrev_b32_sdwa v222, 2, v222 dst_sel:DWORD dst_unused:UNUSED_PAD src0_sel:DWORD src1_sel:BYTE_1\n\t" \
  "v_and_b32_sdwa v225, s56, v224 dst_sel:DWORD dst_unused:UNUSED_PAD src0_sel:DWORD src1_sel:WORD_1\n\t" \
  "v_mfma_f32_16x16x32_bf16 v[148:151], v[212:215], v[112:115], v[148:151]\n\t" \
  "ds_read_b64_tr_b16 v[210:211], v249 offset:13840\n\t" \
  "v_and_b32 v224, s56, v224\n\t" \
  "v_lshlrev_b32_sdwa v227, 2, v226 dst_sel:DWORD dst_unused:UNUSED_PAD src0_sel:DWORD src1_sel:BYTE_3\n\t" \
  "v_mfma_f32_16x16x32_bf16 v[156:159], v[212:215], v[116:119], v[156:159]\n\t" \
  "s_cmp_eq_u32 s37, 0\n\t" \
  "s_cbranch_scc1 .Lcnx_f5\n.Lcnx_g5:\n\t" \
  "ds_read_b32 v221, v221 offset:0\n\t" \
  "ds_read_b32 v220, v220 offset:0\n\t" \
  "v_lshlrev_b32_sdwa v226, 2, v226 dst_sel:DWORD dst_unused:UNUSED_PAD src0_sel:DWORD src1_sel:BYTE_1\n\t" \
  "v_fma_f32 v197, |v197|, v229, v197 mul:4\n\t" \
  "v_mfma_f32_16x16x32_bf16 v[164:167], v[212:215], v[120:123], v[164:167]\n\t" \
  "ds_read_b32 v223, v223 offset:15376\n\t" \
  "ds_read_b32 v222, v222 offset:15376\n\t" \
  "v_fma_f32 v196, |v196|, v228, v196 mul:4\n\t" \
  "v_fma_mix_f32 v231, |v199|, v231, v231 op_sel:[0,1,0] op_sel_hi:[0,1,1] clamp\n\t" \
  "v_mfma_f32_16x16x32_bf16 v[172:175], v[212:215], v[124:127], v[172:175]\n\t" \
  "ds_read_b64_tr_b16 v[212:213], v249 offset:12312\n\t" \
  "ds_read_b64_tr_b16 v[214:215], v249 offset:13848\n\t" \
  "v_fma_mix_f32 v230, |v198|, v230, v230 op_sel:[0,1,0] op_sel_hi:[0,1,1] clamp\n\t" \
  "s_waitcnt lgkmcnt(8)\n\t" \
  "v_fma_f32 v205, |v205|, v233, v205 mul:4\n\t" \
  "v_mfma_f32_16x16x32_bf16 v[148:151], v[216:219], v[128:131], v[148:151]\n\t" \
  "ds_read_b32 v225, v225 offset:0\n\t" \
  "v_fma_f32 v204, |v204|, v232, v204 mul:4\n\t" \
  "v_fma_f32 v207, |v207|, v235, v207 mul:4\n\t" \
  "v_mfma_f32_16x16x32_bf16 v[156:159], v[216:219], v[132:135], v[156:159]\n\t" \
  "ds_read_b32 v224, v224 offset:0\n\t" \
  "v_fma_f32 v206, |v206|, v234, v206 mul:4\n\t" \
  "v_fma_f32 v199, |v199|, v231, v199 mul:4\n\t" \
  "v_mfma_f32_16x16x32_bf16 v[164:167], v[216:219], v[136:139], v[164:167]\n\t" \
  "ds_read_b32 v227, v227 offset:15376\n\t" \
  "v_fma_f32 v198, |v198|, v230, v198 mul:4\n\t" \
  "v_cvt_pk_bf16_f32 v196, v196, v197\n\t" \
  "v_mfma_f32_16x16x32_bf16 v[172:175], v[216:219], v[140:143], v[172:175]\n\t" \
  "ds_read_b64_tr_b16 v[216:217], v249 offset:12368\n\t" \
  "v_cvt_pk_bf16_f32 v197, v198, v199\n\t" \
  "v_cvt_pk_bf16_f32 v204, v204, v205\n\t" \
  "v_cvt_pk_bf16_f32 v205, v206, v207\n\t" \
  "s_setprio 0\n\t" \
  "v_cvt_pk_f16_f32 v228, |v162|, |v163| clamp\n\t" \
  "v_cvt_pknorm_i16_f32 v230, |v160|, |v161|\n\t" \
  "v_mfma_f32_16x16x32_bf16 v[24:27], v[242:245], v[236:239], v[24:27]\n\t" \
  "ds_read_b64_tr_b16 v[218:219], v249 offset:13904\n\t" \
  "v_cvt_pk_f16_f32 v232, |v170|, |v171| clamp\n\t" \
  "v_cvt_pk_f16_f32 v234, |v168|, |v169| clamp\n\t" \
  "v_mfma_f32_16x16x32_bf16 v[28:31], v[240:243], v[236:239], v[28:31]\n\t" \
  "buffer_load_dwordx4 v[236:239], v250, s[8:11], s29 offen offset:64\n\t" \
  "s_cmp_lg_u32 s58, 0\n\t" \
  "s_cbranch_scc1 .Lcnx_c24\n\t" \
  "s_add_u32 m0, s25, 24600\n\t" \
  "s_add_u32 s24, s24, s26\n\t" \
  "buffer_load_dwordx4 v254, s[8:11], s24 offen lds\n\t" \
  "s_cmp_lg_u32 s38, 5\n\t" \
  "s_cbranch_scc1 .Lcnx_c24\n\t" \
  "s_mov_b32 m0, 163632\n\t" \
  "s_add_u32 s30, s30, s31\n\t" \
  "buffer_load_dword v252, s[8:11], s30 offen lds\n\t" \
  ".Lcnx_c24:\n\t" \
  "ds_read_b32 v226, v226 offset:15376\n\t" \
  "v_and_b32_sdwa v229, s56, v228 dst_sel:DWORD dst_unused:UNUSED_PAD src0_sel:DWORD src1_sel:WORD_1\n\t" \
  "v_and_b32 v228, s56, v228\n\t" \
  "s_waitcnt lgkmcnt(12)\n\t" \
  "v_mfma_f32_16x16x32_bf16 v[0:3], v[208:211], v[178:181], v[0:3]\n\t" \
  "ds_read_b32 v229, v229 offset:0\n\t" \
  "v_lshlrev_b32_sdwa v231, 2, v230 dst_sel:DWORD dst_unused:UNUSED_PAD src0_sel:DWORD src1_sel:BYTE_3\n\t" \
  "v_lshlrev_b32_sdwa v230, 2, v230 dst_sel:DWORD dst_unused:UNUSED_PAD src0_sel:DWORD src1_sel:BYTE_1\n\t" \
  "v_mfma_f32_16x16x32_bf16 v[24:27], v[208:211], v[186:189], v[24:27]\n\t" \
  "ds_read_b32 v228, v228 offset:0\n\t" \
  "v_and_b32_sdwa v233, s56, v232 dst_sel:DWORD dst_unused:UNUSED_PAD src0_sel:DWORD src1_sel:WORD_1\n\t" \
  "v_and_b32 v232, s56, v232\n\t" \
  "v_mfma_f32_16x16x32_bf16 v[48:51], v[208:211], v[194:197], v[48:51]\n\t" \
  "ds_read_b32 v231, v231 offset:15376\n\t" \
  "v_and_b32_sdwa v235, s56, v234 dst_sel:DWORD dst_unused:UNUSED_PAD src0_sel:DWORD src1_sel:WORD_1\n\t" \
  "v_and_b32 v234, s56, v234\n\t" \
  "v_mfma_f32_16x16x32_bf16 v[72:75], v[208:211], v[202:205], v[72:75]\n\t" \
  "ds_read_b64_tr_b16 v[208:209], v249 offset:12376\n\t" \
  "ds_read_b64_tr_b16 v[210:211], v249 offset:13912\n\t" \
  "s_waitcnt lgkmcnt(13)\n\t" \
  "v_fma_f32 v147, |v147|, v221, v147 mul:4\n\t" \
  "v_fma_f32 v146, |v146|, v220, v146 mul:4\n\t" \
  "s_waitcnt lgkmcnt(11)\n\t" \
  "v_mfma_f32_16x16x32_bf16 v[4:7], v[212:215], v[178:181], v[4:7]\n\t" \
  "ds_read_b32 v230, v230 offset:15376\n\t" \
  "v_fma_mix_f32 v223, |v145|, v223, v223 op_sel:[0,1,0] op_sel_hi:[0,1,1] clamp\n\t" \
  "v_fma_mix_f32 v222, |v144|, v222, v222 op_sel:[0,1,0] op_sel_hi:[0,1,1] clamp\n\t" \
  "v_mfma_f32_16x16x32_bf16 v[28:31], v[212:215], v[186:189], v[28:31]\n\t" \
  "ds_read_b32 v233, v233 offset:0\n\t" \
  "s_waitcnt lgkmcnt(7)\n\t" \
  "v_fma_f32 v155, |v155|, v225, v155 mul:4\n\t" \
  "v_fma_f32 v154, |v154|, v224, v154 mul:4\n\t" \
  "v_mfma_f32_16x16x32_bf16 v[52:55], v[212:215], v[194:197], v[52:55]\n\t" \
  "ds_read_b32 v232, v232 offset:0\n\t" \
  "v_fma_mix_f32 v227, |v153|, v227, v227 op_sel:[0,1,0] op_sel_hi:[0,1,1] clamp\n\t" \
  "v_fma_mix_f32 v226, |v152|, v226, v226 op_sel:[0,1,0] op_sel_hi:[0,1,1] clamp\n\t" \
  "v_fma_f32 v145, |v145|, v223, v145 mul:4\n\t" \
  "v_mfma_f32_16x16x32_bf16 v[76:79], v[212:215], v[202:205], v[76:79]\n\t" \
  "ds_read_b64_tr_b16 v[212:213], v249 offset:12432\n\t" \
  "v_fma_f32 v144, |v144|, v222, v144 mul:4\n\t" \
  "v_fma_f32 v153, |v153|, v227, v153 mul:4\n\t" \
  "v_mfma_f32_16x16x32_bf16 v[8:11], v[216:219], v[178:181], v[8:11]\n\t" \
  "ds_read_b64_tr_b16 v[214:215], v249 offset:13968\n\t" \
  "v_fma_f32 v152, |v152|, v226, v152 mul:4\n\t" \
  "v_cvt_pk_bf16_f32 v147, v146, v147\n\t" \
  "v_mfma_f32_16x16x32_bf16 v[32:35], v[216:219], v[186:189], v[32:35]\n\t" \
  "ds_read_b32 v235, v235 offset:0\n\t" \
  "v_cvt_pk_bf16_f32 v146, v144, v145\n\t" \
  "v_cvt_pk_bf16_f32 v155, v154, v155\n\t" \
  "v_mfma_f32_16x16x32_bf16 v[56:59], v[216:219], v[194:197], v[56:59]\n\t" \
  "ds_read_b32 v234, v234 offset:0\n\t" \
  "v_cvt_pk_bf16_f32 v154, v152, v153\n\t" \
  "s_setprio 1\n\t" \
  "v_cvt_pk_f16_f32 v220, |v148|, |v149| clamp\n\t" \
  "v_mfma_f32_16x16x32_bf16 v[80:83], v[216:219], v[202:205], v[80:83]\n\t" \
  "ds_read_b64_tr_b16 v[216:217], v249 offset:12440\n\t" \
  "v_cvt_pknorm_i16_f32 v222, |v150|, |v151|\n\t" \
  "v_cvt_pk_f16_f32 v224, |v156|, |v157| clamp\n\t" \
  "s_waitcnt lgkmcnt(8)\n\t" \
  "v_mfma_f32_16x16x32_bf16 v[12:15], v[208:211], v[178:181], v[12:15]\n\t" \
  "ds_read_b64_tr_b16 v[218:219], v249 offset:13976\n\t" \
  "v_cvt_pknorm_i16_f32 v226, |v158|, |v159|\n\t" \
  "v_and_b32_sdwa v221, s56, v220 dst_sel:DWORD dst_unused:UNUSED_PAD src0_sel:DWORD src1_sel:WORD_1\n\t" \
  "v_mfma_f32_16x16x32_bf16 v[36:39], v[208:211], v[186:189], v[36:39]\n\t" \
  "v_and_b32 v220, s56, v220\n\t" \
  "v_lshlrev_b32_sdwa v223, 2, v222 dst_sel:DWORD dst_unused:UNUSED_PAD src0_sel:DWORD src1_sel:BYTE_3\n\t" \
  "v_mfma_f32_16x16x32_bf16 v[60:63], v[208:211], v[194:197], v[60:63]\n\t" \
  "ds_read_b32 v221, v221 offset:0\n\t" \
  "v_lshlrev_b32_sdwa v222, 2, v222 dst_sel:DWORD dst_unused:UNUSED_PAD src0_sel:DWORD src1_sel:BYTE_1\n\t" \
  "v_and_b32_sdwa v225, s56, v224 dst_sel:DWORD dst_unused:UNUSED_PAD src0_sel:DWORD src1_sel:WORD_1\n\t" \
  "v_and_b32 v224, s56, v224\n\t" \
  "v_mfma_f32_16x16x32_bf16 v[84:87], v[208:211], v[202:205], v[84:87]\n\t" \
  "ds_read_b64_tr_b16 v[208:209], v248 offset:49192\n\t" \
  "ds_read_b64_tr_b16 v[210:211], v248 offset:50216\n\t" \
  "v_lshlrev_b32_sdwa v227, 2, v226 dst_sel:DWORD dst_unused:UNUSED_PAD src0_sel:DWORD src1_sel:BYTE_3\n\t" \
  "v_lshlrev_b32_sdwa v226, 2, v226 dst_sel:DWORD dst_unused:UNUSED_PAD src0_sel:DWORD src1_sel:BYTE_1\n\t" \
  "s_waitcnt lgkmcnt(7)\n\t" \
  "v_mfma_f32_16x16x32_bf16 v[16:19], v[212:215], v[178:181], v[16:19]\n\t" \
  "ds_read_b32 v220, v220 offset:0\n\t" \
  "ds_read_b32 v223, v223 offset:15376\n\t" \
  "v_fma_f32 v163, |v163|, v229, v163 mul:4\n\t" \
  "v_fma_f32 v162, |v162|, v228, v162 mul:4\n\t" \
  "v_mfma_f32_16x16x32_bf16 v[40:43], v[212:215], v[186:189], v[40:43]\n\t" \
  "ds_read_b32 v222, v222 offset:15376\n\t" \
  "v_fma_mix_f32 v231, |v161|, v231, v231 op_sel:[0,1,0] op_sel_hi:[0,1,1] clamp\n\t" \
  "v_fma_mix_f32 v230, |v160|, v230, v230 op_sel:[0,1,0] op_sel_hi:[0,1,1] clamp\n\t" \
  "v_mfma_f32_16x16x32_bf16 v[64:67], v[212:215], v[194:197], v[64:67]\n\t" \
  "ds_read_b32 v225, v225 offset:0\n\t" \
  "s_waitcnt lgkmcnt(9)\n\t" \
  "v_fma_f32 v171, |v171|, v233, v171 mul:4\n\t" \
  "v_fma_f32 v170, |v170|, v232, v170 mul:4\n\t" \
  "v_mfma_f32_16x16x32_bf16 v[88:91], v[212:215], v[202:205], v[88:91]\n\t" \
  "ds_read_b64_tr_b16 v[212:213], v248 offset:52264\n\t" \
  "v_fma_f32 v169, |v169|, v235, v169 mul:4\n\t" \
  "v_fma_f32 v168, |v168|, v234, v168 mul:4\n\t" \
  "s_waitcnt lgkmcnt(8)\n\t" \
  "v_mfma_f32_16x16x32_bf16 v[20:23], v[216:219], v[178:181], v[20:23]\n\t" \
  "ds_read_b64_tr_b16 v[214:215], v248 offset:53288\n\t" \
  "v_fma_f32 v161, |v161|, v231, v161 mul:4\n\t" \
  "v_fma_f32 v160, |v160|, v230, v160 mul:4\n\t" \
  "v_mfma_f32_16x16x32_bf16 v[44:47], v[216:219], v[186:189], v[44:47]\n\t" \
  "ds_read_b32 v224, v224 offset:0\n\t" \
  "v_cvt_pk_bf16_f32 v163, v162, v163\n\t" \
  "v_cvt_pk_bf16_f32 v162, v160, v161\n\t" \
  "v_mfma_f32_16x16x32_bf16 v[68:71], v[216:219], v[194:197], v[68:71]\n\t" \
  "ds_read_b32 v227, v227 offset:15376\n\t" \
  "v_cvt_pk_bf16_f32 v171, v170, v171\n\t" \
  "v_cvt_pk_bf16_f32 v170, v168, v169\n\t" \
  "v_cvt_pk_f16_f32 v228, |v164|, |v165| clamp\n\t" \
  "v_mfma_f32_16x16x32_bf16 v[92:95], v[216:219], v[202:205], v[92:95]\n\t" \
  "ds_read_b64_tr_b16 v[216:217], v248 offset:51240\n\t" \
  "v_cvt_pknorm_i16_f32 v230, |v166|, |v167|\n\t" \
  "s_setprio 2\n\t" \
  "v_cvt_pk_f16_f32 v232, |v172|, |v173| clamp\n\t" \
  "s_waitcnt lgkmcnt(9)\n\t" \
  "v_mfma_f32_16x16x32_bf16 v[176:179], v[208:211], v[96:99], 0\n\t" \
  "ds_read_b64_tr_b16 v[218:219], v248 offset:54312\n\t" \
  "v_cvt_pk_f16_f32 v234, |v174|, |v175| clamp\n\t" \
  "v_and_b32_sdwa v229, s56, v228 dst_sel:DWORD dst_unused:UNUSED_PAD src0_sel:DWORD src1_sel:WORD_1\n\t" \
  "v_mfma_f32_16x16x32_bf16 v[184:187], v[208:211], v[100:103], 0\n\t" \
  "ds_read_b32 v226, v226 offset:15376\n\t" \
  "v_and_b32 v228, s56, v228\n\t" \
  "v_lshlrev_b32_sdwa v231, 2, v230 dst_sel:DWORD dst_unused:UNUSED_PAD src0_sel:DWORD src1_sel:BYTE_3\n\t" \
  "v_mfma_f32_16x16x32_bf16 v[192:195], v[208:211], v[104:107], 0\n\t" \
  "ds_read_b32 v229, v229 offset:0\n\t" \
  "v_lshlrev_b32_sdwa v230, 2, v230 dst_sel:DWORD dst_unused:UNUSED_PAD src0_sel:DWORD src1_sel:BYTE_1\n\t" \
  "v_and_b32_sdwa v233, s56, v232 dst_sel:DWORD dst_unused:UNUSED_PAD src0_sel:DWORD src1_sel:WORD_1\n\t" \
  "v_mfma_f32_16x16x32_bf16 v[200:203], v[208:211], v[108:111], 0\n\t" \
  "ds_read_b64_tr_b16 v[208:209], v248 offset:49704\n\t" \
  "v_and_b32 v232, s56, v232\n\t" \
  "v_and_b32_sdwa v235, s56, v234 dst_sel:DWORD dst_unused:UNUSED_PAD src0_sel:DWORD src1_sel:WORD_1\n\t" \
  "s_waitcnt lgkmcnt(7)\n\t" \
  "v_mfma_f32_16x16x32_bf16 v[176:179], v[212:215], v[112:115], v[176:179]\n\t" \
  "ds_read_b64_tr_b16 v[210:211], v248 offset:50728\n\t" \
  "ds_read_b32 v228, v228 offset:0\n\t" \
  "v_and_b32 v234, s56, v234\n\t" \
  "v_fma_f32 v149, |v149|, v221, v149 mul:4\n\t" \
  "v_mfma_f32_16x16x32_bf16 v[184:187], v[212:215], v[116:119], v[184:187]\n\t" \
  "ds_read_b32 v231, v231 offset:15376\n\t" \
  "ds_read_b32 v230, v230 offset:15376\n\t" \
  "v_fma_f32 v148, |v148|, v220, v148 mul:4\n\t" \
  "v_fma_mix_f32 v223, |v151|, v223, v223 op_sel:[0,1,0] op_sel_hi:[0,1,1] clamp\n\t" \
  "v_mfma_f32_16x16x32_bf16 v[192:195], v[212:215], v[120:123], v[192:195]\n\t" \
  "ds_read_b32 v233, v233 offset:0\n\t" \
  "v_fma_mix_f32 v222, |v150|, v222, v222 op_sel:[0,1,0] op_sel_hi:[0,1,1] clamp\n\t" \
  "s_waitcnt lgkmcnt(7)\n\t" \
  "v_fma_f32 v157, |v157|, v225, v157 mul:4\n\t" \
  "v_mfma_f32_16x16x32_bf16 v[200:203], v[212:215], v[124:127], v[200:203]\n\t" \
  "ds_read_b64_tr_b16 v[212:213], v248 offset:52776\n\t" \
  "v_fma_f32 v156, |v156|, v224, v156 mul:4\n\t" \
  "v_fma_mix_f32 v227, |v159|, v227, v227 op_sel:[0,1,0] op_sel_hi:[0,1,1] clamp\n\t" \
  "v_fma_mix_f32 v226, |v158|, v226, v226 op_sel:[0,1,0] op_sel_hi:[0,1,1] clamp\n\t" \
  "v_mfma_f32_16x16x32_bf16 v[176:179], v[216:219], v[128:131], v[176:179]\n\t" \
  "ds_read_b64_tr_b16 v[214:215], v248 offset:53800\n\t" \
  "v_fma_f32 v151, |v151|, v223, v151 mul:4\n\t" \
  "v_fma_f32 v150, |v150|, v222, v150 mul:4\n\t" \
  "v_mfma_f32_16x16x32_bf16 v[184:187], v[216:219], v[132:135], v[184:187]\n\t" \
  "ds_read_b32 v232, v232 offset:0\n\t" \
  "v_fma_f32 v159, |v159|, v227, v159 mul:4\n\t" \
  "v_fma_f32 v158, |v158|, v226, v158 mul:4\n\t" \
  "v_mfma_f32_16x16x32_bf16 v[192:195], v[216:219], v[136:139], v[192:195]\n\t" \
  "ds_read_b32 v235, v235 offset:0\n\t" \
  "v_cvt_pk_bf16_f32 v148, v148, v149\n\t" \
  "v_cvt_pk_bf16_f32 v149, v150, v151\n\t" \
  "v_mfma_f32_16x16x32_bf16 v[200:203], v[216:219], v[140:143], v[200:203]\n\t" \
  "ds_read_b64_tr_b16 v[216:217], v248 offset:51752\n\t" \
  "v_cvt_pk_bf16_f32 v156, v156, v157\n\t" \
  "v_cvt_pk_bf16_f32 v157, v158, v159\n\t" \
  "s_waitcnt lgkmcnt(9)\n\t" \
  "v_mfma_f32_16x16x32_bf16 v[180:183], v[208:211], v[96:99], 0\n\t" \
  "ds_read_b64_tr_b16 v[218:219], v248 offset:54824\n\t" \
  "v_cvt_pk_f16_f32 v220, |v178|, |v179| clamp\n\t" \
  "s_setprio 3\n\t" \
  "v_cvt_pknorm_i16_f32 v222, |v176|, |v177|\n\t" \
  "v_mfma_f32_16x16x32_bf16 v[188:191], v[208:211], v[100:103], 0\n\t" \
  "s_waitcnt vmcnt(1)\n\t" \
  "s_cmp_lg_u32 s58, 1\n\t" \
  "s_cbranch_scc1 .Lcnx_c12\n\t" \
  "s_waitcnt vmcnt(0)\n\t" \
  ".Lcnx_c12:\n\t" \
  "s_mov_b64 exec, 3\n\t" \
  "ds_add_rtn_u32 v246, v251, v250 offset:47176\n\t" \
  "s_mov_b64 exec, -1\n\t" \
  "ds_read_b32 v234, v234 offset:0\n\t" \
  "v_cvt_pk_f16_f32 v224, |v186|, |v187| clamp\n\t" \
  "v_cvt_pknorm_i16_f32 v226, |v184|, |v185|\n\t" \
  "v_mfma_f32_16x16x32_bf16 v[196:199], v[208:211], v[104:107], 0\n\t" \
  "v_and_b32_sdwa v221, s56, v220 dst_sel:DWORD dst_unused:UNUSED_PAD src0_sel:DWORD src1_sel:WORD_1\n\t" \
  "v_and_b32 v220, s56, v220\n\t" \
  "v_mfma_f32_16x16x32_bf16 v[204:207], v[208:211], v[108:111], 0\n\t" \
  "s_waitcnt lgkmcnt(1)\n\t" \
  "v_readfirstlane_b32 s4, v246\n\t" \
  "s_cmp_lt_u32 s4, s57\n\t" \
  "s_cbranch_scc1 .Lcnx_w60\n.Lcnx_b60:\n\t" \
  "ds_read_b64_tr_b16 v[208:209], v249 offset:24600\n\t" \
  "v_lshlrev_b32_sdwa v223, 2, v222 dst_sel:DWORD dst_unused:UNUSED_PAD src0_sel:DWORD src1_sel:BYTE_3\n\t" \
  "v_lshlrev_b32_sdwa v222, 2, v222 dst_sel:DWORD dst_unused:UNUSED_PAD src0_sel:DWORD src1_sel:BYTE_1\n\t" \
  "v_and_b32_sdwa v225, s56, v224 dst_sel:DWORD dst_unused:UNUSED_PAD src0_sel:DWORD src1_sel:WORD_1\n\t" \
  "v_mfma_f32_16x16x32_bf16 v[180:183], v[212:215], v[112:115], v[180:183]\n\t" \
  "ds_read_b64_tr_b16 v[210:211], v249 offset:26136\n\t" \
  "v_and_b32 v224, s56, v224\n\t" \
  "v_lshlrev_b32_sdwa v227, 2, v226 dst_sel:DWORD dst_unused:UNUSED_PAD src0_sel:DWORD src1_sel:BYTE_3\n\t" \
  "v_mfma_f32_16x16x32_bf16 v[188:191], v[212:215], v[116:119], v[188:191]\n\t" \
  "s_cmp_eq_u32 s37, 0\n\t" \
  "s_cbranch_scc1 .Lcnx_f6\n.Lcnx_g6:\n\t" \
  "ds_read_b32 v221, v221 offset:0\n\t" \
  "ds_read_b32 v220, v220 offset:0\n\t" \
  "v_lshlrev_b32_sdwa v226, 2, v226 dst_sel:DWORD dst_unused:UNUSED_PAD src0_sel:DWORD src1_sel:BYTE_1\n\t" \
  "v_fma_f32 v165, |v165|, v229, v165 mul:4\n\t" \
  "v_mfma_f32_16x16x32_bf16 v[196:199], v[212:215], v[120:123], v[196:199]\n\t" \
  "ds_read_b32 v223, v223 offset:15376\n\t" \
  "ds_read_b32 v222, v222 offset:15376\n\t" \
  "v_fma_f32 v164, |v164|, v228, v164 mul:4\n\t" \
  "v_fma_mix_f32 v231, |v167|, v231, v231 op_sel:[0,1,0] op_sel_hi:[0,1,1] clamp\n\t" \
  "v_mfma_f32_16x16x32_bf16 v[204:207], v[212:215], v[124:127], v[204:207]\n\t" \
  "ds_read_b64_tr_b16 v[212:213], v249 offset:24608\n\t" \
  "ds_read_b64_tr_b16 v[214:215], v249 offset:26144\n\t" \
  "v_fma_mix_f32 v230, |v166|, v230, v230 op_sel:[0,1,0] op_sel_hi:[0,1,1] clamp\n\t" \
  "s_waitcnt lgkmcnt(8)\n\t" \
  "v_fma_f32 v173, |v173|, v233, v173 mul:4\n\t" \
  "v_mfma_f32_16x16x32_bf16 v[180:183], v[216:219], v[128:131], v[180:183]\n\t" \
  "ds_read_b32 v225, v225 offset:0\n\t" \
  "v_fma_f32 v172, |v172|, v232, v172 mul:4\n\t" \
  "v_fma_f32 v175, |v175|, v235, v175 mul:4\n\t" \
  "v_mfma_f32_16x16x32_bf16 v[188:191], v[216:219], v[132:135], v[188:191]\n\t" \
  "ds_read_b32 v224, v224 offset:0\n\t" \
  "v_fma_f32 v174, |v174|, v234, v174 mul:4\n\t" \
  "v_fma_f32 v167, |v167|, v231, v167 mul:4\n\t" \
  "v_mfma_f32_16x16x32_bf16 v[196:199], v[216:219], v[136:139], v[196:199]\n\t" \
  "ds_read_b32 v227, v227 offset:15376\n\t" \
  "v_fma_f32 v166, |v166|, v230, v166 mul:4\n\t" \
  "v_cvt_pk_bf16_f32 v164, v164, v165\n\t" \
  "v_mfma_f32_16x16x32_bf16 v[204:207], v[216:219], v[140:143], v[204:207]\n\t" \
  "ds_read_b64_tr_b16 v[216:217], v249 offset:24664\n\t" \
  "v_cvt_pk_bf16_f32 v165, v166, v167\n\t" \
  "v_cvt_pk_bf16_f32 v172, v172, v173\n\t" \
  "v_cvt_pk_bf16_f32 v173, v174, v175\n\t" \
  "s_setprio 0\n\t" \
  "v_cvt_pk_f16_f32 v228, |v194|, |v195| clamp\n\t" \
  "v_cvt_pknorm_i16_f32 v230, |v192|, |v193|\n\t" \
  "v_mfma_f32_16x16x32_bf16 v[32:35], v[242:245], v[236:239], v[32:35]\n\t" \
  "ds_read_b64_tr_b16 v[218:219], v249 offset:26200\n\t" \
  "v_cvt_pk_f16_f32 v232, |v202|, |v203| clamp\n\t" \
  "v_cvt_pk_f16_f32 v234, |v200|, |v201| clamp\n\t" \
  "v_mfma_f32_16x16x32_bf16 v[36:39], v[240:243], v[236:239], v[36:39]\n\t" \
  "buffer_load_dwordx4 v[236:239], v250, s[8:11], s29 offen offset:128\n\t" \
  "s_cmp_lg_u32 s58, 0\n\t" \
  "s_cbranch_scc1 .Lcnx_c25\n\t" \
  "s_add_u32 m0, s25, 32800\n\t" \
  "s_add_u32 s24, s24, s26\n\t" \
  "buffer_load_dwordx4 v254, s[8:11], s24 offen lds\n\t" \
  "s_cmp_lg_u32 s38, 6\n\t" \
  "s_cbranch_scc1 .Lcnx_c25\n\t" \
  "s_mov_b32 m0, 163632\n\t" \
  "s_add_u32 s30, s30, s31\n\t" \
  "buffer_load_dword v252, s[8:11], s30 offen lds\n\t" \
  ".Lcnx_c25:\n\t" \
  "ds_read_b32 v226, v226 offset:15376\n\t" \
  "v_and_b32_sdwa v229, s56, v228 dst_sel:DWORD dst_unused:UNUSED_PAD src0_sel:DWORD src1_sel:WORD_1\n\t" \
  "v_and_b32 v228, s56, v228\n\t" \
  "s_waitcnt lgkmcnt(12)\n\t" \
  "v_mfma_f32_16x16x32_bf16 v[0:3], v[208:211], v[146:149], v[0:3]\n\t" \
  "ds_read_b32 v229, v229 offset:0\n\t" \
  "v_lshlrev_b32_sdwa v231, 2, v230 dst_sel:DWORD dst_unused:UNUSED_PAD src0_sel:DWORD src1_sel:BYTE_3\n\t" \
  "v_lshlrev_b32_sdwa v230, 2, v230 dst_sel:DWORD dst_unused:UNUSED_PAD src0_sel:DWORD src1_sel:BYTE_1\n\t" \
  "v_mfma_f32_16x16x32_bf16 v[24:27], v[208:211], v[154:157], v[24:27]\n\t" \
  "ds_read_b32 v228, v228 offset:0\n\t" \
  "v_and_b32_sdwa v233, s56, v232 dst_sel:DWORD dst_unused:UNUSED_PAD src0_sel:DWORD src1_sel:WORD_1\n\t" \
  "v_and_b32 v232, s56, v232\n\t" \
  "v_mfma_f32_16x16x32_bf16 v[48:51], v[208:211], v[162:165], v[48:51]\n\t" \
  "ds_read_b32 v231, v231 offset:15376\n\t" \
  "v_and_b32_sdwa v235, s56, v234 dst_sel:DWORD dst_unused:UNUSED_PAD src0_sel:DWORD src1_sel:WORD_1\n\t" \
  "v_and_b32 v234, s56, v234\n\t" \
  "v_mfma_f32_16x16x32_bf16 v[72:75], v[208:211], v[170:173], v[72:75]\n\t" \
  "ds_read_b64_tr_b16 v[208:209], v249 offset:24672\n\t" \
  "ds_read_b64_tr_b16 v[210:211], v249 offset:26208\n\t" \
  "s_waitcnt lgkmcnt(13)\n\t" \
  "v_fma_f32 v179, |v179|, v221, v179 mul:4\n\t" \
  "v_fma_f32 v178, |v178|, v220, v178 mul:4\n\t" \
  "s_waitcnt lgkmcnt(11)\n\t" \
  "v_mfma_f32_16x16x32_bf16 v[4:7], v[212:215], v[146:149], v[4:7]\n\t" \
  "ds_read_b32 v230, v230 offset:15376\n\t" \
  "v_fma_mix_f32 v223, |v177|, v223, v223 op_sel:[0,1,0] op_sel_hi:[0,1,1] clamp\n\t" \
  "v_fma_mix_f32 v222, |v176|, v222, v222 op_sel:[0,1,0] op_sel_hi:[0,1,1] clamp\n\t" \
  "v_mfma_f32_16x16x32_bf16 v[28:31], v[212:215], v[154:157], v[28:31]\n\t" \
  "ds_read_b32 v233, v233 offset:0\n\t" \
  "s_waitcnt lgkmcnt(7)\n\t" \
  "v_fma_f32 v187, |v187|, v225, v187 mul:4\n\t" \
  "v_fma_f32 v186, |v186|, v224, v186 mul:4\n\t" \
  "v_mfma_f32_16x16x32_bf16 v[52:55], v[212:215], v[162:165], v[52:55]\n\t" \
  "ds_read_b32 v232, v232 offset:0\n\t" \
  "v_fma_mix_f32 v227, |v185|, v227, v227 op_sel:[0,1,0] op_sel_hi:[0,1,1] clamp\n\t" \
  "v_fma_mix_f32 v226, |v184|, v226, v226 op_sel:[0,1,0] op_sel_hi:[0,1,1] clamp\n\t" \
  "v_fma_f32 v177, |v177|, v223, v177 mul:4\n\t" \
  "v_mfma_f32_16x16x32_bf16 v[76:79], v[212:215], v[170:173], v[76:79]\n\t" \
  "ds_read_b64_tr_b16 v[212:213], v249 offset:24728\n\t" \
  "v_fma_f32 v176, |v176|, v222, v176 mul:4\n\t" \
  "v_fma_f32 v185, |v185|, v227, v185 mul:4\n\t" \
  "v_mfma_f32_16x16x32_bf16 v[8:11], v[216:219], v[146:149], v[8:11]\n\t" \
  "ds_read_b64_tr_b16 v[214:215], v249 offset:26264\n\t" \
  "v_fma_f32 v184, |v184|, v226, v184 mul:4\n\t" \
  "v_cvt_pk_bf16_f32 v179, v178, v179\n\t" \
  "v_mfma_f32_16x16x32_bf16 v[32:35], v[216:219], v[154:157], v[32:35]\n\t" \
  "ds_read_b32 v235, v235 offset:0\n\t" \
  "v_cvt_pk_bf16_f32 v178, v176, v177\n\t" \
  "v_cvt_pk_bf16_f32 v187, v186, v187\n\t" \
  "v_mfma_f32_16x16x32_bf16 v[56:59], v[216:219], v[162:165], v[56:59]\n\t" \
  "ds_read_b32 v234, v234 offset:0\n\t" \
  "v_cvt_pk_bf16_f32 v186, v184, v185\n\t" \
  "s_setprio 1\n\t" \
  "v_cvt_pk_f16_f32 v220, |v180|, |v181| clamp\n\t" \
  "v_mfma_f32_16x16x32_bf16 v[80:83], v[216:219], v[170:173], v[80:83]\n\t" \
  "ds_read_b64_tr_b16 v[216:217], v249 offset:24736\n\t" \
  "v_cvt_pknorm_i16_f32 v222, |v182|, |v183|\n\t" \
  "v_cvt_pk_f16_f32 v224, |v188|, |v189| clamp\n\t" \
  "s_waitcnt lgkmcnt(8)\n\t" \
  "v_mfma_f32_16x16x32_bf16 v[12:15], v[208:211], v[146:149], v[12:15]\n\t" \
  "ds_read_b64_tr_b16 v[218:219], v249 offset:26272\n\t" \
  "v_cvt_pknorm_i16_f32 v226, |v190|, |v191|\n\t" \
  "v_and_b32_sdwa v221, s56, v220 dst_sel:DWORD dst_unused:UNUSED_PAD src0_sel:DWORD src1_sel:WORD_1\n\t" \
  "v_mfma_f32_16x16x32_bf16 v[36:39], v[208:211], v[154:157], v[36:39]\n\t" \
  "v_and_b32 v220, s56, v220\n\t" \
  "v_lshlrev_b32_sdwa v223, 2, v222 dst_sel:DWORD dst_unused:UNUSED_PAD src0_sel:DWORD src1_sel:BYTE_3\n\t" \
  "v_mfma_f32_16x16x32_bf16 v[60:63], v[208:211], v[162:165], v[60:63]\n\t" \
  "ds_read_b32 v221, v221 offset:0\n\t" \
  "v_lshlrev_b32_sdwa v222, 2, v222 dst_sel:DWORD dst_unused:UNUSED_PAD src0_sel:DWORD src1_sel:BYTE_1\n\t" \
  "v_and_b32_sdwa v225, s56, v224 dst_sel:DWORD dst_unused:UNUSED_PAD src0_sel:DWORD src1_sel:WORD_1\n\t" \
  "v_and_b32 v224, s56, v224\n\t" \
  "v_mfma_f32_16x16x32_bf16 v[84:87], v[208:211], v[170:173], v[84:87]\n\t" \
  "ds_read_b64_tr_b16 v[208:209], v248 offset:36904\n\t" \
  "ds_read_b64_tr_b16 v[210:211], v248 offset:37928\n\t" \
  "v_lshlrev_b32_sdwa v227, 2, v226 dst_sel:DWORD dst_unused:UNUSED_PAD src0_sel:DWORD src1_sel:BYTE_3\n\t" \
  "v_lshlrev_b32_sdwa v226, 2, v226 dst_sel:DWORD dst_unused:UNUSED_PAD src0_sel:DWORD src1_sel:BYTE_1\n\t" \
  "s_waitcnt lgkmcnt(7)\n\t" \
  "v_mfma_f32_16x16x32_bf16 v[16:19], v[212:215], v[146:149], v[16:19]\n\t" \
  "ds_read_b32 v220, v220 offset:0\n\t" \
  "ds_read_b32 v223, v223 offset:15376\n\t" \
  "v_fma_f32 v195, |v195|, v229, v195 mul:4\n\t" \
  "v_fma_f32 v194, |v194|, v228, v194 mul:4\n\t" \
  "v_mfma_f32_16x16x32_bf16 v[40:43], v[212:215], v[154:157], v[40:43]\n\t" \
  "ds_read_b32 v222, v222 offset:15376\n\t" \
  "v_fma_mix_f32 v231, |v193|, v231, v231 op_sel:[0,1,0] op_sel_hi:[0,1,1] clamp\n\t" \
  "v_fma_mix_f32 v230, |v192|, v230, v230 op_sel:[0,1,0] op_sel_hi:[0,1,1] clamp\n\t" \
  "v_mfma_f32_16x16x32_bf16 v[64:67], v[212:215], v[162:165], v[64:67]\n\t" \
  "ds_read_b32 v225, v225 offset:0\n\t" \
  "s_waitcnt lgkmcnt(9)\n\t" \
  "v_fma_f32 v203, |v203|, v233, v203 mul:4\n\t" \
  "v_fma_f32 v202, |v202|, v232, v202 mul:4\n\t" \
  "v_mfma_f32_16x16x32_bf16 v[88:91], v[212:215], v[170:173], v[88:91]\n\t" \
  "ds_read_b64_tr_b16 v[212:213], v248 offset:39976\n\t" \
  "v_fma_f32 v201, |v201|, v235, v201 mul:4\n\t" \
  "v_fma_f32 v200, |v200|, v234, v200 mul:4\n\t" \
  "s_waitcnt lgkmcnt(8)\n\t" \
  "v_mfma_f32_16x16x32_bf16 v[20:23], v[216:219], v[146:149], v[20:23]\n\t" \
  "ds_read_b64_tr_b16 v[214:215], v248 offset:41000\n\t" \
  "v_fma_f32 v193, |v193|, v231, v193 mul:4\n\t" \
  "v_fma_f32 v192, |v192|, v230, v192 mul:4\n\t" \
  "v_mfma_f32_16x16x32_bf16 v[44:47], v[216:219], v[154:157], v[44:47]\n\t" \
  "ds_read_b32 v224, v224 offset:0\n\t" \
  "v_cvt_pk_bf16_f32 v195, v194, v195\n\t" \
  "v_cvt_pk_bf16_f32 v194, v192, v193\n\t" \
  "v_mfma_f32_16x16x32_bf16 v[68:71], v[216:219], v[162:165], v[68:71]\n\t" \
  "ds_read_b32 v227, v227 offset:15376\n\t" \
  "v_cvt_pk_bf16_f32 v203, v202, v203\n\t" \
  "v_cvt_pk_bf16_f32 v202, v200, v201\n\t" \
  "v_cvt_pk_f16_f32 v228, |v196|, |v197| clamp\n\t" \
  "v_mfma_f32_16x16x32_bf16 v[92:95], v[216:219], v[170:173], v[92:95]\n\t" \
  "ds_read_b64_tr_b16 v[216:217], v248 offset:38952\n\t" \
  "v_cvt_pknorm_i16_f32 v230, |v198|, |v199|\n\t" \
  "s_setprio 2\n\t" \
  "v_cvt_pk_f16_f32 v232, |v204|, |v205| clamp\n\t" \
  "s_waitcnt lgkmcnt(9)\n\t" \
  "v_mfma_f32_16x16x32_bf16 v[144:147], v[208:211], v[96:99], 0\n\t" \
  "ds_read_b64_tr_b16 v[218:219], v248 offset:42024\n\t" \
  "v_cvt_pk_f16_f32 v234, |v206|, |v207| clamp\n\t" \
  "v_and_b32_sdwa v229, s56, v228 dst_sel:DWORD dst_unused:UNUSED_PAD src0_sel:DWORD src1_sel:WORD_1\n\t" \
  "v_mfma_f32_16x16x32_bf16 v[152:155], v[208:211], v[100:103], 0\n\t" \
  "ds_read_b32 v226, v226 offset:15376\n\t" \
  "v_and_b32 v228, s56, v228\n\t" \
  "v_lshlrev_b32_sdwa v231, 2, v230 dst_sel:DWORD dst_unused:UNUSED_PAD src0_sel:DWORD src1_sel:BYTE_3\n\t" \
  "v_mfma_f32_16x16x32_bf16 v[160:163], v[208:211], v[104:107], 0\n\t" \
  "ds_read_b32 v229, v229 offset:0\n\t" \
  "v_lshlrev_b32_sdwa v230, 2, v230 dst_sel:DWORD dst_unused:UNUSED_PAD src0_sel:DWORD src1_sel:BYTE_1\n\t" \
  "v_and_b32_sdwa v233, s56, v232 dst_sel:DWORD dst_unused:UNUSED_PAD src0_sel:DWORD src1_sel:WORD_1\n\t" \
  "v_mfma_f32_16x16x32_bf16 v[168:171], v[208:211], v[108:111], 0\n\t" \
  "ds_read_b64_tr_b16 v[208:209], v248 offset:37416\n\t" \
  "v_and_b32 v232, s56, v232\n\t" \
  "v_and_b32_sdwa v235, s56, v234 dst_sel:DWORD dst_unused:UNUSED_PAD src0_sel:DWORD src1_sel:WORD_1\n\t" \
  "s_waitcnt lgkmcnt(7)\n\t" \
  "v_mfma_f32_16x16x32_bf16 v[144:147], v[212:215], v[112:115], v[144:147]\n\t" \
  "ds_read_b64_tr_b16 v[210:211], v248 offset:38440\n\t" \
  "ds_read_b32 v228, v228 offset:0\n\t" \
  "v_and_b32 v234, s56, v234\n\t" \
  "v_fma_f32 v181, |v181|, v221, v181 mul:4\n\t" \
  "v_mfma_f32_16x16x32_bf16 v[152:155], v[212:215], v[116:119], v[152:155]\n\t" \
  "ds_read_b32 v231, v231 offset:15376\n\t" \
  "ds_read_b32 v230, v230 offset:15376\n\t" \
  "v_fma_f32 v180, |v180|, v220, v180 mul:4\n\t" \
  "v_fma_mix_f32 v223, |v183|, v223, v223 op_sel:[0,1,0] op_sel_hi:[0,1,1] clamp\n\t" \
  "v_mfma_f32_16x16x32_bf16 v[160:163], v[212:215], v[120:123], v[160:163]\n\t" \
  "ds_read_b32 v233, v233 offset:0\n\t" \
  "v_fma_mix_f32 v222, |v182|, v222, v222 op_sel:[0,1,0] op_sel_hi:[0,1,1] clamp\n\t" \
  "s_waitcnt lgkmcnt(7)\n\t" \
  "v_fma_f32 v189, |v189|, v225, v189 mul:4\n\t" \
  "v_mfma_f32_16x16x32_bf16 v[168:171], v[212:215], v[124:127], v[168:171]\n\t" \
  "ds_read_b64_tr_b16 v[212:213], v248 offset:40488\n\t" \
  "v_fma_f32 v188, |v188|, v224, v188 mul:4\n\t" \
  "v_fma_mix_f32 v227, |v191|, v227, v227 op_sel:[0,1,0] op_sel_hi:[0,1,1] clamp\n\t" \
  "v_fma_mix_f32 v226, |v190|, v226, v226 op_sel:[0,1,0] op_sel_hi:[0,1,1] clamp\n\t" \
  "v_mfma_f32_16x16x32_bf16 v[144:147], v[216:219], v[128:131], v[144:147]\n\t" \
  "ds_read_b64_tr_b16 v[214:215], v248 offset:41512\n\t" \
  "v_fma_f32 v183, |v183|, v223, v183 mul:4\n\t" \
  "v_fma_f32 v182, |v182|, v222, v182 mul:4\n\t" \
  "v_mfma_f32_16x16x32_bf16 v[152:155], v[216:219], v[132:135], v[152:155]\n\t" \
  "ds_read_b32 v232, v232 offset:0\n\t" \
  "v_fma_f32 v191, |v191|, v227, v191 mul:4\n\t" \
  "v_fma_f32 v190, |v190|, v226, v190 mul:4\n\t" \
  "v_mfma_f32_16x16x32_bf16 v[160:163], v[216:219], v[136:139], v[160:163]\n\t" \
  "ds_read_b32 v235, v235 offset:0\n\t" \
  "v_cvt_pk_bf16_f32 v180, v180, v181\n\t" \
  "v_cvt_pk_bf16_f32 v181, v182, v183\n\t" \
  "v_mfma_f32_16x16x32_bf16 v[168:171], v[216:219], v[140:143], v[168:171]\n\t" \
  "ds_read_b64_tr_b16 v[216:217], v248 offset:39464\n\t" \
  "v_cvt_pk_bf16_f32 v188, v188, v189\n\t" \
  "v_cvt_pk_bf16_f32 v189, v190, v191\n\t" \
  "s_waitcnt lgkmcnt(9)\n\t" \
  "v_mfma_f32_16x16x32_bf16 v[148:151], v[208:211], v[96:99], 0\n\t" \
  "ds_read_b64_tr_b16 v[218:219], v248 offset:42536\n\t" \
  "v_cvt_pk_f16_f32 v220, |v146|, |v147| clamp\n\t" \
  "s_setprio 3\n\t" \
  "v_cvt_pknorm_i16_f32 v222, |v144|, |v145|\n\t" \
  "v_mfma_f32_16x16x32_bf16 v[156:159], v[208:211], v[100:103], 0\n\t" \
  "s_waitcnt vmcnt(1)\n\t" \
  "s_cmp_lg_u32 s58, 1\n\t" \
  "s_cbranch_scc1 .Lcnx_c13\n\t" \
  "s_waitcnt vmcnt(0)\n\t" \
  ".Lcnx_c13:\n\t" \
  "s_mov_b64 exec, 3\n\t" \
  "ds_add_rtn_u32 v246, v251, v250 offset:47184\n\t" \
  "s_mov_b64 exec, -1\n\t" \
  "ds_read_b32 v234, v234 offset:0\n\t" \
  "v_cvt_pk_f16_f32 v224, |v154|, |v155| clamp\n\t" \
  "v_cvt_pknorm_i16_f32 v226, |v152|, |v153|\n\t" \
  "v_mfma_f32_16x16x32_bf16 v[164:167], v[208:211], v[104:107], 0\n\t" \
  "v_and_b32_sdwa v221, s56, v220 dst_sel:DWORD dst_unused:UNUSED_PAD src0_sel:DWORD src1_sel:WORD_1\n\t" \
  "v_and_b32 v220, s56, v220\n\t" \
  "v_mfma_f32_16x16x32_bf16 v[172:175], v[208:211], v[108:111], 0\n\t" \
  "s_waitcnt lgkmcnt(1)\n\t" \
  "v_readfirstlane_b32 s4, v246\n\t" \
  "s_cmp_lt_u32 s4, s57\n\t" \
  "s_cbranch_scc1 .Lcnx_w72\n.Lcnx_b72:\n\t" \
  "ds_read_b64_tr_b16 v[208:209], v249 offset:30752\n\t" \
  "v_lshlrev_b32_sdwa v223, 2, v222 dst_sel:DWORD dst_unused:UNUSED_PAD src0_sel:DWORD src1_sel:BYTE_3\n\t" \
  "v_lshlrev_b32_sdwa v222, 2, v222 dst_sel:DWORD dst_unused:UNUSED_PAD src0_sel:DWORD src1_sel:BYTE_1\n\t" \
  "v_and_b32_sdwa v225, s56, v224 dst_sel:DWORD dst_unused:UNUSED_PAD src0_sel:DWORD src1_sel:WORD_1\n\t" \
  "v_mfma_f32_16x16x32_bf16 v[148:151], v[212:215], v[112:115], v[148:151]\n\t" \
  "ds_read_b64_tr_b16 v[210:211], v249 offset:32288\n\t" \
  "v_and_b32 v224, s56, v224\n\t" \
  "v_lshlrev_b32_sdwa v227, 2, v226 dst_sel:DWORD dst_unused:UNUSED_PAD src0_sel:DWORD src1_sel:BYTE_3\n\t" \
  "v_mfma_f32_16x16x32_bf16 v[156:159], v[212:215], v[116:119], v[156:159]\n\t" \
  "s_cmp_eq_u32 s37, 0\n\t" \
  "s_cbranch_scc1 .Lcnx_f7\n.Lcnx_g7:\n\t" \
  "ds_read_b32 v221, v221 offset:0\n\t" \
  "ds_read_b32 v220, v220 offset:0\n\t" \
  "v_lshlrev_b32_sdwa v226, 2, v226 dst_sel:DWORD dst_unused:UNUSED_PAD src0_sel:DWORD src1_sel:BYTE_1\n\t" \
  "v_fma_f32 v197, |v197|, v229, v197 mul:4\n\t" \
  "v_mfma_f32_16x16x32_bf16 v[164:167], v[212:215], v[120:123], v[164:167]\n\t" \
  "ds_read_b32 v223, v223 offset:15376\n\t" \
  "ds_read_b32 v222, v222 offset:15376\n\t" \
  "v_fma_f32 v196, |v196|, v228, v196 mul:4\n\t" \
  "v_fma_mix_f32 v231, |v199|, v231, v231 op_sel:[0,1,0] op_sel_hi:[0,1,1] clamp\n\t" \
  "v_mfma_f32_16x16x32_bf16 v[172:175], v[212:215], v[124:127], v[172:175]\n\t" \
  "ds_read_b64_tr_b16 v[212:213], v249 offset:30760\n\t" \
  "ds_read_b64_tr_b16 v[214:215], v249 offset:32296\n\t" \
  "v_fma_mix_f32 v230, |v198|, v230, v230 op_sel:[0,1,0] op_sel_hi:[0,1,1] clamp\n\t" \
  "s_waitcnt lgkmcnt(8)\n\t" \
  "v_fma_f32 v205, |v205|, v233, v205 mul:4\n\t" \
  "v_mfma_f32_16x16x32_bf16 v[148:151], v[216:219], v[128:131], v[148:151]\n\t" \
  "ds_read_b32 v225, v225 offset:0\n\t" \
  "v_fma_f32 v204, |v204|, v232, v204 mul:4\n\t" \
  "v_fma_f32 v207, |v207|, v235, v207 mul:4\n\t" \
  "v_mfma_f32_16x16x32_bf16 v[156:159], v[216:219], v[132:135], v[156:159]\n\t" \
  "ds_read_b32 v224, v224 offset:0\n\t" \
  "v_fma_f32 v206, |v206|, v234, v206 mul:4\n\t" \
  "v_fma_f32 v199, |v199|, v231, v199 mul:4\n\t" \
  "v_mfma_f32_16x16x32_bf16 v[164:167], v[216:219], v[136:139], v[164:167]\n\t" \
  "ds_read_b32 v227, v227 offset:15376\n\t" \
  "v_fma_f32 v198, |v198|, v230, v198 mul:4\n\t" \
  "v_cvt_pk_bf16_f32 v196, v196, v197\n\t" \
  "v_mfma_f32_16x16x32_bf16 v[172:175], v[216:219], v[140:143], v[172:175]\n\t" \
  "ds_read_b64_tr_b16 v[216:217], v249 offset:30816\n\t" \
  "v_cvt_pk_bf16_f32 v197, v198, v199\n\t" \
  "v_cvt_pk_bf16_f32 v204, v204, v205\n\t" \
  "v_cvt_pk_bf16_f32 v205, v206, v207\n\t" \
  "s_setprio 0\n\t" \
  "v_cvt_pk_f16_f32 v228, |v162|, |v163| clamp\n\t" \
  "v_cvt_pknorm_i16_f32 v230, |v160|, |v161|\n\t" \
  "v_mfma_f32_16x16x32_bf16 v[40:43], v[242:245], v[236:239], v[40:43]\n\t" \
  "ds_read_b64_tr_b16 v[218:219], v249 offset:32352\n\t" \
  "v_cvt_pk_f16_f32 v232, |v170|, |v171| clamp\n\t" \
  "v_cvt_pk_f16_f32 v234, |v168|, |v169| clamp\n\t" \
  "v_mfma_f32_16x16x32_bf16 v[44:47], v[240:243], v[236:239], v[44:47]\n\t" \
  "buffer_load_dwordx4 v[236:239], v250, s[8:11], s12 offen offset:0\n\t" \
  "s_cmp_lg_u32 s58, 0\n\t" \
  "s_cbranch_scc1 .Lcnx_c26\n\t" \
  "s_add_u32 m0, s25, 41000\n\t" \
  "s_add_u32 s24, s24, s26\n\t" \
  "buffer_load_dwordx4 v254, s[8:11], s24 offen lds\n\t" \
  "s_cmp_lg_u32 s38, 7\n\t" \
  "s_cbranch_scc1 .Lcnx_c26\n\t" \
  "s_mov_b32 m0, 163632\n\t" \
  "s_add_u32 s30, s30, s31\n\t" \
  "buffer_load_dword v252, s[8:11], s30 offen lds\n\t" \
  ".Lcnx_c26:\n\t" \
  "ds_read_b32 v226, v226 offset:15376\n\t" \
  "v_and_b32_sdwa v229, s56, v228 dst_sel:DWORD dst_unused:UNUSED_PAD src0_sel:DWORD src1_sel:WORD_1\n\t" \
  "v_and_b32 v228, s56, v228\n\t" \
  "s_waitcnt lgkmcnt(12)\n\t" \
  "v_mfma_f32_16x16x32_bf16 v[0:3], v[208:211], v[178:181], v[0:3]\n\t" \
  "ds_read_b32 v229, v229 offset:0\n\t" \
  "v_lshlrev_b32_sdwa v231, 2, v230 dst_sel:DWORD dst_unused:UNUSED_PAD src0_sel:DWORD src1_sel:BYTE_3\n\t" \
  "v_lshlrev_b32_sdwa v230, 2, v230 dst_sel:DWORD dst_unused:UNUSED_PAD src0_sel:DWORD src1_sel:BYTE_1\n\t" \
  "v_mfma_f32_16x16x32_bf16 v[24:27], v[208:211], v[186:189], v[24:27]\n\t" \
  "ds_read_b32 v228, v228 offset:0\n\t" \
  "v_and_b32_sdwa v233, s56, v232 dst_sel:DWORD dst_unused:UNUSED_PAD src0_sel:DWORD src1_sel:WORD_1\n\t" \
  "v_and_b32 v232, s56, v232\n\t" \
  "v_mfma_f32_16x16x32_bf16 v[48:51], v[208:211], v[194:197], v[48:51]\n\t" \
  "ds_read_b32 v231, v231 offset:15376\n\t" \
  "v_and_b32_sdwa v235, s56, v234 dst_sel:DWORD dst_unused:UNUSED_PAD src0_sel:DWORD src1_sel:WORD_1\n\t" \
  "v_and_b32 v234, s56, v234\n\t" \
  "v_mfma_f32_16x16x32_bf16 v[72:75], v[208:211], v[202:205], v[72:75]\n\t" \
  "ds_read_b64_tr_b16 v[208:209], v249 offset:30824\n\t" \
  "ds_read_b64_tr_b16 v[210:211], v249 offset:32360\n\t" \
  "s_waitcnt lgkmcnt(13)\n\t" \
  "v_fma_f32 v147, |v147|, v221, v147 mul:4\n\t" \
  "v_fma_f32 v146, |v146|, v220, v146 mul:4\n\t" \
  "s_waitcnt lgkmcnt(11)\n\t" \
  "v_mfma_f32_16x16x32_bf16 v[4:7], v[212:215], v[178:181], v[4:7]\n\t" \
  "ds_read_b32 v230, v230 offset:15376\n\t" \
  "v_fma_mix_f32 v223, |v145|, v223, v223 op_sel:[0,1,0] op_sel_hi:[0,1,1] clamp\n\t" \
  "v_fma_mix_f32 v222, |v144|, v222, v222 op_sel:[0,1,0] op_sel_hi:[0,1,1] clamp\n\t" \
  "v_mfma_f32_16x16x32_bf16 v[28:31], v[212:215], v[186:189], v[28:31]\n\t" \
  "ds_read_b32 v233, v233 offset:0\n\t" \
  "s_waitcnt lgkmcnt(7)\n\t" \
  "v_fma_f32 v155, |v155|, v225, v155 mul:4\n\t" \
  "v_fma_f32 v154, |v154|, v224, v154 mul:4\n\t" \
  "v_mfma_f32_16x16x32_bf16 v[52:55], v[212:215], v[194:197], v[52:55]\n\t" \
  "ds_read_b32 v232, v232 offset:0\n\t" \
  "v_fma_mix_f32 v227, |v153|, v227, v227 op_sel:[0,1,0] op_sel_hi:[0,1,1] clamp\n\t" \
  "v_fma_mix_f32 v226, |v152|, v226, v226 op_sel:[0,1,0] op_sel_hi:[0,1,1] clamp\n\t" \
  "v_fma_f32 v145, |v145|, v223, v145 mul:4\n\t" \
  "v_mfma_f32_16x16x32_bf16 v[76:79], v[212:215], v[202:205], v[76:79]\n\t" \
  "ds_read_b64_tr_b16 v[212:213], v249 offset:30880\n\t" \
  "v_fma_f32 v144, |v144|, v222, v144 mul:4\n\t" \
  "v_fma_f32 v153, |v153|, v227, v153 mul:4\n\t" \
  "v_mfma_f32_16x16x32_bf16 v[8:11], v[216:219], v[178:181], v[8:11]\n\t" \
  "ds_read_b64_tr_b16 v[214:215], v249 offset:32416\n\t" \
  "v_fma_f32 v152, |v152|, v226, v152 mul:4\n\t" \
  "v_cvt_pk_bf16_f32 v147, v146, v147\n\t" \
  "v_mfma_f32_16x16x32_bf16 v[32:35], v[216:219], v[186:189], v[32:35]\n\t" \
  "ds_read_b32 v235, v235 offset:0\n\t" \
  "v_cvt_pk_bf16_f32 v146, v144, v145\n\t" \
  "v_cvt_pk_bf16_f32 v155, v154, v155\n\t" \
  "v_mfma_f32_16x16x32_bf16 v[56:59], v[216:219], v[194:197], v[56:59]\n\t" \
  "ds_read_b32 v234, v234 offset:0\n\t" \
  "v_cvt_pk_bf16_f32 v154, v152, v153\n\t" \
  "s_setprio 1\n\t" \
  "v_cvt_pk_f16_f32 v220, |v148|, |v149| clamp\n\t" \
  "v_mfma_f32_16x16x32_bf16 v[80:83], v[216:219], v[202:205], v[80:83]\n\t" \
  "ds_read_b64_tr_b16 v[216:217], v249 offset:30888\n\t" \
  "v_cvt_pknorm_i16_f32 v222, |v150|, |v151|\n\t" \
  "v_cvt_pk_f16_f32 v224, |v156|, |v157| clamp\n\t" \
  "s_waitcnt lgkmcnt(8)\n\t" \
  "v_mfma_f32_16x16x32_bf16 v[12:15], v[208:211], v[178:181], v[12:15]\n\t" \
  "ds_read_b64_tr_b16 v[218:219], v249 offset:32424\n\t" \
  "v_cvt_pknorm_i16_f32 v226, |v158|, |v159|\n\t" \
  "v_and_b32_sdwa v221, s56, v220 dst_sel:DWORD dst_unused:UNUSED_PAD src0_sel:DWORD src1_sel:WORD_1\n\t" \
  "v_mfma_f32_16x16x32_bf16 v[36:39], v[208:211], v[186:189], v[36:39]\n\t" \
  "v_and_b32 v220, s56, v220\n\t" \
  "v_lshlrev_b32_sdwa v223, 2, v222 dst_sel:DWORD dst_unused:UNUSED_PAD src0_sel:DWORD src1_sel:BYTE_3\n\t" \
  "v_mfma_f32_16x16x32_bf16 v[60:63], v[208:211], v[194:197], v[60:63]\n\t" \
  "ds_read_b32 v221, v221 offset:0\n\t" \
  "v_lshlrev_b32_sdwa v222, 2, v222 dst_sel:DWORD dst_unused:UNUSED_PAD src0_sel:DWORD src1_sel:BYTE_1\n\t" \
  "v_and_b32_sdwa v225, s56, v224 dst_sel:DWORD dst_unused:UNUSED_PAD src0_sel:DWORD src1_sel:WORD_1\n\t" \
  "v_and_b32 v224, s56, v224\n\t" \
  "v_mfma_f32_16x16x32_bf16 v[84:87], v[208:211], v[202:205], v[84:87]\n\t" \
  "ds_read_b64_tr_b16 v[208:209], v248 offset:0\n\t" \
  "ds_read_b64_tr_b16 v[210:211], v248 offset:1024\n\t" \
  "v_lshlrev_b32_sdwa v227, 2, v226 dst_sel:DWORD dst_unused:UNUSED_PAD src0_sel:DWORD src1_sel:BYTE_3\n\t" \
  "v_lshlrev_b32_sdwa v226, 2, v226 dst_sel:DWORD dst_unused:UNUSED_PAD src0_sel:DWORD src1_sel:BYTE_1\n\t" \
  "s_waitcnt lgkmcnt(7)\n\t" \
  "v_mfma_f32_16x16x32_bf16 v[16:19], v[212:215], v[178:181], v[16:19]\n\t" \
  "ds_read_b32 v220, v220 offset:0\n\t" \
  "ds_read_b32 v223, v223 offset:15376\n\t" \
  "v_fma_f32 v163, |v163|, v229, v163 mul:4\n\t" \
  "v_fma_f32 v162, |v162|, v228, v162 mul:4\n\t" \
  "v_mfma_f32_16x16x32_bf16 v[40:43], v[212:215], v[186:189], v[40:43]\n\t" \
  "ds_read_b32 v222, v222 offset:15376\n\t" \
  "v_fma_mix_f32 v231, |v161|, v231, v231 op_sel:[0,1,0] op_sel_hi:[0,1,1] clamp\n\t" \
  "v_fma_mix_f32 v230, |v160|, v230, v230 op_sel:[0,1,0] op_sel_hi:[0,1,1] clamp\n\t" \
  "v_mfma_f32_16x16x32_bf16 v[64:67], v[212:215], v[194:197], v[64:67]\n\t" \
  "ds_read_b32 v225, v225 offset:0\n\t" \
  "s_waitcnt lgkmcnt(9)\n\t" \
  "v_fma_f32 v171, |v171|, v233, v171 mul:4\n\t" \
  "v_fma_f32 v170, |v170|, v232, v170 mul:4\n\t" \
  "v_mfma_f32_16x16x32_bf16 v[88:91], v[212:215], v[202:205], v[88:91]\n\t" \
  "ds_read_b64_tr_b16 v[212:213], v248 offset:3072\n\t" \
  "v_fma_f32 v169, |v169|, v235, v169 mul:4\n\t" \
  "v_fma_f32 v168, |v168|, v234, v168 mul:4\n\t" \
  "s_waitcnt lgkmcnt(8)\n\t" \
  "v_mfma_f32_16x16x32_bf16 v[20:23], v[216:219], v[178:181], v[20:23]\n\t" \
  "ds_read_b64_tr_b16 v[214:215], v248 offset:4096\n\t" \
  "v_fma_f32 v161, |v161|, v231, v161 mul:4\n\t" \
  "v_fma_f32 v160, |v160|, v230, v160 mul:4\n\t" \
  "v_mfma_f32_16x16x32_bf16 v[44:47], v[216:219], v[186:189], v[44:47]\n\t" \
  "ds_read_b32 v224, v224 offset:0\n\t" \
  "v_cvt_pk_bf16_f32 v163, v162, v163\n\t" \
  "v_cvt_pk_bf16_f32 v162, v160, v161\n\t" \
  "v_mfma_f32_16x16x32_bf16 v[68:71], v[216:219], v[194:197], v[68:71]\n\t" \
  "ds_read_b32 v227, v227 offset:15376\n\t" \
  "v_cvt_pk_bf16_f32 v171, v170, v171\n\t" \
  "v_cvt_pk_bf16_f32 v170, v168, v169\n\t" \
  "v_cvt_pk_f16_f32 v228, |v164|, |v165| clamp\n\t" \
  "v_mfma_f32_16x16x32_bf16 v[92:95], v[216:219], v[202:205], v[92:95]\n\t" \
  "ds_read_b64_tr_b16 v[216:217], v248 offset:2048\n\t" \
  "v_cvt_pknorm_i16_f32 v230, |v166|, |v167|\n\t" \
  "s_setprio 2\n\t" \
  "v_cvt_pk_f16_f32 v232, |v172|, |v173| clamp\n\t" \
  "s_waitcnt lgkmcnt(9)\n\t" \
  "v_mfma_f32_16x16x32_bf16 v[176:179], v[208:211], v[96:99], 0\n\t" \
  "ds_read_b64_tr_b16 v[218:219], v248 offset:5120\n\t" \
  "v_cvt_pk_f16_f32 v234, |v174|, |v175| clamp\n\t" \
  "v_and_b32_sdwa v229, s56, v228 dst_sel:DWORD dst_unused:UNUSED_PAD src0_sel:DWORD src1_sel:WORD_1\n\t" \
  "v_mfma_f32_16x16x32_bf16 v[184:187], v[208:211], v[100:103], 0\n\t" \
  "ds_read_b32 v226, v226 offset:15376\n\t" \
  "v_and_b32 v228, s56, v228\n\t" \
  "v_lshlrev_b32_sdwa v231, 2, v230 dst_sel:DWORD dst_unused:UNUSED_PAD src0_sel:DWORD src1_sel:BYTE_3\n\t" \
  "v_mfma_f32_16x16x32_bf16 v[192:195], v[208:211], v[104:107], 0\n\t" \
  "ds_read_b32 v229, v229 offset:0\n\t" \
  "v_lshlrev_b32_sdwa v230, 2, v230 dst_sel:DWORD dst_unused:UNUSED_PAD src0_sel:DWORD src1_sel:BYTE_1\n\t" \
  "v_and_b32_sdwa v233, s56, v232 dst_sel:DWORD dst_unused:UNUSED_PAD src0_sel:DWORD src1_sel:WORD_1\n\t" \
  "v_mfma_f32_16x16x32_bf16 v[200:203], v[208:211], v[108:111], 0\n\t" \
  "ds_read_b64_tr_b16 v[208:209], v248 offset:512\n\t" \
  "v_and_b32 v232, s56, v232\n\t" \
  "v_and_b32_sdwa v235, s56, v234 dst_sel:DWORD dst_unused:UNUSED_PAD src0_sel:DWORD src1_sel:WORD_1\n\t" \
  "s_waitcnt lgkmcnt(7)\n\t" \
  "v_mfma_f32_16x16x32_bf16 v[176:179], v[212:215], v[112:115], v[176:179]\n\t" \
  "ds_read_b64_tr_b16 v[210:211], v248 offset:1536\n\t" \
  "ds_read_b32 v228, v228 offset:0\n\t" \
  "v_and_b32 v234, s56, v234\n\t" \
  "v_fma_f32 v149, |v149|, v221, v149 mul:4\n\t" \
  "v_mfma_f32_16x16x32_bf16 v[184:187], v[212:215], v[116:119], v[184:187]\n\t" \
  "ds_read_b32 v231, v231 offset:15376\n\t" \
  "ds_read_b32 v230, v230 offset:15376\n\t" \
  "v_fma_f32 v148, |v148|, v220, v148 mul:4\n\t" \
  "v_fma_mix_f32 v223, |v151|, v223, v223 op_sel:[0,1,0] op_sel_hi:[0,1,1] clamp\n\t" \
  "v_mfma_f32_16x16x32_bf16 v[192:195], v[212:215], v[120:123], v[192:195]\n\t" \
  "ds_read_b32 v233, v233 offset:0\n\t" \
  "v_fma_mix_f32 v222, |v150|, v222, v222 op_sel:[0,1,0] op_sel_hi:[0,1,1] clamp\n\t" \
  "s_waitcnt lgkmcnt(7)\n\t" \
  "v_fma_f32 v157, |v157|, v225, v157 mul:4\n\t" \
  "v_mfma_f32_16x16x32_bf16 v[200:203], v[212:215], v[124:127], v[200:203]\n\t" \
  "ds_read_b64_tr_b16 v[212:213], v248 offset:3584\n\t" \
  "v_fma_f32 v156, |v156|, v224, v156 mul:4\n\t" \
  "v_fma_mix_f32 v227, |v159|, v227, v227 op_sel:[0,1,0] op_sel_hi:[0,1,1] clamp\n\t" \
  "v_fma_mix_f32 v226, |v158|, v226, v226 op_sel:[0,1,0] op_sel_hi:[0,1,1] clamp\n\t" \
  "v_mfma_f32_16x16x32_bf16 v[176:179], v[216:219], v[128:131], v[176:179]\n\t" \
  "ds_read_b64_tr_b16 v[214:215], v248 offset:4608\n\t" \
  "v_fma_f32 v151, |v151|, v223, v151 mul:4\n\t" \
  "v_fma_f32 v150, |v150|, v222, v150 mul:4\n\t" \
  "v_mfma_f32_16x16x32_bf16 v[184:187], v[216:219], v[132:135], v[184:187]\n\t" \
  "ds_read_b32 v232, v232 offset:0\n\t" \
  "v_fma_f32 v159, |v159|, v227, v159 mul:4\n\t" \
  "v_fma_f32 v158, |v158|, v226, v158 mul:4\n\t" \
  "v_mfma_f32_16x16x32_bf16 v[192:195], v[216:219], v[136:139], v[192:195]\n\t" \
  "ds_read_b32 v235, v235 offset:0\n\t" \
  "v_cvt_pk_bf16_f32 v148, v148, v149\n\t" \
  "v_cvt_pk_bf16_f32 v149, v150, v151\n\t" \
  "v_mfma_f32_16x16x32_bf16 v[200:203], v[216:219], v[140:143], v[200:203]\n\t" \
  "ds_read_b64_tr_b16 v[216:217], v248 offset:2560\n\t" \
  "v_cvt_pk_bf16_f32 v156, v156, v157\n\t" \
  "v_cvt_pk_bf16_f32 v157, v158, v159\n\t" \
  "s_waitcnt lgkmcnt(9)\n\t" \
  "v_mfma_f32_16x16x32_bf16 v[180:183], v[208:211], v[96:99], 0\n\t" \
  "ds_read_b64_tr_b16 v[218:219], v248 offset:5632\n\t" \
  "v_cvt_pk_f16_f32 v220, |v178|, |v179| clamp\n\t" \
  "s_setprio 3\n\t" \
  "v_cvt_pknorm_i16_f32 v222, |v176|, |v177|\n\t" \
  "v_mfma_f32_16x16x32_bf16 v[188:191], v[208:211], v[100:103], 0\n\t" \
  "s_waitcnt vmcnt(1)\n\t" \
  "s_cmp_lg_u32 s58, 1\n\t" \
  "s_cbranch_scc1 .Lcnx_c14\n\t" \
  "s_waitcnt vmcnt(0)\n\t" \
  ".Lcnx_c14:\n\t" \
  "s_mov_b64 exec, 3\n\t" \
  "ds_add_rtn_u32 v246, v251, v250 offset:47192\n\t" \
  "s_mov_b64 exec, -1\n\t" \
  "ds_read_b32 v234, v234 offset:0\n\t" \
  "v_cvt_pk_f16_f32 v224, |v186|, |v187| clamp\n\t" \
  "v_cvt_pknorm_i16_f32 v226, |v184|, |v185|\n\t" \
  "v_mfma_f32_16x16x32_bf16 v[196:199], v[208:211], v[104:107], 0\n\t" \
  "v_and_b32_sdwa v221, s56, v220 dst_sel:DWORD dst_unused:UNUSED_PAD src0_sel:DWORD src1_sel:WORD_1\n\t" \
  "v_and_b32 v220, s56, v220\n\t" \
  "v_mfma_f32_16x16x32_bf16 v[204:207], v[208:211], v[108:111], 0\n\t" \
  "s_waitcnt lgkmcnt(1)\n\t" \
  "v_readfirstlane_b32 s4, v246\n\t" \
  "s_cmp_lt_u32 s4, s57\n\t" \
  "s_cbranch_scc1 .Lcnx_w84\n.Lcnx_b84:\n\t" \
  "s_or_b32 s37, s37, s58\n\t" \
  "ds_read_b64_tr_b16 v[208:209], v249 offset:6152\n\t" \
  "v_lshlrev_b32_sdwa v223, 2, v222 dst_sel:DWORD dst_unused:UNUSED_PAD src0_sel:DWORD src1_sel:BYTE_3\n\t" \
  "v_lshlrev_b32_sdwa v222, 2, v222 dst_sel:DWORD dst_unused:UNUSED_PAD src0_sel:DWORD src1_sel:BYTE_1\n\t" \
  "v_and_b32_sdwa v225, s56, v224 dst_sel:DWORD dst_unused:UNUSED_PAD src0_sel:DWORD src1_sel:WORD_1\n\t" \
  "v_mfma_f32_16x16x32_bf16 v[180:183], v[212:215], v[112:115], v[180:183]\n\t" \
  "ds_read_b64_tr_b16 v[210:211], v249 offset:7688\n\t" \
  "v_and_b32 v224, s56, v224\n\t" \
  "v_lshlrev_b32_sdwa v227, 2, v226 dst_sel:DWORD dst_unused:UNUSED_PAD src0_sel:DWORD src1_sel:BYTE_3\n\t" \
  "v_mfma_f32_16x16x32_bf16 v[188:191], v[212:215], v[116:119], v[188:191]\n\t" \
  "s_cmp_eq_u32 s37, 0\n\t" \
  "s_cbranch_scc1 .Lcnx_f8\n.Lcnx_g8:\n\t" \
  "ds_read_b32 v221, v221 offset:0\n\t" \
  "ds_read_b32 v220, v220 offset:0\n\t" \
  "v_lshlrev_b32_sdwa v226, 2, v226 dst_sel:DWORD dst_unused:UNUSED_PAD src0_sel:DWORD src1_sel:BYTE_1\n\t" \
  "v_fma_f32 v165, |v165|, v229, v165 mul:4\n\t" \
  "v_mfma_f32_16x16x32_bf16 v[196:199], v[212:215], v[120:123], v[196:199]\n\t" \
  "ds_read_b32 v223, v223 offset:15376\n\t" \
  "ds_read_b32 v222, v222 offset:15376\n\t" \
  "v_fma_f32 v164, |v164|, v228, v164 mul:4\n\t" \
  "v_fma_mix_f32 v231, |v167|, v231, v231 op_sel:[0,1,0] op_sel_hi:[0,1,1] clamp\n\t" \
  "v_mfma_f32_16x16x32_bf16 v[204:207], v[212:215], v[124:127], v[204:207]\n\t" \
  "ds_read_b64_tr_b16 v[212:213], v249 offset:6160\n\t" \
  "ds_read_b64_tr_b16 v[214:215], v249 offset:7696\n\t" \
  "v_fma_mix_f32 v230, |v166|, v230, v230 op_sel:[0,1,0] op_sel_hi:[0,1,1] clamp\n\t" \
  "s_waitcnt lgkmcnt(8)\n\t" \
  "v_fma_f32 v173, |v173|, v233, v173 mul:4\n\t" \
  "v_mfma_f32_16x16x32_bf16 v[180:183], v[216:219], v[128:131], v[180:183]\n\t" \
  "ds_read_b32 v225, v225 offset:0\n\t" \
  "v_fma_f32 v172, |v172|, v232, v172 mul:4\n\t" \
  "v_fma_f32 v175, |v175|, v235, v175 mul:4\n\t" \
  "v_mfma_f32_16x16x32_bf16 v[188:191], v[216:219], v[132:135], v[188:191]\n\t" \
  "ds_read_b32 v224, v224 offset:0\n\t" \
  "v_fma_f32 v174, |v174|, v234, v174 mul:4\n\t" \
  "v_fma_f32 v167, |v167|, v231, v167 mul:4\n\t" \
  "v_mfma_f32_16x16x32_bf16 v[196:199], v[216:219], v[136:139], v[196:199]\n\t" \
  "ds_read_b32 v227, v227 offset:15376\n\t" \
  "v_fma_f32 v166, |v166|, v230, v166 mul:4\n\t" \
  "v_cvt_pk_bf16_f32 v164, v164, v165\n\t" \
  "v_mfma_f32_16x16x32_bf16 v[204:207], v[216:219], v[140:143], v[204:207]\n\t" \
  "ds_read_b64_tr_b16 v[216:217], v249 offset:6216\n\t" \
  "v_cvt_pk_bf16_f32 v165, v166, v167\n\t" \
  "v_cvt_pk_bf16_f32 v172, v172, v173\n\t" \
  "v_cvt_pk_bf16_f32 v173, v174, v175\n\t" \
  "s_setprio 0\n\t" \
  "v_cvt_pk_f16_f32 v228, |v194|, |v195| clamp\n\t" \
  "v_cvt_pknorm_i16_f32 v230, |v192|, |v193|\n\t" \
  "v_mfma_f32_16x16x32_bf16 v[48:51], v[242:245], v[236:239], v[48:51]\n\t" \
  "ds_read_b64_tr_b16 v[218:219], v249 offset:7752\n\t" \
  "v_cvt_pk_f16_f32 v232, |v202|, |v203| clamp\n\t" \
  "v_cvt_pk_f16_f32 v234, |v200|, |v201| clamp\n\t" \
  "v_mfma_f32_16x16x32_bf16 v[52:55], v[240:243], v[236:239], v[52:55]\n\t" \
  "buffer_load_dwordx4 v[236:239], v250, s[8:11], s12 offen offset:64\n\t" \
  "s_cmp_lg_u32 s58, 0\n\t" \
  "s_cbranch_scc1 .Lcnx_c27\n\t" \
  "s_add_u32 m0, s25, 49200\n\t" \
  "s_add_u32 s24, s24, s26\n\t" \
  "buffer_load_dwordx4 v254, s[8:11], s24 offen lds\n\t" \
  "s_cmp_lg_u32 s38, 4\n\t" \
  "s_cbranch_scc1 .Lcnx_c27\n\t" \
  "s_mov_b32 m0, 163632\n\t" \
  "s_add_u32 s30, s30, s31\n\t" \
  "buffer_load_dword v252, s[8:11], s30 offen lds\n\t" \
  ".Lcnx_c27:\n\t" \
  "ds_read_b32 v226, v226 offset:15376\n\t" \
  "v_and_b32_sdwa v229, s56, v228 dst_sel:DWORD dst_unused:UNUSED_PAD src0_sel:DWORD src1_sel:WORD_1\n\t" \
  "v_and_b32 v228, s56, v228\n\t" \
  "s_waitcnt lgkmcnt(12)\n\t" \
  "v_mfma_f32_16x16x32_bf16 v[0:3], v[208:211], v[146:149], v[0:3]\n\t" \
  "ds_read_b32 v229, v229 offset:0\n\t" \
  "v_lshlrev_b32_sdwa v231, 2, v230 dst_sel:DWORD dst_unused:UNUSED_PAD src0_sel:DWORD src1_sel:BYTE_3\n\t" \
  "v_lshlrev_b32_sdwa v230, 2, v230 dst_sel:DWORD dst_unused:UNUSED_PAD src0_sel:DWORD src1_sel:BYTE_1\n\t" \
  "v_mfma_f32_16x16x32_bf16 v[24:27], v[208:211], v[154:157], v[24:27]\n\t" \
  "ds_read_b32 v228, v228 offset:0\n\t" \
  "v_and_b32_sdwa v233, s56, v232 dst_sel:DWORD dst_unused:UNUSED_PAD src0_sel:DWORD src1_sel:WORD_1\n\t" \
  "v_and_b32 v232, s56, v232\n\t" \
  "v_mfma_f32_16x16x32_bf16 v[48:51], v[208:211], v[162:165], v[48:51]\n\t" \
  "ds_read_b32 v231, v231 offset:15376\n\t" \
  "v_and_b32_sdwa v235, s56, v234 dst_sel:DWORD dst_unused:UNUSED_PAD src0_sel:DWORD src1_sel:WORD_1\n\t" \
  "v_and_b32 v234, s56, v234\n\t" \
  "v_mfma_f32_16x16x32_bf16 v[72:75], v[208:211], v[170:173], v[72:75]\n\t" \
  "ds_read_b64_tr_b16 v[208:209], v249 offset:6224\n\t" \
  "ds_read_b64_tr_b16 v[210:211], v249 offset:7760\n\t" \
  "s_waitcnt lgkmcnt(13)\n\t" \
  "v_fma_f32 v179, |v179|, v221, v179 mul:4\n\t" \
  "v_fma_f32 v178, |v178|, v220, v178 mul:4\n\t" \
  "s_waitcnt lgkmcnt(11)\n\t" \
  "v_mfma_f32_16x16x32_bf16 v[4:7], v[212:215], v[146:149], v[4:7]\n\t" \
  "ds_read_b32 v230, v230 offset:15376\n\t" \
  "v_fma_mix_f32 v223, |v177|, v223, v223 op_sel:[0,1,0] op_sel_hi:[0,1,1] clamp\n\t" \
  "v_fma_mix_f32 v222, |v176|, v222, v222 op_sel:[0,1,0] op_sel_hi:[0,1,1] clamp\n\t" \
  "v_mfma_f32_16x16x32_bf16 v[28:31], v[212:215], v[154:157], v[28:31]\n\t" \
  "ds_read_b32 v233, v233 offset:0\n\t" \
  "s_waitcnt lgkmcnt(7)\n\t" \
  "v_fma_f32 v187, |v187|, v225, v187 mul:4\n\t" \
  "v_fma_f32 v186, |v186|, v224, v186 mul:4\n\t" \
  "v_mfma_f32_16x16x32_bf16 v[52:55], v[212:215], v[162:165], v[52:55]\n\t" \
  "ds_read_b32 v232, v232 offset:0\n\t" \
  "v_fma_mix_f32 v227, |v185|, v227, v227 op_sel:[0,1,0] op_sel_hi:[0,1,1] clamp\n\t" \
  "v_fma_mix_f32 v226, |v184|, v226, v226 op_sel:[0,1,0] op_sel_hi:[0,1,1] clamp\n\t" \
  "v_fma_f32 v177, |v177|, v223, v177 mul:4\n\t" \
  "v_mfma_f32_16x16x32_bf16 v[76:79], v[212:215], v[170:173], v[76:79]\n\t" \
  "ds_read_b64_tr_b16 v[212:213], v249 offset:6280\n\t" \
  "v_fma_f32 v176, |v176|, v222, v176 mul:4\n\t" \
  "v_fma_f32 v185, |v185|, v227, v185 mul:4\n\t" \
  "v_mfma_f32_16x16x32_bf16 v[8:11], v[216:219], v[146:149], v[8:11]\n\t" \
  "ds_read_b64_tr_b16 v[214:215], v249 offset:7816\n\t" \
  "v_fma_f32 v184, |v184|, v226, v184 mul:4\n\t" \
  "v_cvt_pk_bf16_f32 v179, v178, v179\n\t" \
  "v_mfma_f32_16x16x32_bf16 v[32:35], v[216:219], v[154:157], v[32:35]\n\t" \
  "ds_read_b32 v235, v235 offset:0\n\t" \
  "v_cvt_pk_bf16_f32 v178, v176, v177\n\t" \
  "v_cvt_pk_bf16_f32 v187, v186, v187\n\t" \
  "v_mfma_f32_16x16x32_bf16 v[56:59], v[216:219], v[162:165], v[56:59]\n\t" \
  "ds_read_b32 v234, v234 offset:0\n\t" \
  "v_cvt_pk_bf16_f32 v186, v184, v185\n\t" \
  "s_setprio 1\n\t" \
  "v_cvt_pk_f16_f32 v220, |v180|, |v181| clamp\n\t" \
  "v_mfma_f32_16x16x32_bf16 v[80:83], v[216:219], v[170:173], v[80:83]\n\t" \
  "ds_read_b64_tr_b16 v[216:217], v249 offset:6288\n\t" \
  "v_cvt_pknorm_i16_f32 v222, |v182|, |v183|\n\t" \
  "v_cvt_pk_f16_f32 v224, |v188|, |v189| clamp\n\t" \
  "s_waitcnt lgkmcnt(8)\n\t" \
  "v_mfma_f32_16x16x32_bf16 v[12:15], v[208:211], v[146:149], v[12:15]\n\t" \
  "ds_read_b64_tr_b16 v[218:219], v249 offset:7824\n\t" \
  "v_cvt_pknorm_i16_f32 v226, |v190|, |v191|\n\t" \
  "v_and_b32_sdwa v221, s56, v220 dst_sel:DWORD dst_unused:UNUSED_PAD src0_sel:DWORD src1_sel:WORD_1\n\t" \
  "v_mfma_f32_16x16x32_bf16 v[36:39], v[208:211], v[154:157], v[36:39]\n\t" \
  "v_and_b32 v220, s56, v220\n\t" \
  "v_lshlrev_b32_sdwa v223, 2, v222 dst_sel:DWORD dst_unused:UNUSED_PAD src0_sel:DWORD src1_sel:BYTE_3\n\t" \
  "v_mfma_f32_16x16x32_bf16 v[60:63], v[208:211], v[162:165], v[60:63]\n\t" \
  "ds_read_b32 v221, v221 offset:0\n\t" \
  "v_lshlrev_b32_sdwa v222, 2, v222 dst_sel:DWORD dst_unused:UNUSED_PAD src0_sel:DWORD src1_sel:BYTE_1\n\t" \
  "v_and_b32_sdwa v225, s56, v224 dst_sel:DWORD dst_unused:UNUSED_PAD src0_sel:DWORD src1_sel:WORD_1\n\t" \
  "v_and_b32 v224, s56, v224\n\t" \
  "v_mfma_f32_16x16x32_bf16 v[84:87], v[208:211], v[170:173], v[84:87]\n\t" \
  "ds_read_b64_tr_b16 v[208:209], v248 offset:43048\n\t" \
  "ds_read_b64_tr_b16 v[210:211], v248 offset:44072\n\t" \
  "v_lshlrev_b32_sdwa v227, 2, v226 dst_sel:DWORD dst_unused:UNUSED_PAD src0_sel:DWORD src1_sel:BYTE_3\n\t" \
  "v_lshlrev_b32_sdwa v226, 2, v226 dst_sel:DWORD dst_unused:UNUSED_PAD src0_sel:DWORD src1_sel:BYTE_1\n\t" \
  "s_waitcnt lgkmcnt(7)\n\t" \
  "v_mfma_f32_16x16x32_bf16 v[16:19], v[212:215], v[146:149], v[16:19]\n\t" \
  "ds_read_b32 v220, v220 offset:0\n\t" \
  "ds_read_b32 v223, v223 offset:15376\n\t" \
  "v_fma_f32 v195, |v195|, v229, v195 mul:4\n\t" \
  "v_fma_f32 v194, |v194|, v228, v194 mul:4\n\t" \
  "v_mfma_f32_16x16x32_bf16 v[40:43], v[212:215], v[154:157], v[40:43]\n\t" \
  "ds_read_b32 v222, v222 offset:15376\n\t" \
  "v_fma_mix_f32 v231, |v193|, v231, v231 op_sel:[0,1,0] op_sel_hi:[0,1,1] clamp\n\t" \
  "v_fma_mix_f32 v230, |v192|, v230, v230 op_sel:[0,1,0] op_sel_hi:[0,1,1] clamp\n\t" \
  "v_mfma_f32_16x16x32_bf16 v[64:67], v[212:215], v[162:165], v[64:67]\n\t" \
  "ds_read_b32 v225, v225 offset:0\n\t" \
  "s_waitcnt lgkmcnt(9)\n\t" \
  "v_fma_f32 v203, |v203|, v233, v203 mul:4\n\t" \
  "v_fma_f32 v202, |v202|, v232, v202 mul:4\n\t" \
  "v_mfma_f32_16x16x32_bf16 v[88:91], v[212:215], v[170:173], v[88:91]\n\t" \
  "ds_read_b64_tr_b16 v[212:213], v248 offset:46120\n\t" \
  "v_fma_f32 v201, |v201|, v235, v201 mul:4\n\t" \
  "v_fma_f32 v200, |v200|, v234, v200 mul:4\n\t" \
  "s_waitcnt lgkmcnt(8)\n\t" \
  "v_mfma_f32_16x16x32_bf16 v[20:23], v[216:219], v[146:149], v[20:23]\n\t" \
  "ds_read_b64_tr_b16 v[214:215], v248 offset:47144\n\t" \
  "v_fma_f32 v193, |v193|, v231, v193 mul:4\n\t" \
  "v_fma_f32 v192, |v192|, v230, v192 mul:4\n\t" \
  "v_mfma_f32_16x16x32_bf16 v[44:47], v[216:219], v[154:157], v[44:47]\n\t" \
  "ds_read_b32 v224, v224 offset:0\n\t" \
  "v_cvt_pk_bf16_f32 v195, v194, v195\n\t" \
  "v_cvt_pk_bf16_f32 v194, v192, v193\n\t" \
  "v_mfma_f32_16x16x32_bf16 v[68:71], v[216:219], v[162:165], v[68:71]\n\t" \
  "ds_read_b32 v227, v227 offset:15376\n\t" \
  "v_cvt_pk_bf16_f32 v203, v202, v203\n\t" \
  "v_cvt_pk_bf16_f32 v202, v200, v201\n\t" \
  "v_cvt_pk_f16_f32 v228, |v196|, |v197| clamp\n\t" \
  "v_mfma_f32_16x16x32_bf16 v[92:95], v[216:219], v[170:173], v[92:95]\n\t" \
  "ds_read_b64_tr_b16 v[216:217], v248 offset:45096\n\t" \
  "v_cvt_pknorm_i16_f32 v230, |v198|, |v199|\n\t" \
  "s_setprio 2\n\t" \
  "v_cvt_pk_f16_f32 v232, |v204|, |v205| clamp\n\t" \
  "s_waitcnt lgkmcnt(9)\n\t" \
  "v_mfma_f32_16x16x32_bf16 v[144:147], v[208:211], v[96:99], 0\n\t" \
  "ds_read_b64_tr_b16 v[218:219], v248 offset:48168\n\t" \
  "v_cvt_pk_f16_f32 v234, |v206|, |v207| clamp\n\t" \
  "v_and_b32_sdwa v229, s56, v228 dst_sel:DWORD dst_unused:UNUSED_PAD src0_sel:DWORD src1_sel:WORD_1\n\t" \
  "v_mfma_f32_16x16x32_bf16 v[152:155], v[208:211], v[100:103], 0\n\t" \
  "ds_read_b32 v226, v226 offset:15376\n\t" \
  "v_and_b32 v228, s56, v228\n\t" \
  "v_lshlrev_b32_sdwa v231, 2, v230 dst_sel:DWORD dst_unused:UNUSED_PAD src0_sel:DWORD src1_sel:BYTE_3\n\t" \
  "v_mfma_f32_16x16x32_bf16 v[160:163], v[208:211], v[104:107], 0\n\t" \
  "ds_read_b32 v229, v229 offset:0\n\t" \
  "v_lshlrev_b32_sdwa v230, 2, v230 dst_sel:DWORD dst_unused:UNUSED_PAD src0_sel:DWORD src1_sel:BYTE_1\n\t" \
  "v_and_b32_sdwa v233, s56, v232 dst_sel:DWORD dst_unused:UNUSED_PAD src0_sel:DWORD src1_sel:WORD_1\n\t" \
  "v_mfma_f32_16x16x32_bf16 v[168:171], v[208:211], v[108:111], 0\n\t" \
  "ds_read_b64_tr_b16 v[208:209], v248 offset:43560\n\t" \
  "v_and_b32 v232, s56, v232\n\t" \
  "v_and_b32_sdwa v235, s56, v234 dst_sel:DWORD dst_unused:UNUSED_PAD src0_sel:DWORD src1_sel:WORD_1\n\t" \
  "s_waitcnt lgkmcnt(7)\n\t" \
  "v_mfma_f32_16x16x32_bf16 v[144:147], v[212:215], v[112:115], v[144:147]\n\t" \
  "ds_read_b64_tr_b16 v[210:211], v248 offset:44584\n\t" \
  "ds_read_b32 v228, v228 offset:0\n\t" \
  "v_and_b32 v234, s56, v234\n\t" \
  "v_fma_f32 v181, |v181|, v221, v181 mul:4\n\t" \
  "v_mfma_f32_16x16x32_bf16 v[152:155], v[212:215], v[116:119], v[152:155]\n\t" \
  "ds_read_b32 v231, v231 offset:15376\n\t" \
  "ds_read_b32 v230, v230 offset:15376\n\t" \
  "v_fma_f32 v180, |v180|, v220, v180 mul:4\n\t" \
  "v_fma_mix_f32 v223, |v183|, v223, v223 op_sel:[0,1,0] op_sel_hi:[0,1,1] clamp\n\t" \
  "v_mfma_f32_16x16x32_bf16 v[160:163], v[212:215], v[120:123], v[160:163]\n\t" \
  "ds_read_b32 v233, v233 offset:0\n\t" \
  "v_fma_mix_f32 v222, |v182|, v222, v222 op_sel:[0,1,0] op_sel_hi:[0,1,1] clamp\n\t" \
  "s_waitcnt lgkmcnt(7)\n\t" \
  "v_fma_f32 v189, |v189|, v225, v189 mul:4\n\t" \
  "v_mfma_f32_16x16x32_bf16 v[168:171], v[212:215], v[124:127], v[168:171]\n\t" \
  "ds_read_b64_tr_b16 v[212:213], v248 offset:46632\n\t" \
  "v_fma_f32 v188, |v188|, v224, v188 mul:4\n\t" \
  "v_fma_mix_f32 v227, |v191|, v227, v227 op_sel:[0,1,0] op_sel_hi:[0,1,1] clamp\n\t" \
  "v_fma_mix_f32 v226, |v190|, v226, v226 op_sel:[0,1,0] op_sel_hi:[0,1,1] clamp\n\t" \
  "v_mfma_f32_16x16x32_bf16 v[144:147], v[216:219], v[128:131], v[144:147]\n\t" \
  "ds_read_b64_tr_b16 v[214:215], v248 offset:47656\n\t" \
  "v_fma_f32 v183, |v183|, v223, v183 mul:4\n\t" \
  "v_fma_f32 v182, |v182|, v222, v182 mul:4\n\t" \
  "v_mfma_f32_16x16x32_bf16 v[152:155], v[216:219], v[132:135], v[152:155]\n\t" \
  "ds_read_b32 v232, v232 offset:0\n\t" \
  "v_fma_f32 v191, |v191|, v227, v191 mul:4\n\t" \
  "v_fma_f32 v190, |v190|, v226, v190 mul:4\n\t" \
  "v_mfma_f32_16x16x32_bf16 v[160:163], v[216:219], v[136:139], v[160:163]\n\t" \
  "ds_read_b32 v235, v235 offset:0\n\t" \
  "v_cvt_pk_bf16_f32 v180, v180, v181\n\t" \
  "v_cvt_pk_bf16_f32 v181, v182, v183\n\t" \
  "v_mfma_f32_16x16x32_bf16 v[168:171], v[216:219], v[140:143], v[168:171]\n\t" \
  "ds_read_b64_tr_b16 v[216:217], v248 offset:45608\n\t" \
  "v_cvt_pk_bf16_f32 v188, v188, v189\n\t" \
  "v_cvt_pk_bf16_f32 v189, v190, v191\n\t" \
  "s_waitcnt lgkmcnt(9)\n\t" \
  "v_mfma_f32_16x16x32_bf16 v[148:151], v[208:211], v[96:99], 0\n\t" \
  "ds_read_b64_tr_b16 v[218:219], v248 offset:48680\n\t" \
  "v_cvt_pk_f16_f32 v220, |v146|, |v147| clamp\n\t" \
  "s_setprio 3\n\t" \
  "v_cvt_pknorm_i16_f32 v222, |v144|, |v145|\n\t" \
  "v_mfma_f32_16x16x32_bf16 v[156:159], v[208:211], v[100:103], 0\n\t" \
  "s_waitcnt vmcnt(1)\n\t" \
  "s_cmp_lg_u32 s58, 1\n\t" \
  "s_cbranch_scc1 .Lcnx_c15\n\t" \
  "s_waitcnt vmcnt(0)\n\t" \
  ".Lcnx_c15:\n\t" \
  "s_mov_b64 exec, 3\n\t" \
  "ds_add_rtn_u32 v246, v251, v250 offset:47200\n\t" \
  "s_mov_b64 exec, -1\n\t" \
  "ds_read_b32 v234, v234 offset:0\n\t" \
  "v_cvt_pk_f16_f32 v224, |v154|, |v155| clamp\n\t" \
  "v_cvt_pknorm_i16_f32 v226, |v152|, |v153|\n\t" \
  "v_mfma_f32_16x16x32_bf16 v[164:167], v[208:211], v[104:107], 0\n\t" \
  "v_and_b32_sdwa v221, s56, v220 dst_sel:DWORD dst_unused:UNUSED_PAD src0_sel:DWORD src1_sel:WORD_1\n\t" \
  "v_and_b32 v220, s56, v220\n\t" \
  "v_mfma_f32_16x16x32_bf16 v[172:175], v[208:211], v[108:111], 0\n\t" \
  "s_waitcnt lgkmcnt(1)\n\t" \
  "v_readfirstlane_b32 s4, v246\n\t" \
  "s_cmp_lt_u32 s4, s57\n\t" \
  "s_cbranch_scc1 .Lcnx_w96\n.Lcnx_b96:\n\t" \
  "s_cmp_lg_u32 s58, 1\n\t" \
  "s_cselect_b32 s57, s57, 0\n\t" \
  "ds_read_b64_tr_b16 v[208:209], v249 offset:12304\n\t" \
  "v_lshlrev_b32_sdwa v223, 2, v222 dst_sel:DWORD dst_unused:UNUSED_PAD src0_sel:DWORD src1_sel:BYTE_3\n\t" \
  "v_lshlrev_b32_sdwa v222, 2, v222 dst_sel:DWORD dst_unused:UNUSED_PAD src0_sel:DWORD src1_sel:BYTE_1\n\t" \
  "v_and_b32_sdwa v225, s56, v224 dst_sel:DWORD dst_unused:UNUSED_PAD src0_sel:DWORD src1_sel:WORD_1\n\t" \
  "v_mfma_f32_16x16x32_bf16 v[148:151], v[212:215], v[112:115], v[148:151]\n\t" \
  "ds_read_b64_tr_b16 v[210:211], v249 offset:13840\n\t" \
  "v_and_b32 v224, s56, v224\n\t" \
  "v_lshlrev_b32_sdwa v227, 2, v226 dst_sel:DWORD dst_unused:UNUSED_PAD src0_sel:DWORD src1_sel:BYTE_3\n\t" \
  "v_mfma_f32_16x16x32_bf16 v[156:159], v[212:215], v[116:119], v[156:159]\n\t" \
  "s_cmp_eq_u32 s37, 0\n\t" \
  "s_cbranch_scc1 .Lcnx_f9\n.Lcnx_g9:\n\t" \
  "ds_read_b32 v221, v221 offset:0\n\t" \
  "ds_read_b32 v220, v220 offset:0\n\t" \
  "v_lshlrev_b32_sdwa v226, 2, v226 dst_sel:DWORD dst_unused:UNUSED_PAD src0_sel:DWORD src1_sel:BYTE_1\n\t" \
  "v_fma_f32 v197, |v197|, v229, v197 mul:4\n\t" \
  "v_mfma_f32_16x16x32_bf16 v[164:167], v[212:215], v[120:123], v[164:167]\n\t" \
  "ds_read_b32 v223, v223 offset:15376\n\t" \
  "ds_read_b32 v222, v222 offset:15376\n\t" \
  "v_fma_f32 v196, |v196|, v228, v196 mul:4\n\t" \
  "v_fma_mix_f32 v231, |v199|, v231, v231 op_sel:[0,1,0] op_sel_hi:[0,1,1] clamp\n\t" \
  "v_mfma_f32_16x16x32_bf16 v[172:175], v[212:215], v[124:127], v[172:175]\n\t" \
  "ds_read_b64_tr_b16 v[212:213], v249 offset:12312\n\t" \
  "ds_read_b64_tr_b16 v[214:215], v249 offset:13848\n\t" \
  "v_fma_mix_f32 v230, |v198|, v230, v230 op_sel:[0,1,0] op_sel_hi:[0,1,1] clamp\n\t" \
  "s_waitcnt lgkmcnt(8)\n\t" \
  "v_fma_f32 v205, |v205|, v233, v205 mul:4\n\t" \
  "v_mfma_f32_16x16x32_bf16 v[148:151], v[216:219], v[128:131], v[148:151]\n\t" \
  "ds_read_b32 v225, v225 offset:0\n\t" \
  "v_fma_f32 v204, |v204|, v232, v204 mul:4\n\t" \
  "v_fma_f32 v207, |v207|, v235, v207 mul:4\n\t" \
  "v_mfma_f32_16x16x32_bf16 v[156:159], v[216:219], v[132:135], v[156:159]\n\t" \
  "ds_read_b32 v224, v224 offset:0\n\t" \
  "v_fma_f32 v206, |v206|, v234, v206 mul:4\n\t" \
  "v_fma_f32 v199, |v199|, v231, v199 mul:4\n\t" \
  "v_mfma_f32_16x16x32_bf16 v[164:167], v[216:219], v[136:139], v[164:167]\n\t" \
  "ds_read_b32 v227, v227 offset:15376\n\t" \
  "v_fma_f32 v198, |v198|, v230, v198 mul:4\n\t" \
  "v_cvt_pk_bf16_f32 v196, v196, v197\n\t" \
  "v_mfma_f32_16x16x32_bf16 v[172:175], v[216:219], v[140:143], v[172:175]\n\t" \
  "ds_read_b64_tr_b16 v[216:217], v249 offset:12368\n\t" \
  "v_cvt_pk_bf16_f32 v197, v198, v199\n\t" \
  "v_cvt_pk_bf16_f32 v204, v204, v205\n\t" \
  "v_cvt_pk_bf16_f32 v205, v206, v207\n\t" \
  "s_cmp_lg_u32 s58, 1\n\t" \
  "s_cbranch_scc1 .Lcnx_c16\n\t" \
  "s_mov_b64 vcc, -1\n\t" \
  "s_setprio 0\n\t" \
  "s_cmp_ge_u32 s39, 2048\n\t" \
  "s_cbranch_scc1 .Lcnx_c16\n\t" \
  "s_setprio 3\n\t" \
  ".Lcnx_c16:\n\t" \
  "s_cbranch_vccnz .Lcnx_p9_0\n\ts_setprio 0\n.Lcnx_p9_0:\n\t" \
  "v_cvt_pk_f16_f32 v228, |v162|, |v163| clamp\n\t" \
  "v_cvt_pknorm_i16_f32 v230, |v160|, |v161|\n\t" \
  "v_mfma_f32_16x16x32_bf16 v[56:59], v[242:245], v[236:239], v[56:59]\n\t" \
  "ds_read_b64_tr_b16 v[218:219], v249 offset:13904\n\t" \
  "v_cvt_pk_f16_f32 v232, |v170|, |v171| clamp\n\t" \
  "v_cvt_pk_f16_f32 v234, |v168|, |v169| clamp\n\t" \
  "v_mfma_f32_16x16x32_bf16 v[60:63], v[240:243], v[236:239], v[60:63]\n\t" \
  "buffer_load_dwordx4 v[236:239], v250, s[8:11], s12 offen offset:128\n\t" \
  "s_cmp_lg_u32 s58, 0\n\t" \
  "s_cbranch_scc1 .Lcnx_c28\n\t" \
  "s_add_u32 m0, s25, 57400\n\t" \
  "s_add_u32 s24, s24, s26\n\t" \
  "buffer_load_dwordx4 v254, s[8:11], s24 offen lds\n\t" \
  "s_cmp_lg_u32 s38, 5\n\t" \
  "s_cbranch_scc1 .Lcnx_c28\n\t" \
  "s_mov_b32 m0, 163632\n\t" \
  "s_add_u32 s30, s30, s31\n\t" \
  "buffer_load_dword v252, s[8:11], s30 offen lds\n\t" \
  ".Lcnx_c28:\n\t" \
  "ds_read_b32 v226, v226 offset:15376\n\t" \
  "v_and_b32_sdwa v229, s56, v228 dst_sel:DWORD dst_unused:UNUSED_PAD src0_sel:DWORD src1_sel:WORD_1\n\t" \
  "v_and_b32 v228, s56, v228\n\t" \
  "s_waitcnt lgkmcnt(12)\n\t" \
  "v_mfma_f32_16x16x32_bf16 v[0:3], v[208:211], v[178:181], v[0:3]\n\t" \
  "ds_read_b32 v229, v229 offset:0\n\t" \
  "v_lshlrev_b32_sdwa v231, 2, v230 dst_sel:DWORD dst_unused:UNUSED_PAD src0_sel:DWORD src1_sel:BYTE_3\n\t" \
  "v_lshlrev_b32_sdwa v230, 2, v230 dst_sel:DWORD dst_unused:UNUSED_PAD src0_sel:DWORD src1_sel:BYTE_1\n\t" \
  "v_mfma_f32_16x16x32_bf16 v[24:27], v[208:211], v[186:189], v[24:27]\n\t" \
  "ds_read_b32 v228, v228 offset:0\n\t" \
  "v_and_b32_sdwa v233, s56, v232 dst_sel:DWORD dst_unused:UNUSED_PAD src0_sel:DWORD src1_sel:WORD_1\n\t" \
  "v_and_b32 v232, s56, v232\n\t" \
  "v_mfma_f32_16x16x32_bf16 v[48:51], v[208:211], v[194:197], v[48:51]\n\t" \
  "ds_read_b32 v231, v231 offset:15376\n\t" \
  "v_and_b32_sdwa v235, s56, v234 dst_sel:DWORD dst_unused:UNUSED_PAD src0_sel:DWORD src1_sel:WORD_1\n\t" \
  "v_and_b32 v234, s56, v234\n\t" \
  "v_mfma_f32_16x16x32_bf16 v[72:75], v[208:211], v[202:205], v[72:75]\n\t" \
  "ds_read_b64_tr_b16 v[208:209], v249 offset:12376\n\t" \
  "ds_read_b64_tr_b16 v[210:211], v249 offset:13912\n\t" \
  "s_waitcnt lgkmcnt(13)\n\t" \
  "v_fma_f32 v147, |v147|, v221, v147 mul:4\n\t" \
  "v_fma_f32 v146, |v146|, v220, v146 mul:4\n\t" \
  "s_waitcnt lgkmcnt(11)\n\t" \
  "v_mfma_f32_16x16x32_bf16 v[4:7], v[212:215], v[178:181], v[4:7]\n\t" \
  "ds_read_b32 v230, v230 offset:15376\n\t" \
  "v_fma_mix_f32 v223, |v145|, v223, v223 op_sel:[0,1,0] op_sel_hi:[0,1,1] clamp\n\t" \
  "v_fma_mix_f32 v222, |v144|, v222, v222 op_sel:[0,1,0] op_sel_hi:[0,1,1] clamp\n\t" \
  "v_mfma_f32_16x16x32_bf16 v[28:31], v[212:215], v[186:189], v[28:31]\n\t" \
  "ds_read_b32 v233, v233 offset:0\n\t" \
  "s_waitcnt lgkmcnt(7)\n\t" \
  "v_fma_f32 v155, |v155|, v225, v155 mul:4\n\t" \
  "v_fma_f32 v154, |v154|, v224, v154 mul:4\n\t" \
  "v_mfma_f32_16x16x32_bf16 v[52:55], v[212:215], v[194:197], v[52:55]\n\t" \
  "ds_read_b32 v232, v232 offset:0\n\t" \
  "v_fma_mix_f32 v227, |v153|, v227, v227 op_sel:[0,1,0] op_sel_hi:[0,1,1] clamp\n\t" \
  "v_fma_mix_f32 v226, |v152|, v226, v226 op_sel:[0,1,0] op_sel_hi:[0,1,1] clamp\n\t" \
  "v_fma_f32 v145, |v145|, v223, v145 mul:4\n\t" \
  "v_mfma_f32_16x16x32_bf16 v[76:79], v[212:215], v[202:205], v[76:79]\n\t" \
  "ds_read_b64_tr_b16 v[212:213], v249 offset:12432\n\t" \
  "v_fma_f32 v144, |v144|, v222, v144 mul:4\n\t" \
  "v_fma_f32 v153, |v153|, v227, v153 mul:4\n\t" \
  "v_mfma_f32_16x16x32_bf16 v[8:11], v[216:219], v[178:181], v[8:11]\n\t" \
  "ds_read_b64_tr_b16 v[214:215], v249 offset:13968\n\t" \
  "v_fma_f32 v152, |v152|, v226, v152 mul:4\n\t" \
  "v_cvt_pk_bf16_f32 v147, v146, v147\n\t" \
  "v_mfma_f32_16x16x32_bf16 v[32:35], v[216:219], v[186:189], v[32:35]\n\t" \
  "ds_read_b32 v235, v235 offset:0\n\t" \
  "v_cvt_pk_bf16_f32 v146, v144, v145\n\t" \
  "v_cvt_pk_bf16_f32 v155, v154, v155\n\t" \
  "v_mfma_f32_16x16x32_bf16 v[56:59], v[216:219], v[194:197], v[56:59]\n\t" \
  "ds_read_b32 v234, v234 offset:0\n\t" \
  "v_cvt_pk_bf16_f32 v154, v152, v153\n\t" \
  "s_cbranch_vccnz .Lcnx_p9_1\n\ts_setprio 1\n.Lcnx_p9_1:\n\t" \
  "v_cvt_pk_f16_f32 v220, |v148|, |v149| clamp\n\t" \
  "v_mfma_f32_16x16x32_bf16 v[80:83], v[216:219], v[202:205], v[80:83]\n\t" \
  "ds_read_b64_tr_b16 v[216:217], v249 offset:12440\n\t" \
  "v_cvt_pknorm_i16_f32 v222, |v150|, |v151|\n\t" \
  "v_cvt_pk_f16_f32 v224, |v156|, |v157| clamp\n\t" \
  "s_waitcnt lgkmcnt(8)\n\t" \
  "v_mfma_f32_16x16x32_bf16 v[12:15], v[208:211], v[178:181], v[12:15]\n\t" \
  "ds_read_b64_tr_b16 v[218:219], v249 offset:13976\n\t" \
  "v_cvt_pknorm_i16_f32 v226, |v158|, |v159|\n\t" \
  "v_and_b32_sdwa v221, s56, v220 dst_sel:DWORD dst_unused:UNUSED_PAD src0_sel:DWORD src1_sel:WORD_1\n\t" \
  "v_mfma_f32_16x16x32_bf16 v[36:39], v[208:211], v[186:189], v[36:39]\n\t" \
  "v_and_b32 v220, s56, v220\n\t" \
  "v_lshlrev_b32_sdwa v223, 2, v222 dst_sel:DWORD dst_unused:UNUSED_PAD src0_sel:DWORD src1_sel:BYTE_3\n\t" \
  "v_mfma_f32_16x16x32_bf16 v[60:63], v[208:211], v[194:197], v[60:63]\n\t" \
  "ds_read_b32 v221, v221 offset:0\n\t" \
  "v_lshlrev_b32_sdwa v222, 2, v222 dst_sel:DWORD dst_unused:UNUSED_PAD src0_sel:DWORD src1_sel:BYTE_1\n\t" \
  "v_and_b32_sdwa v225, s56, v224 dst_sel:DWORD dst_unused:UNUSED_PAD src0_sel:DWORD src1_sel:WORD_1\n\t" \
  "v_and_b32 v224, s56, v224\n\t" \
  "v_mfma_f32_16x16x32_bf16 v[84:87], v[208:211], v[202:205], v[84:87]\n\t" \
  "ds_read_b64_tr_b16 v[208:209], v248 offset:49192\n\t" \
  "ds_read_b64_tr_b16 v[210:211], v248 offset:50216\n\t" \
  "v_lshlrev_b32_sdwa v227, 2, v226 dst_sel:DWORD dst_unused:UNUSED_PAD src0_sel:DWORD src1_sel:BYTE_3\n\t" \
  "v_lshlrev_b32_sdwa v226, 2, v226 dst_sel:DWORD dst_unused:UNUSED_PAD src0_sel:DWORD src1_sel:BYTE_1\n\t" \
  "s_waitcnt lgkmcnt(7)\n\t" \
  "v_mfma_f32_16x16x32_bf16 v[16:19], v[212:215], v[178:181], v[16:19]\n\t" \
  "ds_read_b32 v220, v220 offset:0\n\t" \
  "ds_read_b32 v223, v223 offset:15376\n\t" \
  "v_fma_f32 v163, |v163|, v229, v163 mul:4\n\t" \
  "v_fma_f32 v162, |v162|, v228, v162 mul:4\n\t" \
  "v_mfma_f32_16x16x32_bf16 v[40:43], v[212:215], v[186:189], v[40:43]\n\t" \
  "ds_read_b32 v222, v222 offset:15376\n\t" \
  "v_fma_mix_f32 v231, |v161|, v231, v231 op_sel:[0,1,0] op_sel_hi:[0,1,1] clamp\n\t" \
  "v_fma_mix_f32 v230, |v160|, v230, v230 op_sel:[0,1,0] op_sel_hi:[0,1,1] clamp\n\t" \
  "v_mfma_f32_16x16x32_bf16 v[64:67], v[212:215], v[194:197], v[64:67]\n\t" \
  "ds_read_b32 v225, v225 offset:0\n\t" \
  "s_waitcnt lgkmcnt(9)\n\t" \
  "v_fma_f32 v171, |v171|, v233, v171 mul:4\n\t" \
  "v_fma_f32 v170, |v170|, v232, v170 mul:4\n\t" \
  "v_mfma_f32_16x16x32_bf16 v[88:91], v[212:215], v[202:205], v[88:91]\n\t" \
  "ds_read_b64_tr_b16 v[212:213], v248 offset:52264\n\t" \
  "v_fma_f32 v169, |v169|, v235, v169 mul:4\n\t" \
  "v_fma_f32 v168, |v168|, v234, v168 mul:4\n\t" \
  "s_waitcnt lgkmcnt(8)\n\t" \
  "v_mfma_f32_16x16x32_bf16 v[20:23], v[216:219], v[178:181], v[20:23]\n\t" \
  "ds_read_b64_tr_b16 v[214:215], v248 offset:53288\n\t" \
  "v_fma_f32 v161, |v161|, v231, v161 mul:4\n\t" \
  "v_fma_f32 v160, |v160|, v230, v160 mul:4\n\t" \
  "v_mfma_f32_16x16x32_bf16 v[44:47], v[216:219], v[186:189], v[44:47]\n\t" \
  "ds_read_b32 v224, v224 offset:0\n\t" \
  "v_cvt_pk_bf16_f32 v163, v162, v163\n\t" \
  "v_cvt_pk_bf16_f32 v162, v160, v161\n\t" \
  "v_mfma_f32_16x16x32_bf16 v[68:71], v[216:219], v[194:197], v[68:71]\n\t" \
  "ds_read_b32 v227, v227 offset:15376\n\t" \
  "v_cvt_pk_bf16_f32 v171, v170, v171\n\t" \
  "v_cvt_pk_bf16_f32 v170, v168, v169\n\t" \
  "v_cvt_pk_f16_f32 v228, |v164|, |v165| clamp\n\t" \
  "v_mfma_f32_16x16x32_bf16 v[92:95], v[216:219], v[202:205], v[92:95]\n\t" \
  "ds_read_b64_tr_b16 v[216:217], v248 offset:51240\n\t" \
  "v_cvt_pknorm_i16_f32 v230, |v166|, |v167|\n\t" \
  "s_cbranch_vccnz .Lcnx_p9_2\n\ts_setprio 2\n.Lcnx_p9_2:\n\t" \
  "v_cvt_pk_f16_f32 v232, |v172|, |v173| clamp\n\t" \
  "s_waitcnt lgkmcnt(9)\n\t" \
  "v_mfma_f32_16x16x32_bf16 v[176:179], v[208:211], v[96:99], 0\n\t" \
  "ds_read_b64_tr_b16 v[218:219], v248 offset:54312\n\t" \
  "v_cvt_pk_f16_f32 v234, |v174|, |v175| clamp\n\t" \
  "v_and_b32_sdwa v229, s56, v228 dst_sel:DWORD dst_unused:UNUSED_PAD src0_sel:DWORD src1_sel:WORD_1\n\t" \
  "v_mfma_f32_16x16x32_bf16 v[184:187], v[208:211], v[100:103], 0\n\t" \
  "ds_read_b32 v226, v226 offset:15376\n\t" \
  "v_and_b32 v228, s56, v228\n\t" \
  "v_lshlrev_b32_sdwa v231, 2, v230 dst_sel:DWORD dst_unused:UNUSED_PAD src0_sel:DWORD src1_sel:BYTE_3\n\t" \
  "v_mfma_f32_16x16x32_bf16 v[192:195], v[208:211], v[104:107], 0\n\t" \
  "ds_read_b32 v229, v229 offset:0\n\t" \
  "v_lshlrev_b32_sdwa v230, 2, v230 dst_sel:DWORD dst_unused:UNUSED_PAD src0_sel:DWORD src1_sel:BYTE_1\n\t" \
  "v_and_b32_sdwa v233, s56, v232 dst_sel:DWORD dst_unused:UNUSED_PAD src0_sel:DWORD src1_sel:WORD_1\n\t" \
  "v_mfma_f32_16x16x32_bf16 v[200:203], v[208:211], v[108:111], 0\n\t" \
  "ds_read_b64_tr_b16 v[208:209], v248 offset:49704\n\t" \
  "v_and_b32 v232, s56, v232\n\t" \
  "v_and_b32_sdwa v235, s56, v234 dst_sel:DWORD dst_unused:UNUSED_PAD src0_sel:DWORD src1_sel:WORD_1\n\t" \
  "s_waitcnt lgkmcnt(7)\n\t" \
  "v_mfma_f32_16x16x32_bf16 v[176:179], v[212:215], v[112:115], v[176:179]\n\t" \
  "ds_read_b64_tr_b16 v[210:211], v248 offset:50728\n\t" \
  "ds_read_b32 v228, v228 offset:0\n\t" \
  "v_and_b32 v234, s56, v234\n\t" \
  "v_fma_f32 v149, |v149|, v221, v149 mul:4\n\t" \
  "v_mfma_f32_16x16x32_bf16 v[184:187], v[212:215], v[116:119], v[184:187]\n\t" \
  "ds_read_b32 v231, v231 offset:15376\n\t" \
  "ds_read_b32 v230, v230 offset:15376\n\t" \
  "v_fma_f32 v148, |v148|, v220, v148 mul:4\n\t" \
  "v_fma_mix_f32 v223, |v151|, v223, v223 op_sel:[0,1,0] op_sel_hi:[0,1,1] clamp\n\t" \
  "v_mfma_f32_16x16x32_bf16 v[192:195], v[212:215], v[120:123], v[192:195]\n\t" \
  "ds_read_b32 v233, v233 offset:0\n\t" \
  "v_fma_mix_f32 v222, |v150|, v222, v222 op_sel:[0,1,0] op_sel_hi:[0,1,1] clamp\n\t" \
  "s_waitcnt lgkmcnt(7)\n\t" \
  "v_fma_f32 v157, |v157|, v225, v157 mul:4\n\t" \
  "v_mfma_f32_16x16x32_bf16 v[200:203], v[212:215], v[124:127], v[200:203]\n\t" \
  "ds_read_b64_tr_b16 v[212:213], v248 offset:52776\n\t" \
  "v_fma_f32 v156, |v156|, v224, v156 mul:4\n\t" \
  "v_fma_mix_f32 v227, |v159|, v227, v227 op_sel:[0,1,0] op_sel_hi:[0,1,1] clamp\n\t" \
  "v_fma_mix_f32 v226, |v158|, v226, v226 op_sel:[0,1,0] op_sel_hi:[0,1,1] clamp\n\t" \
  "v_mfma_f32_16x16x32_bf16 v[176:179], v[216:219], v[128:131], v[176:179]\n\t" \
  "ds_read_b64_tr_b16 v[214:215], v248 offset:53800\n\t" \
  "v_fma_f32 v151, |v151|, v223, v151 mul:4\n\t" \
  "v_fma_f32 v150, |v150|, v222, v150 mul:4\n\t" \
  "v_mfma_f32_16x16x32_bf16 v[184:187], v[216:219], v[132:135], v[184:187]\n\t" \
  "ds_read_b32 v232, v232 offset:0\n\t" \
  "v_fma_f32 v159, |v159|, v227, v159 mul:4\n\t" \
  "v_fma_f32 v158, |v158|, v226, v158 mul:4\n\t" \
  "v_mfma_f32_16x16x32_bf16 v[192:195], v[216:219], v[136:139], v[192:195]\n\t" \
  "ds_read_b32 v235, v235 offset:0\n\t" \
  "v_cvt_pk_bf16_f32 v148, v148, v149\n\t" \
  "v_cvt_pk_bf16_f32 v149, v150, v151\n\t" \
  "v_mfma_f32_16x16x32_bf16 v[200:203], v[216:219], v[140:143], v[200:203]\n\t" \
  "ds_read_b64_tr_b16 v[216:217], v248 offset:51752\n\t" \
  "v_cvt_pk_bf16_f32 v156, v156, v157\n\t" \
  "v_cvt_pk_bf16_f32 v157, v158, v159\n\t" \
  "s_waitcnt lgkmcnt(9)\n\t" \
  "v_mfma_f32_16x16x32_bf16 v[180:183], v[208:211], v[96:99], 0\n\t" \
  "ds_read_b64_tr_b16 v[218:219], v248 offset:54824\n\t" \
  "v_cvt_pk_f16_f32 v220, |v178|, |v179| clamp\n\t" \
  "s_cbranch_vccnz .Lcnx_p9_3\n\ts_setprio 3\n.Lcnx_p9_3:\n\t" \
  "v_cvt_pknorm_i16_f32 v222, |v176|, |v177|\n\t" \
  "v_mfma_f32_16x16x32_bf16 v[188:191], v[208:211], v[100:103], 0\n\t" \
  "s_waitcnt vmcnt(1)\n\t" \
  "s_cmp_lg_u32 s58, 1\n\t" \
  "s_cbranch_scc1 .Lcnx_c17\n\t" \
  "s_waitcnt vmcnt(0)\n\t" \
  ".Lcnx_c17:\n\t" \
  "s_mov_b64 exec, 3\n\t" \
  "ds_add_rtn_u32 v246, v251, v250 offset:47208\n\t" \
  "s_mov_b64 exec, -1\n\t" \
  "ds_read_b32 v234, v234 offset:0\n\t" \
  "v_cvt_pk_f16_f32 v224, |v186|, |v187| clamp\n\t" \
  "v_cvt_pknorm_i16_f32 v226, |v184|, |v185|\n\t" \
  "v_mfma_f32_16x16x32_bf16 v[196:199], v[208:211], v[104:107], 0\n\t" \
  "v_and_b32_sdwa v221, s56, v220 dst_sel:DWORD dst_unused:UNUSED_PAD src0_sel:DWORD src1_sel:WORD_1\n\t" \
  "v_and_b32 v220, s56, v220\n\t" \
  "v_mfma_f32_16x16x32_bf16 v[204:207], v[208:211], v[108:111], 0\n\t" \
  "s_waitcnt lgkmcnt(1)\n\t" \
  "v_readfirstlane_b32 s4, v246\n\t" \
  "s_cmp_lt_u32 s4, s57\n\t" \
  "s_cbranch_scc1 .Lcnx_w108\n.Lcnx_b108:\n\t" \
  "v_add_u32 v248, s59, v248\n\t" \
  "v_add_u32 v249, s60, v249\n\t" \
  "ds_read_b64_tr_b16 v[208:209], v249 offset:24600\n\t" \
  "v_lshlrev_b32_sdwa v223, 2, v222 dst_sel:DWORD dst_unused:UNUSED_PAD src0_sel:DWORD src1_sel:BYTE_3\n\t" \
  "v_lshlrev_b32_sdwa v222, 2, v222 dst_sel:DWORD dst_unused:UNUSED_PAD src0_sel:DWORD src1_sel:BYTE_1\n\t" \
  "v_and_b32_sdwa v225, s56, v224 dst_sel:DWORD dst_unused:UNUSED_PAD src0_sel:DWORD src1_sel:WORD_1\n\t" \
  "v_mfma_f32_16x16x32_bf16 v[180:183], v[212:215], v[112:115], v[180:183]\n\t" \
  "ds_read_b64_tr_b16 v[210:211], v249 offset:26136\n\t" \
  "v_and_b32 v224, s56, v224\n\t" \
  "v_lshlrev_b32_sdwa v227, 2, v226 dst_sel:DWORD dst_unused:UNUSED_PAD src0_sel:DWORD src1_sel:BYTE_3\n\t" \
  "v_mfma_f32_16x16x32_bf16 v[188:191], v[212:215], v[116:119], v[188:191]\n\t" \
  "s_cmp_eq_u32 s37, 0\n\t" \
  "s_cbranch_scc1 .Lcnx_f10\n.Lcnx_g10:\n\t" \
  "ds_read_b32 v221, v221 offset:0\n\t" \
  "ds_read_b32 v220, v220 offset:0\n\t" \
  "v_lshlrev_b32_sdwa v226, 2, v226 dst_sel:DWORD dst_unused:UNUSED_PAD src0_sel:DWORD src1_sel:BYTE_1\n\t" \
  "v_fma_f32 v165, |v165|, v229, v165 mul:4\n\t" \
  "v_mfma_f32_16x16x32_bf16 v[196:199], v[212:215], v[120:123], v[196:199]\n\t" \
  "ds_read_b32 v223, v223 offset:15376\n\t" \
  "ds_read_b32 v222, v222 offset:15376\n\t" \
  "v_fma_f32 v164, |v164|, v228, v164 mul:4\n\t" \
  "v_fma_mix_f32 v231, |v167|, v231, v231 op_sel:[0,1,0] op_sel_hi:[0,1,1] clamp\n\t" \
  "v_mfma_f32_16x16x32_bf16 v[204:207], v[212:215], v[124:127], v[204:207]\n\t" \
  "ds_read_b64_tr_b16 v[212:213], v249 offset:24608\n\t" \
  "ds_read_b64_tr_b16 v[214:215], v249 offset:26144\n\t" \
  "v_fma_mix_f32 v230, |v166|, v230, v230 op_sel:[0,1,0] op_sel_hi:[0,1,1] clamp\n\t" \
  "s_waitcnt lgkmcnt(8)\n\t" \
  "v_fma_f32 v173, |v173|, v233, v173 mul:4\n\t" \
  "v_mfma_f32_16x16x32_bf16 v[180:183], v[216:219], v[128:131], v[180:183]\n\t" \
  "ds_read_b32 v225, v225 offset:0\n\t" \
  "v_fma_f32 v172, |v172|, v232, v172 mul:4\n\t" \
  "v_fma_f32 v175, |v175|, v235, v175 mul:4\n\t" \
  "v_mfma_f32_16x16x32_bf16 v[188:191], v[216:219], v[132:135], v[188:191]\n\t" \
  "ds_read_b32 v224, v224 offset:0\n\t" \
  "v_fma_f32 v174, |v174|, v234, v174 mul:4\n\t" \
  "v_fma_f32 v167, |v167|, v231, v167 mul:4\n\t" \
  "v_mfma_f32_16x16x32_bf16 v[196:199], v[216:219], v[136:139], v[196:199]\n\t" \
  "ds_read_b32 v227, v227 offset:15376\n\t" \
  "v_fma_f32 v166, |v166|, v230, v166 mul:4\n\t" \
  "v_cvt_pk_bf16_f32 v164, v164, v165\n\t" \
  "v_mfma_f32_16x16x32_bf16 v[204:207], v[216:219], v[140:143], v[204:207]\n\t" \
  "ds_read_b64_tr_b16 v[216:217], v249 offset:24664\n\t" \
  "v_cvt_pk_bf16_f32 v165, v166, v167\n\t" \
  "v_cvt_pk_bf16_f32 v172, v172, v173\n\t" \
  "v_cvt_pk_bf16_f32 v173, v174, v175\n\t" \
  "s_cbranch_vccnz .Lcnx_p10_0\n\ts_setprio 0\n.Lcnx_p10_0:\n\t" \
  "v_cvt_pk_f16_f32 v228, |v194|, |v195| clamp\n\t" \
  "v_cvt_pknorm_i16_f32 v230, |v192|, |v193|\n\t" \
  "v_mfma_f32_16x16x32_bf16 v[64:67], v[242:245], v[236:239], v[64:67]\n\t" \
  "ds_read_b64_tr_b16 v[218:219], v249 offset:26200\n\t" \
  "v_cvt_pk_f16_f32 v232, |v202|, |v203| clamp\n\t" \
  "v_cvt_pk_f16_f32 v234, |v200|, |v201| clamp\n\t" \
  "v_mfma_f32_16x16x32_bf16 v[68:71], v[240:243], v[236:239], v[68:71]\n\t" \
  "buffer_load_dwordx4 v[236:239], v250, s[8:11], s13 offen offset:0\n\t" \
  "s_cmp_lg_u32 s58, 0\n\t" \
  "s_cbranch_scc1 .Lcnx_c29\n\t" \
  "s_add_u32 m0, s25, 65600\n\t" \
  "s_add_u32 s24, s24, s26\n\t" \
  "buffer_load_dwordx4 v254, s[8:11], s24 offen lds\n\t" \
  "s_cmp_lg_u32 s38, 6\n\t" \
  "s_cbranch_scc1 .Lcnx_c29\n\t" \
  "s_mov_b32 m0, 163632\n\t" \
  "s_add_u32 s30, s30, s31\n\t" \
  "buffer_load_dword v252, s[8:11], s30 offen lds\n\t" \
  ".Lcnx_c29:\n\t" \
  "ds_read_b32 v226, v226 offset:15376\n\t" \
  "v_and_b32_sdwa v229, s56, v228 dst_sel:DWORD dst_unused:UNUSED_PAD src0_sel:DWORD src1_sel:WORD_1\n\t" \
  "v_and_b32 v228, s56, v228\n\t" \
  "s_waitcnt lgkmcnt(12)\n\t" \
  "v_mfma_f32_16x16x32_bf16 v[0:3], v[208:211], v[146:149], v[0:3]\n\t" \
  "ds_read_b32 v229, v229 offset:0\n\t" \
  "v_lshlrev_b32_sdwa v231, 2, v230 dst_sel:DWORD dst_unused:UNUSED_PAD src0_sel:DWORD src1_sel:BYTE_3\n\t" \
  "v_lshlrev_b32_sdwa v230, 2, v230 dst_sel:DWORD dst_unused:UNUSED_PAD src0_sel:DWORD src1_sel:BYTE_1\n\t" \
  "v_mfma_f32_16x16x32_bf16 v[24:27], v[208:211], v[154:157], v[24:27]\n\t" \
  "ds_read_b32 v228, v228 offset:0\n\t" \
  "v_and_b32_sdwa v233, s56, v232 dst_sel:DWORD dst_unused:UNUSED_PAD src0_sel:DWORD src1_sel:WORD_1\n\t" \
  "v_and_b32 v232, s56, v232\n\t" \
  "v_mfma_f32_16x16x32_bf16 v[48:51], v[208:211], v[162:165], v[48:51]\n\t" \
  "ds_read_b32 v231, v231 offset:15376\n\t" \
  "v_and_b32_sdwa v235, s56, v234 dst_sel:DWORD dst_unused:UNUSED_PAD src0_sel:DWORD src1_sel:WORD_1\n\t" \
  "v_and_b32 v234, s56, v234\n\t" \
  "v_mfma_f32_16x16x32_bf16 v[72:75], v[208:211], v[170:173], v[72:75]\n\t" \
  "ds_read_b64_tr_b16 v[208:209], v249 offset:24672\n\t" \
  "ds_read_b64_tr_b16 v[210:211], v249 offset:26208\n\t" \
  "s_waitcnt lgkmcnt(13)\n\t" \
  "v_fma_f32 v179, |v179|, v221, v179 mul:4\n\t" \
  "v_fma_f32 v178, |v178|, v220, v178 mul:4\n\t" \
  "s_waitcnt lgkmcnt(11)\n\t" \
  "v_mfma_f32_16x16x32_bf16 v[4:7], v[212:215], v[146:149], v[4:7]\n\t" \
  "ds_read_b32 v230, v230 offset:15376\n\t" \
  "v_fma_mix_f32 v223, |v177|, v223, v223 op_sel:[0,1,0] op_sel_hi:[0,1,1] clamp\n\t" \
  "v_fma_mix_f32 v222, |v176|, v222, v222 op_sel:[0,1,0] op_sel_hi:[0,1,1] clamp\n\t" \
  "v_mfma_f32_16x16x32_bf16 v[28:31], v[212:215], v[154:157], v[28:31]\n\t" \
  "ds_read_b32 v233, v233 offset:0\n\t" \
  "s_waitcnt lgkmcnt(7)\n\t" \
  "v_fma_f32 v187, |v187|, v225, v187 mul:4\n\t" \
  "v_fma_f32 v186, |v186|, v224, v186 mul:4\n\t" \
  "v_mfma_f32_16x16x32_bf16 v[52:55], v[212:215], v[162:165], v[52:55]\n\t" \
  "ds_read_b32 v232, v232 offset:0\n\t" \
  "v_fma_mix_f32 v227, |v185|, v227, v227 op_sel:[0,1,0] op_sel_hi:[0,1,1] clamp\n\t" \
  "v_fma_mix_f32 v226, |v184|, v226, v226 op_sel:[0,1,0] op_sel_hi:[0,1,1] clamp\n\t" \
  "v_fma_f32 v177, |v177|, v223, v177 mul:4\n\t" \
  "v_mfma_f32_16x16x32_bf16 v[76:79], v[212:215], v[170:173], v[76:79]\n\t" \
  "ds_read_b64_tr_b16 v[212:213], v249 offset:24728\n\t" \
  "v_fma_f32 v176, |v176|, v222, v176 mul:4\n\t" \
  "v_fma_f32 v185, |v185|, v227, v185 mul:4\n\t" \
  "v_mfma_f32_16x16x32_bf16 v[8:11], v[216:219], v[146:149], v[8:11]\n\t" \
  "ds_read_b64_tr_b16 v[214:215], v249 offset:26264\n\t" \
  "v_fma_f32 v184, |v184|, v226, v184 mul:4\n\t" \
  "v_cvt_pk_bf16_f32 v179, v178, v179\n\t" \
  "v_mfma_f32_16x16x32_bf16 v[32:35], v[216:219], v[154:157], v[32:35]\n\t" \
  "ds_read_b32 v235, v235 offset:0\n\t" \
  "v_cvt_pk_bf16_f32 v178, v176, v177\n\t" \
  "v_cvt_pk_bf16_f32 v187, v186, v187\n\t" \
  "v_mfma_f32_16x16x32_bf16 v[56:59], v[216:219], v[162:165], v[56:59]\n\t" \
  "ds_read_b32 v234, v234 offset:0\n\t" \
  "v_cvt_pk_bf16_f32 v186, v184, v185\n\t" \
  "s_cbranch_vccnz .Lcnx_p10_1\n\ts_setprio 1\n.Lcnx_p10_1:\n\t" \
  "v_cvt_pk_f16_f32 v220, |v180|, |v181| clamp\n\t" \
  "v_mfma_f32_16x16x32_bf16 v[80:83], v[216:219], v[170:173], v[80:83]\n\t" \
  "ds_read_b64_tr_b16 v[216:217], v249 offset:24736\n\t" \
  "v_cvt_pknorm_i16_f32 v222, |v182|, |v183|\n\t" \
  "v_cvt_pk_f16_f32 v224, |v188|, |v189| clamp\n\t" \
  "s_waitcnt lgkmcnt(8)\n\t" \
  "v_mfma_f32_16x16x32_bf16 v[12:15], v[208:211], v[146:149], v[12:15]\n\t" \
  "ds_read_b64_tr_b16 v[218:219], v249 offset:26272\n\t" \
  "v_cvt_pknorm_i16_f32 v226, |v190|, |v191|\n\t" \
  "v_and_b32_sdwa v221, s56, v220 dst_sel:DWORD dst_unused:UNUSED_PAD src0_sel:DWORD src1_sel:WORD_1\n\t" \
  "v_mfma_f32_16x16x32_bf16 v[36:39], v[208:211], v[154:157], v[36:39]\n\t" \
  "v_and_b32 v220, s56, v220\n\t" \
  "v_lshlrev_b32_sdwa v223, 2, v222 dst_sel:DWORD dst_unused:UNUSED_PAD src0_sel:DWORD src1_sel:BYTE_3\n\t" \
  "v_mfma_f32_16x16x32_bf16 v[60:63], v[208:211], v[162:165], v[60:63]\n\t" \
  "ds_read_b32 v221, v221 offset:0\n\t" \
  "v_lshlrev_b32_sdwa v222, 2, v222 dst_sel:DWORD dst_unused:UNUSED_PAD src0_sel:DWORD src1_sel:BYTE_1\n\t" \
  "v_and_b32_sdwa v225, s56, v224 dst_sel:DWORD dst_unused:UNUSED_PAD src0_sel:DWORD src1_sel:WORD_1\n\t" \
  "v_and_b32 v224, s56, v224\n\t" \
  "v_mfma_f32_16x16x32_bf16 v[84:87], v[208:211], v[170:173], v[84:87]\n\t" \
  "ds_read_b64_tr_b16 v[208:209], v248 offset:36904\n\t" \
  "ds_read_b64_tr_b16 v[210:211], v248 offset:37928\n\t" \
  "v_lshlrev_b32_sdwa v227, 2, v226 dst_sel:DWORD dst_unused:UNUSED_PAD src0_sel:DWORD src1_sel:BYTE_3\n\t" \
  "v_lshlrev_b32_sdwa v226, 2, v226 dst_sel:DWORD dst_unused:UNUSED_PAD src0_sel:DWORD src1_sel:BYTE_1\n\t" \
  "s_waitcnt lgkmcnt(7)\n\t" \
  "v_mfma_f32_16x16x32_bf16 v[16:19], v[212:215], v[146:149], v[16:19]\n\t" \
  "ds_read_b32 v220, v220 offset:0\n\t" \
  "ds_read_b32 v223, v223 offset:15376\n\t" \
  "v_fma_f32 v195, |v195|, v229, v195 mul:4\n\t" \
  "v_fma_f32 v194, |v194|, v228, v194 mul:4\n\t" \
  "v_mfma_f32_16x16x32_bf16 v[40:43], v[212:215], v[154:157], v[40:43]\n\t" \
  "ds_read_b32 v222, v222 offset:15376\n\t" \
  "v_fma_mix_f32 v231, |v193|, v231, v231 op_sel:[0,1,0] op_sel_hi:[0,1,1] clamp\n\t" \
  "v_fma_mix_f32 v230, |v192|, v230, v230 op_sel:[0,1,0] op_sel_hi:[0,1,1] clamp\n\t" \
  "v_mfma_f32_16x16x32_bf16 v[64:67], v[212:215], v[162:165], v[64:67]\n\t" \
  "ds_read_b32 v225, v225 offset:0\n\t" \
  "s_waitcnt lgkmcnt(9)\n\t" \
  "v_fma_f32 v203, |v203|, v233, v203 mul:4\n\t" \
  "v_fma_f32 v202, |v202|, v232, v202 mul:4\n\t" \
  "v_mfma_f32_16x16x32_bf16 v[88:91], v[212:215], v[170:173], v[88:91]\n\t" \
  "ds_read_b64_tr_b16 v[212:213], v248 offset:39976\n\t" \
  "v_fma_f32 v201, |v201|, v235, v201 mul:4\n\t" \
  "v_fma_f32 v200, |v200|, v234, v200 mul:4\n\t" \
  "s_waitcnt lgkmcnt(8)\n\t" \
  "v_mfma_f32_16x16x32_bf16 v[20:23], v[216:219], v[146:149], v[20:23]\n\t" \
  "ds_read_b64_tr_b16 v[214:215], v248 offset:41000\n\t" \
  "v_fma_f32 v193, |v193|, v231, v193 mul:4\n\t" \
  "v_fma_f32 v192, |v192|, v230, v192 mul:4\n\t" \
  "v_mfma_f32_16x16x32_bf16 v[44:47], v[216:219], v[154:157], v[44:47]\n\t" \
  "ds_read_b32 v224, v224 offset:0\n\t" \
  "v_cvt_pk_bf16_f32 v195, v194, v195\n\t" \
  "v_cvt_pk_bf16_f32 v194, v192, v193\n\t" \
  "v_mfma_f32_16x16x32_bf16 v[68:71], v[216:219], v[162:165], v[68:71]\n\t" \
  "ds_read_b32 v227, v227 offset:15376\n\t" \
  "v_cvt_pk_bf16_f32 v203, v202, v203\n\t" \
  "v_cvt_pk_bf16_f32 v202, v200, v201\n\t" \
  "v_cvt_pk_f16_f32 v228, |v196|, |v197| clamp\n\t" \
  "v_mfma_f32_16x16x32_bf16 v[92:95], v[216:219], v[170:173], v[92:95]\n\t" \
  "ds_read_b64_tr_b16 v[216:217], v248 offset:38952\n\t" \
  "v_cvt_pknorm_i16_f32 v230, |v198|, |v199|\n\t" \
  "s_cbranch_vccnz .Lcnx_p10_2\n\ts_setprio 2\n.Lcnx_p10_2:\n\t" \
  "v_cvt_pk_f16_f32 v232, |v204|, |v205| clamp\n\t" \
  "s_waitcnt lgkmcnt(9)\n\t" \
  "v_mfma_f32_16x16x32_bf16 v[144:147], v[208:211], v[96:99], 0\n\t" \
  "ds_read_b64_tr_b16 v[218:219], v248 offset:42024\n\t" \
  "v_cvt_pk_f16_f32 v234, |v206|, |v207| clamp\n\t" \
  "v_and_b32_sdwa v229, s56, v228 dst_sel:DWORD dst_unused:UNUSED_PAD src0_sel:DWORD src1_sel:WORD_1\n\t" \
  "v_mfma_f32_16x16x32_bf16 v[152:155], v[208:211], v[100:103], 0\n\t" \
  "ds_read_b32 v226, v226 offset:15376\n\t" \
  "v_and_b32 v228, s56, v228\n\t" \
  "v_lshlrev_b32_sdwa v231, 2, v230 dst_sel:DWORD dst_unused:UNUSED_PAD src0_sel:DWORD src1_sel:BYTE_3\n\t" \
  "v_mfma_f32_16x16x32_bf16 v[160:163], v[208:211], v[104:107], 0\n\t" \
  "ds_read_b32 v229, v229 offset:0\n\t" \
  "v_lshlrev_b32_sdwa v230, 2, v230 dst_sel:DWORD dst_unused:UNUSED_PAD src0_sel:DWORD src1_sel:BYTE_1\n\t" \
  "v_and_b32_sdwa v233, s56, v232 dst_sel:DWORD dst_unused:UNUSED_PAD src0_sel:DWORD src1_sel:WORD_1\n\t" \
  "v_mfma_f32_16x16x32_bf16 v[168:171], v[208:211], v[108:111], 0\n\t" \
  "ds_read_b64_tr_b16 v[208:209], v248 offset:37416\n\t" \
  "v_and_b32 v232, s56, v232\n\t" \
  "v_and_b32_sdwa v235, s56, v234 dst_sel:DWORD dst_unused:UNUSED_PAD src0_sel:DWORD src1_sel:WORD_1\n\t" \
  "s_waitcnt lgkmcnt(7)\n\t" \
  "v_mfma_f32_16x16x32_bf16 v[144:147], v[212:215], v[112:115], v[144:147]\n\t" \
  "ds_read_b64_tr_b16 v[210:211], v248 offset:38440\n\t" \
  "ds_read_b32 v228, v228 offset:0\n\t" \
  "v_and_b32 v234, s56, v234\n\t" \
  "v_fma_f32 v181, |v181|, v221, v181 mul:4\n\t" \
  "v_mfma_f32_16x16x32_bf16 v[152:155], v[212:215], v[116:119], v[152:155]\n\t" \
  "ds_read_b32 v231, v231 offset:15376\n\t" \
  "ds_read_b32 v230, v230 offset:15376\n\t" \
  "v_fma_f32 v180, |v180|, v220, v180 mul:4\n\t" \
  "v_fma_mix_f32 v223, |v183|, v223, v223 op_sel:[0,1,0] op_sel_hi:[0,1,1] clamp\n\t" \
  "v_mfma_f32_16x16x32_bf16 v[160:163], v[212:215], v[120:123], v[160:163]\n\t" \
  "ds_read_b32 v233, v233 offset:0\n\t" \
  "v_fma_mix_f32 v222, |v182|, v222, v222 op_sel:[0,1,0] op_sel_hi:[0,1,1] clamp\n\t" \
  "s_waitcnt lgkmcnt(7)\n\t" \
  "v_fma_f32 v189, |v189|, v225, v189 mul:4\n\t" \
  "v_mfma_f32_16x16x32_bf16 v[168:171], v[212:215], v[124:127], v[168:171]\n\t" \
  "ds_read_b64_tr_b16 v[212:213], v248 offset:40488\n\t" \
  "v_fma_f32 v188, |v188|, v224, v188 mul:4\n\t" \
  "v_fma_mix_f32 v227, |v191|, v227, v227 op_sel:[0,1,0] op_sel_hi:[0,1,1] clamp\n\t" \
  "v_fma_mix_f32 v226, |v190|, v226, v226 op_sel:[0,1,0] op_sel_hi:[0,1,1] clamp\n\t" \
  "v_mfma_f32_16x16x32_bf16 v[144:147], v[216:219], v[128:131], v[144:147]\n\t" \
  "ds_read_b64_tr_b16 v[214:215], v248 offset:41512\n\t" \
  "v_fma_f32 v183, |v183|, v223, v183 mul:4\n\t" \
  "v_fma_f32 v182, |v182|, v222, v182 mul:4\n\t" \
  "v_mfma_f32_16x16x32_bf16 v[152:155], v[216:219], v[132:135], v[152:155]\n\t" \
  "ds_read_b32 v232, v232 offset:0\n\t" \
  "v_fma_f32 v191, |v191|, v227, v191 mul:4\n\t" \
  "v_fma_f32 v190, |v190|, v226, v190 mul:4\n\t" \
  "v_mfma_f32_16x16x32_bf16 v[160:163], v[216:219], v[136:139], v[160:163]\n\t" \
  "ds_read_b32 v235, v235 offset:0\n\t" \
  "v_cvt_pk_bf16_f32 v180, v180, v181\n\t" \
  "v_cvt_pk_bf16_f32 v181, v182, v183\n\t" \
  "v_mfma_f32_16x16x32_bf16 v[168:171], v[216:219], v[140:143], v[168:171]\n\t" \
  "ds_read_b64_tr_b16 v[216:217], v248 offset:39464\n\t" \
  "v_cvt_pk_bf16_f32 v188, v188, v189\n\t" \
  "v_cvt_pk_bf16_f32 v189, v190, v191\n\t" \
  "s_waitcnt lgkmcnt(9)\n\t" \
  "v_mfma_f32_16x16x32_bf16 v[148:151], v[208:211], v[96:99], 0\n\t" \
  "ds_read_b64_tr_b16 v[218:219], v248 offset:42536\n\t" \
  "v_cvt_pk_f16_f32 v220, |v146|, |v147| clamp\n\t" \
  "s_cbranch_vccnz .Lcnx_p10_3\n\ts_setprio 3\n.Lcnx_p10_3:\n\t" \
  "v_cvt_pknorm_i16_f32 v222, |v144|, |v145|\n\t" \
  "v_mfma_f32_16x16x32_bf16 v[156:159], v[208:211], v[100:103], 0\n\t" \
  "s_waitcnt vmcnt(1)\n\t" \
  "s_cmp_lg_u32 s58, 1\n\t" \
  "s_cbranch_scc1 .Lcnx_c18\n\t" \
  "s_waitcnt vmcnt(0)\n\t" \
  ".Lcnx_c18:\n\t" \
  "s_mov_b64 exec, 3\n\t" \
  "ds_add_rtn_u32 v246, v251, v250 offset:47216\n\t" \
  "s_mov_b64 exec, -1\n\t" \
  "ds_read_b32 v234, v234 offset:0\n\t" \
  "v_cvt_pk_f16_f32 v224, |v154|, |v155| clamp\n\t" \
  "v_cvt_pknorm_i16_f32 v226, |v152|, |v153|\n\t" \
  "v_mfma_f32_16x16x32_bf16 v[164:167], v[208:211], v[104:107], 0\n\t" \
  "v_and_b32_sdwa v221, s56, v220 dst_sel:DWORD dst_unused:UNUSED_PAD src0_sel:DWORD src1_sel:WORD_1\n\t" \
  "v_and_b32 v220, s56, v220\n\t" \
  "v_mfma_f32_16x16x32_bf16 v[172:175], v[208:211], v[108:111], 0\n\t" \
  "s_waitcnt lgkmcnt(1)\n\t" \
  "v_readfirstlane_b32 s4, v246\n\t" \
  "s_cmp_lt_u32 s4, s57\n\t" \
  "s_cbranch_scc1 .Lcnx_w120\n.Lcnx_b120:\n\t" \
  "ds_read_b64_tr_b16 v[208:209], v249 offset:30752\n\t" \
  "v_lshlrev_b32_sdwa v223, 2, v222 dst_sel:DWORD dst_unused:UNUSED_PAD src0_sel:DWORD src1_sel:BYTE_3\n\t" \
  "v_lshlrev_b32_sdwa v222, 2, v222 dst_sel:DWORD dst_unused:UNUSED_PAD src0_sel:DWORD src1_sel:BYTE_1\n\t" \
  "v_and_b32_sdwa v225, s56, v224 dst_sel:DWORD dst_unused:UNUSED_PAD src0_sel:DWORD src1_sel:WORD_1\n\t" \
  "v_mfma_f32_16x16x32_bf16 v[148:151], v[212:215], v[112:115], v[148:151]\n\t" \
  "ds_read_b64_tr_b16 v[210:211], v249 offset:32288\n\t" \
  "v_and_b32 v224, s56, v224\n\t" \
  "v_lshlrev_b32_sdwa v227, 2, v226 dst_sel:DWORD dst_unused:UNUSED_PAD src0_sel:DWORD src1_sel:BYTE_3\n\t" \
  "v_mfma_f32_16x16x32_bf16 v[156:159], v[212:215], v[116:119], v[156:159]\n\t" \
  "s_cmp_eq_u32 s37, 0\n\t" \
  "s_cbranch_scc1 .Lcnx_f11\n.Lcnx_g11:\n\t" \
  "ds_read_b32 v221, v221 offset:0\n\t" \
  "ds_read_b32 v220, v220 offset:0\n\t" \
  "v_lshlrev_b32_sdwa v226, 2, v226 dst_sel:DWORD dst_unused:UNUSED_PAD src0_sel:DWORD src1_sel:BYTE_1\n\t" \
  "v_fma_f32 v197, |v197|, v229, v197 mul:4\n\t" \
  "v_mfma_f32_16x16x32_bf16 v[164:167], v[212:215], v[120:123], v[164:167]\n\t" \
  "ds_read_b32 v223, v223 offset:15376\n\t" \
  "ds_read_b32 v222, v222 offset:15376\n\t" \
  "v_fma_f32 v196, |v196|, v228, v196 mul:4\n\t" \
  "v_fma_mix_f32 v231, |v199|, v231, v231 op_sel:[0,1,0] op_sel_hi:[0,1,1] clamp\n\t" \
  "v_mfma_f32_16x16x32_bf16 v[172:175], v[212:215], v[124:127], v[172:175]\n\t" \
  "ds_read_b64_tr_b16 v[212:213], v249 offset:30760\n\t" \
  "ds_read_b64_tr_b16 v[214:215], v249 offset:32296\n\t" \
  "v_fma_mix_f32 v230, |v198|, v230, v230 op_sel:[0,1,0] op_sel_hi:[0,1,1] clamp\n\t" \
  "s_waitcnt lgkmcnt(8)\n\t" \
  "v_fma_f32 v205, |v205|, v233, v205 mul:4\n\t" \
  "v_mfma_f32_16x16x32_bf16 v[148:151], v[216:219], v[128:131], v[148:151]\n\t" \
  "ds_read_b32 v225, v225 offset:0\n\t" \
  "v_fma_f32 v204, |v204|, v232, v204 mul:4\n\t" \
  "v_fma_f32 v207, |v207|, v235, v207 mul:4\n\t" \
  "v_mfma_f32_16x16x32_bf16 v[156:159], v[216:219], v[132:135], v[156:159]\n\t" \
  "ds_read_b32 v224, v224 offset:0\n\t" \
  "v_fma_f32 v206, |v206|, v234, v206 mul:4\n\t" \
  "v_fma_f32 v199, |v199|, v231, v199 mul:4\n\t" \
  "v_mfma_f32_16x16x32_bf16 v[164:167], v[216:219], v[136:139], v[164:167]\n\t" \
  "ds_read_b32 v227, v227 offset:15376\n\t" \
  "v_fma_f32 v198, |v198|, v230, v198 mul:4\n\t" \
  "v_cvt_pk_bf16_f32 v196, v196, v197\n\t" \
  "v_mfma_f32_16x16x32_bf16 v[172:175], v[216:219], v[140:143], v[172:175]\n\t" \
  "ds_read_b64_tr_b16 v[216:217], v249 offset:30816\n\t" \
  "v_cvt_pk_bf16_f32 v197, v198, v199\n\t" \
  "v_cvt_pk_bf16_f32 v204, v204, v205\n\t" \
  "v_cvt_pk_bf16_f32 v205, v206, v207\n\t" \
  "s_cbranch_vccnz .Lcnx_p11_0\n\ts_setprio 0\n.Lcnx_p11_0:\n\t" \
  "v_cvt_pk_f16_f32 v228, |v162|, |v163| clamp\n\t" \
  "v_cvt_pknorm_i16_f32 v230, |v160|, |v161|\n\t" \
  "v_mfma_f32_16x16x32_bf16 v[72:75], v[242:245], v[236:239], v[72:75]\n\t" \
  "ds_read_b64_tr_b16 v[218:219], v249 offset:32352\n\t" \
  "v_cvt_pk_f16_f32 v232, |v170|, |v171| clamp\n\t" \
  "v_cvt_pk_f16_f32 v234, |v168|, |v169| clamp\n\t" \
  "v_mfma_f32_16x16x32_bf16 v[76:79], v[240:243], v[236:239], v[76:79]\n\t" \
  "buffer_load_dwordx4 v[236:239], v250, s[8:11], s13 offen offset:64\n\t" \
  "s_cmp_lg_u32 s38, 7\n\t" \
  "s_cbranch_scc1 .Lcnx_c30\n\t" \
  "s_mov_b32 m0, 163632\n\t" \
  "s_add_u32 s30, s30, s31\n\t" \
  "buffer_load_dword v252, s[8:11], s30 offen lds\n\t" \
  ".Lcnx_c30:\n\t" \
  "ds_read_b32 v226, v226 offset:15376\n\t" \
  "v_and_b32_sdwa v229, s56, v228 dst_sel:DWORD dst_unused:UNUSED_PAD src0_sel:DWORD src1_sel:WORD_1\n\t" \
  "v_and_b32 v228, s56, v228\n\t" \
  "s_waitcnt lgkmcnt(12)\n\t" \
  "v_mfma_f32_16x16x32_bf16 v[0:3], v[208:211], v[178:181], v[0:3]\n\t" \
  "ds_read_b32 v229, v229 offset:0\n\t" \
  "v_lshlrev_b32_sdwa v231, 2, v230 dst_sel:DWORD dst_unused:UNUSED_PAD src0_sel:DWORD src1_sel:BYTE_3\n\t" \
  "v_lshlrev_b32_sdwa v230, 2, v230 dst_sel:DWORD dst_unused:UNUSED_PAD src0_sel:DWORD src1_sel:BYTE_1\n\t" \
  "v_mfma_f32_16x16x32_bf16 v[24:27], v[208:211], v[186:189], v[24:27]\n\t" \
  "ds_read_b32 v228, v228 offset:0\n\t" \
  "v_and_b32_sdwa v233, s56, v232 dst_sel:DWORD dst_unused:UNUSED_PAD src0_sel:DWORD src1_sel:WORD_1\n\t" \
  "v_and_b32 v232, s56, v232\n\t" \
  "v_mfma_f32_16x16x32_bf16 v[48:51], v[208:211], v[194:197], v[48:51]\n\t" \
  "ds_read_b32 v231, v231 offset:15376\n\t" \
  "v_and_b32_sdwa v235, s56, v234 dst_sel:DWORD dst_unused:UNUSED_PAD src0_sel:DWORD src1_sel:WORD_1\n\t" \
  "v_and_b32 v234, s56, v234\n\t" \
  "v_mfma_f32_16x16x32_bf16 v[72:75], v[208:211], v[202:205], v[72:75]\n\t" \
  "ds_read_b64_tr_b16 v[208:209], v249 offset:30824\n\t" \
  "ds_read_b64_tr_b16 v[210:211], v249 offset:32360\n\t" \
  "s_waitcnt lgkmcnt(13)\n\t" \
  "v_fma_f32 v147, |v147|, v221, v147 mul:4\n\t" \
  "v_fma_f32 v146, |v146|, v220, v146 mul:4\n\t" \
  "s_waitcnt lgkmcnt(11)\n\t" \
  "v_mfma_f32_16x16x32_bf16 v[4:7], v[212:215], v[178:181], v[4:7]\n\t" \
  "ds_read_b32 v230, v230 offset:15376\n\t" \
  "v_fma_mix_f32 v223, |v145|, v223, v223 op_sel:[0,1,0] op_sel_hi:[0,1,1] clamp\n\t" \
  "v_fma_mix_f32 v222, |v144|, v222, v222 op_sel:[0,1,0] op_sel_hi:[0,1,1] clamp\n\t" \
  "v_mfma_f32_16x16x32_bf16 v[28:31], v[212:215], v[186:189], v[28:31]\n\t" \
  "ds_read_b32 v233, v233 offset:0\n\t" \
  "s_waitcnt lgkmcnt(7)\n\t" \
  "v_fma_f32 v155, |v155|, v225, v155 mul:4\n\t" \
  "v_fma_f32 v154, |v154|, v224, v154 mul:4\n\t" \
  "v_mfma_f32_16x16x32_bf16 v[52:55], v[212:215], v[194:197], v[52:55]\n\t" \
  "ds_read_b32 v232, v232 offset:0\n\t" \
  "v_fma_mix_f32 v227, |v153|, v227, v227 op_sel:[0,1,0] op_sel_hi:[0,1,1] clamp\n\t" \
  "v_fma_mix_f32 v226, |v152|, v226, v226 op_sel:[0,1,0] op_sel_hi:[0,1,1] clamp\n\t" \
  "v_fma_f32 v145, |v145|, v223, v145 mul:4\n\t" \
  "v_mfma_f32_16x16x32_bf16 v[76:79], v[212:215], v[202:205], v[76:79]\n\t" \
  "ds_read_b64_tr_b16 v[212:213], v249 offset:30880\n\t" \
  "v_fma_f32 v144, |v144|, v222, v144 mul:4\n\t" \
  "v_fma_f32 v153, |v153|, v227, v153 mul:4\n\t" \
  "v_mfma_f32_16x16x32_bf16 v[8:11], v[216:219], v[178:181], v[8:11]\n\t" \
  "ds_read_b64_tr_b16 v[214:215], v249 offset:32416\n\t" \
  "v_fma_f32 v152, |v152|, v226, v152 mul:4\n\t" \
  "v_cvt_pk_bf16_f32 v147, v146, v147\n\t" \
  "v_mfma_f32_16x16x32_bf16 v[32:35], v[216:219], v[186:189], v[32:35]\n\t" \
  "ds_read_b32 v235, v235 offset:0\n\t" \
  "v_cvt_pk_bf16_f32 v146, v144, v145\n\t" \
  "v_cvt_pk_bf16_f32 v155, v154, v155\n\t" \
  "v_mfma_f32_16x16x32_bf16 v[56:59], v[216:219], v[194:197], v[56:59]\n\t" \
  "ds_read_b32 v234, v234 offset:0\n\t" \
  "v_cvt_pk_bf16_f32 v154, v152, v153\n\t" \
  "s_cbranch_vccnz .Lcnx_p11_1\n\ts_setprio 1\n.Lcnx_p11_1:\n\t" \
  "v_cvt_pk_f16_f32 v220, |v148|, |v149| clamp\n\t" \
  "v_mfma_f32_16x16x32_bf16 v[80:83], v[216:219], v[202:205], v[80:83]\n\t" \
  "ds_read_b64_tr_b16 v[216:217], v249 offset:30888\n\t" \
  "v_cvt_pknorm_i16_f32 v222, |v150|, |v151|\n\t" \
  "v_cvt_pk_f16_f32 v224, |v156|, |v157| clamp\n\t" \
  "s_waitcnt lgkmcnt(8)\n\t" \
  "v_mfma_f32_16x16x32_bf16 v[12:15], v[208:211], v[178:181], v[12:15]\n\t" \
  "ds_read_b64_tr_b16 v[218:219], v249 offset:32424\n\t" \
  "v_cvt_pknorm_i16_f32 v226, |v158|, |v159|\n\t" \
  "v_and_b32_sdwa v221, s56, v220 dst_sel:DWORD dst_unused:UNUSED_PAD src0_sel:DWORD src1_sel:WORD_1\n\t" \
  "v_mfma_f32_16x16x32_bf16 v[36:39], v[208:211], v[186:189], v[36:39]\n\t" \
  "v_and_b32 v220, s56, v220\n\t" \
  "v_lshlrev_b32_sdwa v223, 2, v222 dst_sel:DWORD dst_unused:UNUSED_PAD src0_sel:DWORD src1_sel:BYTE_3\n\t" \
  "v_mfma_f32_16x16x32_bf16 v[60:63], v[208:211], v[194:197], v[60:63]\n\t" \
  "ds_read_b32 v221, v221 offset:0\n\t" \
  "v_lshlrev_b32_sdwa v222, 2, v222 dst_sel:DWORD dst_unused:UNUSED_PAD src0_sel:DWORD src1_sel:BYTE_1\n\t" \
  "v_and_b32_sdwa v225, s56, v224 dst_sel:DWORD dst_unused:UNUSED_PAD src0_sel:DWORD src1_sel:WORD_1\n\t" \
  "v_and_b32 v224, s56, v224\n\t" \
  "v_mfma_f32_16x16x32_bf16 v[84:87], v[208:211], v[202:205], v[84:87]\n\t" \
  "ds_read_b64_tr_b16 v[208:209], v248 offset:0\n\t" \
  "ds_read_b64_tr_b16 v[210:211], v248 offset:1024\n\t" \
  "v_lshlrev_b32_sdwa v227, 2, v226 dst_sel:DWORD dst_unused:UNUSED_PAD src0_sel:DWORD src1_sel:BYTE_3\n\t" \
  "v_lshlrev_b32_sdwa v226, 2, v226 dst_sel:DWORD dst_unused:UNUSED_PAD src0_sel:DWORD src1_sel:BYTE_1\n\t" \
  "s_waitcnt lgkmcnt(7)\n\t" \
  "v_mfma_f32_16x16x32_bf16 v[16:19], v[212:215], v[178:181], v[16:19]\n\t" \
  "ds_read_b32 v220, v220 offset:0\n\t" \
  "ds_read_b32 v223, v223 offset:15376\n\t" \
  "v_fma_f32 v163, |v163|, v229, v163 mul:4\n\t" \
  "v_fma_f32 v162, |v162|, v228, v162 mul:4\n\t" \
  "v_mfma_f32_16x16x32_bf16 v[40:43], v[212:215], v[186:189], v[40:43]\n\t" \
  "ds_read_b32 v222, v222 offset:15376\n\t" \
  "v_fma_mix_f32 v231, |v161|, v231, v231 op_sel:[0,1,0] op_sel_hi:[0,1,1] clamp\n\t" \
  "v_fma_mix_f32 v230, |v160|, v230, v230 op_sel:[0,1,0] op_sel_hi:[0,1,1] clamp\n\t" \
  "v_mfma_f32_16x16x32_bf16 v[64:67], v[212:215], v[194:197], v[64:67]\n\t" \
  "ds_read_b32 v225, v225 offset:0\n\t" \
  "s_waitcnt lgkmcnt(9)\n\t" \
  "v_fma_f32 v171, |v171|, v233, v171 mul:4\n\t" \
  "v_fma_f32 v170, |v170|, v232, v170 mul:4\n\t" \
  "v_mfma_f32_16x16x32_bf16 v[88:91], v[212:215], v[202:205], v[88:91]\n\t" \
  "ds_read_b64_tr_b16 v[212:213], v248 offset:3072\n\t" \
  "v_fma_f32 v169, |v169|, v235, v169 mul:4\n\t" \
  "v_fma_f32 v168, |v168|, v234, v168 mul:4\n\t" \
  "s_waitcnt lgkmcnt(8)\n\t" \
  "v_mfma_f32_16x16x32_bf16 v[20:23], v[216:219], v[178:181], v[20:23]\n\t" \
  "ds_read_b64_tr_b16 v[214:215], v248 offset:4096\n\t" \
  "v_fma_f32 v161, |v161|, v231, v161 mul:4\n\t" \
  "v_fma_f32 v160, |v160|, v230, v160 mul:4\n\t" \
  "v_mfma_f32_16x16x32_bf16 v[44:47], v[216:219], v[186:189], v[44:47]\n\t" \
  "ds_read_b32 v224, v224 offset:0\n\t" \
  "v_cvt_pk_bf16_f32 v163, v162, v163\n\t" \
  "v_cvt_pk_bf16_f32 v162, v160, v161\n\t" \
  "v_mfma_f32_16x16x32_bf16 v[68:71], v[216:219], v[194:197], v[68:71]\n\t" \
  "ds_read_b32 v227, v227 offset:15376\n\t" \
  "v_cvt_pk_bf16_f32 v171, v170, v171\n\t" \
  "v_cvt_pk_bf16_f32 v170, v168, v169\n\t" \
  "v_cvt_pk_f16_f32 v228, |v164|, |v165| clamp\n\t" \
  "v_mfma_f32_16x16x32_bf16 v[92:95], v[216:219], v[202:205], v[92:95]\n\t" \
  "ds_read_b64_tr_b16 v[216:217], v248 offset:2048\n\t" \
  "v_cvt_pknorm_i16_f32 v230, |v166|, |v167|\n\t" \
  "s_cbranch_vccnz .Lcnx_p11_2\n\ts_setprio 2\n.Lcnx_p11_2:\n\t" \
  "v_cvt_pk_f16_f32 v232, |v172|, |v173| clamp\n\t" \
  "s_waitcnt lgkmcnt(9)\n\t" \
  "v_mfma_f32_16x16x32_bf16 v[176:179], v[208:211], v[96:99], 0\n\t" \
  "ds_read_b64_tr_b16 v[218:219], v248 offset:5120\n\t" \
  "v_cvt_pk_f16_f32 v234, |v174|, |v175| clamp\n\t" \
  "v_and_b32_sdwa v229, s56, v228 dst_sel:DWORD dst_unused:UNUSED_PAD src0_sel:DWORD src1_sel:WORD_1\n\t" \
  "v_mfma_f32_16x16x32_bf16 v[184:187], v[208:211], v[100:103], 0\n\t" \
  "ds_read_b32 v226, v226 offset:15376\n\t" \
  "v_and_b32 v228, s56, v228\n\t" \
  "v_lshlrev_b32_sdwa v231, 2, v230 dst_sel:DWORD dst_unused:UNUSED_PAD src0_sel:DWORD src1_sel:BYTE_3\n\t" \
  "v_mfma_f32_16x16x32_bf16 v[192:195], v[208:211], v[104:107], 0\n\t" \
  "ds_read_b32 v229, v229 offset:0\n\t" \
  "v_lshlrev_b32_sdwa v230, 2, v230 dst_sel:DWORD dst_unused:UNUSED_PAD src0_sel:DWORD src1_sel:BYTE_1\n\t" \
  "v_and_b32_sdwa v233, s56, v232 dst_sel:DWORD dst_unused:UNUSED_PAD src0_sel:DWORD src1_sel:WORD_1\n\t" \
  "v_mfma_f32_16x16x32_bf16 v[200:203], v[208:211], v[108:111], 0\n\t" \
  "ds_read_b64_tr_b16 v[208:209], v248 offset:512\n\t" \
  "v_and_b32 v232, s56, v232\n\t" \
  "v_and_b32_sdwa v235, s56, v234 dst_sel:DWORD dst_unused:UNUSED_PAD src0_sel:DWORD src1_sel:WORD_1\n\t" \
  "s_waitcnt lgkmcnt(7)\n\t" \
  "v_mfma_f32_16x16x32_bf16 v[176:179], v[212:215], v[112:115], v[176:179]\n\t" \
  "ds_read_b64_tr_b16 v[210:211], v248 offset:1536\n\t" \
  "ds_read_b32 v228, v228 offset:0\n\t" \
  "v_and_b32 v234, s56, v234\n\t" \
  "v_fma_f32 v149, |v149|, v221, v149 mul:4\n\t" \
  "v_mfma_f32_16x16x32_bf16 v[184:187], v[212:215], v[116:119], v[184:187]\n\t" \
  "ds_read_b32 v231, v231 offset:15376\n\t" \
  "ds_read_b32 v230, v230 offset:15376\n\t" \
  "v_fma_f32 v148, |v148|, v220, v148 mul:4\n\t" \
  "v_fma_mix_f32 v223, |v151|, v223, v223 op_sel:[0,1,0] op_sel_hi:[0,1,1] clamp\n\t" \
  "v_mfma_f32_16x16x32_bf16 v[192:195], v[212:215], v[120:123], v[192:195]\n\t" \
  "ds_read_b32 v233, v233 offset:0\n\t" \
  "v_fma_mix_f32 v222, |v150|, v222, v222 op_sel:[0,1,0] op_sel_hi:[0,1,1] clamp\n\t" \
  "s_waitcnt lgkmcnt(7)\n\t" \
  "v_fma_f32 v157, |v157|, v225, v157 mul:4\n\t" \
  "v_mfma_f32_16x16x32_bf16 v[200:203], v[212:215], v[124:127], v[200:203]\n\t" \
  "ds_read_b64_tr_b16 v[212:213], v248 offset:3584\n\t" \
  "v_fma_f32 v156, |v156|, v224, v156 mul:4\n\t" \
  "v_fma_mix_f32 v227, |v159|, v227, v227 op_sel:[0,1,0] op_sel_hi:[0,1,1] clamp\n\t" \
  "v_fma_mix_f32 v226, |v158|, v226, v226 op_sel:[0,1,0] op_sel_hi:[0,1,1] clamp\n\t" \
  "v_mfma_f32_16x16x32_bf16 v[176:179], v[216:219], v[128:131], v[176:179]\n\t" \
  "ds_read_b64_tr_b16 v[214:215], v248 offset:4608\n\t" \
  "v_fma_f32 v151, |v151|, v223, v151 mul:4\n\t" \
  "v_fma_f32 v150, |v150|, v222, v150 mul:4\n\t" \
  "v_mfma_f32_16x16x32_bf16 v[184:187], v[216:219], v[132:135], v[184:187]\n\t" \
  "ds_read_b32 v232, v232 offset:0\n\t" \
  "v_fma_f32 v159, |v159|, v227, v159 mul:4\n\t" \
  "v_fma_f32 v158, |v158|, v226, v158 mul:4\n\t" \
  "v_mfma_f32_16x16x32_bf16 v[192:195], v[216:219], v[136:139], v[192:195]\n\t" \
  "ds_read_b32 v235, v235 offset:0\n\t" \
  "v_cvt_pk_bf16_f32 v148, v148, v149\n\t" \
  "v_cvt_pk_bf16_f32 v149, v150, v151\n\t" \
  "v_mfma_f32_16x16x32_bf16 v[200:203], v[216:219], v[140:143], v[200:203]\n\t" \
  "ds_read_b64_tr_b16 v[216:217], v248 offset:2560\n\t" \
  "v_cvt_pk_bf16_f32 v156, v156, v157\n\t" \
  "v_cvt_pk_bf16_f32 v157, v158, v159\n\t" \
  "s_waitcnt lgkmcnt(9)\n\t" \
  "v_mfma_f32_16x16x32_bf16 v[180:183], v[208:211], v[96:99], 0\n\t" \
  "ds_read_b64_tr_b16 v[218:219], v248 offset:5632\n\t" \
  "v_cvt_pk_f16_f32 v220, |v178|, |v179| clamp\n\t" \
  "s_cbranch_vccnz .Lcnx_p11_3\n\ts_setprio 3\n.Lcnx_p11_3:\n\t" \
  "v_cvt_pknorm_i16_f32 v222, |v176|, |v177|\n\t" \
  "v_mfma_f32_16x16x32_bf16 v[188:191], v[208:211], v[100:103], 0\n\t" \
  "s_waitcnt vmcnt(0)\n\t" \
  "s_mov_b64 exec, 3\n\t" \
  "ds_add_rtn_u32 v246, v251, v250 offset:47224\n\t" \
  "s_mov_b64 exec, -1\n\t" \
  "ds_read_b32 v234, v234 offset:0\n\t" \
  "v_cvt_pk_f16_f32 v224, |v186|, |v187| clamp\n\t" \
  "v_cvt_pknorm_i16_f32 v226, |v184|, |v185|\n\t" \
  "v_mfma_f32_16x16x32_bf16 v[196:199], v[208:211], v[104:107], 0\n\t" \
  "v_and_b32_sdwa v221, s56, v220 dst_sel:DWORD dst_unused:UNUSED_PAD src0_sel:DWORD src1_sel:WORD_1\n\t" \
  "v_and_b32 v220, s56, v220\n\t" \
  "v_mfma_f32_16x16x32_bf16 v[204:207], v[208:211], v[108:111], 0\n\t" \
  "s_waitcnt lgkmcnt(1)\n\t" \
  "v_readfirstlane_b32 s4, v246\n\t" \
  "s_cmp_lt_u32 s4, s57\n\t" \
  "s_cbranch_scc1 .Lcnx_w132\n.Lcnx_b132:\n\t" \
  "ds_read_b64_tr_b16 v[208:209], v249 offset:6152\n\t" \
  "v_lshlrev_b32_sdwa v223, 2, v222 dst_sel:DWORD dst_unused:UNUSED_PAD src0_sel:DWORD src1_sel:BYTE_3\n\t" \
  "v_lshlrev_b32_sdwa v222, 2, v222 dst_sel:DWORD dst_unused:UNUSED_PAD src0_sel:DWORD src1_sel:BYTE_1\n\t" \
  "v_and_b32_sdwa v225, s56, v224 dst_sel:DWORD dst_unused:UNUSED_PAD src0_sel:DWORD src1_sel:WORD_1\n\t" \
  "v_mfma_f32_16x16x32_bf16 v[180:183], v[212:215], v[112:115], v[180:183]\n\t" \
  "ds_read_b64_tr_b16 v[210:211], v249 offset:7688\n\t" \
  "v_and_b32 v224, s56, v224\n\t" \
  "v_lshlrev_b32_sdwa v227, 2, v226 dst_sel:DWORD dst_unused:UNUSED_PAD src0_sel:DWORD src1_sel:BYTE_3\n\t" \
  "v_mfma_f32_16x16x32_bf16 v[188:191], v[212:215], v[116:119], v[188:191]\n\t" \
  "ds_read_b32 v221, v221 offset:0\n\t" \
  "ds_read_b32 v220, v220 offset:0\n\t" \
  "v_lshlrev_b32_sdwa v226, 2, v226 dst_sel:DWORD dst_unused:UNUSED_PAD src0_sel:DWORD src1_sel:BYTE_1\n\t" \
  "v_fma_f32 v165, |v165|, v229, v165 mul:4\n\t" \
  "v_mfma_f32_16x16x32_bf16 v[196:199], v[212:215], v[120:123], v[196:199]\n\t" \
  "ds_read_b32 v223, v223 offset:15376\n\t" \
  "ds_read_b32 v222, v222 offset:15376\n\t" \
  "v_fma_f32 v164, |v164|, v228, v164 mul:4\n\t" \
  "v_fma_mix_f32 v231, |v167|, v231, v231 op_sel:[0,1,0] op_sel_hi:[0,1,1] clamp\n\t" \
  "v_mfma_f32_16x16x32_bf16 v[204:207], v[212:215], v[124:127], v[204:207]\n\t" \
  "ds_read_b64_tr_b16 v[212:213], v249 offset:6160\n\t" \
  "ds_read_b64_tr_b16 v[214:215], v249 offset:7696\n\t" \
  "v_fma_mix_f32 v230, |v166|, v230, v230 op_sel:[0,1,0] op_sel_hi:[0,1,1] clamp\n\t" \
  "s_waitcnt lgkmcnt(8)\n\t" \
  "v_fma_f32 v173, |v173|, v233, v173 mul:4\n\t" \
  "v_mfma_f32_16x16x32_bf16 v[180:183], v[216:219], v[128:131], v[180:183]\n\t" \
  "ds_read_b32 v225, v225 offset:0\n\t" \
  "v_fma_f32 v172, |v172|, v232, v172 mul:4\n\t" \
  "v_fma_f32 v175, |v175|, v235, v175 mul:4\n\t" \
  "v_mfma_f32_16x16x32_bf16 v[188:191], v[216:219], v[132:135], v[188:191]\n\t" \
  "ds_read_b32 v224, v224 offset:0\n\t" \
  "v_fma_f32 v174, |v174|, v234, v174 mul:4\n\t" \
  "v_fma_f32 v167, |v167|, v231, v167 mul:4\n\t" \
  "v_mfma_f32_16x16x32_bf16 v[196:199], v[216:219], v[136:139], v[196:199]\n\t" \
  "ds_read_b32 v227, v227 offset:15376\n\t" \
  "v_fma_f32 v166, |v166|, v230, v166 mul:4\n\t" \
  "v_cvt_pk_bf16_f32 v164, v164, v165\n\t" \
  "v_mfma_f32_16x16x32_bf16 v[204:207], v[216:219], v[140:143], v[204:207]\n\t" \
  "ds_read_b64_tr_b16 v[216:217], v249 offset:6216\n\t" \
  "v_cvt_pk_bf16_f32 v165, v166, v167\n\t" \
  "v_cvt_pk_bf16_f32 v172, v172, v173\n\t" \
  "v_cvt_pk_bf16_f32 v173, v174, v175\n\t" \
  "s_cbranch_vccnz .Lcnx_p12_0\n\ts_setprio 3\n.Lcnx_p12_0:\n\t" \
  "v_cvt_pk_f16_f32 v228, |v194|, |v195| clamp\n\t" \
  "v_cvt_pknorm_i16_f32 v230, |v192|, |v193|\n\t" \
  "v_mfma_f32_16x16x32_bf16 v[80:83], v[242:245], v[236:239], v[80:83]\n\t" \
  "ds_read_b64_tr_b16 v[218:219], v249 offset:7752\n\t" \
  "v_cvt_pk_f16_f32 v232, |v202|, |v203| clamp\n\t" \
  "v_cvt_pk_f16_f32 v234, |v200|, |v201| clamp\n\t" \
  "v_and_b32_sdwa v229, s56, v228 dst_sel:DWORD dst_unused:UNUSED_PAD src0_sel:DWORD src1_sel:WORD_1\n\t" \
  "v_mfma_f32_16x16x32_bf16 v[84:87], v[240:243], v[236:239], v[84:87]\n\t" \
  "buffer_load_dwordx4 v[236:239], v250, s[8:11], s13 offen offset:128\n\t" \
  "s_cmp_lg_u32 s38, 4\n\t" \
  "s_cbranch_scc1 .Lcnx_c31\n\t" \
  "s_mov_b32 m0, 163632\n\t" \
  "s_add_u32 s30, s30, s31\n\t" \
  "buffer_load_dword v252, s[8:11], s30 offen lds\n\t" \
  ".Lcnx_c31:\n\t" \
  "ds_read_b32 v226, v226 offset:15376\n\t" \
  "v_and_b32 v228, s56, v228\n\t" \
  "v_lshlrev_b32_sdwa v231, 2, v230 dst_sel:DWORD dst_unused:UNUSED_PAD src0_sel:DWORD src1_sel:BYTE_3\n\t" \
  "v_lshlrev_b32_sdwa v230, 2, v230 dst_sel:DWORD dst_unused:UNUSED_PAD src0_sel:DWORD src1_sel:BYTE_1\n\t" \
  "v_and_b32_sdwa v233, s56, v232 dst_sel:DWORD dst_unused:UNUSED_PAD src0_sel:DWORD src1_sel:WORD_1\n\t" \
  "s_waitcnt lgkmcnt(12)\n\t" \
  "v_mfma_f32_16x16x32_bf16 v[0:3], v[208:211], v[146:149], v[0:3]\n\t" \
  "ds_read_b32 v229, v229 offset:0\n\t" \
  "v_and_b32 v232, s56, v232\n\t" \
  "v_and_b32_sdwa v235, s56, v234 dst_sel:DWORD dst_unused:UNUSED_PAD src0_sel:DWORD src1_sel:WORD_1\n\t" \
  "v_and_b32 v234, s56, v234\n\t" \
  "v_mfma_f32_16x16x32_bf16 v[24:27], v[208:211], v[154:157], v[24:27]\n\t" \
  "ds_read_b32 v228, v228 offset:0\n\t" \
  "ds_read_b32 v231, v231 offset:15376\n\t" \
  "s_waitcnt lgkmcnt(11)\n\t" \
  "v_fma_f32 v179, |v179|, v221, v179 mul:4\n\t" \
  "v_fma_f32 v178, |v178|, v220, v178 mul:4\n\t" \
  "v_fma_mix_f32 v223, |v177|, v223, v223 op_sel:[0,1,0] op_sel_hi:[0,1,1] clamp\n\t" \
  "v_fma_mix_f32 v222, |v176|, v222, v222 op_sel:[0,1,0] op_sel_hi:[0,1,1] clamp\n\t" \
  "v_mfma_f32_16x16x32_bf16 v[48:51], v[208:211], v[162:165], v[48:51]\n\t" \
  "ds_read_b32 v230, v230 offset:15376\n\t" \
  "s_waitcnt lgkmcnt(4)\n\t" \
  "v_fma_f32 v187, |v187|, v225, v187 mul:4\n\t" \
  "v_fma_f32 v186, |v186|, v224, v186 mul:4\n\t" \
  "v_fma_mix_f32 v227, |v185|, v227, v227 op_sel:[0,1,0] op_sel_hi:[0,1,1] clamp\n\t" \
  "v_fma_mix_f32 v226, |v184|, v226, v226 op_sel:[0,1,0] op_sel_hi:[0,1,1] clamp\n\t" \
  "v_mfma_f32_16x16x32_bf16 v[72:75], v[208:211], v[170:173], v[72:75]\n\t" \
  "ds_read_b64_tr_b16 v[208:209], v249 offset:6224\n\t" \
  "ds_read_b64_tr_b16 v[210:211], v249 offset:7760\n\t" \
  "v_fma_f32 v177, |v177|, v223, v177 mul:4\n\t" \
  "v_fma_f32 v176, |v176|, v222, v176 mul:4\n\t" \
  "v_fma_f32 v185, |v185|, v227, v185 mul:4\n\t" \
  "v_mfma_f32_16x16x32_bf16 v[4:7], v[212:215], v[146:149], v[4:7]\n\t" \
  "ds_read_b32 v233, v233 offset:0\n\t" \
  "v_fma_f32 v184, |v184|, v226, v184 mul:4\n\t" \
  "v_cvt_pk_bf16_f32 v179, v178, v179\n\t" \
  "s_cbranch_vccnz .Lcnx_p12_1\n\ts_setprio 2\n.Lcnx_p12_1:\n\t" \
  "v_cvt_pk_bf16_f32 v178, v176, v177\n\t" \
  "v_cvt_pk_bf16_f32 v187, v186, v187\n\t" \
  "v_mfma_f32_16x16x32_bf16 v[28:31], v[212:215], v[154:157], v[28:31]\n\t" \
  "ds_read_b32 v232, v232 offset:0\n\t" \
  "v_cvt_pk_bf16_f32 v186, v184, v185\n\t" \
  "v_cvt_pk_f16_f32 v220, |v180|, |v181| clamp\n\t" \
  "v_cvt_pknorm_i16_f32 v222, |v182|, |v183|\n\t" \
  "v_mfma_f32_16x16x32_bf16 v[52:55], v[212:215], v[162:165], v[52:55]\n\t" \
  "ds_read_b32 v235, v235 offset:0\n\t" \
  "v_cvt_pk_f16_f32 v224, |v188|, |v189| clamp\n\t" \
  "v_cvt_pknorm_i16_f32 v226, |v190|, |v191|\n\t" \
  "v_and_b32_sdwa v221, s56, v220 dst_sel:DWORD dst_unused:UNUSED_PAD src0_sel:DWORD src1_sel:WORD_1\n\t" \
  "v_and_b32 v220, s56, v220\n\t" \
  "v_mfma_f32_16x16x32_bf16 v[76:79], v[212:215], v[170:173], v[76:79]\n\t" \
  "ds_read_b64_tr_b16 v[212:213], v249 offset:6280\n\t" \
  "v_lshlrev_b32_sdwa v223, 2, v222 dst_sel:DWORD dst_unused:UNUSED_PAD src0_sel:DWORD src1_sel:BYTE_3\n\t" \
  "v_lshlrev_b32_sdwa v222, 2, v222 dst_sel:DWORD dst_unused:UNUSED_PAD src0_sel:DWORD src1_sel:BYTE_1\n\t" \
  "v_and_b32_sdwa v225, s56, v224 dst_sel:DWORD dst_unused:UNUSED_PAD src0_sel:DWORD src1_sel:WORD_1\n\t" \
  "v_and_b32 v224, s56, v224\n\t" \
  "v_mfma_f32_16x16x32_bf16 v[8:11], v[216:219], v[146:149], v[8:11]\n\t" \
  "ds_read_b64_tr_b16 v[214:215], v249 offset:7816\n\t" \
  "ds_read_b32 v234, v234 offset:0\n\t" \
  "v_lshlrev_b32_sdwa v227, 2, v226 dst_sel:DWORD dst_unused:UNUSED_PAD src0_sel:DWORD src1_sel:BYTE_3\n\t" \
  "v_lshlrev_b32_sdwa v226, 2, v226 dst_sel:DWORD dst_unused:UNUSED_PAD src0_sel:DWORD src1_sel:BYTE_1\n\t" \
  "s_waitcnt lgkmcnt(8)\n\t" \
  "v_fma_f32 v195, |v195|, v229, v195 mul:4\n\t" \
  "v_mfma_f32_16x16x32_bf16 v[32:35], v[216:219], v[154:157], v[32:35]\n\t" \
  "ds_read_b32 v221, v221 offset:0\n\t" \
  "ds_read_b32 v220, v220 offset:0\n\t" \
  "v_fma_f32 v194, |v194|, v228, v194 mul:4\n\t" \
  "v_fma_mix_f32 v231, |v193|, v231, v231 op_sel:[0,1,0] op_sel_hi:[0,1,1] clamp\n\t" \
  "v_fma_mix_f32 v230, |v192|, v230, v230 op_sel:[0,1,0] op_sel_hi:[0,1,1] clamp\n\t" \
  "s_waitcnt lgkmcnt(2)\n\t" \
  "v_fma_f32 v203, |v203|, v233, v203 mul:4\n\t" \
  "v_mfma_f32_16x16x32_bf16 v[56:59], v[216:219], v[162:165], v[56:59]\n\t" \
  "ds_read_b32 v223, v223 offset:15376\n\t" \
  "ds_read_b32 v222, v222 offset:15376\n\t" \
  "v_fma_f32 v202, |v202|, v232, v202 mul:4\n\t" \
  "v_fma_f32 v201, |v201|, v235, v201 mul:4\n\t" \
  "v_fma_f32 v200, |v200|, v234, v200 mul:4\n\t" \
  "v_fma_f32 v193, |v193|, v231, v193 mul:4\n\t" \
  "v_mfma_f32_16x16x32_bf16 v[80:83], v[216:219], v[170:173], v[80:83]\n\t" \
  "ds_read_b64_tr_b16 v[216:217], v249 offset:6288\n\t" \
  "ds_read_b64_tr_b16 v[218:219], v249 offset:7824\n\t" \
  "s_cbranch_vccnz .Lcnx_p12_2\n\ts_setprio 1\n.Lcnx_p12_2:\n\t" \
  "v_fma_f32 v192, |v192|, v230, v192 mul:4\n\t" \
  "v_cvt_pk_bf16_f32 v195, v194, v195\n\t" \
  "v_cvt_pk_bf16_f32 v194, v192, v193\n\t" \
  "v_mfma_f32_16x16x32_bf16 v[12:15], v[208:211], v[146:149], v[12:15]\n\t" \
  "ds_read_b32 v225, v225 offset:0\n\t" \
  "v_cvt_pk_bf16_f32 v203, v202, v203\n\t" \
  "v_cvt_pk_bf16_f32 v202, v200, v201\n\t" \
  "v_cvt_pk_f16_f32 v228, |v196|, |v197| clamp\n\t" \
  "v_cvt_pknorm_i16_f32 v230, |v198|, |v199|\n\t" \
  "v_mfma_f32_16x16x32_bf16 v[36:39], v[208:211], v[154:157], v[36:39]\n\t" \
  "s_waitcnt vmcnt(0)\n\t" \
  "s_mov_b64 exec, 3\n\t" \
  "ds_add_rtn_u32 v246, v251, v250 offset:47232\n\t" \
  "s_mov_b64 exec, -1\n\t" \
  "ds_read_b32 v224, v224 offset:0\n\t" \
  "v_cvt_pk_f16_f32 v232, |v204|, |v205| clamp\n\t" \
  "v_cvt_pk_f16_f32 v234, |v206|, |v207| clamp\n\t" \
  "v_and_b32_sdwa v229, s56, v228 dst_sel:DWORD dst_unused:UNUSED_PAD src0_sel:DWORD src1_sel:WORD_1\n\t" \
  "v_mfma_f32_16x16x32_bf16 v[60:63], v[208:211], v[162:165], v[60:63]\n\t" \
  "ds_read_b32 v227, v227 offset:15376\n\t" \
  "v_and_b32 v228, s56, v228\n\t" \
  "v_lshlrev_b32_sdwa v231, 2, v230 dst_sel:DWORD dst_unused:UNUSED_PAD src0_sel:DWORD src1_sel:BYTE_3\n\t" \
  "v_lshlrev_b32_sdwa v230, 2, v230 dst_sel:DWORD dst_unused:UNUSED_PAD src0_sel:DWORD src1_sel:BYTE_1\n\t" \
  "v_and_b32_sdwa v233, s56, v232 dst_sel:DWORD dst_unused:UNUSED_PAD src0_sel:DWORD src1_sel:WORD_1\n\t" \
  "v_mfma_f32_16x16x32_bf16 v[84:87], v[208:211], v[170:173], v[84:87]\n\t" \
  "s_waitcnt lgkmcnt(2)\n\t" \
  "v_readfirstlane_b32 s4, v246\n\t" \
  "s_cmp_lt_u32 s4, s57\n\t" \
  "s_cbranch_scc1 .Lcnx_w138\n.Lcnx_b138:\n\t" \
  "ds_read_b64_tr_b16 v[208:209], v249 offset:12304\n\t" \
  "ds_read_b64_tr_b16 v[210:211], v249 offset:13840\n\t" \
  "v_and_b32 v232, s56, v232\n\t" \
  "v_and_b32_sdwa v235, s56, v234 dst_sel:DWORD dst_unused:UNUSED_PAD src0_sel:DWORD src1_sel:WORD_1\n\t" \
  "v_and_b32 v234, s56, v234\n\t" \
  "ds_read_b32 v226, v226 offset:15376\n\t" \
  "ds_read_b32 v229, v229 offset:0\n\t" \
  "ds_read_b32 v228, v228 offset:0\n\t" \
  "ds_read_b32 v231, v231 offset:15376\n\t" \
  "ds_read_b32 v230, v230 offset:15376\n\t" \
  "ds_read_b32 v233, v233 offset:0\n\t" \
  "ds_read_b32 v232, v232 offset:0\n\t" \
  "ds_read_b32 v235, v235 offset:0\n\t" \
  "v_fma_f32 v181, |v181|, v221, v181 mul:4\n\t" \
  "s_cbranch_vccnz .Lcnx_p12_3\n\ts_setprio 0\n.Lcnx_p12_3:\n\t" \
  "v_mfma_f32_16x16x32_bf16 v[16:19], v[212:215], v[146:149], v[16:19]\n\t" \
  "ds_read_b32 v234, v234 offset:0\n\t" \
  "v_fma_f32 v180, |v180|, v220, v180 mul:4\n\t" \
  "v_fma_mix_f32 v223, |v183|, v223, v223 op_sel:[0,1,0] op_sel_hi:[0,1,1] clamp\n\t" \
  "v_fma_mix_f32 v222, |v182|, v222, v222 op_sel:[0,1,0] op_sel_hi:[0,1,1] clamp\n\t" \
  "v_mfma_f32_16x16x32_bf16 v[40:43], v[212:215], v[154:157], v[40:43]\n\t" \
  "s_waitcnt lgkmcnt(8)\n\t" \
  "v_fma_f32 v189, |v189|, v225, v189 mul:4\n\t" \
  "v_fma_f32 v188, |v188|, v224, v188 mul:4\n\t" \
  "v_fma_mix_f32 v227, |v191|, v227, v227 op_sel:[0,1,0] op_sel_hi:[0,1,1] clamp\n\t" \
  "v_fma_mix_f32 v226, |v190|, v226, v226 op_sel:[0,1,0] op_sel_hi:[0,1,1] clamp\n\t" \
  "v_mfma_f32_16x16x32_bf16 v[64:67], v[212:215], v[162:165], v[64:67]\n\t" \
  "v_fma_f32 v183, |v183|, v223, v183 mul:4\n\t" \
  "v_fma_f32 v182, |v182|, v222, v182 mul:4\n\t" \
  "v_fma_f32 v191, |v191|, v227, v191 mul:4\n\t" \
  "v_mfma_f32_16x16x32_bf16 v[88:91], v[212:215], v[170:173], v[88:91]\n\t" \
  "ds_read_b64_tr_b16 v[212:213], v249 offset:12312\n\t" \
  "v_fma_f32 v190, |v190|, v226, v190 mul:4\n\t" \
  "v_cvt_pk_bf16_f32 v180, v180, v181\n\t" \
  "v_cvt_pk_bf16_f32 v181, v182, v183\n\t" \
  "v_cvt_pk_bf16_f32 v188, v188, v189\n\t" \
  "v_mfma_f32_16x16x32_bf16 v[20:23], v[216:219], v[146:149], v[20:23]\n\t" \
  "ds_read_b64_tr_b16 v[214:215], v249 offset:13848\n\t" \
  "v_cvt_pk_bf16_f32 v189, v190, v191\n\t" \
  "s_waitcnt lgkmcnt(6)\n\t" \
  "v_fma_f32 v197, |v197|, v229, v197 mul:4\n\t" \
  "v_fma_f32 v196, |v196|, v228, v196 mul:4\n\t" \
  "v_fma_mix_f32 v231, |v199|, v231, v231 op_sel:[0,1,0] op_sel_hi:[0,1,1] clamp\n\t" \
  "v_mfma_f32_16x16x32_bf16 v[44:47], v[216:219], v[154:157], v[44:47]\n\t" \
  "v_fma_mix_f32 v230, |v198|, v230, v230 op_sel:[0,1,0] op_sel_hi:[0,1,1] clamp\n\t" \
  "s_waitcnt lgkmcnt(2)\n\t" \
  "v_fma_f32 v205, |v205|, v233, v205 mul:4\n\t" \
  "v_fma_f32 v204, |v204|, v232, v204 mul:4\n\t" \
  "v_mfma_f32_16x16x32_bf16 v[68:71], v[216:219], v[162:165], v[68:71]\n\t" \
  "v_fma_f32 v207, |v207|, v235, v207 mul:4\n\t" \
  "v_fma_f32 v206, |v206|, v234, v206 mul:4\n\t" \
  "v_fma_f32 v199, |v199|, v231, v199 mul:4\n\t" \
  "v_fma_f32 v198, |v198|, v230, v198 mul:4\n\t" \
  "v_mfma_f32_16x16x32_bf16 v[92:95], v[216:219], v[170:173], v[92:95]\n\t" \
  "ds_read_b64_tr_b16 v[216:217], v249 offset:12368\n\t" \
  "v_cvt_pk_bf16_f32 v196, v196, v197\n\t" \
  "v_cvt_pk_bf16_f32 v197, v198, v199\n\t" \
  "v_cvt_pk_bf16_f32 v204, v204, v205\n\t" \
  "v_cvt_pk_bf16_f32 v205, v206, v207\n\t" \
  "s_cmp_lg_u32 s58, 0\n\t" \
  "s_cbranch_scc1 .Lcnx_fin\n\t" \
  "v_mfma_f32_32x32x16_bf16 v[144:159], v[242:245], v[240:243], 0\n\t" \
  "v_mfma_f32_32x32x16_bf16 v[160:175], v[242:245], v[240:243], 0\n\t" \
  "v_mfma_f32_16x16x32_bf16 v[88:91], v[242:245], v[236:239], v[88:91]\n\t" \
  "ds_read_b64_tr_b16 v[218:219], v249 offset:13904\n\t" \
  "v_add_u32 v232, 1536, v247\n\t" \
  "v_mfma_f32_16x16x32_bf16 v[92:95], v[240:243], v[236:239], v[92:95]\n\t" \
  "ds_read2_b32 v[98:99], v247 offset0:0 offset1:6\n\t" \
  "v_mfma_f32_16x16x32_bf16 v[0:3], v[208:211], v[178:181], v[0:3]\n\t" \
  "ds_read2_b32 v[114:115], v247 offset0:192 offset1:198\n\t" \
  "v_mfma_f32_16x16x32_bf16 v[24:27], v[208:211], v[186:189], v[24:27]\n\t" \
  "ds_read2_b32 v[130:131], v232 offset0:0 offset1:6\n\t" \
  "v_mfma_f32_16x16x32_bf16 v[48:51], v[208:211], v[194:197], v[48:51]\n\t" \
  "ds_read2_b32 v[102:103], v247 offset0:12 offset1:18\n\t" \
  "v_mfma_f32_16x16x32_bf16 v[72:75], v[208:211], v[202:205], v[72:75]\n\t" \
  "ds_read_b64_tr_b16 v[208:209], v249 offset:12376\n\t" \
  "ds_read_b64_tr_b16 v[210:211], v249 offset:13912\n\t" \
  "v_bfe_u32 v236, v251, 3, 2\n\t" \
  "s_waitcnt lgkmcnt(8)\n\t" \
  "v_mfma_f32_16x16x32_bf16 v[4:7], v[212:215], v[178:181], v[4:7]\n\t" \
  "ds_read2_b32 v[118:119], v247 offset0:204 offset1:210\n\t" \
  "ds_read2_b32 v[134:135], v232 offset0:12 offset1:18\n\t" \
  "v_mul_u32_u24 v236, 0x5555, v236\n\t" \
  "v_mfma_f32_16x16x32_bf16 v[28:31], v[212:215], v[186:189], v[28:31]\n\t" \
  "ds_read2_b32 v[106:107], v247 offset0:24 offset1:30\n\t" \
  "ds_read2_b32 v[122:123], v247 offset0:216 offset1:222\n\t" \
  "v_mfma_f32_16x16x32_bf16 v[52:55], v[212:215], v[194:197], v[52:55]\n\t" \
  "ds_read2_b32 v[138:139], v232 offset0:24 offset1:30\n\t" \
  "v_mfma_f32_16x16x32_bf16 v[76:79], v[212:215], v[202:205], v[76:79]\n\t" \
  "ds_read_b64_tr_b16 v[212:213], v249 offset:12432\n\t" \
  "s_waitcnt lgkmcnt(12)\n\t" \
  "v_mfma_f32_16x16x32_bf16 v[8:11], v[216:219], v[178:181], v[8:11]\n\t" \
  "ds_read_b64_tr_b16 v[214:215], v249 offset:13968\n\t" \
  "v_mfma_f32_16x16x32_bf16 v[32:35], v[216:219], v[186:189], v[32:35]\n\t" \
  "ds_read2_b32 v[110:111], v247 offset0:36 offset1:42\n\t" \
  "v_mfma_f32_16x16x32_bf16 v[56:59], v[216:219], v[194:197], v[56:59]\n\t" \
  "ds_read2_b32 v[126:127], v247 offset0:228 offset1:234\n\t" \
  "v_mfma_f32_16x16x32_bf16 v[80:83], v[216:219], v[202:205], v[80:83]\n\t" \
  "ds_read_b64_tr_b16 v[216:217], v249 offset:12440\n\t" \
  "v_cvt_pk_bf16_f32 v48, v48, v49\n\t" \
  "v_cvt_pk_bf16_f32 v49, v50, v51\n\t" \
  "v_cvt_pk_bf16_f32 v50, v52, v53\n\t" \
  "v_cvt_pk_bf16_f32 v51, v54, v55\n\t" \
  "v_cvt_pk_bf16_f32 v52, v0, v1\n\t" \
  "v_cvt_pk_bf16_f32 v53, v2, v3\n\t" \
  "v_cvt_pk_bf16_f32 v54, v4, v5\n\t" \
  "v_cvt_pk_bf16_f32 v55, v6, v7\n\t" \
  "v_cvt_pk_bf16_f32 v72, v72, v73\n\t" \
  "v_cvt_pk_bf16_f32 v73, v74, v75\n\t" \
  "v_cvt_pk_bf16_f32 v74, v76, v77\n\t" \
  "v_cvt_pk_bf16_f32 v75, v78, v79\n\t" \
  "v_cvt_pk_bf16_f32 v76, v24, v25\n\t" \
  "v_cvt_pk_bf16_f32 v77, v26, v27\n\t" \
  "v_cvt_pk_bf16_f32 v78, v28, v29\n\t" \
  "v_cvt_pk_bf16_f32 v79, v30, v31\n\t" \
  "s_waitcnt lgkmcnt(10)\n\t" \
  "v_mfma_f32_16x16x32_bf16 v[12:15], v[208:211], v[178:181], v[12:15]\n\t" \
  "ds_read_b64_tr_b16 v[218:219], v249 offset:13976\n\t" \
  "v_mfma_f32_16x16x32_bf16 v[36:39], v[208:211], v[186:189], v[36:39]\n\t" \
  "ds_read2_b32 v[142:143], v232 offset0:36 offset1:42\n\t" \
  "v_mfma_f32_16x16x32_bf16 v[60:63], v[208:211], v[194:197], v[60:63]\n\t" \
  "v_mfma_f32_16x16x32_bf16 v[84:87], v[208:211], v[202:205], v[84:87]\n\t" \
  "s_waitcnt lgkmcnt(5)\n\t" \
  "v_mfma_f32_16x16x32_bf16 v[16:19], v[212:215], v[178:181], v[16:19]\n\t" \
  "v_mfma_f32_16x16x32_bf16 v[40:43], v[212:215], v[186:189], v[40:43]\n\t" \
  "v_mfma_f32_16x16x32_bf16 v[64:67], v[212:215], v[194:197], v[64:67]\n\t" \
  "v_mfma_f32_16x16x32_bf16 v[88:91], v[212:215], v[202:205], v[88:91]\n\t" \
  "v_cvt_pk_bf16_f32 v56, v56, v57\n\t" \
  "v_cvt_pk_bf16_f32 v57, v58, v59\n\t" \
  "v_cvt_pk_bf16_f32 v58, v60, v61\n\t" \
  "v_cvt_pk_bf16_f32 v59, v62, v63\n\t" \
  "v_cvt_pk_bf16_f32 v60, v8, v9\n\t" \
  "v_cvt_pk_bf16_f32 v61, v10, v11\n\t" \
  "v_cvt_pk_bf16_f32 v62, v12, v13\n\t" \
  "v_cvt_pk_bf16_f32 v63, v14, v15\n\t" \
  "v_cvt_pk_bf16_f32 v80, v80, v81\n\t" \
  "v_cvt_pk_bf16_f32 v81, v82, v83\n\t" \
  "v_cvt_pk_bf16_f32 v82, v84, v85\n\t" \
  "v_cvt_pk_bf16_f32 v83, v86, v87\n\t" \
  "v_cvt_pk_bf16_f32 v84, v32, v33\n\t" \
  "v_cvt_pk_bf16_f32 v85, v34, v35\n\t" \
  "v_cvt_pk_bf16_f32 v86, v36, v37\n\t" \
  "v_cvt_pk_bf16_f32 v87, v38, v39\n\t" \
  "s_waitcnt lgkmcnt(1)\n\t" \
  "v_mfma_f32_16x16x32_bf16 v[20:23], v[216:219], v[178:181], v[20:23]\n\t" \
  "v_mfma_f32_16x16x32_bf16 v[44:47], v[216:219], v[186:189], v[44:47]\n\t" \
  "v_mfma_f32_16x16x32_bf16 v[68:71], v[216:219], v[194:197], v[68:71]\n\t" \
  "v_mfma_f32_16x16x32_bf16 v[92:95], v[216:219], v[202:205], v[92:95]\n\t" \
  "v_cvt_pk_bf16_f32 v64, v64, v65\n\t" \
  "v_cvt_pk_bf16_f32 v65, v66, v67\n\t" \
  "s_nop 4\n\t" \
  "v_cvt_pk_bf16_f32 v66, v68, v69\n\t" \
  "v_cvt_pk_bf16_f32 v67, v70, v71\n\t" \
  "v_cvt_pk_bf16_f32 v68, v16, v17\n\t" \
  "v_cvt_pk_bf16_f32 v69, v18, v19\n\t" \
  "v_cvt_pk_bf16_f32 v70, v20, v21\n\t" \
  "v_cvt_pk_bf16_f32 v71, v22, v23\n\t" \
  "v_cvt_pk_bf16_f32 v88, v88, v89\n\t" \
  "v_cvt_pk_bf16_f32 v89, v90, v91\n\t" \
  "v_cvt_pk_bf16_f32 v90, v92, v93\n\t" \
  "v_cvt_pk_bf16_f32 v91, v94, v95\n\t" \
  "v_cvt_pk_bf16_f32 v92, v40, v41\n\t" \
  "v_cvt_pk_bf16_f32 v93, v42, v43\n\t" \
  "v_cvt_pk_bf16_f32 v94, v44, v45\n\t" \
  "v_cvt_pk_bf16_f32 v95, v46, v47\n\t" \
  "s_nop 7\n\t" \
  "s_mov_b32 s57, 6144\n\t" \
  "s_mov_b32 s58, 1\n\t" \
  "s_mov_b32 s38, 0\n\t" \
  "s_mov_b32 s59, -73800\n\t" \
  "s_mov_b32 s60, -73800\n\t" \
  "s_mov_b32 s36, 2\n\t" \
  "s_mov_b32 s61, s28\n\t" \
  "s_mov_b32 s62, s29\n\t" \
  "s_mov_b32 s14, s12\n\t" \
  "s_mov_b32 s15, s13\n\t" \
  "s_add_u32 s12, s12, s65\n\t" \
  "s_add_u32 s13, s13, s65\n\t" \
  "s_add_u32 s28, s28, s65\n\t" \
  "s_add_u32 s29, s29, s65\n\t" \
  "s_sub_u32 s66, s66, 232704\n\t" \
  "s_subb_u32 s67, s67, 0\n\t" \
  "v_lshlrev_b32 v198, 1, v251\n\t" \
  "global_load_dwordx4 v[192:195], v198, s[66:67]\n\t" \
  "s_branch .Lcnx_round\n\t" \
  ".Lcnx_w12:\n\t" \
  "ds_read_b32 v246, v251 offset:47144\n\t" \
  "s_waitcnt lgkmcnt(0)\n\t" \
  "v_readfirstlane_b32 s4, v246\n\t" \
  "s_cmp_lt_u32 s4, s57\n\t" \
  "s_cbranch_scc1 .Lcnx_w12\n\t" \
  "s_branch .Lcnx_b12\n\t" \
  ".Lcnx_w24:\n\t" \
  "ds_read_b32 v246, v251 offset:47152\n\t" \
  "s_waitcnt lgkmcnt(0)\n\t" \
  "v_readfirstlane_b32 s4, v246\n\t" \
  "s_cmp_lt_u32 s4, s57\n\t" \
  "s_cbranch_scc1 .Lcnx_w24\n\t" \
  "s_branch .Lcnx_b24\n\t" \
  ".Lcnx_w36:\n\t" \
  "ds_read_b32 v246, v251 offset:47160\n\t" \
  "s_waitcnt lgkmcnt(0)\n\t" \
  "v_readfirstlane_b32 s4, v246\n\t" \
  "s_cmp_lt_u32 s4, s57\n\t" \
  "s_cbranch_scc1 .Lcnx_w36\n\t" \
  "s_branch .Lcnx_b36\n\t" \
  ".Lcnx_w48:\n\t" \
  "ds_read_b32 v246, v251 offset:47168\n\t" \
  "s_waitcnt lgkmcnt(0)\n\t" \
  "v_readfirstlane_b32 s4, v246\n\t" \
  "s_cmp_lt_u32 s4, s57\n\t" \
  "s_cbranch_scc1 .Lcnx_w48\n\t" \
  "s_branch .Lcnx_b48\n\t" \
  ".Lcnx_w60:\n\t" \
  "ds_read_b32 v246, v251 offset:47176\n\t" \
  "s_waitcnt lgkmcnt(0)\n\t" \
  "v_readfirstlane_b32 s4, v246\n\t" \
  "s_cmp_lt_u32 s4, s57\n\t" \
  "s_cbranch_scc1 .Lcnx_w60\n\t" \
  "s_branch .Lcnx_b60\n\t" \
  ".Lcnx_w72:\n\t" \
  "ds_read_b32 v246, v251 offset:47184\n\t" \
  "s_waitcnt lgkmcnt(0)\n\t" \
  "v_readfirstlane_b32 s4, v246\n\t" \
  "s_cmp_lt_u32 s4, s57\n\t" \
  "s_cbranch_scc1 .Lcnx_w72\n\t" \
  "s_branch .Lcnx_b72\n\t" \
  ".Lcnx_w84:\n\t" \
  "ds_read_b32 v246, v251 offset:47192\n\t" \
  "s_waitcnt lgkmcnt(0)\n\t" \
  "v_readfirstlane_b32 s4, v246\n\t" \
  "s_cmp_lt_u32 s4, s57\n\t" \
  "s_cbranch_scc1 .Lcnx_w84\n\t" \
  "s_branch .Lcnx_b84\n\t" \
  ".Lcnx_w96:\n\t" \
  "ds_read_b32 v246, v251 offset:47200\n\t" \
  "s_waitcnt lgkmcnt(0)\n\t" \
  "v_readfirstlane_b32 s4, v246\n\t" \
  "s_cmp_lt_u32 s4, s57\n\t" \
  "s_cbranch_scc1 .Lcnx_w96\n\t" \
  "s_branch .Lcnx_b96\n\t" \
  ".Lcnx_w108:\n\t" \
  "ds_read_b32 v246, v251 offset:47208\n\t" \
  "s_waitcnt lgkmcnt(0)\n\t" \
  "v_readfirstlane_b32 s4, v246\n\t" \
  "s_cmp_lt_u32 s4, s57\n\t" \
  "s_cbranch_scc1 .Lcnx_w108\n\t" \
  "s_branch .Lcnx_b108\n\t" \
  ".Lcnx_w120:\n\t" \
  "ds_read_b32 v246, v251 offset:47216\n\t" \
  "s_waitcnt lgkmcnt(0)\n\t" \
  "v_readfirstlane_b32 s4, v246\n\t" \
  "s_cmp_lt_u32 s4, s57\n\t" \
  "s_cbranch_scc1 .Lcnx_w120\n\t" \
  "s_branch .Lcnx_b120\n\t" \
  ".Lcnx_w132:\n\t" \
  "ds_read_b32 v246, v251 offset:47224\n\t" \
  "s_waitcnt lgkmcnt(0)\n\t" \
  "v_readfirstlane_b32 s4, v246\n\t" \
  "s_cmp_lt_u32 s4, s57\n\t" \
  "s_cbranch_scc1 .Lcnx_w132\n\t" \
  "s_branch .Lcnx_b132\n\t" \
  ".Lcnx_w138:\n\t" \
  "ds_read_b32 v246, v251 offset:47232\n\t" \
  "s_waitcnt lgkmcnt(0)\n\t" \
  "v_readfirstlane_b32 s4, v246\n\t" \
  "s_cmp_lt_u32 s4, s57\n\t" \
  "s_cbranch_scc1 .Lcnx_w138\n\t" \
  "s_branch .Lcnx_b138\n\t" \
  ".Lcnx_f0:\n\t" \
  "s_mov_b32 m0, s44\n\t" \
  "s_nop 0\n\t" \
  "buffer_load_dwordx4 v252, s[16:19], s33 offen lds\n\t" \
  "buffer_load_dwordx4 v252, s[16:19], s34 offen offset:1024 lds\n\t" \
  "buffer_load_dwordx4 v252, s[16:19], s35 offen offset:2048 lds\n\t" \
  "s_add_u32 s33, s33, s32\n\t" \
  "s_add_u32 s34, s34, s32\n\t" \
  "s_add_u32 s35, s35, s32\n\t" \
  "s_branch .Lcnx_g0\n\t" \
  ".Lcnx_f1:\n\t" \
  "s_mov_b32 m0, s45\n\t" \
  "s_nop 0\n\t" \
  "buffer_load_dwordx4 v252, s[16:19], s33 offen lds\n\t" \
  "buffer_load_dwordx4 v252, s[16:19], s34 offen offset:1024 lds\n\t" \
  "buffer_load_dwordx4 v252, s[16:19], s35 offen offset:2048 lds\n\t" \
  "s_add_u32 s33, s33, s32\n\t" \
  "s_add_u32 s34, s34, s32\n\t" \
  "s_add_u32 s35, s35, s32\n\t" \
  "s_branch .Lcnx_g1\n\t" \
  ".Lcnx_f2:\n\t" \
  "s_mov_b32 m0, s46\n\t" \
  "s_nop 0\n\t" \
  "buffer_load_dwordx4 v252, s[16:19], s33 offen lds\n\t" \
  "buffer_load_dwordx4 v252, s[16:19], s34 offen offset:1024 lds\n\t" \
  "buffer_load_dwordx4 v252, s[16:19], s35 offen offset:2048 lds\n\t" \
  "s_add_u32 s33, s33, s32\n\t" \
  "s_add_u32 s34, s34, s32\n\t" \
  "s_add_u32 s35, s35, s32\n\t" \
  "s_branch .Lcnx_g2\n\t" \
  ".Lcnx_f3:\n\t" \
  "s_mov_b32 m0, s47\n\t" \
  "s_nop 0\n\t" \
  "buffer_load_dwordx4 v252, s[16:19], s33 offen lds\n\t" \
  "buffer_load_dwordx4 v252, s[16:19], s34 offen offset:1024 lds\n\t" \
  "buffer_load_dwordx4 v252, s[16:19], s35 offen offset:2048 lds\n\t" \
  "s_add_u32 s33, s33, s32\n\t" \
  "s_add_u32 s34, s34, s32\n\t" \
  "s_add_u32 s35, s35, s32\n\t" \
  "s_branch .Lcnx_g3\n\t" \
  ".Lcnx_f4:\n\t" \
  "s_mov_b32 m0, s44\n\t" \
  "s_nop 0\n\t" \
  "buffer_load_dwordx4 v252, s[16:19], s33 offen lds\n\t" \
  "buffer_load_dwordx4 v252, s[16:19], s34 offen offset:1024 lds\n\t" \
  "buffer_load_dwordx4 v252, s[16:19], s35 offen offset:2048 lds\n\t" \
  "s_add_u32 s33, s33, s32\n\t" \
  "s_add_u32 s34, s34, s32\n\t" \
  "s_add_u32 s35, s35, s32\n\t" \
  "s_cmp_lg_u32 s58, 1\n\t" \
  "s_cbranch_scc1 .Lcnx_g4\n\t" \
  "s_add_u32 m0, s44, s50\n\t" \
  "s_add_u32 s4, s33, s51\n\t" \
  "buffer_load_dwordx4 v252, s[16:19], s4 offen offset:0 lds\n\t" \
  "s_add_u32 s4, s34, s51\n\t" \
  "buffer_load_dwordx4 v252, s[16:19], s4 offen offset:1024 lds\n\t" \
  "s_add_u32 s4, s35, s51\n\t" \
  "buffer_load_dwordx4 v252, s[16:19], s4 offen offset:2048 lds\n\t" \
  "s_branch .Lcnx_g4\n\t" \
  ".Lcnx_f5:\n\t" \
  "s_mov_b32 m0, s45\n\t" \
  "s_nop 0\n\t" \
  "buffer_load_dwordx4 v252, s[16:19], s33 offen lds\n\t" \
  "buffer_load_dwordx4 v252, s[16:19], s34 offen offset:1024 lds\n\t" \
  "buffer_load_dwordx4 v252, s[16:19], s35 offen offset:2048 lds\n\t" \
  "s_add_u32 s33, s33, s32\n\t" \
  "s_add_u32 s34, s34, s32\n\t" \
  "s_add_u32 s35, s35, s32\n\t" \
  "s_cmp_lg_u32 s58, 1\n\t" \
  "s_cbranch_scc1 .Lcnx_g5\n\t" \
  "s_add_u32 m0, s45, s50\n\t" \
  "s_add_u32 s4, s33, s51\n\t" \
  "buffer_load_dwordx4 v252, s[16:19], s4 offen offset:0 lds\n\t" \
  "s_add_u32 s4, s34, s51\n\t" \
  "buffer_load_dwordx4 v252, s[16:19], s4 offen offset:1024 lds\n\t" \
  "s_add_u32 s4, s35, s51\n\t" \
  "buffer_load_dwordx4 v252, s[16:19], s4 offen offset:2048 lds\n\t" \
  "s_branch .Lcnx_g5\n\t" \
  ".Lcnx_f6:\n\t" \
  "s_mov_b32 m0, s46\n\t" \
  "s_nop 0\n\t" \
  "buffer_load_dwordx4 v252, s[16:19], s33 offen lds\n\t" \
  "buffer_load_dwordx4 v252, s[16:19], s34 offen offset:1024 lds\n\t" \
  "buffer_load_dwordx4 v252, s[16:19], s35 offen offset:2048 lds\n\t" \
  "s_add_u32 s33, s33, s32\n\t" \
  "s_add_u32 s34, s34, s32\n\t" \
  "s_add_u32 s35, s35, s32\n\t" \
  "s_cmp_lg_u32 s58, 1\n\t" \
  "s_cbranch_scc1 .Lcnx_g6\n\t" \
  "s_cmp_lt_u32 s39, 1024\n\t" \
  "s_cbranch_scc1 .Lcnx_g6\n\t" \
  "s_add_u32 m0, s46, s50\n\t" \
  "s_add_u32 s4, s33, s51\n\t" \
  "buffer_load_dwordx4 v252, s[16:19], s4 offen offset:0 lds\n\t" \
  "s_add_u32 s4, s34, s51\n\t" \
  "buffer_load_dwordx4 v252, s[16:19], s4 offen offset:1024 lds\n\t" \
  "s_add_u32 s4, s35, s51\n\t" \
  "buffer_load_dwordx4 v252, s[16:19], s4 offen offset:2048 lds\n\t" \
  "s_branch .Lcnx_g6\n\t" \
  ".Lcnx_f7:\n\t" \
  "s_mov_b32 m0, s47\n\t" \
  "s_nop 0\n\t" \
  "buffer_load_dwordx4 v252, s[16:19], s33 offen lds\n\t" \
  "buffer_load_dwordx4 v252, s[16:19], s34 offen offset:1024 lds\n\t" \
  "buffer_load_dwordx4 v252, s[16:19], s35 offen offset:2048 lds\n\t" \
  "s_add_u32 s33, s33, s32\n\t" \
  "s_add_u32 s34, s34, s32\n\t" \
  "s_add_u32 s35, s35, s32\n\t" \
  "s_cmp_lg_u32 s58, 1\n\t" \
  "s_cbranch_scc1 .Lcnx_g7\n\t" \
  "s_cmp_lt_u32 s39, 1024\n\t" \
  "s_cbranch_scc1 .Lcnx_g7\n\t" \
  "s_add_u32 m0, s47, s50\n\t" \
  "s_add_u32 s4, s33, s51\n\t" \
  "buffer_load_dwordx4 v252, s[16:19], s4 offen offset:0 lds\n\t" \
  "s_add_u32 s4, s34, s51\n\t" \
  "buffer_load_dwordx4 v252, s[16:19], s4 offen offset:1024 lds\n\t" \
  "s_add_u32 s4, s35, s51\n\t" \
  "buffer_load_dwordx4 v252, s[16:19], s4 offen offset:2048 lds\n\t" \
  "s_branch .Lcnx_g7\n\t" \
  ".Lcnx_f8:\n\t" \
  "s_mov_b32 m0, s44\n\t" \
  "s_nop 0\n\t" \
  "buffer_load_dwordx4 v252, s[16:19], s33 offen lds\n\t" \
  "buffer_load_dwordx4 v252, s[16:19], s34 offen offset:1024 lds\n\t" \
  "buffer_load_dwordx4 v252, s[16:19], s35 offen offset:2048 lds\n\t" \
  "s_add_u32 s33, s33, s32\n\t" \
  "s_add_u32 s34, s34, s32\n\t" \
  "s_add_u32 s35, s35, s32\n\t" \
  "s_branch .Lcnx_g8\n\t" \
  ".Lcnx_f9:\n\t" \
  "s_mov_b32 m0, s45\n\t" \
  "s_nop 0\n\t" \
  "buffer_load_dwordx4 v252, s[16:19], s33 offen lds\n\t" \
  "buffer_load_dwordx4 v252, s[16:19], s34 offen offset:1024 lds\n\t" \
  "buffer_load_dwordx4 v252, s[16:19], s35 offen offset:2048 lds\n\t" \
  "s_add_u32 s33, s33, s32\n\t" \
  "s_add_u32 s34, s34, s32\n\t" \
  "s_add_u32 s35, s35, s32\n\t" \
  "s_sub_u32 s33, s33, s48\n\t" \
  "s_sub_u32 s34, s34, s48\n\t" \
  "s_sub_u32 s35, s35, s48\n\t" \
  "s_branch .Lcnx_g9\n\t" \
  ".Lcnx_f10:\n\t" \
  "s_mov_b32 m0, s46\n\t" \
  "s_nop 0\n\t" \
  "buffer_load_dwordx4 v252, s[16:19], s33 offen lds\n\t" \
  "buffer_load_dwordx4 v252, s[16:19], s34 offen offset:1024 lds\n\t" \
  "buffer_load_dwordx4 v252, s[16:19], s35 offen offset:2048 lds\n\t" \
  "s_add_u32 s33, s33, s32\n\t" \
  "s_add_u32 s34, s34, s32\n\t" \
  "s_add_u32 s35, s35, s32\n\t" \
  "s_branch .Lcnx_g10\n\t" \
  ".Lcnx_f11:\n\t" \
  "s_mov_b32 m0, s47\n\t" \
  "s_nop 0\n\t" \
  "buffer_load_dwordx4 v252, s[16:19], s33 offen lds\n\t" \
  "buffer_load_dwordx4 v252, s[16:19], s34 offen offset:1024 lds\n\t" \
  "buffer_load_dwordx4 v252, s[16:19], s35 offen offset:2048 lds\n\t" \
  "s_add_u32 s33, s33, s32\n\t" \
  "s_add_u32 s34, s34, s32\n\t" \
  "s_add_u32 s35, s35, s32\n\t" \
  "s_sub_u32 s33, s33, s49\n\t" \
  "s_sub_u32 s34, s34, s49\n\t" \
  "s_sub_u32 s35, s35, s49\n\t" \
  "s_branch .Lcnx_g11\n\t" \
  ".Lcnx_fin:\n\t" \
  "v_mfma_f32_16x16x32_bf16 v[88:91], v[242:245], v[236:239], v[88:91]\n\t" \
  "ds_read_b64_tr_b16 v[218:219], v249 offset:13904\n\t" \
  "v_mfma_f32_16x16x32_bf16 v[92:95], v[240:243], v[236:239], v[92:95]\n\t" \
  "v_mfma_f32_16x16x32_bf16 v[0:3], v[208:211], v[178:181], v[0:3]\n\t" \
  "v_mfma_f32_16x16x32_bf16 v[24:27], v[208:211], v[186:189], v[24:27]\n\t" \
  "v_mfma_f32_16x16x32_bf16 v[48:51], v[208:211], v[194:197], v[48:51]\n\t" \
  "v_mfma_f32_16x16x32_bf16 v[72:75], v[208:211], v[202:205], v[72:75]\n\t" \
  "ds_read_b64_tr_b16 v[208:209], v249 offset:12376\n\t" \
  "s_waitcnt lgkmcnt(3)\n\t" \
  "v_mfma_f32_16x16x32_bf16 v[4:7], v[212:215], v[178:181], v[4:7]\n\t" \
  "ds_read_b64_tr_b16 v[210:211], v249 offset:13912\n\t" \
  "v_mfma_f32_16x16x32_bf16 v[28:31], v[212:215], v[186:189], v[28:31]\n\t" \
  "v_mfma_f32_16x16x32_bf16 v[52:55], v[212:215], v[194:197], v[52:55]\n\t" \
  "v_mfma_f32_16x16x32_bf16 v[76:79], v[212:215], v[202:205], v[76:79]\n\t" \
  "ds_read_b64_tr_b16 v[212:213], v249 offset:12432\n\t" \
  "s_waitcnt lgkmcnt(3)\n\t" \
  "v_mfma_f32_16x16x32_bf16 v[8:11], v[216:219], v[178:181], v[8:11]\n\t" \
  "ds_read_b64_tr_b16 v[214:215], v249 offset:13968\n\t" \
  "v_mfma_f32_16x16x32_bf16 v[32:35], v[216:219], v[186:189], v[32:35]\n\t" \
  "v_mfma_f32_16x16x32_bf16 v[56:59], v[216:219], v[194:197], v[56:59]\n\t" \
  "v_mfma_f32_16x16x32_bf16 v[80:83], v[216:219], v[202:205], v[80:83]\n\t" \
  "ds_read_b64_tr_b16 v[216:217], v249 offset:12440\n\t" \
  "v_cvt_pk_bf16_f32 v0, v0, v1\n\t" \
  "v_cvt_pk_bf16_f32 v1, v2, v3\n\t" \
  "v_cvt_pk_bf16_f32 v2, v4, v5\n\t" \
  "v_cvt_pk_bf16_f32 v3, v6, v7\n\t" \
  "buffer_store_dwordx4 v[0:3], v250, s[20:23], s28 offen offset:0 sc1\n\t" \
  "v_cvt_pk_bf16_f32 v24, v24, v25\n\t" \
  "v_cvt_pk_bf16_f32 v25, v26, v27\n\t" \
  "v_cvt_pk_bf16_f32 v26, v28, v29\n\t" \
  "v_cvt_pk_bf16_f32 v27, v30, v31\n\t" \
  "buffer_store_dwordx4 v[24:27], v250, s[20:23], s29 offen offset:0 sc1\n\t" \
  "v_cvt_pk_bf16_f32 v48, v48, v49\n\t" \
  "v_cvt_pk_bf16_f32 v49, v50, v51\n\t" \
  "v_cvt_pk_bf16_f32 v50, v52, v53\n\t" \
  "v_cvt_pk_bf16_f32 v51, v54, v55\n\t" \
  "buffer_store_dwordx4 v[48:51], v250, s[20:23], s12 offen offset:0 sc1\n\t" \
  "v_cvt_pk_bf16_f32 v72, v72, v73\n\t" \
  "v_cvt_pk_bf16_f32 v73, v74, v75\n\t" \
  "v_cvt_pk_bf16_f32 v74, v76, v77\n\t" \
  "v_cvt_pk_bf16_f32 v75, v78, v79\n\t" \
  "buffer_store_dwordx4 v[72:75], v250, s[20:23], s13 offen offset:0 sc1\n\t" \
  "s_waitcnt lgkmcnt(3)\n\t" \
  "v_mfma_f32_16x16x32_bf16 v[12:15], v[208:211], v[178:181], v[12:15]\n\t" \
  "ds_read_b64_tr_b16 v[218:219], v249 offset:13976\n\t" \
  "v_mfma_f32_16x16x32_bf16 v[36:39], v[208:211], v[186:189], v[36:39]\n\t" \
  "v_mfma_f32_16x16x32_bf16 v[60:63], v[208:211], v[194:197], v[60:63]\n\t" \
  "v_mfma_f32_16x16x32_bf16 v[84:87], v[208:211], v[202:205], v[84:87]\n\t" \
  "s_waitcnt lgkmcnt(2)\n\t" \
  "v_mfma_f32_16x16x32_bf16 v[16:19], v[212:215], v[178:181], v[16:19]\n\t" \
  "v_mfma_f32_16x16x32_bf16 v[40:43], v[212:215], v[186:189], v[40:43]\n\t" \
  "v_mfma_f32_16x16x32_bf16 v[64:67], v[212:215], v[194:197], v[64:67]\n\t" \
  "v_mfma_f32_16x16x32_bf16 v[88:91], v[212:215], v[202:205], v[88:91]\n\t" \
  "v_cvt_pk_bf16_f32 v8, v8, v9\n\t" \
  "v_cvt_pk_bf16_f32 v9, v10, v11\n\t" \
  "v_cvt_pk_bf16_f32 v10, v12, v13\n\t" \
  "v_cvt_pk_bf16_f32 v11, v14, v15\n\t" \
  "buffer_store_dwordx4 v[8:11], v250, s[20:23], s28 offen offset:64 sc1\n\t" \
  "v_cvt_pk_bf16_f32 v32, v32, v33\n\t" \
  "v_cvt_pk_bf16_f32 v33, v34, v35\n\t" \
  "v_cvt_pk_bf16_f32 v34, v36, v37\n\t" \
  "v_cvt_pk_bf16_f32 v35, v38, v39\n\t" \
  "buffer_store_dwordx4 v[32:35], v250, s[20:23], s29 offen offset:64 sc1\n\t" \
  "v_cvt_pk_bf16_f32 v56, v56, v57\n\t" \
  "v_cvt_pk_bf16_f32 v57, v58, v59\n\t" \
  "v_cvt_pk_bf16_f32 v58, v60, v61\n\t" \
  "v_cvt_pk_bf16_f32 v59, v62, v63\n\t" \
  "buffer_store_dwordx4 v[56:59], v250, s[20:23], s12 offen offset:64 sc1\n\t" \
  "v_cvt_pk_bf16_f32 v80, v80, v81\n\t" \
  "v_cvt_pk_bf16_f32 v81, v82, v83\n\t" \
  "v_cvt_pk_bf16_f32 v82, v84, v85\n\t" \
  "v_cvt_pk_bf16_f32 v83, v86, v87\n\t" \
  "buffer_store_dwordx4 v[80:83], v250, s[20:23], s13 offen offset:64 sc1\n\t" \
  "s_waitcnt lgkmcnt(0)\n\t" \
  "v_mfma_f32_16x16x32_bf16 v[20:23], v[216:219], v[178:181], v[20:23]\n\t" \
  "v_mfma_f32_16x16x32_bf16 v[44:47], v[216:219], v[186:189], v[44:47]\n\t" \
  "v_mfma_f32_16x16x32_bf16 v[68:71], v[216:219], v[194:197], v[68:71]\n\t" \
  "v_mfma_f32_16x16x32_bf16 v[92:95], v[216:219], v[202:205], v[92:95]\n\t" \
  "v_cvt_pk_bf16_f32 v16, v16, v17\n\t" \
  "v_cvt_pk_bf16_f32 v17, v18, v19\n\t" \
  "s_nop 2\n\t" \
  "v_cvt_pk_bf16_f32 v18, v20, v21\n\t" \
  "v_cvt_pk_bf16_f32 v19, v22, v23\n\t" \
  "buffer_store_dwordx4 v[16:19], v250, s[20:23], s28 offen offset:128 sc1\n\t" \
  "v_cvt_pk_bf16_f32 v40, v40, v41\n\t" \
  "v_cvt_pk_bf16_f32 v41, v42, v43\n\t" \
  "v_cvt_pk_bf16_f32 v42, v44, v45\n\t" \
  "v_cvt_pk_bf16_f32 v43, v46, v47\n\t" \
  "buffer_store_dwordx4 v[40:43], v250, s[20:23], s29 offen offset:128 sc1\n\t" \
  "v_cvt_pk_bf16_f32 v64, v64, v65\n\t" \
  "v_cvt_pk_bf16_f32 v65, v66, v67\n\t" \
  "v_cvt_pk_bf16_f32 v66, v68, v69\n\t" \
  "v_cvt_pk_bf16_f32 v67, v70, v71\n\t" \
  "buffer_store_dwordx4 v[64:67], v250, s[20:23], s12 offen offset:128 sc1\n\t" \
  "v_cvt_pk_bf16_f32 v88, v88, v89\n\t" \
  "v_cvt_pk_bf16_f32 v89, v90, v91\n\t" \
  "v_cvt_pk_bf16_f32 v90, v92, v93\n\t" \
  "v_cvt_pk_bf16_f32 v91, v94, v95\n\t" \
  "buffer_store_dwordx4 v[88:91], v250, s[20:23], s13 offen offset:128 sc1\n\t" \
  "s_waitcnt vmcnt(0)\n\t" \
  ".Lcnx_end:\n\t" \
  ""


__device__ __forceinline__ unsigned lds_addr(const char* p) { return (unsigned)(uintptr_t)(lds_ptr_t)p; }
__device__ __forceinline__ unsigned sg(unsigned v) { return __builtin_amdgcn_readfirstlane(v); }

constexpr double tanh_u(double y) {
    const double v = 8 * y, a = 2 * 0.79788456080286536 * (v + 0.044715 * v * v * v) / 1024;
    double m = 1;
    for (int i = 12; i > 1; --i) m = 1 + m * a / i;
    m *= a;
    for (int i = 0; i < 10; ++i) m *= m + 2;
    return m / (m + 2);
}
constexpr double h2d(int h) {
    if (h < 1024) return 0;
    double x = (1024 + (h & 1023)) * 0x1p-24;
    for (int e = 1; e < (h >> 10); ++e) x *= 2;
    return x;
}
constexpr int d2h(double x) {
    int h = 0;
    for (int s = 1 << 14; s; s >>= 1) if (h2d(h + s) <= x) h += s;
    return h;
}
constexpr unsigned ip_entry(int k) {
    if (k == 127) return 0x3c003c00u;
    const double y0 = k ? (256 * k - 0.5) / 32767 : 0, dy = ((256 * k + 255.5) / 32767 - y0) / 8;
    double t[9] = {};
    for (int i = 0; i < 9; ++i) t[i] = tanh_u(y0 + i * dy);
    const double b0 = (t[8] - t[0]) / (8 * dy), ym = y0 + 4 * dy, tm = (t[4] + (t[0] + t[8]) / 2) / 2;
    unsigned best = 0;
    double err = 1;
    for (int ha = d2h(tm - b0 * ym) - 1, i = 0; i < 4; ++i, ++ha)
        for (int hb = d2h((tm - h2d(ha)) / ym), j = 0; j < 2; ++j, ++hb) {
            double e = 0;
            for (int n = 0; n < 9; ++n) {
                const double s = h2d(ha) + h2d(hb) * (y0 + n * dy), d = (s < 1 ? s : 1) - t[n];
                e = d > e ? d : -d > e ? -d : e;
            }
            if (e < err) err = e, best = (ha < 1024 ? 0 : ha) | (hb < 1024 ? 0 : hb) << 16;
        }
    return best;
}
struct TanhTab { unsigned v[1024]; };
constexpr TanhTab tanh_tab(int k0) {
    TanhTab t = {};
    for (int k = k0; k < k0 + 1024 && k < DWOFF / 4; ++k) {
        double y = ((k & 255) + 0.375 + (k >= 256 ? 256 : 0)) * 0x1p-22;
        for (int e = 1; e < (k >> 8); ++e) y *= 2;
        t.v[k - k0] = k < IPOFF / 4 ? __builtin_bit_cast(unsigned, (float)tanh_u(y)) : ip_entry(k - IPOFF / 4);
    }
    return t;
}
struct WaveTab { unsigned v192[64][4], v244[64][4], v16[64][4], s32[32]; };
struct RegTab { unsigned v240[64][4], v248[64][4]; WaveTab w[8]; };
constexpr unsigned cpos(unsigned w, unsigned px) { return (w >> 1) * 1024 + (px ^ (w & 2) << 2) * 16 + ((w ^ px >> 4) & 1) * 8; }
constexpr RegTab reg_tab() {
    RegTab t = {};
    for (unsigned l = 0; l < 64; ++l) {
        const unsigned G = l >> 4, n = l & 15, q = n >> 2, p = l & 3;
        t.v240[l][2 + (p >> 1)] = G == q ? (p & 1 ? 0x3f800000u : 0x3f80u) : 0u;
        t.v248[l][0] = WB + (l >> 2) * 32 + (p & 1) * 16 + (p >> 1) * 8;
        t.v248[l][1] = WB + (G & 1) * 3080 + ((G >> 1) * 4 + q) * 192 + p * 16;
        t.v248[l][2] = (n & 7) * 384 + (n >> 3) * 192 + G * 16;
        t.v248[l][3] = PB + l * 8;
        for (unsigned w = 0; w < 8; ++w) {
            t.w[w].v244[l][2] = XOFF + n * 400 + (G & 1) * XROW + (G >> 1) * 192 + w * 24;
            t.w[w].v244[l][3] = DWOFF + p * 192 + (1 - (q >> 1)) * 24 + (1 + (G & 1) - (q & 1)) * 8 + ((G ^ p) >> 1) * 4 + w * 2304;
            const unsigned pl = PTOP + (w >> 2) * 12288, blk = (w & 3) * 16;
            t.w[w].v192[l][0] = XOFF + cpos(w, (G & 2) * 16 + (n & 8) * 2 + (G & 1) * 8 + (n & 7));
            t.w[w].v192[l][1] = t.w[w].v192[l][0] + PTOP - XOFF;
            t.w[w].v192[l][2] = pl + (cpos((G & 1) * 4 + (G & 2), blk + n) & ~8u);
            t.w[w].v192[l][3] = pl + cpos(p, blk + (l >> 2));
            t.w[w].v16[l][0] = w >= 4 ? (l < 48 ? l * 128 : OOB)
                             : w < 2  ? (12 * ((l >> 3) & 3) + ((l >> 1) & 3)) * (HID * 2) + (2 * (l & 1) + (l >> 5)) * 16 : l * 16;
            const unsigned d = DWOFF + (w * 8 + (l >> 5) * 4 + p) * 768 + ((l >> 2) & 7) * 24;
            t.w[w].v16[l][1] = w < 3 ? d + (l & 1) * 192 : XOFF - 32 + p * 8;
            t.w[w].v16[l][2] = d + (~l & 1) * 192;
            t.w[w].v16[l][3] = l & 1 ? 0x07060302u : 0x05040100u;
        }
    }
    for (unsigned w = 0; w < 8; ++w) {
        const bool w1 = w < 2;
        const unsigned wpd = w1 ? 4 * HID * 2 - 1024 : 0, wl = WB + (w & 1) * (w1 ? 3072 : 3080), wc = w1 ? 64 : 6144;
        const unsigned so = (w & 1) * (w1 ? 48 * HID * 2 : 3072);
        const unsigned s[32] = {wc, so, so + wpd, so + 2 * wpd, w, w >> 2, w, w * 512,
                                0xaaaaaaaau, 0xaaaaaaaau, 0xccccccccu, 0xccccccccu,
                                wl + (w1 ? WA + 12304 : WA), wl + (w1 ? 0 : WA + 6152), wl + (w1 ? WA + 18448 : 6152),
                                wl + (w1 ? WA + 18448 + 6144 : 12304), w1 ? 12 * wc : 0, w1 ? 0 : 12 * wc,
                                0u - 9 * XROW, 3 * wc, ~0x00f0000fu, ~0xf0000f00u, (w >> 2) * (PTOP - XOFF - 5 * 12288), 0x07060302, 0x3ffc, 3072, 0, 0, 0,
                                OOB, OOB, 8 * (unsigned)sizeof(WaveTab) - 2 * PB};
        for (int i = 0; i < 32; ++i) t.w[w].s32[i] = s[i];
    }
    return t;
}
struct Tables { RegTab r; TanhTab t[4]; };
static_assert(sizeof(WaveTab) == 64 * 50 && sizeof(RegTab) == 27648, "table layout");
__device__ const __attribute__((aligned(128))) Tables TABLES = {reg_tab(), {tanh_tab(0), tanh_tab(1024), tanh_tab(2048), tanh_tab(3072)}};

#define R10(r, a) r #a "0", r #a "1", r #a "2", r #a "3", r #a "4", r #a "5", r #a "6", r #a "7", r #a "8", r #a "9"
#define V10(a) R10("v", a)
#define S10(a) R10("s", a)
#define MAIN_CLOB "memory", "scc", "vcc", "v1", "v2", "v3", "v4", "v5", "v6", "v7", "v8", "v9", V10(1), V10(2), V10(3), V10(4), V10(5),  \
    V10(6), V10(7), V10(8), V10(9), V10(10), V10(11), V10(12), V10(13), V10(14), V10(15), V10(16), V10(17), V10(18), V10(19),        \
    V10(20), V10(21), V10(22), V10(23), V10(24), "v250", "v251", "v252", "v253", "v254", "s4", "s5", "s8", "s9", S10(1),       \
    S10(2), S10(3), S10(4), S10(5), "s60", "s61", "s62", "s63", "s64", "s65", "s66", "s67"

__global__ __launch_bounds__(512, 2) void convnext_kernel(const __hip_bfloat16* __restrict__ x,
                                                          const __hip_bfloat16* __restrict__ dw,
                                                          const __hip_bfloat16* __restrict__ w1,
                                                          const __hip_bfloat16* __restrict__ w2,
                                                          __hip_bfloat16* __restrict__ out) {
    __shared__ __attribute__((aligned(16))) char smem[LDS_BYTES];

    unsigned tid = threadIdx.x, lds = lds_addr(smem);
    const unsigned bid = blockIdx.x;
    asm volatile(MAIN_ASM
                 : "+{v0}"(tid), "+{s3}"(lds)
                 : "{s[0:1]}"(__builtin_amdgcn_kernarg_segment_ptr()), "{s2}"(bid), "{s[6:7]}"(TABLES.r.w)
                 : MAIN_CLOB);

}

}

extern "C" void kernel_launch(void* const* d_in, const int* in_sizes, int n_in, void* d_out, int out_size,
                              void* d_ws, size_t ws_size, hipStream_t stream) {
    (void)in_sizes; (void)n_in; (void)out_size; (void)d_ws; (void)ws_size;
    const __hip_bfloat16* x = (const __hip_bfloat16*)d_in[0];
    const __hip_bfloat16* dw = (const __hip_bfloat16*)d_in[1];
    const __hip_bfloat16* w1 = (const __hip_bfloat16*)d_in[2];
    const __hip_bfloat16* w2 = (const __hip_bfloat16*)d_in[3];
    __hip_bfloat16* out = (__hip_bfloat16*)d_out;
    convnext_kernel<<<256, 512, 0, stream>>>(x, dw, w1, w2, out);
}
```

```cpp
#include <hip/hip_runtime.h>
#include <hip/hip_bf16.h>
#include <stdint.h>

namespace {

typedef __attribute__((address_space(3))) void* lds_ptr_t;

constexpr int IMG = 512;
constexpr int CH = 96;
constexpr int HID = 384;
constexpr int ROWB = IMG * CH * 2;
constexpr int LUTN = 3841;
constexpr int IPOFF = 15376;
constexpr int DWOFF = IPOFF + 512;
constexpr int XOFF = DWOFF + 96 * 192 + 32;
constexpr int XROW = 8200;
constexpr int PB = XOFF + 10 * XROW;
constexpr int WB = XOFF + 9 * XROW;
constexpr int WA = 3 * XROW;
constexpr int PTOP = XOFF + 12 * XROW - 12288;
constexpr int CNTOFF = WB + WA + 18448 + 12288;
constexpr int PFOFF = CNTOFF + 144;
constexpr int LDS_BYTES = 160 * 1024;
static_assert(PB == 116352 && CNTOFF - PB == 47136 && LUTN * 4 <= IPOFF && XOFF % 16 == 0 && PFOFF + 48 * 4 <= LDS_BYTES, "LDS map");
constexpr unsigned OOB = 0x40000000u;

#define MAIN_ASM \
  "s_load_dwordx2 s[8:9], s[0:1], 0x0\n\t" \
  "s_load_dwordx2 s[12:13], s[0:1], 0x8\n\t" \
  "s_load_dwordx2 s[20:21], s[0:1], 0x20\n\t" \
  "s_load_dwordx4 s[32:35], s[0:1], 0x10\n\t" \
  "v_readfirstlane_b32 s5, v0\n\t" \
  "v_and_b32 v21, 63, v0\n\t" \
  "s_mov_b32 m0, 0\n\t" \
  "s_cmp_lt_u32 s5, 128\n\t" \
  "s_cselect_b32 s3, 16, 24\n\t" \
  "v_lshlrev_b32 v22, 4, v21\n\t" \
  "s_load_dwordx2 s[16:17], s[0:1], s3\n\t" \
  "s_mul_i32 s4, s5, 50\n\t" \
  "s_add_u32 s66, s6, s4\n\t" \
  "s_addc_u32 s67, s7, 0\n\t" \
  "global_load_dwordx4 v[244:247], v22, s[66:67] offset:1024\n\t" \
  "global_load_dwordx4 v[16:19], v22, s[66:67] offset:2048\n\t" \
  "global_load_dwordx4 v[240:243], v22, s[6:7] offset:-2048\n\t" \
  "global_load_dwordx4 v[248:251], v22, s[6:7] offset:-1024\n\t" \
  "global_load_dwordx4 v[192:195], v22, s[66:67]\n\t" \
  "s_cmp_lg_u32 s5, 192\n\t" \
  "s_cbranch_scc1 .Lcnx_nc\n\t" \
  "s_getpc_b64 s[14:15]\n\t" \
  ".Lcnx_pc:\n\t" \
  "s_lshr_b32 s3, s2, 3\n\t" \
  "s_lshl_b32 s3, s3, 11\n\t" \
  "v_lshl_add_u32 v1, v21, 5, s3\n\t" \
  "v_min_u32 v1, .Lcnx_end-.Lcnx_pc, v1\n\t" \
  "global_load_lds_dword v1, s[14:15]\n\t" \
  ".Lcnx_nc:\n\t" \
  "v_mul_u32_u24 v254, 0x147b, v0\n\t" \
  "s_bfe_u32 s27, s2, 0x40003\n\t" \
  "v_bfe_u32 v1, v0, 2, 3\n\t" \
  "s_mul_i32 s27, s27, 6144\n\t" \
  "v_lshrrev_b32 v254, 17, v254\n\t" \
  "s_sub_u32 s3, s27, 576\n\t" \
  "v_and_b32 v20, 3, v0\n\t" \
  "s_add_u32 s4, s27, 6720\n\t" \
  "v_sub_u32 v254, v0, v254\n\t" \
  "s_min_u32 s4, s4, 0x18000\n\t" \
  "v_and_b32 v2, 0x1e0, v0\n\t" \
  "s_and_b32 s24, s2, 7\n\t" \
  "v_lshl_add_u32 v254, v254, 4, s3\n\t" \
  "s_lshl_b32 s24, s24, 6\n\t" \
  "v_lshl_or_b32 v20, v20, 3, v2\n\t" \
  "s_mov_b32 s26, 0x18000\n\t" \
  "v_cmp_gt_u32 vcc, s4, v254\n\t" \
  "s_cmp_ge_u32 s2, 128\n\t" \
  "s_cselect_b32 s3, 7, 0\n\t" \
  "s_cselect_b32 s4, 0, 7\n\t" \
  "v_add_u32 v1, s3, v1\n\t" \
  "s_cselect_b32 s3, 28, 35\n\t" \
  "v_cndmask_b32 v254, 2.0, v254, vcc\n\t" \
  "s_cselect_b32 s26, s26, 0xfffe8000\n\t" \
  "v_xor_b32 v1, s4, v1\n\t" \
  "s_add_u32 s24, s24, s3\n\t" \
  "v_and_b32 v1, 7, v1\n\t" \
  "s_movk_i32 s3, 1344\n\t" \
  "s_mul_i32 s24, s24, 0x18000\n\t" \
  "v_mad_u32_u24 v1, v1, s3, v20\n\t" \
  "s_lshl4_add_u32 s25, s5, 34352\n\t" \
  "s_mov_b32 s10, 0x3000000\n\t" \
  "s_mov_b32 s11, 0x20000\n\t" \
  "s_mov_b32 s14, 9408\n\t" \
  "s_mov_b32 s15, 0x20000\n\t" \
  "s_mov_b32 s18, 73728\n\t" \
  "s_mov_b32 s19, 0x20000\n\t" \
  "s_mov_b32 s22, 0x3000000\n\t" \
  "s_mov_b32 s23, 0x20000\n\t" \
  "s_add_u32 s3, s24, s26\n\t" \
  "s_add_u32 s3, s3, s27\n\t" \
  "s_mul_i32 s4, s26, 10\n\t" \
  "v_mov_b32 v24, s3\n\t" \
  "v_mad_i32_i24 v24, v21, s4, v24\n\t" \
  "s_setreg_imm32_b32 hwreg(HW_REG_MODE, 4, 2), 0\n\t" \
  "s_setreg_imm32_b32 hwreg(HW_REG_MODE, 9, 1), 0\n\t" \
  "s_lshl_b32 s65, s26, 4\n\t" \
  "s_lshr_b32 s3, s5, 7\n\t" \
  "s_add_u32 s3, s3, 8\n\t" \
  "s_mul_i32 s3, s3, s26\n\t" \
  "s_add_u32 s29, s24, s27\n\t" \
  "s_add_u32 s29, s29, s3\n\t" \
  "s_and_b32 s3, s5, 64\n\t" \
  "s_mul_i32 s3, s3, 48\n\t" \
  "s_add_u32 s29, s29, s3\n\t" \
  "s_bfe_u32 s3, s5, 0x20006\n\t" \
  "s_add_u32 s3, s3, 6\n\t" \
  "s_mul_i32 s3, s3, s26\n\t" \
  "s_add_u32 s30, s24, s65\n\t" \
  "s_add_u32 s30, s30, s3\n\t" \
  "s_lshl_b32 s31, s26, 2\n\t" \
  "s_mul_i32 s64, s26, 22\n\t" \
  "s_sub_u32 s64, s64, s65\n\t" \
  "s_cmp_ge_u32 s5, 256\n\t" \
  "s_cselect_b32 s27, s27, 0\n\t" \
  "s_cselect_b32 s3, -6, -2\n\t" \
  "s_mul_i32 s3, s3, s26\n\t" \
  "s_add_u32 s28, s29, s3\n\t" \
  "s_mov_b64 vcc, 0\n\t" \
  "s_waitcnt lgkmcnt(0)\n\t" \
  "v_add_co_u32 v24, vcc, s8, v24\n\t" \
  "v_mov_b32 v25, s9\n\t" \
  "v_addc_co_u32 v25, vcc, 0, v25, vcc\n\t" \
  "v_writelane_b32 v24, s12, 3\n\t" \
  "v_writelane_b32 v25, s13, 3\n\t" \
  "v_writelane_b32 v24, s32, 4\n\t" \
  "v_writelane_b32 v25, s33, 4\n\t" \
  "v_writelane_b32 v24, s34, 5\n\t" \
  "v_writelane_b32 v25, s35, 5\n\t" \
  "s_mov_b64 exec, 63\n\t" \
  "global_load_dword v26, v[24:25], off\n\t" \
  "s_mov_b64 exec, -1\n\t" \
  "s_mov_b64 vcc, 0\n\t" \
  "s_and_b32 s13, s13, 0xffff\n\t" \
  "s_and_b32 s9, s9, 0xffff\n\t" \
  "s_and_b32 s17, s17, 0xffff\n\t" \
  "s_cmp_ge_u32 s5, 192\n\t" \
  "s_cbranch_scc1 .Lcnx_nd\n\t" \
  "buffer_load_dwordx2 v[2:3], v1, s[12:15], 0 offen offset:0\n\t" \
  "buffer_load_dwordx2 v[4:5], v1, s[12:15], 0 offen offset:192\n\t" \
  "buffer_load_dwordx2 v[6:7], v1, s[12:15], 0 offen offset:384\n\t" \
  "buffer_load_dwordx2 v[8:9], v1, s[12:15], 0 offen offset:576\n\t" \
  "buffer_load_dwordx2 v[10:11], v1, s[12:15], 0 offen offset:768\n\t" \
  "buffer_load_dwordx2 v[12:13], v1, s[12:15], 0 offen offset:960\n\t" \
  "buffer_load_dwordx2 v[14:15], v1, s[12:15], 0 offen offset:1152\n\t" \
  ".Lcnx_nd:\n\t" \
  "s_and_b32 s3, s5, 0x140\n\t" \
  "s_cmp_lg_u32 s3, 64\n\t" \
  "s_cbranch_scc1 .Lcnx_nw\n\t" \
  "s_lshr_b32 s3, s2, 3\n\t" \
  "s_mul_i32 s3, s3, 2304\n\t" \
  "s_mov_b32 m0, 0\n\t" \
  "v_mul_u32_u24 v21, 36, v21\n\t" \
  "buffer_load_dword v21, s[16:19], s3 offen lds\n\t" \
  ".Lcnx_nw:\n\t" \
  "s_add_u32 m0, s25, 0\n\t" \
  "s_add_u32 s24, s24, s26\n\t" \
  "buffer_load_dwordx4 v254, s[8:11], s24 offen lds\n\t" \
  "s_add_u32 m0, s25, 8200\n\t" \
  "s_add_u32 s24, s24, s26\n\t" \
  "buffer_load_dwordx4 v254, s[8:11], s24 offen lds\n\t" \
  "s_add_u32 m0, s25, 16400\n\t" \
  "s_add_u32 s24, s24, s26\n\t" \
  "buffer_load_dwordx4 v254, s[8:11], s24 offen lds\n\t" \
  "s_add_u32 m0, s25, 24600\n\t" \
  "s_add_u32 s24, s24, s26\n\t" \
  "buffer_load_dwordx4 v254, s[8:11], s24 offen lds\n\t" \
  "s_load_dwordx16 s[32:47], s[66:67], 0xc00\n\t" \
  "s_load_dwordx16 s[48:63], s[66:67], 0xc40\n\t" \
  "s_and_b32 s21, s21, 0xffff\n\t" \
  "s_waitcnt vmcnt(5)\n\t" \
  "v_mfma_f32_32x32x16_bf16 v[144:159], v[242:245], v[240:243], 0\n\t" \
  "v_mfma_f32_32x32x16_bf16 v[160:175], v[242:245], v[240:243], 0\n\t" \
  "v_bfe_u32 v236, v251, 3, 2\n\t" \
  "v_mul_u32_u24 v236, 0x5555, v236\n\t" \
  "v_add_u32 v252, s27, v16\n\t" \
  "s_lshl2_add_u32 s12, s26, s29\n\t" \
  "s_lshl3_add_u32 s13, s26, s29\n\t" \
  "v_mov_b32 v253, v246\n\t" \
  "s_mov_b32 s14, 2.0\n\t" \
  "s_mov_b32 s15, 2.0\n\t" \
  "s_cmp_gt_u32 s5, 192\n\t" \
  "s_cbranch_scc1 .Lcnx_e2\n\t" \
  "s_cmp_eq_u32 s5, 192\n\t" \
  "s_cbranch_scc1 .Lcnx_e1\n\t" \
  "v_xor_b32 v23, 0x02020202, v19\n\t" \
  "s_waitcnt vmcnt(4)\n\t" \
  "v_perm_b32 v24, v4, 0, v19\n\t" \
  "v_perm_b32 v25, v12, v8, v19\n\t" \
  "v_perm_b32 v26, v6, v2, v19\n\t" \
  "v_perm_b32 v27, v14, v10, v19\n\t" \
  "v_perm_b32 v28, v8, v4, v19\n\t" \
  "v_perm_b32 v29, 0, v12, v19\n\t" \
  "ds_write_b64 v17, v[24:25] offset:0\n\t" \
  "ds_write_b64 v17, v[26:27] offset:8\n\t" \
  "ds_write_b64 v17, v[28:29] offset:16\n\t" \
  "v_perm_b32 v30, v4, 0, v23\n\t" \
  "v_perm_b32 v31, v12, v8, v23\n\t" \
  "v_perm_b32 v32, v6, v2, v23\n\t" \
  "v_perm_b32 v33, v14, v10, v23\n\t" \
  "v_perm_b32 v34, v8, v4, v23\n\t" \
  "v_perm_b32 v35, 0, v12, v23\n\t" \
  "ds_write_b64 v18, v[30:31] offset:0\n\t" \
  "ds_write_b64 v18, v[32:33] offset:8\n\t" \
  "ds_write_b64 v18, v[34:35] offset:16\n\t" \
  "v_perm_b32 v37, v5, 0, v19\n\t" \
  "v_perm_b32 v36, v13, v9, v19\n\t" \
  "v_perm_b32 v39, v7, v3, v19\n\t" \
  "v_perm_b32 v38, v15, v11, v19\n\t" \
  "v_perm_b32 v41, v9, v5, v19\n\t" \
  "v_perm_b32 v40, 0, v13, v19\n\t" \
  "ds_write_b64 v17, v[36:37] offset:384\n\t" \
  "ds_write_b64 v17, v[38:39] offset:392\n\t" \
  "ds_write_b64 v17, v[40:41] offset:400\n\t" \
  "v_perm_b32 v43, v5, 0, v23\n\t" \
  "v_perm_b32 v42, v13, v9, v23\n\t" \
  "v_perm_b32 v45, v7, v3, v23\n\t" \
  "v_perm_b32 v44, v15, v11, v23\n\t" \
  "v_perm_b32 v47, v9, v5, v23\n\t" \
  "v_perm_b32 v46, 0, v13, v23\n\t" \
  "ds_write_b64 v18, v[42:43] offset:384\n\t" \
  "ds_write_b64 v18, v[44:45] offset:392\n\t" \
  "ds_write_b64 v18, v[46:47] offset:400\n\t" \
  "s_branch .Lcnx_e3\n\t" \
  ".Lcnx_e1:\n\t" \
  "s_mov_b64 exec, 0x3ffff\n\t" \
  "ds_write_b64 v251, v[240:241] offset:47136\n\t" \
  "s_mov_b64 exec, 15\n\t" \
  "ds_write_b64 v17, v[240:241]\n\t" \
  "s_mov_b64 exec, -1\n\t" \
  "s_branch .Lcnx_e3\n\t" \
  ".Lcnx_e2:\n\t" \
  "s_setprio 1\n\t" \
  ".Lcnx_e3:\n\t" \
  "s_add_u32 m0, s25, 32800\n\t" \
  "s_add_u32 s24, s24, s26\n\t" \
  "buffer_load_dwordx4 v254, s[8:11], s24 offen lds\n\t" \
  "s_add_u32 m0, s25, 41000\n\t" \
  "s_add_u32 s24, s24, s26\n\t" \
  "buffer_load_dwordx4 v254, s[8:11], s24 offen lds\n\t" \
  "s_add_u32 m0, s25, 49200\n\t" \
  "s_add_u32 s24, s24, s26\n\t" \
  "buffer_load_dwordx4 v254, s[8:11], s24 offen lds\n\t" \
  "s_add_u32 m0, s25, 57400\n\t" \
  "s_add_u32 s24, s24, s26\n\t" \
  "buffer_load_dwordx4 v254, s[8:11], s24 offen lds\n\t" \
  "s_waitcnt vmcnt(6) lgkmcnt(0)\n\t" \
  "s_barrier\n\t" \
  "v_add_u32 v232, 1536, v247\n\t" \
  "ds_read2_b32 v[98:99], v247 offset0:0 offset1:6\n\t" \
  "ds_read2_b32 v[114:115], v247 offset0:192 offset1:198\n\t" \
  "ds_read2_b32 v[130:131], v232 offset0:0 offset1:6\n\t" \
  "ds_read2_b32 v[102:103], v247 offset0:12 offset1:18\n\t" \
  "ds_read2_b32 v[118:119], v247 offset0:204 offset1:210\n\t" \
  "ds_read2_b32 v[134:135], v232 offset0:12 offset1:18\n\t" \
  "ds_read2_b32 v[106:107], v247 offset0:24 offset1:30\n\t" \
  "ds_read2_b32 v[122:123], v247 offset0:216 offset1:222\n\t" \
  "ds_read2_b32 v[138:139], v232 offset0:24 offset1:30\n\t" \
  "ds_read2_b32 v[110:111], v247 offset0:36 offset1:42\n\t" \
  "ds_read2_b32 v[126:127], v247 offset0:228 offset1:234\n\t" \
  "ds_read2_b32 v[142:143], v232 offset0:36 offset1:42\n\t" \
  ".Lcnx_round:\n\t" \
  "v_add_u32 v232, 49200, v253\n\t" \
  "v_add_u32 v233, s39, v251\n\t" \
  "v_mfma_f32_32x32x16_bf16 v[176:191], v[242:245], v[240:243], 0\n\t" \
  "ds_read_b64 v[200:201], v253 offset:0\n\t" \
  "ds_read_b64 v[202:203], v253 offset:400\n\t" \
  "ds_read_b64 v[204:205], v253 offset:800\n\t" \
  "ds_read_b64 v[206:207], v253 offset:1200\n\t" \
  "ds_read_b64 v[208:209], v253 offset:8\n\t" \
  "ds_read_b64 v[210:211], v253 offset:408\n\t" \
  "ds_read_b64 v[212:213], v253 offset:808\n\t" \
  "ds_read_b64 v[214:215], v253 offset:1208\n\t" \
  "ds_read_b64 v[216:217], v253 offset:16\n\t" \
  "ds_read_b64 v[218:219], v253 offset:416\n\t" \
  "ds_read_b64 v[220:221], v253 offset:816\n\t" \
  "ds_read_b64 v[222:223], v253 offset:1216\n\t" \
  "s_cmp_lg_u32 s58, 0\n\t" \
  "s_cbranch_scc1 .Lcnx_c1\n\t" \
  "s_add_u32 m0, s25, 65600\n\t" \
  "s_add_u32 s24, s24, s26\n\t" \
  "buffer_load_dwordx4 v254, s[8:11], s24 offen lds\n\t" \
  "s_add_u32 m0, s25, 73800\n\t" \
  "s_add_u32 s24, s24, s26\n\t" \
  "buffer_load_dwordx4 v254, s[8:11], s24 offen lds\n\t" \
  "s_add_u32 m0, s25, 82000\n\t" \
  "s_add_u32 s24, s24, s26\n\t" \
  "buffer_load_dwordx4 v254, s[8:11], s24 offen lds\n\t" \
  "s_add_u32 m0, s25, 90200\n\t" \
  "s_add_u32 s24, s24, s26\n\t" \
  "buffer_load_dwordx4 v254, s[8:11], s24 offen lds\n\t" \
  ".Lcnx_c1:\n\t" \
  "s_waitcnt lgkmcnt(15)\n\t" \
  "v_and_b32 v96, 0xffff, v98\n\t" \
  "v_and_b32 v97, 0xffff0000, v98\n\t" \
  "v_and_b32 v98, 0xffff, v99\n\t" \
  "v_and_b32 v99, 0xffff0000, v99\n\t" \
  "v_and_b32 v112, 0xffff, v114\n\t" \
  "v_and_b32 v113, 0xffff0000, v114\n\t" \
  "v_and_b32 v114, 0xffff, v115\n\t" \
  "v_and_b32 v115, 0xffff0000, v115\n\t" \
  "v_and_b32 v128, 0xffff, v130\n\t" \
  "v_and_b32 v129, 0xffff0000, v130\n\t" \
  "v_and_b32 v130, 0xffff, v131\n\t" \
  "v_and_b32 v131, 0xffff0000, v131\n\t" \
  "s_waitcnt lgkmcnt(8)\n\t" \
  "v_smfmac_f32_16x16x64_bf16 v[144:147], v[96:99], v[200:207], v236\n\t" \
  "v_and_b32 v100, 0xffff, v102\n\t" \
  "v_and_b32 v101, 0xffff0000, v102\n\t" \
  "v_and_b32 v102, 0xffff, v103\n\t" \
  "v_and_b32 v103, 0xffff0000, v103\n\t" \
  "s_cmp_lg_u32 s58, 1\n\t" \
  "s_cbranch_scc1 .Lcnx_c2\n\t" \
  "s_waitcnt vmcnt(5)\n\t" \
  ".Lcnx_c2:\n\t" \
  "s_waitcnt vmcnt(8) lgkmcnt(0)\n\t" \
  "s_barrier\n\t" \
  "ds_read_b64 v[224:225], v253 offset:16400\n\t" \
  "ds_read_b64 v[226:227], v253 offset:16800\n\t" \
  "ds_read_b64 v[228:229], v253 offset:17200\n\t" \
  "ds_read_b64 v[230:231], v253 offset:17600\n\t" \
  "v_smfmac_f32_16x16x64_bf16 v[148:151], v[112:115], v[208:215], v236\n\t" \
  "s_cmp_lg_u32 s58, 1\n\t" \
  "s_cbranch_scc1 .Lcnx_c3\n\t" \
  "s_add_u32 m0, s25, 73800\n\t" \
  "s_add_u32 s24, s24, s26\n\t" \
  "buffer_load_dwordx4 v254, s[8:11], s24 offen lds\n\t" \
  ".Lcnx_c3:\n\t" \
  "ds_read_b64 v[200:201], v253 offset:16408\n\t" \
  "ds_read_b64 v[202:203], v253 offset:16808\n\t" \
  "ds_read_b64 v[204:205], v253 offset:17208\n\t" \
  "ds_read_b64 v[206:207], v253 offset:17608\n\t" \
  "v_and_b32 v116, 0xffff, v118\n\t" \
  "v_and_b32 v117, 0xffff0000, v118\n\t" \
  "v_and_b32 v118, 0xffff, v119\n\t" \
  "v_and_b32 v119, 0xffff0000, v119\n\t" \
  "v_smfmac_f32_16x16x64_bf16 v[152:155], v[128:131], v[216:223], v236\n\t" \
  "s_cmp_lg_u32 s58, 1\n\t" \
  "s_cbranch_scc1 .Lcnx_c4\n\t" \
  "s_add_u32 m0, s25, 82000\n\t" \
  "s_add_u32 s24, s24, s26\n\t" \
  "buffer_load_dwordx4 v254, s[8:11], s24 offen lds\n\t" \
  ".Lcnx_c4:\n\t" \
  "ds_read_b64 v[208:209], v253 offset:16416\n\t" \
  "ds_read_b64 v[210:211], v253 offset:16816\n\t" \
  "ds_read_b64 v[212:213], v253 offset:17216\n\t" \
  "ds_read_b64 v[214:215], v253 offset:17616\n\t" \
  "v_and_b32 v132, 0xffff, v134\n\t" \
  "v_and_b32 v133, 0xffff0000, v134\n\t" \
  "v_and_b32 v134, 0xffff, v135\n\t" \
  "v_and_b32 v135, 0xffff0000, v135\n\t" \
  "s_waitcnt lgkmcnt(8)\n\t" \
  "v_smfmac_f32_16x16x64_bf16 v[156:159], v[96:99], v[224:231], v236\n\t" \
  "s_cmp_lg_u32 s58, 1\n\t" \
  "s_cbranch_scc1 .Lcnx_c5\n\t" \
  "s_add_u32 m0, s25, 90200\n\t" \
  "s_add_u32 s24, s24, s26\n\t" \
  "buffer_load_dwordx4 v254, s[8:11], s24 offen lds\n\t" \
  ".Lcnx_c5:\n\t" \
  "v_and_b32 v104, 0xffff, v106\n\t" \
  "v_and_b32 v105, 0xffff0000, v106\n\t" \
  "v_and_b32 v106, 0xffff, v107\n\t" \
  "v_and_b32 v107, 0xffff0000, v107\n\t" \
  "v_smfmac_f32_16x16x64_bf16 v[144:147], v[100:103], v[224:231], v236\n\t" \
  "s_waitcnt vmcnt(6) lgkmcnt(0)\n\t" \
  "s_barrier\n\t" \
  "ds_read_b64 v[216:217], v253 offset:32800\n\t" \
  "ds_read_b64 v[218:219], v253 offset:33200\n\t" \
  "ds_read_b64 v[220:221], v253 offset:33600\n\t" \
  "ds_read_b64 v[222:223], v253 offset:34000\n\t" \
  "s_add_u32 m0, s25, 0\n\t" \
  "s_add_u32 s24, s24, s26\n\t" \
  "v_smfmac_f32_16x16x64_bf16 v[160:163], v[112:115], v[200:207], v236\n\t" \
  "buffer_load_dwordx4 v254, s[8:11], s24 offen lds\n\t" \
  "ds_read_b64 v[224:225], v253 offset:32808\n\t" \
  "ds_read_b64 v[226:227], v253 offset:33208\n\t" \
  "v_and_b32 v120, 0xffff, v122\n\t" \
  "v_and_b32 v121, 0xffff0000, v122\n\t" \
  "v_and_b32 v122, 0xffff, v123\n\t" \
  "v_and_b32 v123, 0xffff0000, v123\n\t" \
  "v_smfmac_f32_16x16x64_bf16 v[148:151], v[116:119], v[200:207], v236\n\t" \
  "ds_read_b64 v[228:229], v253 offset:33608\n\t" \
  "ds_read_b64 v[230:231], v253 offset:34008\n\t" \
  "s_add_u32 m0, s25, 8200\n\t" \
  "s_add_u32 s24, s24, s26\n\t" \
  "v_smfmac_f32_16x16x64_bf16 v[164:167], v[128:131], v[208:215], v236\n\t" \
  "buffer_load_dwordx4 v254, s[8:11], s24 offen lds\n\t" \
  "ds_read_b64 v[200:201], v253 offset:32816\n\t" \
  "ds_read_b64 v[202:203], v253 offset:33216\n\t" \
  "v_and_b32 v136, 0xffff, v138\n\t" \
  "v_and_b32 v137, 0xffff0000, v138\n\t" \
  "v_and_b32 v138, 0xffff, v139\n\t" \
  "v_and_b32 v139, 0xffff0000, v139\n\t" \
  "v_mfma_f32_32x32x16_bf16 v[0:15], v[242:245], v[240:243], 0\n\t" \
  "v_smfmac_f32_16x16x64_bf16 v[152:155], v[132:135], v[208:215], v236\n\t" \
  "ds_read_b64 v[204:205], v253 offset:33616\n\t" \
  "s_waitcnt lgkmcnt(7)\n\t" \
  "v_smfmac_f32_16x16x64_bf16 v[168:171], v[96:99], v[216:223], v236\n\t" \
  "ds_read_b64 v[206:207], v253 offset:34016\n\t" \
  "v_and_b32 v108, 0xffff, v110\n\t" \
  "v_and_b32 v109, 0xffff0000, v110\n\t" \
  "v_and_b32 v110, 0xffff, v111\n\t" \
  "v_and_b32 v111, 0xffff0000, v111\n\t" \
  "s_add_u32 m0, s25, 16400\n\t" \
  "s_add_u32 s24, s24, s26\n\t" \
  "v_smfmac_f32_16x16x64_bf16 v[156:159], v[100:103], v[216:223], v236\n\t" \
  "buffer_load_dwordx4 v254, s[8:11], s24 offen lds\n\t" \
  "v_smfmac_f32_16x16x64_bf16 v[144:147], v[104:107], v[216:223], v236\n\t" \
  "s_waitcnt vmcnt(7) lgkmcnt(0)\n\t" \
  "s_barrier\n\t" \
  "v_smfmac_f32_16x16x64_bf16 v[172:175], v[112:115], v[224:231], v236\n\t" \
  "ds_read_b64 v[208:209], v232 offset:0\n\t" \
  "ds_read_b64 v[210:211], v232 offset:400\n\t" \
  "v_and_b32 v124, 0xffff, v126\n\t" \
  "v_and_b32 v125, 0xffff0000, v126\n\t" \
  "v_and_b32 v126, 0xffff, v127\n\t" \
  "v_and_b32 v127, 0xffff0000, v127\n\t" \
  "s_add_u32 m0, s25, 24600\n\t" \
  "s_add_u32 s24, s24, s26\n\t" \
  "v_smfmac_f32_16x16x64_bf16 v[160:163], v[116:119], v[224:231], v236\n\t" \
  "buffer_load_dwordx4 v254, s[8:11], s24 offen lds\n\t" \
  "ds_read_b64 v[212:213], v232 offset:800\n\t" \
  "ds_read_b64 v[214:215], v232 offset:1200\n\t" \
  "v_smfmac_f32_16x16x64_bf16 v[148:151], v[120:123], v[224:231], v236\n\t" \
  "ds_read_b64 v[216:217], v232 offset:8\n\t" \
  "ds_read_b64 v[218:219], v232 offset:408\n\t" \
  "v_smfmac_f32_16x16x64_bf16 v[176:179], v[128:131], v[200:207], v236\n\t" \
  "ds_read_b64 v[220:221], v232 offset:808\n\t" \
  "ds_read_b64 v[222:223], v232 offset:1208\n\t" \
  "v_and_b32 v140, 0xffff, v142\n\t" \
  "v_and_b32 v141, 0xffff0000, v142\n\t" \
  "v_and_b32 v142, 0xffff, v143\n\t" \
  "v_and_b32 v143, 0xffff0000, v143\n\t" \
  "v_smfmac_f32_16x16x64_bf16 v[164:167], v[132:135], v[200:207], v236\n\t" \
  "ds_read_b64 v[224:225], v232 offset:16\n\t" \
  "s_add_u32 m0, s25, 32800\n\t" \
  "s_add_u32 s24, s24, s26\n\t" \
  "v_smfmac_f32_16x16x64_bf16 v[152:155], v[136:139], v[200:207], v236\n\t" \
  "buffer_load_dwordx4 v254, s[8:11], s24 offen lds\n\t" \
  "ds_read_b64 v[226:227], v232 offset:416\n\t" \
  "s_waitcnt lgkmcnt(6)\n\t" \
  "v_smfmac_f32_16x16x64_bf16 v[180:183], v[96:99], v[208:215], v236\n\t" \
  "ds_read_b64 v[228:229], v232 offset:816\n\t" \
  "v_smfmac_f32_16x16x64_bf16 v[168:171], v[100:103], v[208:215], v236\n\t" \
  "ds_read_b64 v[230:231], v232 offset:1216\n\t" \
  "v_mfma_f32_32x32x16_bf16 v[16:31], v[242:245], v[240:243], 0\n\t" \
  "v_mov_b32 v196, 0x3f003f00\n\t" \
  "v_mov_b32 v197, 0x3f003f00\n\t" \
  "v_smfmac_f32_16x16x64_bf16 v[156:159], v[104:107], v[208:215], v236\n\t" \
  "v_mov_b32 v198, 0x3f003f00\n\t" \
  "v_mov_b32 v199, 0x3f003f00\n\t" \
  "s_add_u32 m0, s25, 41000\n\t" \
  "s_add_u32 s24, s24, s26\n\t" \
  "v_smfmac_f32_16x16x64_bf16 v[144:147], v[108:111], v[208:215], v236\n\t" \
  "buffer_load_dwordx4 v254, s[8:11], s24 offen lds\n\t" \
  "s_waitcnt vmcnt(8) lgkmcnt(0)\n\t" \
  "s_barrier\n\t" \
  "v_smfmac_f32_16x16x64_bf16 v[184:187], v[112:115], v[216:223], v236\n\t" \
  "ds_read_b64 v[200:201], v232 offset:16400\n\t" \
  "ds_read_b64 v[202:203], v232 offset:16800\n\t" \
  "v_smfmac_f32_16x16x64_bf16 v[172:175], v[116:119], v[216:223], v236\n\t" \
  "ds_read_b64 v[204:205], v232 offset:17200\n\t" \
  "ds_read_b64 v[206:207], v232 offset:17600\n\t" \
  "v_smfmac_f32_16x16x64_bf16 v[160:163], v[120:123], v[216:223], v236\n\t" \
  "ds_read_b64 v[208:209], v232 offset:16408\n\t" \
  "v_cvt_pk_bf16_f32 v144, v144, v145\n\t" \
  "v_cvt_pk_bf16_f32 v145, v146, v147\n\t" \
  "s_add_u32 m0, s25, 49200\n\t" \
  "s_add_u32 s24, s24, s26\n\t" \
  "v_smfmac_f32_16x16x64_bf16 v[148:151], v[124:127], v[216:223], v236\n\t" \
  "buffer_load_dwordx4 v254, s[8:11], s24 offen lds\n\t" \
  "ds_read_b64 v[210:211], v232 offset:16808\n\t" \
  "v_smfmac_f32_16x16x64_bf16 v[188:191], v[128:131], v[224:231], v236\n\t" \
  "ds_read_b64 v[212:213], v232 offset:17208\n\t" \
  "v_smfmac_f32_16x16x64_bf16 v[176:179], v[132:135], v[224:231], v236\n\t" \
  "ds_read_b64 v[214:215], v232 offset:17608\n\t" \
  "v_smfmac_f32_16x16x64_bf16 v[164:167], v[136:139], v[224:231], v236\n\t" \
  "ds_read_b64 v[216:217], v232 offset:16416\n\t" \
  "v_cvt_pk_bf16_f32 v148, v148, v149\n\t" \
  "v_cvt_pk_bf16_f32 v149, v150, v151\n\t" \
  "s_add_u32 m0, s25, 57400\n\t" \
  "s_add_u32 s24, s24, s26\n\t" \
  "v_smfmac_f32_16x16x64_bf16 v[152:155], v[140:143], v[224:231], v236\n\t" \
  "buffer_load_dwordx4 v254, s[8:11], s24 offen lds\n\t" \
  "ds_read_b64 v[218:219], v232 offset:16816\n\t" \
  "s_waitcnt lgkmcnt(6)\n\t" \
  "v_smfmac_f32_16x16x64_bf16 v[0:3], v[96:99], v[200:207], v236\n\t" \
  "ds_read_b64 v[220:221], v232 offset:17216\n\t" \
  "v_smfmac_f32_16x16x64_bf16 v[180:183], v[100:103], v[200:207], v236\n\t" \
  "ds_read_b64 v[222:223], v232 offset:17616\n\t" \
  "v_mfma_f32_32x32x16_bf16 v[32:47], v[242:245], v[240:243], 0\n\t" \
  "v_smfmac_f32_16x16x64_bf16 v[168:171], v[104:107], v[200:207], v236\n\t" \
  "v_cvt_pk_bf16_f32 v152, v152, v153\n\t" \
  "v_cvt_pk_bf16_f32 v153, v154, v155\n\t" \
  "v_smfmac_f32_16x16x64_bf16 v[156:159], v[108:111], v[200:207], v236\n\t" \
  "s_waitcnt vmcnt(8) lgkmcnt(0)\n\t" \
  "s_barrier\n\t" \
  "s_add_u32 m0, s25, 65600\n\t" \
  "s_add_u32 s24, s24, s26\n\t" \
  "v_smfmac_f32_16x16x64_bf16 v[4:7], v[112:115], v[208:215], v236\n\t" \
  "buffer_load_dwordx4 v254, s[8:11], s24 offen lds\n\t" \
  "ds_read_b64 v[224:225], v232 offset:32800\n\t" \
  "ds_read_b64 v[226:227], v232 offset:33200\n\t" \
  "v_smfmac_f32_16x16x64_bf16 v[184:187], v[116:119], v[208:215], v236\n\t" \
  "ds_read_b64 v[228:229], v232 offset:33600\n\t" \
  "ds_read_b64 v[230:231], v232 offset:34000\n\t" \
  "s_add_u32 m0, s25, 73800\n\t" \
  "s_add_u32 s24, s24, s26\n\t" \
  "v_smfmac_f32_16x16x64_bf16 v[172:175], v[120:123], v[208:215], v236\n\t" \
  "buffer_load_dwordx4 v254, s[8:11], s24 offen lds\n\t" \
  "ds_read_b64 v[200:201], v232 offset:32808\n\t" \
  "v_cvt_pk_bf16_f32 v156, v156, v157\n\t" \
  "v_cvt_pk_bf16_f32 v157, v158, v159\n\t" \
  "v_smfmac_f32_16x16x64_bf16 v[160:163], v[124:127], v[208:215], v236\n\t" \
  "ds_read_b64 v[202:203], v232 offset:33208\n\t" \
  "v_smfmac_f32_16x16x64_bf16 v[8:11], v[128:131], v[216:223], v236\n\t" \
  "ds_read_b64 v[204:205], v232 offset:33608\n\t" \
  "v_smfmac_f32_16x16x64_bf16 v[188:191], v[132:135], v[216:223], v236\n\t" \
  "ds_read_b64 v[206:207], v232 offset:34008\n\t" \
  "v_smfmac_f32_16x16x64_bf16 v[176:179], v[136:139], v[216:223], v236\n\t" \
  "buffer_store_dwordx4 v[52:55], v250, s[20:23], s61 offen offset:0 sc1\n\t" \
  "ds_read_b64 v[208:209], v232 offset:32816\n\t" \
  "v_cvt_pk_bf16_f32 v160, v160, v161\n\t" \
  "v_cvt_pk_bf16_f32 v161, v162, v163\n\t" \
  "v_smfmac_f32_16x16x64_bf16 v[164:167], v[140:143], v[216:223], v236\n\t" \
  "ds_read_b64 v[210:211], v232 offset:33216\n\t" \
  "s_waitcnt lgkmcnt(6)\n\t" \
  "v_smfmac_f32_16x16x64_bf16 v[12:15], v[96:99], v[224:231], v236\n\t" \
  "ds_read_b64 v[212:213], v232 offset:33616\n\t" \
  "v_smfmac_f32_16x16x64_bf16 v[0:3], v[100:103], v[224:231], v236\n\t" \
  "ds_read_b64 v[214:215], v232 offset:34016\n\t" \
  "v_smfmac_f32_16x16x64_bf16 v[180:183], v[104:107], v[224:231], v236\n\t" \
  "s_nop 0\n\t" \
  "v_cvt_pk_bf16_f32 v164, v164, v165\n\t" \
  "v_cvt_pk_bf16_f32 v165, v166, v167\n\t" \
  "v_smfmac_f32_16x16x64_bf16 v[168:171], v[108:111], v[224:231], v236\n\t" \
  "s_waitcnt vmcnt(9) lgkmcnt(0)\n\t" \
  "s_barrier\n\t" \
  "v_smfmac_f32_16x16x64_bf16 v[16:19], v[112:115], v[200:207], v236\n\t" \
  "ds_write_b64 v193, v[144:145] offset:12288\n\t" \
  "ds_read_b64 v[216:217], v253 offset:0\n\t" \
  "v_smfmac_f32_16x16x64_bf16 v[4:7], v[116:119], v[200:207], v236\n\t" \
  "ds_write_b64 v193, v[148:149] offset:16384\n\t" \
  "ds_read_b64 v[218:219], v253 offset:400\n\t" \
  "v_smfmac_f32_16x16x64_bf16 v[184:187], v[120:123], v[200:207], v236\n\t" \
  "ds_write_b64 v193, v[152:153] offset:20480\n\t" \
  "ds_read_b64 v[220:221], v253 offset:800\n\t" \
  "v_cvt_pk_bf16_f32 v168, v168, v169\n\t" \
  "v_cvt_pk_bf16_f32 v169, v170, v171\n\t" \
  "v_smfmac_f32_16x16x64_bf16 v[172:175], v[124:127], v[200:207], v236\n\t" \
  "ds_write_b64 v193, v[156:157] offset:0\n\t" \
  "ds_read_b64 v[222:223], v253 offset:1200\n\t" \
  "v_smfmac_f32_16x16x64_bf16 v[20:23], v[128:131], v[208:215], v236\n\t" \
  "buffer_store_dwordx4 v[60:63], v250, s[20:23], s61 offen offset:64 sc1\n\t" \
  "ds_read_b64 v[224:225], v253 offset:8\n\t" \
  "ds_read_b64 v[226:227], v253 offset:408\n\t" \
  "v_smfmac_f32_16x16x64_bf16 v[8:11], v[132:135], v[208:215], v236\n\t" \
  "ds_write_b64 v193, v[160:161] offset:4096\n\t" \
  "ds_read_b64 v[228:229], v253 offset:808\n\t" \
  "v_smfmac_f32_16x16x64_bf16 v[188:191], v[136:139], v[208:215], v236\n\t" \
  "ds_write_b64 v193, v[164:165] offset:8192\n\t" \
  "ds_read_b64 v[230:231], v253 offset:1208\n\t" \
  "v_cvt_pk_bf16_f32 v172, v172, v173\n\t" \
  "v_cvt_pk_bf16_f32 v173, v174, v175\n\t" \
  "v_smfmac_f32_16x16x64_bf16 v[176:179], v[140:143], v[208:215], v236\n\t" \
  "ds_read_b64 v[200:201], v253 offset:16\n\t" \
  "ds_read_b64 v[202:203], v253 offset:416\n\t" \
  "s_waitcnt lgkmcnt(8)\n\t" \
  "v_smfmac_f32_16x16x64_bf16 v[24:27], v[96:99], v[216:223], v236\n\t" \
  "ds_read_b64 v[204:205], v253 offset:816\n\t" \
  "v_smfmac_f32_16x16x64_bf16 v[12:15], v[100:103], v[216:223], v236\n\t" \
  "ds_read_b64 v[206:207], v253 offset:1216\n\t" \
  "v_smfmac_f32_16x16x64_bf16 v[0:3], v[104:107], v[216:223], v236\n\t" \
  "v_cvt_pk_bf16_f32 v176, v176, v177\n\t" \
  "v_cvt_pk_bf16_f32 v177, v178, v179\n\t" \
  "v_smfmac_f32_16x16x64_bf16 v[180:183], v[108:111], v[216:223], v236\n\t" \
  "s_waitcnt vmcnt(8) lgkmcnt(0)\n\t" \
  "s_barrier\n\t" \
  "v_smfmac_f32_16x16x64_bf16 v[28:31], v[112:115], v[224:231], v236\n\t" \
  "ds_write_b64 v192, v[168:169] offset:0\n\t" \
  "ds_read_b64 v[208:209], v253 offset:16400\n\t" \
  "ds_read_b128 v[144:147], v194 offset:0\n\t" \
  "v_smfmac_f32_16x16x64_bf16 v[16:19], v[116:119], v[224:231], v236\n\t" \
  "ds_write_b64 v192, v[172:173] offset:4096\n\t" \
  "ds_read_b64 v[210:211], v253 offset:16800\n\t" \
  "ds_read_b128 v[148:151], v194 offset:4096\n\t" \
  "v_smfmac_f32_16x16x64_bf16 v[4:7], v[120:123], v[224:231], v236\n\t" \
  "buffer_store_dwordx4 v[68:71], v250, s[20:23], s61 offen offset:128 sc1\n\t" \
  "ds_read_b64 v[212:213], v253 offset:17200\n\t" \
  "ds_read_b64 v[214:215], v253 offset:17600\n\t" \
  "v_cvt_pk_bf16_f32 v180, v180, v181\n\t" \
  "v_cvt_pk_bf16_f32 v181, v182, v183\n\t" \
  "ds_read_b128 v[152:155], v194 offset:8192\n\t" \
  "v_smfmac_f32_16x16x64_bf16 v[184:187], v[124:127], v[224:231], v236\n\t" \
  "ds_write_b64 v192, v[176:177] offset:8192\n\t" \
  "ds_read_b64 v[216:217], v253 offset:16408\n\t" \
  "v_add_u32 v194, -86112, v194\n\t" \
  "v_smfmac_f32_16x16x64_bf16 v[32:35], v[128:131], v[200:207], v236\n\t" \
  "ds_read_b64 v[218:219], v253 offset:16808\n\t" \
  "ds_read_b64 v[220:221], v253 offset:17208\n\t" \
  "v_smfmac_f32_16x16x64_bf16 v[20:23], v[132:135], v[200:207], v236\n\t" \
  "ds_read_b64 v[222:223], v253 offset:17608\n\t" \
  "s_waitcnt lgkmcnt(11)\n\t" \
  "v_mfma_f32_16x16x32_bf16 v[168:171], v[144:147], v[144:147], 0\n\t" \
  "ds_read_b64 v[224:225], v253 offset:16416\n\t" \
  "v_smfmac_f32_16x16x64_bf16 v[8:11], v[136:139], v[200:207], v236\n\t" \
  "ds_read_b64 v[226:227], v253 offset:16816\n\t" \
  "v_cvt_pk_bf16_f32 v184, v184, v185\n\t" \
  "v_cvt_pk_bf16_f32 v185, v186, v187\n\t" \
  "v_mfma_f32_16x16x32_bf16 v[172:175], v[196:199], v[144:147], 0\n\t" \
  "ds_read_b64 v[228:229], v253 offset:17216\n\t" \
  "v_smfmac_f32_16x16x64_bf16 v[188:191], v[140:143], v[200:207], v236\n\t" \
  "ds_read_b64 v[230:231], v253 offset:17616\n\t" \
  "s_waitcnt lgkmcnt(12)\n\t" \
  "v_mfma_f32_16x16x32_bf16 v[168:171], v[148:151], v[148:151], v[168:171]\n\t" \
  "s_waitcnt lgkmcnt(10)\n\t" \
  "v_smfmac_f32_16x16x64_bf16 v[36:39], v[96:99], v[208:215], v236\n\t" \
  "v_mfma_f32_16x16x32_bf16 v[172:175], v[196:199], v[148:151], v[172:175]\n\t" \
  "v_smfmac_f32_16x16x64_bf16 v[24:27], v[100:103], v[208:215], v236\n\t" \
  "s_waitcnt lgkmcnt(9)\n\t" \
  "v_mfma_f32_16x16x32_bf16 v[168:171], v[152:155], v[152:155], v[168:171]\n\t" \
  "v_smfmac_f32_16x16x64_bf16 v[12:15], v[104:107], v[208:215], v236\n\t" \
  "v_cvt_pk_bf16_f32 v188, v188, v189\n\t" \
  "v_cvt_pk_bf16_f32 v189, v190, v191\n\t" \
  "v_mfma_f32_16x16x32_bf16 v[172:175], v[196:199], v[152:155], v[172:175]\n\t" \
  "v_smfmac_f32_16x16x64_bf16 v[0:3], v[108:111], v[208:215], v236\n\t" \
  "buffer_store_dwordx4 v[76:79], v250, s[20:23], s62 offen offset:0 sc1\n\t" \
  "s_waitcnt vmcnt(8) lgkmcnt(0)\n\t" \
  "s_barrier\n\t" \
  "s_cmp_lg_u32 s58, 0\n\t" \
  "s_cbranch_scc1 .Lcnx_c6\n\t" \
  "v_lshl_add_u32 v48, v233, 1, s63\n\t" \
  "s_lshl_b32 m0, s39, 1\n\t" \
  "v_add_u32 v49, 0x2000, v48\n\t" \
  "global_load_lds_dwordx4 v48, s[6:7]\n\t" \
  "s_add_u32 m0, m0, 0x2000\n\t" \
  "s_cmp_eq_u32 s39, 3584\n\t" \
  "s_cselect_b32 exec_hi, 1, -1\n\t" \
  "global_load_lds_dwordx4 v49, s[6:7]\n\t" \
  "s_mov_b64 exec, -1\n\t" \
  ".Lcnx_c6:\n\t" \
  "v_smfmac_f32_16x16x64_bf16 v[40:43], v[112:115], v[216:223], v236\n\t" \
  "ds_write_b64 v192, v[180:181] offset:12288\n\t" \
  "ds_read_b64 v[200:201], v253 offset:32800\n\t" \
  "v_smfmac_f32_16x16x64_bf16 v[28:31], v[116:119], v[216:223], v236\n\t" \
  "ds_write_b64 v192, v[184:185] offset:16384\n\t" \
  "ds_read_b64 v[202:203], v253 offset:33200\n\t" \
  "v_cndmask_b32 v168, v168, v169, s[40:41]\n\t" \
  "v_cndmask_b32 v170, v170, v171, s[40:41]\n\t" \
  "v_smfmac_f32_16x16x64_bf16 v[16:19], v[120:123], v[216:223], v236\n\t" \
  "ds_write_b64 v192, v[188:189] offset:20480\n\t" \
  "ds_read_b64 v[204:205], v253 offset:33600\n\t" \
  "v_cvt_pk_bf16_f32 v0, v0, v1\n\t" \
  "v_cvt_pk_bf16_f32 v1, v2, v3\n\t" \
  "v_cndmask_b32 v173, v168, v170, s[42:43]\n\t" \
  "v_mov_b32 v174, 0x3927c5ac\n\t" \
  "ds_read_b64_tr_b16 v[156:157], v195 offset:0\n\t" \
  "v_smfmac_f32_16x16x64_bf16 v[4:7], v[124:127], v[216:223], v236\n\t" \
  "ds_read_b64 v[206:207], v253 offset:34000\n\t" \
  "ds_read_b64 v[208:209], v253 offset:32808\n\t" \
  "s_not_b64 vcc, s[52:53]\n\t" \
  "ds_read_b64_tr_b16 v[158:159], v195 offset:4096\n\t" \
  "v_smfmac_f32_16x16x64_bf16 v[44:47], v[128:131], v[224:231], v236\n\t" \
  "ds_read_b64 v[210:211], v253 offset:33208\n\t" \
  "ds_read_b64 v[212:213], v253 offset:33608\n\t" \
  "ds_read_b64_tr_b16 v[160:161], v195 offset:2048\n\t" \
  "v_smfmac_f32_16x16x64_bf16 v[32:35], v[132:135], v[224:231], v236\n\t" \
  "ds_read_b64 v[214:215], v253 offset:34008\n\t" \
  "ds_read_b64 v[216:217], v253 offset:32816\n\t" \
  "ds_read_b64_tr_b16 v[162:163], v195 offset:6144\n\t" \
  "v_smfmac_f32_16x16x64_bf16 v[20:23], v[136:139], v[224:231], v236\n\t" \
  "ds_read_b64 v[218:219], v253 offset:33216\n\t" \
  "v_cvt_pk_bf16_f32 v4, v4, v5\n\t" \
  "v_cvt_pk_bf16_f32 v5, v6, v7\n\t" \
  "ds_read_b64_tr_b16 v[164:165], v195 offset:8192\n\t" \
  "v_smfmac_f32_16x16x64_bf16 v[8:11], v[140:143], v[224:231], v236\n\t" \
  "ds_read_b64 v[220:221], v253 offset:33616\n\t" \
  "ds_read_b64_tr_b16 v[166:167], v195 offset:10240\n\t" \
  "s_waitcnt lgkmcnt(12)\n\t" \
  "v_smfmac_f32_16x16x64_bf16 v[36:39], v[100:103], v[200:207], v236\n\t" \
  "ds_read_b64 v[222:223], v253 offset:34016\n\t" \
  "v_add_u32 v195, -86112, v195\n\t" \
  "v_smfmac_f32_16x16x64_bf16 v[24:27], v[104:107], v[200:207], v236\n\t" \
  "v_smfmac_f32_16x16x64_bf16 v[12:15], v[108:111], v[200:207], v236\n\t" \
  "buffer_store_dwordx4 v[84:87], v250, s[20:23], s62 offen offset:64 sc1\n\t" \
  "v_cvt_pk_bf16_f32 v8, v8, v9\n\t" \
  "v_cvt_pk_bf16_f32 v9, v10, v11\n\t" \
  "s_waitcnt vmcnt(7) lgkmcnt(0)\n\t" \
  "s_barrier\n\t" \
  "s_cmp_ge_u32 s36, 2\n\t" \
  "s_cbranch_scc1 .Lcnx_c7\n\t" \
  "s_mov_b32 m0, s46\n\t" \
  "s_nop 0\n\t" \
  "buffer_load_dwordx4 v252, s[16:19], s33 offen lds\n\t" \
  "buffer_load_dwordx4 v252, s[16:19], s34 offen offset:1024 lds\n\t" \
  "buffer_load_dwordx4 v252, s[16:19], s35 offen offset:2048 lds\n\t" \
  "s_add_u32 s33, s33, s32\n\t" \
  "s_add_u32 s34, s34, s32\n\t" \
  "s_add_u32 s35, s35, s32\n\t" \
  "s_mov_b32 m0, s47\n\t" \
  "s_nop 0\n\t" \
  "buffer_load_dwordx4 v252, s[16:19], s33 offen lds\n\t" \
  "buffer_load_dwordx4 v252, s[16:19], s34 offen offset:1024 lds\n\t" \
  "buffer_load_dwordx4 v252, s[16:19], s35 offen offset:2048 lds\n\t" \
  "s_add_u32 s33, s33, s32\n\t" \
  "s_add_u32 s34, s34, s32\n\t" \
  "s_add_u32 s35, s35, s32\n\t" \
  ".Lcnx_c7:\n\t" \
  "v_smfmac_f32_16x16x64_bf16 v[40:43], v[116:119], v[208:215], v236\n\t" \
  "ds_write_b64 v192, v[0:1] offset:24576\n\t" \
  "ds_read_b64 v[224:225], v232 offset:0\n\t" \
  "ds_read_b128 v[180:183], v194 offset:0\n\t" \
  "ds_read_b128 v[184:187], v194 offset:4096\n\t" \
  "ds_read_b128 v[188:191], v194 offset:8192\n\t" \
  "v_smfmac_f32_16x16x64_bf16 v[28:31], v[120:123], v[208:215], v236\n\t" \
  "ds_write_b64 v192, v[4:5] offset:28672\n\t" \
  "ds_read_b64 v[226:227], v232 offset:400\n\t" \
  "ds_read_b64 v[228:229], v232 offset:800\n\t" \
  "v_smfmac_f32_16x16x64_bf16 v[16:19], v[124:127], v[208:215], v236\n\t" \
  "ds_write_b64 v192, v[8:9] offset:32768\n\t" \
  "ds_read_b64 v[230:231], v232 offset:1200\n\t" \
  "ds_read_b64 v[200:201], v232 offset:8\n\t" \
  "v_cvt_pk_bf16_f32 v12, v12, v13\n\t" \
  "v_cvt_pk_bf16_f32 v13, v14, v15\n\t" \
  "v_smfmac_f32_16x16x64_bf16 v[44:47], v[132:135], v[216:223], v236\n\t" \
  "ds_write_b64 v192, v[12:13] offset:36864\n\t" \
  "ds_read_b64 v[202:203], v232 offset:408\n\t" \
  "ds_read_b64 v[204:205], v232 offset:808\n\t" \
  "s_waitcnt lgkmcnt(11)\n\t" \
  "v_mfma_f32_16x16x32_bf16 v[176:179], v[180:183], v[180:183], 0\n\t" \
  "ds_read_b64 v[206:207], v232 offset:1208\n\t" \
  "v_mfma_f32_16x16x32_bf16 v[168:171], v[196:199], v[180:183], 0\n\t" \
  "ds_read_b64 v[208:209], v232 offset:16\n\t" \
  "v_smfmac_f32_16x16x64_bf16 v[32:35], v[136:139], v[216:223], v236\n\t" \
  "ds_read_b64 v[210:211], v232 offset:416\n\t" \
  "ds_read_b64 v[212:213], v232 offset:816\n\t" \
  "s_waitcnt lgkmcnt(14)\n\t" \
  "v_mfma_f32_16x16x32_bf16 v[176:179], v[184:187], v[184:187], v[176:179]\n\t" \
  "ds_read_b64 v[214:215], v232 offset:1216\n\t" \
  "v_mfma_f32_16x16x32_bf16 v[168:171], v[196:199], v[184:187], v[168:171]\n\t" \
  "v_smfmac_f32_16x16x64_bf16 v[20:23], v[140:143], v[216:223], v236\n\t" \
  "v_cvt_pk_bf16_f32 v16, v16, v17\n\t" \
  "v_cvt_pk_bf16_f32 v17, v18, v19\n\t" \
  "s_waitcnt lgkmcnt(14)\n\t" \
  "v_mfma_f32_16x16x32_bf16 v[176:179], v[188:191], v[188:191], v[176:179]\n\t" \
  "ds_write_b64 v192, v[16:17] offset:40960\n\t" \
  "v_mfma_f32_16x16x32_bf16 v[168:171], v[196:199], v[188:191], v[168:171]\n\t" \
  "s_waitcnt lgkmcnt(10)\n\t" \
  "v_smfmac_f32_16x16x64_bf16 v[36:39], v[104:107], v[224:231], v236\n\t" \
  "v_mov_b32 v183, 0\n\t" \
  "v_mov_b32 v187, 0\n\t" \
  "s_nop 1\n\t" \
  "v_cndmask_b32 v176, v176, v177, s[40:41]\n\t" \
  "v_cndmask_b32 v178, v178, v179, s[40:41]\n\t" \
  "v_cndmask_b32 v176, v176, v178, s[42:43]\n\t" \
  "v_smfmac_f32_16x16x64_bf16 v[24:27], v[108:111], v[224:231], v236\n\t" \
  "v_cndmask_b32_dpp v172, v168, v172, vcc row_ror:8 row_mask:0xf bank_mask:0xf\n\t" \
  "v_mul_f32 v172, 0x3daaaaab, v172\n\t" \
  "v_cndmask_b32_dpp v173, v176, v173, vcc row_ror:8 row_mask:0xf bank_mask:0xf\n\t" \
  "v_fmamk_f32 v173, v173, 0x3e2aaaab, v174\n\t" \
  "s_waitcnt vmcnt(5) lgkmcnt(0)\n\t" \
  "s_barrier\n\t" \
  "ds_read_b64 v[216:217], v232 offset:16400\n\t" \
  "ds_read_b64 v[218:219], v232 offset:16800\n\t" \
  "ds_read_b64 v[220:221], v232 offset:17200\n\t" \
  "ds_read_b64 v[222:223], v232 offset:17600\n\t" \
  "v_smfmac_f32_16x16x64_bf16 v[40:43], v[120:123], v[200:207], v236\n\t" \
  "ds_read_b64 v[224:225], v232 offset:16408\n\t" \
  "ds_read_b64 v[226:227], v232 offset:16808\n\t" \
  "v_cvt_pk_bf16_f32 v20, v20, v21\n\t" \
  "v_cvt_pk_bf16_f32 v21, v22, v23\n\t" \
  "v_fma_f32 v173, -v172, v172, v173\n\t" \
  "v_max_f32 v173, v174, v173\n\t" \
  "v_rsq_f32 v173, v173 div:2\n\t" \
  "s_mov_b64 vcc, s[40:41]\n\t" \
  "v_smfmac_f32_16x16x64_bf16 v[28:31], v[124:127], v[200:207], v236\n\t" \
  "buffer_store_dwordx4 v[92:95], v250, s[20:23], s62 offen offset:128 sc1\n\t" \
  "ds_read_b64 v[228:229], v232 offset:17208\n\t" \
  "ds_read_b64 v[230:231], v232 offset:17608\n\t" \
  "v_cvt_pk_bf16_f32 v175, v173, v173\n\t" \
  "v_and_b32 v173, 0xffff0000, v175\n\t" \
  "v_mul_f32 v174, -v172, v173 div:2\n\t" \
  "v_cndmask_b32_sdwa v175, v175, v173, vcc dst_sel:DWORD dst_unused:UNUSED_PAD src0_sel:WORD_1 src1_sel:DWORD\n\t" \
  "v_smfmac_f32_16x16x64_bf16 v[44:47], v[136:139], v[208:215], v236\n\t" \
  "ds_write_b64 v192, v[20:21] offset:45056\n\t" \
  "ds_read_b64 v[200:201], v232 offset:16416\n\t" \
  "v_cvt_pk_bf16_f32 v24, v24, v25\n\t" \
  "v_cvt_pk_bf16_f32 v25, v26, v27\n\t" \
  "v_and_b32 v172, 0xffff0000, v174\n\t" \
  "s_or_b64 vcc, s[52:53], s[42:43]\n\t" \
  "v_sub_f32 v174, v174, v172\n\t" \
  "v_cndmask_b32 v180, v175, v240, vcc\n\t" \
  "v_smfmac_f32_16x16x64_bf16 v[32:35], v[140:143], v[208:215], v236\n\t" \
  "ds_write_b64 v192, v[24:25] offset:49152\n\t" \
  "ds_read_b64 v[202:203], v232 offset:16816\n\t" \
  "v_perm_b32 v174, v172, v174, s55\n\t" \
  "v_cndmask_b32_dpp v184, v175, v240, vcc row_ror:8 row_mask:0xf bank_mask:0xf\n\t" \
  "s_mov_b64 vcc, s[52:53]\n\t" \
  "v_cndmask_b32 v182, v174, v240, vcc\n\t" \
  "ds_read_b64_tr_b16 v[144:145], v195 offset:0\n\t" \
  "ds_read_b64_tr_b16 v[146:147], v195 offset:4096\n\t" \
  "s_waitcnt lgkmcnt(10)\n\t" \
  "v_smfmac_f32_16x16x64_bf16 v[36:39], v[108:111], v[216:223], v236\n\t" \
  "ds_read_b64 v[204:205], v232 offset:17216\n\t" \
  "ds_read_b64 v[206:207], v232 offset:17616\n\t" \
  "v_cvt_pk_bf16_f32 v28, v28, v29\n\t" \
  "v_cvt_pk_bf16_f32 v29, v30, v31\n\t" \
  "v_cndmask_b32_dpp v186, v174, v240, vcc row_ror:8 row_mask:0xf bank_mask:0xf\n\t" \
  "s_orn2_b64 vcc, s[52:53], s[42:43]\n\t" \
  "v_cndmask_b32 v181, v175, v240, vcc\n\t" \
  "v_cndmask_b32_dpp v185, v175, v240, vcc row_ror:8 row_mask:0xf bank_mask:0xf\n\t" \
  "ds_read_b64_tr_b16 v[148:149], v195 offset:2048\n\t" \
  "ds_read_b64_tr_b16 v[150:151], v195 offset:6144\n\t" \
  "s_waitcnt lgkmcnt(10)\n\t" \
  "v_smfmac_f32_16x16x64_bf16 v[40:43], v[124:127], v[224:231], v236\n\t" \
  "ds_write_b64 v192, v[28:29] offset:53248\n\t" \
  "ds_read_b64_tr_b16 v[152:153], v195 offset:8192\n\t" \
  "ds_read_b64_tr_b16 v[154:155], v195 offset:10240\n\t" \
  "v_mfma_f32_16x16x16_bf16 v[0:3], v[196:197], v[182:183], 0\n\t" \
  "v_mfma_f32_16x16x16_bf16 v[4:7], v[196:197], v[186:187], 0\n\t" \
  "s_waitcnt lgkmcnt(5)\n\t" \
  "v_smfmac_f32_16x16x64_bf16 v[44:47], v[140:143], v[200:207], v236\n\t" \
  "v_cvt_pk_bf16_f32 v32, v32, v33\n\t" \
  "v_cvt_pk_bf16_f32 v33, v34, v35\n\t" \
  "v_mfma_f32_16x16x16_bf16 v[188:191], v[156:157], v[180:181], v[0:3]\n\t" \
  "ds_write_b64 v192, v[32:33] offset:57344\n\t" \
  "v_mfma_f32_16x16x16_bf16 v[176:179], v[158:159], v[180:181], v[0:3]\n\t" \
  "v_cvt_pk_bf16_f32 v36, v36, v37\n\t" \
  "v_cvt_pk_bf16_f32 v37, v38, v39\n\t" \
  "ds_write_b64 v193, v[36:37] offset:0\n\t" \
  "v_cvt_pk_bf16_f32 v40, v40, v41\n\t" \
  "v_cvt_pk_bf16_f32 v41, v42, v43\n\t" \
  "ds_write_b64 v193, v[40:41] offset:4096\n\t" \
  "v_mfma_f32_16x16x16_bf16 v[168:171], v[160:161], v[180:181], v[0:3]\n\t" \
  "v_cvt_pk_bf16_f32 v96, v188, v189\n\t" \
  "v_cvt_pk_bf16_f32 v97, v190, v191\n\t" \
  "v_mfma_f32_16x16x16_bf16 v[172:175], v[162:163], v[180:181], v[0:3]\n\t" \
  "v_cvt_pk_bf16_f32 v44, v44, v45\n\t" \
  "v_cvt_pk_bf16_f32 v45, v46, v47\n\t" \
  "ds_write_b64 v193, v[44:45] offset:8192\n\t" \
  "v_cvt_pk_bf16_f32 v98, v176, v177\n\t" \
  "v_cvt_pk_bf16_f32 v99, v178, v179\n\t" \
  "v_mfma_f32_16x16x16_bf16 v[188:191], v[164:165], v[180:181], v[0:3]\n\t" \
  "v_cvt_pk_bf16_f32 v112, v168, v169\n\t" \
  "v_cvt_pk_bf16_f32 v113, v170, v171\n\t" \
  "v_mfma_f32_16x16x16_bf16 v[176:179], v[166:167], v[180:181], v[0:3]\n\t" \
  "v_cvt_pk_bf16_f32 v114, v172, v173\n\t" \
  "v_cvt_pk_bf16_f32 v115, v174, v175\n\t" \
  "s_nop 2\n\t" \
  "v_cvt_pk_bf16_f32 v128, v188, v189\n\t" \
  "v_cvt_pk_bf16_f32 v129, v190, v191\n\t" \
  "s_nop 0\n\t" \
  "v_cvt_pk_bf16_f32 v130, v176, v177\n\t" \
  "v_cvt_pk_bf16_f32 v131, v178, v179\n\t" \
  "s_cmp_lg_u32 s58, 0\n\t" \
  "s_cbranch_scc1 .Lcnx_c8\n\t" \
  "s_waitcnt vmcnt(1)\n\t" \
  ".Lcnx_c8:\n\t" \
  "s_waitcnt lgkmcnt(0)\n\t" \
  "s_barrier\n\t" \
  "ds_read_b64_tr_b16 v[208:209], v248 offset:43048\n\t" \
  "ds_read_b64_tr_b16 v[210:211], v248 offset:44072\n\t" \
  "ds_read_b128 v[104:107], v194 offset:24576\n\t" \
  "ds_read_b128 v[120:123], v194 offset:28672\n\t" \
  "ds_read_b128 v[136:139], v194 offset:32768\n\t" \
  "v_add_u32 v194, s54, v194\n\t" \
  "ds_read_b128 v[108:111], v194 offset:49152\n\t" \
  "ds_read_b128 v[124:127], v194 offset:53248\n\t" \
  "ds_read_b128 v[140:143], v194 offset:57344\n\t" \
  "v_mfma_f32_16x16x16_bf16 v[168:171], v[144:145], v[184:185], v[4:7]\n\t" \
  "ds_read_b64_tr_b16 v[212:213], v248 offset:46120\n\t" \
  "ds_read_b64_tr_b16 v[214:215], v248 offset:47144\n\t" \
  "v_mfma_f32_16x16x16_bf16 v[172:175], v[146:147], v[184:185], v[4:7]\n\t" \
  "ds_read_b64_tr_b16 v[216:217], v248 offset:45096\n\t" \
  "ds_read_b64_tr_b16 v[218:219], v248 offset:48168\n\t" \
  "s_cmp_eq_u32 s37, 0\n\t" \
  "s_cbranch_scc1 .Lcnx_f0\n.Lcnx_g0:\n\t" \
  "v_mfma_f32_16x16x16_bf16 v[188:191], v[148:149], v[184:185], v[4:7]\n\t" \
  "ds_read_b64_tr_b16 v[160:161], v248 offset:43560\n\t" \
  "ds_read_b64_tr_b16 v[162:163], v248 offset:44584\n\t" \
  "v_cvt_pk_bf16_f32 v100, v168, v169\n\t" \
  "v_cvt_pk_bf16_f32 v101, v170, v171\n\t" \
  "v_mfma_f32_16x16x16_bf16 v[176:179], v[150:151], v[184:185], v[4:7]\n\t" \
  "ds_read_b64_tr_b16 v[164:165], v248 offset:46632\n\t" \
  "ds_read_b64_tr_b16 v[166:167], v248 offset:47656\n\t" \
  "s_cmp_eq_u32 s37, 0\n\t" \
  "s_cbranch_scc1 .Lcnx_f1\n.Lcnx_g1:\n\t" \
  "v_cvt_pk_bf16_f32 v102, v172, v173\n\t" \
  "v_cvt_pk_bf16_f32 v103, v174, v175\n\t" \
  "v_mfma_f32_16x16x16_bf16 v[168:171], v[152:153], v[184:185], v[4:7]\n\t" \
  "buffer_load_dwordx4 v[236:239], v250, s[8:11], s28 offen offset:0 nt\n\t" \
  "ds_read_b64_tr_b16 v[232:233], v248 offset:45608\n\t" \
  "ds_read_b64_tr_b16 v[234:235], v248 offset:48680\n\t" \
  "v_cvt_pk_bf16_f32 v116, v188, v189\n\t" \
  "v_cvt_pk_bf16_f32 v117, v190, v191\n\t" \
  "v_mfma_f32_16x16x16_bf16 v[172:175], v[154:155], v[184:185], v[4:7]\n\t" \
  "ds_read_b64_tr_b16 v[24:25], v195 offset:24576\n\t" \
  "ds_read_b64_tr_b16 v[26:27], v195 offset:28672\n\t" \
  "v_cvt_pk_bf16_f32 v118, v176, v177\n\t" \
  "v_cvt_pk_bf16_f32 v119, v178, v179\n\t" \
  "s_waitcnt lgkmcnt(15)\n\t" \
  "v_mfma_f32_16x16x32_bf16 v[144:147], v[208:211], v[96:99], 0\n\t" \
  "ds_read_b64_tr_b16 v[28:29], v195 offset:26624\n\t" \
  "ds_read_b64_tr_b16 v[30:31], v195 offset:30720\n\t" \
  "v_cvt_pk_bf16_f32 v132, v168, v169\n\t" \
  "v_cvt_pk_bf16_f32 v133, v170, v171\n\t" \
  "s_waitcnt lgkmcnt(12)\n\t" \
  "v_mfma_f32_16x16x32_bf16 v[144:147], v[212:215], v[112:115], v[144:147]\n\t" \
  "buffer_store_dwordx4 v[48:51], v250, s[20:23], s14 offen offset:0 sc1\n\t" \
  "ds_read_b64_tr_b16 v[32:33], v195 offset:32768\n\t" \
  "ds_read_b64_tr_b16 v[34:35], v195 offset:34816\n\t" \
  "v_cvt_pk_bf16_f32 v134, v172, v173\n\t" \
  "v_cvt_pk_bf16_f32 v135, v174, v175\n\t" \
  "v_mfma_f32_16x16x32_bf16 v[152:155], v[208:211], v[100:103], 0\n\t" \
  "v_add_u32 v195, s54, v195\n\t" \
  "ds_read_b64_tr_b16 v[36:37], v195 offset:49152\n\t" \
  "ds_read_b64_tr_b16 v[38:39], v195 offset:53248\n\t" \
  "s_waitcnt lgkmcnt(14)\n\t" \
  "v_mfma_f32_16x16x32_bf16 v[144:147], v[216:219], v[128:131], v[144:147]\n\t" \
  "ds_read_b64_tr_b16 v[40:41], v195 offset:51200\n\t" \
  "v_mfma_f32_16x16x32_bf16 v[152:155], v[212:215], v[116:119], v[152:155]\n\t" \
  "ds_read_b64_tr_b16 v[42:43], v195 offset:55296\n\t" \
  "v_mfma_f32_16x16x32_bf16 v[152:155], v[216:219], v[132:135], v[152:155]\n\t" \
  "ds_read_b64_tr_b16 v[44:45], v195 offset:57344\n\t" \
  "v_mfma_f32_16x16x32_bf16 v[8:11], v[104:107], v[104:107], 0\n\t" \
  "ds_read_b64_tr_b16 v[46:47], v195 offset:59392\n\t" \
  "v_mfma_f32_16x16x32_bf16 v[12:15], v[196:199], v[104:107], 0\n\t" \
  "v_cvt_pk_f16_f32 v220, |v146|, |v147| clamp\n\t" \
  "v_cvt_pknorm_i16_f32 v222, |v144|, |v145|\n\t" \
  "v_mfma_f32_16x16x32_bf16 v[8:11], v[120:123], v[120:123], v[8:11]\n\t" \
  "s_nop 0\n\t" \
  "v_cvt_pk_f16_f32 v224, |v154|, |v155| clamp\n\t" \
  "v_cvt_pknorm_i16_f32 v226, |v152|, |v153|\n\t" \
  "v_mfma_f32_16x16x32_bf16 v[12:15], v[196:199], v[120:123], v[12:15]\n\t" \
  "v_and_b32_sdwa v221, s56, v220 dst_sel:DWORD dst_unused:UNUSED_PAD src0_sel:DWORD src1_sel:WORD_1\n\t" \
  "v_and_b32 v220, s56, v220\n\t" \
  "v_mfma_f32_16x16x32_bf16 v[8:11], v[136:139], v[136:139], v[8:11]\n\t" \
  "ds_read_b32 v221, v221 offset:0\n\t" \
  "v_lshlrev_b32_sdwa v223, 2, v222 dst_sel:DWORD dst_unused:UNUSED_PAD src0_sel:DWORD src1_sel:BYTE_3\n\t" \
  "v_lshlrev_b32_sdwa v222, 2, v222 dst_sel:DWORD dst_unused:UNUSED_PAD src0_sel:DWORD src1_sel:BYTE_1\n\t" \
  "v_mfma_f32_16x16x32_bf16 v[12:15], v[196:199], v[136:139], v[12:15]\n\t" \
  "ds_read_b32 v220, v220 offset:0\n\t" \
  "v_and_b32_sdwa v225, s56, v224 dst_sel:DWORD dst_unused:UNUSED_PAD src0_sel:DWORD src1_sel:WORD_1\n\t" \
  "v_and_b32 v224, s56, v224\n\t" \
  "v_mfma_f32_16x16x32_bf16 v[16:19], v[108:111], v[108:111], 0\n\t" \
  "ds_read_b32 v223, v223 offset:15376\n\t" \
  "v_lshlrev_b32_sdwa v227, 2, v226 dst_sel:DWORD dst_unused:UNUSED_PAD src0_sel:DWORD src1_sel:BYTE_3\n\t" \
  "v_lshlrev_b32_sdwa v226, 2, v226 dst_sel:DWORD dst_unused:UNUSED_PAD src0_sel:DWORD src1_sel:BYTE_1\n\t" \
  "v_mfma_f32_16x16x32_bf16 v[20:23], v[196:199], v[108:111], 0\n\t" \
  "ds_read_b32 v222, v222 offset:15376\n\t" \
  "v_cndmask_b32 v8, v8, v9, s[40:41]\n\t" \
  "v_cndmask_b32 v10, v10, v11, s[40:41]\n\t" \
  "v_mfma_f32_16x16x32_bf16 v[16:19], v[124:127], v[124:127], v[16:19]\n\t" \
  "ds_read_b32 v225, v225 offset:0\n\t" \
  "v_cndmask_b32 v13, v8, v10, s[42:43]\n\t" \
  "v_mov_b32 v14, 0x3927c5ac\n\t" \
  "v_mfma_f32_16x16x32_bf16 v[20:23], v[196:199], v[124:127], v[20:23]\n\t" \
  "buffer_store_dwordx4 v[56:59], v250, s[20:23], s14 offen offset:64 sc1\n\t" \
  "ds_read_b32 v224, v224 offset:0\n\t" \
  "s_not_b64 vcc, s[52:53]\n\t" \
  "v_mfma_f32_16x16x32_bf16 v[16:19], v[140:143], v[140:143], v[16:19]\n\t" \
  "ds_read_b32 v227, v227 offset:15376\n\t" \
  "ds_read_b32 v226, v226 offset:15376\n\t" \
  "s_waitcnt lgkmcnt(4)\n\t" \
  "v_fma_f32 v147, |v147|, v221, v147 mul:4\n\t" \
  "v_fma_f32 v146, |v146|, v220, v146 mul:4\n\t" \
  "v_mfma_f32_16x16x32_bf16 v[20:23], v[196:199], v[140:143], v[20:23]\n\t" \
  "v_fma_mix_f32 v223, |v145|, v223, v223 op_sel:[0,1,0] op_sel_hi:[0,1,1] clamp\n\t" \
  "v_fma_mix_f32 v222, |v144|, v222, v222 op_sel:[0,1,0] op_sel_hi:[0,1,1] clamp\n\t" \
  "v_mfma_f32_16x16x32_bf16 v[148:151], v[160:163], v[96:99], 0\n\t" \
  "s_waitcnt lgkmcnt(0)\n\t" \
  "v_fma_f32 v155, |v155|, v225, v155 mul:4\n\t" \
  "v_fma_f32 v154, |v154|, v224, v154 mul:4\n\t" \
  "v_mfma_f32_16x16x32_bf16 v[156:159], v[160:163], v[100:103], 0\n\t" \
  "v_cndmask_b32 v16, v16, v17, s[40:41]\n\t" \
  "v_cndmask_b32 v18, v18, v19, s[40:41]\n\t" \
  "v_cndmask_b32 v16, v16, v18, s[42:43]\n\t" \
  "v_cndmask_b32_dpp v12, v20, v12, vcc row_ror:8 row_mask:0xf bank_mask:0xf\n\t" \
  "v_mul_f32 v12, 0x3daaaaab, v12\n\t" \
  "v_mfma_f32_16x16x32_bf16 v[148:151], v[164:167], v[112:115], v[148:151]\n\t" \
  "v_cndmask_b32_dpp v13, v16, v13, vcc row_ror:8 row_mask:0xf bank_mask:0xf\n\t" \
  "v_fmamk_f32 v13, v13, 0x3e2aaaab, v14\n\t" \
  "v_fma_f32 v13, -v12, v12, v13\n\t" \
  "v_max_f32 v13, v14, v13\n\t" \
  "v_rsq_f32 v13, v13 div:2\n\t" \
  "v_mfma_f32_16x16x32_bf16 v[156:159], v[164:167], v[116:119], v[156:159]\n\t" \
  "s_mov_b64 vcc, s[40:41]\n\t" \
  "v_cvt_pk_bf16_f32 v15, v13, v13\n\t" \
  "v_and_b32 v13, 0xffff0000, v15\n\t" \
  "v_mul_f32 v14, -v12, v13 div:2\n\t" \
  "v_cndmask_b32_sdwa v15, v15, v13, vcc dst_sel:DWORD dst_unused:UNUSED_PAD src0_sel:WORD_1 src1_sel:DWORD\n\t" \
  "v_and_b32 v12, 0xffff0000, v14\n\t" \
  "v_mfma_f32_16x16x32_bf16 v[148:151], v[232:235], v[128:131], v[148:151]\n\t" \
  "s_or_b64 vcc, s[52:53], s[42:43]\n\t" \
  "v_sub_f32 v14, v14, v12\n\t" \
  "v_cndmask_b32 v180, v15, v240, vcc\n\t" \
  "v_perm_b32 v14, v12, v14, s55\n\t" \
  "v_cndmask_b32_dpp v184, v15, v240, vcc row_ror:8 row_mask:0xf bank_mask:0xf\n\t" \
  "v_mfma_f32_16x16x32_bf16 v[156:159], v[232:235], v[132:135], v[156:159]\n\t" \
  "s_mov_b64 vcc, s[52:53]\n\t" \
  "v_cndmask_b32 v182, v14, v240, vcc\n\t" \
  "v_cndmask_b32_dpp v186, v14, v240, vcc row_ror:8 row_mask:0xf bank_mask:0xf\n\t" \
  "s_orn2_b64 vcc, s[52:53], s[42:43]\n\t" \
  "v_cndmask_b32 v181, v15, v240, vcc\n\t" \
  "v_mfma_f32_16x16x16_bf16 v[0:3], v[196:197], v[182:183], 0\n\t" \
  "v_cndmask_b32_dpp v185, v15, v240, vcc row_ror:8 row_mask:0xf bank_mask:0xf\n\t" \
  "s_mov_b64 vcc, 0\n\t" \
  "v_fma_mix_f32 v227, |v153|, v227, v227 op_sel:[0,1,0] op_sel_hi:[0,1,1] clamp\n\t" \
  "v_mfma_f32_16x16x16_bf16 v[4:7], v[196:197], v[186:187], 0\n\t" \
  "v_fma_mix_f32 v226, |v152|, v226, v226 op_sel:[0,1,0] op_sel_hi:[0,1,1] clamp\n\t" \
  "v_fma_f32 v145, |v145|, v223, v145 mul:4\n\t" \
  "v_mfma_f32_16x16x16_bf16 v[188:191], v[24:25], v[180:181], v[0:3]\n\t" \
  "v_fma_f32 v144, |v144|, v222, v144 mul:4\n\t" \
  "v_fma_f32 v153, |v153|, v227, v153 mul:4\n\t" \
  "v_mfma_f32_16x16x16_bf16 v[176:179], v[26:27], v[180:181], v[0:3]\n\t" \
  "v_fma_f32 v152, |v152|, v226, v152 mul:4\n\t" \
  "v_cvt_pk_bf16_f32 v147, v146, v147\n\t" \
  "v_mfma_f32_16x16x16_bf16 v[200:203], v[36:37], v[184:185], v[4:7]\n\t" \
  "s_nop 1\n\t" \
  "v_cvt_pk_bf16_f32 v104, v188, v189\n\t" \
  "v_cvt_pk_bf16_f32 v105, v190, v191\n\t" \
  "v_mfma_f32_16x16x16_bf16 v[204:207], v[38:39], v[184:185], v[4:7]\n\t" \
  "buffer_store_dwordx4 v[64:67], v250, s[20:23], s14 offen offset:128 sc1\n\t" \
  "v_cvt_pk_bf16_f32 v106, v176, v177\n\t" \
  "v_cvt_pk_bf16_f32 v107, v178, v179\n\t" \
  "v_mfma_f32_16x16x16_bf16 v[196:199], v[28:29], v[180:181], v[0:3]\n\t" \
  "v_cvt_pk_bf16_f32 v108, v200, v201\n\t" \
  "v_cvt_pk_bf16_f32 v109, v202, v203\n\t" \
  "v_mfma_f32_16x16x16_bf16 v[188:191], v[30:31], v[180:181], v[0:3]\n\t" \
  "s_nop 0\n\t" \
  "v_cvt_pk_bf16_f32 v110, v204, v205\n\t" \
  "v_cvt_pk_bf16_f32 v111, v206, v207\n\t" \
  "v_mfma_f32_16x16x32_bf16 v[160:163], v[208:211], v[104:107], 0\n\t" \
  "s_nop 0\n\t" \
  "v_cvt_pk_bf16_f32 v120, v196, v197\n\t" \
  "v_cvt_pk_bf16_f32 v121, v198, v199\n\t" \
  "v_mfma_f32_16x16x16_bf16 v[176:179], v[40:41], v[184:185], v[4:7]\n\t" \
  "v_cvt_pk_bf16_f32 v122, v188, v189\n\t" \
  "v_cvt_pk_bf16_f32 v123, v190, v191\n\t" \
  "v_mfma_f32_16x16x16_bf16 v[200:203], v[42:43], v[184:185], v[4:7]\n\t" \
  "v_cvt_pk_bf16_f32 v146, v144, v145\n\t" \
  "v_cvt_pk_bf16_f32 v155, v154, v155\n\t" \
  "v_mfma_f32_16x16x32_bf16 v[168:171], v[208:211], v[108:111], 0\n\t" \
  "ds_read_b64_tr_b16 v[208:209], v248 offset:43560\n\t" \
  "s_nop 0\n\t" \
  "v_cvt_pk_bf16_f32 v124, v176, v177\n\t" \
  "v_cvt_pk_bf16_f32 v125, v178, v179\n\t" \
  "v_mfma_f32_16x16x16_bf16 v[204:207], v[32:33], v[180:181], v[0:3]\n\t" \
  "ds_read_b64_tr_b16 v[210:211], v248 offset:44584\n\t" \
  "v_cvt_pk_bf16_f32 v126, v200, v201\n\t" \
  "v_cvt_pk_bf16_f32 v127, v202, v203\n\t" \
  "v_mfma_f32_16x16x16_bf16 v[196:199], v[34:35], v[180:181], v[0:3]\n\t" \
  "v_cvt_pk_bf16_f32 v154, v152, v153\n\t" \
  "v_cvt_pk_f16_f32 v228, |v148|, |v149| clamp\n\t" \
  "v_mfma_f32_16x16x32_bf16 v[160:163], v[212:215], v[120:123], v[160:163]\n\t" \
  "s_nop 0\n\t" \
  "v_cvt_pk_bf16_f32 v136, v204, v205\n\t" \
  "v_cvt_pk_bf16_f32 v137, v206, v207\n\t" \
  "v_mfma_f32_16x16x16_bf16 v[188:191], v[44:45], v[184:185], v[4:7]\n\t" \
  "s_nop 0\n\t" \
  "v_cvt_pk_bf16_f32 v138, v196, v197\n\t" \
  "v_cvt_pk_bf16_f32 v139, v198, v199\n\t" \
  "v_mfma_f32_16x16x16_bf16 v[176:179], v[46:47], v[184:185], v[4:7]\n\t" \
  "v_cvt_pknorm_i16_f32 v230, |v150|, |v151|\n\t" \
  "v_cvt_pk_f16_f32 v232, |v156|, |v157| clamp\n\t" \
  "v_mfma_f32_16x16x32_bf16 v[168:171], v[212:215], v[124:127], v[168:171]\n\t" \
  "ds_read_b64_tr_b16 v[212:213], v248 offset:46632\n\t" \
  "v_cvt_pk_bf16_f32 v140, v188, v189\n\t" \
  "v_cvt_pk_bf16_f32 v141, v190, v191\n\t" \
  "v_mfma_f32_16x16x32_bf16 v[160:163], v[216:219], v[136:139], v[160:163]\n\t" \
  "ds_read_b64_tr_b16 v[214:215], v248 offset:47656\n\t" \
  "v_cvt_pk_bf16_f32 v142, v176, v177\n\t" \
  "v_cvt_pk_bf16_f32 v143, v178, v179\n\t" \
  "s_nop 1\n\t" \
  "v_mfma_f32_16x16x32_bf16 v[168:171], v[216:219], v[140:143], v[168:171]\n\t" \
  "ds_read_b64_tr_b16 v[216:217], v248 offset:45608\n\t" \
  "v_cvt_pknorm_i16_f32 v234, |v158|, |v159|\n\t" \
  "v_and_b32_sdwa v229, s56, v228 dst_sel:DWORD dst_unused:UNUSED_PAD src0_sel:DWORD src1_sel:WORD_1\n\t" \
  "s_waitcnt lgkmcnt(3)\n\t" \
  "ds_read_b64_tr_b16 v[218:219], v248 offset:48680\n\t" \
  "s_waitcnt vmcnt(4)\n\t" \
  "s_mov_b64 exec, 3\n\t" \
  "ds_add_rtn_u32 v246, v251, v250 offset:47136\n\t" \
  "s_mov_b64 exec, -1\n\t" \
  "v_mfma_f32_16x16x32_bf16 v[164:167], v[208:211], v[104:107], 0\n\t" \
  "v_and_b32 v228, s56, v228\n\t" \
  "v_lshlrev_b32_sdwa v231, 2, v230 dst_sel:DWORD dst_unused:UNUSED_PAD src0_sel:DWORD src1_sel:BYTE_3\n\t" \
  "v_mfma_f32_16x16x32_bf16 v[172:175], v[208:211], v[108:111], 0\n\t" \
  "ds_read_b64_tr_b16 v[208:209], v248 offset:49192\n\t" \
  "v_lshlrev_b32_sdwa v230, 2, v230 dst_sel:DWORD dst_unused:UNUSED_PAD src0_sel:DWORD src1_sel:BYTE_1\n\t" \
  "v_and_b32_sdwa v233, s56, v232 dst_sel:DWORD dst_unused:UNUSED_PAD src0_sel:DWORD src1_sel:WORD_1\n\t" \
  "s_waitcnt lgkmcnt(4)\n\t" \
  "v_mfma_f32_16x16x32_bf16 v[164:167], v[212:215], v[120:123], v[164:167]\n\t" \
  "ds_read_b64_tr_b16 v[210:211], v248 offset:50216\n\t" \
  "v_and_b32 v232, s56, v232\n\t" \
  "v_lshlrev_b32_sdwa v235, 2, v234 dst_sel:DWORD dst_unused:UNUSED_PAD src0_sel:DWORD src1_sel:BYTE_3\n\t" \
  "v_mfma_f32_16x16x32_bf16 v[172:175], v[212:215], v[124:127], v[172:175]\n\t" \
  "ds_read_b64_tr_b16 v[212:213], v248 offset:52264\n\t" \
  "ds_read_b64_tr_b16 v[214:215], v248 offset:53288\n\t" \
  "v_lshlrev_b32_sdwa v234, 2, v234 dst_sel:DWORD dst_unused:UNUSED_PAD src0_sel:DWORD src1_sel:BYTE_1\n\t" \
  "s_waitcnt lgkmcnt(5)\n\t" \
  "v_mfma_f32_16x16x32_bf16 v[164:167], v[216:219], v[136:139], v[164:167]\n\t" \
  "ds_read_b32 v229, v229 offset:0\n\t" \
  "ds_read_b32 v228, v228 offset:0\n\t" \
  "v_mfma_f32_16x16x32_bf16 v[172:175], v[216:219], v[140:143], v[172:175]\n\t" \
  "ds_read_b64_tr_b16 v[216:217], v248 offset:51240\n\t" \
  "ds_read_b64_tr_b16 v[218:219], v248 offset:54312\n\t" \
  "s_setprio 3\n\t" \
  "v_cvt_pk_f16_f32 v220, |v162|, |v163| clamp\n\t" \
  "v_cvt_pknorm_i16_f32 v222, |v160|, |v161|\n\t" \
  "s_waitcnt lgkmcnt(6)\n\t" \
  "v_mfma_f32_16x16x32_bf16 v[176:179], v[208:211], v[96:99], 0\n\t" \
  "buffer_store_dwordx4 v[72:75], v250, s[20:23], s15 offen offset:0 sc1\n\t" \
  "ds_read_b32 v231, v231 offset:15376\n\t" \
  "ds_read_b32 v230, v230 offset:15376\n\t" \
  "v_cvt_pk_f16_f32 v224, |v170|, |v171| clamp\n\t" \
  "v_cvt_pk_f16_f32 v226, |v168|, |v169| clamp\n\t" \
  "v_and_b32_sdwa v221, s56, v220 dst_sel:DWORD dst_unused:UNUSED_PAD src0_sel:DWORD src1_sel:WORD_1\n\t" \
  "v_mfma_f32_16x16x32_bf16 v[184:187], v[208:211], v[100:103], 0\n\t" \
  "ds_read_b32 v233, v233 offset:0\n\t" \
  "v_and_b32 v220, s56, v220\n\t" \
  "v_lshlrev_b32_sdwa v223, 2, v222 dst_sel:DWORD dst_unused:UNUSED_PAD src0_sel:DWORD src1_sel:BYTE_3\n\t" \
  "v_lshlrev_b32_sdwa v222, 2, v222 dst_sel:DWORD dst_unused:UNUSED_PAD src0_sel:DWORD src1_sel:BYTE_1\n\t" \
  "v_mfma_f32_16x16x32_bf16 v[192:195], v[208:211], v[104:107], 0\n\t" \
  "ds_read_b32 v232, v232 offset:0\n\t" \
  "ds_read_b32 v235, v235 offset:15376\n\t" \
  "v_and_b32_sdwa v225, s56, v224 dst_sel:DWORD dst_unused:UNUSED_PAD src0_sel:DWORD src1_sel:WORD_1\n\t" \
  "v_and_b32 v224, s56, v224\n\t" \
  "v_and_b32_sdwa v227, s56, v226 dst_sel:DWORD dst_unused:UNUSED_PAD src0_sel:DWORD src1_sel:WORD_1\n\t" \
  "v_mfma_f32_16x16x32_bf16 v[200:203], v[208:211], v[108:111], 0\n\t" \
  "ds_read_b64_tr_b16 v[208:209], v248 offset:49704\n\t" \
  "ds_read_b64_tr_b16 v[210:211], v248 offset:50728\n\t" \
  "v_and_b32 v226, s56, v226\n\t" \
  "ds_read_b32 v234, v234 offset:15376\n\t" \
  "ds_read_b32 v221, v221 offset:0\n\t" \
  "ds_read_b32 v220, v220 offset:0\n\t" \
  "ds_read_b32 v223, v223 offset:15376\n\t" \
  "ds_read_b32 v222, v222 offset:15376\n\t" \
  "ds_read_b32 v225, v225 offset:0\n\t" \
  "ds_read_b32 v224, v224 offset:0\n\t" \
  "ds_read_b32 v227, v227 offset:0\n\t" \
  "s_waitcnt lgkmcnt(13)\n\t" \
  "v_fma_f32 v149, |v149|, v229, v149 mul:4\n\t" \
  "v_fma_f32 v148, |v148|, v228, v148 mul:4\n\t" \
  "v_fma_mix_f32 v231, |v151|, v231, v231 op_sel:[0,1,0] op_sel_hi:[0,1,1] clamp\n\t" \
  "v_mfma_f32_16x16x32_bf16 v[176:179], v[212:215], v[112:115], v[176:179]\n\t" \
  "buffer_store_dwordx4 v[80:83], v250, s[20:23], s15 offen offset:64 sc1\n\t" \
  "ds_read_b32 v226, v226 offset:0\n\t" \
  "v_fma_mix_f32 v230, |v150|, v230, v230 op_sel:[0,1,0] op_sel_hi:[0,1,1] clamp\n\t" \
  "s_waitcnt lgkmcnt(8)\n\t" \
  "s_setprio 2\n\t" \
  "v_fma_f32 v157, |v157|, v233, v157 mul:4\n\t" \
  "v_fma_f32 v156, |v156|, v232, v156 mul:4\n\t" \
  "v_mfma_f32_16x16x32_bf16 v[184:187], v[212:215], v[116:119], v[184:187]\n\t" \
  "v_fma_mix_f32 v235, |v159|, v235, v235 op_sel:[0,1,0] op_sel_hi:[0,1,1] clamp\n\t" \
  "v_fma_mix_f32 v234, |v158|, v234, v234 op_sel:[0,1,0] op_sel_hi:[0,1,1] clamp\n\t" \
  "v_fma_f32 v151, |v151|, v231, v151 mul:4\n\t" \
  "v_mfma_f32_16x16x32_bf16 v[192:195], v[212:215], v[120:123], v[192:195]\n\t" \
  "v_fma_f32 v150, |v150|, v230, v150 mul:4\n\t" \
  "v_fma_f32 v159, |v159|, v235, v159 mul:4\n\t" \
  "v_fma_f32 v158, |v158|, v234, v158 mul:4\n\t" \
  "v_mfma_f32_16x16x32_bf16 v[200:203], v[212:215], v[124:127], v[200:203]\n\t" \
  "ds_read_b64_tr_b16 v[212:213], v248 offset:52776\n\t" \
  "v_cvt_pk_bf16_f32 v148, v148, v149\n\t" \
  "v_cvt_pk_bf16_f32 v149, v150, v151\n\t" \
  "v_cvt_pk_bf16_f32 v156, v156, v157\n\t" \
  "v_cvt_pk_bf16_f32 v157, v158, v159\n\t" \
  "v_mfma_f32_16x16x32_bf16 v[176:179], v[216:219], v[128:131], v[176:179]\n\t" \
  "ds_read_b64_tr_b16 v[214:215], v248 offset:53800\n\t" \
  "v_cvt_pk_f16_f32 v228, |v164|, |v165| clamp\n\t" \
  "v_cvt_pknorm_i16_f32 v230, |v166|, |v167|\n\t" \
  "v_cvt_pk_f16_f32 v232, |v172|, |v173| clamp\n\t" \
  "v_mfma_f32_16x16x32_bf16 v[184:187], v[216:219], v[132:135], v[184:187]\n\t" \
  "buffer_store_dwordx4 v[88:91], v250, s[20:23], s15 offen offset:128 sc1\n\t" \
  "v_cvt_pk_f16_f32 v234, |v174|, |v175| clamp\n\t" \
  "v_and_b32_sdwa v229, s56, v228 dst_sel:DWORD dst_unused:UNUSED_PAD src0_sel:DWORD src1_sel:WORD_1\n\t" \
  "v_and_b32 v228, s56, v228\n\t" \
  "v_mfma_f32_16x16x32_bf16 v[192:195], v[216:219], v[136:139], v[192:195]\n\t" \
  "ds_read_b32 v229, v229 offset:0\n\t" \
  "v_lshlrev_b32_sdwa v231, 2, v230 dst_sel:DWORD dst_unused:UNUSED_PAD src0_sel:DWORD src1_sel:BYTE_3\n\t" \
  "v_lshlrev_b32_sdwa v230, 2, v230 dst_sel:DWORD dst_unused:UNUSED_PAD src0_sel:DWORD src1_sel:BYTE_1\n\t" \
  "v_and_b32_sdwa v233, s56, v232 dst_sel:DWORD dst_unused:UNUSED_PAD src0_sel:DWORD src1_sel:WORD_1\n\t" \
  "v_mfma_f32_16x16x32_bf16 v[200:203], v[216:219], v[140:143], v[200:203]\n\t" \
  "ds_read_b64_tr_b16 v[216:217], v248 offset:51752\n\t" \
  "v_and_b32 v232, s56, v232\n\t" \
  "v_and_b32_sdwa v235, s56, v234 dst_sel:DWORD dst_unused:UNUSED_PAD src0_sel:DWORD src1_sel:WORD_1\n\t" \
  "v_and_b32 v234, s56, v234\n\t" \
  "s_waitcnt lgkmcnt(8)\n\t" \
  "v_fma_f32 v163, |v163|, v221, v163 mul:4\n\t" \
  "v_mfma_f32_16x16x32_bf16 v[180:183], v[208:211], v[96:99], 0\n\t" \
  "ds_read_b64_tr_b16 v[218:219], v248 offset:54824\n\t" \
  "ds_read_b32 v228, v228 offset:0\n\t" \
  "v_fma_f32 v162, |v162|, v220, v162 mul:4\n\t" \
  "s_setprio 1\n\t" \
  "v_fma_mix_f32 v223, |v161|, v223, v223 op_sel:[0,1,0] op_sel_hi:[0,1,1] clamp\n\t" \
  "v_fma_mix_f32 v222, |v160|, v222, v222 op_sel:[0,1,0] op_sel_hi:[0,1,1] clamp\n\t" \
  "v_mfma_f32_16x16x32_bf16 v[188:191], v[208:211], v[100:103], 0\n\t" \
  "s_waitcnt vmcnt(6)\n\t" \
  "s_mov_b64 exec, 3\n\t" \
  "ds_add_rtn_u32 v246, v251, v250 offset:47144\n\t" \
  "s_mov_b64 exec, -1\n\t" \
  "ds_read_b32 v231, v231 offset:15376\n\t" \
  "ds_read_b32 v230, v230 offset:15376\n\t" \
  "s_waitcnt lgkmcnt(9)\n\t" \
  "v_fma_f32 v171, |v171|, v225, v171 mul:4\n\t" \
  "v_fma_f32 v170, |v170|, v224, v170 mul:4\n\t" \
  "v_fma_f32 v169, |v169|, v227, v169 mul:4\n\t" \
  "v_mfma_f32_16x16x32_bf16 v[196:199], v[208:211], v[104:107], 0\n\t" \
  "ds_read_b32 v233, v233 offset:0\n\t" \
  "v_fma_f32 v168, |v168|, v226, v168 mul:4\n\t" \
  "v_fma_f32 v161, |v161|, v223, v161 mul:4\n\t" \
  "v_fma_f32 v160, |v160|, v222, v160 mul:4\n\t" \
  "v_mfma_f32_16x16x32_bf16 v[204:207], v[208:211], v[108:111], 0\n\t" \
  "s_waitcnt lgkmcnt(3)\n\t" \
  "v_readfirstlane_b32 s4, v246\n\t" \
  "s_cmp_lt_u32 s4, s57\n\t" \
  "s_cbranch_scc1 .Lcnx_w12\n.Lcnx_b12:\n\t" \
  "ds_read_b64_tr_b16 v[208:209], v249 offset:24600\n\t" \
  "v_cvt_pk_bf16_f32 v163, v162, v163\n\t" \
  "v_cvt_pk_bf16_f32 v162, v160, v161\n\t" \
  "v_cvt_pk_bf16_f32 v171, v170, v171\n\t" \
  "v_cvt_pk_bf16_f32 v170, v168, v169\n\t" \
  "v_mfma_f32_16x16x32_bf16 v[180:183], v[212:215], v[112:115], v[180:183]\n\t" \
  "ds_read_b64_tr_b16 v[210:211], v249 offset:26136\n\t" \
  "v_cvt_pk_f16_f32 v220, |v178|, |v179| clamp\n\t" \
  "v_cvt_pknorm_i16_f32 v222, |v176|, |v177|\n\t" \
  "v_cvt_pk_f16_f32 v224, |v186|, |v187| clamp\n\t" \
  "v_mfma_f32_16x16x32_bf16 v[188:191], v[212:215], v[116:119], v[188:191]\n\t" \
  "s_cmp_eq_u32 s37, 0\n\t" \
  "s_cbranch_scc1 .Lcnx_f2\n.Lcnx_g2:\n\t" \
  "ds_read_b32 v232, v232 offset:0\n\t" \
  "v_cvt_pknorm_i16_f32 v226, |v184|, |v185|\n\t" \
  "v_and_b32_sdwa v221, s56, v220 dst_sel:DWORD dst_unused:UNUSED_PAD src0_sel:DWORD src1_sel:WORD_1\n\t" \
  "v_and_b32 v220, s56, v220\n\t" \
  "s_setprio 0\n\t" \
  "v_mfma_f32_16x16x32_bf16 v[196:199], v[212:215], v[120:123], v[196:199]\n\t" \
  "ds_read_b32 v235, v235 offset:0\n\t" \
  "v_lshlrev_b32_sdwa v223, 2, v222 dst_sel:DWORD dst_unused:UNUSED_PAD src0_sel:DWORD src1_sel:BYTE_3\n\t" \
  "v_lshlrev_b32_sdwa v222, 2, v222 dst_sel:DWORD dst_unused:UNUSED_PAD src0_sel:DWORD src1_sel:BYTE_1\n\t" \
  "v_and_b32_sdwa v225, s56, v224 dst_sel:DWORD dst_unused:UNUSED_PAD src0_sel:DWORD src1_sel:WORD_1\n\t" \
  "v_mfma_f32_16x16x32_bf16 v[204:207], v[212:215], v[124:127], v[204:207]\n\t" \
  "ds_read_b64_tr_b16 v[212:213], v249 offset:24608\n\t" \
  "ds_read_b64_tr_b16 v[214:215], v249 offset:26144\n\t" \
  "v_and_b32 v224, s56, v224\n\t" \
  "v_lshlrev_b32_sdwa v227, 2, v226 dst_sel:DWORD dst_unused:UNUSED_PAD src0_sel:DWORD src1_sel:BYTE_3\n\t" \
  "v_lshlrev_b32_sdwa v226, 2, v226 dst_sel:DWORD dst_unused:UNUSED_PAD src0_sel:DWORD src1_sel:BYTE_1\n\t" \
  "ds_read_b32 v234, v234 offset:0\n\t" \
  "ds_read_b32 v221, v221 offset:0\n\t" \
  "ds_read_b32 v220, v220 offset:0\n\t" \
  "ds_read_b32 v223, v223 offset:15376\n\t" \
  "ds_read_b32 v222, v222 offset:15376\n\t" \
  "ds_read_b32 v225, v225 offset:0\n\t" \
  "ds_read_b32 v224, v224 offset:0\n\t" \
  "ds_read_b32 v227, v227 offset:15376\n\t" \
  "s_waitcnt lgkmcnt(15)\n\t" \
  "v_fma_f32 v165, |v165|, v229, v165 mul:4\n\t" \
  "v_mfma_f32_16x16x32_bf16 v[180:183], v[216:219], v[128:131], v[180:183]\n\t" \
  "ds_read_b32 v226, v226 offset:15376\n\t" \
  "v_fma_f32 v164, |v164|, v228, v164 mul:4\n\t" \
  "v_fma_mix_f32 v231, |v167|, v231, v231 op_sel:[0,1,0] op_sel_hi:[0,1,1] clamp\n\t" \
  "v_fma_mix_f32 v230, |v166|, v230, v230 op_sel:[0,1,0] op_sel_hi:[0,1,1] clamp\n\t" \
  "v_mfma_f32_16x16x32_bf16 v[188:191], v[216:219], v[132:135], v[188:191]\n\t" \
  "s_waitcnt lgkmcnt(8)\n\t" \
  "v_fma_f32 v173, |v173|, v233, v173 mul:4\n\t" \
  "v_fma_f32 v172, |v172|, v232, v172 mul:4\n\t" \
  "v_fma_f32 v175, |v175|, v235, v175 mul:4\n\t" \
  "v_mfma_f32_16x16x32_bf16 v[196:199], v[216:219], v[136:139], v[196:199]\n\t" \
  "v_fma_f32 v174, |v174|, v234, v174 mul:4\n\t" \
  "v_fma_f32 v167, |v167|, v231, v167 mul:4\n\t" \
  "v_fma_f32 v166, |v166|, v230, v166 mul:4\n\t" \
  "v_mfma_f32_16x16x32_bf16 v[204:207], v[216:219], v[140:143], v[204:207]\n\t" \
  "ds_read_b64_tr_b16 v[216:217], v249 offset:24664\n\t" \
  "v_cvt_pk_bf16_f32 v164, v164, v165\n\t" \
  "v_cvt_pk_bf16_f32 v165, v166, v167\n\t" \
  "v_cvt_pk_bf16_f32 v172, v172, v173\n\t" \
  "v_cvt_pk_bf16_f32 v173, v174, v175\n\t" \
  "s_setprio 0\n\t" \
  "v_cvt_pk_f16_f32 v228, |v194|, |v195| clamp\n\t" \
  "v_cvt_pknorm_i16_f32 v230, |v192|, |v193|\n\t" \
  "v_mfma_f32_16x16x32_bf16 v[0:3], v[242:245], v[236:239], 0\n\t" \
  "ds_read_b64_tr_b16 v[218:219], v249 offset:26200\n\t" \
  "v_cvt_pk_f16_f32 v232, |v202|, |v203| clamp\n\t" \
  "v_cvt_pk_f16_f32 v234, |v200|, |v201| clamp\n\t" \
  "v_mfma_f32_16x16x32_bf16 v[4:7], v[240:243], v[236:239], 0\n\t" \
  "buffer_load_dwordx4 v[236:239], v250, s[8:11], s28 offen offset:64 nt\n\t" \
  "s_cmp_lg_u32 s58, 0\n\t" \
  "s_cbranch_scc1 .Lcnx_c21\n\t" \
  "s_sub_u32 s24, s24, s64\n\t" \
  "s_add_u32 m0, s25, 0\n\t" \
  "s_add_u32 s24, s24, s26\n\t" \
  "buffer_load_dwordx4 v254, s[8:11], s24 offen nt lds\n\t" \
  ".Lcnx_c21:\n\t" \
  "v_and_b32_sdwa v229, s56, v228 dst_sel:DWORD dst_unused:UNUSED_PAD src0_sel:DWORD src1_sel:WORD_1\n\t" \
  "v_and_b32 v228, s56, v228\n\t" \
  "v_mfma_f32_16x16x32_bf16 v[0:3], v[208:211], v[146:149], v[0:3]\n\t" \
  "ds_read_b32 v229, v229 offset:0\n\t" \
  "v_lshlrev_b32_sdwa v231, 2, v230 dst_sel:DWORD dst_unused:UNUSED_PAD src0_sel:DWORD src1_sel:BYTE_3\n\t" \
  "v_lshlrev_b32_sdwa v230, 2, v230 dst_sel:DWORD dst_unused:UNUSED_PAD src0_sel:DWORD src1_sel:BYTE_1\n\t" \
  "v_mfma_f32_16x16x32_bf16 v[24:27], v[208:211], v[154:157], 0\n\t" \
  "ds_read_b32 v228, v228 offset:0\n\t" \
  "v_and_b32_sdwa v233, s56, v232 dst_sel:DWORD dst_unused:UNUSED_PAD src0_sel:DWORD src1_sel:WORD_1\n\t" \
  "v_and_b32 v232, s56, v232\n\t" \
  "v_mfma_f32_16x16x32_bf16 v[48:51], v[208:211], v[162:165], 0\n\t" \
  "ds_read_b32 v231, v231 offset:15376\n\t" \
  "v_and_b32_sdwa v235, s56, v234 dst_sel:DWORD dst_unused:UNUSED_PAD src0_sel:DWORD src1_sel:WORD_1\n\t" \
  "v_and_b32 v234, s56, v234\n\t" \
  "v_mfma_f32_16x16x32_bf16 v[72:75], v[208:211], v[170:173], 0\n\t" \
  "ds_read_b64_tr_b16 v[208:209], v249 offset:24672\n\t" \
  "ds_read_b64_tr_b16 v[210:211], v249 offset:26208\n\t" \
  "s_waitcnt lgkmcnt(11)\n\t" \
  "v_fma_f32 v179, |v179|, v221, v179 mul:4\n\t" \
  "v_fma_f32 v178, |v178|, v220, v178 mul:4\n\t" \
  "v_mfma_f32_16x16x32_bf16 v[4:7], v[212:215], v[146:149], v[4:7]\n\t" \
  "ds_read_b32 v230, v230 offset:15376\n\t" \
  "v_fma_mix_f32 v223, |v177|, v223, v223 op_sel:[0,1,0] op_sel_hi:[0,1,1] clamp\n\t" \
  "v_fma_mix_f32 v222, |v176|, v222, v222 op_sel:[0,1,0] op_sel_hi:[0,1,1] clamp\n\t" \
  "v_mfma_f32_16x16x32_bf16 v[28:31], v[212:215], v[154:157], 0\n\t" \
  "ds_read_b32 v233, v233 offset:0\n\t" \
  "s_waitcnt lgkmcnt(9)\n\t" \
  "v_fma_f32 v187, |v187|, v225, v187 mul:4\n\t" \
  "v_fma_f32 v186, |v186|, v224, v186 mul:4\n\t" \
  "v_mfma_f32_16x16x32_bf16 v[52:55], v[212:215], v[162:165], 0\n\t" \
  "ds_read_b32 v232, v232 offset:0\n\t" \
  "v_fma_mix_f32 v227, |v185|, v227, v227 op_sel:[0,1,0] op_sel_hi:[0,1,1] clamp\n\t" \
  "v_fma_mix_f32 v226, |v184|, v226, v226 op_sel:[0,1,0] op_sel_hi:[0,1,1] clamp\n\t" \
  "v_fma_f32 v177, |v177|, v223, v177 mul:4\n\t" \
  "v_mfma_f32_16x16x32_bf16 v[76:79], v[212:215], v[170:173], 0\n\t" \
  "ds_read_b64_tr_b16 v[212:213], v249 offset:24728\n\t" \
  "v_fma_f32 v176, |v176|, v222, v176 mul:4\n\t" \
  "v_fma_f32 v185, |v185|, v227, v185 mul:4\n\t" \
  "s_waitcnt lgkmcnt(9)\n\t" \
  "v_mfma_f32_16x16x32_bf16 v[8:11], v[216:219], v[146:149], 0\n\t" \
  "ds_read_b64_tr_b16 v[214:215], v249 offset:26264\n\t" \
  "v_fma_f32 v184, |v184|, v226, v184 mul:4\n\t" \
  "v_cvt_pk_bf16_f32 v179, v178, v179\n\t" \
  "v_mfma_f32_16x16x32_bf16 v[32:35], v[216:219], v[154:157], 0\n\t" \
  "ds_read_b32 v235, v235 offset:0\n\t" \
  "v_cvt_pk_bf16_f32 v178, v176, v177\n\t" \
  "v_cvt_pk_bf16_f32 v187, v186, v187\n\t" \
  "v_mfma_f32_16x16x32_bf16 v[56:59], v[216:219], v[162:165], 0\n\t" \
  "ds_read_b32 v234, v234 offset:0\n\t" \
  "v_cvt_pk_bf16_f32 v186, v184, v185\n\t" \
  "v_cvt_pk_f16_f32 v220, |v180|, |v181| clamp\n\t" \
  "s_setprio 1\n\t" \
  "v_mfma_f32_16x16x32_bf16 v[80:83], v[216:219], v[170:173], 0\n\t" \
  "ds_read_b64_tr_b16 v[216:217], v249 offset:24736\n\t" \
  "v_cvt_pknorm_i16_f32 v222, |v182|, |v183|\n\t" \
  "v_cvt_pk_f16_f32 v224, |v188|, |v189| clamp\n\t" \
  "s_waitcnt lgkmcnt(8)\n\t" \
  "v_mfma_f32_16x16x32_bf16 v[12:15], v[208:211], v[146:149], 0\n\t" \
  "ds_read_b64_tr_b16 v[218:219], v249 offset:26272\n\t" \
  "v_cvt_pknorm_i16_f32 v226, |v190|, |v191|\n\t" \
  "v_and_b32_sdwa v221, s56, v220 dst_sel:DWORD dst_unused:UNUSED_PAD src0_sel:DWORD src1_sel:WORD_1\n\t" \
  "v_mfma_f32_16x16x32_bf16 v[36:39], v[208:211], v[154:157], 0\n\t" \
  "v_and_b32 v220, s56, v220\n\t" \
  "v_lshlrev_b32_sdwa v223, 2, v222 dst_sel:DWORD dst_unused:UNUSED_PAD src0_sel:DWORD src1_sel:BYTE_3\n\t" \
  "v_mfma_f32_16x16x32_bf16 v[60:63], v[208:211], v[162:165], 0\n\t" \
  "ds_read_b32 v221, v221 offset:0\n\t" \
  "v_lshlrev_b32_sdwa v222, 2, v222 dst_sel:DWORD dst_unused:UNUSED_PAD src0_sel:DWORD src1_sel:BYTE_1\n\t" \
  "v_and_b32_sdwa v225, s56, v224 dst_sel:DWORD dst_unused:UNUSED_PAD src0_sel:DWORD src1_sel:WORD_1\n\t" \
  "v_and_b32 v224, s56, v224\n\t" \
  "v_mfma_f32_16x16x32_bf16 v[84:87], v[208:211], v[170:173], 0\n\t" \
  "ds_read_b64_tr_b16 v[208:209], v248 offset:36904\n\t" \
  "ds_read_b64_tr_b16 v[210:211], v248 offset:37928\n\t" \
  "v_lshlrev_b32_sdwa v227, 2, v226 dst_sel:DWORD dst_unused:UNUSED_PAD src0_sel:DWORD src1_sel:BYTE_3\n\t" \
  "v_lshlrev_b32_sdwa v226, 2, v226 dst_sel:DWORD dst_unused:UNUSED_PAD src0_sel:DWORD src1_sel:BYTE_1\n\t" \
  "s_waitcnt lgkmcnt(7)\n\t" \
  "v_mfma_f32_16x16x32_bf16 v[16:19], v[212:215], v[146:149], 0\n\t" \
  "ds_read_b32 v220, v220 offset:0\n\t" \
  "ds_read_b32 v223, v223 offset:15376\n\t" \
  "v_fma_f32 v195, |v195|, v229, v195 mul:4\n\t" \
  "v_fma_f32 v194, |v194|, v228, v194 mul:4\n\t" \
  "v_mfma_f32_16x16x32_bf16 v[40:43], v[212:215], v[154:157], 0\n\t" \
  "ds_read_b32 v222, v222 offset:15376\n\t" \
  "v_fma_mix_f32 v231, |v193|, v231, v231 op_sel:[0,1,0] op_sel_hi:[0,1,1] clamp\n\t" \
  "v_fma_mix_f32 v230, |v192|, v230, v230 op_sel:[0,1,0] op_sel_hi:[0,1,1] clamp\n\t" \
  "v_mfma_f32_16x16x32_bf16 v[64:67], v[212:215], v[162:165], 0\n\t" \
  "ds_read_b32 v225, v225 offset:0\n\t" \
  "s_waitcnt lgkmcnt(9)\n\t" \
  "v_fma_f32 v203, |v203|, v233, v203 mul:4\n\t" \
  "v_fma_f32 v202, |v202|, v232, v202 mul:4\n\t" \
  "v_mfma_f32_16x16x32_bf16 v[88:91], v[212:215], v[170:173], 0\n\t" \
  "ds_read_b64_tr_b16 v[212:213], v248 offset:39976\n\t" \
  "v_fma_f32 v201, |v201|, v235, v201 mul:4\n\t" \
  "v_fma_f32 v200, |v200|, v234, v200 mul:4\n\t" \
  "s_waitcnt lgkmcnt(8)\n\t" \
  "v_mfma_f32_16x16x32_bf16 v[20:23], v[216:219], v[146:149], 0\n\t" \
  "ds_read_b64_tr_b16 v[214:215], v248 offset:41000\n\t" \
  "v_fma_f32 v193, |v193|, v231, v193 mul:4\n\t" \
  "v_fma_f32 v192, |v192|, v230, v192 mul:4\n\t" \
  "v_mfma_f32_16x16x32_bf16 v[44:47], v[216:219], v[154:157], 0\n\t" \
  "ds_read_b32 v224, v224 offset:0\n\t" \
  "v_cvt_pk_bf16_f32 v195, v194, v195\n\t" \
  "v_cvt_pk_bf16_f32 v194, v192, v193\n\t" \
  "v_mfma_f32_16x16x32_bf16 v[68:71], v[216:219], v[162:165], 0\n\t" \
  "ds_read_b32 v227, v227 offset:15376\n\t" \
  "v_cvt_pk_bf16_f32 v203, v202, v203\n\t" \
  "v_cvt_pk_bf16_f32 v202, v200, v201\n\t" \
  "v_cvt_pk_f16_f32 v228, |v196|, |v197| clamp\n\t" \
  "v_mfma_f32_16x16x32_bf16 v[92:95], v[216:219], v[170:173], 0\n\t" \
  "ds_read_b64_tr_b16 v[216:217], v248 offset:38952\n\t" \
  "v_cvt_pknorm_i16_f32 v230, |v198|, |v199|\n\t" \
  "v_cvt_pk_f16_f32 v232, |v204|, |v205| clamp\n\t" \
  "s_setprio 2\n\t" \
  "s_waitcnt lgkmcnt(9)\n\t" \
  "v_mfma_f32_16x16x32_bf16 v[144:147], v[208:211], v[96:99], 0\n\t" \
  "ds_read_b64_tr_b16 v[218:219], v248 offset:42024\n\t" \
  "v_cvt_pk_f16_f32 v234, |v206|, |v207| clamp\n\t" \
  "v_and_b32_sdwa v229, s56, v228 dst_sel:DWORD dst_unused:UNUSED_PAD src0_sel:DWORD src1_sel:WORD_1\n\t" \
  "v_mfma_f32_16x16x32_bf16 v[152:155], v[208:211], v[100:103], 0\n\t" \
  "ds_read_b32 v226, v226 offset:15376\n\t" \
  "v_and_b32 v228, s56, v228\n\t" \
  "v_lshlrev_b32_sdwa v231, 2, v230 dst_sel:DWORD dst_unused:UNUSED_PAD src0_sel:DWORD src1_sel:BYTE_3\n\t" \
  "v_mfma_f32_16x16x32_bf16 v[160:163], v[208:211], v[104:107], 0\n\t" \
  "ds_read_b32 v229, v229 offset:0\n\t" \
  "v_lshlrev_b32_sdwa v230, 2, v230 dst_sel:DWORD dst_unused:UNUSED_PAD src0_sel:DWORD src1_sel:BYTE_1\n\t" \
  "v_and_b32_sdwa v233, s56, v232 dst_sel:DWORD dst_unused:UNUSED_PAD src0_sel:DWORD src1_sel:WORD_1\n\t" \
  "v_mfma_f32_16x16x32_bf16 v[168:171], v[208:211], v[108:111], 0\n\t" \
  "ds_read_b64_tr_b16 v[208:209], v248 offset:37416\n\t" \
  "v_and_b32 v232, s56, v232\n\t" \
  "v_and_b32_sdwa v235, s56, v234 dst_sel:DWORD dst_unused:UNUSED_PAD src0_sel:DWORD src1_sel:WORD_1\n\t" \
  "s_waitcnt lgkmcnt(7)\n\t" \
  "v_mfma_f32_16x16x32_bf16 v[144:147], v[212:215], v[112:115], v[144:147]\n\t" \
  "ds_read_b64_tr_b16 v[210:211], v248 offset:38440\n\t" \
  "ds_read_b32 v228, v228 offset:0\n\t" \
  "v_and_b32 v234, s56, v234\n\t" \
  "v_fma_f32 v181, |v181|, v221, v181 mul:4\n\t" \
  "v_mfma_f32_16x16x32_bf16 v[152:155], v[212:215], v[116:119], v[152:155]\n\t" \
  "ds_read_b32 v231, v231 offset:15376\n\t" \
  "ds_read_b32 v230, v230 offset:15376\n\t" \
  "v_fma_f32 v180, |v180|, v220, v180 mul:4\n\t" \
  "v_fma_mix_f32 v223, |v183|, v223, v223 op_sel:[0,1,0] op_sel_hi:[0,1,1] clamp\n\t" \
  "v_mfma_f32_16x16x32_bf16 v[160:163], v[212:215], v[120:123], v[160:163]\n\t" \
  "ds_read_b32 v233, v233 offset:0\n\t" \
  "v_fma_mix_f32 v222, |v182|, v222, v222 op_sel:[0,1,0] op_sel_hi:[0,1,1] clamp\n\t" \
  "s_waitcnt lgkmcnt(7)\n\t" \
  "v_fma_f32 v189, |v189|, v225, v189 mul:4\n\t" \
  "v_mfma_f32_16x16x32_bf16 v[168:171], v[212:215], v[124:127], v[168:171]\n\t" \
  "ds_read_b64_tr_b16 v[212:213], v248 offset:40488\n\t" \
  "v_fma_f32 v188, |v188|, v224, v188 mul:4\n\t" \
  "v_fma_mix_f32 v227, |v191|, v227, v227 op_sel:[0,1,0] op_sel_hi:[0,1,1] clamp\n\t" \
  "v_fma_mix_f32 v226, |v190|, v226, v226 op_sel:[0,1,0] op_sel_hi:[0,1,1] clamp\n\t" \
  "v_mfma_f32_16x16x32_bf16 v[144:147], v[216:219], v[128:131], v[144:147]\n\t" \
  "ds_read_b64_tr_b16 v[214:215], v248 offset:41512\n\t" \
  "v_fma_f32 v183, |v183|, v223, v183 mul:4\n\t" \
  "v_fma_f32 v182, |v182|, v222, v182 mul:4\n\t" \
  "v_mfma_f32_16x16x32_bf16 v[152:155], v[216:219], v[132:135], v[152:155]\n\t" \
  "ds_read_b32 v232, v232 offset:0\n\t" \
  "v_fma_f32 v191, |v191|, v227, v191 mul:4\n\t" \
  "v_fma_f32 v190, |v190|, v226, v190 mul:4\n\t" \
  "v_mfma_f32_16x16x32_bf16 v[160:163], v[216:219], v[136:139], v[160:163]\n\t" \
  "ds_read_b32 v235, v235 offset:0\n\t" \
  "v_cvt_pk_bf16_f32 v180, v180, v181\n\t" \
  "v_cvt_pk_bf16_f32 v181, v182, v183\n\t" \
  "v_mfma_f32_16x16x32_bf16 v[168:171], v[216:219], v[140:143], v[168:171]\n\t" \
  "ds_read_b64_tr_b16 v[216:217], v248 offset:39464\n\t" \
  "v_cvt_pk_bf16_f32 v188, v188, v189\n\t" \
  "v_cvt_pk_bf16_f32 v189, v190, v191\n\t" \
  "s_waitcnt lgkmcnt(9)\n\t" \
  "v_mfma_f32_16x16x32_bf16 v[148:151], v[208:211], v[96:99], 0\n\t" \
  "ds_read_b64_tr_b16 v[218:219], v248 offset:42536\n\t" \
  "v_cvt_pk_f16_f32 v220, |v146|, |v147| clamp\n\t" \
  "v_cvt_pknorm_i16_f32 v222, |v144|, |v145|\n\t" \
  "s_setprio 3\n\t" \
  "v_mfma_f32_16x16x32_bf16 v[156:159], v[208:211], v[100:103], 0\n\t" \
  "s_waitcnt vmcnt(1)\n\t" \
  "s_cmp_lg_u32 s58, 1\n\t" \
  "s_cbranch_scc1 .Lcnx_c9\n\t" \
  "s_waitcnt vmcnt(0)\n\t" \
  ".Lcnx_c9:\n\t" \
  "s_mov_b64 exec, 3\n\t" \
  "ds_add_rtn_u32 v246, v251, v250 offset:47152\n\t" \
  "s_mov_b64 exec, -1\n\t" \
  "ds_read_b32 v234, v234 offset:0\n\t" \
  "v_cvt_pk_f16_f32 v224, |v154|, |v155| clamp\n\t" \
  "v_cvt_pknorm_i16_f32 v226, |v152|, |v153|\n\t" \
  "v_mfma_f32_16x16x32_bf16 v[164:167], v[208:211], v[104:107], 0\n\t" \
  "v_and_b32_sdwa v221, s56, v220 dst_sel:DWORD dst_unused:UNUSED_PAD src0_sel:DWORD src1_sel:WORD_1\n\t" \
  "v_and_b32 v220, s56, v220\n\t" \
  "v_mfma_f32_16x16x32_bf16 v[172:175], v[208:211], v[108:111], 0\n\t" \
  "s_waitcnt lgkmcnt(1)\n\t" \
  "v_readfirstlane_b32 s4, v246\n\t" \
  "s_cmp_lt_u32 s4, s57\n\t" \
  "s_cbranch_scc1 .Lcnx_w24\n.Lcnx_b24:\n\t" \
  "ds_read_b64_tr_b16 v[208:209], v249 offset:30752\n\t" \
  "v_lshlrev_b32_sdwa v223, 2, v222 dst_sel:DWORD dst_unused:UNUSED_PAD src0_sel:DWORD src1_sel:BYTE_3\n\t" \
  "v_lshlrev_b32_sdwa v222, 2, v222 dst_sel:DWORD dst_unused:UNUSED_PAD src0_sel:DWORD src1_sel:BYTE_1\n\t" \
  "v_and_b32_sdwa v225, s56, v224 dst_sel:DWORD dst_unused:UNUSED_PAD src0_sel:DWORD src1_sel:WORD_1\n\t" \
  "v_mfma_f32_16x16x32_bf16 v[148:151], v[212:215], v[112:115], v[148:151]\n\t" \
  "ds_read_b64_tr_b16 v[210:211], v249 offset:32288\n\t" \
  "v_and_b32 v224, s56, v224\n\t" \
  "v_lshlrev_b32_sdwa v227, 2, v226 dst_sel:DWORD dst_unused:UNUSED_PAD src0_sel:DWORD src1_sel:BYTE_3\n\t" \
  "v_mfma_f32_16x16x32_bf16 v[156:159], v[212:215], v[116:119], v[156:159]\n\t" \
  "s_cmp_eq_u32 s37, 0\n\t" \
  "s_cbranch_scc1 .Lcnx_f3\n.Lcnx_g3:\n\t" \
  "ds_read_b32 v221, v221 offset:0\n\t" \
  "ds_read_b32 v220, v220 offset:0\n\t" \
  "v_lshlrev_b32_sdwa v226, 2, v226 dst_sel:DWORD dst_unused:UNUSED_PAD src0_sel:DWORD src1_sel:BYTE_1\n\t" \
  "v_fma_f32 v197, |v197|, v229, v197 mul:4\n\t" \
  "v_mfma_f32_16x16x32_bf16 v[164:167], v[212:215], v[120:123], v[164:167]\n\t" \
  "ds_read_b32 v223, v223 offset:15376\n\t" \
  "ds_read_b32 v222, v222 offset:15376\n\t" \
  "v_fma_f32 v196, |v196|, v228, v196 mul:4\n\t" \
  "v_fma_mix_f32 v231, |v199|, v231, v231 op_sel:[0,1,0] op_sel_hi:[0,1,1] clamp\n\t" \
  "v_mfma_f32_16x16x32_bf16 v[172:175], v[212:215], v[124:127], v[172:175]\n\t" \
  "ds_read_b64_tr_b16 v[212:213], v249 offset:30760\n\t" \
  "ds_read_b64_tr_b16 v[214:215], v249 offset:32296\n\t" \
  "v_fma_mix_f32 v230, |v198|, v230, v230 op_sel:[0,1,0] op_sel_hi:[0,1,1] clamp\n\t" \
  "s_waitcnt lgkmcnt(8)\n\t" \
  "v_fma_f32 v205, |v205|, v233, v205 mul:4\n\t" \
  "v_mfma_f32_16x16x32_bf16 v[148:151], v[216:219], v[128:131], v[148:151]\n\t" \
  "ds_read_b32 v225, v225 offset:0\n\t" \
  "v_fma_f32 v204, |v204|, v232, v204 mul:4\n\t" \
  "v_fma_f32 v207, |v207|, v235, v207 mul:4\n\t" \
  "v_mfma_f32_16x16x32_bf16 v[156:159], v[216:219], v[132:135], v[156:159]\n\t" \
  "ds_read_b32 v224, v224 offset:0\n\t" \
  "v_fma_f32 v206, |v206|, v234, v206 mul:4\n\t" \
  "v_fma_f32 v199, |v199|, v231, v199 mul:4\n\t" \
  "v_mfma_f32_16x16x32_bf16 v[164:167], v[216:219], v[136:139], v[164:167]\n\t" \
  "ds_read_b32 v227, v227 offset:15376\n\t" \
  "v_fma_f32 v198, |v198|, v230, v198 mul:4\n\t" \
  "v_cvt_pk_bf16_f32 v196, v196, v197\n\t" \
  "v_mfma_f32_16x16x32_bf16 v[172:175], v[216:219], v[140:143], v[172:175]\n\t" \
  "ds_read_b64_tr_b16 v[216:217], v249 offset:30816\n\t" \
  "v_cvt_pk_bf16_f32 v197, v198, v199\n\t" \
  "v_cvt_pk_bf16_f32 v204, v204, v205\n\t" \
  "v_cvt_pk_bf16_f32 v205, v206, v207\n\t" \
  "s_setprio 0\n\t" \
  "v_cvt_pk_f16_f32 v228, |v162|, |v163| clamp\n\t" \
  "v_cvt_pknorm_i16_f32 v230, |v160|, |v161|\n\t" \
  "v_mfma_f32_16x16x32_bf16 v[8:11], v[242:245], v[236:239], v[8:11]\n\t" \
  "ds_read_b64_tr_b16 v[218:219], v249 offset:32352\n\t" \
  "v_cvt_pk_f16_f32 v232, |v170|, |v171| clamp\n\t" \
  "v_cvt_pk_f16_f32 v234, |v168|, |v169| clamp\n\t" \
  "v_mfma_f32_16x16x32_bf16 v[12:15], v[240:243], v[236:239], v[12:15]\n\t" \
  "buffer_load_dwordx4 v[236:239], v250, s[8:11], s28 offen offset:128 nt\n\t" \
  "s_cmp_lg_u32 s58, 0\n\t" \
  "s_cbranch_scc1 .Lcnx_c22\n\t" \
  "s_add_u32 m0, s25, 8200\n\t" \
  "s_add_u32 s24, s24, s26\n\t" \
  "buffer_load_dwordx4 v254, s[8:11], s24 offen nt lds\n\t" \
  "s_cmp_lg_u32 s38, 4\n\t" \
  "s_cbranch_scc1 .Lcnx_c22\n\t" \
  "s_mov_b32 m0, 163632\n\t" \
  "s_add_u32 s30, s30, s31\n\t" \
  "buffer_load_dword v252, s[8:11], s30 offen lds\n\t" \
  ".Lcnx_c22:\n\t" \
  "ds_read_b32 v226, v226 offset:15376\n\t" \
  "v_and_b32_sdwa v229, s56, v228 dst_sel:DWORD dst_unused:UNUSED_PAD src0_sel:DWORD src1_sel:WORD_1\n\t" \
  "v_and_b32 v228, s56, v228\n\t" \
  "s_waitcnt lgkmcnt(12)\n\t" \
  "v_mfma_f32_16x16x32_bf16 v[0:3], v[208:211], v[178:181], v[0:3]\n\t" \
  "ds_read_b32 v229, v229 offset:0\n\t" \
  "v_lshlrev_b32_sdwa v231, 2, v230 dst_sel:DWORD dst_unused:UNUSED_PAD src0_sel:DWORD src1_sel:BYTE_3\n\t" \
  "v_lshlrev_b32_sdwa v230, 2, v230 dst_sel:DWORD dst_unused:UNUSED_PAD src0_sel:DWORD src1_sel:BYTE_1\n\t" \
  "v_mfma_f32_16x16x32_bf16 v[24:27], v[208:211], v[186:189], v[24:27]\n\t" \
  "ds_read_b32 v228, v228 offset:0\n\t" \
  "v_and_b32_sdwa v233, s56, v232 dst_sel:DWORD dst_unused:UNUSED_PAD src0_sel:DWORD src1_sel:WORD_1\n\t" \
  "v_and_b32 v232, s56, v232\n\t" \
  "v_mfma_f32_16x16x32_bf16 v[48:51], v[208:211], v[194:197], v[48:51]\n\t" \
  "ds_read_b32 v231, v231 offset:15376\n\t" \
  "v_and_b32_sdwa v235, s56, v234 dst_sel:DWORD dst_unused:UNUSED_PAD src0_sel:DWORD src1_sel:WORD_1\n\t" \
  "v_and_b32 v234, s56, v234\n\t" \
  "v_mfma_f32_16x16x32_bf16 v[72:75], v[208:211], v[202:205], v[72:75]\n\t" \
  "ds_read_b64_tr_b16 v[208:209], v249 offset:30824\n\t" \
  "ds_read_b64_tr_b16 v[210:211], v249 offset:32360\n\t" \
  "s_waitcnt lgkmcnt(13)\n\t" \
  "v_fma_f32 v147, |v147|, v221, v147 mul:4\n\t" \
  "v_fma_f32 v146, |v146|, v220, v146 mul:4\n\t" \
  "s_waitcnt lgkmcnt(11)\n\t" \
  "v_mfma_f32_16x16x32_bf16 v[4:7], v[212:215], v[178:181], v[4:7]\n\t" \
  "ds_read_b32 v230, v230 offset:15376\n\t" \
  "v_fma_mix_f32 v223, |v145|, v223, v223 op_sel:[0,1,0] op_sel_hi:[0,1,1] clamp\n\t" \
  "v_fma_mix_f32 v222, |v144|, v222, v222 op_sel:[0,1,0] op_sel_hi:[0,1,1] clamp\n\t" \
  "v_mfma_f32_16x16x32_bf16 v[28:31], v[212:215], v[186:189], v[28:31]\n\t" \
  "ds_read_b32 v233, v233 offset:0\n\t" \
  "s_waitcnt lgkmcnt(7)\n\t" \
  "v_fma_f32 v155, |v155|, v225, v155 mul:4\n\t" \
  "v_fma_f32 v154, |v154|, v224, v154 mul:4\n\t" \
  "v_mfma_f32_16x16x32_bf16 v[52:55], v[212:215], v[194:197], v[52:55]\n\t" \
  "ds_read_b32 v232, v232 offset:0\n\t" \
  "v_fma_mix_f32 v227, |v153|, v227, v227 op_sel:[0,1,0] op_sel_hi:[0,1,1] clamp\n\t" \
  "v_fma_mix_f32 v226, |v152|, v226, v226 op_sel:[0,1,0] op_sel_hi:[0,1,1] clamp\n\t" \
  "v_fma_f32 v145, |v145|, v223, v145 mul:4\n\t" \
  "v_mfma_f32_16x16x32_bf16 v[76:79], v[212:215], v[202:205], v[76:79]\n\t" \
  "ds_read_b64_tr_b16 v[212:213], v249 offset:30880\n\t" \
  "v_fma_f32 v144, |v144|, v222, v144 mul:4\n\t" \
  "v_fma_f32 v153, |v153|, v227, v153 mul:4\n\t" \
  "v_mfma_f32_16x16x32_bf16 v[8:11], v[216:219], v[178:181], v[8:11]\n\t" \
  "ds_read_b64_tr_b16 v[214:215], v249 offset:32416\n\t" \
  "v_fma_f32 v152, |v152|, v226, v152 mul:4\n\t" \
  "v_cvt_pk_bf16_f32 v147, v146, v147\n\t" \
  "v_mfma_f32_16x16x32_bf16 v[32:35], v[216:219], v[186:189], v[32:35]\n\t" \
  "ds_read_b32 v235, v235 offset:0\n\t" \
  "v_cvt_pk_bf16_f32 v146, v144, v145\n\t" \
  "v_cvt_pk_bf16_f32 v155, v154, v155\n\t" \
  "v_mfma_f32_16x16x32_bf16 v[56:59], v[216:219], v[194:197], v[56:59]\n\t" \
  "ds_read_b32 v234, v234 offset:0\n\t" \
  "v_cvt_pk_bf16_f32 v154, v152, v153\n\t" \
  "s_setprio 1\n\t" \
  "v_cvt_pk_f16_f32 v220, |v148|, |v149| clamp\n\t" \
  "v_mfma_f32_16x16x32_bf16 v[80:83], v[216:219], v[202:205], v[80:83]\n\t" \
  "ds_read_b64_tr_b16 v[216:217], v249 offset:30888\n\t" \
  "v_cvt_pknorm_i16_f32 v222, |v150|, |v151|\n\t" \
  "v_cvt_pk_f16_f32 v224, |v156|, |v157| clamp\n\t" \
  "s_waitcnt lgkmcnt(8)\n\t" \
  "v_mfma_f32_16x16x32_bf16 v[12:15], v[208:211], v[178:181], v[12:15]\n\t" \
  "ds_read_b64_tr_b16 v[218:219], v249 offset:32424\n\t" \
  "v_cvt_pknorm_i16_f32 v226, |v158|, |v159|\n\t" \
  "v_and_b32_sdwa v221, s56, v220 dst_sel:DWORD dst_unused:UNUSED_PAD src0_sel:DWORD src1_sel:WORD_1\n\t" \
  "v_mfma_f32_16x16x32_bf16 v[36:39], v[208:211], v[186:189], v[36:39]\n\t" \
  "v_and_b32 v220, s56, v220\n\t" \
  "v_lshlrev_b32_sdwa v223, 2, v222 dst_sel:DWORD dst_unused:UNUSED_PAD src0_sel:DWORD src1_sel:BYTE_3\n\t" \
  "v_mfma_f32_16x16x32_bf16 v[60:63], v[208:211], v[194:197], v[60:63]\n\t" \
  "ds_read_b32 v221, v221 offset:0\n\t" \
  "v_lshlrev_b32_sdwa v222, 2, v222 dst_sel:DWORD dst_unused:UNUSED_PAD src0_sel:DWORD src1_sel:BYTE_1\n\t" \
  "v_and_b32_sdwa v225, s56, v224 dst_sel:DWORD dst_unused:UNUSED_PAD src0_sel:DWORD src1_sel:WORD_1\n\t" \
  "v_and_b32 v224, s56, v224\n\t" \
  "v_mfma_f32_16x16x32_bf16 v[84:87], v[208:211], v[202:205], v[84:87]\n\t" \
  "ds_read_b64_tr_b16 v[208:209], v248 offset:0\n\t" \
  "ds_read_b64_tr_b16 v[210:211], v248 offset:1024\n\t" \
  "v_lshlrev_b32_sdwa v227, 2, v226 dst_sel:DWORD dst_unused:UNUSED_PAD src0_sel:DWORD src1_sel:BYTE_3\n\t" \
  "v_lshlrev_b32_sdwa v226, 2, v226 dst_sel:DWORD dst_unused:UNUSED_PAD src0_sel:DWORD src1_sel:BYTE_1\n\t" \
  "s_waitcnt lgkmcnt(7)\n\t" \
  "v_mfma_f32_16x16x32_bf16 v[16:19], v[212:215], v[178:181], v[16:19]\n\t" \
  "ds_read_b32 v220, v220 offset:0\n\t" \
  "ds_read_b32 v223, v223 offset:15376\n\t" \
  "v_fma_f32 v163, |v163|, v229, v163 mul:4\n\t" \
  "v_fma_f32 v162, |v162|, v228, v162 mul:4\n\t" \
  "v_mfma_f32_16x16x32_bf16 v[40:43], v[212:215], v[186:189], v[40:43]\n\t" \
  "ds_read_b32 v222, v222 offset:15376\n\t" \
  "v_fma_mix_f32 v231, |v161|, v231, v231 op_sel:[0,1,0] op_sel_hi:[0,1,1] clamp\n\t" \
  "v_fma_mix_f32 v230, |v160|, v230, v230 op_sel:[0,1,0] op_sel_hi:[0,1,1] clamp\n\t" \
  "v_mfma_f32_16x16x32_bf16 v[64:67], v[212:215], v[194:197], v[64:67]\n\t" \
  "ds_read_b32 v225, v225 offset:0\n\t" \
  "s_waitcnt lgkmcnt(9)\n\t" \
  "v_fma_f32 v171, |v171|, v233, v171 mul:4\n\t" \
  "v_fma_f32 v170, |v170|, v232, v170 mul:4\n\t" \
  "v_mfma_f32_16x16x32_bf16 v[88:91], v[212:215], v[202:205], v[88:91]\n\t" \
  "ds_read_b64_tr_b16 v[212:213], v248 offset:3072\n\t" \
  "v_fma_f32 v169, |v169|, v235, v169 mul:4\n\t" \
  "v_fma_f32 v168, |v168|, v234, v168 mul:4\n\t" \
  "s_waitcnt lgkmcnt(8)\n\t" \
  "v_mfma_f32_16x16x32_bf16 v[20:23], v[216:219], v[178:181], v[20:23]\n\t" \
  "ds_read_b64_tr_b16 v[214:215], v248 offset:4096\n\t" \
  "v_fma_f32 v161, |v161|, v231, v161 mul:4\n\t" \
  "v_fma_f32 v160, |v160|, v230, v160 mul:4\n\t" \
  "v_mfma_f32_16x16x32_bf16 v[44:47], v[216:219], v[186:189], v[44:47]\n\t" \
  "ds_read_b32 v224, v224 offset:0\n\t" \
  "v_cvt_pk_bf16_f32 v163, v162, v163\n\t" \
  "v_cvt_pk_bf16_f32 v162, v160, v161\n\t" \
  "v_mfma_f32_16x16x32_bf16 v[68:71], v[216:219], v[194:197], v[68:71]\n\t" \
  "ds_read_b32 v227, v227 offset:15376\n\t" \
  "v_cvt_pk_bf16_f32 v171, v170, v171\n\t" \
  "v_cvt_pk_bf16_f32 v170, v168, v169\n\t" \
  "v_cvt_pk_f16_f32 v228, |v164|, |v165| clamp\n\t" \
  "v_mfma_f32_16x16x32_bf16 v[92:95], v[216:219], v[202:205], v[92:95]\n\t" \
  "ds_read_b64_tr_b16 v[216:217], v248 offset:2048\n\t" \
  "v_cvt_pknorm_i16_f32 v230, |v166|, |v167|\n\t" \
  "s_setprio 2\n\t" \
  "v_cvt_pk_f16_f32 v232, |v172|, |v173| clamp\n\t" \
  "s_waitcnt lgkmcnt(9)\n\t" \
  "v_mfma_f32_16x16x32_bf16 v[176:179], v[208:211], v[96:99], 0\n\t" \
  "ds_read_b64_tr_b16 v[218:219], v248 offset:5120\n\t" \
  "v_cvt_pk_f16_f32 v234, |v174|, |v175| clamp\n\t" \
  "v_and_b32_sdwa v229, s56, v228 dst_sel:DWORD dst_unused:UNUSED_PAD src0_sel:DWORD src1_sel:WORD_1\n\t" \
  "v_mfma_f32_16x16x32_bf16 v[184:187], v[208:211], v[100:103], 0\n\t" \
  "ds_read_b32 v226, v226 offset:15376\n\t" \
  "v_and_b32 v228, s56, v228\n\t" \
  "v_lshlrev_b32_sdwa v231, 2, v230 dst_sel:DWORD dst_unused:UNUSED_PAD src0_sel:DWORD src1_sel:BYTE_3\n\t" \
  "v_mfma_f32_16x16x32_bf16 v[192:195], v[208:211], v[104:107], 0\n\t" \
  "ds_read_b32 v229, v229 offset:0\n\t" \
  "v_lshlrev_b32_sdwa v230, 2, v230 dst_sel:DWORD dst_unused:UNUSED_PAD src0_sel:DWORD src1_sel:BYTE_1\n\t" \
  "v_and_b32_sdwa v233, s56, v232 dst_sel:DWORD dst_unused:UNUSED_PAD src0_sel:DWORD src1_sel:WORD_1\n\t" \
  "v_mfma_f32_16x16x32_bf16 v[200:203], v[208:211], v[108:111], 0\n\t" \
  "ds_read_b64_tr_b16 v[208:209], v248 offset:512\n\t" \
  "v_and_b32 v232, s56, v232\n\t" \
  "v_and_b32_sdwa v235, s56, v234 dst_sel:DWORD dst_unused:UNUSED_PAD src0_sel:DWORD src1_sel:WORD_1\n\t" \
  "s_waitcnt lgkmcnt(7)\n\t" \
  "v_mfma_f32_16x16x32_bf16 v[176:179], v[212:215], v[112:115], v[176:179]\n\t" \
  "ds_read_b64_tr_b16 v[210:211], v248 offset:1536\n\t" \
  "ds_read_b32 v228, v228 offset:0\n\t" \
  "v_and_b32 v234, s56, v234\n\t" \
  "v_fma_f32 v149, |v149|, v221, v149 mul:4\n\t" \
  "v_mfma_f32_16x16x32_bf16 v[184:187], v[212:215], v[116:119], v[184:187]\n\t" \
  "ds_read_b32 v231, v231 offset:15376\n\t" \
  "ds_read_b32 v230, v230 offset:15376\n\t" \
  "v_fma_f32 v148, |v148|, v220, v148 mul:4\n\t" \
  "v_fma_mix_f32 v223, |v151|, v223, v223 op_sel:[0,1,0] op_sel_hi:[0,1,1] clamp\n\t" \
  "v_mfma_f32_16x16x32_bf16 v[192:195], v[212:215], v[120:123], v[192:195]\n\t" \
  "ds_read_b32 v233, v233 offset:0\n\t" \
  "v_fma_mix_f32 v222, |v150|, v222, v222 op_sel:[0,1,0] op_sel_hi:[0,1,1] clamp\n\t" \
  "s_waitcnt lgkmcnt(7)\n\t" \
  "v_fma_f32 v157, |v157|, v225, v157 mul:4\n\t" \
  "v_mfma_f32_16x16x32_bf16 v[200:203], v[212:215], v[124:127], v[200:203]\n\t" \
  "ds_read_b64_tr_b16 v[212:213], v248 offset:3584\n\t" \
  "v_fma_f32 v156, |v156|, v224, v156 mul:4\n\t" \
  "v_fma_mix_f32 v227, |v159|, v227, v227 op_sel:[0,1,0] op_sel_hi:[0,1,1] clamp\n\t" \
  "v_fma_mix_f32 v226, |v158|, v226, v226 op_sel:[0,1,0] op_sel_hi:[0,1,1] clamp\n\t" \
  "v_mfma_f32_16x16x32_bf16 v[176:179], v[216:219], v[128:131], v[176:179]\n\t" \
  "ds_read_b64_tr_b16 v[214:215], v248 offset:4608\n\t" \
  "v_fma_f32 v151, |v151|, v223, v151 mul:4\n\t" \
  "v_fma_f32 v150, |v150|, v222, v150 mul:4\n\t" \
  "v_mfma_f32_16x16x32_bf16 v[184:187], v[216:219], v[132:135], v[184:187]\n\t" \
  "ds_read_b32 v232, v232 offset:0\n\t" \
  "v_fma_f32 v159, |v159|, v227, v159 mul:4\n\t" \
  "v_fma_f32 v158, |v158|, v226, v158 mul:4\n\t" \
  "v_mfma_f32_16x16x32_bf16 v[192:195], v[216:219], v[136:139], v[192:195]\n\t" \
  "ds_read_b32 v235, v235 offset:0\n\t" \
  "v_cvt_pk_bf16_f32 v148, v148, v149\n\t" \
  "v_cvt_pk_bf16_f32 v149, v150, v151\n\t" \
  "v_mfma_f32_16x16x32_bf16 v[200:203], v[216:219], v[140:143], v[200:203]\n\t" \
  "ds_read_b64_tr_b16 v[216:217], v248 offset:2560\n\t" \
  "v_cvt_pk_bf16_f32 v156, v156, v157\n\t" \
  "v_cvt_pk_bf16_f32 v157, v158, v159\n\t" \
  "s_waitcnt lgkmcnt(9)\n\t" \
  "v_mfma_f32_16x16x32_bf16 v[180:183], v[208:211], v[96:99], 0\n\t" \
  "ds_read_b64_tr_b16 v[218:219], v248 offset:5632\n\t" \
  "v_cvt_pk_f16_f32 v220, |v178|, |v179| clamp\n\t" \
  "s_setprio 3\n\t" \
  "v_cvt_pknorm_i16_f32 v222, |v176|, |v177|\n\t" \
  "v_mfma_f32_16x16x32_bf16 v[188:191], v[208:211], v[100:103], 0\n\t" \
  "s_waitcnt vmcnt(1)\n\t" \
  "s_cmp_lg_u32 s58, 1\n\t" \
  "s_cbranch_scc1 .Lcnx_c10\n\t" \
  "s_waitcnt vmcnt(0)\n\t" \
  ".Lcnx_c10:\n\t" \
  "s_mov_b64 exec, 3\n\t" \
  "ds_add_rtn_u32 v246, v251, v250 offset:47160\n\t" \
  "s_mov_b64 exec, -1\n\t" \
  "ds_read_b32 v234, v234 offset:0\n\t" \
  "v_cvt_pk_f16_f32 v224, |v186|, |v187| clamp\n\t" \
  "v_cvt_pknorm_i16_f32 v226, |v184|, |v185|\n\t" \
  "v_mfma_f32_16x16x32_bf16 v[196:199], v[208:211], v[104:107], 0\n\t" \
  "v_and_b32_sdwa v221, s56, v220 dst_sel:DWORD dst_unused:UNUSED_PAD src0_sel:DWORD src1_sel:WORD_1\n\t" \
  "v_and_b32 v220, s56, v220\n\t" \
  "v_mfma_f32_16x16x32_bf16 v[204:207], v[208:211], v[108:111], 0\n\t" \
  "s_waitcnt lgkmcnt(1)\n\t" \
  "v_readfirstlane_b32 s4, v246\n\t" \
  "s_cmp_lt_u32 s4, s57\n\t" \
  "s_cbranch_scc1 .Lcnx_w36\n.Lcnx_b36:\n\t" \
  "ds_read_b64_tr_b16 v[208:209], v249 offset:6152\n\t" \
  "v_lshlrev_b32_sdwa v223, 2, v222 dst_sel:DWORD dst_unused:UNUSED_PAD src0_sel:DWORD src1_sel:BYTE_3\n\t" \
  "v_lshlrev_b32_sdwa v222, 2, v222 dst_sel:DWORD dst_unused:UNUSED_PAD src0_sel:DWORD src1_sel:BYTE_1\n\t" \
  "v_and_b32_sdwa v225, s56, v224 dst_sel:DWORD dst_unused:UNUSED_PAD src0_sel:DWORD src1_sel:WORD_1\n\t" \
  "v_mfma_f32_16x16x32_bf16 v[180:183], v[212:215], v[112:115], v[180:183]\n\t" \
  "ds_read_b64_tr_b16 v[210:211], v249 offset:7688\n\t" \
  "v_and_b32 v224, s56, v224\n\t" \
  "v_lshlrev_b32_sdwa v227, 2, v226 dst_sel:DWORD dst_unused:UNUSED_PAD src0_sel:DWORD src1_sel:BYTE_3\n\t" \
  "v_mfma_f32_16x16x32_bf16 v[188:191], v[212:215], v[116:119], v[188:191]\n\t" \
  "s_cmp_eq_u32 s37, 0\n\t" \
  "s_cbranch_scc1 .Lcnx_f4\n.Lcnx_g4:\n\t" \
  "ds_read_b32 v221, v221 offset:0\n\t" \
  "ds_read_b32 v220, v220 offset:0\n\t" \
  "v_lshlrev_b32_sdwa v226, 2, v226 dst_sel:DWORD dst_unused:UNUSED_PAD src0_sel:DWORD src1_sel:BYTE_1\n\t" \
  "v_fma_f32 v165, |v165|, v229, v165 mul:4\n\t" \
  "v_mfma_f32_16x16x32_bf16 v[196:199], v[212:215], v[120:123], v[196:199]\n\t" \
  "ds_read_b32 v223, v223 offset:15376\n\t" \
  "ds_read_b32 v222, v222 offset:15376\n\t" \
  "v_fma_f32 v164, |v164|, v228, v164 mul:4\n\t" \
  "v_fma_mix_f32 v231, |v167|, v231, v231 op_sel:[0,1,0] op_sel_hi:[0,1,1] clamp\n\t" \
  "v_mfma_f32_16x16x32_bf16 v[204:207], v[212:215], v[124:127], v[204:207]\n\t" \
  "ds_read_b64_tr_b16 v[212:213], v249 offset:6160\n\t" \
  "ds_read_b64_tr_b16 v[214:215], v249 offset:7696\n\t" \
  "v_fma_mix_f32 v230, |v166|, v230, v230 op_sel:[0,1,0] op_sel_hi:[0,1,1] clamp\n\t" \
  "s_waitcnt lgkmcnt(8)\n\t" \
  "v_fma_f32 v173, |v173|, v233, v173 mul:4\n\t" \
  "v_mfma_f32_16x16x32_bf16 v[180:183], v[216:219], v[128:131], v[180:183]\n\t" \
  "ds_read_b32 v225, v225 offset:0\n\t" \
  "v_fma_f32 v172, |v172|, v232, v172 mul:4\n\t" \
  "v_fma_f32 v175, |v175|, v235, v175 mul:4\n\t" \
  "v_mfma_f32_16x16x32_bf16 v[188:191], v[216:219], v[132:135], v[188:191]\n\t" \
  "ds_read_b32 v224, v224 offset:0\n\t" \
  "v_fma_f32 v174, |v174|, v234, v174 mul:4\n\t" \
  "v_fma_f32 v167, |v167|, v231, v167 mul:4\n\t" \
  "v_mfma_f32_16x16x32_bf16 v[196:199], v[216:219], v[136:139], v[196:199]\n\t" \
  "ds_read_b32 v227, v227 offset:15376\n\t" \
  "v_fma_f32 v166, |v166|, v230, v166 mul:4\n\t" \
  "v_cvt_pk_bf16_f32 v164, v164, v165\n\t" \
  "v_mfma_f32_16x16x32_bf16 v[204:207], v[216:219], v[140:143], v[204:207]\n\t" \
  "ds_read_b64_tr_b16 v[216:217], v249 offset:6216\n\t" \
  "v_cvt_pk_bf16_f32 v165, v166, v167\n\t" \
  "v_cvt_pk_bf16_f32 v172, v172, v173\n\t" \
  "v_cvt_pk_bf16_f32 v173, v174, v175\n\t" \
  "s_setprio 0\n\t" \
  "v_cvt_pk_f16_f32 v228, |v194|, |v195| clamp\n\t" \
  "v_cvt_pknorm_i16_f32 v230, |v192|, |v193|\n\t" \
  "v_mfma_f32_16x16x32_bf16 v[16:19], v[242:245], v[236:239], v[16:19]\n\t" \
  "ds_read_b64_tr_b16 v[218:219], v249 offset:7752\n\t" \
  "v_cvt_pk_f16_f32 v232, |v202|, |v203| clamp\n\t" \
  "v_cvt_pk_f16_f32 v234, |v200|, |v201| clamp\n\t" \
  "v_mfma_f32_16x16x32_bf16 v[20:23], v[240:243], v[236:239], v[20:23]\n\t" \
  "buffer_load_dwordx4 v[236:239], v250, s[8:11], s29 offen offset:0 nt\n\t" \
  "s_cmp_lg_u32 s58, 0\n\t" \
  "s_cbranch_scc1 .Lcnx_c23\n\t" \
  "s_add_u32 m0, s25, 16400\n\t" \
  "s_add_u32 s24, s24, s26\n\t" \
  "buffer_load_dwordx4 v254, s[8:11], s24 offen nt lds\n\t" \
  "s_cmp_lg_u32 s38, 5\n\t" \
  "s_cbranch_scc1 .Lcnx_c23\n\t" \
  "s_mov_b32 m0, 163632\n\t" \
  "s_add_u32 s30, s30, s31\n\t" \
  "buffer_load_dword v252, s[8:11], s30 offen lds\n\t" \
  ".Lcnx_c23:\n\t" \
  "ds_read_b32 v226, v226 offset:15376\n\t" \
  "v_and_b32_sdwa v229, s56, v228 dst_sel:DWORD dst_unused:UNUSED_PAD src0_sel:DWORD src1_sel:WORD_1\n\t" \
  "v_and_b32 v228, s56, v228\n\t" \
  "s_waitcnt lgkmcnt(12)\n\t" \
  "v_mfma_f32_16x16x32_bf16 v[0:3], v[208:211], v[146:149], v[0:3]\n\t" \
  "ds_read_b32 v229, v229 offset:0\n\t" \
  "v_lshlrev_b32_sdwa v231, 2, v230 dst_sel:DWORD dst_unused:UNUSED_PAD src0_sel:DWORD src1_sel:BYTE_3\n\t" \
  "v_lshlrev_b32_sdwa v230, 2, v230 dst_sel:DWORD dst_unused:UNUSED_PAD src0_sel:DWORD src1_sel:BYTE_1\n\t" \
  "v_mfma_f32_16x16x32_bf16 v[24:27], v[208:211], v[154:157], v[24:27]\n\t" \
  "ds_read_b32 v228, v228 offset:0\n\t" \
  "v_and_b32_sdwa v233, s56, v232 dst_sel:DWORD dst_unused:UNUSED_PAD src0_sel:DWORD src1_sel:WORD_1\n\t" \
  "v_and_b32 v232, s56, v232\n\t" \
  "v_mfma_f32_16x16x32_bf16 v[48:51], v[208:211], v[162:165], v[48:51]\n\t" \
  "ds_read_b32 v231, v231 offset:15376\n\t" \
  "v_and_b32_sdwa v235, s56, v234 dst_sel:DWORD dst_unused:UNUSED_PAD src0_sel:DWORD src1_sel:WORD_1\n\t" \
  "v_and_b32 v234, s56, v234\n\t" \
  "v_mfma_f32_16x16x32_bf16 v[72:75], v[208:211], v[170:173], v[72:75]\n\t" \
  "ds_read_b64_tr_b16 v[208:209], v249 offset:6224\n\t" \
  "ds_read_b64_tr_b16 v[210:211], v249 offset:7760\n\t" \
  "s_waitcnt lgkmcnt(13)\n\t" \
  "v_fma_f32 v179, |v179|, v221, v179 mul:4\n\t" \
  "v_fma_f32 v178, |v178|, v220, v178 mul:4\n\t" \
  "s_waitcnt lgkmcnt(11)\n\t" \
  "v_mfma_f32_16x16x32_bf16 v[4:7], v[212:215], v[146:149], v[4:7]\n\t" \
  "ds_read_b32 v230, v230 offset:15376\n\t" \
  "v_fma_mix_f32 v223, |v177|, v223, v223 op_sel:[0,1,0] op_sel_hi:[0,1,1] clamp\n\t" \
  "v_fma_mix_f32 v222, |v176|, v222, v222 op_sel:[0,1,0] op_sel_hi:[0,1,1] clamp\n\t" \
  "v_mfma_f32_16x16x32_bf16 v[28:31], v[212:215], v[154:157], v[28:31]\n\t" \
  "ds_read_b32 v233, v233 offset:0\n\t" \
  "s_waitcnt lgkmcnt(7)\n\t" \
  "v_fma_f32 v187, |v187|, v225, v187 mul:4\n\t" \
  "v_fma_f32 v186, |v186|, v224, v186 mul:4\n\t" \
  "v_mfma_f32_16x16x32_bf16 v[52:55], v[212:215], v[162:165], v[52:55]\n\t" \
  "ds_read_b32 v232, v232 offset:0\n\t" \
  "v_fma_mix_f32 v227, |v185|, v227, v227 op_sel:[0,1,0] op_sel_hi:[0,1,1] clamp\n\t" \
  "v_fma_mix_f32 v226, |v184|, v226, v226 op_sel:[0,1,0] op_sel_hi:[0,1,1] clamp\n\t" \
  "v_fma_f32 v177, |v177|, v223, v177 mul:4\n\t" \
  "v_mfma_f32_16x16x32_bf16 v[76:79], v[212:215], v[170:173], v[76:79]\n\t" \
  "ds_read_b64_tr_b16 v[212:213], v249 offset:6280\n\t" \
  "v_fma_f32 v176, |v176|, v222, v176 mul:4\n\t" \
  "v_fma_f32 v185, |v185|, v227, v185 mul:4\n\t" \
  "v_mfma_f32_16x16x32_bf16 v[8:11], v[216:219], v[146:149], v[8:11]\n\t" \
  "ds_read_b64_tr_b16 v[214:215], v249 offset:7816\n\t" \
  "v_fma_f32 v184, |v184|, v226, v184 mul:4\n\t" \
  "v_cvt_pk_bf16_f32 v179, v178, v179\n\t" \
  "v_mfma_f32_16x16x32_bf16 v[32:35], v[216:219], v[154:157], v[32:35]\n\t" \
  "ds_read_b32 v235, v235 offset:0\n\t" \
  "v_cvt_pk_bf16_f32 v178, v176, v177\n\t" \
  "v_cvt_pk_bf16_f32 v187, v186, v187\n\t" \
  "v_mfma_f32_16x16x32_bf16 v[56:59], v[216:219], v[162:165], v[56:59]\n\t" \
  "ds_read_b32 v234, v234 offset:0\n\t" \
  "v_cvt_pk_bf16_f32 v186, v184, v185\n\t" \
  "s_setprio 1\n\t" \
  "v_cvt_pk_f16_f32 v220, |v180|, |v181| clamp\n\t" \
  "v_mfma_f32_16x16x32_bf16 v[80:83], v[216:219], v[170:173], v[80:83]\n\t" \
  "ds_read_b64_tr_b16 v[216:217], v249 offset:6288\n\t" \
  "v_cvt_pknorm_i16_f32 v222, |v182|, |v183|\n\t" \
  "v_cvt_pk_f16_f32 v224, |v188|, |v189| clamp\n\t" \
  "s_waitcnt lgkmcnt(8)\n\t" \
  "v_mfma_f32_16x16x32_bf16 v[12:15], v[208:211], v[146:149], v[12:15]\n\t" \
  "ds_read_b64_tr_b16 v[218:219], v249 offset:7824\n\t" \
  "v_cvt_pknorm_i16_f32 v226, |v190|, |v191|\n\t" \
  "v_and_b32_sdwa v221, s56, v220 dst_sel:DWORD dst_unused:UNUSED_PAD src0_sel:DWORD src1_sel:WORD_1\n\t" \
  "v_mfma_f32_16x16x32_bf16 v[36:39], v[208:211], v[154:157], v[36:39]\n\t" \
  "v_and_b32 v220, s56, v220\n\t" \
  "v_lshlrev_b32_sdwa v223, 2, v222 dst_sel:DWORD dst_unused:UNUSED_PAD src0_sel:DWORD src1_sel:BYTE_3\n\t" \
  "v_mfma_f32_16x16x32_bf16 v[60:63], v[208:211], v[162:165], v[60:63]\n\t" \
  "ds_read_b32 v221, v221 offset:0\n\t" \
  "v_lshlrev_b32_sdwa v222, 2, v222 dst_sel:DWORD dst_unused:UNUSED_PAD src0_sel:DWORD src1_sel:BYTE_1\n\t" \
  "v_and_b32_sdwa v225, s56, v224 dst_sel:DWORD dst_unused:UNUSED_PAD src0_sel:DWORD src1_sel:WORD_1\n\t" \
  "v_and_b32 v224, s56, v224\n\t" \
  "v_mfma_f32_16x16x32_bf16 v[84:87], v[208:211], v[170:173], v[84:87]\n\t" \
  "ds_read_b64_tr_b16 v[208:209], v248 offset:43048\n\t" \
  "ds_read_b64_tr_b16 v[210:211], v248 offset:44072\n\t" \
  "v_lshlrev_b32_sdwa v227, 2, v226 dst_sel:DWORD dst_unused:UNUSED_PAD src0_sel:DWORD src1_sel:BYTE_3\n\t" \
  "v_lshlrev_b32_sdwa v226, 2, v226 dst_sel:DWORD dst_unused:UNUSED_PAD src0_sel:DWORD src1_sel:BYTE_1\n\t" \
  "s_waitcnt lgkmcnt(7)\n\t" \
  "v_mfma_f32_16x16x32_bf16 v[16:19], v[212:215], v[146:149], v[16:19]\n\t" \
  "ds_read_b32 v220, v220 offset:0\n\t" \
  "ds_read_b32 v223, v223 offset:15376\n\t" \
  "v_fma_f32 v195, |v195|, v229, v195 mul:4\n\t" \
  "v_fma_f32 v194, |v194|, v228, v194 mul:4\n\t" \
  "v_mfma_f32_16x16x32_bf16 v[40:43], v[212:215], v[154:157], v[40:43]\n\t" \
  "ds_read_b32 v222, v222 offset:15376\n\t" \
  "v_fma_mix_f32 v231, |v193|, v231, v231 op_sel:[0,1,0] op_sel_hi:[0,1,1] clamp\n\t" \
  "v_fma_mix_f32 v230, |v192|, v230, v230 op_sel:[0,1,0] op_sel_hi:[0,1,1] clamp\n\t" \
  "v_mfma_f32_16x16x32_bf16 v[64:67], v[212:215], v[162:165], v[64:67]\n\t" \
  "ds_read_b32 v225, v225 offset:0\n\t" \
  "s_waitcnt lgkmcnt(9)\n\t" \
  "v_fma_f32 v203, |v203|, v233, v203 mul:4\n\t" \
  "v_fma_f32 v202, |v202|, v232, v202 mul:4\n\t" \
  "v_mfma_f32_16x16x32_bf16 v[88:91], v[212:215], v[170:173], v[88:91]\n\t" \
  "ds_read_b64_tr_b16 v[212:213], v248 offset:46120\n\t" \
  "v_fma_f32 v201, |v201|, v235, v201 mul:4\n\t" \
  "v_fma_f32 v200, |v200|, v234, v200 mul:4\n\t" \
  "s_waitcnt lgkmcnt(8)\n\t" \
  "v_mfma_f32_16x16x32_bf16 v[20:23], v[216:219], v[146:149], v[20:23]\n\t" \
  "ds_read_b64_tr_b16 v[214:215], v248 offset:47144\n\t" \
  "v_fma_f32 v193, |v193|, v231, v193 mul:4\n\t" \
  "v_fma_f32 v192, |v192|, v230, v192 mul:4\n\t" \
  "v_mfma_f32_16x16x32_bf16 v[44:47], v[216:219], v[154:157], v[44:47]\n\t" \
  "ds_read_b32 v224, v224 offset:0\n\t" \
  "v_cvt_pk_bf16_f32 v195, v194, v195\n\t" \
  "v_cvt_pk_bf16_f32 v194, v192, v193\n\t" \
  "v_mfma_f32_16x16x32_bf16 v[68:71], v[216:219], v[162:165], v[68:71]\n\t" \
  "ds_read_b32 v227, v227 offset:15376\n\t" \
  "v_cvt_pk_bf16_f32 v203, v202, v203\n\t" \
  "v_cvt_pk_bf16_f32 v202, v200, v201\n\t" \
  "v_cvt_pk_f16_f32 v228, |v196|, |v197| clamp\n\t" \
  "v_mfma_f32_16x16x32_bf16 v[92:95], v[216:219], v[170:173], v[92:95]\n\t" \
  "ds_read_b64_tr_b16 v[216:217], v248 offset:45096\n\t" \
  "v_cvt_pknorm_i16_f32 v230, |v198|, |v199|\n\t" \
  "s_setprio 2\n\t" \
  "v_cvt_pk_f16_f32 v232, |v204|, |v205| clamp\n\t" \
  "s_waitcnt lgkmcnt(9)\n\t" \
  "v_mfma_f32_16x16x32_bf16 v[144:147], v[208:211], v[96:99], 0\n\t" \
  "ds_read_b64_tr_b16 v[218:219], v248 offset:48168\n\t" \
  "v_cvt_pk_f16_f32 v234, |v206|, |v207| clamp\n\t" \
  "v_and_b32_sdwa v229, s56, v228 dst_sel:DWORD dst_unused:UNUSED_PAD src0_sel:DWORD src1_sel:WORD_1\n\t" \
  "v_mfma_f32_16x16x32_bf16 v[152:155], v[208:211], v[100:103], 0\n\t" \
  "ds_read_b32 v226, v226 offset:15376\n\t" \
  "v_and_b32 v228, s56, v228\n\t" \
  "v_lshlrev_b32_sdwa v231, 2, v230 dst_sel:DWORD dst_unused:UNUSED_PAD src0_sel:DWORD src1_sel:BYTE_3\n\t" \
  "v_mfma_f32_16x16x32_bf16 v[160:163], v[208:211], v[104:107], 0\n\t" \
  "ds_read_b32 v229, v229 offset:0\n\t" \
  "v_lshlrev_b32_sdwa v230, 2, v230 dst_sel:DWORD dst_unused:UNUSED_PAD src0_sel:DWORD src1_sel:BYTE_1\n\t" \
  "v_and_b32_sdwa v233, s56, v232 dst_sel:DWORD dst_unused:UNUSED_PAD src0_sel:DWORD src1_sel:WORD_1\n\t" \
  "v_mfma_f32_16x16x32_bf16 v[168:171], v[208:211], v[108:111], 0\n\t" \
  "ds_read_b64_tr_b16 v[208:209], v248 offset:43560\n\t" \
  "v_and_b32 v232, s56, v232\n\t" \
  "v_and_b32_sdwa v235, s56, v234 dst_sel:DWORD dst_unused:UNUSED_PAD src0_sel:DWORD src1_sel:WORD_1\n\t" \
  "s_waitcnt lgkmcnt(7)\n\t" \
  "v_mfma_f32_16x16x32_bf16 v[144:147], v[212:215], v[112:115], v[144:147]\n\t" \
  "ds_read_b64_tr_b16 v[210:211], v248 offset:44584\n\t" \
  "ds_read_b32 v228, v228 offset:0\n\t" \
  "v_and_b32 v234, s56, v234\n\t" \
  "v_fma_f32 v181, |v181|, v221, v181 mul:4\n\t" \
  "v_mfma_f32_16x16x32_bf16 v[152:155], v[212:215], v[116:119], v[152:155]\n\t" \
  "ds_read_b32 v231, v231 offset:15376\n\t" \
  "ds_read_b32 v230, v230 offset:15376\n\t" \
  "v_fma_f32 v180, |v180|, v220, v180 mul:4\n\t" \
  "v_fma_mix_f32 v223, |v183|, v223, v223 op_sel:[0,1,0] op_sel_hi:[0,1,1] clamp\n\t" \
  "v_mfma_f32_16x16x32_bf16 v[160:163], v[212:215], v[120:123], v[160:163]\n\t" \
  "ds_read_b32 v233, v233 offset:0\n\t" \
  "v_fma_mix_f32 v222, |v182|, v222, v222 op_sel:[0,1,0] op_sel_hi:[0,1,1] clamp\n\t" \
  "s_waitcnt lgkmcnt(7)\n\t" \
  "v_fma_f32 v189, |v189|, v225, v189 mul:4\n\t" \
  "v_mfma_f32_16x16x32_bf16 v[168:171], v[212:215], v[124:127], v[168:171]\n\t" \
  "ds_read_b64_tr_b16 v[212:213], v248 offset:46632\n\t" \
  "v_fma_f32 v188, |v188|, v224, v188 mul:4\n\t" \
  "v_fma_mix_f32 v227, |v191|, v227, v227 op_sel:[0,1,0] op_sel_hi:[0,1,1] clamp\n\t" \
  "v_fma_mix_f32 v226, |v190|, v226, v226 op_sel:[0,1,0] op_sel_hi:[0,1,1] clamp\n\t" \
  "v_mfma_f32_16x16x32_bf16 v[144:147], v[216:219], v[128:131], v[144:147]\n\t" \
  "ds_read_b64_tr_b16 v[214:215], v248 offset:47656\n\t" \
  "v_fma_f32 v183, |v183|, v223, v183 mul:4\n\t" \
  "v_fma_f32 v182, |v182|, v222, v182 mul:4\n\t" \
  "v_mfma_f32_16x16x32_bf16 v[152:155], v[216:219], v[132:135], v[152:155]\n\t" \
  "ds_read_b32 v232, v232 offset:0\n\t" \
  "v_fma_f32 v191, |v191|, v227, v191 mul:4\n\t" \
  "v_fma_f32 v190, |v190|, v226, v190 mul:4\n\t" \
  "v_mfma_f32_16x16x32_bf16 v[160:163], v[216:219], v[136:139], v[160:163]\n\t" \
  "ds_read_b32 v235, v235 offset:0\n\t" \
  "v_cvt_pk_bf16_f32 v180, v180, v181\n\t" \
  "v_cvt_pk_bf16_f32 v181, v182, v183\n\t" \
  "v_mfma_f32_16x16x32_bf16 v[168:171], v[216:219], v[140:143], v[168:171]\n\t" \
  "ds_read_b64_tr_b16 v[216:217], v248 offset:45608\n\t" \
  "v_cvt_pk_bf16_f32 v188, v188, v189\n\t" \
  "v_cvt_pk_bf16_f32 v189, v190, v191\n\t" \
  "s_waitcnt lgkmcnt(9)\n\t" \
  "v_mfma_f32_16x16x32_bf16 v[148:151], v[208:211], v[96:99], 0\n\t" \
  "ds_read_b64_tr_b16 v[218:219], v248 offset:48680\n\t" \
  "v_cvt_pk_f16_f32 v220, |v146|, |v147| clamp\n\t" \
  "s_setprio 3\n\t" \
  "v_cvt_pknorm_i16_f32 v222, |v144|, |v145|\n\t" \
  "v_mfma_f32_16x16x32_bf16 v[156:159], v[208:211], v[100:103], 0\n\t" \
  "s_waitcnt vmcnt(1)\n\t" \
  "s_cmp_lg_u32 s58, 1\n\t" \
  "s_cbranch_scc1 .Lcnx_c11\n\t" \
  "s_waitcnt vmcnt(0)\n\t" \
  ".Lcnx_c11:\n\t" \
  "s_mov_b64 exec, 3\n\t" \
  "ds_add_rtn_u32 v246, v251, v250 offset:47168\n\t" \
  "s_mov_b64 exec, -1\n\t" \
  "ds_read_b32 v234, v234 offset:0\n\t" \
  "v_cvt_pk_f16_f32 v224, |v154|, |v155| clamp\n\t" \
  "v_cvt_pknorm_i16_f32 v226, |v152|, |v153|\n\t" \
  "v_mfma_f32_16x16x32_bf16 v[164:167], v[208:211], v[104:107], 0\n\t" \
  "v_and_b32_sdwa v221, s56, v220 dst_sel:DWORD dst_unused:UNUSED_PAD src0_sel:DWORD src1_sel:WORD_1\n\t" \
  "v_and_b32 v220, s56, v220\n\t" \
  "v_mfma_f32_16x16x32_bf16 v[172:175], v[208:211], v[108:111], 0\n\t" \
  "s_waitcnt lgkmcnt(1)\n\t" \
  "v_readfirstlane_b32 s4, v246\n\t" \
  "s_cmp_lt_u32 s4, s57\n\t" \
  "s_cbranch_scc1 .Lcnx_w48\n.Lcnx_b48:\n\t" \
  "ds_read_b64_tr_b16 v[208:209], v249 offset:12304\n\t" \
  "v_lshlrev_b32_sdwa v223, 2, v222 dst_sel:DWORD dst_unused:UNUSED_PAD src0_sel:DWORD src1_sel:BYTE_3\n\t" \
  "v_lshlrev_b32_sdwa v222, 2, v222 dst_sel:DWORD dst_unused:UNUSED_PAD src0_sel:DWORD src1_sel:BYTE_1\n\t" \
  "v_and_b32_sdwa v225, s56, v224 dst_sel:DWORD dst_unused:UNUSED_PAD src0_sel:DWORD src1_sel:WORD_1\n\t" \
  "v_mfma_f32_16x16x32_bf16 v[148:151], v[212:215], v[112:115], v[148:151]\n\t" \
  "ds_read_b64_tr_b16 v[210:211], v249 offset:13840\n\t" \
  "v_and_b32 v224, s56, v224\n\t" \
  "v_lshlrev_b32_sdwa v227, 2, v226 dst_sel:DWORD dst_unused:UNUSED_PAD src0_sel:DWORD src1_sel:BYTE_3\n\t" \
  "v_mfma_f32_16x16x32_bf16 v[156:159], v[212:215], v[116:119], v[156:159]\n\t" \
  "s_cmp_eq_u32 s37, 0\n\t" \
  "s_cbranch_scc1 .Lcnx_f5\n.Lcnx_g5:\n\t" \
  "ds_read_b32 v221, v221 offset:0\n\t" \
  "ds_read_b32 v220, v220 offset:0\n\t" \
  "v_lshlrev_b32_sdwa v226, 2, v226 dst_sel:DWORD dst_unused:UNUSED_PAD src0_sel:DWORD src1_sel:BYTE_1\n\t" \
  "v_fma_f32 v197, |v197|, v229, v197 mul:4\n\t" \
  "v_mfma_f32_16x16x32_bf16 v[164:167], v[212:215], v[120:123], v[164:167]\n\t" \
  "ds_read_b32 v223, v223 offset:15376\n\t" \
  "ds_read_b32 v222, v222 offset:15376\n\t" \
  "v_fma_f32 v196, |v196|, v228, v196 mul:4\n\t" \
  "v_fma_mix_f32 v231, |v199|, v231, v231 op_sel:[0,1,0] op_sel_hi:[0,1,1] clamp\n\t" \
  "v_mfma_f32_16x16x32_bf16 v[172:175], v[212:215], v[124:127], v[172:175]\n\t" \
  "ds_read_b64_tr_b16 v[212:213], v249 offset:12312\n\t" \
  "ds_read_b64_tr_b16 v[214:215], v249 offset:13848\n\t" \
  "v_fma_mix_f32 v230, |v198|, v230, v230 op_sel:[0,1,0] op_sel_hi:[0,1,1] clamp\n\t" \
  "s_waitcnt lgkmcnt(8)\n\t" \
  "v_fma_f32 v205, |v205|, v233, v205 mul:4\n\t" \
  "v_mfma_f32_16x16x32_bf16 v[148:151], v[216:219], v[128:131], v[148:151]\n\t" \
  "ds_read_b32 v225, v225 offset:0\n\t" \
  "v_fma_f32 v204, |v204|, v232, v204 mul:4\n\t" \
  "v_fma_f32 v207, |v207|, v235, v207 mul:4\n\t" \
  "v_mfma_f32_16x16x32_bf16 v[156:159], v[216:219], v[132:135], v[156:159]\n\t" \
  "ds_read_b32 v224, v224 offset:0\n\t" \
  "v_fma_f32 v206, |v206|, v234, v206 mul:4\n\t" \
  "v_fma_f32 v199, |v199|, v231, v199 mul:4\n\t" \
  "v_mfma_f32_16x16x32_bf16 v[164:167], v[216:219], v[136:139], v[164:167]\n\t" \
  "ds_read_b32 v227, v227 offset:15376\n\t" \
  "v_fma_f32 v198, |v198|, v230, v198 mul:4\n\t" \
  "v_cvt_pk_bf16_f32 v196, v196, v197\n\t" \
  "v_mfma_f32_16x16x32_bf16 v[172:175], v[216:219], v[140:143], v[172:175]\n\t" \
  "ds_read_b64_tr_b16 v[216:217], v249 offset:12368\n\t" \
  "v_cvt_pk_bf16_f32 v197, v198, v199\n\t" \
  "v_cvt_pk_bf16_f32 v204, v204, v205\n\t" \
  "v_cvt_pk_bf16_f32 v205, v206, v207\n\t" \
  "s_setprio 0\n\t" \
  "v_cvt_pk_f16_f32 v228, |v162|, |v163| clamp\n\t" \
  "v_cvt_pknorm_i16_f32 v230, |v160|, |v161|\n\t" \
  "v_mfma_f32_16x16x32_bf16 v[24:27], v[242:245], v[236:239], v[24:27]\n\t" \
  "ds_read_b64_tr_b16 v[218:219], v249 offset:13904\n\t" \
  "v_cvt_pk_f16_f32 v232, |v170|, |v171| clamp\n\t" \
  "v_cvt_pk_f16_f32 v234, |v168|, |v169| clamp\n\t" \
  "v_mfma_f32_16x16x32_bf16 v[28:31], v[240:243], v[236:239], v[28:31]\n\t" \
  "buffer_load_dwordx4 v[236:239], v250, s[8:11], s29 offen offset:64 nt\n\t" \
  "s_cmp_lg_u32 s58, 0\n\t" \
  "s_cbranch_scc1 .Lcnx_c24\n\t" \
  "s_add_u32 m0, s25, 24600\n\t" \
  "s_add_u32 s24, s24, s26\n\t" \
  "buffer_load_dwordx4 v254, s[8:11], s24 offen lds\n\t" \
  "s_cmp_lg_u32 s38, 6\n\t" \
  "s_cbranch_scc1 .Lcnx_c24\n\t" \
  "s_mov_b32 m0, 163632\n\t" \
  "s_add_u32 s30, s30, s31\n\t" \
  "buffer_load_dword v252, s[8:11], s30 offen lds\n\t" \
  ".Lcnx_c24:\n\t" \
  "ds_read_b32 v226, v226 offset:15376\n\t" \
  "v_and_b32_sdwa v229, s56, v228 dst_sel:DWORD dst_unused:UNUSED_PAD src0_sel:DWORD src1_sel:WORD_1\n\t" \
  "v_and_b32 v228, s56, v228\n\t" \
  "s_waitcnt lgkmcnt(12)\n\t" \
  "v_mfma_f32_16x16x32_bf16 v[0:3], v[208:211], v[178:181], v[0:3]\n\t" \
  "ds_read_b32 v229, v229 offset:0\n\t" \
  "v_lshlrev_b32_sdwa v231, 2, v230 dst_sel:DWORD dst_unused:UNUSED_PAD src0_sel:DWORD src1_sel:BYTE_3\n\t" \
  "v_lshlrev_b32_sdwa v230, 2, v230 dst_sel:DWORD dst_unused:UNUSED_PAD src0_sel:DWORD src1_sel:BYTE_1\n\t" \
  "v_mfma_f32_16x16x32_bf16 v[24:27], v[208:211], v[186:189], v[24:27]\n\t" \
  "ds_read_b32 v228, v228 offset:0\n\t" \
  "v_and_b32_sdwa v233, s56, v232 dst_sel:DWORD dst_unused:UNUSED_PAD src0_sel:DWORD src1_sel:WORD_1\n\t" \
  "v_and_b32 v232, s56, v232\n\t" \
  "v_mfma_f32_16x16x32_bf16 v[48:51], v[208:211], v[194:197], v[48:51]\n\t" \
  "ds_read_b32 v231, v231 offset:15376\n\t" \
  "v_and_b32_sdwa v235, s56, v234 dst_sel:DWORD dst_unused:UNUSED_PAD src0_sel:DWORD src1_sel:WORD_1\n\t" \
  "v_and_b32 v234, s56, v234\n\t" \
  "v_mfma_f32_16x16x32_bf16 v[72:75], v[208:211], v[202:205], v[72:75]\n\t" \
  "ds_read_b64_tr_b16 v[208:209], v249 offset:12376\n\t" \
  "ds_read_b64_tr_b16 v[210:211], v249 offset:13912\n\t" \
  "s_waitcnt lgkmcnt(13)\n\t" \
  "v_fma_f32 v147, |v147|, v221, v147 mul:4\n\t" \
  "v_fma_f32 v146, |v146|, v220, v146 mul:4\n\t" \
  "s_waitcnt lgkmcnt(11)\n\t" \
  "v_mfma_f32_16x16x32_bf16 v[4:7], v[212:215], v[178:181], v[4:7]\n\t" \
  "ds_read_b32 v230, v230 offset:15376\n\t" \
  "v_fma_mix_f32 v223, |v145|, v223, v223 op_sel:[0,1,0] op_sel_hi:[0,1,1] clamp\n\t" \
  "v_fma_mix_f32 v222, |v144|, v222, v222 op_sel:[0,1,0] op_sel_hi:[0,1,1] clamp\n\t" \
  "v_mfma_f32_16x16x32_bf16 v[28:31], v[212:215], v[186:189], v[28:31]\n\t" \
  "ds_read_b32 v233, v233 offset:0\n\t" \
  "s_waitcnt lgkmcnt(7)\n\t" \
  "v_fma_f32 v155, |v155|, v225, v155 mul:4\n\t" \
  "v_fma_f32 v154, |v154|, v224, v154 mul:4\n\t" \
  "v_mfma_f32_16x16x32_bf16 v[52:55], v[212:215], v[194:197], v[52:55]\n\t" \
  "ds_read_b32 v232, v232 offset:0\n\t" \
  "v_fma_mix_f32 v227, |v153|, v227, v227 op_sel:[0,1,0] op_sel_hi:[0,1,1] clamp\n\t" \
  "v_fma_mix_f32 v226, |v152|, v226, v226 op_sel:[0,1,0] op_sel_hi:[0,1,1] clamp\n\t" \
  "v_fma_f32 v145, |v145|, v223, v145 mul:4\n\t" \
  "v_mfma_f32_16x16x32_bf16 v[76:79], v[212:215], v[202:205], v[76:79]\n\t" \
  "ds_read_b64_tr_b16 v[212:213], v249 offset:12432\n\t" \
  "v_fma_f32 v144, |v144|, v222, v144 mul:4\n\t" \
  "v_fma_f32 v153, |v153|, v227, v153 mul:4\n\t" \
  "v_mfma_f32_16x16x32_bf16 v[8:11], v[216:219], v[178:181], v[8:11]\n\t" \
  "ds_read_b64_tr_b16 v[214:215], v249 offset:13968\n\t" \
  "v_fma_f32 v152, |v152|, v226, v152 mul:4\n\t" \
  "v_cvt_pk_bf16_f32 v147, v146, v147\n\t" \
  "v_mfma_f32_16x16x32_bf16 v[32:35], v[216:219], v[186:189], v[32:35]\n\t" \
  "ds_read_b32 v235, v235 offset:0\n\t" \
  "v_cvt_pk_bf16_f32 v146, v144, v145\n\t" \
  "v_cvt_pk_bf16_f32 v155, v154, v155\n\t" \
  "v_mfma_f32_16x16x32_bf16 v[56:59], v[216:219], v[194:197], v[56:59]\n\t" \
  "ds_read_b32 v234, v234 offset:0\n\t" \
  "v_cvt_pk_bf16_f32 v154, v152, v153\n\t" \
  "s_setprio 1\n\t" \
  "v_cvt_pk_f16_f32 v220, |v148|, |v149| clamp\n\t" \
  "v_mfma_f32_16x16x32_bf16 v[80:83], v[216:219], v[202:205], v[80:83]\n\t" \
  "ds_read_b64_tr_b16 v[216:217], v249 offset:12440\n\t" \
  "v_cvt_pknorm_i16_f32 v222, |v150|, |v151|\n\t" \
  "v_cvt_pk_f16_f32 v224, |v156|, |v157| clamp\n\t" \
  "s_waitcnt lgkmcnt(8)\n\t" \
  "v_mfma_f32_16x16x32_bf16 v[12:15], v[208:211], v[178:181], v[12:15]\n\t" \
  "ds_read_b64_tr_b16 v[218:219], v249 offset:13976\n\t" \
  "v_cvt_pknorm_i16_f32 v226, |v158|, |v159|\n\t" \
  "v_and_b32_sdwa v221, s56, v220 dst_sel:DWORD dst_unused:UNUSED_PAD src0_sel:DWORD src1_sel:WORD_1\n\t" \
  "v_mfma_f32_16x16x32_bf16 v[36:39], v[208:211], v[186:189], v[36:39]\n\t" \
  "v_and_b32 v220, s56, v220\n\t" \
  "v_lshlrev_b32_sdwa v223, 2, v222 dst_sel:DWORD dst_unused:UNUSED_PAD src0_sel:DWORD src1_sel:BYTE_3\n\t" \
  "v_mfma_f32_16x16x32_bf16 v[60:63], v[208:211], v[194:197], v[60:63]\n\t" \
  "ds_read_b32 v221, v221 offset:0\n\t" \
  "v_lshlrev_b32_sdwa v222, 2, v222 dst_sel:DWORD dst_unused:UNUSED_PAD src0_sel:DWORD src1_sel:BYTE_1\n\t" \
  "v_and_b32_sdwa v225, s56, v224 dst_sel:DWORD dst_unused:UNUSED_PAD src0_sel:DWORD src1_sel:WORD_1\n\t" \
  "v_and_b32 v224, s56, v224\n\t" \
  "v_mfma_f32_16x16x32_bf16 v[84:87], v[208:211], v[202:205], v[84:87]\n\t" \
  "ds_read_b64_tr_b16 v[208:209], v248 offset:49192\n\t" \
  "ds_read_b64_tr_b16 v[210:211], v248 offset:50216\n\t" \
  "v_lshlrev_b32_sdwa v227, 2, v226 dst_sel:DWORD dst_unused:UNUSED_PAD src0_sel:DWORD src1_sel:BYTE_3\n\t" \
  "v_lshlrev_b32_sdwa v226, 2, v226 dst_sel:DWORD dst_unused:UNUSED_PAD src0_sel:DWORD src1_sel:BYTE_1\n\t" \
  "s_waitcnt lgkmcnt(7)\n\t" \
  "v_mfma_f32_16x16x32_bf16 v[16:19], v[212:215], v[178:181], v[16:19]\n\t" \
  "ds_read_b32 v220, v220 offset:0\n\t" \
  "ds_read_b32 v223, v223 offset:15376\n\t" \
  "v_fma_f32 v163, |v163|, v229, v163 mul:4\n\t" \
  "v_fma_f32 v162, |v162|, v228, v162 mul:4\n\t" \
  "v_mfma_f32_16x16x32_bf16 v[40:43], v[212:215], v[186:189], v[40:43]\n\t" \
  "ds_read_b32 v222, v222 offset:15376\n\t" \
  "v_fma_mix_f32 v231, |v161|, v231, v231 op_sel:[0,1,0] op_sel_hi:[0,1,1] clamp\n\t" \
  "v_fma_mix_f32 v230, |v160|, v230, v230 op_sel:[0,1,0] op_sel_hi:[0,1,1] clamp\n\t" \
  "v_mfma_f32_16x16x32_bf16 v[64:67], v[212:215], v[194:197], v[64:67]\n\t" \
  "ds_read_b32 v225, v225 offset:0\n\t" \
  "s_waitcnt lgkmcnt(9)\n\t" \
  "v_fma_f32 v171, |v171|, v233, v171 mul:4\n\t" \
  "v_fma_f32 v170, |v170|, v232, v170 mul:4\n\t" \
  "v_mfma_f32_16x16x32_bf16 v[88:91], v[212:215], v[202:205], v[88:91]\n\t" \
  "ds_read_b64_tr_b16 v[212:213], v248 offset:52264\n\t" \
  "v_fma_f32 v169, |v169|, v235, v169 mul:4\n\t" \
  "v_fma_f32 v168, |v168|, v234, v168 mul:4\n\t" \
  "s_waitcnt lgkmcnt(8)\n\t" \
  "v_mfma_f32_16x16x32_bf16 v[20:23], v[216:219], v[178:181], v[20:23]\n\t" \
  "ds_read_b64_tr_b16 v[214:215], v248 offset:53288\n\t" \
  "v_fma_f32 v161, |v161|, v231, v161 mul:4\n\t" \
  "v_fma_f32 v160, |v160|, v230, v160 mul:4\n\t" \
  "v_mfma_f32_16x16x32_bf16 v[44:47], v[216:219], v[186:189], v[44:47]\n\t" \
  "ds_read_b32 v224, v224 offset:0\n\t" \
  "v_cvt_pk_bf16_f32 v163, v162, v163\n\t" \
  "v_cvt_pk_bf16_f32 v162, v160, v161\n\t" \
  "v_mfma_f32_16x16x32_bf16 v[68:71], v[216:219], v[194:197], v[68:71]\n\t" \
  "ds_read_b32 v227, v227 offset:15376\n\t" \
  "v_cvt_pk_bf16_f32 v171, v170, v171\n\t" \
  "v_cvt_pk_bf16_f32 v170, v168, v169\n\t" \
  "v_cvt_pk_f16_f32 v228, |v164|, |v165| clamp\n\t" \
  "v_mfma_f32_16x16x32_bf16 v[92:95], v[216:219], v[202:205], v[92:95]\n\t" \
  "ds_read_b64_tr_b16 v[216:217], v248 offset:51240\n\t" \
  "v_cvt_pknorm_i16_f32 v230, |v166|, |v167|\n\t" \
  "s_setprio 2\n\t" \
  "v_cvt_pk_f16_f32 v232, |v172|, |v173| clamp\n\t" \
  "s_waitcnt lgkmcnt(9)\n\t" \
  "v_mfma_f32_16x16x32_bf16 v[176:179], v[208:211], v[96:99], 0\n\t" \
  "ds_read_b64_tr_b16 v[218:219], v248 offset:54312\n\t" \
  "v_cvt_pk_f16_f32 v234, |v174|, |v175| clamp\n\t" \
  "v_and_b32_sdwa v229, s56, v228 dst_sel:DWORD dst_unused:UNUSED_PAD src0_sel:DWORD src1_sel:WORD_1\n\t" \
  "v_mfma_f32_16x16x32_bf16 v[184:187], v[208:211], v[100:103], 0\n\t" \
  "ds_read_b32 v226, v226 offset:15376\n\t" \
  "v_and_b32 v228, s56, v228\n\t" \
  "v_lshlrev_b32_sdwa v231, 2, v230 dst_sel:DWORD dst_unused:UNUSED_PAD src0_sel:DWORD src1_sel:BYTE_3\n\t" \
  "v_mfma_f32_16x16x32_bf16 v[192:195], v[208:211], v[104:107], 0\n\t" \
  "ds_read_b32 v229, v229 offset:0\n\t" \
  "v_lshlrev_b32_sdwa v230, 2, v230 dst_sel:DWORD dst_unused:UNUSED_PAD src0_sel:DWORD src1_sel:BYTE_1\n\t" \
  "v_and_b32_sdwa v233, s56, v232 dst_sel:DWORD dst_unused:UNUSED_PAD src0_sel:DWORD src1_sel:WORD_1\n\t" \
  "v_mfma_f32_16x16x32_bf16 v[200:203], v[208:211], v[108:111], 0\n\t" \
  "ds_read_b64_tr_b16 v[208:209], v248 offset:49704\n\t" \
  "v_and_b32 v232, s56, v232\n\t" \
  "v_and_b32_sdwa v235, s56, v234 dst_sel:DWORD dst_unused:UNUSED_PAD src0_sel:DWORD src1_sel:WORD_1\n\t" \
  "s_waitcnt lgkmcnt(7)\n\t" \
  "v_mfma_f32_16x16x32_bf16 v[176:179], v[212:215], v[112:115], v[176:179]\n\t" \
  "ds_read_b64_tr_b16 v[210:211], v248 offset:50728\n\t" \
  "ds_read_b32 v228, v228 offset:0\n\t" \
  "v_and_b32 v234, s56, v234\n\t" \
  "v_fma_f32 v149, |v149|, v221, v149 mul:4\n\t" \
  "v_mfma_f32_16x16x32_bf16 v[184:187], v[212:215], v[116:119], v[184:187]\n\t" \
  "ds_read_b32 v231, v231 offset:15376\n\t" \
  "ds_read_b32 v230, v230 offset:15376\n\t" \
  "v_fma_f32 v148, |v148|, v220, v148 mul:4\n\t" \
  "v_fma_mix_f32 v223, |v151|, v223, v223 op_sel:[0,1,0] op_sel_hi:[0,1,1] clamp\n\t" \
  "v_mfma_f32_16x16x32_bf16 v[192:195], v[212:215], v[120:123], v[192:195]\n\t" \
  "ds_read_b32 v233, v233 offset:0\n\t" \
  "v_fma_mix_f32 v222, |v150|, v222, v222 op_sel:[0,1,0] op_sel_hi:[0,1,1] clamp\n\t" \
  "s_waitcnt lgkmcnt(7)\n\t" \
  "v_fma_f32 v157, |v157|, v225, v157 mul:4\n\t" \
  "v_mfma_f32_16x16x32_bf16 v[200:203], v[212:215], v[124:127], v[200:203]\n\t" \
  "ds_read_b64_tr_b16 v[212:213], v248 offset:52776\n\t" \
  "v_fma_f32 v156, |v156|, v224, v156 mul:4\n\t" \
  "v_fma_mix_f32 v227, |v159|, v227, v227 op_sel:[0,1,0] op_sel_hi:[0,1,1] clamp\n\t" \
  "v_fma_mix_f32 v226, |v158|, v226, v226 op_sel:[0,1,0] op_sel_hi:[0,1,1] clamp\n\t" \
  "v_mfma_f32_16x16x32_bf16 v[176:179], v[216:219], v[128:131], v[176:179]\n\t" \
  "ds_read_b64_tr_b16 v[214:215], v248 offset:53800\n\t" \
  "v_fma_f32 v151, |v151|, v223, v151 mul:4\n\t" \
  "v_fma_f32 v150, |v150|, v222, v150 mul:4\n\t" \
  "v_mfma_f32_16x16x32_bf16 v[184:187], v[216:219], v[132:135], v[184:187]\n\t" \
  "ds_read_b32 v232, v232 offset:0\n\t" \
  "v_fma_f32 v159, |v159|, v227, v159 mul:4\n\t" \
  "v_fma_f32 v158, |v158|, v226, v158 mul:4\n\t" \
  "v_mfma_f32_16x16x32_bf16 v[192:195], v[216:219], v[136:139], v[192:195]\n\t" \
  "ds_read_b32 v235, v235 offset:0\n\t" \
  "v_cvt_pk_bf16_f32 v148, v148, v149\n\t" \
  "v_cvt_pk_bf16_f32 v149, v150, v151\n\t" \
  "v_mfma_f32_16x16x32_bf16 v[200:203], v[216:219], v[140:143], v[200:203]\n\t" \
  "ds_read_b64_tr_b16 v[216:217], v248 offset:51752\n\t" \
  "v_cvt_pk_bf16_f32 v156, v156, v157\n\t" \
  "v_cvt_pk_bf16_f32 v157, v158, v159\n\t" \
  "s_waitcnt lgkmcnt(9)\n\t" \
  "v_mfma_f32_16x16x32_bf16 v[180:183], v[208:211], v[96:99], 0\n\t" \
  "ds_read_b64_tr_b16 v[218:219], v248 offset:54824\n\t" \
  "v_cvt_pk_f16_f32 v220, |v178|, |v179| clamp\n\t" \
  "s_setprio 3\n\t" \
  "v_cvt_pknorm_i16_f32 v222, |v176|, |v177|\n\t" \
  "v_mfma_f32_16x16x32_bf16 v[188:191], v[208:211], v[100:103], 0\n\t" \
  "s_waitcnt vmcnt(1)\n\t" \
  "s_cmp_lg_u32 s58, 1\n\t" \
  "s_cbranch_scc1 .Lcnx_c12\n\t" \
  "s_waitcnt vmcnt(0)\n\t" \
  ".Lcnx_c12:\n\t" \
  "s_mov_b64 exec, 3\n\t" \
  "ds_add_rtn_u32 v246, v251, v250 offset:47176\n\t" \
  "s_mov_b64 exec, -1\n\t" \
  "ds_read_b32 v234, v234 offset:0\n\t" \
  "v_cvt_pk_f16_f32 v224, |v186|, |v187| clamp\n\t" \
  "v_cvt_pknorm_i16_f32 v226, |v184|, |v185|\n\t" \
  "v_mfma_f32_16x16x32_bf16 v[196:199], v[208:211], v[104:107], 0\n\t" \
  "v_and_b32_sdwa v221, s56, v220 dst_sel:DWORD dst_unused:UNUSED_PAD src0_sel:DWORD src1_sel:WORD_1\n\t" \
  "v_and_b32 v220, s56, v220\n\t" \
  "v_mfma_f32_16x16x32_bf16 v[204:207], v[208:211], v[108:111], 0\n\t" \
  "s_waitcnt lgkmcnt(1)\n\t" \
  "v_readfirstlane_b32 s4, v246\n\t" \
  "s_cmp_lt_u32 s4, s57\n\t" \
  "s_cbranch_scc1 .Lcnx_w60\n.Lcnx_b60:\n\t" \
  "ds_read_b64_tr_b16 v[208:209], v249 offset:24600\n\t" \
  "v_lshlrev_b32_sdwa v223, 2, v222 dst_sel:DWORD dst_unused:UNUSED_PAD src0_sel:DWORD src1_sel:BYTE_3\n\t" \
  "v_lshlrev_b32_sdwa v222, 2, v222 dst_sel:DWORD dst_unused:UNUSED_PAD src0_sel:DWORD src1_sel:BYTE_1\n\t" \
  "v_and_b32_sdwa v225, s56, v224 dst_sel:DWORD dst_unused:UNUSED_PAD src0_sel:DWORD src1_sel:WORD_1\n\t" \
  "v_mfma_f32_16x16x32_bf16 v[180:183], v[212:215], v[112:115], v[180:183]\n\t" \
  "ds_read_b64_tr_b16 v[210:211], v249 offset:26136\n\t" \
  "v_and_b32 v224, s56, v224\n\t" \
  "v_lshlrev_b32_sdwa v227, 2, v226 dst_sel:DWORD dst_unused:UNUSED_PAD src0_sel:DWORD src1_sel:BYTE_3\n\t" \
  "v_mfma_f32_16x16x32_bf16 v[188:191], v[212:215], v[116:119], v[188:191]\n\t" \
  "s_cmp_eq_u32 s37, 0\n\t" \
  "s_cbranch_scc1 .Lcnx_f6\n.Lcnx_g6:\n\t" \
  "ds_read_b32 v221, v221 offset:0\n\t" \
  "ds_read_b32 v220, v220 offset:0\n\t" \
  "v_lshlrev_b32_sdwa v226, 2, v226 dst_sel:DWORD dst_unused:UNUSED_PAD src0_sel:DWORD src1_sel:BYTE_1\n\t" \
  "v_fma_f32 v165, |v165|, v229, v165 mul:4\n\t" \
  "v_mfma_f32_16x16x32_bf16 v[196:199], v[212:215], v[120:123], v[196:199]\n\t" \
  "ds_read_b32 v223, v223 offset:15376\n\t" \
  "ds_read_b32 v222, v222 offset:15376\n\t" \
  "v_fma_f32 v164, |v164|, v228, v164 mul:4\n\t" \
  "v_fma_mix_f32 v231, |v167|, v231, v231 op_sel:[0,1,0] op_sel_hi:[0,1,1] clamp\n\t" \
  "v_mfma_f32_16x16x32_bf16 v[204:207], v[212:215], v[124:127], v[204:207]\n\t" \
  "ds_read_b64_tr_b16 v[212:213], v249 offset:24608\n\t" \
  "ds_read_b64_tr_b16 v[214:215], v249 offset:26144\n\t" \
  "v_fma_mix_f32 v230, |v166|, v230, v230 op_sel:[0,1,0] op_sel_hi:[0,1,1] clamp\n\t" \
  "s_waitcnt lgkmcnt(8)\n\t" \
  "v_fma_f32 v173, |v173|, v233, v173 mul:4\n\t" \
  "v_mfma_f32_16x16x32_bf16 v[180:183], v[216:219], v[128:131], v[180:183]\n\t" \
  "ds_read_b32 v225, v225 offset:0\n\t" \
  "v_fma_f32 v172, |v172|, v232, v172 mul:4\n\t" \
  "v_fma_f32 v175, |v175|, v235, v175 mul:4\n\t" \
  "v_mfma_f32_16x16x32_bf16 v[188:191], v[216:219], v[132:135], v[188:191]\n\t" \
  "ds_read_b32 v224, v224 offset:0\n\t" \
  "v_fma_f32 v174, |v174|, v234, v174 mul:4\n\t" \
  "v_fma_f32 v167, |v167|, v231, v167 mul:4\n\t" \
  "v_mfma_f32_16x16x32_bf16 v[196:199], v[216:219], v[136:139], v[196:199]\n\t" \
  "ds_read_b32 v227, v227 offset:15376\n\t" \
  "v_fma_f32 v166, |v166|, v230, v166 mul:4\n\t" \
  "v_cvt_pk_bf16_f32 v164, v164, v165\n\t" \
  "v_mfma_f32_16x16x32_bf16 v[204:207], v[216:219], v[140:143], v[204:207]\n\t" \
  "ds_read_b64_tr_b16 v[216:217], v249 offset:24664\n\t" \
  "v_cvt_pk_bf16_f32 v165, v166, v167\n\t" \
  "v_cvt_pk_bf16_f32 v172, v172, v173\n\t" \
  "v_cvt_pk_bf16_f32 v173, v174, v175\n\t" \
  "s_setprio 0\n\t" \
  "v_cvt_pk_f16_f32 v228, |v194|, |v195| clamp\n\t" \
  "v_cvt_pknorm_i16_f32 v230, |v192|, |v193|\n\t" \
  "v_mfma_f32_16x16x32_bf16 v[32:35], v[242:245], v[236:239], v[32:35]\n\t" \
  "ds_read_b64_tr_b16 v[218:219], v249 offset:26200\n\t" \
  "v_cvt_pk_f16_f32 v232, |v202|, |v203| clamp\n\t" \
  "v_cvt_pk_f16_f32 v234, |v200|, |v201| clamp\n\t" \
  "v_mfma_f32_16x16x32_bf16 v[36:39], v[240:243], v[236:239], v[36:39]\n\t" \
  "buffer_load_dwordx4 v[236:239], v250, s[8:11], s29 offen offset:128 nt\n\t" \
  "s_cmp_lg_u32 s58, 0\n\t" \
  "s_cbranch_scc1 .Lcnx_c25\n\t" \
  "s_add_u32 m0, s25, 32800\n\t" \
  "s_add_u32 s24, s24, s26\n\t" \
  "buffer_load_dwordx4 v254, s[8:11], s24 offen lds\n\t" \
  "s_cmp_lg_u32 s38, 7\n\t" \
  "s_cbranch_scc1 .Lcnx_c25\n\t" \
  "s_mov_b32 m0, 163632\n\t" \
  "s_add_u32 s30, s30, s31\n\t" \
  "buffer_load_dword v252, s[8:11], s30 offen lds\n\t" \
  ".Lcnx_c25:\n\t" \
  "ds_read_b32 v226, v226 offset:15376\n\t" \
  "v_and_b32_sdwa v229, s56, v228 dst_sel:DWORD dst_unused:UNUSED_PAD src0_sel:DWORD src1_sel:WORD_1\n\t" \
  "v_and_b32 v228, s56, v228\n\t" \
  "s_waitcnt lgkmcnt(12)\n\t" \
  "v_mfma_f32_16x16x32_bf16 v[0:3], v[208:211], v[146:149], v[0:3]\n\t" \
  "ds_read_b32 v229, v229 offset:0\n\t" \
  "v_lshlrev_b32_sdwa v231, 2, v230 dst_sel:DWORD dst_unused:UNUSED_PAD src0_sel:DWORD src1_sel:BYTE_3\n\t" \
  "v_lshlrev_b32_sdwa v230, 2, v230 dst_sel:DWORD dst_unused:UNUSED_PAD src0_sel:DWORD src1_sel:BYTE_1\n\t" \
  "v_mfma_f32_16x16x32_bf16 v[24:27], v[208:211], v[154:157], v[24:27]\n\t" \
  "ds_read_b32 v228, v228 offset:0\n\t" \
  "v_and_b32_sdwa v233, s56, v232 dst_sel:DWORD dst_unused:UNUSED_PAD src0_sel:DWORD src1_sel:WORD_1\n\t" \
  "v_and_b32 v232, s56, v232\n\t" \
  "v_mfma_f32_16x16x32_bf16 v[48:51], v[208:211], v[162:165], v[48:51]\n\t" \
  "ds_read_b32 v231, v231 offset:15376\n\t" \
  "v_and_b32_sdwa v235, s56, v234 dst_sel:DWORD dst_unused:UNUSED_PAD src0_sel:DWORD src1_sel:WORD_1\n\t" \
  "v_and_b32 v234, s56, v234\n\t" \
  "v_mfma_f32_16x16x32_bf16 v[72:75], v[208:211], v[170:173], v[72:75]\n\t" \
  "ds_read_b64_tr_b16 v[208:209], v249 offset:24672\n\t" \
  "ds_read_b64_tr_b16 v[210:211], v249 offset:26208\n\t" \
  "s_waitcnt lgkmcnt(13)\n\t" \
  "v_fma_f32 v179, |v179|, v221, v179 mul:4\n\t" \
  "v_fma_f32 v178, |v178|, v220, v178 mul:4\n\t" \
  "s_waitcnt lgkmcnt(11)\n\t" \
  "v_mfma_f32_16x16x32_bf16 v[4:7], v[212:215], v[146:149], v[4:7]\n\t" \
  "ds_read_b32 v230, v230 offset:15376\n\t" \
  "v_fma_mix_f32 v223, |v177|, v223, v223 op_sel:[0,1,0] op_sel_hi:[0,1,1] clamp\n\t" \
  "v_fma_mix_f32 v222, |v176|, v222, v222 op_sel:[0,1,0] op_sel_hi:[0,1,1] clamp\n\t" \
  "v_mfma_f32_16x16x32_bf16 v[28:31], v[212:215], v[154:157], v[28:31]\n\t" \
  "ds_read_b32 v233, v233 offset:0\n\t" \
  "s_waitcnt lgkmcnt(7)\n\t" \
  "v_fma_f32 v187, |v187|, v225, v187 mul:4\n\t" \
  "v_fma_f32 v186, |v186|, v224, v186 mul:4\n\t" \
  "v_mfma_f32_16x16x32_bf16 v[52:55], v[212:215], v[162:165], v[52:55]\n\t" \
  "ds_read_b32 v232, v232 offset:0\n\t" \
  "v_fma_mix_f32 v227, |v185|, v227, v227 op_sel:[0,1,0] op_sel_hi:[0,1,1] clamp\n\t" \
  "v_fma_mix_f32 v226, |v184|, v226, v226 op_sel:[0,1,0] op_sel_hi:[0,1,1] clamp\n\t" \
  "v_fma_f32 v177, |v177|, v223, v177 mul:4\n\t" \
  "v_mfma_f32_16x16x32_bf16 v[76:79], v[212:215], v[170:173], v[76:79]\n\t" \
  "ds_read_b64_tr_b16 v[212:213], v249 offset:24728\n\t" \
  "v_fma_f32 v176, |v176|, v222, v176 mul:4\n\t" \
  "v_fma_f32 v185, |v185|, v227, v185 mul:4\n\t" \
  "v_mfma_f32_16x16x32_bf16 v[8:11], v[216:219], v[146:149], v[8:11]\n\t" \
  "ds_read_b64_tr_b16 v[214:215], v249 offset:26264\n\t" \
  "v_fma_f32 v184, |v184|, v226, v184 mul:4\n\t" \
  "v_cvt_pk_bf16_f32 v179, v178, v179\n\t" \
  "v_mfma_f32_16x16x32_bf16 v[32:35], v[216:219], v[154:157], v[32:35]\n\t" \
  "ds_read_b32 v235, v235 offset:0\n\t" \
  "v_cvt_pk_bf16_f32 v178, v176, v177\n\t" \
  "v_cvt_pk_bf16_f32 v187, v186, v187\n\t" \
  "v_mfma_f32_16x16x32_bf16 v[56:59], v[216:219], v[162:165], v[56:59]\n\t" \
  "ds_read_b32 v234, v234 offset:0\n\t" \
  "v_cvt_pk_bf16_f32 v186, v184, v185\n\t" \
  "s_setprio 1\n\t" \
  "v_cvt_pk_f16_f32 v220, |v180|, |v181| clamp\n\t" \
  "v_mfma_f32_16x16x32_bf16 v[80:83], v[216:219], v[170:173], v[80:83]\n\t" \
  "ds_read_b64_tr_b16 v[216:217], v249 offset:24736\n\t" \
  "v_cvt_pknorm_i16_f32 v222, |v182|, |v183|\n\t" \
  "v_cvt_pk_f16_f32 v224, |v188|, |v189| clamp\n\t" \
  "s_waitcnt lgkmcnt(8)\n\t" \
  "v_mfma_f32_16x16x32_bf16 v[12:15], v[208:211], v[146:149], v[12:15]\n\t" \
  "ds_read_b64_tr_b16 v[218:219], v249 offset:26272\n\t" \
  "v_cvt_pknorm_i16_f32 v226, |v190|, |v191|\n\t" \
  "v_and_b32_sdwa v221, s56, v220 dst_sel:DWORD dst_unused:UNUSED_PAD src0_sel:DWORD src1_sel:WORD_1\n\t" \
  "v_mfma_f32_16x16x32_bf16 v[36:39], v[208:211], v[154:157], v[36:39]\n\t" \
  "v_and_b32 v220, s56, v220\n\t" \
  "v_lshlrev_b32_sdwa v223, 2, v222 dst_sel:DWORD dst_unused:UNUSED_PAD src0_sel:DWORD src1_sel:BYTE_3\n\t" \
  "v_mfma_f32_16x16x32_bf16 v[60:63], v[208:211], v[162:165], v[60:63]\n\t" \
  "ds_read_b32 v221, v221 offset:0\n\t" \
  "v_lshlrev_b32_sdwa v222, 2, v222 dst_sel:DWORD dst_unused:UNUSED_PAD src0_sel:DWORD src1_sel:BYTE_1\n\t" \
  "v_and_b32_sdwa v225, s56, v224 dst_sel:DWORD dst_unused:UNUSED_PAD src0_sel:DWORD src1_sel:WORD_1\n\t" \
  "v_and_b32 v224, s56, v224\n\t" \
  "v_mfma_f32_16x16x32_bf16 v[84:87], v[208:211], v[170:173], v[84:87]\n\t" \
  "ds_read_b64_tr_b16 v[208:209], v248 offset:36904\n\t" \
  "ds_read_b64_tr_b16 v[210:211], v248 offset:37928\n\t" \
  "v_lshlrev_b32_sdwa v227, 2, v226 dst_sel:DWORD dst_unused:UNUSED_PAD src0_sel:DWORD src1_sel:BYTE_3\n\t" \
  "v_lshlrev_b32_sdwa v226, 2, v226 dst_sel:DWORD dst_unused:UNUSED_PAD src0_sel:DWORD src1_sel:BYTE_1\n\t" \
  "s_waitcnt lgkmcnt(7)\n\t" \
  "v_mfma_f32_16x16x32_bf16 v[16:19], v[212:215], v[146:149], v[16:19]\n\t" \
  "ds_read_b32 v220, v220 offset:0\n\t" \
  "ds_read_b32 v223, v223 offset:15376\n\t" \
  "v_fma_f32 v195, |v195|, v229, v195 mul:4\n\t" \
  "v_fma_f32 v194, |v194|, v228, v194 mul:4\n\t" \
  "v_mfma_f32_16x16x32_bf16 v[40:43], v[212:215], v[154:157], v[40:43]\n\t" \
  "ds_read_b32 v222, v222 offset:15376\n\t" \
  "v_fma_mix_f32 v231, |v193|, v231, v231 op_sel:[0,1,0] op_sel_hi:[0,1,1] clamp\n\t" \
  "v_fma_mix_f32 v230, |v192|, v230, v230 op_sel:[0,1,0] op_sel_hi:[0,1,1] clamp\n\t" \
  "v_mfma_f32_16x16x32_bf16 v[64:67], v[212:215], v[162:165], v[64:67]\n\t" \
  "ds_read_b32 v225, v225 offset:0\n\t" \
  "s_waitcnt lgkmcnt(9)\n\t" \
  "v_fma_f32 v203, |v203|, v233, v203 mul:4\n\t" \
  "v_fma_f32 v202, |v202|, v232, v202 mul:4\n\t" \
  "v_mfma_f32_16x16x32_bf16 v[88:91], v[212:215], v[170:173], v[88:91]\n\t" \
  "ds_read_b64_tr_b16 v[212:213], v248 offset:39976\n\t" \
  "v_fma_f32 v201, |v201|, v235, v201 mul:4\n\t" \
  "v_fma_f32 v200, |v200|, v234, v200 mul:4\n\t" \
  "s_waitcnt lgkmcnt(8)\n\t" \
  "v_mfma_f32_16x16x32_bf16 v[20:23], v[216:219], v[146:149], v[20:23]\n\t" \
  "ds_read_b64_tr_b16 v[214:215], v248 offset:41000\n\t" \
  "v_fma_f32 v193, |v193|, v231, v193 mul:4\n\t" \
  "v_fma_f32 v192, |v192|, v230, v192 mul:4\n\t" \
  "v_mfma_f32_16x16x32_bf16 v[44:47], v[216:219], v[154:157], v[44:47]\n\t" \
  "ds_read_b32 v224, v224 offset:0\n\t" \
  "v_cvt_pk_bf16_f32 v195, v194, v195\n\t" \
  "v_cvt_pk_bf16_f32 v194, v192, v193\n\t" \
  "v_mfma_f32_16x16x32_bf16 v[68:71], v[216:219], v[162:165], v[68:71]\n\t" \
  "ds_read_b32 v227, v227 offset:15376\n\t" \
  "v_cvt_pk_bf16_f32 v203, v202, v203\n\t" \
  "v_cvt_pk_bf16_f32 v202, v200, v201\n\t" \
  "v_cvt_pk_f16_f32 v228, |v196|, |v197| clamp\n\t" \
  "v_mfma_f32_16x16x32_bf16 v[92:95], v[216:219], v[170:173], v[92:95]\n\t" \
  "ds_read_b64_tr_b16 v[216:217], v248 offset:38952\n\t" \
  "v_cvt_pknorm_i16_f32 v230, |v198|, |v199|\n\t" \
  "s_setprio 2\n\t" \
  "v_cvt_pk_f16_f32 v232, |v204|, |v205| clamp\n\t" \
  "s_waitcnt lgkmcnt(9)\n\t" \
  "v_mfma_f32_16x16x32_bf16 v[144:147], v[208:211], v[96:99], 0\n\t" \
  "ds_read_b64_tr_b16 v[218:219], v248 offset:42024\n\t" \
  "v_cvt_pk_f16_f32 v234, |v206|, |v207| clamp\n\t" \
  "v_and_b32_sdwa v229, s56, v228 dst_sel:DWORD dst_unused:UNUSED_PAD src0_sel:DWORD src1_sel:WORD_1\n\t" \
  "v_mfma_f32_16x16x32_bf16 v[152:155], v[208:211], v[100:103], 0\n\t" \
  "ds_read_b32 v226, v226 offset:15376\n\t" \
  "v_and_b32 v228, s56, v228\n\t" \
  "v_lshlrev_b32_sdwa v231, 2, v230 dst_sel:DWORD dst_unused:UNUSED_PAD src0_sel:DWORD src1_sel:BYTE_3\n\t" \
  "v_mfma_f32_16x16x32_bf16 v[160:163], v[208:211], v[104:107], 0\n\t" \
  "ds_read_b32 v229, v229 offset:0\n\t" \
  "v_lshlrev_b32_sdwa v230, 2, v230 dst_sel:DWORD dst_unused:UNUSED_PAD src0_sel:DWORD src1_sel:BYTE_1\n\t" \
  "v_and_b32_sdwa v233, s56, v232 dst_sel:DWORD dst_unused:UNUSED_PAD src0_sel:DWORD src1_sel:WORD_1\n\t" \
  "v_mfma_f32_16x16x32_bf16 v[168:171], v[208:211], v[108:111], 0\n\t" \
  "ds_read_b64_tr_b16 v[208:209], v248 offset:37416\n\t" \
  "v_and_b32 v232, s56, v232\n\t" \
  "v_and_b32_sdwa v235, s56, v234 dst_sel:DWORD dst_unused:UNUSED_PAD src0_sel:DWORD src1_sel:WORD_1\n\t" \
  "s_waitcnt lgkmcnt(7)\n\t" \
  "v_mfma_f32_16x16x32_bf16 v[144:147], v[212:215], v[112:115], v[144:147]\n\t" \
  "ds_read_b64_tr_b16 v[210:211], v248 offset:38440\n\t" \
  "ds_read_b32 v228, v228 offset:0\n\t" \
  "v_and_b32 v234, s56, v234\n\t" \
  "v_fma_f32 v181, |v181|, v221, v181 mul:4\n\t" \
  "v_mfma_f32_16x16x32_bf16 v[152:155], v[212:215], v[116:119], v[152:155]\n\t" \
  "ds_read_b32 v231, v231 offset:15376\n\t" \
  "ds_read_b32 v230, v230 offset:15376\n\t" \
  "v_fma_f32 v180, |v180|, v220, v180 mul:4\n\t" \
  "v_fma_mix_f32 v223, |v183|, v223, v223 op_sel:[0,1,0] op_sel_hi:[0,1,1] clamp\n\t" \
  "v_mfma_f32_16x16x32_bf16 v[160:163], v[212:215], v[120:123], v[160:163]\n\t" \
  "ds_read_b32 v233, v233 offset:0\n\t" \
  "v_fma_mix_f32 v222, |v182|, v222, v222 op_sel:[0,1,0] op_sel_hi:[0,1,1] clamp\n\t" \
  "s_waitcnt lgkmcnt(7)\n\t" \
  "v_fma_f32 v189, |v189|, v225, v189 mul:4\n\t" \
  "v_mfma_f32_16x16x32_bf16 v[168:171], v[212:215], v[124:127], v[168:171]\n\t" \
  "ds_read_b64_tr_b16 v[212:213], v248 offset:40488\n\t" \
  "v_fma_f32 v188, |v188|, v224, v188 mul:4\n\t" \
  "v_fma_mix_f32 v227, |v191|, v227, v227 op_sel:[0,1,0] op_sel_hi:[0,1,1] clamp\n\t" \
  "v_fma_mix_f32 v226, |v190|, v226, v226 op_sel:[0,1,0] op_sel_hi:[0,1,1] clamp\n\t" \
  "v_mfma_f32_16x16x32_bf16 v[144:147], v[216:219], v[128:131], v[144:147]\n\t" \
  "ds_read_b64_tr_b16 v[214:215], v248 offset:41512\n\t" \
  "v_fma_f32 v183, |v183|, v223, v183 mul:4\n\t" \
  "v_fma_f32 v182, |v182|, v222, v182 mul:4\n\t" \
  "v_mfma_f32_16x16x32_bf16 v[152:155], v[216:219], v[132:135], v[152:155]\n\t" \
  "ds_read_b32 v232, v232 offset:0\n\t" \
  "v_fma_f32 v191, |v191|, v227, v191 mul:4\n\t" \
  "v_fma_f32 v190, |v190|, v226, v190 mul:4\n\t" \
  "v_mfma_f32_16x16x32_bf16 v[160:163], v[216:219], v[136:139], v[160:163]\n\t" \
  "ds_read_b32 v235, v235 offset:0\n\t" \
  "v_cvt_pk_bf16_f32 v180, v180, v181\n\t" \
  "v_cvt_pk_bf16_f32 v181, v182, v183\n\t" \
  "v_mfma_f32_16x16x32_bf16 v[168:171], v[216:219], v[140:143], v[168:171]\n\t" \
  "ds_read_b64_tr_b16 v[216:217], v248 offset:39464\n\t" \
  "v_cvt_pk_bf16_f32 v188, v188, v189\n\t" \
  "v_cvt_pk_bf16_f32 v189, v190, v191\n\t" \
  "s_waitcnt lgkmcnt(9)\n\t" \
  "v_mfma_f32_16x16x32_bf16 v[148:151], v[208:211], v[96:99], 0\n\t" \
  "ds_read_b64_tr_b16 v[218:219], v248 offset:42536\n\t" \
  "v_cvt_pk_f16_f32 v220, |v146|, |v147| clamp\n\t" \
  "s_setprio 3\n\t" \
  "v_cvt_pknorm_i16_f32 v222, |v144|, |v145|\n\t" \
  "v_mfma_f32_16x16x32_bf16 v[156:159], v[208:211], v[100:103], 0\n\t" \
  "s_waitcnt vmcnt(1)\n\t" \
  "s_cmp_lg_u32 s58, 1\n\t" \
  "s_cbranch_scc1 .Lcnx_c13\n\t" \
  "s_waitcnt vmcnt(0)\n\t" \
  ".Lcnx_c13:\n\t" \
  "s_mov_b64 exec, 3\n\t" \
  "ds_add_rtn_u32 v246, v251, v250 offset:47184\n\t" \
  "s_mov_b64 exec, -1\n\t" \
  "ds_read_b32 v234, v234 offset:0\n\t" \
  "v_cvt_pk_f16_f32 v224, |v154|, |v155| clamp\n\t" \
  "v_cvt_pknorm_i16_f32 v226, |v152|, |v153|\n\t" \
  "v_mfma_f32_16x16x32_bf16 v[164:167], v[208:211], v[104:107], 0\n\t" \
  "v_and_b32_sdwa v221, s56, v220 dst_sel:DWORD dst_unused:UNUSED_PAD src0_sel:DWORD src1_sel:WORD_1\n\t" \
  "v_and_b32 v220, s56, v220\n\t" \
  "v_mfma_f32_16x16x32_bf16 v[172:175], v[208:211], v[108:111], 0\n\t" \
  "s_waitcnt lgkmcnt(1)\n\t" \
  "v_readfirstlane_b32 s4, v246\n\t" \
  "s_cmp_lt_u32 s4, s57\n\t" \
  "s_cbranch_scc1 .Lcnx_w72\n.Lcnx_b72:\n\t" \
  "ds_read_b64_tr_b16 v[208:209], v249 offset:30752\n\t" \
  "v_lshlrev_b32_sdwa v223, 2, v222 dst_sel:DWORD dst_unused:UNUSED_PAD src0_sel:DWORD src1_sel:BYTE_3\n\t" \
  "v_lshlrev_b32_sdwa v222, 2, v222 dst_sel:DWORD dst_unused:UNUSED_PAD src0_sel:DWORD src1_sel:BYTE_1\n\t" \
  "v_and_b32_sdwa v225, s56, v224 dst_sel:DWORD dst_unused:UNUSED_PAD src0_sel:DWORD src1_sel:WORD_1\n\t" \
  "v_mfma_f32_16x16x32_bf16 v[148:151], v[212:215], v[112:115], v[148:151]\n\t" \
  "ds_read_b64_tr_b16 v[210:211], v249 offset:32288\n\t" \
  "v_and_b32 v224, s56, v224\n\t" \
  "v_lshlrev_b32_sdwa v227, 2, v226 dst_sel:DWORD dst_unused:UNUSED_PAD src0_sel:DWORD src1_sel:BYTE_3\n\t" \
  "v_mfma_f32_16x16x32_bf16 v[156:159], v[212:215], v[116:119], v[156:159]\n\t" \
  "s_cmp_eq_u32 s37, 0\n\t" \
  "s_cbranch_scc1 .Lcnx_f7\n.Lcnx_g7:\n\t" \
  "ds_read_b32 v221, v221 offset:0\n\t" \
  "ds_read_b32 v220, v220 offset:0\n\t" \
  "v_lshlrev_b32_sdwa v226, 2, v226 dst_sel:DWORD dst_unused:UNUSED_PAD src0_sel:DWORD src1_sel:BYTE_1\n\t" \
  "v_fma_f32 v197, |v197|, v229, v197 mul:4\n\t" \
  "v_mfma_f32_16x16x32_bf16 v[164:167], v[212:215], v[120:123], v[164:167]\n\t" \
  "ds_read_b32 v223, v223 offset:15376\n\t" \
  "ds_read_b32 v222, v222 offset:15376\n\t" \
  "v_fma_f32 v196, |v196|, v228, v196 mul:4\n\t" \
  "v_fma_mix_f32 v231, |v199|, v231, v231 op_sel:[0,1,0] op_sel_hi:[0,1,1] clamp\n\t" \
  "v_mfma_f32_16x16x32_bf16 v[172:175], v[212:215], v[124:127], v[172:175]\n\t" \
  "ds_read_b64_tr_b16 v[212:213], v249 offset:30760\n\t" \
  "ds_read_b64_tr_b16 v[214:215], v249 offset:32296\n\t" \
  "v_fma_mix_f32 v230, |v198|, v230, v230 op_sel:[0,1,0] op_sel_hi:[0,1,1] clamp\n\t" \
  "s_waitcnt lgkmcnt(8)\n\t" \
  "v_fma_f32 v205, |v205|, v233, v205 mul:4\n\t" \
  "v_mfma_f32_16x16x32_bf16 v[148:151], v[216:219], v[128:131], v[148:151]\n\t" \
  "ds_read_b32 v225, v225 offset:0\n\t" \
  "v_fma_f32 v204, |v204|, v232, v204 mul:4\n\t" \
  "v_fma_f32 v207, |v207|, v235, v207 mul:4\n\t" \
  "v_mfma_f32_16x16x32_bf16 v[156:159], v[216:219], v[132:135], v[156:159]\n\t" \
  "ds_read_b32 v224, v224 offset:0\n\t" \
  "v_fma_f32 v206, |v206|, v234, v206 mul:4\n\t" \
  "v_fma_f32 v199, |v199|, v231, v199 mul:4\n\t" \
  "v_mfma_f32_16x16x32_bf16 v[164:167], v[216:219], v[136:139], v[164:167]\n\t" \
  "ds_read_b32 v227, v227 offset:15376\n\t" \
  "v_fma_f32 v198, |v198|, v230, v198 mul:4\n\t" \
  "v_cvt_pk_bf16_f32 v196, v196, v197\n\t" \
  "v_mfma_f32_16x16x32_bf16 v[172:175], v[216:219], v[140:143], v[172:175]\n\t" \
  "ds_read_b64_tr_b16 v[216:217], v249 offset:30816\n\t" \
  "v_cvt_pk_bf16_f32 v197, v198, v199\n\t" \
  "v_cvt_pk_bf16_f32 v204, v204, v205\n\t" \
  "v_cvt_pk_bf16_f32 v205, v206, v207\n\t" \
  "s_setprio 0\n\t" \
  "v_cvt_pk_f16_f32 v228, |v162|, |v163| clamp\n\t" \
  "v_cvt_pknorm_i16_f32 v230, |v160|, |v161|\n\t" \
  "v_mfma_f32_16x16x32_bf16 v[40:43], v[242:245], v[236:239], v[40:43]\n\t" \
  "ds_read_b64_tr_b16 v[218:219], v249 offset:32352\n\t" \
  "v_cvt_pk_f16_f32 v232, |v170|, |v171| clamp\n\t" \
  "v_cvt_pk_f16_f32 v234, |v168|, |v169| clamp\n\t" \
  "v_mfma_f32_16x16x32_bf16 v[44:47], v[240:243], v[236:239], v[44:47]\n\t" \
  "buffer_load_dwordx4 v[236:239], v250, s[8:11], s12 offen offset:0 nt\n\t" \
  "s_cmp_lg_u32 s58, 0\n\t" \
  "s_cbranch_scc1 .Lcnx_c26\n\t" \
  "s_add_u32 m0, s25, 41000\n\t" \
  "s_add_u32 s24, s24, s26\n\t" \
  "buffer_load_dwordx4 v254, s[8:11], s24 offen lds\n\t" \
  "s_cmp_lg_u32 s38, 4\n\t" \
  "s_cbranch_scc1 .Lcnx_c26\n\t" \
  "s_mov_b32 m0, 163632\n\t" \
  "s_add_u32 s30, s30, s31\n\t" \
  "buffer_load_dword v252, s[8:11], s30 offen lds\n\t" \
  ".Lcnx_c26:\n\t" \
  "ds_read_b32 v226, v226 offset:15376\n\t" \
  "v_and_b32_sdwa v229, s56, v228 dst_sel:DWORD dst_unused:UNUSED_PAD src0_sel:DWORD src1_sel:WORD_1\n\t" \
  "v_and_b32 v228, s56, v228\n\t" \
  "s_waitcnt lgkmcnt(12)\n\t" \
  "v_mfma_f32_16x16x32_bf16 v[0:3], v[208:211], v[178:181], v[0:3]\n\t" \
  "ds_read_b32 v229, v229 offset:0\n\t" \
  "v_lshlrev_b32_sdwa v231, 2, v230 dst_sel:DWORD dst_unused:UNUSED_PAD src0_sel:DWORD src1_sel:BYTE_3\n\t" \
  "v_lshlrev_b32_sdwa v230, 2, v230 dst_sel:DWORD dst_unused:UNUSED_PAD src0_sel:DWORD src1_sel:BYTE_1\n\t" \
  "v_mfma_f32_16x16x32_bf16 v[24:27], v[208:211], v[186:189], v[24:27]\n\t" \
  "ds_read_b32 v228, v228 offset:0\n\t" \
  "v_and_b32_sdwa v233, s56, v232 dst_sel:DWORD dst_unused:UNUSED_PAD src0_sel:DWORD src1_sel:WORD_1\n\t" \
  "v_and_b32 v232, s56, v232\n\t" \
  "v_mfma_f32_16x16x32_bf16 v[48:51], v[208:211], v[194:197], v[48:51]\n\t" \
  "ds_read_b32 v231, v231 offset:15376\n\t" \
  "v_and_b32_sdwa v235, s56, v234 dst_sel:DWORD dst_unused:UNUSED_PAD src0_sel:DWORD src1_sel:WORD_1\n\t" \
  "v_and_b32 v234, s56, v234\n\t" \
  "v_mfma_f32_16x16x32_bf16 v[72:75], v[208:211], v[202:205], v[72:75]\n\t" \
  "ds_read_b64_tr_b16 v[208:209], v249 offset:30824\n\t" \
  "ds_read_b64_tr_b16 v[210:211], v249 offset:32360\n\t" \
  "s_waitcnt lgkmcnt(13)\n\t" \
  "v_fma_f32 v147, |v147|, v221, v147 mul:4\n\t" \
  "v_fma_f32 v146, |v146|, v220, v146 mul:4\n\t" \
  "s_waitcnt lgkmcnt(11)\n\t" \
  "v_mfma_f32_16x16x32_bf16 v[4:7], v[212:215], v[178:181], v[4:7]\n\t" \
  "ds_read_b32 v230, v230 offset:15376\n\t" \
  "v_fma_mix_f32 v223, |v145|, v223, v223 op_sel:[0,1,0] op_sel_hi:[0,1,1] clamp\n\t" \
  "v_fma_mix_f32 v222, |v144|, v222, v222 op_sel:[0,1,0] op_sel_hi:[0,1,1] clamp\n\t" \
  "v_mfma_f32_16x16x32_bf16 v[28:31], v[212:215], v[186:189], v[28:31]\n\t" \
  "ds_read_b32 v233, v233 offset:0\n\t" \
  "s_waitcnt lgkmcnt(7)\n\t" \
  "v_fma_f32 v155, |v155|, v225, v155 mul:4\n\t" \
  "v_fma_f32 v154, |v154|, v224, v154 mul:4\n\t" \
  "v_mfma_f32_16x16x32_bf16 v[52:55], v[212:215], v[194:197], v[52:55]\n\t" \
  "ds_read_b32 v232, v232 offset:0\n\t" \
  "v_fma_mix_f32 v227, |v153|, v227, v227 op_sel:[0,1,0] op_sel_hi:[0,1,1] clamp\n\t" \
  "v_fma_mix_f32 v226, |v152|, v226, v226 op_sel:[0,1,0] op_sel_hi:[0,1,1] clamp\n\t" \
  "v_fma_f32 v145, |v145|, v223, v145 mul:4\n\t" \
  "v_mfma_f32_16x16x32_bf16 v[76:79], v[212:215], v[202:205], v[76:79]\n\t" \
  "ds_read_b64_tr_b16 v[212:213], v249 offset:30880\n\t" \
  "v_fma_f32 v144, |v144|, v222, v144 mul:4\n\t" \
  "v_fma_f32 v153, |v153|, v227, v153 mul:4\n\t" \
  "v_mfma_f32_16x16x32_bf16 v[8:11], v[216:219], v[178:181], v[8:11]\n\t" \
  "ds_read_b64_tr_b16 v[214:215], v249 offset:32416\n\t" \
  "v_fma_f32 v152, |v152|, v226, v152 mul:4\n\t" \
  "v_cvt_pk_bf16_f32 v147, v146, v147\n\t" \
  "v_mfma_f32_16x16x32_bf16 v[32:35], v[216:219], v[186:189], v[32:35]\n\t" \
  "ds_read_b32 v235, v235 offset:0\n\t" \
  "v_cvt_pk_bf16_f32 v146, v144, v145\n\t" \
  "v_cvt_pk_bf16_f32 v155, v154, v155\n\t" \
  "v_mfma_f32_16x16x32_bf16 v[56:59], v[216:219], v[194:197], v[56:59]\n\t" \
  "ds_read_b32 v234, v234 offset:0\n\t" \
  "v_cvt_pk_bf16_f32 v154, v152, v153\n\t" \
  "s_setprio 1\n\t" \
  "v_cvt_pk_f16_f32 v220, |v148|, |v149| clamp\n\t" \
  "v_mfma_f32_16x16x32_bf16 v[80:83], v[216:219], v[202:205], v[80:83]\n\t" \
  "ds_read_b64_tr_b16 v[216:217], v249 offset:30888\n\t" \
  "v_cvt_pknorm_i16_f32 v222, |v150|, |v151|\n\t" \
  "v_cvt_pk_f16_f32 v224, |v156|, |v157| clamp\n\t" \
  "s_waitcnt lgkmcnt(8)\n\t" \
  "v_mfma_f32_16x16x32_bf16 v[12:15], v[208:211], v[178:181], v[12:15]\n\t" \
  "ds_read_b64_tr_b16 v[218:219], v249 offset:32424\n\t" \
  "v_cvt_pknorm_i16_f32 v226, |v158|, |v159|\n\t" \
  "v_and_b32_sdwa v221, s56, v220 dst_sel:DWORD dst_unused:UNUSED_PAD src0_sel:DWORD src1_sel:WORD_1\n\t" \
  "v_mfma_f32_16x16x32_bf16 v[36:39], v[208:211], v[186:189], v[36:39]\n\t" \
  "v_and_b32 v220, s56, v220\n\t" \
  "v_lshlrev_b32_sdwa v223, 2, v222 dst_sel:DWORD dst_unused:UNUSED_PAD src0_sel:DWORD src1_sel:BYTE_3\n\t" \
  "v_mfma_f32_16x16x32_bf16 v[60:63], v[208:211], v[194:197], v[60:63]\n\t" \
  "ds_read_b32 v221, v221 offset:0\n\t" \
  "v_lshlrev_b32_sdwa v222, 2, v222 dst_sel:DWORD dst_unused:UNUSED_PAD src0_sel:DWORD src1_sel:BYTE_1\n\t" \
  "v_and_b32_sdwa v225, s56, v224 dst_sel:DWORD dst_unused:UNUSED_PAD src0_sel:DWORD src1_sel:WORD_1\n\t" \
  "v_and_b32 v224, s56, v224\n\t" \
  "v_mfma_f32_16x16x32_bf16 v[84:87], v[208:211], v[202:205], v[84:87]\n\t" \
  "ds_read_b64_tr_b16 v[208:209], v248 offset:0\n\t" \
  "ds_read_b64_tr_b16 v[210:211], v248 offset:1024\n\t" \
  "v_lshlrev_b32_sdwa v227, 2, v226 dst_sel:DWORD dst_unused:UNUSED_PAD src0_sel:DWORD src1_sel:BYTE_3\n\t" \
  "v_lshlrev_b32_sdwa v226, 2, v226 dst_sel:DWORD dst_unused:UNUSED_PAD src0_sel:DWORD src1_sel:BYTE_1\n\t" \
  "s_waitcnt lgkmcnt(7)\n\t" \
  "v_mfma_f32_16x16x32_bf16 v[16:19], v[212:215], v[178:181], v[16:19]\n\t" \
  "ds_read_b32 v220, v220 offset:0\n\t" \
  "ds_read_b32 v223, v223 offset:15376\n\t" \
  "v_fma_f32 v163, |v163|, v229, v163 mul:4\n\t" \
  "v_fma_f32 v162, |v162|, v228, v162 mul:4\n\t" \
  "v_mfma_f32_16x16x32_bf16 v[40:43], v[212:215], v[186:189], v[40:43]\n\t" \
  "ds_read_b32 v222, v222 offset:15376\n\t" \
  "v_fma_mix_f32 v231, |v161|, v231, v231 op_sel:[0,1,0] op_sel_hi:[0,1,1] clamp\n\t" \
  "v_fma_mix_f32 v230, |v160|, v230, v230 op_sel:[0,1,0] op_sel_hi:[0,1,1] clamp\n\t" \
  "v_mfma_f32_16x16x32_bf16 v[64:67], v[212:215], v[194:197], v[64:67]\n\t" \
  "ds_read_b32 v225, v225 offset:0\n\t" \
  "s_waitcnt lgkmcnt(9)\n\t" \
  "v_fma_f32 v171, |v171|, v233, v171 mul:4\n\t" \
  "v_fma_f32 v170, |v170|, v232, v170 mul:4\n\t" \
  "v_mfma_f32_16x16x32_bf16 v[88:91], v[212:215], v[202:205], v[88:91]\n\t" \
  "ds_read_b64_tr_b16 v[212:213], v248 offset:3072\n\t" \
  "v_fma_f32 v169, |v169|, v235, v169 mul:4\n\t" \
  "v_fma_f32 v168, |v168|, v234, v168 mul:4\n\t" \
  "s_waitcnt lgkmcnt(8)\n\t" \
  "v_mfma_f32_16x16x32_bf16 v[20:23], v[216:219], v[178:181], v[20:23]\n\t" \
  "ds_read_b64_tr_b16 v[214:215], v248 offset:4096\n\t" \
  "v_fma_f32 v161, |v161|, v231, v161 mul:4\n\t" \
  "v_fma_f32 v160, |v160|, v230, v160 mul:4\n\t" \
  "v_mfma_f32_16x16x32_bf16 v[44:47], v[216:219], v[186:189], v[44:47]\n\t" \
  "ds_read_b32 v224, v224 offset:0\n\t" \
  "v_cvt_pk_bf16_f32 v163, v162, v163\n\t" \
  "v_cvt_pk_bf16_f32 v162, v160, v161\n\t" \
  "v_mfma_f32_16x16x32_bf16 v[68:71], v[216:219], v[194:197], v[68:71]\n\t" \
  "ds_read_b32 v227, v227 offset:15376\n\t" \
  "v_cvt_pk_bf16_f32 v171, v170, v171\n\t" \
  "v_cvt_pk_bf16_f32 v170, v168, v169\n\t" \
  "v_cvt_pk_f16_f32 v228, |v164|, |v165| clamp\n\t" \
  "v_mfma_f32_16x16x32_bf16 v[92:95], v[216:219], v[202:205], v[92:95]\n\t" \
  "ds_read_b64_tr_b16 v[216:217], v248 offset:2048\n\t" \
  "v_cvt_pknorm_i16_f32 v230, |v166|, |v167|\n\t" \
  "s_setprio 2\n\t" \
  "v_cvt_pk_f16_f32 v232, |v172|, |v173| clamp\n\t" \
  "s_waitcnt lgkmcnt(9)\n\t" \
  "v_mfma_f32_16x16x32_bf16 v[176:179], v[208:211], v[96:99], 0\n\t" \
  "ds_read_b64_tr_b16 v[218:219], v248 offset:5120\n\t" \
  "v_cvt_pk_f16_f32 v234, |v174|, |v175| clamp\n\t" \
  "v_and_b32_sdwa v229, s56, v228 dst_sel:DWORD dst_unused:UNUSED_PAD src0_sel:DWORD src1_sel:WORD_1\n\t" \
  "v_mfma_f32_16x16x32_bf16 v[184:187], v[208:211], v[100:103], 0\n\t" \
  "ds_read_b32 v226, v226 offset:15376\n\t" \
  "v_and_b32 v228, s56, v228\n\t" \
  "v_lshlrev_b32_sdwa v231, 2, v230 dst_sel:DWORD dst_unused:UNUSED_PAD src0_sel:DWORD src1_sel:BYTE_3\n\t" \
  "v_mfma_f32_16x16x32_bf16 v[192:195], v[208:211], v[104:107], 0\n\t" \
  "ds_read_b32 v229, v229 offset:0\n\t" \
  "v_lshlrev_b32_sdwa v230, 2, v230 dst_sel:DWORD dst_unused:UNUSED_PAD src0_sel:DWORD src1_sel:BYTE_1\n\t" \
  "v_and_b32_sdwa v233, s56, v232 dst_sel:DWORD dst_unused:UNUSED_PAD src0_sel:DWORD src1_sel:WORD_1\n\t" \
  "v_mfma_f32_16x16x32_bf16 v[200:203], v[208:211], v[108:111], 0\n\t" \
  "ds_read_b64_tr_b16 v[208:209], v248 offset:512\n\t" \
  "v_and_b32 v232, s56, v232\n\t" \
  "v_and_b32_sdwa v235, s56, v234 dst_sel:DWORD dst_unused:UNUSED_PAD src0_sel:DWORD src1_sel:WORD_1\n\t" \
  "s_waitcnt lgkmcnt(7)\n\t" \
  "v_mfma_f32_16x16x32_bf16 v[176:179], v[212:215], v[112:115], v[176:179]\n\t" \
  "ds_read_b64_tr_b16 v[210:211], v248 offset:1536\n\t" \
  "ds_read_b32 v228, v228 offset:0\n\t" \
  "v_and_b32 v234, s56, v234\n\t" \
  "v_fma_f32 v149, |v149|, v221, v149 mul:4\n\t" \
  "v_mfma_f32_16x16x32_bf16 v[184:187], v[212:215], v[116:119], v[184:187]\n\t" \
  "ds_read_b32 v231, v231 offset:15376\n\t" \
  "ds_read_b32 v230, v230 offset:15376\n\t" \
  "v_fma_f32 v148, |v148|, v220, v148 mul:4\n\t" \
  "v_fma_mix_f32 v223, |v151|, v223, v223 op_sel:[0,1,0] op_sel_hi:[0,1,1] clamp\n\t" \
  "v_mfma_f32_16x16x32_bf16 v[192:195], v[212:215], v[120:123], v[192:195]\n\t" \
  "ds_read_b32 v233, v233 offset:0\n\t" \
  "v_fma_mix_f32 v222, |v150|, v222, v222 op_sel:[0,1,0] op_sel_hi:[0,1,1] clamp\n\t" \
  "s_waitcnt lgkmcnt(7)\n\t" \
  "v_fma_f32 v157, |v157|, v225, v157 mul:4\n\t" \
  "v_mfma_f32_16x16x32_bf16 v[200:203], v[212:215], v[124:127], v[200:203]\n\t" \
  "ds_read_b64_tr_b16 v[212:213], v248 offset:3584\n\t" \
  "v_fma_f32 v156, |v156|, v224, v156 mul:4\n\t" \
  "v_fma_mix_f32 v227, |v159|, v227, v227 op_sel:[0,1,0] op_sel_hi:[0,1,1] clamp\n\t" \
  "v_fma_mix_f32 v226, |v158|, v226, v226 op_sel:[0,1,0] op_sel_hi:[0,1,1] clamp\n\t" \
  "v_mfma_f32_16x16x32_bf16 v[176:179], v[216:219], v[128:131], v[176:179]\n\t" \
  "ds_read_b64_tr_b16 v[214:215], v248 offset:4608\n\t" \
  "v_fma_f32 v151, |v151|, v223, v151 mul:4\n\t" \
  "v_fma_f32 v150, |v150|, v222, v150 mul:4\n\t" \
  "v_mfma_f32_16x16x32_bf16 v[184:187], v[216:219], v[132:135], v[184:187]\n\t" \
  "ds_read_b32 v232, v232 offset:0\n\t" \
  "v_fma_f32 v159, |v159|, v227, v159 mul:4\n\t" \
  "v_fma_f32 v158, |v158|, v226, v158 mul:4\n\t" \
  "v_mfma_f32_16x16x32_bf16 v[192:195], v[216:219], v[136:139], v[192:195]\n\t" \
  "ds_read_b32 v235, v235 offset:0\n\t" \
  "v_cvt_pk_bf16_f32 v148, v148, v149\n\t" \
  "v_cvt_pk_bf16_f32 v149, v150, v151\n\t" \
  "v_mfma_f32_16x16x32_bf16 v[200:203], v[216:219], v[140:143], v[200:203]\n\t" \
  "ds_read_b64_tr_b16 v[216:217], v248 offset:2560\n\t" \
  "v_cvt_pk_bf16_f32 v156, v156, v157\n\t" \
  "v_cvt_pk_bf16_f32 v157, v158, v159\n\t" \
  "s_waitcnt lgkmcnt(9)\n\t" \
  "v_mfma_f32_16x16x32_bf16 v[180:183], v[208:211], v[96:99], 0\n\t" \
  "ds_read_b64_tr_b16 v[218:219], v248 offset:5632\n\t" \
  "v_cvt_pk_f16_f32 v220, |v178|, |v179| clamp\n\t" \
  "s_setprio 3\n\t" \
  "v_cvt_pknorm_i16_f32 v222, |v176|, |v177|\n\t" \
  "v_mfma_f32_16x16x32_bf16 v[188:191], v[208:211], v[100:103], 0\n\t" \
  "s_waitcnt vmcnt(1)\n\t" \
  "s_cmp_lg_u32 s58, 1\n\t" \
  "s_cbranch_scc1 .Lcnx_c14\n\t" \
  "s_waitcnt vmcnt(0)\n\t" \
  ".Lcnx_c14:\n\t" \
  "s_mov_b64 exec, 3\n\t" \
  "ds_add_rtn_u32 v246, v251, v250 offset:47192\n\t" \
  "s_mov_b64 exec, -1\n\t" \
  "ds_read_b32 v234, v234 offset:0\n\t" \
  "v_cvt_pk_f16_f32 v224, |v186|, |v187| clamp\n\t" \
  "v_cvt_pknorm_i16_f32 v226, |v184|, |v185|\n\t" \
  "v_mfma_f32_16x16x32_bf16 v[196:199], v[208:211], v[104:107], 0\n\t" \
  "v_and_b32_sdwa v221, s56, v220 dst_sel:DWORD dst_unused:UNUSED_PAD src0_sel:DWORD src1_sel:WORD_1\n\t" \
  "v_and_b32 v220, s56, v220\n\t" \
  "v_mfma_f32_16x16x32_bf16 v[204:207], v[208:211], v[108:111], 0\n\t" \
  "s_waitcnt lgkmcnt(1)\n\t" \
  "v_readfirstlane_b32 s4, v246\n\t" \
  "s_cmp_lt_u32 s4, s57\n\t" \
  "s_cbranch_scc1 .Lcnx_w84\n.Lcnx_b84:\n\t" \
  "s_or_b32 s37, s37, s58\n\t" \
  "ds_read_b64_tr_b16 v[208:209], v249 offset:6152\n\t" \
  "v_lshlrev_b32_sdwa v223, 2, v222 dst_sel:DWORD dst_unused:UNUSED_PAD src0_sel:DWORD src1_sel:BYTE_3\n\t" \
  "v_lshlrev_b32_sdwa v222, 2, v222 dst_sel:DWORD dst_unused:UNUSED_PAD src0_sel:DWORD src1_sel:BYTE_1\n\t" \
  "v_and_b32_sdwa v225, s56, v224 dst_sel:DWORD dst_unused:UNUSED_PAD src0_sel:DWORD src1_sel:WORD_1\n\t" \
  "v_mfma_f32_16x16x32_bf16 v[180:183], v[212:215], v[112:115], v[180:183]\n\t" \
  "ds_read_b64_tr_b16 v[210:211], v249 offset:7688\n\t" \
  "v_and_b32 v224, s56, v224\n\t" \
  "v_lshlrev_b32_sdwa v227, 2, v226 dst_sel:DWORD dst_unused:UNUSED_PAD src0_sel:DWORD src1_sel:BYTE_3\n\t" \
  "v_mfma_f32_16x16x32_bf16 v[188:191], v[212:215], v[116:119], v[188:191]\n\t" \
  "s_cmp_eq_u32 s37, 0\n\t" \
  "s_cbranch_scc1 .Lcnx_f8\n.Lcnx_g8:\n\t" \
  "ds_read_b32 v221, v221 offset:0\n\t" \
  "ds_read_b32 v220, v220 offset:0\n\t" \
  "v_lshlrev_b32_sdwa v226, 2, v226 dst_sel:DWORD dst_unused:UNUSED_PAD src0_sel:DWORD src1_sel:BYTE_1\n\t" \
  "v_fma_f32 v165, |v165|, v229, v165 mul:4\n\t" \
  "v_mfma_f32_16x16x32_bf16 v[196:199], v[212:215], v[120:123], v[196:199]\n\t" \
  "ds_read_b32 v223, v223 offset:15376\n\t" \
  "ds_read_b32 v222, v222 offset:15376\n\t" \
  "v_fma_f32 v164, |v164|, v228, v164 mul:4\n\t" \
  "v_fma_mix_f32 v231, |v167|, v231, v231 op_sel:[0,1,0] op_sel_hi:[0,1,1] clamp\n\t" \
  "v_mfma_f32_16x16x32_bf16 v[204:207], v[212:215], v[124:127], v[204:207]\n\t" \
  "ds_read_b64_tr_b16 v[212:213], v249 offset:6160\n\t" \
  "ds_read_b64_tr_b16 v[214:215], v249 offset:7696\n\t" \
  "v_fma_mix_f32 v230, |v166|, v230, v230 op_sel:[0,1,0] op_sel_hi:[0,1,1] clamp\n\t" \
  "s_waitcnt lgkmcnt(8)\n\t" \
  "v_fma_f32 v173, |v173|, v233, v173 mul:4\n\t" \
  "v_mfma_f32_16x16x32_bf16 v[180:183], v[216:219], v[128:131], v[180:183]\n\t" \
  "ds_read_b32 v225, v225 offset:0\n\t" \
  "v_fma_f32 v172, |v172|, v232, v172 mul:4\n\t" \
  "v_fma_f32 v175, |v175|, v235, v175 mul:4\n\t" \
  "v_mfma_f32_16x16x32_bf16 v[188:191], v[216:219], v[132:135], v[188:191]\n\t" \
  "ds_read_b32 v224, v224 offset:0\n\t" \
  "v_fma_f32 v174, |v174|, v234, v174 mul:4\n\t" \
  "v_fma_f32 v167, |v167|, v231, v167 mul:4\n\t" \
  "v_mfma_f32_16x16x32_bf16 v[196:199], v[216:219], v[136:139], v[196:199]\n\t" \
  "ds_read_b32 v227, v227 offset:15376\n\t" \
  "v_fma_f32 v166, |v166|, v230, v166 mul:4\n\t" \
  "v_cvt_pk_bf16_f32 v164, v164, v165\n\t" \
  "v_mfma_f32_16x16x32_bf16 v[204:207], v[216:219], v[140:143], v[204:207]\n\t" \
  "ds_read_b64_tr_b16 v[216:217], v249 offset:6216\n\t" \
  "v_cvt_pk_bf16_f32 v165, v166, v167\n\t" \
  "v_cvt_pk_bf16_f32 v172, v172, v173\n\t" \
  "v_cvt_pk_bf16_f32 v173, v174, v175\n\t" \
  "s_setprio 0\n\t" \
  "v_cvt_pk_f16_f32 v228, |v194|, |v195| clamp\n\t" \
  "v_cvt_pknorm_i16_f32 v230, |v192|, |v193|\n\t" \
  "v_mfma_f32_16x16x32_bf16 v[48:51], v[242:245], v[236:239], v[48:51]\n\t" \
  "ds_read_b64_tr_b16 v[218:219], v249 offset:7752\n\t" \
  "v_cvt_pk_f16_f32 v232, |v202|, |v203| clamp\n\t" \
  "v_cvt_pk_f16_f32 v234, |v200|, |v201| clamp\n\t" \
  "v_mfma_f32_16x16x32_bf16 v[52:55], v[240:243], v[236:239], v[52:55]\n\t" \
  "buffer_load_dwordx4 v[236:239], v250, s[8:11], s12 offen offset:64 nt\n\t" \
  "s_cmp_lg_u32 s58, 0\n\t" \
  "s_cbranch_scc1 .Lcnx_c27\n\t" \
  "s_add_u32 m0, s25, 49200\n\t" \
  "s_add_u32 s24, s24, s26\n\t" \
  "buffer_load_dwordx4 v254, s[8:11], s24 offen lds\n\t" \
  "s_cmp_lg_u32 s38, 5\n\t" \
  "s_cbranch_scc1 .Lcnx_c27\n\t" \
  "s_mov_b32 m0, 163632\n\t" \
  "s_add_u32 s30, s30, s31\n\t" \
  "buffer_load_dword v252, s[8:11], s30 offen lds\n\t" \
  ".Lcnx_c27:\n\t" \
  "ds_read_b32 v226, v226 offset:15376\n\t" \
  "v_and_b32_sdwa v229, s56, v228 dst_sel:DWORD dst_unused:UNUSED_PAD src0_sel:DWORD src1_sel:WORD_1\n\t" \
  "v_and_b32 v228, s56, v228\n\t" \
  "s_waitcnt lgkmcnt(12)\n\t" \
  "v_mfma_f32_16x16x32_bf16 v[0:3], v[208:211], v[146:149], v[0:3]\n\t" \
  "ds_read_b32 v229, v229 offset:0\n\t" \
  "v_lshlrev_b32_sdwa v231, 2, v230 dst_sel:DWORD dst_unused:UNUSED_PAD src0_sel:DWORD src1_sel:BYTE_3\n\t" \
  "v_lshlrev_b32_sdwa v230, 2, v230 dst_sel:DWORD dst_unused:UNUSED_PAD src0_sel:DWORD src1_sel:BYTE_1\n\t" \
  "v_mfma_f32_16x16x32_bf16 v[24:27], v[208:211], v[154:157], v[24:27]\n\t" \
  "ds_read_b32 v228, v228 offset:0\n\t" \
  "v_and_b32_sdwa v233, s56, v232 dst_sel:DWORD dst_unused:UNUSED_PAD src0_sel:DWORD src1_sel:WORD_1\n\t" \
  "v_and_b32 v232, s56, v232\n\t" \
  "v_mfma_f32_16x16x32_bf16 v[48:51], v[208:211], v[162:165], v[48:51]\n\t" \
  "ds_read_b32 v231, v231 offset:15376\n\t" \
  "v_and_b32_sdwa v235, s56, v234 dst_sel:DWORD dst_unused:UNUSED_PAD src0_sel:DWORD src1_sel:WORD_1\n\t" \
  "v_and_b32 v234, s56, v234\n\t" \
  "v_mfma_f32_16x16x32_bf16 v[72:75], v[208:211], v[170:173], v[72:75]\n\t" \
  "ds_read_b64_tr_b16 v[208:209], v249 offset:6224\n\t" \
  "ds_read_b64_tr_b16 v[210:211], v249 offset:7760\n\t" \
  "s_waitcnt lgkmcnt(13)\n\t" \
  "v_fma_f32 v179, |v179|, v221, v179 mul:4\n\t" \
  "v_fma_f32 v178, |v178|, v220, v178 mul:4\n\t" \
  "s_waitcnt lgkmcnt(11)\n\t" \
  "v_mfma_f32_16x16x32_bf16 v[4:7], v[212:215], v[146:149], v[4:7]\n\t" \
  "ds_read_b32 v230, v230 offset:15376\n\t" \
  "v_fma_mix_f32 v223, |v177|, v223, v223 op_sel:[0,1,0] op_sel_hi:[0,1,1] clamp\n\t" \
  "v_fma_mix_f32 v222, |v176|, v222, v222 op_sel:[0,1,0] op_sel_hi:[0,1,1] clamp\n\t" \
  "v_mfma_f32_16x16x32_bf16 v[28:31], v[212:215], v[154:157], v[28:31]\n\t" \
  "ds_read_b32 v233, v233 offset:0\n\t" \
  "s_waitcnt lgkmcnt(7)\n\t" \
  "v_fma_f32 v187, |v187|, v225, v187 mul:4\n\t" \
  "v_fma_f32 v186, |v186|, v224, v186 mul:4\n\t" \
  "v_mfma_f32_16x16x32_bf16 v[52:55], v[212:215], v[162:165], v[52:55]\n\t" \
  "ds_read_b32 v232, v232 offset:0\n\t" \
  "v_fma_mix_f32 v227, |v185|, v227, v227 op_sel:[0,1,0] op_sel_hi:[0,1,1] clamp\n\t" \
  "v_fma_mix_f32 v226, |v184|, v226, v226 op_sel:[0,1,0] op_sel_hi:[0,1,1] clamp\n\t" \
  "v_fma_f32 v177, |v177|, v223, v177 mul:4\n\t" \
  "v_mfma_f32_16x16x32_bf16 v[76:79], v[212:215], v[170:173], v[76:79]\n\t" \
  "ds_read_b64_tr_b16 v[212:213], v249 offset:6280\n\t" \
  "v_fma_f32 v176, |v176|, v222, v176 mul:4\n\t" \
  "v_fma_f32 v185, |v185|, v227, v185 mul:4\n\t" \
  "v_mfma_f32_16x16x32_bf16 v[8:11], v[216:219], v[146:149], v[8:11]\n\t" \
  "ds_read_b64_tr_b16 v[214:215], v249 offset:7816\n\t" \
  "v_fma_f32 v184, |v184|, v226, v184 mul:4\n\t" \
  "v_cvt_pk_bf16_f32 v179, v178, v179\n\t" \
  "v_mfma_f32_16x16x32_bf16 v[32:35], v[216:219], v[154:157], v[32:35]\n\t" \
  "ds_read_b32 v235, v235 offset:0\n\t" \
  "v_cvt_pk_bf16_f32 v178, v176, v177\n\t" \
  "v_cvt_pk_bf16_f32 v187, v186, v187\n\t" \
  "v_mfma_f32_16x16x32_bf16 v[56:59], v[216:219], v[162:165], v[56:59]\n\t" \
  "ds_read_b32 v234, v234 offset:0\n\t" \
  "v_cvt_pk_bf16_f32 v186, v184, v185\n\t" \
  "s_setprio 1\n\t" \
  "v_cvt_pk_f16_f32 v220, |v180|, |v181| clamp\n\t" \
  "v_mfma_f32_16x16x32_bf16 v[80:83], v[216:219], v[170:173], v[80:83]\n\t" \
  "ds_read_b64_tr_b16 v[216:217], v249 offset:6288\n\t" \
  "v_cvt_pknorm_i16_f32 v222, |v182|, |v183|\n\t" \
  "v_cvt_pk_f16_f32 v224, |v188|, |v189| clamp\n\t" \
  "s_waitcnt lgkmcnt(8)\n\t" \
  "v_mfma_f32_16x16x32_bf16 v[12:15], v[208:211], v[146:149], v[12:15]\n\t" \
  "ds_read_b64_tr_b16 v[218:219], v249 offset:7824\n\t" \
  "v_cvt_pknorm_i16_f32 v226, |v190|, |v191|\n\t" \
  "v_and_b32_sdwa v221, s56, v220 dst_sel:DWORD dst_unused:UNUSED_PAD src0_sel:DWORD src1_sel:WORD_1\n\t" \
  "v_mfma_f32_16x16x32_bf16 v[36:39], v[208:211], v[154:157], v[36:39]\n\t" \
  "v_and_b32 v220, s56, v220\n\t" \
  "v_lshlrev_b32_sdwa v223, 2, v222 dst_sel:DWORD dst_unused:UNUSED_PAD src0_sel:DWORD src1_sel:BYTE_3\n\t" \
  "v_mfma_f32_16x16x32_bf16 v[60:63], v[208:211], v[162:165], v[60:63]\n\t" \
  "ds_read_b32 v221, v221 offset:0\n\t" \
  "v_lshlrev_b32_sdwa v222, 2, v222 dst_sel:DWORD dst_unused:UNUSED_PAD src0_sel:DWORD src1_sel:BYTE_1\n\t" \
  "v_and_b32_sdwa v225, s56, v224 dst_sel:DWORD dst_unused:UNUSED_PAD src0_sel:DWORD src1_sel:WORD_1\n\t" \
  "v_and_b32 v224, s56, v224\n\t" \
  "v_mfma_f32_16x16x32_bf16 v[84:87], v[208:211], v[170:173], v[84:87]\n\t" \
  "ds_read_b64_tr_b16 v[208:209], v248 offset:43048\n\t" \
  "ds_read_b64_tr_b16 v[210:211], v248 offset:44072\n\t" \
  "v_lshlrev_b32_sdwa v227, 2, v226 dst_sel:DWORD dst_unused:UNUSED_PAD src0_sel:DWORD src1_sel:BYTE_3\n\t" \
  "v_lshlrev_b32_sdwa v226, 2, v226 dst_sel:DWORD dst_unused:UNUSED_PAD src0_sel:DWORD src1_sel:BYTE_1\n\t" \
  "s_waitcnt lgkmcnt(7)\n\t" \
  "v_mfma_f32_16x16x32_bf16 v[16:19], v[212:215], v[146:149], v[16:19]\n\t" \
  "ds_read_b32 v220, v220 offset:0\n\t" \
  "ds_read_b32 v223, v223 offset:15376\n\t" \
  "v_fma_f32 v195, |v195|, v229, v195 mul:4\n\t" \
  "v_fma_f32 v194, |v194|, v228, v194 mul:4\n\t" \
  "v_mfma_f32_16x16x32_bf16 v[40:43], v[212:215], v[154:157], v[40:43]\n\t" \
  "ds_read_b32 v222, v222 offset:15376\n\t" \
  "v_fma_mix_f32 v231, |v193|, v231, v231 op_sel:[0,1,0] op_sel_hi:[0,1,1] clamp\n\t" \
  "v_fma_mix_f32 v230, |v192|, v230, v230 op_sel:[0,1,0] op_sel_hi:[0,1,1] clamp\n\t" \
  "v_mfma_f32_16x16x32_bf16 v[64:67], v[212:215], v[162:165], v[64:67]\n\t" \
  "ds_read_b32 v225, v225 offset:0\n\t" \
  "s_waitcnt lgkmcnt(9)\n\t" \
  "v_fma_f32 v203, |v203|, v233, v203 mul:4\n\t" \
  "v_fma_f32 v202, |v202|, v232, v202 mul:4\n\t" \
  "v_mfma_f32_16x16x32_bf16 v[88:91], v[212:215], v[170:173], v[88:91]\n\t" \
  "ds_read_b64_tr_b16 v[212:213], v248 offset:46120\n\t" \
  "v_fma_f32 v201, |v201|, v235, v201 mul:4\n\t" \
  "v_fma_f32 v200, |v200|, v234, v200 mul:4\n\t" \
  "s_waitcnt lgkmcnt(8)\n\t" \
  "v_mfma_f32_16x16x32_bf16 v[20:23], v[216:219], v[146:149], v[20:23]\n\t" \
  "ds_read_b64_tr_b16 v[214:215], v248 offset:47144\n\t" \
  "v_fma_f32 v193, |v193|, v231, v193 mul:4\n\t" \
  "v_fma_f32 v192, |v192|, v230, v192 mul:4\n\t" \
  "v_mfma_f32_16x16x32_bf16 v[44:47], v[216:219], v[154:157], v[44:47]\n\t" \
  "ds_read_b32 v224, v224 offset:0\n\t" \
  "v_cvt_pk_bf16_f32 v195, v194, v195\n\t" \
  "v_cvt_pk_bf16_f32 v194, v192, v193\n\t" \
  "v_mfma_f32_16x16x32_bf16 v[68:71], v[216:219], v[162:165], v[68:71]\n\t" \
  "ds_read_b32 v227, v227 offset:15376\n\t" \
  "v_cvt_pk_bf16_f32 v203, v202, v203\n\t" \
  "v_cvt_pk_bf16_f32 v202, v200, v201\n\t" \
  "v_cvt_pk_f16_f32 v228, |v196|, |v197| clamp\n\t" \
  "v_mfma_f32_16x16x32_bf16 v[92:95], v[216:219], v[170:173], v[92:95]\n\t" \
  "ds_read_b64_tr_b16 v[216:217], v248 offset:45096\n\t" \
  "v_cvt_pknorm_i16_f32 v230, |v198|, |v199|\n\t" \
  "s_setprio 2\n\t" \
  "v_cvt_pk_f16_f32 v232, |v204|, |v205| clamp\n\t" \
  "s_waitcnt lgkmcnt(9)\n\t" \
  "v_mfma_f32_16x16x32_bf16 v[144:147], v[208:211], v[96:99], 0\n\t" \
  "ds_read_b64_tr_b16 v[218:219], v248 offset:48168\n\t" \
  "v_cvt_pk_f16_f32 v234, |v206|, |v207| clamp\n\t" \
  "v_and_b32_sdwa v229, s56, v228 dst_sel:DWORD dst_unused:UNUSED_PAD src0_sel:DWORD src1_sel:WORD_1\n\t" \
  "v_mfma_f32_16x16x32_bf16 v[152:155], v[208:211], v[100:103], 0\n\t" \
  "ds_read_b32 v226, v226 offset:15376\n\t" \
  "v_and_b32 v228, s56, v228\n\t" \
  "v_lshlrev_b32_sdwa v231, 2, v230 dst_sel:DWORD dst_unused:UNUSED_PAD src0_sel:DWORD src1_sel:BYTE_3\n\t" \
  "v_mfma_f32_16x16x32_bf16 v[160:163], v[208:211], v[104:107], 0\n\t" \
  "ds_read_b32 v229, v229 offset:0\n\t" \
  "v_lshlrev_b32_sdwa v230, 2, v230 dst_sel:DWORD dst_unused:UNUSED_PAD src0_sel:DWORD src1_sel:BYTE_1\n\t" \
  "v_and_b32_sdwa v233, s56, v232 dst_sel:DWORD dst_unused:UNUSED_PAD src0_sel:DWORD src1_sel:WORD_1\n\t" \
  "v_mfma_f32_16x16x32_bf16 v[168:171], v[208:211], v[108:111], 0\n\t" \
  "ds_read_b64_tr_b16 v[208:209], v248 offset:43560\n\t" \
  "v_and_b32 v232, s56, v232\n\t" \
  "v_and_b32_sdwa v235, s56, v234 dst_sel:DWORD dst_unused:UNUSED_PAD src0_sel:DWORD src1_sel:WORD_1\n\t" \
  "s_waitcnt lgkmcnt(7)\n\t" \
  "v_mfma_f32_16x16x32_bf16 v[144:147], v[212:215], v[112:115], v[144:147]\n\t" \
  "ds_read_b64_tr_b16 v[210:211], v248 offset:44584\n\t" \
  "ds_read_b32 v228, v228 offset:0\n\t" \
  "v_and_b32 v234, s56, v234\n\t" \
  "v_fma_f32 v181, |v181|, v221, v181 mul:4\n\t" \
  "v_mfma_f32_16x16x32_bf16 v[152:155], v[212:215], v[116:119], v[152:155]\n\t" \
  "ds_read_b32 v231, v231 offset:15376\n\t" \
  "ds_read_b32 v230, v230 offset:15376\n\t" \
  "v_fma_f32 v180, |v180|, v220, v180 mul:4\n\t" \
  "v_fma_mix_f32 v223, |v183|, v223, v223 op_sel:[0,1,0] op_sel_hi:[0,1,1] clamp\n\t" \
  "v_mfma_f32_16x16x32_bf16 v[160:163], v[212:215], v[120:123], v[160:163]\n\t" \
  "ds_read_b32 v233, v233 offset:0\n\t" \
  "v_fma_mix_f32 v222, |v182|, v222, v222 op_sel:[0,1,0] op_sel_hi:[0,1,1] clamp\n\t" \
  "s_waitcnt lgkmcnt(7)\n\t" \
  "v_fma_f32 v189, |v189|, v225, v189 mul:4\n\t" \
  "v_mfma_f32_16x16x32_bf16 v[168:171], v[212:215], v[124:127], v[168:171]\n\t" \
  "ds_read_b64_tr_b16 v[212:213], v248 offset:46632\n\t" \
  "v_fma_f32 v188, |v188|, v224, v188 mul:4\n\t" \
  "v_fma_mix_f32 v227, |v191|, v227, v227 op_sel:[0,1,0] op_sel_hi:[0,1,1] clamp\n\t" \
  "v_fma_mix_f32 v226, |v190|, v226, v226 op_sel:[0,1,0] op_sel_hi:[0,1,1] clamp\n\t" \
  "v_mfma_f32_16x16x32_bf16 v[144:147], v[216:219], v[128:131], v[144:147]\n\t" \
  "ds_read_b64_tr_b16 v[214:215], v248 offset:47656\n\t" \
  "v_fma_f32 v183, |v183|, v223, v183 mul:4\n\t" \
  "v_fma_f32 v182, |v182|, v222, v182 mul:4\n\t" \
  "v_mfma_f32_16x16x32_bf16 v[152:155], v[216:219], v[132:135], v[152:155]\n\t" \
  "ds_read_b32 v232, v232 offset:0\n\t" \
  "v_fma_f32 v191, |v191|, v227, v191 mul:4\n\t" \
  "v_fma_f32 v190, |v190|, v226, v190 mul:4\n\t" \
  "v_mfma_f32_16x16x32_bf16 v[160:163], v[216:219], v[136:139], v[160:163]\n\t" \
  "ds_read_b32 v235, v235 offset:0\n\t" \
  "v_cvt_pk_bf16_f32 v180, v180, v181\n\t" \
  "v_cvt_pk_bf16_f32 v181, v182, v183\n\t" \
  "v_mfma_f32_16x16x32_bf16 v[168:171], v[216:219], v[140:143], v[168:171]\n\t" \
  "ds_read_b64_tr_b16 v[216:217], v248 offset:45608\n\t" \
  "v_cvt_pk_bf16_f32 v188, v188, v189\n\t" \
  "v_cvt_pk_bf16_f32 v189, v190, v191\n\t" \
  "s_waitcnt lgkmcnt(9)\n\t" \
  "v_mfma_f32_16x16x32_bf16 v[148:151], v[208:211], v[96:99], 0\n\t" \
  "ds_read_b64_tr_b16 v[218:219], v248 offset:48680\n\t" \
  "v_cvt_pk_f16_f32 v220, |v146|, |v147| clamp\n\t" \
  "s_setprio 3\n\t" \
  "v_cvt_pknorm_i16_f32 v222, |v144|, |v145|\n\t" \
  "v_mfma_f32_16x16x32_bf16 v[156:159], v[208:211], v[100:103], 0\n\t" \
  "s_waitcnt vmcnt(1)\n\t" \
  "s_cmp_lg_u32 s58, 1\n\t" \
  "s_cbranch_scc1 .Lcnx_c15\n\t" \
  "s_waitcnt vmcnt(0)\n\t" \
  ".Lcnx_c15:\n\t" \
  "s_mov_b64 exec, 3\n\t" \
  "ds_add_rtn_u32 v246, v251, v250 offset:47200\n\t" \
  "s_mov_b64 exec, -1\n\t" \
  "ds_read_b32 v234, v234 offset:0\n\t" \
  "v_cvt_pk_f16_f32 v224, |v154|, |v155| clamp\n\t" \
  "v_cvt_pknorm_i16_f32 v226, |v152|, |v153|\n\t" \
  "v_mfma_f32_16x16x32_bf16 v[164:167], v[208:211], v[104:107], 0\n\t" \
  "v_and_b32_sdwa v221, s56, v220 dst_sel:DWORD dst_unused:UNUSED_PAD src0_sel:DWORD src1_sel:WORD_1\n\t" \
  "v_and_b32 v220, s56, v220\n\t" \
  "v_mfma_f32_16x16x32_bf16 v[172:175], v[208:211], v[108:111], 0\n\t" \
  "s_waitcnt lgkmcnt(1)\n\t" \
  "v_readfirstlane_b32 s4, v246\n\t" \
  "s_cmp_lt_u32 s4, s57\n\t" \
  "s_cbranch_scc1 .Lcnx_w96\n.Lcnx_b96:\n\t" \
  "s_cmp_lg_u32 s58, 1\n\t" \
  "s_cselect_b32 s57, s57, 0\n\t" \
  "ds_read_b64_tr_b16 v[208:209], v249 offset:12304\n\t" \
  "v_lshlrev_b32_sdwa v223, 2, v222 dst_sel:DWORD dst_unused:UNUSED_PAD src0_sel:DWORD src1_sel:BYTE_3\n\t" \
  "v_lshlrev_b32_sdwa v222, 2, v222 dst_sel:DWORD dst_unused:UNUSED_PAD src0_sel:DWORD src1_sel:BYTE_1\n\t" \
  "v_and_b32_sdwa v225, s56, v224 dst_sel:DWORD dst_unused:UNUSED_PAD src0_sel:DWORD src1_sel:WORD_1\n\t" \
  "v_mfma_f32_16x16x32_bf16 v[148:151], v[212:215], v[112:115], v[148:151]\n\t" \
  "ds_read_b64_tr_b16 v[210:211], v249 offset:13840\n\t" \
  "v_and_b32 v224, s56, v224\n\t" \
  "v_lshlrev_b32_sdwa v227, 2, v226 dst_sel:DWORD dst_unused:UNUSED_PAD src0_sel:DWORD src1_sel:BYTE_3\n\t" \
  "v_mfma_f32_16x16x32_bf16 v[156:159], v[212:215], v[116:119], v[156:159]\n\t" \
  "s_cmp_eq_u32 s37, 0\n\t" \
  "s_cbranch_scc1 .Lcnx_f9\n.Lcnx_g9:\n\t" \
  "ds_read_b32 v221, v221 offset:0\n\t" \
  "ds_read_b32 v220, v220 offset:0\n\t" \
  "v_lshlrev_b32_sdwa v226, 2, v226 dst_sel:DWORD dst_unused:UNUSED_PAD src0_sel:DWORD src1_sel:BYTE_1\n\t" \
  "v_fma_f32 v197, |v197|, v229, v197 mul:4\n\t" \
  "v_mfma_f32_16x16x32_bf16 v[164:167], v[212:215], v[120:123], v[164:167]\n\t" \
  "ds_read_b32 v223, v223 offset:15376\n\t" \
  "ds_read_b32 v222, v222 offset:15376\n\t" \
  "v_fma_f32 v196, |v196|, v228, v196 mul:4\n\t" \
  "v_fma_mix_f32 v231, |v199|, v231, v231 op_sel:[0,1,0] op_sel_hi:[0,1,1] clamp\n\t" \
  "v_mfma_f32_16x16x32_bf16 v[172:175], v[212:215], v[124:127], v[172:175]\n\t" \
  "ds_read_b64_tr_b16 v[212:213], v249 offset:12312\n\t" \
  "ds_read_b64_tr_b16 v[214:215], v249 offset:13848\n\t" \
  "v_fma_mix_f32 v230, |v198|, v230, v230 op_sel:[0,1,0] op_sel_hi:[0,1,1] clamp\n\t" \
  "s_waitcnt lgkmcnt(8)\n\t" \
  "v_fma_f32 v205, |v205|, v233, v205 mul:4\n\t" \
  "v_mfma_f32_16x16x32_bf16 v[148:151], v[216:219], v[128:131], v[148:151]\n\t" \
  "ds_read_b32 v225, v225 offset:0\n\t" \
  "v_fma_f32 v204, |v204|, v232, v204 mul:4\n\t" \
  "v_fma_f32 v207, |v207|, v235, v207 mul:4\n\t" \
  "v_mfma_f32_16x16x32_bf16 v[156:159], v[216:219], v[132:135], v[156:159]\n\t" \
  "ds_read_b32 v224, v224 offset:0\n\t" \
  "v_fma_f32 v206, |v206|, v234, v206 mul:4\n\t" \
  "v_fma_f32 v199, |v199|, v231, v199 mul:4\n\t" \
  "v_mfma_f32_16x16x32_bf16 v[164:167], v[216:219], v[136:139], v[164:167]\n\t" \
  "ds_read_b32 v227, v227 offset:15376\n\t" \
  "v_fma_f32 v198, |v198|, v230, v198 mul:4\n\t" \
  "v_cvt_pk_bf16_f32 v196, v196, v197\n\t" \
  "v_mfma_f32_16x16x32_bf16 v[172:175], v[216:219], v[140:143], v[172:175]\n\t" \
  "ds_read_b64_tr_b16 v[216:217], v249 offset:12368\n\t" \
  "v_cvt_pk_bf16_f32 v197, v198, v199\n\t" \
  "v_cvt_pk_bf16_f32 v204, v204, v205\n\t" \
  "v_cvt_pk_bf16_f32 v205, v206, v207\n\t" \
  "s_cmp_lg_u32 s58, 1\n\t" \
  "s_cbranch_scc1 .Lcnx_c16\n\t" \
  "s_mov_b64 vcc, -1\n\t" \
  "s_setprio 0\n\t" \
  "s_cmp_ge_u32 s39, 2048\n\t" \
  "s_cbranch_scc1 .Lcnx_c16\n\t" \
  "s_setprio 3\n\t" \
  ".Lcnx_c16:\n\t" \
  "s_cbranch_vccnz .Lcnx_p9_0\n\ts_setprio 0\n.Lcnx_p9_0:\n\t" \
  "v_cvt_pk_f16_f32 v228, |v162|, |v163| clamp\n\t" \
  "v_cvt_pknorm_i16_f32 v230, |v160|, |v161|\n\t" \
  "v_mfma_f32_16x16x32_bf16 v[56:59], v[242:245], v[236:239], v[56:59]\n\t" \
  "ds_read_b64_tr_b16 v[218:219], v249 offset:13904\n\t" \
  "v_cvt_pk_f16_f32 v232, |v170|, |v171| clamp\n\t" \
  "v_cvt_pk_f16_f32 v234, |v168|, |v169| clamp\n\t" \
  "v_mfma_f32_16x16x32_bf16 v[60:63], v[240:243], v[236:239], v[60:63]\n\t" \
  "buffer_load_dwordx4 v[236:239], v250, s[8:11], s12 offen offset:128 nt\n\t" \
  "s_cmp_lg_u32 s58, 0\n\t" \
  "s_cbranch_scc1 .Lcnx_c28\n\t" \
  "s_add_u32 m0, s25, 57400\n\t" \
  "s_add_u32 s24, s24, s26\n\t" \
  "buffer_load_dwordx4 v254, s[8:11], s24 offen lds\n\t" \
  "s_cmp_lg_u32 s38, 6\n\t" \
  "s_cbranch_scc1 .Lcnx_c28\n\t" \
  "s_mov_b32 m0, 163632\n\t" \
  "s_add_u32 s30, s30, s31\n\t" \
  "buffer_load_dword v252, s[8:11], s30 offen lds\n\t" \
  ".Lcnx_c28:\n\t" \
  "ds_read_b32 v226, v226 offset:15376\n\t" \
  "v_and_b32_sdwa v229, s56, v228 dst_sel:DWORD dst_unused:UNUSED_PAD src0_sel:DWORD src1_sel:WORD_1\n\t" \
  "v_and_b32 v228, s56, v228\n\t" \
  "s_waitcnt lgkmcnt(12)\n\t" \
  "v_mfma_f32_16x16x32_bf16 v[0:3], v[208:211], v[178:181], v[0:3]\n\t" \
  "ds_read_b32 v229, v229 offset:0\n\t" \
  "v_lshlrev_b32_sdwa v231, 2, v230 dst_sel:DWORD dst_unused:UNUSED_PAD src0_sel:DWORD src1_sel:BYTE_3\n\t" \
  "v_lshlrev_b32_sdwa v230, 2, v230 dst_sel:DWORD dst_unused:UNUSED_PAD src0_sel:DWORD src1_sel:BYTE_1\n\t" \
  "v_mfma_f32_16x16x32_bf16 v[24:27], v[208:211], v[186:189], v[24:27]\n\t" \
  "ds_read_b32 v228, v228 offset:0\n\t" \
  "v_and_b32_sdwa v233, s56, v232 dst_sel:DWORD dst_unused:UNUSED_PAD src0_sel:DWORD src1_sel:WORD_1\n\t" \
  "v_and_b32 v232, s56, v232\n\t" \
  "v_mfma_f32_16x16x32_bf16 v[48:51], v[208:211], v[194:197], v[48:51]\n\t" \
  "ds_read_b32 v231, v231 offset:15376\n\t" \
  "v_and_b32_sdwa v235, s56, v234 dst_sel:DWORD dst_unused:UNUSED_PAD src0_sel:DWORD src1_sel:WORD_1\n\t" \
  "v_and_b32 v234, s56, v234\n\t" \
  "v_mfma_f32_16x16x32_bf16 v[72:75], v[208:211], v[202:205], v[72:75]\n\t" \
  "ds_read_b64_tr_b16 v[208:209], v249 offset:12376\n\t" \
  "ds_read_b64_tr_b16 v[210:211], v249 offset:13912\n\t" \
  "s_waitcnt lgkmcnt(13)\n\t" \
  "v_fma_f32 v147, |v147|, v221, v147 mul:4\n\t" \
  "v_fma_f32 v146, |v146|, v220, v146 mul:4\n\t" \
  "s_waitcnt lgkmcnt(11)\n\t" \
  "v_mfma_f32_16x16x32_bf16 v[4:7], v[212:215], v[178:181], v[4:7]\n\t" \
  "ds_read_b32 v230, v230 offset:15376\n\t" \
  "v_fma_mix_f32 v223, |v145|, v223, v223 op_sel:[0,1,0] op_sel_hi:[0,1,1] clamp\n\t" \
  "v_fma_mix_f32 v222, |v144|, v222, v222 op_sel:[0,1,0] op_sel_hi:[0,1,1] clamp\n\t" \
  "v_mfma_f32_16x16x32_bf16 v[28:31], v[212:215], v[186:189], v[28:31]\n\t" \
  "ds_read_b32 v233, v233 offset:0\n\t" \
  "s_waitcnt lgkmcnt(7)\n\t" \
  "v_fma_f32 v155, |v155|, v225, v155 mul:4\n\t" \
  "v_fma_f32 v154, |v154|, v224, v154 mul:4\n\t" \
  "v_mfma_f32_16x16x32_bf16 v[52:55], v[212:215], v[194:197], v[52:55]\n\t" \
  "ds_read_b32 v232, v232 offset:0\n\t" \
  "v_fma_mix_f32 v227, |v153|, v227, v227 op_sel:[0,1,0] op_sel_hi:[0,1,1] clamp\n\t" \
  "v_fma_mix_f32 v226, |v152|, v226, v226 op_sel:[0,1,0] op_sel_hi:[0,1,1] clamp\n\t" \
  "v_fma_f32 v145, |v145|, v223, v145 mul:4\n\t" \
  "v_mfma_f32_16x16x32_bf16 v[76:79], v[212:215], v[202:205], v[76:79]\n\t" \
  "ds_read_b64_tr_b16 v[212:213], v249 offset:12432\n\t" \
  "v_fma_f32 v144, |v144|, v222, v144 mul:4\n\t" \
  "v_fma_f32 v153, |v153|, v227, v153 mul:4\n\t" \
  "v_mfma_f32_16x16x32_bf16 v[8:11], v[216:219], v[178:181], v[8:11]\n\t" \
  "ds_read_b64_tr_b16 v[214:215], v249 offset:13968\n\t" \
  "v_fma_f32 v152, |v152|, v226, v152 mul:4\n\t" \
  "v_cvt_pk_bf16_f32 v147, v146, v147\n\t" \
  "v_mfma_f32_16x16x32_bf16 v[32:35], v[216:219], v[186:189], v[32:35]\n\t" \
  "ds_read_b32 v235, v235 offset:0\n\t" \
  "v_cvt_pk_bf16_f32 v146, v144, v145\n\t" \
  "v_cvt_pk_bf16_f32 v155, v154, v155\n\t" \
  "v_mfma_f32_16x16x32_bf16 v[56:59], v[216:219], v[194:197], v[56:59]\n\t" \
  "ds_read_b32 v234, v234 offset:0\n\t" \
  "v_cvt_pk_bf16_f32 v154, v152, v153\n\t" \
  "s_cbranch_vccnz .Lcnx_p9_1\n\ts_setprio 1\n.Lcnx_p9_1:\n\t" \
  "v_cvt_pk_f16_f32 v220, |v148|, |v149| clamp\n\t" \
  "v_mfma_f32_16x16x32_bf16 v[80:83], v[216:219], v[202:205], v[80:83]\n\t" \
  "ds_read_b64_tr_b16 v[216:217], v249 offset:12440\n\t" \
  "v_cvt_pknorm_i16_f32 v222, |v150|, |v151|\n\t" \
  "v_cvt_pk_f16_f32 v224, |v156|, |v157| clamp\n\t" \
  "s_waitcnt lgkmcnt(8)\n\t" \
  "v_mfma_f32_16x16x32_bf16 v[12:15], v[208:211], v[178:181], v[12:15]\n\t" \
  "ds_read_b64_tr_b16 v[218:219], v249 offset:13976\n\t" \
  "v_cvt_pknorm_i16_f32 v226, |v158|, |v159|\n\t" \
  "v_and_b32_sdwa v221, s56, v220 dst_sel:DWORD dst_unused:UNUSED_PAD src0_sel:DWORD src1_sel:WORD_1\n\t" \
  "v_mfma_f32_16x16x32_bf16 v[36:39], v[208:211], v[186:189], v[36:39]\n\t" \
  "v_and_b32 v220, s56, v220\n\t" \
  "v_lshlrev_b32_sdwa v223, 2, v222 dst_sel:DWORD dst_unused:UNUSED_PAD src0_sel:DWORD src1_sel:BYTE_3\n\t" \
  "v_mfma_f32_16x16x32_bf16 v[60:63], v[208:211], v[194:197], v[60:63]\n\t" \
  "ds_read_b32 v221, v221 offset:0\n\t" \
  "v_lshlrev_b32_sdwa v222, 2, v222 dst_sel:DWORD dst_unused:UNUSED_PAD src0_sel:DWORD src1_sel:BYTE_1\n\t" \
  "v_and_b32_sdwa v225, s56, v224 dst_sel:DWORD dst_unused:UNUSED_PAD src0_sel:DWORD src1_sel:WORD_1\n\t" \
  "v_and_b32 v224, s56, v224\n\t" \
  "v_mfma_f32_16x16x32_bf16 v[84:87], v[208:211], v[202:205], v[84:87]\n\t" \
  "ds_read_b64_tr_b16 v[208:209], v248 offset:49192\n\t" \
  "ds_read_b64_tr_b16 v[210:211], v248 offset:50216\n\t" \
  "v_lshlrev_b32_sdwa v227, 2, v226 dst_sel:DWORD dst_unused:UNUSED_PAD src0_sel:DWORD src1_sel:BYTE_3\n\t" \
  "v_lshlrev_b32_sdwa v226, 2, v226 dst_sel:DWORD dst_unused:UNUSED_PAD src0_sel:DWORD src1_sel:BYTE_1\n\t" \
  "s_waitcnt lgkmcnt(7)\n\t" \
  "v_mfma_f32_16x16x32_bf16 v[16:19], v[212:215], v[178:181], v[16:19]\n\t" \
  "ds_read_b32 v220, v220 offset:0\n\t" \
  "ds_read_b32 v223, v223 offset:15376\n\t" \
  "v_fma_f32 v163, |v163|, v229, v163 mul:4\n\t" \
  "v_fma_f32 v162, |v162|, v228, v162 mul:4\n\t" \
  "v_mfma_f32_16x16x32_bf16 v[40:43], v[212:215], v[186:189], v[40:43]\n\t" \
  "ds_read_b32 v222, v222 offset:15376\n\t" \
  "v_fma_mix_f32 v231, |v161|, v231, v231 op_sel:[0,1,0] op_sel_hi:[0,1,1] clamp\n\t" \
  "v_fma_mix_f32 v230, |v160|, v230, v230 op_sel:[0,1,0] op_sel_hi:[0,1,1] clamp\n\t" \
  "v_mfma_f32_16x16x32_bf16 v[64:67], v[212:215], v[194:197], v[64:67]\n\t" \
  "ds_read_b32 v225, v225 offset:0\n\t" \
  "s_waitcnt lgkmcnt(9)\n\t" \
  "v_fma_f32 v171, |v171|, v233, v171 mul:4\n\t" \
  "v_fma_f32 v170, |v170|, v232, v170 mul:4\n\t" \
  "v_mfma_f32_16x16x32_bf16 v[88:91], v[212:215], v[202:205], v[88:91]\n\t" \
  "ds_read_b64_tr_b16 v[212:213], v248 offset:52264\n\t" \
  "v_fma_f32 v169, |v169|, v235, v169 mul:4\n\t" \
  "v_fma_f32 v168, |v168|, v234, v168 mul:4\n\t" \
  "s_waitcnt lgkmcnt(8)\n\t" \
  "v_mfma_f32_16x16x32_bf16 v[20:23], v[216:219], v[178:181], v[20:23]\n\t" \
  "ds_read_b64_tr_b16 v[214:215], v248 offset:53288\n\t" \
  "v_fma_f32 v161, |v161|, v231, v161 mul:4\n\t" \
  "v_fma_f32 v160, |v160|, v230, v160 mul:4\n\t" \
  "v_mfma_f32_16x16x32_bf16 v[44:47], v[216:219], v[186:189], v[44:47]\n\t" \
  "ds_read_b32 v224, v224 offset:0\n\t" \
  "v_cvt_pk_bf16_f32 v163, v162, v163\n\t" \
  "v_cvt_pk_bf16_f32 v162, v160, v161\n\t" \
  "v_mfma_f32_16x16x32_bf16 v[68:71], v[216:219], v[194:197], v[68:71]\n\t" \
  "ds_read_b32 v227, v227 offset:15376\n\t" \
  "v_cvt_pk_bf16_f32 v171, v170, v171\n\t" \
  "v_cvt_pk_bf16_f32 v170, v168, v169\n\t" \
  "v_cvt_pk_f16_f32 v228, |v164|, |v165| clamp\n\t" \
  "v_mfma_f32_16x16x32_bf16 v[92:95], v[216:219], v[202:205], v[92:95]\n\t" \
  "ds_read_b64_tr_b16 v[216:217], v248 offset:51240\n\t" \
  "v_cvt_pknorm_i16_f32 v230, |v166|, |v167|\n\t" \
  "s_cbranch_vccnz .Lcnx_p9_2\n\ts_setprio 2\n.Lcnx_p9_2:\n\t" \
  "v_cvt_pk_f16_f32 v232, |v172|, |v173| clamp\n\t" \
  "s_waitcnt lgkmcnt(9)\n\t" \
  "v_mfma_f32_16x16x32_bf16 v[176:179], v[208:211], v[96:99], 0\n\t" \
  "ds_read_b64_tr_b16 v[218:219], v248 offset:54312\n\t" \
  "v_cvt_pk_f16_f32 v234, |v174|, |v175| clamp\n\t" \
  "v_and_b32_sdwa v229, s56, v228 dst_sel:DWORD dst_unused:UNUSED_PAD src0_sel:DWORD src1_sel:WORD_1\n\t" \
  "v_mfma_f32_16x16x32_bf16 v[184:187], v[208:211], v[100:103], 0\n\t" \
  "ds_read_b32 v226, v226 offset:15376\n\t" \
  "v_and_b32 v228, s56, v228\n\t" \
  "v_lshlrev_b32_sdwa v231, 2, v230 dst_sel:DWORD dst_unused:UNUSED_PAD src0_sel:DWORD src1_sel:BYTE_3\n\t" \
  "v_mfma_f32_16x16x32_bf16 v[192:195], v[208:211], v[104:107], 0\n\t" \
  "ds_read_b32 v229, v229 offset:0\n\t" \
  "v_lshlrev_b32_sdwa v230, 2, v230 dst_sel:DWORD dst_unused:UNUSED_PAD src0_sel:DWORD src1_sel:BYTE_1\n\t" \
  "v_and_b32_sdwa v233, s56, v232 dst_sel:DWORD dst_unused:UNUSED_PAD src0_sel:DWORD src1_sel:WORD_1\n\t" \
  "v_mfma_f32_16x16x32_bf16 v[200:203], v[208:211], v[108:111], 0\n\t" \
  "ds_read_b64_tr_b16 v[208:209], v248 offset:49704\n\t" \
  "v_and_b32 v232, s56, v232\n\t" \
  "v_and_b32_sdwa v235, s56, v234 dst_sel:DWORD dst_unused:UNUSED_PAD src0_sel:DWORD src1_sel:WORD_1\n\t" \
  "s_waitcnt lgkmcnt(7)\n\t" \
  "v_mfma_f32_16x16x32_bf16 v[176:179], v[212:215], v[112:115], v[176:179]\n\t" \
  "ds_read_b64_tr_b16 v[210:211], v248 offset:50728\n\t" \
  "ds_read_b32 v228, v228 offset:0\n\t" \
  "v_and_b32 v234, s56, v234\n\t" \
  "v_fma_f32 v149, |v149|, v221, v149 mul:4\n\t" \
  "v_mfma_f32_16x16x32_bf16 v[184:187], v[212:215], v[116:119], v[184:187]\n\t" \
  "ds_read_b32 v231, v231 offset:15376\n\t" \
  "ds_read_b32 v230, v230 offset:15376\n\t" \
  "v_fma_f32 v148, |v148|, v220, v148 mul:4\n\t" \
  "v_fma_mix_f32 v223, |v151|, v223, v223 op_sel:[0,1,0] op_sel_hi:[0,1,1] clamp\n\t" \
  "v_mfma_f32_16x16x32_bf16 v[192:195], v[212:215], v[120:123], v[192:195]\n\t" \
  "ds_read_b32 v233, v233 offset:0\n\t" \
  "v_fma_mix_f32 v222, |v150|, v222, v222 op_sel:[0,1,0] op_sel_hi:[0,1,1] clamp\n\t" \
  "s_waitcnt lgkmcnt(7)\n\t" \
  "v_fma_f32 v157, |v157|, v225, v157 mul:4\n\t" \
  "v_mfma_f32_16x16x32_bf16 v[200:203], v[212:215], v[124:127], v[200:203]\n\t" \
  "ds_read_b64_tr_b16 v[212:213], v248 offset:52776\n\t" \
  "v_fma_f32 v156, |v156|, v224, v156 mul:4\n\t" \
  "v_fma_mix_f32 v227, |v159|, v227, v227 op_sel:[0,1,0] op_sel_hi:[0,1,1] clamp\n\t" \
  "v_fma_mix_f32 v226, |v158|, v226, v226 op_sel:[0,1,0] op_sel_hi:[0,1,1] clamp\n\t" \
  "v_mfma_f32_16x16x32_bf16 v[176:179], v[216:219], v[128:131], v[176:179]\n\t" \
  "ds_read_b64_tr_b16 v[214:215], v248 offset:53800\n\t" \
  "v_fma_f32 v151, |v151|, v223, v151 mul:4\n\t" \
  "v_fma_f32 v150, |v150|, v222, v150 mul:4\n\t" \
  "v_mfma_f32_16x16x32_bf16 v[184:187], v[216:219], v[132:135], v[184:187]\n\t" \
  "ds_read_b32 v232, v232 offset:0\n\t" \
  "v_fma_f32 v159, |v159|, v227, v159 mul:4\n\t" \
  "v_fma_f32 v158, |v158|, v226, v158 mul:4\n\t" \
  "v_mfma_f32_16x16x32_bf16 v[192:195], v[216:219], v[136:139], v[192:195]\n\t" \
  "ds_read_b32 v235, v235 offset:0\n\t" \
  "v_cvt_pk_bf16_f32 v148, v148, v149\n\t" \
  "v_cvt_pk_bf16_f32 v149, v150, v151\n\t" \
  "v_mfma_f32_16x16x32_bf16 v[200:203], v[216:219], v[140:143], v[200:203]\n\t" \
  "ds_read_b64_tr_b16 v[216:217], v248 offset:51752\n\t" \
  "v_cvt_pk_bf16_f32 v156, v156, v157\n\t" \
  "v_cvt_pk_bf16_f32 v157, v158, v159\n\t" \
  "s_waitcnt lgkmcnt(9)\n\t" \
  "v_mfma_f32_16x16x32_bf16 v[180:183], v[208:211], v[96:99], 0\n\t" \
  "ds_read_b64_tr_b16 v[218:219], v248 offset:54824\n\t" \
  "v_cvt_pk_f16_f32 v220, |v178|, |v179| clamp\n\t" \
  "s_cbranch_vccnz .Lcnx_p9_3\n\ts_setprio 3\n.Lcnx_p9_3:\n\t" \
  "v_cvt_pknorm_i16_f32 v222, |v176|, |v177|\n\t" \
  "v_mfma_f32_16x16x32_bf16 v[188:191], v[208:211], v[100:103], 0\n\t" \
  "s_waitcnt vmcnt(1)\n\t" \
  "s_cmp_lg_u32 s58, 1\n\t" \
  "s_cbranch_scc1 .Lcnx_c17\n\t" \
  "s_waitcnt vmcnt(0)\n\t" \
  ".Lcnx_c17:\n\t" \
  "s_mov_b64 exec, 3\n\t" \
  "ds_add_rtn_u32 v246, v251, v250 offset:47208\n\t" \
  "s_mov_b64 exec, -1\n\t" \
  "ds_read_b32 v234, v234 offset:0\n\t" \
  "v_cvt_pk_f16_f32 v224, |v186|, |v187| clamp\n\t" \
  "v_cvt_pknorm_i16_f32 v226, |v184|, |v185|\n\t" \
  "v_mfma_f32_16x16x32_bf16 v[196:199], v[208:211], v[104:107], 0\n\t" \
  "v_and_b32_sdwa v221, s56, v220 dst_sel:DWORD dst_unused:UNUSED_PAD src0_sel:DWORD src1_sel:WORD_1\n\t" \
  "v_and_b32 v220, s56, v220\n\t" \
  "v_mfma_f32_16x16x32_bf16 v[204:207], v[208:211], v[108:111], 0\n\t" \
  "s_waitcnt lgkmcnt(1)\n\t" \
  "v_readfirstlane_b32 s4, v246\n\t" \
  "s_cmp_lt_u32 s4, s57\n\t" \
  "s_cbranch_scc1 .Lcnx_w108\n.Lcnx_b108:\n\t" \
  "v_add_u32 v248, s59, v248\n\t" \
  "v_add_u32 v249, s60, v249\n\t" \
  "ds_read_b64_tr_b16 v[208:209], v249 offset:24600\n\t" \
  "v_lshlrev_b32_sdwa v223, 2, v222 dst_sel:DWORD dst_unused:UNUSED_PAD src0_sel:DWORD src1_sel:BYTE_3\n\t" \
  "v_lshlrev_b32_sdwa v222, 2, v222 dst_sel:DWORD dst_unused:UNUSED_PAD src0_sel:DWORD src1_sel:BYTE_1\n\t" \
  "v_and_b32_sdwa v225, s56, v224 dst_sel:DWORD dst_unused:UNUSED_PAD src0_sel:DWORD src1_sel:WORD_1\n\t" \
  "v_mfma_f32_16x16x32_bf16 v[180:183], v[212:215], v[112:115], v[180:183]\n\t" \
  "ds_read_b64_tr_b16 v[210:211], v249 offset:26136\n\t" \
  "v_and_b32 v224, s56, v224\n\t" \
  "v_lshlrev_b32_sdwa v227, 2, v226 dst_sel:DWORD dst_unused:UNUSED_PAD src0_sel:DWORD src1_sel:BYTE_3\n\t" \
  "v_mfma_f32_16x16x32_bf16 v[188:191], v[212:215], v[116:119], v[188:191]\n\t" \
  "s_cmp_eq_u32 s37, 0\n\t" \
  "s_cbranch_scc1 .Lcnx_f10\n.Lcnx_g10:\n\t" \
  "ds_read_b32 v221, v221 offset:0\n\t" \
  "ds_read_b32 v220, v220 offset:0\n\t" \
  "v_lshlrev_b32_sdwa v226, 2, v226 dst_sel:DWORD dst_unused:UNUSED_PAD src0_sel:DWORD src1_sel:BYTE_1\n\t" \
  "v_fma_f32 v165, |v165|, v229, v165 mul:4\n\t" \
  "v_mfma_f32_16x16x32_bf16 v[196:199], v[212:215], v[120:123], v[196:199]\n\t" \
  "ds_read_b32 v223, v223 offset:15376\n\t" \
  "ds_read_b32 v222, v222 offset:15376\n\t" \
  "v_fma_f32 v164, |v164|, v228, v164 mul:4\n\t" \
  "v_fma_mix_f32 v231, |v167|, v231, v231 op_sel:[0,1,0] op_sel_hi:[0,1,1] clamp\n\t" \
  "v_mfma_f32_16x16x32_bf16 v[204:207], v[212:215], v[124:127], v[204:207]\n\t" \
  "ds_read_b64_tr_b16 v[212:213], v249 offset:24608\n\t" \
  "ds_read_b64_tr_b16 v[214:215], v249 offset:26144\n\t" \
  "v_fma_mix_f32 v230, |v166|, v230, v230 op_sel:[0,1,0] op_sel_hi:[0,1,1] clamp\n\t" \
  "s_waitcnt lgkmcnt(8)\n\t" \
  "v_fma_f32 v173, |v173|, v233, v173 mul:4\n\t" \
  "v_mfma_f32_16x16x32_bf16 v[180:183], v[216:219], v[128:131], v[180:183]\n\t" \
  "ds_read_b32 v225, v225 offset:0\n\t" \
  "v_fma_f32 v172, |v172|, v232, v172 mul:4\n\t" \
  "v_fma_f32 v175, |v175|, v235, v175 mul:4\n\t" \
  "v_mfma_f32_16x16x32_bf16 v[188:191], v[216:219], v[132:135], v[188:191]\n\t" \
  "ds_read_b32 v224, v224 offset:0\n\t" \
  "v_fma_f32 v174, |v174|, v234, v174 mul:4\n\t" \
  "v_fma_f32 v167, |v167|, v231, v167 mul:4\n\t" \
  "v_mfma_f32_16x16x32_bf16 v[196:199], v[216:219], v[136:139], v[196:199]\n\t" \
  "ds_read_b32 v227, v227 offset:15376\n\t" \
  "v_fma_f32 v166, |v166|, v230, v166 mul:4\n\t" \
  "v_cvt_pk_bf16_f32 v164, v164, v165\n\t" \
  "v_mfma_f32_16x16x32_bf16 v[204:207], v[216:219], v[140:143], v[204:207]\n\t" \
  "ds_read_b64_tr_b16 v[216:217], v249 offset:24664\n\t" \
  "v_cvt_pk_bf16_f32 v165, v166, v167\n\t" \
  "v_cvt_pk_bf16_f32 v172, v172, v173\n\t" \
  "v_cvt_pk_bf16_f32 v173, v174, v175\n\t" \
  "s_cbranch_vccnz .Lcnx_p10_0\n\ts_setprio 0\n.Lcnx_p10_0:\n\t" \
  "v_cvt_pk_f16_f32 v228, |v194|, |v195| clamp\n\t" \
  "v_cvt_pknorm_i16_f32 v230, |v192|, |v193|\n\t" \
  "v_mfma_f32_16x16x32_bf16 v[64:67], v[242:245], v[236:239], v[64:67]\n\t" \
  "ds_read_b64_tr_b16 v[218:219], v249 offset:26200\n\t" \
  "v_cvt_pk_f16_f32 v232, |v202|, |v203| clamp\n\t" \
  "v_cvt_pk_f16_f32 v234, |v200|, |v201| clamp\n\t" \
  "v_mfma_f32_16x16x32_bf16 v[68:71], v[240:243], v[236:239], v[68:71]\n\t" \
  "buffer_load_dwordx4 v[236:239], v250, s[8:11], s13 offen offset:0 nt\n\t" \
  "s_cmp_lg_u32 s58, 0\n\t" \
  "s_cbranch_scc1 .Lcnx_c29\n\t" \
  "s_add_u32 m0, s25, 65600\n\t" \
  "s_add_u32 s24, s24, s26\n\t" \
  "buffer_load_dwordx4 v254, s[8:11], s24 offen lds\n\t" \
  "s_cmp_lg_u32 s38, 7\n\t" \
  "s_cbranch_scc1 .Lcnx_c29\n\t" \
  "s_mov_b32 m0, 163632\n\t" \
  "s_add_u32 s30, s30, s31\n\t" \
  "buffer_load_dword v252, s[8:11], s30 offen lds\n\t" \
  ".Lcnx_c29:\n\t" \
  "ds_read_b32 v226, v226 offset:15376\n\t" \
  "v_and_b32_sdwa v229, s56, v228 dst_sel:DWORD dst_unused:UNUSED_PAD src0_sel:DWORD src1_sel:WORD_1\n\t" \
  "v_and_b32 v228, s56, v228\n\t" \
  "s_waitcnt lgkmcnt(12)\n\t" \
  "v_mfma_f32_16x16x32_bf16 v[0:3], v[208:211], v[146:149], v[0:3]\n\t" \
  "ds_read_b32 v229, v229 offset:0\n\t" \
  "v_lshlrev_b32_sdwa v231, 2, v230 dst_sel:DWORD dst_unused:UNUSED_PAD src0_sel:DWORD src1_sel:BYTE_3\n\t" \
  "v_lshlrev_b32_sdwa v230, 2, v230 dst_sel:DWORD dst_unused:UNUSED_PAD src0_sel:DWORD src1_sel:BYTE_1\n\t" \
  "v_mfma_f32_16x16x32_bf16 v[24:27], v[208:211], v[154:157], v[24:27]\n\t" \
  "ds_read_b32 v228, v228 offset:0\n\t" \
  "v_and_b32_sdwa v233, s56, v232 dst_sel:DWORD dst_unused:UNUSED_PAD src0_sel:DWORD src1_sel:WORD_1\n\t" \
  "v_and_b32 v232, s56, v232\n\t" \
  "v_mfma_f32_16x16x32_bf16 v[48:51], v[208:211], v[162:165], v[48:51]\n\t" \
  "ds_read_b32 v231, v231 offset:15376\n\t" \
  "v_and_b32_sdwa v235, s56, v234 dst_sel:DWORD dst_unused:UNUSED_PAD src0_sel:DWORD src1_sel:WORD_1\n\t" \
  "v_and_b32 v234, s56, v234\n\t" \
  "v_mfma_f32_16x16x32_bf16 v[72:75], v[208:211], v[170:173], v[72:75]\n\t" \
  "ds_read_b64_tr_b16 v[208:209], v249 offset:24672\n\t" \
  "ds_read_b64_tr_b16 v[210:211], v249 offset:26208\n\t" \
  "s_waitcnt lgkmcnt(13)\n\t" \
  "v_fma_f32 v179, |v179|, v221, v179 mul:4\n\t" \
  "v_fma_f32 v178, |v178|, v220, v178 mul:4\n\t" \
  "s_waitcnt lgkmcnt(11)\n\t" \
  "v_mfma_f32_16x16x32_bf16 v[4:7], v[212:215], v[146:149], v[4:7]\n\t" \
  "ds_read_b32 v230, v230 offset:15376\n\t" \
  "v_fma_mix_f32 v223, |v177|, v223, v223 op_sel:[0,1,0] op_sel_hi:[0,1,1] clamp\n\t" \
  "v_fma_mix_f32 v222, |v176|, v222, v222 op_sel:[0,1,0] op_sel_hi:[0,1,1] clamp\n\t" \
  "v_mfma_f32_16x16x32_bf16 v[28:31], v[212:215], v[154:157], v[28:31]\n\t" \
  "ds_read_b32 v233, v233 offset:0\n\t" \
  "s_waitcnt lgkmcnt(7)\n\t" \
  "v_fma_f32 v187, |v187|, v225, v187 mul:4\n\t" \
  "v_fma_f32 v186, |v186|, v224, v186 mul:4\n\t" \
  "v_mfma_f32_16x16x32_bf16 v[52:55], v[212:215], v[162:165], v[52:55]\n\t" \
  "ds_read_b32 v232, v232 offset:0\n\t" \
  "v_fma_mix_f32 v227, |v185|, v227, v227 op_sel:[0,1,0] op_sel_hi:[0,1,1] clamp\n\t" \
  "v_fma_mix_f32 v226, |v184|, v226, v226 op_sel:[0,1,0] op_sel_hi:[0,1,1] clamp\n\t" \
  "v_fma_f32 v177, |v177|, v223, v177 mul:4\n\t" \
  "v_mfma_f32_16x16x32_bf16 v[76:79], v[212:215], v[170:173], v[76:79]\n\t" \
  "ds_read_b64_tr_b16 v[212:213], v249 offset:24728\n\t" \
  "v_fma_f32 v176, |v176|, v222, v176 mul:4\n\t" \
  "v_fma_f32 v185, |v185|, v227, v185 mul:4\n\t" \
  "v_mfma_f32_16x16x32_bf16 v[8:11], v[216:219], v[146:149], v[8:11]\n\t" \
  "ds_read_b64_tr_b16 v[214:215], v249 offset:26264\n\t" \
  "v_fma_f32 v184, |v184|, v226, v184 mul:4\n\t" \
  "v_cvt_pk_bf16_f32 v179, v178, v179\n\t" \
  "v_mfma_f32_16x16x32_bf16 v[32:35], v[216:219], v[154:157], v[32:35]\n\t" \
  "ds_read_b32 v235, v235 offset:0\n\t" \
  "v_cvt_pk_bf16_f32 v178, v176, v177\n\t" \
  "v_cvt_pk_bf16_f32 v187, v186, v187\n\t" \
  "v_mfma_f32_16x16x32_bf16 v[56:59], v[216:219], v[162:165], v[56:59]\n\t" \
  "ds_read_b32 v234, v234 offset:0\n\t" \
  "v_cvt_pk_bf16_f32 v186, v184, v185\n\t" \
  "s_cbranch_vccnz .Lcnx_p10_1\n\ts_setprio 1\n.Lcnx_p10_1:\n\t" \
  "v_cvt_pk_f16_f32 v220, |v180|, |v181| clamp\n\t" \
  "v_mfma_f32_16x16x32_bf16 v[80:83], v[216:219], v[170:173], v[80:83]\n\t" \
  "ds_read_b64_tr_b16 v[216:217], v249 offset:24736\n\t" \
  "v_cvt_pknorm_i16_f32 v222, |v182|, |v183|\n\t" \
  "v_cvt_pk_f16_f32 v224, |v188|, |v189| clamp\n\t" \
  "s_waitcnt lgkmcnt(8)\n\t" \
  "v_mfma_f32_16x16x32_bf16 v[12:15], v[208:211], v[146:149], v[12:15]\n\t" \
  "ds_read_b64_tr_b16 v[218:219], v249 offset:26272\n\t" \
  "v_cvt_pknorm_i16_f32 v226, |v190|, |v191|\n\t" \
  "v_and_b32_sdwa v221, s56, v220 dst_sel:DWORD dst_unused:UNUSED_PAD src0_sel:DWORD src1_sel:WORD_1\n\t" \
  "v_mfma_f32_16x16x32_bf16 v[36:39], v[208:211], v[154:157], v[36:39]\n\t" \
  "v_and_b32 v220, s56, v220\n\t" \
  "v_lshlrev_b32_sdwa v223, 2, v222 dst_sel:DWORD dst_unused:UNUSED_PAD src0_sel:DWORD src1_sel:BYTE_3\n\t" \
  "v_mfma_f32_16x16x32_bf16 v[60:63], v[208:211], v[162:165], v[60:63]\n\t" \
  "ds_read_b32 v221, v221 offset:0\n\t" \
  "v_lshlrev_b32_sdwa v222, 2, v222 dst_sel:DWORD dst_unused:UNUSED_PAD src0_sel:DWORD src1_sel:BYTE_1\n\t" \
  "v_and_b32_sdwa v225, s56, v224 dst_sel:DWORD dst_unused:UNUSED_PAD src0_sel:DWORD src1_sel:WORD_1\n\t" \
  "v_and_b32 v224, s56, v224\n\t" \
  "v_mfma_f32_16x16x32_bf16 v[84:87], v[208:211], v[170:173], v[84:87]\n\t" \
  "ds_read_b64_tr_b16 v[208:209], v248 offset:36904\n\t" \
  "ds_read_b64_tr_b16 v[210:211], v248 offset:37928\n\t" \
  "v_lshlrev_b32_sdwa v227, 2, v226 dst_sel:DWORD dst_unused:UNUSED_PAD src0_sel:DWORD src1_sel:BYTE_3\n\t" \
  "v_lshlrev_b32_sdwa v226, 2, v226 dst_sel:DWORD dst_unused:UNUSED_PAD src0_sel:DWORD src1_sel:BYTE_1\n\t" \
  "s_waitcnt lgkmcnt(7)\n\t" \
  "v_mfma_f32_16x16x32_bf16 v[16:19], v[212:215], v[146:149], v[16:19]\n\t" \
  "ds_read_b32 v220, v220 offset:0\n\t" \
  "ds_read_b32 v223, v223 offset:15376\n\t" \
  "v_fma_f32 v195, |v195|, v229, v195 mul:4\n\t" \
  "v_fma_f32 v194, |v194|, v228, v194 mul:4\n\t" \
  "v_mfma_f32_16x16x32_bf16 v[40:43], v[212:215], v[154:157], v[40:43]\n\t" \
  "ds_read_b32 v222, v222 offset:15376\n\t" \
  "v_fma_mix_f32 v231, |v193|, v231, v231 op_sel:[0,1,0] op_sel_hi:[0,1,1] clamp\n\t" \
  "v_fma_mix_f32 v230, |v192|, v230, v230 op_sel:[0,1,0] op_sel_hi:[0,1,1] clamp\n\t" \
  "v_mfma_f32_16x16x32_bf16 v[64:67], v[212:215], v[162:165], v[64:67]\n\t" \
  "ds_read_b32 v225, v225 offset:0\n\t" \
  "s_waitcnt lgkmcnt(9)\n\t" \
  "v_fma_f32 v203, |v203|, v233, v203 mul:4\n\t" \
  "v_fma_f32 v202, |v202|, v232, v202 mul:4\n\t" \
  "v_mfma_f32_16x16x32_bf16 v[88:91], v[212:215], v[170:173], v[88:91]\n\t" \
  "ds_read_b64_tr_b16 v[212:213], v248 offset:39976\n\t" \
  "v_fma_f32 v201, |v201|, v235, v201 mul:4\n\t" \
  "v_fma_f32 v200, |v200|, v234, v200 mul:4\n\t" \
  "s_waitcnt lgkmcnt(8)\n\t" \
  "v_mfma_f32_16x16x32_bf16 v[20:23], v[216:219], v[146:149], v[20:23]\n\t" \
  "ds_read_b64_tr_b16 v[214:215], v248 offset:41000\n\t" \
  "v_fma_f32 v193, |v193|, v231, v193 mul:4\n\t" \
  "v_fma_f32 v192, |v192|, v230, v192 mul:4\n\t" \
  "v_mfma_f32_16x16x32_bf16 v[44:47], v[216:219], v[154:157], v[44:47]\n\t" \
  "ds_read_b32 v224, v224 offset:0\n\t" \
  "v_cvt_pk_bf16_f32 v195, v194, v195\n\t" \
  "v_cvt_pk_bf16_f32 v194, v192, v193\n\t" \
  "v_mfma_f32_16x16x32_bf16 v[68:71], v[216:219], v[162:165], v[68:71]\n\t" \
  "ds_read_b32 v227, v227 offset:15376\n\t" \
  "v_cvt_pk_bf16_f32 v203, v202, v203\n\t" \
  "v_cvt_pk_bf16_f32 v202, v200, v201\n\t" \
  "v_cvt_pk_f16_f32 v228, |v196|, |v197| clamp\n\t" \
  "v_mfma_f32_16x16x32_bf16 v[92:95], v[216:219], v[170:173], v[92:95]\n\t" \
  "ds_read_b64_tr_b16 v[216:217], v248 offset:38952\n\t" \
  "v_cvt_pknorm_i16_f32 v230, |v198|, |v199|\n\t" \
  "s_cbranch_vccnz .Lcnx_p10_2\n\ts_setprio 2\n.Lcnx_p10_2:\n\t" \
  "v_cvt_pk_f16_f32 v232, |v204|, |v205| clamp\n\t" \
  "s_waitcnt lgkmcnt(9)\n\t" \
  "v_mfma_f32_16x16x32_bf16 v[144:147], v[208:211], v[96:99], 0\n\t" \
  "ds_read_b64_tr_b16 v[218:219], v248 offset:42024\n\t" \
  "v_cvt_pk_f16_f32 v234, |v206|, |v207| clamp\n\t" \
  "v_and_b32_sdwa v229, s56, v228 dst_sel:DWORD dst_unused:UNUSED_PAD src0_sel:DWORD src1_sel:WORD_1\n\t" \
  "v_mfma_f32_16x16x32_bf16 v[152:155], v[208:211], v[100:103], 0\n\t" \
  "ds_read_b32 v226, v226 offset:15376\n\t" \
  "v_and_b32 v228, s56, v228\n\t" \
  "v_lshlrev_b32_sdwa v231, 2, v230 dst_sel:DWORD dst_unused:UNUSED_PAD src0_sel:DWORD src1_sel:BYTE_3\n\t" \
  "v_mfma_f32_16x16x32_bf16 v[160:163], v[208:211], v[104:107], 0\n\t" \
  "ds_read_b32 v229, v229 offset:0\n\t" \
  "v_lshlrev_b32_sdwa v230, 2, v230 dst_sel:DWORD dst_unused:UNUSED_PAD src0_sel:DWORD src1_sel:BYTE_1\n\t" \
  "v_and_b32_sdwa v233, s56, v232 dst_sel:DWORD dst_unused:UNUSED_PAD src0_sel:DWORD src1_sel:WORD_1\n\t" \
  "v_mfma_f32_16x16x32_bf16 v[168:171], v[208:211], v[108:111], 0\n\t" \
  "ds_read_b64_tr_b16 v[208:209], v248 offset:37416\n\t" \
  "v_and_b32 v232, s56, v232\n\t" \
  "v_and_b32_sdwa v235, s56, v234 dst_sel:DWORD dst_unused:UNUSED_PAD src0_sel:DWORD src1_sel:WORD_1\n\t" \
  "s_waitcnt lgkmcnt(7)\n\t" \
  "v_mfma_f32_16x16x32_bf16 v[144:147], v[212:215], v[112:115], v[144:147]\n\t" \
  "ds_read_b64_tr_b16 v[210:211], v248 offset:38440\n\t" \
  "ds_read_b32 v228, v228 offset:0\n\t" \
  "v_and_b32 v234, s56, v234\n\t" \
  "v_fma_f32 v181, |v181|, v221, v181 mul:4\n\t" \
  "v_mfma_f32_16x16x32_bf16 v[152:155], v[212:215], v[116:119], v[152:155]\n\t" \
  "ds_read_b32 v231, v231 offset:15376\n\t" \
  "ds_read_b32 v230, v230 offset:15376\n\t" \
  "v_fma_f32 v180, |v180|, v220, v180 mul:4\n\t" \
  "v_fma_mix_f32 v223, |v183|, v223, v223 op_sel:[0,1,0] op_sel_hi:[0,1,1] clamp\n\t" \
  "v_mfma_f32_16x16x32_bf16 v[160:163], v[212:215], v[120:123], v[160:163]\n\t" \
  "ds_read_b32 v233, v233 offset:0\n\t" \
  "v_fma_mix_f32 v222, |v182|, v222, v222 op_sel:[0,1,0] op_sel_hi:[0,1,1] clamp\n\t" \
  "s_waitcnt lgkmcnt(7)\n\t" \
  "v_fma_f32 v189, |v189|, v225, v189 mul:4\n\t" \
  "v_mfma_f32_16x16x32_bf16 v[168:171], v[212:215], v[124:127], v[168:171]\n\t" \
  "ds_read_b64_tr_b16 v[212:213], v248 offset:40488\n\t" \
  "v_fma_f32 v188, |v188|, v224, v188 mul:4\n\t" \
  "v_fma_mix_f32 v227, |v191|, v227, v227 op_sel:[0,1,0] op_sel_hi:[0,1,1] clamp\n\t" \
  "v_fma_mix_f32 v226, |v190|, v226, v226 op_sel:[0,1,0] op_sel_hi:[0,1,1] clamp\n\t" \
  "v_mfma_f32_16x16x32_bf16 v[144:147], v[216:219], v[128:131], v[144:147]\n\t" \
  "ds_read_b64_tr_b16 v[214:215], v248 offset:41512\n\t" \
  "v_fma_f32 v183, |v183|, v223, v183 mul:4\n\t" \
  "v_fma_f32 v182, |v182|, v222, v182 mul:4\n\t" \
  "v_mfma_f32_16x16x32_bf16 v[152:155], v[216:219], v[132:135], v[152:155]\n\t" \
  "ds_read_b32 v232, v232 offset:0\n\t" \
  "v_fma_f32 v191, |v191|, v227, v191 mul:4\n\t" \
  "v_fma_f32 v190, |v190|, v226, v190 mul:4\n\t" \
  "v_mfma_f32_16x16x32_bf16 v[160:163], v[216:219], v[136:139], v[160:163]\n\t" \
  "ds_read_b32 v235, v235 offset:0\n\t" \
  "v_cvt_pk_bf16_f32 v180, v180, v181\n\t" \
  "v_cvt_pk_bf16_f32 v181, v182, v183\n\t" \
  "v_mfma_f32_16x16x32_bf16 v[168:171], v[216:219], v[140:143], v[168:171]\n\t" \
  "ds_read_b64_tr_b16 v[216:217], v248 offset:39464\n\t" \
  "v_cvt_pk_bf16_f32 v188, v188, v189\n\t" \
  "v_cvt_pk_bf16_f32 v189, v190, v191\n\t" \
  "s_waitcnt lgkmcnt(9)\n\t" \
  "v_mfma_f32_16x16x32_bf16 v[148:151], v[208:211], v[96:99], 0\n\t" \
  "ds_read_b64_tr_b16 v[218:219], v248 offset:42536\n\t" \
  "v_cvt_pk_f16_f32 v220, |v146|, |v147| clamp\n\t" \
  "s_cbranch_vccnz .Lcnx_p10_3\n\ts_setprio 3\n.Lcnx_p10_3:\n\t" \
  "v_cvt_pknorm_i16_f32 v222, |v144|, |v145|\n\t" \
  "v_mfma_f32_16x16x32_bf16 v[156:159], v[208:211], v[100:103], 0\n\t" \
  "s_waitcnt vmcnt(1)\n\t" \
  "s_cmp_lg_u32 s58, 1\n\t" \
  "s_cbranch_scc1 .Lcnx_c18\n\t" \
  "s_waitcnt vmcnt(0)\n\t" \
  ".Lcnx_c18:\n\t" \
  "s_mov_b64 exec, 3\n\t" \
  "ds_add_rtn_u32 v246, v251, v250 offset:47216\n\t" \
  "s_mov_b64 exec, -1\n\t" \
  "ds_read_b32 v234, v234 offset:0\n\t" \
  "v_cvt_pk_f16_f32 v224, |v154|, |v155| clamp\n\t" \
  "v_cvt_pknorm_i16_f32 v226, |v152|, |v153|\n\t" \
  "v_mfma_f32_16x16x32_bf16 v[164:167], v[208:211], v[104:107], 0\n\t" \
  "v_and_b32_sdwa v221, s56, v220 dst_sel:DWORD dst_unused:UNUSED_PAD src0_sel:DWORD src1_sel:WORD_1\n\t" \
  "v_and_b32 v220, s56, v220\n\t" \
  "v_mfma_f32_16x16x32_bf16 v[172:175], v[208:211], v[108:111], 0\n\t" \
  "s_waitcnt lgkmcnt(1)\n\t" \
  "v_readfirstlane_b32 s4, v246\n\t" \
  "s_cmp_lt_u32 s4, s57\n\t" \
  "s_cbranch_scc1 .Lcnx_w120\n.Lcnx_b120:\n\t" \
  "ds_read_b64_tr_b16 v[208:209], v249 offset:30752\n\t" \
  "v_lshlrev_b32_sdwa v223, 2, v222 dst_sel:DWORD dst_unused:UNUSED_PAD src0_sel:DWORD src1_sel:BYTE_3\n\t" \
  "v_lshlrev_b32_sdwa v222, 2, v222 dst_sel:DWORD dst_unused:UNUSED_PAD src0_sel:DWORD src1_sel:BYTE_1\n\t" \
  "v_and_b32_sdwa v225, s56, v224 dst_sel:DWORD dst_unused:UNUSED_PAD src0_sel:DWORD src1_sel:WORD_1\n\t" \
  "v_mfma_f32_16x16x32_bf16 v[148:151], v[212:215], v[112:115], v[148:151]\n\t" \
  "ds_read_b64_tr_b16 v[210:211], v249 offset:32288\n\t" \
  "v_and_b32 v224, s56, v224\n\t" \
  "v_lshlrev_b32_sdwa v227, 2, v226 dst_sel:DWORD dst_unused:UNUSED_PAD src0_sel:DWORD src1_sel:BYTE_3\n\t" \
  "v_mfma_f32_16x16x32_bf16 v[156:159], v[212:215], v[116:119], v[156:159]\n\t" \
  "s_cmp_eq_u32 s37, 0\n\t" \
  "s_cbranch_scc1 .Lcnx_f11\n.Lcnx_g11:\n\t" \
  "ds_read_b32 v221, v221 offset:0\n\t" \
  "ds_read_b32 v220, v220 offset:0\n\t" \
  "v_lshlrev_b32_sdwa v226, 2, v226 dst_sel:DWORD dst_unused:UNUSED_PAD src0_sel:DWORD src1_sel:BYTE_1\n\t" \
  "v_fma_f32 v197, |v197|, v229, v197 mul:4\n\t" \
  "v_mfma_f32_16x16x32_bf16 v[164:167], v[212:215], v[120:123], v[164:167]\n\t" \
  "ds_read_b32 v223, v223 offset:15376\n\t" \
  "ds_read_b32 v222, v222 offset:15376\n\t" \
  "v_fma_f32 v196, |v196|, v228, v196 mul:4\n\t" \
  "v_fma_mix_f32 v231, |v199|, v231, v231 op_sel:[0,1,0] op_sel_hi:[0,1,1] clamp\n\t" \
  "v_mfma_f32_16x16x32_bf16 v[172:175], v[212:215], v[124:127], v[172:175]\n\t" \
  "ds_read_b64_tr_b16 v[212:213], v249 offset:30760\n\t" \
  "ds_read_b64_tr_b16 v[214:215], v249 offset:32296\n\t" \
  "v_fma_mix_f32 v230, |v198|, v230, v230 op_sel:[0,1,0] op_sel_hi:[0,1,1] clamp\n\t" \
  "s_waitcnt lgkmcnt(8)\n\t" \
  "v_fma_f32 v205, |v205|, v233, v205 mul:4\n\t" \
  "v_mfma_f32_16x16x32_bf16 v[148:151], v[216:219], v[128:131], v[148:151]\n\t" \
  "ds_read_b32 v225, v225 offset:0\n\t" \
  "v_fma_f32 v204, |v204|, v232, v204 mul:4\n\t" \
  "v_fma_f32 v207, |v207|, v235, v207 mul:4\n\t" \
  "v_mfma_f32_16x16x32_bf16 v[156:159], v[216:219], v[132:135], v[156:159]\n\t" \
  "ds_read_b32 v224, v224 offset:0\n\t" \
  "v_fma_f32 v206, |v206|, v234, v206 mul:4\n\t" \
  "v_fma_f32 v199, |v199|, v231, v199 mul:4\n\t" \
  "v_mfma_f32_16x16x32_bf16 v[164:167], v[216:219], v[136:139], v[164:167]\n\t" \
  "ds_read_b32 v227, v227 offset:15376\n\t" \
  "v_fma_f32 v198, |v198|, v230, v198 mul:4\n\t" \
  "v_cvt_pk_bf16_f32 v196, v196, v197\n\t" \
  "v_mfma_f32_16x16x32_bf16 v[172:175], v[216:219], v[140:143], v[172:175]\n\t" \
  "ds_read_b64_tr_b16 v[216:217], v249 offset:30816\n\t" \
  "v_cvt_pk_bf16_f32 v197, v198, v199\n\t" \
  "v_cvt_pk_bf16_f32 v204, v204, v205\n\t" \
  "v_cvt_pk_bf16_f32 v205, v206, v207\n\t" \
  "s_cbranch_vccnz .Lcnx_p11_0\n\ts_setprio 0\n.Lcnx_p11_0:\n\t" \
  "v_cvt_pk_f16_f32 v228, |v162|, |v163| clamp\n\t" \
  "v_cvt_pknorm_i16_f32 v230, |v160|, |v161|\n\t" \
  "v_mfma_f32_16x16x32_bf16 v[72:75], v[242:245], v[236:239], v[72:75]\n\t" \
  "ds_read_b64_tr_b16 v[218:219], v249 offset:32352\n\t" \
  "v_cvt_pk_f16_f32 v232, |v170|, |v171| clamp\n\t" \
  "v_cvt_pk_f16_f32 v234, |v168|, |v169| clamp\n\t" \
  "v_mfma_f32_16x16x32_bf16 v[76:79], v[240:243], v[236:239], v[76:79]\n\t" \
  "buffer_load_dwordx4 v[236:239], v250, s[8:11], s13 offen offset:64 nt\n\t" \
  "s_cmp_lg_u32 s38, 4\n\t" \
  "s_cbranch_scc1 .Lcnx_c30\n\t" \
  "s_mov_b32 m0, 163632\n\t" \
  "s_add_u32 s30, s30, s31\n\t" \
  "buffer_load_dword v252, s[8:11], s30 offen lds\n\t" \
  ".Lcnx_c30:\n\t" \
  "ds_read_b32 v226, v226 offset:15376\n\t" \
  "v_and_b32_sdwa v229, s56, v228 dst_sel:DWORD dst_unused:UNUSED_PAD src0_sel:DWORD src1_sel:WORD_1\n\t" \
  "v_and_b32 v228, s56, v228\n\t" \
  "s_waitcnt lgkmcnt(12)\n\t" \
  "v_mfma_f32_16x16x32_bf16 v[0:3], v[208:211], v[178:181], v[0:3]\n\t" \
  "ds_read_b32 v229, v229 offset:0\n\t" \
  "v_lshlrev_b32_sdwa v231, 2, v230 dst_sel:DWORD dst_unused:UNUSED_PAD src0_sel:DWORD src1_sel:BYTE_3\n\t" \
  "v_lshlrev_b32_sdwa v230, 2, v230 dst_sel:DWORD dst_unused:UNUSED_PAD src0_sel:DWORD src1_sel:BYTE_1\n\t" \
  "v_mfma_f32_16x16x32_bf16 v[24:27], v[208:211], v[186:189], v[24:27]\n\t" \
  "ds_read_b32 v228, v228 offset:0\n\t" \
  "v_and_b32_sdwa v233, s56, v232 dst_sel:DWORD dst_unused:UNUSED_PAD src0_sel:DWORD src1_sel:WORD_1\n\t" \
  "v_and_b32 v232, s56, v232\n\t" \
  "v_mfma_f32_16x16x32_bf16 v[48:51], v[208:211], v[194:197], v[48:51]\n\t" \
  "ds_read_b32 v231, v231 offset:15376\n\t" \
  "v_and_b32_sdwa v235, s56, v234 dst_sel:DWORD dst_unused:UNUSED_PAD src0_sel:DWORD src1_sel:WORD_1\n\t" \
  "v_and_b32 v234, s56, v234\n\t" \
  "v_mfma_f32_16x16x32_bf16 v[72:75], v[208:211], v[202:205], v[72:75]\n\t" \
  "ds_read_b64_tr_b16 v[208:209], v249 offset:30824\n\t" \
  "ds_read_b64_tr_b16 v[210:211], v249 offset:32360\n\t" \
  "s_waitcnt lgkmcnt(13)\n\t" \
  "v_fma_f32 v147, |v147|, v221, v147 mul:4\n\t" \
  "v_fma_f32 v146, |v146|, v220, v146 mul:4\n\t" \
  "s_waitcnt lgkmcnt(11)\n\t" \
  "v_mfma_f32_16x16x32_bf16 v[4:7], v[212:215], v[178:181], v[4:7]\n\t" \
  "ds_read_b32 v230, v230 offset:15376\n\t" \
  "v_fma_mix_f32 v223, |v145|, v223, v223 op_sel:[0,1,0] op_sel_hi:[0,1,1] clamp\n\t" \
  "v_fma_mix_f32 v222, |v144|, v222, v222 op_sel:[0,1,0] op_sel_hi:[0,1,1] clamp\n\t" \
  "v_mfma_f32_16x16x32_bf16 v[28:31], v[212:215], v[186:189], v[28:31]\n\t" \
  "ds_read_b32 v233, v233 offset:0\n\t" \
  "s_waitcnt lgkmcnt(7)\n\t" \
  "v_fma_f32 v155, |v155|, v225, v155 mul:4\n\t" \
  "v_fma_f32 v154, |v154|, v224, v154 mul:4\n\t" \
  "v_mfma_f32_16x16x32_bf16 v[52:55], v[212:215], v[194:197], v[52:55]\n\t" \
  "ds_read_b32 v232, v232 offset:0\n\t" \
  "v_fma_mix_f32 v227, |v153|, v227, v227 op_sel:[0,1,0] op_sel_hi:[0,1,1] clamp\n\t" \
  "v_fma_mix_f32 v226, |v152|, v226, v226 op_sel:[0,1,0] op_sel_hi:[0,1,1] clamp\n\t" \
  "v_fma_f32 v145, |v145|, v223, v145 mul:4\n\t" \
  "v_mfma_f32_16x16x32_bf16 v[76:79], v[212:215], v[202:205], v[76:79]\n\t" \
  "ds_read_b64_tr_b16 v[212:213], v249 offset:30880\n\t" \
  "v_fma_f32 v144, |v144|, v222, v144 mul:4\n\t" \
  "v_fma_f32 v153, |v153|, v227, v153 mul:4\n\t" \
  "v_mfma_f32_16x16x32_bf16 v[8:11], v[216:219], v[178:181], v[8:11]\n\t" \
  "ds_read_b64_tr_b16 v[214:215], v249 offset:32416\n\t" \
  "v_fma_f32 v152, |v152|, v226, v152 mul:4\n\t" \
  "v_cvt_pk_bf16_f32 v147, v146, v147\n\t" \
  "v_mfma_f32_16x16x32_bf16 v[32:35], v[216:219], v[186:189], v[32:35]\n\t" \
  "ds_read_b32 v235, v235 offset:0\n\t" \
  "v_cvt_pk_bf16_f32 v146, v144, v145\n\t" \
  "v_cvt_pk_bf16_f32 v155, v154, v155\n\t" \
  "v_mfma_f32_16x16x32_bf16 v[56:59], v[216:219], v[194:197], v[56:59]\n\t" \
  "ds_read_b32 v234, v234 offset:0\n\t" \
  "v_cvt_pk_bf16_f32 v154, v152, v153\n\t" \
  "s_cbranch_vccnz .Lcnx_p11_1\n\ts_setprio 1\n.Lcnx_p11_1:\n\t" \
  "v_cvt_pk_f16_f32 v220, |v148|, |v149| clamp\n\t" \
  "v_mfma_f32_16x16x32_bf16 v[80:83], v[216:219], v[202:205], v[80:83]\n\t" \
  "ds_read_b64_tr_b16 v[216:217], v249 offset:30888\n\t" \
  "v_cvt_pknorm_i16_f32 v222, |v150|, |v151|\n\t" \
  "v_cvt_pk_f16_f32 v224, |v156|, |v157| clamp\n\t" \
  "s_waitcnt lgkmcnt(8)\n\t" \
  "v_mfma_f32_16x16x32_bf16 v[12:15], v[208:211], v[178:181], v[12:15]\n\t" \
  "ds_read_b64_tr_b16 v[218:219], v249 offset:32424\n\t" \
  "v_cvt_pknorm_i16_f32 v226, |v158|, |v159|\n\t" \
  "v_and_b32_sdwa v221, s56, v220 dst_sel:DWORD dst_unused:UNUSED_PAD src0_sel:DWORD src1_sel:WORD_1\n\t" \
  "v_mfma_f32_16x16x32_bf16 v[36:39], v[208:211], v[186:189], v[36:39]\n\t" \
  "v_and_b32 v220, s56, v220\n\t" \
  "v_lshlrev_b32_sdwa v223, 2, v222 dst_sel:DWORD dst_unused:UNUSED_PAD src0_sel:DWORD src1_sel:BYTE_3\n\t" \
  "v_mfma_f32_16x16x32_bf16 v[60:63], v[208:211], v[194:197], v[60:63]\n\t" \
  "ds_read_b32 v221, v221 offset:0\n\t" \
  "v_lshlrev_b32_sdwa v222, 2, v222 dst_sel:DWORD dst_unused:UNUSED_PAD src0_sel:DWORD src1_sel:BYTE_1\n\t" \
  "v_and_b32_sdwa v225, s56, v224 dst_sel:DWORD dst_unused:UNUSED_PAD src0_sel:DWORD src1_sel:WORD_1\n\t" \
  "v_and_b32 v224, s56, v224\n\t" \
  "v_mfma_f32_16x16x32_bf16 v[84:87], v[208:211], v[202:205], v[84:87]\n\t" \
  "ds_read_b64_tr_b16 v[208:209], v248 offset:0\n\t" \
  "ds_read_b64_tr_b16 v[210:211], v248 offset:1024\n\t" \
  "v_lshlrev_b32_sdwa v227, 2, v226 dst_sel:DWORD dst_unused:UNUSED_PAD src0_sel:DWORD src1_sel:BYTE_3\n\t" \
  "v_lshlrev_b32_sdwa v226, 2, v226 dst_sel:DWORD dst_unused:UNUSED_PAD src0_sel:DWORD src1_sel:BYTE_1\n\t" \
  "s_waitcnt lgkmcnt(7)\n\t" \
  "v_mfma_f32_16x16x32_bf16 v[16:19], v[212:215], v[178:181], v[16:19]\n\t" \
  "ds_read_b32 v220, v220 offset:0\n\t" \
  "ds_read_b32 v223, v223 offset:15376\n\t" \
  "v_fma_f32 v163, |v163|, v229, v163 mul:4\n\t" \
  "v_fma_f32 v162, |v162|, v228, v162 mul:4\n\t" \
  "v_mfma_f32_16x16x32_bf16 v[40:43], v[212:215], v[186:189], v[40:43]\n\t" \
  "ds_read_b32 v222, v222 offset:15376\n\t" \
  "v_fma_mix_f32 v231, |v161|, v231, v231 op_sel:[0,1,0] op_sel_hi:[0,1,1] clamp\n\t" \
  "v_fma_mix_f32 v230, |v160|, v230, v230 op_sel:[0,1,0] op_sel_hi:[0,1,1] clamp\n\t" \
  "v_mfma_f32_16x16x32_bf16 v[64:67], v[212:215], v[194:197], v[64:67]\n\t" \
  "ds_read_b32 v225, v225 offset:0\n\t" \
  "s_waitcnt lgkmcnt(9)\n\t" \
  "v_fma_f32 v171, |v171|, v233, v171 mul:4\n\t" \
  "v_fma_f32 v170, |v170|, v232, v170 mul:4\n\t" \
  "v_mfma_f32_16x16x32_bf16 v[88:91], v[212:215], v[202:205], v[88:91]\n\t" \
  "ds_read_b64_tr_b16 v[212:213], v248 offset:3072\n\t" \
  "v_fma_f32 v169, |v169|, v235, v169 mul:4\n\t" \
  "v_fma_f32 v168, |v168|, v234, v168 mul:4\n\t" \
  "s_waitcnt lgkmcnt(8)\n\t" \
  "v_mfma_f32_16x16x32_bf16 v[20:23], v[216:219], v[178:181], v[20:23]\n\t" \
  "ds_read_b64_tr_b16 v[214:215], v248 offset:4096\n\t" \
  "v_fma_f32 v161, |v161|, v231, v161 mul:4\n\t" \
  "v_fma_f32 v160, |v160|, v230, v160 mul:4\n\t" \
  "v_mfma_f32_16x16x32_bf16 v[44:47], v[216:219], v[186:189], v[44:47]\n\t" \
  "ds_read_b32 v224, v224 offset:0\n\t" \
  "v_cvt_pk_bf16_f32 v163, v162, v163\n\t" \
  "v_cvt_pk_bf16_f32 v162, v160, v161\n\t" \
  "v_mfma_f32_16x16x32_bf16 v[68:71], v[216:219], v[194:197], v[68:71]\n\t" \
  "ds_read_b32 v227, v227 offset:15376\n\t" \
  "v_cvt_pk_bf16_f32 v171, v170, v171\n\t" \
  "v_cvt_pk_bf16_f32 v170, v168, v169\n\t" \
  "v_cvt_pk_f16_f32 v228, |v164|, |v165| clamp\n\t" \
  "v_mfma_f32_16x16x32_bf16 v[92:95], v[216:219], v[202:205], v[92:95]\n\t" \
  "ds_read_b64_tr_b16 v[216:217], v248 offset:2048\n\t" \
  "v_cvt_pknorm_i16_f32 v230, |v166|, |v167|\n\t" \
  "s_cbranch_vccnz .Lcnx_p11_2\n\ts_setprio 2\n.Lcnx_p11_2:\n\t" \
  "v_cvt_pk_f16_f32 v232, |v172|, |v173| clamp\n\t" \
  "s_waitcnt lgkmcnt(9)\n\t" \
  "v_mfma_f32_16x16x32_bf16 v[176:179], v[208:211], v[96:99], 0\n\t" \
  "ds_read_b64_tr_b16 v[218:219], v248 offset:5120\n\t" \
  "v_cvt_pk_f16_f32 v234, |v174|, |v175| clamp\n\t" \
  "v_and_b32_sdwa v229, s56, v228 dst_sel:DWORD dst_unused:UNUSED_PAD src0_sel:DWORD src1_sel:WORD_1\n\t" \
  "v_mfma_f32_16x16x32_bf16 v[184:187], v[208:211], v[100:103], 0\n\t" \
  "ds_read_b32 v226, v226 offset:15376\n\t" \
  "v_and_b32 v228, s56, v228\n\t" \
  "v_lshlrev_b32_sdwa v231, 2, v230 dst_sel:DWORD dst_unused:UNUSED_PAD src0_sel:DWORD src1_sel:BYTE_3\n\t" \
  "v_mfma_f32_16x16x32_bf16 v[192:195], v[208:211], v[104:107], 0\n\t" \
  "ds_read_b32 v229, v229 offset:0\n\t" \
  "v_lshlrev_b32_sdwa v230, 2, v230 dst_sel:DWORD dst_unused:UNUSED_PAD src0_sel:DWORD src1_sel:BYTE_1\n\t" \
  "v_and_b32_sdwa v233, s56, v232 dst_sel:DWORD dst_unused:UNUSED_PAD src0_sel:DWORD src1_sel:WORD_1\n\t" \
  "v_mfma_f32_16x16x32_bf16 v[200:203], v[208:211], v[108:111], 0\n\t" \
  "ds_read_b64_tr_b16 v[208:209], v248 offset:512\n\t" \
  "v_and_b32 v232, s56, v232\n\t" \
  "v_and_b32_sdwa v235, s56, v234 dst_sel:DWORD dst_unused:UNUSED_PAD src0_sel:DWORD src1_sel:WORD_1\n\t" \
  "s_waitcnt lgkmcnt(7)\n\t" \
  "v_mfma_f32_16x16x32_bf16 v[176:179], v[212:215], v[112:115], v[176:179]\n\t" \
  "ds_read_b64_tr_b16 v[210:211], v248 offset:1536\n\t" \
  "ds_read_b32 v228, v228 offset:0\n\t" \
  "v_and_b32 v234, s56, v234\n\t" \
  "v_fma_f32 v149, |v149|, v221, v149 mul:4\n\t" \
  "v_mfma_f32_16x16x32_bf16 v[184:187], v[212:215], v[116:119], v[184:187]\n\t" \
  "ds_read_b32 v231, v231 offset:15376\n\t" \
  "ds_read_b32 v230, v230 offset:15376\n\t" \
  "v_fma_f32 v148, |v148|, v220, v148 mul:4\n\t" \
  "v_fma_mix_f32 v223, |v151|, v223, v223 op_sel:[0,1,0] op_sel_hi:[0,1,1] clamp\n\t" \
  "v_mfma_f32_16x16x32_bf16 v[192:195], v[212:215], v[120:123], v[192:195]\n\t" \
  "ds_read_b32 v233, v233 offset:0\n\t" \
  "v_fma_mix_f32 v222, |v150|, v222, v222 op_sel:[0,1,0] op_sel_hi:[0,1,1] clamp\n\t" \
  "s_waitcnt lgkmcnt(7)\n\t" \
  "v_fma_f32 v157, |v157|, v225, v157 mul:4\n\t" \
  "v_mfma_f32_16x16x32_bf16 v[200:203], v[212:215], v[124:127], v[200:203]\n\t" \
  "ds_read_b64_tr_b16 v[212:213], v248 offset:3584\n\t" \
  "v_fma_f32 v156, |v156|, v224, v156 mul:4\n\t" \
  "v_fma_mix_f32 v227, |v159|, v227, v227 op_sel:[0,1,0] op_sel_hi:[0,1,1] clamp\n\t" \
  "v_fma_mix_f32 v226, |v158|, v226, v226 op_sel:[0,1,0] op_sel_hi:[0,1,1] clamp\n\t" \
  "v_mfma_f32_16x16x32_bf16 v[176:179], v[216:219], v[128:131], v[176:179]\n\t" \
  "ds_read_b64_tr_b16 v[214:215], v248 offset:4608\n\t" \
  "v_fma_f32 v151, |v151|, v223, v151 mul:4\n\t" \
  "v_fma_f32 v150, |v150|, v222, v150 mul:4\n\t" \
  "v_mfma_f32_16x16x32_bf16 v[184:187], v[216:219], v[132:135], v[184:187]\n\t" \
  "ds_read_b32 v232, v232 offset:0\n\t" \
  "v_fma_f32 v159, |v159|, v227, v159 mul:4\n\t" \
  "v_fma_f32 v158, |v158|, v226, v158 mul:4\n\t" \
  "v_mfma_f32_16x16x32_bf16 v[192:195], v[216:219], v[136:139], v[192:195]\n\t" \
  "ds_read_b32 v235, v235 offset:0\n\t" \
  "v_cvt_pk_bf16_f32 v148, v148, v149\n\t" \
  "v_cvt_pk_bf16_f32 v149, v150, v151\n\t" \
  "v_mfma_f32_16x16x32_bf16 v[200:203], v[216:219], v[140:143], v[200:203]\n\t" \
  "ds_read_b64_tr_b16 v[216:217], v248 offset:2560\n\t" \
  "v_cvt_pk_bf16_f32 v156, v156, v157\n\t" \
  "v_cvt_pk_bf16_f32 v157, v158, v159\n\t" \
  "s_waitcnt lgkmcnt(9)\n\t" \
  "v_mfma_f32_16x16x32_bf16 v[180:183], v[208:211], v[96:99], 0\n\t" \
  "ds_read_b64_tr_b16 v[218:219], v248 offset:5632\n\t" \
  "v_cvt_pk_f16_f32 v220, |v178|, |v179| clamp\n\t" \
  "s_cbranch_vccnz .Lcnx_p11_3\n\ts_setprio 3\n.Lcnx_p11_3:\n\t" \
  "v_cvt_pknorm_i16_f32 v222, |v176|, |v177|\n\t" \
  "v_mfma_f32_16x16x32_bf16 v[188:191], v[208:211], v[100:103], 0\n\t" \
  "s_waitcnt vmcnt(0)\n\t" \
  "s_mov_b64 exec, 3\n\t" \
  "ds_add_rtn_u32 v246, v251, v250 offset:47224\n\t" \
  "s_mov_b64 exec, -1\n\t" \
  "ds_read_b32 v234, v234 offset:0\n\t" \
  "v_cvt_pk_f16_f32 v224, |v186|, |v187| clamp\n\t" \
  "v_cvt_pknorm_i16_f32 v226, |v184|, |v185|\n\t" \
  "v_mfma_f32_16x16x32_bf16 v[196:199], v[208:211], v[104:107], 0\n\t" \
  "v_and_b32_sdwa v221, s56, v220 dst_sel:DWORD dst_unused:UNUSED_PAD src0_sel:DWORD src1_sel:WORD_1\n\t" \
  "v_and_b32 v220, s56, v220\n\t" \
  "v_mfma_f32_16x16x32_bf16 v[204:207], v[208:211], v[108:111], 0\n\t" \
  "s_waitcnt lgkmcnt(1)\n\t" \
  "v_readfirstlane_b32 s4, v246\n\t" \
  "s_cmp_lt_u32 s4, s57\n\t" \
  "s_cbranch_scc1 .Lcnx_w132\n.Lcnx_b132:\n\t" \
  "ds_read_b64_tr_b16 v[208:209], v249 offset:6152\n\t" \
  "v_lshlrev_b32_sdwa v223, 2, v222 dst_sel:DWORD dst_unused:UNUSED_PAD src0_sel:DWORD src1_sel:BYTE_3\n\t" \
  "v_lshlrev_b32_sdwa v222, 2, v222 dst_sel:DWORD dst_unused:UNUSED_PAD src0_sel:DWORD src1_sel:BYTE_1\n\t" \
  "v_and_b32_sdwa v225, s56, v224 dst_sel:DWORD dst_unused:UNUSED_PAD src0_sel:DWORD src1_sel:WORD_1\n\t" \
  "v_mfma_f32_16x16x32_bf16 v[180:183], v[212:215], v[112:115], v[180:183]\n\t" \
  "ds_read_b64_tr_b16 v[210:211], v249 offset:7688\n\t" \
  "v_and_b32 v224, s56, v224\n\t" \
  "v_lshlrev_b32_sdwa v227, 2, v226 dst_sel:DWORD dst_unused:UNUSED_PAD src0_sel:DWORD src1_sel:BYTE_3\n\t" \
  "v_mfma_f32_16x16x32_bf16 v[188:191], v[212:215], v[116:119], v[188:191]\n\t" \
  "ds_read_b32 v221, v221 offset:0\n\t" \
  "ds_read_b32 v220, v220 offset:0\n\t" \
  "v_lshlrev_b32_sdwa v226, 2, v226 dst_sel:DWORD dst_unused:UNUSED_PAD src0_sel:DWORD src1_sel:BYTE_1\n\t" \
  "v_fma_f32 v165, |v165|, v229, v165 mul:4\n\t" \
  "v_mfma_f32_16x16x32_bf16 v[196:199], v[212:215], v[120:123], v[196:199]\n\t" \
  "ds_read_b32 v223, v223 offset:15376\n\t" \
  "ds_read_b32 v222, v222 offset:15376\n\t" \
  "v_fma_f32 v164, |v164|, v228, v164 mul:4\n\t" \
  "v_fma_mix_f32 v231, |v167|, v231, v231 op_sel:[0,1,0] op_sel_hi:[0,1,1] clamp\n\t" \
  "v_mfma_f32_16x16x32_bf16 v[204:207], v[212:215], v[124:127], v[204:207]\n\t" \
  "ds_read_b64_tr_b16 v[212:213], v249 offset:6160\n\t" \
  "ds_read_b64_tr_b16 v[214:215], v249 offset:7696\n\t" \
  "v_fma_mix_f32 v230, |v166|, v230, v230 op_sel:[0,1,0] op_sel_hi:[0,1,1] clamp\n\t" \
  "s_waitcnt lgkmcnt(8)\n\t" \
  "v_fma_f32 v173, |v173|, v233, v173 mul:4\n\t" \
  "v_mfma_f32_16x16x32_bf16 v[180:183], v[216:219], v[128:131], v[180:183]\n\t" \
  "ds_read_b32 v225, v225 offset:0\n\t" \
  "v_fma_f32 v172, |v172|, v232, v172 mul:4\n\t" \
  "v_fma_f32 v175, |v175|, v235, v175 mul:4\n\t" \
  "v_mfma_f32_16x16x32_bf16 v[188:191], v[216:219], v[132:135], v[188:191]\n\t" \
  "ds_read_b32 v224, v224 offset:0\n\t" \
  "v_fma_f32 v174, |v174|, v234, v174 mul:4\n\t" \
  "v_fma_f32 v167, |v167|, v231, v167 mul:4\n\t" \
  "v_mfma_f32_16x16x32_bf16 v[196:199], v[216:219], v[136:139], v[196:199]\n\t" \
  "ds_read_b32 v227, v227 offset:15376\n\t" \
  "v_fma_f32 v166, |v166|, v230, v166 mul:4\n\t" \
  "v_cvt_pk_bf16_f32 v164, v164, v165\n\t" \
  "v_mfma_f32_16x16x32_bf16 v[204:207], v[216:219], v[140:143], v[204:207]\n\t" \
  "ds_read_b64_tr_b16 v[216:217], v249 offset:6216\n\t" \
  "v_cvt_pk_bf16_f32 v165, v166, v167\n\t" \
  "v_cvt_pk_bf16_f32 v172, v172, v173\n\t" \
  "v_cvt_pk_bf16_f32 v173, v174, v175\n\t" \
  "s_cbranch_vccnz .Lcnx_p12_0\n\ts_setprio 3\n.Lcnx_p12_0:\n\t" \
  "v_cvt_pk_f16_f32 v228, |v194|, |v195| clamp\n\t" \
  "v_cvt_pknorm_i16_f32 v230, |v192|, |v193|\n\t" \
  "v_mfma_f32_16x16x32_bf16 v[80:83], v[242:245], v[236:239], v[80:83]\n\t" \
  "ds_read_b64_tr_b16 v[218:219], v249 offset:7752\n\t" \
  "v_cvt_pk_f16_f32 v232, |v202|, |v203| clamp\n\t" \
  "v_cvt_pk_f16_f32 v234, |v200|, |v201| clamp\n\t" \
  "v_and_b32_sdwa v229, s56, v228 dst_sel:DWORD dst_unused:UNUSED_PAD src0_sel:DWORD src1_sel:WORD_1\n\t" \
  "v_mfma_f32_16x16x32_bf16 v[84:87], v[240:243], v[236:239], v[84:87]\n\t" \
  "buffer_load_dwordx4 v[236:239], v250, s[8:11], s13 offen offset:128 nt\n\t" \
  "s_cmp_lg_u32 s38, 5\n\t" \
  "s_cbranch_scc1 .Lcnx_c31\n\t" \
  "s_mov_b32 m0, 163632\n\t" \
  "s_add_u32 s30, s30, s31\n\t" \
  "buffer_load_dword v252, s[8:11], s30 offen lds\n\t" \
  ".Lcnx_c31:\n\t" \
  "ds_read_b32 v226, v226 offset:15376\n\t" \
  "v_and_b32 v228, s56, v228\n\t" \
  "v_lshlrev_b32_sdwa v231, 2, v230 dst_sel:DWORD dst_unused:UNUSED_PAD src0_sel:DWORD src1_sel:BYTE_3\n\t" \
  "v_lshlrev_b32_sdwa v230, 2, v230 dst_sel:DWORD dst_unused:UNUSED_PAD src0_sel:DWORD src1_sel:BYTE_1\n\t" \
  "v_and_b32_sdwa v233, s56, v232 dst_sel:DWORD dst_unused:UNUSED_PAD src0_sel:DWORD src1_sel:WORD_1\n\t" \
  "s_waitcnt lgkmcnt(12)\n\t" \
  "v_mfma_f32_16x16x32_bf16 v[0:3], v[208:211], v[146:149], v[0:3]\n\t" \
  "ds_read_b32 v229, v229 offset:0\n\t" \
  "v_and_b32 v232, s56, v232\n\t" \
  "v_and_b32_sdwa v235, s56, v234 dst_sel:DWORD dst_unused:UNUSED_PAD src0_sel:DWORD src1_sel:WORD_1\n\t" \
  "v_and_b32 v234, s56, v234\n\t" \
  "v_mfma_f32_16x16x32_bf16 v[24:27], v[208:211], v[154:157], v[24:27]\n\t" \
  "ds_read_b32 v228, v228 offset:0\n\t" \
  "ds_read_b32 v231, v231 offset:15376\n\t" \
  "s_waitcnt lgkmcnt(11)\n\t" \
  "v_fma_f32 v179, |v179|, v221, v179 mul:4\n\t" \
  "v_fma_f32 v178, |v178|, v220, v178 mul:4\n\t" \
  "v_fma_mix_f32 v223, |v177|, v223, v223 op_sel:[0,1,0] op_sel_hi:[0,1,1] clamp\n\t" \
  "v_fma_mix_f32 v222, |v176|, v222, v222 op_sel:[0,1,0] op_sel_hi:[0,1,1] clamp\n\t" \
  "v_mfma_f32_16x16x32_bf16 v[48:51], v[208:211], v[162:165], v[48:51]\n\t" \
  "ds_read_b32 v230, v230 offset:15376\n\t" \
  "s_waitcnt lgkmcnt(4)\n\t" \
  "v_fma_f32 v187, |v187|, v225, v187 mul:4\n\t" \
  "v_fma_f32 v186, |v186|, v224, v186 mul:4\n\t" \
  "v_fma_mix_f32 v227, |v185|, v227, v227 op_sel:[0,1,0] op_sel_hi:[0,1,1] clamp\n\t" \
  "v_fma_mix_f32 v226, |v184|, v226, v226 op_sel:[0,1,0] op_sel_hi:[0,1,1] clamp\n\t" \
  "v_mfma_f32_16x16x32_bf16 v[72:75], v[208:211], v[170:173], v[72:75]\n\t" \
  "ds_read_b64_tr_b16 v[208:209], v249 offset:6224\n\t" \
  "ds_read_b64_tr_b16 v[210:211], v249 offset:7760\n\t" \
  "v_fma_f32 v177, |v177|, v223, v177 mul:4\n\t" \
  "v_fma_f32 v176, |v176|, v222, v176 mul:4\n\t" \
  "v_fma_f32 v185, |v185|, v227, v185 mul:4\n\t" \
  "v_mfma_f32_16x16x32_bf16 v[4:7], v[212:215], v[146:149], v[4:7]\n\t" \
  "ds_read_b32 v233, v233 offset:0\n\t" \
  "v_fma_f32 v184, |v184|, v226, v184 mul:4\n\t" \
  "v_cvt_pk_bf16_f32 v179, v178, v179\n\t" \
  "s_cbranch_vccnz .Lcnx_p12_1\n\ts_setprio 2\n.Lcnx_p12_1:\n\t" \
  "v_cvt_pk_bf16_f32 v178, v176, v177\n\t" \
  "v_cvt_pk_bf16_f32 v187, v186, v187\n\t" \
  "v_mfma_f32_16x16x32_bf16 v[28:31], v[212:215], v[154:157], v[28:31]\n\t" \
  "ds_read_b32 v232, v232 offset:0\n\t" \
  "v_cvt_pk_bf16_f32 v186, v184, v185\n\t" \
  "v_cvt_pk_f16_f32 v220, |v180|, |v181| clamp\n\t" \
  "v_cvt_pknorm_i16_f32 v222, |v182|, |v183|\n\t" \
  "v_mfma_f32_16x16x32_bf16 v[52:55], v[212:215], v[162:165], v[52:55]\n\t" \
  "ds_read_b32 v235, v235 offset:0\n\t" \
  "v_cvt_pk_f16_f32 v224, |v188|, |v189| clamp\n\t" \
  "v_cvt_pknorm_i16_f32 v226, |v190|, |v191|\n\t" \
  "v_and_b32_sdwa v221, s56, v220 dst_sel:DWORD dst_unused:UNUSED_PAD src0_sel:DWORD src1_sel:WORD_1\n\t" \
  "v_and_b32 v220, s56, v220\n\t" \
  "v_mfma_f32_16x16x32_bf16 v[76:79], v[212:215], v[170:173], v[76:79]\n\t" \
  "ds_read_b64_tr_b16 v[212:213], v249 offset:6280\n\t" \
  "v_lshlrev_b32_sdwa v223, 2, v222 dst_sel:DWORD dst_unused:UNUSED_PAD src0_sel:DWORD src1_sel:BYTE_3\n\t" \
  "v_lshlrev_b32_sdwa v222, 2, v222 dst_sel:DWORD dst_unused:UNUSED_PAD src0_sel:DWORD src1_sel:BYTE_1\n\t" \
  "v_and_b32_sdwa v225, s56, v224 dst_sel:DWORD dst_unused:UNUSED_PAD src0_sel:DWORD src1_sel:WORD_1\n\t" \
  "v_and_b32 v224, s56, v224\n\t" \
  "v_mfma_f32_16x16x32_bf16 v[8:11], v[216:219], v[146:149], v[8:11]\n\t" \
  "ds_read_b64_tr_b16 v[214:215], v249 offset:7816\n\t" \
  "ds_read_b32 v234, v234 offset:0\n\t" \
  "v_lshlrev_b32_sdwa v227, 2, v226 dst_sel:DWORD dst_unused:UNUSED_PAD src0_sel:DWORD src1_sel:BYTE_3\n\t" \
  "v_lshlrev_b32_sdwa v226, 2, v226 dst_sel:DWORD dst_unused:UNUSED_PAD src0_sel:DWORD src1_sel:BYTE_1\n\t" \
  "s_waitcnt lgkmcnt(8)\n\t" \
  "v_fma_f32 v195, |v195|, v229, v195 mul:4\n\t" \
  "v_mfma_f32_16x16x32_bf16 v[32:35], v[216:219], v[154:157], v[32:35]\n\t" \
  "ds_read_b32 v221, v221 offset:0\n\t" \
  "ds_read_b32 v220, v220 offset:0\n\t" \
  "v_fma_f32 v194, |v194|, v228, v194 mul:4\n\t" \
  "v_fma_mix_f32 v231, |v193|, v231, v231 op_sel:[0,1,0] op_sel_hi:[0,1,1] clamp\n\t" \
  "v_fma_mix_f32 v230, |v192|, v230, v230 op_sel:[0,1,0] op_sel_hi:[0,1,1] clamp\n\t" \
  "s_waitcnt lgkmcnt(2)\n\t" \
  "v_fma_f32 v203, |v203|, v233, v203 mul:4\n\t" \
  "v_mfma_f32_16x16x32_bf16 v[56:59], v[216:219], v[162:165], v[56:59]\n\t" \
  "ds_read_b32 v223, v223 offset:15376\n\t" \
  "ds_read_b32 v222, v222 offset:15376\n\t" \
  "v_fma_f32 v202, |v202|, v232, v202 mul:4\n\t" \
  "v_fma_f32 v201, |v201|, v235, v201 mul:4\n\t" \
  "v_fma_f32 v200, |v200|, v234, v200 mul:4\n\t" \
  "v_fma_f32 v193, |v193|, v231, v193 mul:4\n\t" \
  "v_mfma_f32_16x16x32_bf16 v[80:83], v[216:219], v[170:173], v[80:83]\n\t" \
  "ds_read_b64_tr_b16 v[216:217], v249 offset:6288\n\t" \
  "ds_read_b64_tr_b16 v[218:219], v249 offset:7824\n\t" \
  "s_cbranch_vccnz .Lcnx_p12_2\n\ts_setprio 1\n.Lcnx_p12_2:\n\t" \
  "v_fma_f32 v192, |v192|, v230, v192 mul:4\n\t" \
  "v_cvt_pk_bf16_f32 v195, v194, v195\n\t" \
  "v_cvt_pk_bf16_f32 v194, v192, v193\n\t" \
  "v_mfma_f32_16x16x32_bf16 v[12:15], v[208:211], v[146:149], v[12:15]\n\t" \
  "ds_read_b32 v225, v225 offset:0\n\t" \
  "v_cvt_pk_bf16_f32 v203, v202, v203\n\t" \
  "v_cvt_pk_bf16_f32 v202, v200, v201\n\t" \
  "v_cvt_pk_f16_f32 v228, |v196|, |v197| clamp\n\t" \
  "v_cvt_pknorm_i16_f32 v230, |v198|, |v199|\n\t" \
  "v_mfma_f32_16x16x32_bf16 v[36:39], v[208:211], v[154:157], v[36:39]\n\t" \
  "s_waitcnt vmcnt(0)\n\t" \
  "s_mov_b64 exec, 3\n\t" \
  "ds_add_rtn_u32 v246, v251, v250 offset:47232\n\t" \
  "s_mov_b64 exec, -1\n\t" \
  "ds_read_b32 v224, v224 offset:0\n\t" \
  "v_cvt_pk_f16_f32 v232, |v204|, |v205| clamp\n\t" \
  "v_cvt_pk_f16_f32 v234, |v206|, |v207| clamp\n\t" \
  "v_and_b32_sdwa v229, s56, v228 dst_sel:DWORD dst_unused:UNUSED_PAD src0_sel:DWORD src1_sel:WORD_1\n\t" \
  "v_mfma_f32_16x16x32_bf16 v[60:63], v[208:211], v[162:165], v[60:63]\n\t" \
  "ds_read_b32 v227, v227 offset:15376\n\t" \
  "v_and_b32 v228, s56, v228\n\t" \
  "v_lshlrev_b32_sdwa v231, 2, v230 dst_sel:DWORD dst_unused:UNUSED_PAD src0_sel:DWORD src1_sel:BYTE_3\n\t" \
  "v_lshlrev_b32_sdwa v230, 2, v230 dst_sel:DWORD dst_unused:UNUSED_PAD src0_sel:DWORD src1_sel:BYTE_1\n\t" \
  "v_and_b32_sdwa v233, s56, v232 dst_sel:DWORD dst_unused:UNUSED_PAD src0_sel:DWORD src1_sel:WORD_1\n\t" \
  "v_mfma_f32_16x16x32_bf16 v[84:87], v[208:211], v[170:173], v[84:87]\n\t" \
  "s_waitcnt lgkmcnt(2)\n\t" \
  "v_readfirstlane_b32 s4, v246\n\t" \
  "s_cmp_lt_u32 s4, s57\n\t" \
  "s_cbranch_scc1 .Lcnx_w138\n.Lcnx_b138:\n\t" \
  "ds_read_b64_tr_b16 v[208:209], v249 offset:12304\n\t" \
  "ds_read_b64_tr_b16 v[210:211], v249 offset:13840\n\t" \
  "v_and_b32 v232, s56, v232\n\t" \
  "v_and_b32_sdwa v235, s56, v234 dst_sel:DWORD dst_unused:UNUSED_PAD src0_sel:DWORD src1_sel:WORD_1\n\t" \
  "v_and_b32 v234, s56, v234\n\t" \
  "ds_read_b32 v226, v226 offset:15376\n\t" \
  "ds_read_b32 v229, v229 offset:0\n\t" \
  "ds_read_b32 v228, v228 offset:0\n\t" \
  "ds_read_b32 v231, v231 offset:15376\n\t" \
  "ds_read_b32 v230, v230 offset:15376\n\t" \
  "ds_read_b32 v233, v233 offset:0\n\t" \
  "ds_read_b32 v232, v232 offset:0\n\t" \
  "ds_read_b32 v235, v235 offset:0\n\t" \
  "v_fma_f32 v181, |v181|, v221, v181 mul:4\n\t" \
  "s_cbranch_vccnz .Lcnx_p12_3\n\ts_setprio 0\n.Lcnx_p12_3:\n\t" \
  "v_mfma_f32_16x16x32_bf16 v[16:19], v[212:215], v[146:149], v[16:19]\n\t" \
  "ds_read_b32 v234, v234 offset:0\n\t" \
  "v_fma_f32 v180, |v180|, v220, v180 mul:4\n\t" \
  "v_fma_mix_f32 v223, |v183|, v223, v223 op_sel:[0,1,0] op_sel_hi:[0,1,1] clamp\n\t" \
  "v_fma_mix_f32 v222, |v182|, v222, v222 op_sel:[0,1,0] op_sel_hi:[0,1,1] clamp\n\t" \
  "v_mfma_f32_16x16x32_bf16 v[40:43], v[212:215], v[154:157], v[40:43]\n\t" \
  "s_waitcnt lgkmcnt(8)\n\t" \
  "v_fma_f32 v189, |v189|, v225, v189 mul:4\n\t" \
  "v_fma_f32 v188, |v188|, v224, v188 mul:4\n\t" \
  "v_fma_mix_f32 v227, |v191|, v227, v227 op_sel:[0,1,0] op_sel_hi:[0,1,1] clamp\n\t" \
  "v_fma_mix_f32 v226, |v190|, v226, v226 op_sel:[0,1,0] op_sel_hi:[0,1,1] clamp\n\t" \
  "v_mfma_f32_16x16x32_bf16 v[64:67], v[212:215], v[162:165], v[64:67]\n\t" \
  "v_fma_f32 v183, |v183|, v223, v183 mul:4\n\t" \
  "v_fma_f32 v182, |v182|, v222, v182 mul:4\n\t" \
  "v_fma_f32 v191, |v191|, v227, v191 mul:4\n\t" \
  "v_mfma_f32_16x16x32_bf16 v[88:91], v[212:215], v[170:173], v[88:91]\n\t" \
  "ds_read_b64_tr_b16 v[212:213], v249 offset:12312\n\t" \
  "v_fma_f32 v190, |v190|, v226, v190 mul:4\n\t" \
  "v_cvt_pk_bf16_f32 v180, v180, v181\n\t" \
  "v_cvt_pk_bf16_f32 v181, v182, v183\n\t" \
  "v_cvt_pk_bf16_f32 v188, v188, v189\n\t" \
  "v_mfma_f32_16x16x32_bf16 v[20:23], v[216:219], v[146:149], v[20:23]\n\t" \
  "ds_read_b64_tr_b16 v[214:215], v249 offset:13848\n\t" \
  "v_cvt_pk_bf16_f32 v189, v190, v191\n\t" \
  "s_waitcnt lgkmcnt(6)\n\t" \
  "v_fma_f32 v197, |v197|, v229, v197 mul:4\n\t" \
  "v_fma_f32 v196, |v196|, v228, v196 mul:4\n\t" \
  "v_fma_mix_f32 v231, |v199|, v231, v231 op_sel:[0,1,0] op_sel_hi:[0,1,1] clamp\n\t" \
  "v_mfma_f32_16x16x32_bf16 v[44:47], v[216:219], v[154:157], v[44:47]\n\t" \
  "v_fma_mix_f32 v230, |v198|, v230, v230 op_sel:[0,1,0] op_sel_hi:[0,1,1] clamp\n\t" \
  "s_waitcnt lgkmcnt(2)\n\t" \
  "v_fma_f32 v205, |v205|, v233, v205 mul:4\n\t" \
  "v_fma_f32 v204, |v204|, v232, v204 mul:4\n\t" \
  "v_mfma_f32_16x16x32_bf16 v[68:71], v[216:219], v[162:165], v[68:71]\n\t" \
  "v_fma_f32 v207, |v207|, v235, v207 mul:4\n\t" \
  "v_fma_f32 v206, |v206|, v234, v206 mul:4\n\t" \
  "v_fma_f32 v199, |v199|, v231, v199 mul:4\n\t" \
  "v_fma_f32 v198, |v198|, v230, v198 mul:4\n\t" \
  "v_mfma_f32_16x16x32_bf16 v[92:95], v[216:219], v[170:173], v[92:95]\n\t" \
  "ds_read_b64_tr_b16 v[216:217], v249 offset:12368\n\t" \
  "v_cvt_pk_bf16_f32 v196, v196, v197\n\t" \
  "v_cvt_pk_bf16_f32 v197, v198, v199\n\t" \
  "v_cvt_pk_bf16_f32 v204, v204, v205\n\t" \
  "v_cvt_pk_bf16_f32 v205, v206, v207\n\t" \
  "s_cmp_lg_u32 s58, 0\n\t" \
  "s_cbranch_scc1 .Lcnx_fin\n\t" \
  "v_mfma_f32_32x32x16_bf16 v[144:159], v[242:245], v[240:243], 0\n\t" \
  "v_mfma_f32_32x32x16_bf16 v[160:175], v[242:245], v[240:243], 0\n\t" \
  "s_bitcmp0_b32 208, s38\n\t" \
  "s_cbranch_scc1 .Lcnx_c32\n\t" \
  "s_mov_b32 m0, 163632\n\t" \
  "s_add_u32 s30, s30, s31\n\t" \
  "buffer_load_dword v252, s[8:11], s30 offen lds\n\t" \
  ".Lcnx_c32:\n\t" \
  "v_mfma_f32_16x16x32_bf16 v[88:91], v[242:245], v[236:239], v[88:91]\n\t" \
  "ds_read_b64_tr_b16 v[218:219], v249 offset:13904\n\t" \
  "v_add_u32 v232, 1536, v247\n\t" \
  "v_mfma_f32_16x16x32_bf16 v[92:95], v[240:243], v[236:239], v[92:95]\n\t" \
  "ds_read2_b32 v[98:99], v247 offset0:0 offset1:6\n\t" \
  "v_mfma_f32_16x16x32_bf16 v[0:3], v[208:211], v[178:181], v[0:3]\n\t" \
  "ds_read2_b32 v[114:115], v247 offset0:192 offset1:198\n\t" \
  "v_mfma_f32_16x16x32_bf16 v[24:27], v[208:211], v[186:189], v[24:27]\n\t" \
  "ds_read2_b32 v[130:131], v232 offset0:0 offset1:6\n\t" \
  "v_mfma_f32_16x16x32_bf16 v[48:51], v[208:211], v[194:197], v[48:51]\n\t" \
  "ds_read2_b32 v[102:103], v247 offset0:12 offset1:18\n\t" \
  "v_mfma_f32_16x16x32_bf16 v[72:75], v[208:211], v[202:205], v[72:75]\n\t" \
  "ds_read_b64_tr_b16 v[208:209], v249 offset:12376\n\t" \
  "ds_read_b64_tr_b16 v[210:211], v249 offset:13912\n\t" \
  "v_bfe_u32 v236, v251, 3, 2\n\t" \
  "s_waitcnt lgkmcnt(8)\n\t" \
  "v_mfma_f32_16x16x32_bf16 v[4:7], v[212:215], v[178:181], v[4:7]\n\t" \
  "ds_read2_b32 v[118:119], v247 offset0:204 offset1:210\n\t" \
  "ds_read2_b32 v[134:135], v232 offset0:12 offset1:18\n\t" \
  "v_mul_u32_u24 v236, 0x5555, v236\n\t" \
  "v_mfma_f32_16x16x32_bf16 v[28:31], v[212:215], v[186:189], v[28:31]\n\t" \
  "ds_read2_b32 v[106:107], v247 offset0:24 offset1:30\n\t" \
  "ds_read2_b32 v[122:123], v247 offset0:216 offset1:222\n\t" \
  "v_mfma_f32_16x16x32_bf16 v[52:55], v[212:215], v[194:197], v[52:55]\n\t" \
  "ds_read2_b32 v[138:139], v232 offset0:24 offset1:30\n\t" \
  "v_mfma_f32_16x16x32_bf16 v[76:79], v[212:215], v[202:205], v[76:79]\n\t" \
  "ds_read_b64_tr_b16 v[212:213], v249 offset:12432\n\t" \
  "s_waitcnt lgkmcnt(12)\n\t" \
  "v_mfma_f32_16x16x32_bf16 v[8:11], v[216:219], v[178:181], v[8:11]\n\t" \
  "ds_read_b64_tr_b16 v[214:215], v249 offset:13968\n\t" \
  "v_mfma_f32_16x16x32_bf16 v[32:35], v[216:219], v[186:189], v[32:35]\n\t" \
  "ds_read2_b32 v[110:111], v247 offset0:36 offset1:42\n\t" \
  "v_mfma_f32_16x16x32_bf16 v[56:59], v[216:219], v[194:197], v[56:59]\n\t" \
  "ds_read2_b32 v[126:127], v247 offset0:228 offset1:234\n\t" \
  "v_mfma_f32_16x16x32_bf16 v[80:83], v[216:219], v[202:205], v[80:83]\n\t" \
  "ds_read_b64_tr_b16 v[216:217], v249 offset:12440\n\t" \
  "v_cvt_pk_bf16_f32 v48, v48, v49\n\t" \
  "v_cvt_pk_bf16_f32 v49, v50, v51\n\t" \
  "v_cvt_pk_bf16_f32 v50, v52, v53\n\t" \
  "v_cvt_pk_bf16_f32 v51, v54, v55\n\t" \
  "v_cvt_pk_bf16_f32 v52, v0, v1\n\t" \
  "v_cvt_pk_bf16_f32 v53, v2, v3\n\t" \
  "v_cvt_pk_bf16_f32 v54, v4, v5\n\t" \
  "v_cvt_pk_bf16_f32 v55, v6, v7\n\t" \
  "v_cvt_pk_bf16_f32 v72, v72, v73\n\t" \
  "v_cvt_pk_bf16_f32 v73, v74, v75\n\t" \
  "v_cvt_pk_bf16_f32 v74, v76, v77\n\t" \
  "v_cvt_pk_bf16_f32 v75, v78, v79\n\t" \
  "v_cvt_pk_bf16_f32 v76, v24, v25\n\t" \
  "v_cvt_pk_bf16_f32 v77, v26, v27\n\t" \
  "v_cvt_pk_bf16_f32 v78, v28, v29\n\t" \
  "v_cvt_pk_bf16_f32 v79, v30, v31\n\t" \
  "s_waitcnt lgkmcnt(10)\n\t" \
  "v_mfma_f32_16x16x32_bf16 v[12:15], v[208:211], v[178:181], v[12:15]\n\t" \
  "ds_read_b64_tr_b16 v[218:219], v249 offset:13976\n\t" \
  "v_mfma_f32_16x16x32_bf16 v[36:39], v[208:211], v[186:189], v[36:39]\n\t" \
  "ds_read2_b32 v[142:143], v232 offset0:36 offset1:42\n\t" \
  "v_mfma_f32_16x16x32_bf16 v[60:63], v[208:211], v[194:197], v[60:63]\n\t" \
  "v_mfma_f32_16x16x32_bf16 v[84:87], v[208:211], v[202:205], v[84:87]\n\t" \
  "s_waitcnt lgkmcnt(5)\n\t" \
  "v_mfma_f32_16x16x32_bf16 v[16:19], v[212:215], v[178:181], v[16:19]\n\t" \
  "v_mfma_f32_16x16x32_bf16 v[40:43], v[212:215], v[186:189], v[40:43]\n\t" \
  "v_mfma_f32_16x16x32_bf16 v[64:67], v[212:215], v[194:197], v[64:67]\n\t" \
  "v_mfma_f32_16x16x32_bf16 v[88:91], v[212:215], v[202:205], v[88:91]\n\t" \
  "v_cvt_pk_bf16_f32 v56, v56, v57\n\t" \
  "v_cvt_pk_bf16_f32 v57, v58, v59\n\t" \
  "v_cvt_pk_bf16_f32 v58, v60, v61\n\t" \
  "v_cvt_pk_bf16_f32 v59, v62, v63\n\t" \
  "v_cvt_pk_bf16_f32 v60, v8, v9\n\t" \
  "v_cvt_pk_bf16_f32 v61, v10, v11\n\t" \
  "v_cvt_pk_bf16_f32 v62, v12, v13\n\t" \
  "v_cvt_pk_bf16_f32 v63, v14, v15\n\t" \
  "v_cvt_pk_bf16_f32 v80, v80, v81\n\t" \
  "v_cvt_pk_bf16_f32 v81, v82, v83\n\t" \
  "v_cvt_pk_bf16_f32 v82, v84, v85\n\t" \
  "v_cvt_pk_bf16_f32 v83, v86, v87\n\t" \
  "v_cvt_pk_bf16_f32 v84, v32, v33\n\t" \
  "v_cvt_pk_bf16_f32 v85, v34, v35\n\t" \
  "v_cvt_pk_bf16_f32 v86, v36, v37\n\t" \
  "v_cvt_pk_bf16_f32 v87, v38, v39\n\t" \
  "s_waitcnt lgkmcnt(1)\n\t" \
  "v_mfma_f32_16x16x32_bf16 v[20:23], v[216:219], v[178:181], v[20:23]\n\t" \
  "v_mfma_f32_16x16x32_bf16 v[44:47], v[216:219], v[186:189], v[44:47]\n\t" \
  "v_mfma_f32_16x16x32_bf16 v[68:71], v[216:219], v[194:197], v[68:71]\n\t" \
  "v_mfma_f32_16x16x32_bf16 v[92:95], v[216:219], v[202:205], v[92:95]\n\t" \
  "v_cvt_pk_bf16_f32 v64, v64, v65\n\t" \
  "v_cvt_pk_bf16_f32 v65, v66, v67\n\t" \
  "s_nop 4\n\t" \
  "v_cvt_pk_bf16_f32 v66, v68, v69\n\t" \
  "v_cvt_pk_bf16_f32 v67, v70, v71\n\t" \
  "v_cvt_pk_bf16_f32 v68, v16, v17\n\t" \
  "v_cvt_pk_bf16_f32 v69, v18, v19\n\t" \
  "v_cvt_pk_bf16_f32 v70, v20, v21\n\t" \
  "v_cvt_pk_bf16_f32 v71, v22, v23\n\t" \
  "v_cvt_pk_bf16_f32 v88, v88, v89\n\t" \
  "v_cvt_pk_bf16_f32 v89, v90, v91\n\t" \
  "v_cvt_pk_bf16_f32 v90, v92, v93\n\t" \
  "v_cvt_pk_bf16_f32 v91, v94, v95\n\t" \
  "v_cvt_pk_bf16_f32 v92, v40, v41\n\t" \
  "v_cvt_pk_bf16_f32 v93, v42, v43\n\t" \
  "v_cvt_pk_bf16_f32 v94, v44, v45\n\t" \
  "v_cvt_pk_bf16_f32 v95, v46, v47\n\t" \
  "s_nop 7\n\t" \
  "s_mov_b32 s57, 6144\n\t" \
  "s_mov_b32 s58, 1\n\t" \
  "s_mov_b32 s38, 0\n\t" \
  "s_mov_b32 s59, -73800\n\t" \
  "s_mov_b32 s60, -73800\n\t" \
  "s_mov_b32 s36, 2\n\t" \
  "s_mov_b32 s61, s28\n\t" \
  "s_mov_b32 s62, s29\n\t" \
  "s_mov_b32 s14, s12\n\t" \
  "s_mov_b32 s15, s13\n\t" \
  "s_add_u32 s12, s12, s65\n\t" \
  "s_add_u32 s13, s13, s65\n\t" \
  "s_add_u32 s28, s28, s65\n\t" \
  "s_add_u32 s29, s29, s65\n\t" \
  "s_sub_u32 s66, s66, 232704\n\t" \
  "s_subb_u32 s67, s67, 0\n\t" \
  "v_lshlrev_b32 v198, 1, v251\n\t" \
  "global_load_dwordx4 v[192:195], v198, s[66:67]\n\t" \
  "s_branch .Lcnx_round\n\t" \
  ".Lcnx_w12:\n\t" \
  "ds_read_b32 v246, v251 offset:47144\n\t" \
  "s_waitcnt lgkmcnt(0)\n\t" \
  "v_readfirstlane_b32 s4, v246\n\t" \
  "s_cmp_lt_u32 s4, s57\n\t" \
  "s_cbranch_scc1 .Lcnx_w12\n\t" \
  "s_branch .Lcnx_b12\n\t" \
  ".Lcnx_w24:\n\t" \
  "ds_read_b32 v246, v251 offset:47152\n\t" \
  "s_waitcnt lgkmcnt(0)\n\t" \
  "v_readfirstlane_b32 s4, v246\n\t" \
  "s_cmp_lt_u32 s4, s57\n\t" \
  "s_cbranch_scc1 .Lcnx_w24\n\t" \
  "s_branch .Lcnx_b24\n\t" \
  ".Lcnx_w36:\n\t" \
  "ds_read_b32 v246, v251 offset:47160\n\t" \
  "s_waitcnt lgkmcnt(0)\n\t" \
  "v_readfirstlane_b32 s4, v246\n\t" \
  "s_cmp_lt_u32 s4, s57\n\t" \
  "s_cbranch_scc1 .Lcnx_w36\n\t" \
  "s_branch .Lcnx_b36\n\t" \
  ".Lcnx_w48:\n\t" \
  "ds_read_b32 v246, v251 offset:47168\n\t" \
  "s_waitcnt lgkmcnt(0)\n\t" \
  "v_readfirstlane_b32 s4, v246\n\t" \
  "s_cmp_lt_u32 s4, s57\n\t" \
  "s_cbranch_scc1 .Lcnx_w48\n\t" \
  "s_branch .Lcnx_b48\n\t" \
  ".Lcnx_w60:\n\t" \
  "ds_read_b32 v246, v251 offset:47176\n\t" \
  "s_waitcnt lgkmcnt(0)\n\t" \
  "v_readfirstlane_b32 s4, v246\n\t" \
  "s_cmp_lt_u32 s4, s57\n\t" \
  "s_cbranch_scc1 .Lcnx_w60\n\t" \
  "s_branch .Lcnx_b60\n\t" \
  ".Lcnx_w72:\n\t" \
  "ds_read_b32 v246, v251 offset:47184\n\t" \
  "s_waitcnt lgkmcnt(0)\n\t" \
  "v_readfirstlane_b32 s4, v246\n\t" \
  "s_cmp_lt_u32 s4, s57\n\t" \
  "s_cbranch_scc1 .Lcnx_w72\n\t" \
  "s_branch .Lcnx_b72\n\t" \
  ".Lcnx_w84:\n\t" \
  "ds_read_b32 v246, v251 offset:47192\n\t" \
  "s_waitcnt lgkmcnt(0)\n\t" \
  "v_readfirstlane_b32 s4, v246\n\t" \
  "s_cmp_lt_u32 s4, s57\n\t" \
  "s_cbranch_scc1 .Lcnx_w84\n\t" \
  "s_branch .Lcnx_b84\n\t" \
  ".Lcnx_w96:\n\t" \
  "ds_read_b32 v246, v251 offset:47200\n\t" \
  "s_waitcnt lgkmcnt(0)\n\t" \
  "v_readfirstlane_b32 s4, v246\n\t" \
  "s_cmp_lt_u32 s4, s57\n\t" \
  "s_cbranch_scc1 .Lcnx_w96\n\t" \
  "s_branch .Lcnx_b96\n\t" \
  ".Lcnx_w108:\n\t" \
  "ds_read_b32 v246, v251 offset:47208\n\t" \
  "s_waitcnt lgkmcnt(0)\n\t" \
  "v_readfirstlane_b32 s4, v246\n\t" \
  "s_cmp_lt_u32 s4, s57\n\t" \
  "s_cbranch_scc1 .Lcnx_w108\n\t" \
  "s_branch .Lcnx_b108\n\t" \
  ".Lcnx_w120:\n\t" \
  "ds_read_b32 v246, v251 offset:47216\n\t" \
  "s_waitcnt lgkmcnt(0)\n\t" \
  "v_readfirstlane_b32 s4, v246\n\t" \
  "s_cmp_lt_u32 s4, s57\n\t" \
  "s_cbranch_scc1 .Lcnx_w120\n\t" \
  "s_branch .Lcnx_b120\n\t" \
  ".Lcnx_w132:\n\t" \
  "ds_read_b32 v246, v251 offset:47224\n\t" \
  "s_waitcnt lgkmcnt(0)\n\t" \
  "v_readfirstlane_b32 s4, v246\n\t" \
  "s_cmp_lt_u32 s4, s57\n\t" \
  "s_cbranch_scc1 .Lcnx_w132\n\t" \
  "s_branch .Lcnx_b132\n\t" \
  ".Lcnx_w138:\n\t" \
  "ds_read_b32 v246, v251 offset:47232\n\t" \
  "s_waitcnt lgkmcnt(0)\n\t" \
  "v_readfirstlane_b32 s4, v246\n\t" \
  "s_cmp_lt_u32 s4, s57\n\t" \
  "s_cbranch_scc1 .Lcnx_w138\n\t" \
  "s_branch .Lcnx_b138\n\t" \
  ".Lcnx_f0:\n\t" \
  "s_mov_b32 m0, s44\n\t" \
  "s_nop 0\n\t" \
  "buffer_load_dwordx4 v252, s[16:19], s33 offen lds\n\t" \
  "buffer_load_dwordx4 v252, s[16:19], s34 offen offset:1024 lds\n\t" \
  "buffer_load_dwordx4 v252, s[16:19], s35 offen offset:2048 lds\n\t" \
  "s_add_u32 s33, s33, s32\n\t" \
  "s_add_u32 s34, s34, s32\n\t" \
  "s_add_u32 s35, s35, s32\n\t" \
  "s_branch .Lcnx_g0\n\t" \
  ".Lcnx_f1:\n\t" \
  "s_mov_b32 m0, s45\n\t" \
  "s_nop 0\n\t" \
  "buffer_load_dwordx4 v252, s[16:19], s33 offen lds\n\t" \
  "buffer_load_dwordx4 v252, s[16:19], s34 offen offset:1024 lds\n\t" \
  "buffer_load_dwordx4 v252, s[16:19], s35 offen offset:2048 lds\n\t" \
  "s_add_u32 s33, s33, s32\n\t" \
  "s_add_u32 s34, s34, s32\n\t" \
  "s_add_u32 s35, s35, s32\n\t" \
  "s_branch .Lcnx_g1\n\t" \
  ".Lcnx_f2:\n\t" \
  "s_mov_b32 m0, s46\n\t" \
  "s_nop 0\n\t" \
  "buffer_load_dwordx4 v252, s[16:19], s33 offen lds\n\t" \
  "buffer_load_dwordx4 v252, s[16:19], s34 offen offset:1024 lds\n\t" \
  "buffer_load_dwordx4 v252, s[16:19], s35 offen offset:2048 lds\n\t" \
  "s_add_u32 s33, s33, s32\n\t" \
  "s_add_u32 s34, s34, s32\n\t" \
  "s_add_u32 s35, s35, s32\n\t" \
  "s_branch .Lcnx_g2\n\t" \
  ".Lcnx_f3:\n\t" \
  "s_mov_b32 m0, s47\n\t" \
  "s_nop 0\n\t" \
  "buffer_load_dwordx4 v252, s[16:19], s33 offen lds\n\t" \
  "buffer_load_dwordx4 v252, s[16:19], s34 offen offset:1024 lds\n\t" \
  "buffer_load_dwordx4 v252, s[16:19], s35 offen offset:2048 lds\n\t" \
  "s_add_u32 s33, s33, s32\n\t" \
  "s_add_u32 s34, s34, s32\n\t" \
  "s_add_u32 s35, s35, s32\n\t" \
  "s_branch .Lcnx_g3\n\t" \
  ".Lcnx_f4:\n\t" \
  "s_mov_b32 m0, s44\n\t" \
  "s_nop 0\n\t" \
  "buffer_load_dwordx4 v252, s[16:19], s33 offen lds\n\t" \
  "buffer_load_dwordx4 v252, s[16:19], s34 offen offset:1024 lds\n\t" \
  "buffer_load_dwordx4 v252, s[16:19], s35 offen offset:2048 lds\n\t" \
  "s_add_u32 s33, s33, s32\n\t" \
  "s_add_u32 s34, s34, s32\n\t" \
  "s_add_u32 s35, s35, s32\n\t" \
  "s_cmp_lg_u32 s58, 1\n\t" \
  "s_cbranch_scc1 .Lcnx_g4\n\t" \
  "s_add_u32 m0, s44, s50\n\t" \
  "s_add_u32 s4, s33, s51\n\t" \
  "buffer_load_dwordx4 v252, s[16:19], s4 offen offset:0 lds\n\t" \
  "s_add_u32 s4, s34, s51\n\t" \
  "buffer_load_dwordx4 v252, s[16:19], s4 offen offset:1024 lds\n\t" \
  "s_add_u32 s4, s35, s51\n\t" \
  "buffer_load_dwordx4 v252, s[16:19], s4 offen offset:2048 lds\n\t" \
  "s_branch .Lcnx_g4\n\t" \
  ".Lcnx_f5:\n\t" \
  "s_mov_b32 m0, s45\n\t" \
  "s_nop 0\n\t" \
  "buffer_load_dwordx4 v252, s[16:19], s33 offen lds\n\t" \
  "buffer_load_dwordx4 v252, s[16:19], s34 offen offset:1024 lds\n\t" \
  "buffer_load_dwordx4 v252, s[16:19], s35 offen offset:2048 lds\n\t" \
  "s_add_u32 s33, s33, s32\n\t" \
  "s_add_u32 s34, s34, s32\n\t" \
  "s_add_u32 s35, s35, s32\n\t" \
  "s_cmp_lg_u32 s58, 1\n\t" \
  "s_cbranch_scc1 .Lcnx_g5\n\t" \
  "s_add_u32 m0, s45, s50\n\t" \
  "s_add_u32 s4, s33, s51\n\t" \
  "buffer_load_dwordx4 v252, s[16:19], s4 offen offset:0 lds\n\t" \
  "s_add_u32 s4, s34, s51\n\t" \
  "buffer_load_dwordx4 v252, s[16:19], s4 offen offset:1024 lds\n\t" \
  "s_add_u32 s4, s35, s51\n\t" \
  "buffer_load_dwordx4 v252, s[16:19], s4 offen offset:2048 lds\n\t" \
  "s_branch .Lcnx_g5\n\t" \
  ".Lcnx_f6:\n\t" \
  "s_mov_b32 m0, s46\n\t" \
  "s_nop 0\n\t" \
  "buffer_load_dwordx4 v252, s[16:19], s33 offen lds\n\t" \
  "buffer_load_dwordx4 v252, s[16:19], s34 offen offset:1024 lds\n\t" \
  "buffer_load_dwordx4 v252, s[16:19], s35 offen offset:2048 lds\n\t" \
  "s_add_u32 s33, s33, s32\n\t" \
  "s_add_u32 s34, s34, s32\n\t" \
  "s_add_u32 s35, s35, s32\n\t" \
  "s_cmp_lg_u32 s58, 1\n\t" \
  "s_cbranch_scc1 .Lcnx_g6\n\t" \
  "s_cmp_lt_u32 s39, 1024\n\t" \
  "s_cbranch_scc1 .Lcnx_g6\n\t" \
  "s_add_u32 m0, s46, s50\n\t" \
  "s_add_u32 s4, s33, s51\n\t" \
  "buffer_load_dwordx4 v252, s[16:19], s4 offen offset:0 lds\n\t" \
  "s_add_u32 s4, s34, s51\n\t" \
  "buffer_load_dwordx4 v252, s[16:19], s4 offen offset:1024 lds\n\t" \
  "s_add_u32 s4, s35, s51\n\t" \
  "buffer_load_dwordx4 v252, s[16:19], s4 offen offset:2048 lds\n\t" \
  "s_branch .Lcnx_g6\n\t" \
  ".Lcnx_f7:\n\t" \
  "s_mov_b32 m0, s47\n\t" \
  "s_nop 0\n\t" \
  "buffer_load_dwordx4 v252, s[16:19], s33 offen lds\n\t" \
  "buffer_load_dwordx4 v252, s[16:19], s34 offen offset:1024 lds\n\t" \
  "buffer_load_dwordx4 v252, s[16:19], s35 offen offset:2048 lds\n\t" \
  "s_add_u32 s33, s33, s32\n\t" \
  "s_add_u32 s34, s34, s32\n\t" \
  "s_add_u32 s35, s35, s32\n\t" \
  "s_cmp_lg_u32 s58, 1\n\t" \
  "s_cbranch_scc1 .Lcnx_g7\n\t" \
  "s_cmp_lt_u32 s39, 1024\n\t" \
  "s_cbranch_scc1 .Lcnx_g7\n\t" \
  "s_add_u32 m0, s47, s50\n\t" \
  "s_add_u32 s4, s33, s51\n\t" \
  "buffer_load_dwordx4 v252, s[16:19], s4 offen offset:0 lds\n\t" \
  "s_add_u32 s4, s34, s51\n\t" \
  "buffer_load_dwordx4 v252, s[16:19], s4 offen offset:1024 lds\n\t" \
  "s_add_u32 s4, s35, s51\n\t" \
  "buffer_load_dwordx4 v252, s[16:19], s4 offen offset:2048 lds\n\t" \
  "s_branch .Lcnx_g7\n\t" \
  ".Lcnx_f8:\n\t" \
  "s_mov_b32 m0, s44\n\t" \
  "s_nop 0\n\t" \
  "buffer_load_dwordx4 v252, s[16:19], s33 offen lds\n\t" \
  "buffer_load_dwordx4 v252, s[16:19], s34 offen offset:1024 lds\n\t" \
  "buffer_load_dwordx4 v252, s[16:19], s35 offen offset:2048 lds\n\t" \
  "s_add_u32 s33, s33, s32\n\t" \
  "s_add_u32 s34, s34, s32\n\t" \
  "s_add_u32 s35, s35, s32\n\t" \
  "s_branch .Lcnx_g8\n\t" \
  ".Lcnx_f9:\n\t" \
  "s_mov_b32 m0, s45\n\t" \
  "s_nop 0\n\t" \
  "buffer_load_dwordx4 v252, s[16:19], s33 offen lds\n\t" \
  "buffer_load_dwordx4 v252, s[16:19], s34 offen offset:1024 lds\n\t" \
  "buffer_load_dwordx4 v252, s[16:19], s35 offen offset:2048 lds\n\t" \
  "s_add_u32 s33, s33, s32\n\t" \
  "s_add_u32 s34, s34, s32\n\t" \
  "s_add_u32 s35, s35, s32\n\t" \
  "s_sub_u32 s33, s33, s48\n\t" \
  "s_sub_u32 s34, s34, s48\n\t" \
  "s_sub_u32 s35, s35, s48\n\t" \
  "s_branch .Lcnx_g9\n\t" \
  ".Lcnx_f10:\n\t" \
  "s_mov_b32 m0, s46\n\t" \
  "s_nop 0\n\t" \
  "buffer_load_dwordx4 v252, s[16:19], s33 offen lds\n\t" \
  "buffer_load_dwordx4 v252, s[16:19], s34 offen offset:1024 lds\n\t" \
  "buffer_load_dwordx4 v252, s[16:19], s35 offen offset:2048 lds\n\t" \
  "s_add_u32 s33, s33, s32\n\t" \
  "s_add_u32 s34, s34, s32\n\t" \
  "s_add_u32 s35, s35, s32\n\t" \
  "s_branch .Lcnx_g10\n\t" \
  ".Lcnx_f11:\n\t" \
  "s_mov_b32 m0, s47\n\t" \
  "s_nop 0\n\t" \
  "buffer_load_dwordx4 v252, s[16:19], s33 offen lds\n\t" \
  "buffer_load_dwordx4 v252, s[16:19], s34 offen offset:1024 lds\n\t" \
  "buffer_load_dwordx4 v252, s[16:19], s35 offen offset:2048 lds\n\t" \
  "s_add_u32 s33, s33, s32\n\t" \
  "s_add_u32 s34, s34, s32\n\t" \
  "s_add_u32 s35, s35, s32\n\t" \
  "s_sub_u32 s33, s33, s49\n\t" \
  "s_sub_u32 s34, s34, s49\n\t" \
  "s_sub_u32 s35, s35, s49\n\t" \
  "s_branch .Lcnx_g11\n\t" \
  ".Lcnx_fin:\n\t" \
  "v_mfma_f32_16x16x32_bf16 v[88:91], v[242:245], v[236:239], v[88:91]\n\t" \
  "ds_read_b64_tr_b16 v[218:219], v249 offset:13904\n\t" \
  "v_mfma_f32_16x16x32_bf16 v[92:95], v[240:243], v[236:239], v[92:95]\n\t" \
  "v_mfma_f32_16x16x32_bf16 v[0:3], v[208:211], v[178:181], v[0:3]\n\t" \
  "v_mfma_f32_16x16x32_bf16 v[24:27], v[208:211], v[186:189], v[24:27]\n\t" \
  "v_mfma_f32_16x16x32_bf16 v[48:51], v[208:211], v[194:197], v[48:51]\n\t" \
  "v_mfma_f32_16x16x32_bf16 v[72:75], v[208:211], v[202:205], v[72:75]\n\t" \
  "ds_read_b64_tr_b16 v[208:209], v249 offset:12376\n\t" \
  "s_waitcnt lgkmcnt(3)\n\t" \
  "v_mfma_f32_16x16x32_bf16 v[4:7], v[212:215], v[178:181], v[4:7]\n\t" \
  "ds_read_b64_tr_b16 v[210:211], v249 offset:13912\n\t" \
  "v_mfma_f32_16x16x32_bf16 v[28:31], v[212:215], v[186:189], v[28:31]\n\t" \
  "v_mfma_f32_16x16x32_bf16 v[52:55], v[212:215], v[194:197], v[52:55]\n\t" \
  "v_mfma_f32_16x16x32_bf16 v[76:79], v[212:215], v[202:205], v[76:79]\n\t" \
  "ds_read_b64_tr_b16 v[212:213], v249 offset:12432\n\t" \
  "s_waitcnt lgkmcnt(3)\n\t" \
  "v_mfma_f32_16x16x32_bf16 v[8:11], v[216:219], v[178:181], v[8:11]\n\t" \
  "ds_read_b64_tr_b16 v[214:215], v249 offset:13968\n\t" \
  "v_mfma_f32_16x16x32_bf16 v[32:35], v[216:219], v[186:189], v[32:35]\n\t" \
  "v_mfma_f32_16x16x32_bf16 v[56:59], v[216:219], v[194:197], v[56:59]\n\t" \
  "v_mfma_f32_16x16x32_bf16 v[80:83], v[216:219], v[202:205], v[80:83]\n\t" \
  "ds_read_b64_tr_b16 v[216:217], v249 offset:12440\n\t" \
  "v_cvt_pk_bf16_f32 v0, v0, v1\n\t" \
  "v_cvt_pk_bf16_f32 v1, v2, v3\n\t" \
  "v_cvt_pk_bf16_f32 v2, v4, v5\n\t" \
  "v_cvt_pk_bf16_f32 v3, v6, v7\n\t" \
  "buffer_store_dwordx4 v[0:3], v250, s[20:23], s28 offen offset:0 sc1\n\t" \
  "v_cvt_pk_bf16_f32 v24, v24, v25\n\t" \
  "v_cvt_pk_bf16_f32 v25, v26, v27\n\t" \
  "v_cvt_pk_bf16_f32 v26, v28, v29\n\t" \
  "v_cvt_pk_bf16_f32 v27, v30, v31\n\t" \
  "buffer_store_dwordx4 v[24:27], v250, s[20:23], s29 offen offset:0 sc1\n\t" \
  "v_cvt_pk_bf16_f32 v48, v48, v49\n\t" \
  "v_cvt_pk_bf16_f32 v49, v50, v51\n\t" \
  "v_cvt_pk_bf16_f32 v50, v52, v53\n\t" \
  "v_cvt_pk_bf16_f32 v51, v54, v55\n\t" \
  "buffer_store_dwordx4 v[48:51], v250, s[20:23], s12 offen offset:0 sc1\n\t" \
  "v_cvt_pk_bf16_f32 v72, v72, v73\n\t" \
  "v_cvt_pk_bf16_f32 v73, v74, v75\n\t" \
  "v_cvt_pk_bf16_f32 v74, v76, v77\n\t" \
  "v_cvt_pk_bf16_f32 v75, v78, v79\n\t" \
  "buffer_store_dwordx4 v[72:75], v250, s[20:23], s13 offen offset:0 sc1\n\t" \
  "s_waitcnt lgkmcnt(3)\n\t" \
  "v_mfma_f32_16x16x32_bf16 v[12:15], v[208:211], v[178:181], v[12:15]\n\t" \
  "ds_read_b64_tr_b16 v[218:219], v249 offset:13976\n\t" \
  "v_mfma_f32_16x16x32_bf16 v[36:39], v[208:211], v[186:189], v[36:39]\n\t" \
  "v_mfma_f32_16x16x32_bf16 v[60:63], v[208:211], v[194:197], v[60:63]\n\t" \
  "v_mfma_f32_16x16x32_bf16 v[84:87], v[208:211], v[202:205], v[84:87]\n\t" \
  "s_waitcnt lgkmcnt(2)\n\t" \
  "v_mfma_f32_16x16x32_bf16 v[16:19], v[212:215], v[178:181], v[16:19]\n\t" \
  "v_mfma_f32_16x16x32_bf16 v[40:43], v[212:215], v[186:189], v[40:43]\n\t" \
  "v_mfma_f32_16x16x32_bf16 v[64:67], v[212:215], v[194:197], v[64:67]\n\t" \
  "v_mfma_f32_16x16x32_bf16 v[88:91], v[212:215], v[202:205], v[88:91]\n\t" \
  "v_cvt_pk_bf16_f32 v8, v8, v9\n\t" \
  "v_cvt_pk_bf16_f32 v9, v10, v11\n\t" \
  "v_cvt_pk_bf16_f32 v10, v12, v13\n\t" \
  "v_cvt_pk_bf16_f32 v11, v14, v15\n\t" \
  "buffer_store_dwordx4 v[8:11], v250, s[20:23], s28 offen offset:64 sc1\n\t" \
  "v_cvt_pk_bf16_f32 v32, v32, v33\n\t" \
  "v_cvt_pk_bf16_f32 v33, v34, v35\n\t" \
  "v_cvt_pk_bf16_f32 v34, v36, v37\n\t" \
  "v_cvt_pk_bf16_f32 v35, v38, v39\n\t" \
  "buffer_store_dwordx4 v[32:35], v250, s[20:23], s29 offen offset:64 sc1\n\t" \
  "v_cvt_pk_bf16_f32 v56, v56, v57\n\t" \
  "v_cvt_pk_bf16_f32 v57, v58, v59\n\t" \
  "v_cvt_pk_bf16_f32 v58, v60, v61\n\t" \
  "v_cvt_pk_bf16_f32 v59, v62, v63\n\t" \
  "buffer_store_dwordx4 v[56:59], v250, s[20:23], s12 offen offset:64 sc1\n\t" \
  "v_cvt_pk_bf16_f32 v80, v80, v81\n\t" \
  "v_cvt_pk_bf16_f32 v81, v82, v83\n\t" \
  "v_cvt_pk_bf16_f32 v82, v84, v85\n\t" \
  "v_cvt_pk_bf16_f32 v83, v86, v87\n\t" \
  "buffer_store_dwordx4 v[80:83], v250, s[20:23], s13 offen offset:64 sc1\n\t" \
  "s_waitcnt lgkmcnt(0)\n\t" \
  "v_mfma_f32_16x16x32_bf16 v[20:23], v[216:219], v[178:181], v[20:23]\n\t" \
  "v_mfma_f32_16x16x32_bf16 v[44:47], v[216:219], v[186:189], v[44:47]\n\t" \
  "v_mfma_f32_16x16x32_bf16 v[68:71], v[216:219], v[194:197], v[68:71]\n\t" \
  "v_mfma_f32_16x16x32_bf16 v[92:95], v[216:219], v[202:205], v[92:95]\n\t" \
  "v_cvt_pk_bf16_f32 v16, v16, v17\n\t" \
  "v_cvt_pk_bf16_f32 v17, v18, v19\n\t" \
  "s_nop 2\n\t" \
  "v_cvt_pk_bf16_f32 v18, v20, v21\n\t" \
  "v_cvt_pk_bf16_f32 v19, v22, v23\n\t" \
  "buffer_store_dwordx4 v[16:19], v250, s[20:23], s28 offen offset:128 sc1\n\t" \
  "v_cvt_pk_bf16_f32 v40, v40, v41\n\t" \
  "v_cvt_pk_bf16_f32 v41, v42, v43\n\t" \
  "v_cvt_pk_bf16_f32 v42, v44, v45\n\t" \
  "v_cvt_pk_bf16_f32 v43, v46, v47\n\t" \
  "buffer_store_dwordx4 v[40:43], v250, s[20:23], s29 offen offset:128 sc1\n\t" \
  "v_cvt_pk_bf16_f32 v64, v64, v65\n\t" \
  "v_cvt_pk_bf16_f32 v65, v66, v67\n\t" \
  "v_cvt_pk_bf16_f32 v66, v68, v69\n\t" \
  "v_cvt_pk_bf16_f32 v67, v70, v71\n\t" \
  "buffer_store_dwordx4 v[64:67], v250, s[20:23], s12 offen offset:128 sc1\n\t" \
  "v_cvt_pk_bf16_f32 v88, v88, v89\n\t" \
  "v_cvt_pk_bf16_f32 v89, v90, v91\n\t" \
  "v_cvt_pk_bf16_f32 v90, v92, v93\n\t" \
  "v_cvt_pk_bf16_f32 v91, v94, v95\n\t" \
  "buffer_store_dwordx4 v[88:91], v250, s[20:23], s13 offen offset:128 sc1\n\t" \
  "s_waitcnt vmcnt(0)\n\t" \
  ".Lcnx_end:\n\t" \
  ""


__device__ __forceinline__ unsigned lds_addr(const char* p) { return (unsigned)(uintptr_t)(lds_ptr_t)p; }
__device__ __forceinline__ unsigned sg(unsigned v) { return __builtin_amdgcn_readfirstlane(v); }

constexpr double tanh_u(double y) {
    const double v = 8 * y, a = 2 * 0.79788456080286536 * (v + 0.044715 * v * v * v) / 1024;
    double m = 1;
    for (int i = 12; i > 1; --i) m = 1 + m * a / i;
    m *= a;
    for (int i = 0; i < 10; ++i) m *= m + 2;
    return m / (m + 2);
}
constexpr double h2d(int h) {
    if (h < 1024) return 0;
    double x = (1024 + (h & 1023)) * 0x1p-24;
    for (int e = 1; e < (h >> 10); ++e) x *= 2;
    return x;
}
constexpr int d2h(double x) {
    int h = 0;
    for (int s = 1 << 14; s; s >>= 1) if (h2d(h + s) <= x) h += s;
    return h;
}
constexpr unsigned ip_entry(int k) {
    if (k == 127) return 0x3c003c00u;
    const double y0 = k ? (256 * k - 0.5) / 32767 : 0, dy = ((256 * k + 255.5) / 32767 - y0) / 8;
    double t[9] = {};
    for (int i = 0; i < 9; ++i) t[i] = tanh_u(y0 + i * dy);
    const double b0 = (t[8] - t[0]) / (8 * dy), ym = y0 + 4 * dy, tm = (t[4] + (t[0] + t[8]) / 2) / 2;
    unsigned best = 0;
    double err = 1;
    for (int ha = d2h(tm - b0 * ym) - 1, i = 0; i < 4; ++i, ++ha)
        for (int hb = d2h((tm - h2d(ha)) / ym), j = 0; j < 2; ++j, ++hb) {
            double e = 0;
            for (int n = 0; n < 9; ++n) {
                const double s = h2d(ha) + h2d(hb) * (y0 + n * dy), d = (s < 1 ? s : 1) - t[n];
                e = d > e ? d : -d > e ? -d : e;
            }
            if (e < err) err = e, best = (ha < 1024 ? 0 : ha) | (hb < 1024 ? 0 : hb) << 16;
        }
    return best;
}
struct TanhTab { unsigned v[1024]; };
constexpr TanhTab tanh_tab(int k0) {
    TanhTab t = {};
    for (int k = k0; k < k0 + 1024 && k < DWOFF / 4; ++k) {
        double y = ((k & 255) + 0.375 + (k >= 256 ? 256 : 0)) * 0x1p-22;
        for (int e = 1; e < (k >> 8); ++e) y *= 2;
        t.v[k - k0] = k < IPOFF / 4 ? __builtin_bit_cast(unsigned, (float)tanh_u(y)) : ip_entry(k - IPOFF / 4);
    }
    return t;
}
struct WaveTab { unsigned v192[64][4], v244[64][4], v16[64][4], s32[32]; };
struct RegTab { unsigned v240[64][4], v248[64][4]; WaveTab w[8]; };
constexpr unsigned cpos(unsigned w, unsigned px) { return (w >> 1) * 1024 + (px ^ (w & 2) << 2) * 16 + ((w ^ px >> 4) & 1) * 8; }
constexpr RegTab reg_tab() {
    RegTab t = {};
    for (unsigned l = 0; l < 64; ++l) {
        const unsigned G = l >> 4, n = l & 15, q = n >> 2, p = l & 3;
        t.v240[l][2 + (p >> 1)] = G == q ? (p & 1 ? 0x3f800000u : 0x3f80u) : 0u;
        t.v248[l][0] = WB + (l >> 2) * 32 + (p & 1) * 16 + (p >> 1) * 8;
        t.v248[l][1] = WB + (G & 1) * 3080 + ((G >> 1) * 4 + q) * 192 + p * 16;
        t.v248[l][2] = (n & 7) * 384 + (n >> 3) * 192 + G * 16;
        t.v248[l][3] = PB + l * 8;
        for (unsigned w = 0; w < 8; ++w) {
            t.w[w].v244[l][2] = XOFF + n * 400 + (G & 1) * XROW + (G >> 1) * 192 + w * 24;
            t.w[w].v244[l][3] = DWOFF + p * 192 + (1 - (q >> 1)) * 24 + (1 + (G & 1) - (q & 1)) * 8 + ((G ^ p) >> 1) * 4 + w * 2304;
            const unsigned pl = PTOP + (w >> 2) * 12288, blk = (w & 3) * 16;
            t.w[w].v192[l][0] = XOFF + cpos(w, (G & 2) * 16 + (n & 8) * 2 + (G & 1) * 8 + (n & 7));
            t.w[w].v192[l][1] = t.w[w].v192[l][0] + PTOP - XOFF;
            t.w[w].v192[l][2] = pl + (cpos((G & 1) * 4 + (G & 2), blk + n) & ~8u);
            t.w[w].v192[l][3] = pl + cpos(p, blk + (l >> 2));
            t.w[w].v16[l][0] = w >= 4 ? (l < 48 ? l * 128 : OOB)
                             : w < 2  ? (12 * ((l >> 3) & 3) + ((l >> 1) & 3)) * (HID * 2) + (2 * (l & 1) + (l >> 5)) * 16 : l * 16;
            const unsigned d = DWOFF + (w * 8 + (l >> 5) * 4 + p) * 768 + ((l >> 2) & 7) * 24;
            t.w[w].v16[l][1] = w < 3 ? d + (l & 1) * 192 : XOFF - 32 + p * 8;
            t.w[w].v16[l][2] = d + (~l & 1) * 192;
            t.w[w].v16[l][3] = l & 1 ? 0x07060302u : 0x05040100u;
        }
    }
    for (unsigned w = 0; w < 8; ++w) {
        const bool w1 = w < 2;
        const unsigned wpd = w1 ? 4 * HID * 2 - 1024 : 0, wl = WB + (w & 1) * (w1 ? 3072 : 3080), wc = w1 ? 64 : 6144;
        const unsigned so = (w & 1) * (w1 ? 48 * HID * 2 : 3072);
        const unsigned s[32] = {wc, so, so + wpd, so + 2 * wpd, w, w >> 2, w, w * 512,
                                0xaaaaaaaau, 0xaaaaaaaau, 0xccccccccu, 0xccccccccu,
                                wl + (w1 ? WA + 12304 : WA), wl + (w1 ? 0 : WA + 6152), wl + (w1 ? WA + 18448 : 6152),
                                wl + (w1 ? WA + 18448 + 6144 : 12304), w1 ? 12 * wc : 0, w1 ? 0 : 12 * wc,
                                0u - 9 * XROW, 3 * wc, ~0x00f0000fu, ~0xf0000f00u, (w >> 2) * (PTOP - XOFF - 5 * 12288), 0x07060302, 0x3ffc, 3072, 0, 0, 0,
                                OOB, OOB, 8 * (unsigned)sizeof(WaveTab) - 2 * PB};
        for (int i = 0; i < 32; ++i) t.w[w].s32[i] = s[i];
    }
    return t;
}
struct Tables { RegTab r; TanhTab t[4]; };
static_assert(sizeof(WaveTab) == 64 * 50 && sizeof(RegTab) == 27648, "table layout");
__device__ const __attribute__((aligned(128))) Tables TABLES = {reg_tab(), {tanh_tab(0), tanh_tab(1024), tanh_tab(2048), tanh_tab(3072)}};

#define R10(r, a) r #a "0", r #a "1", r #a "2", r #a "3", r #a "4", r #a "5", r #a "6", r #a "7", r #a "8", r #a "9"
#define V10(a) R10("v", a)
#define S10(a) R10("s", a)
#define MAIN_CLOB "memory", "scc", "vcc", "v1", "v2", "v3", "v4", "v5", "v6", "v7", "v8", "v9", V10(1), V10(2), V10(3), V10(4), V10(5),  \
    V10(6), V10(7), V10(8), V10(9), V10(10), V10(11), V10(12), V10(13), V10(14), V10(15), V10(16), V10(17), V10(18), V10(19),        \
    V10(20), V10(21), V10(22), V10(23), V10(24), "v250", "v251", "v252", "v253", "v254", "s4", "s5", "s8", "s9", S10(1),       \
    S10(2), S10(3), S10(4), S10(5), "s60", "s61", "s62", "s63", "s64", "s65", "s66", "s67"

__global__ __launch_bounds__(512, 2) void convnext_kernel(const __hip_bfloat16* __restrict__ x,
                                                          const __hip_bfloat16* __restrict__ dw,
                                                          const __hip_bfloat16* __restrict__ w1,
                                                          const __hip_bfloat16* __restrict__ w2,
                                                          __hip_bfloat16* __restrict__ out) {
    __shared__ __attribute__((aligned(16))) char smem[LDS_BYTES];

    unsigned tid = threadIdx.x, lds = lds_addr(smem);
    const unsigned bid = blockIdx.x;
    asm volatile(MAIN_ASM
                 : "+{v0}"(tid), "+{s3}"(lds)
                 : "{s[0:1]}"(__builtin_amdgcn_kernarg_segment_ptr()), "{s2}"(bid), "{s[6:7]}"(TABLES.r.w)
                 : MAIN_CLOB);

}

}

extern "C" void kernel_launch(void* const* d_in, const int* in_sizes, int n_in, void* d_out, int out_size,
                              void* d_ws, size_t ws_size, hipStream_t stream) {
    (void)in_sizes; (void)n_in; (void)out_size; (void)d_ws; (void)ws_size;
    const __hip_bfloat16* x = (const __hip_bfloat16*)d_in[0];
    const __hip_bfloat16* dw = (const __hip_bfloat16*)d_in[1];
    const __hip_bfloat16* w1 = (const __hip_bfloat16*)d_in[2];
    const __hip_bfloat16* w2 = (const __hip_bfloat16*)d_in[3];
    __hip_bfloat16* out = (__hip_bfloat16*)d_out;
    convnext_kernel<<<256, 512, 0, stream>>>(x, dw, w1, w2, out);
}
```

```cpp
#include <hip/hip_runtime.h>
#include <hip/hip_bf16.h>
#include <stdint.h>

namespace {

typedef __attribute__((address_space(3))) void* lds_ptr_t;

constexpr int IMG = 512;
constexpr int CH = 96;
constexpr int HID = 384;
constexpr int ROWB = IMG * CH * 2;
constexpr int IPOFF = 13 * 768 + 32;
constexpr int GOFF = IPOFF + 512;
constexpr int GSZ = 30 * 256;
constexpr int DW2 = GOFF + GSZ;
constexpr int XROW = 8200;
constexpr int XOFF = GOFF + 65536 - 6 * XROW;
constexpr int XHI = XOFF + GSZ;
constexpr int PB = XHI + 10 * XROW;
constexpr int WB = XHI + 9 * XROW;
constexpr int WA = 3 * XROW;
constexpr int PTOP = XHI + 12 * XROW - 12288;
constexpr int CNTOFF = WB + WA + 18448 + 12288;
constexpr int LDS_BYTES = 160 * 1024;
static_assert(XOFF == 26864 && CNTOFF - PB == 47136 && DW2 + 11 * 768 + 32 <= XOFF && CNTOFF + 144 <= LDS_BYTES, "LDS map");
constexpr unsigned dw_pos(unsigned u) { return u < 12 ? u * 768 : u == 14 ? 12 * 768 : DW2 + (u - 12 - (u > 14)) * 768; }
constexpr unsigned OOB = 0x40000000u;

#define MAIN_ASM \
  "s_load_dwordx2 s[8:9], s[0:1], 0x0\n\t" \
  "s_load_dwordx2 s[12:13], s[0:1], 0x8\n\t" \
  "s_load_dwordx2 s[20:21], s[0:1], 0x20\n\t" \
  "s_load_dwordx4 s[32:35], s[0:1], 0x10\n\t" \
  "v_readfirstlane_b32 s5, v0\n\t" \
  "v_and_b32 v21, 63, v0\n\t" \
  "s_mov_b32 m0, 107744\n\t" \
  "s_cmp_lt_u32 s5, 128\n\t" \
  "s_cselect_b32 s3, 16, 24\n\t" \
  "v_lshlrev_b32 v22, 4, v21\n\t" \
  "s_load_dwordx2 s[16:17], s[0:1], s3\n\t" \
  "s_mul_i32 s4, s5, 50\n\t" \
  "s_add_u32 s66, s6, s4\n\t" \
  "s_addc_u32 s67, s7, 0\n\t" \
  "global_load_dwordx4 v[244:247], v22, s[66:67] offset:1024\n\t" \
  "global_load_dwordx4 v[16:19], v22, s[66:67] offset:2048\n\t" \
  "global_load_dwordx4 v[240:243], v22, s[6:7] offset:-2048\n\t" \
  "global_load_dwordx4 v[248:251], v22, s[6:7] offset:-1024\n\t" \
  "global_load_dwordx4 v[192:195], v22, s[66:67]\n\t" \
  "s_cmp_lg_u32 s5, 192\n\t" \
  "s_cbranch_scc1 .Lcnx_nc\n\t" \
  "s_getpc_b64 s[14:15]\n\t" \
  ".Lcnx_pc:\n\t" \
  "s_lshr_b32 s3, s2, 3\n\t" \
  "s_lshl_b32 s3, s3, 11\n\t" \
  "v_lshl_add_u32 v1, v21, 5, s3\n\t" \
  "v_min_u32 v1, .Lcnx_end-.Lcnx_pc, v1\n\t" \
  "global_load_lds_dword v1, s[14:15]\n\t" \
  ".Lcnx_nc:\n\t" \
  "v_mul_u32_u24 v254, 0x147b, v0\n\t" \
  "s_bfe_u32 s27, s2, 0x40003\n\t" \
  "v_bfe_u32 v1, v0, 2, 3\n\t" \
  "s_mul_i32 s27, s27, 6144\n\t" \
  "v_lshrrev_b32 v254, 17, v254\n\t" \
  "s_sub_u32 s3, s27, 576\n\t" \
  "v_and_b32 v20, 3, v0\n\t" \
  "s_add_u32 s4, s27, 6720\n\t" \
  "v_sub_u32 v254, v0, v254\n\t" \
  "s_min_u32 s4, s4, 0x18000\n\t" \
  "v_and_b32 v2, 0x1e0, v0\n\t" \
  "s_and_b32 s24, s2, 7\n\t" \
  "v_lshl_add_u32 v254, v254, 4, s3\n\t" \
  "s_lshl_b32 s24, s24, 6\n\t" \
  "v_lshl_or_b32 v20, v20, 3, v2\n\t" \
  "s_mov_b32 s26, 0x18000\n\t" \
  "v_cmp_gt_u32 vcc, s4, v254\n\t" \
  "s_cmp_ge_u32 s2, 128\n\t" \
  "s_cselect_b32 s3, 7, 0\n\t" \
  "s_cselect_b32 s4, 0, 7\n\t" \
  "v_add_u32 v1, s3, v1\n\t" \
  "s_cselect_b32 s3, 28, 35\n\t" \
  "v_cndmask_b32 v254, 2.0, v254, vcc\n\t" \
  "s_cselect_b32 s26, s26, 0xfffe8000\n\t" \
  "v_xor_b32 v1, s4, v1\n\t" \
  "s_add_u32 s24, s24, s3\n\t" \
  "v_and_b32 v1, 7, v1\n\t" \
  "s_movk_i32 s3, 1344\n\t" \
  "s_mul_i32 s24, s24, 0x18000\n\t" \
  "v_mad_u32_u24 v1, v1, s3, v20\n\t" \
  "s_lshl4_add_u32 s25, s5, 26864\n\t" \
  "s_mov_b32 s10, 0x3000000\n\t" \
  "s_mov_b32 s11, 0x20000\n\t" \
  "s_mov_b32 s14, 9408\n\t" \
  "s_mov_b32 s15, 0x20000\n\t" \
  "s_mov_b32 s18, 73728\n\t" \
  "s_mov_b32 s19, 0x20000\n\t" \
  "s_mov_b32 s22, 0x3000000\n\t" \
  "s_mov_b32 s23, 0x20000\n\t" \
  "s_add_u32 s3, s24, s26\n\t" \
  "s_add_u32 s3, s3, s27\n\t" \
  "s_mul_i32 s4, s26, 10\n\t" \
  "v_mov_b32 v24, s3\n\t" \
  "v_mad_i32_i24 v27, v21, s4, v24\n\t" \
  "s_setreg_imm32_b32 hwreg(HW_REG_MODE, 4, 2), 0\n\t" \
  "s_setreg_imm32_b32 hwreg(HW_REG_MODE, 9, 1), 0\n\t" \
  "s_lshl_b32 s65, s26, 4\n\t" \
  "s_lshr_b32 s3, s5, 7\n\t" \
  "s_add_u32 s3, s3, 8\n\t" \
  "s_mul_i32 s3, s3, s26\n\t" \
  "s_add_u32 s29, s24, s27\n\t" \
  "s_add_u32 s29, s29, s3\n\t" \
  "s_and_b32 s3, s5, 64\n\t" \
  "s_mul_i32 s3, s3, 48\n\t" \
  "s_add_u32 s29, s29, s3\n\t" \
  "s_bfe_u32 s3, s5, 0x20006\n\t" \
  "s_add_u32 s3, s3, 6\n\t" \
  "s_mul_i32 s3, s3, s26\n\t" \
  "s_add_u32 s30, s24, s65\n\t" \
  "s_add_u32 s30, s30, s3\n\t" \
  "s_lshl_b32 s31, s26, 2\n\t" \
  "s_mul_i32 s64, s26, 22\n\t" \
  "s_sub_u32 s64, s64, s65\n\t" \
  "s_cmp_ge_u32 s5, 256\n\t" \
  "s_cselect_b32 s27, s27, 0\n\t" \
  "s_cselect_b32 s3, -6, -2\n\t" \
  "s_mul_i32 s3, s3, s26\n\t" \
  "s_add_u32 s28, s29, s3\n\t" \
  "s_mov_b64 vcc, 0\n\t" \
  "s_waitcnt lgkmcnt(0)\n\t" \
  "v_add_co_u32 v24, vcc, s8, v27\n\t" \
  "v_mov_b32 v25, s9\n\t" \
  "v_addc_co_u32 v25, vcc, 0, v25, vcc\n\t" \
  "v_writelane_b32 v24, s12, 3\n\t" \
  "v_writelane_b32 v25, s13, 3\n\t" \
  "v_writelane_b32 v24, s32, 4\n\t" \
  "v_writelane_b32 v25, s33, 4\n\t" \
  "v_writelane_b32 v24, s34, 5\n\t" \
  "v_writelane_b32 v25, s35, 5\n\t" \
  "s_mov_b64 exec, 63\n\t" \
  "global_load_dword v26, v[24:25], off\n\t" \
  "s_mov_b64 exec, -1\n\t" \
  "s_mov_b64 vcc, 0\n\t" \
  "s_and_b32 s13, s13, 0xffff\n\t" \
  "s_and_b32 s9, s9, 0xffff\n\t" \
  "s_and_b32 s17, s17, 0xffff\n\t" \
  "s_cmp_ge_u32 s5, 192\n\t" \
  "s_cbranch_scc1 .Lcnx_nd\n\t" \
  "buffer_load_dwordx2 v[2:3], v1, s[12:15], 0 offen offset:0\n\t" \
  "buffer_load_dwordx2 v[4:5], v1, s[12:15], 0 offen offset:192\n\t" \
  "buffer_load_dwordx2 v[6:7], v1, s[12:15], 0 offen offset:384\n\t" \
  "buffer_load_dwordx2 v[8:9], v1, s[12:15], 0 offen offset:576\n\t" \
  "buffer_load_dwordx2 v[10:11], v1, s[12:15], 0 offen offset:768\n\t" \
  "buffer_load_dwordx2 v[12:13], v1, s[12:15], 0 offen offset:960\n\t" \
  "buffer_load_dwordx2 v[14:15], v1, s[12:15], 0 offen offset:1152\n\t" \
  ".Lcnx_nd:\n\t" \
  "s_and_b32 s3, s5, 0x140\n\t" \
  "s_cmp_lg_u32 s3, 64\n\t" \
  "s_cbranch_scc1 .Lcnx_nw\n\t" \
  "s_lshr_b32 s3, s2, 3\n\t" \
  "s_mul_i32 s3, s3, 2304\n\t" \
  "s_mov_b32 m0, 107744\n\t" \
  "v_mul_u32_u24 v21, 36, v21\n\t" \
  "buffer_load_dword v21, s[16:19], s3 offen lds\n\t" \
  ".Lcnx_nw:\n\t" \
  "s_add_u32 m0, s25, 0\n\t" \
  "s_add_u32 s24, s24, s26\n\t" \
  "buffer_load_dwordx4 v254, s[8:11], s24 offen lds\n\t" \
  "s_add_u32 m0, s25, 8200\n\t" \
  "s_add_u32 s24, s24, s26\n\t" \
  "buffer_load_dwordx4 v254, s[8:11], s24 offen lds\n\t" \
  "s_add_u32 m0, s25, 16400\n\t" \
  "s_add_u32 s24, s24, s26\n\t" \
  "buffer_load_dwordx4 v254, s[8:11], s24 offen lds\n\t" \
  "s_add_u32 m0, s25, 24600\n\t" \
  "s_add_u32 s24, s24, s26\n\t" \
  "buffer_load_dwordx4 v254, s[8:11], s24 offen lds\n\t" \
  "s_load_dwordx16 s[32:47], s[66:67], 0xc00\n\t" \
  "s_load_dwordx16 s[48:63], s[66:67], 0xc40\n\t" \
  "s_and_b32 s21, s21, 0xffff\n\t" \
  "s_waitcnt vmcnt(5)\n\t" \
  "v_mfma_f32_32x32x16_bf16 v[144:159], v[242:245], v[240:243], 0\n\t" \
  "v_mfma_f32_32x32x16_bf16 v[160:175], v[242:245], v[240:243], 0\n\t" \
  "v_bfe_u32 v236, v251, 3, 2\n\t" \
  "v_mul_u32_u24 v236, 0x5555, v236\n\t" \
  "v_add_u32 v252, s27, v16\n\t" \
  "s_lshl2_add_u32 s12, s26, s29\n\t" \
  "s_lshl3_add_u32 s13, s26, s29\n\t" \
  "v_mov_b32 v253, v246\n\t" \
  "s_movk_i32 s68, 0x2b80\n\t" \
  "s_mov_b32 s69, 0x2b800000\n\t" \
  "s_mov_b32 s14, 2.0\n\t" \
  "s_mov_b32 s15, 2.0\n\t" \
  "s_cmp_gt_u32 s5, 192\n\t" \
  "s_cbranch_scc1 .Lcnx_e2\n\t" \
  "s_cmp_eq_u32 s5, 192\n\t" \
  "s_cbranch_scc1 .Lcnx_e1\n\t" \
  "v_xor_b32 v23, 0x02020202, v19\n\t" \
  "s_waitcnt vmcnt(4)\n\t" \
  "v_perm_b32 v24, v4, 0, v19\n\t" \
  "v_perm_b32 v25, v12, v8, v19\n\t" \
  "v_perm_b32 v26, v6, v2, v19\n\t" \
  "v_perm_b32 v27, v14, v10, v19\n\t" \
  "v_perm_b32 v28, v8, v4, v19\n\t" \
  "v_perm_b32 v29, 0, v12, v19\n\t" \
  "ds_write_b64 v17, v[24:25] offset:0\n\t" \
  "ds_write_b64 v17, v[26:27] offset:8\n\t" \
  "ds_write_b64 v17, v[28:29] offset:16\n\t" \
  "v_perm_b32 v30, v4, 0, v23\n\t" \
  "v_perm_b32 v31, v12, v8, v23\n\t" \
  "v_perm_b32 v32, v6, v2, v23\n\t" \
  "v_perm_b32 v33, v14, v10, v23\n\t" \
  "v_perm_b32 v34, v8, v4, v23\n\t" \
  "v_perm_b32 v35, 0, v12, v23\n\t" \
  "ds_write_b64 v18, v[30:31] offset:0\n\t" \
  "ds_write_b64 v18, v[32:33] offset:8\n\t" \
  "ds_write_b64 v18, v[34:35] offset:16\n\t" \
  "v_perm_b32 v37, v5, 0, v19\n\t" \
  "v_perm_b32 v36, v13, v9, v19\n\t" \
  "v_perm_b32 v39, v7, v3, v19\n\t" \
  "v_perm_b32 v38, v15, v11, v19\n\t" \
  "v_perm_b32 v41, v9, v5, v19\n\t" \
  "v_perm_b32 v40, 0, v13, v19\n\t" \
  "ds_write_b64 v17, v[36:37] offset:384\n\t" \
  "ds_write_b64 v17, v[38:39] offset:392\n\t" \
  "ds_write_b64 v17, v[40:41] offset:400\n\t" \
  "v_perm_b32 v43, v5, 0, v23\n\t" \
  "v_perm_b32 v42, v13, v9, v23\n\t" \
  "v_perm_b32 v45, v7, v3, v23\n\t" \
  "v_perm_b32 v44, v15, v11, v23\n\t" \
  "v_perm_b32 v47, v9, v5, v23\n\t" \
  "v_perm_b32 v46, 0, v13, v23\n\t" \
  "ds_write_b64 v18, v[42:43] offset:384\n\t" \
  "ds_write_b64 v18, v[44:45] offset:392\n\t" \
  "ds_write_b64 v18, v[46:47] offset:400\n\t" \
  "s_branch .Lcnx_e3\n\t" \
  ".Lcnx_e1:\n\t" \
  "s_mov_b64 exec, 0x3ffff\n\t" \
  "ds_write_b64 v251, v[240:241] offset:47136\n\t" \
  "s_mov_b64 exec, 0xff\n\t" \
  "ds_write_b64 v17, v[240:241]\n\t" \
  "s_mov_b64 exec, -1\n\t" \
  "s_branch .Lcnx_e3\n\t" \
  ".Lcnx_e2:\n\t" \
  "s_setprio 1\n\t" \
  ".Lcnx_e3:\n\t" \
  "s_add_u32 m0, s25, 32800\n\t" \
  "s_add_u32 s24, s24, s26\n\t" \
  "buffer_load_dwordx4 v254, s[8:11], s24 offen lds\n\t" \
  "s_add_u32 m0, s25, 41000\n\t" \
  "s_add_u32 s24, s24, s26\n\t" \
  "buffer_load_dwordx4 v254, s[8:11], s24 offen lds\n\t" \
  "s_add_u32 m0, s25, 56880\n\t" \
  "s_add_u32 s24, s24, s26\n\t" \
  "buffer_load_dwordx4 v254, s[8:11], s24 offen lds\n\t" \
  "s_add_u32 m0, s25, 65080\n\t" \
  "s_add_u32 s24, s24, s26\n\t" \
  "buffer_load_dwordx4 v254, s[8:11], s24 offen lds\n\t" \
  "s_waitcnt vmcnt(6) lgkmcnt(0)\n\t" \
  "s_barrier\n\t" \
  "s_sub_u32 s66, s66, 233088\n\t" \
  "s_subb_u32 s67, s67, 0\n\t" \
  "v_add_u32 v232, s56, v247\n\t" \
  "ds_read2_b32 v[98:99], v247 offset0:0 offset1:6\n\t" \
  "ds_read2_b32 v[114:115], v247 offset0:192 offset1:198\n\t" \
  "ds_read2_b32 v[130:131], v232 offset0:0 offset1:6\n\t" \
  "ds_read2_b32 v[102:103], v247 offset0:12 offset1:18\n\t" \
  "ds_read2_b32 v[118:119], v247 offset0:204 offset1:210\n\t" \
  "ds_read2_b32 v[134:135], v232 offset0:12 offset1:18\n\t" \
  "ds_read2_b32 v[106:107], v247 offset0:24 offset1:30\n\t" \
  "ds_read2_b32 v[122:123], v247 offset0:216 offset1:222\n\t" \
  "ds_read2_b32 v[138:139], v232 offset0:24 offset1:30\n\t" \
  "ds_read2_b32 v[110:111], v247 offset0:36 offset1:42\n\t" \
  "ds_read2_b32 v[126:127], v247 offset0:228 offset1:234\n\t" \
  "ds_read2_b32 v[142:143], v232 offset0:36 offset1:42\n\t" \
  ".Lcnx_round:\n\t" \
  "v_add_u32 v232, 56880, v253\n\t" \
  "v_add_u32 v233, s39, v251\n\t" \
  "v_mfma_f32_32x32x16_bf16 v[176:191], v[242:245], v[240:243], 0\n\t" \
  "ds_read_b64 v[200:201], v253 offset:0\n\t" \
  "ds_read_b64 v[202:203], v253 offset:400\n\t" \
  "ds_read_b64 v[204:205], v253 offset:800\n\t" \
  "ds_read_b64 v[206:207], v253 offset:1200\n\t" \
  "ds_read_b64 v[208:209], v253 offset:8\n\t" \
  "ds_read_b64 v[210:211], v253 offset:408\n\t" \
  "ds_read_b64 v[212:213], v253 offset:808\n\t" \
  "ds_read_b64 v[214:215], v253 offset:1208\n\t" \
  "ds_read_b64 v[216:217], v253 offset:16\n\t" \
  "ds_read_b64 v[218:219], v253 offset:416\n\t" \
  "ds_read_b64 v[220:221], v253 offset:816\n\t" \
  "ds_read_b64 v[222:223], v253 offset:1216\n\t" \
  "s_cmp_lg_u32 s58, 0\n\t" \
  "s_cbranch_scc1 .Lcnx_c1\n\t" \
  "s_add_u32 m0, s25, 73280\n\t" \
  "s_add_u32 s24, s24, s26\n\t" \
  "buffer_load_dwordx4 v254, s[8:11], s24 offen lds\n\t" \
  "s_add_u32 m0, s25, 81480\n\t" \
  "s_add_u32 s24, s24, s26\n\t" \
  "buffer_load_dwordx4 v254, s[8:11], s24 offen lds\n\t" \
  "s_add_u32 m0, s25, 89680\n\t" \
  "s_add_u32 s24, s24, s26\n\t" \
  "buffer_load_dwordx4 v254, s[8:11], s24 offen lds\n\t" \
  "s_add_u32 m0, s25, 97880\n\t" \
  "s_add_u32 s24, s24, s26\n\t" \
  "buffer_load_dwordx4 v254, s[8:11], s24 offen lds\n\t" \
  ".Lcnx_c1:\n\t" \
  "s_waitcnt lgkmcnt(15)\n\t" \
  "v_dot2_f32_bf16 v96, v98, s68, 0\n\t" \
  "v_dot2_f32_bf16 v97, v98, s69, 0\n\t" \
  "v_dot2_f32_bf16 v98, v99, s68, 0\n\t" \
  "v_dot2_f32_bf16 v99, v99, s69, 0\n\t" \
  "v_dot2_f32_bf16 v112, v114, s68, 0\n\t" \
  "v_dot2_f32_bf16 v113, v114, s69, 0\n\t" \
  "v_dot2_f32_bf16 v114, v115, s68, 0\n\t" \
  "v_dot2_f32_bf16 v115, v115, s69, 0\n\t" \
  "v_dot2_f32_bf16 v128, v130, s68, 0\n\t" \
  "v_dot2_f32_bf16 v129, v130, s69, 0\n\t" \
  "v_dot2_f32_bf16 v130, v131, s68, 0\n\t" \
  "v_dot2_f32_bf16 v131, v131, s69, 0\n\t" \
  "s_waitcnt lgkmcnt(8)\n\t" \
  "v_smfmac_f32_16x16x64_bf16 v[144:147], v[96:99], v[200:207], v236\n\t" \
  "v_dot2_f32_bf16 v100, v102, s68, 0\n\t" \
  "v_dot2_f32_bf16 v101, v102, s69, 0\n\t" \
  "v_dot2_f32_bf16 v102, v103, s68, 0\n\t" \
  "v_dot2_f32_bf16 v103, v103, s69, 0\n\t" \
  "s_cmp_lg_u32 s58, 1\n\t" \
  "s_cbranch_scc1 .Lcnx_c2\n\t" \
  "s_waitcnt vmcnt(5)\n\t" \
  ".Lcnx_c2:\n\t" \
  "s_waitcnt vmcnt(8) lgkmcnt(0)\n\t" \
  "s_barrier\n\t" \
  "ds_read_b64 v[224:225], v253 offset:16400\n\t" \
  "ds_read_b64 v[226:227], v253 offset:16800\n\t" \
  "ds_read_b64 v[228:229], v253 offset:17200\n\t" \
  "ds_read_b64 v[230:231], v253 offset:17600\n\t" \
  "v_smfmac_f32_16x16x64_bf16 v[148:151], v[112:115], v[208:215], v236\n\t" \
  "s_cmp_lg_u32 s58, 1\n\t" \
  "s_cbranch_scc1 .Lcnx_c3\n\t" \
  "s_add_u32 m0, s25, 81480\n\t" \
  "s_add_u32 s24, s24, s26\n\t" \
  "buffer_load_dwordx4 v254, s[8:11], s24 offen lds\n\t" \
  ".Lcnx_c3:\n\t" \
  "ds_read_b64 v[200:201], v253 offset:16408\n\t" \
  "ds_read_b64 v[202:203], v253 offset:16808\n\t" \
  "ds_read_b64 v[204:205], v253 offset:17208\n\t" \
  "ds_read_b64 v[206:207], v253 offset:17608\n\t" \
  "v_dot2_f32_bf16 v116, v118, s68, 0\n\t" \
  "v_dot2_f32_bf16 v117, v118, s69, 0\n\t" \
  "v_dot2_f32_bf16 v118, v119, s68, 0\n\t" \
  "v_dot2_f32_bf16 v119, v119, s69, 0\n\t" \
  "v_smfmac_f32_16x16x64_bf16 v[152:155], v[128:131], v[216:223], v236\n\t" \
  "s_cmp_lg_u32 s58, 1\n\t" \
  "s_cbranch_scc1 .Lcnx_c4\n\t" \
  "s_add_u32 m0, s25, 89680\n\t" \
  "s_add_u32 s24, s24, s26\n\t" \
  "buffer_load_dwordx4 v254, s[8:11], s24 offen lds\n\t" \
  ".Lcnx_c4:\n\t" \
  "ds_read_b64 v[208:209], v253 offset:16416\n\t" \
  "ds_read_b64 v[210:211], v253 offset:16816\n\t" \
  "ds_read_b64 v[212:213], v253 offset:17216\n\t" \
  "ds_read_b64 v[214:215], v253 offset:17616\n\t" \
  "v_dot2_f32_bf16 v132, v134, s68, 0\n\t" \
  "v_dot2_f32_bf16 v133, v134, s69, 0\n\t" \
  "v_dot2_f32_bf16 v134, v135, s68, 0\n\t" \
  "v_dot2_f32_bf16 v135, v135, s69, 0\n\t" \
  "s_waitcnt lgkmcnt(8)\n\t" \
  "v_smfmac_f32_16x16x64_bf16 v[156:159], v[96:99], v[224:231], v236\n\t" \
  "s_cmp_lg_u32 s58, 1\n\t" \
  "s_cbranch_scc1 .Lcnx_c5\n\t" \
  "s_add_u32 m0, s25, 97880\n\t" \
  "s_add_u32 s24, s24, s26\n\t" \
  "buffer_load_dwordx4 v254, s[8:11], s24 offen lds\n\t" \
  ".Lcnx_c5:\n\t" \
  "v_dot2_f32_bf16 v104, v106, s68, 0\n\t" \
  "v_dot2_f32_bf16 v105, v106, s69, 0\n\t" \
  "v_dot2_f32_bf16 v106, v107, s68, 0\n\t" \
  "v_dot2_f32_bf16 v107, v107, s69, 0\n\t" \
  "v_smfmac_f32_16x16x64_bf16 v[144:147], v[100:103], v[224:231], v236\n\t" \
  "s_waitcnt vmcnt(6) lgkmcnt(0)\n\t" \
  "s_barrier\n\t" \
  "ds_read_b64 v[216:217], v253 offset:32800\n\t" \
  "ds_read_b64 v[218:219], v253 offset:33200\n\t" \
  "ds_read_b64 v[220:221], v253 offset:33600\n\t" \
  "ds_read_b64 v[222:223], v253 offset:34000\n\t" \
  "s_add_u32 m0, s25, 0\n\t" \
  "s_add_u32 s24, s24, s26\n\t" \
  "v_smfmac_f32_16x16x64_bf16 v[160:163], v[112:115], v[200:207], v236\n\t" \
  "buffer_load_dwordx4 v254, s[8:11], s24 offen lds\n\t" \
  "ds_read_b64 v[224:225], v253 offset:32808\n\t" \
  "ds_read_b64 v[226:227], v253 offset:33208\n\t" \
  "v_dot2_f32_bf16 v120, v122, s68, 0\n\t" \
  "v_dot2_f32_bf16 v121, v122, s69, 0\n\t" \
  "v_dot2_f32_bf16 v122, v123, s68, 0\n\t" \
  "v_dot2_f32_bf16 v123, v123, s69, 0\n\t" \
  "v_smfmac_f32_16x16x64_bf16 v[148:151], v[116:119], v[200:207], v236\n\t" \
  "ds_read_b64 v[228:229], v253 offset:33608\n\t" \
  "ds_read_b64 v[230:231], v253 offset:34008\n\t" \
  "s_add_u32 m0, s25, 8200\n\t" \
  "s_add_u32 s24, s24, s26\n\t" \
  "v_smfmac_f32_16x16x64_bf16 v[164:167], v[128:131], v[208:215], v236\n\t" \
  "buffer_load_dwordx4 v254, s[8:11], s24 offen lds\n\t" \
  "ds_read_b64 v[200:201], v253 offset:32816\n\t" \
  "ds_read_b64 v[202:203], v253 offset:33216\n\t" \
  "v_dot2_f32_bf16 v136, v138, s68, 0\n\t" \
  "v_dot2_f32_bf16 v137, v138, s69, 0\n\t" \
  "v_dot2_f32_bf16 v138, v139, s68, 0\n\t" \
  "v_dot2_f32_bf16 v139, v139, s69, 0\n\t" \
  "v_mfma_f32_32x32x16_bf16 v[0:15], v[242:245], v[240:243], 0\n\t" \
  "v_smfmac_f32_16x16x64_bf16 v[152:155], v[132:135], v[208:215], v236\n\t" \
  "ds_read_b64 v[204:205], v253 offset:33616\n\t" \
  "s_waitcnt lgkmcnt(7)\n\t" \
  "v_smfmac_f32_16x16x64_bf16 v[168:171], v[96:99], v[216:223], v236\n\t" \
  "ds_read_b64 v[206:207], v253 offset:34016\n\t" \
  "v_dot2_f32_bf16 v108, v110, s68, 0\n\t" \
  "v_dot2_f32_bf16 v109, v110, s69, 0\n\t" \
  "v_dot2_f32_bf16 v110, v111, s68, 0\n\t" \
  "v_dot2_f32_bf16 v111, v111, s69, 0\n\t" \
  "s_add_u32 m0, s25, 16400\n\t" \
  "s_add_u32 s24, s24, s26\n\t" \
  "v_smfmac_f32_16x16x64_bf16 v[156:159], v[100:103], v[216:223], v236\n\t" \
  "buffer_load_dwordx4 v254, s[8:11], s24 offen lds\n\t" \
  "v_smfmac_f32_16x16x64_bf16 v[144:147], v[104:107], v[216:223], v236\n\t" \
  "s_waitcnt vmcnt(7) lgkmcnt(0)\n\t" \
  "s_barrier\n\t" \
  "v_smfmac_f32_16x16x64_bf16 v[172:175], v[112:115], v[224:231], v236\n\t" \
  "ds_read_b64 v[208:209], v232 offset:0\n\t" \
  "ds_read_b64 v[210:211], v232 offset:400\n\t" \
  "v_dot2_f32_bf16 v124, v126, s68, 0\n\t" \
  "v_dot2_f32_bf16 v125, v126, s69, 0\n\t" \
  "v_dot2_f32_bf16 v126, v127, s68, 0\n\t" \
  "v_dot2_f32_bf16 v127, v127, s69, 0\n\t" \
  "s_add_u32 m0, s25, 24600\n\t" \
  "s_add_u32 s24, s24, s26\n\t" \
  "v_smfmac_f32_16x16x64_bf16 v[160:163], v[116:119], v[224:231], v236\n\t" \
  "buffer_load_dwordx4 v254, s[8:11], s24 offen lds\n\t" \
  "ds_read_b64 v[212:213], v232 offset:800\n\t" \
  "ds_read_b64 v[214:215], v232 offset:1200\n\t" \
  "v_smfmac_f32_16x16x64_bf16 v[148:151], v[120:123], v[224:231], v236\n\t" \
  "ds_read_b64 v[216:217], v232 offset:8\n\t" \
  "ds_read_b64 v[218:219], v232 offset:408\n\t" \
  "v_smfmac_f32_16x16x64_bf16 v[176:179], v[128:131], v[200:207], v236\n\t" \
  "ds_read_b64 v[220:221], v232 offset:808\n\t" \
  "ds_read_b64 v[222:223], v232 offset:1208\n\t" \
  "v_dot2_f32_bf16 v140, v142, s68, 0\n\t" \
  "v_dot2_f32_bf16 v141, v142, s69, 0\n\t" \
  "v_dot2_f32_bf16 v142, v143, s68, 0\n\t" \
  "v_dot2_f32_bf16 v143, v143, s69, 0\n\t" \
  "v_smfmac_f32_16x16x64_bf16 v[164:167], v[132:135], v[200:207], v236\n\t" \
  "ds_read_b64 v[224:225], v232 offset:16\n\t" \
  "s_add_u32 m0, s25, 32800\n\t" \
  "s_add_u32 s24, s24, s26\n\t" \
  "v_smfmac_f32_16x16x64_bf16 v[152:155], v[136:139], v[200:207], v236\n\t" \
  "buffer_load_dwordx4 v254, s[8:11], s24 offen lds\n\t" \
  "ds_read_b64 v[226:227], v232 offset:416\n\t" \
  "s_waitcnt lgkmcnt(6)\n\t" \
  "v_smfmac_f32_16x16x64_bf16 v[180:183], v[96:99], v[208:215], v236\n\t" \
  "ds_read_b64 v[228:229], v232 offset:816\n\t" \
  "v_smfmac_f32_16x16x64_bf16 v[168:171], v[100:103], v[208:215], v236\n\t" \
  "ds_read_b64 v[230:231], v232 offset:1216\n\t" \
  "v_mfma_f32_32x32x16_bf16 v[16:31], v[242:245], v[240:243], 0\n\t" \
  "v_mov_b32 v196, 0x3f003f00\n\t" \
  "v_mov_b32 v197, 0x3f003f00\n\t" \
  "v_smfmac_f32_16x16x64_bf16 v[156:159], v[104:107], v[208:215], v236\n\t" \
  "v_mov_b32 v198, 0x3f003f00\n\t" \
  "v_mov_b32 v199, 0x3f003f00\n\t" \
  "s_add_u32 m0, s25, 41000\n\t" \
  "s_add_u32 s24, s24, s26\n\t" \
  "v_smfmac_f32_16x16x64_bf16 v[144:147], v[108:111], v[208:215], v236\n\t" \
  "buffer_load_dwordx4 v254, s[8:11], s24 offen lds\n\t" \
  "s_waitcnt vmcnt(8) lgkmcnt(0)\n\t" \
  "s_barrier\n\t" \
  "v_smfmac_f32_16x16x64_bf16 v[184:187], v[112:115], v[216:223], v236\n\t" \
  "ds_read_b64 v[200:201], v232 offset:16400\n\t" \
  "ds_read_b64 v[202:203], v232 offset:16800\n\t" \
  "v_smfmac_f32_16x16x64_bf16 v[172:175], v[116:119], v[216:223], v236\n\t" \
  "ds_read_b64 v[204:205], v232 offset:17200\n\t" \
  "ds_read_b64 v[206:207], v232 offset:17600\n\t" \
  "v_smfmac_f32_16x16x64_bf16 v[160:163], v[120:123], v[216:223], v236\n\t" \
  "ds_read_b64 v[208:209], v232 offset:16408\n\t" \
  "v_cvt_pk_bf16_f32 v144, v144, v145\n\t" \
  "v_cvt_pk_bf16_f32 v145, v146, v147\n\t" \
  "s_add_u32 m0, s25, 56880\n\t" \
  "s_add_u32 s24, s24, s26\n\t" \
  "v_smfmac_f32_16x16x64_bf16 v[148:151], v[124:127], v[216:223], v236\n\t" \
  "buffer_load_dwordx4 v254, s[8:11], s24 offen lds\n\t" \
  "ds_read_b64 v[210:211], v232 offset:16808\n\t" \
  "v_smfmac_f32_16x16x64_bf16 v[188:191], v[128:131], v[224:231], v236\n\t" \
  "ds_read_b64 v[212:213], v232 offset:17208\n\t" \
  "v_smfmac_f32_16x16x64_bf16 v[176:179], v[132:135], v[224:231], v236\n\t" \
  "ds_read_b64 v[214:215], v232 offset:17608\n\t" \
  "v_smfmac_f32_16x16x64_bf16 v[164:167], v[136:139], v[224:231], v236\n\t" \
  "ds_read_b64 v[216:217], v232 offset:16416\n\t" \
  "v_cvt_pk_bf16_f32 v148, v148, v149\n\t" \
  "v_cvt_pk_bf16_f32 v149, v150, v151\n\t" \
  "s_add_u32 m0, s25, 65080\n\t" \
  "s_add_u32 s24, s24, s26\n\t" \
  "v_smfmac_f32_16x16x64_bf16 v[152:155], v[140:143], v[224:231], v236\n\t" \
  "buffer_load_dwordx4 v254, s[8:11], s24 offen lds\n\t" \
  "ds_read_b64 v[218:219], v232 offset:16816\n\t" \
  "s_waitcnt lgkmcnt(6)\n\t" \
  "v_smfmac_f32_16x16x64_bf16 v[0:3], v[96:99], v[200:207], v236\n\t" \
  "ds_read_b64 v[220:221], v232 offset:17216\n\t" \
  "v_smfmac_f32_16x16x64_bf16 v[180:183], v[100:103], v[200:207], v236\n\t" \
  "ds_read_b64 v[222:223], v232 offset:17616\n\t" \
  "v_mfma_f32_32x32x16_bf16 v[32:47], v[242:245], v[240:243], 0\n\t" \
  "v_smfmac_f32_16x16x64_bf16 v[168:171], v[104:107], v[200:207], v236\n\t" \
  "v_cvt_pk_bf16_f32 v152, v152, v153\n\t" \
  "v_cvt_pk_bf16_f32 v153, v154, v155\n\t" \
  "v_smfmac_f32_16x16x64_bf16 v[156:159], v[108:111], v[200:207], v236\n\t" \
  "s_waitcnt vmcnt(8) lgkmcnt(0)\n\t" \
  "s_barrier\n\t" \
  "s_add_u32 m0, s25, 73280\n\t" \
  "s_add_u32 s24, s24, s26\n\t" \
  "v_smfmac_f32_16x16x64_bf16 v[4:7], v[112:115], v[208:215], v236\n\t" \
  "buffer_load_dwordx4 v254, s[8:11], s24 offen lds\n\t" \
  "ds_read_b64 v[224:225], v232 offset:32800\n\t" \
  "ds_read_b64 v[226:227], v232 offset:33200\n\t" \
  "v_smfmac_f32_16x16x64_bf16 v[184:187], v[116:119], v[208:215], v236\n\t" \
  "ds_read_b64 v[228:229], v232 offset:33600\n\t" \
  "ds_read_b64 v[230:231], v232 offset:34000\n\t" \
  "s_add_u32 m0, s25, 81480\n\t" \
  "s_add_u32 s24, s24, s26\n\t" \
  "v_smfmac_f32_16x16x64_bf16 v[172:175], v[120:123], v[208:215], v236\n\t" \
  "buffer_load_dwordx4 v254, s[8:11], s24 offen lds\n\t" \
  "ds_read_b64 v[200:201], v232 offset:32808\n\t" \
  "v_cvt_pk_bf16_f32 v156, v156, v157\n\t" \
  "v_cvt_pk_bf16_f32 v157, v158, v159\n\t" \
  "v_smfmac_f32_16x16x64_bf16 v[160:163], v[124:127], v[208:215], v236\n\t" \
  "ds_read_b64 v[202:203], v232 offset:33208\n\t" \
  "v_smfmac_f32_16x16x64_bf16 v[8:11], v[128:131], v[216:223], v236\n\t" \
  "ds_read_b64 v[204:205], v232 offset:33608\n\t" \
  "v_smfmac_f32_16x16x64_bf16 v[188:191], v[132:135], v[216:223], v236\n\t" \
  "ds_read_b64 v[206:207], v232 offset:34008\n\t" \
  "v_smfmac_f32_16x16x64_bf16 v[176:179], v[136:139], v[216:223], v236\n\t" \
  "buffer_store_dwordx4 v[52:55], v250, s[20:23], s61 offen offset:0 sc1\n\t" \
  "ds_read_b64 v[208:209], v232 offset:32816\n\t" \
  "v_cvt_pk_bf16_f32 v160, v160, v161\n\t" \
  "v_cvt_pk_bf16_f32 v161, v162, v163\n\t" \
  "v_smfmac_f32_16x16x64_bf16 v[164:167], v[140:143], v[216:223], v236\n\t" \
  "ds_read_b64 v[210:211], v232 offset:33216\n\t" \
  "s_waitcnt lgkmcnt(6)\n\t" \
  "v_smfmac_f32_16x16x64_bf16 v[12:15], v[96:99], v[224:231], v236\n\t" \
  "ds_read_b64 v[212:213], v232 offset:33616\n\t" \
  "v_smfmac_f32_16x16x64_bf16 v[0:3], v[100:103], v[224:231], v236\n\t" \
  "ds_read_b64 v[214:215], v232 offset:34016\n\t" \
  "v_smfmac_f32_16x16x64_bf16 v[180:183], v[104:107], v[224:231], v236\n\t" \
  "s_nop 0\n\t" \
  "v_cvt_pk_bf16_f32 v164, v164, v165\n\t" \
  "v_cvt_pk_bf16_f32 v165, v166, v167\n\t" \
  "v_smfmac_f32_16x16x64_bf16 v[168:171], v[108:111], v[224:231], v236\n\t" \
  "s_waitcnt vmcnt(9) lgkmcnt(0)\n\t" \
  "s_barrier\n\t" \
  "v_smfmac_f32_16x16x64_bf16 v[16:19], v[112:115], v[200:207], v236\n\t" \
  "ds_write_b64 v193, v[144:145] offset:12288\n\t" \
  "ds_read_b64 v[216:217], v253 offset:0\n\t" \
  "v_smfmac_f32_16x16x64_bf16 v[4:7], v[116:119], v[200:207], v236\n\t" \
  "ds_write_b64 v193, v[148:149] offset:16384\n\t" \
  "ds_read_b64 v[218:219], v253 offset:400\n\t" \
  "v_smfmac_f32_16x16x64_bf16 v[184:187], v[120:123], v[200:207], v236\n\t" \
  "ds_write_b64 v193, v[152:153] offset:20480\n\t" \
  "ds_read_b64 v[220:221], v253 offset:800\n\t" \
  "v_cvt_pk_bf16_f32 v168, v168, v169\n\t" \
  "v_cvt_pk_bf16_f32 v169, v170, v171\n\t" \
  "v_smfmac_f32_16x16x64_bf16 v[172:175], v[124:127], v[200:207], v236\n\t" \
  "ds_write_b64 v193, v[156:157] offset:0\n\t" \
  "ds_read_b64 v[222:223], v253 offset:1200\n\t" \
  "v_smfmac_f32_16x16x64_bf16 v[20:23], v[128:131], v[208:215], v236\n\t" \
  "buffer_store_dwordx4 v[60:63], v250, s[20:23], s61 offen offset:64 sc1\n\t" \
  "ds_read_b64 v[224:225], v253 offset:8\n\t" \
  "ds_read_b64 v[226:227], v253 offset:408\n\t" \
  "v_smfmac_f32_16x16x64_bf16 v[8:11], v[132:135], v[208:215], v236\n\t" \
  "ds_write_b64 v193, v[160:161] offset:4096\n\t" \
  "ds_read_b64 v[228:229], v253 offset:808\n\t" \
  "v_smfmac_f32_16x16x64_bf16 v[188:191], v[136:139], v[208:215], v236\n\t" \
  "ds_write_b64 v193, v[164:165] offset:8192\n\t" \
  "ds_read_b64 v[230:231], v253 offset:1208\n\t" \
  "v_cvt_pk_bf16_f32 v172, v172, v173\n\t" \
  "v_cvt_pk_bf16_f32 v173, v174, v175\n\t" \
  "v_smfmac_f32_16x16x64_bf16 v[176:179], v[140:143], v[208:215], v236\n\t" \
  "ds_read_b64 v[200:201], v253 offset:16\n\t" \
  "ds_read_b64 v[202:203], v253 offset:416\n\t" \
  "s_waitcnt lgkmcnt(8)\n\t" \
  "v_smfmac_f32_16x16x64_bf16 v[24:27], v[96:99], v[216:223], v236\n\t" \
  "ds_read_b64 v[204:205], v253 offset:816\n\t" \
  "v_smfmac_f32_16x16x64_bf16 v[12:15], v[100:103], v[216:223], v236\n\t" \
  "ds_read_b64 v[206:207], v253 offset:1216\n\t" \
  "v_smfmac_f32_16x16x64_bf16 v[0:3], v[104:107], v[216:223], v236\n\t" \
  "v_cvt_pk_bf16_f32 v176, v176, v177\n\t" \
  "v_cvt_pk_bf16_f32 v177, v178, v179\n\t" \
  "v_smfmac_f32_16x16x64_bf16 v[180:183], v[108:111], v[216:223], v236\n\t" \
  "s_waitcnt vmcnt(8) lgkmcnt(0)\n\t" \
  "s_barrier\n\t" \
  "v_smfmac_f32_16x16x64_bf16 v[28:31], v[112:115], v[224:231], v236\n\t" \
  "ds_write_b64 v192, v[168:169] offset:0\n\t" \
  "ds_read_b64 v[208:209], v253 offset:16400\n\t" \
  "ds_read_b128 v[144:147], v194 offset:0\n\t" \
  "v_smfmac_f32_16x16x64_bf16 v[16:19], v[116:119], v[224:231], v236\n\t" \
  "ds_write_b64 v192, v[172:173] offset:4096\n\t" \
  "ds_read_b64 v[210:211], v253 offset:16800\n\t" \
  "ds_read_b128 v[148:151], v194 offset:4096\n\t" \
  "v_smfmac_f32_16x16x64_bf16 v[4:7], v[120:123], v[224:231], v236\n\t" \
  "buffer_store_dwordx4 v[68:71], v250, s[20:23], s61 offen offset:128 sc1\n\t" \
  "ds_read_b64 v[212:213], v253 offset:17200\n\t" \
  "ds_read_b64 v[214:215], v253 offset:17600\n\t" \
  "v_cvt_pk_bf16_f32 v180, v180, v181\n\t" \
  "v_cvt_pk_bf16_f32 v181, v182, v183\n\t" \
  "ds_read_b128 v[152:155], v194 offset:8192\n\t" \
  "v_smfmac_f32_16x16x64_bf16 v[184:187], v[124:127], v[224:231], v236\n\t" \
  "ds_write_b64 v192, v[176:177] offset:8192\n\t" \
  "ds_read_b64 v[216:217], v253 offset:16408\n\t" \
  "v_add_u32 v194, -93792, v194\n\t" \
  "v_smfmac_f32_16x16x64_bf16 v[32:35], v[128:131], v[200:207], v236\n\t" \
  "ds_read_b64 v[218:219], v253 offset:16808\n\t" \
  "ds_read_b64 v[220:221], v253 offset:17208\n\t" \
  "v_smfmac_f32_16x16x64_bf16 v[20:23], v[132:135], v[200:207], v236\n\t" \
  "ds_read_b64 v[222:223], v253 offset:17608\n\t" \
  "s_waitcnt lgkmcnt(11)\n\t" \
  "v_mfma_f32_16x16x32_bf16 v[168:171], v[144:147], v[144:147], 0\n\t" \
  "ds_read_b64 v[224:225], v253 offset:16416\n\t" \
  "v_smfmac_f32_16x16x64_bf16 v[8:11], v[136:139], v[200:207], v236\n\t" \
  "ds_read_b64 v[226:227], v253 offset:16816\n\t" \
  "v_cvt_pk_bf16_f32 v184, v184, v185\n\t" \
  "v_cvt_pk_bf16_f32 v185, v186, v187\n\t" \
  "v_mfma_f32_16x16x32_bf16 v[172:175], v[196:199], v[144:147], 0\n\t" \
  "ds_read_b64 v[228:229], v253 offset:17216\n\t" \
  "v_smfmac_f32_16x16x64_bf16 v[188:191], v[140:143], v[200:207], v236\n\t" \
  "ds_read_b64 v[230:231], v253 offset:17616\n\t" \
  "s_waitcnt lgkmcnt(12)\n\t" \
  "v_mfma_f32_16x16x32_bf16 v[168:171], v[148:151], v[148:151], v[168:171]\n\t" \
  "s_waitcnt lgkmcnt(10)\n\t" \
  "v_smfmac_f32_16x16x64_bf16 v[36:39], v[96:99], v[208:215], v236\n\t" \
  "v_mfma_f32_16x16x32_bf16 v[172:175], v[196:199], v[148:151], v[172:175]\n\t" \
  "v_smfmac_f32_16x16x64_bf16 v[24:27], v[100:103], v[208:215], v236\n\t" \
  "s_waitcnt lgkmcnt(9)\n\t" \
  "v_mfma_f32_16x16x32_bf16 v[168:171], v[152:155], v[152:155], v[168:171]\n\t" \
  "v_smfmac_f32_16x16x64_bf16 v[12:15], v[104:107], v[208:215], v236\n\t" \
  "v_cvt_pk_bf16_f32 v188, v188, v189\n\t" \
  "v_cvt_pk_bf16_f32 v189, v190, v191\n\t" \
  "v_mfma_f32_16x16x32_bf16 v[172:175], v[196:199], v[152:155], v[172:175]\n\t" \
  "v_smfmac_f32_16x16x64_bf16 v[0:3], v[108:111], v[208:215], v236\n\t" \
  "buffer_store_dwordx4 v[76:79], v250, s[20:23], s62 offen offset:0 sc1\n\t" \
  "s_waitcnt vmcnt(8) lgkmcnt(0)\n\t" \
  "s_barrier\n\t" \
  "s_cmp_lg_u32 s58, 0\n\t" \
  "s_cbranch_scc1 .Lcnx_c6\n\t" \
  "v_lshl_add_u32 v48, v233, 1, s63\n\t" \
  "s_lshl1_add_u32 m0, s39, 10016\n\t" \
  "v_add_u32 v49, 0x2000, v48\n\t" \
  "global_load_lds_dwordx4 v48, s[6:7]\n\t" \
  "s_add_u32 m0, m0, 66048\n\t" \
  "s_cmp_eq_u32 s39, 3584\n\t" \
  "s_cselect_b32 exec_hi, 0, -1\n\t" \
  "global_load_lds_dwordx4 v49, s[6:7]\n\t" \
  "s_mov_b64 exec, -1\n\t" \
  ".Lcnx_c6:\n\t" \
  "v_smfmac_f32_16x16x64_bf16 v[40:43], v[112:115], v[216:223], v236\n\t" \
  "ds_write_b64 v192, v[180:181] offset:12288\n\t" \
  "ds_read_b64 v[200:201], v253 offset:32800\n\t" \
  "v_smfmac_f32_16x16x64_bf16 v[28:31], v[116:119], v[216:223], v236\n\t" \
  "ds_write_b64 v192, v[184:185] offset:16384\n\t" \
  "ds_read_b64 v[202:203], v253 offset:33200\n\t" \
  "v_cndmask_b32 v168, v168, v169, s[40:41]\n\t" \
  "v_cndmask_b32 v170, v170, v171, s[40:41]\n\t" \
  "v_smfmac_f32_16x16x64_bf16 v[16:19], v[120:123], v[216:223], v236\n\t" \
  "ds_write_b64 v192, v[188:189] offset:20480\n\t" \
  "ds_read_b64 v[204:205], v253 offset:33600\n\t" \
  "v_cvt_pk_bf16_f32 v0, v0, v1\n\t" \
  "v_cvt_pk_bf16_f32 v1, v2, v3\n\t" \
  "v_cndmask_b32 v173, v168, v170, s[42:43]\n\t" \
  "v_mov_b32 v174, 0x1127c5ac\n\t" \
  "ds_read_b64_tr_b16 v[156:157], v195 offset:0\n\t" \
  "v_smfmac_f32_16x16x64_bf16 v[4:7], v[124:127], v[216:223], v236\n\t" \
  "ds_read_b64 v[206:207], v253 offset:34000\n\t" \
  "ds_read_b64 v[208:209], v253 offset:32808\n\t" \
  "s_not_b64 vcc, s[52:53]\n\t" \
  "ds_read_b64_tr_b16 v[158:159], v195 offset:4096\n\t" \
  "v_smfmac_f32_16x16x64_bf16 v[44:47], v[128:131], v[224:231], v236\n\t" \
  "ds_read_b64 v[210:211], v253 offset:33208\n\t" \
  "ds_read_b64 v[212:213], v253 offset:33608\n\t" \
  "ds_read_b64_tr_b16 v[160:161], v195 offset:2048\n\t" \
  "v_smfmac_f32_16x16x64_bf16 v[32:35], v[132:135], v[224:231], v236\n\t" \
  "ds_read_b64 v[214:215], v253 offset:34008\n\t" \
  "ds_read_b64 v[216:217], v253 offset:32816\n\t" \
  "ds_read_b64_tr_b16 v[162:163], v195 offset:6144\n\t" \
  "v_smfmac_f32_16x16x64_bf16 v[20:23], v[136:139], v[224:231], v236\n\t" \
  "ds_read_b64 v[218:219], v253 offset:33216\n\t" \
  "v_cvt_pk_bf16_f32 v4, v4, v5\n\t" \
  "v_cvt_pk_bf16_f32 v5, v6, v7\n\t" \
  "ds_read_b64_tr_b16 v[164:165], v195 offset:8192\n\t" \
  "v_smfmac_f32_16x16x64_bf16 v[8:11], v[140:143], v[224:231], v236\n\t" \
  "ds_read_b64 v[220:221], v253 offset:33616\n\t" \
  "ds_read_b64_tr_b16 v[166:167], v195 offset:10240\n\t" \
  "s_waitcnt lgkmcnt(12)\n\t" \
  "v_smfmac_f32_16x16x64_bf16 v[36:39], v[100:103], v[200:207], v236\n\t" \
  "ds_read_b64 v[222:223], v253 offset:34016\n\t" \
  "v_add_u32 v195, -93792, v195\n\t" \
  "v_smfmac_f32_16x16x64_bf16 v[24:27], v[104:107], v[200:207], v236\n\t" \
  "v_smfmac_f32_16x16x64_bf16 v[12:15], v[108:111], v[200:207], v236\n\t" \
  "buffer_store_dwordx4 v[84:87], v250, s[20:23], s62 offen offset:64 sc1\n\t" \
  "v_cvt_pk_bf16_f32 v8, v8, v9\n\t" \
  "v_cvt_pk_bf16_f32 v9, v10, v11\n\t" \
  "s_waitcnt vmcnt(7) lgkmcnt(0)\n\t" \
  "s_barrier\n\t" \
  "s_cmp_ge_u32 s36, 2\n\t" \
  "s_cbranch_scc1 .Lcnx_c7\n\t" \
  "s_mov_b32 m0, s46\n\t" \
  "s_nop 0\n\t" \
  "buffer_load_dwordx4 v252, s[16:19], s33 offen lds\n\t" \
  "buffer_load_dwordx4 v252, s[16:19], s34 offen offset:1024 lds\n\t" \
  "buffer_load_dwordx4 v252, s[16:19], s35 offen offset:2048 lds\n\t" \
  "s_add_u32 s33, s33, s32\n\t" \
  "s_add_u32 s34, s34, s32\n\t" \
  "s_add_u32 s35, s35, s32\n\t" \
  "s_mov_b32 m0, s47\n\t" \
  "s_nop 0\n\t" \
  "buffer_load_dwordx4 v252, s[16:19], s33 offen lds\n\t" \
  "buffer_load_dwordx4 v252, s[16:19], s34 offen offset:1024 lds\n\t" \
  "buffer_load_dwordx4 v252, s[16:19], s35 offen offset:2048 lds\n\t" \
  "s_add_u32 s33, s33, s32\n\t" \
  "s_add_u32 s34, s34, s32\n\t" \
  "s_add_u32 s35, s35, s32\n\t" \
  ".Lcnx_c7:\n\t" \
  "v_smfmac_f32_16x16x64_bf16 v[40:43], v[116:119], v[208:215], v236\n\t" \
  "ds_write_b64 v192, v[0:1] offset:24576\n\t" \
  "ds_read_b64 v[224:225], v232 offset:0\n\t" \
  "ds_read_b128 v[180:183], v194 offset:0\n\t" \
  "ds_read_b128 v[184:187], v194 offset:4096\n\t" \
  "ds_read_b128 v[188:191], v194 offset:8192\n\t" \
  "v_smfmac_f32_16x16x64_bf16 v[28:31], v[120:123], v[208:215], v236\n\t" \
  "ds_write_b64 v192, v[4:5] offset:28672\n\t" \
  "ds_read_b64 v[226:227], v232 offset:400\n\t" \
  "ds_read_b64 v[228:229], v232 offset:800\n\t" \
  "v_smfmac_f32_16x16x64_bf16 v[16:19], v[124:127], v[208:215], v236\n\t" \
  "ds_write_b64 v192, v[8:9] offset:32768\n\t" \
  "ds_read_b64 v[230:231], v232 offset:1200\n\t" \
  "ds_read_b64 v[200:201], v232 offset:8\n\t" \
  "v_cvt_pk_bf16_f32 v12, v12, v13\n\t" \
  "v_cvt_pk_bf16_f32 v13, v14, v15\n\t" \
  "v_smfmac_f32_16x16x64_bf16 v[44:47], v[132:135], v[216:223], v236\n\t" \
  "ds_write_b64 v192, v[12:13] offset:36864\n\t" \
  "ds_read_b64 v[202:203], v232 offset:408\n\t" \
  "ds_read_b64 v[204:205], v232 offset:808\n\t" \
  "s_waitcnt lgkmcnt(11)\n\t" \
  "v_mfma_f32_16x16x32_bf16 v[176:179], v[180:183], v[180:183], 0\n\t" \
  "ds_read_b64 v[206:207], v232 offset:1208\n\t" \
  "v_mfma_f32_16x16x32_bf16 v[168:171], v[196:199], v[180:183], 0\n\t" \
  "ds_read_b64 v[208:209], v232 offset:16\n\t" \
  "v_smfmac_f32_16x16x64_bf16 v[32:35], v[136:139], v[216:223], v236\n\t" \
  "ds_read_b64 v[210:211], v232 offset:416\n\t" \
  "ds_read_b64 v[212:213], v232 offset:816\n\t" \
  "s_waitcnt lgkmcnt(14)\n\t" \
  "v_mfma_f32_16x16x32_bf16 v[176:179], v[184:187], v[184:187], v[176:179]\n\t" \
  "ds_read_b64 v[214:215], v232 offset:1216\n\t" \
  "v_mfma_f32_16x16x32_bf16 v[168:171], v[196:199], v[184:187], v[168:171]\n\t" \
  "v_smfmac_f32_16x16x64_bf16 v[20:23], v[140:143], v[216:223], v236\n\t" \
  "v_cvt_pk_bf16_f32 v16, v16, v17\n\t" \
  "v_cvt_pk_bf16_f32 v17, v18, v19\n\t" \
  "s_waitcnt lgkmcnt(14)\n\t" \
  "v_mfma_f32_16x16x32_bf16 v[176:179], v[188:191], v[188:191], v[176:179]\n\t" \
  "ds_write_b64 v192, v[16:17] offset:40960\n\t" \
  "v_mfma_f32_16x16x32_bf16 v[168:171], v[196:199], v[188:191], v[168:171]\n\t" \
  "s_waitcnt lgkmcnt(10)\n\t" \
  "v_smfmac_f32_16x16x64_bf16 v[36:39], v[104:107], v[224:231], v236\n\t" \
  "v_mov_b32 v183, 0\n\t" \
  "v_mov_b32 v187, 0\n\t" \
  "s_nop 1\n\t" \
  "v_cndmask_b32 v176, v176, v177, s[40:41]\n\t" \
  "v_cndmask_b32 v178, v178, v179, s[40:41]\n\t" \
  "v_cndmask_b32 v176, v176, v178, s[42:43]\n\t" \
  "v_smfmac_f32_16x16x64_bf16 v[24:27], v[108:111], v[224:231], v236\n\t" \
  "v_cndmask_b32_dpp v172, v168, v172, vcc row_ror:8 row_mask:0xf bank_mask:0xf\n\t" \
  "v_mul_f32 v172, 0x3daaaaab, v172\n\t" \
  "v_cndmask_b32_dpp v173, v176, v173, vcc row_ror:8 row_mask:0xf bank_mask:0xf\n\t" \
  "v_fmamk_f32 v173, v173, 0x3e2aaaab, v174\n\t" \
  "s_waitcnt vmcnt(5) lgkmcnt(0)\n\t" \
  "s_barrier\n\t" \
  "ds_read_b64 v[216:217], v232 offset:16400\n\t" \
  "ds_read_b64 v[218:219], v232 offset:16800\n\t" \
  "ds_read_b64 v[220:221], v232 offset:17200\n\t" \
  "ds_read_b64 v[222:223], v232 offset:17600\n\t" \
  "v_smfmac_f32_16x16x64_bf16 v[40:43], v[120:123], v[200:207], v236\n\t" \
  "ds_read_b64 v[224:225], v232 offset:16408\n\t" \
  "ds_read_b64 v[226:227], v232 offset:16808\n\t" \
  "v_cvt_pk_bf16_f32 v20, v20, v21\n\t" \
  "v_cvt_pk_bf16_f32 v21, v22, v23\n\t" \
  "v_fma_f32 v173, -v172, v172, v173\n\t" \
  "v_max_f32 v173, v174, v173\n\t" \
  "v_rsq_f32 v173, v173 div:2\n\t" \
  "s_mov_b64 exec, s[52:53]\n\tv_mul_f32 v173, 0x08805555, v173\n\ts_mov_b64 exec, -1\n\t" \
  "v_smfmac_f32_16x16x64_bf16 v[28:31], v[124:127], v[200:207], v236\n\t" \
  "buffer_store_dwordx4 v[92:95], v250, s[20:23], s62 offen offset:128 sc1\n\t" \
  "ds_read_b64 v[228:229], v232 offset:17208\n\t" \
  "ds_read_b64 v[230:231], v232 offset:17608\n\t" \
  "s_mov_b64 vcc, s[40:41]\n\t" \
  "v_cvt_pk_bf16_f32 v175, v173, v173\n\t" \
  "v_and_b32 v173, 0xffff0000, v175\n\t" \
  "v_mul_f32 v174, -v172, v173 div:2\n\t" \
  "v_cndmask_b32_sdwa v175, v175, v173, vcc dst_sel:DWORD dst_unused:UNUSED_PAD src0_sel:WORD_1 src1_sel:DWORD\n\t" \
  "v_smfmac_f32_16x16x64_bf16 v[44:47], v[136:139], v[208:215], v236\n\t" \
  "ds_write_b64 v192, v[20:21] offset:45056\n\t" \
  "ds_read_b64 v[200:201], v232 offset:16416\n\t" \
  "v_cvt_pk_bf16_f32 v24, v24, v25\n\t" \
  "v_cvt_pk_bf16_f32 v25, v26, v27\n\t" \
  "v_and_b32 v172, 0xffff0000, v174\n\t" \
  "s_or_b64 vcc, s[52:53], s[42:43]\n\t" \
  "v_sub_f32 v174, v174, v172\n\t" \
  "v_cndmask_b32 v180, v175, v240, vcc\n\t" \
  "v_smfmac_f32_16x16x64_bf16 v[32:35], v[140:143], v[208:215], v236\n\t" \
  "ds_write_b64 v192, v[24:25] offset:56880\n\t" \
  "ds_read_b64 v[202:203], v232 offset:16816\n\t" \
  "v_perm_b32 v174, v172, v174, s55\n\t" \
  "v_cndmask_b32_dpp v184, v175, v240, vcc row_ror:8 row_mask:0xf bank_mask:0xf\n\t" \
  "s_mov_b64 vcc, s[52:53]\n\t" \
  "v_cndmask_b32 v182, v174, v240, vcc\n\t" \
  "ds_read_b64_tr_b16 v[144:145], v195 offset:0\n\t" \
  "ds_read_b64_tr_b16 v[146:147], v195 offset:4096\n\t" \
  "s_waitcnt lgkmcnt(10)\n\t" \
  "v_smfmac_f32_16x16x64_bf16 v[36:39], v[108:111], v[216:223], v236\n\t" \
  "ds_read_b64 v[204:205], v232 offset:17216\n\t" \
  "ds_read_b64 v[206:207], v232 offset:17616\n\t" \
  "v_cvt_pk_bf16_f32 v28, v28, v29\n\t" \
  "v_cvt_pk_bf16_f32 v29, v30, v31\n\t" \
  "v_cndmask_b32_dpp v186, v174, v240, vcc row_ror:8 row_mask:0xf bank_mask:0xf\n\t" \
  "s_orn2_b64 vcc, s[52:53], s[42:43]\n\t" \
  "v_cndmask_b32 v181, v175, v240, vcc\n\t" \
  "v_cndmask_b32_dpp v185, v175, v240, vcc row_ror:8 row_mask:0xf bank_mask:0xf\n\t" \
  "ds_read_b64_tr_b16 v[148:149], v195 offset:2048\n\t" \
  "ds_read_b64_tr_b16 v[150:151], v195 offset:6144\n\t" \
  "s_waitcnt lgkmcnt(10)\n\t" \
  "v_smfmac_f32_16x16x64_bf16 v[40:43], v[124:127], v[224:231], v236\n\t" \
  "ds_write_b64 v192, v[28:29] offset:60976\n\t" \
  "ds_read_b64_tr_b16 v[152:153], v195 offset:8192\n\t" \
  "ds_read_b64_tr_b16 v[154:155], v195 offset:10240\n\t" \
  "v_mfma_f32_16x16x16_bf16 v[0:3], v[196:197], v[182:183], 0\n\t" \
  "v_mfma_f32_16x16x16_bf16 v[4:7], v[196:197], v[186:187], 0\n\t" \
  "s_waitcnt lgkmcnt(5)\n\t" \
  "v_smfmac_f32_16x16x64_bf16 v[44:47], v[140:143], v[200:207], v236\n\t" \
  "v_cvt_pk_bf16_f32 v32, v32, v33\n\t" \
  "v_cvt_pk_bf16_f32 v33, v34, v35\n\t" \
  "v_mfma_f32_16x16x16_bf16 v[188:191], v[156:157], v[180:181], v[0:3]\n\t" \
  "ds_write_b64 v192, v[32:33] offset:65072\n\t" \
  "v_mfma_f32_16x16x16_bf16 v[176:179], v[158:159], v[180:181], v[0:3]\n\t" \
  "v_cvt_pk_bf16_f32 v36, v36, v37\n\t" \
  "v_cvt_pk_bf16_f32 v37, v38, v39\n\t" \
  "ds_write_b64 v193, v[36:37] offset:0\n\t" \
  "v_cvt_pk_bf16_f32 v40, v40, v41\n\t" \
  "v_cvt_pk_bf16_f32 v41, v42, v43\n\t" \
  "ds_write_b64 v193, v[40:41] offset:4096\n\t" \
  "v_mfma_f32_16x16x16_bf16 v[168:171], v[160:161], v[180:181], v[0:3]\n\t" \
  "v_cvt_pk_bf16_f32 v96, v188, v189\n\t" \
  "v_cvt_pk_bf16_f32 v97, v190, v191\n\t" \
  "v_mfma_f32_16x16x16_bf16 v[172:175], v[162:163], v[180:181], v[0:3]\n\t" \
  "v_cvt_pk_bf16_f32 v44, v44, v45\n\t" \
  "v_cvt_pk_bf16_f32 v45, v46, v47\n\t" \
  "ds_write_b64 v193, v[44:45] offset:8192\n\t" \
  "v_cvt_pk_bf16_f32 v98, v176, v177\n\t" \
  "v_cvt_pk_bf16_f32 v99, v178, v179\n\t" \
  "v_mfma_f32_16x16x16_bf16 v[188:191], v[164:165], v[180:181], v[0:3]\n\t" \
  "v_cvt_pk_bf16_f32 v112, v168, v169\n\t" \
  "v_cvt_pk_bf16_f32 v113, v170, v171\n\t" \
  "v_mfma_f32_16x16x16_bf16 v[176:179], v[166:167], v[180:181], v[0:3]\n\t" \
  "v_cvt_pk_bf16_f32 v114, v172, v173\n\t" \
  "v_cvt_pk_bf16_f32 v115, v174, v175\n\t" \
  "s_nop 2\n\t" \
  "v_cvt_pk_bf16_f32 v128, v188, v189\n\t" \
  "v_cvt_pk_bf16_f32 v129, v190, v191\n\t" \
  "s_nop 0\n\t" \
  "v_cvt_pk_bf16_f32 v130, v176, v177\n\t" \
  "v_cvt_pk_bf16_f32 v131, v178, v179\n\t" \
  "s_cmp_lg_u32 s58, 0\n\t" \
  "s_cbranch_scc1 .Lcnx_c8\n\t" \
  "s_waitcnt vmcnt(1)\n\t" \
  ".Lcnx_c8:\n\t" \
  "s_waitcnt lgkmcnt(0)\n\t" \
  "s_barrier\n\t" \
  "ds_read_b64_tr_b16 v[208:209], v248 offset:43048\n\t" \
  "ds_read_b64_tr_b16 v[210:211], v248 offset:44072\n\t" \
  "ds_read_b128 v[104:107], v194 offset:24576\n\t" \
  "ds_read_b128 v[120:123], v194 offset:28672\n\t" \
  "ds_read_b128 v[136:139], v194 offset:32768\n\t" \
  "v_add_u32 v194, s54, v194\n\t" \
  "ds_read_b128 v[108:111], v194 offset:49152\n\t" \
  "ds_read_b128 v[124:127], v194 offset:53248\n\t" \
  "ds_read_b128 v[140:143], v194 offset:57344\n\t" \
  "v_mfma_f32_16x16x16_bf16 v[168:171], v[144:145], v[184:185], v[4:7]\n\t" \
  "ds_read_b64_tr_b16 v[212:213], v248 offset:46120\n\t" \
  "ds_read_b64_tr_b16 v[214:215], v248 offset:47144\n\t" \
  "v_mfma_f32_16x16x16_bf16 v[172:175], v[146:147], v[184:185], v[4:7]\n\t" \
  "ds_read_b64_tr_b16 v[216:217], v248 offset:45096\n\t" \
  "ds_read_b64_tr_b16 v[218:219], v248 offset:48168\n\t" \
  "s_cmp_eq_u32 s37, 0\n\t" \
  "s_cbranch_scc1 .Lcnx_f0\n.Lcnx_g0:\n\t" \
  "v_mfma_f32_16x16x16_bf16 v[188:191], v[148:149], v[184:185], v[4:7]\n\t" \
  "ds_read_b64_tr_b16 v[160:161], v248 offset:43560\n\t" \
  "ds_read_b64_tr_b16 v[162:163], v248 offset:44584\n\t" \
  "v_cvt_pk_bf16_f32 v100, v168, v169\n\t" \
  "v_cvt_pk_bf16_f32 v101, v170, v171\n\t" \
  "v_mfma_f32_16x16x16_bf16 v[176:179], v[150:151], v[184:185], v[4:7]\n\t" \
  "ds_read_b64_tr_b16 v[164:165], v248 offset:46632\n\t" \
  "ds_read_b64_tr_b16 v[166:167], v248 offset:47656\n\t" \
  "s_cmp_eq_u32 s37, 0\n\t" \
  "s_cbranch_scc1 .Lcnx_f1\n.Lcnx_g1:\n\t" \
  "v_cvt_pk_bf16_f32 v102, v172, v173\n\t" \
  "v_cvt_pk_bf16_f32 v103, v174, v175\n\t" \
  "v_mfma_f32_16x16x16_bf16 v[168:171], v[152:153], v[184:185], v[4:7]\n\t" \
  "buffer_load_dwordx4 v[236:239], v250, s[8:11], s28 offen offset:0 nt\n\t" \
  "ds_read_b64_tr_b16 v[232:233], v248 offset:45608\n\t" \
  "ds_read_b64_tr_b16 v[234:235], v248 offset:48680\n\t" \
  "v_cvt_pk_bf16_f32 v116, v188, v189\n\t" \
  "v_cvt_pk_bf16_f32 v117, v190, v191\n\t" \
  "v_mfma_f32_16x16x16_bf16 v[172:175], v[154:155], v[184:185], v[4:7]\n\t" \
  "ds_read_b64_tr_b16 v[24:25], v195 offset:24576\n\t" \
  "ds_read_b64_tr_b16 v[26:27], v195 offset:28672\n\t" \
  "v_cvt_pk_bf16_f32 v118, v176, v177\n\t" \
  "v_cvt_pk_bf16_f32 v119, v178, v179\n\t" \
  "s_waitcnt lgkmcnt(15)\n\t" \
  "v_mfma_f32_16x16x32_bf16 v[144:147], v[208:211], v[96:99], 0\n\t" \
  "ds_read_b64_tr_b16 v[28:29], v195 offset:26624\n\t" \
  "ds_read_b64_tr_b16 v[30:31], v195 offset:30720\n\t" \
  "v_cvt_pk_bf16_f32 v132, v168, v169\n\t" \
  "v_cvt_pk_bf16_f32 v133, v170, v171\n\t" \
  "s_waitcnt lgkmcnt(12)\n\t" \
  "v_mfma_f32_16x16x32_bf16 v[144:147], v[212:215], v[112:115], v[144:147]\n\t" \
  "buffer_store_dwordx4 v[48:51], v250, s[20:23], s14 offen offset:0 sc1\n\t" \
  "ds_read_b64_tr_b16 v[32:33], v195 offset:32768\n\t" \
  "ds_read_b64_tr_b16 v[34:35], v195 offset:34816\n\t" \
  "v_cvt_pk_bf16_f32 v134, v172, v173\n\t" \
  "v_cvt_pk_bf16_f32 v135, v174, v175\n\t" \
  "v_mfma_f32_16x16x32_bf16 v[152:155], v[208:211], v[100:103], 0\n\t" \
  "v_add_u32 v195, s54, v195\n\t" \
  "ds_read_b64_tr_b16 v[36:37], v195 offset:49152\n\t" \
  "ds_read_b64_tr_b16 v[38:39], v195 offset:53248\n\t" \
  "s_waitcnt lgkmcnt(14)\n\t" \
  "v_mfma_f32_16x16x32_bf16 v[144:147], v[216:219], v[128:131], v[144:147]\n\t" \
  "ds_read_b64_tr_b16 v[40:41], v195 offset:51200\n\t" \
  "v_mfma_f32_16x16x32_bf16 v[152:155], v[212:215], v[116:119], v[152:155]\n\t" \
  "ds_read_b64_tr_b16 v[42:43], v195 offset:55296\n\t" \
  "v_mfma_f32_16x16x32_bf16 v[152:155], v[216:219], v[132:135], v[152:155]\n\t" \
  "ds_read_b64_tr_b16 v[44:45], v195 offset:57344\n\t" \
  "v_mfma_f32_16x16x32_bf16 v[8:11], v[104:107], v[104:107], 0\n\t" \
  "ds_read_b64_tr_b16 v[46:47], v195 offset:59392\n\t" \
  "v_mfma_f32_16x16x32_bf16 v[12:15], v[196:199], v[104:107], 0\n\t" \
  "v_cvt_pknorm_i16_f32 v220, |v146|, |v147|\n\t" \
  "v_cvt_pknorm_i16_f32 v222, |v144|, |v145|\n\t" \
  "v_mfma_f32_16x16x32_bf16 v[8:11], v[120:123], v[120:123], v[8:11]\n\t" \
  "v_lshlrev_b32_sdwa v221, 2, v220 dst_sel:DWORD dst_unused:UNUSED_PAD src0_sel:DWORD src1_sel:BYTE_3\n\t" \
  "v_lshlrev_b32_sdwa v220, 2, v220 dst_sel:DWORD dst_unused:UNUSED_PAD src0_sel:DWORD src1_sel:BYTE_1\n\t" \
  "v_mfma_f32_16x16x32_bf16 v[12:15], v[196:199], v[120:123], v[12:15]\n\t" \
  "ds_read_b32 v221, v221 offset:10016\n\t" \
  "v_lshlrev_b32_sdwa v223, 2, v222 dst_sel:DWORD dst_unused:UNUSED_PAD src0_sel:DWORD src1_sel:BYTE_3\n\t" \
  "v_lshlrev_b32_sdwa v222, 2, v222 dst_sel:DWORD dst_unused:UNUSED_PAD src0_sel:DWORD src1_sel:BYTE_1\n\t" \
  "v_mfma_f32_16x16x32_bf16 v[8:11], v[136:139], v[136:139], v[8:11]\n\t" \
  "ds_read_b32 v220, v220 offset:10016\n\t" \
  "v_lshlrev_b32_sdwa v225, 1, v155 dst_sel:DWORD dst_unused:UNUSED_PAD src0_sel:DWORD src1_sel:WORD_1\n\t" \
  "v_lshlrev_b32_sdwa v224, 1, v154 dst_sel:DWORD dst_unused:UNUSED_PAD src0_sel:DWORD src1_sel:WORD_1\n\t" \
  "v_mfma_f32_16x16x32_bf16 v[12:15], v[196:199], v[136:139], v[12:15]\n\t" \
  "ds_read_b32 v223, v223 offset:10016\n\t" \
  "v_lshlrev_b32_sdwa v227, 1, v153 dst_sel:DWORD dst_unused:UNUSED_PAD src0_sel:DWORD src1_sel:WORD_1\n\t" \
  "v_lshlrev_b32_sdwa v226, 1, v152 dst_sel:DWORD dst_unused:UNUSED_PAD src0_sel:DWORD src1_sel:WORD_1\n\t" \
  "v_mfma_f32_16x16x32_bf16 v[16:19], v[108:111], v[108:111], 0\n\t" \
  "ds_read_b32 v222, v222 offset:10016\n\t" \
  "v_mfma_f32_16x16x32_bf16 v[20:23], v[196:199], v[108:111], 0\n\t" \
  "ds_read_u16_d16_hi v225, v225 offset:10528\n\t" \
  "v_cndmask_b32 v8, v8, v9, s[40:41]\n\t" \
  "v_cndmask_b32 v10, v10, v11, s[40:41]\n\t" \
  "v_mfma_f32_16x16x32_bf16 v[16:19], v[124:127], v[124:127], v[16:19]\n\t" \
  "ds_read_u16 v224, v224 offset:10528\n\t" \
  "v_cndmask_b32 v13, v8, v10, s[42:43]\n\t" \
  "v_mov_b32 v14, 0x1127c5ac\n\t" \
  "v_mfma_f32_16x16x32_bf16 v[20:23], v[196:199], v[124:127], v[20:23]\n\t" \
  "buffer_store_dwordx4 v[56:59], v250, s[20:23], s14 offen offset:64 sc1\n\t" \
  "ds_read_u16_d16_hi v227, v227 offset:10528\n\t" \
  "s_not_b64 vcc, s[52:53]\n\t" \
  "v_mfma_f32_16x16x32_bf16 v[16:19], v[140:143], v[140:143], v[16:19]\n\t" \
  "ds_read_u16 v226, v226 offset:10528\n\t" \
  "s_waitcnt lgkmcnt(4)\n\t" \
  "v_fma_mix_f32 v221, |v147|, v221, v221 op_sel:[0,1,0] op_sel_hi:[0,1,1] clamp\n\t" \
  "v_fma_mix_f32 v220, |v146|, v220, v220 op_sel:[0,1,0] op_sel_hi:[0,1,1] clamp\n\t" \
  "v_mfma_f32_16x16x32_bf16 v[20:23], v[196:199], v[140:143], v[20:23]\n\t" \
  "v_fma_mix_f32 v223, |v145|, v223, v223 op_sel:[0,1,0] op_sel_hi:[0,1,1] clamp\n\t" \
  "v_fma_mix_f32 v222, |v144|, v222, v222 op_sel:[0,1,0] op_sel_hi:[0,1,1] clamp\n\t" \
  "v_mfma_f32_16x16x32_bf16 v[148:151], v[160:163], v[96:99], 0\n\t" \
  "s_waitcnt lgkmcnt(0)\n\t" \
  "v_or_b32 v155, v224, v225\n\t" \
  "v_or_b32 v154, v226, v227\n\t" \
  "v_mfma_f32_16x16x32_bf16 v[156:159], v[160:163], v[100:103], 0\n\t" \
  "v_cndmask_b32 v16, v16, v17, s[40:41]\n\t" \
  "v_cndmask_b32 v18, v18, v19, s[40:41]\n\t" \
  "v_cndmask_b32 v16, v16, v18, s[42:43]\n\t" \
  "v_cndmask_b32_dpp v12, v20, v12, vcc row_ror:8 row_mask:0xf bank_mask:0xf\n\t" \
  "v_mul_f32 v12, 0x3daaaaab, v12\n\t" \
  "v_mfma_f32_16x16x32_bf16 v[148:151], v[164:167], v[112:115], v[148:151]\n\t" \
  "v_cndmask_b32_dpp v13, v16, v13, vcc row_ror:8 row_mask:0xf bank_mask:0xf\n\t" \
  "v_fmamk_f32 v13, v13, 0x3e2aaaab, v14\n\t" \
  "v_fma_f32 v13, -v12, v12, v13\n\t" \
  "v_max_f32 v13, v14, v13\n\t" \
  "v_rsq_f32 v13, v13 div:2\n\t" \
  "v_mfma_f32_16x16x32_bf16 v[156:159], v[164:167], v[116:119], v[156:159]\n\t" \
  "s_mov_b64 exec, s[52:53]\n\tv_mul_f32 v13, 0x08805555, v13\n\ts_mov_b64 exec, -1\n\t" \
  "s_mov_b64 vcc, s[40:41]\n\t" \
  "v_cvt_pk_bf16_f32 v15, v13, v13\n\t" \
  "v_and_b32 v13, 0xffff0000, v15\n\t" \
  "v_mul_f32 v14, -v12, v13 div:2\n\t" \
  "v_cndmask_b32_sdwa v15, v15, v13, vcc dst_sel:DWORD dst_unused:UNUSED_PAD src0_sel:WORD_1 src1_sel:DWORD\n\t" \
  "v_and_b32 v12, 0xffff0000, v14\n\t" \
  "v_mfma_f32_16x16x32_bf16 v[148:151], v[232:235], v[128:131], v[148:151]\n\t" \
  "s_or_b64 vcc, s[52:53], s[42:43]\n\t" \
  "v_sub_f32 v14, v14, v12\n\t" \
  "v_cndmask_b32 v180, v15, v240, vcc\n\t" \
  "v_perm_b32 v14, v12, v14, s55\n\t" \
  "v_cndmask_b32_dpp v184, v15, v240, vcc row_ror:8 row_mask:0xf bank_mask:0xf\n\t" \
  "v_mfma_f32_16x16x32_bf16 v[156:159], v[232:235], v[132:135], v[156:159]\n\t" \
  "s_mov_b64 vcc, s[52:53]\n\t" \
  "v_cndmask_b32 v182, v14, v240, vcc\n\t" \
  "v_cndmask_b32_dpp v186, v14, v240, vcc row_ror:8 row_mask:0xf bank_mask:0xf\n\t" \
  "s_orn2_b64 vcc, s[52:53], s[42:43]\n\t" \
  "v_cndmask_b32 v181, v15, v240, vcc\n\t" \
  "v_mfma_f32_16x16x16_bf16 v[0:3], v[196:197], v[182:183], 0\n\t" \
  "v_cndmask_b32_dpp v185, v15, v240, vcc row_ror:8 row_mask:0xf bank_mask:0xf\n\t" \
  "s_mov_b64 vcc, 0\n\t" \
  "v_fma_f32 v147, |v147|, v221, v147 mul:4\n\t" \
  "v_mfma_f32_16x16x16_bf16 v[4:7], v[196:197], v[186:187], 0\n\t" \
  "v_fma_f32 v146, |v146|, v220, v146 mul:4\n\t" \
  "v_fma_f32 v145, |v145|, v223, v145 mul:4\n\t" \
  "v_mfma_f32_16x16x16_bf16 v[188:191], v[24:25], v[180:181], v[0:3]\n\t" \
  "v_fma_f32 v144, |v144|, v222, v144 mul:4\n\t" \
  "v_cvt_pk_bf16_f32 v147, v146, v147\n\t" \
  "v_mfma_f32_16x16x16_bf16 v[176:179], v[26:27], v[180:181], v[0:3]\n\t" \
  "v_cvt_pk_bf16_f32 v146, v144, v145\n\t" \
  "v_cvt_pknorm_i16_f32 v228, |v148|, |v149|\n\t" \
  "v_mfma_f32_16x16x16_bf16 v[200:203], v[36:37], v[184:185], v[4:7]\n\t" \
  "s_nop 1\n\t" \
  "v_cvt_pk_bf16_f32 v104, v188, v189\n\t" \
  "v_cvt_pk_bf16_f32 v105, v190, v191\n\t" \
  "v_mfma_f32_16x16x16_bf16 v[204:207], v[38:39], v[184:185], v[4:7]\n\t" \
  "buffer_store_dwordx4 v[64:67], v250, s[20:23], s14 offen offset:128 sc1\n\t" \
  "v_cvt_pk_bf16_f32 v106, v176, v177\n\t" \
  "v_cvt_pk_bf16_f32 v107, v178, v179\n\t" \
  "v_mfma_f32_16x16x16_bf16 v[196:199], v[28:29], v[180:181], v[0:3]\n\t" \
  "v_cvt_pk_bf16_f32 v108, v200, v201\n\t" \
  "v_cvt_pk_bf16_f32 v109, v202, v203\n\t" \
  "v_mfma_f32_16x16x16_bf16 v[188:191], v[30:31], v[180:181], v[0:3]\n\t" \
  "s_nop 0\n\t" \
  "v_cvt_pk_bf16_f32 v110, v204, v205\n\t" \
  "v_cvt_pk_bf16_f32 v111, v206, v207\n\t" \
  "v_mfma_f32_16x16x32_bf16 v[160:163], v[208:211], v[104:107], 0\n\t" \
  "s_nop 0\n\t" \
  "v_cvt_pk_bf16_f32 v120, v196, v197\n\t" \
  "v_cvt_pk_bf16_f32 v121, v198, v199\n\t" \
  "v_mfma_f32_16x16x16_bf16 v[176:179], v[40:41], v[184:185], v[4:7]\n\t" \
  "v_cvt_pk_bf16_f32 v122, v188, v189\n\t" \
  "v_cvt_pk_bf16_f32 v123, v190, v191\n\t" \
  "v_mfma_f32_16x16x16_bf16 v[200:203], v[42:43], v[184:185], v[4:7]\n\t" \
  "v_cvt_pknorm_i16_f32 v230, |v150|, |v151|\n\t" \
  "v_lshlrev_b32_sdwa v229, 2, v228 dst_sel:DWORD dst_unused:UNUSED_PAD src0_sel:DWORD src1_sel:BYTE_3\n\t" \
  "v_mfma_f32_16x16x32_bf16 v[168:171], v[208:211], v[108:111], 0\n\t" \
  "ds_read_b64_tr_b16 v[208:209], v248 offset:43560\n\t" \
  "s_nop 0\n\t" \
  "v_cvt_pk_bf16_f32 v124, v176, v177\n\t" \
  "v_cvt_pk_bf16_f32 v125, v178, v179\n\t" \
  "v_mfma_f32_16x16x16_bf16 v[204:207], v[32:33], v[180:181], v[0:3]\n\t" \
  "ds_read_b64_tr_b16 v[210:211], v248 offset:44584\n\t" \
  "v_cvt_pk_bf16_f32 v126, v200, v201\n\t" \
  "v_cvt_pk_bf16_f32 v127, v202, v203\n\t" \
  "v_mfma_f32_16x16x16_bf16 v[196:199], v[34:35], v[180:181], v[0:3]\n\t" \
  "v_lshlrev_b32_sdwa v228, 2, v228 dst_sel:DWORD dst_unused:UNUSED_PAD src0_sel:DWORD src1_sel:BYTE_1\n\t" \
  "v_lshlrev_b32_sdwa v231, 2, v230 dst_sel:DWORD dst_unused:UNUSED_PAD src0_sel:DWORD src1_sel:BYTE_3\n\t" \
  "v_mfma_f32_16x16x32_bf16 v[160:163], v[212:215], v[120:123], v[160:163]\n\t" \
  "ds_read_b32 v229, v229 offset:10016\n\t" \
  "v_cvt_pk_bf16_f32 v136, v204, v205\n\t" \
  "v_cvt_pk_bf16_f32 v137, v206, v207\n\t" \
  "v_mfma_f32_16x16x16_bf16 v[188:191], v[44:45], v[184:185], v[4:7]\n\t" \
  "ds_read_b32 v228, v228 offset:10016\n\t" \
  "v_cvt_pk_bf16_f32 v138, v196, v197\n\t" \
  "v_cvt_pk_bf16_f32 v139, v198, v199\n\t" \
  "v_mfma_f32_16x16x16_bf16 v[176:179], v[46:47], v[184:185], v[4:7]\n\t" \
  "v_lshlrev_b32_sdwa v230, 2, v230 dst_sel:DWORD dst_unused:UNUSED_PAD src0_sel:DWORD src1_sel:BYTE_1\n\t" \
  "v_lshlrev_b32_sdwa v233, 1, v157 dst_sel:DWORD dst_unused:UNUSED_PAD src0_sel:DWORD src1_sel:WORD_1\n\t" \
  "v_mfma_f32_16x16x32_bf16 v[168:171], v[212:215], v[124:127], v[168:171]\n\t" \
  "ds_read_b64_tr_b16 v[212:213], v248 offset:46632\n\t" \
  "v_cvt_pk_bf16_f32 v140, v188, v189\n\t" \
  "v_cvt_pk_bf16_f32 v141, v190, v191\n\t" \
  "v_mfma_f32_16x16x32_bf16 v[160:163], v[216:219], v[136:139], v[160:163]\n\t" \
  "ds_read_b64_tr_b16 v[214:215], v248 offset:47656\n\t" \
  "v_cvt_pk_bf16_f32 v142, v176, v177\n\t" \
  "v_cvt_pk_bf16_f32 v143, v178, v179\n\t" \
  "s_nop 1\n\t" \
  "v_mfma_f32_16x16x32_bf16 v[168:171], v[216:219], v[140:143], v[168:171]\n\t" \
  "ds_read_b64_tr_b16 v[216:217], v248 offset:45608\n\t" \
  "v_lshlrev_b32_sdwa v232, 1, v156 dst_sel:DWORD dst_unused:UNUSED_PAD src0_sel:DWORD src1_sel:WORD_1\n\t" \
  "v_lshlrev_b32_sdwa v235, 1, v159 dst_sel:DWORD dst_unused:UNUSED_PAD src0_sel:DWORD src1_sel:WORD_1\n\t" \
  "s_waitcnt lgkmcnt(5)\n\t" \
  "ds_read_b64_tr_b16 v[218:219], v248 offset:48680\n\t" \
  "s_waitcnt vmcnt(4)\n\t" \
  "s_mov_b64 exec, 3\n\t" \
  "ds_add_rtn_u32 v246, v251, v250 offset:47136\n\t" \
  "s_mov_b64 exec, -1\n\t" \
  "v_mfma_f32_16x16x32_bf16 v[164:167], v[208:211], v[104:107], 0\n\t" \
  "ds_read_b32 v231, v231 offset:10016\n\t" \
  "v_lshlrev_b32_sdwa v234, 1, v158 dst_sel:DWORD dst_unused:UNUSED_PAD src0_sel:DWORD src1_sel:WORD_1\n\t" \
  "v_mfma_f32_16x16x32_bf16 v[172:175], v[208:211], v[108:111], 0\n\t" \
  "ds_read_b64_tr_b16 v[208:209], v248 offset:49192\n\t" \
  "ds_read_b64_tr_b16 v[210:211], v248 offset:50216\n\t" \
  "s_waitcnt lgkmcnt(6)\n\t" \
  "v_mfma_f32_16x16x32_bf16 v[164:167], v[212:215], v[120:123], v[164:167]\n\t" \
  "ds_read_b32 v230, v230 offset:10016\n\t" \
  "v_mfma_f32_16x16x32_bf16 v[172:175], v[212:215], v[124:127], v[172:175]\n\t" \
  "ds_read_b64_tr_b16 v[212:213], v248 offset:52264\n\t" \
  "ds_read_b64_tr_b16 v[214:215], v248 offset:53288\n\t" \
  "s_waitcnt lgkmcnt(7)\n\t" \
  "v_mfma_f32_16x16x32_bf16 v[164:167], v[216:219], v[136:139], v[164:167]\n\t" \
  "ds_read_u16_d16_hi v233, v233 offset:10528\n\t" \
  "v_mfma_f32_16x16x32_bf16 v[172:175], v[216:219], v[140:143], v[172:175]\n\t" \
  "ds_read_b64_tr_b16 v[216:217], v248 offset:51240\n\t" \
  "s_setprio 3\n\t" \
  "v_cvt_pknorm_i16_f32 v220, |v162|, |v163|\n\t" \
  "v_cvt_pknorm_i16_f32 v222, |v160|, |v161|\n\t" \
  "s_waitcnt lgkmcnt(5)\n\t" \
  "v_mfma_f32_16x16x32_bf16 v[176:179], v[208:211], v[96:99], 0\n\t" \
  "buffer_store_dwordx4 v[72:75], v250, s[20:23], s15 offen offset:0 sc1\n\t" \
  "ds_read_b64_tr_b16 v[218:219], v248 offset:54312\n\t" \
  "v_lshlrev_b32_sdwa v221, 2, v220 dst_sel:DWORD dst_unused:UNUSED_PAD src0_sel:DWORD src1_sel:BYTE_3\n\t" \
  "v_lshlrev_b32_sdwa v220, 2, v220 dst_sel:DWORD dst_unused:UNUSED_PAD src0_sel:DWORD src1_sel:BYTE_1\n\t" \
  "v_mfma_f32_16x16x32_bf16 v[184:187], v[208:211], v[100:103], 0\n\t" \
  "ds_read_u16 v232, v232 offset:10528\n\t" \
  "v_lshlrev_b32_sdwa v223, 2, v222 dst_sel:DWORD dst_unused:UNUSED_PAD src0_sel:DWORD src1_sel:BYTE_3\n\t" \
  "v_lshlrev_b32_sdwa v222, 2, v222 dst_sel:DWORD dst_unused:UNUSED_PAD src0_sel:DWORD src1_sel:BYTE_1\n\t" \
  "v_lshlrev_b32_sdwa v225, 1, v171 dst_sel:DWORD dst_unused:UNUSED_PAD src0_sel:DWORD src1_sel:WORD_1\n\t" \
  "v_mfma_f32_16x16x32_bf16 v[192:195], v[208:211], v[104:107], 0\n\t" \
  "ds_read_u16_d16_hi v235, v235 offset:10528\n\t" \
  "ds_read_u16 v234, v234 offset:10528\n\t" \
  "v_lshlrev_b32_sdwa v224, 1, v170 dst_sel:DWORD dst_unused:UNUSED_PAD src0_sel:DWORD src1_sel:WORD_1\n\t" \
  "v_lshlrev_b32_sdwa v227, 1, v169 dst_sel:DWORD dst_unused:UNUSED_PAD src0_sel:DWORD src1_sel:WORD_1\n\t" \
  "v_lshlrev_b32_sdwa v226, 1, v168 dst_sel:DWORD dst_unused:UNUSED_PAD src0_sel:DWORD src1_sel:WORD_1\n\t" \
  "v_mfma_f32_16x16x32_bf16 v[200:203], v[208:211], v[108:111], 0\n\t" \
  "ds_read_b64_tr_b16 v[208:209], v248 offset:49704\n\t" \
  "ds_read_b64_tr_b16 v[210:211], v248 offset:50728\n\t" \
  "s_waitcnt lgkmcnt(10)\n\t" \
  "v_fma_mix_f32 v229, |v149|, v229, v229 op_sel:[0,1,0] op_sel_hi:[0,1,1] clamp\n\t" \
  "v_fma_mix_f32 v228, |v148|, v228, v228 op_sel:[0,1,0] op_sel_hi:[0,1,1] clamp\n\t" \
  "s_waitcnt lgkmcnt(8)\n\t" \
  "v_mfma_f32_16x16x32_bf16 v[176:179], v[212:215], v[112:115], v[176:179]\n\t" \
  "buffer_store_dwordx4 v[80:83], v250, s[20:23], s15 offen offset:64 sc1\n\t" \
  "ds_read_b32 v221, v221 offset:10016\n\t" \
  "ds_read_b32 v220, v220 offset:10016\n\t" \
  "v_fma_mix_f32 v231, |v151|, v231, v231 op_sel:[0,1,0] op_sel_hi:[0,1,1] clamp\n\t" \
  "v_fma_mix_f32 v230, |v150|, v230, v230 op_sel:[0,1,0] op_sel_hi:[0,1,1] clamp\n\t" \
  "s_waitcnt lgkmcnt(4)\n\t" \
  "v_or_b32 v156, v232, v233\n\t" \
  "v_mfma_f32_16x16x32_bf16 v[184:187], v[212:215], v[116:119], v[184:187]\n\t" \
  "ds_read_b32 v223, v223 offset:10016\n\t" \
  "ds_read_b32 v222, v222 offset:10016\n\t" \
  "s_setprio 2\n\t" \
  "v_or_b32 v157, v234, v235\n\t" \
  "v_fma_f32 v149, |v149|, v229, v149 mul:4\n\t" \
  "v_fma_f32 v148, |v148|, v228, v148 mul:4\n\t" \
  "v_mfma_f32_16x16x32_bf16 v[192:195], v[212:215], v[120:123], v[192:195]\n\t" \
  "ds_read_u16_d16_hi v225, v225 offset:10528\n\t" \
  "v_fma_f32 v151, |v151|, v231, v151 mul:4\n\t" \
  "v_fma_f32 v150, |v150|, v230, v150 mul:4\n\t" \
  "v_mfma_f32_16x16x32_bf16 v[200:203], v[212:215], v[124:127], v[200:203]\n\t" \
  "ds_read_b64_tr_b16 v[212:213], v248 offset:52776\n\t" \
  "v_cvt_pk_bf16_f32 v148, v148, v149\n\t" \
  "v_cvt_pk_bf16_f32 v149, v150, v151\n\t" \
  "v_cvt_pknorm_i16_f32 v228, |v164|, |v165|\n\t" \
  "v_mfma_f32_16x16x32_bf16 v[176:179], v[216:219], v[128:131], v[176:179]\n\t" \
  "ds_read_b64_tr_b16 v[214:215], v248 offset:53800\n\t" \
  "v_cvt_pknorm_i16_f32 v230, |v166|, |v167|\n\t" \
  "v_lshlrev_b32_sdwa v229, 2, v228 dst_sel:DWORD dst_unused:UNUSED_PAD src0_sel:DWORD src1_sel:BYTE_3\n\t" \
  "v_lshlrev_b32_sdwa v228, 2, v228 dst_sel:DWORD dst_unused:UNUSED_PAD src0_sel:DWORD src1_sel:BYTE_1\n\t" \
  "v_mfma_f32_16x16x32_bf16 v[184:187], v[216:219], v[132:135], v[184:187]\n\t" \
  "buffer_store_dwordx4 v[88:91], v250, s[20:23], s15 offen offset:128 sc1\n\t" \
  "ds_read_u16 v224, v224 offset:10528\n\t" \
  "v_lshlrev_b32_sdwa v231, 2, v230 dst_sel:DWORD dst_unused:UNUSED_PAD src0_sel:DWORD src1_sel:BYTE_3\n\t" \
  "v_lshlrev_b32_sdwa v230, 2, v230 dst_sel:DWORD dst_unused:UNUSED_PAD src0_sel:DWORD src1_sel:BYTE_1\n\t" \
  "v_mfma_f32_16x16x32_bf16 v[192:195], v[216:219], v[136:139], v[192:195]\n\t" \
  "ds_read_u16_d16_hi v227, v227 offset:10528\n\t" \
  "v_lshlrev_b32_sdwa v233, 1, v173 dst_sel:DWORD dst_unused:UNUSED_PAD src0_sel:DWORD src1_sel:WORD_1\n\t" \
  "v_lshlrev_b32_sdwa v232, 1, v172 dst_sel:DWORD dst_unused:UNUSED_PAD src0_sel:DWORD src1_sel:WORD_1\n\t" \
  "v_lshlrev_b32_sdwa v235, 1, v175 dst_sel:DWORD dst_unused:UNUSED_PAD src0_sel:DWORD src1_sel:WORD_1\n\t" \
  "v_mfma_f32_16x16x32_bf16 v[200:203], v[216:219], v[140:143], v[200:203]\n\t" \
  "ds_read_b64_tr_b16 v[216:217], v248 offset:51752\n\t" \
  "ds_read_b64_tr_b16 v[218:219], v248 offset:54824\n\t" \
  "v_lshlrev_b32_sdwa v234, 1, v174 dst_sel:DWORD dst_unused:UNUSED_PAD src0_sel:DWORD src1_sel:WORD_1\n\t" \
  "ds_read_u16 v226, v226 offset:10528\n\t" \
  "ds_read_b32 v229, v229 offset:10016\n\t" \
  "ds_read_b32 v228, v228 offset:10016\n\t" \
  "ds_read_b32 v231, v231 offset:10016\n\t" \
  "ds_read_b32 v230, v230 offset:10016\n\t" \
  "ds_read_u16_d16_hi v233, v233 offset:10528\n\t" \
  "s_setprio 1\n\t" \
  "ds_read_u16 v232, v232 offset:10528\n\t" \
  "ds_read_u16_d16_hi v235, v235 offset:10528\n\t" \
  "s_waitcnt lgkmcnt(15)\n\t" \
  "v_fma_mix_f32 v221, |v163|, v221, v221 op_sel:[0,1,0] op_sel_hi:[0,1,1] clamp\n\t" \
  "v_fma_mix_f32 v220, |v162|, v220, v220 op_sel:[0,1,0] op_sel_hi:[0,1,1] clamp\n\t" \
  "v_mfma_f32_16x16x32_bf16 v[180:183], v[208:211], v[96:99], 0\n\t" \
  "ds_read_u16 v234, v234 offset:10528\n\t" \
  "v_fma_mix_f32 v223, |v161|, v223, v223 op_sel:[0,1,0] op_sel_hi:[0,1,1] clamp\n\t" \
  "v_fma_mix_f32 v222, |v160|, v222, v222 op_sel:[0,1,0] op_sel_hi:[0,1,1] clamp\n\t" \
  "v_mfma_f32_16x16x32_bf16 v[188:191], v[208:211], v[100:103], 0\n\t" \
  "s_waitcnt vmcnt(6)\n\t" \
  "s_mov_b64 exec, 3\n\t" \
  "ds_add_rtn_u32 v246, v251, v250 offset:47144\n\t" \
  "s_mov_b64 exec, -1\n\t" \
  "s_waitcnt lgkmcnt(9)\n\t" \
  "v_or_b32 v171, v224, v225\n\t" \
  "v_or_b32 v170, v226, v227\n\t" \
  "v_fma_f32 v163, |v163|, v221, v163 mul:4\n\t" \
  "v_mfma_f32_16x16x32_bf16 v[196:199], v[208:211], v[104:107], 0\n\t" \
  "v_fma_f32 v162, |v162|, v220, v162 mul:4\n\t" \
  "v_fma_f32 v161, |v161|, v223, v161 mul:4\n\t" \
  "v_fma_f32 v160, |v160|, v222, v160 mul:4\n\t" \
  "v_mfma_f32_16x16x32_bf16 v[204:207], v[208:211], v[108:111], 0\n\t" \
  "s_waitcnt lgkmcnt(0)\n\t" \
  "v_readfirstlane_b32 s4, v246\n\t" \
  "s_cmp_lt_u32 s4, s57\n\t" \
  "s_cbranch_scc1 .Lcnx_w12\n.Lcnx_b12:\n\t" \
  "ds_read_b64_tr_b16 v[208:209], v249 offset:24600\n\t" \
  "v_cvt_pk_bf16_f32 v163, v162, v163\n\t" \
  "v_cvt_pk_bf16_f32 v162, v160, v161\n\t" \
  "v_mfma_f32_16x16x32_bf16 v[180:183], v[212:215], v[112:115], v[180:183]\n\t" \
  "ds_read_b64_tr_b16 v[210:211], v249 offset:26136\n\t" \
  "v_cvt_pknorm_i16_f32 v220, |v178|, |v179|\n\t" \
  "v_cvt_pknorm_i16_f32 v222, |v176|, |v177|\n\t" \
  "s_setprio 0\n\t" \
  "v_lshlrev_b32_sdwa v221, 2, v220 dst_sel:DWORD dst_unused:UNUSED_PAD src0_sel:DWORD src1_sel:BYTE_3\n\t" \
  "v_mfma_f32_16x16x32_bf16 v[188:191], v[212:215], v[116:119], v[188:191]\n\t" \
  "s_cmp_eq_u32 s37, 0\n\t" \
  "s_cbranch_scc1 .Lcnx_f2\n.Lcnx_g2:\n\t" \
  "v_lshlrev_b32_sdwa v220, 2, v220 dst_sel:DWORD dst_unused:UNUSED_PAD src0_sel:DWORD src1_sel:BYTE_1\n\t" \
  "v_lshlrev_b32_sdwa v223, 2, v222 dst_sel:DWORD dst_unused:UNUSED_PAD src0_sel:DWORD src1_sel:BYTE_3\n\t" \
  "v_lshlrev_b32_sdwa v222, 2, v222 dst_sel:DWORD dst_unused:UNUSED_PAD src0_sel:DWORD src1_sel:BYTE_1\n\t" \
  "v_mfma_f32_16x16x32_bf16 v[196:199], v[212:215], v[120:123], v[196:199]\n\t" \
  "ds_read_b32 v221, v221 offset:10016\n\t" \
  "v_lshlrev_b32_sdwa v225, 1, v187 dst_sel:DWORD dst_unused:UNUSED_PAD src0_sel:DWORD src1_sel:WORD_1\n\t" \
  "v_lshlrev_b32_sdwa v224, 1, v186 dst_sel:DWORD dst_unused:UNUSED_PAD src0_sel:DWORD src1_sel:WORD_1\n\t" \
  "v_mfma_f32_16x16x32_bf16 v[204:207], v[212:215], v[124:127], v[204:207]\n\t" \
  "ds_read_b64_tr_b16 v[212:213], v249 offset:24608\n\t" \
  "ds_read_b64_tr_b16 v[214:215], v249 offset:26144\n\t" \
  "v_lshlrev_b32_sdwa v227, 1, v185 dst_sel:DWORD dst_unused:UNUSED_PAD src0_sel:DWORD src1_sel:WORD_1\n\t" \
  "v_lshlrev_b32_sdwa v226, 1, v184 dst_sel:DWORD dst_unused:UNUSED_PAD src0_sel:DWORD src1_sel:WORD_1\n\t" \
  "v_fma_mix_f32 v229, |v165|, v229, v229 op_sel:[0,1,0] op_sel_hi:[0,1,1] clamp\n\t" \
  "v_mfma_f32_16x16x32_bf16 v[180:183], v[216:219], v[128:131], v[180:183]\n\t" \
  "ds_read_b32 v220, v220 offset:10016\n\t" \
  "ds_read_b32 v223, v223 offset:10016\n\t" \
  "v_fma_mix_f32 v228, |v164|, v228, v228 op_sel:[0,1,0] op_sel_hi:[0,1,1] clamp\n\t" \
  "v_fma_mix_f32 v231, |v167|, v231, v231 op_sel:[0,1,0] op_sel_hi:[0,1,1] clamp\n\t" \
  "v_fma_mix_f32 v230, |v166|, v230, v230 op_sel:[0,1,0] op_sel_hi:[0,1,1] clamp\n\t" \
  "v_mfma_f32_16x16x32_bf16 v[188:191], v[216:219], v[132:135], v[188:191]\n\t" \
  "ds_read_b32 v222, v222 offset:10016\n\t" \
  "v_or_b32 v172, v232, v233\n\t" \
  "v_or_b32 v173, v234, v235\n\t" \
  "v_mfma_f32_16x16x32_bf16 v[196:199], v[216:219], v[136:139], v[196:199]\n\t" \
  "ds_read_u16_d16_hi v225, v225 offset:10528\n\t" \
  "v_fma_f32 v165, |v165|, v229, v165 mul:4\n\t" \
  "v_fma_f32 v164, |v164|, v228, v164 mul:4\n\t" \
  "v_fma_f32 v167, |v167|, v231, v167 mul:4\n\t" \
  "v_mfma_f32_16x16x32_bf16 v[204:207], v[216:219], v[140:143], v[204:207]\n\t" \
  "ds_read_b64_tr_b16 v[216:217], v249 offset:24664\n\t" \
  "v_fma_f32 v166, |v166|, v230, v166 mul:4\n\t" \
  "v_cvt_pk_bf16_f32 v164, v164, v165\n\t" \
  "v_cvt_pk_bf16_f32 v165, v166, v167\n\t" \
  "s_setprio 0\n\t" \
  "v_cvt_pknorm_i16_f32 v228, |v194|, |v195|\n\t" \
  "v_cvt_pknorm_i16_f32 v230, |v192|, |v193|\n\t" \
  "v_mfma_f32_16x16x32_bf16 v[0:3], v[242:245], v[236:239], 0\n\t" \
  "ds_read_b64_tr_b16 v[218:219], v249 offset:26200\n\t" \
  "v_lshlrev_b32_sdwa v229, 2, v228 dst_sel:DWORD dst_unused:UNUSED_PAD src0_sel:DWORD src1_sel:BYTE_3\n\t" \
  "v_mfma_f32_16x16x32_bf16 v[4:7], v[240:243], v[236:239], 0\n\t" \
  "buffer_load_dwordx4 v[236:239], v250, s[8:11], s28 offen offset:64 nt\n\t" \
  "s_cmp_lg_u32 s58, 0\n\t" \
  "s_cbranch_scc1 .Lcnx_c21\n\t" \
  "s_sub_u32 s24, s24, s64\n\t" \
  "s_add_u32 m0, s25, 0\n\t" \
  "s_add_u32 s24, s24, s26\n\t" \
  "buffer_load_dwordx4 v254, s[8:11], s24 offen nt lds\n\t" \
  ".Lcnx_c21:\n\t" \
  "ds_read_u16 v224, v224 offset:10528\n\t" \
  "v_lshlrev_b32_sdwa v228, 2, v228 dst_sel:DWORD dst_unused:UNUSED_PAD src0_sel:DWORD src1_sel:BYTE_1\n\t" \
  "v_lshlrev_b32_sdwa v231, 2, v230 dst_sel:DWORD dst_unused:UNUSED_PAD src0_sel:DWORD src1_sel:BYTE_3\n\t" \
  "s_waitcnt lgkmcnt(10)\n\t" \
  "v_mfma_f32_16x16x32_bf16 v[0:3], v[208:211], v[146:149], v[0:3]\n\t" \
  "ds_read_u16_d16_hi v227, v227 offset:10528\n\t" \
  "v_lshlrev_b32_sdwa v230, 2, v230 dst_sel:DWORD dst_unused:UNUSED_PAD src0_sel:DWORD src1_sel:BYTE_1\n\t" \
  "v_lshlrev_b32_sdwa v233, 1, v203 dst_sel:DWORD dst_unused:UNUSED_PAD src0_sel:DWORD src1_sel:WORD_1\n\t" \
  "v_mfma_f32_16x16x32_bf16 v[24:27], v[208:211], v[154:157], 0\n\t" \
  "ds_read_u16 v226, v226 offset:10528\n\t" \
  "v_lshlrev_b32_sdwa v232, 1, v202 dst_sel:DWORD dst_unused:UNUSED_PAD src0_sel:DWORD src1_sel:WORD_1\n\t" \
  "v_mfma_f32_16x16x32_bf16 v[48:51], v[208:211], v[162:165], 0\n\t" \
  "ds_read_b32 v229, v229 offset:10016\n\t" \
  "v_lshlrev_b32_sdwa v235, 1, v201 dst_sel:DWORD dst_unused:UNUSED_PAD src0_sel:DWORD src1_sel:WORD_1\n\t" \
  "v_lshlrev_b32_sdwa v234, 1, v200 dst_sel:DWORD dst_unused:UNUSED_PAD src0_sel:DWORD src1_sel:WORD_1\n\t" \
  "v_mfma_f32_16x16x32_bf16 v[72:75], v[208:211], v[170:173], 0\n\t" \
  "ds_read_b64_tr_b16 v[208:209], v249 offset:24672\n\t" \
  "ds_read_b64_tr_b16 v[210:211], v249 offset:26208\n\t" \
  "s_waitcnt lgkmcnt(9)\n\t" \
  "v_fma_mix_f32 v221, |v179|, v221, v221 op_sel:[0,1,0] op_sel_hi:[0,1,1] clamp\n\t" \
  "v_fma_mix_f32 v220, |v178|, v220, v220 op_sel:[0,1,0] op_sel_hi:[0,1,1] clamp\n\t" \
  "v_mfma_f32_16x16x32_bf16 v[4:7], v[212:215], v[146:149], v[4:7]\n\t" \
  "ds_read_b32 v228, v228 offset:10016\n\t" \
  "ds_read_b32 v231, v231 offset:10016\n\t" \
  "v_fma_mix_f32 v223, |v177|, v223, v223 op_sel:[0,1,0] op_sel_hi:[0,1,1] clamp\n\t" \
  "v_fma_mix_f32 v222, |v176|, v222, v222 op_sel:[0,1,0] op_sel_hi:[0,1,1] clamp\n\t" \
  "v_mfma_f32_16x16x32_bf16 v[28:31], v[212:215], v[154:157], 0\n\t" \
  "ds_read_b32 v230, v230 offset:10016\n\t" \
  "s_waitcnt lgkmcnt(6)\n\t" \
  "v_or_b32 v187, v224, v225\n\t" \
  "v_mfma_f32_16x16x32_bf16 v[52:55], v[212:215], v[162:165], 0\n\t" \
  "ds_read_u16_d16_hi v233, v233 offset:10528\n\t" \
  "v_or_b32 v186, v226, v227\n\t" \
  "v_fma_f32 v179, |v179|, v221, v179 mul:4\n\t" \
  "v_mfma_f32_16x16x32_bf16 v[76:79], v[212:215], v[170:173], 0\n\t" \
  "ds_read_b64_tr_b16 v[212:213], v249 offset:24728\n\t" \
  "v_fma_f32 v178, |v178|, v220, v178 mul:4\n\t" \
  "v_fma_f32 v177, |v177|, v223, v177 mul:4\n\t" \
  "v_mfma_f32_16x16x32_bf16 v[8:11], v[216:219], v[146:149], 0\n\t" \
  "ds_read_b64_tr_b16 v[214:215], v249 offset:26264\n\t" \
  "v_fma_f32 v176, |v176|, v222, v176 mul:4\n\t" \
  "v_mfma_f32_16x16x32_bf16 v[32:35], v[216:219], v[154:157], 0\n\t" \
  "ds_read_u16 v232, v232 offset:10528\n\t" \
  "v_cvt_pk_bf16_f32 v179, v178, v179\n\t" \
  "v_cvt_pk_bf16_f32 v178, v176, v177\n\t" \
  "v_mfma_f32_16x16x32_bf16 v[56:59], v[216:219], v[162:165], 0\n\t" \
  "ds_read_u16_d16_hi v235, v235 offset:10528\n\t" \
  "v_cvt_pknorm_i16_f32 v220, |v180|, |v181|\n\t" \
  "s_setprio 1\n\t" \
  "v_cvt_pknorm_i16_f32 v222, |v182|, |v183|\n\t" \
  "v_mfma_f32_16x16x32_bf16 v[80:83], v[216:219], v[170:173], 0\n\t" \
  "ds_read_b64_tr_b16 v[216:217], v249 offset:24736\n\t" \
  "v_lshlrev_b32_sdwa v221, 2, v220 dst_sel:DWORD dst_unused:UNUSED_PAD src0_sel:DWORD src1_sel:BYTE_3\n\t" \
  "v_lshlrev_b32_sdwa v220, 2, v220 dst_sel:DWORD dst_unused:UNUSED_PAD src0_sel:DWORD src1_sel:BYTE_1\n\t" \
  "s_waitcnt lgkmcnt(9)\n\t" \
  "v_mfma_f32_16x16x32_bf16 v[12:15], v[208:211], v[146:149], 0\n\t" \
  "ds_read_b64_tr_b16 v[218:219], v249 offset:26272\n\t" \
  "v_lshlrev_b32_sdwa v223, 2, v222 dst_sel:DWORD dst_unused:UNUSED_PAD src0_sel:DWORD src1_sel:BYTE_3\n\t" \
  "v_mfma_f32_16x16x32_bf16 v[36:39], v[208:211], v[154:157], 0\n\t" \
  "ds_read_u16 v234, v234 offset:10528\n\t" \
  "v_lshlrev_b32_sdwa v222, 2, v222 dst_sel:DWORD dst_unused:UNUSED_PAD src0_sel:DWORD src1_sel:BYTE_1\n\t" \
  "v_lshlrev_b32_sdwa v225, 1, v189 dst_sel:DWORD dst_unused:UNUSED_PAD src0_sel:DWORD src1_sel:WORD_1\n\t" \
  "v_mfma_f32_16x16x32_bf16 v[60:63], v[208:211], v[162:165], 0\n\t" \
  "ds_read_b32 v221, v221 offset:10016\n\t" \
  "v_lshlrev_b32_sdwa v224, 1, v188 dst_sel:DWORD dst_unused:UNUSED_PAD src0_sel:DWORD src1_sel:WORD_1\n\t" \
  "v_lshlrev_b32_sdwa v227, 1, v191 dst_sel:DWORD dst_unused:UNUSED_PAD src0_sel:DWORD src1_sel:WORD_1\n\t" \
  "v_mfma_f32_16x16x32_bf16 v[84:87], v[208:211], v[170:173], 0\n\t" \
  "ds_read_b64_tr_b16 v[208:209], v248 offset:36904\n\t" \
  "ds_read_b64_tr_b16 v[210:211], v248 offset:37928\n\t" \
  "v_lshlrev_b32_sdwa v226, 1, v190 dst_sel:DWORD dst_unused:UNUSED_PAD src0_sel:DWORD src1_sel:WORD_1\n\t" \
  "s_waitcnt lgkmcnt(8)\n\t" \
  "v_mfma_f32_16x16x32_bf16 v[16:19], v[212:215], v[146:149], 0\n\t" \
  "ds_read_b32 v220, v220 offset:10016\n\t" \
  "ds_read_b32 v223, v223 offset:10016\n\t" \
  "v_fma_mix_f32 v229, |v195|, v229, v229 op_sel:[0,1,0] op_sel_hi:[0,1,1] clamp\n\t" \
  "v_fma_mix_f32 v228, |v194|, v228, v228 op_sel:[0,1,0] op_sel_hi:[0,1,1] clamp\n\t" \
  "v_mfma_f32_16x16x32_bf16 v[40:43], v[212:215], v[154:157], 0\n\t" \
  "ds_read_b32 v222, v222 offset:10016\n\t" \
  "v_fma_mix_f32 v231, |v193|, v231, v231 op_sel:[0,1,0] op_sel_hi:[0,1,1] clamp\n\t" \
  "v_fma_mix_f32 v230, |v192|, v230, v230 op_sel:[0,1,0] op_sel_hi:[0,1,1] clamp\n\t" \
  "v_mfma_f32_16x16x32_bf16 v[64:67], v[212:215], v[162:165], 0\n\t" \
  "ds_read_u16_d16_hi v225, v225 offset:10528\n\t" \
  "s_waitcnt lgkmcnt(7)\n\t" \
  "v_or_b32 v203, v232, v233\n\t" \
  "v_or_b32 v202, v234, v235\n\t" \
  "v_mfma_f32_16x16x32_bf16 v[88:91], v[212:215], v[170:173], 0\n\t" \
  "ds_read_b64_tr_b16 v[212:213], v248 offset:39976\n\t" \
  "v_fma_f32 v195, |v195|, v229, v195 mul:4\n\t" \
  "v_mfma_f32_16x16x32_bf16 v[20:23], v[216:219], v[146:149], 0\n\t" \
  "ds_read_b64_tr_b16 v[214:215], v248 offset:41000\n\t" \
  "v_fma_f32 v194, |v194|, v228, v194 mul:4\n\t" \
  "v_fma_f32 v193, |v193|, v231, v193 mul:4\n\t" \
  "v_mfma_f32_16x16x32_bf16 v[44:47], v[216:219], v[154:157], 0\n\t" \
  "ds_read_u16 v224, v224 offset:10528\n\t" \
  "v_fma_f32 v192, |v192|, v230, v192 mul:4\n\t" \
  "v_cvt_pk_bf16_f32 v195, v194, v195\n\t" \
  "v_mfma_f32_16x16x32_bf16 v[68:71], v[216:219], v[162:165], 0\n\t" \
  "ds_read_u16_d16_hi v227, v227 offset:10528\n\t" \
  "v_cvt_pk_bf16_f32 v194, v192, v193\n\t" \
  "v_cvt_pknorm_i16_f32 v228, |v196|, |v197|\n\t" \
  "v_mfma_f32_16x16x32_bf16 v[92:95], v[216:219], v[170:173], 0\n\t" \
  "ds_read_b64_tr_b16 v[216:217], v248 offset:38952\n\t" \
  "v_cvt_pknorm_i16_f32 v230, |v198|, |v199|\n\t" \
  "s_setprio 2\n\t" \
  "s_waitcnt lgkmcnt(9)\n\t" \
  "v_mfma_f32_16x16x32_bf16 v[144:147], v[208:211], v[96:99], 0\n\t" \
  "ds_read_b64_tr_b16 v[218:219], v248 offset:42024\n\t" \
  "v_lshlrev_b32_sdwa v229, 2, v228 dst_sel:DWORD dst_unused:UNUSED_PAD src0_sel:DWORD src1_sel:BYTE_3\n\t" \
  "v_lshlrev_b32_sdwa v228, 2, v228 dst_sel:DWORD dst_unused:UNUSED_PAD src0_sel:DWORD src1_sel:BYTE_1\n\t" \
  "v_mfma_f32_16x16x32_bf16 v[152:155], v[208:211], v[100:103], 0\n\t" \
  "ds_read_u16 v226, v226 offset:10528\n\t" \
  "v_lshlrev_b32_sdwa v231, 2, v230 dst_sel:DWORD dst_unused:UNUSED_PAD src0_sel:DWORD src1_sel:BYTE_3\n\t" \
  "v_lshlrev_b32_sdwa v230, 2, v230 dst_sel:DWORD dst_unused:UNUSED_PAD src0_sel:DWORD src1_sel:BYTE_1\n\t" \
  "v_mfma_f32_16x16x32_bf16 v[160:163], v[208:211], v[104:107], 0\n\t" \
  "ds_read_b32 v229, v229 offset:10016\n\t" \
  "v_lshlrev_b32_sdwa v233, 1, v205 dst_sel:DWORD dst_unused:UNUSED_PAD src0_sel:DWORD src1_sel:WORD_1\n\t" \
  "v_mfma_f32_16x16x32_bf16 v[168:171], v[208:211], v[108:111], 0\n\t" \
  "ds_read_b64_tr_b16 v[208:209], v248 offset:37416\n\t" \
  "v_lshlrev_b32_sdwa v232, 1, v204 dst_sel:DWORD dst_unused:UNUSED_PAD src0_sel:DWORD src1_sel:WORD_1\n\t" \
  "v_lshlrev_b32_sdwa v235, 1, v207 dst_sel:DWORD dst_unused:UNUSED_PAD src0_sel:DWORD src1_sel:WORD_1\n\t" \
  "s_waitcnt lgkmcnt(7)\n\t" \
  "v_mfma_f32_16x16x32_bf16 v[144:147], v[212:215], v[112:115], v[144:147]\n\t" \
  "ds_read_b64_tr_b16 v[210:211], v248 offset:38440\n\t" \
  "ds_read_b32 v228, v228 offset:10016\n\t" \
  "v_lshlrev_b32_sdwa v234, 1, v206 dst_sel:DWORD dst_unused:UNUSED_PAD src0_sel:DWORD src1_sel:WORD_1\n\t" \
  "v_fma_mix_f32 v221, |v181|, v221, v221 op_sel:[0,1,0] op_sel_hi:[0,1,1] clamp\n\t" \
  "v_mfma_f32_16x16x32_bf16 v[152:155], v[212:215], v[116:119], v[152:155]\n\t" \
  "ds_read_b32 v231, v231 offset:10016\n\t" \
  "ds_read_b32 v230, v230 offset:10016\n\t" \
  "v_fma_mix_f32 v220, |v180|, v220, v220 op_sel:[0,1,0] op_sel_hi:[0,1,1] clamp\n\t" \
  "v_fma_mix_f32 v223, |v183|, v223, v223 op_sel:[0,1,0] op_sel_hi:[0,1,1] clamp\n\t" \
  "v_mfma_f32_16x16x32_bf16 v[160:163], v[212:215], v[120:123], v[160:163]\n\t" \
  "ds_read_u16_d16_hi v233, v233 offset:10528\n\t" \
  "v_fma_mix_f32 v222, |v182|, v222, v222 op_sel:[0,1,0] op_sel_hi:[0,1,1] clamp\n\t" \
  "v_mfma_f32_16x16x32_bf16 v[168:171], v[212:215], v[124:127], v[168:171]\n\t" \
  "ds_read_b64_tr_b16 v[212:213], v248 offset:40488\n\t" \
  "s_waitcnt lgkmcnt(8)\n\t" \
  "v_or_b32 v188, v224, v225\n\t" \
  "v_or_b32 v189, v226, v227\n\t" \
  "v_mfma_f32_16x16x32_bf16 v[144:147], v[216:219], v[128:131], v[144:147]\n\t" \
  "ds_read_b64_tr_b16 v[214:215], v248 offset:41512\n\t" \
  "v_fma_f32 v181, |v181|, v221, v181 mul:4\n\t" \
  "v_fma_f32 v180, |v180|, v220, v180 mul:4\n\t" \
  "v_mfma_f32_16x16x32_bf16 v[152:155], v[216:219], v[132:135], v[152:155]\n\t" \
  "ds_read_u16 v232, v232 offset:10528\n\t" \
  "v_fma_f32 v183, |v183|, v223, v183 mul:4\n\t" \
  "v_mfma_f32_16x16x32_bf16 v[160:163], v[216:219], v[136:139], v[160:163]\n\t" \
  "ds_read_u16_d16_hi v235, v235 offset:10528\n\t" \
  "v_fma_f32 v182, |v182|, v222, v182 mul:4\n\t" \
  "v_cvt_pk_bf16_f32 v180, v180, v181\n\t" \
  "v_mfma_f32_16x16x32_bf16 v[168:171], v[216:219], v[140:143], v[168:171]\n\t" \
  "ds_read_b64_tr_b16 v[216:217], v248 offset:39464\n\t" \
  "v_cvt_pk_bf16_f32 v181, v182, v183\n\t" \
  "v_cvt_pknorm_i16_f32 v220, |v146|, |v147|\n\t" \
  "s_waitcnt lgkmcnt(9)\n\t" \
  "v_mfma_f32_16x16x32_bf16 v[148:151], v[208:211], v[96:99], 0\n\t" \
  "ds_read_b64_tr_b16 v[218:219], v248 offset:42536\n\t" \
  "v_cvt_pknorm_i16_f32 v222, |v144|, |v145|\n\t" \
  "s_setprio 3\n\t" \
  "v_lshlrev_b32_sdwa v221, 2, v220 dst_sel:DWORD dst_unused:UNUSED_PAD src0_sel:DWORD src1_sel:BYTE_3\n\t" \
  "v_mfma_f32_16x16x32_bf16 v[156:159], v[208:211], v[100:103], 0\n\t" \
  "s_waitcnt vmcnt(1)\n\t" \
  "s_cmp_lg_u32 s58, 1\n\t" \
  "s_cbranch_scc1 .Lcnx_c9\n\t" \
  "s_waitcnt vmcnt(0)\n\t" \
  ".Lcnx_c9:\n\t" \
  "s_mov_b64 exec, 3\n\t" \
  "ds_add_rtn_u32 v246, v251, v250 offset:47152\n\t" \
  "s_mov_b64 exec, -1\n\t" \
  "ds_read_u16 v234, v234 offset:10528\n\t" \
  "v_lshlrev_b32_sdwa v220, 2, v220 dst_sel:DWORD dst_unused:UNUSED_PAD src0_sel:DWORD src1_sel:BYTE_1\n\t" \
  "v_mfma_f32_16x16x32_bf16 v[164:167], v[208:211], v[104:107], 0\n\t" \
  "ds_read_b32 v221, v221 offset:10016\n\t" \
  "v_lshlrev_b32_sdwa v223, 2, v222 dst_sel:DWORD dst_unused:UNUSED_PAD src0_sel:DWORD src1_sel:BYTE_3\n\t" \
  "v_lshlrev_b32_sdwa v222, 2, v222 dst_sel:DWORD dst_unused:UNUSED_PAD src0_sel:DWORD src1_sel:BYTE_1\n\t" \
  "v_mfma_f32_16x16x32_bf16 v[172:175], v[208:211], v[108:111], 0\n\t" \
  "s_waitcnt lgkmcnt(2)\n\t" \
  "v_readfirstlane_b32 s4, v246\n\t" \
  "s_cmp_lt_u32 s4, s57\n\t" \
  "s_cbranch_scc1 .Lcnx_w24\n.Lcnx_b24:\n\t" \
  "ds_read_b64_tr_b16 v[208:209], v249 offset:30752\n\t" \
  "v_lshlrev_b32_sdwa v225, 1, v155 dst_sel:DWORD dst_unused:UNUSED_PAD src0_sel:DWORD src1_sel:WORD_1\n\t" \
  "v_lshlrev_b32_sdwa v224, 1, v154 dst_sel:DWORD dst_unused:UNUSED_PAD src0_sel:DWORD src1_sel:WORD_1\n\t" \
  "v_mfma_f32_16x16x32_bf16 v[148:151], v[212:215], v[112:115], v[148:151]\n\t" \
  "ds_read_b64_tr_b16 v[210:211], v249 offset:32288\n\t" \
  "v_lshlrev_b32_sdwa v227, 1, v153 dst_sel:DWORD dst_unused:UNUSED_PAD src0_sel:DWORD src1_sel:WORD_1\n\t" \
  "v_mfma_f32_16x16x32_bf16 v[156:159], v[212:215], v[116:119], v[156:159]\n\t" \
  "s_cmp_eq_u32 s37, 0\n\t" \
  "s_cbranch_scc1 .Lcnx_f3\n.Lcnx_g3:\n\t" \
  "ds_read_b32 v220, v220 offset:10016\n\t" \
  "v_lshlrev_b32_sdwa v226, 1, v152 dst_sel:DWORD dst_unused:UNUSED_PAD src0_sel:DWORD src1_sel:WORD_1\n\t" \
  "v_fma_mix_f32 v229, |v197|, v229, v229 op_sel:[0,1,0] op_sel_hi:[0,1,1] clamp\n\t" \
  "v_mfma_f32_16x16x32_bf16 v[164:167], v[212:215], v[120:123], v[164:167]\n\t" \
  "ds_read_b32 v223, v223 offset:10016\n\t" \
  "ds_read_b32 v222, v222 offset:10016\n\t" \
  "v_fma_mix_f32 v228, |v196|, v228, v228 op_sel:[0,1,0] op_sel_hi:[0,1,1] clamp\n\t" \
  "v_fma_mix_f32 v231, |v199|, v231, v231 op_sel:[0,1,0] op_sel_hi:[0,1,1] clamp\n\t" \
  "v_mfma_f32_16x16x32_bf16 v[172:175], v[212:215], v[124:127], v[172:175]\n\t" \
  "ds_read_b64_tr_b16 v[212:213], v249 offset:30760\n\t" \
  "ds_read_b64_tr_b16 v[214:215], v249 offset:32296\n\t" \
  "v_fma_mix_f32 v230, |v198|, v230, v230 op_sel:[0,1,0] op_sel_hi:[0,1,1] clamp\n\t" \
  "s_waitcnt lgkmcnt(8)\n\t" \
  "v_or_b32 v204, v232, v233\n\t" \
  "v_mfma_f32_16x16x32_bf16 v[148:151], v[216:219], v[128:131], v[148:151]\n\t" \
  "ds_read_u16_d16_hi v225, v225 offset:10528\n\t" \
  "v_or_b32 v205, v234, v235\n\t" \
  "v_mfma_f32_16x16x32_bf16 v[156:159], v[216:219], v[132:135], v[156:159]\n\t" \
  "ds_read_u16 v224, v224 offset:10528\n\t" \
  "v_fma_f32 v197, |v197|, v229, v197 mul:4\n\t" \
  "v_fma_f32 v196, |v196|, v228, v196 mul:4\n\t" \
  "v_mfma_f32_16x16x32_bf16 v[164:167], v[216:219], v[136:139], v[164:167]\n\t" \
  "ds_read_u16_d16_hi v227, v227 offset:10528\n\t" \
  "v_fma_f32 v199, |v199|, v231, v199 mul:4\n\t" \
  "v_fma_f32 v198, |v198|, v230, v198 mul:4\n\t" \
  "v_mfma_f32_16x16x32_bf16 v[172:175], v[216:219], v[140:143], v[172:175]\n\t" \
  "ds_read_b64_tr_b16 v[216:217], v249 offset:30816\n\t" \
  "v_cvt_pk_bf16_f32 v196, v196, v197\n\t" \
  "v_cvt_pk_bf16_f32 v197, v198, v199\n\t" \
  "s_setprio 0\n\t" \
  "v_cvt_pknorm_i16_f32 v228, |v162|, |v163|\n\t" \
  "v_cvt_pknorm_i16_f32 v230, |v160|, |v161|\n\t" \
  "v_mfma_f32_16x16x32_bf16 v[8:11], v[242:245], v[236:239], v[8:11]\n\t" \
  "ds_read_b64_tr_b16 v[218:219], v249 offset:32352\n\t" \
  "v_lshlrev_b32_sdwa v229, 2, v228 dst_sel:DWORD dst_unused:UNUSED_PAD src0_sel:DWORD src1_sel:BYTE_3\n\t" \
  "v_mfma_f32_16x16x32_bf16 v[12:15], v[240:243], v[236:239], v[12:15]\n\t" \
  "buffer_load_dwordx4 v[236:239], v250, s[8:11], s28 offen offset:128 nt\n\t" \
  "s_cmp_lg_u32 s58, 0\n\t" \
  "s_cbranch_scc1 .Lcnx_c22\n\t" \
  "s_add_u32 m0, s25, 8200\n\t" \
  "s_add_u32 s24, s24, s26\n\t" \
  "buffer_load_dwordx4 v254, s[8:11], s24 offen nt lds\n\t" \
  "s_cmp_lg_u32 s38, 4\n\t" \
  "s_cbranch_scc1 .Lcnx_c22\n\t" \
  "s_mov_b32 m0, 107744\n\t" \
  "s_add_u32 s30, s30, s31\n\t" \
  "buffer_load_dword v252, s[8:11], s30 offen lds\n\t" \
  ".Lcnx_c22:\n\t" \
  "ds_read_u16 v226, v226 offset:10528\n\t" \
  "v_lshlrev_b32_sdwa v228, 2, v228 dst_sel:DWORD dst_unused:UNUSED_PAD src0_sel:DWORD src1_sel:BYTE_1\n\t" \
  "v_lshlrev_b32_sdwa v231, 2, v230 dst_sel:DWORD dst_unused:UNUSED_PAD src0_sel:DWORD src1_sel:BYTE_3\n\t" \
  "s_waitcnt lgkmcnt(11)\n\t" \
  "v_mfma_f32_16x16x32_bf16 v[0:3], v[208:211], v[178:181], v[0:3]\n\t" \
  "ds_read_b32 v229, v229 offset:10016\n\t" \
  "v_lshlrev_b32_sdwa v230, 2, v230 dst_sel:DWORD dst_unused:UNUSED_PAD src0_sel:DWORD src1_sel:BYTE_1\n\t" \
  "v_lshlrev_b32_sdwa v233, 1, v171 dst_sel:DWORD dst_unused:UNUSED_PAD src0_sel:DWORD src1_sel:WORD_1\n\t" \
  "v_mfma_f32_16x16x32_bf16 v[24:27], v[208:211], v[186:189], v[24:27]\n\t" \
  "ds_read_b32 v228, v228 offset:10016\n\t" \
  "v_lshlrev_b32_sdwa v232, 1, v170 dst_sel:DWORD dst_unused:UNUSED_PAD src0_sel:DWORD src1_sel:WORD_1\n\t" \
  "v_mfma_f32_16x16x32_bf16 v[48:51], v[208:211], v[194:197], v[48:51]\n\t" \
  "ds_read_b32 v231, v231 offset:10016\n\t" \
  "v_lshlrev_b32_sdwa v235, 1, v169 dst_sel:DWORD dst_unused:UNUSED_PAD src0_sel:DWORD src1_sel:WORD_1\n\t" \
  "v_lshlrev_b32_sdwa v234, 1, v168 dst_sel:DWORD dst_unused:UNUSED_PAD src0_sel:DWORD src1_sel:WORD_1\n\t" \
  "v_mfma_f32_16x16x32_bf16 v[72:75], v[208:211], v[202:205], v[72:75]\n\t" \
  "ds_read_b64_tr_b16 v[208:209], v249 offset:30824\n\t" \
  "ds_read_b64_tr_b16 v[210:211], v249 offset:32360\n\t" \
  "s_waitcnt lgkmcnt(13)\n\t" \
  "v_fma_mix_f32 v221, |v147|, v221, v221 op_sel:[0,1,0] op_sel_hi:[0,1,1] clamp\n\t" \
  "v_fma_mix_f32 v220, |v146|, v220, v220 op_sel:[0,1,0] op_sel_hi:[0,1,1] clamp\n\t" \
  "s_waitcnt lgkmcnt(11)\n\t" \
  "v_mfma_f32_16x16x32_bf16 v[4:7], v[212:215], v[178:181], v[4:7]\n\t" \
  "ds_read_b32 v230, v230 offset:10016\n\t" \
  "v_fma_mix_f32 v223, |v145|, v223, v223 op_sel:[0,1,0] op_sel_hi:[0,1,1] clamp\n\t" \
  "v_fma_mix_f32 v222, |v144|, v222, v222 op_sel:[0,1,0] op_sel_hi:[0,1,1] clamp\n\t" \
  "v_mfma_f32_16x16x32_bf16 v[28:31], v[212:215], v[186:189], v[28:31]\n\t" \
  "ds_read_u16_d16_hi v233, v233 offset:10528\n\t" \
  "s_waitcnt lgkmcnt(7)\n\t" \
  "v_or_b32 v155, v224, v225\n\t" \
  "v_mfma_f32_16x16x32_bf16 v[52:55], v[212:215], v[194:197], v[52:55]\n\t" \
  "ds_read_u16 v232, v232 offset:10528\n\t" \
  "v_or_b32 v154, v226, v227\n\t" \
  "v_fma_f32 v147, |v147|, v221, v147 mul:4\n\t" \
  "v_mfma_f32_16x16x32_bf16 v[76:79], v[212:215], v[202:205], v[76:79]\n\t" \
  "ds_read_b64_tr_b16 v[212:213], v249 offset:30880\n\t" \
  "v_fma_f32 v146, |v146|, v220, v146 mul:4\n\t" \
  "v_fma_f32 v145, |v145|, v223, v145 mul:4\n\t" \
  "v_mfma_f32_16x16x32_bf16 v[8:11], v[216:219], v[178:181], v[8:11]\n\t" \
  "ds_read_b64_tr_b16 v[214:215], v249 offset:32416\n\t" \
  "v_fma_f32 v144, |v144|, v222, v144 mul:4\n\t" \
  "v_mfma_f32_16x16x32_bf16 v[32:35], v[216:219], v[186:189], v[32:35]\n\t" \
  "ds_read_u16_d16_hi v235, v235 offset:10528\n\t" \
  "v_cvt_pk_bf16_f32 v147, v146, v147\n\t" \
  "v_cvt_pk_bf16_f32 v146, v144, v145\n\t" \
  "v_mfma_f32_16x16x32_bf16 v[56:59], v[216:219], v[194:197], v[56:59]\n\t" \
  "ds_read_u16 v234, v234 offset:10528\n\t" \
  "v_cvt_pknorm_i16_f32 v220, |v148|, |v149|\n\t" \
  "s_setprio 1\n\t" \
  "v_cvt_pknorm_i16_f32 v222, |v150|, |v151|\n\t" \
  "v_mfma_f32_16x16x32_bf16 v[80:83], v[216:219], v[202:205], v[80:83]\n\t" \
  "ds_read_b64_tr_b16 v[216:217], v249 offset:30888\n\t" \
  "v_lshlrev_b32_sdwa v221, 2, v220 dst_sel:DWORD dst_unused:UNUSED_PAD src0_sel:DWORD src1_sel:BYTE_3\n\t" \
  "v_lshlrev_b32_sdwa v220, 2, v220 dst_sel:DWORD dst_unused:UNUSED_PAD src0_sel:DWORD src1_sel:BYTE_1\n\t" \
  "s_waitcnt lgkmcnt(8)\n\t" \
  "v_mfma_f32_16x16x32_bf16 v[12:15], v[208:211], v[178:181], v[12:15]\n\t" \
  "ds_read_b64_tr_b16 v[218:219], v249 offset:32424\n\t" \
  "v_lshlrev_b32_sdwa v223, 2, v222 dst_sel:DWORD dst_unused:UNUSED_PAD src0_sel:DWORD src1_sel:BYTE_3\n\t" \
  "v_mfma_f32_16x16x32_bf16 v[36:39], v[208:211], v[186:189], v[36:39]\n\t" \
  "ds_read_b32 v221, v221 offset:10016\n\t" \
  "v_lshlrev_b32_sdwa v222, 2, v222 dst_sel:DWORD dst_unused:UNUSED_PAD src0_sel:DWORD src1_sel:BYTE_1\n\t" \
  "v_lshlrev_b32_sdwa v225, 1, v157 dst_sel:DWORD dst_unused:UNUSED_PAD src0_sel:DWORD src1_sel:WORD_1\n\t" \
  "v_mfma_f32_16x16x32_bf16 v[60:63], v[208:211], v[194:197], v[60:63]\n\t" \
  "ds_read_b32 v220, v220 offset:10016\n\t" \
  "v_lshlrev_b32_sdwa v224, 1, v156 dst_sel:DWORD dst_unused:UNUSED_PAD src0_sel:DWORD src1_sel:WORD_1\n\t" \
  "v_lshlrev_b32_sdwa v227, 1, v159 dst_sel:DWORD dst_unused:UNUSED_PAD src0_sel:DWORD src1_sel:WORD_1\n\t" \
  "v_mfma_f32_16x16x32_bf16 v[84:87], v[208:211], v[202:205], v[84:87]\n\t" \
  "ds_read_b64_tr_b16 v[208:209], v248 offset:0\n\t" \
  "ds_read_b64_tr_b16 v[210:211], v248 offset:1024\n\t" \
  "v_lshlrev_b32_sdwa v226, 1, v158 dst_sel:DWORD dst_unused:UNUSED_PAD src0_sel:DWORD src1_sel:WORD_1\n\t" \
  "s_waitcnt lgkmcnt(8)\n\t" \
  "v_mfma_f32_16x16x32_bf16 v[16:19], v[212:215], v[178:181], v[16:19]\n\t" \
  "ds_read_b32 v223, v223 offset:10016\n\t" \
  "v_fma_mix_f32 v229, |v163|, v229, v229 op_sel:[0,1,0] op_sel_hi:[0,1,1] clamp\n\t" \
  "v_fma_mix_f32 v228, |v162|, v228, v228 op_sel:[0,1,0] op_sel_hi:[0,1,1] clamp\n\t" \
  "v_mfma_f32_16x16x32_bf16 v[40:43], v[212:215], v[186:189], v[40:43]\n\t" \
  "ds_read_b32 v222, v222 offset:10016\n\t" \
  "v_fma_mix_f32 v231, |v161|, v231, v231 op_sel:[0,1,0] op_sel_hi:[0,1,1] clamp\n\t" \
  "v_fma_mix_f32 v230, |v160|, v230, v230 op_sel:[0,1,0] op_sel_hi:[0,1,1] clamp\n\t" \
  "v_mfma_f32_16x16x32_bf16 v[64:67], v[212:215], v[194:197], v[64:67]\n\t" \
  "ds_read_u16_d16_hi v225, v225 offset:10528\n\t" \
  "s_waitcnt lgkmcnt(9)\n\t" \
  "v_or_b32 v171, v232, v233\n\t" \
  "v_or_b32 v170, v234, v235\n\t" \
  "v_mfma_f32_16x16x32_bf16 v[88:91], v[212:215], v[202:205], v[88:91]\n\t" \
  "ds_read_b64_tr_b16 v[212:213], v248 offset:3072\n\t" \
  "v_fma_f32 v163, |v163|, v229, v163 mul:4\n\t" \
  "s_waitcnt lgkmcnt(8)\n\t" \
  "v_mfma_f32_16x16x32_bf16 v[20:23], v[216:219], v[178:181], v[20:23]\n\t" \
  "ds_read_b64_tr_b16 v[214:215], v248 offset:4096\n\t" \
  "v_fma_f32 v162, |v162|, v228, v162 mul:4\n\t" \
  "v_fma_f32 v161, |v161|, v231, v161 mul:4\n\t" \
  "v_mfma_f32_16x16x32_bf16 v[44:47], v[216:219], v[186:189], v[44:47]\n\t" \
  "ds_read_u16 v224, v224 offset:10528\n\t" \
  "v_fma_f32 v160, |v160|, v230, v160 mul:4\n\t" \
  "v_cvt_pk_bf16_f32 v163, v162, v163\n\t" \
  "v_mfma_f32_16x16x32_bf16 v[68:71], v[216:219], v[194:197], v[68:71]\n\t" \
  "ds_read_u16_d16_hi v227, v227 offset:10528\n\t" \
  "v_cvt_pk_bf16_f32 v162, v160, v161\n\t" \
  "v_cvt_pknorm_i16_f32 v228, |v164|, |v165|\n\t" \
  "v_mfma_f32_16x16x32_bf16 v[92:95], v[216:219], v[202:205], v[92:95]\n\t" \
  "ds_read_b64_tr_b16 v[216:217], v248 offset:2048\n\t" \
  "v_cvt_pknorm_i16_f32 v230, |v166|, |v167|\n\t" \
  "s_setprio 2\n\t" \
  "s_waitcnt lgkmcnt(8)\n\t" \
  "v_mfma_f32_16x16x32_bf16 v[176:179], v[208:211], v[96:99], 0\n\t" \
  "ds_read_b64_tr_b16 v[218:219], v248 offset:5120\n\t" \
  "v_lshlrev_b32_sdwa v229, 2, v228 dst_sel:DWORD dst_unused:UNUSED_PAD src0_sel:DWORD src1_sel:BYTE_3\n\t" \
  "v_lshlrev_b32_sdwa v228, 2, v228 dst_sel:DWORD dst_unused:UNUSED_PAD src0_sel:DWORD src1_sel:BYTE_1\n\t" \
  "v_mfma_f32_16x16x32_bf16 v[184:187], v[208:211], v[100:103], 0\n\t" \
  "ds_read_u16 v226, v226 offset:10528\n\t" \
  "v_lshlrev_b32_sdwa v231, 2, v230 dst_sel:DWORD dst_unused:UNUSED_PAD src0_sel:DWORD src1_sel:BYTE_3\n\t" \
  "v_lshlrev_b32_sdwa v230, 2, v230 dst_sel:DWORD dst_unused:UNUSED_PAD src0_sel:DWORD src1_sel:BYTE_1\n\t" \
  "v_mfma_f32_16x16x32_bf16 v[192:195], v[208:211], v[104:107], 0\n\t" \
  "ds_read_b32 v229, v229 offset:10016\n\t" \
  "v_lshlrev_b32_sdwa v233, 1, v173 dst_sel:DWORD dst_unused:UNUSED_PAD src0_sel:DWORD src1_sel:WORD_1\n\t" \
  "v_mfma_f32_16x16x32_bf16 v[200:203], v[208:211], v[108:111], 0\n\t" \
  "ds_read_b64_tr_b16 v[208:209], v248 offset:512\n\t" \
  "v_lshlrev_b32_sdwa v232, 1, v172 dst_sel:DWORD dst_unused:UNUSED_PAD src0_sel:DWORD src1_sel:WORD_1\n\t" \
  "v_lshlrev_b32_sdwa v235, 1, v175 dst_sel:DWORD dst_unused:UNUSED_PAD src0_sel:DWORD src1_sel:WORD_1\n\t" \
  "s_waitcnt lgkmcnt(7)\n\t" \
  "v_mfma_f32_16x16x32_bf16 v[176:179], v[212:215], v[112:115], v[176:179]\n\t" \
  "ds_read_b64_tr_b16 v[210:211], v248 offset:1536\n\t" \
  "ds_read_b32 v228, v228 offset:10016\n\t" \
  "v_lshlrev_b32_sdwa v234, 1, v174 dst_sel:DWORD dst_unused:UNUSED_PAD src0_sel:DWORD src1_sel:WORD_1\n\t" \
  "v_fma_mix_f32 v221, |v149|, v221, v221 op_sel:[0,1,0] op_sel_hi:[0,1,1] clamp\n\t" \
  "v_mfma_f32_16x16x32_bf16 v[184:187], v[212:215], v[116:119], v[184:187]\n\t" \
  "ds_read_b32 v231, v231 offset:10016\n\t" \
  "ds_read_b32 v230, v230 offset:10016\n\t" \
  "v_fma_mix_f32 v220, |v148|, v220, v220 op_sel:[0,1,0] op_sel_hi:[0,1,1] clamp\n\t" \
  "v_fma_mix_f32 v223, |v151|, v223, v223 op_sel:[0,1,0] op_sel_hi:[0,1,1] clamp\n\t" \
  "v_mfma_f32_16x16x32_bf16 v[192:195], v[212:215], v[120:123], v[192:195]\n\t" \
  "ds_read_u16_d16_hi v233, v233 offset:10528\n\t" \
  "v_fma_mix_f32 v222, |v150|, v222, v222 op_sel:[0,1,0] op_sel_hi:[0,1,1] clamp\n\t" \
  "v_mfma_f32_16x16x32_bf16 v[200:203], v[212:215], v[124:127], v[200:203]\n\t" \
  "ds_read_b64_tr_b16 v[212:213], v248 offset:3584\n\t" \
  "s_waitcnt lgkmcnt(8)\n\t" \
  "v_or_b32 v156, v224, v225\n\t" \
  "v_or_b32 v157, v226, v227\n\t" \
  "v_mfma_f32_16x16x32_bf16 v[176:179], v[216:219], v[128:131], v[176:179]\n\t" \
  "ds_read_b64_tr_b16 v[214:215], v248 offset:4608\n\t" \
  "v_fma_f32 v149, |v149|, v221, v149 mul:4\n\t" \
  "v_fma_f32 v148, |v148|, v220, v148 mul:4\n\t" \
  "v_mfma_f32_16x16x32_bf16 v[184:187], v[216:219], v[132:135], v[184:187]\n\t" \
  "ds_read_u16 v232, v232 offset:10528\n\t" \
  "v_fma_f32 v151, |v151|, v223, v151 mul:4\n\t" \
  "v_mfma_f32_16x16x32_bf16 v[192:195], v[216:219], v[136:139], v[192:195]\n\t" \
  "ds_read_u16_d16_hi v235, v235 offset:10528\n\t" \
  "v_fma_f32 v150, |v150|, v222, v150 mul:4\n\t" \
  "v_cvt_pk_bf16_f32 v148, v148, v149\n\t" \
  "v_mfma_f32_16x16x32_bf16 v[200:203], v[216:219], v[140:143], v[200:203]\n\t" \
  "ds_read_b64_tr_b16 v[216:217], v248 offset:2560\n\t" \
  "v_cvt_pk_bf16_f32 v149, v150, v151\n\t" \
  "v_cvt_pknorm_i16_f32 v220, |v178|, |v179|\n\t" \
  "s_waitcnt lgkmcnt(9)\n\t" \
  "v_mfma_f32_16x16x32_bf16 v[180:183], v[208:211], v[96:99], 0\n\t" \
  "ds_read_b64_tr_b16 v[218:219], v248 offset:5632\n\t" \
  "v_cvt_pknorm_i16_f32 v222, |v176|, |v177|\n\t" \
  "s_setprio 3\n\t" \
  "v_lshlrev_b32_sdwa v221, 2, v220 dst_sel:DWORD dst_unused:UNUSED_PAD src0_sel:DWORD src1_sel:BYTE_3\n\t" \
  "v_mfma_f32_16x16x32_bf16 v[188:191], v[208:211], v[100:103], 0\n\t" \
  "s_waitcnt vmcnt(1)\n\t" \
  "s_cmp_lg_u32 s58, 1\n\t" \
  "s_cbranch_scc1 .Lcnx_c10\n\t" \
  "s_waitcnt vmcnt(0)\n\t" \
  ".Lcnx_c10:\n\t" \
  "s_mov_b64 exec, 3\n\t" \
  "ds_add_rtn_u32 v246, v251, v250 offset:47160\n\t" \
  "s_mov_b64 exec, -1\n\t" \
  "ds_read_u16 v234, v234 offset:10528\n\t" \
  "v_lshlrev_b32_sdwa v220, 2, v220 dst_sel:DWORD dst_unused:UNUSED_PAD src0_sel:DWORD src1_sel:BYTE_1\n\t" \
  "v_mfma_f32_16x16x32_bf16 v[196:199], v[208:211], v[104:107], 0\n\t" \
  "ds_read_b32 v221, v221 offset:10016\n\t" \
  "v_lshlrev_b32_sdwa v223, 2, v222 dst_sel:DWORD dst_unused:UNUSED_PAD src0_sel:DWORD src1_sel:BYTE_3\n\t" \
  "v_lshlrev_b32_sdwa v222, 2, v222 dst_sel:DWORD dst_unused:UNUSED_PAD src0_sel:DWORD src1_sel:BYTE_1\n\t" \
  "v_mfma_f32_16x16x32_bf16 v[204:207], v[208:211], v[108:111], 0\n\t" \
  "s_waitcnt lgkmcnt(2)\n\t" \
  "v_readfirstlane_b32 s4, v246\n\t" \
  "s_cmp_lt_u32 s4, s57\n\t" \
  "s_cbranch_scc1 .Lcnx_w36\n.Lcnx_b36:\n\t" \
  "ds_read_b64_tr_b16 v[208:209], v249 offset:6152\n\t" \
  "v_lshlrev_b32_sdwa v225, 1, v187 dst_sel:DWORD dst_unused:UNUSED_PAD src0_sel:DWORD src1_sel:WORD_1\n\t" \
  "v_lshlrev_b32_sdwa v224, 1, v186 dst_sel:DWORD dst_unused:UNUSED_PAD src0_sel:DWORD src1_sel:WORD_1\n\t" \
  "v_mfma_f32_16x16x32_bf16 v[180:183], v[212:215], v[112:115], v[180:183]\n\t" \
  "ds_read_b64_tr_b16 v[210:211], v249 offset:7688\n\t" \
  "v_lshlrev_b32_sdwa v227, 1, v185 dst_sel:DWORD dst_unused:UNUSED_PAD src0_sel:DWORD src1_sel:WORD_1\n\t" \
  "v_mfma_f32_16x16x32_bf16 v[188:191], v[212:215], v[116:119], v[188:191]\n\t" \
  "s_cmp_eq_u32 s37, 0\n\t" \
  "s_cbranch_scc1 .Lcnx_f4\n.Lcnx_g4:\n\t" \
  "ds_read_b32 v220, v220 offset:10016\n\t" \
  "v_lshlrev_b32_sdwa v226, 1, v184 dst_sel:DWORD dst_unused:UNUSED_PAD src0_sel:DWORD src1_sel:WORD_1\n\t" \
  "v_fma_mix_f32 v229, |v165|, v229, v229 op_sel:[0,1,0] op_sel_hi:[0,1,1] clamp\n\t" \
  "v_mfma_f32_16x16x32_bf16 v[196:199], v[212:215], v[120:123], v[196:199]\n\t" \
  "ds_read_b32 v223, v223 offset:10016\n\t" \
  "ds_read_b32 v222, v222 offset:10016\n\t" \
  "v_fma_mix_f32 v228, |v164|, v228, v228 op_sel:[0,1,0] op_sel_hi:[0,1,1] clamp\n\t" \
  "v_fma_mix_f32 v231, |v167|, v231, v231 op_sel:[0,1,0] op_sel_hi:[0,1,1] clamp\n\t" \
  "v_mfma_f32_16x16x32_bf16 v[204:207], v[212:215], v[124:127], v[204:207]\n\t" \
  "ds_read_b64_tr_b16 v[212:213], v249 offset:6160\n\t" \
  "ds_read_b64_tr_b16 v[214:215], v249 offset:7696\n\t" \
  "v_fma_mix_f32 v230, |v166|, v230, v230 op_sel:[0,1,0] op_sel_hi:[0,1,1] clamp\n\t" \
  "s_waitcnt lgkmcnt(8)\n\t" \
  "v_or_b32 v172, v232, v233\n\t" \
  "v_mfma_f32_16x16x32_bf16 v[180:183], v[216:219], v[128:131], v[180:183]\n\t" \
  "ds_read_u16_d16_hi v225, v225 offset:10528\n\t" \
  "v_or_b32 v173, v234, v235\n\t" \
  "v_mfma_f32_16x16x32_bf16 v[188:191], v[216:219], v[132:135], v[188:191]\n\t" \
  "ds_read_u16 v224, v224 offset:10528\n\t" \
  "v_fma_f32 v165, |v165|, v229, v165 mul:4\n\t" \
  "v_fma_f32 v164, |v164|, v228, v164 mul:4\n\t" \
  "v_mfma_f32_16x16x32_bf16 v[196:199], v[216:219], v[136:139], v[196:199]\n\t" \
  "ds_read_u16_d16_hi v227, v227 offset:10528\n\t" \
  "v_fma_f32 v167, |v167|, v231, v167 mul:4\n\t" \
  "v_fma_f32 v166, |v166|, v230, v166 mul:4\n\t" \
  "v_mfma_f32_16x16x32_bf16 v[204:207], v[216:219], v[140:143], v[204:207]\n\t" \
  "ds_read_b64_tr_b16 v[216:217], v249 offset:6216\n\t" \
  "v_cvt_pk_bf16_f32 v164, v164, v165\n\t" \
  "v_cvt_pk_bf16_f32 v165, v166, v167\n\t" \
  "s_setprio 0\n\t" \
  "v_cvt_pknorm_i16_f32 v228, |v194|, |v195|\n\t" \
  "v_cvt_pknorm_i16_f32 v230, |v192|, |v193|\n\t" \
  "v_mfma_f32_16x16x32_bf16 v[16:19], v[242:245], v[236:239], v[16:19]\n\t" \
  "ds_read_b64_tr_b16 v[218:219], v249 offset:7752\n\t" \
  "v_lshlrev_b32_sdwa v229, 2, v228 dst_sel:DWORD dst_unused:UNUSED_PAD src0_sel:DWORD src1_sel:BYTE_3\n\t" \
  "v_mfma_f32_16x16x32_bf16 v[20:23], v[240:243], v[236:239], v[20:23]\n\t" \
  "buffer_load_dwordx4 v[236:239], v250, s[8:11], s29 offen offset:0 nt\n\t" \
  "s_cmp_lg_u32 s58, 0\n\t" \
  "s_cbranch_scc1 .Lcnx_c23\n\t" \
  "s_add_u32 m0, s25, 16400\n\t" \
  "s_add_u32 s24, s24, s26\n\t" \
  "buffer_load_dwordx4 v254, s[8:11], s24 offen nt lds\n\t" \
  "s_cmp_lg_u32 s38, 5\n\t" \
  "s_cbranch_scc1 .Lcnx_c23\n\t" \
  "s_mov_b32 m0, 107744\n\t" \
  "s_add_u32 s30, s30, s31\n\t" \
  "buffer_load_dword v252, s[8:11], s30 offen lds\n\t" \
  ".Lcnx_c23:\n\t" \
  "ds_read_u16 v226, v226 offset:10528\n\t" \
  "v_lshlrev_b32_sdwa v228, 2, v228 dst_sel:DWORD dst_unused:UNUSED_PAD src0_sel:DWORD src1_sel:BYTE_1\n\t" \
  "v_lshlrev_b32_sdwa v231, 2, v230 dst_sel:DWORD dst_unused:UNUSED_PAD src0_sel:DWORD src1_sel:BYTE_3\n\t" \
  "s_waitcnt lgkmcnt(11)\n\t" \
  "v_mfma_f32_16x16x32_bf16 v[0:3], v[208:211], v[146:149], v[0:3]\n\t" \
  "ds_read_b32 v229, v229 offset:10016\n\t" \
  "v_lshlrev_b32_sdwa v230, 2, v230 dst_sel:DWORD dst_unused:UNUSED_PAD src0_sel:DWORD src1_sel:BYTE_1\n\t" \
  "v_lshlrev_b32_sdwa v233, 1, v203 dst_sel:DWORD dst_unused:UNUSED_PAD src0_sel:DWORD src1_sel:WORD_1\n\t" \
  "v_mfma_f32_16x16x32_bf16 v[24:27], v[208:211], v[154:157], v[24:27]\n\t" \
  "ds_read_b32 v228, v228 offset:10016\n\t" \
  "v_lshlrev_b32_sdwa v232, 1, v202 dst_sel:DWORD dst_unused:UNUSED_PAD src0_sel:DWORD src1_sel:WORD_1\n\t" \
  "v_mfma_f32_16x16x32_bf16 v[48:51], v[208:211], v[162:165], v[48:51]\n\t" \
  "ds_read_b32 v231, v231 offset:10016\n\t" \
  "v_lshlrev_b32_sdwa v235, 1, v201 dst_sel:DWORD dst_unused:UNUSED_PAD src0_sel:DWORD src1_sel:WORD_1\n\t" \
  "v_lshlrev_b32_sdwa v234, 1, v200 dst_sel:DWORD dst_unused:UNUSED_PAD src0_sel:DWORD src1_sel:WORD_1\n\t" \
  "v_mfma_f32_16x16x32_bf16 v[72:75], v[208:211], v[170:173], v[72:75]\n\t" \
  "ds_read_b64_tr_b16 v[208:209], v249 offset:6224\n\t" \
  "ds_read_b64_tr_b16 v[210:211], v249 offset:7760\n\t" \
  "s_waitcnt lgkmcnt(13)\n\t" \
  "v_fma_mix_f32 v221, |v179|, v221, v221 op_sel:[0,1,0] op_sel_hi:[0,1,1] clamp\n\t" \
  "v_fma_mix_f32 v220, |v178|, v220, v220 op_sel:[0,1,0] op_sel_hi:[0,1,1] clamp\n\t" \
  "s_waitcnt lgkmcnt(11)\n\t" \
  "v_mfma_f32_16x16x32_bf16 v[4:7], v[212:215], v[146:149], v[4:7]\n\t" \
  "ds_read_b32 v230, v230 offset:10016\n\t" \
  "v_fma_mix_f32 v223, |v177|, v223, v223 op_sel:[0,1,0] op_sel_hi:[0,1,1] clamp\n\t" \
  "v_fma_mix_f32 v222, |v176|, v222, v222 op_sel:[0,1,0] op_sel_hi:[0,1,1] clamp\n\t" \
  "v_mfma_f32_16x16x32_bf16 v[28:31], v[212:215], v[154:157], v[28:31]\n\t" \
  "ds_read_u16_d16_hi v233, v233 offset:10528\n\t" \
  "s_waitcnt lgkmcnt(7)\n\t" \
  "v_or_b32 v187, v224, v225\n\t" \
  "v_mfma_f32_16x16x32_bf16 v[52:55], v[212:215], v[162:165], v[52:55]\n\t" \
  "ds_read_u16 v232, v232 offset:10528\n\t" \
  "v_or_b32 v186, v226, v227\n\t" \
  "v_fma_f32 v179, |v179|, v221, v179 mul:4\n\t" \
  "v_mfma_f32_16x16x32_bf16 v[76:79], v[212:215], v[170:173], v[76:79]\n\t" \
  "ds_read_b64_tr_b16 v[212:213], v249 offset:6280\n\t" \
  "v_fma_f32 v178, |v178|, v220, v178 mul:4\n\t" \
  "v_fma_f32 v177, |v177|, v223, v177 mul:4\n\t" \
  "v_mfma_f32_16x16x32_bf16 v[8:11], v[216:219], v[146:149], v[8:11]\n\t" \
  "ds_read_b64_tr_b16 v[214:215], v249 offset:7816\n\t" \
  "v_fma_f32 v176, |v176|, v222, v176 mul:4\n\t" \
  "v_mfma_f32_16x16x32_bf16 v[32:35], v[216:219], v[154:157], v[32:35]\n\t" \
  "ds_read_u16_d16_hi v235, v235 offset:10528\n\t" \
  "v_cvt_pk_bf16_f32 v179, v178, v179\n\t" \
  "v_cvt_pk_bf16_f32 v178, v176, v177\n\t" \
  "v_mfma_f32_16x16x32_bf16 v[56:59], v[216:219], v[162:165], v[56:59]\n\t" \
  "ds_read_u16 v234, v234 offset:10528\n\t" \
  "v_cvt_pknorm_i16_f32 v220, |v180|, |v181|\n\t" \
  "s_setprio 1\n\t" \
  "v_cvt_pknorm_i16_f32 v222, |v182|, |v183|\n\t" \
  "v_mfma_f32_16x16x32_bf16 v[80:83], v[216:219], v[170:173], v[80:83]\n\t" \
  "ds_read_b64_tr_b16 v[216:217], v249 offset:6288\n\t" \
  "v_lshlrev_b32_sdwa v221, 2, v220 dst_sel:DWORD dst_unused:UNUSED_PAD src0_sel:DWORD src1_sel:BYTE_3\n\t" \
  "v_lshlrev_b32_sdwa v220, 2, v220 dst_sel:DWORD dst_unused:UNUSED_PAD src0_sel:DWORD src1_sel:BYTE_1\n\t" \
  "s_waitcnt lgkmcnt(8)\n\t" \
  "v_mfma_f32_16x16x32_bf16 v[12:15], v[208:211], v[146:149], v[12:15]\n\t" \
  "ds_read_b64_tr_b16 v[218:219], v249 offset:7824\n\t" \
  "v_lshlrev_b32_sdwa v223, 2, v222 dst_sel:DWORD dst_unused:UNUSED_PAD src0_sel:DWORD src1_sel:BYTE_3\n\t" \
  "v_mfma_f32_16x16x32_bf16 v[36:39], v[208:211], v[154:157], v[36:39]\n\t" \
  "ds_read_b32 v221, v221 offset:10016\n\t" \
  "v_lshlrev_b32_sdwa v222, 2, v222 dst_sel:DWORD dst_unused:UNUSED_PAD src0_sel:DWORD src1_sel:BYTE_1\n\t" \
  "v_lshlrev_b32_sdwa v225, 1, v189 dst_sel:DWORD dst_unused:UNUSED_PAD src0_sel:DWORD src1_sel:WORD_1\n\t" \
  "v_mfma_f32_16x16x32_bf16 v[60:63], v[208:211], v[162:165], v[60:63]\n\t" \
  "ds_read_b32 v220, v220 offset:10016\n\t" \
  "v_lshlrev_b32_sdwa v224, 1, v188 dst_sel:DWORD dst_unused:UNUSED_PAD src0_sel:DWORD src1_sel:WORD_1\n\t" \
  "v_lshlrev_b32_sdwa v227, 1, v191 dst_sel:DWORD dst_unused:UNUSED_PAD src0_sel:DWORD src1_sel:WORD_1\n\t" \
  "v_mfma_f32_16x16x32_bf16 v[84:87], v[208:211], v[170:173], v[84:87]\n\t" \
  "ds_read_b64_tr_b16 v[208:209], v248 offset:43048\n\t" \
  "ds_read_b64_tr_b16 v[210:211], v248 offset:44072\n\t" \
  "v_lshlrev_b32_sdwa v226, 1, v190 dst_sel:DWORD dst_unused:UNUSED_PAD src0_sel:DWORD src1_sel:WORD_1\n\t" \
  "s_waitcnt lgkmcnt(8)\n\t" \
  "v_mfma_f32_16x16x32_bf16 v[16:19], v[212:215], v[146:149], v[16:19]\n\t" \
  "ds_read_b32 v223, v223 offset:10016\n\t" \
  "v_fma_mix_f32 v229, |v195|, v229, v229 op_sel:[0,1,0] op_sel_hi:[0,1,1] clamp\n\t" \
  "v_fma_mix_f32 v228, |v194|, v228, v228 op_sel:[0,1,0] op_sel_hi:[0,1,1] clamp\n\t" \
  "v_mfma_f32_16x16x32_bf16 v[40:43], v[212:215], v[154:157], v[40:43]\n\t" \
  "ds_read_b32 v222, v222 offset:10016\n\t" \
  "v_fma_mix_f32 v231, |v193|, v231, v231 op_sel:[0,1,0] op_sel_hi:[0,1,1] clamp\n\t" \
  "v_fma_mix_f32 v230, |v192|, v230, v230 op_sel:[0,1,0] op_sel_hi:[0,1,1] clamp\n\t" \
  "v_mfma_f32_16x16x32_bf16 v[64:67], v[212:215], v[162:165], v[64:67]\n\t" \
  "ds_read_u16_d16_hi v225, v225 offset:10528\n\t" \
  "s_waitcnt lgkmcnt(9)\n\t" \
  "v_or_b32 v203, v232, v233\n\t" \
  "v_or_b32 v202, v234, v235\n\t" \
  "v_mfma_f32_16x16x32_bf16 v[88:91], v[212:215], v[170:173], v[88:91]\n\t" \
  "ds_read_b64_tr_b16 v[212:213], v248 offset:46120\n\t" \
  "v_fma_f32 v195, |v195|, v229, v195 mul:4\n\t" \
  "s_waitcnt lgkmcnt(8)\n\t" \
  "v_mfma_f32_16x16x32_bf16 v[20:23], v[216:219], v[146:149], v[20:23]\n\t" \
  "ds_read_b64_tr_b16 v[214:215], v248 offset:47144\n\t" \
  "v_fma_f32 v194, |v194|, v228, v194 mul:4\n\t" \
  "v_fma_f32 v193, |v193|, v231, v193 mul:4\n\t" \
  "v_mfma_f32_16x16x32_bf16 v[44:47], v[216:219], v[154:157], v[44:47]\n\t" \
  "ds_read_u16 v224, v224 offset:10528\n\t" \
  "v_fma_f32 v192, |v192|, v230, v192 mul:4\n\t" \
  "v_cvt_pk_bf16_f32 v195, v194, v195\n\t" \
  "v_mfma_f32_16x16x32_bf16 v[68:71], v[216:219], v[162:165], v[68:71]\n\t" \
  "ds_read_u16_d16_hi v227, v227 offset:10528\n\t" \
  "v_cvt_pk_bf16_f32 v194, v192, v193\n\t" \
  "v_cvt_pknorm_i16_f32 v228, |v196|, |v197|\n\t" \
  "v_mfma_f32_16x16x32_bf16 v[92:95], v[216:219], v[170:173], v[92:95]\n\t" \
  "ds_read_b64_tr_b16 v[216:217], v248 offset:45096\n\t" \
  "v_cvt_pknorm_i16_f32 v230, |v198|, |v199|\n\t" \
  "s_setprio 2\n\t" \
  "s_waitcnt lgkmcnt(8)\n\t" \
  "v_mfma_f32_16x16x32_bf16 v[144:147], v[208:211], v[96:99], 0\n\t" \
  "ds_read_b64_tr_b16 v[218:219], v248 offset:48168\n\t" \
  "v_lshlrev_b32_sdwa v229, 2, v228 dst_sel:DWORD dst_unused:UNUSED_PAD src0_sel:DWORD src1_sel:BYTE_3\n\t" \
  "v_lshlrev_b32_sdwa v228, 2, v228 dst_sel:DWORD dst_unused:UNUSED_PAD src0_sel:DWORD src1_sel:BYTE_1\n\t" \
  "v_mfma_f32_16x16x32_bf16 v[152:155], v[208:211], v[100:103], 0\n\t" \
  "ds_read_u16 v226, v226 offset:10528\n\t" \
  "v_lshlrev_b32_sdwa v231, 2, v230 dst_sel:DWORD dst_unused:UNUSED_PAD src0_sel:DWORD src1_sel:BYTE_3\n\t" \
  "v_lshlrev_b32_sdwa v230, 2, v230 dst_sel:DWORD dst_unused:UNUSED_PAD src0_sel:DWORD src1_sel:BYTE_1\n\t" \
  "v_mfma_f32_16x16x32_bf16 v[160:163], v[208:211], v[104:107], 0\n\t" \
  "ds_read_b32 v229, v229 offset:10016\n\t" \
  "v_lshlrev_b32_sdwa v233, 1, v205 dst_sel:DWORD dst_unused:UNUSED_PAD src0_sel:DWORD src1_sel:WORD_1\n\t" \
  "v_mfma_f32_16x16x32_bf16 v[168:171], v[208:211], v[108:111], 0\n\t" \
  "ds_read_b64_tr_b16 v[208:209], v248 offset:43560\n\t" \
  "v_lshlrev_b32_sdwa v232, 1, v204 dst_sel:DWORD dst_unused:UNUSED_PAD src0_sel:DWORD src1_sel:WORD_1\n\t" \
  "v_lshlrev_b32_sdwa v235, 1, v207 dst_sel:DWORD dst_unused:UNUSED_PAD src0_sel:DWORD src1_sel:WORD_1\n\t" \
  "s_waitcnt lgkmcnt(7)\n\t" \
  "v_mfma_f32_16x16x32_bf16 v[144:147], v[212:215], v[112:115], v[144:147]\n\t" \
  "ds_read_b64_tr_b16 v[210:211], v248 offset:44584\n\t" \
  "ds_read_b32 v228, v228 offset:10016\n\t" \
  "v_lshlrev_b32_sdwa v234, 1, v206 dst_sel:DWORD dst_unused:UNUSED_PAD src0_sel:DWORD src1_sel:WORD_1\n\t" \
  "v_fma_mix_f32 v221, |v181|, v221, v221 op_sel:[0,1,0] op_sel_hi:[0,1,1] clamp\n\t" \
  "v_mfma_f32_16x16x32_bf16 v[152:155], v[212:215], v[116:119], v[152:155]\n\t" \
  "ds_read_b32 v231, v231 offset:10016\n\t" \
  "ds_read_b32 v230, v230 offset:10016\n\t" \
  "v_fma_mix_f32 v220, |v180|, v220, v220 op_sel:[0,1,0] op_sel_hi:[0,1,1] clamp\n\t" \
  "v_fma_mix_f32 v223, |v183|, v223, v223 op_sel:[0,1,0] op_sel_hi:[0,1,1] clamp\n\t" \
  "v_mfma_f32_16x16x32_bf16 v[160:163], v[212:215], v[120:123], v[160:163]\n\t" \
  "ds_read_u16_d16_hi v233, v233 offset:10528\n\t" \
  "v_fma_mix_f32 v222, |v182|, v222, v222 op_sel:[0,1,0] op_sel_hi:[0,1,1] clamp\n\t" \
  "v_mfma_f32_16x16x32_bf16 v[168:171], v[212:215], v[124:127], v[168:171]\n\t" \
  "ds_read_b64_tr_b16 v[212:213], v248 offset:46632\n\t" \
  "s_waitcnt lgkmcnt(8)\n\t" \
  "v_or_b32 v188, v224, v225\n\t" \
  "v_or_b32 v189, v226, v227\n\t" \
  "v_mfma_f32_16x16x32_bf16 v[144:147], v[216:219], v[128:131], v[144:147]\n\t" \
  "ds_read_b64_tr_b16 v[214:215], v248 offset:47656\n\t" \
  "v_fma_f32 v181, |v181|, v221, v181 mul:4\n\t" \
  "v_fma_f32 v180, |v180|, v220, v180 mul:4\n\t" \
  "v_mfma_f32_16x16x32_bf16 v[152:155], v[216:219], v[132:135], v[152:155]\n\t" \
  "ds_read_u16 v232, v232 offset:10528\n\t" \
  "v_fma_f32 v183, |v183|, v223, v183 mul:4\n\t" \
  "v_mfma_f32_16x16x32_bf16 v[160:163], v[216:219], v[136:139], v[160:163]\n\t" \
  "ds_read_u16_d16_hi v235, v235 offset:10528\n\t" \
  "v_fma_f32 v182, |v182|, v222, v182 mul:4\n\t" \
  "v_cvt_pk_bf16_f32 v180, v180, v181\n\t" \
  "v_mfma_f32_16x16x32_bf16 v[168:171], v[216:219], v[140:143], v[168:171]\n\t" \
  "ds_read_b64_tr_b16 v[216:217], v248 offset:45608\n\t" \
  "v_cvt_pk_bf16_f32 v181, v182, v183\n\t" \
  "v_cvt_pknorm_i16_f32 v220, |v146|, |v147|\n\t" \
  "s_waitcnt lgkmcnt(9)\n\t" \
  "v_mfma_f32_16x16x32_bf16 v[148:151], v[208:211], v[96:99], 0\n\t" \
  "ds_read_b64_tr_b16 v[218:219], v248 offset:48680\n\t" \
  "v_cvt_pknorm_i16_f32 v222, |v144|, |v145|\n\t" \
  "s_setprio 3\n\t" \
  "v_lshlrev_b32_sdwa v221, 2, v220 dst_sel:DWORD dst_unused:UNUSED_PAD src0_sel:DWORD src1_sel:BYTE_3\n\t" \
  "v_mfma_f32_16x16x32_bf16 v[156:159], v[208:211], v[100:103], 0\n\t" \
  "s_waitcnt vmcnt(1)\n\t" \
  "s_cmp_lg_u32 s58, 1\n\t" \
  "s_cbranch_scc1 .Lcnx_c11\n\t" \
  "s_waitcnt vmcnt(0)\n\t" \
  ".Lcnx_c11:\n\t" \
  "s_mov_b64 exec, 3\n\t" \
  "ds_add_rtn_u32 v246, v251, v250 offset:47168\n\t" \
  "s_mov_b64 exec, -1\n\t" \
  "ds_read_u16 v234, v234 offset:10528\n\t" \
  "v_lshlrev_b32_sdwa v220, 2, v220 dst_sel:DWORD dst_unused:UNUSED_PAD src0_sel:DWORD src1_sel:BYTE_1\n\t" \
  "v_mfma_f32_16x16x32_bf16 v[164:167], v[208:211], v[104:107], 0\n\t" \
  "ds_read_b32 v221, v221 offset:10016\n\t" \
  "v_lshlrev_b32_sdwa v223, 2, v222 dst_sel:DWORD dst_unused:UNUSED_PAD src0_sel:DWORD src1_sel:BYTE_3\n\t" \
  "v_lshlrev_b32_sdwa v222, 2, v222 dst_sel:DWORD dst_unused:UNUSED_PAD src0_sel:DWORD src1_sel:BYTE_1\n\t" \
  "v_mfma_f32_16x16x32_bf16 v[172:175], v[208:211], v[108:111], 0\n\t" \
  "s_waitcnt lgkmcnt(2)\n\t" \
  "v_readfirstlane_b32 s4, v246\n\t" \
  "s_cmp_lt_u32 s4, s57\n\t" \
  "s_cbranch_scc1 .Lcnx_w48\n.Lcnx_b48:\n\t" \
  "ds_read_b64_tr_b16 v[208:209], v249 offset:12304\n\t" \
  "v_lshlrev_b32_sdwa v225, 1, v155 dst_sel:DWORD dst_unused:UNUSED_PAD src0_sel:DWORD src1_sel:WORD_1\n\t" \
  "v_lshlrev_b32_sdwa v224, 1, v154 dst_sel:DWORD dst_unused:UNUSED_PAD src0_sel:DWORD src1_sel:WORD_1\n\t" \
  "v_mfma_f32_16x16x32_bf16 v[148:151], v[212:215], v[112:115], v[148:151]\n\t" \
  "ds_read_b64_tr_b16 v[210:211], v249 offset:13840\n\t" \
  "v_lshlrev_b32_sdwa v227, 1, v153 dst_sel:DWORD dst_unused:UNUSED_PAD src0_sel:DWORD src1_sel:WORD_1\n\t" \
  "v_mfma_f32_16x16x32_bf16 v[156:159], v[212:215], v[116:119], v[156:159]\n\t" \
  "s_cmp_eq_u32 s37, 0\n\t" \
  "s_cbranch_scc1 .Lcnx_f5\n.Lcnx_g5:\n\t" \
  "ds_read_b32 v220, v220 offset:10016\n\t" \
  "v_lshlrev_b32_sdwa v226, 1, v152 dst_sel:DWORD dst_unused:UNUSED_PAD src0_sel:DWORD src1_sel:WORD_1\n\t" \
  "v_fma_mix_f32 v229, |v197|, v229, v229 op_sel:[0,1,0] op_sel_hi:[0,1,1] clamp\n\t" \
  "v_mfma_f32_16x16x32_bf16 v[164:167], v[212:215], v[120:123], v[164:167]\n\t" \
  "ds_read_b32 v223, v223 offset:10016\n\t" \
  "ds_read_b32 v222, v222 offset:10016\n\t" \
  "v_fma_mix_f32 v228, |v196|, v228, v228 op_sel:[0,1,0] op_sel_hi:[0,1,1] clamp\n\t" \
  "v_fma_mix_f32 v231, |v199|, v231, v231 op_sel:[0,1,0] op_sel_hi:[0,1,1] clamp\n\t" \
  "v_mfma_f32_16x16x32_bf16 v[172:175], v[212:215], v[124:127], v[172:175]\n\t" \
  "ds_read_b64_tr_b16 v[212:213], v249 offset:12312\n\t" \
  "ds_read_b64_tr_b16 v[214:215], v249 offset:13848\n\t" \
  "v_fma_mix_f32 v230, |v198|, v230, v230 op_sel:[0,1,0] op_sel_hi:[0,1,1] clamp\n\t" \
  "s_waitcnt lgkmcnt(8)\n\t" \
  "v_or_b32 v204, v232, v233\n\t" \
  "v_mfma_f32_16x16x32_bf16 v[148:151], v[216:219], v[128:131], v[148:151]\n\t" \
  "ds_read_u16_d16_hi v225, v225 offset:10528\n\t" \
  "v_or_b32 v205, v234, v235\n\t" \
  "v_mfma_f32_16x16x32_bf16 v[156:159], v[216:219], v[132:135], v[156:159]\n\t" \
  "ds_read_u16 v224, v224 offset:10528\n\t" \
  "v_fma_f32 v197, |v197|, v229, v197 mul:4\n\t" \
  "v_fma_f32 v196, |v196|, v228, v196 mul:4\n\t" \
  "v_mfma_f32_16x16x32_bf16 v[164:167], v[216:219], v[136:139], v[164:167]\n\t" \
  "ds_read_u16_d16_hi v227, v227 offset:10528\n\t" \
  "v_fma_f32 v199, |v199|, v231, v199 mul:4\n\t" \
  "v_fma_f32 v198, |v198|, v230, v198 mul:4\n\t" \
  "v_mfma_f32_16x16x32_bf16 v[172:175], v[216:219], v[140:143], v[172:175]\n\t" \
  "ds_read_b64_tr_b16 v[216:217], v249 offset:12368\n\t" \
  "v_cvt_pk_bf16_f32 v196, v196, v197\n\t" \
  "v_cvt_pk_bf16_f32 v197, v198, v199\n\t" \
  "s_setprio 0\n\t" \
  "v_cvt_pknorm_i16_f32 v228, |v162|, |v163|\n\t" \
  "v_cvt_pknorm_i16_f32 v230, |v160|, |v161|\n\t" \
  "v_mfma_f32_16x16x32_bf16 v[24:27], v[242:245], v[236:239], v[24:27]\n\t" \
  "ds_read_b64_tr_b16 v[218:219], v249 offset:13904\n\t" \
  "v_lshlrev_b32_sdwa v229, 2, v228 dst_sel:DWORD dst_unused:UNUSED_PAD src0_sel:DWORD src1_sel:BYTE_3\n\t" \
  "v_mfma_f32_16x16x32_bf16 v[28:31], v[240:243], v[236:239], v[28:31]\n\t" \
  "buffer_load_dwordx4 v[236:239], v250, s[8:11], s29 offen offset:64 nt\n\t" \
  "s_cmp_lg_u32 s58, 0\n\t" \
  "s_cbranch_scc1 .Lcnx_c24\n\t" \
  "s_add_u32 m0, s25, 24600\n\t" \
  "s_add_u32 s24, s24, s26\n\t" \
  "buffer_load_dwordx4 v254, s[8:11], s24 offen lds\n\t" \
  "s_cmp_lg_u32 s38, 6\n\t" \
  "s_cbranch_scc1 .Lcnx_c24\n\t" \
  "s_mov_b32 m0, 107744\n\t" \
  "s_add_u32 s30, s30, s31\n\t" \
  "buffer_load_dword v252, s[8:11], s30 offen lds\n\t" \
  ".Lcnx_c24:\n\t" \
  "ds_read_u16 v226, v226 offset:10528\n\t" \
  "v_lshlrev_b32_sdwa v228, 2, v228 dst_sel:DWORD dst_unused:UNUSED_PAD src0_sel:DWORD src1_sel:BYTE_1\n\t" \
  "v_lshlrev_b32_sdwa v231, 2, v230 dst_sel:DWORD dst_unused:UNUSED_PAD src0_sel:DWORD src1_sel:BYTE_3\n\t" \
  "s_waitcnt lgkmcnt(11)\n\t" \
  "v_mfma_f32_16x16x32_bf16 v[0:3], v[208:211], v[178:181], v[0:3]\n\t" \
  "ds_read_b32 v229, v229 offset:10016\n\t" \
  "v_lshlrev_b32_sdwa v230, 2, v230 dst_sel:DWORD dst_unused:UNUSED_PAD src0_sel:DWORD src1_sel:BYTE_1\n\t" \
  "v_lshlrev_b32_sdwa v233, 1, v171 dst_sel:DWORD dst_unused:UNUSED_PAD src0_sel:DWORD src1_sel:WORD_1\n\t" \
  "v_mfma_f32_16x16x32_bf16 v[24:27], v[208:211], v[186:189], v[24:27]\n\t" \
  "ds_read_b32 v228, v228 offset:10016\n\t" \
  "v_lshlrev_b32_sdwa v232, 1, v170 dst_sel:DWORD dst_unused:UNUSED_PAD src0_sel:DWORD src1_sel:WORD_1\n\t" \
  "v_mfma_f32_16x16x32_bf16 v[48:51], v[208:211], v[194:197], v[48:51]\n\t" \
  "ds_read_b32 v231, v231 offset:10016\n\t" \
  "v_lshlrev_b32_sdwa v235, 1, v169 dst_sel:DWORD dst_unused:UNUSED_PAD src0_sel:DWORD src1_sel:WORD_1\n\t" \
  "v_lshlrev_b32_sdwa v234, 1, v168 dst_sel:DWORD dst_unused:UNUSED_PAD src0_sel:DWORD src1_sel:WORD_1\n\t" \
  "v_mfma_f32_16x16x32_bf16 v[72:75], v[208:211], v[202:205], v[72:75]\n\t" \
  "ds_read_b64_tr_b16 v[208:209], v249 offset:12376\n\t" \
  "ds_read_b64_tr_b16 v[210:211], v249 offset:13912\n\t" \
  "s_waitcnt lgkmcnt(13)\n\t" \
  "v_fma_mix_f32 v221, |v147|, v221, v221 op_sel:[0,1,0] op_sel_hi:[0,1,1] clamp\n\t" \
  "v_fma_mix_f32 v220, |v146|, v220, v220 op_sel:[0,1,0] op_sel_hi:[0,1,1] clamp\n\t" \
  "s_waitcnt lgkmcnt(11)\n\t" \
  "v_mfma_f32_16x16x32_bf16 v[4:7], v[212:215], v[178:181], v[4:7]\n\t" \
  "ds_read_b32 v230, v230 offset:10016\n\t" \
  "v_fma_mix_f32 v223, |v145|, v223, v223 op_sel:[0,1,0] op_sel_hi:[0,1,1] clamp\n\t" \
  "v_fma_mix_f32 v222, |v144|, v222, v222 op_sel:[0,1,0] op_sel_hi:[0,1,1] clamp\n\t" \
  "v_mfma_f32_16x16x32_bf16 v[28:31], v[212:215], v[186:189], v[28:31]\n\t" \
  "ds_read_u16_d16_hi v233, v233 offset:10528\n\t" \
  "s_waitcnt lgkmcnt(7)\n\t" \
  "v_or_b32 v155, v224, v225\n\t" \
  "v_mfma_f32_16x16x32_bf16 v[52:55], v[212:215], v[194:197], v[52:55]\n\t" \
  "ds_read_u16 v232, v232 offset:10528\n\t" \
  "v_or_b32 v154, v226, v227\n\t" \
  "v_fma_f32 v147, |v147|, v221, v147 mul:4\n\t" \
  "v_mfma_f32_16x16x32_bf16 v[76:79], v[212:215], v[202:205], v[76:79]\n\t" \
  "ds_read_b64_tr_b16 v[212:213], v249 offset:12432\n\t" \
  "v_fma_f32 v146, |v146|, v220, v146 mul:4\n\t" \
  "v_fma_f32 v145, |v145|, v223, v145 mul:4\n\t" \
  "v_mfma_f32_16x16x32_bf16 v[8:11], v[216:219], v[178:181], v[8:11]\n\t" \
  "ds_read_b64_tr_b16 v[214:215], v249 offset:13968\n\t" \
  "v_fma_f32 v144, |v144|, v222, v144 mul:4\n\t" \
  "v_mfma_f32_16x16x32_bf16 v[32:35], v[216:219], v[186:189], v[32:35]\n\t" \
  "ds_read_u16_d16_hi v235, v235 offset:10528\n\t" \
  "v_cvt_pk_bf16_f32 v147, v146, v147\n\t" \
  "v_cvt_pk_bf16_f32 v146, v144, v145\n\t" \
  "v_mfma_f32_16x16x32_bf16 v[56:59], v[216:219], v[194:197], v[56:59]\n\t" \
  "ds_read_u16 v234, v234 offset:10528\n\t" \
  "v_cvt_pknorm_i16_f32 v220, |v148|, |v149|\n\t" \
  "s_setprio 1\n\t" \
  "v_cvt_pknorm_i16_f32 v222, |v150|, |v151|\n\t" \
  "v_mfma_f32_16x16x32_bf16 v[80:83], v[216:219], v[202:205], v[80:83]\n\t" \
  "ds_read_b64_tr_b16 v[216:217], v249 offset:12440\n\t" \
  "v_lshlrev_b32_sdwa v221, 2, v220 dst_sel:DWORD dst_unused:UNUSED_PAD src0_sel:DWORD src1_sel:BYTE_3\n\t" \
  "v_lshlrev_b32_sdwa v220, 2, v220 dst_sel:DWORD dst_unused:UNUSED_PAD src0_sel:DWORD src1_sel:BYTE_1\n\t" \
  "s_waitcnt lgkmcnt(8)\n\t" \
  "v_mfma_f32_16x16x32_bf16 v[12:15], v[208:211], v[178:181], v[12:15]\n\t" \
  "ds_read_b64_tr_b16 v[218:219], v249 offset:13976\n\t" \
  "v_lshlrev_b32_sdwa v223, 2, v222 dst_sel:DWORD dst_unused:UNUSED_PAD src0_sel:DWORD src1_sel:BYTE_3\n\t" \
  "v_mfma_f32_16x16x32_bf16 v[36:39], v[208:211], v[186:189], v[36:39]\n\t" \
  "ds_read_b32 v221, v221 offset:10016\n\t" \
  "v_lshlrev_b32_sdwa v222, 2, v222 dst_sel:DWORD dst_unused:UNUSED_PAD src0_sel:DWORD src1_sel:BYTE_1\n\t" \
  "v_lshlrev_b32_sdwa v225, 1, v157 dst_sel:DWORD dst_unused:UNUSED_PAD src0_sel:DWORD src1_sel:WORD_1\n\t" \
  "v_mfma_f32_16x16x32_bf16 v[60:63], v[208:211], v[194:197], v[60:63]\n\t" \
  "ds_read_b32 v220, v220 offset:10016\n\t" \
  "v_lshlrev_b32_sdwa v224, 1, v156 dst_sel:DWORD dst_unused:UNUSED_PAD src0_sel:DWORD src1_sel:WORD_1\n\t" \
  "v_lshlrev_b32_sdwa v227, 1, v159 dst_sel:DWORD dst_unused:UNUSED_PAD src0_sel:DWORD src1_sel:WORD_1\n\t" \
  "v_mfma_f32_16x16x32_bf16 v[84:87], v[208:211], v[202:205], v[84:87]\n\t" \
  "ds_read_b64_tr_b16 v[208:209], v248 offset:49192\n\t" \
  "ds_read_b64_tr_b16 v[210:211], v248 offset:50216\n\t" \
  "v_lshlrev_b32_sdwa v226, 1, v158 dst_sel:DWORD dst_unused:UNUSED_PAD src0_sel:DWORD src1_sel:WORD_1\n\t" \
  "s_waitcnt lgkmcnt(8)\n\t" \
  "v_mfma_f32_16x16x32_bf16 v[16:19], v[212:215], v[178:181], v[16:19]\n\t" \
  "ds_read_b32 v223, v223 offset:10016\n\t" \
  "v_fma_mix_f32 v229, |v163|, v229, v229 op_sel:[0,1,0] op_sel_hi:[0,1,1] clamp\n\t" \
  "v_fma_mix_f32 v228, |v162|, v228, v228 op_sel:[0,1,0] op_sel_hi:[0,1,1] clamp\n\t" \
  "v_mfma_f32_16x16x32_bf16 v[40:43], v[212:215], v[186:189], v[40:43]\n\t" \
  "ds_read_b32 v222, v222 offset:10016\n\t" \
  "v_fma_mix_f32 v231, |v161|, v231, v231 op_sel:[0,1,0] op_sel_hi:[0,1,1] clamp\n\t" \
  "v_fma_mix_f32 v230, |v160|, v230, v230 op_sel:[0,1,0] op_sel_hi:[0,1,1] clamp\n\t" \
  "v_mfma_f32_16x16x32_bf16 v[64:67], v[212:215], v[194:197], v[64:67]\n\t" \
  "ds_read_u16_d16_hi v225, v225 offset:10528\n\t" \
  "s_waitcnt lgkmcnt(9)\n\t" \
  "v_or_b32 v171, v232, v233\n\t" \
  "v_or_b32 v170, v234, v235\n\t" \
  "v_mfma_f32_16x16x32_bf16 v[88:91], v[212:215], v[202:205], v[88:91]\n\t" \
  "ds_read_b64_tr_b16 v[212:213], v248 offset:52264\n\t" \
  "v_fma_f32 v163, |v163|, v229, v163 mul:4\n\t" \
  "s_waitcnt lgkmcnt(8)\n\t" \
  "v_mfma_f32_16x16x32_bf16 v[20:23], v[216:219], v[178:181], v[20:23]\n\t" \
  "ds_read_b64_tr_b16 v[214:215], v248 offset:53288\n\t" \
  "v_fma_f32 v162, |v162|, v228, v162 mul:4\n\t" \
  "v_fma_f32 v161, |v161|, v231, v161 mul:4\n\t" \
  "v_mfma_f32_16x16x32_bf16 v[44:47], v[216:219], v[186:189], v[44:47]\n\t" \
  "ds_read_u16 v224, v224 offset:10528\n\t" \
  "v_fma_f32 v160, |v160|, v230, v160 mul:4\n\t" \
  "v_cvt_pk_bf16_f32 v163, v162, v163\n\t" \
  "v_mfma_f32_16x16x32_bf16 v[68:71], v[216:219], v[194:197], v[68:71]\n\t" \
  "ds_read_u16_d16_hi v227, v227 offset:10528\n\t" \
  "v_cvt_pk_bf16_f32 v162, v160, v161\n\t" \
  "v_cvt_pknorm_i16_f32 v228, |v164|, |v165|\n\t" \
  "v_mfma_f32_16x16x32_bf16 v[92:95], v[216:219], v[202:205], v[92:95]\n\t" \
  "ds_read_b64_tr_b16 v[216:217], v248 offset:51240\n\t" \
  "v_cvt_pknorm_i16_f32 v230, |v166|, |v167|\n\t" \
  "s_setprio 2\n\t" \
  "s_waitcnt lgkmcnt(8)\n\t" \
  "v_mfma_f32_16x16x32_bf16 v[176:179], v[208:211], v[96:99], 0\n\t" \
  "ds_read_b64_tr_b16 v[218:219], v248 offset:54312\n\t" \
  "v_lshlrev_b32_sdwa v229, 2, v228 dst_sel:DWORD dst_unused:UNUSED_PAD src0_sel:DWORD src1_sel:BYTE_3\n\t" \
  "v_lshlrev_b32_sdwa v228, 2, v228 dst_sel:DWORD dst_unused:UNUSED_PAD src0_sel:DWORD src1_sel:BYTE_1\n\t" \
  "v_mfma_f32_16x16x32_bf16 v[184:187], v[208:211], v[100:103], 0\n\t" \
  "ds_read_u16 v226, v226 offset:10528\n\t" \
  "v_lshlrev_b32_sdwa v231, 2, v230 dst_sel:DWORD dst_unused:UNUSED_PAD src0_sel:DWORD src1_sel:BYTE_3\n\t" \
  "v_lshlrev_b32_sdwa v230, 2, v230 dst_sel:DWORD dst_unused:UNUSED_PAD src0_sel:DWORD src1_sel:BYTE_1\n\t" \
  "v_mfma_f32_16x16x32_bf16 v[192:195], v[208:211], v[104:107], 0\n\t" \
  "ds_read_b32 v229, v229 offset:10016\n\t" \
  "v_lshlrev_b32_sdwa v233, 1, v173 dst_sel:DWORD dst_unused:UNUSED_PAD src0_sel:DWORD src1_sel:WORD_1\n\t" \
  "v_mfma_f32_16x16x32_bf16 v[200:203], v[208:211], v[108:111], 0\n\t" \
  "ds_read_b64_tr_b16 v[208:209], v248 offset:49704\n\t" \
  "v_lshlrev_b32_sdwa v232, 1, v172 dst_sel:DWORD dst_unused:UNUSED_PAD src0_sel:DWORD src1_sel:WORD_1\n\t" \
  "v_lshlrev_b32_sdwa v235, 1, v175 dst_sel:DWORD dst_unused:UNUSED_PAD src0_sel:DWORD src1_sel:WORD_1\n\t" \
  "s_waitcnt lgkmcnt(7)\n\t" \
  "v_mfma_f32_16x16x32_bf16 v[176:179], v[212:215], v[112:115], v[176:179]\n\t" \
  "ds_read_b64_tr_b16 v[210:211], v248 offset:50728\n\t" \
  "ds_read_b32 v228, v228 offset:10016\n\t" \
  "v_lshlrev_b32_sdwa v234, 1, v174 dst_sel:DWORD dst_unused:UNUSED_PAD src0_sel:DWORD src1_sel:WORD_1\n\t" \
  "v_fma_mix_f32 v221, |v149|, v221, v221 op_sel:[0,1,0] op_sel_hi:[0,1,1] clamp\n\t" \
  "v_mfma_f32_16x16x32_bf16 v[184:187], v[212:215], v[116:119], v[184:187]\n\t" \
  "ds_read_b32 v231, v231 offset:10016\n\t" \
  "ds_read_b32 v230, v230 offset:10016\n\t" \
  "v_fma_mix_f32 v220, |v148|, v220, v220 op_sel:[0,1,0] op_sel_hi:[0,1,1] clamp\n\t" \
  "v_fma_mix_f32 v223, |v151|, v223, v223 op_sel:[0,1,0] op_sel_hi:[0,1,1] clamp\n\t" \
  "v_mfma_f32_16x16x32_bf16 v[192:195], v[212:215], v[120:123], v[192:195]\n\t" \
  "ds_read_u16_d16_hi v233, v233 offset:10528\n\t" \
  "v_fma_mix_f32 v222, |v150|, v222, v222 op_sel:[0,1,0] op_sel_hi:[0,1,1] clamp\n\t" \
  "v_mfma_f32_16x16x32_bf16 v[200:203], v[212:215], v[124:127], v[200:203]\n\t" \
  "ds_read_b64_tr_b16 v[212:213], v248 offset:52776\n\t" \
  "s_waitcnt lgkmcnt(8)\n\t" \
  "v_or_b32 v156, v224, v225\n\t" \
  "v_or_b32 v157, v226, v227\n\t" \
  "v_mfma_f32_16x16x32_bf16 v[176:179], v[216:219], v[128:131], v[176:179]\n\t" \
  "ds_read_b64_tr_b16 v[214:215], v248 offset:53800\n\t" \
  "v_fma_f32 v149, |v149|, v221, v149 mul:4\n\t" \
  "v_fma_f32 v148, |v148|, v220, v148 mul:4\n\t" \
  "v_mfma_f32_16x16x32_bf16 v[184:187], v[216:219], v[132:135], v[184:187]\n\t" \
  "ds_read_u16 v232, v232 offset:10528\n\t" \
  "v_fma_f32 v151, |v151|, v223, v151 mul:4\n\t" \
  "v_mfma_f32_16x16x32_bf16 v[192:195], v[216:219], v[136:139], v[192:195]\n\t" \
  "ds_read_u16_d16_hi v235, v235 offset:10528\n\t" \
  "v_fma_f32 v150, |v150|, v222, v150 mul:4\n\t" \
  "v_cvt_pk_bf16_f32 v148, v148, v149\n\t" \
  "v_mfma_f32_16x16x32_bf16 v[200:203], v[216:219], v[140:143], v[200:203]\n\t" \
  "ds_read_b64_tr_b16 v[216:217], v248 offset:51752\n\t" \
  "v_cvt_pk_bf16_f32 v149, v150, v151\n\t" \
  "v_cvt_pknorm_i16_f32 v220, |v178|, |v179|\n\t" \
  "s_waitcnt lgkmcnt(9)\n\t" \
  "v_mfma_f32_16x16x32_bf16 v[180:183], v[208:211], v[96:99], 0\n\t" \
  "ds_read_b64_tr_b16 v[218:219], v248 offset:54824\n\t" \
  "v_cvt_pknorm_i16_f32 v222, |v176|, |v177|\n\t" \
  "s_setprio 3\n\t" \
  "v_lshlrev_b32_sdwa v221, 2, v220 dst_sel:DWORD dst_unused:UNUSED_PAD src0_sel:DWORD src1_sel:BYTE_3\n\t" \
  "v_mfma_f32_16x16x32_bf16 v[188:191], v[208:211], v[100:103], 0\n\t" \
  "s_waitcnt vmcnt(1)\n\t" \
  "s_cmp_lg_u32 s58, 1\n\t" \
  "s_cbranch_scc1 .Lcnx_c12\n\t" \
  "s_waitcnt vmcnt(0)\n\t" \
  ".Lcnx_c12:\n\t" \
  "s_mov_b64 exec, 3\n\t" \
  "ds_add_rtn_u32 v246, v251, v250 offset:47176\n\t" \
  "s_mov_b64 exec, -1\n\t" \
  "ds_read_u16 v234, v234 offset:10528\n\t" \
  "v_lshlrev_b32_sdwa v220, 2, v220 dst_sel:DWORD dst_unused:UNUSED_PAD src0_sel:DWORD src1_sel:BYTE_1\n\t" \
  "v_mfma_f32_16x16x32_bf16 v[196:199], v[208:211], v[104:107], 0\n\t" \
  "ds_read_b32 v221, v221 offset:10016\n\t" \
  "v_lshlrev_b32_sdwa v223, 2, v222 dst_sel:DWORD dst_unused:UNUSED_PAD src0_sel:DWORD src1_sel:BYTE_3\n\t" \
  "v_lshlrev_b32_sdwa v222, 2, v222 dst_sel:DWORD dst_unused:UNUSED_PAD src0_sel:DWORD src1_sel:BYTE_1\n\t" \
  "v_mfma_f32_16x16x32_bf16 v[204:207], v[208:211], v[108:111], 0\n\t" \
  "s_waitcnt lgkmcnt(2)\n\t" \
  "v_readfirstlane_b32 s4, v246\n\t" \
  "s_cmp_lt_u32 s4, s57\n\t" \
  "s_cbranch_scc1 .Lcnx_w60\n.Lcnx_b60:\n\t" \
  "ds_read_b64_tr_b16 v[208:209], v249 offset:24600\n\t" \
  "v_lshlrev_b32_sdwa v225, 1, v187 dst_sel:DWORD dst_unused:UNUSED_PAD src0_sel:DWORD src1_sel:WORD_1\n\t" \
  "v_lshlrev_b32_sdwa v224, 1, v186 dst_sel:DWORD dst_unused:UNUSED_PAD src0_sel:DWORD src1_sel:WORD_1\n\t" \
  "v_mfma_f32_16x16x32_bf16 v[180:183], v[212:215], v[112:115], v[180:183]\n\t" \
  "ds_read_b64_tr_b16 v[210:211], v249 offset:26136\n\t" \
  "v_lshlrev_b32_sdwa v227, 1, v185 dst_sel:DWORD dst_unused:UNUSED_PAD src0_sel:DWORD src1_sel:WORD_1\n\t" \
  "v_mfma_f32_16x16x32_bf16 v[188:191], v[212:215], v[116:119], v[188:191]\n\t" \
  "s_cmp_eq_u32 s37, 0\n\t" \
  "s_cbranch_scc1 .Lcnx_f6\n.Lcnx_g6:\n\t" \
  "ds_read_b32 v220, v220 offset:10016\n\t" \
  "v_lshlrev_b32_sdwa v226, 1, v184 dst_sel:DWORD dst_unused:UNUSED_PAD src0_sel:DWORD src1_sel:WORD_1\n\t" \
  "v_fma_mix_f32 v229, |v165|, v229, v229 op_sel:[0,1,0] op_sel_hi:[0,1,1] clamp\n\t" \
  "v_mfma_f32_16x16x32_bf16 v[196:199], v[212:215], v[120:123], v[196:199]\n\t" \
  "ds_read_b32 v223, v223 offset:10016\n\t" \
  "ds_read_b32 v222, v222 offset:10016\n\t" \
  "v_fma_mix_f32 v228, |v164|, v228, v228 op_sel:[0,1,0] op_sel_hi:[0,1,1] clamp\n\t" \
  "v_fma_mix_f32 v231, |v167|, v231, v231 op_sel:[0,1,0] op_sel_hi:[0,1,1] clamp\n\t" \
  "v_mfma_f32_16x16x32_bf16 v[204:207], v[212:215], v[124:127], v[204:207]\n\t" \
  "ds_read_b64_tr_b16 v[212:213], v249 offset:24608\n\t" \
  "ds_read_b64_tr_b16 v[214:215], v249 offset:26144\n\t" \
  "v_fma_mix_f32 v230, |v166|, v230, v230 op_sel:[0,1,0] op_sel_hi:[0,1,1] clamp\n\t" \
  "s_waitcnt lgkmcnt(8)\n\t" \
  "v_or_b32 v172, v232, v233\n\t" \
  "v_mfma_f32_16x16x32_bf16 v[180:183], v[216:219], v[128:131], v[180:183]\n\t" \
  "ds_read_u16_d16_hi v225, v225 offset:10528\n\t" \
  "v_or_b32 v173, v234, v235\n\t" \
  "v_mfma_f32_16x16x32_bf16 v[188:191], v[216:219], v[132:135], v[188:191]\n\t" \
  "ds_read_u16 v224, v224 offset:10528\n\t" \
  "v_fma_f32 v165, |v165|, v229, v165 mul:4\n\t" \
  "v_fma_f32 v164, |v164|, v228, v164 mul:4\n\t" \
  "v_mfma_f32_16x16x32_bf16 v[196:199], v[216:219], v[136:139], v[196:199]\n\t" \
  "ds_read_u16_d16_hi v227, v227 offset:10528\n\t" \
  "v_fma_f32 v167, |v167|, v231, v167 mul:4\n\t" \
  "v_fma_f32 v166, |v166|, v230, v166 mul:4\n\t" \
  "v_mfma_f32_16x16x32_bf16 v[204:207], v[216:219], v[140:143], v[204:207]\n\t" \
  "ds_read_b64_tr_b16 v[216:217], v249 offset:24664\n\t" \
  "v_cvt_pk_bf16_f32 v164, v164, v165\n\t" \
  "v_cvt_pk_bf16_f32 v165, v166, v167\n\t" \
  "s_setprio 0\n\t" \
  "v_cvt_pknorm_i16_f32 v228, |v194|, |v195|\n\t" \
  "v_cvt_pknorm_i16_f32 v230, |v192|, |v193|\n\t" \
  "v_mfma_f32_16x16x32_bf16 v[32:35], v[242:245], v[236:239], v[32:35]\n\t" \
  "ds_read_b64_tr_b16 v[218:219], v249 offset:26200\n\t" \
  "v_lshlrev_b32_sdwa v229, 2, v228 dst_sel:DWORD dst_unused:UNUSED_PAD src0_sel:DWORD src1_sel:BYTE_3\n\t" \
  "v_mfma_f32_16x16x32_bf16 v[36:39], v[240:243], v[236:239], v[36:39]\n\t" \
  "buffer_load_dwordx4 v[236:239], v250, s[8:11], s29 offen offset:128 nt\n\t" \
  "s_cmp_lg_u32 s58, 0\n\t" \
  "s_cbranch_scc1 .Lcnx_c25\n\t" \
  "s_add_u32 m0, s25, 32800\n\t" \
  "s_add_u32 s24, s24, s26\n\t" \
  "buffer_load_dwordx4 v254, s[8:11], s24 offen lds\n\t" \
  "s_cmp_lg_u32 s38, 7\n\t" \
  "s_cbranch_scc1 .Lcnx_c25\n\t" \
  "s_mov_b32 m0, 107744\n\t" \
  "s_add_u32 s30, s30, s31\n\t" \
  "buffer_load_dword v252, s[8:11], s30 offen lds\n\t" \
  ".Lcnx_c25:\n\t" \
  "ds_read_u16 v226, v226 offset:10528\n\t" \
  "v_lshlrev_b32_sdwa v228, 2, v228 dst_sel:DWORD dst_unused:UNUSED_PAD src0_sel:DWORD src1_sel:BYTE_1\n\t" \
  "v_lshlrev_b32_sdwa v231, 2, v230 dst_sel:DWORD dst_unused:UNUSED_PAD src0_sel:DWORD src1_sel:BYTE_3\n\t" \
  "s_waitcnt lgkmcnt(11)\n\t" \
  "v_mfma_f32_16x16x32_bf16 v[0:3], v[208:211], v[146:149], v[0:3]\n\t" \
  "ds_read_b32 v229, v229 offset:10016\n\t" \
  "v_lshlrev_b32_sdwa v230, 2, v230 dst_sel:DWORD dst_unused:UNUSED_PAD src0_sel:DWORD src1_sel:BYTE_1\n\t" \
  "v_lshlrev_b32_sdwa v233, 1, v203 dst_sel:DWORD dst_unused:UNUSED_PAD src0_sel:DWORD src1_sel:WORD_1\n\t" \
  "v_mfma_f32_16x16x32_bf16 v[24:27], v[208:211], v[154:157], v[24:27]\n\t" \
  "ds_read_b32 v228, v228 offset:10016\n\t" \
  "v_lshlrev_b32_sdwa v232, 1, v202 dst_sel:DWORD dst_unused:UNUSED_PAD src0_sel:DWORD src1_sel:WORD_1\n\t" \
  "v_mfma_f32_16x16x32_bf16 v[48:51], v[208:211], v[162:165], v[48:51]\n\t" \
  "ds_read_b32 v231, v231 offset:10016\n\t" \
  "v_lshlrev_b32_sdwa v235, 1, v201 dst_sel:DWORD dst_unused:UNUSED_PAD src0_sel:DWORD src1_sel:WORD_1\n\t" \
  "v_lshlrev_b32_sdwa v234, 1, v200 dst_sel:DWORD dst_unused:UNUSED_PAD src0_sel:DWORD src1_sel:WORD_1\n\t" \
  "v_mfma_f32_16x16x32_bf16 v[72:75], v[208:211], v[170:173], v[72:75]\n\t" \
  "ds_read_b64_tr_b16 v[208:209], v249 offset:24672\n\t" \
  "ds_read_b64_tr_b16 v[210:211], v249 offset:26208\n\t" \
  "s_waitcnt lgkmcnt(13)\n\t" \
  "v_fma_mix_f32 v221, |v179|, v221, v221 op_sel:[0,1,0] op_sel_hi:[0,1,1] clamp\n\t" \
  "v_fma_mix_f32 v220, |v178|, v220, v220 op_sel:[0,1,0] op_sel_hi:[0,1,1] clamp\n\t" \
  "s_waitcnt lgkmcnt(11)\n\t" \
  "v_mfma_f32_16x16x32_bf16 v[4:7], v[212:215], v[146:149], v[4:7]\n\t" \
  "ds_read_b32 v230, v230 offset:10016\n\t" \
  "v_fma_mix_f32 v223, |v177|, v223, v223 op_sel:[0,1,0] op_sel_hi:[0,1,1] clamp\n\t" \
  "v_fma_mix_f32 v222, |v176|, v222, v222 op_sel:[0,1,0] op_sel_hi:[0,1,1] clamp\n\t" \
  "v_mfma_f32_16x16x32_bf16 v[28:31], v[212:215], v[154:157], v[28:31]\n\t" \
  "ds_read_u16_d16_hi v233, v233 offset:10528\n\t" \
  "s_waitcnt lgkmcnt(7)\n\t" \
  "v_or_b32 v187, v224, v225\n\t" \
  "v_mfma_f32_16x16x32_bf16 v[52:55], v[212:215], v[162:165], v[52:55]\n\t" \
  "ds_read_u16 v232, v232 offset:10528\n\t" \
  "v_or_b32 v186, v226, v227\n\t" \
  "v_fma_f32 v179, |v179|, v221, v179 mul:4\n\t" \
  "v_mfma_f32_16x16x32_bf16 v[76:79], v[212:215], v[170:173], v[76:79]\n\t" \
  "ds_read_b64_tr_b16 v[212:213], v249 offset:24728\n\t" \
  "v_fma_f32 v178, |v178|, v220, v178 mul:4\n\t" \
  "v_fma_f32 v177, |v177|, v223, v177 mul:4\n\t" \
  "v_mfma_f32_16x16x32_bf16 v[8:11], v[216:219], v[146:149], v[8:11]\n\t" \
  "ds_read_b64_tr_b16 v[214:215], v249 offset:26264\n\t" \
  "v_fma_f32 v176, |v176|, v222, v176 mul:4\n\t" \
  "v_mfma_f32_16x16x32_bf16 v[32:35], v[216:219], v[154:157], v[32:35]\n\t" \
  "ds_read_u16_d16_hi v235, v235 offset:10528\n\t" \
  "v_cvt_pk_bf16_f32 v179, v178, v179\n\t" \
  "v_cvt_pk_bf16_f32 v178, v176, v177\n\t" \
  "v_mfma_f32_16x16x32_bf16 v[56:59], v[216:219], v[162:165], v[56:59]\n\t" \
  "ds_read_u16 v234, v234 offset:10528\n\t" \
  "v_cvt_pknorm_i16_f32 v220, |v180|, |v181|\n\t" \
  "s_setprio 1\n\t" \
  "v_cvt_pknorm_i16_f32 v222, |v182|, |v183|\n\t" \
  "v_mfma_f32_16x16x32_bf16 v[80:83], v[216:219], v[170:173], v[80:83]\n\t" \
  "ds_read_b64_tr_b16 v[216:217], v249 offset:24736\n\t" \
  "v_lshlrev_b32_sdwa v221, 2, v220 dst_sel:DWORD dst_unused:UNUSED_PAD src0_sel:DWORD src1_sel:BYTE_3\n\t" \
  "v_lshlrev_b32_sdwa v220, 2, v220 dst_sel:DWORD dst_unused:UNUSED_PAD src0_sel:DWORD src1_sel:BYTE_1\n\t" \
  "s_waitcnt lgkmcnt(8)\n\t" \
  "v_mfma_f32_16x16x32_bf16 v[12:15], v[208:211], v[146:149], v[12:15]\n\t" \
  "ds_read_b64_tr_b16 v[218:219], v249 offset:26272\n\t" \
  "v_lshlrev_b32_sdwa v223, 2, v222 dst_sel:DWORD dst_unused:UNUSED_PAD src0_sel:DWORD src1_sel:BYTE_3\n\t" \
  "v_mfma_f32_16x16x32_bf16 v[36:39], v[208:211], v[154:157], v[36:39]\n\t" \
  "ds_read_b32 v221, v221 offset:10016\n\t" \
  "v_lshlrev_b32_sdwa v222, 2, v222 dst_sel:DWORD dst_unused:UNUSED_PAD src0_sel:DWORD src1_sel:BYTE_1\n\t" \
  "v_lshlrev_b32_sdwa v225, 1, v189 dst_sel:DWORD dst_unused:UNUSED_PAD src0_sel:DWORD src1_sel:WORD_1\n\t" \
  "v_mfma_f32_16x16x32_bf16 v[60:63], v[208:211], v[162:165], v[60:63]\n\t" \
  "ds_read_b32 v220, v220 offset:10016\n\t" \
  "v_lshlrev_b32_sdwa v224, 1, v188 dst_sel:DWORD dst_unused:UNUSED_PAD src0_sel:DWORD src1_sel:WORD_1\n\t" \
  "v_lshlrev_b32_sdwa v227, 1, v191 dst_sel:DWORD dst_unused:UNUSED_PAD src0_sel:DWORD src1_sel:WORD_1\n\t" \
  "v_mfma_f32_16x16x32_bf16 v[84:87], v[208:211], v[170:173], v[84:87]\n\t" \
  "ds_read_b64_tr_b16 v[208:209], v248 offset:36904\n\t" \
  "ds_read_b64_tr_b16 v[210:211], v248 offset:37928\n\t" \
  "v_lshlrev_b32_sdwa v226, 1, v190 dst_sel:DWORD dst_unused:UNUSED_PAD src0_sel:DWORD src1_sel:WORD_1\n\t" \
  "s_waitcnt lgkmcnt(8)\n\t" \
  "v_mfma_f32_16x16x32_bf16 v[16:19], v[212:215], v[146:149], v[16:19]\n\t" \
  "ds_read_b32 v223, v223 offset:10016\n\t" \
  "v_fma_mix_f32 v229, |v195|, v229, v229 op_sel:[0,1,0] op_sel_hi:[0,1,1] clamp\n\t" \
  "v_fma_mix_f32 v228, |v194|, v228, v228 op_sel:[0,1,0] op_sel_hi:[0,1,1] clamp\n\t" \
  "v_mfma_f32_16x16x32_bf16 v[40:43], v[212:215], v[154:157], v[40:43]\n\t" \
  "ds_read_b32 v222, v222 offset:10016\n\t" \
  "v_fma_mix_f32 v231, |v193|, v231, v231 op_sel:[0,1,0] op_sel_hi:[0,1,1] clamp\n\t" \
  "v_fma_mix_f32 v230, |v192|, v230, v230 op_sel:[0,1,0] op_sel_hi:[0,1,1] clamp\n\t" \
  "v_mfma_f32_16x16x32_bf16 v[64:67], v[212:215], v[162:165], v[64:67]\n\t" \
  "ds_read_u16_d16_hi v225, v225 offset:10528\n\t" \
  "s_waitcnt lgkmcnt(9)\n\t" \
  "v_or_b32 v203, v232, v233\n\t" \
  "v_or_b32 v202, v234, v235\n\t" \
  "v_mfma_f32_16x16x32_bf16 v[88:91], v[212:215], v[170:173], v[88:91]\n\t" \
  "ds_read_b64_tr_b16 v[212:213], v248 offset:39976\n\t" \
  "v_fma_f32 v195, |v195|, v229, v195 mul:4\n\t" \
  "s_waitcnt lgkmcnt(8)\n\t" \
  "v_mfma_f32_16x16x32_bf16 v[20:23], v[216:219], v[146:149], v[20:23]\n\t" \
  "ds_read_b64_tr_b16 v[214:215], v248 offset:41000\n\t" \
  "v_fma_f32 v194, |v194|, v228, v194 mul:4\n\t" \
  "v_fma_f32 v193, |v193|, v231, v193 mul:4\n\t" \
  "v_mfma_f32_16x16x32_bf16 v[44:47], v[216:219], v[154:157], v[44:47]\n\t" \
  "ds_read_u16 v224, v224 offset:10528\n\t" \
  "v_fma_f32 v192, |v192|, v230, v192 mul:4\n\t" \
  "v_cvt_pk_bf16_f32 v195, v194, v195\n\t" \
  "v_mfma_f32_16x16x32_bf16 v[68:71], v[216:219], v[162:165], v[68:71]\n\t" \
  "ds_read_u16_d16_hi v227, v227 offset:10528\n\t" \
  "v_cvt_pk_bf16_f32 v194, v192, v193\n\t" \
  "v_cvt_pknorm_i16_f32 v228, |v196|, |v197|\n\t" \
  "v_mfma_f32_16x16x32_bf16 v[92:95], v[216:219], v[170:173], v[92:95]\n\t" \
  "ds_read_b64_tr_b16 v[216:217], v248 offset:38952\n\t" \
  "v_cvt_pknorm_i16_f32 v230, |v198|, |v199|\n\t" \
  "s_setprio 2\n\t" \
  "s_waitcnt lgkmcnt(8)\n\t" \
  "v_mfma_f32_16x16x32_bf16 v[144:147], v[208:211], v[96:99], 0\n\t" \
  "ds_read_b64_tr_b16 v[218:219], v248 offset:42024\n\t" \
  "v_lshlrev_b32_sdwa v229, 2, v228 dst_sel:DWORD dst_unused:UNUSED_PAD src0_sel:DWORD src1_sel:BYTE_3\n\t" \
  "v_lshlrev_b32_sdwa v228, 2, v228 dst_sel:DWORD dst_unused:UNUSED_PAD src0_sel:DWORD src1_sel:BYTE_1\n\t" \
  "v_mfma_f32_16x16x32_bf16 v[152:155], v[208:211], v[100:103], 0\n\t" \
  "ds_read_u16 v226, v226 offset:10528\n\t" \
  "v_lshlrev_b32_sdwa v231, 2, v230 dst_sel:DWORD dst_unused:UNUSED_PAD src0_sel:DWORD src1_sel:BYTE_3\n\t" \
  "v_lshlrev_b32_sdwa v230, 2, v230 dst_sel:DWORD dst_unused:UNUSED_PAD src0_sel:DWORD src1_sel:BYTE_1\n\t" \
  "v_mfma_f32_16x16x32_bf16 v[160:163], v[208:211], v[104:107], 0\n\t" \
  "ds_read_b32 v229, v229 offset:10016\n\t" \
  "v_lshlrev_b32_sdwa v233, 1, v205 dst_sel:DWORD dst_unused:UNUSED_PAD src0_sel:DWORD src1_sel:WORD_1\n\t" \
  "v_mfma_f32_16x16x32_bf16 v[168:171], v[208:211], v[108:111], 0\n\t" \
  "ds_read_b64_tr_b16 v[208:209], v248 offset:37416\n\t" \
  "v_lshlrev_b32_sdwa v232, 1, v204 dst_sel:DWORD dst_unused:UNUSED_PAD src0_sel:DWORD src1_sel:WORD_1\n\t" \
  "v_lshlrev_b32_sdwa v235, 1, v207 dst_sel:DWORD dst_unused:UNUSED_PAD src0_sel:DWORD src1_sel:WORD_1\n\t" \
  "s_waitcnt lgkmcnt(7)\n\t" \
  "v_mfma_f32_16x16x32_bf16 v[144:147], v[212:215], v[112:115], v[144:147]\n\t" \
  "ds_read_b64_tr_b16 v[210:211], v248 offset:38440\n\t" \
  "ds_read_b32 v228, v228 offset:10016\n\t" \
  "v_lshlrev_b32_sdwa v234, 1, v206 dst_sel:DWORD dst_unused:UNUSED_PAD src0_sel:DWORD src1_sel:WORD_1\n\t" \
  "v_fma_mix_f32 v221, |v181|, v221, v221 op_sel:[0,1,0] op_sel_hi:[0,1,1] clamp\n\t" \
  "v_mfma_f32_16x16x32_bf16 v[152:155], v[212:215], v[116:119], v[152:155]\n\t" \
  "ds_read_b32 v231, v231 offset:10016\n\t" \
  "ds_read_b32 v230, v230 offset:10016\n\t" \
  "v_fma_mix_f32 v220, |v180|, v220, v220 op_sel:[0,1,0] op_sel_hi:[0,1,1] clamp\n\t" \
  "v_fma_mix_f32 v223, |v183|, v223, v223 op_sel:[0,1,0] op_sel_hi:[0,1,1] clamp\n\t" \
  "v_mfma_f32_16x16x32_bf16 v[160:163], v[212:215], v[120:123], v[160:163]\n\t" \
  "ds_read_u16_d16_hi v233, v233 offset:10528\n\t" \
  "v_fma_mix_f32 v222, |v182|, v222, v222 op_sel:[0,1,0] op_sel_hi:[0,1,1] clamp\n\t" \
  "v_mfma_f32_16x16x32_bf16 v[168:171], v[212:215], v[124:127], v[168:171]\n\t" \
  "ds_read_b64_tr_b16 v[212:213], v248 offset:40488\n\t" \
  "s_waitcnt lgkmcnt(8)\n\t" \
  "v_or_b32 v188, v224, v225\n\t" \
  "v_or_b32 v189, v226, v227\n\t" \
  "v_mfma_f32_16x16x32_bf16 v[144:147], v[216:219], v[128:131], v[144:147]\n\t" \
  "ds_read_b64_tr_b16 v[214:215], v248 offset:41512\n\t" \
  "v_fma_f32 v181, |v181|, v221, v181 mul:4\n\t" \
  "v_fma_f32 v180, |v180|, v220, v180 mul:4\n\t" \
  "v_mfma_f32_16x16x32_bf16 v[152:155], v[216:219], v[132:135], v[152:155]\n\t" \
  "ds_read_u16 v232, v232 offset:10528\n\t" \
  "v_fma_f32 v183, |v183|, v223, v183 mul:4\n\t" \
  "v_mfma_f32_16x16x32_bf16 v[160:163], v[216:219], v[136:139], v[160:163]\n\t" \
  "ds_read_u16_d16_hi v235, v235 offset:10528\n\t" \
  "v_fma_f32 v182, |v182|, v222, v182 mul:4\n\t" \
  "v_cvt_pk_bf16_f32 v180, v180, v181\n\t" \
  "v_mfma_f32_16x16x32_bf16 v[168:171], v[216:219], v[140:143], v[168:171]\n\t" \
  "ds_read_b64_tr_b16 v[216:217], v248 offset:39464\n\t" \
  "v_cvt_pk_bf16_f32 v181, v182, v183\n\t" \
  "v_cvt_pknorm_i16_f32 v220, |v146|, |v147|\n\t" \
  "s_waitcnt lgkmcnt(9)\n\t" \
  "v_mfma_f32_16x16x32_bf16 v[148:151], v[208:211], v[96:99], 0\n\t" \
  "ds_read_b64_tr_b16 v[218:219], v248 offset:42536\n\t" \
  "v_cvt_pknorm_i16_f32 v222, |v144|, |v145|\n\t" \
  "s_setprio 3\n\t" \
  "v_lshlrev_b32_sdwa v221, 2, v220 dst_sel:DWORD dst_unused:UNUSED_PAD src0_sel:DWORD src1_sel:BYTE_3\n\t" \
  "v_mfma_f32_16x16x32_bf16 v[156:159], v[208:211], v[100:103], 0\n\t" \
  "s_waitcnt vmcnt(1)\n\t" \
  "s_cmp_lg_u32 s58, 1\n\t" \
  "s_cbranch_scc1 .Lcnx_c13\n\t" \
  "s_waitcnt vmcnt(0)\n\t" \
  ".Lcnx_c13:\n\t" \
  "s_mov_b64 exec, 3\n\t" \
  "ds_add_rtn_u32 v246, v251, v250 offset:47184\n\t" \
  "s_mov_b64 exec, -1\n\t" \
  "ds_read_u16 v234, v234 offset:10528\n\t" \
  "v_lshlrev_b32_sdwa v220, 2, v220 dst_sel:DWORD dst_unused:UNUSED_PAD src0_sel:DWORD src1_sel:BYTE_1\n\t" \
  "v_mfma_f32_16x16x32_bf16 v[164:167], v[208:211], v[104:107], 0\n\t" \
  "ds_read_b32 v221, v221 offset:10016\n\t" \
  "v_lshlrev_b32_sdwa v223, 2, v222 dst_sel:DWORD dst_unused:UNUSED_PAD src0_sel:DWORD src1_sel:BYTE_3\n\t" \
  "v_lshlrev_b32_sdwa v222, 2, v222 dst_sel:DWORD dst_unused:UNUSED_PAD src0_sel:DWORD src1_sel:BYTE_1\n\t" \
  "v_mfma_f32_16x16x32_bf16 v[172:175], v[208:211], v[108:111], 0\n\t" \
  "s_waitcnt lgkmcnt(2)\n\t" \
  "v_readfirstlane_b32 s4, v246\n\t" \
  "s_cmp_lt_u32 s4, s57\n\t" \
  "s_cbranch_scc1 .Lcnx_w72\n.Lcnx_b72:\n\t" \
  "ds_read_b64_tr_b16 v[208:209], v249 offset:30752\n\t" \
  "v_lshlrev_b32_sdwa v225, 1, v155 dst_sel:DWORD dst_unused:UNUSED_PAD src0_sel:DWORD src1_sel:WORD_1\n\t" \
  "v_lshlrev_b32_sdwa v224, 1, v154 dst_sel:DWORD dst_unused:UNUSED_PAD src0_sel:DWORD src1_sel:WORD_1\n\t" \
  "v_mfma_f32_16x16x32_bf16 v[148:151], v[212:215], v[112:115], v[148:151]\n\t" \
  "ds_read_b64_tr_b16 v[210:211], v249 offset:32288\n\t" \
  "v_lshlrev_b32_sdwa v227, 1, v153 dst_sel:DWORD dst_unused:UNUSED_PAD src0_sel:DWORD src1_sel:WORD_1\n\t" \
  "v_mfma_f32_16x16x32_bf16 v[156:159], v[212:215], v[116:119], v[156:159]\n\t" \
  "s_cmp_eq_u32 s37, 0\n\t" \
  "s_cbranch_scc1 .Lcnx_f7\n.Lcnx_g7:\n\t" \
  "ds_read_b32 v220, v220 offset:10016\n\t" \
  "v_lshlrev_b32_sdwa v226, 1, v152 dst_sel:DWORD dst_unused:UNUSED_PAD src0_sel:DWORD src1_sel:WORD_1\n\t" \
  "v_fma_mix_f32 v229, |v197|, v229, v229 op_sel:[0,1,0] op_sel_hi:[0,1,1] clamp\n\t" \
  "v_mfma_f32_16x16x32_bf16 v[164:167], v[212:215], v[120:123], v[164:167]\n\t" \
  "ds_read_b32 v223, v223 offset:10016\n\t" \
  "ds_read_b32 v222, v222 offset:10016\n\t" \
  "v_fma_mix_f32 v228, |v196|, v228, v228 op_sel:[0,1,0] op_sel_hi:[0,1,1] clamp\n\t" \
  "v_fma_mix_f32 v231, |v199|, v231, v231 op_sel:[0,1,0] op_sel_hi:[0,1,1] clamp\n\t" \
  "v_mfma_f32_16x16x32_bf16 v[172:175], v[212:215], v[124:127], v[172:175]\n\t" \
  "ds_read_b64_tr_b16 v[212:213], v249 offset:30760\n\t" \
  "ds_read_b64_tr_b16 v[214:215], v249 offset:32296\n\t" \
  "v_fma_mix_f32 v230, |v198|, v230, v230 op_sel:[0,1,0] op_sel_hi:[0,1,1] clamp\n\t" \
  "s_waitcnt lgkmcnt(8)\n\t" \
  "v_or_b32 v204, v232, v233\n\t" \
  "v_mfma_f32_16x16x32_bf16 v[148:151], v[216:219], v[128:131], v[148:151]\n\t" \
  "ds_read_u16_d16_hi v225, v225 offset:10528\n\t" \
  "v_or_b32 v205, v234, v235\n\t" \
  "v_mfma_f32_16x16x32_bf16 v[156:159], v[216:219], v[132:135], v[156:159]\n\t" \
  "ds_read_u16 v224, v224 offset:10528\n\t" \
  "v_fma_f32 v197, |v197|, v229, v197 mul:4\n\t" \
  "v_fma_f32 v196, |v196|, v228, v196 mul:4\n\t" \
  "v_mfma_f32_16x16x32_bf16 v[164:167], v[216:219], v[136:139], v[164:167]\n\t" \
  "ds_read_u16_d16_hi v227, v227 offset:10528\n\t" \
  "v_fma_f32 v199, |v199|, v231, v199 mul:4\n\t" \
  "v_fma_f32 v198, |v198|, v230, v198 mul:4\n\t" \
  "v_mfma_f32_16x16x32_bf16 v[172:175], v[216:219], v[140:143], v[172:175]\n\t" \
  "ds_read_b64_tr_b16 v[216:217], v249 offset:30816\n\t" \
  "v_cvt_pk_bf16_f32 v196, v196, v197\n\t" \
  "v_cvt_pk_bf16_f32 v197, v198, v199\n\t" \
  "s_setprio 0\n\t" \
  "v_cvt_pknorm_i16_f32 v228, |v162|, |v163|\n\t" \
  "v_cvt_pknorm_i16_f32 v230, |v160|, |v161|\n\t" \
  "v_mfma_f32_16x16x32_bf16 v[40:43], v[242:245], v[236:239], v[40:43]\n\t" \
  "ds_read_b64_tr_b16 v[218:219], v249 offset:32352\n\t" \
  "v_lshlrev_b32_sdwa v229, 2, v228 dst_sel:DWORD dst_unused:UNUSED_PAD src0_sel:DWORD src1_sel:BYTE_3\n\t" \
  "v_mfma_f32_16x16x32_bf16 v[44:47], v[240:243], v[236:239], v[44:47]\n\t" \
  "buffer_load_dwordx4 v[236:239], v250, s[8:11], s12 offen offset:0 nt\n\t" \
  "s_cmp_lg_u32 s58, 0\n\t" \
  "s_cbranch_scc1 .Lcnx_c26\n\t" \
  "s_add_u32 m0, s25, 41000\n\t" \
  "s_add_u32 s24, s24, s26\n\t" \
  "buffer_load_dwordx4 v254, s[8:11], s24 offen lds\n\t" \
  "s_cmp_lg_u32 s38, 4\n\t" \
  "s_cbranch_scc1 .Lcnx_c26\n\t" \
  "s_mov_b32 m0, 107744\n\t" \
  "s_add_u32 s30, s30, s31\n\t" \
  "buffer_load_dword v252, s[8:11], s30 offen lds\n\t" \
  ".Lcnx_c26:\n\t" \
  "ds_read_u16 v226, v226 offset:10528\n\t" \
  "v_lshlrev_b32_sdwa v228, 2, v228 dst_sel:DWORD dst_unused:UNUSED_PAD src0_sel:DWORD src1_sel:BYTE_1\n\t" \
  "v_lshlrev_b32_sdwa v231, 2, v230 dst_sel:DWORD dst_unused:UNUSED_PAD src0_sel:DWORD src1_sel:BYTE_3\n\t" \
  "s_waitcnt lgkmcnt(11)\n\t" \
  "v_mfma_f32_16x16x32_bf16 v[0:3], v[208:211], v[178:181], v[0:3]\n\t" \
  "ds_read_b32 v229, v229 offset:10016\n\t" \
  "v_lshlrev_b32_sdwa v230, 2, v230 dst_sel:DWORD dst_unused:UNUSED_PAD src0_sel:DWORD src1_sel:BYTE_1\n\t" \
  "v_lshlrev_b32_sdwa v233, 1, v171 dst_sel:DWORD dst_unused:UNUSED_PAD src0_sel:DWORD src1_sel:WORD_1\n\t" \
  "v_mfma_f32_16x16x32_bf16 v[24:27], v[208:211], v[186:189], v[24:27]\n\t" \
  "ds_read_b32 v228, v228 offset:10016\n\t" \
  "v_lshlrev_b32_sdwa v232, 1, v170 dst_sel:DWORD dst_unused:UNUSED_PAD src0_sel:DWORD src1_sel:WORD_1\n\t" \
  "v_mfma_f32_16x16x32_bf16 v[48:51], v[208:211], v[194:197], v[48:51]\n\t" \
  "ds_read_b32 v231, v231 offset:10016\n\t" \
  "v_lshlrev_b32_sdwa v235, 1, v169 dst_sel:DWORD dst_unused:UNUSED_PAD src0_sel:DWORD src1_sel:WORD_1\n\t" \
  "v_lshlrev_b32_sdwa v234, 1, v168 dst_sel:DWORD dst_unused:UNUSED_PAD src0_sel:DWORD src1_sel:WORD_1\n\t" \
  "v_mfma_f32_16x16x32_bf16 v[72:75], v[208:211], v[202:205], v[72:75]\n\t" \
  "ds_read_b64_tr_b16 v[208:209], v249 offset:30824\n\t" \
  "ds_read_b64_tr_b16 v[210:211], v249 offset:32360\n\t" \
  "s_waitcnt lgkmcnt(13)\n\t" \
  "v_fma_mix_f32 v221, |v147|, v221, v221 op_sel:[0,1,0] op_sel_hi:[0,1,1] clamp\n\t" \
  "v_fma_mix_f32 v220, |v146|, v220, v220 op_sel:[0,1,0] op_sel_hi:[0,1,1] clamp\n\t" \
  "s_waitcnt lgkmcnt(11)\n\t" \
  "v_mfma_f32_16x16x32_bf16 v[4:7], v[212:215], v[178:181], v[4:7]\n\t" \
  "ds_read_b32 v230, v230 offset:10016\n\t" \
  "v_fma_mix_f32 v223, |v145|, v223, v223 op_sel:[0,1,0] op_sel_hi:[0,1,1] clamp\n\t" \
  "v_fma_mix_f32 v222, |v144|, v222, v222 op_sel:[0,1,0] op_sel_hi:[0,1,1] clamp\n\t" \
  "v_mfma_f32_16x16x32_bf16 v[28:31], v[212:215], v[186:189], v[28:31]\n\t" \
  "ds_read_u16_d16_hi v233, v233 offset:10528\n\t" \
  "s_waitcnt lgkmcnt(7)\n\t" \
  "v_or_b32 v155, v224, v225\n\t" \
  "v_mfma_f32_16x16x32_bf16 v[52:55], v[212:215], v[194:197], v[52:55]\n\t" \
  "ds_read_u16 v232, v232 offset:10528\n\t" \
  "v_or_b32 v154, v226, v227\n\t" \
  "v_fma_f32 v147, |v147|, v221, v147 mul:4\n\t" \
  "v_mfma_f32_16x16x32_bf16 v[76:79], v[212:215], v[202:205], v[76:79]\n\t" \
  "ds_read_b64_tr_b16 v[212:213], v249 offset:30880\n\t" \
  "v_fma_f32 v146, |v146|, v220, v146 mul:4\n\t" \
  "v_fma_f32 v145, |v145|, v223, v145 mul:4\n\t" \
  "v_mfma_f32_16x16x32_bf16 v[8:11], v[216:219], v[178:181], v[8:11]\n\t" \
  "ds_read_b64_tr_b16 v[214:215], v249 offset:32416\n\t" \
  "v_fma_f32 v144, |v144|, v222, v144 mul:4\n\t" \
  "v_mfma_f32_16x16x32_bf16 v[32:35], v[216:219], v[186:189], v[32:35]\n\t" \
  "ds_read_u16_d16_hi v235, v235 offset:10528\n\t" \
  "v_cvt_pk_bf16_f32 v147, v146, v147\n\t" \
  "v_cvt_pk_bf16_f32 v146, v144, v145\n\t" \
  "v_mfma_f32_16x16x32_bf16 v[56:59], v[216:219], v[194:197], v[56:59]\n\t" \
  "ds_read_u16 v234, v234 offset:10528\n\t" \
  "v_cvt_pknorm_i16_f32 v220, |v148|, |v149|\n\t" \
  "s_setprio 1\n\t" \
  "v_cvt_pknorm_i16_f32 v222, |v150|, |v151|\n\t" \
  "v_mfma_f32_16x16x32_bf16 v[80:83], v[216:219], v[202:205], v[80:83]\n\t" \
  "ds_read_b64_tr_b16 v[216:217], v249 offset:30888\n\t" \
  "v_lshlrev_b32_sdwa v221, 2, v220 dst_sel:DWORD dst_unused:UNUSED_PAD src0_sel:DWORD src1_sel:BYTE_3\n\t" \
  "v_lshlrev_b32_sdwa v220, 2, v220 dst_sel:DWORD dst_unused:UNUSED_PAD src0_sel:DWORD src1_sel:BYTE_1\n\t" \
  "s_waitcnt lgkmcnt(8)\n\t" \
  "v_mfma_f32_16x16x32_bf16 v[12:15], v[208:211], v[178:181], v[12:15]\n\t" \
  "ds_read_b64_tr_b16 v[218:219], v249 offset:32424\n\t" \
  "v_lshlrev_b32_sdwa v223, 2, v222 dst_sel:DWORD dst_unused:UNUSED_PAD src0_sel:DWORD src1_sel:BYTE_3\n\t" \
  "v_mfma_f32_16x16x32_bf16 v[36:39], v[208:211], v[186:189], v[36:39]\n\t" \
  "ds_read_b32 v221, v221 offset:10016\n\t" \
  "v_lshlrev_b32_sdwa v222, 2, v222 dst_sel:DWORD dst_unused:UNUSED_PAD src0_sel:DWORD src1_sel:BYTE_1\n\t" \
  "v_lshlrev_b32_sdwa v225, 1, v157 dst_sel:DWORD dst_unused:UNUSED_PAD src0_sel:DWORD src1_sel:WORD_1\n\t" \
  "v_mfma_f32_16x16x32_bf16 v[60:63], v[208:211], v[194:197], v[60:63]\n\t" \
  "ds_read_b32 v220, v220 offset:10016\n\t" \
  "v_lshlrev_b32_sdwa v224, 1, v156 dst_sel:DWORD dst_unused:UNUSED_PAD src0_sel:DWORD src1_sel:WORD_1\n\t" \
  "v_lshlrev_b32_sdwa v227, 1, v159 dst_sel:DWORD dst_unused:UNUSED_PAD src0_sel:DWORD src1_sel:WORD_1\n\t" \
  "v_mfma_f32_16x16x32_bf16 v[84:87], v[208:211], v[202:205], v[84:87]\n\t" \
  "ds_read_b64_tr_b16 v[208:209], v248 offset:0\n\t" \
  "ds_read_b64_tr_b16 v[210:211], v248 offset:1024\n\t" \
  "v_lshlrev_b32_sdwa v226, 1, v158 dst_sel:DWORD dst_unused:UNUSED_PAD src0_sel:DWORD src1_sel:WORD_1\n\t" \
  "s_waitcnt lgkmcnt(8)\n\t" \
  "v_mfma_f32_16x16x32_bf16 v[16:19], v[212:215], v[178:181], v[16:19]\n\t" \
  "ds_read_b32 v223, v223 offset:10016\n\t" \
  "v_fma_mix_f32 v229, |v163|, v229, v229 op_sel:[0,1,0] op_sel_hi:[0,1,1] clamp\n\t" \
  "v_fma_mix_f32 v228, |v162|, v228, v228 op_sel:[0,1,0] op_sel_hi:[0,1,1] clamp\n\t" \
  "v_mfma_f32_16x16x32_bf16 v[40:43], v[212:215], v[186:189], v[40:43]\n\t" \
  "ds_read_b32 v222, v222 offset:10016\n\t" \
  "v_fma_mix_f32 v231, |v161|, v231, v231 op_sel:[0,1,0] op_sel_hi:[0,1,1] clamp\n\t" \
  "v_fma_mix_f32 v230, |v160|, v230, v230 op_sel:[0,1,0] op_sel_hi:[0,1,1] clamp\n\t" \
  "v_mfma_f32_16x16x32_bf16 v[64:67], v[212:215], v[194:197], v[64:67]\n\t" \
  "ds_read_u16_d16_hi v225, v225 offset:10528\n\t" \
  "s_waitcnt lgkmcnt(9)\n\t" \
  "v_or_b32 v171, v232, v233\n\t" \
  "v_or_b32 v170, v234, v235\n\t" \
  "v_mfma_f32_16x16x32_bf16 v[88:91], v[212:215], v[202:205], v[88:91]\n\t" \
  "ds_read_b64_tr_b16 v[212:213], v248 offset:3072\n\t" \
  "v_fma_f32 v163, |v163|, v229, v163 mul:4\n\t" \
  "s_waitcnt lgkmcnt(8)\n\t" \
  "v_mfma_f32_16x16x32_bf16 v[20:23], v[216:219], v[178:181], v[20:23]\n\t" \
  "ds_read_b64_tr_b16 v[214:215], v248 offset:4096\n\t" \
  "v_fma_f32 v162, |v162|, v228, v162 mul:4\n\t" \
  "v_fma_f32 v161, |v161|, v231, v161 mul:4\n\t" \
  "v_mfma_f32_16x16x32_bf16 v[44:47], v[216:219], v[186:189], v[44:47]\n\t" \
  "ds_read_u16 v224, v224 offset:10528\n\t" \
  "v_fma_f32 v160, |v160|, v230, v160 mul:4\n\t" \
  "v_cvt_pk_bf16_f32 v163, v162, v163\n\t" \
  "v_mfma_f32_16x16x32_bf16 v[68:71], v[216:219], v[194:197], v[68:71]\n\t" \
  "ds_read_u16_d16_hi v227, v227 offset:10528\n\t" \
  "v_cvt_pk_bf16_f32 v162, v160, v161\n\t" \
  "v_cvt_pknorm_i16_f32 v228, |v164|, |v165|\n\t" \
  "v_mfma_f32_16x16x32_bf16 v[92:95], v[216:219], v[202:205], v[92:95]\n\t" \
  "ds_read_b64_tr_b16 v[216:217], v248 offset:2048\n\t" \
  "v_cvt_pknorm_i16_f32 v230, |v166|, |v167|\n\t" \
  "s_setprio 2\n\t" \
  "s_waitcnt lgkmcnt(8)\n\t" \
  "v_mfma_f32_16x16x32_bf16 v[176:179], v[208:211], v[96:99], 0\n\t" \
  "ds_read_b64_tr_b16 v[218:219], v248 offset:5120\n\t" \
  "v_lshlrev_b32_sdwa v229, 2, v228 dst_sel:DWORD dst_unused:UNUSED_PAD src0_sel:DWORD src1_sel:BYTE_3\n\t" \
  "v_lshlrev_b32_sdwa v228, 2, v228 dst_sel:DWORD dst_unused:UNUSED_PAD src0_sel:DWORD src1_sel:BYTE_1\n\t" \
  "v_mfma_f32_16x16x32_bf16 v[184:187], v[208:211], v[100:103], 0\n\t" \
  "ds_read_u16 v226, v226 offset:10528\n\t" \
  "v_lshlrev_b32_sdwa v231, 2, v230 dst_sel:DWORD dst_unused:UNUSED_PAD src0_sel:DWORD src1_sel:BYTE_3\n\t" \
  "v_lshlrev_b32_sdwa v230, 2, v230 dst_sel:DWORD dst_unused:UNUSED_PAD src0_sel:DWORD src1_sel:BYTE_1\n\t" \
  "v_mfma_f32_16x16x32_bf16 v[192:195], v[208:211], v[104:107], 0\n\t" \
  "ds_read_b32 v229, v229 offset:10016\n\t" \
  "v_lshlrev_b32_sdwa v233, 1, v173 dst_sel:DWORD dst_unused:UNUSED_PAD src0_sel:DWORD src1_sel:WORD_1\n\t" \
  "v_mfma_f32_16x16x32_bf16 v[200:203], v[208:211], v[108:111], 0\n\t" \
  "ds_read_b64_tr_b16 v[208:209], v248 offset:512\n\t" \
  "v_lshlrev_b32_sdwa v232, 1, v172 dst_sel:DWORD dst_unused:UNUSED_PAD src0_sel:DWORD src1_sel:WORD_1\n\t" \
  "v_lshlrev_b32_sdwa v235, 1, v175 dst_sel:DWORD dst_unused:UNUSED_PAD src0_sel:DWORD src1_sel:WORD_1\n\t" \
  "s_waitcnt lgkmcnt(7)\n\t" \
  "v_mfma_f32_16x16x32_bf16 v[176:179], v[212:215], v[112:115], v[176:179]\n\t" \
  "ds_read_b64_tr_b16 v[210:211], v248 offset:1536\n\t" \
  "ds_read_b32 v228, v228 offset:10016\n\t" \
  "v_lshlrev_b32_sdwa v234, 1, v174 dst_sel:DWORD dst_unused:UNUSED_PAD src0_sel:DWORD src1_sel:WORD_1\n\t" \
  "v_fma_mix_f32 v221, |v149|, v221, v221 op_sel:[0,1,0] op_sel_hi:[0,1,1] clamp\n\t" \
  "v_mfma_f32_16x16x32_bf16 v[184:187], v[212:215], v[116:119], v[184:187]\n\t" \
  "ds_read_b32 v231, v231 offset:10016\n\t" \
  "ds_read_b32 v230, v230 offset:10016\n\t" \
  "v_fma_mix_f32 v220, |v148|, v220, v220 op_sel:[0,1,0] op_sel_hi:[0,1,1] clamp\n\t" \
  "v_fma_mix_f32 v223, |v151|, v223, v223 op_sel:[0,1,0] op_sel_hi:[0,1,1] clamp\n\t" \
  "v_mfma_f32_16x16x32_bf16 v[192:195], v[212:215], v[120:123], v[192:195]\n\t" \
  "ds_read_u16_d16_hi v233, v233 offset:10528\n\t" \
  "v_fma_mix_f32 v222, |v150|, v222, v222 op_sel:[0,1,0] op_sel_hi:[0,1,1] clamp\n\t" \
  "v_mfma_f32_16x16x32_bf16 v[200:203], v[212:215], v[124:127], v[200:203]\n\t" \
  "ds_read_b64_tr_b16 v[212:213], v248 offset:3584\n\t" \
  "s_waitcnt lgkmcnt(8)\n\t" \
  "v_or_b32 v156, v224, v225\n\t" \
  "v_or_b32 v157, v226, v227\n\t" \
  "v_mfma_f32_16x16x32_bf16 v[176:179], v[216:219], v[128:131], v[176:179]\n\t" \
  "ds_read_b64_tr_b16 v[214:215], v248 offset:4608\n\t" \
  "v_fma_f32 v149, |v149|, v221, v149 mul:4\n\t" \
  "v_fma_f32 v148, |v148|, v220, v148 mul:4\n\t" \
  "v_mfma_f32_16x16x32_bf16 v[184:187], v[216:219], v[132:135], v[184:187]\n\t" \
  "ds_read_u16 v232, v232 offset:10528\n\t" \
  "v_fma_f32 v151, |v151|, v223, v151 mul:4\n\t" \
  "v_mfma_f32_16x16x32_bf16 v[192:195], v[216:219], v[136:139], v[192:195]\n\t" \
  "ds_read_u16_d16_hi v235, v235 offset:10528\n\t" \
  "v_fma_f32 v150, |v150|, v222, v150 mul:4\n\t" \
  "v_cvt_pk_bf16_f32 v148, v148, v149\n\t" \
  "v_mfma_f32_16x16x32_bf16 v[200:203], v[216:219], v[140:143], v[200:203]\n\t" \
  "ds_read_b64_tr_b16 v[216:217], v248 offset:2560\n\t" \
  "v_cvt_pk_bf16_f32 v149, v150, v151\n\t" \
  "v_cvt_pknorm_i16_f32 v220, |v178|, |v179|\n\t" \
  "s_waitcnt lgkmcnt(9)\n\t" \
  "v_mfma_f32_16x16x32_bf16 v[180:183], v[208:211], v[96:99], 0\n\t" \
  "ds_read_b64_tr_b16 v[218:219], v248 offset:5632\n\t" \
  "v_cvt_pknorm_i16_f32 v222, |v176|, |v177|\n\t" \
  "s_setprio 3\n\t" \
  "v_lshlrev_b32_sdwa v221, 2, v220 dst_sel:DWORD dst_unused:UNUSED_PAD src0_sel:DWORD src1_sel:BYTE_3\n\t" \
  "v_mfma_f32_16x16x32_bf16 v[188:191], v[208:211], v[100:103], 0\n\t" \
  "s_waitcnt vmcnt(1)\n\t" \
  "s_cmp_lg_u32 s58, 1\n\t" \
  "s_cbranch_scc1 .Lcnx_c14\n\t" \
  "s_waitcnt vmcnt(0)\n\t" \
  ".Lcnx_c14:\n\t" \
  "s_mov_b64 exec, 3\n\t" \
  "ds_add_rtn_u32 v246, v251, v250 offset:47192\n\t" \
  "s_mov_b64 exec, -1\n\t" \
  "ds_read_u16 v234, v234 offset:10528\n\t" \
  "v_lshlrev_b32_sdwa v220, 2, v220 dst_sel:DWORD dst_unused:UNUSED_PAD src0_sel:DWORD src1_sel:BYTE_1\n\t" \
  "v_mfma_f32_16x16x32_bf16 v[196:199], v[208:211], v[104:107], 0\n\t" \
  "ds_read_b32 v221, v221 offset:10016\n\t" \
  "v_lshlrev_b32_sdwa v223, 2, v222 dst_sel:DWORD dst_unused:UNUSED_PAD src0_sel:DWORD src1_sel:BYTE_3\n\t" \
  "v_lshlrev_b32_sdwa v222, 2, v222 dst_sel:DWORD dst_unused:UNUSED_PAD src0_sel:DWORD src1_sel:BYTE_1\n\t" \
  "v_mfma_f32_16x16x32_bf16 v[204:207], v[208:211], v[108:111], 0\n\t" \
  "s_waitcnt lgkmcnt(2)\n\t" \
  "v_readfirstlane_b32 s4, v246\n\t" \
  "s_cmp_lt_u32 s4, s57\n\t" \
  "s_cbranch_scc1 .Lcnx_w84\n.Lcnx_b84:\n\t" \
  "s_or_b32 s37, s37, s58\n\t" \
  "ds_read_b64_tr_b16 v[208:209], v249 offset:6152\n\t" \
  "v_lshlrev_b32_sdwa v225, 1, v187 dst_sel:DWORD dst_unused:UNUSED_PAD src0_sel:DWORD src1_sel:WORD_1\n\t" \
  "v_lshlrev_b32_sdwa v224, 1, v186 dst_sel:DWORD dst_unused:UNUSED_PAD src0_sel:DWORD src1_sel:WORD_1\n\t" \
  "v_mfma_f32_16x16x32_bf16 v[180:183], v[212:215], v[112:115], v[180:183]\n\t" \
  "ds_read_b64_tr_b16 v[210:211], v249 offset:7688\n\t" \
  "v_lshlrev_b32_sdwa v227, 1, v185 dst_sel:DWORD dst_unused:UNUSED_PAD src0_sel:DWORD src1_sel:WORD_1\n\t" \
  "v_mfma_f32_16x16x32_bf16 v[188:191], v[212:215], v[116:119], v[188:191]\n\t" \
  "s_cmp_eq_u32 s37, 0\n\t" \
  "s_cbranch_scc1 .Lcnx_f8\n.Lcnx_g8:\n\t" \
  "ds_read_b32 v220, v220 offset:10016\n\t" \
  "v_lshlrev_b32_sdwa v226, 1, v184 dst_sel:DWORD dst_unused:UNUSED_PAD src0_sel:DWORD src1_sel:WORD_1\n\t" \
  "v_fma_mix_f32 v229, |v165|, v229, v229 op_sel:[0,1,0] op_sel_hi:[0,1,1] clamp\n\t" \
  "v_mfma_f32_16x16x32_bf16 v[196:199], v[212:215], v[120:123], v[196:199]\n\t" \
  "ds_read_b32 v223, v223 offset:10016\n\t" \
  "ds_read_b32 v222, v222 offset:10016\n\t" \
  "v_fma_mix_f32 v228, |v164|, v228, v228 op_sel:[0,1,0] op_sel_hi:[0,1,1] clamp\n\t" \
  "v_fma_mix_f32 v231, |v167|, v231, v231 op_sel:[0,1,0] op_sel_hi:[0,1,1] clamp\n\t" \
  "v_mfma_f32_16x16x32_bf16 v[204:207], v[212:215], v[124:127], v[204:207]\n\t" \
  "ds_read_b64_tr_b16 v[212:213], v249 offset:6160\n\t" \
  "ds_read_b64_tr_b16 v[214:215], v249 offset:7696\n\t" \
  "v_fma_mix_f32 v230, |v166|, v230, v230 op_sel:[0,1,0] op_sel_hi:[0,1,1] clamp\n\t" \
  "s_waitcnt lgkmcnt(8)\n\t" \
  "v_or_b32 v172, v232, v233\n\t" \
  "v_mfma_f32_16x16x32_bf16 v[180:183], v[216:219], v[128:131], v[180:183]\n\t" \
  "ds_read_u16_d16_hi v225, v225 offset:10528\n\t" \
  "v_or_b32 v173, v234, v235\n\t" \
  "v_mfma_f32_16x16x32_bf16 v[188:191], v[216:219], v[132:135], v[188:191]\n\t" \
  "ds_read_u16 v224, v224 offset:10528\n\t" \
  "v_fma_f32 v165, |v165|, v229, v165 mul:4\n\t" \
  "v_fma_f32 v164, |v164|, v228, v164 mul:4\n\t" \
  "v_mfma_f32_16x16x32_bf16 v[196:199], v[216:219], v[136:139], v[196:199]\n\t" \
  "ds_read_u16_d16_hi v227, v227 offset:10528\n\t" \
  "v_fma_f32 v167, |v167|, v231, v167 mul:4\n\t" \
  "v_fma_f32 v166, |v166|, v230, v166 mul:4\n\t" \
  "v_mfma_f32_16x16x32_bf16 v[204:207], v[216:219], v[140:143], v[204:207]\n\t" \
  "ds_read_b64_tr_b16 v[216:217], v249 offset:6216\n\t" \
  "v_cvt_pk_bf16_f32 v164, v164, v165\n\t" \
  "v_cvt_pk_bf16_f32 v165, v166, v167\n\t" \
  "s_setprio 0\n\t" \
  "v_cvt_pknorm_i16_f32 v228, |v194|, |v195|\n\t" \
  "v_cvt_pknorm_i16_f32 v230, |v192|, |v193|\n\t" \
  "v_mfma_f32_16x16x32_bf16 v[48:51], v[242:245], v[236:239], v[48:51]\n\t" \
  "ds_read_b64_tr_b16 v[218:219], v249 offset:7752\n\t" \
  "v_lshlrev_b32_sdwa v229, 2, v228 dst_sel:DWORD dst_unused:UNUSED_PAD src0_sel:DWORD src1_sel:BYTE_3\n\t" \
  "v_mfma_f32_16x16x32_bf16 v[52:55], v[240:243], v[236:239], v[52:55]\n\t" \
  "buffer_load_dwordx4 v[236:239], v250, s[8:11], s12 offen offset:64 nt\n\t" \
  "s_cmp_lg_u32 s58, 0\n\t" \
  "s_cbranch_scc1 .Lcnx_c27\n\t" \
  "s_add_u32 m0, s25, 56880\n\t" \
  "s_add_u32 s24, s24, s26\n\t" \
  "buffer_load_dwordx4 v254, s[8:11], s24 offen lds\n\t" \
  "s_cmp_lg_u32 s38, 5\n\t" \
  "s_cbranch_scc1 .Lcnx_c27\n\t" \
  "s_mov_b32 m0, 107744\n\t" \
  "s_add_u32 s30, s30, s31\n\t" \
  "buffer_load_dword v252, s[8:11], s30 offen lds\n\t" \
  ".Lcnx_c27:\n\t" \
  "ds_read_u16 v226, v226 offset:10528\n\t" \
  "v_lshlrev_b32_sdwa v228, 2, v228 dst_sel:DWORD dst_unused:UNUSED_PAD src0_sel:DWORD src1_sel:BYTE_1\n\t" \
  "v_lshlrev_b32_sdwa v231, 2, v230 dst_sel:DWORD dst_unused:UNUSED_PAD src0_sel:DWORD src1_sel:BYTE_3\n\t" \
  "s_waitcnt lgkmcnt(11)\n\t" \
  "v_mfma_f32_16x16x32_bf16 v[0:3], v[208:211], v[146:149], v[0:3]\n\t" \
  "ds_read_b32 v229, v229 offset:10016\n\t" \
  "v_lshlrev_b32_sdwa v230, 2, v230 dst_sel:DWORD dst_unused:UNUSED_PAD src0_sel:DWORD src1_sel:BYTE_1\n\t" \
  "v_lshlrev_b32_sdwa v233, 1, v203 dst_sel:DWORD dst_unused:UNUSED_PAD src0_sel:DWORD src1_sel:WORD_1\n\t" \
  "v_mfma_f32_16x16x32_bf16 v[24:27], v[208:211], v[154:157], v[24:27]\n\t" \
  "ds_read_b32 v228, v228 offset:10016\n\t" \
  "v_lshlrev_b32_sdwa v232, 1, v202 dst_sel:DWORD dst_unused:UNUSED_PAD src0_sel:DWORD src1_sel:WORD_1\n\t" \
  "v_mfma_f32_16x16x32_bf16 v[48:51], v[208:211], v[162:165], v[48:51]\n\t" \
  "ds_read_b32 v231, v231 offset:10016\n\t" \
  "v_lshlrev_b32_sdwa v235, 1, v201 dst_sel:DWORD dst_unused:UNUSED_PAD src0_sel:DWORD src1_sel:WORD_1\n\t" \
  "v_lshlrev_b32_sdwa v234, 1, v200 dst_sel:DWORD dst_unused:UNUSED_PAD src0_sel:DWORD src1_sel:WORD_1\n\t" \
  "v_mfma_f32_16x16x32_bf16 v[72:75], v[208:211], v[170:173], v[72:75]\n\t" \
  "ds_read_b64_tr_b16 v[208:209], v249 offset:6224\n\t" \
  "ds_read_b64_tr_b16 v[210:211], v249 offset:7760\n\t" \
  "s_waitcnt lgkmcnt(13)\n\t" \
  "v_fma_mix_f32 v221, |v179|, v221, v221 op_sel:[0,1,0] op_sel_hi:[0,1,1] clamp\n\t" \
  "v_fma_mix_f32 v220, |v178|, v220, v220 op_sel:[0,1,0] op_sel_hi:[0,1,1] clamp\n\t" \
  "s_waitcnt lgkmcnt(11)\n\t" \
  "v_mfma_f32_16x16x32_bf16 v[4:7], v[212:215], v[146:149], v[4:7]\n\t" \
  "ds_read_b32 v230, v230 offset:10016\n\t" \
  "v_fma_mix_f32 v223, |v177|, v223, v223 op_sel:[0,1,0] op_sel_hi:[0,1,1] clamp\n\t" \
  "v_fma_mix_f32 v222, |v176|, v222, v222 op_sel:[0,1,0] op_sel_hi:[0,1,1] clamp\n\t" \
  "v_mfma_f32_16x16x32_bf16 v[28:31], v[212:215], v[154:157], v[28:31]\n\t" \
  "ds_read_u16_d16_hi v233, v233 offset:10528\n\t" \
  "s_waitcnt lgkmcnt(7)\n\t" \
  "v_or_b32 v187, v224, v225\n\t" \
  "v_mfma_f32_16x16x32_bf16 v[52:55], v[212:215], v[162:165], v[52:55]\n\t" \
  "ds_read_u16 v232, v232 offset:10528\n\t" \
  "v_or_b32 v186, v226, v227\n\t" \
  "v_fma_f32 v179, |v179|, v221, v179 mul:4\n\t" \
  "v_mfma_f32_16x16x32_bf16 v[76:79], v[212:215], v[170:173], v[76:79]\n\t" \
  "ds_read_b64_tr_b16 v[212:213], v249 offset:6280\n\t" \
  "v_fma_f32 v178, |v178|, v220, v178 mul:4\n\t" \
  "v_fma_f32 v177, |v177|, v223, v177 mul:4\n\t" \
  "v_mfma_f32_16x16x32_bf16 v[8:11], v[216:219], v[146:149], v[8:11]\n\t" \
  "ds_read_b64_tr_b16 v[214:215], v249 offset:7816\n\t" \
  "v_fma_f32 v176, |v176|, v222, v176 mul:4\n\t" \
  "v_mfma_f32_16x16x32_bf16 v[32:35], v[216:219], v[154:157], v[32:35]\n\t" \
  "ds_read_u16_d16_hi v235, v235 offset:10528\n\t" \
  "v_cvt_pk_bf16_f32 v179, v178, v179\n\t" \
  "v_cvt_pk_bf16_f32 v178, v176, v177\n\t" \
  "v_mfma_f32_16x16x32_bf16 v[56:59], v[216:219], v[162:165], v[56:59]\n\t" \
  "ds_read_u16 v234, v234 offset:10528\n\t" \
  "v_cvt_pknorm_i16_f32 v220, |v180|, |v181|\n\t" \
  "s_setprio 1\n\t" \
  "v_cvt_pknorm_i16_f32 v222, |v182|, |v183|\n\t" \
  "v_mfma_f32_16x16x32_bf16 v[80:83], v[216:219], v[170:173], v[80:83]\n\t" \
  "ds_read_b64_tr_b16 v[216:217], v249 offset:6288\n\t" \
  "v_lshlrev_b32_sdwa v221, 2, v220 dst_sel:DWORD dst_unused:UNUSED_PAD src0_sel:DWORD src1_sel:BYTE_3\n\t" \
  "v_lshlrev_b32_sdwa v220, 2, v220 dst_sel:DWORD dst_unused:UNUSED_PAD src0_sel:DWORD src1_sel:BYTE_1\n\t" \
  "s_waitcnt lgkmcnt(8)\n\t" \
  "v_mfma_f32_16x16x32_bf16 v[12:15], v[208:211], v[146:149], v[12:15]\n\t" \
  "ds_read_b64_tr_b16 v[218:219], v249 offset:7824\n\t" \
  "v_lshlrev_b32_sdwa v223, 2, v222 dst_sel:DWORD dst_unused:UNUSED_PAD src0_sel:DWORD src1_sel:BYTE_3\n\t" \
  "v_mfma_f32_16x16x32_bf16 v[36:39], v[208:211], v[154:157], v[36:39]\n\t" \
  "ds_read_b32 v221, v221 offset:10016\n\t" \
  "v_lshlrev_b32_sdwa v222, 2, v222 dst_sel:DWORD dst_unused:UNUSED_PAD src0_sel:DWORD src1_sel:BYTE_1\n\t" \
  "v_lshlrev_b32_sdwa v225, 1, v189 dst_sel:DWORD dst_unused:UNUSED_PAD src0_sel:DWORD src1_sel:WORD_1\n\t" \
  "v_mfma_f32_16x16x32_bf16 v[60:63], v[208:211], v[162:165], v[60:63]\n\t" \
  "ds_read_b32 v220, v220 offset:10016\n\t" \
  "v_lshlrev_b32_sdwa v224, 1, v188 dst_sel:DWORD dst_unused:UNUSED_PAD src0_sel:DWORD src1_sel:WORD_1\n\t" \
  "v_lshlrev_b32_sdwa v227, 1, v191 dst_sel:DWORD dst_unused:UNUSED_PAD src0_sel:DWORD src1_sel:WORD_1\n\t" \
  "v_mfma_f32_16x16x32_bf16 v[84:87], v[208:211], v[170:173], v[84:87]\n\t" \
  "ds_read_b64_tr_b16 v[208:209], v248 offset:43048\n\t" \
  "ds_read_b64_tr_b16 v[210:211], v248 offset:44072\n\t" \
  "v_lshlrev_b32_sdwa v226, 1, v190 dst_sel:DWORD dst_unused:UNUSED_PAD src0_sel:DWORD src1_sel:WORD_1\n\t" \
  "s_waitcnt lgkmcnt(8)\n\t" \
  "v_mfma_f32_16x16x32_bf16 v[16:19], v[212:215], v[146:149], v[16:19]\n\t" \
  "ds_read_b32 v223, v223 offset:10016\n\t" \
  "v_fma_mix_f32 v229, |v195|, v229, v229 op_sel:[0,1,0] op_sel_hi:[0,1,1] clamp\n\t" \
  "v_fma_mix_f32 v228, |v194|, v228, v228 op_sel:[0,1,0] op_sel_hi:[0,1,1] clamp\n\t" \
  "v_mfma_f32_16x16x32_bf16 v[40:43], v[212:215], v[154:157], v[40:43]\n\t" \
  "ds_read_b32 v222, v222 offset:10016\n\t" \
  "v_fma_mix_f32 v231, |v193|, v231, v231 op_sel:[0,1,0] op_sel_hi:[0,1,1] clamp\n\t" \
  "v_fma_mix_f32 v230, |v192|, v230, v230 op_sel:[0,1,0] op_sel_hi:[0,1,1] clamp\n\t" \
  "v_mfma_f32_16x16x32_bf16 v[64:67], v[212:215], v[162:165], v[64:67]\n\t" \
  "ds_read_u16_d16_hi v225, v225 offset:10528\n\t" \
  "s_waitcnt lgkmcnt(9)\n\t" \
  "v_or_b32 v203, v232, v233\n\t" \
  "v_or_b32 v202, v234, v235\n\t" \
  "v_mfma_f32_16x16x32_bf16 v[88:91], v[212:215], v[170:173], v[88:91]\n\t" \
  "ds_read_b64_tr_b16 v[212:213], v248 offset:46120\n\t" \
  "v_fma_f32 v195, |v195|, v229, v195 mul:4\n\t" \
  "s_waitcnt lgkmcnt(8)\n\t" \
  "v_mfma_f32_16x16x32_bf16 v[20:23], v[216:219], v[146:149], v[20:23]\n\t" \
  "ds_read_b64_tr_b16 v[214:215], v248 offset:47144\n\t" \
  "v_fma_f32 v194, |v194|, v228, v194 mul:4\n\t" \
  "v_fma_f32 v193, |v193|, v231, v193 mul:4\n\t" \
  "v_mfma_f32_16x16x32_bf16 v[44:47], v[216:219], v[154:157], v[44:47]\n\t" \
  "ds_read_u16 v224, v224 offset:10528\n\t" \
  "v_fma_f32 v192, |v192|, v230, v192 mul:4\n\t" \
  "v_cvt_pk_bf16_f32 v195, v194, v195\n\t" \
  "v_mfma_f32_16x16x32_bf16 v[68:71], v[216:219], v[162:165], v[68:71]\n\t" \
  "ds_read_u16_d16_hi v227, v227 offset:10528\n\t" \
  "v_cvt_pk_bf16_f32 v194, v192, v193\n\t" \
  "v_cvt_pknorm_i16_f32 v228, |v196|, |v197|\n\t" \
  "v_mfma_f32_16x16x32_bf16 v[92:95], v[216:219], v[170:173], v[92:95]\n\t" \
  "ds_read_b64_tr_b16 v[216:217], v248 offset:45096\n\t" \
  "v_cvt_pknorm_i16_f32 v230, |v198|, |v199|\n\t" \
  "s_setprio 2\n\t" \
  "s_waitcnt lgkmcnt(8)\n\t" \
  "v_mfma_f32_16x16x32_bf16 v[144:147], v[208:211], v[96:99], 0\n\t" \
  "ds_read_b64_tr_b16 v[218:219], v248 offset:48168\n\t" \
  "v_lshlrev_b32_sdwa v229, 2, v228 dst_sel:DWORD dst_unused:UNUSED_PAD src0_sel:DWORD src1_sel:BYTE_3\n\t" \
  "v_lshlrev_b32_sdwa v228, 2, v228 dst_sel:DWORD dst_unused:UNUSED_PAD src0_sel:DWORD src1_sel:BYTE_1\n\t" \
  "v_mfma_f32_16x16x32_bf16 v[152:155], v[208:211], v[100:103], 0\n\t" \
  "ds_read_u16 v226, v226 offset:10528\n\t" \
  "v_lshlrev_b32_sdwa v231, 2, v230 dst_sel:DWORD dst_unused:UNUSED_PAD src0_sel:DWORD src1_sel:BYTE_3\n\t" \
  "v_lshlrev_b32_sdwa v230, 2, v230 dst_sel:DWORD dst_unused:UNUSED_PAD src0_sel:DWORD src1_sel:BYTE_1\n\t" \
  "v_mfma_f32_16x16x32_bf16 v[160:163], v[208:211], v[104:107], 0\n\t" \
  "ds_read_b32 v229, v229 offset:10016\n\t" \
  "v_lshlrev_b32_sdwa v233, 1, v205 dst_sel:DWORD dst_unused:UNUSED_PAD src0_sel:DWORD src1_sel:WORD_1\n\t" \
  "v_mfma_f32_16x16x32_bf16 v[168:171], v[208:211], v[108:111], 0\n\t" \
  "ds_read_b64_tr_b16 v[208:209], v248 offset:43560\n\t" \
  "v_lshlrev_b32_sdwa v232, 1, v204 dst_sel:DWORD dst_unused:UNUSED_PAD src0_sel:DWORD src1_sel:WORD_1\n\t" \
  "v_lshlrev_b32_sdwa v235, 1, v207 dst_sel:DWORD dst_unused:UNUSED_PAD src0_sel:DWORD src1_sel:WORD_1\n\t" \
  "s_waitcnt lgkmcnt(7)\n\t" \
  "v_mfma_f32_16x16x32_bf16 v[144:147], v[212:215], v[112:115], v[144:147]\n\t" \
  "ds_read_b64_tr_b16 v[210:211], v248 offset:44584\n\t" \
  "ds_read_b32 v228, v228 offset:10016\n\t" \
  "v_lshlrev_b32_sdwa v234, 1, v206 dst_sel:DWORD dst_unused:UNUSED_PAD src0_sel:DWORD src1_sel:WORD_1\n\t" \
  "v_fma_mix_f32 v221, |v181|, v221, v221 op_sel:[0,1,0] op_sel_hi:[0,1,1] clamp\n\t" \
  "v_mfma_f32_16x16x32_bf16 v[152:155], v[212:215], v[116:119], v[152:155]\n\t" \
  "ds_read_b32 v231, v231 offset:10016\n\t" \
  "ds_read_b32 v230, v230 offset:10016\n\t" \
  "v_fma_mix_f32 v220, |v180|, v220, v220 op_sel:[0,1,0] op_sel_hi:[0,1,1] clamp\n\t" \
  "v_fma_mix_f32 v223, |v183|, v223, v223 op_sel:[0,1,0] op_sel_hi:[0,1,1] clamp\n\t" \
  "v_mfma_f32_16x16x32_bf16 v[160:163], v[212:215], v[120:123], v[160:163]\n\t" \
  "ds_read_u16_d16_hi v233, v233 offset:10528\n\t" \
  "v_fma_mix_f32 v222, |v182|, v222, v222 op_sel:[0,1,0] op_sel_hi:[0,1,1] clamp\n\t" \
  "v_mfma_f32_16x16x32_bf16 v[168:171], v[212:215], v[124:127], v[168:171]\n\t" \
  "ds_read_b64_tr_b16 v[212:213], v248 offset:46632\n\t" \
  "s_waitcnt lgkmcnt(8)\n\t" \
  "v_or_b32 v188, v224, v225\n\t" \
  "v_or_b32 v189, v226, v227\n\t" \
  "v_mfma_f32_16x16x32_bf16 v[144:147], v[216:219], v[128:131], v[144:147]\n\t" \
  "ds_read_b64_tr_b16 v[214:215], v248 offset:47656\n\t" \
  "v_fma_f32 v181, |v181|, v221, v181 mul:4\n\t" \
  "v_fma_f32 v180, |v180|, v220, v180 mul:4\n\t" \
  "v_mfma_f32_16x16x32_bf16 v[152:155], v[216:219], v[132:135], v[152:155]\n\t" \
  "ds_read_u16 v232, v232 offset:10528\n\t" \
  "v_fma_f32 v183, |v183|, v223, v183 mul:4\n\t" \
  "v_mfma_f32_16x16x32_bf16 v[160:163], v[216:219], v[136:139], v[160:163]\n\t" \
  "ds_read_u16_d16_hi v235, v235 offset:10528\n\t" \
  "v_fma_f32 v182, |v182|, v222, v182 mul:4\n\t" \
  "v_cvt_pk_bf16_f32 v180, v180, v181\n\t" \
  "v_mfma_f32_16x16x32_bf16 v[168:171], v[216:219], v[140:143], v[168:171]\n\t" \
  "ds_read_b64_tr_b16 v[216:217], v248 offset:45608\n\t" \
  "v_cvt_pk_bf16_f32 v181, v182, v183\n\t" \
  "v_cvt_pknorm_i16_f32 v220, |v146|, |v147|\n\t" \
  "s_waitcnt lgkmcnt(9)\n\t" \
  "v_mfma_f32_16x16x32_bf16 v[148:151], v[208:211], v[96:99], 0\n\t" \
  "ds_read_b64_tr_b16 v[218:219], v248 offset:48680\n\t" \
  "v_cvt_pknorm_i16_f32 v222, |v144|, |v145|\n\t" \
  "s_setprio 3\n\t" \
  "v_lshlrev_b32_sdwa v221, 2, v220 dst_sel:DWORD dst_unused:UNUSED_PAD src0_sel:DWORD src1_sel:BYTE_3\n\t" \
  "v_mfma_f32_16x16x32_bf16 v[156:159], v[208:211], v[100:103], 0\n\t" \
  "s_waitcnt vmcnt(1)\n\t" \
  "s_cmp_lg_u32 s58, 1\n\t" \
  "s_cbranch_scc1 .Lcnx_c15\n\t" \
  "s_waitcnt vmcnt(0)\n\t" \
  ".Lcnx_c15:\n\t" \
  "s_mov_b64 exec, 3\n\t" \
  "ds_add_rtn_u32 v246, v251, v250 offset:47200\n\t" \
  "s_mov_b64 exec, -1\n\t" \
  "ds_read_u16 v234, v234 offset:10528\n\t" \
  "v_lshlrev_b32_sdwa v220, 2, v220 dst_sel:DWORD dst_unused:UNUSED_PAD src0_sel:DWORD src1_sel:BYTE_1\n\t" \
  "v_mfma_f32_16x16x32_bf16 v[164:167], v[208:211], v[104:107], 0\n\t" \
  "ds_read_b32 v221, v221 offset:10016\n\t" \
  "v_lshlrev_b32_sdwa v223, 2, v222 dst_sel:DWORD dst_unused:UNUSED_PAD src0_sel:DWORD src1_sel:BYTE_3\n\t" \
  "v_lshlrev_b32_sdwa v222, 2, v222 dst_sel:DWORD dst_unused:UNUSED_PAD src0_sel:DWORD src1_sel:BYTE_1\n\t" \
  "v_mfma_f32_16x16x32_bf16 v[172:175], v[208:211], v[108:111], 0\n\t" \
  "s_waitcnt lgkmcnt(2)\n\t" \
  "v_readfirstlane_b32 s4, v246\n\t" \
  "s_cmp_lt_u32 s4, s57\n\t" \
  "s_cbranch_scc1 .Lcnx_w96\n.Lcnx_b96:\n\t" \
  "s_cmp_lg_u32 s58, 1\n\t" \
  "s_cselect_b32 s57, s57, 0\n\t" \
  "ds_read_b64_tr_b16 v[208:209], v249 offset:12304\n\t" \
  "v_lshlrev_b32_sdwa v225, 1, v155 dst_sel:DWORD dst_unused:UNUSED_PAD src0_sel:DWORD src1_sel:WORD_1\n\t" \
  "v_lshlrev_b32_sdwa v224, 1, v154 dst_sel:DWORD dst_unused:UNUSED_PAD src0_sel:DWORD src1_sel:WORD_1\n\t" \
  "v_mfma_f32_16x16x32_bf16 v[148:151], v[212:215], v[112:115], v[148:151]\n\t" \
  "ds_read_b64_tr_b16 v[210:211], v249 offset:13840\n\t" \
  "v_lshlrev_b32_sdwa v227, 1, v153 dst_sel:DWORD dst_unused:UNUSED_PAD src0_sel:DWORD src1_sel:WORD_1\n\t" \
  "v_mfma_f32_16x16x32_bf16 v[156:159], v[212:215], v[116:119], v[156:159]\n\t" \
  "s_cmp_eq_u32 s37, 0\n\t" \
  "s_cbranch_scc1 .Lcnx_f9\n.Lcnx_g9:\n\t" \
  "ds_read_b32 v220, v220 offset:10016\n\t" \
  "v_lshlrev_b32_sdwa v226, 1, v152 dst_sel:DWORD dst_unused:UNUSED_PAD src0_sel:DWORD src1_sel:WORD_1\n\t" \
  "v_fma_mix_f32 v229, |v197|, v229, v229 op_sel:[0,1,0] op_sel_hi:[0,1,1] clamp\n\t" \
  "v_mfma_f32_16x16x32_bf16 v[164:167], v[212:215], v[120:123], v[164:167]\n\t" \
  "ds_read_b32 v223, v223 offset:10016\n\t" \
  "ds_read_b32 v222, v222 offset:10016\n\t" \
  "v_fma_mix_f32 v228, |v196|, v228, v228 op_sel:[0,1,0] op_sel_hi:[0,1,1] clamp\n\t" \
  "v_fma_mix_f32 v231, |v199|, v231, v231 op_sel:[0,1,0] op_sel_hi:[0,1,1] clamp\n\t" \
  "v_mfma_f32_16x16x32_bf16 v[172:175], v[212:215], v[124:127], v[172:175]\n\t" \
  "ds_read_b64_tr_b16 v[212:213], v249 offset:12312\n\t" \
  "ds_read_b64_tr_b16 v[214:215], v249 offset:13848\n\t" \
  "v_fma_mix_f32 v230, |v198|, v230, v230 op_sel:[0,1,0] op_sel_hi:[0,1,1] clamp\n\t" \
  "s_waitcnt lgkmcnt(8)\n\t" \
  "v_or_b32 v204, v232, v233\n\t" \
  "v_mfma_f32_16x16x32_bf16 v[148:151], v[216:219], v[128:131], v[148:151]\n\t" \
  "ds_read_u16_d16_hi v225, v225 offset:10528\n\t" \
  "v_or_b32 v205, v234, v235\n\t" \
  "v_mfma_f32_16x16x32_bf16 v[156:159], v[216:219], v[132:135], v[156:159]\n\t" \
  "ds_read_u16 v224, v224 offset:10528\n\t" \
  "v_fma_f32 v197, |v197|, v229, v197 mul:4\n\t" \
  "v_fma_f32 v196, |v196|, v228, v196 mul:4\n\t" \
  "v_mfma_f32_16x16x32_bf16 v[164:167], v[216:219], v[136:139], v[164:167]\n\t" \
  "ds_read_u16_d16_hi v227, v227 offset:10528\n\t" \
  "v_fma_f32 v199, |v199|, v231, v199 mul:4\n\t" \
  "v_fma_f32 v198, |v198|, v230, v198 mul:4\n\t" \
  "v_mfma_f32_16x16x32_bf16 v[172:175], v[216:219], v[140:143], v[172:175]\n\t" \
  "ds_read_b64_tr_b16 v[216:217], v249 offset:12368\n\t" \
  "v_cvt_pk_bf16_f32 v196, v196, v197\n\t" \
  "v_cvt_pk_bf16_f32 v197, v198, v199\n\t" \
  "s_cmp_lg_u32 s58, 1\n\t" \
  "s_cbranch_scc1 .Lcnx_c16\n\t" \
  "s_mov_b64 vcc, -1\n\t" \
  "s_setprio 0\n\t" \
  "s_cmp_ge_u32 s39, 2048\n\t" \
  "s_cbranch_scc1 .Lcnx_c16\n\t" \
  "s_setprio 3\n\t" \
  ".Lcnx_c16:\n\t" \
  "s_cbranch_vccnz .Lcnx_p9_0\n\ts_setprio 0\n.Lcnx_p9_0:\n\t" \
  "v_cvt_pknorm_i16_f32 v228, |v162|, |v163|\n\t" \
  "v_cvt_pknorm_i16_f32 v230, |v160|, |v161|\n\t" \
  "v_mfma_f32_16x16x32_bf16 v[56:59], v[242:245], v[236:239], v[56:59]\n\t" \
  "ds_read_b64_tr_b16 v[218:219], v249 offset:13904\n\t" \
  "v_lshlrev_b32_sdwa v229, 2, v228 dst_sel:DWORD dst_unused:UNUSED_PAD src0_sel:DWORD src1_sel:BYTE_3\n\t" \
  "v_mfma_f32_16x16x32_bf16 v[60:63], v[240:243], v[236:239], v[60:63]\n\t" \
  "buffer_load_dwordx4 v[236:239], v250, s[8:11], s12 offen offset:128 nt\n\t" \
  "s_cmp_lg_u32 s58, 0\n\t" \
  "s_cbranch_scc1 .Lcnx_c28\n\t" \
  "s_add_u32 m0, s25, 65080\n\t" \
  "s_add_u32 s24, s24, s26\n\t" \
  "buffer_load_dwordx4 v254, s[8:11], s24 offen lds\n\t" \
  "s_cmp_lg_u32 s38, 6\n\t" \
  "s_cbranch_scc1 .Lcnx_c28\n\t" \
  "s_mov_b32 m0, 107744\n\t" \
  "s_add_u32 s30, s30, s31\n\t" \
  "buffer_load_dword v252, s[8:11], s30 offen lds\n\t" \
  ".Lcnx_c28:\n\t" \
  "ds_read_u16 v226, v226 offset:10528\n\t" \
  "v_lshlrev_b32_sdwa v228, 2, v228 dst_sel:DWORD dst_unused:UNUSED_PAD src0_sel:DWORD src1_sel:BYTE_1\n\t" \
  "v_lshlrev_b32_sdwa v231, 2, v230 dst_sel:DWORD dst_unused:UNUSED_PAD src0_sel:DWORD src1_sel:BYTE_3\n\t" \
  "s_waitcnt lgkmcnt(11)\n\t" \
  "v_mfma_f32_16x16x32_bf16 v[0:3], v[208:211], v[178:181], v[0:3]\n\t" \
  "ds_read_b32 v229, v229 offset:10016\n\t" \
  "v_lshlrev_b32_sdwa v230, 2, v230 dst_sel:DWORD dst_unused:UNUSED_PAD src0_sel:DWORD src1_sel:BYTE_1\n\t" \
  "v_lshlrev_b32_sdwa v233, 1, v171 dst_sel:DWORD dst_unused:UNUSED_PAD src0_sel:DWORD src1_sel:WORD_1\n\t" \
  "v_mfma_f32_16x16x32_bf16 v[24:27], v[208:211], v[186:189], v[24:27]\n\t" \
  "ds_read_b32 v228, v228 offset:10016\n\t" \
  "v_lshlrev_b32_sdwa v232, 1, v170 dst_sel:DWORD dst_unused:UNUSED_PAD src0_sel:DWORD src1_sel:WORD_1\n\t" \
  "v_mfma_f32_16x16x32_bf16 v[48:51], v[208:211], v[194:197], v[48:51]\n\t" \
  "ds_read_b32 v231, v231 offset:10016\n\t" \
  "v_lshlrev_b32_sdwa v235, 1, v169 dst_sel:DWORD dst_unused:UNUSED_PAD src0_sel:DWORD src1_sel:WORD_1\n\t" \
  "v_lshlrev_b32_sdwa v234, 1, v168 dst_sel:DWORD dst_unused:UNUSED_PAD src0_sel:DWORD src1_sel:WORD_1\n\t" \
  "v_mfma_f32_16x16x32_bf16 v[72:75], v[208:211], v[202:205], v[72:75]\n\t" \
  "ds_read_b64_tr_b16 v[208:209], v249 offset:12376\n\t" \
  "ds_read_b64_tr_b16 v[210:211], v249 offset:13912\n\t" \
  "s_waitcnt lgkmcnt(13)\n\t" \
  "v_fma_mix_f32 v221, |v147|, v221, v221 op_sel:[0,1,0] op_sel_hi:[0,1,1] clamp\n\t" \
  "v_fma_mix_f32 v220, |v146|, v220, v220 op_sel:[0,1,0] op_sel_hi:[0,1,1] clamp\n\t" \
  "s_waitcnt lgkmcnt(11)\n\t" \
  "v_mfma_f32_16x16x32_bf16 v[4:7], v[212:215], v[178:181], v[4:7]\n\t" \
  "ds_read_b32 v230, v230 offset:10016\n\t" \
  "v_fma_mix_f32 v223, |v145|, v223, v223 op_sel:[0,1,0] op_sel_hi:[0,1,1] clamp\n\t" \
  "v_fma_mix_f32 v222, |v144|, v222, v222 op_sel:[0,1,0] op_sel_hi:[0,1,1] clamp\n\t" \
  "v_mfma_f32_16x16x32_bf16 v[28:31], v[212:215], v[186:189], v[28:31]\n\t" \
  "ds_read_u16_d16_hi v233, v233 offset:10528\n\t" \
  "s_waitcnt lgkmcnt(7)\n\t" \
  "v_or_b32 v155, v224, v225\n\t" \
  "v_mfma_f32_16x16x32_bf16 v[52:55], v[212:215], v[194:197], v[52:55]\n\t" \
  "ds_read_u16 v232, v232 offset:10528\n\t" \
  "v_or_b32 v154, v226, v227\n\t" \
  "v_fma_f32 v147, |v147|, v221, v147 mul:4\n\t" \
  "v_mfma_f32_16x16x32_bf16 v[76:79], v[212:215], v[202:205], v[76:79]\n\t" \
  "ds_read_b64_tr_b16 v[212:213], v249 offset:12432\n\t" \
  "v_fma_f32 v146, |v146|, v220, v146 mul:4\n\t" \
  "v_fma_f32 v145, |v145|, v223, v145 mul:4\n\t" \
  "v_mfma_f32_16x16x32_bf16 v[8:11], v[216:219], v[178:181], v[8:11]\n\t" \
  "ds_read_b64_tr_b16 v[214:215], v249 offset:13968\n\t" \
  "v_fma_f32 v144, |v144|, v222, v144 mul:4\n\t" \
  "v_mfma_f32_16x16x32_bf16 v[32:35], v[216:219], v[186:189], v[32:35]\n\t" \
  "ds_read_u16_d16_hi v235, v235 offset:10528\n\t" \
  "v_cvt_pk_bf16_f32 v147, v146, v147\n\t" \
  "v_cvt_pk_bf16_f32 v146, v144, v145\n\t" \
  "v_mfma_f32_16x16x32_bf16 v[56:59], v[216:219], v[194:197], v[56:59]\n\t" \
  "ds_read_u16 v234, v234 offset:10528\n\t" \
  "v_cvt_pknorm_i16_f32 v220, |v148|, |v149|\n\t" \
  "s_cbranch_vccnz .Lcnx_p9_1\n\ts_setprio 1\n.Lcnx_p9_1:\n\t" \
  "v_cvt_pknorm_i16_f32 v222, |v150|, |v151|\n\t" \
  "v_mfma_f32_16x16x32_bf16 v[80:83], v[216:219], v[202:205], v[80:83]\n\t" \
  "ds_read_b64_tr_b16 v[216:217], v249 offset:12440\n\t" \
  "v_lshlrev_b32_sdwa v221, 2, v220 dst_sel:DWORD dst_unused:UNUSED_PAD src0_sel:DWORD src1_sel:BYTE_3\n\t" \
  "v_lshlrev_b32_sdwa v220, 2, v220 dst_sel:DWORD dst_unused:UNUSED_PAD src0_sel:DWORD src1_sel:BYTE_1\n\t" \
  "s_waitcnt lgkmcnt(8)\n\t" \
  "v_mfma_f32_16x16x32_bf16 v[12:15], v[208:211], v[178:181], v[12:15]\n\t" \
  "ds_read_b64_tr_b16 v[218:219], v249 offset:13976\n\t" \
  "v_lshlrev_b32_sdwa v223, 2, v222 dst_sel:DWORD dst_unused:UNUSED_PAD src0_sel:DWORD src1_sel:BYTE_3\n\t" \
  "v_mfma_f32_16x16x32_bf16 v[36:39], v[208:211], v[186:189], v[36:39]\n\t" \
  "ds_read_b32 v221, v221 offset:10016\n\t" \
  "v_lshlrev_b32_sdwa v222, 2, v222 dst_sel:DWORD dst_unused:UNUSED_PAD src0_sel:DWORD src1_sel:BYTE_1\n\t" \
  "v_lshlrev_b32_sdwa v225, 1, v157 dst_sel:DWORD dst_unused:UNUSED_PAD src0_sel:DWORD src1_sel:WORD_1\n\t" \
  "v_mfma_f32_16x16x32_bf16 v[60:63], v[208:211], v[194:197], v[60:63]\n\t" \
  "ds_read_b32 v220, v220 offset:10016\n\t" \
  "v_lshlrev_b32_sdwa v224, 1, v156 dst_sel:DWORD dst_unused:UNUSED_PAD src0_sel:DWORD src1_sel:WORD_1\n\t" \
  "v_lshlrev_b32_sdwa v227, 1, v159 dst_sel:DWORD dst_unused:UNUSED_PAD src0_sel:DWORD src1_sel:WORD_1\n\t" \
  "v_mfma_f32_16x16x32_bf16 v[84:87], v[208:211], v[202:205], v[84:87]\n\t" \
  "ds_read_b64_tr_b16 v[208:209], v248 offset:49192\n\t" \
  "ds_read_b64_tr_b16 v[210:211], v248 offset:50216\n\t" \
  "v_lshlrev_b32_sdwa v226, 1, v158 dst_sel:DWORD dst_unused:UNUSED_PAD src0_sel:DWORD src1_sel:WORD_1\n\t" \
  "s_waitcnt lgkmcnt(8)\n\t" \
  "v_mfma_f32_16x16x32_bf16 v[16:19], v[212:215], v[178:181], v[16:19]\n\t" \
  "ds_read_b32 v223, v223 offset:10016\n\t" \
  "v_fma_mix_f32 v229, |v163|, v229, v229 op_sel:[0,1,0] op_sel_hi:[0,1,1] clamp\n\t" \
  "v_fma_mix_f32 v228, |v162|, v228, v228 op_sel:[0,1,0] op_sel_hi:[0,1,1] clamp\n\t" \
  "v_mfma_f32_16x16x32_bf16 v[40:43], v[212:215], v[186:189], v[40:43]\n\t" \
  "ds_read_b32 v222, v222 offset:10016\n\t" \
  "v_fma_mix_f32 v231, |v161|, v231, v231 op_sel:[0,1,0] op_sel_hi:[0,1,1] clamp\n\t" \
  "v_fma_mix_f32 v230, |v160|, v230, v230 op_sel:[0,1,0] op_sel_hi:[0,1,1] clamp\n\t" \
  "v_mfma_f32_16x16x32_bf16 v[64:67], v[212:215], v[194:197], v[64:67]\n\t" \
  "ds_read_u16_d16_hi v225, v225 offset:10528\n\t" \
  "s_waitcnt lgkmcnt(9)\n\t" \
  "v_or_b32 v171, v232, v233\n\t" \
  "v_or_b32 v170, v234, v235\n\t" \
  "v_mfma_f32_16x16x32_bf16 v[88:91], v[212:215], v[202:205], v[88:91]\n\t" \
  "ds_read_b64_tr_b16 v[212:213], v248 offset:52264\n\t" \
  "v_fma_f32 v163, |v163|, v229, v163 mul:4\n\t" \
  "s_waitcnt lgkmcnt(8)\n\t" \
  "v_mfma_f32_16x16x32_bf16 v[20:23], v[216:219], v[178:181], v[20:23]\n\t" \
  "ds_read_b64_tr_b16 v[214:215], v248 offset:53288\n\t" \
  "v_fma_f32 v162, |v162|, v228, v162 mul:4\n\t" \
  "v_fma_f32 v161, |v161|, v231, v161 mul:4\n\t" \
  "v_mfma_f32_16x16x32_bf16 v[44:47], v[216:219], v[186:189], v[44:47]\n\t" \
  "ds_read_u16 v224, v224 offset:10528\n\t" \
  "v_fma_f32 v160, |v160|, v230, v160 mul:4\n\t" \
  "v_cvt_pk_bf16_f32 v163, v162, v163\n\t" \
  "v_mfma_f32_16x16x32_bf16 v[68:71], v[216:219], v[194:197], v[68:71]\n\t" \
  "ds_read_u16_d16_hi v227, v227 offset:10528\n\t" \
  "v_cvt_pk_bf16_f32 v162, v160, v161\n\t" \
  "v_cvt_pknorm_i16_f32 v228, |v164|, |v165|\n\t" \
  "v_mfma_f32_16x16x32_bf16 v[92:95], v[216:219], v[202:205], v[92:95]\n\t" \
  "ds_read_b64_tr_b16 v[216:217], v248 offset:51240\n\t" \
  "v_cvt_pknorm_i16_f32 v230, |v166|, |v167|\n\t" \
  "s_cbranch_vccnz .Lcnx_p9_2\n\ts_setprio 2\n.Lcnx_p9_2:\n\t" \
  "s_waitcnt lgkmcnt(8)\n\t" \
  "v_mfma_f32_16x16x32_bf16 v[176:179], v[208:211], v[96:99], 0\n\t" \
  "ds_read_b64_tr_b16 v[218:219], v248 offset:54312\n\t" \
  "v_lshlrev_b32_sdwa v229, 2, v228 dst_sel:DWORD dst_unused:UNUSED_PAD src0_sel:DWORD src1_sel:BYTE_3\n\t" \
  "v_lshlrev_b32_sdwa v228, 2, v228 dst_sel:DWORD dst_unused:UNUSED_PAD src0_sel:DWORD src1_sel:BYTE_1\n\t" \
  "v_mfma_f32_16x16x32_bf16 v[184:187], v[208:211], v[100:103], 0\n\t" \
  "ds_read_u16 v226, v226 offset:10528\n\t" \
  "v_lshlrev_b32_sdwa v231, 2, v230 dst_sel:DWORD dst_unused:UNUSED_PAD src0_sel:DWORD src1_sel:BYTE_3\n\t" \
  "v_lshlrev_b32_sdwa v230, 2, v230 dst_sel:DWORD dst_unused:UNUSED_PAD src0_sel:DWORD src1_sel:BYTE_1\n\t" \
  "v_mfma_f32_16x16x32_bf16 v[192:195], v[208:211], v[104:107], 0\n\t" \
  "ds_read_b32 v229, v229 offset:10016\n\t" \
  "v_lshlrev_b32_sdwa v233, 1, v173 dst_sel:DWORD dst_unused:UNUSED_PAD src0_sel:DWORD src1_sel:WORD_1\n\t" \
  "v_mfma_f32_16x16x32_bf16 v[200:203], v[208:211], v[108:111], 0\n\t" \
  "ds_read_b64_tr_b16 v[208:209], v248 offset:49704\n\t" \
  "v_lshlrev_b32_sdwa v232, 1, v172 dst_sel:DWORD dst_unused:UNUSED_PAD src0_sel:DWORD src1_sel:WORD_1\n\t" \
  "v_lshlrev_b32_sdwa v235, 1, v175 dst_sel:DWORD dst_unused:UNUSED_PAD src0_sel:DWORD src1_sel:WORD_1\n\t" \
  "s_waitcnt lgkmcnt(7)\n\t" \
  "v_mfma_f32_16x16x32_bf16 v[176:179], v[212:215], v[112:115], v[176:179]\n\t" \
  "ds_read_b64_tr_b16 v[210:211], v248 offset:50728\n\t" \
  "ds_read_b32 v228, v228 offset:10016\n\t" \
  "v_lshlrev_b32_sdwa v234, 1, v174 dst_sel:DWORD dst_unused:UNUSED_PAD src0_sel:DWORD src1_sel:WORD_1\n\t" \
  "v_fma_mix_f32 v221, |v149|, v221, v221 op_sel:[0,1,0] op_sel_hi:[0,1,1] clamp\n\t" \
  "v_mfma_f32_16x16x32_bf16 v[184:187], v[212:215], v[116:119], v[184:187]\n\t" \
  "ds_read_b32 v231, v231 offset:10016\n\t" \
  "ds_read_b32 v230, v230 offset:10016\n\t" \
  "v_fma_mix_f32 v220, |v148|, v220, v220 op_sel:[0,1,0] op_sel_hi:[0,1,1] clamp\n\t" \
  "v_fma_mix_f32 v223, |v151|, v223, v223 op_sel:[0,1,0] op_sel_hi:[0,1,1] clamp\n\t" \
  "v_mfma_f32_16x16x32_bf16 v[192:195], v[212:215], v[120:123], v[192:195]\n\t" \
  "ds_read_u16_d16_hi v233, v233 offset:10528\n\t" \
  "v_fma_mix_f32 v222, |v150|, v222, v222 op_sel:[0,1,0] op_sel_hi:[0,1,1] clamp\n\t" \
  "v_mfma_f32_16x16x32_bf16 v[200:203], v[212:215], v[124:127], v[200:203]\n\t" \
  "ds_read_b64_tr_b16 v[212:213], v248 offset:52776\n\t" \
  "s_waitcnt lgkmcnt(8)\n\t" \
  "v_or_b32 v156, v224, v225\n\t" \
  "v_or_b32 v157, v226, v227\n\t" \
  "v_mfma_f32_16x16x32_bf16 v[176:179], v[216:219], v[128:131], v[176:179]\n\t" \
  "ds_read_b64_tr_b16 v[214:215], v248 offset:53800\n\t" \
  "v_fma_f32 v149, |v149|, v221, v149 mul:4\n\t" \
  "v_fma_f32 v148, |v148|, v220, v148 mul:4\n\t" \
  "v_mfma_f32_16x16x32_bf16 v[184:187], v[216:219], v[132:135], v[184:187]\n\t" \
  "ds_read_u16 v232, v232 offset:10528\n\t" \
  "v_fma_f32 v151, |v151|, v223, v151 mul:4\n\t" \
  "v_mfma_f32_16x16x32_bf16 v[192:195], v[216:219], v[136:139], v[192:195]\n\t" \
  "ds_read_u16_d16_hi v235, v235 offset:10528\n\t" \
  "v_fma_f32 v150, |v150|, v222, v150 mul:4\n\t" \
  "v_cvt_pk_bf16_f32 v148, v148, v149\n\t" \
  "v_mfma_f32_16x16x32_bf16 v[200:203], v[216:219], v[140:143], v[200:203]\n\t" \
  "ds_read_b64_tr_b16 v[216:217], v248 offset:51752\n\t" \
  "v_cvt_pk_bf16_f32 v149, v150, v151\n\t" \
  "v_cvt_pknorm_i16_f32 v220, |v178|, |v179|\n\t" \
  "s_waitcnt lgkmcnt(9)\n\t" \
  "v_mfma_f32_16x16x32_bf16 v[180:183], v[208:211], v[96:99], 0\n\t" \
  "ds_read_b64_tr_b16 v[218:219], v248 offset:54824\n\t" \
  "v_cvt_pknorm_i16_f32 v222, |v176|, |v177|\n\t" \
  "s_cbranch_vccnz .Lcnx_p9_3\n\ts_setprio 3\n.Lcnx_p9_3:\n\t" \
  "v_lshlrev_b32_sdwa v221, 2, v220 dst_sel:DWORD dst_unused:UNUSED_PAD src0_sel:DWORD src1_sel:BYTE_3\n\t" \
  "v_mfma_f32_16x16x32_bf16 v[188:191], v[208:211], v[100:103], 0\n\t" \
  "s_waitcnt vmcnt(1)\n\t" \
  "s_cmp_lg_u32 s58, 1\n\t" \
  "s_cbranch_scc1 .Lcnx_c17\n\t" \
  "s_waitcnt vmcnt(0)\n\t" \
  ".Lcnx_c17:\n\t" \
  "s_mov_b64 exec, 3\n\t" \
  "ds_add_rtn_u32 v246, v251, v250 offset:47208\n\t" \
  "s_mov_b64 exec, -1\n\t" \
  "ds_read_u16 v234, v234 offset:10528\n\t" \
  "v_lshlrev_b32_sdwa v220, 2, v220 dst_sel:DWORD dst_unused:UNUSED_PAD src0_sel:DWORD src1_sel:BYTE_1\n\t" \
  "v_mfma_f32_16x16x32_bf16 v[196:199], v[208:211], v[104:107], 0\n\t" \
  "ds_read_b32 v221, v221 offset:10016\n\t" \
  "v_lshlrev_b32_sdwa v223, 2, v222 dst_sel:DWORD dst_unused:UNUSED_PAD src0_sel:DWORD src1_sel:BYTE_3\n\t" \
  "v_lshlrev_b32_sdwa v222, 2, v222 dst_sel:DWORD dst_unused:UNUSED_PAD src0_sel:DWORD src1_sel:BYTE_1\n\t" \
  "v_mfma_f32_16x16x32_bf16 v[204:207], v[208:211], v[108:111], 0\n\t" \
  "s_waitcnt lgkmcnt(2)\n\t" \
  "v_readfirstlane_b32 s4, v246\n\t" \
  "s_cmp_lt_u32 s4, s57\n\t" \
  "s_cbranch_scc1 .Lcnx_w108\n.Lcnx_b108:\n\t" \
  "v_add_u32 v248, s59, v248\n\t" \
  "v_add_u32 v249, s60, v249\n\t" \
  "ds_read_b64_tr_b16 v[208:209], v249 offset:24600\n\t" \
  "v_lshlrev_b32_sdwa v225, 1, v187 dst_sel:DWORD dst_unused:UNUSED_PAD src0_sel:DWORD src1_sel:WORD_1\n\t" \
  "v_lshlrev_b32_sdwa v224, 1, v186 dst_sel:DWORD dst_unused:UNUSED_PAD src0_sel:DWORD src1_sel:WORD_1\n\t" \
  "v_mfma_f32_16x16x32_bf16 v[180:183], v[212:215], v[112:115], v[180:183]\n\t" \
  "ds_read_b64_tr_b16 v[210:211], v249 offset:26136\n\t" \
  "v_lshlrev_b32_sdwa v227, 1, v185 dst_sel:DWORD dst_unused:UNUSED_PAD src0_sel:DWORD src1_sel:WORD_1\n\t" \
  "v_mfma_f32_16x16x32_bf16 v[188:191], v[212:215], v[116:119], v[188:191]\n\t" \
  "s_cmp_eq_u32 s37, 0\n\t" \
  "s_cbranch_scc1 .Lcnx_f10\n.Lcnx_g10:\n\t" \
  "ds_read_b32 v220, v220 offset:10016\n\t" \
  "v_lshlrev_b32_sdwa v226, 1, v184 dst_sel:DWORD dst_unused:UNUSED_PAD src0_sel:DWORD src1_sel:WORD_1\n\t" \
  "v_fma_mix_f32 v229, |v165|, v229, v229 op_sel:[0,1,0] op_sel_hi:[0,1,1] clamp\n\t" \
  "v_mfma_f32_16x16x32_bf16 v[196:199], v[212:215], v[120:123], v[196:199]\n\t" \
  "ds_read_b32 v223, v223 offset:10016\n\t" \
  "ds_read_b32 v222, v222 offset:10016\n\t" \
  "v_fma_mix_f32 v228, |v164|, v228, v228 op_sel:[0,1,0] op_sel_hi:[0,1,1] clamp\n\t" \
  "v_fma_mix_f32 v231, |v167|, v231, v231 op_sel:[0,1,0] op_sel_hi:[0,1,1] clamp\n\t" \
  "v_mfma_f32_16x16x32_bf16 v[204:207], v[212:215], v[124:127], v[204:207]\n\t" \
  "ds_read_b64_tr_b16 v[212:213], v249 offset:24608\n\t" \
  "ds_read_b64_tr_b16 v[214:215], v249 offset:26144\n\t" \
  "v_fma_mix_f32 v230, |v166|, v230, v230 op_sel:[0,1,0] op_sel_hi:[0,1,1] clamp\n\t" \
  "s_waitcnt lgkmcnt(8)\n\t" \
  "v_or_b32 v172, v232, v233\n\t" \
  "v_mfma_f32_16x16x32_bf16 v[180:183], v[216:219], v[128:131], v[180:183]\n\t" \
  "ds_read_u16_d16_hi v225, v225 offset:10528\n\t" \
  "v_or_b32 v173, v234, v235\n\t" \
  "v_mfma_f32_16x16x32_bf16 v[188:191], v[216:219], v[132:135], v[188:191]\n\t" \
  "ds_read_u16 v224, v224 offset:10528\n\t" \
  "v_fma_f32 v165, |v165|, v229, v165 mul:4\n\t" \
  "v_fma_f32 v164, |v164|, v228, v164 mul:4\n\t" \
  "v_mfma_f32_16x16x32_bf16 v[196:199], v[216:219], v[136:139], v[196:199]\n\t" \
  "ds_read_u16_d16_hi v227, v227 offset:10528\n\t" \
  "v_fma_f32 v167, |v167|, v231, v167 mul:4\n\t" \
  "v_fma_f32 v166, |v166|, v230, v166 mul:4\n\t" \
  "v_mfma_f32_16x16x32_bf16 v[204:207], v[216:219], v[140:143], v[204:207]\n\t" \
  "ds_read_b64_tr_b16 v[216:217], v249 offset:24664\n\t" \
  "v_cvt_pk_bf16_f32 v164, v164, v165\n\t" \
  "v_cvt_pk_bf16_f32 v165, v166, v167\n\t" \
  "s_cbranch_vccnz .Lcnx_p10_0\n\ts_setprio 0\n.Lcnx_p10_0:\n\t" \
  "v_cvt_pknorm_i16_f32 v228, |v194|, |v195|\n\t" \
  "v_cvt_pknorm_i16_f32 v230, |v192|, |v193|\n\t" \
  "v_mfma_f32_16x16x32_bf16 v[64:67], v[242:245], v[236:239], v[64:67]\n\t" \
  "ds_read_b64_tr_b16 v[218:219], v249 offset:26200\n\t" \
  "v_lshlrev_b32_sdwa v229, 2, v228 dst_sel:DWORD dst_unused:UNUSED_PAD src0_sel:DWORD src1_sel:BYTE_3\n\t" \
  "v_mfma_f32_16x16x32_bf16 v[68:71], v[240:243], v[236:239], v[68:71]\n\t" \
  "buffer_load_dwordx4 v[236:239], v250, s[8:11], s13 offen offset:0 nt\n\t" \
  "s_cmp_lg_u32 s58, 0\n\t" \
  "s_cbranch_scc1 .Lcnx_c29\n\t" \
  "s_add_u32 m0, s25, 73280\n\t" \
  "s_add_u32 s24, s24, s26\n\t" \
  "buffer_load_dwordx4 v254, s[8:11], s24 offen lds\n\t" \
  "s_cmp_lg_u32 s38, 7\n\t" \
  "s_cbranch_scc1 .Lcnx_c29\n\t" \
  "s_mov_b32 m0, 107744\n\t" \
  "s_add_u32 s30, s30, s31\n\t" \
  "buffer_load_dword v252, s[8:11], s30 offen lds\n\t" \
  ".Lcnx_c29:\n\t" \
  "ds_read_u16 v226, v226 offset:10528\n\t" \
  "v_lshlrev_b32_sdwa v228, 2, v228 dst_sel:DWORD dst_unused:UNUSED_PAD src0_sel:DWORD src1_sel:BYTE_1\n\t" \
  "v_lshlrev_b32_sdwa v231, 2, v230 dst_sel:DWORD dst_unused:UNUSED_PAD src0_sel:DWORD src1_sel:BYTE_3\n\t" \
  "s_waitcnt lgkmcnt(11)\n\t" \
  "v_mfma_f32_16x16x32_bf16 v[0:3], v[208:211], v[146:149], v[0:3]\n\t" \
  "ds_read_b32 v229, v229 offset:10016\n\t" \
  "v_lshlrev_b32_sdwa v230, 2, v230 dst_sel:DWORD dst_unused:UNUSED_PAD src0_sel:DWORD src1_sel:BYTE_1\n\t" \
  "v_lshlrev_b32_sdwa v233, 1, v203 dst_sel:DWORD dst_unused:UNUSED_PAD src0_sel:DWORD src1_sel:WORD_1\n\t" \
  "v_mfma_f32_16x16x32_bf16 v[24:27], v[208:211], v[154:157], v[24:27]\n\t" \
  "ds_read_b32 v228, v228 offset:10016\n\t" \
  "v_lshlrev_b32_sdwa v232, 1, v202 dst_sel:DWORD dst_unused:UNUSED_PAD src0_sel:DWORD src1_sel:WORD_1\n\t" \
  "v_mfma_f32_16x16x32_bf16 v[48:51], v[208:211], v[162:165], v[48:51]\n\t" \
  "ds_read_b32 v231, v231 offset:10016\n\t" \
  "v_lshlrev_b32_sdwa v235, 1, v201 dst_sel:DWORD dst_unused:UNUSED_PAD src0_sel:DWORD src1_sel:WORD_1\n\t" \
  "v_lshlrev_b32_sdwa v234, 1, v200 dst_sel:DWORD dst_unused:UNUSED_PAD src0_sel:DWORD src1_sel:WORD_1\n\t" \
  "v_mfma_f32_16x16x32_bf16 v[72:75], v[208:211], v[170:173], v[72:75]\n\t" \
  "ds_read_b64_tr_b16 v[208:209], v249 offset:24672\n\t" \
  "ds_read_b64_tr_b16 v[210:211], v249 offset:26208\n\t" \
  "s_waitcnt lgkmcnt(13)\n\t" \
  "v_fma_mix_f32 v221, |v179|, v221, v221 op_sel:[0,1,0] op_sel_hi:[0,1,1] clamp\n\t" \
  "v_fma_mix_f32 v220, |v178|, v220, v220 op_sel:[0,1,0] op_sel_hi:[0,1,1] clamp\n\t" \
  "s_waitcnt lgkmcnt(11)\n\t" \
  "v_mfma_f32_16x16x32_bf16 v[4:7], v[212:215], v[146:149], v[4:7]\n\t" \
  "ds_read_b32 v230, v230 offset:10016\n\t" \
  "v_fma_mix_f32 v223, |v177|, v223, v223 op_sel:[0,1,0] op_sel_hi:[0,1,1] clamp\n\t" \
  "v_fma_mix_f32 v222, |v176|, v222, v222 op_sel:[0,1,0] op_sel_hi:[0,1,1] clamp\n\t" \
  "v_mfma_f32_16x16x32_bf16 v[28:31], v[212:215], v[154:157], v[28:31]\n\t" \
  "ds_read_u16_d16_hi v233, v233 offset:10528\n\t" \
  "s_waitcnt lgkmcnt(7)\n\t" \
  "v_or_b32 v187, v224, v225\n\t" \
  "v_mfma_f32_16x16x32_bf16 v[52:55], v[212:215], v[162:165], v[52:55]\n\t" \
  "ds_read_u16 v232, v232 offset:10528\n\t" \
  "v_or_b32 v186, v226, v227\n\t" \
  "v_fma_f32 v179, |v179|, v221, v179 mul:4\n\t" \
  "v_mfma_f32_16x16x32_bf16 v[76:79], v[212:215], v[170:173], v[76:79]\n\t" \
  "ds_read_b64_tr_b16 v[212:213], v249 offset:24728\n\t" \
  "v_fma_f32 v178, |v178|, v220, v178 mul:4\n\t" \
  "v_fma_f32 v177, |v177|, v223, v177 mul:4\n\t" \
  "v_mfma_f32_16x16x32_bf16 v[8:11], v[216:219], v[146:149], v[8:11]\n\t" \
  "ds_read_b64_tr_b16 v[214:215], v249 offset:26264\n\t" \
  "v_fma_f32 v176, |v176|, v222, v176 mul:4\n\t" \
  "v_mfma_f32_16x16x32_bf16 v[32:35], v[216:219], v[154:157], v[32:35]\n\t" \
  "ds_read_u16_d16_hi v235, v235 offset:10528\n\t" \
  "v_cvt_pk_bf16_f32 v179, v178, v179\n\t" \
  "v_cvt_pk_bf16_f32 v178, v176, v177\n\t" \
  "v_mfma_f32_16x16x32_bf16 v[56:59], v[216:219], v[162:165], v[56:59]\n\t" \
  "ds_read_u16 v234, v234 offset:10528\n\t" \
  "v_cvt_pknorm_i16_f32 v220, |v180|, |v181|\n\t" \
  "s_cbranch_vccnz .Lcnx_p10_1\n\ts_setprio 1\n.Lcnx_p10_1:\n\t" \
  "v_cvt_pknorm_i16_f32 v222, |v182|, |v183|\n\t" \
  "v_mfma_f32_16x16x32_bf16 v[80:83], v[216:219], v[170:173], v[80:83]\n\t" \
  "ds_read_b64_tr_b16 v[216:217], v249 offset:24736\n\t" \
  "v_lshlrev_b32_sdwa v221, 2, v220 dst_sel:DWORD dst_unused:UNUSED_PAD src0_sel:DWORD src1_sel:BYTE_3\n\t" \
  "v_lshlrev_b32_sdwa v220, 2, v220 dst_sel:DWORD dst_unused:UNUSED_PAD src0_sel:DWORD src1_sel:BYTE_1\n\t" \
  "s_waitcnt lgkmcnt(8)\n\t" \
  "v_mfma_f32_16x16x32_bf16 v[12:15], v[208:211], v[146:149], v[12:15]\n\t" \
  "ds_read_b64_tr_b16 v[218:219], v249 offset:26272\n\t" \
  "v_lshlrev_b32_sdwa v223, 2, v222 dst_sel:DWORD dst_unused:UNUSED_PAD src0_sel:DWORD src1_sel:BYTE_3\n\t" \
  "v_mfma_f32_16x16x32_bf16 v[36:39], v[208:211], v[154:157], v[36:39]\n\t" \
  "ds_read_b32 v221, v221 offset:10016\n\t" \
  "v_lshlrev_b32_sdwa v222, 2, v222 dst_sel:DWORD dst_unused:UNUSED_PAD src0_sel:DWORD src1_sel:BYTE_1\n\t" \
  "v_lshlrev_b32_sdwa v225, 1, v189 dst_sel:DWORD dst_unused:UNUSED_PAD src0_sel:DWORD src1_sel:WORD_1\n\t" \
  "v_mfma_f32_16x16x32_bf16 v[60:63], v[208:211], v[162:165], v[60:63]\n\t" \
  "ds_read_b32 v220, v220 offset:10016\n\t" \
  "v_lshlrev_b32_sdwa v224, 1, v188 dst_sel:DWORD dst_unused:UNUSED_PAD src0_sel:DWORD src1_sel:WORD_1\n\t" \
  "v_lshlrev_b32_sdwa v227, 1, v191 dst_sel:DWORD dst_unused:UNUSED_PAD src0_sel:DWORD src1_sel:WORD_1\n\t" \
  "v_mfma_f32_16x16x32_bf16 v[84:87], v[208:211], v[170:173], v[84:87]\n\t" \
  "ds_read_b64_tr_b16 v[208:209], v248 offset:36904\n\t" \
  "ds_read_b64_tr_b16 v[210:211], v248 offset:37928\n\t" \
  "v_lshlrev_b32_sdwa v226, 1, v190 dst_sel:DWORD dst_unused:UNUSED_PAD src0_sel:DWORD src1_sel:WORD_1\n\t" \
  "s_waitcnt lgkmcnt(8)\n\t" \
  "v_mfma_f32_16x16x32_bf16 v[16:19], v[212:215], v[146:149], v[16:19]\n\t" \
  "ds_read_b32 v223, v223 offset:10016\n\t" \
  "v_fma_mix_f32 v229, |v195|, v229, v229 op_sel:[0,1,0] op_sel_hi:[0,1,1] clamp\n\t" \
  "v_fma_mix_f32 v228, |v194|, v228, v228 op_sel:[0,1,0] op_sel_hi:[0,1,1] clamp\n\t" \
  "v_mfma_f32_16x16x32_bf16 v[40:43], v[212:215], v[154:157], v[40:43]\n\t" \
  "ds_read_b32 v222, v222 offset:10016\n\t" \
  "v_fma_mix_f32 v231, |v193|, v231, v231 op_sel:[0,1,0] op_sel_hi:[0,1,1] clamp\n\t" \
  "v_fma_mix_f32 v230, |v192|, v230, v230 op_sel:[0,1,0] op_sel_hi:[0,1,1] clamp\n\t" \
  "v_mfma_f32_16x16x32_bf16 v[64:67], v[212:215], v[162:165], v[64:67]\n\t" \
  "ds_read_u16_d16_hi v225, v225 offset:10528\n\t" \
  "s_waitcnt lgkmcnt(9)\n\t" \
  "v_or_b32 v203, v232, v233\n\t" \
  "v_or_b32 v202, v234, v235\n\t" \
  "v_mfma_f32_16x16x32_bf16 v[88:91], v[212:215], v[170:173], v[88:91]\n\t" \
  "ds_read_b64_tr_b16 v[212:213], v248 offset:39976\n\t" \
  "v_fma_f32 v195, |v195|, v229, v195 mul:4\n\t" \
  "s_waitcnt lgkmcnt(8)\n\t" \
  "v_mfma_f32_16x16x32_bf16 v[20:23], v[216:219], v[146:149], v[20:23]\n\t" \
  "ds_read_b64_tr_b16 v[214:215], v248 offset:41000\n\t" \
  "v_fma_f32 v194, |v194|, v228, v194 mul:4\n\t" \
  "v_fma_f32 v193, |v193|, v231, v193 mul:4\n\t" \
  "v_mfma_f32_16x16x32_bf16 v[44:47], v[216:219], v[154:157], v[44:47]\n\t" \
  "ds_read_u16 v224, v224 offset:10528\n\t" \
  "v_fma_f32 v192, |v192|, v230, v192 mul:4\n\t" \
  "v_cvt_pk_bf16_f32 v195, v194, v195\n\t" \
  "v_mfma_f32_16x16x32_bf16 v[68:71], v[216:219], v[162:165], v[68:71]\n\t" \
  "ds_read_u16_d16_hi v227, v227 offset:10528\n\t" \
  "v_cvt_pk_bf16_f32 v194, v192, v193\n\t" \
  "v_cvt_pknorm_i16_f32 v228, |v196|, |v197|\n\t" \
  "v_mfma_f32_16x16x32_bf16 v[92:95], v[216:219], v[170:173], v[92:95]\n\t" \
  "ds_read_b64_tr_b16 v[216:217], v248 offset:38952\n\t" \
  "v_cvt_pknorm_i16_f32 v230, |v198|, |v199|\n\t" \
  "s_cbranch_vccnz .Lcnx_p10_2\n\ts_setprio 2\n.Lcnx_p10_2:\n\t" \
  "s_waitcnt lgkmcnt(8)\n\t" \
  "v_mfma_f32_16x16x32_bf16 v[144:147], v[208:211], v[96:99], 0\n\t" \
  "ds_read_b64_tr_b16 v[218:219], v248 offset:42024\n\t" \
  "v_lshlrev_b32_sdwa v229, 2, v228 dst_sel:DWORD dst_unused:UNUSED_PAD src0_sel:DWORD src1_sel:BYTE_3\n\t" \
  "v_lshlrev_b32_sdwa v228, 2, v228 dst_sel:DWORD dst_unused:UNUSED_PAD src0_sel:DWORD src1_sel:BYTE_1\n\t" \
  "v_mfma_f32_16x16x32_bf16 v[152:155], v[208:211], v[100:103], 0\n\t" \
  "ds_read_u16 v226, v226 offset:10528\n\t" \
  "v_lshlrev_b32_sdwa v231, 2, v230 dst_sel:DWORD dst_unused:UNUSED_PAD src0_sel:DWORD src1_sel:BYTE_3\n\t" \
  "v_lshlrev_b32_sdwa v230, 2, v230 dst_sel:DWORD dst_unused:UNUSED_PAD src0_sel:DWORD src1_sel:BYTE_1\n\t" \
  "v_mfma_f32_16x16x32_bf16 v[160:163], v[208:211], v[104:107], 0\n\t" \
  "ds_read_b32 v229, v229 offset:10016\n\t" \
  "v_lshlrev_b32_sdwa v233, 1, v205 dst_sel:DWORD dst_unused:UNUSED_PAD src0_sel:DWORD src1_sel:WORD_1\n\t" \
  "v_mfma_f32_16x16x32_bf16 v[168:171], v[208:211], v[108:111], 0\n\t" \
  "ds_read_b64_tr_b16 v[208:209], v248 offset:37416\n\t" \
  "v_lshlrev_b32_sdwa v232, 1, v204 dst_sel:DWORD dst_unused:UNUSED_PAD src0_sel:DWORD src1_sel:WORD_1\n\t" \
  "v_lshlrev_b32_sdwa v235, 1, v207 dst_sel:DWORD dst_unused:UNUSED_PAD src0_sel:DWORD src1_sel:WORD_1\n\t" \
  "s_waitcnt lgkmcnt(7)\n\t" \
  "v_mfma_f32_16x16x32_bf16 v[144:147], v[212:215], v[112:115], v[144:147]\n\t" \
  "ds_read_b64_tr_b16 v[210:211], v248 offset:38440\n\t" \
  "ds_read_b32 v228, v228 offset:10016\n\t" \
  "v_lshlrev_b32_sdwa v234, 1, v206 dst_sel:DWORD dst_unused:UNUSED_PAD src0_sel:DWORD src1_sel:WORD_1\n\t" \
  "v_fma_mix_f32 v221, |v181|, v221, v221 op_sel:[0,1,0] op_sel_hi:[0,1,1] clamp\n\t" \
  "v_mfma_f32_16x16x32_bf16 v[152:155], v[212:215], v[116:119], v[152:155]\n\t" \
  "ds_read_b32 v231, v231 offset:10016\n\t" \
  "ds_read_b32 v230, v230 offset:10016\n\t" \
  "v_fma_mix_f32 v220, |v180|, v220, v220 op_sel:[0,1,0] op_sel_hi:[0,1,1] clamp\n\t" \
  "v_fma_mix_f32 v223, |v183|, v223, v223 op_sel:[0,1,0] op_sel_hi:[0,1,1] clamp\n\t" \
  "v_mfma_f32_16x16x32_bf16 v[160:163], v[212:215], v[120:123], v[160:163]\n\t" \
  "ds_read_u16_d16_hi v233, v233 offset:10528\n\t" \
  "v_fma_mix_f32 v222, |v182|, v222, v222 op_sel:[0,1,0] op_sel_hi:[0,1,1] clamp\n\t" \
  "v_mfma_f32_16x16x32_bf16 v[168:171], v[212:215], v[124:127], v[168:171]\n\t" \
  "ds_read_b64_tr_b16 v[212:213], v248 offset:40488\n\t" \
  "s_waitcnt lgkmcnt(8)\n\t" \
  "v_or_b32 v188, v224, v225\n\t" \
  "v_or_b32 v189, v226, v227\n\t" \
  "v_mfma_f32_16x16x32_bf16 v[144:147], v[216:219], v[128:131], v[144:147]\n\t" \
  "ds_read_b64_tr_b16 v[214:215], v248 offset:41512\n\t" \
  "v_fma_f32 v181, |v181|, v221, v181 mul:4\n\t" \
  "v_fma_f32 v180, |v180|, v220, v180 mul:4\n\t" \
  "v_mfma_f32_16x16x32_bf16 v[152:155], v[216:219], v[132:135], v[152:155]\n\t" \
  "ds_read_u16 v232, v232 offset:10528\n\t" \
  "v_fma_f32 v183, |v183|, v223, v183 mul:4\n\t" \
  "v_mfma_f32_16x16x32_bf16 v[160:163], v[216:219], v[136:139], v[160:163]\n\t" \
  "ds_read_u16_d16_hi v235, v235 offset:10528\n\t" \
  "v_fma_f32 v182, |v182|, v222, v182 mul:4\n\t" \
  "v_cvt_pk_bf16_f32 v180, v180, v181\n\t" \
  "v_mfma_f32_16x16x32_bf16 v[168:171], v[216:219], v[140:143], v[168:171]\n\t" \
  "ds_read_b64_tr_b16 v[216:217], v248 offset:39464\n\t" \
  "v_cvt_pk_bf16_f32 v181, v182, v183\n\t" \
  "v_cvt_pknorm_i16_f32 v220, |v146|, |v147|\n\t" \
  "s_waitcnt lgkmcnt(9)\n\t" \
  "v_mfma_f32_16x16x32_bf16 v[148:151], v[208:211], v[96:99], 0\n\t" \
  "ds_read_b64_tr_b16 v[218:219], v248 offset:42536\n\t" \
  "v_cvt_pknorm_i16_f32 v222, |v144|, |v145|\n\t" \
  "s_cbranch_vccnz .Lcnx_p10_3\n\ts_setprio 3\n.Lcnx_p10_3:\n\t" \
  "v_lshlrev_b32_sdwa v221, 2, v220 dst_sel:DWORD dst_unused:UNUSED_PAD src0_sel:DWORD src1_sel:BYTE_3\n\t" \
  "v_mfma_f32_16x16x32_bf16 v[156:159], v[208:211], v[100:103], 0\n\t" \
  "s_waitcnt vmcnt(1)\n\t" \
  "s_cmp_lg_u32 s58, 1\n\t" \
  "s_cbranch_scc1 .Lcnx_c18\n\t" \
  "s_waitcnt vmcnt(0)\n\t" \
  ".Lcnx_c18:\n\t" \
  "s_mov_b64 exec, 3\n\t" \
  "ds_add_rtn_u32 v246, v251, v250 offset:47216\n\t" \
  "s_mov_b64 exec, -1\n\t" \
  "ds_read_u16 v234, v234 offset:10528\n\t" \
  "v_lshlrev_b32_sdwa v220, 2, v220 dst_sel:DWORD dst_unused:UNUSED_PAD src0_sel:DWORD src1_sel:BYTE_1\n\t" \
  "v_mfma_f32_16x16x32_bf16 v[164:167], v[208:211], v[104:107], 0\n\t" \
  "ds_read_b32 v221, v221 offset:10016\n\t" \
  "v_lshlrev_b32_sdwa v223, 2, v222 dst_sel:DWORD dst_unused:UNUSED_PAD src0_sel:DWORD src1_sel:BYTE_3\n\t" \
  "v_lshlrev_b32_sdwa v222, 2, v222 dst_sel:DWORD dst_unused:UNUSED_PAD src0_sel:DWORD src1_sel:BYTE_1\n\t" \
  "v_mfma_f32_16x16x32_bf16 v[172:175], v[208:211], v[108:111], 0\n\t" \
  "s_waitcnt lgkmcnt(2)\n\t" \
  "v_readfirstlane_b32 s4, v246\n\t" \
  "s_cmp_lt_u32 s4, s57\n\t" \
  "s_cbranch_scc1 .Lcnx_w120\n.Lcnx_b120:\n\t" \
  "ds_read_b64_tr_b16 v[208:209], v249 offset:30752\n\t" \
  "v_lshlrev_b32_sdwa v225, 1, v155 dst_sel:DWORD dst_unused:UNUSED_PAD src0_sel:DWORD src1_sel:WORD_1\n\t" \
  "v_lshlrev_b32_sdwa v224, 1, v154 dst_sel:DWORD dst_unused:UNUSED_PAD src0_sel:DWORD src1_sel:WORD_1\n\t" \
  "v_mfma_f32_16x16x32_bf16 v[148:151], v[212:215], v[112:115], v[148:151]\n\t" \
  "ds_read_b64_tr_b16 v[210:211], v249 offset:32288\n\t" \
  "v_lshlrev_b32_sdwa v227, 1, v153 dst_sel:DWORD dst_unused:UNUSED_PAD src0_sel:DWORD src1_sel:WORD_1\n\t" \
  "v_mfma_f32_16x16x32_bf16 v[156:159], v[212:215], v[116:119], v[156:159]\n\t" \
  "s_cmp_eq_u32 s37, 0\n\t" \
  "s_cbranch_scc1 .Lcnx_f11\n.Lcnx_g11:\n\t" \
  "ds_read_b32 v220, v220 offset:10016\n\t" \
  "v_lshlrev_b32_sdwa v226, 1, v152 dst_sel:DWORD dst_unused:UNUSED_PAD src0_sel:DWORD src1_sel:WORD_1\n\t" \
  "v_fma_mix_f32 v229, |v197|, v229, v229 op_sel:[0,1,0] op_sel_hi:[0,1,1] clamp\n\t" \
  "v_mfma_f32_16x16x32_bf16 v[164:167], v[212:215], v[120:123], v[164:167]\n\t" \
  "ds_read_b32 v223, v223 offset:10016\n\t" \
  "ds_read_b32 v222, v222 offset:10016\n\t" \
  "v_fma_mix_f32 v228, |v196|, v228, v228 op_sel:[0,1,0] op_sel_hi:[0,1,1] clamp\n\t" \
  "v_fma_mix_f32 v231, |v199|, v231, v231 op_sel:[0,1,0] op_sel_hi:[0,1,1] clamp\n\t" \
  "v_mfma_f32_16x16x32_bf16 v[172:175], v[212:215], v[124:127], v[172:175]\n\t" \
  "ds_read_b64_tr_b16 v[212:213], v249 offset:30760\n\t" \
  "ds_read_b64_tr_b16 v[214:215], v249 offset:32296\n\t" \
  "v_fma_mix_f32 v230, |v198|, v230, v230 op_sel:[0,1,0] op_sel_hi:[0,1,1] clamp\n\t" \
  "s_waitcnt lgkmcnt(8)\n\t" \
  "v_or_b32 v204, v232, v233\n\t" \
  "v_mfma_f32_16x16x32_bf16 v[148:151], v[216:219], v[128:131], v[148:151]\n\t" \
  "ds_read_u16_d16_hi v225, v225 offset:10528\n\t" \
  "v_or_b32 v205, v234, v235\n\t" \
  "v_mfma_f32_16x16x32_bf16 v[156:159], v[216:219], v[132:135], v[156:159]\n\t" \
  "ds_read_u16 v224, v224 offset:10528\n\t" \
  "v_fma_f32 v197, |v197|, v229, v197 mul:4\n\t" \
  "v_fma_f32 v196, |v196|, v228, v196 mul:4\n\t" \
  "v_mfma_f32_16x16x32_bf16 v[164:167], v[216:219], v[136:139], v[164:167]\n\t" \
  "ds_read_u16_d16_hi v227, v227 offset:10528\n\t" \
  "v_fma_f32 v199, |v199|, v231, v199 mul:4\n\t" \
  "v_fma_f32 v198, |v198|, v230, v198 mul:4\n\t" \
  "v_mfma_f32_16x16x32_bf16 v[172:175], v[216:219], v[140:143], v[172:175]\n\t" \
  "ds_read_b64_tr_b16 v[216:217], v249 offset:30816\n\t" \
  "v_cvt_pk_bf16_f32 v196, v196, v197\n\t" \
  "v_cvt_pk_bf16_f32 v197, v198, v199\n\t" \
  "s_cbranch_vccnz .Lcnx_p11_0\n\ts_setprio 0\n.Lcnx_p11_0:\n\t" \
  "v_cvt_pknorm_i16_f32 v228, |v162|, |v163|\n\t" \
  "v_cvt_pknorm_i16_f32 v230, |v160|, |v161|\n\t" \
  "v_mfma_f32_16x16x32_bf16 v[72:75], v[242:245], v[236:239], v[72:75]\n\t" \
  "ds_read_b64_tr_b16 v[218:219], v249 offset:32352\n\t" \
  "v_lshlrev_b32_sdwa v229, 2, v228 dst_sel:DWORD dst_unused:UNUSED_PAD src0_sel:DWORD src1_sel:BYTE_3\n\t" \
  "v_mfma_f32_16x16x32_bf16 v[76:79], v[240:243], v[236:239], v[76:79]\n\t" \
  "buffer_load_dwordx4 v[236:239], v250, s[8:11], s13 offen offset:64 nt\n\t" \
  "s_cmp_lg_u32 s38, 4\n\t" \
  "s_cbranch_scc1 .Lcnx_c30\n\t" \
  "s_mov_b32 m0, 107744\n\t" \
  "s_add_u32 s30, s30, s31\n\t" \
  "buffer_load_dword v252, s[8:11], s30 offen lds\n\t" \
  ".Lcnx_c30:\n\t" \
  "ds_read_u16 v226, v226 offset:10528\n\t" \
  "v_lshlrev_b32_sdwa v228, 2, v228 dst_sel:DWORD dst_unused:UNUSED_PAD src0_sel:DWORD src1_sel:BYTE_1\n\t" \
  "v_lshlrev_b32_sdwa v231, 2, v230 dst_sel:DWORD dst_unused:UNUSED_PAD src0_sel:DWORD src1_sel:BYTE_3\n\t" \
  "s_waitcnt lgkmcnt(11)\n\t" \
  "v_mfma_f32_16x16x32_bf16 v[0:3], v[208:211], v[178:181], v[0:3]\n\t" \
  "ds_read_b32 v229, v229 offset:10016\n\t" \
  "v_lshlrev_b32_sdwa v230, 2, v230 dst_sel:DWORD dst_unused:UNUSED_PAD src0_sel:DWORD src1_sel:BYTE_1\n\t" \
  "v_lshlrev_b32_sdwa v233, 1, v171 dst_sel:DWORD dst_unused:UNUSED_PAD src0_sel:DWORD src1_sel:WORD_1\n\t" \
  "v_mfma_f32_16x16x32_bf16 v[24:27], v[208:211], v[186:189], v[24:27]\n\t" \
  "ds_read_b32 v228, v228 offset:10016\n\t" \
  "v_lshlrev_b32_sdwa v232, 1, v170 dst_sel:DWORD dst_unused:UNUSED_PAD src0_sel:DWORD src1_sel:WORD_1\n\t" \
  "v_mfma_f32_16x16x32_bf16 v[48:51], v[208:211], v[194:197], v[48:51]\n\t" \
  "ds_read_b32 v231, v231 offset:10016\n\t" \
  "v_lshlrev_b32_sdwa v235, 1, v169 dst_sel:DWORD dst_unused:UNUSED_PAD src0_sel:DWORD src1_sel:WORD_1\n\t" \
  "v_lshlrev_b32_sdwa v234, 1, v168 dst_sel:DWORD dst_unused:UNUSED_PAD src0_sel:DWORD src1_sel:WORD_1\n\t" \
  "v_mfma_f32_16x16x32_bf16 v[72:75], v[208:211], v[202:205], v[72:75]\n\t" \
  "ds_read_b64_tr_b16 v[208:209], v249 offset:30824\n\t" \
  "ds_read_b64_tr_b16 v[210:211], v249 offset:32360\n\t" \
  "s_waitcnt lgkmcnt(13)\n\t" \
  "v_fma_mix_f32 v221, |v147|, v221, v221 op_sel:[0,1,0] op_sel_hi:[0,1,1] clamp\n\t" \
  "v_fma_mix_f32 v220, |v146|, v220, v220 op_sel:[0,1,0] op_sel_hi:[0,1,1] clamp\n\t" \
  "s_waitcnt lgkmcnt(11)\n\t" \
  "v_mfma_f32_16x16x32_bf16 v[4:7], v[212:215], v[178:181], v[4:7]\n\t" \
  "ds_read_b32 v230, v230 offset:10016\n\t" \
  "v_fma_mix_f32 v223, |v145|, v223, v223 op_sel:[0,1,0] op_sel_hi:[0,1,1] clamp\n\t" \
  "v_fma_mix_f32 v222, |v144|, v222, v222 op_sel:[0,1,0] op_sel_hi:[0,1,1] clamp\n\t" \
  "v_mfma_f32_16x16x32_bf16 v[28:31], v[212:215], v[186:189], v[28:31]\n\t" \
  "ds_read_u16_d16_hi v233, v233 offset:10528\n\t" \
  "s_waitcnt lgkmcnt(7)\n\t" \
  "v_or_b32 v155, v224, v225\n\t" \
  "v_mfma_f32_16x16x32_bf16 v[52:55], v[212:215], v[194:197], v[52:55]\n\t" \
  "ds_read_u16 v232, v232 offset:10528\n\t" \
  "v_or_b32 v154, v226, v227\n\t" \
  "v_fma_f32 v147, |v147|, v221, v147 mul:4\n\t" \
  "v_mfma_f32_16x16x32_bf16 v[76:79], v[212:215], v[202:205], v[76:79]\n\t" \
  "ds_read_b64_tr_b16 v[212:213], v249 offset:30880\n\t" \
  "v_fma_f32 v146, |v146|, v220, v146 mul:4\n\t" \
  "v_fma_f32 v145, |v145|, v223, v145 mul:4\n\t" \
  "v_mfma_f32_16x16x32_bf16 v[8:11], v[216:219], v[178:181], v[8:11]\n\t" \
  "ds_read_b64_tr_b16 v[214:215], v249 offset:32416\n\t" \
  "v_fma_f32 v144, |v144|, v222, v144 mul:4\n\t" \
  "v_mfma_f32_16x16x32_bf16 v[32:35], v[216:219], v[186:189], v[32:35]\n\t" \
  "ds_read_u16_d16_hi v235, v235 offset:10528\n\t" \
  "v_cvt_pk_bf16_f32 v147, v146, v147\n\t" \
  "v_cvt_pk_bf16_f32 v146, v144, v145\n\t" \
  "v_mfma_f32_16x16x32_bf16 v[56:59], v[216:219], v[194:197], v[56:59]\n\t" \
  "ds_read_u16 v234, v234 offset:10528\n\t" \
  "v_cvt_pknorm_i16_f32 v220, |v148|, |v149|\n\t" \
  "s_cbranch_vccnz .Lcnx_p11_1\n\ts_setprio 1\n.Lcnx_p11_1:\n\t" \
  "v_cvt_pknorm_i16_f32 v222, |v150|, |v151|\n\t" \
  "v_mfma_f32_16x16x32_bf16 v[80:83], v[216:219], v[202:205], v[80:83]\n\t" \
  "ds_read_b64_tr_b16 v[216:217], v249 offset:30888\n\t" \
  "v_lshlrev_b32_sdwa v221, 2, v220 dst_sel:DWORD dst_unused:UNUSED_PAD src0_sel:DWORD src1_sel:BYTE_3\n\t" \
  "v_lshlrev_b32_sdwa v220, 2, v220 dst_sel:DWORD dst_unused:UNUSED_PAD src0_sel:DWORD src1_sel:BYTE_1\n\t" \
  "s_waitcnt lgkmcnt(8)\n\t" \
  "v_mfma_f32_16x16x32_bf16 v[12:15], v[208:211], v[178:181], v[12:15]\n\t" \
  "ds_read_b64_tr_b16 v[218:219], v249 offset:32424\n\t" \
  "v_lshlrev_b32_sdwa v223, 2, v222 dst_sel:DWORD dst_unused:UNUSED_PAD src0_sel:DWORD src1_sel:BYTE_3\n\t" \
  "v_mfma_f32_16x16x32_bf16 v[36:39], v[208:211], v[186:189], v[36:39]\n\t" \
  "ds_read_b32 v221, v221 offset:10016\n\t" \
  "v_lshlrev_b32_sdwa v222, 2, v222 dst_sel:DWORD dst_unused:UNUSED_PAD src0_sel:DWORD src1_sel:BYTE_1\n\t" \
  "v_lshlrev_b32_sdwa v225, 1, v157 dst_sel:DWORD dst_unused:UNUSED_PAD src0_sel:DWORD src1_sel:WORD_1\n\t" \
  "v_mfma_f32_16x16x32_bf16 v[60:63], v[208:211], v[194:197], v[60:63]\n\t" \
  "ds_read_b32 v220, v220 offset:10016\n\t" \
  "v_lshlrev_b32_sdwa v224, 1, v156 dst_sel:DWORD dst_unused:UNUSED_PAD src0_sel:DWORD src1_sel:WORD_1\n\t" \
  "v_lshlrev_b32_sdwa v227, 1, v159 dst_sel:DWORD dst_unused:UNUSED_PAD src0_sel:DWORD src1_sel:WORD_1\n\t" \
  "v_mfma_f32_16x16x32_bf16 v[84:87], v[208:211], v[202:205], v[84:87]\n\t" \
  "ds_read_b64_tr_b16 v[208:209], v248 offset:0\n\t" \
  "ds_read_b64_tr_b16 v[210:211], v248 offset:1024\n\t" \
  "v_lshlrev_b32_sdwa v226, 1, v158 dst_sel:DWORD dst_unused:UNUSED_PAD src0_sel:DWORD src1_sel:WORD_1\n\t" \
  "s_waitcnt lgkmcnt(8)\n\t" \
  "v_mfma_f32_16x16x32_bf16 v[16:19], v[212:215], v[178:181], v[16:19]\n\t" \
  "ds_read_b32 v223, v223 offset:10016\n\t" \
  "v_fma_mix_f32 v229, |v163|, v229, v229 op_sel:[0,1,0] op_sel_hi:[0,1,1] clamp\n\t" \
  "v_fma_mix_f32 v228, |v162|, v228, v228 op_sel:[0,1,0] op_sel_hi:[0,1,1] clamp\n\t" \
  "v_mfma_f32_16x16x32_bf16 v[40:43], v[212:215], v[186:189], v[40:43]\n\t" \
  "ds_read_b32 v222, v222 offset:10016\n\t" \
  "v_fma_mix_f32 v231, |v161|, v231, v231 op_sel:[0,1,0] op_sel_hi:[0,1,1] clamp\n\t" \
  "v_fma_mix_f32 v230, |v160|, v230, v230 op_sel:[0,1,0] op_sel_hi:[0,1,1] clamp\n\t" \
  "v_mfma_f32_16x16x32_bf16 v[64:67], v[212:215], v[194:197], v[64:67]\n\t" \
  "ds_read_u16_d16_hi v225, v225 offset:10528\n\t" \
  "s_waitcnt lgkmcnt(9)\n\t" \
  "v_or_b32 v171, v232, v233\n\t" \
  "v_or_b32 v170, v234, v235\n\t" \
  "v_mfma_f32_16x16x32_bf16 v[88:91], v[212:215], v[202:205], v[88:91]\n\t" \
  "ds_read_b64_tr_b16 v[212:213], v248 offset:3072\n\t" \
  "v_fma_f32 v163, |v163|, v229, v163 mul:4\n\t" \
  "s_waitcnt lgkmcnt(8)\n\t" \
  "v_mfma_f32_16x16x32_bf16 v[20:23], v[216:219], v[178:181], v[20:23]\n\t" \
  "ds_read_b64_tr_b16 v[214:215], v248 offset:4096\n\t" \
  "v_fma_f32 v162, |v162|, v228, v162 mul:4\n\t" \
  "v_fma_f32 v161, |v161|, v231, v161 mul:4\n\t" \
  "v_mfma_f32_16x16x32_bf16 v[44:47], v[216:219], v[186:189], v[44:47]\n\t" \
  "ds_read_u16 v224, v224 offset:10528\n\t" \
  "v_fma_f32 v160, |v160|, v230, v160 mul:4\n\t" \
  "v_cvt_pk_bf16_f32 v163, v162, v163\n\t" \
  "v_mfma_f32_16x16x32_bf16 v[68:71], v[216:219], v[194:197], v[68:71]\n\t" \
  "ds_read_u16_d16_hi v227, v227 offset:10528\n\t" \
  "v_cvt_pk_bf16_f32 v162, v160, v161\n\t" \
  "v_cvt_pknorm_i16_f32 v228, |v164|, |v165|\n\t" \
  "v_mfma_f32_16x16x32_bf16 v[92:95], v[216:219], v[202:205], v[92:95]\n\t" \
  "ds_read_b64_tr_b16 v[216:217], v248 offset:2048\n\t" \
  "v_cvt_pknorm_i16_f32 v230, |v166|, |v167|\n\t" \
  "s_cbranch_vccnz .Lcnx_p11_2\n\ts_setprio 2\n.Lcnx_p11_2:\n\t" \
  "s_waitcnt lgkmcnt(8)\n\t" \
  "v_mfma_f32_16x16x32_bf16 v[176:179], v[208:211], v[96:99], 0\n\t" \
  "ds_read_b64_tr_b16 v[218:219], v248 offset:5120\n\t" \
  "v_lshlrev_b32_sdwa v229, 2, v228 dst_sel:DWORD dst_unused:UNUSED_PAD src0_sel:DWORD src1_sel:BYTE_3\n\t" \
  "v_lshlrev_b32_sdwa v228, 2, v228 dst_sel:DWORD dst_unused:UNUSED_PAD src0_sel:DWORD src1_sel:BYTE_1\n\t" \
  "v_mfma_f32_16x16x32_bf16 v[184:187], v[208:211], v[100:103], 0\n\t" \
  "ds_read_u16 v226, v226 offset:10528\n\t" \
  "v_lshlrev_b32_sdwa v231, 2, v230 dst_sel:DWORD dst_unused:UNUSED_PAD src0_sel:DWORD src1_sel:BYTE_3\n\t" \
  "v_lshlrev_b32_sdwa v230, 2, v230 dst_sel:DWORD dst_unused:UNUSED_PAD src0_sel:DWORD src1_sel:BYTE_1\n\t" \
  "v_mfma_f32_16x16x32_bf16 v[192:195], v[208:211], v[104:107], 0\n\t" \
  "ds_read_b32 v229, v229 offset:10016\n\t" \
  "v_lshlrev_b32_sdwa v233, 1, v173 dst_sel:DWORD dst_unused:UNUSED_PAD src0_sel:DWORD src1_sel:WORD_1\n\t" \
  "v_mfma_f32_16x16x32_bf16 v[200:203], v[208:211], v[108:111], 0\n\t" \
  "ds_read_b64_tr_b16 v[208:209], v248 offset:512\n\t" \
  "v_lshlrev_b32_sdwa v232, 1, v172 dst_sel:DWORD dst_unused:UNUSED_PAD src0_sel:DWORD src1_sel:WORD_1\n\t" \
  "v_lshlrev_b32_sdwa v235, 1, v175 dst_sel:DWORD dst_unused:UNUSED_PAD src0_sel:DWORD src1_sel:WORD_1\n\t" \
  "s_waitcnt lgkmcnt(7)\n\t" \
  "v_mfma_f32_16x16x32_bf16 v[176:179], v[212:215], v[112:115], v[176:179]\n\t" \
  "ds_read_b64_tr_b16 v[210:211], v248 offset:1536\n\t" \
  "ds_read_b32 v228, v228 offset:10016\n\t" \
  "v_lshlrev_b32_sdwa v234, 1, v174 dst_sel:DWORD dst_unused:UNUSED_PAD src0_sel:DWORD src1_sel:WORD_1\n\t" \
  "v_fma_mix_f32 v221, |v149|, v221, v221 op_sel:[0,1,0] op_sel_hi:[0,1,1] clamp\n\t" \
  "v_mfma_f32_16x16x32_bf16 v[184:187], v[212:215], v[116:119], v[184:187]\n\t" \
  "ds_read_b32 v231, v231 offset:10016\n\t" \
  "ds_read_b32 v230, v230 offset:10016\n\t" \
  "v_fma_mix_f32 v220, |v148|, v220, v220 op_sel:[0,1,0] op_sel_hi:[0,1,1] clamp\n\t" \
  "v_fma_mix_f32 v223, |v151|, v223, v223 op_sel:[0,1,0] op_sel_hi:[0,1,1] clamp\n\t" \
  "v_mfma_f32_16x16x32_bf16 v[192:195], v[212:215], v[120:123], v[192:195]\n\t" \
  "ds_read_u16_d16_hi v233, v233 offset:10528\n\t" \
  "v_fma_mix_f32 v222, |v150|, v222, v222 op_sel:[0,1,0] op_sel_hi:[0,1,1] clamp\n\t" \
  "v_mfma_f32_16x16x32_bf16 v[200:203], v[212:215], v[124:127], v[200:203]\n\t" \
  "ds_read_b64_tr_b16 v[212:213], v248 offset:3584\n\t" \
  "s_waitcnt lgkmcnt(8)\n\t" \
  "v_or_b32 v156, v224, v225\n\t" \
  "v_or_b32 v157, v226, v227\n\t" \
  "v_mfma_f32_16x16x32_bf16 v[176:179], v[216:219], v[128:131], v[176:179]\n\t" \
  "ds_read_b64_tr_b16 v[214:215], v248 offset:4608\n\t" \
  "v_fma_f32 v149, |v149|, v221, v149 mul:4\n\t" \
  "v_fma_f32 v148, |v148|, v220, v148 mul:4\n\t" \
  "v_mfma_f32_16x16x32_bf16 v[184:187], v[216:219], v[132:135], v[184:187]\n\t" \
  "ds_read_u16 v232, v232 offset:10528\n\t" \
  "v_fma_f32 v151, |v151|, v223, v151 mul:4\n\t" \
  "v_mfma_f32_16x16x32_bf16 v[192:195], v[216:219], v[136:139], v[192:195]\n\t" \
  "ds_read_u16_d16_hi v235, v235 offset:10528\n\t" \
  "v_fma_f32 v150, |v150|, v222, v150 mul:4\n\t" \
  "v_cvt_pk_bf16_f32 v148, v148, v149\n\t" \
  "v_mfma_f32_16x16x32_bf16 v[200:203], v[216:219], v[140:143], v[200:203]\n\t" \
  "ds_read_b64_tr_b16 v[216:217], v248 offset:2560\n\t" \
  "v_cvt_pk_bf16_f32 v149, v150, v151\n\t" \
  "v_cvt_pknorm_i16_f32 v220, |v178|, |v179|\n\t" \
  "s_waitcnt lgkmcnt(9)\n\t" \
  "v_mfma_f32_16x16x32_bf16 v[180:183], v[208:211], v[96:99], 0\n\t" \
  "ds_read_b64_tr_b16 v[218:219], v248 offset:5632\n\t" \
  "v_cvt_pknorm_i16_f32 v222, |v176|, |v177|\n\t" \
  "s_cbranch_vccnz .Lcnx_p11_3\n\ts_setprio 3\n.Lcnx_p11_3:\n\t" \
  "v_lshlrev_b32_sdwa v221, 2, v220 dst_sel:DWORD dst_unused:UNUSED_PAD src0_sel:DWORD src1_sel:BYTE_3\n\t" \
  "v_mfma_f32_16x16x32_bf16 v[188:191], v[208:211], v[100:103], 0\n\t" \
  "s_waitcnt vmcnt(0)\n\t" \
  "s_mov_b64 exec, 3\n\t" \
  "ds_add_rtn_u32 v246, v251, v250 offset:47224\n\t" \
  "s_mov_b64 exec, -1\n\t" \
  "ds_read_u16 v234, v234 offset:10528\n\t" \
  "v_lshlrev_b32_sdwa v220, 2, v220 dst_sel:DWORD dst_unused:UNUSED_PAD src0_sel:DWORD src1_sel:BYTE_1\n\t" \
  "v_mfma_f32_16x16x32_bf16 v[196:199], v[208:211], v[104:107], 0\n\t" \
  "ds_read_b32 v221, v221 offset:10016\n\t" \
  "v_lshlrev_b32_sdwa v223, 2, v222 dst_sel:DWORD dst_unused:UNUSED_PAD src0_sel:DWORD src1_sel:BYTE_3\n\t" \
  "v_lshlrev_b32_sdwa v222, 2, v222 dst_sel:DWORD dst_unused:UNUSED_PAD src0_sel:DWORD src1_sel:BYTE_1\n\t" \
  "v_mfma_f32_16x16x32_bf16 v[204:207], v[208:211], v[108:111], 0\n\t" \
  "s_waitcnt lgkmcnt(2)\n\t" \
  "v_readfirstlane_b32 s4, v246\n\t" \
  "s_cmp_lt_u32 s4, s57\n\t" \
  "s_cbranch_scc1 .Lcnx_w132\n.Lcnx_b132:\n\t" \
  "ds_read_b64_tr_b16 v[208:209], v249 offset:6152\n\t" \
  "v_lshlrev_b32_sdwa v225, 1, v187 dst_sel:DWORD dst_unused:UNUSED_PAD src0_sel:DWORD src1_sel:WORD_1\n\t" \
  "v_lshlrev_b32_sdwa v224, 1, v186 dst_sel:DWORD dst_unused:UNUSED_PAD src0_sel:DWORD src1_sel:WORD_1\n\t" \
  "v_mfma_f32_16x16x32_bf16 v[180:183], v[212:215], v[112:115], v[180:183]\n\t" \
  "ds_read_b64_tr_b16 v[210:211], v249 offset:7688\n\t" \
  "v_lshlrev_b32_sdwa v227, 1, v185 dst_sel:DWORD dst_unused:UNUSED_PAD src0_sel:DWORD src1_sel:WORD_1\n\t" \
  "v_mfma_f32_16x16x32_bf16 v[188:191], v[212:215], v[116:119], v[188:191]\n\t" \
  "ds_read_b32 v220, v220 offset:10016\n\t" \
  "v_lshlrev_b32_sdwa v226, 1, v184 dst_sel:DWORD dst_unused:UNUSED_PAD src0_sel:DWORD src1_sel:WORD_1\n\t" \
  "v_fma_mix_f32 v229, |v165|, v229, v229 op_sel:[0,1,0] op_sel_hi:[0,1,1] clamp\n\t" \
  "v_mfma_f32_16x16x32_bf16 v[196:199], v[212:215], v[120:123], v[196:199]\n\t" \
  "ds_read_b32 v223, v223 offset:10016\n\t" \
  "ds_read_b32 v222, v222 offset:10016\n\t" \
  "v_fma_mix_f32 v228, |v164|, v228, v228 op_sel:[0,1,0] op_sel_hi:[0,1,1] clamp\n\t" \
  "v_fma_mix_f32 v231, |v167|, v231, v231 op_sel:[0,1,0] op_sel_hi:[0,1,1] clamp\n\t" \
  "v_mfma_f32_16x16x32_bf16 v[204:207], v[212:215], v[124:127], v[204:207]\n\t" \
  "ds_read_b64_tr_b16 v[212:213], v249 offset:6160\n\t" \
  "ds_read_b64_tr_b16 v[214:215], v249 offset:7696\n\t" \
  "v_fma_mix_f32 v230, |v166|, v230, v230 op_sel:[0,1,0] op_sel_hi:[0,1,1] clamp\n\t" \
  "s_waitcnt lgkmcnt(8)\n\t" \
  "v_or_b32 v172, v232, v233\n\t" \
  "v_mfma_f32_16x16x32_bf16 v[180:183], v[216:219], v[128:131], v[180:183]\n\t" \
  "ds_read_u16_d16_hi v225, v225 offset:10528\n\t" \
  "v_or_b32 v173, v234, v235\n\t" \
  "v_mfma_f32_16x16x32_bf16 v[188:191], v[216:219], v[132:135], v[188:191]\n\t" \
  "ds_read_u16 v224, v224 offset:10528\n\t" \
  "v_fma_f32 v165, |v165|, v229, v165 mul:4\n\t" \
  "v_fma_f32 v164, |v164|, v228, v164 mul:4\n\t" \
  "v_mfma_f32_16x16x32_bf16 v[196:199], v[216:219], v[136:139], v[196:199]\n\t" \
  "ds_read_u16_d16_hi v227, v227 offset:10528\n\t" \
  "v_fma_f32 v167, |v167|, v231, v167 mul:4\n\t" \
  "v_fma_f32 v166, |v166|, v230, v166 mul:4\n\t" \
  "v_mfma_f32_16x16x32_bf16 v[204:207], v[216:219], v[140:143], v[204:207]\n\t" \
  "ds_read_b64_tr_b16 v[216:217], v249 offset:6216\n\t" \
  "v_cvt_pk_bf16_f32 v164, v164, v165\n\t" \
  "v_cvt_pk_bf16_f32 v165, v166, v167\n\t" \
  "s_cbranch_vccnz .Lcnx_p12_0\n\ts_setprio 3\n.Lcnx_p12_0:\n\t" \
  "v_cvt_pknorm_i16_f32 v228, |v194|, |v195|\n\t" \
  "v_cvt_pknorm_i16_f32 v230, |v192|, |v193|\n\t" \
  "v_mfma_f32_16x16x32_bf16 v[80:83], v[242:245], v[236:239], v[80:83]\n\t" \
  "ds_read_b64_tr_b16 v[218:219], v249 offset:7752\n\t" \
  "v_lshlrev_b32_sdwa v229, 2, v228 dst_sel:DWORD dst_unused:UNUSED_PAD src0_sel:DWORD src1_sel:BYTE_3\n\t" \
  "v_lshlrev_b32_sdwa v228, 2, v228 dst_sel:DWORD dst_unused:UNUSED_PAD src0_sel:DWORD src1_sel:BYTE_1\n\t" \
  "v_mfma_f32_16x16x32_bf16 v[84:87], v[240:243], v[236:239], v[84:87]\n\t" \
  "buffer_load_dwordx4 v[236:239], v250, s[8:11], s13 offen offset:128 nt\n\t" \
  "s_cmp_lg_u32 s38, 5\n\t" \
  "s_cbranch_scc1 .Lcnx_c31\n\t" \
  "s_mov_b32 m0, 107744\n\t" \
  "s_add_u32 s30, s30, s31\n\t" \
  "buffer_load_dword v252, s[8:11], s30 offen lds\n\t" \
  ".Lcnx_c31:\n\t" \
  "ds_read_u16 v226, v226 offset:10528\n\t" \
  "v_lshlrev_b32_sdwa v231, 2, v230 dst_sel:DWORD dst_unused:UNUSED_PAD src0_sel:DWORD src1_sel:BYTE_3\n\t" \
  "v_lshlrev_b32_sdwa v230, 2, v230 dst_sel:DWORD dst_unused:UNUSED_PAD src0_sel:DWORD src1_sel:BYTE_1\n\t" \
  "v_lshlrev_b32_sdwa v233, 1, v203 dst_sel:DWORD dst_unused:UNUSED_PAD src0_sel:DWORD src1_sel:WORD_1\n\t" \
  "s_waitcnt lgkmcnt(11)\n\t" \
  "v_mfma_f32_16x16x32_bf16 v[0:3], v[208:211], v[146:149], v[0:3]\n\t" \
  "ds_read_b32 v229, v229 offset:10016\n\t" \
  "v_lshlrev_b32_sdwa v232, 1, v202 dst_sel:DWORD dst_unused:UNUSED_PAD src0_sel:DWORD src1_sel:WORD_1\n\t" \
  "v_lshlrev_b32_sdwa v235, 1, v201 dst_sel:DWORD dst_unused:UNUSED_PAD src0_sel:DWORD src1_sel:WORD_1\n\t" \
  "v_lshlrev_b32_sdwa v234, 1, v200 dst_sel:DWORD dst_unused:UNUSED_PAD src0_sel:DWORD src1_sel:WORD_1\n\t" \
  "v_mfma_f32_16x16x32_bf16 v[24:27], v[208:211], v[154:157], v[24:27]\n\t" \
  "ds_read_b32 v228, v228 offset:10016\n\t" \
  "ds_read_b32 v231, v231 offset:10016\n\t" \
  "s_waitcnt lgkmcnt(11)\n\t" \
  "v_fma_mix_f32 v221, |v179|, v221, v221 op_sel:[0,1,0] op_sel_hi:[0,1,1] clamp\n\t" \
  "v_fma_mix_f32 v220, |v178|, v220, v220 op_sel:[0,1,0] op_sel_hi:[0,1,1] clamp\n\t" \
  "v_fma_mix_f32 v223, |v177|, v223, v223 op_sel:[0,1,0] op_sel_hi:[0,1,1] clamp\n\t" \
  "v_mfma_f32_16x16x32_bf16 v[48:51], v[208:211], v[162:165], v[48:51]\n\t" \
  "ds_read_b32 v230, v230 offset:10016\n\t" \
  "v_fma_mix_f32 v222, |v176|, v222, v222 op_sel:[0,1,0] op_sel_hi:[0,1,1] clamp\n\t" \
  "s_waitcnt lgkmcnt(4)\n\t" \
  "v_or_b32 v187, v224, v225\n\t" \
  "v_or_b32 v186, v226, v227\n\t" \
  "v_mfma_f32_16x16x32_bf16 v[72:75], v[208:211], v[170:173], v[72:75]\n\t" \
  "ds_read_b64_tr_b16 v[208:209], v249 offset:6224\n\t" \
  "ds_read_b64_tr_b16 v[210:211], v249 offset:7760\n\t" \
  "v_fma_f32 v179, |v179|, v221, v179 mul:4\n\t" \
  "v_fma_f32 v178, |v178|, v220, v178 mul:4\n\t" \
  "v_fma_f32 v177, |v177|, v223, v177 mul:4\n\t" \
  "v_mfma_f32_16x16x32_bf16 v[4:7], v[212:215], v[146:149], v[4:7]\n\t" \
  "ds_read_u16_d16_hi v233, v233 offset:10528\n\t" \
  "v_fma_f32 v176, |v176|, v222, v176 mul:4\n\t" \
  "s_cbranch_vccnz .Lcnx_p12_1\n\ts_setprio 2\n.Lcnx_p12_1:\n\t" \
  "v_cvt_pk_bf16_f32 v179, v178, v179\n\t" \
  "v_cvt_pk_bf16_f32 v178, v176, v177\n\t" \
  "v_mfma_f32_16x16x32_bf16 v[28:31], v[212:215], v[154:157], v[28:31]\n\t" \
  "ds_read_u16 v232, v232 offset:10528\n\t" \
  "v_cvt_pknorm_i16_f32 v220, |v180|, |v181|\n\t" \
  "v_cvt_pknorm_i16_f32 v222, |v182|, |v183|\n\t" \
  "v_lshlrev_b32_sdwa v221, 2, v220 dst_sel:DWORD dst_unused:UNUSED_PAD src0_sel:DWORD src1_sel:BYTE_3\n\t" \
  "v_mfma_f32_16x16x32_bf16 v[52:55], v[212:215], v[162:165], v[52:55]\n\t" \
  "ds_read_u16_d16_hi v235, v235 offset:10528\n\t" \
  "v_lshlrev_b32_sdwa v220, 2, v220 dst_sel:DWORD dst_unused:UNUSED_PAD src0_sel:DWORD src1_sel:BYTE_1\n\t" \
  "v_lshlrev_b32_sdwa v223, 2, v222 dst_sel:DWORD dst_unused:UNUSED_PAD src0_sel:DWORD src1_sel:BYTE_3\n\t" \
  "v_lshlrev_b32_sdwa v222, 2, v222 dst_sel:DWORD dst_unused:UNUSED_PAD src0_sel:DWORD src1_sel:BYTE_1\n\t" \
  "v_mfma_f32_16x16x32_bf16 v[76:79], v[212:215], v[170:173], v[76:79]\n\t" \
  "ds_read_b64_tr_b16 v[212:213], v249 offset:6280\n\t" \
  "ds_read_b64_tr_b16 v[214:215], v249 offset:7816\n\t" \
  "v_lshlrev_b32_sdwa v225, 1, v189 dst_sel:DWORD dst_unused:UNUSED_PAD src0_sel:DWORD src1_sel:WORD_1\n\t" \
  "v_lshlrev_b32_sdwa v224, 1, v188 dst_sel:DWORD dst_unused:UNUSED_PAD src0_sel:DWORD src1_sel:WORD_1\n\t" \
  "v_lshlrev_b32_sdwa v227, 1, v191 dst_sel:DWORD dst_unused:UNUSED_PAD src0_sel:DWORD src1_sel:WORD_1\n\t" \
  "v_mfma_f32_16x16x32_bf16 v[8:11], v[216:219], v[146:149], v[8:11]\n\t" \
  "ds_read_u16 v234, v234 offset:10528\n\t" \
  "ds_read_b32 v221, v221 offset:10016\n\t" \
  "v_lshlrev_b32_sdwa v226, 1, v190 dst_sel:DWORD dst_unused:UNUSED_PAD src0_sel:DWORD src1_sel:WORD_1\n\t" \
  "s_waitcnt lgkmcnt(9)\n\t" \
  "v_fma_mix_f32 v229, |v195|, v229, v229 op_sel:[0,1,0] op_sel_hi:[0,1,1] clamp\n\t" \
  "v_fma_mix_f32 v228, |v194|, v228, v228 op_sel:[0,1,0] op_sel_hi:[0,1,1] clamp\n\t" \
  "v_mfma_f32_16x16x32_bf16 v[32:35], v[216:219], v[154:157], v[32:35]\n\t" \
  "ds_read_b32 v220, v220 offset:10016\n\t" \
  "ds_read_b32 v223, v223 offset:10016\n\t" \
  "v_fma_mix_f32 v231, |v193|, v231, v231 op_sel:[0,1,0] op_sel_hi:[0,1,1] clamp\n\t" \
  "v_fma_mix_f32 v230, |v192|, v230, v230 op_sel:[0,1,0] op_sel_hi:[0,1,1] clamp\n\t" \
  "s_waitcnt lgkmcnt(3)\n\t" \
  "v_or_b32 v203, v232, v233\n\t" \
  "v_mfma_f32_16x16x32_bf16 v[56:59], v[216:219], v[162:165], v[56:59]\n\t" \
  "ds_read_b32 v222, v222 offset:10016\n\t" \
  "v_or_b32 v202, v234, v235\n\t" \
  "v_fma_f32 v195, |v195|, v229, v195 mul:4\n\t" \
  "v_fma_f32 v194, |v194|, v228, v194 mul:4\n\t" \
  "v_mfma_f32_16x16x32_bf16 v[80:83], v[216:219], v[170:173], v[80:83]\n\t" \
  "ds_read_b64_tr_b16 v[216:217], v249 offset:6288\n\t" \
  "ds_read_b64_tr_b16 v[218:219], v249 offset:7824\n\t" \
  "s_cbranch_vccnz .Lcnx_p12_2\n\ts_setprio 1\n.Lcnx_p12_2:\n\t" \
  "v_fma_f32 v193, |v193|, v231, v193 mul:4\n\t" \
  "v_fma_f32 v192, |v192|, v230, v192 mul:4\n\t" \
  "v_mfma_f32_16x16x32_bf16 v[12:15], v[208:211], v[146:149], v[12:15]\n\t" \
  "ds_read_u16_d16_hi v225, v225 offset:10528\n\t" \
  "v_cvt_pk_bf16_f32 v195, v194, v195\n\t" \
  "v_cvt_pk_bf16_f32 v194, v192, v193\n\t" \
  "v_cvt_pknorm_i16_f32 v228, |v196|, |v197|\n\t" \
  "v_mfma_f32_16x16x32_bf16 v[36:39], v[208:211], v[154:157], v[36:39]\n\t" \
  "s_waitcnt vmcnt(0)\n\t" \
  "s_mov_b64 exec, 3\n\t" \
  "ds_add_rtn_u32 v246, v251, v250 offset:47232\n\t" \
  "s_mov_b64 exec, -1\n\t" \
  "ds_read_u16 v224, v224 offset:10528\n\t" \
  "v_cvt_pknorm_i16_f32 v230, |v198|, |v199|\n\t" \
  "v_lshlrev_b32_sdwa v229, 2, v228 dst_sel:DWORD dst_unused:UNUSED_PAD src0_sel:DWORD src1_sel:BYTE_3\n\t" \
  "v_lshlrev_b32_sdwa v228, 2, v228 dst_sel:DWORD dst_unused:UNUSED_PAD src0_sel:DWORD src1_sel:BYTE_1\n\t" \
  "v_mfma_f32_16x16x32_bf16 v[60:63], v[208:211], v[162:165], v[60:63]\n\t" \
  "ds_read_u16_d16_hi v227, v227 offset:10528\n\t" \
  "v_lshlrev_b32_sdwa v231, 2, v230 dst_sel:DWORD dst_unused:UNUSED_PAD src0_sel:DWORD src1_sel:BYTE_3\n\t" \
  "v_lshlrev_b32_sdwa v230, 2, v230 dst_sel:DWORD dst_unused:UNUSED_PAD src0_sel:DWORD src1_sel:BYTE_1\n\t" \
  "v_lshlrev_b32_sdwa v233, 1, v205 dst_sel:DWORD dst_unused:UNUSED_PAD src0_sel:DWORD src1_sel:WORD_1\n\t" \
  "v_mfma_f32_16x16x32_bf16 v[84:87], v[208:211], v[170:173], v[84:87]\n\t" \
  "s_waitcnt lgkmcnt(2)\n\t" \
  "v_readfirstlane_b32 s4, v246\n\t" \
  "s_cmp_lt_u32 s4, s57\n\t" \
  "s_cbranch_scc1 .Lcnx_w138\n.Lcnx_b138:\n\t" \
  "ds_read_b64_tr_b16 v[208:209], v249 offset:12304\n\t" \
  "ds_read_b64_tr_b16 v[210:211], v249 offset:13840\n\t" \
  "v_lshlrev_b32_sdwa v232, 1, v204 dst_sel:DWORD dst_unused:UNUSED_PAD src0_sel:DWORD src1_sel:WORD_1\n\t" \
  "v_lshlrev_b32_sdwa v235, 1, v207 dst_sel:DWORD dst_unused:UNUSED_PAD src0_sel:DWORD src1_sel:WORD_1\n\t" \
  "v_lshlrev_b32_sdwa v234, 1, v206 dst_sel:DWORD dst_unused:UNUSED_PAD src0_sel:DWORD src1_sel:WORD_1\n\t" \
  "v_mfma_f32_16x16x32_bf16 v[16:19], v[212:215], v[146:149], v[16:19]\n\t" \
  "ds_read_u16 v226, v226 offset:10528\n\t" \
  "ds_read_b32 v229, v229 offset:10016\n\t" \
  "v_fma_mix_f32 v221, |v181|, v221, v221 op_sel:[0,1,0] op_sel_hi:[0,1,1] clamp\n\t" \
  "v_fma_mix_f32 v220, |v180|, v220, v220 op_sel:[0,1,0] op_sel_hi:[0,1,1] clamp\n\t" \
  "v_fma_mix_f32 v223, |v183|, v223, v223 op_sel:[0,1,0] op_sel_hi:[0,1,1] clamp\n\t" \
  "v_mfma_f32_16x16x32_bf16 v[40:43], v[212:215], v[154:157], v[40:43]\n\t" \
  "s_cbranch_vccnz .Lcnx_p12_3\n\ts_setprio 0\n.Lcnx_p12_3:\n\t" \
  "ds_read_b32 v228, v228 offset:10016\n\t" \
  "ds_read_b32 v231, v231 offset:10016\n\t" \
  "v_fma_mix_f32 v222, |v182|, v222, v222 op_sel:[0,1,0] op_sel_hi:[0,1,1] clamp\n\t" \
  "s_waitcnt lgkmcnt(3)\n\t" \
  "v_or_b32 v188, v224, v225\n\t" \
  "v_or_b32 v189, v226, v227\n\t" \
  "v_mfma_f32_16x16x32_bf16 v[64:67], v[212:215], v[162:165], v[64:67]\n\t" \
  "ds_read_b32 v230, v230 offset:10016\n\t" \
  "v_fma_f32 v181, |v181|, v221, v181 mul:4\n\t" \
  "v_fma_f32 v180, |v180|, v220, v180 mul:4\n\t" \
  "v_fma_f32 v183, |v183|, v223, v183 mul:4\n\t" \
  "v_mfma_f32_16x16x32_bf16 v[88:91], v[212:215], v[170:173], v[88:91]\n\t" \
  "ds_read_b64_tr_b16 v[212:213], v249 offset:12312\n\t" \
  "ds_read_b64_tr_b16 v[214:215], v249 offset:13848\n\t" \
  "v_fma_f32 v182, |v182|, v222, v182 mul:4\n\t" \
  "v_cvt_pk_bf16_f32 v180, v180, v181\n\t" \
  "v_cvt_pk_bf16_f32 v181, v182, v183\n\t" \
  "v_mfma_f32_16x16x32_bf16 v[20:23], v[216:219], v[146:149], v[20:23]\n\t" \
  "ds_read_u16_d16_hi v233, v233 offset:10528\n\t" \
  "ds_read_u16 v232, v232 offset:10528\n\t" \
  "ds_read_u16_d16_hi v235, v235 offset:10528\n\t" \
  "ds_read_u16 v234, v234 offset:10528\n\t" \
  "s_waitcnt lgkmcnt(6)\n\t" \
  "v_fma_mix_f32 v229, |v197|, v229, v229 op_sel:[0,1,0] op_sel_hi:[0,1,1] clamp\n\t" \
  "v_fma_mix_f32 v228, |v196|, v228, v228 op_sel:[0,1,0] op_sel_hi:[0,1,1] clamp\n\t" \
  "v_fma_mix_f32 v231, |v199|, v231, v231 op_sel:[0,1,0] op_sel_hi:[0,1,1] clamp\n\t" \
  "v_mfma_f32_16x16x32_bf16 v[44:47], v[216:219], v[154:157], v[44:47]\n\t" \
  "v_fma_mix_f32 v230, |v198|, v230, v230 op_sel:[0,1,0] op_sel_hi:[0,1,1] clamp\n\t" \
  "s_waitcnt lgkmcnt(0)\n\t" \
  "v_or_b32 v204, v232, v233\n\t" \
  "v_or_b32 v205, v234, v235\n\t" \
  "v_mfma_f32_16x16x32_bf16 v[68:71], v[216:219], v[162:165], v[68:71]\n\t" \
  "v_fma_f32 v197, |v197|, v229, v197 mul:4\n\t" \
  "v_fma_f32 v196, |v196|, v228, v196 mul:4\n\t" \
  "v_fma_f32 v199, |v199|, v231, v199 mul:4\n\t" \
  "v_mfma_f32_16x16x32_bf16 v[92:95], v[216:219], v[170:173], v[92:95]\n\t" \
  "ds_read_b64_tr_b16 v[216:217], v249 offset:12368\n\t" \
  "v_fma_f32 v198, |v198|, v230, v198 mul:4\n\t" \
  "v_cvt_pk_bf16_f32 v196, v196, v197\n\t" \
  "v_cvt_pk_bf16_f32 v197, v198, v199\n\t" \
  "s_cmp_lg_u32 s58, 0\n\t" \
  "s_cbranch_scc1 .Lcnx_fin\n\t" \
  "v_mfma_f32_32x32x16_bf16 v[144:159], v[242:245], v[240:243], 0\n\t" \
  "v_mfma_f32_32x32x16_bf16 v[160:175], v[242:245], v[240:243], 0\n\t" \
  "s_bitcmp0_b32 208, s38\n\t" \
  "s_cbranch_scc1 .Lcnx_c32\n\t" \
  "s_mov_b32 m0, 107744\n\t" \
  "s_add_u32 s30, s30, s31\n\t" \
  "buffer_load_dword v252, s[8:11], s30 offen lds\n\t" \
  ".Lcnx_c32:\n\t" \
  "v_mfma_f32_16x16x32_bf16 v[88:91], v[242:245], v[236:239], v[88:91]\n\t" \
  "ds_read_b64_tr_b16 v[218:219], v249 offset:13904\n\t" \
  "v_add_u32 v232, s56, v247\n\t" \
  "v_mfma_f32_16x16x32_bf16 v[92:95], v[240:243], v[236:239], v[92:95]\n\t" \
  "ds_read2_b32 v[98:99], v247 offset0:0 offset1:6\n\t" \
  "v_mfma_f32_16x16x32_bf16 v[0:3], v[208:211], v[178:181], v[0:3]\n\t" \
  "ds_read2_b32 v[114:115], v247 offset0:192 offset1:198\n\t" \
  "v_mfma_f32_16x16x32_bf16 v[24:27], v[208:211], v[186:189], v[24:27]\n\t" \
  "ds_read2_b32 v[130:131], v232 offset0:0 offset1:6\n\t" \
  "v_mfma_f32_16x16x32_bf16 v[48:51], v[208:211], v[194:197], v[48:51]\n\t" \
  "ds_read2_b32 v[102:103], v247 offset0:12 offset1:18\n\t" \
  "v_mfma_f32_16x16x32_bf16 v[72:75], v[208:211], v[202:205], v[72:75]\n\t" \
  "ds_read_b64_tr_b16 v[208:209], v249 offset:12376\n\t" \
  "ds_read_b64_tr_b16 v[210:211], v249 offset:13912\n\t" \
  "v_bfe_u32 v236, v251, 3, 2\n\t" \
  "v_mfma_f32_16x16x32_bf16 v[4:7], v[212:215], v[178:181], v[4:7]\n\t" \
  "ds_read2_b32 v[118:119], v247 offset0:204 offset1:210\n\t" \
  "ds_read2_b32 v[134:135], v232 offset0:12 offset1:18\n\t" \
  "v_mul_u32_u24 v236, 0x5555, v236\n\t" \
  "v_mfma_f32_16x16x32_bf16 v[28:31], v[212:215], v[186:189], v[28:31]\n\t" \
  "ds_read2_b32 v[106:107], v247 offset0:24 offset1:30\n\t" \
  "ds_read2_b32 v[122:123], v247 offset0:216 offset1:222\n\t" \
  "v_mfma_f32_16x16x32_bf16 v[52:55], v[212:215], v[194:197], v[52:55]\n\t" \
  "ds_read2_b32 v[138:139], v232 offset0:24 offset1:30\n\t" \
  "v_mfma_f32_16x16x32_bf16 v[76:79], v[212:215], v[202:205], v[76:79]\n\t" \
  "ds_read_b64_tr_b16 v[212:213], v249 offset:12432\n\t" \
  "s_waitcnt lgkmcnt(12)\n\t" \
  "v_mfma_f32_16x16x32_bf16 v[8:11], v[216:219], v[178:181], v[8:11]\n\t" \
  "ds_read_b64_tr_b16 v[214:215], v249 offset:13968\n\t" \
  "v_mfma_f32_16x16x32_bf16 v[32:35], v[216:219], v[186:189], v[32:35]\n\t" \
  "ds_read2_b32 v[110:111], v247 offset0:36 offset1:42\n\t" \
  "v_mfma_f32_16x16x32_bf16 v[56:59], v[216:219], v[194:197], v[56:59]\n\t" \
  "ds_read2_b32 v[126:127], v247 offset0:228 offset1:234\n\t" \
  "v_mfma_f32_16x16x32_bf16 v[80:83], v[216:219], v[202:205], v[80:83]\n\t" \
  "ds_read_b64_tr_b16 v[216:217], v249 offset:12440\n\t" \
  "v_cvt_pk_bf16_f32 v48, v48, v49\n\t" \
  "v_cvt_pk_bf16_f32 v49, v50, v51\n\t" \
  "v_cvt_pk_bf16_f32 v50, v52, v53\n\t" \
  "v_cvt_pk_bf16_f32 v51, v54, v55\n\t" \
  "v_cvt_pk_bf16_f32 v52, v0, v1\n\t" \
  "v_cvt_pk_bf16_f32 v53, v2, v3\n\t" \
  "v_cvt_pk_bf16_f32 v54, v4, v5\n\t" \
  "v_cvt_pk_bf16_f32 v55, v6, v7\n\t" \
  "v_cvt_pk_bf16_f32 v72, v72, v73\n\t" \
  "v_cvt_pk_bf16_f32 v73, v74, v75\n\t" \
  "v_cvt_pk_bf16_f32 v74, v76, v77\n\t" \
  "v_cvt_pk_bf16_f32 v75, v78, v79\n\t" \
  "v_cvt_pk_bf16_f32 v76, v24, v25\n\t" \
  "v_cvt_pk_bf16_f32 v77, v26, v27\n\t" \
  "v_cvt_pk_bf16_f32 v78, v28, v29\n\t" \
  "v_cvt_pk_bf16_f32 v79, v30, v31\n\t" \
  "s_waitcnt lgkmcnt(10)\n\t" \
  "v_mfma_f32_16x16x32_bf16 v[12:15], v[208:211], v[178:181], v[12:15]\n\t" \
  "ds_read_b64_tr_b16 v[218:219], v249 offset:13976\n\t" \
  "v_mfma_f32_16x16x32_bf16 v[36:39], v[208:211], v[186:189], v[36:39]\n\t" \
  "ds_read2_b32 v[142:143], v232 offset0:36 offset1:42\n\t" \
  "v_mfma_f32_16x16x32_bf16 v[60:63], v[208:211], v[194:197], v[60:63]\n\t" \
  "v_mfma_f32_16x16x32_bf16 v[84:87], v[208:211], v[202:205], v[84:87]\n\t" \
  "s_waitcnt lgkmcnt(5)\n\t" \
  "v_mfma_f32_16x16x32_bf16 v[16:19], v[212:215], v[178:181], v[16:19]\n\t" \
  "v_mfma_f32_16x16x32_bf16 v[40:43], v[212:215], v[186:189], v[40:43]\n\t" \
  "v_mfma_f32_16x16x32_bf16 v[64:67], v[212:215], v[194:197], v[64:67]\n\t" \
  "v_mfma_f32_16x16x32_bf16 v[88:91], v[212:215], v[202:205], v[88:91]\n\t" \
  "v_cvt_pk_bf16_f32 v56, v56, v57\n\t" \
  "v_cvt_pk_bf16_f32 v57, v58, v59\n\t" \
  "v_cvt_pk_bf16_f32 v58, v60, v61\n\t" \
  "v_cvt_pk_bf16_f32 v59, v62, v63\n\t" \
  "v_cvt_pk_bf16_f32 v60, v8, v9\n\t" \
  "v_cvt_pk_bf16_f32 v61, v10, v11\n\t" \
  "v_cvt_pk_bf16_f32 v62, v12, v13\n\t" \
  "v_cvt_pk_bf16_f32 v63, v14, v15\n\t" \
  "v_cvt_pk_bf16_f32 v80, v80, v81\n\t" \
  "v_cvt_pk_bf16_f32 v81, v82, v83\n\t" \
  "v_cvt_pk_bf16_f32 v82, v84, v85\n\t" \
  "v_cvt_pk_bf16_f32 v83, v86, v87\n\t" \
  "v_cvt_pk_bf16_f32 v84, v32, v33\n\t" \
  "v_cvt_pk_bf16_f32 v85, v34, v35\n\t" \
  "v_cvt_pk_bf16_f32 v86, v36, v37\n\t" \
  "v_cvt_pk_bf16_f32 v87, v38, v39\n\t" \
  "s_waitcnt lgkmcnt(1)\n\t" \
  "v_mfma_f32_16x16x32_bf16 v[20:23], v[216:219], v[178:181], v[20:23]\n\t" \
  "v_mfma_f32_16x16x32_bf16 v[44:47], v[216:219], v[186:189], v[44:47]\n\t" \
  "v_mfma_f32_16x16x32_bf16 v[68:71], v[216:219], v[194:197], v[68:71]\n\t" \
  "v_mfma_f32_16x16x32_bf16 v[92:95], v[216:219], v[202:205], v[92:95]\n\t" \
  "v_cvt_pk_bf16_f32 v64, v64, v65\n\t" \
  "v_cvt_pk_bf16_f32 v65, v66, v67\n\t" \
  "s_nop 4\n\t" \
  "v_cvt_pk_bf16_f32 v66, v68, v69\n\t" \
  "v_cvt_pk_bf16_f32 v67, v70, v71\n\t" \
  "v_cvt_pk_bf16_f32 v68, v16, v17\n\t" \
  "v_cvt_pk_bf16_f32 v69, v18, v19\n\t" \
  "v_cvt_pk_bf16_f32 v70, v20, v21\n\t" \
  "v_cvt_pk_bf16_f32 v71, v22, v23\n\t" \
  "v_cvt_pk_bf16_f32 v88, v88, v89\n\t" \
  "v_cvt_pk_bf16_f32 v89, v90, v91\n\t" \
  "v_cvt_pk_bf16_f32 v90, v92, v93\n\t" \
  "v_cvt_pk_bf16_f32 v91, v94, v95\n\t" \
  "v_cvt_pk_bf16_f32 v92, v40, v41\n\t" \
  "v_cvt_pk_bf16_f32 v93, v42, v43\n\t" \
  "v_cvt_pk_bf16_f32 v94, v44, v45\n\t" \
  "v_cvt_pk_bf16_f32 v95, v46, v47\n\t" \
  "s_nop 7\n\t" \
  "s_mov_b32 s57, 6144\n\t" \
  "s_mov_b32 s58, 1\n\t" \
  "s_mov_b32 s38, 0\n\t" \
  "s_mov_b32 s59, -81480\n\t" \
  "s_mov_b32 s60, -81480\n\t" \
  "s_mov_b32 s36, 2\n\t" \
  "s_mov_b32 s61, s28\n\t" \
  "s_mov_b32 s62, s29\n\t" \
  "s_mov_b32 s14, s12\n\t" \
  "s_mov_b32 s15, s13\n\t" \
  "s_add_u32 s12, s12, s65\n\t" \
  "s_add_u32 s13, s13, s65\n\t" \
  "s_add_u32 s28, s28, s65\n\t" \
  "s_add_u32 s29, s29, s65\n\t" \
  "v_lshlrev_b32 v198, 1, v251\n\t" \
  "global_load_dwordx4 v[192:195], v198, s[66:67]\n\t" \
  "s_branch .Lcnx_round\n\t" \
  ".Lcnx_w12:\n\t" \
  "ds_read_b32 v246, v251 offset:47144\n\t" \
  "s_waitcnt lgkmcnt(0)\n\t" \
  "v_readfirstlane_b32 s4, v246\n\t" \
  "s_cmp_lt_u32 s4, s57\n\t" \
  "s_cbranch_scc1 .Lcnx_w12\n\t" \
  "s_branch .Lcnx_b12\n\t" \
  ".Lcnx_w24:\n\t" \
  "ds_read_b32 v246, v251 offset:47152\n\t" \
  "s_waitcnt lgkmcnt(0)\n\t" \
  "v_readfirstlane_b32 s4, v246\n\t" \
  "s_cmp_lt_u32 s4, s57\n\t" \
  "s_cbranch_scc1 .Lcnx_w24\n\t" \
  "s_branch .Lcnx_b24\n\t" \
  ".Lcnx_w36:\n\t" \
  "ds_read_b32 v246, v251 offset:47160\n\t" \
  "s_waitcnt lgkmcnt(0)\n\t" \
  "v_readfirstlane_b32 s4, v246\n\t" \
  "s_cmp_lt_u32 s4, s57\n\t" \
  "s_cbranch_scc1 .Lcnx_w36\n\t" \
  "s_branch .Lcnx_b36\n\t" \
  ".Lcnx_w48:\n\t" \
  "ds_read_b32 v246, v251 offset:47168\n\t" \
  "s_waitcnt lgkmcnt(0)\n\t" \
  "v_readfirstlane_b32 s4, v246\n\t" \
  "s_cmp_lt_u32 s4, s57\n\t" \
  "s_cbranch_scc1 .Lcnx_w48\n\t" \
  "s_branch .Lcnx_b48\n\t" \
  ".Lcnx_w60:\n\t" \
  "ds_read_b32 v246, v251 offset:47176\n\t" \
  "s_waitcnt lgkmcnt(0)\n\t" \
  "v_readfirstlane_b32 s4, v246\n\t" \
  "s_cmp_lt_u32 s4, s57\n\t" \
  "s_cbranch_scc1 .Lcnx_w60\n\t" \
  "s_branch .Lcnx_b60\n\t" \
  ".Lcnx_w72:\n\t" \
  "ds_read_b32 v246, v251 offset:47184\n\t" \
  "s_waitcnt lgkmcnt(0)\n\t" \
  "v_readfirstlane_b32 s4, v246\n\t" \
  "s_cmp_lt_u32 s4, s57\n\t" \
  "s_cbranch_scc1 .Lcnx_w72\n\t" \
  "s_branch .Lcnx_b72\n\t" \
  ".Lcnx_w84:\n\t" \
  "ds_read_b32 v246, v251 offset:47192\n\t" \
  "s_waitcnt lgkmcnt(0)\n\t" \
  "v_readfirstlane_b32 s4, v246\n\t" \
  "s_cmp_lt_u32 s4, s57\n\t" \
  "s_cbranch_scc1 .Lcnx_w84\n\t" \
  "s_branch .Lcnx_b84\n\t" \
  ".Lcnx_w96:\n\t" \
  "ds_read_b32 v246, v251 offset:47200\n\t" \
  "s_waitcnt lgkmcnt(0)\n\t" \
  "v_readfirstlane_b32 s4, v246\n\t" \
  "s_cmp_lt_u32 s4, s57\n\t" \
  "s_cbranch_scc1 .Lcnx_w96\n\t" \
  "s_branch .Lcnx_b96\n\t" \
  ".Lcnx_w108:\n\t" \
  "ds_read_b32 v246, v251 offset:47208\n\t" \
  "s_waitcnt lgkmcnt(0)\n\t" \
  "v_readfirstlane_b32 s4, v246\n\t" \
  "s_cmp_lt_u32 s4, s57\n\t" \
  "s_cbranch_scc1 .Lcnx_w108\n\t" \
  "s_branch .Lcnx_b108\n\t" \
  ".Lcnx_w120:\n\t" \
  "ds_read_b32 v246, v251 offset:47216\n\t" \
  "s_waitcnt lgkmcnt(0)\n\t" \
  "v_readfirstlane_b32 s4, v246\n\t" \
  "s_cmp_lt_u32 s4, s57\n\t" \
  "s_cbranch_scc1 .Lcnx_w120\n\t" \
  "s_branch .Lcnx_b120\n\t" \
  ".Lcnx_w132:\n\t" \
  "ds_read_b32 v246, v251 offset:47224\n\t" \
  "s_waitcnt lgkmcnt(0)\n\t" \
  "v_readfirstlane_b32 s4, v246\n\t" \
  "s_cmp_lt_u32 s4, s57\n\t" \
  "s_cbranch_scc1 .Lcnx_w132\n\t" \
  "s_branch .Lcnx_b132\n\t" \
  ".Lcnx_w138:\n\t" \
  "ds_read_b32 v246, v251 offset:47232\n\t" \
  "s_waitcnt lgkmcnt(0)\n\t" \
  "v_readfirstlane_b32 s4, v246\n\t" \
  "s_cmp_lt_u32 s4, s57\n\t" \
  "s_cbranch_scc1 .Lcnx_w138\n\t" \
  "s_branch .Lcnx_b138\n\t" \
  ".Lcnx_f0:\n\t" \
  "s_mov_b32 m0, s44\n\t" \
  "s_nop 0\n\t" \
  "buffer_load_dwordx4 v252, s[16:19], s33 offen lds\n\t" \
  "buffer_load_dwordx4 v252, s[16:19], s34 offen offset:1024 lds\n\t" \
  "buffer_load_dwordx4 v252, s[16:19], s35 offen offset:2048 lds\n\t" \
  "s_add_u32 s33, s33, s32\n\t" \
  "s_add_u32 s34, s34, s32\n\t" \
  "s_add_u32 s35, s35, s32\n\t" \
  "s_branch .Lcnx_g0\n\t" \
  ".Lcnx_f1:\n\t" \
  "s_mov_b32 m0, s45\n\t" \
  "s_nop 0\n\t" \
  "buffer_load_dwordx4 v252, s[16:19], s33 offen lds\n\t" \
  "buffer_load_dwordx4 v252, s[16:19], s34 offen offset:1024 lds\n\t" \
  "buffer_load_dwordx4 v252, s[16:19], s35 offen offset:2048 lds\n\t" \
  "s_add_u32 s33, s33, s32\n\t" \
  "s_add_u32 s34, s34, s32\n\t" \
  "s_add_u32 s35, s35, s32\n\t" \
  "s_branch .Lcnx_g1\n\t" \
  ".Lcnx_f2:\n\t" \
  "s_mov_b32 m0, s46\n\t" \
  "s_nop 0\n\t" \
  "buffer_load_dwordx4 v252, s[16:19], s33 offen lds\n\t" \
  "buffer_load_dwordx4 v252, s[16:19], s34 offen offset:1024 lds\n\t" \
  "buffer_load_dwordx4 v252, s[16:19], s35 offen offset:2048 lds\n\t" \
  "s_add_u32 s33, s33, s32\n\t" \
  "s_add_u32 s34, s34, s32\n\t" \
  "s_add_u32 s35, s35, s32\n\t" \
  "s_branch .Lcnx_g2\n\t" \
  ".Lcnx_f3:\n\t" \
  "s_mov_b32 m0, s47\n\t" \
  "s_nop 0\n\t" \
  "buffer_load_dwordx4 v252, s[16:19], s33 offen lds\n\t" \
  "buffer_load_dwordx4 v252, s[16:19], s34 offen offset:1024 lds\n\t" \
  "buffer_load_dwordx4 v252, s[16:19], s35 offen offset:2048 lds\n\t" \
  "s_add_u32 s33, s33, s32\n\t" \
  "s_add_u32 s34, s34, s32\n\t" \
  "s_add_u32 s35, s35, s32\n\t" \
  "s_branch .Lcnx_g3\n\t" \
  ".Lcnx_f4:\n\t" \
  "s_mov_b32 m0, s44\n\t" \
  "s_nop 0\n\t" \
  "buffer_load_dwordx4 v252, s[16:19], s33 offen lds\n\t" \
  "buffer_load_dwordx4 v252, s[16:19], s34 offen offset:1024 lds\n\t" \
  "buffer_load_dwordx4 v252, s[16:19], s35 offen offset:2048 lds\n\t" \
  "s_add_u32 s33, s33, s32\n\t" \
  "s_add_u32 s34, s34, s32\n\t" \
  "s_add_u32 s35, s35, s32\n\t" \
  "s_cmp_lg_u32 s58, 1\n\t" \
  "s_cbranch_scc1 .Lcnx_g4\n\t" \
  "s_add_u32 m0, s44, s50\n\t" \
  "s_add_u32 s4, s33, s51\n\t" \
  "buffer_load_dwordx4 v252, s[16:19], s4 offen offset:0 lds\n\t" \
  "s_add_u32 s4, s34, s51\n\t" \
  "buffer_load_dwordx4 v252, s[16:19], s4 offen offset:1024 lds\n\t" \
  "s_add_u32 s4, s35, s51\n\t" \
  "buffer_load_dwordx4 v252, s[16:19], s4 offen offset:2048 lds\n\t" \
  "s_branch .Lcnx_g4\n\t" \
  ".Lcnx_f5:\n\t" \
  "s_mov_b32 m0, s45\n\t" \
  "s_nop 0\n\t" \
  "buffer_load_dwordx4 v252, s[16:19], s33 offen lds\n\t" \
  "buffer_load_dwordx4 v252, s[16:19], s34 offen offset:1024 lds\n\t" \
  "buffer_load_dwordx4 v252, s[16:19], s35 offen offset:2048 lds\n\t" \
  "s_add_u32 s33, s33, s32\n\t" \
  "s_add_u32 s34, s34, s32\n\t" \
  "s_add_u32 s35, s35, s32\n\t" \
  "s_cmp_lg_u32 s58, 1\n\t" \
  "s_cbranch_scc1 .Lcnx_g5\n\t" \
  "s_add_u32 m0, s45, s50\n\t" \
  "s_add_u32 s4, s33, s51\n\t" \
  "buffer_load_dwordx4 v252, s[16:19], s4 offen offset:0 lds\n\t" \
  "s_add_u32 s4, s34, s51\n\t" \
  "buffer_load_dwordx4 v252, s[16:19], s4 offen offset:1024 lds\n\t" \
  "s_add_u32 s4, s35, s51\n\t" \
  "buffer_load_dwordx4 v252, s[16:19], s4 offen offset:2048 lds\n\t" \
  "s_branch .Lcnx_g5\n\t" \
  ".Lcnx_f6:\n\t" \
  "s_mov_b32 m0, s46\n\t" \
  "s_nop 0\n\t" \
  "buffer_load_dwordx4 v252, s[16:19], s33 offen lds\n\t" \
  "buffer_load_dwordx4 v252, s[16:19], s34 offen offset:1024 lds\n\t" \
  "buffer_load_dwordx4 v252, s[16:19], s35 offen offset:2048 lds\n\t" \
  "s_add_u32 s33, s33, s32\n\t" \
  "s_add_u32 s34, s34, s32\n\t" \
  "s_add_u32 s35, s35, s32\n\t" \
  "s_cmp_lg_u32 s58, 1\n\t" \
  "s_cbranch_scc1 .Lcnx_g6\n\t" \
  "s_cmp_lt_u32 s39, 1024\n\t" \
  "s_cbranch_scc1 .Lcnx_g6\n\t" \
  "s_add_u32 m0, s46, s50\n\t" \
  "s_add_u32 s4, s33, s51\n\t" \
  "buffer_load_dwordx4 v252, s[16:19], s4 offen offset:0 lds\n\t" \
  "s_add_u32 s4, s34, s51\n\t" \
  "buffer_load_dwordx4 v252, s[16:19], s4 offen offset:1024 lds\n\t" \
  "s_add_u32 s4, s35, s51\n\t" \
  "buffer_load_dwordx4 v252, s[16:19], s4 offen offset:2048 lds\n\t" \
  "s_branch .Lcnx_g6\n\t" \
  ".Lcnx_f7:\n\t" \
  "s_mov_b32 m0, s47\n\t" \
  "s_nop 0\n\t" \
  "buffer_load_dwordx4 v252, s[16:19], s33 offen lds\n\t" \
  "buffer_load_dwordx4 v252, s[16:19], s34 offen offset:1024 lds\n\t" \
  "buffer_load_dwordx4 v252, s[16:19], s35 offen offset:2048 lds\n\t" \
  "s_add_u32 s33, s33, s32\n\t" \
  "s_add_u32 s34, s34, s32\n\t" \
  "s_add_u32 s35, s35, s32\n\t" \
  "s_cmp_lg_u32 s58, 1\n\t" \
  "s_cbranch_scc1 .Lcnx_g7\n\t" \
  "s_cmp_lt_u32 s39, 1024\n\t" \
  "s_cbranch_scc1 .Lcnx_g7\n\t" \
  "s_add_u32 m0, s47, s50\n\t" \
  "s_add_u32 s4, s33, s51\n\t" \
  "buffer_load_dwordx4 v252, s[16:19], s4 offen offset:0 lds\n\t" \
  "s_add_u32 s4, s34, s51\n\t" \
  "buffer_load_dwordx4 v252, s[16:19], s4 offen offset:1024 lds\n\t" \
  "s_add_u32 s4, s35, s51\n\t" \
  "buffer_load_dwordx4 v252, s[16:19], s4 offen offset:2048 lds\n\t" \
  "s_branch .Lcnx_g7\n\t" \
  ".Lcnx_f8:\n\t" \
  "s_mov_b32 m0, s44\n\t" \
  "s_nop 0\n\t" \
  "buffer_load_dwordx4 v252, s[16:19], s33 offen lds\n\t" \
  "buffer_load_dwordx4 v252, s[16:19], s34 offen offset:1024 lds\n\t" \
  "buffer_load_dwordx4 v252, s[16:19], s35 offen offset:2048 lds\n\t" \
  "s_add_u32 s33, s33, s32\n\t" \
  "s_add_u32 s34, s34, s32\n\t" \
  "s_add_u32 s35, s35, s32\n\t" \
  "s_branch .Lcnx_g8\n\t" \
  ".Lcnx_f9:\n\t" \
  "s_mov_b32 m0, s45\n\t" \
  "s_nop 0\n\t" \
  "buffer_load_dwordx4 v252, s[16:19], s33 offen lds\n\t" \
  "buffer_load_dwordx4 v252, s[16:19], s34 offen offset:1024 lds\n\t" \
  "buffer_load_dwordx4 v252, s[16:19], s35 offen offset:2048 lds\n\t" \
  "s_add_u32 s33, s33, s32\n\t" \
  "s_add_u32 s34, s34, s32\n\t" \
  "s_add_u32 s35, s35, s32\n\t" \
  "s_sub_u32 s33, s33, s48\n\t" \
  "s_sub_u32 s34, s34, s48\n\t" \
  "s_sub_u32 s35, s35, s48\n\t" \
  "s_branch .Lcnx_g9\n\t" \
  ".Lcnx_f10:\n\t" \
  "s_mov_b32 m0, s46\n\t" \
  "s_nop 0\n\t" \
  "buffer_load_dwordx4 v252, s[16:19], s33 offen lds\n\t" \
  "buffer_load_dwordx4 v252, s[16:19], s34 offen offset:1024 lds\n\t" \
  "buffer_load_dwordx4 v252, s[16:19], s35 offen offset:2048 lds\n\t" \
  "s_add_u32 s33, s33, s32\n\t" \
  "s_add_u32 s34, s34, s32\n\t" \
  "s_add_u32 s35, s35, s32\n\t" \
  "s_branch .Lcnx_g10\n\t" \
  ".Lcnx_f11:\n\t" \
  "s_mov_b32 m0, s47\n\t" \
  "s_nop 0\n\t" \
  "buffer_load_dwordx4 v252, s[16:19], s33 offen lds\n\t" \
  "buffer_load_dwordx4 v252, s[16:19], s34 offen offset:1024 lds\n\t" \
  "buffer_load_dwordx4 v252, s[16:19], s35 offen offset:2048 lds\n\t" \
  "s_add_u32 s33, s33, s32\n\t" \
  "s_add_u32 s34, s34, s32\n\t" \
  "s_add_u32 s35, s35, s32\n\t" \
  "s_sub_u32 s33, s33, s49\n\t" \
  "s_sub_u32 s34, s34, s49\n\t" \
  "s_sub_u32 s35, s35, s49\n\t" \
  "s_branch .Lcnx_g11\n\t" \
  ".Lcnx_fin:\n\t" \
  "v_mfma_f32_16x16x32_bf16 v[88:91], v[242:245], v[236:239], v[88:91]\n\t" \
  "ds_read_b64_tr_b16 v[218:219], v249 offset:13904\n\t" \
  "v_mfma_f32_16x16x32_bf16 v[92:95], v[240:243], v[236:239], v[92:95]\n\t" \
  "v_mfma_f32_16x16x32_bf16 v[0:3], v[208:211], v[178:181], v[0:3]\n\t" \
  "v_mfma_f32_16x16x32_bf16 v[24:27], v[208:211], v[186:189], v[24:27]\n\t" \
  "v_mfma_f32_16x16x32_bf16 v[48:51], v[208:211], v[194:197], v[48:51]\n\t" \
  "v_mfma_f32_16x16x32_bf16 v[72:75], v[208:211], v[202:205], v[72:75]\n\t" \
  "ds_read_b64_tr_b16 v[208:209], v249 offset:12376\n\t" \
  "v_mfma_f32_16x16x32_bf16 v[4:7], v[212:215], v[178:181], v[4:7]\n\t" \
  "ds_read_b64_tr_b16 v[210:211], v249 offset:13912\n\t" \
  "v_mfma_f32_16x16x32_bf16 v[28:31], v[212:215], v[186:189], v[28:31]\n\t" \
  "v_mfma_f32_16x16x32_bf16 v[52:55], v[212:215], v[194:197], v[52:55]\n\t" \
  "v_mfma_f32_16x16x32_bf16 v[76:79], v[212:215], v[202:205], v[76:79]\n\t" \
  "ds_read_b64_tr_b16 v[212:213], v249 offset:12432\n\t" \
  "s_waitcnt lgkmcnt(3)\n\t" \
  "v_mfma_f32_16x16x32_bf16 v[8:11], v[216:219], v[178:181], v[8:11]\n\t" \
  "ds_read_b64_tr_b16 v[214:215], v249 offset:13968\n\t" \
  "v_mfma_f32_16x16x32_bf16 v[32:35], v[216:219], v[186:189], v[32:35]\n\t" \
  "v_mfma_f32_16x16x32_bf16 v[56:59], v[216:219], v[194:197], v[56:59]\n\t" \
  "v_mfma_f32_16x16x32_bf16 v[80:83], v[216:219], v[202:205], v[80:83]\n\t" \
  "ds_read_b64_tr_b16 v[216:217], v249 offset:12440\n\t" \
  "v_cvt_pk_bf16_f32 v0, v0, v1\n\t" \
  "v_cvt_pk_bf16_f32 v1, v2, v3\n\t" \
  "v_cvt_pk_bf16_f32 v2, v4, v5\n\t" \
  "v_cvt_pk_bf16_f32 v3, v6, v7\n\t" \
  "buffer_store_dwordx4 v[0:3], v250, s[20:23], s28 offen offset:0 sc1\n\t" \
  "v_cvt_pk_bf16_f32 v24, v24, v25\n\t" \
  "v_cvt_pk_bf16_f32 v25, v26, v27\n\t" \
  "v_cvt_pk_bf16_f32 v26, v28, v29\n\t" \
  "v_cvt_pk_bf16_f32 v27, v30, v31\n\t" \
  "buffer_store_dwordx4 v[24:27], v250, s[20:23], s29 offen offset:0 sc1\n\t" \
  "v_cvt_pk_bf16_f32 v48, v48, v49\n\t" \
  "v_cvt_pk_bf16_f32 v49, v50, v51\n\t" \
  "v_cvt_pk_bf16_f32 v50, v52, v53\n\t" \
  "v_cvt_pk_bf16_f32 v51, v54, v55\n\t" \
  "buffer_store_dwordx4 v[48:51], v250, s[20:23], s12 offen offset:0 sc1\n\t" \
  "v_cvt_pk_bf16_f32 v72, v72, v73\n\t" \
  "v_cvt_pk_bf16_f32 v73, v74, v75\n\t" \
  "v_cvt_pk_bf16_f32 v74, v76, v77\n\t" \
  "v_cvt_pk_bf16_f32 v75, v78, v79\n\t" \
  "buffer_store_dwordx4 v[72:75], v250, s[20:23], s13 offen offset:0 sc1\n\t" \
  "s_waitcnt lgkmcnt(3)\n\t" \
  "v_mfma_f32_16x16x32_bf16 v[12:15], v[208:211], v[178:181], v[12:15]\n\t" \
  "ds_read_b64_tr_b16 v[218:219], v249 offset:13976\n\t" \
  "v_mfma_f32_16x16x32_bf16 v[36:39], v[208:211], v[186:189], v[36:39]\n\t" \
  "v_mfma_f32_16x16x32_bf16 v[60:63], v[208:211], v[194:197], v[60:63]\n\t" \
  "v_mfma_f32_16x16x32_bf16 v[84:87], v[208:211], v[202:205], v[84:87]\n\t" \
  "s_waitcnt lgkmcnt(2)\n\t" \
  "v_mfma_f32_16x16x32_bf16 v[16:19], v[212:215], v[178:181], v[16:19]\n\t" \
  "v_mfma_f32_16x16x32_bf16 v[40:43], v[212:215], v[186:189], v[40:43]\n\t" \
  "v_mfma_f32_16x16x32_bf16 v[64:67], v[212:215], v[194:197], v[64:67]\n\t" \
  "v_mfma_f32_16x16x32_bf16 v[88:91], v[212:215], v[202:205], v[88:91]\n\t" \
  "v_cvt_pk_bf16_f32 v8, v8, v9\n\t" \
  "v_cvt_pk_bf16_f32 v9, v10, v11\n\t" \
  "v_cvt_pk_bf16_f32 v10, v12, v13\n\t" \
  "v_cvt_pk_bf16_f32 v11, v14, v15\n\t" \
  "buffer_store_dwordx4 v[8:11], v250, s[20:23], s28 offen offset:64 sc1\n\t" \
  "v_cvt_pk_bf16_f32 v32, v32, v33\n\t" \
  "v_cvt_pk_bf16_f32 v33, v34, v35\n\t" \
  "v_cvt_pk_bf16_f32 v34, v36, v37\n\t" \
  "v_cvt_pk_bf16_f32 v35, v38, v39\n\t" \
  "buffer_store_dwordx4 v[32:35], v250, s[20:23], s29 offen offset:64 sc1\n\t" \
  "v_cvt_pk_bf16_f32 v56, v56, v57\n\t" \
  "v_cvt_pk_bf16_f32 v57, v58, v59\n\t" \
  "v_cvt_pk_bf16_f32 v58, v60, v61\n\t" \
  "v_cvt_pk_bf16_f32 v59, v62, v63\n\t" \
  "buffer_store_dwordx4 v[56:59], v250, s[20:23], s12 offen offset:64 sc1\n\t" \
  "v_cvt_pk_bf16_f32 v80, v80, v81\n\t" \
  "v_cvt_pk_bf16_f32 v81, v82, v83\n\t" \
  "v_cvt_pk_bf16_f32 v82, v84, v85\n\t" \
  "v_cvt_pk_bf16_f32 v83, v86, v87\n\t" \
  "buffer_store_dwordx4 v[80:83], v250, s[20:23], s13 offen offset:64 sc1\n\t" \
  "s_waitcnt lgkmcnt(0)\n\t" \
  "v_mfma_f32_16x16x32_bf16 v[20:23], v[216:219], v[178:181], v[20:23]\n\t" \
  "v_mfma_f32_16x16x32_bf16 v[44:47], v[216:219], v[186:189], v[44:47]\n\t" \
  "v_mfma_f32_16x16x32_bf16 v[68:71], v[216:219], v[194:197], v[68:71]\n\t" \
  "v_mfma_f32_16x16x32_bf16 v[92:95], v[216:219], v[202:205], v[92:95]\n\t" \
  "v_cvt_pk_bf16_f32 v16, v16, v17\n\t" \
  "v_cvt_pk_bf16_f32 v17, v18, v19\n\t" \
  "s_nop 2\n\t" \
  "v_cvt_pk_bf16_f32 v18, v20, v21\n\t" \
  "v_cvt_pk_bf16_f32 v19, v22, v23\n\t" \
  "buffer_store_dwordx4 v[16:19], v250, s[20:23], s28 offen offset:128 sc1\n\t" \
  "v_cvt_pk_bf16_f32 v40, v40, v41\n\t" \
  "v_cvt_pk_bf16_f32 v41, v42, v43\n\t" \
  "v_cvt_pk_bf16_f32 v42, v44, v45\n\t" \
  "v_cvt_pk_bf16_f32 v43, v46, v47\n\t" \
  "buffer_store_dwordx4 v[40:43], v250, s[20:23], s29 offen offset:128 sc1\n\t" \
  "v_cvt_pk_bf16_f32 v64, v64, v65\n\t" \
  "v_cvt_pk_bf16_f32 v65, v66, v67\n\t" \
  "v_cvt_pk_bf16_f32 v66, v68, v69\n\t" \
  "v_cvt_pk_bf16_f32 v67, v70, v71\n\t" \
  "buffer_store_dwordx4 v[64:67], v250, s[20:23], s12 offen offset:128 sc1\n\t" \
  "v_cvt_pk_bf16_f32 v88, v88, v89\n\t" \
  "v_cvt_pk_bf16_f32 v89, v90, v91\n\t" \
  "v_cvt_pk_bf16_f32 v90, v92, v93\n\t" \
  "v_cvt_pk_bf16_f32 v91, v94, v95\n\t" \
  "buffer_store_dwordx4 v[88:91], v250, s[20:23], s13 offen offset:128 sc1\n\t" \
  "s_waitcnt vmcnt(0)\n\t" \
  ".Lcnx_end:\n\t" \
  ""


__device__ __forceinline__ unsigned lds_addr(const char* p) { return (unsigned)(uintptr_t)(lds_ptr_t)p; }
__device__ __forceinline__ unsigned sg(unsigned v) { return __builtin_amdgcn_readfirstlane(v); }

constexpr double tanh_u(double y) {
    const double v = 8 * y, a = 2 * 0.79788456080286536 * (v + 0.044715 * v * v * v) / 1024;
    double m = 1;
    for (int i = 12; i > 1; --i) m = 1 + m * a / i;
    m *= a;
    for (int i = 0; i < 10; ++i) m *= m + 2;
    return m / (m + 2);
}
constexpr double h2d(int h) {
    if (h < 1024) return 0;
    double x = (1024 + (h & 1023)) * 0x1p-24;
    for (int e = 1; e < (h >> 10); ++e) x *= 2;
    return x;
}
constexpr int d2h(double x) {
    int h = 0;
    for (int s = 1 << 14; s; s >>= 1) if (h2d(h + s) <= x) h += s;
    return h;
}
constexpr unsigned ip_entry(int k) {
    if (k == 127) return 0x3c003c00u;
    const double y0 = k ? (256 * k - 0.5) / 32767 : 0, dy = ((256 * k + 255.5) / 32767 - y0) / 8;
    double t[9] = {};
    for (int i = 0; i < 9; ++i) t[i] = tanh_u(y0 + i * dy);
    const double b0 = (t[8] - t[0]) / (8 * dy), ym = y0 + 4 * dy, tm = (t[4] + (t[0] + t[8]) / 2) / 2;
    unsigned best = 0;
    double err = 1;
    for (int ha = d2h(tm - b0 * ym) - 1, i = 0; i < 4; ++i, ++ha)
        for (int hb = d2h((tm - h2d(ha)) / ym), j = 0; j < 2; ++j, ++hb) {
            double e = 0;
            for (int n = 0; n < 9; ++n) {
                const double s = h2d(ha) + h2d(hb) * (y0 + n * dy), d = (s < 1 ? s : 1) - t[n];
                e = d > e ? d : -d > e ? -d : e;
            }
            if (e < err) err = e, best = (ha < 1024 ? 0 : ha) | (hb < 1024 ? 0 : hb) << 16;
        }
    return best;
}
constexpr unsigned g_entry(int e, bool neg) {
    double y = e ? ((e & 127) + 0.5 + (e >> 7 ? 128 : 0)) * 0x1p-23 / (1 + 1.0 / 384) : 0;
    for (int i = 1; i < (e >> 7); ++i) y *= 2;
    const double t = y < 1 ? tanh_u(y) : 1;
    const unsigned u = __builtin_bit_cast(unsigned, (float)(4 * y * (neg ? t - 1 : t + 1)));
    return (u + 0x7fff + (u >> 16 & 1)) >> 16;
}
struct TanhTab { unsigned v[1024]; };
constexpr TanhTab gelu_tab(int k0) {
    TanhTab t = {};
    for (int k = k0; k < k0 + 1024 && k < 128 + GSZ / 2; ++k) {
        const int e = 2 * (k < 2048 ? k - 128 : k - 2048);
        t.v[k - k0] = k < 128 ? ip_entry(k) : g_entry(e, k >= 2048) | g_entry(e + 1, k >= 2048) << 16;
    }
    return t;
}
struct WaveTab { unsigned v192[64][4], v244[64][4], v16[64][4], s32[32]; };
struct RegTab { unsigned v240[64][4], v248[64][4]; WaveTab w[8]; };
constexpr unsigned cpos(unsigned w, unsigned px) { return (w >> 1) * 1024 + (px ^ (w & 2) << 2) * 16 + ((w ^ px >> 4) & 1) * 8; }
constexpr RegTab reg_tab() {
    RegTab t = {};
    for (unsigned l = 0; l < 64; ++l) {
        const unsigned G = l >> 4, n = l & 15, q = n >> 2, p = l & 3;
        t.v240[l][2 + (p >> 1)] = G == q ? (p & 1 ? 0x3f800000u : 0x3f80u) : 0u;
        t.v248[l][0] = WB + (l >> 2) * 32 + (p & 1) * 16 + (p >> 1) * 8;
        t.v248[l][1] = WB + (G & 1) * 3080 + ((G >> 1) * 4 + q) * 192 + p * 16;
        t.v248[l][2] = (n & 7) * 384 + (n >> 3) * 192 + G * 16;
        t.v248[l][3] = PB + l * 8;
        for (unsigned w = 0; w < 8; ++w) {
            t.w[w].v244[l][2] = XOFF + n * 400 + (G & 1) * XROW + (G >> 1) * 192 + w * 24;
            t.w[w].v244[l][3] = dw_pos(3 * w) + p * 192 + (1 - (q >> 1)) * 24 + (1 + (G & 1) - (q & 1)) * 8 + ((G ^ p) >> 1) * 4;
            const unsigned pl = PTOP + (w >> 2) * 12288, blk = (w & 3) * 16;
            t.w[w].v192[l][0] = XOFF + cpos(w, (G & 2) * 16 + (n & 8) * 2 + (G & 1) * 8 + (n & 7));
            t.w[w].v192[l][1] = t.w[w].v192[l][0] + PTOP - XOFF;
            t.w[w].v192[l][2] = pl + (cpos((G & 1) * 4 + (G & 2), blk + n) & ~8u);
            t.w[w].v192[l][3] = pl + cpos(p, blk + (l >> 2));
            t.w[w].v16[l][0] = w >= 4 ? (l < 48 ? l * 128 : OOB)
                             : w < 2  ? (12 * ((l >> 3) & 3) + ((l >> 1) & 3)) * (HID * 2) + (2 * (l & 1) + (l >> 5)) * 16 : l * 16;
            const unsigned d = dw_pos(w * 8 + (l >> 5) * 4 + p) + ((l >> 2) & 7) * 24;
            t.w[w].v16[l][1] = w < 3 ? d + (l & 1) * 192 : (l & 4 ? DW2 + 11 * 768 : 13 * 768) + p * 8;
            t.w[w].v16[l][2] = d + (~l & 1) * 192;
            t.w[w].v16[l][3] = l & 1 ? 0x07060302u : 0x05040100u;
        }
    }
    for (unsigned w = 0; w < 8; ++w) {
        const bool w1 = w < 2;
        const unsigned wpd = w1 ? 4 * HID * 2 - 1024 : 0, wl = WB + (w & 1) * (w1 ? 3072 : 3080), wc = w1 ? 64 : 6144;
        const unsigned so = (w & 1) * (w1 ? 48 * HID * 2 : 3072);
        const unsigned s[32] = {wc, so, so + wpd, so + 2 * wpd, w, w >> 2, w, w * 512,
                                0xaaaaaaaau, 0xaaaaaaaau, 0xccccccccu, 0xccccccccu,
                                wl + (w1 ? WA + 12304 : WA), wl + (w1 ? 0 : WA + 6152), wl + (w1 ? WA + 18448 : 6152),
                                wl + (w1 ? WA + 18448 + 6144 : 12304), w1 ? 12 * wc : 0, w1 ? 0 : 12 * wc,
                                0u + XOFF - WB, 3 * wc, ~0x00f0000fu, ~0xf0000f00u,
                                (w >> 2 ? PTOP - 12288u : XHI + 6 * XROW) - XOFF - 4 * 12288, 0x07060302, dw_pos(3 * w + 2) - dw_pos(3 * w), 3072, 0, 0, 0,
                                OOB, OOB, 8 * (unsigned)sizeof(WaveTab) - 2 * PB};
        for (int i = 0; i < 32; ++i) t.w[w].s32[i] = s[i];
    }
    return t;
}
struct Tables { RegTab r; TanhTab t[4]; };
static_assert(sizeof(WaveTab) == 64 * 50 && sizeof(RegTab) == 27648, "table layout");
__device__ const __attribute__((aligned(128))) Tables TABLES = {reg_tab(), {gelu_tab(0), gelu_tab(1024), gelu_tab(2048), gelu_tab(3072)}};

#define R10(r, a) r #a "0", r #a "1", r #a "2", r #a "3", r #a "4", r #a "5", r #a "6", r #a "7", r #a "8", r #a "9"
#define V10(a) R10("v", a)
#define S10(a) R10("s", a)
#define MAIN_CLOB "memory", "scc", "vcc", "v1", "v2", "v3", "v4", "v5", "v6", "v7", "v8", "v9", V10(1), V10(2), V10(3), V10(4), V10(5),  \
    V10(6), V10(7), V10(8), V10(9), V10(10), V10(11), V10(12), V10(13), V10(14), V10(15), V10(16), V10(17), V10(18), V10(19),        \
    V10(20), V10(21), V10(22), V10(23), V10(24), "v250", "v251", "v252", "v253", "v254", "s4", "s5", "s8", "s9", S10(1),       \
    S10(2), S10(3), S10(4), S10(5), "s60", "s61", "s62", "s63", "s64", "s65", "s66", "s67", "s68", "s69"

__global__ __launch_bounds__(512, 2) void convnext_kernel(const __hip_bfloat16* __restrict__ x,
                                                          const __hip_bfloat16* __restrict__ dw,
                                                          const __hip_bfloat16* __restrict__ w1,
                                                          const __hip_bfloat16* __restrict__ w2,
                                                          __hip_bfloat16* __restrict__ out) {
    __shared__ __attribute__((aligned(16))) char smem[LDS_BYTES];

    unsigned tid = threadIdx.x, lds = lds_addr(smem);
    const unsigned bid = blockIdx.x;
    asm volatile(MAIN_ASM
                 : "+{v0}"(tid), "+{s3}"(lds)
                 : "{s[0:1]}"(__builtin_amdgcn_kernarg_segment_ptr()), "{s2}"(bid), "{s[6:7]}"(TABLES.r.w)
                 : MAIN_CLOB);

}

}

extern "C" void kernel_launch(void* const* d_in, const int* in_sizes, int n_in, void* d_out, int out_size,
                              void* d_ws, size_t ws_size, hipStream_t stream) {
    (void)in_sizes; (void)n_in; (void)out_size; (void)d_ws; (void)ws_size;
    const __hip_bfloat16* x = (const __hip_bfloat16*)d_in[0];
    const __hip_bfloat16* dw = (const __hip_bfloat16*)d_in[1];
    const __hip_bfloat16* w1 = (const __hip_bfloat16*)d_in[2];
    const __hip_bfloat16* w2 = (const __hip_bfloat16*)d_in[3];
    __hip_bfloat16* out = (__hip_bfloat16*)d_out;
    convnext_kernel<<<256, 512, 0, stream>>>(x, dw, w1, w2, out);
}
```

```cpp
#include <hip/hip_runtime.h>
#include <hip/hip_bf16.h>
#include <stdint.h>

namespace {

typedef __attribute__((address_space(3))) void* lds_ptr_t;

constexpr int IMG = 512;
constexpr int CH = 96;
constexpr int HID = 384;
constexpr int ROWB = IMG * CH * 2;
constexpr int IPOFF = 13 * 768 + 32;
constexpr int GOFF = IPOFF + 512;
constexpr int GSZ = 30 * 256;
constexpr int DW2 = GOFF + GSZ;
constexpr int XROW = 8200;
constexpr int XOFF = GOFF + 65536 - 6 * XROW;
constexpr int XHI = XOFF + GSZ;
constexpr int PB = XHI + 10 * XROW;
constexpr int WB = XHI + 9 * XROW;
constexpr int WA = 3 * XROW;
constexpr int PTOP = XHI + 12 * XROW - 12288;
constexpr int CNTOFF = WB + WA + 18448 + 12288;
constexpr int LDS_BYTES = 160 * 1024;
static_assert(XOFF == 26864 && CNTOFF - PB == 47136 && DW2 + 11 * 768 + 32 <= XOFF && CNTOFF + 144 <= LDS_BYTES, "LDS map");
constexpr unsigned dw_pos(unsigned u) { return u < 12 ? u * 768 : u == 14 ? 12 * 768 : DW2 + (u - 12 - (u > 14)) * 768; }
constexpr unsigned OOB = 0x40000000u;

#define MAIN_ASM \
  "s_load_dwordx2 s[8:9], s[0:1], 0x0\n\t" \
  "s_load_dwordx2 s[12:13], s[0:1], 0x8\n\t" \
  "s_load_dwordx2 s[20:21], s[0:1], 0x20\n\t" \
  "s_load_dwordx4 s[32:35], s[0:1], 0x10\n\t" \
  "v_readfirstlane_b32 s5, v0\n\t" \
  "v_and_b32 v21, 63, v0\n\t" \
  "s_mov_b32 m0, 107744\n\t" \
  "s_cmp_lt_u32 s5, 128\n\t" \
  "s_cselect_b32 s3, 16, 24\n\t" \
  "v_lshlrev_b32 v22, 4, v21\n\t" \
  "s_load_dwordx2 s[16:17], s[0:1], s3\n\t" \
  "s_mul_i32 s4, s5, 50\n\t" \
  "s_add_u32 s66, s6, s4\n\t" \
  "s_addc_u32 s67, s7, 0\n\t" \
  "global_load_dwordx4 v[244:247], v22, s[66:67] offset:1024\n\t" \
  "global_load_dwordx4 v[16:19], v22, s[66:67] offset:2048\n\t" \
  "global_load_dwordx4 v[240:243], v22, s[6:7] offset:-2048\n\t" \
  "global_load_dwordx4 v[248:251], v22, s[6:7] offset:-1024\n\t" \
  "global_load_dwordx4 v[192:195], v22, s[66:67]\n\t" \
  "s_cmp_lg_u32 s5, 192\n\t" \
  "s_cbranch_scc1 .Lcnx_nc\n\t" \
  "s_getpc_b64 s[14:15]\n\t" \
  ".Lcnx_pc:\n\t" \
  "s_lshr_b32 s3, s2, 3\n\t" \
  "s_lshl_b32 s3, s3, 11\n\t" \
  "v_lshl_add_u32 v1, v21, 5, s3\n\t" \
  "v_min_u32 v1, .Lcnx_end-.Lcnx_pc, v1\n\t" \
  "global_load_lds_dword v1, s[14:15]\n\t" \
  ".Lcnx_nc:\n\t" \
  "v_mul_u32_u24 v254, 0x147b, v0\n\t" \
  "s_bfe_u32 s27, s2, 0x40003\n\t" \
  "v_bfe_u32 v1, v0, 2, 3\n\t" \
  "s_mul_i32 s27, s27, 6144\n\t" \
  "v_lshrrev_b32 v254, 17, v254\n\t" \
  "s_sub_u32 s3, s27, 576\n\t" \
  "v_and_b32 v20, 3, v0\n\t" \
  "s_add_u32 s4, s27, 6720\n\t" \
  "v_sub_u32 v254, v0, v254\n\t" \
  "s_min_u32 s4, s4, 0x18000\n\t" \
  "v_and_b32 v2, 0x1e0, v0\n\t" \
  "s_and_b32 s24, s2, 7\n\t" \
  "v_lshl_add_u32 v254, v254, 4, s3\n\t" \
  "s_lshl_b32 s24, s24, 6\n\t" \
  "v_lshl_or_b32 v20, v20, 3, v2\n\t" \
  "s_mov_b32 s26, 0x18000\n\t" \
  "v_cmp_gt_u32 vcc, s4, v254\n\t" \
  "s_cmp_ge_u32 s2, 128\n\t" \
  "s_cselect_b32 s3, 7, 0\n\t" \
  "s_cselect_b32 s4, 0, 7\n\t" \
  "v_add_u32 v1, s3, v1\n\t" \
  "s_cselect_b32 s3, 28, 35\n\t" \
  "v_cndmask_b32 v254, 2.0, v254, vcc\n\t" \
  "s_cselect_b32 s26, s26, 0xfffe8000\n\t" \
  "v_xor_b32 v1, s4, v1\n\t" \
  "s_add_u32 s24, s24, s3\n\t" \
  "v_and_b32 v1, 7, v1\n\t" \
  "s_movk_i32 s3, 1344\n\t" \
  "s_mul_i32 s24, s24, 0x18000\n\t" \
  "v_mad_u32_u24 v1, v1, s3, v20\n\t" \
  "s_lshl4_add_u32 s25, s5, 26864\n\t" \
  "s_mov_b32 s10, 0x3000000\n\t" \
  "s_mov_b32 s11, 0x20000\n\t" \
  "s_mov_b32 s14, 9408\n\t" \
  "s_mov_b32 s15, 0x20000\n\t" \
  "s_mov_b32 s18, 73728\n\t" \
  "s_mov_b32 s19, 0x20000\n\t" \
  "s_mov_b32 s22, 0x3000000\n\t" \
  "s_mov_b32 s23, 0x20000\n\t" \
  "s_add_u32 s3, s24, s26\n\t" \
  "s_add_u32 s3, s3, s27\n\t" \
  "s_mul_i32 s4, s26, 10\n\t" \
  "v_mov_b32 v24, s3\n\t" \
  "v_mad_i32_i24 v27, v21, s4, v24\n\t" \
  "s_setreg_imm32_b32 hwreg(HW_REG_MODE, 4, 2), 0\n\t" \
  "s_setreg_imm32_b32 hwreg(HW_REG_MODE, 9, 1), 0\n\t" \
  "s_lshl_b32 s65, s26, 4\n\t" \
  "s_lshr_b32 s3, s5, 7\n\t" \
  "s_add_u32 s3, s3, 8\n\t" \
  "s_mul_i32 s3, s3, s26\n\t" \
  "s_add_u32 s29, s24, s27\n\t" \
  "s_add_u32 s29, s29, s3\n\t" \
  "s_and_b32 s3, s5, 64\n\t" \
  "s_mul_i32 s3, s3, 48\n\t" \
  "s_add_u32 s29, s29, s3\n\t" \
  "s_bfe_u32 s3, s5, 0x20006\n\t" \
  "s_add_u32 s3, s3, 6\n\t" \
  "s_mul_i32 s3, s3, s26\n\t" \
  "s_add_u32 s30, s24, s65\n\t" \
  "s_add_u32 s30, s30, s3\n\t" \
  "s_lshl_b32 s31, s26, 2\n\t" \
  "s_mul_i32 s64, s26, 22\n\t" \
  "s_sub_u32 s64, s64, s65\n\t" \
  "s_cmp_ge_u32 s5, 256\n\t" \
  "s_cselect_b32 s27, s27, 0\n\t" \
  "s_cselect_b32 s3, -6, -2\n\t" \
  "s_mul_i32 s3, s3, s26\n\t" \
  "s_add_u32 s28, s29, s3\n\t" \
  "s_mov_b64 vcc, 0\n\t" \
  "s_waitcnt lgkmcnt(0)\n\t" \
  "v_add_co_u32 v24, vcc, s8, v27\n\t" \
  "v_mov_b32 v25, s9\n\t" \
  "v_addc_co_u32 v25, vcc, 0, v25, vcc\n\t" \
  "v_writelane_b32 v24, s12, 3\n\t" \
  "v_writelane_b32 v25, s13, 3\n\t" \
  "v_writelane_b32 v24, s32, 4\n\t" \
  "v_writelane_b32 v25, s33, 4\n\t" \
  "v_writelane_b32 v24, s34, 5\n\t" \
  "v_writelane_b32 v25, s35, 5\n\t" \
  "s_mov_b64 exec, 63\n\t" \
  "global_load_dword v26, v[24:25], off\n\t" \
  "s_mov_b64 exec, -1\n\t" \
  "s_mov_b64 vcc, 0\n\t" \
  "s_and_b32 s13, s13, 0xffff\n\t" \
  "s_and_b32 s9, s9, 0xffff\n\t" \
  "s_and_b32 s17, s17, 0xffff\n\t" \
  "s_cmp_ge_u32 s5, 192\n\t" \
  "s_cbranch_scc1 .Lcnx_nd\n\t" \
  "buffer_load_dwordx2 v[2:3], v1, s[12:15], 0 offen offset:0\n\t" \
  "buffer_load_dwordx2 v[4:5], v1, s[12:15], 0 offen offset:192\n\t" \
  "buffer_load_dwordx2 v[6:7], v1, s[12:15], 0 offen offset:384\n\t" \
  "buffer_load_dwordx2 v[8:9], v1, s[12:15], 0 offen offset:576\n\t" \
  "buffer_load_dwordx2 v[10:11], v1, s[12:15], 0 offen offset:768\n\t" \
  "buffer_load_dwordx2 v[12:13], v1, s[12:15], 0 offen offset:960\n\t" \
  "buffer_load_dwordx2 v[14:15], v1, s[12:15], 0 offen offset:1152\n\t" \
  ".Lcnx_nd:\n\t" \
  "s_and_b32 s3, s5, 0x140\n\t" \
  "s_cmp_lg_u32 s3, 64\n\t" \
  "s_cbranch_scc1 .Lcnx_nw\n\t" \
  "s_lshr_b32 s3, s2, 3\n\t" \
  "s_mul_i32 s3, s3, 2304\n\t" \
  "s_mov_b32 m0, 107744\n\t" \
  "v_mul_u32_u24 v21, 36, v21\n\t" \
  "buffer_load_dword v21, s[16:19], s3 offen lds\n\t" \
  ".Lcnx_nw:\n\t" \
  "s_add_u32 m0, s25, 0\n\t" \
  "s_add_u32 s24, s24, s26\n\t" \
  "buffer_load_dwordx4 v254, s[8:11], s24 offen lds\n\t" \
  "s_add_u32 m0, s25, 8200\n\t" \
  "s_add_u32 s24, s24, s26\n\t" \
  "buffer_load_dwordx4 v254, s[8:11], s24 offen lds\n\t" \
  "s_add_u32 m0, s25, 16400\n\t" \
  "s_add_u32 s24, s24, s26\n\t" \
  "buffer_load_dwordx4 v254, s[8:11], s24 offen lds\n\t" \
  "s_add_u32 m0, s25, 24600\n\t" \
  "s_add_u32 s24, s24, s26\n\t" \
  "buffer_load_dwordx4 v254, s[8:11], s24 offen lds\n\t" \
  "s_load_dwordx16 s[32:47], s[66:67], 0xc00\n\t" \
  "s_load_dwordx16 s[48:63], s[66:67], 0xc40\n\t" \
  "s_and_b32 s21, s21, 0xffff\n\t" \
  "s_waitcnt vmcnt(5)\n\t" \
  "v_mfma_f32_32x32x16_bf16 v[144:159], v[242:245], v[240:243], 0\n\t" \
  "v_mfma_f32_32x32x16_bf16 v[160:175], v[242:245], v[240:243], 0\n\t" \
  "v_bfe_u32 v236, v251, 3, 2\n\t" \
  "v_mul_u32_u24 v236, 0x5555, v236\n\t" \
  "v_add_u32 v252, s27, v16\n\t" \
  "s_lshl2_add_u32 s12, s26, s29\n\t" \
  "s_lshl3_add_u32 s13, s26, s29\n\t" \
  "v_mov_b32 v253, v246\n\t" \
  "s_movk_i32 s68, 0x2b80\n\t" \
  "s_mov_b32 s69, 0x2b800000\n\t" \
  "s_cmp_gt_u32 s5, 192\n\t" \
  "s_cbranch_scc1 .Lcnx_e2\n\t" \
  "s_cmp_eq_u32 s5, 192\n\t" \
  "s_cbranch_scc1 .Lcnx_e1\n\t" \
  "v_xor_b32 v23, 0x02020202, v19\n\t" \
  "s_waitcnt vmcnt(4)\n\t" \
  "v_perm_b32 v24, v4, 0, v19\n\t" \
  "v_perm_b32 v25, v12, v8, v19\n\t" \
  "v_perm_b32 v26, v6, v2, v19\n\t" \
  "v_perm_b32 v27, v14, v10, v19\n\t" \
  "v_perm_b32 v28, v8, v4, v19\n\t" \
  "v_perm_b32 v29, 0, v12, v19\n\t" \
  "ds_write_b64 v17, v[24:25] offset:0\n\t" \
  "ds_write_b64 v17, v[26:27] offset:8\n\t" \
  "ds_write_b64 v17, v[28:29] offset:16\n\t" \
  "v_perm_b32 v30, v4, 0, v23\n\t" \
  "v_perm_b32 v31, v12, v8, v23\n\t" \
  "v_perm_b32 v32, v6, v2, v23\n\t" \
  "v_perm_b32 v33, v14, v10, v23\n\t" \
  "v_perm_b32 v34, v8, v4, v23\n\t" \
  "v_perm_b32 v35, 0, v12, v23\n\t" \
  "ds_write_b64 v18, v[30:31] offset:0\n\t" \
  "ds_write_b64 v18, v[32:33] offset:8\n\t" \
  "ds_write_b64 v18, v[34:35] offset:16\n\t" \
  "v_perm_b32 v37, v5, 0, v19\n\t" \
  "v_perm_b32 v36, v13, v9, v19\n\t" \
  "v_perm_b32 v39, v7, v3, v19\n\t" \
  "v_perm_b32 v38, v15, v11, v19\n\t" \
  "v_perm_b32 v41, v9, v5, v19\n\t" \
  "v_perm_b32 v40, 0, v13, v19\n\t" \
  "ds_write_b64 v17, v[36:37] offset:384\n\t" \
  "ds_write_b64 v17, v[38:39] offset:392\n\t" \
  "ds_write_b64 v17, v[40:41] offset:400\n\t" \
  "v_perm_b32 v43, v5, 0, v23\n\t" \
  "v_perm_b32 v42, v13, v9, v23\n\t" \
  "v_perm_b32 v45, v7, v3, v23\n\t" \
  "v_perm_b32 v44, v15, v11, v23\n\t" \
  "v_perm_b32 v47, v9, v5, v23\n\t" \
  "v_perm_b32 v46, 0, v13, v23\n\t" \
  "ds_write_b64 v18, v[42:43] offset:384\n\t" \
  "ds_write_b64 v18, v[44:45] offset:392\n\t" \
  "ds_write_b64 v18, v[46:47] offset:400\n\t" \
  "s_branch .Lcnx_e3\n\t" \
  ".Lcnx_e1:\n\t" \
  "s_mov_b64 exec, 0x3ffff\n\t" \
  "ds_write_b64 v251, v[240:241] offset:47136\n\t" \
  "s_mov_b64 exec, 0xff\n\t" \
  "ds_write_b64 v17, v[240:241]\n\t" \
  "s_mov_b64 exec, -1\n\t" \
  "s_branch .Lcnx_e3\n\t" \
  ".Lcnx_e2:\n\t" \
  "s_setprio 1\n\t" \
  ".Lcnx_e3:\n\t" \
  "s_add_u32 m0, s25, 32800\n\t" \
  "s_add_u32 s24, s24, s26\n\t" \
  "buffer_load_dwordx4 v254, s[8:11], s24 offen lds\n\t" \
  "s_add_u32 m0, s25, 41000\n\t" \
  "s_add_u32 s24, s24, s26\n\t" \
  "buffer_load_dwordx4 v254, s[8:11], s24 offen lds\n\t" \
  "s_add_u32 m0, s25, 56880\n\t" \
  "s_add_u32 s24, s24, s26\n\t" \
  "buffer_load_dwordx4 v254, s[8:11], s24 offen lds\n\t" \
  "s_add_u32 m0, s25, 65080\n\t" \
  "s_add_u32 s24, s24, s26\n\t" \
  "buffer_load_dwordx4 v254, s[8:11], s24 offen lds\n\t" \
  "s_waitcnt vmcnt(6) lgkmcnt(0)\n\t" \
  "s_barrier\n\t" \
  "s_sub_u32 s66, s66, 233088\n\t" \
  "s_subb_u32 s67, s67, 0\n\t" \
  "v_add_u32 v232, s56, v247\n\t" \
  "ds_read2_b32 v[98:99], v247 offset0:0 offset1:6\n\t" \
  "ds_read2_b32 v[114:115], v247 offset0:192 offset1:198\n\t" \
  "ds_read2_b32 v[130:131], v232 offset0:0 offset1:6\n\t" \
  "ds_read2_b32 v[102:103], v247 offset0:12 offset1:18\n\t" \
  "ds_read2_b32 v[118:119], v247 offset0:204 offset1:210\n\t" \
  "ds_read2_b32 v[134:135], v232 offset0:12 offset1:18\n\t" \
  "ds_read2_b32 v[106:107], v247 offset0:24 offset1:30\n\t" \
  "ds_read2_b32 v[122:123], v247 offset0:216 offset1:222\n\t" \
  "ds_read2_b32 v[138:139], v232 offset0:24 offset1:30\n\t" \
  "ds_read2_b32 v[110:111], v247 offset0:36 offset1:42\n\t" \
  "ds_read2_b32 v[126:127], v247 offset0:228 offset1:234\n\t" \
  "ds_read2_b32 v[142:143], v232 offset0:36 offset1:42\n\t" \
  "v_add_u32 v232, 56880, v253\n\t" \
  "v_add_u32 v233, s39, v251\n\t" \
  "v_mfma_f32_32x32x16_bf16 v[176:191], v[242:245], v[240:243], 0\n\t" \
  "ds_read_b64 v[200:201], v253 offset:0\n\t" \
  "ds_read_b64 v[202:203], v253 offset:400\n\t" \
  "ds_read_b64 v[204:205], v253 offset:800\n\t" \
  "ds_read_b64 v[206:207], v253 offset:1200\n\t" \
  "ds_read_b64 v[208:209], v253 offset:8\n\t" \
  "ds_read_b64 v[210:211], v253 offset:408\n\t" \
  "ds_read_b64 v[212:213], v253 offset:808\n\t" \
  "ds_read_b64 v[214:215], v253 offset:1208\n\t" \
  "ds_read_b64 v[216:217], v253 offset:16\n\t" \
  "ds_read_b64 v[218:219], v253 offset:416\n\t" \
  "ds_read_b64 v[220:221], v253 offset:816\n\t" \
  "ds_read_b64 v[222:223], v253 offset:1216\n\t" \
  "s_add_u32 m0, s25, 73280\n\t" \
  "s_add_u32 s24, s24, s26\n\t" \
  "buffer_load_dwordx4 v254, s[8:11], s24 offen lds\n\t" \
  "s_add_u32 m0, s25, 81480\n\t" \
  "s_add_u32 s24, s24, s26\n\t" \
  "buffer_load_dwordx4 v254, s[8:11], s24 offen lds\n\t" \
  "s_add_u32 m0, s25, 89680\n\t" \
  "s_add_u32 s24, s24, s26\n\t" \
  "buffer_load_dwordx4 v254, s[8:11], s24 offen lds\n\t" \
  "s_add_u32 m0, s25, 97880\n\t" \
  "s_add_u32 s24, s24, s26\n\t" \
  "buffer_load_dwordx4 v254, s[8:11], s24 offen lds\n\t" \
  "s_waitcnt lgkmcnt(15)\n\t" \
  "v_dot2_f32_bf16 v96, v98, s68, 0\n\t" \
  "v_dot2_f32_bf16 v97, v98, s69, 0\n\t" \
  "v_dot2_f32_bf16 v98, v99, s68, 0\n\t" \
  "v_dot2_f32_bf16 v99, v99, s69, 0\n\t" \
  "v_dot2_f32_bf16 v112, v114, s68, 0\n\t" \
  "v_dot2_f32_bf16 v113, v114, s69, 0\n\t" \
  "v_dot2_f32_bf16 v114, v115, s68, 0\n\t" \
  "v_dot2_f32_bf16 v115, v115, s69, 0\n\t" \
  "v_dot2_f32_bf16 v128, v130, s68, 0\n\t" \
  "v_dot2_f32_bf16 v129, v130, s69, 0\n\t" \
  "v_dot2_f32_bf16 v130, v131, s68, 0\n\t" \
  "v_dot2_f32_bf16 v131, v131, s69, 0\n\t" \
  "s_waitcnt lgkmcnt(8)\n\t" \
  "v_smfmac_f32_16x16x64_bf16 v[144:147], v[96:99], v[200:207], v236\n\t" \
  "v_dot2_f32_bf16 v100, v102, s68, 0\n\t" \
  "v_dot2_f32_bf16 v101, v102, s69, 0\n\t" \
  "v_dot2_f32_bf16 v102, v103, s68, 0\n\t" \
  "v_dot2_f32_bf16 v103, v103, s69, 0\n\t" \
  "s_waitcnt vmcnt(8) lgkmcnt(0)\n\t" \
  "s_barrier\n\t" \
  "ds_read_b64 v[224:225], v253 offset:16400\n\t" \
  "ds_read_b64 v[226:227], v253 offset:16800\n\t" \
  "ds_read_b64 v[228:229], v253 offset:17200\n\t" \
  "ds_read_b64 v[230:231], v253 offset:17600\n\t" \
  "v_smfmac_f32_16x16x64_bf16 v[148:151], v[112:115], v[208:215], v236\n\t" \
  "ds_read_b64 v[200:201], v253 offset:16408\n\t" \
  "ds_read_b64 v[202:203], v253 offset:16808\n\t" \
  "ds_read_b64 v[204:205], v253 offset:17208\n\t" \
  "ds_read_b64 v[206:207], v253 offset:17608\n\t" \
  "v_dot2_f32_bf16 v116, v118, s68, 0\n\t" \
  "v_dot2_f32_bf16 v117, v118, s69, 0\n\t" \
  "v_dot2_f32_bf16 v118, v119, s68, 0\n\t" \
  "v_dot2_f32_bf16 v119, v119, s69, 0\n\t" \
  "v_smfmac_f32_16x16x64_bf16 v[152:155], v[128:131], v[216:223], v236\n\t" \
  "ds_read_b64 v[208:209], v253 offset:16416\n\t" \
  "ds_read_b64 v[210:211], v253 offset:16816\n\t" \
  "ds_read_b64 v[212:213], v253 offset:17216\n\t" \
  "ds_read_b64 v[214:215], v253 offset:17616\n\t" \
  "v_dot2_f32_bf16 v132, v134, s68, 0\n\t" \
  "v_dot2_f32_bf16 v133, v134, s69, 0\n\t" \
  "v_dot2_f32_bf16 v134, v135, s68, 0\n\t" \
  "v_dot2_f32_bf16 v135, v135, s69, 0\n\t" \
  "s_waitcnt lgkmcnt(8)\n\t" \
  "v_smfmac_f32_16x16x64_bf16 v[156:159], v[96:99], v[224:231], v236\n\t" \
  "v_dot2_f32_bf16 v104, v106, s68, 0\n\t" \
  "v_dot2_f32_bf16 v105, v106, s69, 0\n\t" \
  "v_dot2_f32_bf16 v106, v107, s68, 0\n\t" \
  "v_dot2_f32_bf16 v107, v107, s69, 0\n\t" \
  "v_smfmac_f32_16x16x64_bf16 v[144:147], v[100:103], v[224:231], v236\n\t" \
  "s_waitcnt vmcnt(6) lgkmcnt(0)\n\t" \
  "s_barrier\n\t" \
  "ds_read_b64 v[216:217], v253 offset:32800\n\t" \
  "ds_read_b64 v[218:219], v253 offset:33200\n\t" \
  "ds_read_b64 v[220:221], v253 offset:33600\n\t" \
  "ds_read_b64 v[222:223], v253 offset:34000\n\t" \
  "s_add_u32 m0, s25, 0\n\t" \
  "s_add_u32 s24, s24, s26\n\t" \
  "v_smfmac_f32_16x16x64_bf16 v[160:163], v[112:115], v[200:207], v236\n\t" \
  "buffer_load_dwordx4 v254, s[8:11], s24 offen lds\n\t" \
  "ds_read_b64 v[224:225], v253 offset:32808\n\t" \
  "ds_read_b64 v[226:227], v253 offset:33208\n\t" \
  "v_dot2_f32_bf16 v120, v122, s68, 0\n\t" \
  "v_dot2_f32_bf16 v121, v122, s69, 0\n\t" \
  "v_dot2_f32_bf16 v122, v123, s68, 0\n\t" \
  "v_dot2_f32_bf16 v123, v123, s69, 0\n\t" \
  "v_smfmac_f32_16x16x64_bf16 v[148:151], v[116:119], v[200:207], v236\n\t" \
  "ds_read_b64 v[228:229], v253 offset:33608\n\t" \
  "ds_read_b64 v[230:231], v253 offset:34008\n\t" \
  "s_add_u32 m0, s25, 8200\n\t" \
  "s_add_u32 s24, s24, s26\n\t" \
  "v_smfmac_f32_16x16x64_bf16 v[164:167], v[128:131], v[208:215], v236\n\t" \
  "buffer_load_dwordx4 v254, s[8:11], s24 offen lds\n\t" \
  "ds_read_b64 v[200:201], v253 offset:32816\n\t" \
  "ds_read_b64 v[202:203], v253 offset:33216\n\t" \
  "v_dot2_f32_bf16 v136, v138, s68, 0\n\t" \
  "v_dot2_f32_bf16 v137, v138, s69, 0\n\t" \
  "v_dot2_f32_bf16 v138, v139, s68, 0\n\t" \
  "v_dot2_f32_bf16 v139, v139, s69, 0\n\t" \
  "v_mfma_f32_32x32x16_bf16 v[0:15], v[242:245], v[240:243], 0\n\t" \
  "v_smfmac_f32_16x16x64_bf16 v[152:155], v[132:135], v[208:215], v236\n\t" \
  "ds_read_b64 v[204:205], v253 offset:33616\n\t" \
  "s_waitcnt lgkmcnt(7)\n\t" \
  "v_smfmac_f32_16x16x64_bf16 v[168:171], v[96:99], v[216:223], v236\n\t" \
  "ds_read_b64 v[206:207], v253 offset:34016\n\t" \
  "v_dot2_f32_bf16 v108, v110, s68, 0\n\t" \
  "v_dot2_f32_bf16 v109, v110, s69, 0\n\t" \
  "v_dot2_f32_bf16 v110, v111, s68, 0\n\t" \
  "v_dot2_f32_bf16 v111, v111, s69, 0\n\t" \
  "s_add_u32 m0, s25, 16400\n\t" \
  "s_add_u32 s24, s24, s26\n\t" \
  "v_smfmac_f32_16x16x64_bf16 v[156:159], v[100:103], v[216:223], v236\n\t" \
  "buffer_load_dwordx4 v254, s[8:11], s24 offen lds\n\t" \
  "v_smfmac_f32_16x16x64_bf16 v[144:147], v[104:107], v[216:223], v236\n\t" \
  "s_waitcnt vmcnt(7) lgkmcnt(0)\n\t" \
  "s_barrier\n\t" \
  "v_smfmac_f32_16x16x64_bf16 v[172:175], v[112:115], v[224:231], v236\n\t" \
  "ds_read_b64 v[208:209], v232 offset:0\n\t" \
  "ds_read_b64 v[210:211], v232 offset:400\n\t" \
  "v_dot2_f32_bf16 v124, v126, s68, 0\n\t" \
  "v_dot2_f32_bf16 v125, v126, s69, 0\n\t" \
  "v_dot2_f32_bf16 v126, v127, s68, 0\n\t" \
  "v_dot2_f32_bf16 v127, v127, s69, 0\n\t" \
  "s_add_u32 m0, s25, 24600\n\t" \
  "s_add_u32 s24, s24, s26\n\t" \
  "v_smfmac_f32_16x16x64_bf16 v[160:163], v[116:119], v[224:231], v236\n\t" \
  "buffer_load_dwordx4 v254, s[8:11], s24 offen lds\n\t" \
  "ds_read_b64 v[212:213], v232 offset:800\n\t" \
  "ds_read_b64 v[214:215], v232 offset:1200\n\t" \
  "v_smfmac_f32_16x16x64_bf16 v[148:151], v[120:123], v[224:231], v236\n\t" \
  "ds_read_b64 v[216:217], v232 offset:8\n\t" \
  "ds_read_b64 v[218:219], v232 offset:408\n\t" \
  "v_smfmac_f32_16x16x64_bf16 v[176:179], v[128:131], v[200:207], v236\n\t" \
  "ds_read_b64 v[220:221], v232 offset:808\n\t" \
  "ds_read_b64 v[222:223], v232 offset:1208\n\t" \
  "v_dot2_f32_bf16 v140, v142, s68, 0\n\t" \
  "v_dot2_f32_bf16 v141, v142, s69, 0\n\t" \
  "v_dot2_f32_bf16 v142, v143, s68, 0\n\t" \
  "v_dot2_f32_bf16 v143, v143, s69, 0\n\t" \
  "v_smfmac_f32_16x16x64_bf16 v[164:167], v[132:135], v[200:207], v236\n\t" \
  "ds_read_b64 v[224:225], v232 offset:16\n\t" \
  "s_add_u32 m0, s25, 32800\n\t" \
  "s_add_u32 s24, s24, s26\n\t" \
  "v_smfmac_f32_16x16x64_bf16 v[152:155], v[136:139], v[200:207], v236\n\t" \
  "buffer_load_dwordx4 v254, s[8:11], s24 offen lds\n\t" \
  "ds_read_b64 v[226:227], v232 offset:416\n\t" \
  "s_waitcnt lgkmcnt(6)\n\t" \
  "v_smfmac_f32_16x16x64_bf16 v[180:183], v[96:99], v[208:215], v236\n\t" \
  "ds_read_b64 v[228:229], v232 offset:816\n\t" \
  "v_smfmac_f32_16x16x64_bf16 v[168:171], v[100:103], v[208:215], v236\n\t" \
  "ds_read_b64 v[230:231], v232 offset:1216\n\t" \
  "v_mfma_f32_32x32x16_bf16 v[16:31], v[242:245], v[240:243], 0\n\t" \
  "v_mov_b32 v196, 0x3f003f00\n\t" \
  "v_mov_b32 v197, 0x3f003f00\n\t" \
  "v_smfmac_f32_16x16x64_bf16 v[156:159], v[104:107], v[208:215], v236\n\t" \
  "v_mov_b32 v198, 0x3f003f00\n\t" \
  "v_mov_b32 v199, 0x3f003f00\n\t" \
  "s_add_u32 m0, s25, 41000\n\t" \
  "s_add_u32 s24, s24, s26\n\t" \
  "v_smfmac_f32_16x16x64_bf16 v[144:147], v[108:111], v[208:215], v236\n\t" \
  "buffer_load_dwordx4 v254, s[8:11], s24 offen lds\n\t" \
  "s_waitcnt vmcnt(8) lgkmcnt(0)\n\t" \
  "s_barrier\n\t" \
  "v_smfmac_f32_16x16x64_bf16 v[184:187], v[112:115], v[216:223], v236\n\t" \
  "ds_read_b64 v[200:201], v232 offset:16400\n\t" \
  "ds_read_b64 v[202:203], v232 offset:16800\n\t" \
  "v_smfmac_f32_16x16x64_bf16 v[172:175], v[116:119], v[216:223], v236\n\t" \
  "ds_read_b64 v[204:205], v232 offset:17200\n\t" \
  "ds_read_b64 v[206:207], v232 offset:17600\n\t" \
  "v_smfmac_f32_16x16x64_bf16 v[160:163], v[120:123], v[216:223], v236\n\t" \
  "ds_read_b64 v[208:209], v232 offset:16408\n\t" \
  "v_cvt_pk_bf16_f32 v144, v144, v145\n\t" \
  "v_cvt_pk_bf16_f32 v145, v146, v147\n\t" \
  "s_add_u32 m0, s25, 56880\n\t" \
  "s_add_u32 s24, s24, s26\n\t" \
  "v_smfmac_f32_16x16x64_bf16 v[148:151], v[124:127], v[216:223], v236\n\t" \
  "buffer_load_dwordx4 v254, s[8:11], s24 offen lds\n\t" \
  "ds_read_b64 v[210:211], v232 offset:16808\n\t" \
  "v_smfmac_f32_16x16x64_bf16 v[188:191], v[128:131], v[224:231], v236\n\t" \
  "ds_read_b64 v[212:213], v232 offset:17208\n\t" \
  "v_smfmac_f32_16x16x64_bf16 v[176:179], v[132:135], v[224:231], v236\n\t" \
  "ds_read_b64 v[214:215], v232 offset:17608\n\t" \
  "v_smfmac_f32_16x16x64_bf16 v[164:167], v[136:139], v[224:231], v236\n\t" \
  "ds_read_b64 v[216:217], v232 offset:16416\n\t" \
  "v_cvt_pk_bf16_f32 v148, v148, v149\n\t" \
  "v_cvt_pk_bf16_f32 v149, v150, v151\n\t" \
  "s_add_u32 m0, s25, 65080\n\t" \
  "s_add_u32 s24, s24, s26\n\t" \
  "v_smfmac_f32_16x16x64_bf16 v[152:155], v[140:143], v[224:231], v236\n\t" \
  "buffer_load_dwordx4 v254, s[8:11], s24 offen lds\n\t" \
  "ds_read_b64 v[218:219], v232 offset:16816\n\t" \
  "s_waitcnt lgkmcnt(6)\n\t" \
  "v_smfmac_f32_16x16x64_bf16 v[0:3], v[96:99], v[200:207], v236\n\t" \
  "ds_read_b64 v[220:221], v232 offset:17216\n\t" \
  "v_smfmac_f32_16x16x64_bf16 v[180:183], v[100:103], v[200:207], v236\n\t" \
  "ds_read_b64 v[222:223], v232 offset:17616\n\t" \
  "v_mfma_f32_32x32x16_bf16 v[32:47], v[242:245], v[240:243], 0\n\t" \
  "v_smfmac_f32_16x16x64_bf16 v[168:171], v[104:107], v[200:207], v236\n\t" \
  "v_cvt_pk_bf16_f32 v152, v152, v153\n\t" \
  "v_cvt_pk_bf16_f32 v153, v154, v155\n\t" \
  "v_smfmac_f32_16x16x64_bf16 v[156:159], v[108:111], v[200:207], v236\n\t" \
  "s_waitcnt vmcnt(8) lgkmcnt(0)\n\t" \
  "s_barrier\n\t" \
  "s_add_u32 m0, s25, 73280\n\t" \
  "s_add_u32 s24, s24, s26\n\t" \
  "v_smfmac_f32_16x16x64_bf16 v[4:7], v[112:115], v[208:215], v236\n\t" \
  "buffer_load_dwordx4 v254, s[8:11], s24 offen lds\n\t" \
  "ds_read_b64 v[224:225], v232 offset:32800\n\t" \
  "ds_read_b64 v[226:227], v232 offset:33200\n\t" \
  "v_smfmac_f32_16x16x64_bf16 v[184:187], v[116:119], v[208:215], v236\n\t" \
  "ds_read_b64 v[228:229], v232 offset:33600\n\t" \
  "ds_read_b64 v[230:231], v232 offset:34000\n\t" \
  "s_add_u32 m0, s25, 81480\n\t" \
  "s_add_u32 s24, s24, s26\n\t" \
  "v_smfmac_f32_16x16x64_bf16 v[172:175], v[120:123], v[208:215], v236\n\t" \
  "buffer_load_dwordx4 v254, s[8:11], s24 offen lds\n\t" \
  "ds_read_b64 v[200:201], v232 offset:32808\n\t" \
  "v_cvt_pk_bf16_f32 v156, v156, v157\n\t" \
  "v_cvt_pk_bf16_f32 v157, v158, v159\n\t" \
  "v_smfmac_f32_16x16x64_bf16 v[160:163], v[124:127], v[208:215], v236\n\t" \
  "ds_read_b64 v[202:203], v232 offset:33208\n\t" \
  "v_smfmac_f32_16x16x64_bf16 v[8:11], v[128:131], v[216:223], v236\n\t" \
  "ds_read_b64 v[204:205], v232 offset:33608\n\t" \
  "v_smfmac_f32_16x16x64_bf16 v[188:191], v[132:135], v[216:223], v236\n\t" \
  "ds_read_b64 v[206:207], v232 offset:34008\n\t" \
  "v_smfmac_f32_16x16x64_bf16 v[176:179], v[136:139], v[216:223], v236\n\t" \
  "ds_read_b64 v[208:209], v232 offset:32816\n\t" \
  "s_nop 0\n\t" \
  "v_cvt_pk_bf16_f32 v160, v160, v161\n\t" \
  "v_cvt_pk_bf16_f32 v161, v162, v163\n\t" \
  "v_smfmac_f32_16x16x64_bf16 v[164:167], v[140:143], v[216:223], v236\n\t" \
  "ds_read_b64 v[210:211], v232 offset:33216\n\t" \
  "s_waitcnt lgkmcnt(6)\n\t" \
  "v_smfmac_f32_16x16x64_bf16 v[12:15], v[96:99], v[224:231], v236\n\t" \
  "ds_read_b64 v[212:213], v232 offset:33616\n\t" \
  "v_smfmac_f32_16x16x64_bf16 v[0:3], v[100:103], v[224:231], v236\n\t" \
  "ds_read_b64 v[214:215], v232 offset:34016\n\t" \
  "v_smfmac_f32_16x16x64_bf16 v[180:183], v[104:107], v[224:231], v236\n\t" \
  "s_nop 0\n\t" \
  "v_cvt_pk_bf16_f32 v164, v164, v165\n\t" \
  "v_cvt_pk_bf16_f32 v165, v166, v167\n\t" \
  "v_smfmac_f32_16x16x64_bf16 v[168:171], v[108:111], v[224:231], v236\n\t" \
  "s_waitcnt vmcnt(8) lgkmcnt(0)\n\t" \
  "s_barrier\n\t" \
  "v_smfmac_f32_16x16x64_bf16 v[16:19], v[112:115], v[200:207], v236\n\t" \
  "ds_write_b64 v193, v[144:145] offset:12288\n\t" \
  "ds_read_b64 v[216:217], v253 offset:0\n\t" \
  "v_smfmac_f32_16x16x64_bf16 v[4:7], v[116:119], v[200:207], v236\n\t" \
  "ds_write_b64 v193, v[148:149] offset:16384\n\t" \
  "ds_read_b64 v[218:219], v253 offset:400\n\t" \
  "v_smfmac_f32_16x16x64_bf16 v[184:187], v[120:123], v[200:207], v236\n\t" \
  "ds_write_b64 v193, v[152:153] offset:20480\n\t" \
  "ds_read_b64 v[220:221], v253 offset:800\n\t" \
  "v_cvt_pk_bf16_f32 v168, v168, v169\n\t" \
  "v_cvt_pk_bf16_f32 v169, v170, v171\n\t" \
  "v_smfmac_f32_16x16x64_bf16 v[172:175], v[124:127], v[200:207], v236\n\t" \
  "ds_write_b64 v193, v[156:157] offset:0\n\t" \
  "ds_read_b64 v[222:223], v253 offset:1200\n\t" \
  "v_smfmac_f32_16x16x64_bf16 v[20:23], v[128:131], v[208:215], v236\n\t" \
  "ds_write_b64 v193, v[160:161] offset:4096\n\t" \
  "ds_read_b64 v[224:225], v253 offset:8\n\t" \
  "v_smfmac_f32_16x16x64_bf16 v[8:11], v[132:135], v[208:215], v236\n\t" \
  "ds_write_b64 v193, v[164:165] offset:8192\n\t" \
  "ds_read_b64 v[226:227], v253 offset:408\n\t" \
  "v_smfmac_f32_16x16x64_bf16 v[188:191], v[136:139], v[208:215], v236\n\t" \
  "ds_read_b64 v[228:229], v253 offset:808\n\t" \
  "ds_read_b64 v[230:231], v253 offset:1208\n\t" \
  "v_cvt_pk_bf16_f32 v172, v172, v173\n\t" \
  "v_cvt_pk_bf16_f32 v173, v174, v175\n\t" \
  "v_smfmac_f32_16x16x64_bf16 v[176:179], v[140:143], v[208:215], v236\n\t" \
  "ds_read_b64 v[200:201], v253 offset:16\n\t" \
  "ds_read_b64 v[202:203], v253 offset:416\n\t" \
  "s_waitcnt lgkmcnt(8)\n\t" \
  "v_smfmac_f32_16x16x64_bf16 v[24:27], v[96:99], v[216:223], v236\n\t" \
  "ds_read_b64 v[204:205], v253 offset:816\n\t" \
  "v_smfmac_f32_16x16x64_bf16 v[12:15], v[100:103], v[216:223], v236\n\t" \
  "ds_read_b64 v[206:207], v253 offset:1216\n\t" \
  "v_smfmac_f32_16x16x64_bf16 v[0:3], v[104:107], v[216:223], v236\n\t" \
  "v_cvt_pk_bf16_f32 v176, v176, v177\n\t" \
  "v_cvt_pk_bf16_f32 v177, v178, v179\n\t" \
  "v_smfmac_f32_16x16x64_bf16 v[180:183], v[108:111], v[216:223], v236\n\t" \
  "s_waitcnt vmcnt(6) lgkmcnt(0)\n\t" \
  "s_barrier\n\t" \
  "v_smfmac_f32_16x16x64_bf16 v[28:31], v[112:115], v[224:231], v236\n\t" \
  "ds_write_b64 v192, v[168:169] offset:0\n\t" \
  "ds_read_b64 v[208:209], v253 offset:16400\n\t" \
  "ds_read_b128 v[144:147], v194 offset:0\n\t" \
  "v_smfmac_f32_16x16x64_bf16 v[16:19], v[116:119], v[224:231], v236\n\t" \
  "ds_write_b64 v192, v[172:173] offset:4096\n\t" \
  "ds_read_b64 v[210:211], v253 offset:16800\n\t" \
  "ds_read_b128 v[148:151], v194 offset:4096\n\t" \
  "v_smfmac_f32_16x16x64_bf16 v[4:7], v[120:123], v[224:231], v236\n\t" \
  "ds_write_b64 v192, v[176:177] offset:8192\n\t" \
  "ds_read_b64 v[212:213], v253 offset:17200\n\t" \
  "v_cvt_pk_bf16_f32 v180, v180, v181\n\t" \
  "v_cvt_pk_bf16_f32 v181, v182, v183\n\t" \
  "ds_read_b128 v[152:155], v194 offset:8192\n\t" \
  "v_smfmac_f32_16x16x64_bf16 v[184:187], v[124:127], v[224:231], v236\n\t" \
  "ds_read_b64 v[214:215], v253 offset:17600\n\t" \
  "ds_read_b64 v[216:217], v253 offset:16408\n\t" \
  "v_add_u32 v194, -93792, v194\n\t" \
  "v_smfmac_f32_16x16x64_bf16 v[32:35], v[128:131], v[200:207], v236\n\t" \
  "ds_read_b64 v[218:219], v253 offset:16808\n\t" \
  "ds_read_b64 v[220:221], v253 offset:17208\n\t" \
  "v_smfmac_f32_16x16x64_bf16 v[20:23], v[132:135], v[200:207], v236\n\t" \
  "ds_read_b64 v[222:223], v253 offset:17608\n\t" \
  "s_waitcnt lgkmcnt(11)\n\t" \
  "v_mfma_f32_16x16x32_bf16 v[168:171], v[144:147], v[144:147], 0\n\t" \
  "ds_read_b64 v[224:225], v253 offset:16416\n\t" \
  "v_smfmac_f32_16x16x64_bf16 v[8:11], v[136:139], v[200:207], v236\n\t" \
  "ds_read_b64 v[226:227], v253 offset:16816\n\t" \
  "v_cvt_pk_bf16_f32 v184, v184, v185\n\t" \
  "v_cvt_pk_bf16_f32 v185, v186, v187\n\t" \
  "v_mfma_f32_16x16x32_bf16 v[172:175], v[196:199], v[144:147], 0\n\t" \
  "ds_read_b64 v[228:229], v253 offset:17216\n\t" \
  "v_smfmac_f32_16x16x64_bf16 v[188:191], v[140:143], v[200:207], v236\n\t" \
  "ds_read_b64 v[230:231], v253 offset:17616\n\t" \
  "s_waitcnt lgkmcnt(12)\n\t" \
  "v_mfma_f32_16x16x32_bf16 v[168:171], v[148:151], v[148:151], v[168:171]\n\t" \
  "s_waitcnt lgkmcnt(8)\n\t" \
  "v_smfmac_f32_16x16x64_bf16 v[36:39], v[96:99], v[208:215], v236\n\t" \
  "v_mfma_f32_16x16x32_bf16 v[172:175], v[196:199], v[148:151], v[172:175]\n\t" \
  "v_smfmac_f32_16x16x64_bf16 v[24:27], v[100:103], v[208:215], v236\n\t" \
  "v_mfma_f32_16x16x32_bf16 v[168:171], v[152:155], v[152:155], v[168:171]\n\t" \
  "v_smfmac_f32_16x16x64_bf16 v[12:15], v[104:107], v[208:215], v236\n\t" \
  "v_cvt_pk_bf16_f32 v188, v188, v189\n\t" \
  "v_cvt_pk_bf16_f32 v189, v190, v191\n\t" \
  "v_mfma_f32_16x16x32_bf16 v[172:175], v[196:199], v[152:155], v[172:175]\n\t" \
  "v_smfmac_f32_16x16x64_bf16 v[0:3], v[108:111], v[208:215], v236\n\t" \
  "s_waitcnt vmcnt(4) lgkmcnt(0)\n\t" \
  "s_barrier\n\t" \
  "v_lshl_add_u32 v48, v233, 1, s63\n\t" \
  "s_lshl1_add_u32 m0, s39, 10016\n\t" \
  "v_add_u32 v49, 0x2000, v48\n\t" \
  "global_load_lds_dwordx4 v48, s[6:7]\n\t" \
  "s_add_u32 m0, m0, 66048\n\t" \
  "s_cmp_eq_u32 s39, 3584\n\t" \
  "s_cselect_b32 exec_hi, 0, -1\n\t" \
  "global_load_lds_dwordx4 v49, s[6:7]\n\t" \
  "s_mov_b64 exec, -1\n\t" \
  "v_smfmac_f32_16x16x64_bf16 v[40:43], v[112:115], v[216:223], v236\n\t" \
  "ds_write_b64 v192, v[180:181] offset:12288\n\t" \
  "ds_read_b64 v[200:201], v253 offset:32800\n\t" \
  "v_smfmac_f32_16x16x64_bf16 v[28:31], v[116:119], v[216:223], v236\n\t" \
  "ds_write_b64 v192, v[184:185] offset:16384\n\t" \
  "ds_read_b64 v[202:203], v253 offset:33200\n\t" \
  "v_cndmask_b32 v168, v168, v169, s[40:41]\n\t" \
  "v_cndmask_b32 v170, v170, v171, s[40:41]\n\t" \
  "v_smfmac_f32_16x16x64_bf16 v[16:19], v[120:123], v[216:223], v236\n\t" \
  "ds_write_b64 v192, v[188:189] offset:20480\n\t" \
  "ds_read_b64 v[204:205], v253 offset:33600\n\t" \
  "v_cvt_pk_bf16_f32 v0, v0, v1\n\t" \
  "v_cvt_pk_bf16_f32 v1, v2, v3\n\t" \
  "v_cndmask_b32 v173, v168, v170, s[42:43]\n\t" \
  "v_mov_b32 v174, 0x1127c5ac\n\t" \
  "ds_read_b64_tr_b16 v[156:157], v195 offset:0\n\t" \
  "v_smfmac_f32_16x16x64_bf16 v[4:7], v[124:127], v[216:223], v236\n\t" \
  "ds_read_b64 v[206:207], v253 offset:34000\n\t" \
  "ds_read_b64 v[208:209], v253 offset:32808\n\t" \
  "s_not_b64 vcc, s[52:53]\n\t" \
  "ds_read_b64_tr_b16 v[158:159], v195 offset:4096\n\t" \
  "v_smfmac_f32_16x16x64_bf16 v[44:47], v[128:131], v[224:231], v236\n\t" \
  "ds_read_b64 v[210:211], v253 offset:33208\n\t" \
  "ds_read_b64 v[212:213], v253 offset:33608\n\t" \
  "ds_read_b64_tr_b16 v[160:161], v195 offset:2048\n\t" \
  "v_smfmac_f32_16x16x64_bf16 v[32:35], v[132:135], v[224:231], v236\n\t" \
  "ds_read_b64 v[214:215], v253 offset:34008\n\t" \
  "ds_read_b64 v[216:217], v253 offset:32816\n\t" \
  "ds_read_b64_tr_b16 v[162:163], v195 offset:6144\n\t" \
  "v_smfmac_f32_16x16x64_bf16 v[20:23], v[136:139], v[224:231], v236\n\t" \
  "ds_read_b64 v[218:219], v253 offset:33216\n\t" \
  "v_cvt_pk_bf16_f32 v4, v4, v5\n\t" \
  "v_cvt_pk_bf16_f32 v5, v6, v7\n\t" \
  "ds_read_b64_tr_b16 v[164:165], v195 offset:8192\n\t" \
  "v_smfmac_f32_16x16x64_bf16 v[8:11], v[140:143], v[224:231], v236\n\t" \
  "ds_read_b64 v[220:221], v253 offset:33616\n\t" \
  "ds_read_b64_tr_b16 v[166:167], v195 offset:10240\n\t" \
  "s_waitcnt lgkmcnt(12)\n\t" \
  "v_smfmac_f32_16x16x64_bf16 v[36:39], v[100:103], v[200:207], v236\n\t" \
  "ds_read_b64 v[222:223], v253 offset:34016\n\t" \
  "v_add_u32 v195, -93792, v195\n\t" \
  "v_smfmac_f32_16x16x64_bf16 v[24:27], v[104:107], v[200:207], v236\n\t" \
  "v_smfmac_f32_16x16x64_bf16 v[12:15], v[108:111], v[200:207], v236\n\t" \
  "v_cvt_pk_bf16_f32 v8, v8, v9\n\t" \
  "v_cvt_pk_bf16_f32 v9, v10, v11\n\t" \
  "s_waitcnt vmcnt(2) lgkmcnt(0)\n\t" \
  "s_barrier\n\t" \
  "s_cmp_ge_u32 s36, 2\n\t" \
  "s_cbranch_scc1 .Lcnx_c2\n\t" \
  "s_mov_b32 m0, s46\n\t" \
  "s_nop 0\n\t" \
  "buffer_load_dwordx4 v252, s[16:19], s33 offen lds\n\t" \
  "buffer_load_dwordx4 v252, s[16:19], s34 offen offset:1024 lds\n\t" \
  "buffer_load_dwordx4 v252, s[16:19], s35 offen offset:2048 lds\n\t" \
  "s_add_u32 s33, s33, s32\n\t" \
  "s_add_u32 s34, s34, s32\n\t" \
  "s_add_u32 s35, s35, s32\n\t" \
  "s_mov_b32 m0, s47\n\t" \
  "s_nop 0\n\t" \
  "buffer_load_dwordx4 v252, s[16:19], s33 offen lds\n\t" \
  "buffer_load_dwordx4 v252, s[16:19], s34 offen offset:1024 lds\n\t" \
  "buffer_load_dwordx4 v252, s[16:19], s35 offen offset:2048 lds\n\t" \
  "s_add_u32 s33, s33, s32\n\t" \
  "s_add_u32 s34, s34, s32\n\t" \
  "s_add_u32 s35, s35, s32\n\t" \
  ".Lcnx_c2:\n\t" \
  "v_smfmac_f32_16x16x64_bf16 v[40:43], v[116:119], v[208:215], v236\n\t" \
  "ds_write_b64 v192, v[0:1] offset:24576\n\t" \
  "ds_read_b64 v[224:225], v232 offset:0\n\t" \
  "ds_read_b128 v[180:183], v194 offset:0\n\t" \
  "ds_read_b128 v[184:187], v194 offset:4096\n\t" \
  "ds_read_b128 v[188:191], v194 offset:8192\n\t" \
  "v_smfmac_f32_16x16x64_bf16 v[28:31], v[120:123], v[208:215], v236\n\t" \
  "ds_write_b64 v192, v[4:5] offset:28672\n\t" \
  "ds_read_b64 v[226:227], v232 offset:400\n\t" \
  "ds_read_b64 v[228:229], v232 offset:800\n\t" \
  "v_smfmac_f32_16x16x64_bf16 v[16:19], v[124:127], v[208:215], v236\n\t" \
  "ds_write_b64 v192, v[8:9] offset:32768\n\t" \
  "ds_read_b64 v[230:231], v232 offset:1200\n\t" \
  "ds_read_b64 v[200:201], v232 offset:8\n\t" \
  "v_cvt_pk_bf16_f32 v12, v12, v13\n\t" \
  "v_cvt_pk_bf16_f32 v13, v14, v15\n\t" \
  "v_smfmac_f32_16x16x64_bf16 v[44:47], v[132:135], v[216:223], v236\n\t" \
  "ds_write_b64 v192, v[12:13] offset:36864\n\t" \
  "ds_read_b64 v[202:203], v232 offset:408\n\t" \
  "ds_read_b64 v[204:205], v232 offset:808\n\t" \
  "s_waitcnt lgkmcnt(11)\n\t" \
  "v_mfma_f32_16x16x32_bf16 v[176:179], v[180:183], v[180:183], 0\n\t" \
  "ds_read_b64 v[206:207], v232 offset:1208\n\t" \
  "v_mfma_f32_16x16x32_bf16 v[168:171], v[196:199], v[180:183], 0\n\t" \
  "ds_read_b64 v[208:209], v232 offset:16\n\t" \
  "v_smfmac_f32_16x16x64_bf16 v[32:35], v[136:139], v[216:223], v236\n\t" \
  "ds_read_b64 v[210:211], v232 offset:416\n\t" \
  "ds_read_b64 v[212:213], v232 offset:816\n\t" \
  "s_waitcnt lgkmcnt(14)\n\t" \
  "v_mfma_f32_16x16x32_bf16 v[176:179], v[184:187], v[184:187], v[176:179]\n\t" \
  "ds_read_b64 v[214:215], v232 offset:1216\n\t" \
  "v_mfma_f32_16x16x32_bf16 v[168:171], v[196:199], v[184:187], v[168:171]\n\t" \
  "v_smfmac_f32_16x16x64_bf16 v[20:23], v[140:143], v[216:223], v236\n\t" \
  "v_cvt_pk_bf16_f32 v16, v16, v17\n\t" \
  "v_cvt_pk_bf16_f32 v17, v18, v19\n\t" \
  "s_waitcnt lgkmcnt(14)\n\t" \
  "v_mfma_f32_16x16x32_bf16 v[176:179], v[188:191], v[188:191], v[176:179]\n\t" \
  "ds_write_b64 v192, v[16:17] offset:40960\n\t" \
  "v_mfma_f32_16x16x32_bf16 v[168:171], v[196:199], v[188:191], v[168:171]\n\t" \
  "s_waitcnt lgkmcnt(10)\n\t" \
  "v_smfmac_f32_16x16x64_bf16 v[36:39], v[104:107], v[224:231], v236\n\t" \
  "v_mov_b32 v183, 0\n\t" \
  "v_mov_b32 v187, 0\n\t" \
  "s_nop 1\n\t" \
  "v_cndmask_b32 v176, v176, v177, s[40:41]\n\t" \
  "v_cndmask_b32 v178, v178, v179, s[40:41]\n\t" \
  "v_cndmask_b32 v176, v176, v178, s[42:43]\n\t" \
  "v_smfmac_f32_16x16x64_bf16 v[24:27], v[108:111], v[224:231], v236\n\t" \
  "v_cndmask_b32_dpp v172, v168, v172, vcc row_ror:8 row_mask:0xf bank_mask:0xf\n\t" \
  "v_mul_f32 v172, 0x3daaaaab, v172\n\t" \
  "v_cndmask_b32_dpp v173, v176, v173, vcc row_ror:8 row_mask:0xf bank_mask:0xf\n\t" \
  "v_fmamk_f32 v173, v173, 0x3e2aaaab, v174\n\t" \
  "s_waitcnt vmcnt(0) lgkmcnt(0)\n\t" \
  "s_barrier\n\t" \
  "ds_read_b64 v[216:217], v232 offset:16400\n\t" \
  "ds_read_b64 v[218:219], v232 offset:16800\n\t" \
  "ds_read_b64 v[220:221], v232 offset:17200\n\t" \
  "ds_read_b64 v[222:223], v232 offset:17600\n\t" \
  "v_smfmac_f32_16x16x64_bf16 v[40:43], v[120:123], v[200:207], v236\n\t" \
  "ds_read_b64 v[224:225], v232 offset:16408\n\t" \
  "ds_read_b64 v[226:227], v232 offset:16808\n\t" \
  "v_cvt_pk_bf16_f32 v20, v20, v21\n\t" \
  "v_cvt_pk_bf16_f32 v21, v22, v23\n\t" \
  "v_fma_f32 v173, -v172, v172, v173\n\t" \
  "v_max_f32 v173, v174, v173\n\t" \
  "v_rsq_f32 v173, v173 div:2\n\t" \
  "s_mov_b64 exec, s[52:53]\n\tv_mul_f32 v173, 0x08805555, v173\n\ts_mov_b64 exec, -1\n\t" \
  "v_smfmac_f32_16x16x64_bf16 v[28:31], v[124:127], v[200:207], v236\n\t" \
  "ds_write_b64 v192, v[20:21] offset:45056\n\t" \
  "ds_read_b64 v[228:229], v232 offset:17208\n\t" \
  "s_mov_b64 vcc, s[40:41]\n\t" \
  "v_cvt_pk_bf16_f32 v175, v173, v173\n\t" \
  "v_and_b32 v173, 0xffff0000, v175\n\t" \
  "v_mul_f32 v174, -v172, v173 div:2\n\t" \
  "v_cndmask_b32_sdwa v175, v175, v173, vcc dst_sel:DWORD dst_unused:UNUSED_PAD src0_sel:WORD_1 src1_sel:DWORD\n\t" \
  "v_smfmac_f32_16x16x64_bf16 v[44:47], v[136:139], v[208:215], v236\n\t" \
  "ds_read_b64 v[230:231], v232 offset:17608\n\t" \
  "ds_read_b64 v[200:201], v232 offset:16416\n\t" \
  "v_cvt_pk_bf16_f32 v24, v24, v25\n\t" \
  "v_cvt_pk_bf16_f32 v25, v26, v27\n\t" \
  "v_and_b32 v172, 0xffff0000, v174\n\t" \
  "s_or_b64 vcc, s[52:53], s[42:43]\n\t" \
  "v_sub_f32 v174, v174, v172\n\t" \
  "v_cndmask_b32 v180, v175, v240, vcc\n\t" \
  "v_smfmac_f32_16x16x64_bf16 v[32:35], v[140:143], v[208:215], v236\n\t" \
  "ds_write_b64 v192, v[24:25] offset:56880\n\t" \
  "ds_read_b64 v[202:203], v232 offset:16816\n\t" \
  "v_perm_b32 v174, v172, v174, s55\n\t" \
  "v_cndmask_b32_dpp v184, v175, v240, vcc row_ror:8 row_mask:0xf bank_mask:0xf\n\t" \
  "s_mov_b64 vcc, s[52:53]\n\t" \
  "v_cndmask_b32 v182, v174, v240, vcc\n\t" \
  "ds_read_b64_tr_b16 v[144:145], v195 offset:0\n\t" \
  "ds_read_b64_tr_b16 v[146:147], v195 offset:4096\n\t" \
  "s_waitcnt lgkmcnt(10)\n\t" \
  "v_smfmac_f32_16x16x64_bf16 v[36:39], v[108:111], v[216:223], v236\n\t" \
  "ds_read_b64 v[204:205], v232 offset:17216\n\t" \
  "ds_read_b64 v[206:207], v232 offset:17616\n\t" \
  "v_cvt_pk_bf16_f32 v28, v28, v29\n\t" \
  "v_cvt_pk_bf16_f32 v29, v30, v31\n\t" \
  "v_cndmask_b32_dpp v186, v174, v240, vcc row_ror:8 row_mask:0xf bank_mask:0xf\n\t" \
  "s_orn2_b64 vcc, s[52:53], s[42:43]\n\t" \
  "v_cndmask_b32 v181, v175, v240, vcc\n\t" \
  "v_cndmask_b32_dpp v185, v175, v240, vcc row_ror:8 row_mask:0xf bank_mask:0xf\n\t" \
  "ds_read_b64_tr_b16 v[148:149], v195 offset:2048\n\t" \
  "ds_read_b64_tr_b16 v[150:151], v195 offset:6144\n\t" \
  "s_waitcnt lgkmcnt(9)\n\t" \
  "v_smfmac_f32_16x16x64_bf16 v[40:43], v[124:127], v[224:231], v236\n\t" \
  "ds_write_b64 v192, v[28:29] offset:60976\n\t" \
  "ds_read_b64_tr_b16 v[152:153], v195 offset:8192\n\t" \
  "ds_read_b64_tr_b16 v[154:155], v195 offset:10240\n\t" \
  "v_mfma_f32_16x16x16_bf16 v[0:3], v[196:197], v[182:183], 0\n\t" \
  "v_mfma_f32_16x16x16_bf16 v[4:7], v[196:197], v[186:187], 0\n\t" \
  "s_waitcnt lgkmcnt(5)\n\t" \
  "v_smfmac_f32_16x16x64_bf16 v[44:47], v[140:143], v[200:207], v236\n\t" \
  "v_cvt_pk_bf16_f32 v32, v32, v33\n\t" \
  "v_cvt_pk_bf16_f32 v33, v34, v35\n\t" \
  "v_mfma_f32_16x16x16_bf16 v[188:191], v[156:157], v[180:181], v[0:3]\n\t" \
  "ds_write_b64 v192, v[32:33] offset:65072\n\t" \
  "v_mfma_f32_16x16x16_bf16 v[176:179], v[158:159], v[180:181], v[0:3]\n\t" \
  "v_cvt_pk_bf16_f32 v36, v36, v37\n\t" \
  "v_cvt_pk_bf16_f32 v37, v38, v39\n\t" \
  "ds_write_b64 v193, v[36:37] offset:0\n\t" \
  "v_cvt_pk_bf16_f32 v40, v40, v41\n\t" \
  "v_cvt_pk_bf16_f32 v41, v42, v43\n\t" \
  "ds_write_b64 v193, v[40:41] offset:4096\n\t" \
  "v_mfma_f32_16x16x16_bf16 v[168:171], v[160:161], v[180:181], v[0:3]\n\t" \
  "v_cvt_pk_bf16_f32 v96, v188, v189\n\t" \
  "v_cvt_pk_bf16_f32 v97, v190, v191\n\t" \
  "v_mfma_f32_16x16x16_bf16 v[172:175], v[162:163], v[180:181], v[0:3]\n\t" \
  "v_cvt_pk_bf16_f32 v44, v44, v45\n\t" \
  "v_cvt_pk_bf16_f32 v45, v46, v47\n\t" \
  "ds_write_b64 v193, v[44:45] offset:8192\n\t" \
  "v_cvt_pk_bf16_f32 v98, v176, v177\n\t" \
  "v_cvt_pk_bf16_f32 v99, v178, v179\n\t" \
  "v_mfma_f32_16x16x16_bf16 v[188:191], v[164:165], v[180:181], v[0:3]\n\t" \
  "v_cvt_pk_bf16_f32 v112, v168, v169\n\t" \
  "v_cvt_pk_bf16_f32 v113, v170, v171\n\t" \
  "v_mfma_f32_16x16x16_bf16 v[176:179], v[166:167], v[180:181], v[0:3]\n\t" \
  "v_cvt_pk_bf16_f32 v114, v172, v173\n\t" \
  "v_cvt_pk_bf16_f32 v115, v174, v175\n\t" \
  "s_nop 2\n\t" \
  "v_cvt_pk_bf16_f32 v128, v188, v189\n\t" \
  "v_cvt_pk_bf16_f32 v129, v190, v191\n\t" \
  "s_nop 0\n\t" \
  "v_cvt_pk_bf16_f32 v130, v176, v177\n\t" \
  "v_cvt_pk_bf16_f32 v131, v178, v179\n\t" \
  "s_waitcnt vmcnt(0) lgkmcnt(0)\n\t" \
  "s_barrier\n\t" \
  "ds_read_b64_tr_b16 v[208:209], v248 offset:43048\n\t" \
  "ds_read_b64_tr_b16 v[210:211], v248 offset:44072\n\t" \
  "ds_read_b128 v[104:107], v194 offset:24576\n\t" \
  "ds_read_b128 v[120:123], v194 offset:28672\n\t" \
  "ds_read_b128 v[136:139], v194 offset:32768\n\t" \
  "v_add_u32 v194, s54, v194\n\t" \
  "ds_read_b128 v[108:111], v194 offset:49152\n\t" \
  "ds_read_b128 v[124:127], v194 offset:53248\n\t" \
  "ds_read_b128 v[140:143], v194 offset:57344\n\t" \
  "v_mfma_f32_16x16x16_bf16 v[168:171], v[144:145], v[184:185], v[4:7]\n\t" \
  "ds_read_b64_tr_b16 v[212:213], v248 offset:46120\n\t" \
  "ds_read_b64_tr_b16 v[214:215], v248 offset:47144\n\t" \
  "v_mfma_f32_16x16x16_bf16 v[172:175], v[146:147], v[184:185], v[4:7]\n\t" \
  "ds_read_b64_tr_b16 v[216:217], v248 offset:45096\n\t" \
  "ds_read_b64_tr_b16 v[218:219], v248 offset:48168\n\t" \
  "s_cmp_eq_u32 s37, 0\n\t" \
  "s_cbranch_scc1 .Lcnx_f0_0\n.Lcnx_g0_0:\n\t" \
  "v_mfma_f32_16x16x16_bf16 v[188:191], v[148:149], v[184:185], v[4:7]\n\t" \
  "ds_read_b64_tr_b16 v[160:161], v248 offset:43560\n\t" \
  "ds_read_b64_tr_b16 v[162:163], v248 offset:44584\n\t" \
  "v_cvt_pk_bf16_f32 v100, v168, v169\n\t" \
  "v_cvt_pk_bf16_f32 v101, v170, v171\n\t" \
  "v_mfma_f32_16x16x16_bf16 v[176:179], v[150:151], v[184:185], v[4:7]\n\t" \
  "ds_read_b64_tr_b16 v[164:165], v248 offset:46632\n\t" \
  "ds_read_b64_tr_b16 v[166:167], v248 offset:47656\n\t" \
  "s_cmp_eq_u32 s37, 0\n\t" \
  "s_cbranch_scc1 .Lcnx_f0_1\n.Lcnx_g0_1:\n\t" \
  "v_cvt_pk_bf16_f32 v102, v172, v173\n\t" \
  "v_cvt_pk_bf16_f32 v103, v174, v175\n\t" \
  "v_mfma_f32_16x16x16_bf16 v[168:171], v[152:153], v[184:185], v[4:7]\n\t" \
  "buffer_load_dwordx4 v[236:239], v250, s[8:11], s28 offen offset:0 nt\n\t" \
  "ds_read_b64_tr_b16 v[232:233], v248 offset:45608\n\t" \
  "ds_read_b64_tr_b16 v[234:235], v248 offset:48680\n\t" \
  "v_cvt_pk_bf16_f32 v116, v188, v189\n\t" \
  "v_cvt_pk_bf16_f32 v117, v190, v191\n\t" \
  "v_mfma_f32_16x16x16_bf16 v[172:175], v[154:155], v[184:185], v[4:7]\n\t" \
  "ds_read_b64_tr_b16 v[24:25], v195 offset:24576\n\t" \
  "ds_read_b64_tr_b16 v[26:27], v195 offset:28672\n\t" \
  "v_cvt_pk_bf16_f32 v118, v176, v177\n\t" \
  "v_cvt_pk_bf16_f32 v119, v178, v179\n\t" \
  "s_waitcnt lgkmcnt(15)\n\t" \
  "v_mfma_f32_16x16x32_bf16 v[144:147], v[208:211], v[96:99], 0\n\t" \
  "ds_read_b64_tr_b16 v[28:29], v195 offset:26624\n\t" \
  "ds_read_b64_tr_b16 v[30:31], v195 offset:30720\n\t" \
  "v_cvt_pk_bf16_f32 v132, v168, v169\n\t" \
  "v_cvt_pk_bf16_f32 v133, v170, v171\n\t" \
  "s_waitcnt lgkmcnt(12)\n\t" \
  "v_mfma_f32_16x16x32_bf16 v[144:147], v[212:215], v[112:115], v[144:147]\n\t" \
  "ds_read_b64_tr_b16 v[32:33], v195 offset:32768\n\t" \
  "ds_read_b64_tr_b16 v[34:35], v195 offset:34816\n\t" \
  "v_cvt_pk_bf16_f32 v134, v172, v173\n\t" \
  "v_cvt_pk_bf16_f32 v135, v174, v175\n\t" \
  "v_mfma_f32_16x16x32_bf16 v[152:155], v[208:211], v[100:103], 0\n\t" \
  "v_add_u32 v195, s54, v195\n\t" \
  "ds_read_b64_tr_b16 v[36:37], v195 offset:49152\n\t" \
  "ds_read_b64_tr_b16 v[38:39], v195 offset:53248\n\t" \
  "s_waitcnt lgkmcnt(14)\n\t" \
  "v_mfma_f32_16x16x32_bf16 v[144:147], v[216:219], v[128:131], v[144:147]\n\t" \
  "ds_read_b64_tr_b16 v[40:41], v195 offset:51200\n\t" \
  "v_mfma_f32_16x16x32_bf16 v[152:155], v[212:215], v[116:119], v[152:155]\n\t" \
  "ds_read_b64_tr_b16 v[42:43], v195 offset:55296\n\t" \
  "v_mfma_f32_16x16x32_bf16 v[152:155], v[216:219], v[132:135], v[152:155]\n\t" \
  "ds_read_b64_tr_b16 v[44:45], v195 offset:57344\n\t" \
  "v_mfma_f32_16x16x32_bf16 v[8:11], v[104:107], v[104:107], 0\n\t" \
  "ds_read_b64_tr_b16 v[46:47], v195 offset:59392\n\t" \
  "v_mfma_f32_16x16x32_bf16 v[12:15], v[196:199], v[104:107], 0\n\t" \
  "v_cvt_pknorm_i16_f32 v220, |v146|, |v147|\n\t" \
  "v_cvt_pknorm_i16_f32 v222, |v144|, |v145|\n\t" \
  "v_mfma_f32_16x16x32_bf16 v[8:11], v[120:123], v[120:123], v[8:11]\n\t" \
  "v_lshlrev_b32_sdwa v221, 2, v220 dst_sel:DWORD dst_unused:UNUSED_PAD src0_sel:DWORD src1_sel:BYTE_3\n\t" \
  "v_lshlrev_b32_sdwa v220, 2, v220 dst_sel:DWORD dst_unused:UNUSED_PAD src0_sel:DWORD src1_sel:BYTE_1\n\t" \
  "v_mfma_f32_16x16x32_bf16 v[12:15], v[196:199], v[120:123], v[12:15]\n\t" \
  "ds_read_b32 v221, v221 offset:10016\n\t" \
  "v_lshlrev_b32_sdwa v223, 2, v222 dst_sel:DWORD dst_unused:UNUSED_PAD src0_sel:DWORD src1_sel:BYTE_3\n\t" \
  "v_lshlrev_b32_sdwa v222, 2, v222 dst_sel:DWORD dst_unused:UNUSED_PAD src0_sel:DWORD src1_sel:BYTE_1\n\t" \
  "v_mfma_f32_16x16x32_bf16 v[8:11], v[136:139], v[136:139], v[8:11]\n\t" \
  "ds_read_b32 v220, v220 offset:10016\n\t" \
  "v_lshlrev_b32_sdwa v225, 1, v155 dst_sel:DWORD dst_unused:UNUSED_PAD src0_sel:DWORD src1_sel:WORD_1\n\t" \
  "v_lshlrev_b32_sdwa v224, 1, v154 dst_sel:DWORD dst_unused:UNUSED_PAD src0_sel:DWORD src1_sel:WORD_1\n\t" \
  "v_mfma_f32_16x16x32_bf16 v[12:15], v[196:199], v[136:139], v[12:15]\n\t" \
  "ds_read_b32 v223, v223 offset:10016\n\t" \
  "v_lshlrev_b32_sdwa v227, 1, v153 dst_sel:DWORD dst_unused:UNUSED_PAD src0_sel:DWORD src1_sel:WORD_1\n\t" \
  "v_lshlrev_b32_sdwa v226, 1, v152 dst_sel:DWORD dst_unused:UNUSED_PAD src0_sel:DWORD src1_sel:WORD_1\n\t" \
  "v_mfma_f32_16x16x32_bf16 v[16:19], v[108:111], v[108:111], 0\n\t" \
  "ds_read_b32 v222, v222 offset:10016\n\t" \
  "v_mfma_f32_16x16x32_bf16 v[20:23], v[196:199], v[108:111], 0\n\t" \
  "ds_read_u16_d16_hi v225, v225 offset:10528\n\t" \
  "v_cndmask_b32 v8, v8, v9, s[40:41]\n\t" \
  "v_cndmask_b32 v10, v10, v11, s[40:41]\n\t" \
  "v_mfma_f32_16x16x32_bf16 v[16:19], v[124:127], v[124:127], v[16:19]\n\t" \
  "ds_read_u16 v224, v224 offset:10528\n\t" \
  "v_cndmask_b32 v13, v8, v10, s[42:43]\n\t" \
  "v_mov_b32 v14, 0x1127c5ac\n\t" \
  "v_mfma_f32_16x16x32_bf16 v[20:23], v[196:199], v[124:127], v[20:23]\n\t" \
  "ds_read_u16_d16_hi v227, v227 offset:10528\n\t" \
  "s_not_b64 vcc, s[52:53]\n\t" \
  "v_mfma_f32_16x16x32_bf16 v[16:19], v[140:143], v[140:143], v[16:19]\n\t" \
  "ds_read_u16 v226, v226 offset:10528\n\t" \
  "s_waitcnt lgkmcnt(4)\n\t" \
  "v_fma_mix_f32 v221, |v147|, v221, v221 op_sel:[0,1,0] op_sel_hi:[0,1,1] clamp\n\t" \
  "v_fma_mix_f32 v220, |v146|, v220, v220 op_sel:[0,1,0] op_sel_hi:[0,1,1] clamp\n\t" \
  "v_mfma_f32_16x16x32_bf16 v[20:23], v[196:199], v[140:143], v[20:23]\n\t" \
  "v_fma_mix_f32 v223, |v145|, v223, v223 op_sel:[0,1,0] op_sel_hi:[0,1,1] clamp\n\t" \
  "v_fma_mix_f32 v222, |v144|, v222, v222 op_sel:[0,1,0] op_sel_hi:[0,1,1] clamp\n\t" \
  "v_mfma_f32_16x16x32_bf16 v[148:151], v[160:163], v[96:99], 0\n\t" \
  "s_waitcnt lgkmcnt(0)\n\t" \
  "v_or_b32 v155, v224, v225\n\t" \
  "v_or_b32 v154, v226, v227\n\t" \
  "v_mfma_f32_16x16x32_bf16 v[156:159], v[160:163], v[100:103], 0\n\t" \
  "v_cndmask_b32 v16, v16, v17, s[40:41]\n\t" \
  "v_cndmask_b32 v18, v18, v19, s[40:41]\n\t" \
  "v_cndmask_b32 v16, v16, v18, s[42:43]\n\t" \
  "v_cndmask_b32_dpp v12, v20, v12, vcc row_ror:8 row_mask:0xf bank_mask:0xf\n\t" \
  "v_mul_f32 v12, 0x3daaaaab, v12\n\t" \
  "v_mfma_f32_16x16x32_bf16 v[148:151], v[164:167], v[112:115], v[148:151]\n\t" \
  "v_cndmask_b32_dpp v13, v16, v13, vcc row_ror:8 row_mask:0xf bank_mask:0xf\n\t" \
  "v_fmamk_f32 v13, v13, 0x3e2aaaab, v14\n\t" \
  "v_fma_f32 v13, -v12, v12, v13\n\t" \
  "v_max_f32 v13, v14, v13\n\t" \
  "v_rsq_f32 v13, v13 div:2\n\t" \
  "v_mfma_f32_16x16x32_bf16 v[156:159], v[164:167], v[116:119], v[156:159]\n\t" \
  "s_mov_b64 exec, s[52:53]\n\tv_mul_f32 v13, 0x08805555, v13\n\ts_mov_b64 exec, -1\n\t" \
  "s_mov_b64 vcc, s[40:41]\n\t" \
  "v_cvt_pk_bf16_f32 v15, v13, v13\n\t" \
  "v_and_b32 v13, 0xffff0000, v15\n\t" \
  "v_mul_f32 v14, -v12, v13 div:2\n\t" \
  "v_cndmask_b32_sdwa v15, v15, v13, vcc dst_sel:DWORD dst_unused:UNUSED_PAD src0_sel:WORD_1 src1_sel:DWORD\n\t" \
  "v_and_b32 v12, 0xffff0000, v14\n\t" \
  "v_mfma_f32_16x16x32_bf16 v[148:151], v[232:235], v[128:131], v[148:151]\n\t" \
  "s_or_b64 vcc, s[52:53], s[42:43]\n\t" \
  "v_sub_f32 v14, v14, v12\n\t" \
  "v_cndmask_b32 v180, v15, v240, vcc\n\t" \
  "v_perm_b32 v14, v12, v14, s55\n\t" \
  "v_cndmask_b32_dpp v184, v15, v240, vcc row_ror:8 row_mask:0xf bank_mask:0xf\n\t" \
  "v_mfma_f32_16x16x32_bf16 v[156:159], v[232:235], v[132:135], v[156:159]\n\t" \
  "s_mov_b64 vcc, s[52:53]\n\t" \
  "v_cndmask_b32 v182, v14, v240, vcc\n\t" \
  "v_cndmask_b32_dpp v186, v14, v240, vcc row_ror:8 row_mask:0xf bank_mask:0xf\n\t" \
  "s_orn2_b64 vcc, s[52:53], s[42:43]\n\t" \
  "v_cndmask_b32 v181, v15, v240, vcc\n\t" \
  "v_mfma_f32_16x16x16_bf16 v[0:3], v[196:197], v[182:183], 0\n\t" \
  "v_cndmask_b32_dpp v185, v15, v240, vcc row_ror:8 row_mask:0xf bank_mask:0xf\n\t" \
  "s_mov_b64 vcc, 0\n\t" \
  "v_fma_f32 v147, |v147|, v221, v147 mul:4\n\t" \
  "v_mfma_f32_16x16x16_bf16 v[4:7], v[196:197], v[186:187], 0\n\t" \
  "v_fma_f32 v146, |v146|, v220, v146 mul:4\n\t" \
  "v_fma_f32 v145, |v145|, v223, v145 mul:4\n\t" \
  "v_mfma_f32_16x16x16_bf16 v[188:191], v[24:25], v[180:181], v[0:3]\n\t" \
  "v_fma_f32 v144, |v144|, v222, v144 mul:4\n\t" \
  "v_cvt_pk_bf16_f32 v147, v146, v147\n\t" \
  "v_mfma_f32_16x16x16_bf16 v[176:179], v[26:27], v[180:181], v[0:3]\n\t" \
  "v_cvt_pk_bf16_f32 v146, v144, v145\n\t" \
  "v_cvt_pknorm_i16_f32 v228, |v148|, |v149|\n\t" \
  "v_mfma_f32_16x16x16_bf16 v[200:203], v[36:37], v[184:185], v[4:7]\n\t" \
  "s_nop 1\n\t" \
  "v_cvt_pk_bf16_f32 v104, v188, v189\n\t" \
  "v_cvt_pk_bf16_f32 v105, v190, v191\n\t" \
  "v_mfma_f32_16x16x16_bf16 v[204:207], v[38:39], v[184:185], v[4:7]\n\t" \
  "v_cvt_pk_bf16_f32 v106, v176, v177\n\t" \
  "v_cvt_pk_bf16_f32 v107, v178, v179\n\t" \
  "v_mfma_f32_16x16x16_bf16 v[196:199], v[28:29], v[180:181], v[0:3]\n\t" \
  "v_cvt_pk_bf16_f32 v108, v200, v201\n\t" \
  "v_cvt_pk_bf16_f32 v109, v202, v203\n\t" \
  "v_mfma_f32_16x16x16_bf16 v[188:191], v[30:31], v[180:181], v[0:3]\n\t" \
  "s_nop 1\n\t" \
  "v_cvt_pk_bf16_f32 v110, v204, v205\n\t" \
  "v_cvt_pk_bf16_f32 v111, v206, v207\n\t" \
  "v_mfma_f32_16x16x32_bf16 v[160:163], v[208:211], v[104:107], 0\n\t" \
  "v_cvt_pk_bf16_f32 v120, v196, v197\n\t" \
  "v_cvt_pk_bf16_f32 v121, v198, v199\n\t" \
  "v_mfma_f32_16x16x16_bf16 v[176:179], v[40:41], v[184:185], v[4:7]\n\t" \
  "v_cvt_pk_bf16_f32 v122, v188, v189\n\t" \
  "v_cvt_pk_bf16_f32 v123, v190, v191\n\t" \
  "v_mfma_f32_16x16x16_bf16 v[200:203], v[42:43], v[184:185], v[4:7]\n\t" \
  "v_cvt_pknorm_i16_f32 v230, |v150|, |v151|\n\t" \
  "v_lshlrev_b32_sdwa v229, 2, v228 dst_sel:DWORD dst_unused:UNUSED_PAD src0_sel:DWORD src1_sel:BYTE_3\n\t" \
  "v_mfma_f32_16x16x32_bf16 v[168:171], v[208:211], v[108:111], 0\n\t" \
  "ds_read_b64_tr_b16 v[208:209], v248 offset:43560\n\t" \
  "s_nop 0\n\t" \
  "v_cvt_pk_bf16_f32 v124, v176, v177\n\t" \
  "v_cvt_pk_bf16_f32 v125, v178, v179\n\t" \
  "v_mfma_f32_16x16x16_bf16 v[204:207], v[32:33], v[180:181], v[0:3]\n\t" \
  "ds_read_b64_tr_b16 v[210:211], v248 offset:44584\n\t" \
  "v_cvt_pk_bf16_f32 v126, v200, v201\n\t" \
  "v_cvt_pk_bf16_f32 v127, v202, v203\n\t" \
  "v_mfma_f32_16x16x16_bf16 v[196:199], v[34:35], v[180:181], v[0:3]\n\t" \
  "v_lshlrev_b32_sdwa v228, 2, v228 dst_sel:DWORD dst_unused:UNUSED_PAD src0_sel:DWORD src1_sel:BYTE_1\n\t" \
  "v_lshlrev_b32_sdwa v231, 2, v230 dst_sel:DWORD dst_unused:UNUSED_PAD src0_sel:DWORD src1_sel:BYTE_3\n\t" \
  "v_mfma_f32_16x16x32_bf16 v[160:163], v[212:215], v[120:123], v[160:163]\n\t" \
  "ds_read_b32 v229, v229 offset:10016\n\t" \
  "v_cvt_pk_bf16_f32 v136, v204, v205\n\t" \
  "v_cvt_pk_bf16_f32 v137, v206, v207\n\t" \
  "v_mfma_f32_16x16x16_bf16 v[188:191], v[44:45], v[184:185], v[4:7]\n\t" \
  "ds_read_b32 v228, v228 offset:10016\n\t" \
  "v_cvt_pk_bf16_f32 v138, v196, v197\n\t" \
  "v_cvt_pk_bf16_f32 v139, v198, v199\n\t" \
  "v_mfma_f32_16x16x16_bf16 v[176:179], v[46:47], v[184:185], v[4:7]\n\t" \
  "v_lshlrev_b32_sdwa v230, 2, v230 dst_sel:DWORD dst_unused:UNUSED_PAD src0_sel:DWORD src1_sel:BYTE_1\n\t" \
  "v_lshlrev_b32_sdwa v233, 1, v157 dst_sel:DWORD dst_unused:UNUSED_PAD src0_sel:DWORD src1_sel:WORD_1\n\t" \
  "v_mfma_f32_16x16x32_bf16 v[168:171], v[212:215], v[124:127], v[168:171]\n\t" \
  "ds_read_b64_tr_b16 v[212:213], v248 offset:46632\n\t" \
  "v_cvt_pk_bf16_f32 v140, v188, v189\n\t" \
  "v_cvt_pk_bf16_f32 v141, v190, v191\n\t" \
  "v_mfma_f32_16x16x32_bf16 v[160:163], v[216:219], v[136:139], v[160:163]\n\t" \
  "ds_read_b64_tr_b16 v[214:215], v248 offset:47656\n\t" \
  "v_cvt_pk_bf16_f32 v142, v176, v177\n\t" \
  "v_cvt_pk_bf16_f32 v143, v178, v179\n\t" \
  "s_nop 1\n\t" \
  "v_mfma_f32_16x16x32_bf16 v[168:171], v[216:219], v[140:143], v[168:171]\n\t" \
  "ds_read_b64_tr_b16 v[216:217], v248 offset:45608\n\t" \
  "v_lshlrev_b32_sdwa v232, 1, v156 dst_sel:DWORD dst_unused:UNUSED_PAD src0_sel:DWORD src1_sel:WORD_1\n\t" \
  "v_lshlrev_b32_sdwa v235, 1, v159 dst_sel:DWORD dst_unused:UNUSED_PAD src0_sel:DWORD src1_sel:WORD_1\n\t" \
  "s_waitcnt lgkmcnt(5)\n\t" \
  "ds_read_b64_tr_b16 v[218:219], v248 offset:48680\n\t" \
  "s_waitcnt vmcnt(1)\n\t" \
  "s_mov_b64 exec, 3\n\t" \
  "ds_add_rtn_u32 v246, v251, v250 offset:47136\n\t" \
  "s_mov_b64 exec, -1\n\t" \
  "v_mfma_f32_16x16x32_bf16 v[164:167], v[208:211], v[104:107], 0\n\t" \
  "ds_read_b32 v231, v231 offset:10016\n\t" \
  "v_lshlrev_b32_sdwa v234, 1, v158 dst_sel:DWORD dst_unused:UNUSED_PAD src0_sel:DWORD src1_sel:WORD_1\n\t" \
  "v_mfma_f32_16x16x32_bf16 v[172:175], v[208:211], v[108:111], 0\n\t" \
  "ds_read_b64_tr_b16 v[208:209], v248 offset:49192\n\t" \
  "ds_read_b64_tr_b16 v[210:211], v248 offset:50216\n\t" \
  "s_waitcnt lgkmcnt(6)\n\t" \
  "v_mfma_f32_16x16x32_bf16 v[164:167], v[212:215], v[120:123], v[164:167]\n\t" \
  "ds_read_b32 v230, v230 offset:10016\n\t" \
  "v_mfma_f32_16x16x32_bf16 v[172:175], v[212:215], v[124:127], v[172:175]\n\t" \
  "ds_read_b64_tr_b16 v[212:213], v248 offset:52264\n\t" \
  "ds_read_b64_tr_b16 v[214:215], v248 offset:53288\n\t" \
  "s_waitcnt lgkmcnt(7)\n\t" \
  "v_mfma_f32_16x16x32_bf16 v[164:167], v[216:219], v[136:139], v[164:167]\n\t" \
  "ds_read_u16_d16_hi v233, v233 offset:10528\n\t" \
  "v_mfma_f32_16x16x32_bf16 v[172:175], v[216:219], v[140:143], v[172:175]\n\t" \
  "ds_read_b64_tr_b16 v[216:217], v248 offset:51240\n\t" \
  "s_waitcnt lgkmcnt(5)\n\t" \
  "v_mfma_f32_16x16x32_bf16 v[176:179], v[208:211], v[96:99], 0\n\t" \
  "ds_read_b64_tr_b16 v[218:219], v248 offset:54312\n\t" \
  "v_cvt_pknorm_i16_f32 v220, |v162|, |v163|\n\t" \
  "s_setprio 3\n\t" \
  "v_cvt_pknorm_i16_f32 v222, |v160|, |v161|\n\t" \
  "v_lshlrev_b32_sdwa v221, 2, v220 dst_sel:DWORD dst_unused:UNUSED_PAD src0_sel:DWORD src1_sel:BYTE_3\n\t" \
  "v_mfma_f32_16x16x32_bf16 v[184:187], v[208:211], v[100:103], 0\n\t" \
  "ds_read_u16 v232, v232 offset:10528\n\t" \
  "v_lshlrev_b32_sdwa v220, 2, v220 dst_sel:DWORD dst_unused:UNUSED_PAD src0_sel:DWORD src1_sel:BYTE_1\n\t" \
  "v_lshlrev_b32_sdwa v223, 2, v222 dst_sel:DWORD dst_unused:UNUSED_PAD src0_sel:DWORD src1_sel:BYTE_3\n\t" \
  "v_lshlrev_b32_sdwa v222, 2, v222 dst_sel:DWORD dst_unused:UNUSED_PAD src0_sel:DWORD src1_sel:BYTE_1\n\t" \
  "v_mfma_f32_16x16x32_bf16 v[192:195], v[208:211], v[104:107], 0\n\t" \
  "ds_read_u16_d16_hi v235, v235 offset:10528\n\t" \
  "v_lshlrev_b32_sdwa v225, 1, v171 dst_sel:DWORD dst_unused:UNUSED_PAD src0_sel:DWORD src1_sel:WORD_1\n\t" \
  "v_lshlrev_b32_sdwa v224, 1, v170 dst_sel:DWORD dst_unused:UNUSED_PAD src0_sel:DWORD src1_sel:WORD_1\n\t" \
  "v_lshlrev_b32_sdwa v227, 1, v169 dst_sel:DWORD dst_unused:UNUSED_PAD src0_sel:DWORD src1_sel:WORD_1\n\t" \
  "v_mfma_f32_16x16x32_bf16 v[200:203], v[208:211], v[108:111], 0\n\t" \
  "ds_read_b64_tr_b16 v[208:209], v248 offset:49704\n\t" \
  "ds_read_u16 v234, v234 offset:10528\n\t" \
  "v_lshlrev_b32_sdwa v226, 1, v168 dst_sel:DWORD dst_unused:UNUSED_PAD src0_sel:DWORD src1_sel:WORD_1\n\t" \
  "s_waitcnt lgkmcnt(9)\n\t" \
  "v_fma_mix_f32 v229, |v149|, v229, v229 op_sel:[0,1,0] op_sel_hi:[0,1,1] clamp\n\t" \
  "v_fma_mix_f32 v228, |v148|, v228, v228 op_sel:[0,1,0] op_sel_hi:[0,1,1] clamp\n\t" \
  "s_waitcnt lgkmcnt(7)\n\t" \
  "v_mfma_f32_16x16x32_bf16 v[176:179], v[212:215], v[112:115], v[176:179]\n\t" \
  "ds_read_b64_tr_b16 v[210:211], v248 offset:50728\n\t" \
  "ds_read_b32 v221, v221 offset:10016\n\t" \
  "v_fma_mix_f32 v231, |v151|, v231, v231 op_sel:[0,1,0] op_sel_hi:[0,1,1] clamp\n\t" \
  "v_fma_mix_f32 v230, |v150|, v230, v230 op_sel:[0,1,0] op_sel_hi:[0,1,1] clamp\n\t" \
  "s_waitcnt lgkmcnt(2)\n\t" \
  "v_or_b32 v156, v232, v233\n\t" \
  "v_mfma_f32_16x16x32_bf16 v[184:187], v[212:215], v[116:119], v[184:187]\n\t" \
  "ds_read_b32 v220, v220 offset:10016\n\t" \
  "ds_read_b32 v223, v223 offset:10016\n\t" \
  "v_or_b32 v157, v234, v235\n\t" \
  "v_fma_f32 v149, |v149|, v229, v149 mul:4\n\t" \
  "v_fma_f32 v148, |v148|, v228, v148 mul:4\n\t" \
  "v_mfma_f32_16x16x32_bf16 v[192:195], v[212:215], v[120:123], v[192:195]\n\t" \
  "ds_read_b32 v222, v222 offset:10016\n\t" \
  "v_fma_f32 v151, |v151|, v231, v151 mul:4\n\t" \
  "s_setprio 2\n\t" \
  "v_fma_f32 v150, |v150|, v230, v150 mul:4\n\t" \
  "v_cvt_pk_bf16_f32 v148, v148, v149\n\t" \
  "v_mfma_f32_16x16x32_bf16 v[200:203], v[212:215], v[124:127], v[200:203]\n\t" \
  "ds_read_b64_tr_b16 v[212:213], v248 offset:52776\n\t" \
  "ds_read_u16_d16_hi v225, v225 offset:10528\n\t" \
  "v_cvt_pk_bf16_f32 v149, v150, v151\n\t" \
  "v_cvt_pknorm_i16_f32 v228, |v164|, |v165|\n\t" \
  "v_cvt_pknorm_i16_f32 v230, |v166|, |v167|\n\t" \
  "v_mfma_f32_16x16x32_bf16 v[176:179], v[216:219], v[128:131], v[176:179]\n\t" \
  "ds_read_b64_tr_b16 v[214:215], v248 offset:53800\n\t" \
  "v_lshlrev_b32_sdwa v229, 2, v228 dst_sel:DWORD dst_unused:UNUSED_PAD src0_sel:DWORD src1_sel:BYTE_3\n\t" \
  "v_lshlrev_b32_sdwa v228, 2, v228 dst_sel:DWORD dst_unused:UNUSED_PAD src0_sel:DWORD src1_sel:BYTE_1\n\t" \
  "v_lshlrev_b32_sdwa v231, 2, v230 dst_sel:DWORD dst_unused:UNUSED_PAD src0_sel:DWORD src1_sel:BYTE_3\n\t" \
  "v_mfma_f32_16x16x32_bf16 v[184:187], v[216:219], v[132:135], v[184:187]\n\t" \
  "ds_read_u16 v224, v224 offset:10528\n\t" \
  "v_lshlrev_b32_sdwa v230, 2, v230 dst_sel:DWORD dst_unused:UNUSED_PAD src0_sel:DWORD src1_sel:BYTE_1\n\t" \
  "v_lshlrev_b32_sdwa v233, 1, v173 dst_sel:DWORD dst_unused:UNUSED_PAD src0_sel:DWORD src1_sel:WORD_1\n\t" \
  "v_lshlrev_b32_sdwa v232, 1, v172 dst_sel:DWORD dst_unused:UNUSED_PAD src0_sel:DWORD src1_sel:WORD_1\n\t" \
  "v_mfma_f32_16x16x32_bf16 v[192:195], v[216:219], v[136:139], v[192:195]\n\t" \
  "ds_read_u16_d16_hi v227, v227 offset:10528\n\t" \
  "ds_read_u16 v226, v226 offset:10528\n\t" \
  "v_lshlrev_b32_sdwa v235, 1, v175 dst_sel:DWORD dst_unused:UNUSED_PAD src0_sel:DWORD src1_sel:WORD_1\n\t" \
  "v_lshlrev_b32_sdwa v234, 1, v174 dst_sel:DWORD dst_unused:UNUSED_PAD src0_sel:DWORD src1_sel:WORD_1\n\t" \
  "s_waitcnt lgkmcnt(6)\n\t" \
  "v_fma_mix_f32 v221, |v163|, v221, v221 op_sel:[0,1,0] op_sel_hi:[0,1,1] clamp\n\t" \
  "v_mfma_f32_16x16x32_bf16 v[200:203], v[216:219], v[140:143], v[200:203]\n\t" \
  "ds_read_b64_tr_b16 v[216:217], v248 offset:51752\n\t" \
  "ds_read_b32 v229, v229 offset:10016\n\t" \
  "v_fma_mix_f32 v220, |v162|, v220, v220 op_sel:[0,1,0] op_sel_hi:[0,1,1] clamp\n\t" \
  "v_fma_mix_f32 v223, |v161|, v223, v223 op_sel:[0,1,0] op_sel_hi:[0,1,1] clamp\n\t" \
  "v_fma_mix_f32 v222, |v160|, v222, v222 op_sel:[0,1,0] op_sel_hi:[0,1,1] clamp\n\t" \
  "s_waitcnt lgkmcnt(2)\n\t" \
  "v_mfma_f32_16x16x32_bf16 v[180:183], v[208:211], v[96:99], 0\n\t" \
  "ds_read_b64_tr_b16 v[218:219], v248 offset:54824\n\t" \
  "ds_read_b32 v228, v228 offset:10016\n\t" \
  "v_or_b32 v171, v224, v225\n\t" \
  "s_waitcnt vmcnt(0)\n\t" \
  "v_or_b32 v170, v226, v227\n\t" \
  "v_fma_f32 v163, |v163|, v221, v163 mul:4\n\t" \
  "v_mfma_f32_16x16x32_bf16 v[188:191], v[208:211], v[100:103], 0\n\t" \
  "s_mov_b64 exec, 3\n\t" \
  "ds_add_rtn_u32 v246, v251, v250 offset:47144\n\t" \
  "s_mov_b64 exec, -1\n\t" \
  "v_fma_f32 v162, |v162|, v220, v162 mul:4\n\t" \
  "v_mfma_f32_16x16x32_bf16 v[196:199], v[208:211], v[104:107], 0\n\t" \
  "ds_read_b32 v231, v231 offset:10016\n\t" \
  "ds_read_b32 v230, v230 offset:10016\n\t" \
  "v_fma_f32 v161, |v161|, v223, v161 mul:4\n\t" \
  "s_setprio 1\n\t" \
  "v_fma_f32 v160, |v160|, v222, v160 mul:4\n\t" \
  "v_cvt_pk_bf16_f32 v163, v162, v163\n\t" \
  "v_mfma_f32_16x16x32_bf16 v[204:207], v[208:211], v[108:111], 0\n\t" \
  "ds_read_u16_d16_hi v233, v233 offset:10528\n\t" \
  "v_cvt_pk_bf16_f32 v162, v160, v161\n\t" \
  "v_cvt_pknorm_i16_f32 v220, |v178|, |v179|\n\t" \
  "s_waitcnt lgkmcnt(3)\n\t" \
  "v_readfirstlane_b32 s4, v246\n\t" \
  "v_mfma_f32_16x16x32_bf16 v[180:183], v[212:215], v[112:115], v[180:183]\n\t" \
  "ds_read_u16 v232, v232 offset:10528\n\t" \
  "v_cvt_pknorm_i16_f32 v222, |v176|, |v177|\n\t" \
  "s_cmp_lt_u32 s4, s57\n\t" \
  "s_cbranch_scc1 .Lcnx_f0_2\n.Lcnx_g0_2:\n\t" \
  "v_mfma_f32_16x16x32_bf16 v[188:191], v[212:215], v[116:119], v[188:191]\n\t" \
  "ds_read_b64_tr_b16 v[208:209], v249 offset:24600\n\t" \
  "v_lshlrev_b32_sdwa v221, 2, v220 dst_sel:DWORD dst_unused:UNUSED_PAD src0_sel:DWORD src1_sel:BYTE_3\n\t" \
  "v_lshlrev_b32_sdwa v220, 2, v220 dst_sel:DWORD dst_unused:UNUSED_PAD src0_sel:DWORD src1_sel:BYTE_1\n\t" \
  "v_lshlrev_b32_sdwa v223, 2, v222 dst_sel:DWORD dst_unused:UNUSED_PAD src0_sel:DWORD src1_sel:BYTE_3\n\t" \
  "v_mfma_f32_16x16x32_bf16 v[196:199], v[212:215], v[120:123], v[196:199]\n\t" \
  "ds_read_b64_tr_b16 v[210:211], v249 offset:26136\n\t" \
  "v_lshlrev_b32_sdwa v222, 2, v222 dst_sel:DWORD dst_unused:UNUSED_PAD src0_sel:DWORD src1_sel:BYTE_1\n\t" \
  "s_setprio 0\n\t" \
  "v_lshlrev_b32_sdwa v225, 1, v187 dst_sel:DWORD dst_unused:UNUSED_PAD src0_sel:DWORD src1_sel:WORD_1\n\t" \
  "v_lshlrev_b32_sdwa v224, 1, v186 dst_sel:DWORD dst_unused:UNUSED_PAD src0_sel:DWORD src1_sel:WORD_1\n\t" \
  "v_mfma_f32_16x16x32_bf16 v[204:207], v[212:215], v[124:127], v[204:207]\n\t" \
  "ds_read_b64_tr_b16 v[212:213], v249 offset:24608\n\t" \
  "ds_read_u16_d16_hi v235, v235 offset:10528\n\t" \
  "v_lshlrev_b32_sdwa v227, 1, v185 dst_sel:DWORD dst_unused:UNUSED_PAD src0_sel:DWORD src1_sel:WORD_1\n\t" \
  "v_lshlrev_b32_sdwa v226, 1, v184 dst_sel:DWORD dst_unused:UNUSED_PAD src0_sel:DWORD src1_sel:WORD_1\n\t" \
  "ds_read_u16 v234, v234 offset:10528\n\t" \
  "s_waitcnt lgkmcnt(7)\n\t" \
  "v_fma_mix_f32 v229, |v165|, v229, v229 op_sel:[0,1,0] op_sel_hi:[0,1,1] clamp\n\t" \
  "v_mfma_f32_16x16x32_bf16 v[180:183], v[216:219], v[128:131], v[180:183]\n\t" \
  "ds_read_b64_tr_b16 v[214:215], v249 offset:26144\n\t" \
  "ds_read_b32 v221, v221 offset:10016\n\t" \
  "v_fma_mix_f32 v228, |v164|, v228, v228 op_sel:[0,1,0] op_sel_hi:[0,1,1] clamp\n\t" \
  "v_fma_mix_f32 v231, |v167|, v231, v231 op_sel:[0,1,0] op_sel_hi:[0,1,1] clamp\n\t" \
  "v_fma_mix_f32 v230, |v166|, v230, v230 op_sel:[0,1,0] op_sel_hi:[0,1,1] clamp\n\t" \
  "s_waitcnt lgkmcnt(2)\n\t" \
  "v_mfma_f32_16x16x32_bf16 v[188:191], v[216:219], v[132:135], v[188:191]\n\t" \
  "ds_read_b32 v220, v220 offset:10016\n\t" \
  "ds_read_b32 v223, v223 offset:10016\n\t" \
  "v_or_b32 v172, v232, v233\n\t" \
  "v_or_b32 v173, v234, v235\n\t" \
  "v_fma_f32 v165, |v165|, v229, v165 mul:4\n\t" \
  "v_mfma_f32_16x16x32_bf16 v[196:199], v[216:219], v[136:139], v[196:199]\n\t" \
  "ds_read_b32 v222, v222 offset:10016\n\t" \
  "v_fma_f32 v164, |v164|, v228, v164 mul:4\n\t" \
  "v_fma_f32 v167, |v167|, v231, v167 mul:4\n\t" \
  "v_mfma_f32_16x16x32_bf16 v[204:207], v[216:219], v[140:143], v[204:207]\n\t" \
  "ds_read_b64_tr_b16 v[216:217], v249 offset:24664\n\t" \
  "ds_read_u16_d16_hi v225, v225 offset:10528\n\t" \
  "v_fma_f32 v166, |v166|, v230, v166 mul:4\n\t" \
  "v_cvt_pk_bf16_f32 v164, v164, v165\n\t" \
  "v_cvt_pk_bf16_f32 v165, v166, v167\n\t" \
  "v_mfma_f32_16x16x32_bf16 v[0:3], v[242:245], v[236:239], 0\n\t" \
  "ds_read_b64_tr_b16 v[218:219], v249 offset:26200\n\t" \
  "v_cvt_pknorm_i16_f32 v228, |v194|, |v195|\n\t" \
  "s_setprio 0\n\t" \
  "v_cvt_pknorm_i16_f32 v230, |v192|, |v193|\n\t" \
  "v_mfma_f32_16x16x32_bf16 v[4:7], v[240:243], v[236:239], 0\n\t" \
  "ds_read_u16 v224, v224 offset:10528\n\t" \
  "buffer_load_dwordx4 v[236:239], v250, s[8:11], s28 offen offset:64 nt\n\t" \
  "v_lshlrev_b32_sdwa v229, 2, v228 dst_sel:DWORD dst_unused:UNUSED_PAD src0_sel:DWORD src1_sel:BYTE_3\n\t" \
  "v_mfma_f32_16x16x32_bf16 v[0:3], v[208:211], v[146:149], v[0:3]\n\t" \
  "ds_read_u16_d16_hi v227, v227 offset:10528\n\t" \
  "v_lshlrev_b32_sdwa v228, 2, v228 dst_sel:DWORD dst_unused:UNUSED_PAD src0_sel:DWORD src1_sel:BYTE_1\n\t" \
  "s_add_u32 m0, s25, 0\n\t" \
  "s_add_u32 s24, s24, s26\n\t" \
  "v_lshlrev_b32_sdwa v231, 2, v230 dst_sel:DWORD dst_unused:UNUSED_PAD src0_sel:DWORD src1_sel:BYTE_3\n\t" \
  "s_sub_u32 s24, s24, s64\n\t" \
  "v_mfma_f32_16x16x32_bf16 v[24:27], v[208:211], v[154:157], 0\n\t" \
  "ds_read_u16 v226, v226 offset:10528\n\t" \
  "buffer_load_dwordx4 v254, s[8:11], s24 offen nt lds\n\t" \
  "v_lshlrev_b32_sdwa v230, 2, v230 dst_sel:DWORD dst_unused:UNUSED_PAD src0_sel:DWORD src1_sel:BYTE_1\n\t" \
  "v_mfma_f32_16x16x32_bf16 v[48:51], v[208:211], v[162:165], 0\n\t" \
  "ds_read_b32 v229, v229 offset:10016\n\t" \
  "v_lshlrev_b32_sdwa v233, 1, v203 dst_sel:DWORD dst_unused:UNUSED_PAD src0_sel:DWORD src1_sel:WORD_1\n\t" \
  "v_lshlrev_b32_sdwa v232, 1, v202 dst_sel:DWORD dst_unused:UNUSED_PAD src0_sel:DWORD src1_sel:WORD_1\n\t" \
  "v_mfma_f32_16x16x32_bf16 v[72:75], v[208:211], v[170:173], 0\n\t" \
  "ds_read_b64_tr_b16 v[208:209], v249 offset:24672\n\t" \
  "ds_read_b32 v228, v228 offset:10016\n\t" \
  "v_lshlrev_b32_sdwa v235, 1, v201 dst_sel:DWORD dst_unused:UNUSED_PAD src0_sel:DWORD src1_sel:WORD_1\n\t" \
  "v_lshlrev_b32_sdwa v234, 1, v200 dst_sel:DWORD dst_unused:UNUSED_PAD src0_sel:DWORD src1_sel:WORD_1\n\t" \
  "s_waitcnt lgkmcnt(13)\n\t" \
  "v_mfma_f32_16x16x32_bf16 v[4:7], v[212:215], v[146:149], v[4:7]\n\t" \
  "ds_read_b64_tr_b16 v[210:211], v249 offset:26208\n\t" \
  "ds_read_b32 v231, v231 offset:10016\n\t" \
  "s_waitcnt lgkmcnt(11)\n\t" \
  "v_fma_mix_f32 v221, |v179|, v221, v221 op_sel:[0,1,0] op_sel_hi:[0,1,1] clamp\n\t" \
  "v_mfma_f32_16x16x32_bf16 v[28:31], v[212:215], v[154:157], 0\n\t" \
  "ds_read_b32 v230, v230 offset:10016\n\t" \
  "v_fma_mix_f32 v220, |v178|, v220, v220 op_sel:[0,1,0] op_sel_hi:[0,1,1] clamp\n\t" \
  "v_fma_mix_f32 v223, |v177|, v223, v223 op_sel:[0,1,0] op_sel_hi:[0,1,1] clamp\n\t" \
  "v_mfma_f32_16x16x32_bf16 v[52:55], v[212:215], v[162:165], 0\n\t" \
  "ds_read_u16_d16_hi v233, v233 offset:10528\n\t" \
  "v_fma_mix_f32 v222, |v176|, v222, v222 op_sel:[0,1,0] op_sel_hi:[0,1,1] clamp\n\t" \
  "s_waitcnt lgkmcnt(7)\n\t" \
  "v_or_b32 v187, v224, v225\n\t" \
  "v_mfma_f32_16x16x32_bf16 v[76:79], v[212:215], v[170:173], 0\n\t" \
  "ds_read_b64_tr_b16 v[212:213], v249 offset:24728\n\t" \
  "v_or_b32 v186, v226, v227\n\t" \
  "v_fma_f32 v179, |v179|, v221, v179 mul:4\n\t" \
  "v_mfma_f32_16x16x32_bf16 v[8:11], v[216:219], v[146:149], 0\n\t" \
  "ds_read_b64_tr_b16 v[214:215], v249 offset:26264\n\t" \
  "v_fma_f32 v178, |v178|, v220, v178 mul:4\n\t" \
  "v_fma_f32 v177, |v177|, v223, v177 mul:4\n\t" \
  "v_mfma_f32_16x16x32_bf16 v[32:35], v[216:219], v[154:157], 0\n\t" \
  "ds_read_u16 v232, v232 offset:10528\n\t" \
  "v_fma_f32 v176, |v176|, v222, v176 mul:4\n\t" \
  "v_cvt_pk_bf16_f32 v179, v178, v179\n\t" \
  "v_mfma_f32_16x16x32_bf16 v[56:59], v[216:219], v[162:165], 0\n\t" \
  "ds_read_u16_d16_hi v235, v235 offset:10528\n\t" \
  "v_cvt_pk_bf16_f32 v178, v176, v177\n\t" \
  "s_setprio 1\n\t" \
  "v_cvt_pknorm_i16_f32 v220, |v180|, |v181|\n\t" \
  "v_mfma_f32_16x16x32_bf16 v[80:83], v[216:219], v[170:173], 0\n\t" \
  "ds_read_b64_tr_b16 v[216:217], v249 offset:24736\n\t" \
  "v_cvt_pknorm_i16_f32 v222, |v182|, |v183|\n\t" \
  "v_lshlrev_b32_sdwa v221, 2, v220 dst_sel:DWORD dst_unused:UNUSED_PAD src0_sel:DWORD src1_sel:BYTE_3\n\t" \
  "s_waitcnt lgkmcnt(8)\n\t" \
  "v_mfma_f32_16x16x32_bf16 v[12:15], v[208:211], v[146:149], 0\n\t" \
  "ds_read_b64_tr_b16 v[218:219], v249 offset:26272\n\t" \
  "v_lshlrev_b32_sdwa v220, 2, v220 dst_sel:DWORD dst_unused:UNUSED_PAD src0_sel:DWORD src1_sel:BYTE_1\n\t" \
  "v_lshlrev_b32_sdwa v223, 2, v222 dst_sel:DWORD dst_unused:UNUSED_PAD src0_sel:DWORD src1_sel:BYTE_3\n\t" \
  "v_mfma_f32_16x16x32_bf16 v[36:39], v[208:211], v[154:157], 0\n\t" \
  "ds_read_u16 v234, v234 offset:10528\n\t" \
  "v_lshlrev_b32_sdwa v222, 2, v222 dst_sel:DWORD dst_unused:UNUSED_PAD src0_sel:DWORD src1_sel:BYTE_1\n\t" \
  "v_lshlrev_b32_sdwa v225, 1, v189 dst_sel:DWORD dst_unused:UNUSED_PAD src0_sel:DWORD src1_sel:WORD_1\n\t" \
  "v_mfma_f32_16x16x32_bf16 v[60:63], v[208:211], v[162:165], 0\n\t" \
  "ds_read_b32 v221, v221 offset:10016\n\t" \
  "v_lshlrev_b32_sdwa v224, 1, v188 dst_sel:DWORD dst_unused:UNUSED_PAD src0_sel:DWORD src1_sel:WORD_1\n\t" \
  "v_lshlrev_b32_sdwa v227, 1, v191 dst_sel:DWORD dst_unused:UNUSED_PAD src0_sel:DWORD src1_sel:WORD_1\n\t" \
  "v_mfma_f32_16x16x32_bf16 v[84:87], v[208:211], v[170:173], 0\n\t" \
  "ds_read_b64_tr_b16 v[208:209], v248 offset:36904\n\t" \
  "ds_read_b32 v220, v220 offset:10016\n\t" \
  "v_lshlrev_b32_sdwa v226, 1, v190 dst_sel:DWORD dst_unused:UNUSED_PAD src0_sel:DWORD src1_sel:WORD_1\n\t" \
  "s_waitcnt lgkmcnt(8)\n\t" \
  "v_mfma_f32_16x16x32_bf16 v[16:19], v[212:215], v[146:149], 0\n\t" \
  "ds_read_b64_tr_b16 v[210:211], v248 offset:37928\n\t" \
  "ds_read_b32 v223, v223 offset:10016\n\t" \
  "v_fma_mix_f32 v229, |v195|, v229, v229 op_sel:[0,1,0] op_sel_hi:[0,1,1] clamp\n\t" \
  "v_mfma_f32_16x16x32_bf16 v[40:43], v[212:215], v[154:157], 0\n\t" \
  "ds_read_b32 v222, v222 offset:10016\n\t" \
  "v_fma_mix_f32 v228, |v194|, v228, v228 op_sel:[0,1,0] op_sel_hi:[0,1,1] clamp\n\t" \
  "v_fma_mix_f32 v231, |v193|, v231, v231 op_sel:[0,1,0] op_sel_hi:[0,1,1] clamp\n\t" \
  "v_mfma_f32_16x16x32_bf16 v[64:67], v[212:215], v[162:165], 0\n\t" \
  "ds_read_u16_d16_hi v225, v225 offset:10528\n\t" \
  "v_fma_mix_f32 v230, |v192|, v230, v230 op_sel:[0,1,0] op_sel_hi:[0,1,1] clamp\n\t" \
  "s_waitcnt lgkmcnt(7)\n\t" \
  "v_or_b32 v203, v232, v233\n\t" \
  "v_mfma_f32_16x16x32_bf16 v[88:91], v[212:215], v[170:173], 0\n\t" \
  "ds_read_b64_tr_b16 v[212:213], v248 offset:39976\n\t" \
  "v_or_b32 v202, v234, v235\n\t" \
  "v_fma_f32 v195, |v195|, v229, v195 mul:4\n\t" \
  "v_mfma_f32_16x16x32_bf16 v[20:23], v[216:219], v[146:149], 0\n\t" \
  "ds_read_b64_tr_b16 v[214:215], v248 offset:41000\n\t" \
  "v_fma_f32 v194, |v194|, v228, v194 mul:4\n\t" \
  "v_fma_f32 v193, |v193|, v231, v193 mul:4\n\t" \
  "v_mfma_f32_16x16x32_bf16 v[44:47], v[216:219], v[154:157], 0\n\t" \
  "ds_read_u16 v224, v224 offset:10528\n\t" \
  "v_fma_f32 v192, |v192|, v230, v192 mul:4\n\t" \
  "v_cvt_pk_bf16_f32 v195, v194, v195\n\t" \
  "v_mfma_f32_16x16x32_bf16 v[68:71], v[216:219], v[162:165], 0\n\t" \
  "ds_read_u16_d16_hi v227, v227 offset:10528\n\t" \
  "v_cvt_pk_bf16_f32 v194, v192, v193\n\t" \
  "v_cvt_pknorm_i16_f32 v228, |v196|, |v197|\n\t" \
  "v_mfma_f32_16x16x32_bf16 v[92:95], v[216:219], v[170:173], 0\n\t" \
  "ds_read_b64_tr_b16 v[216:217], v248 offset:38952\n\t" \
  "v_cvt_pknorm_i16_f32 v230, |v198|, |v199|\n\t" \
  "s_setprio 2\n\t" \
  "v_lshlrev_b32_sdwa v229, 2, v228 dst_sel:DWORD dst_unused:UNUSED_PAD src0_sel:DWORD src1_sel:BYTE_3\n\t" \
  "s_waitcnt lgkmcnt(8)\n\t" \
  "v_mfma_f32_16x16x32_bf16 v[144:147], v[208:211], v[96:99], 0\n\t" \
  "ds_read_b64_tr_b16 v[218:219], v248 offset:42024\n\t" \
  "v_lshlrev_b32_sdwa v228, 2, v228 dst_sel:DWORD dst_unused:UNUSED_PAD src0_sel:DWORD src1_sel:BYTE_1\n\t" \
  "v_lshlrev_b32_sdwa v231, 2, v230 dst_sel:DWORD dst_unused:UNUSED_PAD src0_sel:DWORD src1_sel:BYTE_3\n\t" \
  "v_mfma_f32_16x16x32_bf16 v[152:155], v[208:211], v[100:103], 0\n\t" \
  "ds_read_u16 v226, v226 offset:10528\n\t" \
  "v_lshlrev_b32_sdwa v230, 2, v230 dst_sel:DWORD dst_unused:UNUSED_PAD src0_sel:DWORD src1_sel:BYTE_1\n\t" \
  "v_mfma_f32_16x16x32_bf16 v[160:163], v[208:211], v[104:107], 0\n\t" \
  "ds_read_b32 v229, v229 offset:10016\n\t" \
  "v_lshlrev_b32_sdwa v233, 1, v205 dst_sel:DWORD dst_unused:UNUSED_PAD src0_sel:DWORD src1_sel:WORD_1\n\t" \
  "v_lshlrev_b32_sdwa v232, 1, v204 dst_sel:DWORD dst_unused:UNUSED_PAD src0_sel:DWORD src1_sel:WORD_1\n\t" \
  "v_mfma_f32_16x16x32_bf16 v[168:171], v[208:211], v[108:111], 0\n\t" \
  "ds_read_b64_tr_b16 v[208:209], v248 offset:37416\n\t" \
  "ds_read_b32 v228, v228 offset:10016\n\t" \
  "v_lshlrev_b32_sdwa v235, 1, v207 dst_sel:DWORD dst_unused:UNUSED_PAD src0_sel:DWORD src1_sel:WORD_1\n\t" \
  "s_waitcnt lgkmcnt(8)\n\t" \
  "v_mfma_f32_16x16x32_bf16 v[144:147], v[212:215], v[112:115], v[144:147]\n\t" \
  "ds_read_b64_tr_b16 v[210:211], v248 offset:38440\n\t" \
  "v_lshlrev_b32_sdwa v234, 1, v206 dst_sel:DWORD dst_unused:UNUSED_PAD src0_sel:DWORD src1_sel:WORD_1\n\t" \
  "v_fma_mix_f32 v221, |v181|, v221, v221 op_sel:[0,1,0] op_sel_hi:[0,1,1] clamp\n\t" \
  "v_mfma_f32_16x16x32_bf16 v[152:155], v[212:215], v[116:119], v[152:155]\n\t" \
  "ds_read_b32 v231, v231 offset:10016\n\t" \
  "ds_read_b32 v230, v230 offset:10016\n\t" \
  "v_fma_mix_f32 v220, |v180|, v220, v220 op_sel:[0,1,0] op_sel_hi:[0,1,1] clamp\n\t" \
  "v_mfma_f32_16x16x32_bf16 v[160:163], v[212:215], v[120:123], v[160:163]\n\t" \
  "ds_read_u16_d16_hi v233, v233 offset:10528\n\t" \
  "v_fma_mix_f32 v223, |v183|, v223, v223 op_sel:[0,1,0] op_sel_hi:[0,1,1] clamp\n\t" \
  "v_fma_mix_f32 v222, |v182|, v222, v222 op_sel:[0,1,0] op_sel_hi:[0,1,1] clamp\n\t" \
  "s_waitcnt lgkmcnt(7)\n\t" \
  "v_mfma_f32_16x16x32_bf16 v[168:171], v[212:215], v[124:127], v[168:171]\n\t" \
  "ds_read_b64_tr_b16 v[212:213], v248 offset:40488\n\t" \
  "v_or_b32 v188, v224, v225\n\t" \
  "v_or_b32 v189, v226, v227\n\t" \
  "v_mfma_f32_16x16x32_bf16 v[144:147], v[216:219], v[128:131], v[144:147]\n\t" \
  "ds_read_b64_tr_b16 v[214:215], v248 offset:41512\n\t" \
  "v_fma_f32 v181, |v181|, v221, v181 mul:4\n\t" \
  "v_fma_f32 v180, |v180|, v220, v180 mul:4\n\t" \
  "v_mfma_f32_16x16x32_bf16 v[152:155], v[216:219], v[132:135], v[152:155]\n\t" \
  "ds_read_u16 v232, v232 offset:10528\n\t" \
  "v_fma_f32 v183, |v183|, v223, v183 mul:4\n\t" \
  "v_fma_f32 v182, |v182|, v222, v182 mul:4\n\t" \
  "v_mfma_f32_16x16x32_bf16 v[160:163], v[216:219], v[136:139], v[160:163]\n\t" \
  "ds_read_u16_d16_hi v235, v235 offset:10528\n\t" \
  "v_cvt_pk_bf16_f32 v180, v180, v181\n\t" \
  "v_cvt_pk_bf16_f32 v181, v182, v183\n\t" \
  "v_mfma_f32_16x16x32_bf16 v[168:171], v[216:219], v[140:143], v[168:171]\n\t" \
  "ds_read_b64_tr_b16 v[216:217], v248 offset:39464\n\t" \
  "v_cvt_pknorm_i16_f32 v220, |v146|, |v147|\n\t" \
  "s_setprio 3\n\t" \
  "v_cvt_pknorm_i16_f32 v222, |v144|, |v145|\n\t" \
  "s_waitcnt lgkmcnt(8)\n\t" \
  "v_mfma_f32_16x16x32_bf16 v[148:151], v[208:211], v[96:99], 0\n\t" \
  "ds_read_b64_tr_b16 v[218:219], v248 offset:42536\n\t" \
  "v_lshlrev_b32_sdwa v221, 2, v220 dst_sel:DWORD dst_unused:UNUSED_PAD src0_sel:DWORD src1_sel:BYTE_3\n\t" \
  "s_waitcnt vmcnt(1)\n\t" \
  "v_lshlrev_b32_sdwa v220, 2, v220 dst_sel:DWORD dst_unused:UNUSED_PAD src0_sel:DWORD src1_sel:BYTE_1\n\t" \
  "v_mfma_f32_16x16x32_bf16 v[156:159], v[208:211], v[100:103], 0\n\t" \
  "s_mov_b64 exec, 3\n\t" \
  "ds_add_rtn_u32 v246, v251, v250 offset:47152\n\t" \
  "s_mov_b64 exec, -1\n\t" \
  "v_lshlrev_b32_sdwa v223, 2, v222 dst_sel:DWORD dst_unused:UNUSED_PAD src0_sel:DWORD src1_sel:BYTE_3\n\t" \
  "v_mfma_f32_16x16x32_bf16 v[164:167], v[208:211], v[104:107], 0\n\t" \
  "ds_read_u16 v234, v234 offset:10528\n\t" \
  "v_lshlrev_b32_sdwa v222, 2, v222 dst_sel:DWORD dst_unused:UNUSED_PAD src0_sel:DWORD src1_sel:BYTE_1\n\t" \
  "v_lshlrev_b32_sdwa v225, 1, v155 dst_sel:DWORD dst_unused:UNUSED_PAD src0_sel:DWORD src1_sel:WORD_1\n\t" \
  "v_mfma_f32_16x16x32_bf16 v[172:175], v[208:211], v[108:111], 0\n\t" \
  "ds_read_b32 v221, v221 offset:10016\n\t" \
  "v_lshlrev_b32_sdwa v224, 1, v154 dst_sel:DWORD dst_unused:UNUSED_PAD src0_sel:DWORD src1_sel:WORD_1\n\t" \
  "s_waitcnt lgkmcnt(2)\n\t" \
  "v_readfirstlane_b32 s4, v246\n\t" \
  "v_mfma_f32_16x16x32_bf16 v[148:151], v[212:215], v[112:115], v[148:151]\n\t" \
  "ds_read_b32 v220, v220 offset:10016\n\t" \
  "v_lshlrev_b32_sdwa v227, 1, v153 dst_sel:DWORD dst_unused:UNUSED_PAD src0_sel:DWORD src1_sel:WORD_1\n\t" \
  "s_cmp_lt_u32 s4, s57\n\t" \
  "s_cbranch_scc1 .Lcnx_f0_3\n.Lcnx_g0_3:\n\t" \
  "v_mfma_f32_16x16x32_bf16 v[156:159], v[212:215], v[116:119], v[156:159]\n\t" \
  "ds_read_b64_tr_b16 v[208:209], v249 offset:30752\n\t" \
  "ds_read_b32 v223, v223 offset:10016\n\t" \
  "v_lshlrev_b32_sdwa v226, 1, v152 dst_sel:DWORD dst_unused:UNUSED_PAD src0_sel:DWORD src1_sel:WORD_1\n\t" \
  "v_mfma_f32_16x16x32_bf16 v[164:167], v[212:215], v[120:123], v[164:167]\n\t" \
  "ds_read_b64_tr_b16 v[210:211], v249 offset:32288\n\t" \
  "v_fma_mix_f32 v229, |v197|, v229, v229 op_sel:[0,1,0] op_sel_hi:[0,1,1] clamp\n\t" \
  "v_fma_mix_f32 v228, |v196|, v228, v228 op_sel:[0,1,0] op_sel_hi:[0,1,1] clamp\n\t" \
  "v_mfma_f32_16x16x32_bf16 v[172:175], v[212:215], v[124:127], v[172:175]\n\t" \
  "ds_read_b64_tr_b16 v[212:213], v249 offset:30760\n\t" \
  "ds_read_b32 v222, v222 offset:10016\n\t" \
  "v_fma_mix_f32 v231, |v199|, v231, v231 op_sel:[0,1,0] op_sel_hi:[0,1,1] clamp\n\t" \
  "v_fma_mix_f32 v230, |v198|, v230, v230 op_sel:[0,1,0] op_sel_hi:[0,1,1] clamp\n\t" \
  "s_waitcnt lgkmcnt(7)\n\t" \
  "v_mfma_f32_16x16x32_bf16 v[148:151], v[216:219], v[128:131], v[148:151]\n\t" \
  "ds_read_b64_tr_b16 v[214:215], v249 offset:32296\n\t" \
  "v_or_b32 v204, v232, v233\n\t" \
  "v_or_b32 v205, v234, v235\n\t" \
  "v_mfma_f32_16x16x32_bf16 v[156:159], v[216:219], v[132:135], v[156:159]\n\t" \
  "ds_read_u16_d16_hi v225, v225 offset:10528\n\t" \
  "v_fma_f32 v197, |v197|, v229, v197 mul:4\n\t" \
  "v_fma_f32 v196, |v196|, v228, v196 mul:4\n\t" \
  "v_mfma_f32_16x16x32_bf16 v[164:167], v[216:219], v[136:139], v[164:167]\n\t" \
  "ds_read_u16 v224, v224 offset:10528\n\t" \
  "v_fma_f32 v199, |v199|, v231, v199 mul:4\n\t" \
  "v_fma_f32 v198, |v198|, v230, v198 mul:4\n\t" \
  "v_mfma_f32_16x16x32_bf16 v[172:175], v[216:219], v[140:143], v[172:175]\n\t" \
  "ds_read_b64_tr_b16 v[216:217], v249 offset:30816\n\t" \
  "v_cvt_pk_bf16_f32 v196, v196, v197\n\t" \
  "v_cvt_pk_bf16_f32 v197, v198, v199\n\t" \
  "v_mfma_f32_16x16x32_bf16 v[8:11], v[242:245], v[236:239], v[8:11]\n\t" \
  "ds_read_b64_tr_b16 v[218:219], v249 offset:32352\n\t" \
  "v_cvt_pknorm_i16_f32 v228, |v162|, |v163|\n\t" \
  "s_setprio 0\n\t" \
  "v_cvt_pknorm_i16_f32 v230, |v160|, |v161|\n\t" \
  "v_mfma_f32_16x16x32_bf16 v[12:15], v[240:243], v[236:239], v[12:15]\n\t" \
  "ds_read_u16_d16_hi v227, v227 offset:10528\n\t" \
  "buffer_load_dwordx4 v[236:239], v250, s[8:11], s28 offen offset:128 nt\n\t" \
  "v_lshlrev_b32_sdwa v229, 2, v228 dst_sel:DWORD dst_unused:UNUSED_PAD src0_sel:DWORD src1_sel:BYTE_3\n\t" \
  "s_waitcnt lgkmcnt(8)\n\t" \
  "v_mfma_f32_16x16x32_bf16 v[0:3], v[208:211], v[178:181], v[0:3]\n\t" \
  "ds_read_u16 v226, v226 offset:10528\n\t" \
  "v_lshlrev_b32_sdwa v228, 2, v228 dst_sel:DWORD dst_unused:UNUSED_PAD src0_sel:DWORD src1_sel:BYTE_1\n\t" \
  "s_add_u32 m0, s25, 8200\n\t" \
  "s_add_u32 s24, s24, s26\n\t" \
  "v_lshlrev_b32_sdwa v231, 2, v230 dst_sel:DWORD dst_unused:UNUSED_PAD src0_sel:DWORD src1_sel:BYTE_3\n\t" \
  "v_mfma_f32_16x16x32_bf16 v[24:27], v[208:211], v[186:189], v[24:27]\n\t" \
  "ds_read_b32 v229, v229 offset:10016\n\t" \
  "buffer_load_dwordx4 v254, s[8:11], s24 offen nt lds\n\t" \
  "v_lshlrev_b32_sdwa v230, 2, v230 dst_sel:DWORD dst_unused:UNUSED_PAD src0_sel:DWORD src1_sel:BYTE_1\n\t" \
  "v_mfma_f32_16x16x32_bf16 v[48:51], v[208:211], v[194:197], v[48:51]\n\t" \
  "ds_read_b32 v228, v228 offset:10016\n\t" \
  "v_lshlrev_b32_sdwa v233, 1, v171 dst_sel:DWORD dst_unused:UNUSED_PAD src0_sel:DWORD src1_sel:WORD_1\n\t" \
  "v_lshlrev_b32_sdwa v232, 1, v170 dst_sel:DWORD dst_unused:UNUSED_PAD src0_sel:DWORD src1_sel:WORD_1\n\t" \
  "v_mfma_f32_16x16x32_bf16 v[72:75], v[208:211], v[202:205], v[72:75]\n\t" \
  "ds_read_b64_tr_b16 v[208:209], v249 offset:30824\n\t" \
  "v_lshlrev_b32_sdwa v235, 1, v169 dst_sel:DWORD dst_unused:UNUSED_PAD src0_sel:DWORD src1_sel:WORD_1\n\t" \
  "s_cmp_eq_u32 s38, 4\n\t" \
  "s_cbranch_scc1 .Lcnx_h3\n.Lcnx_i3:\n\t" \
  "s_waitcnt lgkmcnt(9)\n\t" \
  "v_mfma_f32_16x16x32_bf16 v[4:7], v[212:215], v[178:181], v[4:7]\n\t" \
  "ds_read_b64_tr_b16 v[210:211], v249 offset:32360\n\t" \
  "v_lshlrev_b32_sdwa v234, 1, v168 dst_sel:DWORD dst_unused:UNUSED_PAD src0_sel:DWORD src1_sel:WORD_1\n\t" \
  "v_fma_mix_f32 v221, |v147|, v221, v221 op_sel:[0,1,0] op_sel_hi:[0,1,1] clamp\n\t" \
  "v_mfma_f32_16x16x32_bf16 v[28:31], v[212:215], v[186:189], v[28:31]\n\t" \
  "ds_read_b32 v231, v231 offset:10016\n\t" \
  "ds_read_b32 v230, v230 offset:10016\n\t" \
  "v_fma_mix_f32 v220, |v146|, v220, v220 op_sel:[0,1,0] op_sel_hi:[0,1,1] clamp\n\t" \
  "v_fma_mix_f32 v223, |v145|, v223, v223 op_sel:[0,1,0] op_sel_hi:[0,1,1] clamp\n\t" \
  "v_mfma_f32_16x16x32_bf16 v[52:55], v[212:215], v[194:197], v[52:55]\n\t" \
  "ds_read_u16_d16_hi v233, v233 offset:10528\n\t" \
  "v_fma_mix_f32 v222, |v144|, v222, v222 op_sel:[0,1,0] op_sel_hi:[0,1,1] clamp\n\t" \
  "s_waitcnt lgkmcnt(7)\n\t" \
  "v_or_b32 v155, v224, v225\n\t" \
  "v_mfma_f32_16x16x32_bf16 v[76:79], v[212:215], v[202:205], v[76:79]\n\t" \
  "ds_read_b64_tr_b16 v[212:213], v249 offset:30880\n\t" \
  "v_or_b32 v154, v226, v227\n\t" \
  "v_fma_f32 v147, |v147|, v221, v147 mul:4\n\t" \
  "v_mfma_f32_16x16x32_bf16 v[8:11], v[216:219], v[178:181], v[8:11]\n\t" \
  "ds_read_b64_tr_b16 v[214:215], v249 offset:32416\n\t" \
  "v_fma_f32 v146, |v146|, v220, v146 mul:4\n\t" \
  "v_fma_f32 v145, |v145|, v223, v145 mul:4\n\t" \
  "v_mfma_f32_16x16x32_bf16 v[32:35], v[216:219], v[186:189], v[32:35]\n\t" \
  "ds_read_u16 v232, v232 offset:10528\n\t" \
  "v_fma_f32 v144, |v144|, v222, v144 mul:4\n\t" \
  "v_cvt_pk_bf16_f32 v147, v146, v147\n\t" \
  "v_mfma_f32_16x16x32_bf16 v[56:59], v[216:219], v[194:197], v[56:59]\n\t" \
  "ds_read_u16_d16_hi v235, v235 offset:10528\n\t" \
  "v_cvt_pk_bf16_f32 v146, v144, v145\n\t" \
  "s_setprio 1\n\t" \
  "v_cvt_pknorm_i16_f32 v220, |v148|, |v149|\n\t" \
  "v_mfma_f32_16x16x32_bf16 v[80:83], v[216:219], v[202:205], v[80:83]\n\t" \
  "ds_read_b64_tr_b16 v[216:217], v249 offset:30888\n\t" \
  "v_cvt_pknorm_i16_f32 v222, |v150|, |v151|\n\t" \
  "v_lshlrev_b32_sdwa v221, 2, v220 dst_sel:DWORD dst_unused:UNUSED_PAD src0_sel:DWORD src1_sel:BYTE_3\n\t" \
  "s_waitcnt lgkmcnt(8)\n\t" \
  "v_mfma_f32_16x16x32_bf16 v[12:15], v[208:211], v[178:181], v[12:15]\n\t" \
  "ds_read_b64_tr_b16 v[218:219], v249 offset:32424\n\t" \
  "v_lshlrev_b32_sdwa v220, 2, v220 dst_sel:DWORD dst_unused:UNUSED_PAD src0_sel:DWORD src1_sel:BYTE_1\n\t" \
  "v_lshlrev_b32_sdwa v223, 2, v222 dst_sel:DWORD dst_unused:UNUSED_PAD src0_sel:DWORD src1_sel:BYTE_3\n\t" \
  "v_mfma_f32_16x16x32_bf16 v[36:39], v[208:211], v[186:189], v[36:39]\n\t" \
  "ds_read_u16 v234, v234 offset:10528\n\t" \
  "v_lshlrev_b32_sdwa v222, 2, v222 dst_sel:DWORD dst_unused:UNUSED_PAD src0_sel:DWORD src1_sel:BYTE_1\n\t" \
  "v_lshlrev_b32_sdwa v225, 1, v157 dst_sel:DWORD dst_unused:UNUSED_PAD src0_sel:DWORD src1_sel:WORD_1\n\t" \
  "v_mfma_f32_16x16x32_bf16 v[60:63], v[208:211], v[194:197], v[60:63]\n\t" \
  "ds_read_b32 v221, v221 offset:10016\n\t" \
  "v_lshlrev_b32_sdwa v224, 1, v156 dst_sel:DWORD dst_unused:UNUSED_PAD src0_sel:DWORD src1_sel:WORD_1\n\t" \
  "v_lshlrev_b32_sdwa v227, 1, v159 dst_sel:DWORD dst_unused:UNUSED_PAD src0_sel:DWORD src1_sel:WORD_1\n\t" \
  "v_mfma_f32_16x16x32_bf16 v[84:87], v[208:211], v[202:205], v[84:87]\n\t" \
  "ds_read_b64_tr_b16 v[208:209], v248 offset:0\n\t" \
  "ds_read_b32 v220, v220 offset:10016\n\t" \
  "v_lshlrev_b32_sdwa v226, 1, v158 dst_sel:DWORD dst_unused:UNUSED_PAD src0_sel:DWORD src1_sel:WORD_1\n\t" \
  "s_waitcnt lgkmcnt(8)\n\t" \
  "v_mfma_f32_16x16x32_bf16 v[16:19], v[212:215], v[178:181], v[16:19]\n\t" \
  "ds_read_b64_tr_b16 v[210:211], v248 offset:1024\n\t" \
  "ds_read_b32 v223, v223 offset:10016\n\t" \
  "v_fma_mix_f32 v229, |v163|, v229, v229 op_sel:[0,1,0] op_sel_hi:[0,1,1] clamp\n\t" \
  "v_mfma_f32_16x16x32_bf16 v[40:43], v[212:215], v[186:189], v[40:43]\n\t" \
  "ds_read_b32 v222, v222 offset:10016\n\t" \
  "v_fma_mix_f32 v228, |v162|, v228, v228 op_sel:[0,1,0] op_sel_hi:[0,1,1] clamp\n\t" \
  "v_fma_mix_f32 v231, |v161|, v231, v231 op_sel:[0,1,0] op_sel_hi:[0,1,1] clamp\n\t" \
  "v_mfma_f32_16x16x32_bf16 v[64:67], v[212:215], v[194:197], v[64:67]\n\t" \
  "ds_read_u16_d16_hi v225, v225 offset:10528\n\t" \
  "v_fma_mix_f32 v230, |v160|, v230, v230 op_sel:[0,1,0] op_sel_hi:[0,1,1] clamp\n\t" \
  "s_waitcnt lgkmcnt(7)\n\t" \
  "v_or_b32 v171, v232, v233\n\t" \
  "v_mfma_f32_16x16x32_bf16 v[88:91], v[212:215], v[202:205], v[88:91]\n\t" \
  "ds_read_b64_tr_b16 v[212:213], v248 offset:3072\n\t" \
  "v_or_b32 v170, v234, v235\n\t" \
  "v_fma_f32 v163, |v163|, v229, v163 mul:4\n\t" \
  "v_mfma_f32_16x16x32_bf16 v[20:23], v[216:219], v[178:181], v[20:23]\n\t" \
  "ds_read_b64_tr_b16 v[214:215], v248 offset:4096\n\t" \
  "v_fma_f32 v162, |v162|, v228, v162 mul:4\n\t" \
  "v_fma_f32 v161, |v161|, v231, v161 mul:4\n\t" \
  "v_mfma_f32_16x16x32_bf16 v[44:47], v[216:219], v[186:189], v[44:47]\n\t" \
  "ds_read_u16 v224, v224 offset:10528\n\t" \
  "v_fma_f32 v160, |v160|, v230, v160 mul:4\n\t" \
  "v_cvt_pk_bf16_f32 v163, v162, v163\n\t" \
  "v_mfma_f32_16x16x32_bf16 v[68:71], v[216:219], v[194:197], v[68:71]\n\t" \
  "ds_read_u16_d16_hi v227, v227 offset:10528\n\t" \
  "v_cvt_pk_bf16_f32 v162, v160, v161\n\t" \
  "v_cvt_pknorm_i16_f32 v228, |v164|, |v165|\n\t" \
  "v_mfma_f32_16x16x32_bf16 v[92:95], v[216:219], v[202:205], v[92:95]\n\t" \
  "ds_read_b64_tr_b16 v[216:217], v248 offset:2048\n\t" \
  "v_cvt_pknorm_i16_f32 v230, |v166|, |v167|\n\t" \
  "s_setprio 2\n\t" \
  "v_lshlrev_b32_sdwa v229, 2, v228 dst_sel:DWORD dst_unused:UNUSED_PAD src0_sel:DWORD src1_sel:BYTE_3\n\t" \
  "s_waitcnt lgkmcnt(8)\n\t" \
  "v_mfma_f32_16x16x32_bf16 v[176:179], v[208:211], v[96:99], 0\n\t" \
  "ds_read_b64_tr_b16 v[218:219], v248 offset:5120\n\t" \
  "v_lshlrev_b32_sdwa v228, 2, v228 dst_sel:DWORD dst_unused:UNUSED_PAD src0_sel:DWORD src1_sel:BYTE_1\n\t" \
  "v_lshlrev_b32_sdwa v231, 2, v230 dst_sel:DWORD dst_unused:UNUSED_PAD src0_sel:DWORD src1_sel:BYTE_3\n\t" \
  "v_mfma_f32_16x16x32_bf16 v[184:187], v[208:211], v[100:103], 0\n\t" \
  "ds_read_u16 v226, v226 offset:10528\n\t" \
  "v_lshlrev_b32_sdwa v230, 2, v230 dst_sel:DWORD dst_unused:UNUSED_PAD src0_sel:DWORD src1_sel:BYTE_1\n\t" \
  "v_mfma_f32_16x16x32_bf16 v[192:195], v[208:211], v[104:107], 0\n\t" \
  "ds_read_b32 v229, v229 offset:10016\n\t" \
  "v_lshlrev_b32_sdwa v233, 1, v173 dst_sel:DWORD dst_unused:UNUSED_PAD src0_sel:DWORD src1_sel:WORD_1\n\t" \
  "v_lshlrev_b32_sdwa v232, 1, v172 dst_sel:DWORD dst_unused:UNUSED_PAD src0_sel:DWORD src1_sel:WORD_1\n\t" \
  "v_mfma_f32_16x16x32_bf16 v[200:203], v[208:211], v[108:111], 0\n\t" \
  "ds_read_b64_tr_b16 v[208:209], v248 offset:512\n\t" \
  "ds_read_b32 v228, v228 offset:10016\n\t" \
  "v_lshlrev_b32_sdwa v235, 1, v175 dst_sel:DWORD dst_unused:UNUSED_PAD src0_sel:DWORD src1_sel:WORD_1\n\t" \
  "s_waitcnt lgkmcnt(8)\n\t" \
  "v_mfma_f32_16x16x32_bf16 v[176:179], v[212:215], v[112:115], v[176:179]\n\t" \
  "ds_read_b64_tr_b16 v[210:211], v248 offset:1536\n\t" \
  "v_lshlrev_b32_sdwa v234, 1, v174 dst_sel:DWORD dst_unused:UNUSED_PAD src0_sel:DWORD src1_sel:WORD_1\n\t" \
  "v_fma_mix_f32 v221, |v149|, v221, v221 op_sel:[0,1,0] op_sel_hi:[0,1,1] clamp\n\t" \
  "v_mfma_f32_16x16x32_bf16 v[184:187], v[212:215], v[116:119], v[184:187]\n\t" \
  "ds_read_b32 v231, v231 offset:10016\n\t" \
  "ds_read_b32 v230, v230 offset:10016\n\t" \
  "v_fma_mix_f32 v220, |v148|, v220, v220 op_sel:[0,1,0] op_sel_hi:[0,1,1] clamp\n\t" \
  "v_mfma_f32_16x16x32_bf16 v[192:195], v[212:215], v[120:123], v[192:195]\n\t" \
  "ds_read_u16_d16_hi v233, v233 offset:10528\n\t" \
  "v_fma_mix_f32 v223, |v151|, v223, v223 op_sel:[0,1,0] op_sel_hi:[0,1,1] clamp\n\t" \
  "v_fma_mix_f32 v222, |v150|, v222, v222 op_sel:[0,1,0] op_sel_hi:[0,1,1] clamp\n\t" \
  "s_waitcnt lgkmcnt(7)\n\t" \
  "v_mfma_f32_16x16x32_bf16 v[200:203], v[212:215], v[124:127], v[200:203]\n\t" \
  "ds_read_b64_tr_b16 v[212:213], v248 offset:3584\n\t" \
  "v_or_b32 v156, v224, v225\n\t" \
  "v_or_b32 v157, v226, v227\n\t" \
  "v_mfma_f32_16x16x32_bf16 v[176:179], v[216:219], v[128:131], v[176:179]\n\t" \
  "ds_read_b64_tr_b16 v[214:215], v248 offset:4608\n\t" \
  "v_fma_f32 v149, |v149|, v221, v149 mul:4\n\t" \
  "v_fma_f32 v148, |v148|, v220, v148 mul:4\n\t" \
  "v_mfma_f32_16x16x32_bf16 v[184:187], v[216:219], v[132:135], v[184:187]\n\t" \
  "ds_read_u16 v232, v232 offset:10528\n\t" \
  "v_fma_f32 v151, |v151|, v223, v151 mul:4\n\t" \
  "v_fma_f32 v150, |v150|, v222, v150 mul:4\n\t" \
  "v_mfma_f32_16x16x32_bf16 v[192:195], v[216:219], v[136:139], v[192:195]\n\t" \
  "ds_read_u16_d16_hi v235, v235 offset:10528\n\t" \
  "v_cvt_pk_bf16_f32 v148, v148, v149\n\t" \
  "v_cvt_pk_bf16_f32 v149, v150, v151\n\t" \
  "v_mfma_f32_16x16x32_bf16 v[200:203], v[216:219], v[140:143], v[200:203]\n\t" \
  "ds_read_b64_tr_b16 v[216:217], v248 offset:2560\n\t" \
  "v_cvt_pknorm_i16_f32 v220, |v178|, |v179|\n\t" \
  "s_setprio 3\n\t" \
  "v_cvt_pknorm_i16_f32 v222, |v176|, |v177|\n\t" \
  "s_waitcnt lgkmcnt(8)\n\t" \
  "v_mfma_f32_16x16x32_bf16 v[180:183], v[208:211], v[96:99], 0\n\t" \
  "ds_read_b64_tr_b16 v[218:219], v248 offset:5632\n\t" \
  "v_lshlrev_b32_sdwa v221, 2, v220 dst_sel:DWORD dst_unused:UNUSED_PAD src0_sel:DWORD src1_sel:BYTE_3\n\t" \
  "s_waitcnt vmcnt(1)\n\t" \
  "v_lshlrev_b32_sdwa v220, 2, v220 dst_sel:DWORD dst_unused:UNUSED_PAD src0_sel:DWORD src1_sel:BYTE_1\n\t" \
  "v_mfma_f32_16x16x32_bf16 v[188:191], v[208:211], v[100:103], 0\n\t" \
  "s_mov_b64 exec, 3\n\t" \
  "ds_add_rtn_u32 v246, v251, v250 offset:47160\n\t" \
  "s_mov_b64 exec, -1\n\t" \
  "v_lshlrev_b32_sdwa v223, 2, v222 dst_sel:DWORD dst_unused:UNUSED_PAD src0_sel:DWORD src1_sel:BYTE_3\n\t" \
  "v_mfma_f32_16x16x32_bf16 v[196:199], v[208:211], v[104:107], 0\n\t" \
  "ds_read_u16 v234, v234 offset:10528\n\t" \
  "v_lshlrev_b32_sdwa v222, 2, v222 dst_sel:DWORD dst_unused:UNUSED_PAD src0_sel:DWORD src1_sel:BYTE_1\n\t" \
  "v_lshlrev_b32_sdwa v225, 1, v187 dst_sel:DWORD dst_unused:UNUSED_PAD src0_sel:DWORD src1_sel:WORD_1\n\t" \
  "v_mfma_f32_16x16x32_bf16 v[204:207], v[208:211], v[108:111], 0\n\t" \
  "ds_read_b32 v221, v221 offset:10016\n\t" \
  "v_lshlrev_b32_sdwa v224, 1, v186 dst_sel:DWORD dst_unused:UNUSED_PAD src0_sel:DWORD src1_sel:WORD_1\n\t" \
  "s_waitcnt lgkmcnt(2)\n\t" \
  "v_readfirstlane_b32 s4, v246\n\t" \
  "v_mfma_f32_16x16x32_bf16 v[180:183], v[212:215], v[112:115], v[180:183]\n\t" \
  "ds_read_b32 v220, v220 offset:10016\n\t" \
  "v_lshlrev_b32_sdwa v227, 1, v185 dst_sel:DWORD dst_unused:UNUSED_PAD src0_sel:DWORD src1_sel:WORD_1\n\t" \
  "s_cmp_lt_u32 s4, s57\n\t" \
  "s_cbranch_scc1 .Lcnx_f0_4\n.Lcnx_g0_4:\n\t" \
  "v_mfma_f32_16x16x32_bf16 v[188:191], v[212:215], v[116:119], v[188:191]\n\t" \
  "ds_read_b64_tr_b16 v[208:209], v249 offset:6152\n\t" \
  "ds_read_b32 v223, v223 offset:10016\n\t" \
  "v_lshlrev_b32_sdwa v226, 1, v184 dst_sel:DWORD dst_unused:UNUSED_PAD src0_sel:DWORD src1_sel:WORD_1\n\t" \
  "v_mfma_f32_16x16x32_bf16 v[196:199], v[212:215], v[120:123], v[196:199]\n\t" \
  "ds_read_b64_tr_b16 v[210:211], v249 offset:7688\n\t" \
  "v_fma_mix_f32 v229, |v165|, v229, v229 op_sel:[0,1,0] op_sel_hi:[0,1,1] clamp\n\t" \
  "v_fma_mix_f32 v228, |v164|, v228, v228 op_sel:[0,1,0] op_sel_hi:[0,1,1] clamp\n\t" \
  "v_mfma_f32_16x16x32_bf16 v[204:207], v[212:215], v[124:127], v[204:207]\n\t" \
  "ds_read_b64_tr_b16 v[212:213], v249 offset:6160\n\t" \
  "ds_read_b32 v222, v222 offset:10016\n\t" \
  "v_fma_mix_f32 v231, |v167|, v231, v231 op_sel:[0,1,0] op_sel_hi:[0,1,1] clamp\n\t" \
  "v_fma_mix_f32 v230, |v166|, v230, v230 op_sel:[0,1,0] op_sel_hi:[0,1,1] clamp\n\t" \
  "s_waitcnt lgkmcnt(7)\n\t" \
  "v_mfma_f32_16x16x32_bf16 v[180:183], v[216:219], v[128:131], v[180:183]\n\t" \
  "ds_read_b64_tr_b16 v[214:215], v249 offset:7696\n\t" \
  "v_or_b32 v172, v232, v233\n\t" \
  "v_or_b32 v173, v234, v235\n\t" \
  "v_mfma_f32_16x16x32_bf16 v[188:191], v[216:219], v[132:135], v[188:191]\n\t" \
  "ds_read_u16_d16_hi v225, v225 offset:10528\n\t" \
  "v_fma_f32 v165, |v165|, v229, v165 mul:4\n\t" \
  "v_fma_f32 v164, |v164|, v228, v164 mul:4\n\t" \
  "v_mfma_f32_16x16x32_bf16 v[196:199], v[216:219], v[136:139], v[196:199]\n\t" \
  "ds_read_u16 v224, v224 offset:10528\n\t" \
  "v_fma_f32 v167, |v167|, v231, v167 mul:4\n\t" \
  "v_fma_f32 v166, |v166|, v230, v166 mul:4\n\t" \
  "v_mfma_f32_16x16x32_bf16 v[204:207], v[216:219], v[140:143], v[204:207]\n\t" \
  "ds_read_b64_tr_b16 v[216:217], v249 offset:6216\n\t" \
  "v_cvt_pk_bf16_f32 v164, v164, v165\n\t" \
  "v_cvt_pk_bf16_f32 v165, v166, v167\n\t" \
  "v_mfma_f32_16x16x32_bf16 v[16:19], v[242:245], v[236:239], v[16:19]\n\t" \
  "ds_read_b64_tr_b16 v[218:219], v249 offset:7752\n\t" \
  "v_cvt_pknorm_i16_f32 v228, |v194|, |v195|\n\t" \
  "s_setprio 0\n\t" \
  "v_cvt_pknorm_i16_f32 v230, |v192|, |v193|\n\t" \
  "v_mfma_f32_16x16x32_bf16 v[20:23], v[240:243], v[236:239], v[20:23]\n\t" \
  "ds_read_u16_d16_hi v227, v227 offset:10528\n\t" \
  "buffer_load_dwordx4 v[236:239], v250, s[8:11], s29 offen offset:0 nt\n\t" \
  "v_lshlrev_b32_sdwa v229, 2, v228 dst_sel:DWORD dst_unused:UNUSED_PAD src0_sel:DWORD src1_sel:BYTE_3\n\t" \
  "s_waitcnt lgkmcnt(8)\n\t" \
  "v_mfma_f32_16x16x32_bf16 v[0:3], v[208:211], v[146:149], v[0:3]\n\t" \
  "ds_read_u16 v226, v226 offset:10528\n\t" \
  "v_lshlrev_b32_sdwa v228, 2, v228 dst_sel:DWORD dst_unused:UNUSED_PAD src0_sel:DWORD src1_sel:BYTE_1\n\t" \
  "s_add_u32 m0, s25, 16400\n\t" \
  "s_add_u32 s24, s24, s26\n\t" \
  "v_lshlrev_b32_sdwa v231, 2, v230 dst_sel:DWORD dst_unused:UNUSED_PAD src0_sel:DWORD src1_sel:BYTE_3\n\t" \
  "v_mfma_f32_16x16x32_bf16 v[24:27], v[208:211], v[154:157], v[24:27]\n\t" \
  "ds_read_b32 v229, v229 offset:10016\n\t" \
  "buffer_load_dwordx4 v254, s[8:11], s24 offen nt lds\n\t" \
  "v_lshlrev_b32_sdwa v230, 2, v230 dst_sel:DWORD dst_unused:UNUSED_PAD src0_sel:DWORD src1_sel:BYTE_1\n\t" \
  "v_mfma_f32_16x16x32_bf16 v[48:51], v[208:211], v[162:165], v[48:51]\n\t" \
  "ds_read_b32 v228, v228 offset:10016\n\t" \
  "v_lshlrev_b32_sdwa v233, 1, v203 dst_sel:DWORD dst_unused:UNUSED_PAD src0_sel:DWORD src1_sel:WORD_1\n\t" \
  "v_lshlrev_b32_sdwa v232, 1, v202 dst_sel:DWORD dst_unused:UNUSED_PAD src0_sel:DWORD src1_sel:WORD_1\n\t" \
  "v_mfma_f32_16x16x32_bf16 v[72:75], v[208:211], v[170:173], v[72:75]\n\t" \
  "ds_read_b64_tr_b16 v[208:209], v249 offset:6224\n\t" \
  "v_lshlrev_b32_sdwa v235, 1, v201 dst_sel:DWORD dst_unused:UNUSED_PAD src0_sel:DWORD src1_sel:WORD_1\n\t" \
  "s_cmp_eq_u32 s38, 5\n\t" \
  "s_cbranch_scc1 .Lcnx_h4\n.Lcnx_i4:\n\t" \
  "s_waitcnt lgkmcnt(9)\n\t" \
  "v_mfma_f32_16x16x32_bf16 v[4:7], v[212:215], v[146:149], v[4:7]\n\t" \
  "ds_read_b64_tr_b16 v[210:211], v249 offset:7760\n\t" \
  "v_lshlrev_b32_sdwa v234, 1, v200 dst_sel:DWORD dst_unused:UNUSED_PAD src0_sel:DWORD src1_sel:WORD_1\n\t" \
  "v_fma_mix_f32 v221, |v179|, v221, v221 op_sel:[0,1,0] op_sel_hi:[0,1,1] clamp\n\t" \
  "v_mfma_f32_16x16x32_bf16 v[28:31], v[212:215], v[154:157], v[28:31]\n\t" \
  "ds_read_b32 v231, v231 offset:10016\n\t" \
  "ds_read_b32 v230, v230 offset:10016\n\t" \
  "v_fma_mix_f32 v220, |v178|, v220, v220 op_sel:[0,1,0] op_sel_hi:[0,1,1] clamp\n\t" \
  "v_fma_mix_f32 v223, |v177|, v223, v223 op_sel:[0,1,0] op_sel_hi:[0,1,1] clamp\n\t" \
  "v_mfma_f32_16x16x32_bf16 v[52:55], v[212:215], v[162:165], v[52:55]\n\t" \
  "ds_read_u16_d16_hi v233, v233 offset:10528\n\t" \
  "v_fma_mix_f32 v222, |v176|, v222, v222 op_sel:[0,1,0] op_sel_hi:[0,1,1] clamp\n\t" \
  "s_waitcnt lgkmcnt(7)\n\t" \
  "v_or_b32 v187, v224, v225\n\t" \
  "v_mfma_f32_16x16x32_bf16 v[76:79], v[212:215], v[170:173], v[76:79]\n\t" \
  "ds_read_b64_tr_b16 v[212:213], v249 offset:6280\n\t" \
  "v_or_b32 v186, v226, v227\n\t" \
  "v_fma_f32 v179, |v179|, v221, v179 mul:4\n\t" \
  "v_mfma_f32_16x16x32_bf16 v[8:11], v[216:219], v[146:149], v[8:11]\n\t" \
  "ds_read_b64_tr_b16 v[214:215], v249 offset:7816\n\t" \
  "v_fma_f32 v178, |v178|, v220, v178 mul:4\n\t" \
  "v_fma_f32 v177, |v177|, v223, v177 mul:4\n\t" \
  "v_mfma_f32_16x16x32_bf16 v[32:35], v[216:219], v[154:157], v[32:35]\n\t" \
  "ds_read_u16 v232, v232 offset:10528\n\t" \
  "v_fma_f32 v176, |v176|, v222, v176 mul:4\n\t" \
  "v_cvt_pk_bf16_f32 v179, v178, v179\n\t" \
  "v_mfma_f32_16x16x32_bf16 v[56:59], v[216:219], v[162:165], v[56:59]\n\t" \
  "ds_read_u16_d16_hi v235, v235 offset:10528\n\t" \
  "v_cvt_pk_bf16_f32 v178, v176, v177\n\t" \
  "s_setprio 1\n\t" \
  "v_cvt_pknorm_i16_f32 v220, |v180|, |v181|\n\t" \
  "v_mfma_f32_16x16x32_bf16 v[80:83], v[216:219], v[170:173], v[80:83]\n\t" \
  "ds_read_b64_tr_b16 v[216:217], v249 offset:6288\n\t" \
  "v_cvt_pknorm_i16_f32 v222, |v182|, |v183|\n\t" \
  "v_lshlrev_b32_sdwa v221, 2, v220 dst_sel:DWORD dst_unused:UNUSED_PAD src0_sel:DWORD src1_sel:BYTE_3\n\t" \
  "s_waitcnt lgkmcnt(8)\n\t" \
  "v_mfma_f32_16x16x32_bf16 v[12:15], v[208:211], v[146:149], v[12:15]\n\t" \
  "ds_read_b64_tr_b16 v[218:219], v249 offset:7824\n\t" \
  "v_lshlrev_b32_sdwa v220, 2, v220 dst_sel:DWORD dst_unused:UNUSED_PAD src0_sel:DWORD src1_sel:BYTE_1\n\t" \
  "v_lshlrev_b32_sdwa v223, 2, v222 dst_sel:DWORD dst_unused:UNUSED_PAD src0_sel:DWORD src1_sel:BYTE_3\n\t" \
  "v_mfma_f32_16x16x32_bf16 v[36:39], v[208:211], v[154:157], v[36:39]\n\t" \
  "ds_read_u16 v234, v234 offset:10528\n\t" \
  "v_lshlrev_b32_sdwa v222, 2, v222 dst_sel:DWORD dst_unused:UNUSED_PAD src0_sel:DWORD src1_sel:BYTE_1\n\t" \
  "v_lshlrev_b32_sdwa v225, 1, v189 dst_sel:DWORD dst_unused:UNUSED_PAD src0_sel:DWORD src1_sel:WORD_1\n\t" \
  "v_mfma_f32_16x16x32_bf16 v[60:63], v[208:211], v[162:165], v[60:63]\n\t" \
  "ds_read_b32 v221, v221 offset:10016\n\t" \
  "v_lshlrev_b32_sdwa v224, 1, v188 dst_sel:DWORD dst_unused:UNUSED_PAD src0_sel:DWORD src1_sel:WORD_1\n\t" \
  "v_lshlrev_b32_sdwa v227, 1, v191 dst_sel:DWORD dst_unused:UNUSED_PAD src0_sel:DWORD src1_sel:WORD_1\n\t" \
  "v_mfma_f32_16x16x32_bf16 v[84:87], v[208:211], v[170:173], v[84:87]\n\t" \
  "ds_read_b64_tr_b16 v[208:209], v248 offset:43048\n\t" \
  "ds_read_b32 v220, v220 offset:10016\n\t" \
  "v_lshlrev_b32_sdwa v226, 1, v190 dst_sel:DWORD dst_unused:UNUSED_PAD src0_sel:DWORD src1_sel:WORD_1\n\t" \
  "s_waitcnt lgkmcnt(8)\n\t" \
  "v_mfma_f32_16x16x32_bf16 v[16:19], v[212:215], v[146:149], v[16:19]\n\t" \
  "ds_read_b64_tr_b16 v[210:211], v248 offset:44072\n\t" \
  "ds_read_b32 v223, v223 offset:10016\n\t" \
  "v_fma_mix_f32 v229, |v195|, v229, v229 op_sel:[0,1,0] op_sel_hi:[0,1,1] clamp\n\t" \
  "v_mfma_f32_16x16x32_bf16 v[40:43], v[212:215], v[154:157], v[40:43]\n\t" \
  "ds_read_b32 v222, v222 offset:10016\n\t" \
  "v_fma_mix_f32 v228, |v194|, v228, v228 op_sel:[0,1,0] op_sel_hi:[0,1,1] clamp\n\t" \
  "v_fma_mix_f32 v231, |v193|, v231, v231 op_sel:[0,1,0] op_sel_hi:[0,1,1] clamp\n\t" \
  "v_mfma_f32_16x16x32_bf16 v[64:67], v[212:215], v[162:165], v[64:67]\n\t" \
  "ds_read_u16_d16_hi v225, v225 offset:10528\n\t" \
  "v_fma_mix_f32 v230, |v192|, v230, v230 op_sel:[0,1,0] op_sel_hi:[0,1,1] clamp\n\t" \
  "s_waitcnt lgkmcnt(7)\n\t" \
  "v_or_b32 v203, v232, v233\n\t" \
  "v_mfma_f32_16x16x32_bf16 v[88:91], v[212:215], v[170:173], v[88:91]\n\t" \
  "ds_read_b64_tr_b16 v[212:213], v248 offset:46120\n\t" \
  "v_or_b32 v202, v234, v235\n\t" \
  "v_fma_f32 v195, |v195|, v229, v195 mul:4\n\t" \
  "v_mfma_f32_16x16x32_bf16 v[20:23], v[216:219], v[146:149], v[20:23]\n\t" \
  "ds_read_b64_tr_b16 v[214:215], v248 offset:47144\n\t" \
  "v_fma_f32 v194, |v194|, v228, v194 mul:4\n\t" \
  "v_fma_f32 v193, |v193|, v231, v193 mul:4\n\t" \
  "v_mfma_f32_16x16x32_bf16 v[44:47], v[216:219], v[154:157], v[44:47]\n\t" \
  "ds_read_u16 v224, v224 offset:10528\n\t" \
  "v_fma_f32 v192, |v192|, v230, v192 mul:4\n\t" \
  "v_cvt_pk_bf16_f32 v195, v194, v195\n\t" \
  "v_mfma_f32_16x16x32_bf16 v[68:71], v[216:219], v[162:165], v[68:71]\n\t" \
  "ds_read_u16_d16_hi v227, v227 offset:10528\n\t" \
  "v_cvt_pk_bf16_f32 v194, v192, v193\n\t" \
  "v_cvt_pknorm_i16_f32 v228, |v196|, |v197|\n\t" \
  "v_mfma_f32_16x16x32_bf16 v[92:95], v[216:219], v[170:173], v[92:95]\n\t" \
  "ds_read_b64_tr_b16 v[216:217], v248 offset:45096\n\t" \
  "v_cvt_pknorm_i16_f32 v230, |v198|, |v199|\n\t" \
  "s_setprio 2\n\t" \
  "v_lshlrev_b32_sdwa v229, 2, v228 dst_sel:DWORD dst_unused:UNUSED_PAD src0_sel:DWORD src1_sel:BYTE_3\n\t" \
  "s_waitcnt lgkmcnt(8)\n\t" \
  "v_mfma_f32_16x16x32_bf16 v[144:147], v[208:211], v[96:99], 0\n\t" \
  "ds_read_b64_tr_b16 v[218:219], v248 offset:48168\n\t" \
  "v_lshlrev_b32_sdwa v228, 2, v228 dst_sel:DWORD dst_unused:UNUSED_PAD src0_sel:DWORD src1_sel:BYTE_1\n\t" \
  "v_lshlrev_b32_sdwa v231, 2, v230 dst_sel:DWORD dst_unused:UNUSED_PAD src0_sel:DWORD src1_sel:BYTE_3\n\t" \
  "v_mfma_f32_16x16x32_bf16 v[152:155], v[208:211], v[100:103], 0\n\t" \
  "ds_read_u16 v226, v226 offset:10528\n\t" \
  "v_lshlrev_b32_sdwa v230, 2, v230 dst_sel:DWORD dst_unused:UNUSED_PAD src0_sel:DWORD src1_sel:BYTE_1\n\t" \
  "v_mfma_f32_16x16x32_bf16 v[160:163], v[208:211], v[104:107], 0\n\t" \
  "ds_read_b32 v229, v229 offset:10016\n\t" \
  "v_lshlrev_b32_sdwa v233, 1, v205 dst_sel:DWORD dst_unused:UNUSED_PAD src0_sel:DWORD src1_sel:WORD_1\n\t" \
  "v_lshlrev_b32_sdwa v232, 1, v204 dst_sel:DWORD dst_unused:UNUSED_PAD src0_sel:DWORD src1_sel:WORD_1\n\t" \
  "v_mfma_f32_16x16x32_bf16 v[168:171], v[208:211], v[108:111], 0\n\t" \
  "ds_read_b64_tr_b16 v[208:209], v248 offset:43560\n\t" \
  "ds_read_b32 v228, v228 offset:10016\n\t" \
  "v_lshlrev_b32_sdwa v235, 1, v207 dst_sel:DWORD dst_unused:UNUSED_PAD src0_sel:DWORD src1_sel:WORD_1\n\t" \
  "s_waitcnt lgkmcnt(8)\n\t" \
  "v_mfma_f32_16x16x32_bf16 v[144:147], v[212:215], v[112:115], v[144:147]\n\t" \
  "ds_read_b64_tr_b16 v[210:211], v248 offset:44584\n\t" \
  "v_lshlrev_b32_sdwa v234, 1, v206 dst_sel:DWORD dst_unused:UNUSED_PAD src0_sel:DWORD src1_sel:WORD_1\n\t" \
  "v_fma_mix_f32 v221, |v181|, v221, v221 op_sel:[0,1,0] op_sel_hi:[0,1,1] clamp\n\t" \
  "v_mfma_f32_16x16x32_bf16 v[152:155], v[212:215], v[116:119], v[152:155]\n\t" \
  "ds_read_b32 v231, v231 offset:10016\n\t" \
  "ds_read_b32 v230, v230 offset:10016\n\t" \
  "v_fma_mix_f32 v220, |v180|, v220, v220 op_sel:[0,1,0] op_sel_hi:[0,1,1] clamp\n\t" \
  "v_mfma_f32_16x16x32_bf16 v[160:163], v[212:215], v[120:123], v[160:163]\n\t" \
  "ds_read_u16_d16_hi v233, v233 offset:10528\n\t" \
  "v_fma_mix_f32 v223, |v183|, v223, v223 op_sel:[0,1,0] op_sel_hi:[0,1,1] clamp\n\t" \
  "v_fma_mix_f32 v222, |v182|, v222, v222 op_sel:[0,1,0] op_sel_hi:[0,1,1] clamp\n\t" \
  "s_waitcnt lgkmcnt(7)\n\t" \
  "v_mfma_f32_16x16x32_bf16 v[168:171], v[212:215], v[124:127], v[168:171]\n\t" \
  "ds_read_b64_tr_b16 v[212:213], v248 offset:46632\n\t" \
  "v_or_b32 v188, v224, v225\n\t" \
  "v_or_b32 v189, v226, v227\n\t" \
  "v_mfma_f32_16x16x32_bf16 v[144:147], v[216:219], v[128:131], v[144:147]\n\t" \
  "ds_read_b64_tr_b16 v[214:215], v248 offset:47656\n\t" \
  "v_fma_f32 v181, |v181|, v221, v181 mul:4\n\t" \
  "v_fma_f32 v180, |v180|, v220, v180 mul:4\n\t" \
  "v_mfma_f32_16x16x32_bf16 v[152:155], v[216:219], v[132:135], v[152:155]\n\t" \
  "ds_read_u16 v232, v232 offset:10528\n\t" \
  "v_fma_f32 v183, |v183|, v223, v183 mul:4\n\t" \
  "v_fma_f32 v182, |v182|, v222, v182 mul:4\n\t" \
  "v_mfma_f32_16x16x32_bf16 v[160:163], v[216:219], v[136:139], v[160:163]\n\t" \
  "ds_read_u16_d16_hi v235, v235 offset:10528\n\t" \
  "v_cvt_pk_bf16_f32 v180, v180, v181\n\t" \
  "v_cvt_pk_bf16_f32 v181, v182, v183\n\t" \
  "v_mfma_f32_16x16x32_bf16 v[168:171], v[216:219], v[140:143], v[168:171]\n\t" \
  "ds_read_b64_tr_b16 v[216:217], v248 offset:45608\n\t" \
  "v_cvt_pknorm_i16_f32 v220, |v146|, |v147|\n\t" \
  "s_setprio 3\n\t" \
  "v_cvt_pknorm_i16_f32 v222, |v144|, |v145|\n\t" \
  "s_waitcnt lgkmcnt(8)\n\t" \
  "v_mfma_f32_16x16x32_bf16 v[148:151], v[208:211], v[96:99], 0\n\t" \
  "ds_read_b64_tr_b16 v[218:219], v248 offset:48680\n\t" \
  "v_lshlrev_b32_sdwa v221, 2, v220 dst_sel:DWORD dst_unused:UNUSED_PAD src0_sel:DWORD src1_sel:BYTE_3\n\t" \
  "s_waitcnt vmcnt(1)\n\t" \
  "v_lshlrev_b32_sdwa v220, 2, v220 dst_sel:DWORD dst_unused:UNUSED_PAD src0_sel:DWORD src1_sel:BYTE_1\n\t" \
  "v_mfma_f32_16x16x32_bf16 v[156:159], v[208:211], v[100:103], 0\n\t" \
  "s_mov_b64 exec, 3\n\t" \
  "ds_add_rtn_u32 v246, v251, v250 offset:47168\n\t" \
  "s_mov_b64 exec, -1\n\t" \
  "v_lshlrev_b32_sdwa v223, 2, v222 dst_sel:DWORD dst_unused:UNUSED_PAD src0_sel:DWORD src1_sel:BYTE_3\n\t" \
  "v_mfma_f32_16x16x32_bf16 v[164:167], v[208:211], v[104:107], 0\n\t" \
  "ds_read_u16 v234, v234 offset:10528\n\t" \
  "v_lshlrev_b32_sdwa v222, 2, v222 dst_sel:DWORD dst_unused:UNUSED_PAD src0_sel:DWORD src1_sel:BYTE_1\n\t" \
  "v_lshlrev_b32_sdwa v225, 1, v155 dst_sel:DWORD dst_unused:UNUSED_PAD src0_sel:DWORD src1_sel:WORD_1\n\t" \
  "v_mfma_f32_16x16x32_bf16 v[172:175], v[208:211], v[108:111], 0\n\t" \
  "ds_read_b32 v221, v221 offset:10016\n\t" \
  "v_lshlrev_b32_sdwa v224, 1, v154 dst_sel:DWORD dst_unused:UNUSED_PAD src0_sel:DWORD src1_sel:WORD_1\n\t" \
  "s_waitcnt lgkmcnt(2)\n\t" \
  "v_readfirstlane_b32 s4, v246\n\t" \
  "v_mfma_f32_16x16x32_bf16 v[148:151], v[212:215], v[112:115], v[148:151]\n\t" \
  "ds_read_b32 v220, v220 offset:10016\n\t" \
  "v_lshlrev_b32_sdwa v227, 1, v153 dst_sel:DWORD dst_unused:UNUSED_PAD src0_sel:DWORD src1_sel:WORD_1\n\t" \
  "s_cmp_lt_u32 s4, s57\n\t" \
  "s_cbranch_scc1 .Lcnx_f0_5\n.Lcnx_g0_5:\n\t" \
  "v_mfma_f32_16x16x32_bf16 v[156:159], v[212:215], v[116:119], v[156:159]\n\t" \
  "ds_read_b64_tr_b16 v[208:209], v249 offset:12304\n\t" \
  "ds_read_b32 v223, v223 offset:10016\n\t" \
  "v_lshlrev_b32_sdwa v226, 1, v152 dst_sel:DWORD dst_unused:UNUSED_PAD src0_sel:DWORD src1_sel:WORD_1\n\t" \
  "v_mfma_f32_16x16x32_bf16 v[164:167], v[212:215], v[120:123], v[164:167]\n\t" \
  "ds_read_b64_tr_b16 v[210:211], v249 offset:13840\n\t" \
  "v_fma_mix_f32 v229, |v197|, v229, v229 op_sel:[0,1,0] op_sel_hi:[0,1,1] clamp\n\t" \
  "v_fma_mix_f32 v228, |v196|, v228, v228 op_sel:[0,1,0] op_sel_hi:[0,1,1] clamp\n\t" \
  "v_mfma_f32_16x16x32_bf16 v[172:175], v[212:215], v[124:127], v[172:175]\n\t" \
  "ds_read_b64_tr_b16 v[212:213], v249 offset:12312\n\t" \
  "ds_read_b32 v222, v222 offset:10016\n\t" \
  "v_fma_mix_f32 v231, |v199|, v231, v231 op_sel:[0,1,0] op_sel_hi:[0,1,1] clamp\n\t" \
  "v_fma_mix_f32 v230, |v198|, v230, v230 op_sel:[0,1,0] op_sel_hi:[0,1,1] clamp\n\t" \
  "s_waitcnt lgkmcnt(7)\n\t" \
  "v_mfma_f32_16x16x32_bf16 v[148:151], v[216:219], v[128:131], v[148:151]\n\t" \
  "ds_read_b64_tr_b16 v[214:215], v249 offset:13848\n\t" \
  "v_or_b32 v204, v232, v233\n\t" \
  "v_or_b32 v205, v234, v235\n\t" \
  "v_mfma_f32_16x16x32_bf16 v[156:159], v[216:219], v[132:135], v[156:159]\n\t" \
  "ds_read_u16_d16_hi v225, v225 offset:10528\n\t" \
  "v_fma_f32 v197, |v197|, v229, v197 mul:4\n\t" \
  "v_fma_f32 v196, |v196|, v228, v196 mul:4\n\t" \
  "v_mfma_f32_16x16x32_bf16 v[164:167], v[216:219], v[136:139], v[164:167]\n\t" \
  "ds_read_u16 v224, v224 offset:10528\n\t" \
  "v_fma_f32 v199, |v199|, v231, v199 mul:4\n\t" \
  "v_fma_f32 v198, |v198|, v230, v198 mul:4\n\t" \
  "v_mfma_f32_16x16x32_bf16 v[172:175], v[216:219], v[140:143], v[172:175]\n\t" \
  "ds_read_b64_tr_b16 v[216:217], v249 offset:12368\n\t" \
  "v_cvt_pk_bf16_f32 v196, v196, v197\n\t" \
  "v_cvt_pk_bf16_f32 v197, v198, v199\n\t" \
  "v_mfma_f32_16x16x32_bf16 v[24:27], v[242:245], v[236:239], v[24:27]\n\t" \
  "ds_read_b64_tr_b16 v[218:219], v249 offset:13904\n\t" \
  "v_cvt_pknorm_i16_f32 v228, |v162|, |v163|\n\t" \
  "s_setprio 0\n\t" \
  "v_cvt_pknorm_i16_f32 v230, |v160|, |v161|\n\t" \
  "v_mfma_f32_16x16x32_bf16 v[28:31], v[240:243], v[236:239], v[28:31]\n\t" \
  "ds_read_u16_d16_hi v227, v227 offset:10528\n\t" \
  "buffer_load_dwordx4 v[236:239], v250, s[8:11], s29 offen offset:64 nt\n\t" \
  "v_lshlrev_b32_sdwa v229, 2, v228 dst_sel:DWORD dst_unused:UNUSED_PAD src0_sel:DWORD src1_sel:BYTE_3\n\t" \
  "s_waitcnt lgkmcnt(8)\n\t" \
  "v_mfma_f32_16x16x32_bf16 v[0:3], v[208:211], v[178:181], v[0:3]\n\t" \
  "ds_read_u16 v226, v226 offset:10528\n\t" \
  "v_lshlrev_b32_sdwa v228, 2, v228 dst_sel:DWORD dst_unused:UNUSED_PAD src0_sel:DWORD src1_sel:BYTE_1\n\t" \
  "s_add_u32 m0, s25, 24600\n\t" \
  "s_add_u32 s24, s24, s26\n\t" \
  "v_lshlrev_b32_sdwa v231, 2, v230 dst_sel:DWORD dst_unused:UNUSED_PAD src0_sel:DWORD src1_sel:BYTE_3\n\t" \
  "v_mfma_f32_16x16x32_bf16 v[24:27], v[208:211], v[186:189], v[24:27]\n\t" \
  "ds_read_b32 v229, v229 offset:10016\n\t" \
  "buffer_load_dwordx4 v254, s[8:11], s24 offen lds\n\t" \
  "v_lshlrev_b32_sdwa v230, 2, v230 dst_sel:DWORD dst_unused:UNUSED_PAD src0_sel:DWORD src1_sel:BYTE_1\n\t" \
  "v_mfma_f32_16x16x32_bf16 v[48:51], v[208:211], v[194:197], v[48:51]\n\t" \
  "ds_read_b32 v228, v228 offset:10016\n\t" \
  "v_lshlrev_b32_sdwa v233, 1, v171 dst_sel:DWORD dst_unused:UNUSED_PAD src0_sel:DWORD src1_sel:WORD_1\n\t" \
  "v_lshlrev_b32_sdwa v232, 1, v170 dst_sel:DWORD dst_unused:UNUSED_PAD src0_sel:DWORD src1_sel:WORD_1\n\t" \
  "v_mfma_f32_16x16x32_bf16 v[72:75], v[208:211], v[202:205], v[72:75]\n\t" \
  "ds_read_b64_tr_b16 v[208:209], v249 offset:12376\n\t" \
  "v_lshlrev_b32_sdwa v235, 1, v169 dst_sel:DWORD dst_unused:UNUSED_PAD src0_sel:DWORD src1_sel:WORD_1\n\t" \
  "s_cmp_eq_u32 s38, 6\n\t" \
  "s_cbranch_scc1 .Lcnx_h5\n.Lcnx_i5:\n\t" \
  "s_waitcnt lgkmcnt(9)\n\t" \
  "v_mfma_f32_16x16x32_bf16 v[4:7], v[212:215], v[178:181], v[4:7]\n\t" \
  "ds_read_b64_tr_b16 v[210:211], v249 offset:13912\n\t" \
  "v_lshlrev_b32_sdwa v234, 1, v168 dst_sel:DWORD dst_unused:UNUSED_PAD src0_sel:DWORD src1_sel:WORD_1\n\t" \
  "v_fma_mix_f32 v221, |v147|, v221, v221 op_sel:[0,1,0] op_sel_hi:[0,1,1] clamp\n\t" \
  "v_mfma_f32_16x16x32_bf16 v[28:31], v[212:215], v[186:189], v[28:31]\n\t" \
  "ds_read_b32 v231, v231 offset:10016\n\t" \
  "ds_read_b32 v230, v230 offset:10016\n\t" \
  "v_fma_mix_f32 v220, |v146|, v220, v220 op_sel:[0,1,0] op_sel_hi:[0,1,1] clamp\n\t" \
  "v_fma_mix_f32 v223, |v145|, v223, v223 op_sel:[0,1,0] op_sel_hi:[0,1,1] clamp\n\t" \
  "v_mfma_f32_16x16x32_bf16 v[52:55], v[212:215], v[194:197], v[52:55]\n\t" \
  "ds_read_u16_d16_hi v233, v233 offset:10528\n\t" \
  "v_fma_mix_f32 v222, |v144|, v222, v222 op_sel:[0,1,0] op_sel_hi:[0,1,1] clamp\n\t" \
  "s_waitcnt lgkmcnt(7)\n\t" \
  "v_or_b32 v155, v224, v225\n\t" \
  "v_mfma_f32_16x16x32_bf16 v[76:79], v[212:215], v[202:205], v[76:79]\n\t" \
  "ds_read_b64_tr_b16 v[212:213], v249 offset:12432\n\t" \
  "v_or_b32 v154, v226, v227\n\t" \
  "v_fma_f32 v147, |v147|, v221, v147 mul:4\n\t" \
  "v_mfma_f32_16x16x32_bf16 v[8:11], v[216:219], v[178:181], v[8:11]\n\t" \
  "ds_read_b64_tr_b16 v[214:215], v249 offset:13968\n\t" \
  "v_fma_f32 v146, |v146|, v220, v146 mul:4\n\t" \
  "v_fma_f32 v145, |v145|, v223, v145 mul:4\n\t" \
  "v_mfma_f32_16x16x32_bf16 v[32:35], v[216:219], v[186:189], v[32:35]\n\t" \
  "ds_read_u16 v232, v232 offset:10528\n\t" \
  "v_fma_f32 v144, |v144|, v222, v144 mul:4\n\t" \
  "v_cvt_pk_bf16_f32 v147, v146, v147\n\t" \
  "v_mfma_f32_16x16x32_bf16 v[56:59], v[216:219], v[194:197], v[56:59]\n\t" \
  "ds_read_u16_d16_hi v235, v235 offset:10528\n\t" \
  "v_cvt_pk_bf16_f32 v146, v144, v145\n\t" \
  "s_setprio 1\n\t" \
  "v_cvt_pknorm_i16_f32 v220, |v148|, |v149|\n\t" \
  "v_mfma_f32_16x16x32_bf16 v[80:83], v[216:219], v[202:205], v[80:83]\n\t" \
  "ds_read_b64_tr_b16 v[216:217], v249 offset:12440\n\t" \
  "v_cvt_pknorm_i16_f32 v222, |v150|, |v151|\n\t" \
  "v_lshlrev_b32_sdwa v221, 2, v220 dst_sel:DWORD dst_unused:UNUSED_PAD src0_sel:DWORD src1_sel:BYTE_3\n\t" \
  "s_waitcnt lgkmcnt(8)\n\t" \
  "v_mfma_f32_16x16x32_bf16 v[12:15], v[208:211], v[178:181], v[12:15]\n\t" \
  "ds_read_b64_tr_b16 v[218:219], v249 offset:13976\n\t" \
  "v_lshlrev_b32_sdwa v220, 2, v220 dst_sel:DWORD dst_unused:UNUSED_PAD src0_sel:DWORD src1_sel:BYTE_1\n\t" \
  "v_lshlrev_b32_sdwa v223, 2, v222 dst_sel:DWORD dst_unused:UNUSED_PAD src0_sel:DWORD src1_sel:BYTE_3\n\t" \
  "v_mfma_f32_16x16x32_bf16 v[36:39], v[208:211], v[186:189], v[36:39]\n\t" \
  "ds_read_u16 v234, v234 offset:10528\n\t" \
  "v_lshlrev_b32_sdwa v222, 2, v222 dst_sel:DWORD dst_unused:UNUSED_PAD src0_sel:DWORD src1_sel:BYTE_1\n\t" \
  "v_lshlrev_b32_sdwa v225, 1, v157 dst_sel:DWORD dst_unused:UNUSED_PAD src0_sel:DWORD src1_sel:WORD_1\n\t" \
  "v_mfma_f32_16x16x32_bf16 v[60:63], v[208:211], v[194:197], v[60:63]\n\t" \
  "ds_read_b32 v221, v221 offset:10016\n\t" \
  "v_lshlrev_b32_sdwa v224, 1, v156 dst_sel:DWORD dst_unused:UNUSED_PAD src0_sel:DWORD src1_sel:WORD_1\n\t" \
  "v_lshlrev_b32_sdwa v227, 1, v159 dst_sel:DWORD dst_unused:UNUSED_PAD src0_sel:DWORD src1_sel:WORD_1\n\t" \
  "v_mfma_f32_16x16x32_bf16 v[84:87], v[208:211], v[202:205], v[84:87]\n\t" \
  "ds_read_b64_tr_b16 v[208:209], v248 offset:49192\n\t" \
  "ds_read_b32 v220, v220 offset:10016\n\t" \
  "v_lshlrev_b32_sdwa v226, 1, v158 dst_sel:DWORD dst_unused:UNUSED_PAD src0_sel:DWORD src1_sel:WORD_1\n\t" \
  "s_waitcnt lgkmcnt(8)\n\t" \
  "v_mfma_f32_16x16x32_bf16 v[16:19], v[212:215], v[178:181], v[16:19]\n\t" \
  "ds_read_b64_tr_b16 v[210:211], v248 offset:50216\n\t" \
  "ds_read_b32 v223, v223 offset:10016\n\t" \
  "v_fma_mix_f32 v229, |v163|, v229, v229 op_sel:[0,1,0] op_sel_hi:[0,1,1] clamp\n\t" \
  "v_mfma_f32_16x16x32_bf16 v[40:43], v[212:215], v[186:189], v[40:43]\n\t" \
  "ds_read_b32 v222, v222 offset:10016\n\t" \
  "v_fma_mix_f32 v228, |v162|, v228, v228 op_sel:[0,1,0] op_sel_hi:[0,1,1] clamp\n\t" \
  "v_fma_mix_f32 v231, |v161|, v231, v231 op_sel:[0,1,0] op_sel_hi:[0,1,1] clamp\n\t" \
  "v_mfma_f32_16x16x32_bf16 v[64:67], v[212:215], v[194:197], v[64:67]\n\t" \
  "ds_read_u16_d16_hi v225, v225 offset:10528\n\t" \
  "v_fma_mix_f32 v230, |v160|, v230, v230 op_sel:[0,1,0] op_sel_hi:[0,1,1] clamp\n\t" \
  "s_waitcnt lgkmcnt(7)\n\t" \
  "v_or_b32 v171, v232, v233\n\t" \
  "v_mfma_f32_16x16x32_bf16 v[88:91], v[212:215], v[202:205], v[88:91]\n\t" \
  "ds_read_b64_tr_b16 v[212:213], v248 offset:52264\n\t" \
  "v_or_b32 v170, v234, v235\n\t" \
  "v_fma_f32 v163, |v163|, v229, v163 mul:4\n\t" \
  "v_mfma_f32_16x16x32_bf16 v[20:23], v[216:219], v[178:181], v[20:23]\n\t" \
  "ds_read_b64_tr_b16 v[214:215], v248 offset:53288\n\t" \
  "v_fma_f32 v162, |v162|, v228, v162 mul:4\n\t" \
  "v_fma_f32 v161, |v161|, v231, v161 mul:4\n\t" \
  "v_mfma_f32_16x16x32_bf16 v[44:47], v[216:219], v[186:189], v[44:47]\n\t" \
  "ds_read_u16 v224, v224 offset:10528\n\t" \
  "v_fma_f32 v160, |v160|, v230, v160 mul:4\n\t" \
  "v_cvt_pk_bf16_f32 v163, v162, v163\n\t" \
  "v_mfma_f32_16x16x32_bf16 v[68:71], v[216:219], v[194:197], v[68:71]\n\t" \
  "ds_read_u16_d16_hi v227, v227 offset:10528\n\t" \
  "v_cvt_pk_bf16_f32 v162, v160, v161\n\t" \
  "v_cvt_pknorm_i16_f32 v228, |v164|, |v165|\n\t" \
  "v_mfma_f32_16x16x32_bf16 v[92:95], v[216:219], v[202:205], v[92:95]\n\t" \
  "ds_read_b64_tr_b16 v[216:217], v248 offset:51240\n\t" \
  "v_cvt_pknorm_i16_f32 v230, |v166|, |v167|\n\t" \
  "s_setprio 2\n\t" \
  "v_lshlrev_b32_sdwa v229, 2, v228 dst_sel:DWORD dst_unused:UNUSED_PAD src0_sel:DWORD src1_sel:BYTE_3\n\t" \
  "s_waitcnt lgkmcnt(8)\n\t" \
  "v_mfma_f32_16x16x32_bf16 v[176:179], v[208:211], v[96:99], 0\n\t" \
  "ds_read_b64_tr_b16 v[218:219], v248 offset:54312\n\t" \
  "v_lshlrev_b32_sdwa v228, 2, v228 dst_sel:DWORD dst_unused:UNUSED_PAD src0_sel:DWORD src1_sel:BYTE_1\n\t" \
  "v_lshlrev_b32_sdwa v231, 2, v230 dst_sel:DWORD dst_unused:UNUSED_PAD src0_sel:DWORD src1_sel:BYTE_3\n\t" \
  "v_mfma_f32_16x16x32_bf16 v[184:187], v[208:211], v[100:103], 0\n\t" \
  "ds_read_u16 v226, v226 offset:10528\n\t" \
  "v_lshlrev_b32_sdwa v230, 2, v230 dst_sel:DWORD dst_unused:UNUSED_PAD src0_sel:DWORD src1_sel:BYTE_1\n\t" \
  "v_mfma_f32_16x16x32_bf16 v[192:195], v[208:211], v[104:107], 0\n\t" \
  "ds_read_b32 v229, v229 offset:10016\n\t" \
  "v_lshlrev_b32_sdwa v233, 1, v173 dst_sel:DWORD dst_unused:UNUSED_PAD src0_sel:DWORD src1_sel:WORD_1\n\t" \
  "v_lshlrev_b32_sdwa v232, 1, v172 dst_sel:DWORD dst_unused:UNUSED_PAD src0_sel:DWORD src1_sel:WORD_1\n\t" \
  "v_mfma_f32_16x16x32_bf16 v[200:203], v[208:211], v[108:111], 0\n\t" \
  "ds_read_b64_tr_b16 v[208:209], v248 offset:49704\n\t" \
  "ds_read_b32 v228, v228 offset:10016\n\t" \
  "v_lshlrev_b32_sdwa v235, 1, v175 dst_sel:DWORD dst_unused:UNUSED_PAD src0_sel:DWORD src1_sel:WORD_1\n\t" \
  "s_waitcnt lgkmcnt(8)\n\t" \
  "v_mfma_f32_16x16x32_bf16 v[176:179], v[212:215], v[112:115], v[176:179]\n\t" \
  "ds_read_b64_tr_b16 v[210:211], v248 offset:50728\n\t" \
  "v_lshlrev_b32_sdwa v234, 1, v174 dst_sel:DWORD dst_unused:UNUSED_PAD src0_sel:DWORD src1_sel:WORD_1\n\t" \
  "v_fma_mix_f32 v221, |v149|, v221, v221 op_sel:[0,1,0] op_sel_hi:[0,1,1] clamp\n\t" \
  "v_mfma_f32_16x16x32_bf16 v[184:187], v[212:215], v[116:119], v[184:187]\n\t" \
  "ds_read_b32 v231, v231 offset:10016\n\t" \
  "ds_read_b32 v230, v230 offset:10016\n\t" \
  "v_fma_mix_f32 v220, |v148|, v220, v220 op_sel:[0,1,0] op_sel_hi:[0,1,1] clamp\n\t" \
  "v_mfma_f32_16x16x32_bf16 v[192:195], v[212:215], v[120:123], v[192:195]\n\t" \
  "ds_read_u16_d16_hi v233, v233 offset:10528\n\t" \
  "v_fma_mix_f32 v223, |v151|, v223, v223 op_sel:[0,1,0] op_sel_hi:[0,1,1] clamp\n\t" \
  "v_fma_mix_f32 v222, |v150|, v222, v222 op_sel:[0,1,0] op_sel_hi:[0,1,1] clamp\n\t" \
  "s_waitcnt lgkmcnt(7)\n\t" \
  "v_mfma_f32_16x16x32_bf16 v[200:203], v[212:215], v[124:127], v[200:203]\n\t" \
  "ds_read_b64_tr_b16 v[212:213], v248 offset:52776\n\t" \
  "v_or_b32 v156, v224, v225\n\t" \
  "v_or_b32 v157, v226, v227\n\t" \
  "v_mfma_f32_16x16x32_bf16 v[176:179], v[216:219], v[128:131], v[176:179]\n\t" \
  "ds_read_b64_tr_b16 v[214:215], v248 offset:53800\n\t" \
  "v_fma_f32 v149, |v149|, v221, v149 mul:4\n\t" \
  "v_fma_f32 v148, |v148|, v220, v148 mul:4\n\t" \
  "v_mfma_f32_16x16x32_bf16 v[184:187], v[216:219], v[132:135], v[184:187]\n\t" \
  "ds_read_u16 v232, v232 offset:10528\n\t" \
  "v_fma_f32 v151, |v151|, v223, v151 mul:4\n\t" \
  "v_fma_f32 v150, |v150|, v222, v150 mul:4\n\t" \
  "v_mfma_f32_16x16x32_bf16 v[192:195], v[216:219], v[136:139], v[192:195]\n\t" \
  "ds_read_u16_d16_hi v235, v235 offset:10528\n\t" \
  "v_cvt_pk_bf16_f32 v148, v148, v149\n\t" \
  "v_cvt_pk_bf16_f32 v149, v150, v151\n\t" \
  "v_mfma_f32_16x16x32_bf16 v[200:203], v[216:219], v[140:143], v[200:203]\n\t" \
  "ds_read_b64_tr_b16 v[216:217], v248 offset:51752\n\t" \
  "v_cvt_pknorm_i16_f32 v220, |v178|, |v179|\n\t" \
  "s_setprio 3\n\t" \
  "v_cvt_pknorm_i16_f32 v222, |v176|, |v177|\n\t" \
  "s_waitcnt lgkmcnt(8)\n\t" \
  "v_mfma_f32_16x16x32_bf16 v[180:183], v[208:211], v[96:99], 0\n\t" \
  "ds_read_b64_tr_b16 v[218:219], v248 offset:54824\n\t" \
  "v_lshlrev_b32_sdwa v221, 2, v220 dst_sel:DWORD dst_unused:UNUSED_PAD src0_sel:DWORD src1_sel:BYTE_3\n\t" \
  "s_waitcnt vmcnt(1)\n\t" \
  "v_lshlrev_b32_sdwa v220, 2, v220 dst_sel:DWORD dst_unused:UNUSED_PAD src0_sel:DWORD src1_sel:BYTE_1\n\t" \
  "v_mfma_f32_16x16x32_bf16 v[188:191], v[208:211], v[100:103], 0\n\t" \
  "s_mov_b64 exec, 3\n\t" \
  "ds_add_rtn_u32 v246, v251, v250 offset:47176\n\t" \
  "s_mov_b64 exec, -1\n\t" \
  "v_lshlrev_b32_sdwa v223, 2, v222 dst_sel:DWORD dst_unused:UNUSED_PAD src0_sel:DWORD src1_sel:BYTE_3\n\t" \
  "v_mfma_f32_16x16x32_bf16 v[196:199], v[208:211], v[104:107], 0\n\t" \
  "ds_read_u16 v234, v234 offset:10528\n\t" \
  "v_lshlrev_b32_sdwa v222, 2, v222 dst_sel:DWORD dst_unused:UNUSED_PAD src0_sel:DWORD src1_sel:BYTE_1\n\t" \
  "v_lshlrev_b32_sdwa v225, 1, v187 dst_sel:DWORD dst_unused:UNUSED_PAD src0_sel:DWORD src1_sel:WORD_1\n\t" \
  "v_mfma_f32_16x16x32_bf16 v[204:207], v[208:211], v[108:111], 0\n\t" \
  "ds_read_b32 v221, v221 offset:10016\n\t" \
  "v_lshlrev_b32_sdwa v224, 1, v186 dst_sel:DWORD dst_unused:UNUSED_PAD src0_sel:DWORD src1_sel:WORD_1\n\t" \
  "s_waitcnt lgkmcnt(2)\n\t" \
  "v_readfirstlane_b32 s4, v246\n\t" \
  "v_mfma_f32_16x16x32_bf16 v[180:183], v[212:215], v[112:115], v[180:183]\n\t" \
  "ds_read_b32 v220, v220 offset:10016\n\t" \
  "v_lshlrev_b32_sdwa v227, 1, v185 dst_sel:DWORD dst_unused:UNUSED_PAD src0_sel:DWORD src1_sel:WORD_1\n\t" \
  "s_cmp_lt_u32 s4, s57\n\t" \
  "s_cbranch_scc1 .Lcnx_f0_6\n.Lcnx_g0_6:\n\t" \
  "v_mfma_f32_16x16x32_bf16 v[188:191], v[212:215], v[116:119], v[188:191]\n\t" \
  "ds_read_b64_tr_b16 v[208:209], v249 offset:24600\n\t" \
  "ds_read_b32 v223, v223 offset:10016\n\t" \
  "v_lshlrev_b32_sdwa v226, 1, v184 dst_sel:DWORD dst_unused:UNUSED_PAD src0_sel:DWORD src1_sel:WORD_1\n\t" \
  "v_mfma_f32_16x16x32_bf16 v[196:199], v[212:215], v[120:123], v[196:199]\n\t" \
  "ds_read_b64_tr_b16 v[210:211], v249 offset:26136\n\t" \
  "v_fma_mix_f32 v229, |v165|, v229, v229 op_sel:[0,1,0] op_sel_hi:[0,1,1] clamp\n\t" \
  "v_fma_mix_f32 v228, |v164|, v228, v228 op_sel:[0,1,0] op_sel_hi:[0,1,1] clamp\n\t" \
  "v_mfma_f32_16x16x32_bf16 v[204:207], v[212:215], v[124:127], v[204:207]\n\t" \
  "ds_read_b64_tr_b16 v[212:213], v249 offset:24608\n\t" \
  "ds_read_b32 v222, v222 offset:10016\n\t" \
  "v_fma_mix_f32 v231, |v167|, v231, v231 op_sel:[0,1,0] op_sel_hi:[0,1,1] clamp\n\t" \
  "v_fma_mix_f32 v230, |v166|, v230, v230 op_sel:[0,1,0] op_sel_hi:[0,1,1] clamp\n\t" \
  "s_waitcnt lgkmcnt(7)\n\t" \
  "v_mfma_f32_16x16x32_bf16 v[180:183], v[216:219], v[128:131], v[180:183]\n\t" \
  "ds_read_b64_tr_b16 v[214:215], v249 offset:26144\n\t" \
  "v_or_b32 v172, v232, v233\n\t" \
  "v_or_b32 v173, v234, v235\n\t" \
  "v_mfma_f32_16x16x32_bf16 v[188:191], v[216:219], v[132:135], v[188:191]\n\t" \
  "ds_read_u16_d16_hi v225, v225 offset:10528\n\t" \
  "v_fma_f32 v165, |v165|, v229, v165 mul:4\n\t" \
  "v_fma_f32 v164, |v164|, v228, v164 mul:4\n\t" \
  "v_mfma_f32_16x16x32_bf16 v[196:199], v[216:219], v[136:139], v[196:199]\n\t" \
  "ds_read_u16 v224, v224 offset:10528\n\t" \
  "v_fma_f32 v167, |v167|, v231, v167 mul:4\n\t" \
  "v_fma_f32 v166, |v166|, v230, v166 mul:4\n\t" \
  "v_mfma_f32_16x16x32_bf16 v[204:207], v[216:219], v[140:143], v[204:207]\n\t" \
  "ds_read_b64_tr_b16 v[216:217], v249 offset:24664\n\t" \
  "v_cvt_pk_bf16_f32 v164, v164, v165\n\t" \
  "v_cvt_pk_bf16_f32 v165, v166, v167\n\t" \
  "v_mfma_f32_16x16x32_bf16 v[32:35], v[242:245], v[236:239], v[32:35]\n\t" \
  "ds_read_b64_tr_b16 v[218:219], v249 offset:26200\n\t" \
  "v_cvt_pknorm_i16_f32 v228, |v194|, |v195|\n\t" \
  "s_setprio 0\n\t" \
  "v_cvt_pknorm_i16_f32 v230, |v192|, |v193|\n\t" \
  "v_mfma_f32_16x16x32_bf16 v[36:39], v[240:243], v[236:239], v[36:39]\n\t" \
  "ds_read_u16_d16_hi v227, v227 offset:10528\n\t" \
  "buffer_load_dwordx4 v[236:239], v250, s[8:11], s29 offen offset:128 nt\n\t" \
  "v_lshlrev_b32_sdwa v229, 2, v228 dst_sel:DWORD dst_unused:UNUSED_PAD src0_sel:DWORD src1_sel:BYTE_3\n\t" \
  "s_waitcnt lgkmcnt(8)\n\t" \
  "v_mfma_f32_16x16x32_bf16 v[0:3], v[208:211], v[146:149], v[0:3]\n\t" \
  "ds_read_u16 v226, v226 offset:10528\n\t" \
  "v_lshlrev_b32_sdwa v228, 2, v228 dst_sel:DWORD dst_unused:UNUSED_PAD src0_sel:DWORD src1_sel:BYTE_1\n\t" \
  "s_add_u32 m0, s25, 32800\n\t" \
  "s_add_u32 s24, s24, s26\n\t" \
  "v_lshlrev_b32_sdwa v231, 2, v230 dst_sel:DWORD dst_unused:UNUSED_PAD src0_sel:DWORD src1_sel:BYTE_3\n\t" \
  "v_mfma_f32_16x16x32_bf16 v[24:27], v[208:211], v[154:157], v[24:27]\n\t" \
  "ds_read_b32 v229, v229 offset:10016\n\t" \
  "buffer_load_dwordx4 v254, s[8:11], s24 offen lds\n\t" \
  "v_lshlrev_b32_sdwa v230, 2, v230 dst_sel:DWORD dst_unused:UNUSED_PAD src0_sel:DWORD src1_sel:BYTE_1\n\t" \
  "v_mfma_f32_16x16x32_bf16 v[48:51], v[208:211], v[162:165], v[48:51]\n\t" \
  "ds_read_b32 v228, v228 offset:10016\n\t" \
  "v_lshlrev_b32_sdwa v233, 1, v203 dst_sel:DWORD dst_unused:UNUSED_PAD src0_sel:DWORD src1_sel:WORD_1\n\t" \
  "v_lshlrev_b32_sdwa v232, 1, v202 dst_sel:DWORD dst_unused:UNUSED_PAD src0_sel:DWORD src1_sel:WORD_1\n\t" \
  "v_mfma_f32_16x16x32_bf16 v[72:75], v[208:211], v[170:173], v[72:75]\n\t" \
  "ds_read_b64_tr_b16 v[208:209], v249 offset:24672\n\t" \
  "v_lshlrev_b32_sdwa v235, 1, v201 dst_sel:DWORD dst_unused:UNUSED_PAD src0_sel:DWORD src1_sel:WORD_1\n\t" \
  "s_cmp_eq_u32 s38, 7\n\t" \
  "s_cbranch_scc1 .Lcnx_h6\n.Lcnx_i6:\n\t" \
  "s_waitcnt lgkmcnt(9)\n\t" \
  "v_mfma_f32_16x16x32_bf16 v[4:7], v[212:215], v[146:149], v[4:7]\n\t" \
  "ds_read_b64_tr_b16 v[210:211], v249 offset:26208\n\t" \
  "v_lshlrev_b32_sdwa v234, 1, v200 dst_sel:DWORD dst_unused:UNUSED_PAD src0_sel:DWORD src1_sel:WORD_1\n\t" \
  "v_fma_mix_f32 v221, |v179|, v221, v221 op_sel:[0,1,0] op_sel_hi:[0,1,1] clamp\n\t" \
  "v_mfma_f32_16x16x32_bf16 v[28:31], v[212:215], v[154:157], v[28:31]\n\t" \
  "ds_read_b32 v231, v231 offset:10016\n\t" \
  "ds_read_b32 v230, v230 offset:10016\n\t" \
  "v_fma_mix_f32 v220, |v178|, v220, v220 op_sel:[0,1,0] op_sel_hi:[0,1,1] clamp\n\t" \
  "v_fma_mix_f32 v223, |v177|, v223, v223 op_sel:[0,1,0] op_sel_hi:[0,1,1] clamp\n\t" \
  "v_mfma_f32_16x16x32_bf16 v[52:55], v[212:215], v[162:165], v[52:55]\n\t" \
  "ds_read_u16_d16_hi v233, v233 offset:10528\n\t" \
  "v_fma_mix_f32 v222, |v176|, v222, v222 op_sel:[0,1,0] op_sel_hi:[0,1,1] clamp\n\t" \
  "s_waitcnt lgkmcnt(7)\n\t" \
  "v_or_b32 v187, v224, v225\n\t" \
  "v_mfma_f32_16x16x32_bf16 v[76:79], v[212:215], v[170:173], v[76:79]\n\t" \
  "ds_read_b64_tr_b16 v[212:213], v249 offset:24728\n\t" \
  "v_or_b32 v186, v226, v227\n\t" \
  "v_fma_f32 v179, |v179|, v221, v179 mul:4\n\t" \
  "v_mfma_f32_16x16x32_bf16 v[8:11], v[216:219], v[146:149], v[8:11]\n\t" \
  "ds_read_b64_tr_b16 v[214:215], v249 offset:26264\n\t" \
  "v_fma_f32 v178, |v178|, v220, v178 mul:4\n\t" \
  "v_fma_f32 v177, |v177|, v223, v177 mul:4\n\t" \
  "v_mfma_f32_16x16x32_bf16 v[32:35], v[216:219], v[154:157], v[32:35]\n\t" \
  "ds_read_u16 v232, v232 offset:10528\n\t" \
  "v_fma_f32 v176, |v176|, v222, v176 mul:4\n\t" \
  "v_cvt_pk_bf16_f32 v179, v178, v179\n\t" \
  "v_mfma_f32_16x16x32_bf16 v[56:59], v[216:219], v[162:165], v[56:59]\n\t" \
  "ds_read_u16_d16_hi v235, v235 offset:10528\n\t" \
  "v_cvt_pk_bf16_f32 v178, v176, v177\n\t" \
  "s_setprio 1\n\t" \
  "v_cvt_pknorm_i16_f32 v220, |v180|, |v181|\n\t" \
  "v_mfma_f32_16x16x32_bf16 v[80:83], v[216:219], v[170:173], v[80:83]\n\t" \
  "ds_read_b64_tr_b16 v[216:217], v249 offset:24736\n\t" \
  "v_cvt_pknorm_i16_f32 v222, |v182|, |v183|\n\t" \
  "v_lshlrev_b32_sdwa v221, 2, v220 dst_sel:DWORD dst_unused:UNUSED_PAD src0_sel:DWORD src1_sel:BYTE_3\n\t" \
  "s_waitcnt lgkmcnt(8)\n\t" \
  "v_mfma_f32_16x16x32_bf16 v[12:15], v[208:211], v[146:149], v[12:15]\n\t" \
  "ds_read_b64_tr_b16 v[218:219], v249 offset:26272\n\t" \
  "v_lshlrev_b32_sdwa v220, 2, v220 dst_sel:DWORD dst_unused:UNUSED_PAD src0_sel:DWORD src1_sel:BYTE_1\n\t" \
  "v_lshlrev_b32_sdwa v223, 2, v222 dst_sel:DWORD dst_unused:UNUSED_PAD src0_sel:DWORD src1_sel:BYTE_3\n\t" \
  "v_mfma_f32_16x16x32_bf16 v[36:39], v[208:211], v[154:157], v[36:39]\n\t" \
  "ds_read_u16 v234, v234 offset:10528\n\t" \
  "v_lshlrev_b32_sdwa v222, 2, v222 dst_sel:DWORD dst_unused:UNUSED_PAD src0_sel:DWORD src1_sel:BYTE_1\n\t" \
  "v_lshlrev_b32_sdwa v225, 1, v189 dst_sel:DWORD dst_unused:UNUSED_PAD src0_sel:DWORD src1_sel:WORD_1\n\t" \
  "v_mfma_f32_16x16x32_bf16 v[60:63], v[208:211], v[162:165], v[60:63]\n\t" \
  "ds_read_b32 v221, v221 offset:10016\n\t" \
  "v_lshlrev_b32_sdwa v224, 1, v188 dst_sel:DWORD dst_unused:UNUSED_PAD src0_sel:DWORD src1_sel:WORD_1\n\t" \
  "v_lshlrev_b32_sdwa v227, 1, v191 dst_sel:DWORD dst_unused:UNUSED_PAD src0_sel:DWORD src1_sel:WORD_1\n\t" \
  "v_mfma_f32_16x16x32_bf16 v[84:87], v[208:211], v[170:173], v[84:87]\n\t" \
  "ds_read_b64_tr_b16 v[208:209], v248 offset:36904\n\t" \
  "ds_read_b32 v220, v220 offset:10016\n\t" \
  "v_lshlrev_b32_sdwa v226, 1, v190 dst_sel:DWORD dst_unused:UNUSED_PAD src0_sel:DWORD src1_sel:WORD_1\n\t" \
  "s_waitcnt lgkmcnt(8)\n\t" \
  "v_mfma_f32_16x16x32_bf16 v[16:19], v[212:215], v[146:149], v[16:19]\n\t" \
  "ds_read_b64_tr_b16 v[210:211], v248 offset:37928\n\t" \
  "ds_read_b32 v223, v223 offset:10016\n\t" \
  "v_fma_mix_f32 v229, |v195|, v229, v229 op_sel:[0,1,0] op_sel_hi:[0,1,1] clamp\n\t" \
  "v_mfma_f32_16x16x32_bf16 v[40:43], v[212:215], v[154:157], v[40:43]\n\t" \
  "ds_read_b32 v222, v222 offset:10016\n\t" \
  "v_fma_mix_f32 v228, |v194|, v228, v228 op_sel:[0,1,0] op_sel_hi:[0,1,1] clamp\n\t" \
  "v_fma_mix_f32 v231, |v193|, v231, v231 op_sel:[0,1,0] op_sel_hi:[0,1,1] clamp\n\t" \
  "v_mfma_f32_16x16x32_bf16 v[64:67], v[212:215], v[162:165], v[64:67]\n\t" \
  "ds_read_u16_d16_hi v225, v225 offset:10528\n\t" \
  "v_fma_mix_f32 v230, |v192|, v230, v230 op_sel:[0,1,0] op_sel_hi:[0,1,1] clamp\n\t" \
  "s_waitcnt lgkmcnt(7)\n\t" \
  "v_or_b32 v203, v232, v233\n\t" \
  "v_mfma_f32_16x16x32_bf16 v[88:91], v[212:215], v[170:173], v[88:91]\n\t" \
  "ds_read_b64_tr_b16 v[212:213], v248 offset:39976\n\t" \
  "v_or_b32 v202, v234, v235\n\t" \
  "v_fma_f32 v195, |v195|, v229, v195 mul:4\n\t" \
  "v_mfma_f32_16x16x32_bf16 v[20:23], v[216:219], v[146:149], v[20:23]\n\t" \
  "ds_read_b64_tr_b16 v[214:215], v248 offset:41000\n\t" \
  "v_fma_f32 v194, |v194|, v228, v194 mul:4\n\t" \
  "v_fma_f32 v193, |v193|, v231, v193 mul:4\n\t" \
  "v_mfma_f32_16x16x32_bf16 v[44:47], v[216:219], v[154:157], v[44:47]\n\t" \
  "ds_read_u16 v224, v224 offset:10528\n\t" \
  "v_fma_f32 v192, |v192|, v230, v192 mul:4\n\t" \
  "v_cvt_pk_bf16_f32 v195, v194, v195\n\t" \
  "v_mfma_f32_16x16x32_bf16 v[68:71], v[216:219], v[162:165], v[68:71]\n\t" \
  "ds_read_u16_d16_hi v227, v227 offset:10528\n\t" \
  "v_cvt_pk_bf16_f32 v194, v192, v193\n\t" \
  "v_cvt_pknorm_i16_f32 v228, |v196|, |v197|\n\t" \
  "v_mfma_f32_16x16x32_bf16 v[92:95], v[216:219], v[170:173], v[92:95]\n\t" \
  "ds_read_b64_tr_b16 v[216:217], v248 offset:38952\n\t" \
  "v_cvt_pknorm_i16_f32 v230, |v198|, |v199|\n\t" \
  "s_setprio 2\n\t" \
  "v_lshlrev_b32_sdwa v229, 2, v228 dst_sel:DWORD dst_unused:UNUSED_PAD src0_sel:DWORD src1_sel:BYTE_3\n\t" \
  "s_waitcnt lgkmcnt(8)\n\t" \
  "v_mfma_f32_16x16x32_bf16 v[144:147], v[208:211], v[96:99], 0\n\t" \
  "ds_read_b64_tr_b16 v[218:219], v248 offset:42024\n\t" \
  "v_lshlrev_b32_sdwa v228, 2, v228 dst_sel:DWORD dst_unused:UNUSED_PAD src0_sel:DWORD src1_sel:BYTE_1\n\t" \
  "v_lshlrev_b32_sdwa v231, 2, v230 dst_sel:DWORD dst_unused:UNUSED_PAD src0_sel:DWORD src1_sel:BYTE_3\n\t" \
  "v_mfma_f32_16x16x32_bf16 v[152:155], v[208:211], v[100:103], 0\n\t" \
  "ds_read_u16 v226, v226 offset:10528\n\t" \
  "v_lshlrev_b32_sdwa v230, 2, v230 dst_sel:DWORD dst_unused:UNUSED_PAD src0_sel:DWORD src1_sel:BYTE_1\n\t" \
  "v_mfma_f32_16x16x32_bf16 v[160:163], v[208:211], v[104:107], 0\n\t" \
  "ds_read_b32 v229, v229 offset:10016\n\t" \
  "v_lshlrev_b32_sdwa v233, 1, v205 dst_sel:DWORD dst_unused:UNUSED_PAD src0_sel:DWORD src1_sel:WORD_1\n\t" \
  "v_lshlrev_b32_sdwa v232, 1, v204 dst_sel:DWORD dst_unused:UNUSED_PAD src0_sel:DWORD src1_sel:WORD_1\n\t" \
  "v_mfma_f32_16x16x32_bf16 v[168:171], v[208:211], v[108:111], 0\n\t" \
  "ds_read_b64_tr_b16 v[208:209], v248 offset:37416\n\t" \
  "ds_read_b32 v228, v228 offset:10016\n\t" \
  "v_lshlrev_b32_sdwa v235, 1, v207 dst_sel:DWORD dst_unused:UNUSED_PAD src0_sel:DWORD src1_sel:WORD_1\n\t" \
  "s_waitcnt lgkmcnt(8)\n\t" \
  "v_mfma_f32_16x16x32_bf16 v[144:147], v[212:215], v[112:115], v[144:147]\n\t" \
  "ds_read_b64_tr_b16 v[210:211], v248 offset:38440\n\t" \
  "v_lshlrev_b32_sdwa v234, 1, v206 dst_sel:DWORD dst_unused:UNUSED_PAD src0_sel:DWORD src1_sel:WORD_1\n\t" \
  "v_fma_mix_f32 v221, |v181|, v221, v221 op_sel:[0,1,0] op_sel_hi:[0,1,1] clamp\n\t" \
  "v_mfma_f32_16x16x32_bf16 v[152:155], v[212:215], v[116:119], v[152:155]\n\t" \
  "ds_read_b32 v231, v231 offset:10016\n\t" \
  "ds_read_b32 v230, v230 offset:10016\n\t" \
  "v_fma_mix_f32 v220, |v180|, v220, v220 op_sel:[0,1,0] op_sel_hi:[0,1,1] clamp\n\t" \
  "v_mfma_f32_16x16x32_bf16 v[160:163], v[212:215], v[120:123], v[160:163]\n\t" \
  "ds_read_u16_d16_hi v233, v233 offset:10528\n\t" \
  "v_fma_mix_f32 v223, |v183|, v223, v223 op_sel:[0,1,0] op_sel_hi:[0,1,1] clamp\n\t" \
  "v_fma_mix_f32 v222, |v182|, v222, v222 op_sel:[0,1,0] op_sel_hi:[0,1,1] clamp\n\t" \
  "s_waitcnt lgkmcnt(7)\n\t" \
  "v_mfma_f32_16x16x32_bf16 v[168:171], v[212:215], v[124:127], v[168:171]\n\t" \
  "ds_read_b64_tr_b16 v[212:213], v248 offset:40488\n\t" \
  "v_or_b32 v188, v224, v225\n\t" \
  "v_or_b32 v189, v226, v227\n\t" \
  "v_mfma_f32_16x16x32_bf16 v[144:147], v[216:219], v[128:131], v[144:147]\n\t" \
  "ds_read_b64_tr_b16 v[214:215], v248 offset:41512\n\t" \
  "v_fma_f32 v181, |v181|, v221, v181 mul:4\n\t" \
  "v_fma_f32 v180, |v180|, v220, v180 mul:4\n\t" \
  "v_mfma_f32_16x16x32_bf16 v[152:155], v[216:219], v[132:135], v[152:155]\n\t" \
  "ds_read_u16 v232, v232 offset:10528\n\t" \
  "v_fma_f32 v183, |v183|, v223, v183 mul:4\n\t" \
  "v_fma_f32 v182, |v182|, v222, v182 mul:4\n\t" \
  "v_mfma_f32_16x16x32_bf16 v[160:163], v[216:219], v[136:139], v[160:163]\n\t" \
  "ds_read_u16_d16_hi v235, v235 offset:10528\n\t" \
  "v_cvt_pk_bf16_f32 v180, v180, v181\n\t" \
  "v_cvt_pk_bf16_f32 v181, v182, v183\n\t" \
  "v_mfma_f32_16x16x32_bf16 v[168:171], v[216:219], v[140:143], v[168:171]\n\t" \
  "ds_read_b64_tr_b16 v[216:217], v248 offset:39464\n\t" \
  "v_cvt_pknorm_i16_f32 v220, |v146|, |v147|\n\t" \
  "s_setprio 3\n\t" \
  "v_cvt_pknorm_i16_f32 v222, |v144|, |v145|\n\t" \
  "s_waitcnt lgkmcnt(8)\n\t" \
  "v_mfma_f32_16x16x32_bf16 v[148:151], v[208:211], v[96:99], 0\n\t" \
  "ds_read_b64_tr_b16 v[218:219], v248 offset:42536\n\t" \
  "v_lshlrev_b32_sdwa v221, 2, v220 dst_sel:DWORD dst_unused:UNUSED_PAD src0_sel:DWORD src1_sel:BYTE_3\n\t" \
  "s_waitcnt vmcnt(1)\n\t" \
  "v_lshlrev_b32_sdwa v220, 2, v220 dst_sel:DWORD dst_unused:UNUSED_PAD src0_sel:DWORD src1_sel:BYTE_1\n\t" \
  "v_mfma_f32_16x16x32_bf16 v[156:159], v[208:211], v[100:103], 0\n\t" \
  "s_mov_b64 exec, 3\n\t" \
  "ds_add_rtn_u32 v246, v251, v250 offset:47184\n\t" \
  "s_mov_b64 exec, -1\n\t" \
  "v_lshlrev_b32_sdwa v223, 2, v222 dst_sel:DWORD dst_unused:UNUSED_PAD src0_sel:DWORD src1_sel:BYTE_3\n\t" \
  "v_mfma_f32_16x16x32_bf16 v[164:167], v[208:211], v[104:107], 0\n\t" \
  "ds_read_u16 v234, v234 offset:10528\n\t" \
  "v_lshlrev_b32_sdwa v222, 2, v222 dst_sel:DWORD dst_unused:UNUSED_PAD src0_sel:DWORD src1_sel:BYTE_1\n\t" \
  "v_lshlrev_b32_sdwa v225, 1, v155 dst_sel:DWORD dst_unused:UNUSED_PAD src0_sel:DWORD src1_sel:WORD_1\n\t" \
  "v_mfma_f32_16x16x32_bf16 v[172:175], v[208:211], v[108:111], 0\n\t" \
  "ds_read_b32 v221, v221 offset:10016\n\t" \
  "v_lshlrev_b32_sdwa v224, 1, v154 dst_sel:DWORD dst_unused:UNUSED_PAD src0_sel:DWORD src1_sel:WORD_1\n\t" \
  "s_waitcnt lgkmcnt(2)\n\t" \
  "v_readfirstlane_b32 s4, v246\n\t" \
  "v_mfma_f32_16x16x32_bf16 v[148:151], v[212:215], v[112:115], v[148:151]\n\t" \
  "ds_read_b32 v220, v220 offset:10016\n\t" \
  "v_lshlrev_b32_sdwa v227, 1, v153 dst_sel:DWORD dst_unused:UNUSED_PAD src0_sel:DWORD src1_sel:WORD_1\n\t" \
  "s_cmp_lt_u32 s4, s57\n\t" \
  "s_cbranch_scc1 .Lcnx_f0_7\n.Lcnx_g0_7:\n\t" \
  "v_mfma_f32_16x16x32_bf16 v[156:159], v[212:215], v[116:119], v[156:159]\n\t" \
  "ds_read_b64_tr_b16 v[208:209], v249 offset:30752\n\t" \
  "ds_read_b32 v223, v223 offset:10016\n\t" \
  "v_lshlrev_b32_sdwa v226, 1, v152 dst_sel:DWORD dst_unused:UNUSED_PAD src0_sel:DWORD src1_sel:WORD_1\n\t" \
  "v_mfma_f32_16x16x32_bf16 v[164:167], v[212:215], v[120:123], v[164:167]\n\t" \
  "ds_read_b64_tr_b16 v[210:211], v249 offset:32288\n\t" \
  "v_fma_mix_f32 v229, |v197|, v229, v229 op_sel:[0,1,0] op_sel_hi:[0,1,1] clamp\n\t" \
  "v_fma_mix_f32 v228, |v196|, v228, v228 op_sel:[0,1,0] op_sel_hi:[0,1,1] clamp\n\t" \
  "v_mfma_f32_16x16x32_bf16 v[172:175], v[212:215], v[124:127], v[172:175]\n\t" \
  "ds_read_b64_tr_b16 v[212:213], v249 offset:30760\n\t" \
  "ds_read_b32 v222, v222 offset:10016\n\t" \
  "v_fma_mix_f32 v231, |v199|, v231, v231 op_sel:[0,1,0] op_sel_hi:[0,1,1] clamp\n\t" \
  "v_fma_mix_f32 v230, |v198|, v230, v230 op_sel:[0,1,0] op_sel_hi:[0,1,1] clamp\n\t" \
  "s_waitcnt lgkmcnt(7)\n\t" \
  "v_mfma_f32_16x16x32_bf16 v[148:151], v[216:219], v[128:131], v[148:151]\n\t" \
  "ds_read_b64_tr_b16 v[214:215], v249 offset:32296\n\t" \
  "v_or_b32 v204, v232, v233\n\t" \
  "v_or_b32 v205, v234, v235\n\t" \
  "v_mfma_f32_16x16x32_bf16 v[156:159], v[216:219], v[132:135], v[156:159]\n\t" \
  "ds_read_u16_d16_hi v225, v225 offset:10528\n\t" \
  "v_fma_f32 v197, |v197|, v229, v197 mul:4\n\t" \
  "v_fma_f32 v196, |v196|, v228, v196 mul:4\n\t" \
  "v_mfma_f32_16x16x32_bf16 v[164:167], v[216:219], v[136:139], v[164:167]\n\t" \
  "ds_read_u16 v224, v224 offset:10528\n\t" \
  "v_fma_f32 v199, |v199|, v231, v199 mul:4\n\t" \
  "v_fma_f32 v198, |v198|, v230, v198 mul:4\n\t" \
  "v_mfma_f32_16x16x32_bf16 v[172:175], v[216:219], v[140:143], v[172:175]\n\t" \
  "ds_read_b64_tr_b16 v[216:217], v249 offset:30816\n\t" \
  "v_cvt_pk_bf16_f32 v196, v196, v197\n\t" \
  "v_cvt_pk_bf16_f32 v197, v198, v199\n\t" \
  "v_mfma_f32_16x16x32_bf16 v[40:43], v[242:245], v[236:239], v[40:43]\n\t" \
  "ds_read_b64_tr_b16 v[218:219], v249 offset:32352\n\t" \
  "v_cvt_pknorm_i16_f32 v228, |v162|, |v163|\n\t" \
  "s_setprio 0\n\t" \
  "v_cvt_pknorm_i16_f32 v230, |v160|, |v161|\n\t" \
  "v_mfma_f32_16x16x32_bf16 v[44:47], v[240:243], v[236:239], v[44:47]\n\t" \
  "ds_read_u16_d16_hi v227, v227 offset:10528\n\t" \
  "buffer_load_dwordx4 v[236:239], v250, s[8:11], s12 offen offset:0 nt\n\t" \
  "v_lshlrev_b32_sdwa v229, 2, v228 dst_sel:DWORD dst_unused:UNUSED_PAD src0_sel:DWORD src1_sel:BYTE_3\n\t" \
  "s_waitcnt lgkmcnt(8)\n\t" \
  "v_mfma_f32_16x16x32_bf16 v[0:3], v[208:211], v[178:181], v[0:3]\n\t" \
  "ds_read_u16 v226, v226 offset:10528\n\t" \
  "v_lshlrev_b32_sdwa v228, 2, v228 dst_sel:DWORD dst_unused:UNUSED_PAD src0_sel:DWORD src1_sel:BYTE_1\n\t" \
  "s_add_u32 m0, s25, 41000\n\t" \
  "s_add_u32 s24, s24, s26\n\t" \
  "v_lshlrev_b32_sdwa v231, 2, v230 dst_sel:DWORD dst_unused:UNUSED_PAD src0_sel:DWORD src1_sel:BYTE_3\n\t" \
  "v_mfma_f32_16x16x32_bf16 v[24:27], v[208:211], v[186:189], v[24:27]\n\t" \
  "ds_read_b32 v229, v229 offset:10016\n\t" \
  "buffer_load_dwordx4 v254, s[8:11], s24 offen lds\n\t" \
  "v_lshlrev_b32_sdwa v230, 2, v230 dst_sel:DWORD dst_unused:UNUSED_PAD src0_sel:DWORD src1_sel:BYTE_1\n\t" \
  "v_mfma_f32_16x16x32_bf16 v[48:51], v[208:211], v[194:197], v[48:51]\n\t" \
  "ds_read_b32 v228, v228 offset:10016\n\t" \
  "v_lshlrev_b32_sdwa v233, 1, v171 dst_sel:DWORD dst_unused:UNUSED_PAD src0_sel:DWORD src1_sel:WORD_1\n\t" \
  "v_lshlrev_b32_sdwa v232, 1, v170 dst_sel:DWORD dst_unused:UNUSED_PAD src0_sel:DWORD src1_sel:WORD_1\n\t" \
  "v_mfma_f32_16x16x32_bf16 v[72:75], v[208:211], v[202:205], v[72:75]\n\t" \
  "ds_read_b64_tr_b16 v[208:209], v249 offset:30824\n\t" \
  "v_lshlrev_b32_sdwa v235, 1, v169 dst_sel:DWORD dst_unused:UNUSED_PAD src0_sel:DWORD src1_sel:WORD_1\n\t" \
  "s_cmp_eq_u32 s38, 4\n\t" \
  "s_cbranch_scc1 .Lcnx_h7\n.Lcnx_i7:\n\t" \
  "s_waitcnt lgkmcnt(9)\n\t" \
  "v_mfma_f32_16x16x32_bf16 v[4:7], v[212:215], v[178:181], v[4:7]\n\t" \
  "ds_read_b64_tr_b16 v[210:211], v249 offset:32360\n\t" \
  "v_lshlrev_b32_sdwa v234, 1, v168 dst_sel:DWORD dst_unused:UNUSED_PAD src0_sel:DWORD src1_sel:WORD_1\n\t" \
  "v_fma_mix_f32 v221, |v147|, v221, v221 op_sel:[0,1,0] op_sel_hi:[0,1,1] clamp\n\t" \
  "v_mfma_f32_16x16x32_bf16 v[28:31], v[212:215], v[186:189], v[28:31]\n\t" \
  "ds_read_b32 v231, v231 offset:10016\n\t" \
  "ds_read_b32 v230, v230 offset:10016\n\t" \
  "v_fma_mix_f32 v220, |v146|, v220, v220 op_sel:[0,1,0] op_sel_hi:[0,1,1] clamp\n\t" \
  "v_fma_mix_f32 v223, |v145|, v223, v223 op_sel:[0,1,0] op_sel_hi:[0,1,1] clamp\n\t" \
  "v_mfma_f32_16x16x32_bf16 v[52:55], v[212:215], v[194:197], v[52:55]\n\t" \
  "ds_read_u16_d16_hi v233, v233 offset:10528\n\t" \
  "v_fma_mix_f32 v222, |v144|, v222, v222 op_sel:[0,1,0] op_sel_hi:[0,1,1] clamp\n\t" \
  "s_waitcnt lgkmcnt(7)\n\t" \
  "v_or_b32 v155, v224, v225\n\t" \
  "v_mfma_f32_16x16x32_bf16 v[76:79], v[212:215], v[202:205], v[76:79]\n\t" \
  "ds_read_b64_tr_b16 v[212:213], v249 offset:30880\n\t" \
  "v_or_b32 v154, v226, v227\n\t" \
  "v_fma_f32 v147, |v147|, v221, v147 mul:4\n\t" \
  "v_mfma_f32_16x16x32_bf16 v[8:11], v[216:219], v[178:181], v[8:11]\n\t" \
  "ds_read_b64_tr_b16 v[214:215], v249 offset:32416\n\t" \
  "v_fma_f32 v146, |v146|, v220, v146 mul:4\n\t" \
  "v_fma_f32 v145, |v145|, v223, v145 mul:4\n\t" \
  "v_mfma_f32_16x16x32_bf16 v[32:35], v[216:219], v[186:189], v[32:35]\n\t" \
  "ds_read_u16 v232, v232 offset:10528\n\t" \
  "v_fma_f32 v144, |v144|, v222, v144 mul:4\n\t" \
  "v_cvt_pk_bf16_f32 v147, v146, v147\n\t" \
  "v_mfma_f32_16x16x32_bf16 v[56:59], v[216:219], v[194:197], v[56:59]\n\t" \
  "ds_read_u16_d16_hi v235, v235 offset:10528\n\t" \
  "v_cvt_pk_bf16_f32 v146, v144, v145\n\t" \
  "s_setprio 1\n\t" \
  "v_cvt_pknorm_i16_f32 v220, |v148|, |v149|\n\t" \
  "v_mfma_f32_16x16x32_bf16 v[80:83], v[216:219], v[202:205], v[80:83]\n\t" \
  "ds_read_b64_tr_b16 v[216:217], v249 offset:30888\n\t" \
  "v_cvt_pknorm_i16_f32 v222, |v150|, |v151|\n\t" \
  "v_lshlrev_b32_sdwa v221, 2, v220 dst_sel:DWORD dst_unused:UNUSED_PAD src0_sel:DWORD src1_sel:BYTE_3\n\t" \
  "s_waitcnt lgkmcnt(8)\n\t" \
  "v_mfma_f32_16x16x32_bf16 v[12:15], v[208:211], v[178:181], v[12:15]\n\t" \
  "ds_read_b64_tr_b16 v[218:219], v249 offset:32424\n\t" \
  "v_lshlrev_b32_sdwa v220, 2, v220 dst_sel:DWORD dst_unused:UNUSED_PAD src0_sel:DWORD src1_sel:BYTE_1\n\t" \
  "v_lshlrev_b32_sdwa v223, 2, v222 dst_sel:DWORD dst_unused:UNUSED_PAD src0_sel:DWORD src1_sel:BYTE_3\n\t" \
  "v_mfma_f32_16x16x32_bf16 v[36:39], v[208:211], v[186:189], v[36:39]\n\t" \
  "ds_read_u16 v234, v234 offset:10528\n\t" \
  "v_lshlrev_b32_sdwa v222, 2, v222 dst_sel:DWORD dst_unused:UNUSED_PAD src0_sel:DWORD src1_sel:BYTE_1\n\t" \
  "v_lshlrev_b32_sdwa v225, 1, v157 dst_sel:DWORD dst_unused:UNUSED_PAD src0_sel:DWORD src1_sel:WORD_1\n\t" \
  "v_mfma_f32_16x16x32_bf16 v[60:63], v[208:211], v[194:197], v[60:63]\n\t" \
  "ds_read_b32 v221, v221 offset:10016\n\t" \
  "v_lshlrev_b32_sdwa v224, 1, v156 dst_sel:DWORD dst_unused:UNUSED_PAD src0_sel:DWORD src1_sel:WORD_1\n\t" \
  "v_lshlrev_b32_sdwa v227, 1, v159 dst_sel:DWORD dst_unused:UNUSED_PAD src0_sel:DWORD src1_sel:WORD_1\n\t" \
  "v_mfma_f32_16x16x32_bf16 v[84:87], v[208:211], v[202:205], v[84:87]\n\t" \
  "ds_read_b64_tr_b16 v[208:209], v248 offset:0\n\t" \
  "ds_read_b32 v220, v220 offset:10016\n\t" \
  "v_lshlrev_b32_sdwa v226, 1, v158 dst_sel:DWORD dst_unused:UNUSED_PAD src0_sel:DWORD src1_sel:WORD_1\n\t" \
  "s_waitcnt lgkmcnt(8)\n\t" \
  "v_mfma_f32_16x16x32_bf16 v[16:19], v[212:215], v[178:181], v[16:19]\n\t" \
  "ds_read_b64_tr_b16 v[210:211], v248 offset:1024\n\t" \
  "ds_read_b32 v223, v223 offset:10016\n\t" \
  "v_fma_mix_f32 v229, |v163|, v229, v229 op_sel:[0,1,0] op_sel_hi:[0,1,1] clamp\n\t" \
  "v_mfma_f32_16x16x32_bf16 v[40:43], v[212:215], v[186:189], v[40:43]\n\t" \
  "ds_read_b32 v222, v222 offset:10016\n\t" \
  "v_fma_mix_f32 v228, |v162|, v228, v228 op_sel:[0,1,0] op_sel_hi:[0,1,1] clamp\n\t" \
  "v_fma_mix_f32 v231, |v161|, v231, v231 op_sel:[0,1,0] op_sel_hi:[0,1,1] clamp\n\t" \
  "v_mfma_f32_16x16x32_bf16 v[64:67], v[212:215], v[194:197], v[64:67]\n\t" \
  "ds_read_u16_d16_hi v225, v225 offset:10528\n\t" \
  "v_fma_mix_f32 v230, |v160|, v230, v230 op_sel:[0,1,0] op_sel_hi:[0,1,1] clamp\n\t" \
  "s_waitcnt lgkmcnt(7)\n\t" \
  "v_or_b32 v171, v232, v233\n\t" \
  "v_mfma_f32_16x16x32_bf16 v[88:91], v[212:215], v[202:205], v[88:91]\n\t" \
  "ds_read_b64_tr_b16 v[212:213], v248 offset:3072\n\t" \
  "v_or_b32 v170, v234, v235\n\t" \
  "v_fma_f32 v163, |v163|, v229, v163 mul:4\n\t" \
  "v_mfma_f32_16x16x32_bf16 v[20:23], v[216:219], v[178:181], v[20:23]\n\t" \
  "ds_read_b64_tr_b16 v[214:215], v248 offset:4096\n\t" \
  "v_fma_f32 v162, |v162|, v228, v162 mul:4\n\t" \
  "v_fma_f32 v161, |v161|, v231, v161 mul:4\n\t" \
  "v_mfma_f32_16x16x32_bf16 v[44:47], v[216:219], v[186:189], v[44:47]\n\t" \
  "ds_read_u16 v224, v224 offset:10528\n\t" \
  "v_fma_f32 v160, |v160|, v230, v160 mul:4\n\t" \
  "v_cvt_pk_bf16_f32 v163, v162, v163\n\t" \
  "v_mfma_f32_16x16x32_bf16 v[68:71], v[216:219], v[194:197], v[68:71]\n\t" \
  "ds_read_u16_d16_hi v227, v227 offset:10528\n\t" \
  "v_cvt_pk_bf16_f32 v162, v160, v161\n\t" \
  "v_cvt_pknorm_i16_f32 v228, |v164|, |v165|\n\t" \
  "v_mfma_f32_16x16x32_bf16 v[92:95], v[216:219], v[202:205], v[92:95]\n\t" \
  "ds_read_b64_tr_b16 v[216:217], v248 offset:2048\n\t" \
  "v_cvt_pknorm_i16_f32 v230, |v166|, |v167|\n\t" \
  "s_setprio 2\n\t" \
  "v_lshlrev_b32_sdwa v229, 2, v228 dst_sel:DWORD dst_unused:UNUSED_PAD src0_sel:DWORD src1_sel:BYTE_3\n\t" \
  "s_waitcnt lgkmcnt(8)\n\t" \
  "v_mfma_f32_16x16x32_bf16 v[176:179], v[208:211], v[96:99], 0\n\t" \
  "ds_read_b64_tr_b16 v[218:219], v248 offset:5120\n\t" \
  "v_lshlrev_b32_sdwa v228, 2, v228 dst_sel:DWORD dst_unused:UNUSED_PAD src0_sel:DWORD src1_sel:BYTE_1\n\t" \
  "v_lshlrev_b32_sdwa v231, 2, v230 dst_sel:DWORD dst_unused:UNUSED_PAD src0_sel:DWORD src1_sel:BYTE_3\n\t" \
  "v_mfma_f32_16x16x32_bf16 v[184:187], v[208:211], v[100:103], 0\n\t" \
  "ds_read_u16 v226, v226 offset:10528\n\t" \
  "v_lshlrev_b32_sdwa v230, 2, v230 dst_sel:DWORD dst_unused:UNUSED_PAD src0_sel:DWORD src1_sel:BYTE_1\n\t" \
  "v_mfma_f32_16x16x32_bf16 v[192:195], v[208:211], v[104:107], 0\n\t" \
  "ds_read_b32 v229, v229 offset:10016\n\t" \
  "v_lshlrev_b32_sdwa v233, 1, v173 dst_sel:DWORD dst_unused:UNUSED_PAD src0_sel:DWORD src1_sel:WORD_1\n\t" \
  "v_lshlrev_b32_sdwa v232, 1, v172 dst_sel:DWORD dst_unused:UNUSED_PAD src0_sel:DWORD src1_sel:WORD_1\n\t" \
  "v_mfma_f32_16x16x32_bf16 v[200:203], v[208:211], v[108:111], 0\n\t" \
  "ds_read_b64_tr_b16 v[208:209], v248 offset:512\n\t" \
  "ds_read_b32 v228, v228 offset:10016\n\t" \
  "v_lshlrev_b32_sdwa v235, 1, v175 dst_sel:DWORD dst_unused:UNUSED_PAD src0_sel:DWORD src1_sel:WORD_1\n\t" \
  "s_waitcnt lgkmcnt(8)\n\t" \
  "v_mfma_f32_16x16x32_bf16 v[176:179], v[212:215], v[112:115], v[176:179]\n\t" \
  "ds_read_b64_tr_b16 v[210:211], v248 offset:1536\n\t" \
  "v_lshlrev_b32_sdwa v234, 1, v174 dst_sel:DWORD dst_unused:UNUSED_PAD src0_sel:DWORD src1_sel:WORD_1\n\t" \
  "v_fma_mix_f32 v221, |v149|, v221, v221 op_sel:[0,1,0] op_sel_hi:[0,1,1] clamp\n\t" \
  "v_mfma_f32_16x16x32_bf16 v[184:187], v[212:215], v[116:119], v[184:187]\n\t" \
  "ds_read_b32 v231, v231 offset:10016\n\t" \
  "ds_read_b32 v230, v230 offset:10016\n\t" \
  "v_fma_mix_f32 v220, |v148|, v220, v220 op_sel:[0,1,0] op_sel_hi:[0,1,1] clamp\n\t" \
  "v_mfma_f32_16x16x32_bf16 v[192:195], v[212:215], v[120:123], v[192:195]\n\t" \
  "ds_read_u16_d16_hi v233, v233 offset:10528\n\t" \
  "v_fma_mix_f32 v223, |v151|, v223, v223 op_sel:[0,1,0] op_sel_hi:[0,1,1] clamp\n\t" \
  "v_fma_mix_f32 v222, |v150|, v222, v222 op_sel:[0,1,0] op_sel_hi:[0,1,1] clamp\n\t" \
  "s_waitcnt lgkmcnt(7)\n\t" \
  "v_mfma_f32_16x16x32_bf16 v[200:203], v[212:215], v[124:127], v[200:203]\n\t" \
  "ds_read_b64_tr_b16 v[212:213], v248 offset:3584\n\t" \
  "v_or_b32 v156, v224, v225\n\t" \
  "v_or_b32 v157, v226, v227\n\t" \
  "v_mfma_f32_16x16x32_bf16 v[176:179], v[216:219], v[128:131], v[176:179]\n\t" \
  "ds_read_b64_tr_b16 v[214:215], v248 offset:4608\n\t" \
  "v_fma_f32 v149, |v149|, v221, v149 mul:4\n\t" \
  "v_fma_f32 v148, |v148|, v220, v148 mul:4\n\t" \
  "v_mfma_f32_16x16x32_bf16 v[184:187], v[216:219], v[132:135], v[184:187]\n\t" \
  "ds_read_u16 v232, v232 offset:10528\n\t" \
  "v_fma_f32 v151, |v151|, v223, v151 mul:4\n\t" \
  "v_fma_f32 v150, |v150|, v222, v150 mul:4\n\t" \
  "v_mfma_f32_16x16x32_bf16 v[192:195], v[216:219], v[136:139], v[192:195]\n\t" \
  "ds_read_u16_d16_hi v235, v235 offset:10528\n\t" \
  "v_cvt_pk_bf16_f32 v148, v148, v149\n\t" \
  "v_cvt_pk_bf16_f32 v149, v150, v151\n\t" \
  "v_mfma_f32_16x16x32_bf16 v[200:203], v[216:219], v[140:143], v[200:203]\n\t" \
  "ds_read_b64_tr_b16 v[216:217], v248 offset:2560\n\t" \
  "v_cvt_pknorm_i16_f32 v220, |v178|, |v179|\n\t" \
  "s_setprio 3\n\t" \
  "v_cvt_pknorm_i16_f32 v222, |v176|, |v177|\n\t" \
  "s_waitcnt lgkmcnt(8)\n\t" \
  "v_mfma_f32_16x16x32_bf16 v[180:183], v[208:211], v[96:99], 0\n\t" \
  "ds_read_b64_tr_b16 v[218:219], v248 offset:5632\n\t" \
  "v_lshlrev_b32_sdwa v221, 2, v220 dst_sel:DWORD dst_unused:UNUSED_PAD src0_sel:DWORD src1_sel:BYTE_3\n\t" \
  "s_waitcnt vmcnt(1)\n\t" \
  "v_lshlrev_b32_sdwa v220, 2, v220 dst_sel:DWORD dst_unused:UNUSED_PAD src0_sel:DWORD src1_sel:BYTE_1\n\t" \
  "v_mfma_f32_16x16x32_bf16 v[188:191], v[208:211], v[100:103], 0\n\t" \
  "s_mov_b64 exec, 3\n\t" \
  "ds_add_rtn_u32 v246, v251, v250 offset:47192\n\t" \
  "s_mov_b64 exec, -1\n\t" \
  "v_lshlrev_b32_sdwa v223, 2, v222 dst_sel:DWORD dst_unused:UNUSED_PAD src0_sel:DWORD src1_sel:BYTE_3\n\t" \
  "v_mfma_f32_16x16x32_bf16 v[196:199], v[208:211], v[104:107], 0\n\t" \
  "ds_read_u16 v234, v234 offset:10528\n\t" \
  "v_lshlrev_b32_sdwa v222, 2, v222 dst_sel:DWORD dst_unused:UNUSED_PAD src0_sel:DWORD src1_sel:BYTE_1\n\t" \
  "v_lshlrev_b32_sdwa v225, 1, v187 dst_sel:DWORD dst_unused:UNUSED_PAD src0_sel:DWORD src1_sel:WORD_1\n\t" \
  "v_mfma_f32_16x16x32_bf16 v[204:207], v[208:211], v[108:111], 0\n\t" \
  "ds_read_b32 v221, v221 offset:10016\n\t" \
  "v_lshlrev_b32_sdwa v224, 1, v186 dst_sel:DWORD dst_unused:UNUSED_PAD src0_sel:DWORD src1_sel:WORD_1\n\t" \
  "s_waitcnt lgkmcnt(2)\n\t" \
  "v_readfirstlane_b32 s4, v246\n\t" \
  "v_mfma_f32_16x16x32_bf16 v[180:183], v[212:215], v[112:115], v[180:183]\n\t" \
  "ds_read_b32 v220, v220 offset:10016\n\t" \
  "v_lshlrev_b32_sdwa v227, 1, v185 dst_sel:DWORD dst_unused:UNUSED_PAD src0_sel:DWORD src1_sel:WORD_1\n\t" \
  "s_cmp_lt_u32 s4, s57\n\t" \
  "s_cbranch_scc1 .Lcnx_f0_8\n.Lcnx_g0_8:\n\t" \
  "v_mfma_f32_16x16x32_bf16 v[188:191], v[212:215], v[116:119], v[188:191]\n\t" \
  "ds_read_b64_tr_b16 v[208:209], v249 offset:6152\n\t" \
  "ds_read_b32 v223, v223 offset:10016\n\t" \
  "v_lshlrev_b32_sdwa v226, 1, v184 dst_sel:DWORD dst_unused:UNUSED_PAD src0_sel:DWORD src1_sel:WORD_1\n\t" \
  "v_mfma_f32_16x16x32_bf16 v[196:199], v[212:215], v[120:123], v[196:199]\n\t" \
  "ds_read_b64_tr_b16 v[210:211], v249 offset:7688\n\t" \
  "v_fma_mix_f32 v229, |v165|, v229, v229 op_sel:[0,1,0] op_sel_hi:[0,1,1] clamp\n\t" \
  "v_fma_mix_f32 v228, |v164|, v228, v228 op_sel:[0,1,0] op_sel_hi:[0,1,1] clamp\n\t" \
  "v_mfma_f32_16x16x32_bf16 v[204:207], v[212:215], v[124:127], v[204:207]\n\t" \
  "ds_read_b64_tr_b16 v[212:213], v249 offset:6160\n\t" \
  "ds_read_b32 v222, v222 offset:10016\n\t" \
  "v_fma_mix_f32 v231, |v167|, v231, v231 op_sel:[0,1,0] op_sel_hi:[0,1,1] clamp\n\t" \
  "v_fma_mix_f32 v230, |v166|, v230, v230 op_sel:[0,1,0] op_sel_hi:[0,1,1] clamp\n\t" \
  "s_waitcnt lgkmcnt(7)\n\t" \
  "v_mfma_f32_16x16x32_bf16 v[180:183], v[216:219], v[128:131], v[180:183]\n\t" \
  "ds_read_b64_tr_b16 v[214:215], v249 offset:7696\n\t" \
  "v_or_b32 v172, v232, v233\n\t" \
  "v_or_b32 v173, v234, v235\n\t" \
  "v_mfma_f32_16x16x32_bf16 v[188:191], v[216:219], v[132:135], v[188:191]\n\t" \
  "ds_read_u16_d16_hi v225, v225 offset:10528\n\t" \
  "v_fma_f32 v165, |v165|, v229, v165 mul:4\n\t" \
  "v_fma_f32 v164, |v164|, v228, v164 mul:4\n\t" \
  "v_mfma_f32_16x16x32_bf16 v[196:199], v[216:219], v[136:139], v[196:199]\n\t" \
  "ds_read_u16 v224, v224 offset:10528\n\t" \
  "v_fma_f32 v167, |v167|, v231, v167 mul:4\n\t" \
  "v_fma_f32 v166, |v166|, v230, v166 mul:4\n\t" \
  "v_mfma_f32_16x16x32_bf16 v[204:207], v[216:219], v[140:143], v[204:207]\n\t" \
  "ds_read_b64_tr_b16 v[216:217], v249 offset:6216\n\t" \
  "v_cvt_pk_bf16_f32 v164, v164, v165\n\t" \
  "v_cvt_pk_bf16_f32 v165, v166, v167\n\t" \
  "v_mfma_f32_16x16x32_bf16 v[48:51], v[242:245], v[236:239], v[48:51]\n\t" \
  "ds_read_b64_tr_b16 v[218:219], v249 offset:7752\n\t" \
  "v_cvt_pknorm_i16_f32 v228, |v194|, |v195|\n\t" \
  "s_setprio 0\n\t" \
  "v_cvt_pknorm_i16_f32 v230, |v192|, |v193|\n\t" \
  "v_mfma_f32_16x16x32_bf16 v[52:55], v[240:243], v[236:239], v[52:55]\n\t" \
  "ds_read_u16_d16_hi v227, v227 offset:10528\n\t" \
  "buffer_load_dwordx4 v[236:239], v250, s[8:11], s12 offen offset:64 nt\n\t" \
  "v_lshlrev_b32_sdwa v229, 2, v228 dst_sel:DWORD dst_unused:UNUSED_PAD src0_sel:DWORD src1_sel:BYTE_3\n\t" \
  "s_waitcnt lgkmcnt(8)\n\t" \
  "v_mfma_f32_16x16x32_bf16 v[0:3], v[208:211], v[146:149], v[0:3]\n\t" \
  "ds_read_u16 v226, v226 offset:10528\n\t" \
  "v_lshlrev_b32_sdwa v228, 2, v228 dst_sel:DWORD dst_unused:UNUSED_PAD src0_sel:DWORD src1_sel:BYTE_1\n\t" \
  "s_add_u32 m0, s25, 56880\n\t" \
  "s_add_u32 s24, s24, s26\n\t" \
  "v_lshlrev_b32_sdwa v231, 2, v230 dst_sel:DWORD dst_unused:UNUSED_PAD src0_sel:DWORD src1_sel:BYTE_3\n\t" \
  "v_mfma_f32_16x16x32_bf16 v[24:27], v[208:211], v[154:157], v[24:27]\n\t" \
  "ds_read_b32 v229, v229 offset:10016\n\t" \
  "buffer_load_dwordx4 v254, s[8:11], s24 offen lds\n\t" \
  "v_lshlrev_b32_sdwa v230, 2, v230 dst_sel:DWORD dst_unused:UNUSED_PAD src0_sel:DWORD src1_sel:BYTE_1\n\t" \
  "v_mfma_f32_16x16x32_bf16 v[48:51], v[208:211], v[162:165], v[48:51]\n\t" \
  "ds_read_b32 v228, v228 offset:10016\n\t" \
  "v_lshlrev_b32_sdwa v233, 1, v203 dst_sel:DWORD dst_unused:UNUSED_PAD src0_sel:DWORD src1_sel:WORD_1\n\t" \
  "v_lshlrev_b32_sdwa v232, 1, v202 dst_sel:DWORD dst_unused:UNUSED_PAD src0_sel:DWORD src1_sel:WORD_1\n\t" \
  "v_mfma_f32_16x16x32_bf16 v[72:75], v[208:211], v[170:173], v[72:75]\n\t" \
  "ds_read_b64_tr_b16 v[208:209], v249 offset:6224\n\t" \
  "v_lshlrev_b32_sdwa v235, 1, v201 dst_sel:DWORD dst_unused:UNUSED_PAD src0_sel:DWORD src1_sel:WORD_1\n\t" \
  "s_cmp_eq_u32 s38, 5\n\t" \
  "s_cbranch_scc1 .Lcnx_h8\n.Lcnx_i8:\n\t" \
  "s_waitcnt lgkmcnt(9)\n\t" \
  "v_mfma_f32_16x16x32_bf16 v[4:7], v[212:215], v[146:149], v[4:7]\n\t" \
  "ds_read_b64_tr_b16 v[210:211], v249 offset:7760\n\t" \
  "v_lshlrev_b32_sdwa v234, 1, v200 dst_sel:DWORD dst_unused:UNUSED_PAD src0_sel:DWORD src1_sel:WORD_1\n\t" \
  "v_fma_mix_f32 v221, |v179|, v221, v221 op_sel:[0,1,0] op_sel_hi:[0,1,1] clamp\n\t" \
  "v_mfma_f32_16x16x32_bf16 v[28:31], v[212:215], v[154:157], v[28:31]\n\t" \
  "ds_read_b32 v231, v231 offset:10016\n\t" \
  "ds_read_b32 v230, v230 offset:10016\n\t" \
  "v_fma_mix_f32 v220, |v178|, v220, v220 op_sel:[0,1,0] op_sel_hi:[0,1,1] clamp\n\t" \
  "v_fma_mix_f32 v223, |v177|, v223, v223 op_sel:[0,1,0] op_sel_hi:[0,1,1] clamp\n\t" \
  "v_mfma_f32_16x16x32_bf16 v[52:55], v[212:215], v[162:165], v[52:55]\n\t" \
  "ds_read_u16_d16_hi v233, v233 offset:10528\n\t" \
  "v_fma_mix_f32 v222, |v176|, v222, v222 op_sel:[0,1,0] op_sel_hi:[0,1,1] clamp\n\t" \
  "s_waitcnt lgkmcnt(7)\n\t" \
  "v_or_b32 v187, v224, v225\n\t" \
  "v_mfma_f32_16x16x32_bf16 v[76:79], v[212:215], v[170:173], v[76:79]\n\t" \
  "ds_read_b64_tr_b16 v[212:213], v249 offset:6280\n\t" \
  "v_or_b32 v186, v226, v227\n\t" \
  "v_fma_f32 v179, |v179|, v221, v179 mul:4\n\t" \
  "v_mfma_f32_16x16x32_bf16 v[8:11], v[216:219], v[146:149], v[8:11]\n\t" \
  "ds_read_b64_tr_b16 v[214:215], v249 offset:7816\n\t" \
  "v_fma_f32 v178, |v178|, v220, v178 mul:4\n\t" \
  "v_fma_f32 v177, |v177|, v223, v177 mul:4\n\t" \
  "v_mfma_f32_16x16x32_bf16 v[32:35], v[216:219], v[154:157], v[32:35]\n\t" \
  "ds_read_u16 v232, v232 offset:10528\n\t" \
  "v_fma_f32 v176, |v176|, v222, v176 mul:4\n\t" \
  "v_cvt_pk_bf16_f32 v179, v178, v179\n\t" \
  "v_mfma_f32_16x16x32_bf16 v[56:59], v[216:219], v[162:165], v[56:59]\n\t" \
  "ds_read_u16_d16_hi v235, v235 offset:10528\n\t" \
  "v_cvt_pk_bf16_f32 v178, v176, v177\n\t" \
  "s_setprio 1\n\t" \
  "v_cvt_pknorm_i16_f32 v220, |v180|, |v181|\n\t" \
  "v_mfma_f32_16x16x32_bf16 v[80:83], v[216:219], v[170:173], v[80:83]\n\t" \
  "ds_read_b64_tr_b16 v[216:217], v249 offset:6288\n\t" \
  "v_cvt_pknorm_i16_f32 v222, |v182|, |v183|\n\t" \
  "v_lshlrev_b32_sdwa v221, 2, v220 dst_sel:DWORD dst_unused:UNUSED_PAD src0_sel:DWORD src1_sel:BYTE_3\n\t" \
  "s_waitcnt lgkmcnt(8)\n\t" \
  "v_mfma_f32_16x16x32_bf16 v[12:15], v[208:211], v[146:149], v[12:15]\n\t" \
  "ds_read_b64_tr_b16 v[218:219], v249 offset:7824\n\t" \
  "v_lshlrev_b32_sdwa v220, 2, v220 dst_sel:DWORD dst_unused:UNUSED_PAD src0_sel:DWORD src1_sel:BYTE_1\n\t" \
  "v_lshlrev_b32_sdwa v223, 2, v222 dst_sel:DWORD dst_unused:UNUSED_PAD src0_sel:DWORD src1_sel:BYTE_3\n\t" \
  "v_mfma_f32_16x16x32_bf16 v[36:39], v[208:211], v[154:157], v[36:39]\n\t" \
  "ds_read_u16 v234, v234 offset:10528\n\t" \
  "v_lshlrev_b32_sdwa v222, 2, v222 dst_sel:DWORD dst_unused:UNUSED_PAD src0_sel:DWORD src1_sel:BYTE_1\n\t" \
  "v_lshlrev_b32_sdwa v225, 1, v189 dst_sel:DWORD dst_unused:UNUSED_PAD src0_sel:DWORD src1_sel:WORD_1\n\t" \
  "v_mfma_f32_16x16x32_bf16 v[60:63], v[208:211], v[162:165], v[60:63]\n\t" \
  "ds_read_b32 v221, v221 offset:10016\n\t" \
  "v_lshlrev_b32_sdwa v224, 1, v188 dst_sel:DWORD dst_unused:UNUSED_PAD src0_sel:DWORD src1_sel:WORD_1\n\t" \
  "v_lshlrev_b32_sdwa v227, 1, v191 dst_sel:DWORD dst_unused:UNUSED_PAD src0_sel:DWORD src1_sel:WORD_1\n\t" \
  "v_mfma_f32_16x16x32_bf16 v[84:87], v[208:211], v[170:173], v[84:87]\n\t" \
  "ds_read_b64_tr_b16 v[208:209], v248 offset:43048\n\t" \
  "ds_read_b32 v220, v220 offset:10016\n\t" \
  "v_lshlrev_b32_sdwa v226, 1, v190 dst_sel:DWORD dst_unused:UNUSED_PAD src0_sel:DWORD src1_sel:WORD_1\n\t" \
  "s_waitcnt lgkmcnt(8)\n\t" \
  "v_mfma_f32_16x16x32_bf16 v[16:19], v[212:215], v[146:149], v[16:19]\n\t" \
  "ds_read_b64_tr_b16 v[210:211], v248 offset:44072\n\t" \
  "ds_read_b32 v223, v223 offset:10016\n\t" \
  "v_fma_mix_f32 v229, |v195|, v229, v229 op_sel:[0,1,0] op_sel_hi:[0,1,1] clamp\n\t" \
  "v_mfma_f32_16x16x32_bf16 v[40:43], v[212:215], v[154:157], v[40:43]\n\t" \
  "ds_read_b32 v222, v222 offset:10016\n\t" \
  "v_fma_mix_f32 v228, |v194|, v228, v228 op_sel:[0,1,0] op_sel_hi:[0,1,1] clamp\n\t" \
  "v_fma_mix_f32 v231, |v193|, v231, v231 op_sel:[0,1,0] op_sel_hi:[0,1,1] clamp\n\t" \
  "v_mfma_f32_16x16x32_bf16 v[64:67], v[212:215], v[162:165], v[64:67]\n\t" \
  "ds_read_u16_d16_hi v225, v225 offset:10528\n\t" \
  "v_fma_mix_f32 v230, |v192|, v230, v230 op_sel:[0,1,0] op_sel_hi:[0,1,1] clamp\n\t" \
  "s_waitcnt lgkmcnt(7)\n\t" \
  "v_or_b32 v203, v232, v233\n\t" \
  "v_mfma_f32_16x16x32_bf16 v[88:91], v[212:215], v[170:173], v[88:91]\n\t" \
  "ds_read_b64_tr_b16 v[212:213], v248 offset:46120\n\t" \
  "v_or_b32 v202, v234, v235\n\t" \
  "v_fma_f32 v195, |v195|, v229, v195 mul:4\n\t" \
  "v_mfma_f32_16x16x32_bf16 v[20:23], v[216:219], v[146:149], v[20:23]\n\t" \
  "ds_read_b64_tr_b16 v[214:215], v248 offset:47144\n\t" \
  "v_fma_f32 v194, |v194|, v228, v194 mul:4\n\t" \
  "v_fma_f32 v193, |v193|, v231, v193 mul:4\n\t" \
  "v_mfma_f32_16x16x32_bf16 v[44:47], v[216:219], v[154:157], v[44:47]\n\t" \
  "ds_read_u16 v224, v224 offset:10528\n\t" \
  "v_fma_f32 v192, |v192|, v230, v192 mul:4\n\t" \
  "v_cvt_pk_bf16_f32 v195, v194, v195\n\t" \
  "v_mfma_f32_16x16x32_bf16 v[68:71], v[216:219], v[162:165], v[68:71]\n\t" \
  "ds_read_u16_d16_hi v227, v227 offset:10528\n\t" \
  "v_cvt_pk_bf16_f32 v194, v192, v193\n\t" \
  "v_cvt_pknorm_i16_f32 v228, |v196|, |v197|\n\t" \
  "v_mfma_f32_16x16x32_bf16 v[92:95], v[216:219], v[170:173], v[92:95]\n\t" \
  "ds_read_b64_tr_b16 v[216:217], v248 offset:45096\n\t" \
  "v_cvt_pknorm_i16_f32 v230, |v198|, |v199|\n\t" \
  "s_setprio 2\n\t" \
  "v_lshlrev_b32_sdwa v229, 2, v228 dst_sel:DWORD dst_unused:UNUSED_PAD src0_sel:DWORD src1_sel:BYTE_3\n\t" \
  "s_waitcnt lgkmcnt(8)\n\t" \
  "v_mfma_f32_16x16x32_bf16 v[144:147], v[208:211], v[96:99], 0\n\t" \
  "ds_read_b64_tr_b16 v[218:219], v248 offset:48168\n\t" \
  "v_lshlrev_b32_sdwa v228, 2, v228 dst_sel:DWORD dst_unused:UNUSED_PAD src0_sel:DWORD src1_sel:BYTE_1\n\t" \
  "v_lshlrev_b32_sdwa v231, 2, v230 dst_sel:DWORD dst_unused:UNUSED_PAD src0_sel:DWORD src1_sel:BYTE_3\n\t" \
  "v_mfma_f32_16x16x32_bf16 v[152:155], v[208:211], v[100:103], 0\n\t" \
  "ds_read_u16 v226, v226 offset:10528\n\t" \
  "v_lshlrev_b32_sdwa v230, 2, v230 dst_sel:DWORD dst_unused:UNUSED_PAD src0_sel:DWORD src1_sel:BYTE_1\n\t" \
  "v_mfma_f32_16x16x32_bf16 v[160:163], v[208:211], v[104:107], 0\n\t" \
  "ds_read_b32 v229, v229 offset:10016\n\t" \
  "v_lshlrev_b32_sdwa v233, 1, v205 dst_sel:DWORD dst_unused:UNUSED_PAD src0_sel:DWORD src1_sel:WORD_1\n\t" \
  "v_lshlrev_b32_sdwa v232, 1, v204 dst_sel:DWORD dst_unused:UNUSED_PAD src0_sel:DWORD src1_sel:WORD_1\n\t" \
  "v_mfma_f32_16x16x32_bf16 v[168:171], v[208:211], v[108:111], 0\n\t" \
  "ds_read_b64_tr_b16 v[208:209], v248 offset:43560\n\t" \
  "ds_read_b32 v228, v228 offset:10016\n\t" \
  "v_lshlrev_b32_sdwa v235, 1, v207 dst_sel:DWORD dst_unused:UNUSED_PAD src0_sel:DWORD src1_sel:WORD_1\n\t" \
  "s_waitcnt lgkmcnt(8)\n\t" \
  "v_mfma_f32_16x16x32_bf16 v[144:147], v[212:215], v[112:115], v[144:147]\n\t" \
  "ds_read_b64_tr_b16 v[210:211], v248 offset:44584\n\t" \
  "v_lshlrev_b32_sdwa v234, 1, v206 dst_sel:DWORD dst_unused:UNUSED_PAD src0_sel:DWORD src1_sel:WORD_1\n\t" \
  "v_fma_mix_f32 v221, |v181|, v221, v221 op_sel:[0,1,0] op_sel_hi:[0,1,1] clamp\n\t" \
  "v_mfma_f32_16x16x32_bf16 v[152:155], v[212:215], v[116:119], v[152:155]\n\t" \
  "ds_read_b32 v231, v231 offset:10016\n\t" \
  "ds_read_b32 v230, v230 offset:10016\n\t" \
  "v_fma_mix_f32 v220, |v180|, v220, v220 op_sel:[0,1,0] op_sel_hi:[0,1,1] clamp\n\t" \
  "v_mfma_f32_16x16x32_bf16 v[160:163], v[212:215], v[120:123], v[160:163]\n\t" \
  "ds_read_u16_d16_hi v233, v233 offset:10528\n\t" \
  "v_fma_mix_f32 v223, |v183|, v223, v223 op_sel:[0,1,0] op_sel_hi:[0,1,1] clamp\n\t" \
  "v_fma_mix_f32 v222, |v182|, v222, v222 op_sel:[0,1,0] op_sel_hi:[0,1,1] clamp\n\t" \
  "s_waitcnt lgkmcnt(7)\n\t" \
  "v_mfma_f32_16x16x32_bf16 v[168:171], v[212:215], v[124:127], v[168:171]\n\t" \
  "ds_read_b64_tr_b16 v[212:213], v248 offset:46632\n\t" \
  "v_or_b32 v188, v224, v225\n\t" \
  "v_or_b32 v189, v226, v227\n\t" \
  "v_mfma_f32_16x16x32_bf16 v[144:147], v[216:219], v[128:131], v[144:147]\n\t" \
  "ds_read_b64_tr_b16 v[214:215], v248 offset:47656\n\t" \
  "v_fma_f32 v181, |v181|, v221, v181 mul:4\n\t" \
  "v_fma_f32 v180, |v180|, v220, v180 mul:4\n\t" \
  "v_mfma_f32_16x16x32_bf16 v[152:155], v[216:219], v[132:135], v[152:155]\n\t" \
  "ds_read_u16 v232, v232 offset:10528\n\t" \
  "v_fma_f32 v183, |v183|, v223, v183 mul:4\n\t" \
  "v_fma_f32 v182, |v182|, v222, v182 mul:4\n\t" \
  "v_mfma_f32_16x16x32_bf16 v[160:163], v[216:219], v[136:139], v[160:163]\n\t" \
  "ds_read_u16_d16_hi v235, v235 offset:10528\n\t" \
  "v_cvt_pk_bf16_f32 v180, v180, v181\n\t" \
  "v_cvt_pk_bf16_f32 v181, v182, v183\n\t" \
  "v_mfma_f32_16x16x32_bf16 v[168:171], v[216:219], v[140:143], v[168:171]\n\t" \
  "ds_read_b64_tr_b16 v[216:217], v248 offset:45608\n\t" \
  "v_cvt_pknorm_i16_f32 v220, |v146|, |v147|\n\t" \
  "s_setprio 3\n\t" \
  "v_cvt_pknorm_i16_f32 v222, |v144|, |v145|\n\t" \
  "s_waitcnt lgkmcnt(8)\n\t" \
  "v_mfma_f32_16x16x32_bf16 v[148:151], v[208:211], v[96:99], 0\n\t" \
  "ds_read_b64_tr_b16 v[218:219], v248 offset:48680\n\t" \
  "v_lshlrev_b32_sdwa v221, 2, v220 dst_sel:DWORD dst_unused:UNUSED_PAD src0_sel:DWORD src1_sel:BYTE_3\n\t" \
  "s_waitcnt vmcnt(1)\n\t" \
  "v_lshlrev_b32_sdwa v220, 2, v220 dst_sel:DWORD dst_unused:UNUSED_PAD src0_sel:DWORD src1_sel:BYTE_1\n\t" \
  "v_mfma_f32_16x16x32_bf16 v[156:159], v[208:211], v[100:103], 0\n\t" \
  "s_mov_b64 exec, 3\n\t" \
  "ds_add_rtn_u32 v246, v251, v250 offset:47200\n\t" \
  "s_mov_b64 exec, -1\n\t" \
  "v_lshlrev_b32_sdwa v223, 2, v222 dst_sel:DWORD dst_unused:UNUSED_PAD src0_sel:DWORD src1_sel:BYTE_3\n\t" \
  "v_mfma_f32_16x16x32_bf16 v[164:167], v[208:211], v[104:107], 0\n\t" \
  "ds_read_u16 v234, v234 offset:10528\n\t" \
  "v_lshlrev_b32_sdwa v222, 2, v222 dst_sel:DWORD dst_unused:UNUSED_PAD src0_sel:DWORD src1_sel:BYTE_1\n\t" \
  "v_lshlrev_b32_sdwa v225, 1, v155 dst_sel:DWORD dst_unused:UNUSED_PAD src0_sel:DWORD src1_sel:WORD_1\n\t" \
  "v_mfma_f32_16x16x32_bf16 v[172:175], v[208:211], v[108:111], 0\n\t" \
  "ds_read_b32 v221, v221 offset:10016\n\t" \
  "v_lshlrev_b32_sdwa v224, 1, v154 dst_sel:DWORD dst_unused:UNUSED_PAD src0_sel:DWORD src1_sel:WORD_1\n\t" \
  "s_waitcnt lgkmcnt(2)\n\t" \
  "v_readfirstlane_b32 s4, v246\n\t" \
  "v_mfma_f32_16x16x32_bf16 v[148:151], v[212:215], v[112:115], v[148:151]\n\t" \
  "ds_read_b32 v220, v220 offset:10016\n\t" \
  "v_lshlrev_b32_sdwa v227, 1, v153 dst_sel:DWORD dst_unused:UNUSED_PAD src0_sel:DWORD src1_sel:WORD_1\n\t" \
  "s_cmp_lt_u32 s4, s57\n\t" \
  "s_cbranch_scc1 .Lcnx_f0_9\n.Lcnx_g0_9:\n\t" \
  "v_mfma_f32_16x16x32_bf16 v[156:159], v[212:215], v[116:119], v[156:159]\n\t" \
  "ds_read_b64_tr_b16 v[208:209], v249 offset:12304\n\t" \
  "ds_read_b32 v223, v223 offset:10016\n\t" \
  "v_lshlrev_b32_sdwa v226, 1, v152 dst_sel:DWORD dst_unused:UNUSED_PAD src0_sel:DWORD src1_sel:WORD_1\n\t" \
  "v_mfma_f32_16x16x32_bf16 v[164:167], v[212:215], v[120:123], v[164:167]\n\t" \
  "ds_read_b64_tr_b16 v[210:211], v249 offset:13840\n\t" \
  "v_fma_mix_f32 v229, |v197|, v229, v229 op_sel:[0,1,0] op_sel_hi:[0,1,1] clamp\n\t" \
  "v_fma_mix_f32 v228, |v196|, v228, v228 op_sel:[0,1,0] op_sel_hi:[0,1,1] clamp\n\t" \
  "v_mfma_f32_16x16x32_bf16 v[172:175], v[212:215], v[124:127], v[172:175]\n\t" \
  "ds_read_b64_tr_b16 v[212:213], v249 offset:12312\n\t" \
  "ds_read_b32 v222, v222 offset:10016\n\t" \
  "v_fma_mix_f32 v231, |v199|, v231, v231 op_sel:[0,1,0] op_sel_hi:[0,1,1] clamp\n\t" \
  "v_fma_mix_f32 v230, |v198|, v230, v230 op_sel:[0,1,0] op_sel_hi:[0,1,1] clamp\n\t" \
  "s_waitcnt lgkmcnt(7)\n\t" \
  "v_mfma_f32_16x16x32_bf16 v[148:151], v[216:219], v[128:131], v[148:151]\n\t" \
  "ds_read_b64_tr_b16 v[214:215], v249 offset:13848\n\t" \
  "v_or_b32 v204, v232, v233\n\t" \
  "v_or_b32 v205, v234, v235\n\t" \
  "v_mfma_f32_16x16x32_bf16 v[156:159], v[216:219], v[132:135], v[156:159]\n\t" \
  "ds_read_u16_d16_hi v225, v225 offset:10528\n\t" \
  "v_fma_f32 v197, |v197|, v229, v197 mul:4\n\t" \
  "v_fma_f32 v196, |v196|, v228, v196 mul:4\n\t" \
  "v_mfma_f32_16x16x32_bf16 v[164:167], v[216:219], v[136:139], v[164:167]\n\t" \
  "ds_read_u16 v224, v224 offset:10528\n\t" \
  "v_fma_f32 v199, |v199|, v231, v199 mul:4\n\t" \
  "v_fma_f32 v198, |v198|, v230, v198 mul:4\n\t" \
  "v_mfma_f32_16x16x32_bf16 v[172:175], v[216:219], v[140:143], v[172:175]\n\t" \
  "ds_read_b64_tr_b16 v[216:217], v249 offset:12368\n\t" \
  "v_cvt_pk_bf16_f32 v196, v196, v197\n\t" \
  "v_cvt_pk_bf16_f32 v197, v198, v199\n\t" \
  "v_mfma_f32_16x16x32_bf16 v[56:59], v[242:245], v[236:239], v[56:59]\n\t" \
  "ds_read_b64_tr_b16 v[218:219], v249 offset:13904\n\t" \
  "v_cvt_pknorm_i16_f32 v228, |v162|, |v163|\n\t" \
  "s_setprio 0\n\t" \
  "v_cvt_pknorm_i16_f32 v230, |v160|, |v161|\n\t" \
  "v_mfma_f32_16x16x32_bf16 v[60:63], v[240:243], v[236:239], v[60:63]\n\t" \
  "ds_read_u16_d16_hi v227, v227 offset:10528\n\t" \
  "buffer_load_dwordx4 v[236:239], v250, s[8:11], s12 offen offset:128 nt\n\t" \
  "v_lshlrev_b32_sdwa v229, 2, v228 dst_sel:DWORD dst_unused:UNUSED_PAD src0_sel:DWORD src1_sel:BYTE_3\n\t" \
  "s_waitcnt lgkmcnt(8)\n\t" \
  "v_mfma_f32_16x16x32_bf16 v[0:3], v[208:211], v[178:181], v[0:3]\n\t" \
  "ds_read_u16 v226, v226 offset:10528\n\t" \
  "v_lshlrev_b32_sdwa v228, 2, v228 dst_sel:DWORD dst_unused:UNUSED_PAD src0_sel:DWORD src1_sel:BYTE_1\n\t" \
  "s_add_u32 m0, s25, 65080\n\t" \
  "s_add_u32 s24, s24, s26\n\t" \
  "v_lshlrev_b32_sdwa v231, 2, v230 dst_sel:DWORD dst_unused:UNUSED_PAD src0_sel:DWORD src1_sel:BYTE_3\n\t" \
  "v_mfma_f32_16x16x32_bf16 v[24:27], v[208:211], v[186:189], v[24:27]\n\t" \
  "ds_read_b32 v229, v229 offset:10016\n\t" \
  "buffer_load_dwordx4 v254, s[8:11], s24 offen lds\n\t" \
  "v_lshlrev_b32_sdwa v230, 2, v230 dst_sel:DWORD dst_unused:UNUSED_PAD src0_sel:DWORD src1_sel:BYTE_1\n\t" \
  "v_mfma_f32_16x16x32_bf16 v[48:51], v[208:211], v[194:197], v[48:51]\n\t" \
  "ds_read_b32 v228, v228 offset:10016\n\t" \
  "v_lshlrev_b32_sdwa v233, 1, v171 dst_sel:DWORD dst_unused:UNUSED_PAD src0_sel:DWORD src1_sel:WORD_1\n\t" \
  "v_lshlrev_b32_sdwa v232, 1, v170 dst_sel:DWORD dst_unused:UNUSED_PAD src0_sel:DWORD src1_sel:WORD_1\n\t" \
  "v_mfma_f32_16x16x32_bf16 v[72:75], v[208:211], v[202:205], v[72:75]\n\t" \
  "ds_read_b64_tr_b16 v[208:209], v249 offset:12376\n\t" \
  "v_lshlrev_b32_sdwa v235, 1, v169 dst_sel:DWORD dst_unused:UNUSED_PAD src0_sel:DWORD src1_sel:WORD_1\n\t" \
  "s_cmp_eq_u32 s38, 6\n\t" \
  "s_cbranch_scc1 .Lcnx_h9\n.Lcnx_i9:\n\t" \
  "s_waitcnt lgkmcnt(9)\n\t" \
  "v_mfma_f32_16x16x32_bf16 v[4:7], v[212:215], v[178:181], v[4:7]\n\t" \
  "ds_read_b64_tr_b16 v[210:211], v249 offset:13912\n\t" \
  "v_lshlrev_b32_sdwa v234, 1, v168 dst_sel:DWORD dst_unused:UNUSED_PAD src0_sel:DWORD src1_sel:WORD_1\n\t" \
  "v_fma_mix_f32 v221, |v147|, v221, v221 op_sel:[0,1,0] op_sel_hi:[0,1,1] clamp\n\t" \
  "v_mfma_f32_16x16x32_bf16 v[28:31], v[212:215], v[186:189], v[28:31]\n\t" \
  "ds_read_b32 v231, v231 offset:10016\n\t" \
  "ds_read_b32 v230, v230 offset:10016\n\t" \
  "v_fma_mix_f32 v220, |v146|, v220, v220 op_sel:[0,1,0] op_sel_hi:[0,1,1] clamp\n\t" \
  "v_fma_mix_f32 v223, |v145|, v223, v223 op_sel:[0,1,0] op_sel_hi:[0,1,1] clamp\n\t" \
  "v_mfma_f32_16x16x32_bf16 v[52:55], v[212:215], v[194:197], v[52:55]\n\t" \
  "ds_read_u16_d16_hi v233, v233 offset:10528\n\t" \
  "v_fma_mix_f32 v222, |v144|, v222, v222 op_sel:[0,1,0] op_sel_hi:[0,1,1] clamp\n\t" \
  "s_waitcnt lgkmcnt(7)\n\t" \
  "v_or_b32 v155, v224, v225\n\t" \
  "v_mfma_f32_16x16x32_bf16 v[76:79], v[212:215], v[202:205], v[76:79]\n\t" \
  "ds_read_b64_tr_b16 v[212:213], v249 offset:12432\n\t" \
  "v_or_b32 v154, v226, v227\n\t" \
  "v_fma_f32 v147, |v147|, v221, v147 mul:4\n\t" \
  "v_mfma_f32_16x16x32_bf16 v[8:11], v[216:219], v[178:181], v[8:11]\n\t" \
  "ds_read_b64_tr_b16 v[214:215], v249 offset:13968\n\t" \
  "v_fma_f32 v146, |v146|, v220, v146 mul:4\n\t" \
  "v_fma_f32 v145, |v145|, v223, v145 mul:4\n\t" \
  "v_mfma_f32_16x16x32_bf16 v[32:35], v[216:219], v[186:189], v[32:35]\n\t" \
  "ds_read_u16 v232, v232 offset:10528\n\t" \
  "v_fma_f32 v144, |v144|, v222, v144 mul:4\n\t" \
  "v_cvt_pk_bf16_f32 v147, v146, v147\n\t" \
  "v_mfma_f32_16x16x32_bf16 v[56:59], v[216:219], v[194:197], v[56:59]\n\t" \
  "ds_read_u16_d16_hi v235, v235 offset:10528\n\t" \
  "v_cvt_pk_bf16_f32 v146, v144, v145\n\t" \
  "s_setprio 1\n\t" \
  "v_cvt_pknorm_i16_f32 v220, |v148|, |v149|\n\t" \
  "v_mfma_f32_16x16x32_bf16 v[80:83], v[216:219], v[202:205], v[80:83]\n\t" \
  "ds_read_b64_tr_b16 v[216:217], v249 offset:12440\n\t" \
  "v_cvt_pknorm_i16_f32 v222, |v150|, |v151|\n\t" \
  "v_lshlrev_b32_sdwa v221, 2, v220 dst_sel:DWORD dst_unused:UNUSED_PAD src0_sel:DWORD src1_sel:BYTE_3\n\t" \
  "s_waitcnt lgkmcnt(8)\n\t" \
  "v_mfma_f32_16x16x32_bf16 v[12:15], v[208:211], v[178:181], v[12:15]\n\t" \
  "ds_read_b64_tr_b16 v[218:219], v249 offset:13976\n\t" \
  "v_lshlrev_b32_sdwa v220, 2, v220 dst_sel:DWORD dst_unused:UNUSED_PAD src0_sel:DWORD src1_sel:BYTE_1\n\t" \
  "v_lshlrev_b32_sdwa v223, 2, v222 dst_sel:DWORD dst_unused:UNUSED_PAD src0_sel:DWORD src1_sel:BYTE_3\n\t" \
  "v_mfma_f32_16x16x32_bf16 v[36:39], v[208:211], v[186:189], v[36:39]\n\t" \
  "ds_read_u16 v234, v234 offset:10528\n\t" \
  "v_lshlrev_b32_sdwa v222, 2, v222 dst_sel:DWORD dst_unused:UNUSED_PAD src0_sel:DWORD src1_sel:BYTE_1\n\t" \
  "v_lshlrev_b32_sdwa v225, 1, v157 dst_sel:DWORD dst_unused:UNUSED_PAD src0_sel:DWORD src1_sel:WORD_1\n\t" \
  "v_mfma_f32_16x16x32_bf16 v[60:63], v[208:211], v[194:197], v[60:63]\n\t" \
  "ds_read_b32 v221, v221 offset:10016\n\t" \
  "v_lshlrev_b32_sdwa v224, 1, v156 dst_sel:DWORD dst_unused:UNUSED_PAD src0_sel:DWORD src1_sel:WORD_1\n\t" \
  "v_lshlrev_b32_sdwa v227, 1, v159 dst_sel:DWORD dst_unused:UNUSED_PAD src0_sel:DWORD src1_sel:WORD_1\n\t" \
  "v_mfma_f32_16x16x32_bf16 v[84:87], v[208:211], v[202:205], v[84:87]\n\t" \
  "ds_read_b64_tr_b16 v[208:209], v248 offset:49192\n\t" \
  "ds_read_b32 v220, v220 offset:10016\n\t" \
  "v_lshlrev_b32_sdwa v226, 1, v158 dst_sel:DWORD dst_unused:UNUSED_PAD src0_sel:DWORD src1_sel:WORD_1\n\t" \
  "s_waitcnt lgkmcnt(8)\n\t" \
  "v_mfma_f32_16x16x32_bf16 v[16:19], v[212:215], v[178:181], v[16:19]\n\t" \
  "ds_read_b64_tr_b16 v[210:211], v248 offset:50216\n\t" \
  "ds_read_b32 v223, v223 offset:10016\n\t" \
  "v_fma_mix_f32 v229, |v163|, v229, v229 op_sel:[0,1,0] op_sel_hi:[0,1,1] clamp\n\t" \
  "v_mfma_f32_16x16x32_bf16 v[40:43], v[212:215], v[186:189], v[40:43]\n\t" \
  "ds_read_b32 v222, v222 offset:10016\n\t" \
  "v_fma_mix_f32 v228, |v162|, v228, v228 op_sel:[0,1,0] op_sel_hi:[0,1,1] clamp\n\t" \
  "v_fma_mix_f32 v231, |v161|, v231, v231 op_sel:[0,1,0] op_sel_hi:[0,1,1] clamp\n\t" \
  "v_mfma_f32_16x16x32_bf16 v[64:67], v[212:215], v[194:197], v[64:67]\n\t" \
  "ds_read_u16_d16_hi v225, v225 offset:10528\n\t" \
  "v_fma_mix_f32 v230, |v160|, v230, v230 op_sel:[0,1,0] op_sel_hi:[0,1,1] clamp\n\t" \
  "s_waitcnt lgkmcnt(7)\n\t" \
  "v_or_b32 v171, v232, v233\n\t" \
  "v_mfma_f32_16x16x32_bf16 v[88:91], v[212:215], v[202:205], v[88:91]\n\t" \
  "ds_read_b64_tr_b16 v[212:213], v248 offset:52264\n\t" \
  "v_or_b32 v170, v234, v235\n\t" \
  "v_fma_f32 v163, |v163|, v229, v163 mul:4\n\t" \
  "v_mfma_f32_16x16x32_bf16 v[20:23], v[216:219], v[178:181], v[20:23]\n\t" \
  "ds_read_b64_tr_b16 v[214:215], v248 offset:53288\n\t" \
  "v_fma_f32 v162, |v162|, v228, v162 mul:4\n\t" \
  "v_fma_f32 v161, |v161|, v231, v161 mul:4\n\t" \
  "v_mfma_f32_16x16x32_bf16 v[44:47], v[216:219], v[186:189], v[44:47]\n\t" \
  "ds_read_u16 v224, v224 offset:10528\n\t" \
  "v_fma_f32 v160, |v160|, v230, v160 mul:4\n\t" \
  "v_cvt_pk_bf16_f32 v163, v162, v163\n\t" \
  "v_mfma_f32_16x16x32_bf16 v[68:71], v[216:219], v[194:197], v[68:71]\n\t" \
  "ds_read_u16_d16_hi v227, v227 offset:10528\n\t" \
  "v_cvt_pk_bf16_f32 v162, v160, v161\n\t" \
  "v_cvt_pknorm_i16_f32 v228, |v164|, |v165|\n\t" \
  "v_mfma_f32_16x16x32_bf16 v[92:95], v[216:219], v[202:205], v[92:95]\n\t" \
  "ds_read_b64_tr_b16 v[216:217], v248 offset:51240\n\t" \
  "v_cvt_pknorm_i16_f32 v230, |v166|, |v167|\n\t" \
  "s_setprio 2\n\t" \
  "v_lshlrev_b32_sdwa v229, 2, v228 dst_sel:DWORD dst_unused:UNUSED_PAD src0_sel:DWORD src1_sel:BYTE_3\n\t" \
  "s_waitcnt lgkmcnt(8)\n\t" \
  "v_mfma_f32_16x16x32_bf16 v[176:179], v[208:211], v[96:99], 0\n\t" \
  "ds_read_b64_tr_b16 v[218:219], v248 offset:54312\n\t" \
  "v_lshlrev_b32_sdwa v228, 2, v228 dst_sel:DWORD dst_unused:UNUSED_PAD src0_sel:DWORD src1_sel:BYTE_1\n\t" \
  "v_lshlrev_b32_sdwa v231, 2, v230 dst_sel:DWORD dst_unused:UNUSED_PAD src0_sel:DWORD src1_sel:BYTE_3\n\t" \
  "v_mfma_f32_16x16x32_bf16 v[184:187], v[208:211], v[100:103], 0\n\t" \
  "ds_read_u16 v226, v226 offset:10528\n\t" \
  "v_lshlrev_b32_sdwa v230, 2, v230 dst_sel:DWORD dst_unused:UNUSED_PAD src0_sel:DWORD src1_sel:BYTE_1\n\t" \
  "v_mfma_f32_16x16x32_bf16 v[192:195], v[208:211], v[104:107], 0\n\t" \
  "ds_read_b32 v229, v229 offset:10016\n\t" \
  "v_lshlrev_b32_sdwa v233, 1, v173 dst_sel:DWORD dst_unused:UNUSED_PAD src0_sel:DWORD src1_sel:WORD_1\n\t" \
  "v_lshlrev_b32_sdwa v232, 1, v172 dst_sel:DWORD dst_unused:UNUSED_PAD src0_sel:DWORD src1_sel:WORD_1\n\t" \
  "v_mfma_f32_16x16x32_bf16 v[200:203], v[208:211], v[108:111], 0\n\t" \
  "ds_read_b64_tr_b16 v[208:209], v248 offset:49704\n\t" \
  "ds_read_b32 v228, v228 offset:10016\n\t" \
  "v_lshlrev_b32_sdwa v235, 1, v175 dst_sel:DWORD dst_unused:UNUSED_PAD src0_sel:DWORD src1_sel:WORD_1\n\t" \
  "s_waitcnt lgkmcnt(8)\n\t" \
  "v_mfma_f32_16x16x32_bf16 v[176:179], v[212:215], v[112:115], v[176:179]\n\t" \
  "ds_read_b64_tr_b16 v[210:211], v248 offset:50728\n\t" \
  "v_lshlrev_b32_sdwa v234, 1, v174 dst_sel:DWORD dst_unused:UNUSED_PAD src0_sel:DWORD src1_sel:WORD_1\n\t" \
  "v_fma_mix_f32 v221, |v149|, v221, v221 op_sel:[0,1,0] op_sel_hi:[0,1,1] clamp\n\t" \
  "v_mfma_f32_16x16x32_bf16 v[184:187], v[212:215], v[116:119], v[184:187]\n\t" \
  "ds_read_b32 v231, v231 offset:10016\n\t" \
  "ds_read_b32 v230, v230 offset:10016\n\t" \
  "v_fma_mix_f32 v220, |v148|, v220, v220 op_sel:[0,1,0] op_sel_hi:[0,1,1] clamp\n\t" \
  "v_mfma_f32_16x16x32_bf16 v[192:195], v[212:215], v[120:123], v[192:195]\n\t" \
  "ds_read_u16_d16_hi v233, v233 offset:10528\n\t" \
  "v_fma_mix_f32 v223, |v151|, v223, v223 op_sel:[0,1,0] op_sel_hi:[0,1,1] clamp\n\t" \
  "v_fma_mix_f32 v222, |v150|, v222, v222 op_sel:[0,1,0] op_sel_hi:[0,1,1] clamp\n\t" \
  "s_waitcnt lgkmcnt(7)\n\t" \
  "v_mfma_f32_16x16x32_bf16 v[200:203], v[212:215], v[124:127], v[200:203]\n\t" \
  "ds_read_b64_tr_b16 v[212:213], v248 offset:52776\n\t" \
  "v_or_b32 v156, v224, v225\n\t" \
  "v_or_b32 v157, v226, v227\n\t" \
  "v_mfma_f32_16x16x32_bf16 v[176:179], v[216:219], v[128:131], v[176:179]\n\t" \
  "ds_read_b64_tr_b16 v[214:215], v248 offset:53800\n\t" \
  "v_fma_f32 v149, |v149|, v221, v149 mul:4\n\t" \
  "v_fma_f32 v148, |v148|, v220, v148 mul:4\n\t" \
  "v_mfma_f32_16x16x32_bf16 v[184:187], v[216:219], v[132:135], v[184:187]\n\t" \
  "ds_read_u16 v232, v232 offset:10528\n\t" \
  "v_fma_f32 v151, |v151|, v223, v151 mul:4\n\t" \
  "v_fma_f32 v150, |v150|, v222, v150 mul:4\n\t" \
  "v_mfma_f32_16x16x32_bf16 v[192:195], v[216:219], v[136:139], v[192:195]\n\t" \
  "ds_read_u16_d16_hi v235, v235 offset:10528\n\t" \
  "v_cvt_pk_bf16_f32 v148, v148, v149\n\t" \
  "v_cvt_pk_bf16_f32 v149, v150, v151\n\t" \
  "v_mfma_f32_16x16x32_bf16 v[200:203], v[216:219], v[140:143], v[200:203]\n\t" \
  "ds_read_b64_tr_b16 v[216:217], v248 offset:51752\n\t" \
  "v_cvt_pknorm_i16_f32 v220, |v178|, |v179|\n\t" \
  "s_setprio 3\n\t" \
  "v_cvt_pknorm_i16_f32 v222, |v176|, |v177|\n\t" \
  "s_waitcnt lgkmcnt(8)\n\t" \
  "v_mfma_f32_16x16x32_bf16 v[180:183], v[208:211], v[96:99], 0\n\t" \
  "ds_read_b64_tr_b16 v[218:219], v248 offset:54824\n\t" \
  "v_lshlrev_b32_sdwa v221, 2, v220 dst_sel:DWORD dst_unused:UNUSED_PAD src0_sel:DWORD src1_sel:BYTE_3\n\t" \
  "s_waitcnt vmcnt(1)\n\t" \
  "v_lshlrev_b32_sdwa v220, 2, v220 dst_sel:DWORD dst_unused:UNUSED_PAD src0_sel:DWORD src1_sel:BYTE_1\n\t" \
  "v_mfma_f32_16x16x32_bf16 v[188:191], v[208:211], v[100:103], 0\n\t" \
  "s_mov_b64 exec, 3\n\t" \
  "ds_add_rtn_u32 v246, v251, v250 offset:47208\n\t" \
  "s_mov_b64 exec, -1\n\t" \
  "v_lshlrev_b32_sdwa v223, 2, v222 dst_sel:DWORD dst_unused:UNUSED_PAD src0_sel:DWORD src1_sel:BYTE_3\n\t" \
  "v_mfma_f32_16x16x32_bf16 v[196:199], v[208:211], v[104:107], 0\n\t" \
  "ds_read_u16 v234, v234 offset:10528\n\t" \
  "v_lshlrev_b32_sdwa v222, 2, v222 dst_sel:DWORD dst_unused:UNUSED_PAD src0_sel:DWORD src1_sel:BYTE_1\n\t" \
  "v_lshlrev_b32_sdwa v225, 1, v187 dst_sel:DWORD dst_unused:UNUSED_PAD src0_sel:DWORD src1_sel:WORD_1\n\t" \
  "v_mfma_f32_16x16x32_bf16 v[204:207], v[208:211], v[108:111], 0\n\t" \
  "ds_read_b32 v221, v221 offset:10016\n\t" \
  "v_lshlrev_b32_sdwa v224, 1, v186 dst_sel:DWORD dst_unused:UNUSED_PAD src0_sel:DWORD src1_sel:WORD_1\n\t" \
  "s_waitcnt lgkmcnt(2)\n\t" \
  "v_readfirstlane_b32 s4, v246\n\t" \
  "v_mfma_f32_16x16x32_bf16 v[180:183], v[212:215], v[112:115], v[180:183]\n\t" \
  "ds_read_b32 v220, v220 offset:10016\n\t" \
  "v_lshlrev_b32_sdwa v227, 1, v185 dst_sel:DWORD dst_unused:UNUSED_PAD src0_sel:DWORD src1_sel:WORD_1\n\t" \
  "s_cmp_lt_u32 s4, s57\n\t" \
  "s_cbranch_scc1 .Lcnx_f0_10\n.Lcnx_g0_10:\n\t" \
  "v_mfma_f32_16x16x32_bf16 v[188:191], v[212:215], v[116:119], v[188:191]\n\t" \
  "ds_read_b64_tr_b16 v[208:209], v249 offset:24600\n\t" \
  "ds_read_b32 v223, v223 offset:10016\n\t" \
  "v_lshlrev_b32_sdwa v226, 1, v184 dst_sel:DWORD dst_unused:UNUSED_PAD src0_sel:DWORD src1_sel:WORD_1\n\t" \
  "v_mfma_f32_16x16x32_bf16 v[196:199], v[212:215], v[120:123], v[196:199]\n\t" \
  "ds_read_b64_tr_b16 v[210:211], v249 offset:26136\n\t" \
  "v_fma_mix_f32 v229, |v165|, v229, v229 op_sel:[0,1,0] op_sel_hi:[0,1,1] clamp\n\t" \
  "v_fma_mix_f32 v228, |v164|, v228, v228 op_sel:[0,1,0] op_sel_hi:[0,1,1] clamp\n\t" \
  "v_mfma_f32_16x16x32_bf16 v[204:207], v[212:215], v[124:127], v[204:207]\n\t" \
  "ds_read_b64_tr_b16 v[212:213], v249 offset:24608\n\t" \
  "ds_read_b32 v222, v222 offset:10016\n\t" \
  "v_fma_mix_f32 v231, |v167|, v231, v231 op_sel:[0,1,0] op_sel_hi:[0,1,1] clamp\n\t" \
  "v_fma_mix_f32 v230, |v166|, v230, v230 op_sel:[0,1,0] op_sel_hi:[0,1,1] clamp\n\t" \
  "s_waitcnt lgkmcnt(7)\n\t" \
  "v_mfma_f32_16x16x32_bf16 v[180:183], v[216:219], v[128:131], v[180:183]\n\t" \
  "ds_read_b64_tr_b16 v[214:215], v249 offset:26144\n\t" \
  "v_or_b32 v172, v232, v233\n\t" \
  "v_or_b32 v173, v234, v235\n\t" \
  "v_mfma_f32_16x16x32_bf16 v[188:191], v[216:219], v[132:135], v[188:191]\n\t" \
  "ds_read_u16_d16_hi v225, v225 offset:10528\n\t" \
  "v_fma_f32 v165, |v165|, v229, v165 mul:4\n\t" \
  "v_fma_f32 v164, |v164|, v228, v164 mul:4\n\t" \
  "v_mfma_f32_16x16x32_bf16 v[196:199], v[216:219], v[136:139], v[196:199]\n\t" \
  "ds_read_u16 v224, v224 offset:10528\n\t" \
  "v_fma_f32 v167, |v167|, v231, v167 mul:4\n\t" \
  "v_fma_f32 v166, |v166|, v230, v166 mul:4\n\t" \
  "v_mfma_f32_16x16x32_bf16 v[204:207], v[216:219], v[140:143], v[204:207]\n\t" \
  "ds_read_b64_tr_b16 v[216:217], v249 offset:24664\n\t" \
  "v_cvt_pk_bf16_f32 v164, v164, v165\n\t" \
  "v_cvt_pk_bf16_f32 v165, v166, v167\n\t" \
  "v_mfma_f32_16x16x32_bf16 v[64:67], v[242:245], v[236:239], v[64:67]\n\t" \
  "ds_read_b64_tr_b16 v[218:219], v249 offset:26200\n\t" \
  "v_cvt_pknorm_i16_f32 v228, |v194|, |v195|\n\t" \
  "s_setprio 0\n\t" \
  "v_cvt_pknorm_i16_f32 v230, |v192|, |v193|\n\t" \
  "v_mfma_f32_16x16x32_bf16 v[68:71], v[240:243], v[236:239], v[68:71]\n\t" \
  "ds_read_u16_d16_hi v227, v227 offset:10528\n\t" \
  "buffer_load_dwordx4 v[236:239], v250, s[8:11], s13 offen offset:0 nt\n\t" \
  "v_lshlrev_b32_sdwa v229, 2, v228 dst_sel:DWORD dst_unused:UNUSED_PAD src0_sel:DWORD src1_sel:BYTE_3\n\t" \
  "s_waitcnt lgkmcnt(8)\n\t" \
  "v_mfma_f32_16x16x32_bf16 v[0:3], v[208:211], v[146:149], v[0:3]\n\t" \
  "ds_read_u16 v226, v226 offset:10528\n\t" \
  "v_lshlrev_b32_sdwa v228, 2, v228 dst_sel:DWORD dst_unused:UNUSED_PAD src0_sel:DWORD src1_sel:BYTE_1\n\t" \
  "s_add_u32 m0, s25, 73280\n\t" \
  "s_add_u32 s24, s24, s26\n\t" \
  "v_lshlrev_b32_sdwa v231, 2, v230 dst_sel:DWORD dst_unused:UNUSED_PAD src0_sel:DWORD src1_sel:BYTE_3\n\t" \
  "v_mfma_f32_16x16x32_bf16 v[24:27], v[208:211], v[154:157], v[24:27]\n\t" \
  "ds_read_b32 v229, v229 offset:10016\n\t" \
  "buffer_load_dwordx4 v254, s[8:11], s24 offen lds\n\t" \
  "v_lshlrev_b32_sdwa v230, 2, v230 dst_sel:DWORD dst_unused:UNUSED_PAD src0_sel:DWORD src1_sel:BYTE_1\n\t" \
  "v_mfma_f32_16x16x32_bf16 v[48:51], v[208:211], v[162:165], v[48:51]\n\t" \
  "ds_read_b32 v228, v228 offset:10016\n\t" \
  "v_lshlrev_b32_sdwa v233, 1, v203 dst_sel:DWORD dst_unused:UNUSED_PAD src0_sel:DWORD src1_sel:WORD_1\n\t" \
  "v_lshlrev_b32_sdwa v232, 1, v202 dst_sel:DWORD dst_unused:UNUSED_PAD src0_sel:DWORD src1_sel:WORD_1\n\t" \
  "v_mfma_f32_16x16x32_bf16 v[72:75], v[208:211], v[170:173], v[72:75]\n\t" \
  "ds_read_b64_tr_b16 v[208:209], v249 offset:24672\n\t" \
  "v_lshlrev_b32_sdwa v235, 1, v201 dst_sel:DWORD dst_unused:UNUSED_PAD src0_sel:DWORD src1_sel:WORD_1\n\t" \
  "s_cmp_eq_u32 s38, 7\n\t" \
  "s_cbranch_scc1 .Lcnx_h10\n.Lcnx_i10:\n\t" \
  "s_waitcnt lgkmcnt(9)\n\t" \
  "v_mfma_f32_16x16x32_bf16 v[4:7], v[212:215], v[146:149], v[4:7]\n\t" \
  "ds_read_b64_tr_b16 v[210:211], v249 offset:26208\n\t" \
  "v_lshlrev_b32_sdwa v234, 1, v200 dst_sel:DWORD dst_unused:UNUSED_PAD src0_sel:DWORD src1_sel:WORD_1\n\t" \
  "v_fma_mix_f32 v221, |v179|, v221, v221 op_sel:[0,1,0] op_sel_hi:[0,1,1] clamp\n\t" \
  "v_mfma_f32_16x16x32_bf16 v[28:31], v[212:215], v[154:157], v[28:31]\n\t" \
  "ds_read_b32 v231, v231 offset:10016\n\t" \
  "ds_read_b32 v230, v230 offset:10016\n\t" \
  "v_fma_mix_f32 v220, |v178|, v220, v220 op_sel:[0,1,0] op_sel_hi:[0,1,1] clamp\n\t" \
  "v_fma_mix_f32 v223, |v177|, v223, v223 op_sel:[0,1,0] op_sel_hi:[0,1,1] clamp\n\t" \
  "v_mfma_f32_16x16x32_bf16 v[52:55], v[212:215], v[162:165], v[52:55]\n\t" \
  "ds_read_u16_d16_hi v233, v233 offset:10528\n\t" \
  "v_fma_mix_f32 v222, |v176|, v222, v222 op_sel:[0,1,0] op_sel_hi:[0,1,1] clamp\n\t" \
  "s_waitcnt lgkmcnt(7)\n\t" \
  "v_or_b32 v187, v224, v225\n\t" \
  "v_mfma_f32_16x16x32_bf16 v[76:79], v[212:215], v[170:173], v[76:79]\n\t" \
  "ds_read_b64_tr_b16 v[212:213], v249 offset:24728\n\t" \
  "v_or_b32 v186, v226, v227\n\t" \
  "v_fma_f32 v179, |v179|, v221, v179 mul:4\n\t" \
  "v_mfma_f32_16x16x32_bf16 v[8:11], v[216:219], v[146:149], v[8:11]\n\t" \
  "ds_read_b64_tr_b16 v[214:215], v249 offset:26264\n\t" \
  "v_fma_f32 v178, |v178|, v220, v178 mul:4\n\t" \
  "v_fma_f32 v177, |v177|, v223, v177 mul:4\n\t" \
  "v_mfma_f32_16x16x32_bf16 v[32:35], v[216:219], v[154:157], v[32:35]\n\t" \
  "ds_read_u16 v232, v232 offset:10528\n\t" \
  "v_fma_f32 v176, |v176|, v222, v176 mul:4\n\t" \
  "v_cvt_pk_bf16_f32 v179, v178, v179\n\t" \
  "v_mfma_f32_16x16x32_bf16 v[56:59], v[216:219], v[162:165], v[56:59]\n\t" \
  "ds_read_u16_d16_hi v235, v235 offset:10528\n\t" \
  "v_cvt_pk_bf16_f32 v178, v176, v177\n\t" \
  "s_setprio 1\n\t" \
  "v_cvt_pknorm_i16_f32 v220, |v180|, |v181|\n\t" \
  "v_mfma_f32_16x16x32_bf16 v[80:83], v[216:219], v[170:173], v[80:83]\n\t" \
  "ds_read_b64_tr_b16 v[216:217], v249 offset:24736\n\t" \
  "v_cvt_pknorm_i16_f32 v222, |v182|, |v183|\n\t" \
  "v_lshlrev_b32_sdwa v221, 2, v220 dst_sel:DWORD dst_unused:UNUSED_PAD src0_sel:DWORD src1_sel:BYTE_3\n\t" \
  "s_waitcnt lgkmcnt(8)\n\t" \
  "v_mfma_f32_16x16x32_bf16 v[12:15], v[208:211], v[146:149], v[12:15]\n\t" \
  "ds_read_b64_tr_b16 v[218:219], v249 offset:26272\n\t" \
  "v_lshlrev_b32_sdwa v220, 2, v220 dst_sel:DWORD dst_unused:UNUSED_PAD src0_sel:DWORD src1_sel:BYTE_1\n\t" \
  "v_lshlrev_b32_sdwa v223, 2, v222 dst_sel:DWORD dst_unused:UNUSED_PAD src0_sel:DWORD src1_sel:BYTE_3\n\t" \
  "v_mfma_f32_16x16x32_bf16 v[36:39], v[208:211], v[154:157], v[36:39]\n\t" \
  "ds_read_u16 v234, v234 offset:10528\n\t" \
  "v_lshlrev_b32_sdwa v222, 2, v222 dst_sel:DWORD dst_unused:UNUSED_PAD src0_sel:DWORD src1_sel:BYTE_1\n\t" \
  "v_lshlrev_b32_sdwa v225, 1, v189 dst_sel:DWORD dst_unused:UNUSED_PAD src0_sel:DWORD src1_sel:WORD_1\n\t" \
  "v_mfma_f32_16x16x32_bf16 v[60:63], v[208:211], v[162:165], v[60:63]\n\t" \
  "ds_read_b32 v221, v221 offset:10016\n\t" \
  "v_lshlrev_b32_sdwa v224, 1, v188 dst_sel:DWORD dst_unused:UNUSED_PAD src0_sel:DWORD src1_sel:WORD_1\n\t" \
  "v_lshlrev_b32_sdwa v227, 1, v191 dst_sel:DWORD dst_unused:UNUSED_PAD src0_sel:DWORD src1_sel:WORD_1\n\t" \
  "v_mfma_f32_16x16x32_bf16 v[84:87], v[208:211], v[170:173], v[84:87]\n\t" \
  "ds_read_b64_tr_b16 v[208:209], v248 offset:36904\n\t" \
  "ds_read_b32 v220, v220 offset:10016\n\t" \
  "v_lshlrev_b32_sdwa v226, 1, v190 dst_sel:DWORD dst_unused:UNUSED_PAD src0_sel:DWORD src1_sel:WORD_1\n\t" \
  "s_waitcnt lgkmcnt(8)\n\t" \
  "v_mfma_f32_16x16x32_bf16 v[16:19], v[212:215], v[146:149], v[16:19]\n\t" \
  "ds_read_b64_tr_b16 v[210:211], v248 offset:37928\n\t" \
  "ds_read_b32 v223, v223 offset:10016\n\t" \
  "v_fma_mix_f32 v229, |v195|, v229, v229 op_sel:[0,1,0] op_sel_hi:[0,1,1] clamp\n\t" \
  "v_mfma_f32_16x16x32_bf16 v[40:43], v[212:215], v[154:157], v[40:43]\n\t" \
  "ds_read_b32 v222, v222 offset:10016\n\t" \
  "v_fma_mix_f32 v228, |v194|, v228, v228 op_sel:[0,1,0] op_sel_hi:[0,1,1] clamp\n\t" \
  "v_fma_mix_f32 v231, |v193|, v231, v231 op_sel:[0,1,0] op_sel_hi:[0,1,1] clamp\n\t" \
  "v_mfma_f32_16x16x32_bf16 v[64:67], v[212:215], v[162:165], v[64:67]\n\t" \
  "ds_read_u16_d16_hi v225, v225 offset:10528\n\t" \
  "v_fma_mix_f32 v230, |v192|, v230, v230 op_sel:[0,1,0] op_sel_hi:[0,1,1] clamp\n\t" \
  "s_waitcnt lgkmcnt(7)\n\t" \
  "v_or_b32 v203, v232, v233\n\t" \
  "v_mfma_f32_16x16x32_bf16 v[88:91], v[212:215], v[170:173], v[88:91]\n\t" \
  "ds_read_b64_tr_b16 v[212:213], v248 offset:39976\n\t" \
  "v_or_b32 v202, v234, v235\n\t" \
  "v_fma_f32 v195, |v195|, v229, v195 mul:4\n\t" \
  "v_mfma_f32_16x16x32_bf16 v[20:23], v[216:219], v[146:149], v[20:23]\n\t" \
  "ds_read_b64_tr_b16 v[214:215], v248 offset:41000\n\t" \
  "v_fma_f32 v194, |v194|, v228, v194 mul:4\n\t" \
  "v_fma_f32 v193, |v193|, v231, v193 mul:4\n\t" \
  "v_mfma_f32_16x16x32_bf16 v[44:47], v[216:219], v[154:157], v[44:47]\n\t" \
  "ds_read_u16 v224, v224 offset:10528\n\t" \
  "v_fma_f32 v192, |v192|, v230, v192 mul:4\n\t" \
  "v_cvt_pk_bf16_f32 v195, v194, v195\n\t" \
  "v_mfma_f32_16x16x32_bf16 v[68:71], v[216:219], v[162:165], v[68:71]\n\t" \
  "ds_read_u16_d16_hi v227, v227 offset:10528\n\t" \
  "v_cvt_pk_bf16_f32 v194, v192, v193\n\t" \
  "v_cvt_pknorm_i16_f32 v228, |v196|, |v197|\n\t" \
  "v_mfma_f32_16x16x32_bf16 v[92:95], v[216:219], v[170:173], v[92:95]\n\t" \
  "ds_read_b64_tr_b16 v[216:217], v248 offset:38952\n\t" \
  "v_cvt_pknorm_i16_f32 v230, |v198|, |v199|\n\t" \
  "s_setprio 2\n\t" \
  "v_lshlrev_b32_sdwa v229, 2, v228 dst_sel:DWORD dst_unused:UNUSED_PAD src0_sel:DWORD src1_sel:BYTE_3\n\t" \
  "s_waitcnt lgkmcnt(8)\n\t" \
  "v_mfma_f32_16x16x32_bf16 v[144:147], v[208:211], v[96:99], 0\n\t" \
  "ds_read_b64_tr_b16 v[218:219], v248 offset:42024\n\t" \
  "v_lshlrev_b32_sdwa v228, 2, v228 dst_sel:DWORD dst_unused:UNUSED_PAD src0_sel:DWORD src1_sel:BYTE_1\n\t" \
  "v_lshlrev_b32_sdwa v231, 2, v230 dst_sel:DWORD dst_unused:UNUSED_PAD src0_sel:DWORD src1_sel:BYTE_3\n\t" \
  "v_mfma_f32_16x16x32_bf16 v[152:155], v[208:211], v[100:103], 0\n\t" \
  "ds_read_u16 v226, v226 offset:10528\n\t" \
  "v_lshlrev_b32_sdwa v230, 2, v230 dst_sel:DWORD dst_unused:UNUSED_PAD src0_sel:DWORD src1_sel:BYTE_1\n\t" \
  "v_mfma_f32_16x16x32_bf16 v[160:163], v[208:211], v[104:107], 0\n\t" \
  "ds_read_b32 v229, v229 offset:10016\n\t" \
  "v_lshlrev_b32_sdwa v233, 1, v205 dst_sel:DWORD dst_unused:UNUSED_PAD src0_sel:DWORD src1_sel:WORD_1\n\t" \
  "v_lshlrev_b32_sdwa v232, 1, v204 dst_sel:DWORD dst_unused:UNUSED_PAD src0_sel:DWORD src1_sel:WORD_1\n\t" \
  "v_mfma_f32_16x16x32_bf16 v[168:171], v[208:211], v[108:111], 0\n\t" \
  "ds_read_b64_tr_b16 v[208:209], v248 offset:37416\n\t" \
  "ds_read_b32 v228, v228 offset:10016\n\t" \
  "v_lshlrev_b32_sdwa v235, 1, v207 dst_sel:DWORD dst_unused:UNUSED_PAD src0_sel:DWORD src1_sel:WORD_1\n\t" \
  "s_waitcnt lgkmcnt(8)\n\t" \
  "v_mfma_f32_16x16x32_bf16 v[144:147], v[212:215], v[112:115], v[144:147]\n\t" \
  "ds_read_b64_tr_b16 v[210:211], v248 offset:38440\n\t" \
  "v_lshlrev_b32_sdwa v234, 1, v206 dst_sel:DWORD dst_unused:UNUSED_PAD src0_sel:DWORD src1_sel:WORD_1\n\t" \
  "v_fma_mix_f32 v221, |v181|, v221, v221 op_sel:[0,1,0] op_sel_hi:[0,1,1] clamp\n\t" \
  "v_mfma_f32_16x16x32_bf16 v[152:155], v[212:215], v[116:119], v[152:155]\n\t" \
  "ds_read_b32 v231, v231 offset:10016\n\t" \
  "ds_read_b32 v230, v230 offset:10016\n\t" \
  "v_fma_mix_f32 v220, |v180|, v220, v220 op_sel:[0,1,0] op_sel_hi:[0,1,1] clamp\n\t" \
  "v_mfma_f32_16x16x32_bf16 v[160:163], v[212:215], v[120:123], v[160:163]\n\t" \
  "ds_read_u16_d16_hi v233, v233 offset:10528\n\t" \
  "v_fma_mix_f32 v223, |v183|, v223, v223 op_sel:[0,1,0] op_sel_hi:[0,1,1] clamp\n\t" \
  "v_fma_mix_f32 v222, |v182|, v222, v222 op_sel:[0,1,0] op_sel_hi:[0,1,1] clamp\n\t" \
  "s_waitcnt lgkmcnt(7)\n\t" \
  "v_mfma_f32_16x16x32_bf16 v[168:171], v[212:215], v[124:127], v[168:171]\n\t" \
  "ds_read_b64_tr_b16 v[212:213], v248 offset:40488\n\t" \
  "v_or_b32 v188, v224, v225\n\t" \
  "v_or_b32 v189, v226, v227\n\t" \
  "v_mfma_f32_16x16x32_bf16 v[144:147], v[216:219], v[128:131], v[144:147]\n\t" \
  "ds_read_b64_tr_b16 v[214:215], v248 offset:41512\n\t" \
  "v_fma_f32 v181, |v181|, v221, v181 mul:4\n\t" \
  "v_fma_f32 v180, |v180|, v220, v180 mul:4\n\t" \
  "v_mfma_f32_16x16x32_bf16 v[152:155], v[216:219], v[132:135], v[152:155]\n\t" \
  "ds_read_u16 v232, v232 offset:10528\n\t" \
  "v_fma_f32 v183, |v183|, v223, v183 mul:4\n\t" \
  "v_fma_f32 v182, |v182|, v222, v182 mul:4\n\t" \
  "v_mfma_f32_16x16x32_bf16 v[160:163], v[216:219], v[136:139], v[160:163]\n\t" \
  "ds_read_u16_d16_hi v235, v235 offset:10528\n\t" \
  "v_cvt_pk_bf16_f32 v180, v180, v181\n\t" \
  "v_cvt_pk_bf16_f32 v181, v182, v183\n\t" \
  "v_mfma_f32_16x16x32_bf16 v[168:171], v[216:219], v[140:143], v[168:171]\n\t" \
  "ds_read_b64_tr_b16 v[216:217], v248 offset:39464\n\t" \
  "v_cvt_pknorm_i16_f32 v220, |v146|, |v147|\n\t" \
  "s_setprio 3\n\t" \
  "v_cvt_pknorm_i16_f32 v222, |v144|, |v145|\n\t" \
  "s_waitcnt lgkmcnt(8)\n\t" \
  "v_mfma_f32_16x16x32_bf16 v[148:151], v[208:211], v[96:99], 0\n\t" \
  "ds_read_b64_tr_b16 v[218:219], v248 offset:42536\n\t" \
  "v_lshlrev_b32_sdwa v221, 2, v220 dst_sel:DWORD dst_unused:UNUSED_PAD src0_sel:DWORD src1_sel:BYTE_3\n\t" \
  "s_waitcnt vmcnt(1)\n\t" \
  "v_lshlrev_b32_sdwa v220, 2, v220 dst_sel:DWORD dst_unused:UNUSED_PAD src0_sel:DWORD src1_sel:BYTE_1\n\t" \
  "v_mfma_f32_16x16x32_bf16 v[156:159], v[208:211], v[100:103], 0\n\t" \
  "s_mov_b64 exec, 3\n\t" \
  "ds_add_rtn_u32 v246, v251, v250 offset:47216\n\t" \
  "s_mov_b64 exec, -1\n\t" \
  "v_lshlrev_b32_sdwa v223, 2, v222 dst_sel:DWORD dst_unused:UNUSED_PAD src0_sel:DWORD src1_sel:BYTE_3\n\t" \
  "v_mfma_f32_16x16x32_bf16 v[164:167], v[208:211], v[104:107], 0\n\t" \
  "ds_read_u16 v234, v234 offset:10528\n\t" \
  "v_lshlrev_b32_sdwa v222, 2, v222 dst_sel:DWORD dst_unused:UNUSED_PAD src0_sel:DWORD src1_sel:BYTE_1\n\t" \
  "v_lshlrev_b32_sdwa v225, 1, v155 dst_sel:DWORD dst_unused:UNUSED_PAD src0_sel:DWORD src1_sel:WORD_1\n\t" \
  "v_mfma_f32_16x16x32_bf16 v[172:175], v[208:211], v[108:111], 0\n\t" \
  "ds_read_b32 v221, v221 offset:10016\n\t" \
  "v_lshlrev_b32_sdwa v224, 1, v154 dst_sel:DWORD dst_unused:UNUSED_PAD src0_sel:DWORD src1_sel:WORD_1\n\t" \
  "s_waitcnt lgkmcnt(2)\n\t" \
  "v_readfirstlane_b32 s4, v246\n\t" \
  "v_mfma_f32_16x16x32_bf16 v[148:151], v[212:215], v[112:115], v[148:151]\n\t" \
  "ds_read_b32 v220, v220 offset:10016\n\t" \
  "v_lshlrev_b32_sdwa v227, 1, v153 dst_sel:DWORD dst_unused:UNUSED_PAD src0_sel:DWORD src1_sel:WORD_1\n\t" \
  "s_cmp_lt_u32 s4, s57\n\t" \
  "s_cbranch_scc1 .Lcnx_f0_11\n.Lcnx_g0_11:\n\t" \
  "v_mfma_f32_16x16x32_bf16 v[156:159], v[212:215], v[116:119], v[156:159]\n\t" \
  "ds_read_b64_tr_b16 v[208:209], v249 offset:30752\n\t" \
  "ds_read_b32 v223, v223 offset:10016\n\t" \
  "v_lshlrev_b32_sdwa v226, 1, v152 dst_sel:DWORD dst_unused:UNUSED_PAD src0_sel:DWORD src1_sel:WORD_1\n\t" \
  "v_mfma_f32_16x16x32_bf16 v[164:167], v[212:215], v[120:123], v[164:167]\n\t" \
  "ds_read_b64_tr_b16 v[210:211], v249 offset:32288\n\t" \
  "v_fma_mix_f32 v229, |v197|, v229, v229 op_sel:[0,1,0] op_sel_hi:[0,1,1] clamp\n\t" \
  "v_fma_mix_f32 v228, |v196|, v228, v228 op_sel:[0,1,0] op_sel_hi:[0,1,1] clamp\n\t" \
  "v_mfma_f32_16x16x32_bf16 v[172:175], v[212:215], v[124:127], v[172:175]\n\t" \
  "ds_read_b64_tr_b16 v[212:213], v249 offset:30760\n\t" \
  "ds_read_b32 v222, v222 offset:10016\n\t" \
  "v_fma_mix_f32 v231, |v199|, v231, v231 op_sel:[0,1,0] op_sel_hi:[0,1,1] clamp\n\t" \
  "v_fma_mix_f32 v230, |v198|, v230, v230 op_sel:[0,1,0] op_sel_hi:[0,1,1] clamp\n\t" \
  "s_waitcnt lgkmcnt(7)\n\t" \
  "v_mfma_f32_16x16x32_bf16 v[148:151], v[216:219], v[128:131], v[148:151]\n\t" \
  "ds_read_b64_tr_b16 v[214:215], v249 offset:32296\n\t" \
  "v_or_b32 v204, v232, v233\n\t" \
  "v_or_b32 v205, v234, v235\n\t" \
  "v_mfma_f32_16x16x32_bf16 v[156:159], v[216:219], v[132:135], v[156:159]\n\t" \
  "ds_read_u16_d16_hi v225, v225 offset:10528\n\t" \
  "v_fma_f32 v197, |v197|, v229, v197 mul:4\n\t" \
  "v_fma_f32 v196, |v196|, v228, v196 mul:4\n\t" \
  "v_mfma_f32_16x16x32_bf16 v[164:167], v[216:219], v[136:139], v[164:167]\n\t" \
  "ds_read_u16 v224, v224 offset:10528\n\t" \
  "v_fma_f32 v199, |v199|, v231, v199 mul:4\n\t" \
  "v_fma_f32 v198, |v198|, v230, v198 mul:4\n\t" \
  "v_mfma_f32_16x16x32_bf16 v[172:175], v[216:219], v[140:143], v[172:175]\n\t" \
  "ds_read_b64_tr_b16 v[216:217], v249 offset:30816\n\t" \
  "v_cvt_pk_bf16_f32 v196, v196, v197\n\t" \
  "v_cvt_pk_bf16_f32 v197, v198, v199\n\t" \
  "v_mfma_f32_16x16x32_bf16 v[72:75], v[242:245], v[236:239], v[72:75]\n\t" \
  "ds_read_b64_tr_b16 v[218:219], v249 offset:32352\n\t" \
  "v_cvt_pknorm_i16_f32 v228, |v162|, |v163|\n\t" \
  "s_setprio 0\n\t" \
  "v_cvt_pknorm_i16_f32 v230, |v160|, |v161|\n\t" \
  "v_mfma_f32_16x16x32_bf16 v[76:79], v[240:243], v[236:239], v[76:79]\n\t" \
  "ds_read_u16_d16_hi v227, v227 offset:10528\n\t" \
  "buffer_load_dwordx4 v[236:239], v250, s[8:11], s13 offen offset:64 nt\n\t" \
  "v_lshlrev_b32_sdwa v229, 2, v228 dst_sel:DWORD dst_unused:UNUSED_PAD src0_sel:DWORD src1_sel:BYTE_3\n\t" \
  "s_waitcnt lgkmcnt(8)\n\t" \
  "v_mfma_f32_16x16x32_bf16 v[0:3], v[208:211], v[178:181], v[0:3]\n\t" \
  "ds_read_u16 v226, v226 offset:10528\n\t" \
  "v_lshlrev_b32_sdwa v228, 2, v228 dst_sel:DWORD dst_unused:UNUSED_PAD src0_sel:DWORD src1_sel:BYTE_1\n\t" \
  "v_lshlrev_b32_sdwa v231, 2, v230 dst_sel:DWORD dst_unused:UNUSED_PAD src0_sel:DWORD src1_sel:BYTE_3\n\t" \
  "v_mfma_f32_16x16x32_bf16 v[24:27], v[208:211], v[186:189], v[24:27]\n\t" \
  "ds_read_b32 v229, v229 offset:10016\n\t" \
  "v_lshlrev_b32_sdwa v230, 2, v230 dst_sel:DWORD dst_unused:UNUSED_PAD src0_sel:DWORD src1_sel:BYTE_1\n\t" \
  "v_lshlrev_b32_sdwa v233, 1, v171 dst_sel:DWORD dst_unused:UNUSED_PAD src0_sel:DWORD src1_sel:WORD_1\n\t" \
  "v_mfma_f32_16x16x32_bf16 v[48:51], v[208:211], v[194:197], v[48:51]\n\t" \
  "ds_read_b32 v228, v228 offset:10016\n\t" \
  "v_lshlrev_b32_sdwa v232, 1, v170 dst_sel:DWORD dst_unused:UNUSED_PAD src0_sel:DWORD src1_sel:WORD_1\n\t" \
  "v_lshlrev_b32_sdwa v235, 1, v169 dst_sel:DWORD dst_unused:UNUSED_PAD src0_sel:DWORD src1_sel:WORD_1\n\t" \
  "v_mfma_f32_16x16x32_bf16 v[72:75], v[208:211], v[202:205], v[72:75]\n\t" \
  "ds_read_b64_tr_b16 v[208:209], v249 offset:30824\n\t" \
  "v_lshlrev_b32_sdwa v234, 1, v168 dst_sel:DWORD dst_unused:UNUSED_PAD src0_sel:DWORD src1_sel:WORD_1\n\t" \
  "s_cmp_eq_u32 s38, 4\n\t" \
  "s_cbranch_scc1 .Lcnx_h11\n.Lcnx_i11:\n\t" \
  "s_waitcnt lgkmcnt(9)\n\t" \
  "v_mfma_f32_16x16x32_bf16 v[4:7], v[212:215], v[178:181], v[4:7]\n\t" \
  "ds_read_b64_tr_b16 v[210:211], v249 offset:32360\n\t" \
  "ds_read_b32 v231, v231 offset:10016\n\t" \
  "v_fma_mix_f32 v221, |v147|, v221, v221 op_sel:[0,1,0] op_sel_hi:[0,1,1] clamp\n\t" \
  "v_mfma_f32_16x16x32_bf16 v[28:31], v[212:215], v[186:189], v[28:31]\n\t" \
  "ds_read_b32 v230, v230 offset:10016\n\t" \
  "v_fma_mix_f32 v220, |v146|, v220, v220 op_sel:[0,1,0] op_sel_hi:[0,1,1] clamp\n\t" \
  "v_fma_mix_f32 v223, |v145|, v223, v223 op_sel:[0,1,0] op_sel_hi:[0,1,1] clamp\n\t" \
  "v_mfma_f32_16x16x32_bf16 v[52:55], v[212:215], v[194:197], v[52:55]\n\t" \
  "ds_read_u16_d16_hi v233, v233 offset:10528\n\t" \
  "v_fma_mix_f32 v222, |v144|, v222, v222 op_sel:[0,1,0] op_sel_hi:[0,1,1] clamp\n\t" \
  "s_waitcnt lgkmcnt(7)\n\t" \
  "v_or_b32 v155, v224, v225\n\t" \
  "v_mfma_f32_16x16x32_bf16 v[76:79], v[212:215], v[202:205], v[76:79]\n\t" \
  "ds_read_b64_tr_b16 v[212:213], v249 offset:30880\n\t" \
  "v_or_b32 v154, v226, v227\n\t" \
  "v_fma_f32 v147, |v147|, v221, v147 mul:4\n\t" \
  "v_mfma_f32_16x16x32_bf16 v[8:11], v[216:219], v[178:181], v[8:11]\n\t" \
  "ds_read_b64_tr_b16 v[214:215], v249 offset:32416\n\t" \
  "v_fma_f32 v146, |v146|, v220, v146 mul:4\n\t" \
  "v_fma_f32 v145, |v145|, v223, v145 mul:4\n\t" \
  "v_mfma_f32_16x16x32_bf16 v[32:35], v[216:219], v[186:189], v[32:35]\n\t" \
  "ds_read_u16 v232, v232 offset:10528\n\t" \
  "v_fma_f32 v144, |v144|, v222, v144 mul:4\n\t" \
  "v_cvt_pk_bf16_f32 v147, v146, v147\n\t" \
  "v_mfma_f32_16x16x32_bf16 v[56:59], v[216:219], v[194:197], v[56:59]\n\t" \
  "ds_read_u16_d16_hi v235, v235 offset:10528\n\t" \
  "v_cvt_pk_bf16_f32 v146, v144, v145\n\t" \
  "s_setprio 1\n\t" \
  "v_cvt_pknorm_i16_f32 v220, |v148|, |v149|\n\t" \
  "v_mfma_f32_16x16x32_bf16 v[80:83], v[216:219], v[202:205], v[80:83]\n\t" \
  "ds_read_b64_tr_b16 v[216:217], v249 offset:30888\n\t" \
  "v_cvt_pknorm_i16_f32 v222, |v150|, |v151|\n\t" \
  "v_lshlrev_b32_sdwa v221, 2, v220 dst_sel:DWORD dst_unused:UNUSED_PAD src0_sel:DWORD src1_sel:BYTE_3\n\t" \
  "s_waitcnt lgkmcnt(8)\n\t" \
  "v_mfma_f32_16x16x32_bf16 v[12:15], v[208:211], v[178:181], v[12:15]\n\t" \
  "ds_read_b64_tr_b16 v[218:219], v249 offset:32424\n\t" \
  "v_lshlrev_b32_sdwa v220, 2, v220 dst_sel:DWORD dst_unused:UNUSED_PAD src0_sel:DWORD src1_sel:BYTE_1\n\t" \
  "v_lshlrev_b32_sdwa v223, 2, v222 dst_sel:DWORD dst_unused:UNUSED_PAD src0_sel:DWORD src1_sel:BYTE_3\n\t" \
  "v_mfma_f32_16x16x32_bf16 v[36:39], v[208:211], v[186:189], v[36:39]\n\t" \
  "ds_read_u16 v234, v234 offset:10528\n\t" \
  "v_lshlrev_b32_sdwa v222, 2, v222 dst_sel:DWORD dst_unused:UNUSED_PAD src0_sel:DWORD src1_sel:BYTE_1\n\t" \
  "v_lshlrev_b32_sdwa v225, 1, v157 dst_sel:DWORD dst_unused:UNUSED_PAD src0_sel:DWORD src1_sel:WORD_1\n\t" \
  "v_mfma_f32_16x16x32_bf16 v[60:63], v[208:211], v[194:197], v[60:63]\n\t" \
  "ds_read_b32 v221, v221 offset:10016\n\t" \
  "v_lshlrev_b32_sdwa v224, 1, v156 dst_sel:DWORD dst_unused:UNUSED_PAD src0_sel:DWORD src1_sel:WORD_1\n\t" \
  "v_lshlrev_b32_sdwa v227, 1, v159 dst_sel:DWORD dst_unused:UNUSED_PAD src0_sel:DWORD src1_sel:WORD_1\n\t" \
  "v_mfma_f32_16x16x32_bf16 v[84:87], v[208:211], v[202:205], v[84:87]\n\t" \
  "ds_read_b64_tr_b16 v[208:209], v248 offset:0\n\t" \
  "ds_read_b32 v220, v220 offset:10016\n\t" \
  "v_lshlrev_b32_sdwa v226, 1, v158 dst_sel:DWORD dst_unused:UNUSED_PAD src0_sel:DWORD src1_sel:WORD_1\n\t" \
  "s_waitcnt lgkmcnt(8)\n\t" \
  "v_mfma_f32_16x16x32_bf16 v[16:19], v[212:215], v[178:181], v[16:19]\n\t" \
  "ds_read_b64_tr_b16 v[210:211], v248 offset:1024\n\t" \
  "ds_read_b32 v223, v223 offset:10016\n\t" \
  "v_fma_mix_f32 v229, |v163|, v229, v229 op_sel:[0,1,0] op_sel_hi:[0,1,1] clamp\n\t" \
  "v_mfma_f32_16x16x32_bf16 v[40:43], v[212:215], v[186:189], v[40:43]\n\t" \
  "ds_read_b32 v222, v222 offset:10016\n\t" \
  "v_fma_mix_f32 v228, |v162|, v228, v228 op_sel:[0,1,0] op_sel_hi:[0,1,1] clamp\n\t" \
  "v_fma_mix_f32 v231, |v161|, v231, v231 op_sel:[0,1,0] op_sel_hi:[0,1,1] clamp\n\t" \
  "v_mfma_f32_16x16x32_bf16 v[64:67], v[212:215], v[194:197], v[64:67]\n\t" \
  "ds_read_u16_d16_hi v225, v225 offset:10528\n\t" \
  "v_fma_mix_f32 v230, |v160|, v230, v230 op_sel:[0,1,0] op_sel_hi:[0,1,1] clamp\n\t" \
  "s_waitcnt lgkmcnt(7)\n\t" \
  "v_or_b32 v171, v232, v233\n\t" \
  "v_mfma_f32_16x16x32_bf16 v[88:91], v[212:215], v[202:205], v[88:91]\n\t" \
  "ds_read_b64_tr_b16 v[212:213], v248 offset:3072\n\t" \
  "v_or_b32 v170, v234, v235\n\t" \
  "v_fma_f32 v163, |v163|, v229, v163 mul:4\n\t" \
  "v_mfma_f32_16x16x32_bf16 v[20:23], v[216:219], v[178:181], v[20:23]\n\t" \
  "ds_read_b64_tr_b16 v[214:215], v248 offset:4096\n\t" \
  "v_fma_f32 v162, |v162|, v228, v162 mul:4\n\t" \
  "v_fma_f32 v161, |v161|, v231, v161 mul:4\n\t" \
  "v_mfma_f32_16x16x32_bf16 v[44:47], v[216:219], v[186:189], v[44:47]\n\t" \
  "ds_read_u16 v224, v224 offset:10528\n\t" \
  "v_fma_f32 v160, |v160|, v230, v160 mul:4\n\t" \
  "v_cvt_pk_bf16_f32 v163, v162, v163\n\t" \
  "v_mfma_f32_16x16x32_bf16 v[68:71], v[216:219], v[194:197], v[68:71]\n\t" \
  "ds_read_u16_d16_hi v227, v227 offset:10528\n\t" \
  "v_cvt_pk_bf16_f32 v162, v160, v161\n\t" \
  "v_cvt_pknorm_i16_f32 v228, |v164|, |v165|\n\t" \
  "v_mfma_f32_16x16x32_bf16 v[92:95], v[216:219], v[202:205], v[92:95]\n\t" \
  "ds_read_b64_tr_b16 v[216:217], v248 offset:2048\n\t" \
  "v_cvt_pknorm_i16_f32 v230, |v166|, |v167|\n\t" \
  "s_setprio 2\n\t" \
  "v_lshlrev_b32_sdwa v229, 2, v228 dst_sel:DWORD dst_unused:UNUSED_PAD src0_sel:DWORD src1_sel:BYTE_3\n\t" \
  "s_waitcnt lgkmcnt(8)\n\t" \
  "v_mfma_f32_16x16x32_bf16 v[176:179], v[208:211], v[96:99], 0\n\t" \
  "ds_read_b64_tr_b16 v[218:219], v248 offset:5120\n\t" \
  "v_lshlrev_b32_sdwa v228, 2, v228 dst_sel:DWORD dst_unused:UNUSED_PAD src0_sel:DWORD src1_sel:BYTE_1\n\t" \
  "v_lshlrev_b32_sdwa v231, 2, v230 dst_sel:DWORD dst_unused:UNUSED_PAD src0_sel:DWORD src1_sel:BYTE_3\n\t" \
  "v_mfma_f32_16x16x32_bf16 v[184:187], v[208:211], v[100:103], 0\n\t" \
  "ds_read_u16 v226, v226 offset:10528\n\t" \
  "v_lshlrev_b32_sdwa v230, 2, v230 dst_sel:DWORD dst_unused:UNUSED_PAD src0_sel:DWORD src1_sel:BYTE_1\n\t" \
  "v_mfma_f32_16x16x32_bf16 v[192:195], v[208:211], v[104:107], 0\n\t" \
  "ds_read_b32 v229, v229 offset:10016\n\t" \
  "v_lshlrev_b32_sdwa v233, 1, v173 dst_sel:DWORD dst_unused:UNUSED_PAD src0_sel:DWORD src1_sel:WORD_1\n\t" \
  "v_lshlrev_b32_sdwa v232, 1, v172 dst_sel:DWORD dst_unused:UNUSED_PAD src0_sel:DWORD src1_sel:WORD_1\n\t" \
  "v_mfma_f32_16x16x32_bf16 v[200:203], v[208:211], v[108:111], 0\n\t" \
  "ds_read_b64_tr_b16 v[208:209], v248 offset:512\n\t" \
  "ds_read_b32 v228, v228 offset:10016\n\t" \
  "v_lshlrev_b32_sdwa v235, 1, v175 dst_sel:DWORD dst_unused:UNUSED_PAD src0_sel:DWORD src1_sel:WORD_1\n\t" \
  "s_waitcnt lgkmcnt(8)\n\t" \
  "v_mfma_f32_16x16x32_bf16 v[176:179], v[212:215], v[112:115], v[176:179]\n\t" \
  "ds_read_b64_tr_b16 v[210:211], v248 offset:1536\n\t" \
  "v_lshlrev_b32_sdwa v234, 1, v174 dst_sel:DWORD dst_unused:UNUSED_PAD src0_sel:DWORD src1_sel:WORD_1\n\t" \
  "v_fma_mix_f32 v221, |v149|, v221, v221 op_sel:[0,1,0] op_sel_hi:[0,1,1] clamp\n\t" \
  "v_mfma_f32_16x16x32_bf16 v[184:187], v[212:215], v[116:119], v[184:187]\n\t" \
  "ds_read_b32 v231, v231 offset:10016\n\t" \
  "ds_read_b32 v230, v230 offset:10016\n\t" \
  "v_fma_mix_f32 v220, |v148|, v220, v220 op_sel:[0,1,0] op_sel_hi:[0,1,1] clamp\n\t" \
  "v_mfma_f32_16x16x32_bf16 v[192:195], v[212:215], v[120:123], v[192:195]\n\t" \
  "ds_read_u16_d16_hi v233, v233 offset:10528\n\t" \
  "v_fma_mix_f32 v223, |v151|, v223, v223 op_sel:[0,1,0] op_sel_hi:[0,1,1] clamp\n\t" \
  "v_fma_mix_f32 v222, |v150|, v222, v222 op_sel:[0,1,0] op_sel_hi:[0,1,1] clamp\n\t" \
  "s_waitcnt lgkmcnt(7)\n\t" \
  "v_mfma_f32_16x16x32_bf16 v[200:203], v[212:215], v[124:127], v[200:203]\n\t" \
  "ds_read_b64_tr_b16 v[212:213], v248 offset:3584\n\t" \
  "v_or_b32 v156, v224, v225\n\t" \
  "v_or_b32 v157, v226, v227\n\t" \
  "v_mfma_f32_16x16x32_bf16 v[176:179], v[216:219], v[128:131], v[176:179]\n\t" \
  "ds_read_b64_tr_b16 v[214:215], v248 offset:4608\n\t" \
  "v_fma_f32 v149, |v149|, v221, v149 mul:4\n\t" \
  "v_fma_f32 v148, |v148|, v220, v148 mul:4\n\t" \
  "v_mfma_f32_16x16x32_bf16 v[184:187], v[216:219], v[132:135], v[184:187]\n\t" \
  "ds_read_u16 v232, v232 offset:10528\n\t" \
  "v_fma_f32 v151, |v151|, v223, v151 mul:4\n\t" \
  "v_fma_f32 v150, |v150|, v222, v150 mul:4\n\t" \
  "v_mfma_f32_16x16x32_bf16 v[192:195], v[216:219], v[136:139], v[192:195]\n\t" \
  "ds_read_u16_d16_hi v235, v235 offset:10528\n\t" \
  "v_cvt_pk_bf16_f32 v148, v148, v149\n\t" \
  "v_cvt_pk_bf16_f32 v149, v150, v151\n\t" \
  "v_mfma_f32_16x16x32_bf16 v[200:203], v[216:219], v[140:143], v[200:203]\n\t" \
  "ds_read_b64_tr_b16 v[216:217], v248 offset:2560\n\t" \
  "v_cvt_pknorm_i16_f32 v220, |v178|, |v179|\n\t" \
  "s_setprio 3\n\t" \
  "v_cvt_pknorm_i16_f32 v222, |v176|, |v177|\n\t" \
  "s_waitcnt lgkmcnt(8)\n\t" \
  "v_mfma_f32_16x16x32_bf16 v[180:183], v[208:211], v[96:99], 0\n\t" \
  "ds_read_b64_tr_b16 v[218:219], v248 offset:5632\n\t" \
  "v_lshlrev_b32_sdwa v221, 2, v220 dst_sel:DWORD dst_unused:UNUSED_PAD src0_sel:DWORD src1_sel:BYTE_3\n\t" \
  "s_waitcnt vmcnt(0)\n\t" \
  "v_lshlrev_b32_sdwa v220, 2, v220 dst_sel:DWORD dst_unused:UNUSED_PAD src0_sel:DWORD src1_sel:BYTE_1\n\t" \
  "v_mfma_f32_16x16x32_bf16 v[188:191], v[208:211], v[100:103], 0\n\t" \
  "s_mov_b64 exec, 3\n\t" \
  "ds_add_rtn_u32 v246, v251, v250 offset:47224\n\t" \
  "s_mov_b64 exec, -1\n\t" \
  "v_lshlrev_b32_sdwa v223, 2, v222 dst_sel:DWORD dst_unused:UNUSED_PAD src0_sel:DWORD src1_sel:BYTE_3\n\t" \
  "v_mfma_f32_16x16x32_bf16 v[196:199], v[208:211], v[104:107], 0\n\t" \
  "ds_read_u16 v234, v234 offset:10528\n\t" \
  "v_lshlrev_b32_sdwa v222, 2, v222 dst_sel:DWORD dst_unused:UNUSED_PAD src0_sel:DWORD src1_sel:BYTE_1\n\t" \
  "v_lshlrev_b32_sdwa v225, 1, v187 dst_sel:DWORD dst_unused:UNUSED_PAD src0_sel:DWORD src1_sel:WORD_1\n\t" \
  "v_mfma_f32_16x16x32_bf16 v[204:207], v[208:211], v[108:111], 0\n\t" \
  "ds_read_b32 v221, v221 offset:10016\n\t" \
  "v_lshlrev_b32_sdwa v224, 1, v186 dst_sel:DWORD dst_unused:UNUSED_PAD src0_sel:DWORD src1_sel:WORD_1\n\t" \
  "s_waitcnt lgkmcnt(2)\n\t" \
  "v_readfirstlane_b32 s4, v246\n\t" \
  "v_mfma_f32_16x16x32_bf16 v[180:183], v[212:215], v[112:115], v[180:183]\n\t" \
  "ds_read_b32 v220, v220 offset:10016\n\t" \
  "v_lshlrev_b32_sdwa v227, 1, v185 dst_sel:DWORD dst_unused:UNUSED_PAD src0_sel:DWORD src1_sel:WORD_1\n\t" \
  "s_cmpk_lt_u32 s4, 3072\n\t" \
  "s_cbranch_scc1 .Lcnx_w0_12\n.Lcnx_g0_12:\n\t" \
  "v_mfma_f32_16x16x32_bf16 v[188:191], v[212:215], v[116:119], v[188:191]\n\t" \
  "ds_read_b64_tr_b16 v[208:209], v249 offset:6152\n\t" \
  "ds_read_b32 v223, v223 offset:10016\n\t" \
  "v_lshlrev_b32_sdwa v226, 1, v184 dst_sel:DWORD dst_unused:UNUSED_PAD src0_sel:DWORD src1_sel:WORD_1\n\t" \
  "v_mfma_f32_16x16x32_bf16 v[196:199], v[212:215], v[120:123], v[196:199]\n\t" \
  "ds_read_b64_tr_b16 v[210:211], v249 offset:7688\n\t" \
  "v_fma_mix_f32 v229, |v165|, v229, v229 op_sel:[0,1,0] op_sel_hi:[0,1,1] clamp\n\t" \
  "v_fma_mix_f32 v228, |v164|, v228, v228 op_sel:[0,1,0] op_sel_hi:[0,1,1] clamp\n\t" \
  "v_mfma_f32_16x16x32_bf16 v[204:207], v[212:215], v[124:127], v[204:207]\n\t" \
  "ds_read_b64_tr_b16 v[212:213], v249 offset:6160\n\t" \
  "ds_read_b32 v222, v222 offset:10016\n\t" \
  "v_fma_mix_f32 v231, |v167|, v231, v231 op_sel:[0,1,0] op_sel_hi:[0,1,1] clamp\n\t" \
  "v_fma_mix_f32 v230, |v166|, v230, v230 op_sel:[0,1,0] op_sel_hi:[0,1,1] clamp\n\t" \
  "s_waitcnt lgkmcnt(7)\n\t" \
  "v_mfma_f32_16x16x32_bf16 v[180:183], v[216:219], v[128:131], v[180:183]\n\t" \
  "ds_read_b64_tr_b16 v[214:215], v249 offset:7696\n\t" \
  "v_or_b32 v172, v232, v233\n\t" \
  "v_or_b32 v173, v234, v235\n\t" \
  "v_mfma_f32_16x16x32_bf16 v[188:191], v[216:219], v[132:135], v[188:191]\n\t" \
  "ds_read_u16_d16_hi v225, v225 offset:10528\n\t" \
  "v_fma_f32 v165, |v165|, v229, v165 mul:4\n\t" \
  "v_fma_f32 v164, |v164|, v228, v164 mul:4\n\t" \
  "v_mfma_f32_16x16x32_bf16 v[196:199], v[216:219], v[136:139], v[196:199]\n\t" \
  "ds_read_u16 v224, v224 offset:10528\n\t" \
  "v_fma_f32 v167, |v167|, v231, v167 mul:4\n\t" \
  "v_fma_f32 v166, |v166|, v230, v166 mul:4\n\t" \
  "v_mfma_f32_16x16x32_bf16 v[204:207], v[216:219], v[140:143], v[204:207]\n\t" \
  "ds_read_b64_tr_b16 v[216:217], v249 offset:6216\n\t" \
  "v_cvt_pk_bf16_f32 v164, v164, v165\n\t" \
  "v_cvt_pk_bf16_f32 v165, v166, v167\n\t" \
  "v_mfma_f32_16x16x32_bf16 v[80:83], v[242:245], v[236:239], v[80:83]\n\t" \
  "ds_read_b64_tr_b16 v[218:219], v249 offset:7752\n\t" \
  "v_cvt_pknorm_i16_f32 v228, |v194|, |v195|\n\t" \
  "s_setprio 3\n\t" \
  "v_cvt_pknorm_i16_f32 v230, |v192|, |v193|\n\t" \
  "v_lshlrev_b32_sdwa v229, 2, v228 dst_sel:DWORD dst_unused:UNUSED_PAD src0_sel:DWORD src1_sel:BYTE_3\n\t" \
  "v_mfma_f32_16x16x32_bf16 v[84:87], v[240:243], v[236:239], v[84:87]\n\t" \
  "ds_read_u16_d16_hi v227, v227 offset:10528\n\t" \
  "buffer_load_dwordx4 v[236:239], v250, s[8:11], s13 offen offset:128 nt\n\t" \
  "v_lshlrev_b32_sdwa v228, 2, v228 dst_sel:DWORD dst_unused:UNUSED_PAD src0_sel:DWORD src1_sel:BYTE_1\n\t" \
  "s_waitcnt lgkmcnt(8)\n\t" \
  "v_mfma_f32_16x16x32_bf16 v[0:3], v[208:211], v[146:149], v[0:3]\n\t" \
  "ds_read_u16 v226, v226 offset:10528\n\t" \
  "v_lshlrev_b32_sdwa v231, 2, v230 dst_sel:DWORD dst_unused:UNUSED_PAD src0_sel:DWORD src1_sel:BYTE_3\n\t" \
  "v_lshlrev_b32_sdwa v230, 2, v230 dst_sel:DWORD dst_unused:UNUSED_PAD src0_sel:DWORD src1_sel:BYTE_1\n\t" \
  "v_lshlrev_b32_sdwa v233, 1, v203 dst_sel:DWORD dst_unused:UNUSED_PAD src0_sel:DWORD src1_sel:WORD_1\n\t" \
  "v_mfma_f32_16x16x32_bf16 v[24:27], v[208:211], v[154:157], v[24:27]\n\t" \
  "ds_read_b32 v229, v229 offset:10016\n\t" \
  "v_lshlrev_b32_sdwa v232, 1, v202 dst_sel:DWORD dst_unused:UNUSED_PAD src0_sel:DWORD src1_sel:WORD_1\n\t" \
  "v_lshlrev_b32_sdwa v235, 1, v201 dst_sel:DWORD dst_unused:UNUSED_PAD src0_sel:DWORD src1_sel:WORD_1\n\t" \
  "v_lshlrev_b32_sdwa v234, 1, v200 dst_sel:DWORD dst_unused:UNUSED_PAD src0_sel:DWORD src1_sel:WORD_1\n\t" \
  "s_waitcnt lgkmcnt(8)\n\t" \
  "v_mfma_f32_16x16x32_bf16 v[48:51], v[208:211], v[162:165], v[48:51]\n\t" \
  "ds_read_b32 v228, v228 offset:10016\n\t" \
  "ds_read_b32 v231, v231 offset:10016\n\t" \
  "v_fma_mix_f32 v221, |v179|, v221, v221 op_sel:[0,1,0] op_sel_hi:[0,1,1] clamp\n\t" \
  "v_fma_mix_f32 v220, |v178|, v220, v220 op_sel:[0,1,0] op_sel_hi:[0,1,1] clamp\n\t" \
  "v_fma_mix_f32 v223, |v177|, v223, v223 op_sel:[0,1,0] op_sel_hi:[0,1,1] clamp\n\t" \
  "v_mfma_f32_16x16x32_bf16 v[72:75], v[208:211], v[170:173], v[72:75]\n\t" \
  "ds_read_b64_tr_b16 v[208:209], v249 offset:6224\n\t" \
  "ds_read_b32 v230, v230 offset:10016\n\t" \
  "v_fma_mix_f32 v222, |v176|, v222, v222 op_sel:[0,1,0] op_sel_hi:[0,1,1] clamp\n\t" \
  "s_cmp_eq_u32 s38, 5\n\t" \
  "s_cbranch_scc1 .Lcnx_h12\n.Lcnx_i12:\n\t" \
  "s_waitcnt lgkmcnt(5)\n\t" \
  "v_mfma_f32_16x16x32_bf16 v[4:7], v[212:215], v[146:149], v[4:7]\n\t" \
  "ds_read_b64_tr_b16 v[210:211], v249 offset:7760\n\t" \
  "v_or_b32 v187, v224, v225\n\t" \
  "s_setprio 2\n\t" \
  "v_or_b32 v186, v226, v227\n\t" \
  "v_fma_f32 v179, |v179|, v221, v179 mul:4\n\t" \
  "v_fma_f32 v178, |v178|, v220, v178 mul:4\n\t" \
  "v_mfma_f32_16x16x32_bf16 v[28:31], v[212:215], v[154:157], v[28:31]\n\t" \
  "ds_read_u16_d16_hi v233, v233 offset:10528\n\t" \
  "v_fma_f32 v177, |v177|, v223, v177 mul:4\n\t" \
  "v_fma_f32 v176, |v176|, v222, v176 mul:4\n\t" \
  "v_cvt_pk_bf16_f32 v179, v178, v179\n\t" \
  "v_mfma_f32_16x16x32_bf16 v[52:55], v[212:215], v[162:165], v[52:55]\n\t" \
  "ds_read_u16 v232, v232 offset:10528\n\t" \
  "v_cvt_pk_bf16_f32 v178, v176, v177\n\t" \
  "v_cvt_pknorm_i16_f32 v220, |v180|, |v181|\n\t" \
  "v_cvt_pknorm_i16_f32 v222, |v182|, |v183|\n\t" \
  "v_lshlrev_b32_sdwa v221, 2, v220 dst_sel:DWORD dst_unused:UNUSED_PAD src0_sel:DWORD src1_sel:BYTE_3\n\t" \
  "v_mfma_f32_16x16x32_bf16 v[76:79], v[212:215], v[170:173], v[76:79]\n\t" \
  "ds_read_b64_tr_b16 v[212:213], v249 offset:6280\n\t" \
  "v_lshlrev_b32_sdwa v220, 2, v220 dst_sel:DWORD dst_unused:UNUSED_PAD src0_sel:DWORD src1_sel:BYTE_1\n\t" \
  "v_lshlrev_b32_sdwa v223, 2, v222 dst_sel:DWORD dst_unused:UNUSED_PAD src0_sel:DWORD src1_sel:BYTE_3\n\t" \
  "v_lshlrev_b32_sdwa v222, 2, v222 dst_sel:DWORD dst_unused:UNUSED_PAD src0_sel:DWORD src1_sel:BYTE_1\n\t" \
  "v_mfma_f32_16x16x32_bf16 v[8:11], v[216:219], v[146:149], v[8:11]\n\t" \
  "ds_read_b64_tr_b16 v[214:215], v249 offset:7816\n\t" \
  "ds_read_u16_d16_hi v235, v235 offset:10528\n\t" \
  "v_lshlrev_b32_sdwa v225, 1, v189 dst_sel:DWORD dst_unused:UNUSED_PAD src0_sel:DWORD src1_sel:WORD_1\n\t" \
  "v_lshlrev_b32_sdwa v224, 1, v188 dst_sel:DWORD dst_unused:UNUSED_PAD src0_sel:DWORD src1_sel:WORD_1\n\t" \
  "v_lshlrev_b32_sdwa v227, 1, v191 dst_sel:DWORD dst_unused:UNUSED_PAD src0_sel:DWORD src1_sel:WORD_1\n\t" \
  "v_lshlrev_b32_sdwa v226, 1, v190 dst_sel:DWORD dst_unused:UNUSED_PAD src0_sel:DWORD src1_sel:WORD_1\n\t" \
  "s_waitcnt lgkmcnt(6)\n\t" \
  "v_mfma_f32_16x16x32_bf16 v[32:35], v[216:219], v[154:157], v[32:35]\n\t" \
  "ds_read_u16 v234, v234 offset:10528\n\t" \
  "ds_read_b32 v221, v221 offset:10016\n\t" \
  "v_fma_mix_f32 v229, |v195|, v229, v229 op_sel:[0,1,0] op_sel_hi:[0,1,1] clamp\n\t" \
  "v_fma_mix_f32 v228, |v194|, v228, v228 op_sel:[0,1,0] op_sel_hi:[0,1,1] clamp\n\t" \
  "v_fma_mix_f32 v231, |v193|, v231, v231 op_sel:[0,1,0] op_sel_hi:[0,1,1] clamp\n\t" \
  "v_mfma_f32_16x16x32_bf16 v[56:59], v[216:219], v[162:165], v[56:59]\n\t" \
  "ds_read_b32 v220, v220 offset:10016\n\t" \
  "ds_read_b32 v223, v223 offset:10016\n\t" \
  "v_fma_mix_f32 v230, |v192|, v230, v230 op_sel:[0,1,0] op_sel_hi:[0,1,1] clamp\n\t" \
  "s_waitcnt lgkmcnt(3)\n\t" \
  "v_or_b32 v203, v232, v233\n\t" \
  "v_or_b32 v202, v234, v235\n\t" \
  "v_fma_f32 v195, |v195|, v229, v195 mul:4\n\t" \
  "v_mfma_f32_16x16x32_bf16 v[80:83], v[216:219], v[170:173], v[80:83]\n\t" \
  "ds_read_b64_tr_b16 v[216:217], v249 offset:6288\n\t" \
  "ds_read_b32 v222, v222 offset:10016\n\t" \
  "v_fma_f32 v194, |v194|, v228, v194 mul:4\n\t" \
  "s_setprio 1\n\t" \
  "v_fma_f32 v193, |v193|, v231, v193 mul:4\n\t" \
  "v_fma_f32 v192, |v192|, v230, v192 mul:4\n\t" \
  "v_mfma_f32_16x16x32_bf16 v[12:15], v[208:211], v[146:149], v[12:15]\n\t" \
  "ds_read_b64_tr_b16 v[218:219], v249 offset:7824\n\t" \
  "v_cvt_pk_bf16_f32 v195, v194, v195\n\t" \
  "s_waitcnt vmcnt(0)\n\t" \
  "v_cvt_pk_bf16_f32 v194, v192, v193\n\t" \
  "v_cvt_pknorm_i16_f32 v228, |v196|, |v197|\n\t" \
  "v_cvt_pknorm_i16_f32 v230, |v198|, |v199|\n\t" \
  "v_mfma_f32_16x16x32_bf16 v[36:39], v[208:211], v[154:157], v[36:39]\n\t" \
  "s_mov_b64 exec, 3\n\t" \
  "ds_add_rtn_u32 v246, v251, v250 offset:47232\n\t" \
  "s_mov_b64 exec, -1\n\t" \
  "v_lshlrev_b32_sdwa v229, 2, v228 dst_sel:DWORD dst_unused:UNUSED_PAD src0_sel:DWORD src1_sel:BYTE_3\n\t" \
  "v_mfma_f32_16x16x32_bf16 v[60:63], v[208:211], v[162:165], v[60:63]\n\t" \
  "ds_read_u16_d16_hi v225, v225 offset:10528\n\t" \
  "v_lshlrev_b32_sdwa v228, 2, v228 dst_sel:DWORD dst_unused:UNUSED_PAD src0_sel:DWORD src1_sel:BYTE_1\n\t" \
  "v_lshlrev_b32_sdwa v231, 2, v230 dst_sel:DWORD dst_unused:UNUSED_PAD src0_sel:DWORD src1_sel:BYTE_3\n\t" \
  "v_lshlrev_b32_sdwa v230, 2, v230 dst_sel:DWORD dst_unused:UNUSED_PAD src0_sel:DWORD src1_sel:BYTE_1\n\t" \
  "v_mfma_f32_16x16x32_bf16 v[84:87], v[208:211], v[170:173], v[84:87]\n\t" \
  "ds_read_u16 v224, v224 offset:10528\n\t" \
  "ds_read_u16_d16_hi v227, v227 offset:10528\n\t" \
  "v_lshlrev_b32_sdwa v233, 1, v205 dst_sel:DWORD dst_unused:UNUSED_PAD src0_sel:DWORD src1_sel:WORD_1\n\t" \
  "v_lshlrev_b32_sdwa v232, 1, v204 dst_sel:DWORD dst_unused:UNUSED_PAD src0_sel:DWORD src1_sel:WORD_1\n\t" \
  "v_lshlrev_b32_sdwa v235, 1, v207 dst_sel:DWORD dst_unused:UNUSED_PAD src0_sel:DWORD src1_sel:WORD_1\n\t" \
  "s_waitcnt lgkmcnt(3)\n\t" \
  "v_readfirstlane_b32 s4, v246\n\t" \
  "v_mfma_f32_16x16x32_bf16 v[16:19], v[212:215], v[146:149], v[16:19]\n\t" \
  "ds_read_u16 v226, v226 offset:10528\n\t" \
  "ds_read_b32 v229, v229 offset:10016\n\t" \
  "v_lshlrev_b32_sdwa v234, 1, v206 dst_sel:DWORD dst_unused:UNUSED_PAD src0_sel:DWORD src1_sel:WORD_1\n\t" \
  "s_cmpk_lt_u32 s4, 3072\n\t" \
  "s_cbranch_scc1 .Lcnx_w0_13\n.Lcnx_g0_13:\n\t" \
  "v_mfma_f32_16x16x32_bf16 v[40:43], v[212:215], v[154:157], v[40:43]\n\t" \
  "ds_read_b64_tr_b16 v[208:209], v249 offset:12304\n\t" \
  "ds_read_b32 v228, v228 offset:10016\n\t" \
  "v_fma_mix_f32 v221, |v181|, v221, v221 op_sel:[0,1,0] op_sel_hi:[0,1,1] clamp\n\t" \
  "s_setprio 0\n\t" \
  "v_fma_mix_f32 v220, |v180|, v220, v220 op_sel:[0,1,0] op_sel_hi:[0,1,1] clamp\n\t" \
  "v_fma_mix_f32 v223, |v183|, v223, v223 op_sel:[0,1,0] op_sel_hi:[0,1,1] clamp\n\t" \
  "v_mfma_f32_16x16x32_bf16 v[64:67], v[212:215], v[162:165], v[64:67]\n\t" \
  "ds_read_b64_tr_b16 v[210:211], v249 offset:13840\n\t" \
  "ds_read_b32 v231, v231 offset:10016\n\t" \
  "v_fma_mix_f32 v222, |v182|, v222, v222 op_sel:[0,1,0] op_sel_hi:[0,1,1] clamp\n\t" \
  "s_waitcnt lgkmcnt(5)\n\t" \
  "v_or_b32 v188, v224, v225\n\t" \
  "v_or_b32 v189, v226, v227\n\t" \
  "v_fma_f32 v181, |v181|, v221, v181 mul:4\n\t" \
  "v_mfma_f32_16x16x32_bf16 v[88:91], v[212:215], v[170:173], v[88:91]\n\t" \
  "ds_read_b64_tr_b16 v[212:213], v249 offset:12312\n\t" \
  "ds_read_b32 v230, v230 offset:10016\n\t" \
  "v_fma_f32 v180, |v180|, v220, v180 mul:4\n\t" \
  "v_fma_f32 v183, |v183|, v223, v183 mul:4\n\t" \
  "v_fma_f32 v182, |v182|, v222, v182 mul:4\n\t" \
  "v_mfma_f32_16x16x32_bf16 v[20:23], v[216:219], v[146:149], v[20:23]\n\t" \
  "ds_read_b64_tr_b16 v[214:215], v249 offset:13848\n\t" \
  "v_cvt_pk_bf16_f32 v180, v180, v181\n\t" \
  "v_cvt_pk_bf16_f32 v181, v182, v183\n\t" \
  "s_waitcnt lgkmcnt(1)\n\t" \
  "v_fma_mix_f32 v229, |v197|, v229, v229 op_sel:[0,1,0] op_sel_hi:[0,1,1] clamp\n\t" \
  "v_fma_mix_f32 v228, |v196|, v228, v228 op_sel:[0,1,0] op_sel_hi:[0,1,1] clamp\n\t" \
  "v_mfma_f32_16x16x32_bf16 v[44:47], v[216:219], v[154:157], v[44:47]\n\t" \
  "ds_read_u16_d16_hi v233, v233 offset:10528\n\t" \
  "ds_read_u16 v232, v232 offset:10528\n\t" \
  "ds_read_u16_d16_hi v235, v235 offset:10528\n\t" \
  "ds_read_u16 v234, v234 offset:10528\n\t" \
  "v_fma_mix_f32 v231, |v199|, v231, v231 op_sel:[0,1,0] op_sel_hi:[0,1,1] clamp\n\t" \
  "v_fma_mix_f32 v230, |v198|, v230, v230 op_sel:[0,1,0] op_sel_hi:[0,1,1] clamp\n\t" \
  "s_waitcnt lgkmcnt(0)\n\t" \
  "v_or_b32 v204, v232, v233\n\t" \
  "v_mfma_f32_16x16x32_bf16 v[68:71], v[216:219], v[162:165], v[68:71]\n\t" \
  "v_or_b32 v205, v234, v235\n\t" \
  "v_fma_f32 v197, |v197|, v229, v197 mul:4\n\t" \
  "v_fma_f32 v196, |v196|, v228, v196 mul:4\n\t" \
  "v_fma_f32 v199, |v199|, v231, v199 mul:4\n\t" \
  "v_mfma_f32_16x16x32_bf16 v[92:95], v[216:219], v[170:173], v[92:95]\n\t" \
  "ds_read_b64_tr_b16 v[216:217], v249 offset:12368\n\t" \
  "v_fma_f32 v198, |v198|, v230, v198 mul:4\n\t" \
  "v_cvt_pk_bf16_f32 v196, v196, v197\n\t" \
  "v_cvt_pk_bf16_f32 v197, v198, v199\n\t" \
  "v_mfma_f32_32x32x16_bf16 v[144:159], v[242:245], v[240:243], 0\n\t" \
  "v_mfma_f32_32x32x16_bf16 v[160:175], v[242:245], v[240:243], 0\n\t" \
  "v_mfma_f32_16x16x32_bf16 v[88:91], v[242:245], v[236:239], v[88:91]\n\t" \
  "ds_read_b64_tr_b16 v[218:219], v249 offset:13904\n\t" \
  "v_add_u32 v232, s56, v247\n\t" \
  "v_mfma_f32_16x16x32_bf16 v[92:95], v[240:243], v[236:239], v[92:95]\n\t" \
  "ds_read2_b32 v[98:99], v247 offset0:0 offset1:6\n\t" \
  "v_mfma_f32_16x16x32_bf16 v[0:3], v[208:211], v[178:181], v[0:3]\n\t" \
  "ds_read2_b32 v[114:115], v247 offset0:192 offset1:198\n\t" \
  "v_mfma_f32_16x16x32_bf16 v[24:27], v[208:211], v[186:189], v[24:27]\n\t" \
  "ds_read2_b32 v[130:131], v232 offset0:0 offset1:6\n\t" \
  "v_mfma_f32_16x16x32_bf16 v[48:51], v[208:211], v[194:197], v[48:51]\n\t" \
  "ds_read2_b32 v[102:103], v247 offset0:12 offset1:18\n\t" \
  "v_mfma_f32_16x16x32_bf16 v[72:75], v[208:211], v[202:205], v[72:75]\n\t" \
  "ds_read_b64_tr_b16 v[208:209], v249 offset:12376\n\t" \
  "ds_read2_b32 v[118:119], v247 offset0:204 offset1:210\n\t" \
  "s_bitcmp1_b32 208, s38\n\t" \
  "s_cbranch_scc1 .Lcnx_h13\n.Lcnx_i13:\n\t" \
  "v_bfe_u32 v236, v251, 3, 2\n\t" \
  "v_mfma_f32_16x16x32_bf16 v[4:7], v[212:215], v[178:181], v[4:7]\n\t" \
  "ds_read_b64_tr_b16 v[210:211], v249 offset:13912\n\t" \
  "ds_read2_b32 v[134:135], v232 offset0:12 offset1:18\n\t" \
  "v_mul_u32_u24 v236, 0x5555, v236\n\t" \
  "v_mfma_f32_16x16x32_bf16 v[28:31], v[212:215], v[186:189], v[28:31]\n\t" \
  "ds_read2_b32 v[106:107], v247 offset0:24 offset1:30\n\t" \
  "ds_read2_b32 v[122:123], v247 offset0:216 offset1:222\n\t" \
  "v_mfma_f32_16x16x32_bf16 v[52:55], v[212:215], v[194:197], v[52:55]\n\t" \
  "ds_read2_b32 v[138:139], v232 offset0:24 offset1:30\n\t" \
  "v_mfma_f32_16x16x32_bf16 v[76:79], v[212:215], v[202:205], v[76:79]\n\t" \
  "ds_read_b64_tr_b16 v[212:213], v249 offset:12432\n\t" \
  "s_waitcnt lgkmcnt(12)\n\t" \
  "v_mfma_f32_16x16x32_bf16 v[8:11], v[216:219], v[178:181], v[8:11]\n\t" \
  "ds_read_b64_tr_b16 v[214:215], v249 offset:13968\n\t" \
  "v_mfma_f32_16x16x32_bf16 v[32:35], v[216:219], v[186:189], v[32:35]\n\t" \
  "ds_read2_b32 v[110:111], v247 offset0:36 offset1:42\n\t" \
  "v_mfma_f32_16x16x32_bf16 v[56:59], v[216:219], v[194:197], v[56:59]\n\t" \
  "ds_read2_b32 v[126:127], v247 offset0:228 offset1:234\n\t" \
  "v_mfma_f32_16x16x32_bf16 v[80:83], v[216:219], v[202:205], v[80:83]\n\t" \
  "ds_read_b64_tr_b16 v[216:217], v249 offset:12440\n\t" \
  "v_cvt_pk_bf16_f32 v48, v48, v49\n\t" \
  "v_cvt_pk_bf16_f32 v49, v50, v51\n\t" \
  "v_cvt_pk_bf16_f32 v50, v52, v53\n\t" \
  "v_cvt_pk_bf16_f32 v51, v54, v55\n\t" \
  "v_cvt_pk_bf16_f32 v52, v0, v1\n\t" \
  "v_cvt_pk_bf16_f32 v53, v2, v3\n\t" \
  "v_cvt_pk_bf16_f32 v54, v4, v5\n\t" \
  "v_cvt_pk_bf16_f32 v55, v6, v7\n\t" \
  "v_cvt_pk_bf16_f32 v72, v72, v73\n\t" \
  "v_cvt_pk_bf16_f32 v73, v74, v75\n\t" \
  "v_cvt_pk_bf16_f32 v74, v76, v77\n\t" \
  "v_cvt_pk_bf16_f32 v75, v78, v79\n\t" \
  "v_cvt_pk_bf16_f32 v76, v24, v25\n\t" \
  "v_cvt_pk_bf16_f32 v77, v26, v27\n\t" \
  "v_cvt_pk_bf16_f32 v78, v28, v29\n\t" \
  "v_cvt_pk_bf16_f32 v79, v30, v31\n\t" \
  "s_waitcnt lgkmcnt(9)\n\t" \
  "v_mfma_f32_16x16x32_bf16 v[12:15], v[208:211], v[178:181], v[12:15]\n\t" \
  "ds_read_b64_tr_b16 v[218:219], v249 offset:13976\n\t" \
  "v_mfma_f32_16x16x32_bf16 v[36:39], v[208:211], v[186:189], v[36:39]\n\t" \
  "ds_read2_b32 v[142:143], v232 offset0:36 offset1:42\n\t" \
  "v_mfma_f32_16x16x32_bf16 v[60:63], v[208:211], v[194:197], v[60:63]\n\t" \
  "v_mfma_f32_16x16x32_bf16 v[84:87], v[208:211], v[202:205], v[84:87]\n\t" \
  "s_waitcnt lgkmcnt(5)\n\t" \
  "v_mfma_f32_16x16x32_bf16 v[16:19], v[212:215], v[178:181], v[16:19]\n\t" \
  "v_mfma_f32_16x16x32_bf16 v[40:43], v[212:215], v[186:189], v[40:43]\n\t" \
  "v_mfma_f32_16x16x32_bf16 v[64:67], v[212:215], v[194:197], v[64:67]\n\t" \
  "v_mfma_f32_16x16x32_bf16 v[88:91], v[212:215], v[202:205], v[88:91]\n\t" \
  "v_cvt_pk_bf16_f32 v56, v56, v57\n\t" \
  "v_cvt_pk_bf16_f32 v57, v58, v59\n\t" \
  "v_cvt_pk_bf16_f32 v58, v60, v61\n\t" \
  "v_cvt_pk_bf16_f32 v59, v62, v63\n\t" \
  "v_cvt_pk_bf16_f32 v60, v8, v9\n\t" \
  "v_cvt_pk_bf16_f32 v61, v10, v11\n\t" \
  "v_cvt_pk_bf16_f32 v62, v12, v13\n\t" \
  "v_cvt_pk_bf16_f32 v63, v14, v15\n\t" \
  "v_cvt_pk_bf16_f32 v80, v80, v81\n\t" \
  "v_cvt_pk_bf16_f32 v81, v82, v83\n\t" \
  "v_cvt_pk_bf16_f32 v82, v84, v85\n\t" \
  "v_cvt_pk_bf16_f32 v83, v86, v87\n\t" \
  "v_cvt_pk_bf16_f32 v84, v32, v33\n\t" \
  "v_cvt_pk_bf16_f32 v85, v34, v35\n\t" \
  "v_cvt_pk_bf16_f32 v86, v36, v37\n\t" \
  "v_cvt_pk_bf16_f32 v87, v38, v39\n\t" \
  "s_waitcnt lgkmcnt(1)\n\t" \
  "v_mfma_f32_16x16x32_bf16 v[20:23], v[216:219], v[178:181], v[20:23]\n\t" \
  "v_mfma_f32_16x16x32_bf16 v[44:47], v[216:219], v[186:189], v[44:47]\n\t" \
  "v_mfma_f32_16x16x32_bf16 v[68:71], v[216:219], v[194:197], v[68:71]\n\t" \
  "v_mfma_f32_16x16x32_bf16 v[92:95], v[216:219], v[202:205], v[92:95]\n\t" \
  "v_cvt_pk_bf16_f32 v64, v64, v65\n\t" \
  "v_cvt_pk_bf16_f32 v65, v66, v67\n\t" \
  "s_nop 4\n\t" \
  "v_cvt_pk_bf16_f32 v66, v68, v69\n\t" \
  "v_cvt_pk_bf16_f32 v67, v70, v71\n\t" \
  "v_cvt_pk_bf16_f32 v68, v16, v17\n\t" \
  "v_cvt_pk_bf16_f32 v69, v18, v19\n\t" \
  "v_cvt_pk_bf16_f32 v70, v20, v21\n\t" \
  "v_cvt_pk_bf16_f32 v71, v22, v23\n\t" \
  "v_cvt_pk_bf16_f32 v88, v88, v89\n\t" \
  "v_cvt_pk_bf16_f32 v89, v90, v91\n\t" \
  "v_cvt_pk_bf16_f32 v90, v92, v93\n\t" \
  "v_cvt_pk_bf16_f32 v91, v94, v95\n\t" \
  "v_cvt_pk_bf16_f32 v92, v40, v41\n\t" \
  "v_cvt_pk_bf16_f32 v93, v42, v43\n\t" \
  "v_cvt_pk_bf16_f32 v94, v44, v45\n\t" \
  "v_cvt_pk_bf16_f32 v95, v46, v47\n\t" \
  "s_lshl_b32 s57, s57, 1\n\t" \
  "s_add_u32 s61, s28, s65\n\t" \
  "s_add_u32 s62, s29, s65\n\t" \
  "s_add_u32 s14, s12, s65\n\t" \
  "s_add_u32 s15, s13, s65\n\t" \
  "v_lshlrev_b32 v198, 1, v251\n\t" \
  "global_load_dwordx4 v[192:195], v198, s[66:67]\n\t" \
  "s_branch .Lcnx_mid\n\t" \
  ".Lcnx_f0_0:\n\t" \
  "s_mov_b32 m0, s44\n\t" \
  "s_nop 0\n\t" \
  "buffer_load_dwordx4 v252, s[16:19], s33 offen lds\n\t" \
  "buffer_load_dwordx4 v252, s[16:19], s34 offen offset:1024 lds\n\t" \
  "buffer_load_dwordx4 v252, s[16:19], s35 offen offset:2048 lds\n\t" \
  "s_add_u32 s33, s33, s32\n\t" \
  "s_add_u32 s34, s34, s32\n\t" \
  "s_add_u32 s35, s35, s32\n\t" \
  "s_branch .Lcnx_g0_0\n\t" \
  ".Lcnx_f0_1:\n\t" \
  "s_mov_b32 m0, s45\n\t" \
  "s_nop 0\n\t" \
  "buffer_load_dwordx4 v252, s[16:19], s33 offen lds\n\t" \
  "buffer_load_dwordx4 v252, s[16:19], s34 offen offset:1024 lds\n\t" \
  "buffer_load_dwordx4 v252, s[16:19], s35 offen offset:2048 lds\n\t" \
  "s_add_u32 s33, s33, s32\n\t" \
  "s_add_u32 s34, s34, s32\n\t" \
  "s_add_u32 s35, s35, s32\n\t" \
  "s_branch .Lcnx_g0_1\n\t" \
  ".Lcnx_w0_2:\n\t" \
  "ds_read_b32 v246, v251 offset:47144\n\t" \
  "s_waitcnt lgkmcnt(0)\n\t" \
  "v_readfirstlane_b32 s4, v246\n\t" \
  ".Lcnx_f0_2:\n\t" \
  "s_cmp_lt_u32 s4, 3072\n\t" \
  "s_cbranch_scc1 .Lcnx_w0_2\n\t" \
  "s_cmp_lg_u32 s37, 0\n\t" \
  "s_cbranch_scc1 .Lcnx_g0_2\n\t" \
  "s_mov_b32 m0, s46\n\t" \
  "s_nop 0\n\t" \
  "buffer_load_dwordx4 v252, s[16:19], s33 offen lds\n\t" \
  "buffer_load_dwordx4 v252, s[16:19], s34 offen offset:1024 lds\n\t" \
  "buffer_load_dwordx4 v252, s[16:19], s35 offen offset:2048 lds\n\t" \
  "s_add_u32 s33, s33, s32\n\t" \
  "s_add_u32 s34, s34, s32\n\t" \
  "s_add_u32 s35, s35, s32\n\t" \
  "s_branch .Lcnx_g0_2\n\t" \
  ".Lcnx_w0_3:\n\t" \
  "ds_read_b32 v246, v251 offset:47152\n\t" \
  "s_waitcnt lgkmcnt(0)\n\t" \
  "v_readfirstlane_b32 s4, v246\n\t" \
  ".Lcnx_f0_3:\n\t" \
  "s_cmp_lt_u32 s4, 3072\n\t" \
  "s_cbranch_scc1 .Lcnx_w0_3\n\t" \
  "s_cmp_lg_u32 s37, 0\n\t" \
  "s_cbranch_scc1 .Lcnx_g0_3\n\t" \
  "s_mov_b32 m0, s47\n\t" \
  "s_nop 0\n\t" \
  "buffer_load_dwordx4 v252, s[16:19], s33 offen lds\n\t" \
  "buffer_load_dwordx4 v252, s[16:19], s34 offen offset:1024 lds\n\t" \
  "buffer_load_dwordx4 v252, s[16:19], s35 offen offset:2048 lds\n\t" \
  "s_add_u32 s33, s33, s32\n\t" \
  "s_add_u32 s34, s34, s32\n\t" \
  "s_add_u32 s35, s35, s32\n\t" \
  "s_branch .Lcnx_g0_3\n\t" \
  ".Lcnx_h3:\n\t" \
  "s_mov_b32 m0, 107744\n\t" \
  "s_add_u32 s30, s30, s31\n\t" \
  "buffer_load_dword v252, s[8:11], s30 offen lds\n\t" \
  "s_branch .Lcnx_i3\n\t" \
  ".Lcnx_w0_4:\n\t" \
  "ds_read_b32 v246, v251 offset:47160\n\t" \
  "s_waitcnt lgkmcnt(0)\n\t" \
  "v_readfirstlane_b32 s4, v246\n\t" \
  ".Lcnx_f0_4:\n\t" \
  "s_cmp_lt_u32 s4, 3072\n\t" \
  "s_cbranch_scc1 .Lcnx_w0_4\n\t" \
  "s_cmp_lg_u32 s37, 0\n\t" \
  "s_cbranch_scc1 .Lcnx_g0_4\n\t" \
  "s_mov_b32 m0, s44\n\t" \
  "s_nop 0\n\t" \
  "buffer_load_dwordx4 v252, s[16:19], s33 offen lds\n\t" \
  "buffer_load_dwordx4 v252, s[16:19], s34 offen offset:1024 lds\n\t" \
  "buffer_load_dwordx4 v252, s[16:19], s35 offen offset:2048 lds\n\t" \
  "s_add_u32 s33, s33, s32\n\t" \
  "s_add_u32 s34, s34, s32\n\t" \
  "s_add_u32 s35, s35, s32\n\t" \
  "s_branch .Lcnx_g0_4\n\t" \
  ".Lcnx_h4:\n\t" \
  "s_mov_b32 m0, 107744\n\t" \
  "s_add_u32 s30, s30, s31\n\t" \
  "buffer_load_dword v252, s[8:11], s30 offen lds\n\t" \
  "s_branch .Lcnx_i4\n\t" \
  ".Lcnx_w0_5:\n\t" \
  "ds_read_b32 v246, v251 offset:47168\n\t" \
  "s_waitcnt lgkmcnt(0)\n\t" \
  "v_readfirstlane_b32 s4, v246\n\t" \
  ".Lcnx_f0_5:\n\t" \
  "s_cmp_lt_u32 s4, 3072\n\t" \
  "s_cbranch_scc1 .Lcnx_w0_5\n\t" \
  "s_cmp_lg_u32 s37, 0\n\t" \
  "s_cbranch_scc1 .Lcnx_g0_5\n\t" \
  "s_mov_b32 m0, s45\n\t" \
  "s_nop 0\n\t" \
  "buffer_load_dwordx4 v252, s[16:19], s33 offen lds\n\t" \
  "buffer_load_dwordx4 v252, s[16:19], s34 offen offset:1024 lds\n\t" \
  "buffer_load_dwordx4 v252, s[16:19], s35 offen offset:2048 lds\n\t" \
  "s_add_u32 s33, s33, s32\n\t" \
  "s_add_u32 s34, s34, s32\n\t" \
  "s_add_u32 s35, s35, s32\n\t" \
  "s_branch .Lcnx_g0_5\n\t" \
  ".Lcnx_h5:\n\t" \
  "s_mov_b32 m0, 107744\n\t" \
  "s_add_u32 s30, s30, s31\n\t" \
  "buffer_load_dword v252, s[8:11], s30 offen lds\n\t" \
  "s_branch .Lcnx_i5\n\t" \
  ".Lcnx_w0_6:\n\t" \
  "ds_read_b32 v246, v251 offset:47176\n\t" \
  "s_waitcnt lgkmcnt(0)\n\t" \
  "v_readfirstlane_b32 s4, v246\n\t" \
  ".Lcnx_f0_6:\n\t" \
  "s_cmp_lt_u32 s4, 3072\n\t" \
  "s_cbranch_scc1 .Lcnx_w0_6\n\t" \
  "s_cmp_lg_u32 s37, 0\n\t" \
  "s_cbranch_scc1 .Lcnx_g0_6\n\t" \
  "s_mov_b32 m0, s46\n\t" \
  "s_nop 0\n\t" \
  "buffer_load_dwordx4 v252, s[16:19], s33 offen lds\n\t" \
  "buffer_load_dwordx4 v252, s[16:19], s34 offen offset:1024 lds\n\t" \
  "buffer_load_dwordx4 v252, s[16:19], s35 offen offset:2048 lds\n\t" \
  "s_add_u32 s33, s33, s32\n\t" \
  "s_add_u32 s34, s34, s32\n\t" \
  "s_add_u32 s35, s35, s32\n\t" \
  "s_branch .Lcnx_g0_6\n\t" \
  ".Lcnx_h6:\n\t" \
  "s_mov_b32 m0, 107744\n\t" \
  "s_add_u32 s30, s30, s31\n\t" \
  "buffer_load_dword v252, s[8:11], s30 offen lds\n\t" \
  "s_branch .Lcnx_i6\n\t" \
  ".Lcnx_w0_7:\n\t" \
  "ds_read_b32 v246, v251 offset:47184\n\t" \
  "s_waitcnt lgkmcnt(0)\n\t" \
  "v_readfirstlane_b32 s4, v246\n\t" \
  ".Lcnx_f0_7:\n\t" \
  "s_cmp_lt_u32 s4, 3072\n\t" \
  "s_cbranch_scc1 .Lcnx_w0_7\n\t" \
  "s_cmp_lg_u32 s37, 0\n\t" \
  "s_cbranch_scc1 .Lcnx_g0_7\n\t" \
  "s_mov_b32 m0, s47\n\t" \
  "s_nop 0\n\t" \
  "buffer_load_dwordx4 v252, s[16:19], s33 offen lds\n\t" \
  "buffer_load_dwordx4 v252, s[16:19], s34 offen offset:1024 lds\n\t" \
  "buffer_load_dwordx4 v252, s[16:19], s35 offen offset:2048 lds\n\t" \
  "s_add_u32 s33, s33, s32\n\t" \
  "s_add_u32 s34, s34, s32\n\t" \
  "s_add_u32 s35, s35, s32\n\t" \
  "s_branch .Lcnx_g0_7\n\t" \
  ".Lcnx_h7:\n\t" \
  "s_mov_b32 m0, 107744\n\t" \
  "s_add_u32 s30, s30, s31\n\t" \
  "buffer_load_dword v252, s[8:11], s30 offen lds\n\t" \
  "s_branch .Lcnx_i7\n\t" \
  ".Lcnx_w0_8:\n\t" \
  "ds_read_b32 v246, v251 offset:47192\n\t" \
  "s_waitcnt lgkmcnt(0)\n\t" \
  "v_readfirstlane_b32 s4, v246\n\t" \
  ".Lcnx_f0_8:\n\t" \
  "s_cmp_lt_u32 s4, 3072\n\t" \
  "s_cbranch_scc1 .Lcnx_w0_8\n\t" \
  "s_cmp_lg_u32 s37, 0\n\t" \
  "s_cbranch_scc1 .Lcnx_g0_8\n\t" \
  "s_mov_b32 m0, s44\n\t" \
  "s_nop 0\n\t" \
  "buffer_load_dwordx4 v252, s[16:19], s33 offen lds\n\t" \
  "buffer_load_dwordx4 v252, s[16:19], s34 offen offset:1024 lds\n\t" \
  "buffer_load_dwordx4 v252, s[16:19], s35 offen offset:2048 lds\n\t" \
  "s_add_u32 s33, s33, s32\n\t" \
  "s_add_u32 s34, s34, s32\n\t" \
  "s_add_u32 s35, s35, s32\n\t" \
  "s_branch .Lcnx_g0_8\n\t" \
  ".Lcnx_h8:\n\t" \
  "s_mov_b32 m0, 107744\n\t" \
  "s_add_u32 s30, s30, s31\n\t" \
  "buffer_load_dword v252, s[8:11], s30 offen lds\n\t" \
  "s_branch .Lcnx_i8\n\t" \
  ".Lcnx_w0_9:\n\t" \
  "ds_read_b32 v246, v251 offset:47200\n\t" \
  "s_waitcnt lgkmcnt(0)\n\t" \
  "v_readfirstlane_b32 s4, v246\n\t" \
  ".Lcnx_f0_9:\n\t" \
  "s_cmp_lt_u32 s4, 3072\n\t" \
  "s_cbranch_scc1 .Lcnx_w0_9\n\t" \
  "s_cmp_lg_u32 s37, 0\n\t" \
  "s_cbranch_scc1 .Lcnx_g0_9\n\t" \
  "s_mov_b32 m0, s45\n\t" \
  "s_nop 0\n\t" \
  "buffer_load_dwordx4 v252, s[16:19], s33 offen lds\n\t" \
  "buffer_load_dwordx4 v252, s[16:19], s34 offen offset:1024 lds\n\t" \
  "buffer_load_dwordx4 v252, s[16:19], s35 offen offset:2048 lds\n\t" \
  "s_add_u32 s33, s33, s32\n\t" \
  "s_add_u32 s34, s34, s32\n\t" \
  "s_add_u32 s35, s35, s32\n\t" \
  "s_sub_u32 s33, s33, s48\n\t" \
  "s_sub_u32 s34, s34, s48\n\t" \
  "s_sub_u32 s35, s35, s48\n\t" \
  "s_branch .Lcnx_g0_9\n\t" \
  ".Lcnx_h9:\n\t" \
  "s_mov_b32 m0, 107744\n\t" \
  "s_add_u32 s30, s30, s31\n\t" \
  "buffer_load_dword v252, s[8:11], s30 offen lds\n\t" \
  "s_branch .Lcnx_i9\n\t" \
  ".Lcnx_w0_10:\n\t" \
  "ds_read_b32 v246, v251 offset:47208\n\t" \
  "s_waitcnt lgkmcnt(0)\n\t" \
  "v_readfirstlane_b32 s4, v246\n\t" \
  ".Lcnx_f0_10:\n\t" \
  "s_cmp_lt_u32 s4, 3072\n\t" \
  "s_cbranch_scc1 .Lcnx_w0_10\n\t" \
  "s_cmp_lg_u32 s37, 0\n\t" \
  "s_cbranch_scc1 .Lcnx_g0_10\n\t" \
  "s_mov_b32 m0, s46\n\t" \
  "s_nop 0\n\t" \
  "buffer_load_dwordx4 v252, s[16:19], s33 offen lds\n\t" \
  "buffer_load_dwordx4 v252, s[16:19], s34 offen offset:1024 lds\n\t" \
  "buffer_load_dwordx4 v252, s[16:19], s35 offen offset:2048 lds\n\t" \
  "s_add_u32 s33, s33, s32\n\t" \
  "s_add_u32 s34, s34, s32\n\t" \
  "s_add_u32 s35, s35, s32\n\t" \
  "s_branch .Lcnx_g0_10\n\t" \
  ".Lcnx_h10:\n\t" \
  "s_mov_b32 m0, 107744\n\t" \
  "s_add_u32 s30, s30, s31\n\t" \
  "buffer_load_dword v252, s[8:11], s30 offen lds\n\t" \
  "s_branch .Lcnx_i10\n\t" \
  ".Lcnx_w0_11:\n\t" \
  "ds_read_b32 v246, v251 offset:47216\n\t" \
  "s_waitcnt lgkmcnt(0)\n\t" \
  "v_readfirstlane_b32 s4, v246\n\t" \
  ".Lcnx_f0_11:\n\t" \
  "s_cmp_lt_u32 s4, 3072\n\t" \
  "s_cbranch_scc1 .Lcnx_w0_11\n\t" \
  "s_cmp_lg_u32 s37, 0\n\t" \
  "s_cbranch_scc1 .Lcnx_g0_11\n\t" \
  "s_mov_b32 m0, s47\n\t" \
  "s_nop 0\n\t" \
  "buffer_load_dwordx4 v252, s[16:19], s33 offen lds\n\t" \
  "buffer_load_dwordx4 v252, s[16:19], s34 offen offset:1024 lds\n\t" \
  "buffer_load_dwordx4 v252, s[16:19], s35 offen offset:2048 lds\n\t" \
  "s_add_u32 s33, s33, s32\n\t" \
  "s_add_u32 s34, s34, s32\n\t" \
  "s_add_u32 s35, s35, s32\n\t" \
  "s_sub_u32 s33, s33, s49\n\t" \
  "s_sub_u32 s34, s34, s49\n\t" \
  "s_sub_u32 s35, s35, s49\n\t" \
  "s_branch .Lcnx_g0_11\n\t" \
  ".Lcnx_h11:\n\t" \
  "s_mov_b32 m0, 107744\n\t" \
  "s_add_u32 s30, s30, s31\n\t" \
  "buffer_load_dword v252, s[8:11], s30 offen lds\n\t" \
  "s_branch .Lcnx_i11\n\t" \
  ".Lcnx_w0_12:\n\t" \
  "ds_read_b32 v246, v251 offset:47224\n\t" \
  "s_waitcnt lgkmcnt(0)\n\t" \
  "v_readfirstlane_b32 s4, v246\n\t" \
  "s_cmp_lt_u32 s4, 3072\n\t" \
  "s_cbranch_scc1 .Lcnx_w0_12\n\t" \
  "s_branch .Lcnx_g0_12\n\t" \
  ".Lcnx_h12:\n\t" \
  "s_mov_b32 m0, 107744\n\t" \
  "s_add_u32 s30, s30, s31\n\t" \
  "buffer_load_dword v252, s[8:11], s30 offen lds\n\t" \
  "s_branch .Lcnx_i12\n\t" \
  ".Lcnx_w0_13:\n\t" \
  "ds_read_b32 v246, v251 offset:47232\n\t" \
  "s_waitcnt lgkmcnt(0)\n\t" \
  "v_readfirstlane_b32 s4, v246\n\t" \
  "s_cmp_lt_u32 s4, 3072\n\t" \
  "s_cbranch_scc1 .Lcnx_w0_13\n\t" \
  "s_branch .Lcnx_g0_13\n\t" \
  ".Lcnx_h13:\n\t" \
  "s_mov_b32 m0, 107744\n\t" \
  "s_add_u32 s30, s30, s31\n\t" \
  "buffer_load_dword v252, s[8:11], s30 offen lds\n\t" \
  "s_branch .Lcnx_i13\n\t" \
  ".Lcnx_f1_0:\n\t" \
  "s_mov_b32 m0, s44\n\t" \
  "s_nop 0\n\t" \
  "buffer_load_dwordx4 v252, s[16:19], s33 offen lds\n\t" \
  "buffer_load_dwordx4 v252, s[16:19], s34 offen offset:1024 lds\n\t" \
  "buffer_load_dwordx4 v252, s[16:19], s35 offen offset:2048 lds\n\t" \
  "s_add_u32 s33, s33, s32\n\t" \
  "s_add_u32 s34, s34, s32\n\t" \
  "s_add_u32 s35, s35, s32\n\t" \
  "s_branch .Lcnx_g1_0\n\t" \
  ".Lcnx_f1_1:\n\t" \
  "s_mov_b32 m0, s45\n\t" \
  "s_nop 0\n\t" \
  "buffer_load_dwordx4 v252, s[16:19], s33 offen lds\n\t" \
  "buffer_load_dwordx4 v252, s[16:19], s34 offen offset:1024 lds\n\t" \
  "buffer_load_dwordx4 v252, s[16:19], s35 offen offset:2048 lds\n\t" \
  "s_add_u32 s33, s33, s32\n\t" \
  "s_add_u32 s34, s34, s32\n\t" \
  "s_add_u32 s35, s35, s32\n\t" \
  "s_branch .Lcnx_g1_1\n\t" \
  ".Lcnx_w1_2:\n\t" \
  "ds_read_b32 v246, v251 offset:47144\n\t" \
  "s_waitcnt lgkmcnt(0)\n\t" \
  "v_readfirstlane_b32 s4, v246\n\t" \
  ".Lcnx_f1_2:\n\t" \
  "s_cmp_lt_u32 s4, 6144\n\t" \
  "s_cbranch_scc1 .Lcnx_w1_2\n\t" \
  "s_cmp_lg_u32 s37, 0\n\t" \
  "s_cbranch_scc1 .Lcnx_g1_2\n\t" \
  "s_mov_b32 m0, s46\n\t" \
  "s_nop 0\n\t" \
  "buffer_load_dwordx4 v252, s[16:19], s33 offen lds\n\t" \
  "buffer_load_dwordx4 v252, s[16:19], s34 offen offset:1024 lds\n\t" \
  "buffer_load_dwordx4 v252, s[16:19], s35 offen offset:2048 lds\n\t" \
  "s_add_u32 s33, s33, s32\n\t" \
  "s_add_u32 s34, s34, s32\n\t" \
  "s_add_u32 s35, s35, s32\n\t" \
  "s_branch .Lcnx_g1_2\n\t" \
  ".Lcnx_w1_3:\n\t" \
  "ds_read_b32 v246, v251 offset:47152\n\t" \
  "s_waitcnt lgkmcnt(0)\n\t" \
  "v_readfirstlane_b32 s4, v246\n\t" \
  ".Lcnx_f1_3:\n\t" \
  "s_cmp_lt_u32 s4, 6144\n\t" \
  "s_cbranch_scc1 .Lcnx_w1_3\n\t" \
  "s_cmp_lg_u32 s37, 0\n\t" \
  "s_cbranch_scc1 .Lcnx_g1_3\n\t" \
  "s_mov_b32 m0, s47\n\t" \
  "s_nop 0\n\t" \
  "buffer_load_dwordx4 v252, s[16:19], s33 offen lds\n\t" \
  "buffer_load_dwordx4 v252, s[16:19], s34 offen offset:1024 lds\n\t" \
  "buffer_load_dwordx4 v252, s[16:19], s35 offen offset:2048 lds\n\t" \
  "s_add_u32 s33, s33, s32\n\t" \
  "s_add_u32 s34, s34, s32\n\t" \
  "s_add_u32 s35, s35, s32\n\t" \
  "s_branch .Lcnx_g1_3\n\t" \
  ".Lcnx_w1_4:\n\t" \
  "ds_read_b32 v246, v251 offset:47160\n\t" \
  "s_waitcnt lgkmcnt(0)\n\t" \
  "v_readfirstlane_b32 s4, v246\n\t" \
  ".Lcnx_f1_4:\n\t" \
  "s_cmp_lt_u32 s4, 6144\n\t" \
  "s_cbranch_scc1 .Lcnx_w1_4\n\t" \
  "s_cmp_lg_u32 s37, 0\n\t" \
  "s_cbranch_scc1 .Lcnx_g1_4\n\t" \
  "s_mov_b32 m0, s44\n\t" \
  "s_nop 0\n\t" \
  "buffer_load_dwordx4 v252, s[16:19], s33 offen lds\n\t" \
  "buffer_load_dwordx4 v252, s[16:19], s34 offen offset:1024 lds\n\t" \
  "buffer_load_dwordx4 v252, s[16:19], s35 offen offset:2048 lds\n\t" \
  "s_add_u32 s33, s33, s32\n\t" \
  "s_add_u32 s34, s34, s32\n\t" \
  "s_add_u32 s35, s35, s32\n\t" \
  "s_add_u32 m0, s44, s50\n\t" \
  "s_add_u32 s4, s33, s51\n\t" \
  "buffer_load_dwordx4 v252, s[16:19], s4 offen offset:0 lds\n\t" \
  "s_add_u32 s4, s34, s51\n\t" \
  "buffer_load_dwordx4 v252, s[16:19], s4 offen offset:1024 lds\n\t" \
  "s_add_u32 s4, s35, s51\n\t" \
  "buffer_load_dwordx4 v252, s[16:19], s4 offen offset:2048 lds\n\t" \
  "s_branch .Lcnx_g1_4\n\t" \
  ".Lcnx_w1_5:\n\t" \
  "ds_read_b32 v246, v251 offset:47168\n\t" \
  "s_waitcnt lgkmcnt(0)\n\t" \
  "v_readfirstlane_b32 s4, v246\n\t" \
  ".Lcnx_f1_5:\n\t" \
  "s_cmp_lt_u32 s4, 6144\n\t" \
  "s_cbranch_scc1 .Lcnx_w1_5\n\t" \
  "s_cmp_lg_u32 s37, 0\n\t" \
  "s_cbranch_scc1 .Lcnx_g1_5\n\t" \
  "s_mov_b32 m0, s45\n\t" \
  "s_nop 0\n\t" \
  "buffer_load_dwordx4 v252, s[16:19], s33 offen lds\n\t" \
  "buffer_load_dwordx4 v252, s[16:19], s34 offen offset:1024 lds\n\t" \
  "buffer_load_dwordx4 v252, s[16:19], s35 offen offset:2048 lds\n\t" \
  "s_add_u32 s33, s33, s32\n\t" \
  "s_add_u32 s34, s34, s32\n\t" \
  "s_add_u32 s35, s35, s32\n\t" \
  "s_add_u32 m0, s45, s50\n\t" \
  "s_add_u32 s4, s33, s51\n\t" \
  "buffer_load_dwordx4 v252, s[16:19], s4 offen offset:0 lds\n\t" \
  "s_add_u32 s4, s34, s51\n\t" \
  "buffer_load_dwordx4 v252, s[16:19], s4 offen offset:1024 lds\n\t" \
  "s_add_u32 s4, s35, s51\n\t" \
  "buffer_load_dwordx4 v252, s[16:19], s4 offen offset:2048 lds\n\t" \
  "s_branch .Lcnx_g1_5\n\t" \
  ".Lcnx_w1_6:\n\t" \
  "ds_read_b32 v246, v251 offset:47176\n\t" \
  "s_waitcnt lgkmcnt(0)\n\t" \
  "v_readfirstlane_b32 s4, v246\n\t" \
  ".Lcnx_f1_6:\n\t" \
  "s_cmp_lt_u32 s4, 6144\n\t" \
  "s_cbranch_scc1 .Lcnx_w1_6\n\t" \
  "s_cmp_lg_u32 s37, 0\n\t" \
  "s_cbranch_scc1 .Lcnx_g1_6\n\t" \
  "s_mov_b32 m0, s46\n\t" \
  "s_nop 0\n\t" \
  "buffer_load_dwordx4 v252, s[16:19], s33 offen lds\n\t" \
  "buffer_load_dwordx4 v252, s[16:19], s34 offen offset:1024 lds\n\t" \
  "buffer_load_dwordx4 v252, s[16:19], s35 offen offset:2048 lds\n\t" \
  "s_add_u32 s33, s33, s32\n\t" \
  "s_add_u32 s34, s34, s32\n\t" \
  "s_add_u32 s35, s35, s32\n\t" \
  "s_cmp_lt_u32 s39, 1024\n\t" \
  "s_cbranch_scc1 .Lcnx_g1_6\n\t" \
  "s_add_u32 m0, s46, s50\n\t" \
  "s_add_u32 s4, s33, s51\n\t" \
  "buffer_load_dwordx4 v252, s[16:19], s4 offen offset:0 lds\n\t" \
  "s_add_u32 s4, s34, s51\n\t" \
  "buffer_load_dwordx4 v252, s[16:19], s4 offen offset:1024 lds\n\t" \
  "s_add_u32 s4, s35, s51\n\t" \
  "buffer_load_dwordx4 v252, s[16:19], s4 offen offset:2048 lds\n\t" \
  "s_branch .Lcnx_g1_6\n\t" \
  ".Lcnx_w1_7:\n\t" \
  "ds_read_b32 v246, v251 offset:47184\n\t" \
  "s_waitcnt lgkmcnt(0)\n\t" \
  "v_readfirstlane_b32 s4, v246\n\t" \
  ".Lcnx_f1_7:\n\t" \
  "s_cmp_lt_u32 s4, 6144\n\t" \
  "s_cbranch_scc1 .Lcnx_w1_7\n\t" \
  "s_cmp_lg_u32 s37, 0\n\t" \
  "s_cbranch_scc1 .Lcnx_g1_7\n\t" \
  "s_mov_b32 m0, s47\n\t" \
  "s_nop 0\n\t" \
  "buffer_load_dwordx4 v252, s[16:19], s33 offen lds\n\t" \
  "buffer_load_dwordx4 v252, s[16:19], s34 offen offset:1024 lds\n\t" \
  "buffer_load_dwordx4 v252, s[16:19], s35 offen offset:2048 lds\n\t" \
  "s_add_u32 s33, s33, s32\n\t" \
  "s_add_u32 s34, s34, s32\n\t" \
  "s_add_u32 s35, s35, s32\n\t" \
  "s_cmp_lt_u32 s39, 1024\n\t" \
  "s_cbranch_scc1 .Lcnx_g1_7\n\t" \
  "s_add_u32 m0, s47, s50\n\t" \
  "s_add_u32 s4, s33, s51\n\t" \
  "buffer_load_dwordx4 v252, s[16:19], s4 offen offset:0 lds\n\t" \
  "s_add_u32 s4, s34, s51\n\t" \
  "buffer_load_dwordx4 v252, s[16:19], s4 offen offset:1024 lds\n\t" \
  "s_add_u32 s4, s35, s51\n\t" \
  "buffer_load_dwordx4 v252, s[16:19], s4 offen offset:2048 lds\n\t" \
  "s_branch .Lcnx_g1_7\n\t" \
  ".Lcnx_w1_8:\n\t" \
  "ds_read_b32 v246, v251 offset:47192\n\t" \
  "s_waitcnt lgkmcnt(0)\n\t" \
  "v_readfirstlane_b32 s4, v246\n\t" \
  "s_cmp_lt_u32 s4, 6144\n\t" \
  "s_cbranch_scc1 .Lcnx_w1_8\n\t" \
  "s_branch .Lcnx_g1_8\n\t" \
  ".Lcnx_w1_9:\n\t" \
  "ds_read_b32 v246, v251 offset:47200\n\t" \
  "s_waitcnt lgkmcnt(0)\n\t" \
  "v_readfirstlane_b32 s4, v246\n\t" \
  "s_cmp_lt_u32 s4, 6144\n\t" \
  "s_cbranch_scc1 .Lcnx_w1_9\n\t" \
  "s_branch .Lcnx_g1_9\n\t" \
  ".Lcnx_mid:\n\t" \
  "v_add_u32 v232, 56880, v253\n\t" \
  "v_add_u32 v233, s39, v251\n\t" \
  "v_mfma_f32_32x32x16_bf16 v[176:191], v[242:245], v[240:243], 0\n\t" \
  "ds_read_b64 v[200:201], v253 offset:0\n\t" \
  "ds_read_b64 v[202:203], v253 offset:400\n\t" \
  "ds_read_b64 v[204:205], v253 offset:800\n\t" \
  "ds_read_b64 v[206:207], v253 offset:1200\n\t" \
  "ds_read_b64 v[208:209], v253 offset:8\n\t" \
  "ds_read_b64 v[210:211], v253 offset:408\n\t" \
  "ds_read_b64 v[212:213], v253 offset:808\n\t" \
  "ds_read_b64 v[214:215], v253 offset:1208\n\t" \
  "ds_read_b64 v[216:217], v253 offset:16\n\t" \
  "ds_read_b64 v[218:219], v253 offset:416\n\t" \
  "ds_read_b64 v[220:221], v253 offset:816\n\t" \
  "ds_read_b64 v[222:223], v253 offset:1216\n\t" \
  "s_waitcnt lgkmcnt(15)\n\t" \
  "v_dot2_f32_bf16 v96, v98, s68, 0\n\t" \
  "v_dot2_f32_bf16 v97, v98, s69, 0\n\t" \
  "v_dot2_f32_bf16 v98, v99, s68, 0\n\t" \
  "v_dot2_f32_bf16 v99, v99, s69, 0\n\t" \
  "v_dot2_f32_bf16 v112, v114, s68, 0\n\t" \
  "v_dot2_f32_bf16 v113, v114, s69, 0\n\t" \
  "v_dot2_f32_bf16 v114, v115, s68, 0\n\t" \
  "v_dot2_f32_bf16 v115, v115, s69, 0\n\t" \
  "v_dot2_f32_bf16 v128, v130, s68, 0\n\t" \
  "v_dot2_f32_bf16 v129, v130, s69, 0\n\t" \
  "v_dot2_f32_bf16 v130, v131, s68, 0\n\t" \
  "v_dot2_f32_bf16 v131, v131, s69, 0\n\t" \
  "s_waitcnt lgkmcnt(8)\n\t" \
  "v_smfmac_f32_16x16x64_bf16 v[144:147], v[96:99], v[200:207], v236\n\t" \
  "v_dot2_f32_bf16 v100, v102, s68, 0\n\t" \
  "v_dot2_f32_bf16 v101, v102, s69, 0\n\t" \
  "v_dot2_f32_bf16 v102, v103, s68, 0\n\t" \
  "v_dot2_f32_bf16 v103, v103, s69, 0\n\t" \
  "s_waitcnt vmcnt(5) lgkmcnt(0)\n\t" \
  "s_barrier\n\t" \
  "ds_read_b64 v[224:225], v253 offset:16400\n\t" \
  "ds_read_b64 v[226:227], v253 offset:16800\n\t" \
  "ds_read_b64 v[228:229], v253 offset:17200\n\t" \
  "ds_read_b64 v[230:231], v253 offset:17600\n\t" \
  "s_add_u32 m0, s25, 81480\n\t" \
  "s_add_u32 s24, s24, s26\n\t" \
  "v_smfmac_f32_16x16x64_bf16 v[148:151], v[112:115], v[208:215], v236\n\t" \
  "buffer_load_dwordx4 v254, s[8:11], s24 offen lds\n\t" \
  "ds_read_b64 v[200:201], v253 offset:16408\n\t" \
  "ds_read_b64 v[202:203], v253 offset:16808\n\t" \
  "ds_read_b64 v[204:205], v253 offset:17208\n\t" \
  "ds_read_b64 v[206:207], v253 offset:17608\n\t" \
  "v_dot2_f32_bf16 v116, v118, s68, 0\n\t" \
  "v_dot2_f32_bf16 v117, v118, s69, 0\n\t" \
  "v_dot2_f32_bf16 v118, v119, s68, 0\n\t" \
  "v_dot2_f32_bf16 v119, v119, s69, 0\n\t" \
  "s_add_u32 m0, s25, 89680\n\t" \
  "s_add_u32 s24, s24, s26\n\t" \
  "v_smfmac_f32_16x16x64_bf16 v[152:155], v[128:131], v[216:223], v236\n\t" \
  "buffer_load_dwordx4 v254, s[8:11], s24 offen lds\n\t" \
  "ds_read_b64 v[208:209], v253 offset:16416\n\t" \
  "ds_read_b64 v[210:211], v253 offset:16816\n\t" \
  "ds_read_b64 v[212:213], v253 offset:17216\n\t" \
  "ds_read_b64 v[214:215], v253 offset:17616\n\t" \
  "v_dot2_f32_bf16 v132, v134, s68, 0\n\t" \
  "v_dot2_f32_bf16 v133, v134, s69, 0\n\t" \
  "v_dot2_f32_bf16 v134, v135, s68, 0\n\t" \
  "v_dot2_f32_bf16 v135, v135, s69, 0\n\t" \
  "s_waitcnt lgkmcnt(8)\n\t" \
  "s_add_u32 m0, s25, 97880\n\t" \
  "s_add_u32 s24, s24, s26\n\t" \
  "v_smfmac_f32_16x16x64_bf16 v[156:159], v[96:99], v[224:231], v236\n\t" \
  "buffer_load_dwordx4 v254, s[8:11], s24 offen lds\n\t" \
  "v_dot2_f32_bf16 v104, v106, s68, 0\n\t" \
  "v_dot2_f32_bf16 v105, v106, s69, 0\n\t" \
  "v_dot2_f32_bf16 v106, v107, s68, 0\n\t" \
  "v_dot2_f32_bf16 v107, v107, s69, 0\n\t" \
  "v_smfmac_f32_16x16x64_bf16 v[144:147], v[100:103], v[224:231], v236\n\t" \
  "s_waitcnt vmcnt(6) lgkmcnt(0)\n\t" \
  "s_barrier\n\t" \
  "ds_read_b64 v[216:217], v253 offset:32800\n\t" \
  "ds_read_b64 v[218:219], v253 offset:33200\n\t" \
  "ds_read_b64 v[220:221], v253 offset:33600\n\t" \
  "ds_read_b64 v[222:223], v253 offset:34000\n\t" \
  "s_add_u32 m0, s25, 0\n\t" \
  "s_add_u32 s24, s24, s26\n\t" \
  "v_smfmac_f32_16x16x64_bf16 v[160:163], v[112:115], v[200:207], v236\n\t" \
  "buffer_load_dwordx4 v254, s[8:11], s24 offen lds\n\t" \
  "ds_read_b64 v[224:225], v253 offset:32808\n\t" \
  "ds_read_b64 v[226:227], v253 offset:33208\n\t" \
  "v_dot2_f32_bf16 v120, v122, s68, 0\n\t" \
  "v_dot2_f32_bf16 v121, v122, s69, 0\n\t" \
  "v_dot2_f32_bf16 v122, v123, s68, 0\n\t" \
  "v_dot2_f32_bf16 v123, v123, s69, 0\n\t" \
  "v_smfmac_f32_16x16x64_bf16 v[148:151], v[116:119], v[200:207], v236\n\t" \
  "ds_read_b64 v[228:229], v253 offset:33608\n\t" \
  "ds_read_b64 v[230:231], v253 offset:34008\n\t" \
  "s_add_u32 m0, s25, 8200\n\t" \
  "s_add_u32 s24, s24, s26\n\t" \
  "v_smfmac_f32_16x16x64_bf16 v[164:167], v[128:131], v[208:215], v236\n\t" \
  "buffer_load_dwordx4 v254, s[8:11], s24 offen lds\n\t" \
  "ds_read_b64 v[200:201], v253 offset:32816\n\t" \
  "ds_read_b64 v[202:203], v253 offset:33216\n\t" \
  "v_dot2_f32_bf16 v136, v138, s68, 0\n\t" \
  "v_dot2_f32_bf16 v137, v138, s69, 0\n\t" \
  "v_dot2_f32_bf16 v138, v139, s68, 0\n\t" \
  "v_dot2_f32_bf16 v139, v139, s69, 0\n\t" \
  "v_mfma_f32_32x32x16_bf16 v[0:15], v[242:245], v[240:243], 0\n\t" \
  "v_smfmac_f32_16x16x64_bf16 v[152:155], v[132:135], v[208:215], v236\n\t" \
  "ds_read_b64 v[204:205], v253 offset:33616\n\t" \
  "s_waitcnt lgkmcnt(7)\n\t" \
  "v_smfmac_f32_16x16x64_bf16 v[168:171], v[96:99], v[216:223], v236\n\t" \
  "ds_read_b64 v[206:207], v253 offset:34016\n\t" \
  "v_dot2_f32_bf16 v108, v110, s68, 0\n\t" \
  "v_dot2_f32_bf16 v109, v110, s69, 0\n\t" \
  "v_dot2_f32_bf16 v110, v111, s68, 0\n\t" \
  "v_dot2_f32_bf16 v111, v111, s69, 0\n\t" \
  "s_add_u32 m0, s25, 16400\n\t" \
  "s_add_u32 s24, s24, s26\n\t" \
  "v_smfmac_f32_16x16x64_bf16 v[156:159], v[100:103], v[216:223], v236\n\t" \
  "buffer_load_dwordx4 v254, s[8:11], s24 offen lds\n\t" \
  "v_smfmac_f32_16x16x64_bf16 v[144:147], v[104:107], v[216:223], v236\n\t" \
  "s_waitcnt vmcnt(7) lgkmcnt(0)\n\t" \
  "s_barrier\n\t" \
  "v_smfmac_f32_16x16x64_bf16 v[172:175], v[112:115], v[224:231], v236\n\t" \
  "ds_read_b64 v[208:209], v232 offset:0\n\t" \
  "ds_read_b64 v[210:211], v232 offset:400\n\t" \
  "v_dot2_f32_bf16 v124, v126, s68, 0\n\t" \
  "v_dot2_f32_bf16 v125, v126, s69, 0\n\t" \
  "v_dot2_f32_bf16 v126, v127, s68, 0\n\t" \
  "v_dot2_f32_bf16 v127, v127, s69, 0\n\t" \
  "s_add_u32 m0, s25, 24600\n\t" \
  "s_add_u32 s24, s24, s26\n\t" \
  "v_smfmac_f32_16x16x64_bf16 v[160:163], v[116:119], v[224:231], v236\n\t" \
  "buffer_load_dwordx4 v254, s[8:11], s24 offen lds\n\t" \
  "ds_read_b64 v[212:213], v232 offset:800\n\t" \
  "ds_read_b64 v[214:215], v232 offset:1200\n\t" \
  "v_smfmac_f32_16x16x64_bf16 v[148:151], v[120:123], v[224:231], v236\n\t" \
  "ds_read_b64 v[216:217], v232 offset:8\n\t" \
  "ds_read_b64 v[218:219], v232 offset:408\n\t" \
  "v_smfmac_f32_16x16x64_bf16 v[176:179], v[128:131], v[200:207], v236\n\t" \
  "ds_read_b64 v[220:221], v232 offset:808\n\t" \
  "ds_read_b64 v[222:223], v232 offset:1208\n\t" \
  "v_dot2_f32_bf16 v140, v142, s68, 0\n\t" \
  "v_dot2_f32_bf16 v141, v142, s69, 0\n\t" \
  "v_dot2_f32_bf16 v142, v143, s68, 0\n\t" \
  "v_dot2_f32_bf16 v143, v143, s69, 0\n\t" \
  "v_smfmac_f32_16x16x64_bf16 v[164:167], v[132:135], v[200:207], v236\n\t" \
  "ds_read_b64 v[224:225], v232 offset:16\n\t" \
  "s_add_u32 m0, s25, 32800\n\t" \
  "s_add_u32 s24, s24, s26\n\t" \
  "v_smfmac_f32_16x16x64_bf16 v[152:155], v[136:139], v[200:207], v236\n\t" \
  "buffer_load_dwordx4 v254, s[8:11], s24 offen lds\n\t" \
  "ds_read_b64 v[226:227], v232 offset:416\n\t" \
  "s_waitcnt lgkmcnt(6)\n\t" \
  "v_smfmac_f32_16x16x64_bf16 v[180:183], v[96:99], v[208:215], v236\n\t" \
  "ds_read_b64 v[228:229], v232 offset:816\n\t" \
  "v_smfmac_f32_16x16x64_bf16 v[168:171], v[100:103], v[208:215], v236\n\t" \
  "ds_read_b64 v[230:231], v232 offset:1216\n\t" \
  "v_mfma_f32_32x32x16_bf16 v[16:31], v[242:245], v[240:243], 0\n\t" \
  "v_mov_b32 v196, 0x3f003f00\n\t" \
  "v_mov_b32 v197, 0x3f003f00\n\t" \
  "v_smfmac_f32_16x16x64_bf16 v[156:159], v[104:107], v[208:215], v236\n\t" \
  "v_mov_b32 v198, 0x3f003f00\n\t" \
  "v_mov_b32 v199, 0x3f003f00\n\t" \
  "s_add_u32 m0, s25, 41000\n\t" \
  "s_add_u32 s24, s24, s26\n\t" \
  "v_smfmac_f32_16x16x64_bf16 v[144:147], v[108:111], v[208:215], v236\n\t" \
  "buffer_load_dwordx4 v254, s[8:11], s24 offen lds\n\t" \
  "s_waitcnt vmcnt(8) lgkmcnt(0)\n\t" \
  "s_barrier\n\t" \
  "v_smfmac_f32_16x16x64_bf16 v[184:187], v[112:115], v[216:223], v236\n\t" \
  "ds_read_b64 v[200:201], v232 offset:16400\n\t" \
  "ds_read_b64 v[202:203], v232 offset:16800\n\t" \
  "v_smfmac_f32_16x16x64_bf16 v[172:175], v[116:119], v[216:223], v236\n\t" \
  "ds_read_b64 v[204:205], v232 offset:17200\n\t" \
  "ds_read_b64 v[206:207], v232 offset:17600\n\t" \
  "v_smfmac_f32_16x16x64_bf16 v[160:163], v[120:123], v[216:223], v236\n\t" \
  "ds_read_b64 v[208:209], v232 offset:16408\n\t" \
  "v_cvt_pk_bf16_f32 v144, v144, v145\n\t" \
  "v_cvt_pk_bf16_f32 v145, v146, v147\n\t" \
  "s_add_u32 m0, s25, 56880\n\t" \
  "s_add_u32 s24, s24, s26\n\t" \
  "v_smfmac_f32_16x16x64_bf16 v[148:151], v[124:127], v[216:223], v236\n\t" \
  "buffer_load_dwordx4 v254, s[8:11], s24 offen lds\n\t" \
  "ds_read_b64 v[210:211], v232 offset:16808\n\t" \
  "v_smfmac_f32_16x16x64_bf16 v[188:191], v[128:131], v[224:231], v236\n\t" \
  "ds_read_b64 v[212:213], v232 offset:17208\n\t" \
  "v_smfmac_f32_16x16x64_bf16 v[176:179], v[132:135], v[224:231], v236\n\t" \
  "ds_read_b64 v[214:215], v232 offset:17608\n\t" \
  "v_smfmac_f32_16x16x64_bf16 v[164:167], v[136:139], v[224:231], v236\n\t" \
  "ds_read_b64 v[216:217], v232 offset:16416\n\t" \
  "v_cvt_pk_bf16_f32 v148, v148, v149\n\t" \
  "v_cvt_pk_bf16_f32 v149, v150, v151\n\t" \
  "s_add_u32 m0, s25, 65080\n\t" \
  "s_add_u32 s24, s24, s26\n\t" \
  "v_smfmac_f32_16x16x64_bf16 v[152:155], v[140:143], v[224:231], v236\n\t" \
  "buffer_load_dwordx4 v254, s[8:11], s24 offen lds\n\t" \
  "ds_read_b64 v[218:219], v232 offset:16816\n\t" \
  "s_waitcnt lgkmcnt(6)\n\t" \
  "v_smfmac_f32_16x16x64_bf16 v[0:3], v[96:99], v[200:207], v236\n\t" \
  "ds_read_b64 v[220:221], v232 offset:17216\n\t" \
  "v_smfmac_f32_16x16x64_bf16 v[180:183], v[100:103], v[200:207], v236\n\t" \
  "ds_read_b64 v[222:223], v232 offset:17616\n\t" \
  "v_mfma_f32_32x32x16_bf16 v[32:47], v[242:245], v[240:243], 0\n\t" \
  "v_smfmac_f32_16x16x64_bf16 v[168:171], v[104:107], v[200:207], v236\n\t" \
  "v_cvt_pk_bf16_f32 v152, v152, v153\n\t" \
  "v_cvt_pk_bf16_f32 v153, v154, v155\n\t" \
  "v_smfmac_f32_16x16x64_bf16 v[156:159], v[108:111], v[200:207], v236\n\t" \
  "s_waitcnt vmcnt(8) lgkmcnt(0)\n\t" \
  "s_barrier\n\t" \
  "s_add_u32 m0, s25, 73280\n\t" \
  "s_add_u32 s24, s24, s26\n\t" \
  "v_smfmac_f32_16x16x64_bf16 v[4:7], v[112:115], v[208:215], v236\n\t" \
  "buffer_load_dwordx4 v254, s[8:11], s24 offen lds\n\t" \
  "ds_read_b64 v[224:225], v232 offset:32800\n\t" \
  "ds_read_b64 v[226:227], v232 offset:33200\n\t" \
  "v_smfmac_f32_16x16x64_bf16 v[184:187], v[116:119], v[208:215], v236\n\t" \
  "ds_read_b64 v[228:229], v232 offset:33600\n\t" \
  "ds_read_b64 v[230:231], v232 offset:34000\n\t" \
  "s_add_u32 m0, s25, 81480\n\t" \
  "s_add_u32 s24, s24, s26\n\t" \
  "v_smfmac_f32_16x16x64_bf16 v[172:175], v[120:123], v[208:215], v236\n\t" \
  "buffer_load_dwordx4 v254, s[8:11], s24 offen lds\n\t" \
  "ds_read_b64 v[200:201], v232 offset:32808\n\t" \
  "v_cvt_pk_bf16_f32 v156, v156, v157\n\t" \
  "v_cvt_pk_bf16_f32 v157, v158, v159\n\t" \
  "v_smfmac_f32_16x16x64_bf16 v[160:163], v[124:127], v[208:215], v236\n\t" \
  "ds_read_b64 v[202:203], v232 offset:33208\n\t" \
  "v_smfmac_f32_16x16x64_bf16 v[8:11], v[128:131], v[216:223], v236\n\t" \
  "ds_read_b64 v[204:205], v232 offset:33608\n\t" \
  "v_smfmac_f32_16x16x64_bf16 v[188:191], v[132:135], v[216:223], v236\n\t" \
  "ds_read_b64 v[206:207], v232 offset:34008\n\t" \
  "v_smfmac_f32_16x16x64_bf16 v[176:179], v[136:139], v[216:223], v236\n\t" \
  "buffer_store_dwordx4 v[52:55], v250, s[20:23], s28 offen offset:0 sc1\n\t" \
  "ds_read_b64 v[208:209], v232 offset:32816\n\t" \
  "v_cvt_pk_bf16_f32 v160, v160, v161\n\t" \
  "v_cvt_pk_bf16_f32 v161, v162, v163\n\t" \
  "v_smfmac_f32_16x16x64_bf16 v[164:167], v[140:143], v[216:223], v236\n\t" \
  "ds_read_b64 v[210:211], v232 offset:33216\n\t" \
  "s_waitcnt lgkmcnt(6)\n\t" \
  "v_smfmac_f32_16x16x64_bf16 v[12:15], v[96:99], v[224:231], v236\n\t" \
  "ds_read_b64 v[212:213], v232 offset:33616\n\t" \
  "v_smfmac_f32_16x16x64_bf16 v[0:3], v[100:103], v[224:231], v236\n\t" \
  "ds_read_b64 v[214:215], v232 offset:34016\n\t" \
  "v_smfmac_f32_16x16x64_bf16 v[180:183], v[104:107], v[224:231], v236\n\t" \
  "s_nop 0\n\t" \
  "v_cvt_pk_bf16_f32 v164, v164, v165\n\t" \
  "v_cvt_pk_bf16_f32 v165, v166, v167\n\t" \
  "v_smfmac_f32_16x16x64_bf16 v[168:171], v[108:111], v[224:231], v236\n\t" \
  "s_waitcnt vmcnt(9) lgkmcnt(0)\n\t" \
  "s_barrier\n\t" \
  "v_smfmac_f32_16x16x64_bf16 v[16:19], v[112:115], v[200:207], v236\n\t" \
  "ds_write_b64 v193, v[144:145] offset:12288\n\t" \
  "ds_read_b64 v[216:217], v253 offset:0\n\t" \
  "v_smfmac_f32_16x16x64_bf16 v[4:7], v[116:119], v[200:207], v236\n\t" \
  "ds_write_b64 v193, v[148:149] offset:16384\n\t" \
  "ds_read_b64 v[218:219], v253 offset:400\n\t" \
  "v_smfmac_f32_16x16x64_bf16 v[184:187], v[120:123], v[200:207], v236\n\t" \
  "ds_write_b64 v193, v[152:153] offset:20480\n\t" \
  "ds_read_b64 v[220:221], v253 offset:800\n\t" \
  "v_cvt_pk_bf16_f32 v168, v168, v169\n\t" \
  "v_cvt_pk_bf16_f32 v169, v170, v171\n\t" \
  "v_smfmac_f32_16x16x64_bf16 v[172:175], v[124:127], v[200:207], v236\n\t" \
  "ds_write_b64 v193, v[156:157] offset:0\n\t" \
  "ds_read_b64 v[222:223], v253 offset:1200\n\t" \
  "v_smfmac_f32_16x16x64_bf16 v[20:23], v[128:131], v[208:215], v236\n\t" \
  "buffer_store_dwordx4 v[60:63], v250, s[20:23], s28 offen offset:64 sc1\n\t" \
  "ds_read_b64 v[224:225], v253 offset:8\n\t" \
  "ds_read_b64 v[226:227], v253 offset:408\n\t" \
  "v_smfmac_f32_16x16x64_bf16 v[8:11], v[132:135], v[208:215], v236\n\t" \
  "ds_write_b64 v193, v[160:161] offset:4096\n\t" \
  "ds_read_b64 v[228:229], v253 offset:808\n\t" \
  "v_smfmac_f32_16x16x64_bf16 v[188:191], v[136:139], v[208:215], v236\n\t" \
  "ds_write_b64 v193, v[164:165] offset:8192\n\t" \
  "ds_read_b64 v[230:231], v253 offset:1208\n\t" \
  "v_cvt_pk_bf16_f32 v172, v172, v173\n\t" \
  "v_cvt_pk_bf16_f32 v173, v174, v175\n\t" \
  "v_smfmac_f32_16x16x64_bf16 v[176:179], v[140:143], v[208:215], v236\n\t" \
  "ds_read_b64 v[200:201], v253 offset:16\n\t" \
  "ds_read_b64 v[202:203], v253 offset:416\n\t" \
  "s_waitcnt lgkmcnt(8)\n\t" \
  "v_smfmac_f32_16x16x64_bf16 v[24:27], v[96:99], v[216:223], v236\n\t" \
  "ds_read_b64 v[204:205], v253 offset:816\n\t" \
  "v_smfmac_f32_16x16x64_bf16 v[12:15], v[100:103], v[216:223], v236\n\t" \
  "ds_read_b64 v[206:207], v253 offset:1216\n\t" \
  "v_smfmac_f32_16x16x64_bf16 v[0:3], v[104:107], v[216:223], v236\n\t" \
  "v_cvt_pk_bf16_f32 v176, v176, v177\n\t" \
  "v_cvt_pk_bf16_f32 v177, v178, v179\n\t" \
  "v_smfmac_f32_16x16x64_bf16 v[180:183], v[108:111], v[216:223], v236\n\t" \
  "s_waitcnt vmcnt(8) lgkmcnt(0)\n\t" \
  "s_barrier\n\t" \
  "v_smfmac_f32_16x16x64_bf16 v[28:31], v[112:115], v[224:231], v236\n\t" \
  "ds_write_b64 v192, v[168:169] offset:0\n\t" \
  "ds_read_b64 v[208:209], v253 offset:16400\n\t" \
  "ds_read_b128 v[144:147], v194 offset:0\n\t" \
  "v_smfmac_f32_16x16x64_bf16 v[16:19], v[116:119], v[224:231], v236\n\t" \
  "ds_write_b64 v192, v[172:173] offset:4096\n\t" \
  "ds_read_b64 v[210:211], v253 offset:16800\n\t" \
  "ds_read_b128 v[148:151], v194 offset:4096\n\t" \
  "v_smfmac_f32_16x16x64_bf16 v[4:7], v[120:123], v[224:231], v236\n\t" \
  "buffer_store_dwordx4 v[68:71], v250, s[20:23], s28 offen offset:128 sc1\n\t" \
  "ds_read_b64 v[212:213], v253 offset:17200\n\t" \
  "ds_read_b64 v[214:215], v253 offset:17600\n\t" \
  "v_cvt_pk_bf16_f32 v180, v180, v181\n\t" \
  "v_cvt_pk_bf16_f32 v181, v182, v183\n\t" \
  "ds_read_b128 v[152:155], v194 offset:8192\n\t" \
  "v_smfmac_f32_16x16x64_bf16 v[184:187], v[124:127], v[224:231], v236\n\t" \
  "ds_write_b64 v192, v[176:177] offset:8192\n\t" \
  "ds_read_b64 v[216:217], v253 offset:16408\n\t" \
  "v_add_u32 v194, -93792, v194\n\t" \
  "v_smfmac_f32_16x16x64_bf16 v[32:35], v[128:131], v[200:207], v236\n\t" \
  "ds_read_b64 v[218:219], v253 offset:16808\n\t" \
  "ds_read_b64 v[220:221], v253 offset:17208\n\t" \
  "v_smfmac_f32_16x16x64_bf16 v[20:23], v[132:135], v[200:207], v236\n\t" \
  "ds_read_b64 v[222:223], v253 offset:17608\n\t" \
  "s_waitcnt lgkmcnt(11)\n\t" \
  "v_mfma_f32_16x16x32_bf16 v[168:171], v[144:147], v[144:147], 0\n\t" \
  "ds_read_b64 v[224:225], v253 offset:16416\n\t" \
  "v_smfmac_f32_16x16x64_bf16 v[8:11], v[136:139], v[200:207], v236\n\t" \
  "ds_read_b64 v[226:227], v253 offset:16816\n\t" \
  "v_cvt_pk_bf16_f32 v184, v184, v185\n\t" \
  "v_cvt_pk_bf16_f32 v185, v186, v187\n\t" \
  "v_mfma_f32_16x16x32_bf16 v[172:175], v[196:199], v[144:147], 0\n\t" \
  "ds_read_b64 v[228:229], v253 offset:17216\n\t" \
  "v_smfmac_f32_16x16x64_bf16 v[188:191], v[140:143], v[200:207], v236\n\t" \
  "ds_read_b64 v[230:231], v253 offset:17616\n\t" \
  "s_waitcnt lgkmcnt(12)\n\t" \
  "v_mfma_f32_16x16x32_bf16 v[168:171], v[148:151], v[148:151], v[168:171]\n\t" \
  "s_waitcnt lgkmcnt(10)\n\t" \
  "v_smfmac_f32_16x16x64_bf16 v[36:39], v[96:99], v[208:215], v236\n\t" \
  "v_mfma_f32_16x16x32_bf16 v[172:175], v[196:199], v[148:151], v[172:175]\n\t" \
  "v_smfmac_f32_16x16x64_bf16 v[24:27], v[100:103], v[208:215], v236\n\t" \
  "s_waitcnt lgkmcnt(9)\n\t" \
  "v_mfma_f32_16x16x32_bf16 v[168:171], v[152:155], v[152:155], v[168:171]\n\t" \
  "v_smfmac_f32_16x16x64_bf16 v[12:15], v[104:107], v[208:215], v236\n\t" \
  "v_cvt_pk_bf16_f32 v188, v188, v189\n\t" \
  "v_cvt_pk_bf16_f32 v189, v190, v191\n\t" \
  "v_mfma_f32_16x16x32_bf16 v[172:175], v[196:199], v[152:155], v[172:175]\n\t" \
  "v_smfmac_f32_16x16x64_bf16 v[0:3], v[108:111], v[208:215], v236\n\t" \
  "buffer_store_dwordx4 v[76:79], v250, s[20:23], s29 offen offset:0 sc1\n\t" \
  "s_waitcnt vmcnt(8) lgkmcnt(0)\n\t" \
  "s_barrier\n\t" \
  "v_smfmac_f32_16x16x64_bf16 v[40:43], v[112:115], v[216:223], v236\n\t" \
  "ds_write_b64 v192, v[180:181] offset:12288\n\t" \
  "ds_read_b64 v[200:201], v253 offset:32800\n\t" \
  "v_smfmac_f32_16x16x64_bf16 v[28:31], v[116:119], v[216:223], v236\n\t" \
  "ds_write_b64 v192, v[184:185] offset:16384\n\t" \
  "ds_read_b64 v[202:203], v253 offset:33200\n\t" \
  "v_cndmask_b32 v168, v168, v169, s[40:41]\n\t" \
  "v_cndmask_b32 v170, v170, v171, s[40:41]\n\t" \
  "v_smfmac_f32_16x16x64_bf16 v[16:19], v[120:123], v[216:223], v236\n\t" \
  "ds_write_b64 v192, v[188:189] offset:20480\n\t" \
  "ds_read_b64 v[204:205], v253 offset:33600\n\t" \
  "v_cvt_pk_bf16_f32 v0, v0, v1\n\t" \
  "v_cvt_pk_bf16_f32 v1, v2, v3\n\t" \
  "v_cndmask_b32 v173, v168, v170, s[42:43]\n\t" \
  "v_mov_b32 v174, 0x1127c5ac\n\t" \
  "ds_read_b64_tr_b16 v[156:157], v195 offset:0\n\t" \
  "v_smfmac_f32_16x16x64_bf16 v[4:7], v[124:127], v[216:223], v236\n\t" \
  "ds_read_b64 v[206:207], v253 offset:34000\n\t" \
  "ds_read_b64 v[208:209], v253 offset:32808\n\t" \
  "s_not_b64 vcc, s[52:53]\n\t" \
  "ds_read_b64_tr_b16 v[158:159], v195 offset:4096\n\t" \
  "v_smfmac_f32_16x16x64_bf16 v[44:47], v[128:131], v[224:231], v236\n\t" \
  "ds_read_b64 v[210:211], v253 offset:33208\n\t" \
  "ds_read_b64 v[212:213], v253 offset:33608\n\t" \
  "ds_read_b64_tr_b16 v[160:161], v195 offset:2048\n\t" \
  "v_smfmac_f32_16x16x64_bf16 v[32:35], v[132:135], v[224:231], v236\n\t" \
  "ds_read_b64 v[214:215], v253 offset:34008\n\t" \
  "ds_read_b64 v[216:217], v253 offset:32816\n\t" \
  "ds_read_b64_tr_b16 v[162:163], v195 offset:6144\n\t" \
  "v_smfmac_f32_16x16x64_bf16 v[20:23], v[136:139], v[224:231], v236\n\t" \
  "ds_read_b64 v[218:219], v253 offset:33216\n\t" \
  "v_cvt_pk_bf16_f32 v4, v4, v5\n\t" \
  "v_cvt_pk_bf16_f32 v5, v6, v7\n\t" \
  "ds_read_b64_tr_b16 v[164:165], v195 offset:8192\n\t" \
  "v_smfmac_f32_16x16x64_bf16 v[8:11], v[140:143], v[224:231], v236\n\t" \
  "ds_read_b64 v[220:221], v253 offset:33616\n\t" \
  "ds_read_b64_tr_b16 v[166:167], v195 offset:10240\n\t" \
  "s_waitcnt lgkmcnt(12)\n\t" \
  "v_smfmac_f32_16x16x64_bf16 v[36:39], v[100:103], v[200:207], v236\n\t" \
  "ds_read_b64 v[222:223], v253 offset:34016\n\t" \
  "v_add_u32 v195, -93792, v195\n\t" \
  "v_smfmac_f32_16x16x64_bf16 v[24:27], v[104:107], v[200:207], v236\n\t" \
  "v_smfmac_f32_16x16x64_bf16 v[12:15], v[108:111], v[200:207], v236\n\t" \
  "buffer_store_dwordx4 v[84:87], v250, s[20:23], s29 offen offset:64 sc1\n\t" \
  "v_cvt_pk_bf16_f32 v8, v8, v9\n\t" \
  "v_cvt_pk_bf16_f32 v9, v10, v11\n\t" \
  "s_waitcnt vmcnt(7) lgkmcnt(0)\n\t" \
  "s_barrier\n\t" \
  "v_smfmac_f32_16x16x64_bf16 v[40:43], v[116:119], v[208:215], v236\n\t" \
  "ds_write_b64 v192, v[0:1] offset:24576\n\t" \
  "ds_read_b64 v[224:225], v232 offset:0\n\t" \
  "ds_read_b128 v[180:183], v194 offset:0\n\t" \
  "ds_read_b128 v[184:187], v194 offset:4096\n\t" \
  "ds_read_b128 v[188:191], v194 offset:8192\n\t" \
  "v_smfmac_f32_16x16x64_bf16 v[28:31], v[120:123], v[208:215], v236\n\t" \
  "ds_write_b64 v192, v[4:5] offset:28672\n\t" \
  "ds_read_b64 v[226:227], v232 offset:400\n\t" \
  "ds_read_b64 v[228:229], v232 offset:800\n\t" \
  "v_smfmac_f32_16x16x64_bf16 v[16:19], v[124:127], v[208:215], v236\n\t" \
  "ds_write_b64 v192, v[8:9] offset:32768\n\t" \
  "ds_read_b64 v[230:231], v232 offset:1200\n\t" \
  "ds_read_b64 v[200:201], v232 offset:8\n\t" \
  "v_cvt_pk_bf16_f32 v12, v12, v13\n\t" \
  "v_cvt_pk_bf16_f32 v13, v14, v15\n\t" \
  "v_smfmac_f32_16x16x64_bf16 v[44:47], v[132:135], v[216:223], v236\n\t" \
  "ds_write_b64 v192, v[12:13] offset:36864\n\t" \
  "ds_read_b64 v[202:203], v232 offset:408\n\t" \
  "ds_read_b64 v[204:205], v232 offset:808\n\t" \
  "s_waitcnt lgkmcnt(11)\n\t" \
  "v_mfma_f32_16x16x32_bf16 v[176:179], v[180:183], v[180:183], 0\n\t" \
  "ds_read_b64 v[206:207], v232 offset:1208\n\t" \
  "v_mfma_f32_16x16x32_bf16 v[168:171], v[196:199], v[180:183], 0\n\t" \
  "ds_read_b64 v[208:209], v232 offset:16\n\t" \
  "v_smfmac_f32_16x16x64_bf16 v[32:35], v[136:139], v[216:223], v236\n\t" \
  "ds_read_b64 v[210:211], v232 offset:416\n\t" \
  "ds_read_b64 v[212:213], v232 offset:816\n\t" \
  "s_waitcnt lgkmcnt(14)\n\t" \
  "v_mfma_f32_16x16x32_bf16 v[176:179], v[184:187], v[184:187], v[176:179]\n\t" \
  "ds_read_b64 v[214:215], v232 offset:1216\n\t" \
  "v_mfma_f32_16x16x32_bf16 v[168:171], v[196:199], v[184:187], v[168:171]\n\t" \
  "v_smfmac_f32_16x16x64_bf16 v[20:23], v[140:143], v[216:223], v236\n\t" \
  "v_cvt_pk_bf16_f32 v16, v16, v17\n\t" \
  "v_cvt_pk_bf16_f32 v17, v18, v19\n\t" \
  "s_waitcnt lgkmcnt(14)\n\t" \
  "v_mfma_f32_16x16x32_bf16 v[176:179], v[188:191], v[188:191], v[176:179]\n\t" \
  "ds_write_b64 v192, v[16:17] offset:40960\n\t" \
  "v_mfma_f32_16x16x32_bf16 v[168:171], v[196:199], v[188:191], v[168:171]\n\t" \
  "s_waitcnt lgkmcnt(10)\n\t" \
  "v_smfmac_f32_16x16x64_bf16 v[36:39], v[104:107], v[224:231], v236\n\t" \
  "v_mov_b32 v183, 0\n\t" \
  "v_mov_b32 v187, 0\n\t" \
  "s_nop 1\n\t" \
  "v_cndmask_b32 v176, v176, v177, s[40:41]\n\t" \
  "v_cndmask_b32 v178, v178, v179, s[40:41]\n\t" \
  "v_cndmask_b32 v176, v176, v178, s[42:43]\n\t" \
  "v_smfmac_f32_16x16x64_bf16 v[24:27], v[108:111], v[224:231], v236\n\t" \
  "v_cndmask_b32_dpp v172, v168, v172, vcc row_ror:8 row_mask:0xf bank_mask:0xf\n\t" \
  "v_mul_f32 v172, 0x3daaaaab, v172\n\t" \
  "v_cndmask_b32_dpp v173, v176, v173, vcc row_ror:8 row_mask:0xf bank_mask:0xf\n\t" \
  "v_fmamk_f32 v173, v173, 0x3e2aaaab, v174\n\t" \
  "s_waitcnt vmcnt(5) lgkmcnt(0)\n\t" \
  "s_barrier\n\t" \
  "ds_read_b64 v[216:217], v232 offset:16400\n\t" \
  "ds_read_b64 v[218:219], v232 offset:16800\n\t" \
  "ds_read_b64 v[220:221], v232 offset:17200\n\t" \
  "ds_read_b64 v[222:223], v232 offset:17600\n\t" \
  "v_smfmac_f32_16x16x64_bf16 v[40:43], v[120:123], v[200:207], v236\n\t" \
  "ds_read_b64 v[224:225], v232 offset:16408\n\t" \
  "ds_read_b64 v[226:227], v232 offset:16808\n\t" \
  "v_cvt_pk_bf16_f32 v20, v20, v21\n\t" \
  "v_cvt_pk_bf16_f32 v21, v22, v23\n\t" \
  "v_fma_f32 v173, -v172, v172, v173\n\t" \
  "v_max_f32 v173, v174, v173\n\t" \
  "v_rsq_f32 v173, v173 div:2\n\t" \
  "s_mov_b64 exec, s[52:53]\n\tv_mul_f32 v173, 0x08805555, v173\n\ts_mov_b64 exec, -1\n\t" \
  "v_smfmac_f32_16x16x64_bf16 v[28:31], v[124:127], v[200:207], v236\n\t" \
  "buffer_store_dwordx4 v[92:95], v250, s[20:23], s29 offen offset:128 sc1\n\t" \
  "ds_read_b64 v[228:229], v232 offset:17208\n\t" \
  "ds_read_b64 v[230:231], v232 offset:17608\n\t" \
  "s_mov_b64 vcc, s[40:41]\n\t" \
  "v_cvt_pk_bf16_f32 v175, v173, v173\n\t" \
  "v_and_b32 v173, 0xffff0000, v175\n\t" \
  "v_mul_f32 v174, -v172, v173 div:2\n\t" \
  "v_cndmask_b32_sdwa v175, v175, v173, vcc dst_sel:DWORD dst_unused:UNUSED_PAD src0_sel:WORD_1 src1_sel:DWORD\n\t" \
  "v_smfmac_f32_16x16x64_bf16 v[44:47], v[136:139], v[208:215], v236\n\t" \
  "ds_write_b64 v192, v[20:21] offset:45056\n\t" \
  "ds_read_b64 v[200:201], v232 offset:16416\n\t" \
  "v_cvt_pk_bf16_f32 v24, v24, v25\n\t" \
  "v_cvt_pk_bf16_f32 v25, v26, v27\n\t" \
  "v_and_b32 v172, 0xffff0000, v174\n\t" \
  "s_or_b64 vcc, s[52:53], s[42:43]\n\t" \
  "v_sub_f32 v174, v174, v172\n\t" \
  "v_cndmask_b32 v180, v175, v240, vcc\n\t" \
  "v_smfmac_f32_16x16x64_bf16 v[32:35], v[140:143], v[208:215], v236\n\t" \
  "ds_write_b64 v192, v[24:25] offset:56880\n\t" \
  "ds_read_b64 v[202:203], v232 offset:16816\n\t" \
  "v_perm_b32 v174, v172, v174, s55\n\t" \
  "v_cndmask_b32_dpp v184, v175, v240, vcc row_ror:8 row_mask:0xf bank_mask:0xf\n\t" \
  "s_mov_b64 vcc, s[52:53]\n\t" \
  "v_cndmask_b32 v182, v174, v240, vcc\n\t" \
  "ds_read_b64_tr_b16 v[144:145], v195 offset:0\n\t" \
  "ds_read_b64_tr_b16 v[146:147], v195 offset:4096\n\t" \
  "s_waitcnt lgkmcnt(10)\n\t" \
  "v_smfmac_f32_16x16x64_bf16 v[36:39], v[108:111], v[216:223], v236\n\t" \
  "ds_read_b64 v[204:205], v232 offset:17216\n\t" \
  "ds_read_b64 v[206:207], v232 offset:17616\n\t" \
  "v_cvt_pk_bf16_f32 v28, v28, v29\n\t" \
  "v_cvt_pk_bf16_f32 v29, v30, v31\n\t" \
  "v_cndmask_b32_dpp v186, v174, v240, vcc row_ror:8 row_mask:0xf bank_mask:0xf\n\t" \
  "s_orn2_b64 vcc, s[52:53], s[42:43]\n\t" \
  "v_cndmask_b32 v181, v175, v240, vcc\n\t" \
  "v_cndmask_b32_dpp v185, v175, v240, vcc row_ror:8 row_mask:0xf bank_mask:0xf\n\t" \
  "ds_read_b64_tr_b16 v[148:149], v195 offset:2048\n\t" \
  "ds_read_b64_tr_b16 v[150:151], v195 offset:6144\n\t" \
  "s_waitcnt lgkmcnt(10)\n\t" \
  "v_smfmac_f32_16x16x64_bf16 v[40:43], v[124:127], v[224:231], v236\n\t" \
  "ds_write_b64 v192, v[28:29] offset:60976\n\t" \
  "ds_read_b64_tr_b16 v[152:153], v195 offset:8192\n\t" \
  "ds_read_b64_tr_b16 v[154:155], v195 offset:10240\n\t" \
  "v_mfma_f32_16x16x16_bf16 v[0:3], v[196:197], v[182:183], 0\n\t" \
  "v_mfma_f32_16x16x16_bf16 v[4:7], v[196:197], v[186:187], 0\n\t" \
  "s_waitcnt lgkmcnt(5)\n\t" \
  "v_smfmac_f32_16x16x64_bf16 v[44:47], v[140:143], v[200:207], v236\n\t" \
  "v_cvt_pk_bf16_f32 v32, v32, v33\n\t" \
  "v_cvt_pk_bf16_f32 v33, v34, v35\n\t" \
  "v_mfma_f32_16x16x16_bf16 v[188:191], v[156:157], v[180:181], v[0:3]\n\t" \
  "ds_write_b64 v192, v[32:33] offset:65072\n\t" \
  "v_mfma_f32_16x16x16_bf16 v[176:179], v[158:159], v[180:181], v[0:3]\n\t" \
  "v_cvt_pk_bf16_f32 v36, v36, v37\n\t" \
  "v_cvt_pk_bf16_f32 v37, v38, v39\n\t" \
  "ds_write_b64 v193, v[36:37] offset:0\n\t" \
  "v_cvt_pk_bf16_f32 v40, v40, v41\n\t" \
  "v_cvt_pk_bf16_f32 v41, v42, v43\n\t" \
  "ds_write_b64 v193, v[40:41] offset:4096\n\t" \
  "v_mfma_f32_16x16x16_bf16 v[168:171], v[160:161], v[180:181], v[0:3]\n\t" \
  "v_cvt_pk_bf16_f32 v96, v188, v189\n\t" \
  "v_cvt_pk_bf16_f32 v97, v190, v191\n\t" \
  "v_mfma_f32_16x16x16_bf16 v[172:175], v[162:163], v[180:181], v[0:3]\n\t" \
  "v_cvt_pk_bf16_f32 v44, v44, v45\n\t" \
  "v_cvt_pk_bf16_f32 v45, v46, v47\n\t" \
  "ds_write_b64 v193, v[44:45] offset:8192\n\t" \
  "v_cvt_pk_bf16_f32 v98, v176, v177\n\t" \
  "v_cvt_pk_bf16_f32 v99, v178, v179\n\t" \
  "v_mfma_f32_16x16x16_bf16 v[188:191], v[164:165], v[180:181], v[0:3]\n\t" \
  "v_cvt_pk_bf16_f32 v112, v168, v169\n\t" \
  "v_cvt_pk_bf16_f32 v113, v170, v171\n\t" \
  "v_mfma_f32_16x16x16_bf16 v[176:179], v[166:167], v[180:181], v[0:3]\n\t" \
  "v_cvt_pk_bf16_f32 v114, v172, v173\n\t" \
  "v_cvt_pk_bf16_f32 v115, v174, v175\n\t" \
  "s_nop 2\n\t" \
  "v_cvt_pk_bf16_f32 v128, v188, v189\n\t" \
  "v_cvt_pk_bf16_f32 v129, v190, v191\n\t" \
  "s_nop 0\n\t" \
  "v_cvt_pk_bf16_f32 v130, v176, v177\n\t" \
  "v_cvt_pk_bf16_f32 v131, v178, v179\n\t" \
  "s_waitcnt lgkmcnt(0)\n\t" \
  "s_barrier\n\t" \
  "ds_read_b64_tr_b16 v[208:209], v248 offset:43048\n\t" \
  "ds_read_b64_tr_b16 v[210:211], v248 offset:44072\n\t" \
  "ds_read_b128 v[104:107], v194 offset:24576\n\t" \
  "ds_read_b128 v[120:123], v194 offset:28672\n\t" \
  "ds_read_b128 v[136:139], v194 offset:32768\n\t" \
  "v_add_u32 v194, s54, v194\n\t" \
  "ds_read_b128 v[108:111], v194 offset:49152\n\t" \
  "ds_read_b128 v[124:127], v194 offset:53248\n\t" \
  "ds_read_b128 v[140:143], v194 offset:57344\n\t" \
  "v_mfma_f32_16x16x16_bf16 v[168:171], v[144:145], v[184:185], v[4:7]\n\t" \
  "ds_read_b64_tr_b16 v[212:213], v248 offset:46120\n\t" \
  "ds_read_b64_tr_b16 v[214:215], v248 offset:47144\n\t" \
  "v_mfma_f32_16x16x16_bf16 v[172:175], v[146:147], v[184:185], v[4:7]\n\t" \
  "ds_read_b64_tr_b16 v[216:217], v248 offset:45096\n\t" \
  "ds_read_b64_tr_b16 v[218:219], v248 offset:48168\n\t" \
  "s_cmp_eq_u32 s37, 0\n\t" \
  "s_cbranch_scc1 .Lcnx_f1_0\n.Lcnx_g1_0:\n\t" \
  "v_mfma_f32_16x16x16_bf16 v[188:191], v[148:149], v[184:185], v[4:7]\n\t" \
  "ds_read_b64_tr_b16 v[160:161], v248 offset:43560\n\t" \
  "ds_read_b64_tr_b16 v[162:163], v248 offset:44584\n\t" \
  "v_cvt_pk_bf16_f32 v100, v168, v169\n\t" \
  "v_cvt_pk_bf16_f32 v101, v170, v171\n\t" \
  "v_mfma_f32_16x16x16_bf16 v[176:179], v[150:151], v[184:185], v[4:7]\n\t" \
  "ds_read_b64_tr_b16 v[164:165], v248 offset:46632\n\t" \
  "ds_read_b64_tr_b16 v[166:167], v248 offset:47656\n\t" \
  "s_cmp_eq_u32 s37, 0\n\t" \
  "s_cbranch_scc1 .Lcnx_f1_1\n.Lcnx_g1_1:\n\t" \
  "v_cvt_pk_bf16_f32 v102, v172, v173\n\t" \
  "v_cvt_pk_bf16_f32 v103, v174, v175\n\t" \
  "v_mfma_f32_16x16x16_bf16 v[168:171], v[152:153], v[184:185], v[4:7]\n\t" \
  "buffer_load_dwordx4 v[236:239], v250, s[8:11], s61 offen offset:0 nt\n\t" \
  "ds_read_b64_tr_b16 v[232:233], v248 offset:45608\n\t" \
  "ds_read_b64_tr_b16 v[234:235], v248 offset:48680\n\t" \
  "v_cvt_pk_bf16_f32 v116, v188, v189\n\t" \
  "v_cvt_pk_bf16_f32 v117, v190, v191\n\t" \
  "v_mfma_f32_16x16x16_bf16 v[172:175], v[154:155], v[184:185], v[4:7]\n\t" \
  "ds_read_b64_tr_b16 v[24:25], v195 offset:24576\n\t" \
  "ds_read_b64_tr_b16 v[26:27], v195 offset:28672\n\t" \
  "v_cvt_pk_bf16_f32 v118, v176, v177\n\t" \
  "v_cvt_pk_bf16_f32 v119, v178, v179\n\t" \
  "s_waitcnt lgkmcnt(15)\n\t" \
  "v_mfma_f32_16x16x32_bf16 v[144:147], v[208:211], v[96:99], 0\n\t" \
  "ds_read_b64_tr_b16 v[28:29], v195 offset:26624\n\t" \
  "ds_read_b64_tr_b16 v[30:31], v195 offset:30720\n\t" \
  "v_cvt_pk_bf16_f32 v132, v168, v169\n\t" \
  "v_cvt_pk_bf16_f32 v133, v170, v171\n\t" \
  "s_waitcnt lgkmcnt(12)\n\t" \
  "v_mfma_f32_16x16x32_bf16 v[144:147], v[212:215], v[112:115], v[144:147]\n\t" \
  "buffer_store_dwordx4 v[48:51], v250, s[20:23], s12 offen offset:0 sc1\n\t" \
  "ds_read_b64_tr_b16 v[32:33], v195 offset:32768\n\t" \
  "ds_read_b64_tr_b16 v[34:35], v195 offset:34816\n\t" \
  "v_cvt_pk_bf16_f32 v134, v172, v173\n\t" \
  "v_cvt_pk_bf16_f32 v135, v174, v175\n\t" \
  "v_mfma_f32_16x16x32_bf16 v[152:155], v[208:211], v[100:103], 0\n\t" \
  "v_add_u32 v195, s54, v195\n\t" \
  "ds_read_b64_tr_b16 v[36:37], v195 offset:49152\n\t" \
  "ds_read_b64_tr_b16 v[38:39], v195 offset:53248\n\t" \
  "s_waitcnt lgkmcnt(14)\n\t" \
  "v_mfma_f32_16x16x32_bf16 v[144:147], v[216:219], v[128:131], v[144:147]\n\t" \
  "ds_read_b64_tr_b16 v[40:41], v195 offset:51200\n\t" \
  "v_mfma_f32_16x16x32_bf16 v[152:155], v[212:215], v[116:119], v[152:155]\n\t" \
  "ds_read_b64_tr_b16 v[42:43], v195 offset:55296\n\t" \
  "v_mfma_f32_16x16x32_bf16 v[152:155], v[216:219], v[132:135], v[152:155]\n\t" \
  "ds_read_b64_tr_b16 v[44:45], v195 offset:57344\n\t" \
  "v_mfma_f32_16x16x32_bf16 v[8:11], v[104:107], v[104:107], 0\n\t" \
  "ds_read_b64_tr_b16 v[46:47], v195 offset:59392\n\t" \
  "v_mfma_f32_16x16x32_bf16 v[12:15], v[196:199], v[104:107], 0\n\t" \
  "v_cvt_pknorm_i16_f32 v220, |v146|, |v147|\n\t" \
  "v_cvt_pknorm_i16_f32 v222, |v144|, |v145|\n\t" \
  "v_mfma_f32_16x16x32_bf16 v[8:11], v[120:123], v[120:123], v[8:11]\n\t" \
  "v_lshlrev_b32_sdwa v221, 2, v220 dst_sel:DWORD dst_unused:UNUSED_PAD src0_sel:DWORD src1_sel:BYTE_3\n\t" \
  "v_lshlrev_b32_sdwa v220, 2, v220 dst_sel:DWORD dst_unused:UNUSED_PAD src0_sel:DWORD src1_sel:BYTE_1\n\t" \
  "v_mfma_f32_16x16x32_bf16 v[12:15], v[196:199], v[120:123], v[12:15]\n\t" \
  "ds_read_b32 v221, v221 offset:10016\n\t" \
  "v_lshlrev_b32_sdwa v223, 2, v222 dst_sel:DWORD dst_unused:UNUSED_PAD src0_sel:DWORD src1_sel:BYTE_3\n\t" \
  "v_lshlrev_b32_sdwa v222, 2, v222 dst_sel:DWORD dst_unused:UNUSED_PAD src0_sel:DWORD src1_sel:BYTE_1\n\t" \
  "v_mfma_f32_16x16x32_bf16 v[8:11], v[136:139], v[136:139], v[8:11]\n\t" \
  "ds_read_b32 v220, v220 offset:10016\n\t" \
  "v_lshlrev_b32_sdwa v225, 1, v155 dst_sel:DWORD dst_unused:UNUSED_PAD src0_sel:DWORD src1_sel:WORD_1\n\t" \
  "v_lshlrev_b32_sdwa v224, 1, v154 dst_sel:DWORD dst_unused:UNUSED_PAD src0_sel:DWORD src1_sel:WORD_1\n\t" \
  "v_mfma_f32_16x16x32_bf16 v[12:15], v[196:199], v[136:139], v[12:15]\n\t" \
  "ds_read_b32 v223, v223 offset:10016\n\t" \
  "v_lshlrev_b32_sdwa v227, 1, v153 dst_sel:DWORD dst_unused:UNUSED_PAD src0_sel:DWORD src1_sel:WORD_1\n\t" \
  "v_lshlrev_b32_sdwa v226, 1, v152 dst_sel:DWORD dst_unused:UNUSED_PAD src0_sel:DWORD src1_sel:WORD_1\n\t" \
  "v_mfma_f32_16x16x32_bf16 v[16:19], v[108:111], v[108:111], 0\n\t" \
  "ds_read_b32 v222, v222 offset:10016\n\t" \
  "v_mfma_f32_16x16x32_bf16 v[20:23], v[196:199], v[108:111], 0\n\t" \
  "ds_read_u16_d16_hi v225, v225 offset:10528\n\t" \
  "v_cndmask_b32 v8, v8, v9, s[40:41]\n\t" \
  "v_cndmask_b32 v10, v10, v11, s[40:41]\n\t" \
  "v_mfma_f32_16x16x32_bf16 v[16:19], v[124:127], v[124:127], v[16:19]\n\t" \
  "ds_read_u16 v224, v224 offset:10528\n\t" \
  "v_cndmask_b32 v13, v8, v10, s[42:43]\n\t" \
  "v_mov_b32 v14, 0x1127c5ac\n\t" \
  "v_mfma_f32_16x16x32_bf16 v[20:23], v[196:199], v[124:127], v[20:23]\n\t" \
  "buffer_store_dwordx4 v[56:59], v250, s[20:23], s12 offen offset:64 sc1\n\t" \
  "ds_read_u16_d16_hi v227, v227 offset:10528\n\t" \
  "s_not_b64 vcc, s[52:53]\n\t" \
  "v_mfma_f32_16x16x32_bf16 v[16:19], v[140:143], v[140:143], v[16:19]\n\t" \
  "ds_read_u16 v226, v226 offset:10528\n\t" \
  "s_waitcnt lgkmcnt(4)\n\t" \
  "v_fma_mix_f32 v221, |v147|, v221, v221 op_sel:[0,1,0] op_sel_hi:[0,1,1] clamp\n\t" \
  "v_fma_mix_f32 v220, |v146|, v220, v220 op_sel:[0,1,0] op_sel_hi:[0,1,1] clamp\n\t" \
  "v_mfma_f32_16x16x32_bf16 v[20:23], v[196:199], v[140:143], v[20:23]\n\t" \
  "v_fma_mix_f32 v223, |v145|, v223, v223 op_sel:[0,1,0] op_sel_hi:[0,1,1] clamp\n\t" \
  "v_fma_mix_f32 v222, |v144|, v222, v222 op_sel:[0,1,0] op_sel_hi:[0,1,1] clamp\n\t" \
  "v_mfma_f32_16x16x32_bf16 v[148:151], v[160:163], v[96:99], 0\n\t" \
  "s_waitcnt lgkmcnt(0)\n\t" \
  "v_or_b32 v155, v224, v225\n\t" \
  "v_or_b32 v154, v226, v227\n\t" \
  "v_mfma_f32_16x16x32_bf16 v[156:159], v[160:163], v[100:103], 0\n\t" \
  "v_cndmask_b32 v16, v16, v17, s[40:41]\n\t" \
  "v_cndmask_b32 v18, v18, v19, s[40:41]\n\t" \
  "v_cndmask_b32 v16, v16, v18, s[42:43]\n\t" \
  "v_cndmask_b32_dpp v12, v20, v12, vcc row_ror:8 row_mask:0xf bank_mask:0xf\n\t" \
  "v_mul_f32 v12, 0x3daaaaab, v12\n\t" \
  "v_mfma_f32_16x16x32_bf16 v[148:151], v[164:167], v[112:115], v[148:151]\n\t" \
  "v_cndmask_b32_dpp v13, v16, v13, vcc row_ror:8 row_mask:0xf bank_mask:0xf\n\t" \
  "v_fmamk_f32 v13, v13, 0x3e2aaaab, v14\n\t" \
  "v_fma_f32 v13, -v12, v12, v13\n\t" \
  "v_max_f32 v13, v14, v13\n\t" \
  "v_rsq_f32 v13, v13 div:2\n\t" \
  "v_mfma_f32_16x16x32_bf16 v[156:159], v[164:167], v[116:119], v[156:159]\n\t" \
  "s_mov_b64 exec, s[52:53]\n\tv_mul_f32 v13, 0x08805555, v13\n\ts_mov_b64 exec, -1\n\t" \
  "s_mov_b64 vcc, s[40:41]\n\t" \
  "v_cvt_pk_bf16_f32 v15, v13, v13\n\t" \
  "v_and_b32 v13, 0xffff0000, v15\n\t" \
  "v_mul_f32 v14, -v12, v13 div:2\n\t" \
  "v_cndmask_b32_sdwa v15, v15, v13, vcc dst_sel:DWORD dst_unused:UNUSED_PAD src0_sel:WORD_1 src1_sel:DWORD\n\t" \
  "v_and_b32 v12, 0xffff0000, v14\n\t" \
  "v_mfma_f32_16x16x32_bf16 v[148:151], v[232:235], v[128:131], v[148:151]\n\t" \
  "s_or_b64 vcc, s[52:53], s[42:43]\n\t" \
  "v_sub_f32 v14, v14, v12\n\t" \
  "v_cndmask_b32 v180, v15, v240, vcc\n\t" \
  "v_perm_b32 v14, v12, v14, s55\n\t" \
  "v_cndmask_b32_dpp v184, v15, v240, vcc row_ror:8 row_mask:0xf bank_mask:0xf\n\t" \
  "v_mfma_f32_16x16x32_bf16 v[156:159], v[232:235], v[132:135], v[156:159]\n\t" \
  "s_mov_b64 vcc, s[52:53]\n\t" \
  "v_cndmask_b32 v182, v14, v240, vcc\n\t" \
  "v_cndmask_b32_dpp v186, v14, v240, vcc row_ror:8 row_mask:0xf bank_mask:0xf\n\t" \
  "s_orn2_b64 vcc, s[52:53], s[42:43]\n\t" \
  "v_cndmask_b32 v181, v15, v240, vcc\n\t" \
  "v_mfma_f32_16x16x16_bf16 v[0:3], v[196:197], v[182:183], 0\n\t" \
  "v_cndmask_b32_dpp v185, v15, v240, vcc row_ror:8 row_mask:0xf bank_mask:0xf\n\t" \
  "s_mov_b64 vcc, 0\n\t" \
  "v_fma_f32 v147, |v147|, v221, v147 mul:4\n\t" \
  "v_mfma_f32_16x16x16_bf16 v[4:7], v[196:197], v[186:187], 0\n\t" \
  "v_fma_f32 v146, |v146|, v220, v146 mul:4\n\t" \
  "v_fma_f32 v145, |v145|, v223, v145 mul:4\n\t" \
  "v_mfma_f32_16x16x16_bf16 v[188:191], v[24:25], v[180:181], v[0:3]\n\t" \
  "v_fma_f32 v144, |v144|, v222, v144 mul:4\n\t" \
  "v_cvt_pk_bf16_f32 v147, v146, v147\n\t" \
  "v_mfma_f32_16x16x16_bf16 v[176:179], v[26:27], v[180:181], v[0:3]\n\t" \
  "v_cvt_pk_bf16_f32 v146, v144, v145\n\t" \
  "v_cvt_pknorm_i16_f32 v228, |v148|, |v149|\n\t" \
  "v_mfma_f32_16x16x16_bf16 v[200:203], v[36:37], v[184:185], v[4:7]\n\t" \
  "s_nop 1\n\t" \
  "v_cvt_pk_bf16_f32 v104, v188, v189\n\t" \
  "v_cvt_pk_bf16_f32 v105, v190, v191\n\t" \
  "v_mfma_f32_16x16x16_bf16 v[204:207], v[38:39], v[184:185], v[4:7]\n\t" \
  "buffer_store_dwordx4 v[64:67], v250, s[20:23], s12 offen offset:128 sc1\n\t" \
  "v_cvt_pk_bf16_f32 v106, v176, v177\n\t" \
  "v_cvt_pk_bf16_f32 v107, v178, v179\n\t" \
  "v_mfma_f32_16x16x16_bf16 v[196:199], v[28:29], v[180:181], v[0:3]\n\t" \
  "v_cvt_pk_bf16_f32 v108, v200, v201\n\t" \
  "v_cvt_pk_bf16_f32 v109, v202, v203\n\t" \
  "v_mfma_f32_16x16x16_bf16 v[188:191], v[30:31], v[180:181], v[0:3]\n\t" \
  "s_nop 0\n\t" \
  "v_cvt_pk_bf16_f32 v110, v204, v205\n\t" \
  "v_cvt_pk_bf16_f32 v111, v206, v207\n\t" \
  "v_mfma_f32_16x16x32_bf16 v[160:163], v[208:211], v[104:107], 0\n\t" \
  "s_nop 0\n\t" \
  "v_cvt_pk_bf16_f32 v120, v196, v197\n\t" \
  "v_cvt_pk_bf16_f32 v121, v198, v199\n\t" \
  "v_mfma_f32_16x16x16_bf16 v[176:179], v[40:41], v[184:185], v[4:7]\n\t" \
  "v_cvt_pk_bf16_f32 v122, v188, v189\n\t" \
  "v_cvt_pk_bf16_f32 v123, v190, v191\n\t" \
  "v_mfma_f32_16x16x16_bf16 v[200:203], v[42:43], v[184:185], v[4:7]\n\t" \
  "v_cvt_pknorm_i16_f32 v230, |v150|, |v151|\n\t" \
  "v_lshlrev_b32_sdwa v229, 2, v228 dst_sel:DWORD dst_unused:UNUSED_PAD src0_sel:DWORD src1_sel:BYTE_3\n\t" \
  "v_mfma_f32_16x16x32_bf16 v[168:171], v[208:211], v[108:111], 0\n\t" \
  "ds_read_b64_tr_b16 v[208:209], v248 offset:43560\n\t" \
  "s_nop 0\n\t" \
  "v_cvt_pk_bf16_f32 v124, v176, v177\n\t" \
  "v_cvt_pk_bf16_f32 v125, v178, v179\n\t" \
  "v_mfma_f32_16x16x16_bf16 v[204:207], v[32:33], v[180:181], v[0:3]\n\t" \
  "ds_read_b64_tr_b16 v[210:211], v248 offset:44584\n\t" \
  "v_cvt_pk_bf16_f32 v126, v200, v201\n\t" \
  "v_cvt_pk_bf16_f32 v127, v202, v203\n\t" \
  "v_mfma_f32_16x16x16_bf16 v[196:199], v[34:35], v[180:181], v[0:3]\n\t" \
  "v_lshlrev_b32_sdwa v228, 2, v228 dst_sel:DWORD dst_unused:UNUSED_PAD src0_sel:DWORD src1_sel:BYTE_1\n\t" \
  "v_lshlrev_b32_sdwa v231, 2, v230 dst_sel:DWORD dst_unused:UNUSED_PAD src0_sel:DWORD src1_sel:BYTE_3\n\t" \
  "v_mfma_f32_16x16x32_bf16 v[160:163], v[212:215], v[120:123], v[160:163]\n\t" \
  "ds_read_b32 v229, v229 offset:10016\n\t" \
  "v_cvt_pk_bf16_f32 v136, v204, v205\n\t" \
  "v_cvt_pk_bf16_f32 v137, v206, v207\n\t" \
  "v_mfma_f32_16x16x16_bf16 v[188:191], v[44:45], v[184:185], v[4:7]\n\t" \
  "ds_read_b32 v228, v228 offset:10016\n\t" \
  "v_cvt_pk_bf16_f32 v138, v196, v197\n\t" \
  "v_cvt_pk_bf16_f32 v139, v198, v199\n\t" \
  "v_mfma_f32_16x16x16_bf16 v[176:179], v[46:47], v[184:185], v[4:7]\n\t" \
  "v_lshlrev_b32_sdwa v230, 2, v230 dst_sel:DWORD dst_unused:UNUSED_PAD src0_sel:DWORD src1_sel:BYTE_1\n\t" \
  "v_lshlrev_b32_sdwa v233, 1, v157 dst_sel:DWORD dst_unused:UNUSED_PAD src0_sel:DWORD src1_sel:WORD_1\n\t" \
  "v_mfma_f32_16x16x32_bf16 v[168:171], v[212:215], v[124:127], v[168:171]\n\t" \
  "ds_read_b64_tr_b16 v[212:213], v248 offset:46632\n\t" \
  "v_cvt_pk_bf16_f32 v140, v188, v189\n\t" \
  "v_cvt_pk_bf16_f32 v141, v190, v191\n\t" \
  "v_mfma_f32_16x16x32_bf16 v[160:163], v[216:219], v[136:139], v[160:163]\n\t" \
  "ds_read_b64_tr_b16 v[214:215], v248 offset:47656\n\t" \
  "v_cvt_pk_bf16_f32 v142, v176, v177\n\t" \
  "v_cvt_pk_bf16_f32 v143, v178, v179\n\t" \
  "s_nop 1\n\t" \
  "v_mfma_f32_16x16x32_bf16 v[168:171], v[216:219], v[140:143], v[168:171]\n\t" \
  "ds_read_b64_tr_b16 v[216:217], v248 offset:45608\n\t" \
  "v_lshlrev_b32_sdwa v232, 1, v156 dst_sel:DWORD dst_unused:UNUSED_PAD src0_sel:DWORD src1_sel:WORD_1\n\t" \
  "v_lshlrev_b32_sdwa v235, 1, v159 dst_sel:DWORD dst_unused:UNUSED_PAD src0_sel:DWORD src1_sel:WORD_1\n\t" \
  "s_waitcnt lgkmcnt(5)\n\t" \
  "ds_read_b64_tr_b16 v[218:219], v248 offset:48680\n\t" \
  "s_waitcnt vmcnt(4)\n\t" \
  "s_mov_b64 exec, 3\n\t" \
  "ds_add_rtn_u32 v246, v251, v250 offset:47136\n\t" \
  "s_mov_b64 exec, -1\n\t" \
  "v_mfma_f32_16x16x32_bf16 v[164:167], v[208:211], v[104:107], 0\n\t" \
  "ds_read_b32 v231, v231 offset:10016\n\t" \
  "v_lshlrev_b32_sdwa v234, 1, v158 dst_sel:DWORD dst_unused:UNUSED_PAD src0_sel:DWORD src1_sel:WORD_1\n\t" \
  "v_mfma_f32_16x16x32_bf16 v[172:175], v[208:211], v[108:111], 0\n\t" \
  "ds_read_b64_tr_b16 v[208:209], v248 offset:49192\n\t" \
  "ds_read_b64_tr_b16 v[210:211], v248 offset:50216\n\t" \
  "s_waitcnt lgkmcnt(6)\n\t" \
  "v_mfma_f32_16x16x32_bf16 v[164:167], v[212:215], v[120:123], v[164:167]\n\t" \
  "ds_read_b32 v230, v230 offset:10016\n\t" \
  "v_mfma_f32_16x16x32_bf16 v[172:175], v[212:215], v[124:127], v[172:175]\n\t" \
  "ds_read_b64_tr_b16 v[212:213], v248 offset:52264\n\t" \
  "ds_read_b64_tr_b16 v[214:215], v248 offset:53288\n\t" \
  "s_waitcnt lgkmcnt(7)\n\t" \
  "v_mfma_f32_16x16x32_bf16 v[164:167], v[216:219], v[136:139], v[164:167]\n\t" \
  "ds_read_u16_d16_hi v233, v233 offset:10528\n\t" \
  "v_mfma_f32_16x16x32_bf16 v[172:175], v[216:219], v[140:143], v[172:175]\n\t" \
  "ds_read_b64_tr_b16 v[216:217], v248 offset:51240\n\t" \
  "s_waitcnt lgkmcnt(5)\n\t" \
  "v_mfma_f32_16x16x32_bf16 v[176:179], v[208:211], v[96:99], 0\n\t" \
  "ds_read_b64_tr_b16 v[218:219], v248 offset:54312\n\t" \
  "buffer_store_dwordx4 v[72:75], v250, s[20:23], s13 offen offset:0 sc1\n\t" \
  "v_cvt_pknorm_i16_f32 v220, |v162|, |v163|\n\t" \
  "v_mfma_f32_16x16x32_bf16 v[184:187], v[208:211], v[100:103], 0\n\t" \
  "ds_read_u16 v232, v232 offset:10528\n\t" \
  "v_cvt_pknorm_i16_f32 v222, |v160|, |v161|\n\t" \
  "s_setprio 3\n\t" \
  "v_lshlrev_b32_sdwa v221, 2, v220 dst_sel:DWORD dst_unused:UNUSED_PAD src0_sel:DWORD src1_sel:BYTE_3\n\t" \
  "v_lshlrev_b32_sdwa v220, 2, v220 dst_sel:DWORD dst_unused:UNUSED_PAD src0_sel:DWORD src1_sel:BYTE_1\n\t" \
  "v_mfma_f32_16x16x32_bf16 v[192:195], v[208:211], v[104:107], 0\n\t" \
  "ds_read_u16_d16_hi v235, v235 offset:10528\n\t" \
  "v_lshlrev_b32_sdwa v223, 2, v222 dst_sel:DWORD dst_unused:UNUSED_PAD src0_sel:DWORD src1_sel:BYTE_3\n\t" \
  "v_lshlrev_b32_sdwa v222, 2, v222 dst_sel:DWORD dst_unused:UNUSED_PAD src0_sel:DWORD src1_sel:BYTE_1\n\t" \
  "v_lshlrev_b32_sdwa v225, 1, v171 dst_sel:DWORD dst_unused:UNUSED_PAD src0_sel:DWORD src1_sel:WORD_1\n\t" \
  "v_mfma_f32_16x16x32_bf16 v[200:203], v[208:211], v[108:111], 0\n\t" \
  "ds_read_b64_tr_b16 v[208:209], v248 offset:49704\n\t" \
  "ds_read_u16 v234, v234 offset:10528\n\t" \
  "v_lshlrev_b32_sdwa v224, 1, v170 dst_sel:DWORD dst_unused:UNUSED_PAD src0_sel:DWORD src1_sel:WORD_1\n\t" \
  "v_lshlrev_b32_sdwa v227, 1, v169 dst_sel:DWORD dst_unused:UNUSED_PAD src0_sel:DWORD src1_sel:WORD_1\n\t" \
  "v_lshlrev_b32_sdwa v226, 1, v168 dst_sel:DWORD dst_unused:UNUSED_PAD src0_sel:DWORD src1_sel:WORD_1\n\t" \
  "s_waitcnt lgkmcnt(7)\n\t" \
  "v_mfma_f32_16x16x32_bf16 v[176:179], v[212:215], v[112:115], v[176:179]\n\t" \
  "ds_read_b64_tr_b16 v[210:211], v248 offset:50728\n\t" \
  "ds_read_b32 v221, v221 offset:10016\n\t" \
  "buffer_store_dwordx4 v[80:83], v250, s[20:23], s13 offen offset:64 sc1\n\t" \
  "v_fma_mix_f32 v229, |v149|, v229, v229 op_sel:[0,1,0] op_sel_hi:[0,1,1] clamp\n\t" \
  "v_mfma_f32_16x16x32_bf16 v[184:187], v[212:215], v[116:119], v[184:187]\n\t" \
  "ds_read_b32 v220, v220 offset:10016\n\t" \
  "ds_read_b32 v223, v223 offset:10016\n\t" \
  "v_fma_mix_f32 v228, |v148|, v228, v228 op_sel:[0,1,0] op_sel_hi:[0,1,1] clamp\n\t" \
  "v_fma_mix_f32 v231, |v151|, v231, v231 op_sel:[0,1,0] op_sel_hi:[0,1,1] clamp\n\t" \
  "v_fma_mix_f32 v230, |v150|, v230, v230 op_sel:[0,1,0] op_sel_hi:[0,1,1] clamp\n\t" \
  "s_waitcnt lgkmcnt(4)\n\t" \
  "v_mfma_f32_16x16x32_bf16 v[192:195], v[212:215], v[120:123], v[192:195]\n\t" \
  "ds_read_b32 v222, v222 offset:10016\n\t" \
  "v_or_b32 v156, v232, v233\n\t" \
  "s_setprio 2\n\t" \
  "v_or_b32 v157, v234, v235\n\t" \
  "v_fma_f32 v149, |v149|, v229, v149 mul:4\n\t" \
  "v_mfma_f32_16x16x32_bf16 v[200:203], v[212:215], v[124:127], v[200:203]\n\t" \
  "ds_read_b64_tr_b16 v[212:213], v248 offset:52776\n\t" \
  "ds_read_u16_d16_hi v225, v225 offset:10528\n\t" \
  "v_fma_f32 v148, |v148|, v228, v148 mul:4\n\t" \
  "v_fma_f32 v151, |v151|, v231, v151 mul:4\n\t" \
  "v_fma_f32 v150, |v150|, v230, v150 mul:4\n\t" \
  "v_mfma_f32_16x16x32_bf16 v[176:179], v[216:219], v[128:131], v[176:179]\n\t" \
  "ds_read_b64_tr_b16 v[214:215], v248 offset:53800\n\t" \
  "buffer_store_dwordx4 v[88:91], v250, s[20:23], s13 offen offset:128 sc1\n\t" \
  "v_cvt_pk_bf16_f32 v148, v148, v149\n\t" \
  "v_mfma_f32_16x16x32_bf16 v[184:187], v[216:219], v[132:135], v[184:187]\n\t" \
  "ds_read_u16 v224, v224 offset:10528\n\t" \
  "v_cvt_pk_bf16_f32 v149, v150, v151\n\t" \
  "v_cvt_pknorm_i16_f32 v228, |v164|, |v165|\n\t" \
  "v_cvt_pknorm_i16_f32 v230, |v166|, |v167|\n\t" \
  "v_mfma_f32_16x16x32_bf16 v[192:195], v[216:219], v[136:139], v[192:195]\n\t" \
  "ds_read_u16_d16_hi v227, v227 offset:10528\n\t" \
  "v_lshlrev_b32_sdwa v229, 2, v228 dst_sel:DWORD dst_unused:UNUSED_PAD src0_sel:DWORD src1_sel:BYTE_3\n\t" \
  "v_lshlrev_b32_sdwa v228, 2, v228 dst_sel:DWORD dst_unused:UNUSED_PAD src0_sel:DWORD src1_sel:BYTE_1\n\t" \
  "v_lshlrev_b32_sdwa v231, 2, v230 dst_sel:DWORD dst_unused:UNUSED_PAD src0_sel:DWORD src1_sel:BYTE_3\n\t" \
  "v_mfma_f32_16x16x32_bf16 v[200:203], v[216:219], v[140:143], v[200:203]\n\t" \
  "ds_read_b64_tr_b16 v[216:217], v248 offset:51752\n\t" \
  "v_lshlrev_b32_sdwa v230, 2, v230 dst_sel:DWORD dst_unused:UNUSED_PAD src0_sel:DWORD src1_sel:BYTE_1\n\t" \
  "v_lshlrev_b32_sdwa v233, 1, v173 dst_sel:DWORD dst_unused:UNUSED_PAD src0_sel:DWORD src1_sel:WORD_1\n\t" \
  "v_lshlrev_b32_sdwa v232, 1, v172 dst_sel:DWORD dst_unused:UNUSED_PAD src0_sel:DWORD src1_sel:WORD_1\n\t" \
  "s_waitcnt lgkmcnt(10)\n\t" \
  "v_mfma_f32_16x16x32_bf16 v[180:183], v[208:211], v[96:99], 0\n\t" \
  "ds_read_b64_tr_b16 v[218:219], v248 offset:54824\n\t" \
  "ds_read_u16 v226, v226 offset:10528\n\t" \
  "v_lshlrev_b32_sdwa v235, 1, v175 dst_sel:DWORD dst_unused:UNUSED_PAD src0_sel:DWORD src1_sel:WORD_1\n\t" \
  "s_waitcnt vmcnt(6)\n\t" \
  "v_lshlrev_b32_sdwa v234, 1, v174 dst_sel:DWORD dst_unused:UNUSED_PAD src0_sel:DWORD src1_sel:WORD_1\n\t" \
  "s_waitcnt lgkmcnt(8)\n\t" \
  "v_fma_mix_f32 v221, |v163|, v221, v221 op_sel:[0,1,0] op_sel_hi:[0,1,1] clamp\n\t" \
  "v_fma_mix_f32 v220, |v162|, v220, v220 op_sel:[0,1,0] op_sel_hi:[0,1,1] clamp\n\t" \
  "v_mfma_f32_16x16x32_bf16 v[188:191], v[208:211], v[100:103], 0\n\t" \
  "s_mov_b64 exec, 3\n\t" \
  "ds_add_rtn_u32 v246, v251, v250 offset:47144\n\t" \
  "s_mov_b64 exec, -1\n\t" \
  "v_fma_mix_f32 v223, |v161|, v223, v223 op_sel:[0,1,0] op_sel_hi:[0,1,1] clamp\n\t" \
  "v_mfma_f32_16x16x32_bf16 v[196:199], v[208:211], v[104:107], 0\n\t" \
  "ds_read_b32 v229, v229 offset:10016\n\t" \
  "ds_read_b32 v228, v228 offset:10016\n\t" \
  "v_fma_mix_f32 v222, |v160|, v222, v222 op_sel:[0,1,0] op_sel_hi:[0,1,1] clamp\n\t" \
  "s_setprio 1\n\t" \
  "s_waitcnt lgkmcnt(3)\n\t" \
  "v_or_b32 v171, v224, v225\n\t" \
  "v_or_b32 v170, v226, v227\n\t" \
  "v_mfma_f32_16x16x32_bf16 v[204:207], v[208:211], v[108:111], 0\n\t" \
  "ds_read_b32 v231, v231 offset:10016\n\t" \
  "ds_read_b32 v230, v230 offset:10016\n\t" \
  "v_fma_f32 v163, |v163|, v221, v163 mul:4\n\t" \
  "v_fma_f32 v162, |v162|, v220, v162 mul:4\n\t" \
  "v_fma_f32 v161, |v161|, v223, v161 mul:4\n\t" \
  "s_waitcnt lgkmcnt(4)\n\t" \
  "v_readfirstlane_b32 s4, v246\n\t" \
  "v_mfma_f32_16x16x32_bf16 v[180:183], v[212:215], v[112:115], v[180:183]\n\t" \
  "ds_read_u16_d16_hi v233, v233 offset:10528\n\t" \
  "v_fma_f32 v160, |v160|, v222, v160 mul:4\n\t" \
  "s_cmp_lt_u32 s4, s57\n\t" \
  "s_cbranch_scc1 .Lcnx_f1_2\n.Lcnx_g1_2:\n\t" \
  "v_mfma_f32_16x16x32_bf16 v[188:191], v[212:215], v[116:119], v[188:191]\n\t" \
  "ds_read_b64_tr_b16 v[208:209], v249 offset:24600\n\t" \
  "v_cvt_pk_bf16_f32 v163, v162, v163\n\t" \
  "v_cvt_pk_bf16_f32 v162, v160, v161\n\t" \
  "v_cvt_pknorm_i16_f32 v220, |v178|, |v179|\n\t" \
  "v_mfma_f32_16x16x32_bf16 v[196:199], v[212:215], v[120:123], v[196:199]\n\t" \
  "ds_read_b64_tr_b16 v[210:211], v249 offset:26136\n\t" \
  "v_cvt_pknorm_i16_f32 v222, |v176|, |v177|\n\t" \
  "s_setprio 0\n\t" \
  "v_lshlrev_b32_sdwa v221, 2, v220 dst_sel:DWORD dst_unused:UNUSED_PAD src0_sel:DWORD src1_sel:BYTE_3\n\t" \
  "v_lshlrev_b32_sdwa v220, 2, v220 dst_sel:DWORD dst_unused:UNUSED_PAD src0_sel:DWORD src1_sel:BYTE_1\n\t" \
  "v_lshlrev_b32_sdwa v223, 2, v222 dst_sel:DWORD dst_unused:UNUSED_PAD src0_sel:DWORD src1_sel:BYTE_3\n\t" \
  "v_mfma_f32_16x16x32_bf16 v[204:207], v[212:215], v[124:127], v[204:207]\n\t" \
  "ds_read_b64_tr_b16 v[212:213], v249 offset:24608\n\t" \
  "ds_read_u16 v232, v232 offset:10528\n\t" \
  "v_lshlrev_b32_sdwa v222, 2, v222 dst_sel:DWORD dst_unused:UNUSED_PAD src0_sel:DWORD src1_sel:BYTE_1\n\t" \
  "v_lshlrev_b32_sdwa v225, 1, v187 dst_sel:DWORD dst_unused:UNUSED_PAD src0_sel:DWORD src1_sel:WORD_1\n\t" \
  "v_lshlrev_b32_sdwa v224, 1, v186 dst_sel:DWORD dst_unused:UNUSED_PAD src0_sel:DWORD src1_sel:WORD_1\n\t" \
  "v_mfma_f32_16x16x32_bf16 v[180:183], v[216:219], v[128:131], v[180:183]\n\t" \
  "ds_read_b64_tr_b16 v[214:215], v249 offset:26144\n\t" \
  "ds_read_u16_d16_hi v235, v235 offset:10528\n\t" \
  "v_lshlrev_b32_sdwa v227, 1, v185 dst_sel:DWORD dst_unused:UNUSED_PAD src0_sel:DWORD src1_sel:WORD_1\n\t" \
  "v_lshlrev_b32_sdwa v226, 1, v184 dst_sel:DWORD dst_unused:UNUSED_PAD src0_sel:DWORD src1_sel:WORD_1\n\t" \
  "ds_read_u16 v234, v234 offset:10528\n\t" \
  "s_waitcnt lgkmcnt(8)\n\t" \
  "v_fma_mix_f32 v229, |v165|, v229, v229 op_sel:[0,1,0] op_sel_hi:[0,1,1] clamp\n\t" \
  "v_fma_mix_f32 v228, |v164|, v228, v228 op_sel:[0,1,0] op_sel_hi:[0,1,1] clamp\n\t" \
  "v_mfma_f32_16x16x32_bf16 v[188:191], v[216:219], v[132:135], v[188:191]\n\t" \
  "ds_read_b32 v221, v221 offset:10016\n\t" \
  "ds_read_b32 v220, v220 offset:10016\n\t" \
  "v_fma_mix_f32 v231, |v167|, v231, v231 op_sel:[0,1,0] op_sel_hi:[0,1,1] clamp\n\t" \
  "v_fma_mix_f32 v230, |v166|, v230, v230 op_sel:[0,1,0] op_sel_hi:[0,1,1] clamp\n\t" \
  "s_waitcnt lgkmcnt(2)\n\t" \
  "v_or_b32 v172, v232, v233\n\t" \
  "v_mfma_f32_16x16x32_bf16 v[196:199], v[216:219], v[136:139], v[196:199]\n\t" \
  "ds_read_b32 v223, v223 offset:10016\n\t" \
  "ds_read_b32 v222, v222 offset:10016\n\t" \
  "v_or_b32 v173, v234, v235\n\t" \
  "v_fma_f32 v165, |v165|, v229, v165 mul:4\n\t" \
  "v_fma_f32 v164, |v164|, v228, v164 mul:4\n\t" \
  "v_fma_f32 v167, |v167|, v231, v167 mul:4\n\t" \
  "v_mfma_f32_16x16x32_bf16 v[204:207], v[216:219], v[140:143], v[204:207]\n\t" \
  "ds_read_b64_tr_b16 v[216:217], v249 offset:24664\n\t" \
  "ds_read_u16_d16_hi v225, v225 offset:10528\n\t" \
  "v_fma_f32 v166, |v166|, v230, v166 mul:4\n\t" \
  "v_cvt_pk_bf16_f32 v164, v164, v165\n\t" \
  "v_cvt_pk_bf16_f32 v165, v166, v167\n\t" \
  "v_mfma_f32_16x16x32_bf16 v[0:3], v[242:245], v[236:239], 0\n\t" \
  "ds_read_b64_tr_b16 v[218:219], v249 offset:26200\n\t" \
  "v_cvt_pknorm_i16_f32 v228, |v194|, |v195|\n\t" \
  "s_setprio 0\n\t" \
  "v_cvt_pknorm_i16_f32 v230, |v192|, |v193|\n\t" \
  "v_mfma_f32_16x16x32_bf16 v[4:7], v[240:243], v[236:239], 0\n\t" \
  "ds_read_u16 v224, v224 offset:10528\n\t" \
  "buffer_load_dwordx4 v[236:239], v250, s[8:11], s61 offen offset:64 nt\n\t" \
  "v_lshlrev_b32_sdwa v229, 2, v228 dst_sel:DWORD dst_unused:UNUSED_PAD src0_sel:DWORD src1_sel:BYTE_3\n\t" \
  "v_mfma_f32_16x16x32_bf16 v[0:3], v[208:211], v[146:149], v[0:3]\n\t" \
  "ds_read_u16_d16_hi v227, v227 offset:10528\n\t" \
  "v_lshlrev_b32_sdwa v228, 2, v228 dst_sel:DWORD dst_unused:UNUSED_PAD src0_sel:DWORD src1_sel:BYTE_1\n\t" \
  "v_lshlrev_b32_sdwa v231, 2, v230 dst_sel:DWORD dst_unused:UNUSED_PAD src0_sel:DWORD src1_sel:BYTE_3\n\t" \
  "v_mfma_f32_16x16x32_bf16 v[24:27], v[208:211], v[154:157], 0\n\t" \
  "ds_read_u16 v226, v226 offset:10528\n\t" \
  "v_lshlrev_b32_sdwa v230, 2, v230 dst_sel:DWORD dst_unused:UNUSED_PAD src0_sel:DWORD src1_sel:BYTE_1\n\t" \
  "v_lshlrev_b32_sdwa v233, 1, v203 dst_sel:DWORD dst_unused:UNUSED_PAD src0_sel:DWORD src1_sel:WORD_1\n\t" \
  "v_mfma_f32_16x16x32_bf16 v[48:51], v[208:211], v[162:165], 0\n\t" \
  "ds_read_b32 v229, v229 offset:10016\n\t" \
  "v_lshlrev_b32_sdwa v232, 1, v202 dst_sel:DWORD dst_unused:UNUSED_PAD src0_sel:DWORD src1_sel:WORD_1\n\t" \
  "v_lshlrev_b32_sdwa v235, 1, v201 dst_sel:DWORD dst_unused:UNUSED_PAD src0_sel:DWORD src1_sel:WORD_1\n\t" \
  "v_mfma_f32_16x16x32_bf16 v[72:75], v[208:211], v[170:173], 0\n\t" \
  "ds_read_b64_tr_b16 v[208:209], v249 offset:24672\n\t" \
  "ds_read_b32 v228, v228 offset:10016\n\t" \
  "v_lshlrev_b32_sdwa v234, 1, v200 dst_sel:DWORD dst_unused:UNUSED_PAD src0_sel:DWORD src1_sel:WORD_1\n\t" \
  "v_mfma_f32_16x16x32_bf16 v[4:7], v[212:215], v[146:149], v[4:7]\n\t" \
  "ds_read_b64_tr_b16 v[210:211], v249 offset:26208\n\t" \
  "ds_read_b32 v231, v231 offset:10016\n\t" \
  "s_waitcnt lgkmcnt(11)\n\t" \
  "v_fma_mix_f32 v221, |v179|, v221, v221 op_sel:[0,1,0] op_sel_hi:[0,1,1] clamp\n\t" \
  "v_mfma_f32_16x16x32_bf16 v[28:31], v[212:215], v[154:157], 0\n\t" \
  "ds_read_b32 v230, v230 offset:10016\n\t" \
  "v_fma_mix_f32 v220, |v178|, v220, v220 op_sel:[0,1,0] op_sel_hi:[0,1,1] clamp\n\t" \
  "v_fma_mix_f32 v223, |v177|, v223, v223 op_sel:[0,1,0] op_sel_hi:[0,1,1] clamp\n\t" \
  "v_mfma_f32_16x16x32_bf16 v[52:55], v[212:215], v[162:165], 0\n\t" \
  "ds_read_u16_d16_hi v233, v233 offset:10528\n\t" \
  "v_fma_mix_f32 v222, |v176|, v222, v222 op_sel:[0,1,0] op_sel_hi:[0,1,1] clamp\n\t" \
  "s_waitcnt lgkmcnt(7)\n\t" \
  "v_or_b32 v187, v224, v225\n\t" \
  "v_mfma_f32_16x16x32_bf16 v[76:79], v[212:215], v[170:173], 0\n\t" \
  "ds_read_b64_tr_b16 v[212:213], v249 offset:24728\n\t" \
  "v_or_b32 v186, v226, v227\n\t" \
  "v_fma_f32 v179, |v179|, v221, v179 mul:4\n\t" \
  "v_mfma_f32_16x16x32_bf16 v[8:11], v[216:219], v[146:149], 0\n\t" \
  "ds_read_b64_tr_b16 v[214:215], v249 offset:26264\n\t" \
  "v_fma_f32 v178, |v178|, v220, v178 mul:4\n\t" \
  "v_fma_f32 v177, |v177|, v223, v177 mul:4\n\t" \
  "v_mfma_f32_16x16x32_bf16 v[32:35], v[216:219], v[154:157], 0\n\t" \
  "ds_read_u16 v232, v232 offset:10528\n\t" \
  "v_fma_f32 v176, |v176|, v222, v176 mul:4\n\t" \
  "v_cvt_pk_bf16_f32 v179, v178, v179\n\t" \
  "v_mfma_f32_16x16x32_bf16 v[56:59], v[216:219], v[162:165], 0\n\t" \
  "ds_read_u16_d16_hi v235, v235 offset:10528\n\t" \
  "v_cvt_pk_bf16_f32 v178, v176, v177\n\t" \
  "s_setprio 1\n\t" \
  "v_cvt_pknorm_i16_f32 v220, |v180|, |v181|\n\t" \
  "v_mfma_f32_16x16x32_bf16 v[80:83], v[216:219], v[170:173], 0\n\t" \
  "ds_read_b64_tr_b16 v[216:217], v249 offset:24736\n\t" \
  "v_cvt_pknorm_i16_f32 v222, |v182|, |v183|\n\t" \
  "v_lshlrev_b32_sdwa v221, 2, v220 dst_sel:DWORD dst_unused:UNUSED_PAD src0_sel:DWORD src1_sel:BYTE_3\n\t" \
  "s_waitcnt lgkmcnt(8)\n\t" \
  "v_mfma_f32_16x16x32_bf16 v[12:15], v[208:211], v[146:149], 0\n\t" \
  "ds_read_b64_tr_b16 v[218:219], v249 offset:26272\n\t" \
  "v_lshlrev_b32_sdwa v220, 2, v220 dst_sel:DWORD dst_unused:UNUSED_PAD src0_sel:DWORD src1_sel:BYTE_1\n\t" \
  "v_lshlrev_b32_sdwa v223, 2, v222 dst_sel:DWORD dst_unused:UNUSED_PAD src0_sel:DWORD src1_sel:BYTE_3\n\t" \
  "v_mfma_f32_16x16x32_bf16 v[36:39], v[208:211], v[154:157], 0\n\t" \
  "ds_read_u16 v234, v234 offset:10528\n\t" \
  "v_lshlrev_b32_sdwa v222, 2, v222 dst_sel:DWORD dst_unused:UNUSED_PAD src0_sel:DWORD src1_sel:BYTE_1\n\t" \
  "v_lshlrev_b32_sdwa v225, 1, v189 dst_sel:DWORD dst_unused:UNUSED_PAD src0_sel:DWORD src1_sel:WORD_1\n\t" \
  "v_mfma_f32_16x16x32_bf16 v[60:63], v[208:211], v[162:165], 0\n\t" \
  "ds_read_b32 v221, v221 offset:10016\n\t" \
  "v_lshlrev_b32_sdwa v224, 1, v188 dst_sel:DWORD dst_unused:UNUSED_PAD src0_sel:DWORD src1_sel:WORD_1\n\t" \
  "v_lshlrev_b32_sdwa v227, 1, v191 dst_sel:DWORD dst_unused:UNUSED_PAD src0_sel:DWORD src1_sel:WORD_1\n\t" \
  "v_mfma_f32_16x16x32_bf16 v[84:87], v[208:211], v[170:173], 0\n\t" \
  "ds_read_b64_tr_b16 v[208:209], v248 offset:36904\n\t" \
  "ds_read_b32 v220, v220 offset:10016\n\t" \
  "v_lshlrev_b32_sdwa v226, 1, v190 dst_sel:DWORD dst_unused:UNUSED_PAD src0_sel:DWORD src1_sel:WORD_1\n\t" \
  "s_waitcnt lgkmcnt(8)\n\t" \
  "v_mfma_f32_16x16x32_bf16 v[16:19], v[212:215], v[146:149], 0\n\t" \
  "ds_read_b64_tr_b16 v[210:211], v248 offset:37928\n\t" \
  "ds_read_b32 v223, v223 offset:10016\n\t" \
  "v_fma_mix_f32 v229, |v195|, v229, v229 op_sel:[0,1,0] op_sel_hi:[0,1,1] clamp\n\t" \
  "v_mfma_f32_16x16x32_bf16 v[40:43], v[212:215], v[154:157], 0\n\t" \
  "ds_read_b32 v222, v222 offset:10016\n\t" \
  "v_fma_mix_f32 v228, |v194|, v228, v228 op_sel:[0,1,0] op_sel_hi:[0,1,1] clamp\n\t" \
  "v_fma_mix_f32 v231, |v193|, v231, v231 op_sel:[0,1,0] op_sel_hi:[0,1,1] clamp\n\t" \
  "v_mfma_f32_16x16x32_bf16 v[64:67], v[212:215], v[162:165], 0\n\t" \
  "ds_read_u16_d16_hi v225, v225 offset:10528\n\t" \
  "v_fma_mix_f32 v230, |v192|, v230, v230 op_sel:[0,1,0] op_sel_hi:[0,1,1] clamp\n\t" \
  "s_waitcnt lgkmcnt(7)\n\t" \
  "v_or_b32 v203, v232, v233\n\t" \
  "v_mfma_f32_16x16x32_bf16 v[88:91], v[212:215], v[170:173], 0\n\t" \
  "ds_read_b64_tr_b16 v[212:213], v248 offset:39976\n\t" \
  "v_or_b32 v202, v234, v235\n\t" \
  "v_fma_f32 v195, |v195|, v229, v195 mul:4\n\t" \
  "v_mfma_f32_16x16x32_bf16 v[20:23], v[216:219], v[146:149], 0\n\t" \
  "ds_read_b64_tr_b16 v[214:215], v248 offset:41000\n\t" \
  "v_fma_f32 v194, |v194|, v228, v194 mul:4\n\t" \
  "v_fma_f32 v193, |v193|, v231, v193 mul:4\n\t" \
  "v_mfma_f32_16x16x32_bf16 v[44:47], v[216:219], v[154:157], 0\n\t" \
  "ds_read_u16 v224, v224 offset:10528\n\t" \
  "v_fma_f32 v192, |v192|, v230, v192 mul:4\n\t" \
  "v_cvt_pk_bf16_f32 v195, v194, v195\n\t" \
  "v_mfma_f32_16x16x32_bf16 v[68:71], v[216:219], v[162:165], 0\n\t" \
  "ds_read_u16_d16_hi v227, v227 offset:10528\n\t" \
  "v_cvt_pk_bf16_f32 v194, v192, v193\n\t" \
  "v_cvt_pknorm_i16_f32 v228, |v196|, |v197|\n\t" \
  "v_mfma_f32_16x16x32_bf16 v[92:95], v[216:219], v[170:173], 0\n\t" \
  "ds_read_b64_tr_b16 v[216:217], v248 offset:38952\n\t" \
  "v_cvt_pknorm_i16_f32 v230, |v198|, |v199|\n\t" \
  "s_setprio 2\n\t" \
  "v_lshlrev_b32_sdwa v229, 2, v228 dst_sel:DWORD dst_unused:UNUSED_PAD src0_sel:DWORD src1_sel:BYTE_3\n\t" \
  "s_waitcnt lgkmcnt(8)\n\t" \
  "v_mfma_f32_16x16x32_bf16 v[144:147], v[208:211], v[96:99], 0\n\t" \
  "ds_read_b64_tr_b16 v[218:219], v248 offset:42024\n\t" \
  "v_lshlrev_b32_sdwa v228, 2, v228 dst_sel:DWORD dst_unused:UNUSED_PAD src0_sel:DWORD src1_sel:BYTE_1\n\t" \
  "v_lshlrev_b32_sdwa v231, 2, v230 dst_sel:DWORD dst_unused:UNUSED_PAD src0_sel:DWORD src1_sel:BYTE_3\n\t" \
  "v_mfma_f32_16x16x32_bf16 v[152:155], v[208:211], v[100:103], 0\n\t" \
  "ds_read_u16 v226, v226 offset:10528\n\t" \
  "v_lshlrev_b32_sdwa v230, 2, v230 dst_sel:DWORD dst_unused:UNUSED_PAD src0_sel:DWORD src1_sel:BYTE_1\n\t" \
  "v_mfma_f32_16x16x32_bf16 v[160:163], v[208:211], v[104:107], 0\n\t" \
  "ds_read_b32 v229, v229 offset:10016\n\t" \
  "v_lshlrev_b32_sdwa v233, 1, v205 dst_sel:DWORD dst_unused:UNUSED_PAD src0_sel:DWORD src1_sel:WORD_1\n\t" \
  "v_lshlrev_b32_sdwa v232, 1, v204 dst_sel:DWORD dst_unused:UNUSED_PAD src0_sel:DWORD src1_sel:WORD_1\n\t" \
  "v_mfma_f32_16x16x32_bf16 v[168:171], v[208:211], v[108:111], 0\n\t" \
  "ds_read_b64_tr_b16 v[208:209], v248 offset:37416\n\t" \
  "ds_read_b32 v228, v228 offset:10016\n\t" \
  "v_lshlrev_b32_sdwa v235, 1, v207 dst_sel:DWORD dst_unused:UNUSED_PAD src0_sel:DWORD src1_sel:WORD_1\n\t" \
  "s_waitcnt lgkmcnt(8)\n\t" \
  "v_mfma_f32_16x16x32_bf16 v[144:147], v[212:215], v[112:115], v[144:147]\n\t" \
  "ds_read_b64_tr_b16 v[210:211], v248 offset:38440\n\t" \
  "v_lshlrev_b32_sdwa v234, 1, v206 dst_sel:DWORD dst_unused:UNUSED_PAD src0_sel:DWORD src1_sel:WORD_1\n\t" \
  "v_fma_mix_f32 v221, |v181|, v221, v221 op_sel:[0,1,0] op_sel_hi:[0,1,1] clamp\n\t" \
  "v_mfma_f32_16x16x32_bf16 v[152:155], v[212:215], v[116:119], v[152:155]\n\t" \
  "ds_read_b32 v231, v231 offset:10016\n\t" \
  "ds_read_b32 v230, v230 offset:10016\n\t" \
  "v_fma_mix_f32 v220, |v180|, v220, v220 op_sel:[0,1,0] op_sel_hi:[0,1,1] clamp\n\t" \
  "v_mfma_f32_16x16x32_bf16 v[160:163], v[212:215], v[120:123], v[160:163]\n\t" \
  "ds_read_u16_d16_hi v233, v233 offset:10528\n\t" \
  "v_fma_mix_f32 v223, |v183|, v223, v223 op_sel:[0,1,0] op_sel_hi:[0,1,1] clamp\n\t" \
  "v_fma_mix_f32 v222, |v182|, v222, v222 op_sel:[0,1,0] op_sel_hi:[0,1,1] clamp\n\t" \
  "s_waitcnt lgkmcnt(7)\n\t" \
  "v_mfma_f32_16x16x32_bf16 v[168:171], v[212:215], v[124:127], v[168:171]\n\t" \
  "ds_read_b64_tr_b16 v[212:213], v248 offset:40488\n\t" \
  "v_or_b32 v188, v224, v225\n\t" \
  "v_or_b32 v189, v226, v227\n\t" \
  "v_mfma_f32_16x16x32_bf16 v[144:147], v[216:219], v[128:131], v[144:147]\n\t" \
  "ds_read_b64_tr_b16 v[214:215], v248 offset:41512\n\t" \
  "v_fma_f32 v181, |v181|, v221, v181 mul:4\n\t" \
  "v_fma_f32 v180, |v180|, v220, v180 mul:4\n\t" \
  "v_mfma_f32_16x16x32_bf16 v[152:155], v[216:219], v[132:135], v[152:155]\n\t" \
  "ds_read_u16 v232, v232 offset:10528\n\t" \
  "v_fma_f32 v183, |v183|, v223, v183 mul:4\n\t" \
  "v_fma_f32 v182, |v182|, v222, v182 mul:4\n\t" \
  "v_mfma_f32_16x16x32_bf16 v[160:163], v[216:219], v[136:139], v[160:163]\n\t" \
  "ds_read_u16_d16_hi v235, v235 offset:10528\n\t" \
  "v_cvt_pk_bf16_f32 v180, v180, v181\n\t" \
  "v_cvt_pk_bf16_f32 v181, v182, v183\n\t" \
  "v_mfma_f32_16x16x32_bf16 v[168:171], v[216:219], v[140:143], v[168:171]\n\t" \
  "ds_read_b64_tr_b16 v[216:217], v248 offset:39464\n\t" \
  "v_cvt_pknorm_i16_f32 v220, |v146|, |v147|\n\t" \
  "s_setprio 3\n\t" \
  "v_cvt_pknorm_i16_f32 v222, |v144|, |v145|\n\t" \
  "s_waitcnt lgkmcnt(8)\n\t" \
  "v_mfma_f32_16x16x32_bf16 v[148:151], v[208:211], v[96:99], 0\n\t" \
  "ds_read_b64_tr_b16 v[218:219], v248 offset:42536\n\t" \
  "v_lshlrev_b32_sdwa v221, 2, v220 dst_sel:DWORD dst_unused:UNUSED_PAD src0_sel:DWORD src1_sel:BYTE_3\n\t" \
  "s_waitcnt vmcnt(0)\n\t" \
  "v_lshlrev_b32_sdwa v220, 2, v220 dst_sel:DWORD dst_unused:UNUSED_PAD src0_sel:DWORD src1_sel:BYTE_1\n\t" \
  "v_mfma_f32_16x16x32_bf16 v[156:159], v[208:211], v[100:103], 0\n\t" \
  "s_mov_b64 exec, 3\n\t" \
  "ds_add_rtn_u32 v246, v251, v250 offset:47152\n\t" \
  "s_mov_b64 exec, -1\n\t" \
  "v_lshlrev_b32_sdwa v223, 2, v222 dst_sel:DWORD dst_unused:UNUSED_PAD src0_sel:DWORD src1_sel:BYTE_3\n\t" \
  "v_mfma_f32_16x16x32_bf16 v[164:167], v[208:211], v[104:107], 0\n\t" \
  "ds_read_u16 v234, v234 offset:10528\n\t" \
  "v_lshlrev_b32_sdwa v222, 2, v222 dst_sel:DWORD dst_unused:UNUSED_PAD src0_sel:DWORD src1_sel:BYTE_1\n\t" \
  "v_lshlrev_b32_sdwa v225, 1, v155 dst_sel:DWORD dst_unused:UNUSED_PAD src0_sel:DWORD src1_sel:WORD_1\n\t" \
  "v_mfma_f32_16x16x32_bf16 v[172:175], v[208:211], v[108:111], 0\n\t" \
  "ds_read_b32 v221, v221 offset:10016\n\t" \
  "v_lshlrev_b32_sdwa v224, 1, v154 dst_sel:DWORD dst_unused:UNUSED_PAD src0_sel:DWORD src1_sel:WORD_1\n\t" \
  "s_waitcnt lgkmcnt(2)\n\t" \
  "v_readfirstlane_b32 s4, v246\n\t" \
  "v_mfma_f32_16x16x32_bf16 v[148:151], v[212:215], v[112:115], v[148:151]\n\t" \
  "ds_read_b32 v220, v220 offset:10016\n\t" \
  "v_lshlrev_b32_sdwa v227, 1, v153 dst_sel:DWORD dst_unused:UNUSED_PAD src0_sel:DWORD src1_sel:WORD_1\n\t" \
  "s_cmp_lt_u32 s4, s57\n\t" \
  "s_cbranch_scc1 .Lcnx_f1_3\n.Lcnx_g1_3:\n\t" \
  "v_mfma_f32_16x16x32_bf16 v[156:159], v[212:215], v[116:119], v[156:159]\n\t" \
  "ds_read_b64_tr_b16 v[208:209], v249 offset:30752\n\t" \
  "ds_read_b32 v223, v223 offset:10016\n\t" \
  "v_lshlrev_b32_sdwa v226, 1, v152 dst_sel:DWORD dst_unused:UNUSED_PAD src0_sel:DWORD src1_sel:WORD_1\n\t" \
  "v_mfma_f32_16x16x32_bf16 v[164:167], v[212:215], v[120:123], v[164:167]\n\t" \
  "ds_read_b64_tr_b16 v[210:211], v249 offset:32288\n\t" \
  "v_fma_mix_f32 v229, |v197|, v229, v229 op_sel:[0,1,0] op_sel_hi:[0,1,1] clamp\n\t" \
  "v_fma_mix_f32 v228, |v196|, v228, v228 op_sel:[0,1,0] op_sel_hi:[0,1,1] clamp\n\t" \
  "v_mfma_f32_16x16x32_bf16 v[172:175], v[212:215], v[124:127], v[172:175]\n\t" \
  "ds_read_b64_tr_b16 v[212:213], v249 offset:30760\n\t" \
  "ds_read_b32 v222, v222 offset:10016\n\t" \
  "v_fma_mix_f32 v231, |v199|, v231, v231 op_sel:[0,1,0] op_sel_hi:[0,1,1] clamp\n\t" \
  "v_fma_mix_f32 v230, |v198|, v230, v230 op_sel:[0,1,0] op_sel_hi:[0,1,1] clamp\n\t" \
  "s_waitcnt lgkmcnt(7)\n\t" \
  "v_mfma_f32_16x16x32_bf16 v[148:151], v[216:219], v[128:131], v[148:151]\n\t" \
  "ds_read_b64_tr_b16 v[214:215], v249 offset:32296\n\t" \
  "v_or_b32 v204, v232, v233\n\t" \
  "v_or_b32 v205, v234, v235\n\t" \
  "v_mfma_f32_16x16x32_bf16 v[156:159], v[216:219], v[132:135], v[156:159]\n\t" \
  "ds_read_u16_d16_hi v225, v225 offset:10528\n\t" \
  "v_fma_f32 v197, |v197|, v229, v197 mul:4\n\t" \
  "v_fma_f32 v196, |v196|, v228, v196 mul:4\n\t" \
  "v_mfma_f32_16x16x32_bf16 v[164:167], v[216:219], v[136:139], v[164:167]\n\t" \
  "ds_read_u16 v224, v224 offset:10528\n\t" \
  "v_fma_f32 v199, |v199|, v231, v199 mul:4\n\t" \
  "v_fma_f32 v198, |v198|, v230, v198 mul:4\n\t" \
  "v_mfma_f32_16x16x32_bf16 v[172:175], v[216:219], v[140:143], v[172:175]\n\t" \
  "ds_read_b64_tr_b16 v[216:217], v249 offset:30816\n\t" \
  "v_cvt_pk_bf16_f32 v196, v196, v197\n\t" \
  "v_cvt_pk_bf16_f32 v197, v198, v199\n\t" \
  "v_mfma_f32_16x16x32_bf16 v[8:11], v[242:245], v[236:239], v[8:11]\n\t" \
  "ds_read_b64_tr_b16 v[218:219], v249 offset:32352\n\t" \
  "v_cvt_pknorm_i16_f32 v228, |v162|, |v163|\n\t" \
  "s_setprio 0\n\t" \
  "v_cvt_pknorm_i16_f32 v230, |v160|, |v161|\n\t" \
  "v_mfma_f32_16x16x32_bf16 v[12:15], v[240:243], v[236:239], v[12:15]\n\t" \
  "ds_read_u16_d16_hi v227, v227 offset:10528\n\t" \
  "buffer_load_dwordx4 v[236:239], v250, s[8:11], s61 offen offset:128 nt\n\t" \
  "v_lshlrev_b32_sdwa v229, 2, v228 dst_sel:DWORD dst_unused:UNUSED_PAD src0_sel:DWORD src1_sel:BYTE_3\n\t" \
  "s_waitcnt lgkmcnt(8)\n\t" \
  "v_mfma_f32_16x16x32_bf16 v[0:3], v[208:211], v[178:181], v[0:3]\n\t" \
  "ds_read_u16 v226, v226 offset:10528\n\t" \
  "v_lshlrev_b32_sdwa v228, 2, v228 dst_sel:DWORD dst_unused:UNUSED_PAD src0_sel:DWORD src1_sel:BYTE_1\n\t" \
  "v_lshlrev_b32_sdwa v231, 2, v230 dst_sel:DWORD dst_unused:UNUSED_PAD src0_sel:DWORD src1_sel:BYTE_3\n\t" \
  "v_mfma_f32_16x16x32_bf16 v[24:27], v[208:211], v[186:189], v[24:27]\n\t" \
  "ds_read_b32 v229, v229 offset:10016\n\t" \
  "v_lshlrev_b32_sdwa v230, 2, v230 dst_sel:DWORD dst_unused:UNUSED_PAD src0_sel:DWORD src1_sel:BYTE_1\n\t" \
  "v_lshlrev_b32_sdwa v233, 1, v171 dst_sel:DWORD dst_unused:UNUSED_PAD src0_sel:DWORD src1_sel:WORD_1\n\t" \
  "v_mfma_f32_16x16x32_bf16 v[48:51], v[208:211], v[194:197], v[48:51]\n\t" \
  "ds_read_b32 v228, v228 offset:10016\n\t" \
  "v_lshlrev_b32_sdwa v232, 1, v170 dst_sel:DWORD dst_unused:UNUSED_PAD src0_sel:DWORD src1_sel:WORD_1\n\t" \
  "v_lshlrev_b32_sdwa v235, 1, v169 dst_sel:DWORD dst_unused:UNUSED_PAD src0_sel:DWORD src1_sel:WORD_1\n\t" \
  "v_mfma_f32_16x16x32_bf16 v[72:75], v[208:211], v[202:205], v[72:75]\n\t" \
  "ds_read_b64_tr_b16 v[208:209], v249 offset:30824\n\t" \
  "ds_read_b32 v231, v231 offset:10016\n\t" \
  "v_lshlrev_b32_sdwa v234, 1, v168 dst_sel:DWORD dst_unused:UNUSED_PAD src0_sel:DWORD src1_sel:WORD_1\n\t" \
  "s_waitcnt lgkmcnt(10)\n\t" \
  "v_mfma_f32_16x16x32_bf16 v[4:7], v[212:215], v[178:181], v[4:7]\n\t" \
  "ds_read_b64_tr_b16 v[210:211], v249 offset:32360\n\t" \
  "v_fma_mix_f32 v221, |v147|, v221, v221 op_sel:[0,1,0] op_sel_hi:[0,1,1] clamp\n\t" \
  "v_fma_mix_f32 v220, |v146|, v220, v220 op_sel:[0,1,0] op_sel_hi:[0,1,1] clamp\n\t" \
  "v_mfma_f32_16x16x32_bf16 v[28:31], v[212:215], v[186:189], v[28:31]\n\t" \
  "ds_read_b32 v230, v230 offset:10016\n\t" \
  "v_fma_mix_f32 v223, |v145|, v223, v223 op_sel:[0,1,0] op_sel_hi:[0,1,1] clamp\n\t" \
  "v_fma_mix_f32 v222, |v144|, v222, v222 op_sel:[0,1,0] op_sel_hi:[0,1,1] clamp\n\t" \
  "s_waitcnt lgkmcnt(6)\n\t" \
  "v_mfma_f32_16x16x32_bf16 v[52:55], v[212:215], v[194:197], v[52:55]\n\t" \
  "ds_read_u16_d16_hi v233, v233 offset:10528\n\t" \
  "v_or_b32 v155, v224, v225\n\t" \
  "v_or_b32 v154, v226, v227\n\t" \
  "v_mfma_f32_16x16x32_bf16 v[76:79], v[212:215], v[202:205], v[76:79]\n\t" \
  "ds_read_b64_tr_b16 v[212:213], v249 offset:30880\n\t" \
  "v_fma_f32 v147, |v147|, v221, v147 mul:4\n\t" \
  "v_fma_f32 v146, |v146|, v220, v146 mul:4\n\t" \
  "v_mfma_f32_16x16x32_bf16 v[8:11], v[216:219], v[178:181], v[8:11]\n\t" \
  "ds_read_b64_tr_b16 v[214:215], v249 offset:32416\n\t" \
  "v_fma_f32 v145, |v145|, v223, v145 mul:4\n\t" \
  "v_fma_f32 v144, |v144|, v222, v144 mul:4\n\t" \
  "v_mfma_f32_16x16x32_bf16 v[32:35], v[216:219], v[186:189], v[32:35]\n\t" \
  "ds_read_u16 v232, v232 offset:10528\n\t" \
  "v_cvt_pk_bf16_f32 v147, v146, v147\n\t" \
  "v_cvt_pk_bf16_f32 v146, v144, v145\n\t" \
  "v_mfma_f32_16x16x32_bf16 v[56:59], v[216:219], v[194:197], v[56:59]\n\t" \
  "ds_read_u16_d16_hi v235, v235 offset:10528\n\t" \
  "v_cvt_pknorm_i16_f32 v220, |v148|, |v149|\n\t" \
  "s_setprio 1\n\t" \
  "v_cvt_pknorm_i16_f32 v222, |v150|, |v151|\n\t" \
  "v_mfma_f32_16x16x32_bf16 v[80:83], v[216:219], v[202:205], v[80:83]\n\t" \
  "ds_read_b64_tr_b16 v[216:217], v249 offset:30888\n\t" \
  "v_lshlrev_b32_sdwa v221, 2, v220 dst_sel:DWORD dst_unused:UNUSED_PAD src0_sel:DWORD src1_sel:BYTE_3\n\t" \
  "s_waitcnt lgkmcnt(7)\n\t" \
  "v_mfma_f32_16x16x32_bf16 v[12:15], v[208:211], v[178:181], v[12:15]\n\t" \
  "ds_read_b64_tr_b16 v[218:219], v249 offset:32424\n\t" \
  "v_lshlrev_b32_sdwa v220, 2, v220 dst_sel:DWORD dst_unused:UNUSED_PAD src0_sel:DWORD src1_sel:BYTE_1\n\t" \
  "v_lshlrev_b32_sdwa v223, 2, v222 dst_sel:DWORD dst_unused:UNUSED_PAD src0_sel:DWORD src1_sel:BYTE_3\n\t" \
  "v_mfma_f32_16x16x32_bf16 v[36:39], v[208:211], v[186:189], v[36:39]\n\t" \
  "ds_read_u16 v234, v234 offset:10528\n\t" \
  "v_lshlrev_b32_sdwa v222, 2, v222 dst_sel:DWORD dst_unused:UNUSED_PAD src0_sel:DWORD src1_sel:BYTE_1\n\t" \
  "v_lshlrev_b32_sdwa v225, 1, v157 dst_sel:DWORD dst_unused:UNUSED_PAD src0_sel:DWORD src1_sel:WORD_1\n\t" \
  "v_mfma_f32_16x16x32_bf16 v[60:63], v[208:211], v[194:197], v[60:63]\n\t" \
  "ds_read_b32 v221, v221 offset:10016\n\t" \
  "v_lshlrev_b32_sdwa v224, 1, v156 dst_sel:DWORD dst_unused:UNUSED_PAD src0_sel:DWORD src1_sel:WORD_1\n\t" \
  "v_lshlrev_b32_sdwa v227, 1, v159 dst_sel:DWORD dst_unused:UNUSED_PAD src0_sel:DWORD src1_sel:WORD_1\n\t" \
  "v_mfma_f32_16x16x32_bf16 v[84:87], v[208:211], v[202:205], v[84:87]\n\t" \
  "ds_read_b64_tr_b16 v[208:209], v248 offset:0\n\t" \
  "ds_read_b32 v220, v220 offset:10016\n\t" \
  "v_lshlrev_b32_sdwa v226, 1, v158 dst_sel:DWORD dst_unused:UNUSED_PAD src0_sel:DWORD src1_sel:WORD_1\n\t" \
  "s_waitcnt lgkmcnt(8)\n\t" \
  "v_mfma_f32_16x16x32_bf16 v[16:19], v[212:215], v[178:181], v[16:19]\n\t" \
  "ds_read_b64_tr_b16 v[210:211], v248 offset:1024\n\t" \
  "ds_read_b32 v223, v223 offset:10016\n\t" \
  "v_fma_mix_f32 v229, |v163|, v229, v229 op_sel:[0,1,0] op_sel_hi:[0,1,1] clamp\n\t" \
  "v_mfma_f32_16x16x32_bf16 v[40:43], v[212:215], v[186:189], v[40:43]\n\t" \
  "ds_read_b32 v222, v222 offset:10016\n\t" \
  "v_fma_mix_f32 v228, |v162|, v228, v228 op_sel:[0,1,0] op_sel_hi:[0,1,1] clamp\n\t" \
  "v_fma_mix_f32 v231, |v161|, v231, v231 op_sel:[0,1,0] op_sel_hi:[0,1,1] clamp\n\t" \
  "v_mfma_f32_16x16x32_bf16 v[64:67], v[212:215], v[194:197], v[64:67]\n\t" \
  "ds_read_u16_d16_hi v225, v225 offset:10528\n\t" \
  "v_fma_mix_f32 v230, |v160|, v230, v230 op_sel:[0,1,0] op_sel_hi:[0,1,1] clamp\n\t" \
  "s_waitcnt lgkmcnt(7)\n\t" \
  "v_or_b32 v171, v232, v233\n\t" \
  "v_mfma_f32_16x16x32_bf16 v[88:91], v[212:215], v[202:205], v[88:91]\n\t" \
  "ds_read_b64_tr_b16 v[212:213], v248 offset:3072\n\t" \
  "v_or_b32 v170, v234, v235\n\t" \
  "v_fma_f32 v163, |v163|, v229, v163 mul:4\n\t" \
  "v_mfma_f32_16x16x32_bf16 v[20:23], v[216:219], v[178:181], v[20:23]\n\t" \
  "ds_read_b64_tr_b16 v[214:215], v248 offset:4096\n\t" \
  "v_fma_f32 v162, |v162|, v228, v162 mul:4\n\t" \
  "v_fma_f32 v161, |v161|, v231, v161 mul:4\n\t" \
  "v_mfma_f32_16x16x32_bf16 v[44:47], v[216:219], v[186:189], v[44:47]\n\t" \
  "ds_read_u16 v224, v224 offset:10528\n\t" \
  "v_fma_f32 v160, |v160|, v230, v160 mul:4\n\t" \
  "v_cvt_pk_bf16_f32 v163, v162, v163\n\t" \
  "v_mfma_f32_16x16x32_bf16 v[68:71], v[216:219], v[194:197], v[68:71]\n\t" \
  "ds_read_u16_d16_hi v227, v227 offset:10528\n\t" \
  "v_cvt_pk_bf16_f32 v162, v160, v161\n\t" \
  "v_cvt_pknorm_i16_f32 v228, |v164|, |v165|\n\t" \
  "v_mfma_f32_16x16x32_bf16 v[92:95], v[216:219], v[202:205], v[92:95]\n\t" \
  "ds_read_b64_tr_b16 v[216:217], v248 offset:2048\n\t" \
  "v_cvt_pknorm_i16_f32 v230, |v166|, |v167|\n\t" \
  "s_setprio 2\n\t" \
  "v_lshlrev_b32_sdwa v229, 2, v228 dst_sel:DWORD dst_unused:UNUSED_PAD src0_sel:DWORD src1_sel:BYTE_3\n\t" \
  "s_waitcnt lgkmcnt(8)\n\t" \
  "v_mfma_f32_16x16x32_bf16 v[176:179], v[208:211], v[96:99], 0\n\t" \
  "ds_read_b64_tr_b16 v[218:219], v248 offset:5120\n\t" \
  "v_lshlrev_b32_sdwa v228, 2, v228 dst_sel:DWORD dst_unused:UNUSED_PAD src0_sel:DWORD src1_sel:BYTE_1\n\t" \
  "v_lshlrev_b32_sdwa v231, 2, v230 dst_sel:DWORD dst_unused:UNUSED_PAD src0_sel:DWORD src1_sel:BYTE_3\n\t" \
  "v_mfma_f32_16x16x32_bf16 v[184:187], v[208:211], v[100:103], 0\n\t" \
  "ds_read_u16 v226, v226 offset:10528\n\t" \
  "v_lshlrev_b32_sdwa v230, 2, v230 dst_sel:DWORD dst_unused:UNUSED_PAD src0_sel:DWORD src1_sel:BYTE_1\n\t" \
  "v_mfma_f32_16x16x32_bf16 v[192:195], v[208:211], v[104:107], 0\n\t" \
  "ds_read_b32 v229, v229 offset:10016\n\t" \
  "v_lshlrev_b32_sdwa v233, 1, v173 dst_sel:DWORD dst_unused:UNUSED_PAD src0_sel:DWORD src1_sel:WORD_1\n\t" \
  "v_lshlrev_b32_sdwa v232, 1, v172 dst_sel:DWORD dst_unused:UNUSED_PAD src0_sel:DWORD src1_sel:WORD_1\n\t" \
  "v_mfma_f32_16x16x32_bf16 v[200:203], v[208:211], v[108:111], 0\n\t" \
  "ds_read_b64_tr_b16 v[208:209], v248 offset:512\n\t" \
  "ds_read_b32 v228, v228 offset:10016\n\t" \
  "v_lshlrev_b32_sdwa v235, 1, v175 dst_sel:DWORD dst_unused:UNUSED_PAD src0_sel:DWORD src1_sel:WORD_1\n\t" \
  "s_waitcnt lgkmcnt(8)\n\t" \
  "v_mfma_f32_16x16x32_bf16 v[176:179], v[212:215], v[112:115], v[176:179]\n\t" \
  "ds_read_b64_tr_b16 v[210:211], v248 offset:1536\n\t" \
  "v_lshlrev_b32_sdwa v234, 1, v174 dst_sel:DWORD dst_unused:UNUSED_PAD src0_sel:DWORD src1_sel:WORD_1\n\t" \
  "v_fma_mix_f32 v221, |v149|, v221, v221 op_sel:[0,1,0] op_sel_hi:[0,1,1] clamp\n\t" \
  "v_mfma_f32_16x16x32_bf16 v[184:187], v[212:215], v[116:119], v[184:187]\n\t" \
  "ds_read_b32 v231, v231 offset:10016\n\t" \
  "ds_read_b32 v230, v230 offset:10016\n\t" \
  "v_fma_mix_f32 v220, |v148|, v220, v220 op_sel:[0,1,0] op_sel_hi:[0,1,1] clamp\n\t" \
  "v_mfma_f32_16x16x32_bf16 v[192:195], v[212:215], v[120:123], v[192:195]\n\t" \
  "ds_read_u16_d16_hi v233, v233 offset:10528\n\t" \
  "v_fma_mix_f32 v223, |v151|, v223, v223 op_sel:[0,1,0] op_sel_hi:[0,1,1] clamp\n\t" \
  "v_fma_mix_f32 v222, |v150|, v222, v222 op_sel:[0,1,0] op_sel_hi:[0,1,1] clamp\n\t" \
  "s_waitcnt lgkmcnt(7)\n\t" \
  "v_mfma_f32_16x16x32_bf16 v[200:203], v[212:215], v[124:127], v[200:203]\n\t" \
  "ds_read_b64_tr_b16 v[212:213], v248 offset:3584\n\t" \
  "v_or_b32 v156, v224, v225\n\t" \
  "v_or_b32 v157, v226, v227\n\t" \
  "v_mfma_f32_16x16x32_bf16 v[176:179], v[216:219], v[128:131], v[176:179]\n\t" \
  "ds_read_b64_tr_b16 v[214:215], v248 offset:4608\n\t" \
  "v_fma_f32 v149, |v149|, v221, v149 mul:4\n\t" \
  "v_fma_f32 v148, |v148|, v220, v148 mul:4\n\t" \
  "v_mfma_f32_16x16x32_bf16 v[184:187], v[216:219], v[132:135], v[184:187]\n\t" \
  "ds_read_u16 v232, v232 offset:10528\n\t" \
  "v_fma_f32 v151, |v151|, v223, v151 mul:4\n\t" \
  "v_fma_f32 v150, |v150|, v222, v150 mul:4\n\t" \
  "v_mfma_f32_16x16x32_bf16 v[192:195], v[216:219], v[136:139], v[192:195]\n\t" \
  "ds_read_u16_d16_hi v235, v235 offset:10528\n\t" \
  "v_cvt_pk_bf16_f32 v148, v148, v149\n\t" \
  "v_cvt_pk_bf16_f32 v149, v150, v151\n\t" \
  "v_mfma_f32_16x16x32_bf16 v[200:203], v[216:219], v[140:143], v[200:203]\n\t" \
  "ds_read_b64_tr_b16 v[216:217], v248 offset:2560\n\t" \
  "v_cvt_pknorm_i16_f32 v220, |v178|, |v179|\n\t" \
  "s_setprio 3\n\t" \
  "v_cvt_pknorm_i16_f32 v222, |v176|, |v177|\n\t" \
  "s_waitcnt lgkmcnt(8)\n\t" \
  "v_mfma_f32_16x16x32_bf16 v[180:183], v[208:211], v[96:99], 0\n\t" \
  "ds_read_b64_tr_b16 v[218:219], v248 offset:5632\n\t" \
  "v_lshlrev_b32_sdwa v221, 2, v220 dst_sel:DWORD dst_unused:UNUSED_PAD src0_sel:DWORD src1_sel:BYTE_3\n\t" \
  "s_waitcnt vmcnt(0)\n\t" \
  "v_lshlrev_b32_sdwa v220, 2, v220 dst_sel:DWORD dst_unused:UNUSED_PAD src0_sel:DWORD src1_sel:BYTE_1\n\t" \
  "v_mfma_f32_16x16x32_bf16 v[188:191], v[208:211], v[100:103], 0\n\t" \
  "s_mov_b64 exec, 3\n\t" \
  "ds_add_rtn_u32 v246, v251, v250 offset:47160\n\t" \
  "s_mov_b64 exec, -1\n\t" \
  "v_lshlrev_b32_sdwa v223, 2, v222 dst_sel:DWORD dst_unused:UNUSED_PAD src0_sel:DWORD src1_sel:BYTE_3\n\t" \
  "v_mfma_f32_16x16x32_bf16 v[196:199], v[208:211], v[104:107], 0\n\t" \
  "ds_read_u16 v234, v234 offset:10528\n\t" \
  "v_lshlrev_b32_sdwa v222, 2, v222 dst_sel:DWORD dst_unused:UNUSED_PAD src0_sel:DWORD src1_sel:BYTE_1\n\t" \
  "v_lshlrev_b32_sdwa v225, 1, v187 dst_sel:DWORD dst_unused:UNUSED_PAD src0_sel:DWORD src1_sel:WORD_1\n\t" \
  "v_mfma_f32_16x16x32_bf16 v[204:207], v[208:211], v[108:111], 0\n\t" \
  "ds_read_b32 v221, v221 offset:10016\n\t" \
  "v_lshlrev_b32_sdwa v224, 1, v186 dst_sel:DWORD dst_unused:UNUSED_PAD src0_sel:DWORD src1_sel:WORD_1\n\t" \
  "s_waitcnt lgkmcnt(2)\n\t" \
  "v_readfirstlane_b32 s4, v246\n\t" \
  "v_mfma_f32_16x16x32_bf16 v[180:183], v[212:215], v[112:115], v[180:183]\n\t" \
  "ds_read_b32 v220, v220 offset:10016\n\t" \
  "v_lshlrev_b32_sdwa v227, 1, v185 dst_sel:DWORD dst_unused:UNUSED_PAD src0_sel:DWORD src1_sel:WORD_1\n\t" \
  "s_cmp_lt_u32 s4, s57\n\t" \
  "s_cbranch_scc1 .Lcnx_f1_4\n.Lcnx_g1_4:\n\t" \
  "v_mfma_f32_16x16x32_bf16 v[188:191], v[212:215], v[116:119], v[188:191]\n\t" \
  "ds_read_b64_tr_b16 v[208:209], v249 offset:6152\n\t" \
  "ds_read_b32 v223, v223 offset:10016\n\t" \
  "v_lshlrev_b32_sdwa v226, 1, v184 dst_sel:DWORD dst_unused:UNUSED_PAD src0_sel:DWORD src1_sel:WORD_1\n\t" \
  "v_mfma_f32_16x16x32_bf16 v[196:199], v[212:215], v[120:123], v[196:199]\n\t" \
  "ds_read_b64_tr_b16 v[210:211], v249 offset:7688\n\t" \
  "v_fma_mix_f32 v229, |v165|, v229, v229 op_sel:[0,1,0] op_sel_hi:[0,1,1] clamp\n\t" \
  "v_fma_mix_f32 v228, |v164|, v228, v228 op_sel:[0,1,0] op_sel_hi:[0,1,1] clamp\n\t" \
  "v_mfma_f32_16x16x32_bf16 v[204:207], v[212:215], v[124:127], v[204:207]\n\t" \
  "ds_read_b64_tr_b16 v[212:213], v249 offset:6160\n\t" \
  "ds_read_b32 v222, v222 offset:10016\n\t" \
  "v_fma_mix_f32 v231, |v167|, v231, v231 op_sel:[0,1,0] op_sel_hi:[0,1,1] clamp\n\t" \
  "v_fma_mix_f32 v230, |v166|, v230, v230 op_sel:[0,1,0] op_sel_hi:[0,1,1] clamp\n\t" \
  "s_waitcnt lgkmcnt(7)\n\t" \
  "v_mfma_f32_16x16x32_bf16 v[180:183], v[216:219], v[128:131], v[180:183]\n\t" \
  "ds_read_b64_tr_b16 v[214:215], v249 offset:7696\n\t" \
  "v_or_b32 v172, v232, v233\n\t" \
  "v_or_b32 v173, v234, v235\n\t" \
  "v_mfma_f32_16x16x32_bf16 v[188:191], v[216:219], v[132:135], v[188:191]\n\t" \
  "ds_read_u16_d16_hi v225, v225 offset:10528\n\t" \
  "v_fma_f32 v165, |v165|, v229, v165 mul:4\n\t" \
  "v_fma_f32 v164, |v164|, v228, v164 mul:4\n\t" \
  "v_mfma_f32_16x16x32_bf16 v[196:199], v[216:219], v[136:139], v[196:199]\n\t" \
  "ds_read_u16 v224, v224 offset:10528\n\t" \
  "v_fma_f32 v167, |v167|, v231, v167 mul:4\n\t" \
  "v_fma_f32 v166, |v166|, v230, v166 mul:4\n\t" \
  "v_mfma_f32_16x16x32_bf16 v[204:207], v[216:219], v[140:143], v[204:207]\n\t" \
  "ds_read_b64_tr_b16 v[216:217], v249 offset:6216\n\t" \
  "v_cvt_pk_bf16_f32 v164, v164, v165\n\t" \
  "v_cvt_pk_bf16_f32 v165, v166, v167\n\t" \
  "v_mfma_f32_16x16x32_bf16 v[16:19], v[242:245], v[236:239], v[16:19]\n\t" \
  "ds_read_b64_tr_b16 v[218:219], v249 offset:7752\n\t" \
  "v_cvt_pknorm_i16_f32 v228, |v194|, |v195|\n\t" \
  "s_setprio 0\n\t" \
  "v_cvt_pknorm_i16_f32 v230, |v192|, |v193|\n\t" \
  "v_mfma_f32_16x16x32_bf16 v[20:23], v[240:243], v[236:239], v[20:23]\n\t" \
  "ds_read_u16_d16_hi v227, v227 offset:10528\n\t" \
  "buffer_load_dwordx4 v[236:239], v250, s[8:11], s62 offen offset:0 nt\n\t" \
  "v_lshlrev_b32_sdwa v229, 2, v228 dst_sel:DWORD dst_unused:UNUSED_PAD src0_sel:DWORD src1_sel:BYTE_3\n\t" \
  "s_waitcnt lgkmcnt(8)\n\t" \
  "v_mfma_f32_16x16x32_bf16 v[0:3], v[208:211], v[146:149], v[0:3]\n\t" \
  "ds_read_u16 v226, v226 offset:10528\n\t" \
  "v_lshlrev_b32_sdwa v228, 2, v228 dst_sel:DWORD dst_unused:UNUSED_PAD src0_sel:DWORD src1_sel:BYTE_1\n\t" \
  "v_lshlrev_b32_sdwa v231, 2, v230 dst_sel:DWORD dst_unused:UNUSED_PAD src0_sel:DWORD src1_sel:BYTE_3\n\t" \
  "v_mfma_f32_16x16x32_bf16 v[24:27], v[208:211], v[154:157], v[24:27]\n\t" \
  "ds_read_b32 v229, v229 offset:10016\n\t" \
  "v_lshlrev_b32_sdwa v230, 2, v230 dst_sel:DWORD dst_unused:UNUSED_PAD src0_sel:DWORD src1_sel:BYTE_1\n\t" \
  "v_lshlrev_b32_sdwa v233, 1, v203 dst_sel:DWORD dst_unused:UNUSED_PAD src0_sel:DWORD src1_sel:WORD_1\n\t" \
  "v_mfma_f32_16x16x32_bf16 v[48:51], v[208:211], v[162:165], v[48:51]\n\t" \
  "ds_read_b32 v228, v228 offset:10016\n\t" \
  "v_lshlrev_b32_sdwa v232, 1, v202 dst_sel:DWORD dst_unused:UNUSED_PAD src0_sel:DWORD src1_sel:WORD_1\n\t" \
  "v_lshlrev_b32_sdwa v235, 1, v201 dst_sel:DWORD dst_unused:UNUSED_PAD src0_sel:DWORD src1_sel:WORD_1\n\t" \
  "v_mfma_f32_16x16x32_bf16 v[72:75], v[208:211], v[170:173], v[72:75]\n\t" \
  "ds_read_b64_tr_b16 v[208:209], v249 offset:6224\n\t" \
  "ds_read_b32 v231, v231 offset:10016\n\t" \
  "v_lshlrev_b32_sdwa v234, 1, v200 dst_sel:DWORD dst_unused:UNUSED_PAD src0_sel:DWORD src1_sel:WORD_1\n\t" \
  "s_waitcnt lgkmcnt(10)\n\t" \
  "v_mfma_f32_16x16x32_bf16 v[4:7], v[212:215], v[146:149], v[4:7]\n\t" \
  "ds_read_b64_tr_b16 v[210:211], v249 offset:7760\n\t" \
  "v_fma_mix_f32 v221, |v179|, v221, v221 op_sel:[0,1,0] op_sel_hi:[0,1,1] clamp\n\t" \
  "v_fma_mix_f32 v220, |v178|, v220, v220 op_sel:[0,1,0] op_sel_hi:[0,1,1] clamp\n\t" \
  "v_mfma_f32_16x16x32_bf16 v[28:31], v[212:215], v[154:157], v[28:31]\n\t" \
  "ds_read_b32 v230, v230 offset:10016\n\t" \
  "v_fma_mix_f32 v223, |v177|, v223, v223 op_sel:[0,1,0] op_sel_hi:[0,1,1] clamp\n\t" \
  "v_fma_mix_f32 v222, |v176|, v222, v222 op_sel:[0,1,0] op_sel_hi:[0,1,1] clamp\n\t" \
  "s_waitcnt lgkmcnt(6)\n\t" \
  "v_mfma_f32_16x16x32_bf16 v[52:55], v[212:215], v[162:165], v[52:55]\n\t" \
  "ds_read_u16_d16_hi v233, v233 offset:10528\n\t" \
  "v_or_b32 v187, v224, v225\n\t" \
  "v_or_b32 v186, v226, v227\n\t" \
  "v_mfma_f32_16x16x32_bf16 v[76:79], v[212:215], v[170:173], v[76:79]\n\t" \
  "ds_read_b64_tr_b16 v[212:213], v249 offset:6280\n\t" \
  "v_fma_f32 v179, |v179|, v221, v179 mul:4\n\t" \
  "v_fma_f32 v178, |v178|, v220, v178 mul:4\n\t" \
  "v_mfma_f32_16x16x32_bf16 v[8:11], v[216:219], v[146:149], v[8:11]\n\t" \
  "ds_read_b64_tr_b16 v[214:215], v249 offset:7816\n\t" \
  "v_fma_f32 v177, |v177|, v223, v177 mul:4\n\t" \
  "v_fma_f32 v176, |v176|, v222, v176 mul:4\n\t" \
  "v_mfma_f32_16x16x32_bf16 v[32:35], v[216:219], v[154:157], v[32:35]\n\t" \
  "ds_read_u16 v232, v232 offset:10528\n\t" \
  "v_cvt_pk_bf16_f32 v179, v178, v179\n\t" \
  "v_cvt_pk_bf16_f32 v178, v176, v177\n\t" \
  "v_mfma_f32_16x16x32_bf16 v[56:59], v[216:219], v[162:165], v[56:59]\n\t" \
  "ds_read_u16_d16_hi v235, v235 offset:10528\n\t" \
  "v_cvt_pknorm_i16_f32 v220, |v180|, |v181|\n\t" \
  "s_setprio 1\n\t" \
  "v_cvt_pknorm_i16_f32 v222, |v182|, |v183|\n\t" \
  "v_mfma_f32_16x16x32_bf16 v[80:83], v[216:219], v[170:173], v[80:83]\n\t" \
  "ds_read_b64_tr_b16 v[216:217], v249 offset:6288\n\t" \
  "v_lshlrev_b32_sdwa v221, 2, v220 dst_sel:DWORD dst_unused:UNUSED_PAD src0_sel:DWORD src1_sel:BYTE_3\n\t" \
  "s_waitcnt lgkmcnt(7)\n\t" \
  "v_mfma_f32_16x16x32_bf16 v[12:15], v[208:211], v[146:149], v[12:15]\n\t" \
  "ds_read_b64_tr_b16 v[218:219], v249 offset:7824\n\t" \
  "v_lshlrev_b32_sdwa v220, 2, v220 dst_sel:DWORD dst_unused:UNUSED_PAD src0_sel:DWORD src1_sel:BYTE_1\n\t" \
  "v_lshlrev_b32_sdwa v223, 2, v222 dst_sel:DWORD dst_unused:UNUSED_PAD src0_sel:DWORD src1_sel:BYTE_3\n\t" \
  "v_mfma_f32_16x16x32_bf16 v[36:39], v[208:211], v[154:157], v[36:39]\n\t" \
  "ds_read_u16 v234, v234 offset:10528\n\t" \
  "v_lshlrev_b32_sdwa v222, 2, v222 dst_sel:DWORD dst_unused:UNUSED_PAD src0_sel:DWORD src1_sel:BYTE_1\n\t" \
  "v_lshlrev_b32_sdwa v225, 1, v189 dst_sel:DWORD dst_unused:UNUSED_PAD src0_sel:DWORD src1_sel:WORD_1\n\t" \
  "v_mfma_f32_16x16x32_bf16 v[60:63], v[208:211], v[162:165], v[60:63]\n\t" \
  "ds_read_b32 v221, v221 offset:10016\n\t" \
  "v_lshlrev_b32_sdwa v224, 1, v188 dst_sel:DWORD dst_unused:UNUSED_PAD src0_sel:DWORD src1_sel:WORD_1\n\t" \
  "v_lshlrev_b32_sdwa v227, 1, v191 dst_sel:DWORD dst_unused:UNUSED_PAD src0_sel:DWORD src1_sel:WORD_1\n\t" \
  "v_mfma_f32_16x16x32_bf16 v[84:87], v[208:211], v[170:173], v[84:87]\n\t" \
  "ds_read_b64_tr_b16 v[208:209], v248 offset:43048\n\t" \
  "ds_read_b32 v220, v220 offset:10016\n\t" \
  "v_lshlrev_b32_sdwa v226, 1, v190 dst_sel:DWORD dst_unused:UNUSED_PAD src0_sel:DWORD src1_sel:WORD_1\n\t" \
  "s_waitcnt lgkmcnt(8)\n\t" \
  "v_mfma_f32_16x16x32_bf16 v[16:19], v[212:215], v[146:149], v[16:19]\n\t" \
  "ds_read_b64_tr_b16 v[210:211], v248 offset:44072\n\t" \
  "ds_read_b32 v223, v223 offset:10016\n\t" \
  "v_fma_mix_f32 v229, |v195|, v229, v229 op_sel:[0,1,0] op_sel_hi:[0,1,1] clamp\n\t" \
  "v_mfma_f32_16x16x32_bf16 v[40:43], v[212:215], v[154:157], v[40:43]\n\t" \
  "ds_read_b32 v222, v222 offset:10016\n\t" \
  "v_fma_mix_f32 v228, |v194|, v228, v228 op_sel:[0,1,0] op_sel_hi:[0,1,1] clamp\n\t" \
  "v_fma_mix_f32 v231, |v193|, v231, v231 op_sel:[0,1,0] op_sel_hi:[0,1,1] clamp\n\t" \
  "v_mfma_f32_16x16x32_bf16 v[64:67], v[212:215], v[162:165], v[64:67]\n\t" \
  "ds_read_u16_d16_hi v225, v225 offset:10528\n\t" \
  "v_fma_mix_f32 v230, |v192|, v230, v230 op_sel:[0,1,0] op_sel_hi:[0,1,1] clamp\n\t" \
  "s_waitcnt lgkmcnt(7)\n\t" \
  "v_or_b32 v203, v232, v233\n\t" \
  "v_mfma_f32_16x16x32_bf16 v[88:91], v[212:215], v[170:173], v[88:91]\n\t" \
  "ds_read_b64_tr_b16 v[212:213], v248 offset:46120\n\t" \
  "v_or_b32 v202, v234, v235\n\t" \
  "v_fma_f32 v195, |v195|, v229, v195 mul:4\n\t" \
  "v_mfma_f32_16x16x32_bf16 v[20:23], v[216:219], v[146:149], v[20:23]\n\t" \
  "ds_read_b64_tr_b16 v[214:215], v248 offset:47144\n\t" \
  "v_fma_f32 v194, |v194|, v228, v194 mul:4\n\t" \
  "v_fma_f32 v193, |v193|, v231, v193 mul:4\n\t" \
  "v_mfma_f32_16x16x32_bf16 v[44:47], v[216:219], v[154:157], v[44:47]\n\t" \
  "ds_read_u16 v224, v224 offset:10528\n\t" \
  "v_fma_f32 v192, |v192|, v230, v192 mul:4\n\t" \
  "v_cvt_pk_bf16_f32 v195, v194, v195\n\t" \
  "v_mfma_f32_16x16x32_bf16 v[68:71], v[216:219], v[162:165], v[68:71]\n\t" \
  "ds_read_u16_d16_hi v227, v227 offset:10528\n\t" \
  "v_cvt_pk_bf16_f32 v194, v192, v193\n\t" \
  "v_cvt_pknorm_i16_f32 v228, |v196|, |v197|\n\t" \
  "v_mfma_f32_16x16x32_bf16 v[92:95], v[216:219], v[170:173], v[92:95]\n\t" \
  "ds_read_b64_tr_b16 v[216:217], v248 offset:45096\n\t" \
  "v_cvt_pknorm_i16_f32 v230, |v198|, |v199|\n\t" \
  "s_setprio 2\n\t" \
  "v_lshlrev_b32_sdwa v229, 2, v228 dst_sel:DWORD dst_unused:UNUSED_PAD src0_sel:DWORD src1_sel:BYTE_3\n\t" \
  "s_waitcnt lgkmcnt(8)\n\t" \
  "v_mfma_f32_16x16x32_bf16 v[144:147], v[208:211], v[96:99], 0\n\t" \
  "ds_read_b64_tr_b16 v[218:219], v248 offset:48168\n\t" \
  "v_lshlrev_b32_sdwa v228, 2, v228 dst_sel:DWORD dst_unused:UNUSED_PAD src0_sel:DWORD src1_sel:BYTE_1\n\t" \
  "v_lshlrev_b32_sdwa v231, 2, v230 dst_sel:DWORD dst_unused:UNUSED_PAD src0_sel:DWORD src1_sel:BYTE_3\n\t" \
  "v_mfma_f32_16x16x32_bf16 v[152:155], v[208:211], v[100:103], 0\n\t" \
  "ds_read_u16 v226, v226 offset:10528\n\t" \
  "v_lshlrev_b32_sdwa v230, 2, v230 dst_sel:DWORD dst_unused:UNUSED_PAD src0_sel:DWORD src1_sel:BYTE_1\n\t" \
  "v_mfma_f32_16x16x32_bf16 v[160:163], v[208:211], v[104:107], 0\n\t" \
  "ds_read_b32 v229, v229 offset:10016\n\t" \
  "v_lshlrev_b32_sdwa v233, 1, v205 dst_sel:DWORD dst_unused:UNUSED_PAD src0_sel:DWORD src1_sel:WORD_1\n\t" \
  "v_lshlrev_b32_sdwa v232, 1, v204 dst_sel:DWORD dst_unused:UNUSED_PAD src0_sel:DWORD src1_sel:WORD_1\n\t" \
  "v_mfma_f32_16x16x32_bf16 v[168:171], v[208:211], v[108:111], 0\n\t" \
  "ds_read_b64_tr_b16 v[208:209], v248 offset:43560\n\t" \
  "ds_read_b32 v228, v228 offset:10016\n\t" \
  "v_lshlrev_b32_sdwa v235, 1, v207 dst_sel:DWORD dst_unused:UNUSED_PAD src0_sel:DWORD src1_sel:WORD_1\n\t" \
  "s_waitcnt lgkmcnt(8)\n\t" \
  "v_mfma_f32_16x16x32_bf16 v[144:147], v[212:215], v[112:115], v[144:147]\n\t" \
  "ds_read_b64_tr_b16 v[210:211], v248 offset:44584\n\t" \
  "v_lshlrev_b32_sdwa v234, 1, v206 dst_sel:DWORD dst_unused:UNUSED_PAD src0_sel:DWORD src1_sel:WORD_1\n\t" \
  "v_fma_mix_f32 v221, |v181|, v221, v221 op_sel:[0,1,0] op_sel_hi:[0,1,1] clamp\n\t" \
  "v_mfma_f32_16x16x32_bf16 v[152:155], v[212:215], v[116:119], v[152:155]\n\t" \
  "ds_read_b32 v231, v231 offset:10016\n\t" \
  "ds_read_b32 v230, v230 offset:10016\n\t" \
  "v_fma_mix_f32 v220, |v180|, v220, v220 op_sel:[0,1,0] op_sel_hi:[0,1,1] clamp\n\t" \
  "v_mfma_f32_16x16x32_bf16 v[160:163], v[212:215], v[120:123], v[160:163]\n\t" \
  "ds_read_u16_d16_hi v233, v233 offset:10528\n\t" \
  "v_fma_mix_f32 v223, |v183|, v223, v223 op_sel:[0,1,0] op_sel_hi:[0,1,1] clamp\n\t" \
  "v_fma_mix_f32 v222, |v182|, v222, v222 op_sel:[0,1,0] op_sel_hi:[0,1,1] clamp\n\t" \
  "s_waitcnt lgkmcnt(7)\n\t" \
  "v_mfma_f32_16x16x32_bf16 v[168:171], v[212:215], v[124:127], v[168:171]\n\t" \
  "ds_read_b64_tr_b16 v[212:213], v248 offset:46632\n\t" \
  "v_or_b32 v188, v224, v225\n\t" \
  "v_or_b32 v189, v226, v227\n\t" \
  "v_mfma_f32_16x16x32_bf16 v[144:147], v[216:219], v[128:131], v[144:147]\n\t" \
  "ds_read_b64_tr_b16 v[214:215], v248 offset:47656\n\t" \
  "v_fma_f32 v181, |v181|, v221, v181 mul:4\n\t" \
  "v_fma_f32 v180, |v180|, v220, v180 mul:4\n\t" \
  "v_mfma_f32_16x16x32_bf16 v[152:155], v[216:219], v[132:135], v[152:155]\n\t" \
  "ds_read_u16 v232, v232 offset:10528\n\t" \
  "v_fma_f32 v183, |v183|, v223, v183 mul:4\n\t" \
  "v_fma_f32 v182, |v182|, v222, v182 mul:4\n\t" \
  "v_mfma_f32_16x16x32_bf16 v[160:163], v[216:219], v[136:139], v[160:163]\n\t" \
  "ds_read_u16_d16_hi v235, v235 offset:10528\n\t" \
  "v_cvt_pk_bf16_f32 v180, v180, v181\n\t" \
  "v_cvt_pk_bf16_f32 v181, v182, v183\n\t" \
  "v_mfma_f32_16x16x32_bf16 v[168:171], v[216:219], v[140:143], v[168:171]\n\t" \
  "ds_read_b64_tr_b16 v[216:217], v248 offset:45608\n\t" \
  "v_cvt_pknorm_i16_f32 v220, |v146|, |v147|\n\t" \
  "s_setprio 3\n\t" \
  "v_cvt_pknorm_i16_f32 v222, |v144|, |v145|\n\t" \
  "s_waitcnt lgkmcnt(8)\n\t" \
  "v_mfma_f32_16x16x32_bf16 v[148:151], v[208:211], v[96:99], 0\n\t" \
  "ds_read_b64_tr_b16 v[218:219], v248 offset:48680\n\t" \
  "v_lshlrev_b32_sdwa v221, 2, v220 dst_sel:DWORD dst_unused:UNUSED_PAD src0_sel:DWORD src1_sel:BYTE_3\n\t" \
  "s_waitcnt vmcnt(0)\n\t" \
  "v_lshlrev_b32_sdwa v220, 2, v220 dst_sel:DWORD dst_unused:UNUSED_PAD src0_sel:DWORD src1_sel:BYTE_1\n\t" \
  "v_mfma_f32_16x16x32_bf16 v[156:159], v[208:211], v[100:103], 0\n\t" \
  "s_mov_b64 exec, 3\n\t" \
  "ds_add_rtn_u32 v246, v251, v250 offset:47168\n\t" \
  "s_mov_b64 exec, -1\n\t" \
  "v_lshlrev_b32_sdwa v223, 2, v222 dst_sel:DWORD dst_unused:UNUSED_PAD src0_sel:DWORD src1_sel:BYTE_3\n\t" \
  "v_mfma_f32_16x16x32_bf16 v[164:167], v[208:211], v[104:107], 0\n\t" \
  "ds_read_u16 v234, v234 offset:10528\n\t" \
  "v_lshlrev_b32_sdwa v222, 2, v222 dst_sel:DWORD dst_unused:UNUSED_PAD src0_sel:DWORD src1_sel:BYTE_1\n\t" \
  "v_lshlrev_b32_sdwa v225, 1, v155 dst_sel:DWORD dst_unused:UNUSED_PAD src0_sel:DWORD src1_sel:WORD_1\n\t" \
  "v_mfma_f32_16x16x32_bf16 v[172:175], v[208:211], v[108:111], 0\n\t" \
  "ds_read_b32 v221, v221 offset:10016\n\t" \
  "v_lshlrev_b32_sdwa v224, 1, v154 dst_sel:DWORD dst_unused:UNUSED_PAD src0_sel:DWORD src1_sel:WORD_1\n\t" \
  "s_waitcnt lgkmcnt(2)\n\t" \
  "v_readfirstlane_b32 s4, v246\n\t" \
  "v_mfma_f32_16x16x32_bf16 v[148:151], v[212:215], v[112:115], v[148:151]\n\t" \
  "ds_read_b32 v220, v220 offset:10016\n\t" \
  "v_lshlrev_b32_sdwa v227, 1, v153 dst_sel:DWORD dst_unused:UNUSED_PAD src0_sel:DWORD src1_sel:WORD_1\n\t" \
  "s_cmp_lt_u32 s4, s57\n\t" \
  "s_cbranch_scc1 .Lcnx_f1_5\n.Lcnx_g1_5:\n\t" \
  "v_mfma_f32_16x16x32_bf16 v[156:159], v[212:215], v[116:119], v[156:159]\n\t" \
  "ds_read_b64_tr_b16 v[208:209], v249 offset:12304\n\t" \
  "ds_read_b32 v223, v223 offset:10016\n\t" \
  "v_lshlrev_b32_sdwa v226, 1, v152 dst_sel:DWORD dst_unused:UNUSED_PAD src0_sel:DWORD src1_sel:WORD_1\n\t" \
  "v_mfma_f32_16x16x32_bf16 v[164:167], v[212:215], v[120:123], v[164:167]\n\t" \
  "ds_read_b64_tr_b16 v[210:211], v249 offset:13840\n\t" \
  "v_fma_mix_f32 v229, |v197|, v229, v229 op_sel:[0,1,0] op_sel_hi:[0,1,1] clamp\n\t" \
  "v_fma_mix_f32 v228, |v196|, v228, v228 op_sel:[0,1,0] op_sel_hi:[0,1,1] clamp\n\t" \
  "v_mfma_f32_16x16x32_bf16 v[172:175], v[212:215], v[124:127], v[172:175]\n\t" \
  "ds_read_b64_tr_b16 v[212:213], v249 offset:12312\n\t" \
  "ds_read_b32 v222, v222 offset:10016\n\t" \
  "v_fma_mix_f32 v231, |v199|, v231, v231 op_sel:[0,1,0] op_sel_hi:[0,1,1] clamp\n\t" \
  "v_fma_mix_f32 v230, |v198|, v230, v230 op_sel:[0,1,0] op_sel_hi:[0,1,1] clamp\n\t" \
  "s_waitcnt lgkmcnt(7)\n\t" \
  "v_mfma_f32_16x16x32_bf16 v[148:151], v[216:219], v[128:131], v[148:151]\n\t" \
  "ds_read_b64_tr_b16 v[214:215], v249 offset:13848\n\t" \
  "v_or_b32 v204, v232, v233\n\t" \
  "v_or_b32 v205, v234, v235\n\t" \
  "v_mfma_f32_16x16x32_bf16 v[156:159], v[216:219], v[132:135], v[156:159]\n\t" \
  "ds_read_u16_d16_hi v225, v225 offset:10528\n\t" \
  "v_fma_f32 v197, |v197|, v229, v197 mul:4\n\t" \
  "v_fma_f32 v196, |v196|, v228, v196 mul:4\n\t" \
  "v_mfma_f32_16x16x32_bf16 v[164:167], v[216:219], v[136:139], v[164:167]\n\t" \
  "ds_read_u16 v224, v224 offset:10528\n\t" \
  "v_fma_f32 v199, |v199|, v231, v199 mul:4\n\t" \
  "v_fma_f32 v198, |v198|, v230, v198 mul:4\n\t" \
  "v_mfma_f32_16x16x32_bf16 v[172:175], v[216:219], v[140:143], v[172:175]\n\t" \
  "ds_read_b64_tr_b16 v[216:217], v249 offset:12368\n\t" \
  "v_cvt_pk_bf16_f32 v196, v196, v197\n\t" \
  "v_cvt_pk_bf16_f32 v197, v198, v199\n\t" \
  "v_mfma_f32_16x16x32_bf16 v[24:27], v[242:245], v[236:239], v[24:27]\n\t" \
  "ds_read_b64_tr_b16 v[218:219], v249 offset:13904\n\t" \
  "v_cvt_pknorm_i16_f32 v228, |v162|, |v163|\n\t" \
  "s_setprio 0\n\t" \
  "v_cvt_pknorm_i16_f32 v230, |v160|, |v161|\n\t" \
  "v_mfma_f32_16x16x32_bf16 v[28:31], v[240:243], v[236:239], v[28:31]\n\t" \
  "ds_read_u16_d16_hi v227, v227 offset:10528\n\t" \
  "buffer_load_dwordx4 v[236:239], v250, s[8:11], s62 offen offset:64 nt\n\t" \
  "v_lshlrev_b32_sdwa v229, 2, v228 dst_sel:DWORD dst_unused:UNUSED_PAD src0_sel:DWORD src1_sel:BYTE_3\n\t" \
  "s_waitcnt lgkmcnt(8)\n\t" \
  "v_mfma_f32_16x16x32_bf16 v[0:3], v[208:211], v[178:181], v[0:3]\n\t" \
  "ds_read_u16 v226, v226 offset:10528\n\t" \
  "v_lshlrev_b32_sdwa v228, 2, v228 dst_sel:DWORD dst_unused:UNUSED_PAD src0_sel:DWORD src1_sel:BYTE_1\n\t" \
  "v_lshlrev_b32_sdwa v231, 2, v230 dst_sel:DWORD dst_unused:UNUSED_PAD src0_sel:DWORD src1_sel:BYTE_3\n\t" \
  "v_mfma_f32_16x16x32_bf16 v[24:27], v[208:211], v[186:189], v[24:27]\n\t" \
  "ds_read_b32 v229, v229 offset:10016\n\t" \
  "v_lshlrev_b32_sdwa v230, 2, v230 dst_sel:DWORD dst_unused:UNUSED_PAD src0_sel:DWORD src1_sel:BYTE_1\n\t" \
  "v_lshlrev_b32_sdwa v233, 1, v171 dst_sel:DWORD dst_unused:UNUSED_PAD src0_sel:DWORD src1_sel:WORD_1\n\t" \
  "v_mfma_f32_16x16x32_bf16 v[48:51], v[208:211], v[194:197], v[48:51]\n\t" \
  "ds_read_b32 v228, v228 offset:10016\n\t" \
  "v_lshlrev_b32_sdwa v232, 1, v170 dst_sel:DWORD dst_unused:UNUSED_PAD src0_sel:DWORD src1_sel:WORD_1\n\t" \
  "v_lshlrev_b32_sdwa v235, 1, v169 dst_sel:DWORD dst_unused:UNUSED_PAD src0_sel:DWORD src1_sel:WORD_1\n\t" \
  "v_mfma_f32_16x16x32_bf16 v[72:75], v[208:211], v[202:205], v[72:75]\n\t" \
  "ds_read_b64_tr_b16 v[208:209], v249 offset:12376\n\t" \
  "ds_read_b32 v231, v231 offset:10016\n\t" \
  "v_lshlrev_b32_sdwa v234, 1, v168 dst_sel:DWORD dst_unused:UNUSED_PAD src0_sel:DWORD src1_sel:WORD_1\n\t" \
  "s_waitcnt lgkmcnt(10)\n\t" \
  "v_mfma_f32_16x16x32_bf16 v[4:7], v[212:215], v[178:181], v[4:7]\n\t" \
  "ds_read_b64_tr_b16 v[210:211], v249 offset:13912\n\t" \
  "v_fma_mix_f32 v221, |v147|, v221, v221 op_sel:[0,1,0] op_sel_hi:[0,1,1] clamp\n\t" \
  "v_fma_mix_f32 v220, |v146|, v220, v220 op_sel:[0,1,0] op_sel_hi:[0,1,1] clamp\n\t" \
  "v_mfma_f32_16x16x32_bf16 v[28:31], v[212:215], v[186:189], v[28:31]\n\t" \
  "ds_read_b32 v230, v230 offset:10016\n\t" \
  "v_fma_mix_f32 v223, |v145|, v223, v223 op_sel:[0,1,0] op_sel_hi:[0,1,1] clamp\n\t" \
  "v_fma_mix_f32 v222, |v144|, v222, v222 op_sel:[0,1,0] op_sel_hi:[0,1,1] clamp\n\t" \
  "s_waitcnt lgkmcnt(6)\n\t" \
  "v_mfma_f32_16x16x32_bf16 v[52:55], v[212:215], v[194:197], v[52:55]\n\t" \
  "ds_read_u16_d16_hi v233, v233 offset:10528\n\t" \
  "v_or_b32 v155, v224, v225\n\t" \
  "v_or_b32 v154, v226, v227\n\t" \
  "v_mfma_f32_16x16x32_bf16 v[76:79], v[212:215], v[202:205], v[76:79]\n\t" \
  "ds_read_b64_tr_b16 v[212:213], v249 offset:12432\n\t" \
  "v_fma_f32 v147, |v147|, v221, v147 mul:4\n\t" \
  "v_fma_f32 v146, |v146|, v220, v146 mul:4\n\t" \
  "v_mfma_f32_16x16x32_bf16 v[8:11], v[216:219], v[178:181], v[8:11]\n\t" \
  "ds_read_b64_tr_b16 v[214:215], v249 offset:13968\n\t" \
  "v_fma_f32 v145, |v145|, v223, v145 mul:4\n\t" \
  "v_fma_f32 v144, |v144|, v222, v144 mul:4\n\t" \
  "v_mfma_f32_16x16x32_bf16 v[32:35], v[216:219], v[186:189], v[32:35]\n\t" \
  "ds_read_u16 v232, v232 offset:10528\n\t" \
  "v_cvt_pk_bf16_f32 v147, v146, v147\n\t" \
  "v_cvt_pk_bf16_f32 v146, v144, v145\n\t" \
  "v_mfma_f32_16x16x32_bf16 v[56:59], v[216:219], v[194:197], v[56:59]\n\t" \
  "ds_read_u16_d16_hi v235, v235 offset:10528\n\t" \
  "v_cvt_pknorm_i16_f32 v220, |v148|, |v149|\n\t" \
  "s_setprio 1\n\t" \
  "v_cvt_pknorm_i16_f32 v222, |v150|, |v151|\n\t" \
  "v_mfma_f32_16x16x32_bf16 v[80:83], v[216:219], v[202:205], v[80:83]\n\t" \
  "ds_read_b64_tr_b16 v[216:217], v249 offset:12440\n\t" \
  "v_lshlrev_b32_sdwa v221, 2, v220 dst_sel:DWORD dst_unused:UNUSED_PAD src0_sel:DWORD src1_sel:BYTE_3\n\t" \
  "s_waitcnt lgkmcnt(7)\n\t" \
  "v_mfma_f32_16x16x32_bf16 v[12:15], v[208:211], v[178:181], v[12:15]\n\t" \
  "ds_read_b64_tr_b16 v[218:219], v249 offset:13976\n\t" \
  "v_lshlrev_b32_sdwa v220, 2, v220 dst_sel:DWORD dst_unused:UNUSED_PAD src0_sel:DWORD src1_sel:BYTE_1\n\t" \
  "v_lshlrev_b32_sdwa v223, 2, v222 dst_sel:DWORD dst_unused:UNUSED_PAD src0_sel:DWORD src1_sel:BYTE_3\n\t" \
  "v_mfma_f32_16x16x32_bf16 v[36:39], v[208:211], v[186:189], v[36:39]\n\t" \
  "ds_read_u16 v234, v234 offset:10528\n\t" \
  "v_lshlrev_b32_sdwa v222, 2, v222 dst_sel:DWORD dst_unused:UNUSED_PAD src0_sel:DWORD src1_sel:BYTE_1\n\t" \
  "v_lshlrev_b32_sdwa v225, 1, v157 dst_sel:DWORD dst_unused:UNUSED_PAD src0_sel:DWORD src1_sel:WORD_1\n\t" \
  "v_mfma_f32_16x16x32_bf16 v[60:63], v[208:211], v[194:197], v[60:63]\n\t" \
  "ds_read_b32 v221, v221 offset:10016\n\t" \
  "v_lshlrev_b32_sdwa v224, 1, v156 dst_sel:DWORD dst_unused:UNUSED_PAD src0_sel:DWORD src1_sel:WORD_1\n\t" \
  "v_lshlrev_b32_sdwa v227, 1, v159 dst_sel:DWORD dst_unused:UNUSED_PAD src0_sel:DWORD src1_sel:WORD_1\n\t" \
  "v_mfma_f32_16x16x32_bf16 v[84:87], v[208:211], v[202:205], v[84:87]\n\t" \
  "ds_read_b64_tr_b16 v[208:209], v248 offset:49192\n\t" \
  "ds_read_b32 v220, v220 offset:10016\n\t" \
  "v_lshlrev_b32_sdwa v226, 1, v158 dst_sel:DWORD dst_unused:UNUSED_PAD src0_sel:DWORD src1_sel:WORD_1\n\t" \
  "s_waitcnt lgkmcnt(8)\n\t" \
  "v_mfma_f32_16x16x32_bf16 v[16:19], v[212:215], v[178:181], v[16:19]\n\t" \
  "ds_read_b64_tr_b16 v[210:211], v248 offset:50216\n\t" \
  "ds_read_b32 v223, v223 offset:10016\n\t" \
  "v_fma_mix_f32 v229, |v163|, v229, v229 op_sel:[0,1,0] op_sel_hi:[0,1,1] clamp\n\t" \
  "v_mfma_f32_16x16x32_bf16 v[40:43], v[212:215], v[186:189], v[40:43]\n\t" \
  "ds_read_b32 v222, v222 offset:10016\n\t" \
  "v_fma_mix_f32 v228, |v162|, v228, v228 op_sel:[0,1,0] op_sel_hi:[0,1,1] clamp\n\t" \
  "v_fma_mix_f32 v231, |v161|, v231, v231 op_sel:[0,1,0] op_sel_hi:[0,1,1] clamp\n\t" \
  "v_mfma_f32_16x16x32_bf16 v[64:67], v[212:215], v[194:197], v[64:67]\n\t" \
  "ds_read_u16_d16_hi v225, v225 offset:10528\n\t" \
  "v_fma_mix_f32 v230, |v160|, v230, v230 op_sel:[0,1,0] op_sel_hi:[0,1,1] clamp\n\t" \
  "s_waitcnt lgkmcnt(7)\n\t" \
  "v_or_b32 v171, v232, v233\n\t" \
  "v_mfma_f32_16x16x32_bf16 v[88:91], v[212:215], v[202:205], v[88:91]\n\t" \
  "ds_read_b64_tr_b16 v[212:213], v248 offset:52264\n\t" \
  "v_or_b32 v170, v234, v235\n\t" \
  "v_fma_f32 v163, |v163|, v229, v163 mul:4\n\t" \
  "v_mfma_f32_16x16x32_bf16 v[20:23], v[216:219], v[178:181], v[20:23]\n\t" \
  "ds_read_b64_tr_b16 v[214:215], v248 offset:53288\n\t" \
  "v_fma_f32 v162, |v162|, v228, v162 mul:4\n\t" \
  "v_fma_f32 v161, |v161|, v231, v161 mul:4\n\t" \
  "v_mfma_f32_16x16x32_bf16 v[44:47], v[216:219], v[186:189], v[44:47]\n\t" \
  "ds_read_u16 v224, v224 offset:10528\n\t" \
  "v_fma_f32 v160, |v160|, v230, v160 mul:4\n\t" \
  "v_cvt_pk_bf16_f32 v163, v162, v163\n\t" \
  "v_mfma_f32_16x16x32_bf16 v[68:71], v[216:219], v[194:197], v[68:71]\n\t" \
  "ds_read_u16_d16_hi v227, v227 offset:10528\n\t" \
  "v_cvt_pk_bf16_f32 v162, v160, v161\n\t" \
  "v_cvt_pknorm_i16_f32 v228, |v164|, |v165|\n\t" \
  "v_mfma_f32_16x16x32_bf16 v[92:95], v[216:219], v[202:205], v[92:95]\n\t" \
  "ds_read_b64_tr_b16 v[216:217], v248 offset:51240\n\t" \
  "v_cvt_pknorm_i16_f32 v230, |v166|, |v167|\n\t" \
  "s_setprio 2\n\t" \
  "v_lshlrev_b32_sdwa v229, 2, v228 dst_sel:DWORD dst_unused:UNUSED_PAD src0_sel:DWORD src1_sel:BYTE_3\n\t" \
  "s_waitcnt lgkmcnt(8)\n\t" \
  "v_mfma_f32_16x16x32_bf16 v[176:179], v[208:211], v[96:99], 0\n\t" \
  "ds_read_b64_tr_b16 v[218:219], v248 offset:54312\n\t" \
  "v_lshlrev_b32_sdwa v228, 2, v228 dst_sel:DWORD dst_unused:UNUSED_PAD src0_sel:DWORD src1_sel:BYTE_1\n\t" \
  "v_lshlrev_b32_sdwa v231, 2, v230 dst_sel:DWORD dst_unused:UNUSED_PAD src0_sel:DWORD src1_sel:BYTE_3\n\t" \
  "v_mfma_f32_16x16x32_bf16 v[184:187], v[208:211], v[100:103], 0\n\t" \
  "ds_read_u16 v226, v226 offset:10528\n\t" \
  "v_lshlrev_b32_sdwa v230, 2, v230 dst_sel:DWORD dst_unused:UNUSED_PAD src0_sel:DWORD src1_sel:BYTE_1\n\t" \
  "v_mfma_f32_16x16x32_bf16 v[192:195], v[208:211], v[104:107], 0\n\t" \
  "ds_read_b32 v229, v229 offset:10016\n\t" \
  "v_lshlrev_b32_sdwa v233, 1, v173 dst_sel:DWORD dst_unused:UNUSED_PAD src0_sel:DWORD src1_sel:WORD_1\n\t" \
  "v_lshlrev_b32_sdwa v232, 1, v172 dst_sel:DWORD dst_unused:UNUSED_PAD src0_sel:DWORD src1_sel:WORD_1\n\t" \
  "v_mfma_f32_16x16x32_bf16 v[200:203], v[208:211], v[108:111], 0\n\t" \
  "ds_read_b64_tr_b16 v[208:209], v248 offset:49704\n\t" \
  "ds_read_b32 v228, v228 offset:10016\n\t" \
  "v_lshlrev_b32_sdwa v235, 1, v175 dst_sel:DWORD dst_unused:UNUSED_PAD src0_sel:DWORD src1_sel:WORD_1\n\t" \
  "s_waitcnt lgkmcnt(8)\n\t" \
  "v_mfma_f32_16x16x32_bf16 v[176:179], v[212:215], v[112:115], v[176:179]\n\t" \
  "ds_read_b64_tr_b16 v[210:211], v248 offset:50728\n\t" \
  "v_lshlrev_b32_sdwa v234, 1, v174 dst_sel:DWORD dst_unused:UNUSED_PAD src0_sel:DWORD src1_sel:WORD_1\n\t" \
  "v_fma_mix_f32 v221, |v149|, v221, v221 op_sel:[0,1,0] op_sel_hi:[0,1,1] clamp\n\t" \
  "v_mfma_f32_16x16x32_bf16 v[184:187], v[212:215], v[116:119], v[184:187]\n\t" \
  "ds_read_b32 v231, v231 offset:10016\n\t" \
  "ds_read_b32 v230, v230 offset:10016\n\t" \
  "v_fma_mix_f32 v220, |v148|, v220, v220 op_sel:[0,1,0] op_sel_hi:[0,1,1] clamp\n\t" \
  "v_mfma_f32_16x16x32_bf16 v[192:195], v[212:215], v[120:123], v[192:195]\n\t" \
  "ds_read_u16_d16_hi v233, v233 offset:10528\n\t" \
  "v_fma_mix_f32 v223, |v151|, v223, v223 op_sel:[0,1,0] op_sel_hi:[0,1,1] clamp\n\t" \
  "v_fma_mix_f32 v222, |v150|, v222, v222 op_sel:[0,1,0] op_sel_hi:[0,1,1] clamp\n\t" \
  "s_waitcnt lgkmcnt(7)\n\t" \
  "v_mfma_f32_16x16x32_bf16 v[200:203], v[212:215], v[124:127], v[200:203]\n\t" \
  "ds_read_b64_tr_b16 v[212:213], v248 offset:52776\n\t" \
  "v_or_b32 v156, v224, v225\n\t" \
  "v_or_b32 v157, v226, v227\n\t" \
  "v_mfma_f32_16x16x32_bf16 v[176:179], v[216:219], v[128:131], v[176:179]\n\t" \
  "ds_read_b64_tr_b16 v[214:215], v248 offset:53800\n\t" \
  "v_fma_f32 v149, |v149|, v221, v149 mul:4\n\t" \
  "v_fma_f32 v148, |v148|, v220, v148 mul:4\n\t" \
  "v_mfma_f32_16x16x32_bf16 v[184:187], v[216:219], v[132:135], v[184:187]\n\t" \
  "ds_read_u16 v232, v232 offset:10528\n\t" \
  "v_fma_f32 v151, |v151|, v223, v151 mul:4\n\t" \
  "v_fma_f32 v150, |v150|, v222, v150 mul:4\n\t" \
  "v_mfma_f32_16x16x32_bf16 v[192:195], v[216:219], v[136:139], v[192:195]\n\t" \
  "ds_read_u16_d16_hi v235, v235 offset:10528\n\t" \
  "v_cvt_pk_bf16_f32 v148, v148, v149\n\t" \
  "v_cvt_pk_bf16_f32 v149, v150, v151\n\t" \
  "v_mfma_f32_16x16x32_bf16 v[200:203], v[216:219], v[140:143], v[200:203]\n\t" \
  "ds_read_b64_tr_b16 v[216:217], v248 offset:51752\n\t" \
  "v_cvt_pknorm_i16_f32 v220, |v178|, |v179|\n\t" \
  "s_setprio 3\n\t" \
  "v_cvt_pknorm_i16_f32 v222, |v176|, |v177|\n\t" \
  "s_waitcnt lgkmcnt(8)\n\t" \
  "v_mfma_f32_16x16x32_bf16 v[180:183], v[208:211], v[96:99], 0\n\t" \
  "ds_read_b64_tr_b16 v[218:219], v248 offset:54824\n\t" \
  "v_lshlrev_b32_sdwa v221, 2, v220 dst_sel:DWORD dst_unused:UNUSED_PAD src0_sel:DWORD src1_sel:BYTE_3\n\t" \
  "s_waitcnt vmcnt(0)\n\t" \
  "v_lshlrev_b32_sdwa v220, 2, v220 dst_sel:DWORD dst_unused:UNUSED_PAD src0_sel:DWORD src1_sel:BYTE_1\n\t" \
  "v_mfma_f32_16x16x32_bf16 v[188:191], v[208:211], v[100:103], 0\n\t" \
  "s_mov_b64 exec, 3\n\t" \
  "ds_add_rtn_u32 v246, v251, v250 offset:47176\n\t" \
  "s_mov_b64 exec, -1\n\t" \
  "v_lshlrev_b32_sdwa v223, 2, v222 dst_sel:DWORD dst_unused:UNUSED_PAD src0_sel:DWORD src1_sel:BYTE_3\n\t" \
  "v_mfma_f32_16x16x32_bf16 v[196:199], v[208:211], v[104:107], 0\n\t" \
  "ds_read_u16 v234, v234 offset:10528\n\t" \
  "v_lshlrev_b32_sdwa v222, 2, v222 dst_sel:DWORD dst_unused:UNUSED_PAD src0_sel:DWORD src1_sel:BYTE_1\n\t" \
  "v_lshlrev_b32_sdwa v225, 1, v187 dst_sel:DWORD dst_unused:UNUSED_PAD src0_sel:DWORD src1_sel:WORD_1\n\t" \
  "v_mfma_f32_16x16x32_bf16 v[204:207], v[208:211], v[108:111], 0\n\t" \
  "ds_read_b32 v221, v221 offset:10016\n\t" \
  "v_lshlrev_b32_sdwa v224, 1, v186 dst_sel:DWORD dst_unused:UNUSED_PAD src0_sel:DWORD src1_sel:WORD_1\n\t" \
  "s_waitcnt lgkmcnt(2)\n\t" \
  "v_readfirstlane_b32 s4, v246\n\t" \
  "v_mfma_f32_16x16x32_bf16 v[180:183], v[212:215], v[112:115], v[180:183]\n\t" \
  "ds_read_b32 v220, v220 offset:10016\n\t" \
  "v_lshlrev_b32_sdwa v227, 1, v185 dst_sel:DWORD dst_unused:UNUSED_PAD src0_sel:DWORD src1_sel:WORD_1\n\t" \
  "s_cmp_lt_u32 s4, s57\n\t" \
  "s_cbranch_scc1 .Lcnx_f1_6\n.Lcnx_g1_6:\n\t" \
  "v_mfma_f32_16x16x32_bf16 v[188:191], v[212:215], v[116:119], v[188:191]\n\t" \
  "ds_read_b64_tr_b16 v[208:209], v249 offset:24600\n\t" \
  "ds_read_b32 v223, v223 offset:10016\n\t" \
  "v_lshlrev_b32_sdwa v226, 1, v184 dst_sel:DWORD dst_unused:UNUSED_PAD src0_sel:DWORD src1_sel:WORD_1\n\t" \
  "v_mfma_f32_16x16x32_bf16 v[196:199], v[212:215], v[120:123], v[196:199]\n\t" \
  "ds_read_b64_tr_b16 v[210:211], v249 offset:26136\n\t" \
  "v_fma_mix_f32 v229, |v165|, v229, v229 op_sel:[0,1,0] op_sel_hi:[0,1,1] clamp\n\t" \
  "v_fma_mix_f32 v228, |v164|, v228, v228 op_sel:[0,1,0] op_sel_hi:[0,1,1] clamp\n\t" \
  "v_mfma_f32_16x16x32_bf16 v[204:207], v[212:215], v[124:127], v[204:207]\n\t" \
  "ds_read_b64_tr_b16 v[212:213], v249 offset:24608\n\t" \
  "ds_read_b32 v222, v222 offset:10016\n\t" \
  "v_fma_mix_f32 v231, |v167|, v231, v231 op_sel:[0,1,0] op_sel_hi:[0,1,1] clamp\n\t" \
  "v_fma_mix_f32 v230, |v166|, v230, v230 op_sel:[0,1,0] op_sel_hi:[0,1,1] clamp\n\t" \
  "s_waitcnt lgkmcnt(7)\n\t" \
  "v_mfma_f32_16x16x32_bf16 v[180:183], v[216:219], v[128:131], v[180:183]\n\t" \
  "ds_read_b64_tr_b16 v[214:215], v249 offset:26144\n\t" \
  "v_or_b32 v172, v232, v233\n\t" \
  "v_or_b32 v173, v234, v235\n\t" \
  "v_mfma_f32_16x16x32_bf16 v[188:191], v[216:219], v[132:135], v[188:191]\n\t" \
  "ds_read_u16_d16_hi v225, v225 offset:10528\n\t" \
  "v_fma_f32 v165, |v165|, v229, v165 mul:4\n\t" \
  "v_fma_f32 v164, |v164|, v228, v164 mul:4\n\t" \
  "v_mfma_f32_16x16x32_bf16 v[196:199], v[216:219], v[136:139], v[196:199]\n\t" \
  "ds_read_u16 v224, v224 offset:10528\n\t" \
  "v_fma_f32 v167, |v167|, v231, v167 mul:4\n\t" \
  "v_fma_f32 v166, |v166|, v230, v166 mul:4\n\t" \
  "v_mfma_f32_16x16x32_bf16 v[204:207], v[216:219], v[140:143], v[204:207]\n\t" \
  "ds_read_b64_tr_b16 v[216:217], v249 offset:24664\n\t" \
  "v_cvt_pk_bf16_f32 v164, v164, v165\n\t" \
  "v_cvt_pk_bf16_f32 v165, v166, v167\n\t" \
  "v_mfma_f32_16x16x32_bf16 v[32:35], v[242:245], v[236:239], v[32:35]\n\t" \
  "ds_read_b64_tr_b16 v[218:219], v249 offset:26200\n\t" \
  "v_cvt_pknorm_i16_f32 v228, |v194|, |v195|\n\t" \
  "s_setprio 0\n\t" \
  "v_cvt_pknorm_i16_f32 v230, |v192|, |v193|\n\t" \
  "v_mfma_f32_16x16x32_bf16 v[36:39], v[240:243], v[236:239], v[36:39]\n\t" \
  "ds_read_u16_d16_hi v227, v227 offset:10528\n\t" \
  "buffer_load_dwordx4 v[236:239], v250, s[8:11], s62 offen offset:128 nt\n\t" \
  "v_lshlrev_b32_sdwa v229, 2, v228 dst_sel:DWORD dst_unused:UNUSED_PAD src0_sel:DWORD src1_sel:BYTE_3\n\t" \
  "s_waitcnt lgkmcnt(8)\n\t" \
  "v_mfma_f32_16x16x32_bf16 v[0:3], v[208:211], v[146:149], v[0:3]\n\t" \
  "ds_read_u16 v226, v226 offset:10528\n\t" \
  "v_lshlrev_b32_sdwa v228, 2, v228 dst_sel:DWORD dst_unused:UNUSED_PAD src0_sel:DWORD src1_sel:BYTE_1\n\t" \
  "v_lshlrev_b32_sdwa v231, 2, v230 dst_sel:DWORD dst_unused:UNUSED_PAD src0_sel:DWORD src1_sel:BYTE_3\n\t" \
  "v_mfma_f32_16x16x32_bf16 v[24:27], v[208:211], v[154:157], v[24:27]\n\t" \
  "ds_read_b32 v229, v229 offset:10016\n\t" \
  "v_lshlrev_b32_sdwa v230, 2, v230 dst_sel:DWORD dst_unused:UNUSED_PAD src0_sel:DWORD src1_sel:BYTE_1\n\t" \
  "v_lshlrev_b32_sdwa v233, 1, v203 dst_sel:DWORD dst_unused:UNUSED_PAD src0_sel:DWORD src1_sel:WORD_1\n\t" \
  "v_mfma_f32_16x16x32_bf16 v[48:51], v[208:211], v[162:165], v[48:51]\n\t" \
  "ds_read_b32 v228, v228 offset:10016\n\t" \
  "v_lshlrev_b32_sdwa v232, 1, v202 dst_sel:DWORD dst_unused:UNUSED_PAD src0_sel:DWORD src1_sel:WORD_1\n\t" \
  "v_lshlrev_b32_sdwa v235, 1, v201 dst_sel:DWORD dst_unused:UNUSED_PAD src0_sel:DWORD src1_sel:WORD_1\n\t" \
  "v_mfma_f32_16x16x32_bf16 v[72:75], v[208:211], v[170:173], v[72:75]\n\t" \
  "ds_read_b64_tr_b16 v[208:209], v249 offset:24672\n\t" \
  "ds_read_b32 v231, v231 offset:10016\n\t" \
  "v_lshlrev_b32_sdwa v234, 1, v200 dst_sel:DWORD dst_unused:UNUSED_PAD src0_sel:DWORD src1_sel:WORD_1\n\t" \
  "s_waitcnt lgkmcnt(10)\n\t" \
  "v_mfma_f32_16x16x32_bf16 v[4:7], v[212:215], v[146:149], v[4:7]\n\t" \
  "ds_read_b64_tr_b16 v[210:211], v249 offset:26208\n\t" \
  "v_fma_mix_f32 v221, |v179|, v221, v221 op_sel:[0,1,0] op_sel_hi:[0,1,1] clamp\n\t" \
  "v_fma_mix_f32 v220, |v178|, v220, v220 op_sel:[0,1,0] op_sel_hi:[0,1,1] clamp\n\t" \
  "v_mfma_f32_16x16x32_bf16 v[28:31], v[212:215], v[154:157], v[28:31]\n\t" \
  "ds_read_b32 v230, v230 offset:10016\n\t" \
  "v_fma_mix_f32 v223, |v177|, v223, v223 op_sel:[0,1,0] op_sel_hi:[0,1,1] clamp\n\t" \
  "v_fma_mix_f32 v222, |v176|, v222, v222 op_sel:[0,1,0] op_sel_hi:[0,1,1] clamp\n\t" \
  "s_waitcnt lgkmcnt(6)\n\t" \
  "v_mfma_f32_16x16x32_bf16 v[52:55], v[212:215], v[162:165], v[52:55]\n\t" \
  "ds_read_u16_d16_hi v233, v233 offset:10528\n\t" \
  "v_or_b32 v187, v224, v225\n\t" \
  "v_or_b32 v186, v226, v227\n\t" \
  "v_mfma_f32_16x16x32_bf16 v[76:79], v[212:215], v[170:173], v[76:79]\n\t" \
  "ds_read_b64_tr_b16 v[212:213], v249 offset:24728\n\t" \
  "v_fma_f32 v179, |v179|, v221, v179 mul:4\n\t" \
  "v_fma_f32 v178, |v178|, v220, v178 mul:4\n\t" \
  "v_mfma_f32_16x16x32_bf16 v[8:11], v[216:219], v[146:149], v[8:11]\n\t" \
  "ds_read_b64_tr_b16 v[214:215], v249 offset:26264\n\t" \
  "v_fma_f32 v177, |v177|, v223, v177 mul:4\n\t" \
  "v_fma_f32 v176, |v176|, v222, v176 mul:4\n\t" \
  "v_mfma_f32_16x16x32_bf16 v[32:35], v[216:219], v[154:157], v[32:35]\n\t" \
  "ds_read_u16 v232, v232 offset:10528\n\t" \
  "v_cvt_pk_bf16_f32 v179, v178, v179\n\t" \
  "v_cvt_pk_bf16_f32 v178, v176, v177\n\t" \
  "v_mfma_f32_16x16x32_bf16 v[56:59], v[216:219], v[162:165], v[56:59]\n\t" \
  "ds_read_u16_d16_hi v235, v235 offset:10528\n\t" \
  "v_cvt_pknorm_i16_f32 v220, |v180|, |v181|\n\t" \
  "s_setprio 1\n\t" \
  "v_cvt_pknorm_i16_f32 v222, |v182|, |v183|\n\t" \
  "v_mfma_f32_16x16x32_bf16 v[80:83], v[216:219], v[170:173], v[80:83]\n\t" \
  "ds_read_b64_tr_b16 v[216:217], v249 offset:24736\n\t" \
  "v_lshlrev_b32_sdwa v221, 2, v220 dst_sel:DWORD dst_unused:UNUSED_PAD src0_sel:DWORD src1_sel:BYTE_3\n\t" \
  "s_waitcnt lgkmcnt(7)\n\t" \
  "v_mfma_f32_16x16x32_bf16 v[12:15], v[208:211], v[146:149], v[12:15]\n\t" \
  "ds_read_b64_tr_b16 v[218:219], v249 offset:26272\n\t" \
  "v_lshlrev_b32_sdwa v220, 2, v220 dst_sel:DWORD dst_unused:UNUSED_PAD src0_sel:DWORD src1_sel:BYTE_1\n\t" \
  "v_lshlrev_b32_sdwa v223, 2, v222 dst_sel:DWORD dst_unused:UNUSED_PAD src0_sel:DWORD src1_sel:BYTE_3\n\t" \
  "v_mfma_f32_16x16x32_bf16 v[36:39], v[208:211], v[154:157], v[36:39]\n\t" \
  "ds_read_u16 v234, v234 offset:10528\n\t" \
  "v_lshlrev_b32_sdwa v222, 2, v222 dst_sel:DWORD dst_unused:UNUSED_PAD src0_sel:DWORD src1_sel:BYTE_1\n\t" \
  "v_lshlrev_b32_sdwa v225, 1, v189 dst_sel:DWORD dst_unused:UNUSED_PAD src0_sel:DWORD src1_sel:WORD_1\n\t" \
  "v_mfma_f32_16x16x32_bf16 v[60:63], v[208:211], v[162:165], v[60:63]\n\t" \
  "ds_read_b32 v221, v221 offset:10016\n\t" \
  "v_lshlrev_b32_sdwa v224, 1, v188 dst_sel:DWORD dst_unused:UNUSED_PAD src0_sel:DWORD src1_sel:WORD_1\n\t" \
  "v_lshlrev_b32_sdwa v227, 1, v191 dst_sel:DWORD dst_unused:UNUSED_PAD src0_sel:DWORD src1_sel:WORD_1\n\t" \
  "v_mfma_f32_16x16x32_bf16 v[84:87], v[208:211], v[170:173], v[84:87]\n\t" \
  "ds_read_b64_tr_b16 v[208:209], v248 offset:36904\n\t" \
  "ds_read_b32 v220, v220 offset:10016\n\t" \
  "v_lshlrev_b32_sdwa v226, 1, v190 dst_sel:DWORD dst_unused:UNUSED_PAD src0_sel:DWORD src1_sel:WORD_1\n\t" \
  "s_waitcnt lgkmcnt(8)\n\t" \
  "v_mfma_f32_16x16x32_bf16 v[16:19], v[212:215], v[146:149], v[16:19]\n\t" \
  "ds_read_b64_tr_b16 v[210:211], v248 offset:37928\n\t" \
  "ds_read_b32 v223, v223 offset:10016\n\t" \
  "v_fma_mix_f32 v229, |v195|, v229, v229 op_sel:[0,1,0] op_sel_hi:[0,1,1] clamp\n\t" \
  "v_mfma_f32_16x16x32_bf16 v[40:43], v[212:215], v[154:157], v[40:43]\n\t" \
  "ds_read_b32 v222, v222 offset:10016\n\t" \
  "v_fma_mix_f32 v228, |v194|, v228, v228 op_sel:[0,1,0] op_sel_hi:[0,1,1] clamp\n\t" \
  "v_fma_mix_f32 v231, |v193|, v231, v231 op_sel:[0,1,0] op_sel_hi:[0,1,1] clamp\n\t" \
  "v_mfma_f32_16x16x32_bf16 v[64:67], v[212:215], v[162:165], v[64:67]\n\t" \
  "ds_read_u16_d16_hi v225, v225 offset:10528\n\t" \
  "v_fma_mix_f32 v230, |v192|, v230, v230 op_sel:[0,1,0] op_sel_hi:[0,1,1] clamp\n\t" \
  "s_waitcnt lgkmcnt(7)\n\t" \
  "v_or_b32 v203, v232, v233\n\t" \
  "v_mfma_f32_16x16x32_bf16 v[88:91], v[212:215], v[170:173], v[88:91]\n\t" \
  "ds_read_b64_tr_b16 v[212:213], v248 offset:39976\n\t" \
  "v_or_b32 v202, v234, v235\n\t" \
  "v_fma_f32 v195, |v195|, v229, v195 mul:4\n\t" \
  "v_mfma_f32_16x16x32_bf16 v[20:23], v[216:219], v[146:149], v[20:23]\n\t" \
  "ds_read_b64_tr_b16 v[214:215], v248 offset:41000\n\t" \
  "v_fma_f32 v194, |v194|, v228, v194 mul:4\n\t" \
  "v_fma_f32 v193, |v193|, v231, v193 mul:4\n\t" \
  "v_mfma_f32_16x16x32_bf16 v[44:47], v[216:219], v[154:157], v[44:47]\n\t" \
  "ds_read_u16 v224, v224 offset:10528\n\t" \
  "v_fma_f32 v192, |v192|, v230, v192 mul:4\n\t" \
  "v_cvt_pk_bf16_f32 v195, v194, v195\n\t" \
  "v_mfma_f32_16x16x32_bf16 v[68:71], v[216:219], v[162:165], v[68:71]\n\t" \
  "ds_read_u16_d16_hi v227, v227 offset:10528\n\t" \
  "v_cvt_pk_bf16_f32 v194, v192, v193\n\t" \
  "v_cvt_pknorm_i16_f32 v228, |v196|, |v197|\n\t" \
  "v_mfma_f32_16x16x32_bf16 v[92:95], v[216:219], v[170:173], v[92:95]\n\t" \
  "ds_read_b64_tr_b16 v[216:217], v248 offset:38952\n\t" \
  "v_cvt_pknorm_i16_f32 v230, |v198|, |v199|\n\t" \
  "s_setprio 2\n\t" \
  "v_lshlrev_b32_sdwa v229, 2, v228 dst_sel:DWORD dst_unused:UNUSED_PAD src0_sel:DWORD src1_sel:BYTE_3\n\t" \
  "s_waitcnt lgkmcnt(8)\n\t" \
  "v_mfma_f32_16x16x32_bf16 v[144:147], v[208:211], v[96:99], 0\n\t" \
  "ds_read_b64_tr_b16 v[218:219], v248 offset:42024\n\t" \
  "v_lshlrev_b32_sdwa v228, 2, v228 dst_sel:DWORD dst_unused:UNUSED_PAD src0_sel:DWORD src1_sel:BYTE_1\n\t" \
  "v_lshlrev_b32_sdwa v231, 2, v230 dst_sel:DWORD dst_unused:UNUSED_PAD src0_sel:DWORD src1_sel:BYTE_3\n\t" \
  "v_mfma_f32_16x16x32_bf16 v[152:155], v[208:211], v[100:103], 0\n\t" \
  "ds_read_u16 v226, v226 offset:10528\n\t" \
  "v_lshlrev_b32_sdwa v230, 2, v230 dst_sel:DWORD dst_unused:UNUSED_PAD src0_sel:DWORD src1_sel:BYTE_1\n\t" \
  "v_mfma_f32_16x16x32_bf16 v[160:163], v[208:211], v[104:107], 0\n\t" \
  "ds_read_b32 v229, v229 offset:10016\n\t" \
  "v_lshlrev_b32_sdwa v233, 1, v205 dst_sel:DWORD dst_unused:UNUSED_PAD src0_sel:DWORD src1_sel:WORD_1\n\t" \
  "v_lshlrev_b32_sdwa v232, 1, v204 dst_sel:DWORD dst_unused:UNUSED_PAD src0_sel:DWORD src1_sel:WORD_1\n\t" \
  "v_mfma_f32_16x16x32_bf16 v[168:171], v[208:211], v[108:111], 0\n\t" \
  "ds_read_b64_tr_b16 v[208:209], v248 offset:37416\n\t" \
  "ds_read_b32 v228, v228 offset:10016\n\t" \
  "v_lshlrev_b32_sdwa v235, 1, v207 dst_sel:DWORD dst_unused:UNUSED_PAD src0_sel:DWORD src1_sel:WORD_1\n\t" \
  "s_waitcnt lgkmcnt(8)\n\t" \
  "v_mfma_f32_16x16x32_bf16 v[144:147], v[212:215], v[112:115], v[144:147]\n\t" \
  "ds_read_b64_tr_b16 v[210:211], v248 offset:38440\n\t" \
  "v_lshlrev_b32_sdwa v234, 1, v206 dst_sel:DWORD dst_unused:UNUSED_PAD src0_sel:DWORD src1_sel:WORD_1\n\t" \
  "v_fma_mix_f32 v221, |v181|, v221, v221 op_sel:[0,1,0] op_sel_hi:[0,1,1] clamp\n\t" \
  "v_mfma_f32_16x16x32_bf16 v[152:155], v[212:215], v[116:119], v[152:155]\n\t" \
  "ds_read_b32 v231, v231 offset:10016\n\t" \
  "ds_read_b32 v230, v230 offset:10016\n\t" \
  "v_fma_mix_f32 v220, |v180|, v220, v220 op_sel:[0,1,0] op_sel_hi:[0,1,1] clamp\n\t" \
  "v_mfma_f32_16x16x32_bf16 v[160:163], v[212:215], v[120:123], v[160:163]\n\t" \
  "ds_read_u16_d16_hi v233, v233 offset:10528\n\t" \
  "v_fma_mix_f32 v223, |v183|, v223, v223 op_sel:[0,1,0] op_sel_hi:[0,1,1] clamp\n\t" \
  "v_fma_mix_f32 v222, |v182|, v222, v222 op_sel:[0,1,0] op_sel_hi:[0,1,1] clamp\n\t" \
  "s_waitcnt lgkmcnt(7)\n\t" \
  "v_mfma_f32_16x16x32_bf16 v[168:171], v[212:215], v[124:127], v[168:171]\n\t" \
  "ds_read_b64_tr_b16 v[212:213], v248 offset:40488\n\t" \
  "v_or_b32 v188, v224, v225\n\t" \
  "v_or_b32 v189, v226, v227\n\t" \
  "v_mfma_f32_16x16x32_bf16 v[144:147], v[216:219], v[128:131], v[144:147]\n\t" \
  "ds_read_b64_tr_b16 v[214:215], v248 offset:41512\n\t" \
  "v_fma_f32 v181, |v181|, v221, v181 mul:4\n\t" \
  "v_fma_f32 v180, |v180|, v220, v180 mul:4\n\t" \
  "v_mfma_f32_16x16x32_bf16 v[152:155], v[216:219], v[132:135], v[152:155]\n\t" \
  "ds_read_u16 v232, v232 offset:10528\n\t" \
  "v_fma_f32 v183, |v183|, v223, v183 mul:4\n\t" \
  "v_fma_f32 v182, |v182|, v222, v182 mul:4\n\t" \
  "v_mfma_f32_16x16x32_bf16 v[160:163], v[216:219], v[136:139], v[160:163]\n\t" \
  "ds_read_u16_d16_hi v235, v235 offset:10528\n\t" \
  "v_cvt_pk_bf16_f32 v180, v180, v181\n\t" \
  "v_cvt_pk_bf16_f32 v181, v182, v183\n\t" \
  "v_mfma_f32_16x16x32_bf16 v[168:171], v[216:219], v[140:143], v[168:171]\n\t" \
  "ds_read_b64_tr_b16 v[216:217], v248 offset:39464\n\t" \
  "v_cvt_pknorm_i16_f32 v220, |v146|, |v147|\n\t" \
  "s_setprio 3\n\t" \
  "v_cvt_pknorm_i16_f32 v222, |v144|, |v145|\n\t" \
  "s_waitcnt lgkmcnt(8)\n\t" \
  "v_mfma_f32_16x16x32_bf16 v[148:151], v[208:211], v[96:99], 0\n\t" \
  "ds_read_b64_tr_b16 v[218:219], v248 offset:42536\n\t" \
  "v_lshlrev_b32_sdwa v221, 2, v220 dst_sel:DWORD dst_unused:UNUSED_PAD src0_sel:DWORD src1_sel:BYTE_3\n\t" \
  "s_waitcnt vmcnt(0)\n\t" \
  "v_lshlrev_b32_sdwa v220, 2, v220 dst_sel:DWORD dst_unused:UNUSED_PAD src0_sel:DWORD src1_sel:BYTE_1\n\t" \
  "v_mfma_f32_16x16x32_bf16 v[156:159], v[208:211], v[100:103], 0\n\t" \
  "s_mov_b64 exec, 3\n\t" \
  "ds_add_rtn_u32 v246, v251, v250 offset:47184\n\t" \
  "s_mov_b64 exec, -1\n\t" \
  "v_lshlrev_b32_sdwa v223, 2, v222 dst_sel:DWORD dst_unused:UNUSED_PAD src0_sel:DWORD src1_sel:BYTE_3\n\t" \
  "v_mfma_f32_16x16x32_bf16 v[164:167], v[208:211], v[104:107], 0\n\t" \
  "ds_read_u16 v234, v234 offset:10528\n\t" \
  "v_lshlrev_b32_sdwa v222, 2, v222 dst_sel:DWORD dst_unused:UNUSED_PAD src0_sel:DWORD src1_sel:BYTE_1\n\t" \
  "v_lshlrev_b32_sdwa v225, 1, v155 dst_sel:DWORD dst_unused:UNUSED_PAD src0_sel:DWORD src1_sel:WORD_1\n\t" \
  "v_mfma_f32_16x16x32_bf16 v[172:175], v[208:211], v[108:111], 0\n\t" \
  "ds_read_b32 v221, v221 offset:10016\n\t" \
  "v_lshlrev_b32_sdwa v224, 1, v154 dst_sel:DWORD dst_unused:UNUSED_PAD src0_sel:DWORD src1_sel:WORD_1\n\t" \
  "s_waitcnt lgkmcnt(2)\n\t" \
  "v_readfirstlane_b32 s4, v246\n\t" \
  "v_mfma_f32_16x16x32_bf16 v[148:151], v[212:215], v[112:115], v[148:151]\n\t" \
  "ds_read_b32 v220, v220 offset:10016\n\t" \
  "v_lshlrev_b32_sdwa v227, 1, v153 dst_sel:DWORD dst_unused:UNUSED_PAD src0_sel:DWORD src1_sel:WORD_1\n\t" \
  "s_cmp_lt_u32 s4, s57\n\t" \
  "s_cbranch_scc1 .Lcnx_f1_7\n.Lcnx_g1_7:\n\t" \
  "v_mfma_f32_16x16x32_bf16 v[156:159], v[212:215], v[116:119], v[156:159]\n\t" \
  "ds_read_b64_tr_b16 v[208:209], v249 offset:30752\n\t" \
  "ds_read_b32 v223, v223 offset:10016\n\t" \
  "v_lshlrev_b32_sdwa v226, 1, v152 dst_sel:DWORD dst_unused:UNUSED_PAD src0_sel:DWORD src1_sel:WORD_1\n\t" \
  "v_mfma_f32_16x16x32_bf16 v[164:167], v[212:215], v[120:123], v[164:167]\n\t" \
  "ds_read_b64_tr_b16 v[210:211], v249 offset:32288\n\t" \
  "v_fma_mix_f32 v229, |v197|, v229, v229 op_sel:[0,1,0] op_sel_hi:[0,1,1] clamp\n\t" \
  "v_fma_mix_f32 v228, |v196|, v228, v228 op_sel:[0,1,0] op_sel_hi:[0,1,1] clamp\n\t" \
  "v_mfma_f32_16x16x32_bf16 v[172:175], v[212:215], v[124:127], v[172:175]\n\t" \
  "ds_read_b64_tr_b16 v[212:213], v249 offset:30760\n\t" \
  "ds_read_b32 v222, v222 offset:10016\n\t" \
  "v_fma_mix_f32 v231, |v199|, v231, v231 op_sel:[0,1,0] op_sel_hi:[0,1,1] clamp\n\t" \
  "v_fma_mix_f32 v230, |v198|, v230, v230 op_sel:[0,1,0] op_sel_hi:[0,1,1] clamp\n\t" \
  "s_waitcnt lgkmcnt(7)\n\t" \
  "v_mfma_f32_16x16x32_bf16 v[148:151], v[216:219], v[128:131], v[148:151]\n\t" \
  "ds_read_b64_tr_b16 v[214:215], v249 offset:32296\n\t" \
  "v_or_b32 v204, v232, v233\n\t" \
  "v_or_b32 v205, v234, v235\n\t" \
  "v_mfma_f32_16x16x32_bf16 v[156:159], v[216:219], v[132:135], v[156:159]\n\t" \
  "ds_read_u16_d16_hi v225, v225 offset:10528\n\t" \
  "v_fma_f32 v197, |v197|, v229, v197 mul:4\n\t" \
  "v_fma_f32 v196, |v196|, v228, v196 mul:4\n\t" \
  "v_mfma_f32_16x16x32_bf16 v[164:167], v[216:219], v[136:139], v[164:167]\n\t" \
  "ds_read_u16 v224, v224 offset:10528\n\t" \
  "v_fma_f32 v199, |v199|, v231, v199 mul:4\n\t" \
  "v_fma_f32 v198, |v198|, v230, v198 mul:4\n\t" \
  "v_mfma_f32_16x16x32_bf16 v[172:175], v[216:219], v[140:143], v[172:175]\n\t" \
  "ds_read_b64_tr_b16 v[216:217], v249 offset:30816\n\t" \
  "v_cvt_pk_bf16_f32 v196, v196, v197\n\t" \
  "v_cvt_pk_bf16_f32 v197, v198, v199\n\t" \
  "v_mfma_f32_16x16x32_bf16 v[40:43], v[242:245], v[236:239], v[40:43]\n\t" \
  "ds_read_b64_tr_b16 v[218:219], v249 offset:32352\n\t" \
  "v_cvt_pknorm_i16_f32 v228, |v162|, |v163|\n\t" \
  "s_setprio 0\n\t" \
  "v_cvt_pknorm_i16_f32 v230, |v160|, |v161|\n\t" \
  "v_mfma_f32_16x16x32_bf16 v[44:47], v[240:243], v[236:239], v[44:47]\n\t" \
  "ds_read_u16_d16_hi v227, v227 offset:10528\n\t" \
  "buffer_load_dwordx4 v[236:239], v250, s[8:11], s14 offen offset:0 nt\n\t" \
  "v_lshlrev_b32_sdwa v229, 2, v228 dst_sel:DWORD dst_unused:UNUSED_PAD src0_sel:DWORD src1_sel:BYTE_3\n\t" \
  "s_waitcnt lgkmcnt(8)\n\t" \
  "v_mfma_f32_16x16x32_bf16 v[0:3], v[208:211], v[178:181], v[0:3]\n\t" \
  "ds_read_u16 v226, v226 offset:10528\n\t" \
  "v_lshlrev_b32_sdwa v228, 2, v228 dst_sel:DWORD dst_unused:UNUSED_PAD src0_sel:DWORD src1_sel:BYTE_1\n\t" \
  "v_lshlrev_b32_sdwa v231, 2, v230 dst_sel:DWORD dst_unused:UNUSED_PAD src0_sel:DWORD src1_sel:BYTE_3\n\t" \
  "v_mfma_f32_16x16x32_bf16 v[24:27], v[208:211], v[186:189], v[24:27]\n\t" \
  "ds_read_b32 v229, v229 offset:10016\n\t" \
  "v_lshlrev_b32_sdwa v230, 2, v230 dst_sel:DWORD dst_unused:UNUSED_PAD src0_sel:DWORD src1_sel:BYTE_1\n\t" \
  "v_lshlrev_b32_sdwa v233, 1, v171 dst_sel:DWORD dst_unused:UNUSED_PAD src0_sel:DWORD src1_sel:WORD_1\n\t" \
  "v_mfma_f32_16x16x32_bf16 v[48:51], v[208:211], v[194:197], v[48:51]\n\t" \
  "ds_read_b32 v228, v228 offset:10016\n\t" \
  "v_lshlrev_b32_sdwa v232, 1, v170 dst_sel:DWORD dst_unused:UNUSED_PAD src0_sel:DWORD src1_sel:WORD_1\n\t" \
  "v_lshlrev_b32_sdwa v235, 1, v169 dst_sel:DWORD dst_unused:UNUSED_PAD src0_sel:DWORD src1_sel:WORD_1\n\t" \
  "v_mfma_f32_16x16x32_bf16 v[72:75], v[208:211], v[202:205], v[72:75]\n\t" \
  "ds_read_b64_tr_b16 v[208:209], v249 offset:30824\n\t" \
  "ds_read_b32 v231, v231 offset:10016\n\t" \
  "v_lshlrev_b32_sdwa v234, 1, v168 dst_sel:DWORD dst_unused:UNUSED_PAD src0_sel:DWORD src1_sel:WORD_1\n\t" \
  "s_waitcnt lgkmcnt(10)\n\t" \
  "v_mfma_f32_16x16x32_bf16 v[4:7], v[212:215], v[178:181], v[4:7]\n\t" \
  "ds_read_b64_tr_b16 v[210:211], v249 offset:32360\n\t" \
  "v_fma_mix_f32 v221, |v147|, v221, v221 op_sel:[0,1,0] op_sel_hi:[0,1,1] clamp\n\t" \
  "v_fma_mix_f32 v220, |v146|, v220, v220 op_sel:[0,1,0] op_sel_hi:[0,1,1] clamp\n\t" \
  "v_mfma_f32_16x16x32_bf16 v[28:31], v[212:215], v[186:189], v[28:31]\n\t" \
  "ds_read_b32 v230, v230 offset:10016\n\t" \
  "v_fma_mix_f32 v223, |v145|, v223, v223 op_sel:[0,1,0] op_sel_hi:[0,1,1] clamp\n\t" \
  "v_fma_mix_f32 v222, |v144|, v222, v222 op_sel:[0,1,0] op_sel_hi:[0,1,1] clamp\n\t" \
  "s_waitcnt lgkmcnt(6)\n\t" \
  "v_mfma_f32_16x16x32_bf16 v[52:55], v[212:215], v[194:197], v[52:55]\n\t" \
  "ds_read_u16_d16_hi v233, v233 offset:10528\n\t" \
  "v_or_b32 v155, v224, v225\n\t" \
  "v_or_b32 v154, v226, v227\n\t" \
  "v_mfma_f32_16x16x32_bf16 v[76:79], v[212:215], v[202:205], v[76:79]\n\t" \
  "ds_read_b64_tr_b16 v[212:213], v249 offset:30880\n\t" \
  "v_fma_f32 v147, |v147|, v221, v147 mul:4\n\t" \
  "v_fma_f32 v146, |v146|, v220, v146 mul:4\n\t" \
  "v_mfma_f32_16x16x32_bf16 v[8:11], v[216:219], v[178:181], v[8:11]\n\t" \
  "ds_read_b64_tr_b16 v[214:215], v249 offset:32416\n\t" \
  "v_fma_f32 v145, |v145|, v223, v145 mul:4\n\t" \
  "v_fma_f32 v144, |v144|, v222, v144 mul:4\n\t" \
  "v_mfma_f32_16x16x32_bf16 v[32:35], v[216:219], v[186:189], v[32:35]\n\t" \
  "ds_read_u16 v232, v232 offset:10528\n\t" \
  "v_cvt_pk_bf16_f32 v147, v146, v147\n\t" \
  "v_cvt_pk_bf16_f32 v146, v144, v145\n\t" \
  "v_mfma_f32_16x16x32_bf16 v[56:59], v[216:219], v[194:197], v[56:59]\n\t" \
  "ds_read_u16_d16_hi v235, v235 offset:10528\n\t" \
  "v_cvt_pknorm_i16_f32 v220, |v148|, |v149|\n\t" \
  "s_setprio 1\n\t" \
  "v_cvt_pknorm_i16_f32 v222, |v150|, |v151|\n\t" \
  "v_mfma_f32_16x16x32_bf16 v[80:83], v[216:219], v[202:205], v[80:83]\n\t" \
  "ds_read_b64_tr_b16 v[216:217], v249 offset:30888\n\t" \
  "v_lshlrev_b32_sdwa v221, 2, v220 dst_sel:DWORD dst_unused:UNUSED_PAD src0_sel:DWORD src1_sel:BYTE_3\n\t" \
  "s_waitcnt lgkmcnt(7)\n\t" \
  "v_mfma_f32_16x16x32_bf16 v[12:15], v[208:211], v[178:181], v[12:15]\n\t" \
  "ds_read_b64_tr_b16 v[218:219], v249 offset:32424\n\t" \
  "v_lshlrev_b32_sdwa v220, 2, v220 dst_sel:DWORD dst_unused:UNUSED_PAD src0_sel:DWORD src1_sel:BYTE_1\n\t" \
  "v_lshlrev_b32_sdwa v223, 2, v222 dst_sel:DWORD dst_unused:UNUSED_PAD src0_sel:DWORD src1_sel:BYTE_3\n\t" \
  "v_mfma_f32_16x16x32_bf16 v[36:39], v[208:211], v[186:189], v[36:39]\n\t" \
  "ds_read_u16 v234, v234 offset:10528\n\t" \
  "v_lshlrev_b32_sdwa v222, 2, v222 dst_sel:DWORD dst_unused:UNUSED_PAD src0_sel:DWORD src1_sel:BYTE_1\n\t" \
  "v_lshlrev_b32_sdwa v225, 1, v157 dst_sel:DWORD dst_unused:UNUSED_PAD src0_sel:DWORD src1_sel:WORD_1\n\t" \
  "v_mfma_f32_16x16x32_bf16 v[60:63], v[208:211], v[194:197], v[60:63]\n\t" \
  "ds_read_b32 v221, v221 offset:10016\n\t" \
  "v_lshlrev_b32_sdwa v224, 1, v156 dst_sel:DWORD dst_unused:UNUSED_PAD src0_sel:DWORD src1_sel:WORD_1\n\t" \
  "v_lshlrev_b32_sdwa v227, 1, v159 dst_sel:DWORD dst_unused:UNUSED_PAD src0_sel:DWORD src1_sel:WORD_1\n\t" \
  "v_mfma_f32_16x16x32_bf16 v[84:87], v[208:211], v[202:205], v[84:87]\n\t" \
  "ds_read_b64_tr_b16 v[208:209], v248 offset:0\n\t" \
  "ds_read_b32 v220, v220 offset:10016\n\t" \
  "v_lshlrev_b32_sdwa v226, 1, v158 dst_sel:DWORD dst_unused:UNUSED_PAD src0_sel:DWORD src1_sel:WORD_1\n\t" \
  "s_waitcnt lgkmcnt(8)\n\t" \
  "v_mfma_f32_16x16x32_bf16 v[16:19], v[212:215], v[178:181], v[16:19]\n\t" \
  "ds_read_b64_tr_b16 v[210:211], v248 offset:1024\n\t" \
  "ds_read_b32 v223, v223 offset:10016\n\t" \
  "v_fma_mix_f32 v229, |v163|, v229, v229 op_sel:[0,1,0] op_sel_hi:[0,1,1] clamp\n\t" \
  "v_mfma_f32_16x16x32_bf16 v[40:43], v[212:215], v[186:189], v[40:43]\n\t" \
  "ds_read_b32 v222, v222 offset:10016\n\t" \
  "v_fma_mix_f32 v228, |v162|, v228, v228 op_sel:[0,1,0] op_sel_hi:[0,1,1] clamp\n\t" \
  "v_fma_mix_f32 v231, |v161|, v231, v231 op_sel:[0,1,0] op_sel_hi:[0,1,1] clamp\n\t" \
  "v_mfma_f32_16x16x32_bf16 v[64:67], v[212:215], v[194:197], v[64:67]\n\t" \
  "ds_read_u16_d16_hi v225, v225 offset:10528\n\t" \
  "v_fma_mix_f32 v230, |v160|, v230, v230 op_sel:[0,1,0] op_sel_hi:[0,1,1] clamp\n\t" \
  "s_waitcnt lgkmcnt(7)\n\t" \
  "v_or_b32 v171, v232, v233\n\t" \
  "v_mfma_f32_16x16x32_bf16 v[88:91], v[212:215], v[202:205], v[88:91]\n\t" \
  "ds_read_b64_tr_b16 v[212:213], v248 offset:3072\n\t" \
  "v_or_b32 v170, v234, v235\n\t" \
  "v_fma_f32 v163, |v163|, v229, v163 mul:4\n\t" \
  "v_mfma_f32_16x16x32_bf16 v[20:23], v[216:219], v[178:181], v[20:23]\n\t" \
  "ds_read_b64_tr_b16 v[214:215], v248 offset:4096\n\t" \
  "v_fma_f32 v162, |v162|, v228, v162 mul:4\n\t" \
  "v_fma_f32 v161, |v161|, v231, v161 mul:4\n\t" \
  "v_mfma_f32_16x16x32_bf16 v[44:47], v[216:219], v[186:189], v[44:47]\n\t" \
  "ds_read_u16 v224, v224 offset:10528\n\t" \
  "v_fma_f32 v160, |v160|, v230, v160 mul:4\n\t" \
  "v_cvt_pk_bf16_f32 v163, v162, v163\n\t" \
  "v_mfma_f32_16x16x32_bf16 v[68:71], v[216:219], v[194:197], v[68:71]\n\t" \
  "ds_read_u16_d16_hi v227, v227 offset:10528\n\t" \
  "v_cvt_pk_bf16_f32 v162, v160, v161\n\t" \
  "v_cvt_pknorm_i16_f32 v228, |v164|, |v165|\n\t" \
  "v_mfma_f32_16x16x32_bf16 v[92:95], v[216:219], v[202:205], v[92:95]\n\t" \
  "ds_read_b64_tr_b16 v[216:217], v248 offset:2048\n\t" \
  "v_cvt_pknorm_i16_f32 v230, |v166|, |v167|\n\t" \
  "s_setprio 2\n\t" \
  "v_lshlrev_b32_sdwa v229, 2, v228 dst_sel:DWORD dst_unused:UNUSED_PAD src0_sel:DWORD src1_sel:BYTE_3\n\t" \
  "s_waitcnt lgkmcnt(8)\n\t" \
  "v_mfma_f32_16x16x32_bf16 v[176:179], v[208:211], v[96:99], 0\n\t" \
  "ds_read_b64_tr_b16 v[218:219], v248 offset:5120\n\t" \
  "v_lshlrev_b32_sdwa v228, 2, v228 dst_sel:DWORD dst_unused:UNUSED_PAD src0_sel:DWORD src1_sel:BYTE_1\n\t" \
  "v_lshlrev_b32_sdwa v231, 2, v230 dst_sel:DWORD dst_unused:UNUSED_PAD src0_sel:DWORD src1_sel:BYTE_3\n\t" \
  "v_mfma_f32_16x16x32_bf16 v[184:187], v[208:211], v[100:103], 0\n\t" \
  "ds_read_u16 v226, v226 offset:10528\n\t" \
  "v_lshlrev_b32_sdwa v230, 2, v230 dst_sel:DWORD dst_unused:UNUSED_PAD src0_sel:DWORD src1_sel:BYTE_1\n\t" \
  "v_mfma_f32_16x16x32_bf16 v[192:195], v[208:211], v[104:107], 0\n\t" \
  "ds_read_b32 v229, v229 offset:10016\n\t" \
  "v_lshlrev_b32_sdwa v233, 1, v173 dst_sel:DWORD dst_unused:UNUSED_PAD src0_sel:DWORD src1_sel:WORD_1\n\t" \
  "v_lshlrev_b32_sdwa v232, 1, v172 dst_sel:DWORD dst_unused:UNUSED_PAD src0_sel:DWORD src1_sel:WORD_1\n\t" \
  "v_mfma_f32_16x16x32_bf16 v[200:203], v[208:211], v[108:111], 0\n\t" \
  "ds_read_b64_tr_b16 v[208:209], v248 offset:512\n\t" \
  "ds_read_b32 v228, v228 offset:10016\n\t" \
  "v_lshlrev_b32_sdwa v235, 1, v175 dst_sel:DWORD dst_unused:UNUSED_PAD src0_sel:DWORD src1_sel:WORD_1\n\t" \
  "s_waitcnt lgkmcnt(8)\n\t" \
  "v_mfma_f32_16x16x32_bf16 v[176:179], v[212:215], v[112:115], v[176:179]\n\t" \
  "ds_read_b64_tr_b16 v[210:211], v248 offset:1536\n\t" \
  "v_lshlrev_b32_sdwa v234, 1, v174 dst_sel:DWORD dst_unused:UNUSED_PAD src0_sel:DWORD src1_sel:WORD_1\n\t" \
  "v_fma_mix_f32 v221, |v149|, v221, v221 op_sel:[0,1,0] op_sel_hi:[0,1,1] clamp\n\t" \
  "v_mfma_f32_16x16x32_bf16 v[184:187], v[212:215], v[116:119], v[184:187]\n\t" \
  "ds_read_b32 v231, v231 offset:10016\n\t" \
  "ds_read_b32 v230, v230 offset:10016\n\t" \
  "v_fma_mix_f32 v220, |v148|, v220, v220 op_sel:[0,1,0] op_sel_hi:[0,1,1] clamp\n\t" \
  "v_mfma_f32_16x16x32_bf16 v[192:195], v[212:215], v[120:123], v[192:195]\n\t" \
  "ds_read_u16_d16_hi v233, v233 offset:10528\n\t" \
  "v_fma_mix_f32 v223, |v151|, v223, v223 op_sel:[0,1,0] op_sel_hi:[0,1,1] clamp\n\t" \
  "v_fma_mix_f32 v222, |v150|, v222, v222 op_sel:[0,1,0] op_sel_hi:[0,1,1] clamp\n\t" \
  "s_waitcnt lgkmcnt(7)\n\t" \
  "v_mfma_f32_16x16x32_bf16 v[200:203], v[212:215], v[124:127], v[200:203]\n\t" \
  "ds_read_b64_tr_b16 v[212:213], v248 offset:3584\n\t" \
  "v_or_b32 v156, v224, v225\n\t" \
  "v_or_b32 v157, v226, v227\n\t" \
  "v_mfma_f32_16x16x32_bf16 v[176:179], v[216:219], v[128:131], v[176:179]\n\t" \
  "ds_read_b64_tr_b16 v[214:215], v248 offset:4608\n\t" \
  "v_fma_f32 v149, |v149|, v221, v149 mul:4\n\t" \
  "v_fma_f32 v148, |v148|, v220, v148 mul:4\n\t" \
  "v_mfma_f32_16x16x32_bf16 v[184:187], v[216:219], v[132:135], v[184:187]\n\t" \
  "ds_read_u16 v232, v232 offset:10528\n\t" \
  "v_fma_f32 v151, |v151|, v223, v151 mul:4\n\t" \
  "v_fma_f32 v150, |v150|, v222, v150 mul:4\n\t" \
  "v_mfma_f32_16x16x32_bf16 v[192:195], v[216:219], v[136:139], v[192:195]\n\t" \
  "ds_read_u16_d16_hi v235, v235 offset:10528\n\t" \
  "v_cvt_pk_bf16_f32 v148, v148, v149\n\t" \
  "v_cvt_pk_bf16_f32 v149, v150, v151\n\t" \
  "v_mfma_f32_16x16x32_bf16 v[200:203], v[216:219], v[140:143], v[200:203]\n\t" \
  "ds_read_b64_tr_b16 v[216:217], v248 offset:2560\n\t" \
  "v_cvt_pknorm_i16_f32 v220, |v178|, |v179|\n\t" \
  "s_setprio 3\n\t" \
  "v_cvt_pknorm_i16_f32 v222, |v176|, |v177|\n\t" \
  "s_waitcnt lgkmcnt(8)\n\t" \
  "v_mfma_f32_16x16x32_bf16 v[180:183], v[208:211], v[96:99], 0\n\t" \
  "ds_read_b64_tr_b16 v[218:219], v248 offset:5632\n\t" \
  "v_lshlrev_b32_sdwa v221, 2, v220 dst_sel:DWORD dst_unused:UNUSED_PAD src0_sel:DWORD src1_sel:BYTE_3\n\t" \
  "s_waitcnt vmcnt(0)\n\t" \
  "v_lshlrev_b32_sdwa v220, 2, v220 dst_sel:DWORD dst_unused:UNUSED_PAD src0_sel:DWORD src1_sel:BYTE_1\n\t" \
  "v_mfma_f32_16x16x32_bf16 v[188:191], v[208:211], v[100:103], 0\n\t" \
  "s_mov_b64 exec, 3\n\t" \
  "ds_add_rtn_u32 v246, v251, v250 offset:47192\n\t" \
  "s_mov_b64 exec, -1\n\t" \
  "v_lshlrev_b32_sdwa v223, 2, v222 dst_sel:DWORD dst_unused:UNUSED_PAD src0_sel:DWORD src1_sel:BYTE_3\n\t" \
  "v_mfma_f32_16x16x32_bf16 v[196:199], v[208:211], v[104:107], 0\n\t" \
  "ds_read_u16 v234, v234 offset:10528\n\t" \
  "v_lshlrev_b32_sdwa v222, 2, v222 dst_sel:DWORD dst_unused:UNUSED_PAD src0_sel:DWORD src1_sel:BYTE_1\n\t" \
  "v_lshlrev_b32_sdwa v225, 1, v187 dst_sel:DWORD dst_unused:UNUSED_PAD src0_sel:DWORD src1_sel:WORD_1\n\t" \
  "v_mfma_f32_16x16x32_bf16 v[204:207], v[208:211], v[108:111], 0\n\t" \
  "ds_read_b32 v221, v221 offset:10016\n\t" \
  "v_lshlrev_b32_sdwa v224, 1, v186 dst_sel:DWORD dst_unused:UNUSED_PAD src0_sel:DWORD src1_sel:WORD_1\n\t" \
  "s_waitcnt lgkmcnt(2)\n\t" \
  "v_readfirstlane_b32 s4, v246\n\t" \
  "v_mfma_f32_16x16x32_bf16 v[180:183], v[212:215], v[112:115], v[180:183]\n\t" \
  "ds_read_b32 v220, v220 offset:10016\n\t" \
  "v_lshlrev_b32_sdwa v227, 1, v185 dst_sel:DWORD dst_unused:UNUSED_PAD src0_sel:DWORD src1_sel:WORD_1\n\t" \
  "s_cmpk_lt_u32 s4, 6144\n\t" \
  "s_cbranch_scc1 .Lcnx_w1_8\n.Lcnx_g1_8:\n\t" \
  "v_mfma_f32_16x16x32_bf16 v[188:191], v[212:215], v[116:119], v[188:191]\n\t" \
  "ds_read_b64_tr_b16 v[208:209], v249 offset:6152\n\t" \
  "ds_read_b32 v223, v223 offset:10016\n\t" \
  "v_lshlrev_b32_sdwa v226, 1, v184 dst_sel:DWORD dst_unused:UNUSED_PAD src0_sel:DWORD src1_sel:WORD_1\n\t" \
  "v_mfma_f32_16x16x32_bf16 v[196:199], v[212:215], v[120:123], v[196:199]\n\t" \
  "ds_read_b64_tr_b16 v[210:211], v249 offset:7688\n\t" \
  "v_fma_mix_f32 v229, |v165|, v229, v229 op_sel:[0,1,0] op_sel_hi:[0,1,1] clamp\n\t" \
  "v_fma_mix_f32 v228, |v164|, v228, v228 op_sel:[0,1,0] op_sel_hi:[0,1,1] clamp\n\t" \
  "v_mfma_f32_16x16x32_bf16 v[204:207], v[212:215], v[124:127], v[204:207]\n\t" \
  "ds_read_b64_tr_b16 v[212:213], v249 offset:6160\n\t" \
  "ds_read_b32 v222, v222 offset:10016\n\t" \
  "v_fma_mix_f32 v231, |v167|, v231, v231 op_sel:[0,1,0] op_sel_hi:[0,1,1] clamp\n\t" \
  "v_fma_mix_f32 v230, |v166|, v230, v230 op_sel:[0,1,0] op_sel_hi:[0,1,1] clamp\n\t" \
  "s_waitcnt lgkmcnt(7)\n\t" \
  "v_mfma_f32_16x16x32_bf16 v[180:183], v[216:219], v[128:131], v[180:183]\n\t" \
  "ds_read_b64_tr_b16 v[214:215], v249 offset:7696\n\t" \
  "v_or_b32 v172, v232, v233\n\t" \
  "v_or_b32 v173, v234, v235\n\t" \
  "v_mfma_f32_16x16x32_bf16 v[188:191], v[216:219], v[132:135], v[188:191]\n\t" \
  "ds_read_u16_d16_hi v225, v225 offset:10528\n\t" \
  "v_fma_f32 v165, |v165|, v229, v165 mul:4\n\t" \
  "v_fma_f32 v164, |v164|, v228, v164 mul:4\n\t" \
  "v_mfma_f32_16x16x32_bf16 v[196:199], v[216:219], v[136:139], v[196:199]\n\t" \
  "ds_read_u16 v224, v224 offset:10528\n\t" \
  "v_fma_f32 v167, |v167|, v231, v167 mul:4\n\t" \
  "v_fma_f32 v166, |v166|, v230, v166 mul:4\n\t" \
  "v_mfma_f32_16x16x32_bf16 v[204:207], v[216:219], v[140:143], v[204:207]\n\t" \
  "ds_read_b64_tr_b16 v[216:217], v249 offset:6216\n\t" \
  "v_cvt_pk_bf16_f32 v164, v164, v165\n\t" \
  "v_cvt_pk_bf16_f32 v165, v166, v167\n\t" \
  "v_mfma_f32_16x16x32_bf16 v[48:51], v[242:245], v[236:239], v[48:51]\n\t" \
  "ds_read_b64_tr_b16 v[218:219], v249 offset:7752\n\t" \
  "v_cvt_pknorm_i16_f32 v228, |v194|, |v195|\n\t" \
  "s_setprio 0\n\t" \
  "v_cvt_pknorm_i16_f32 v230, |v192|, |v193|\n\t" \
  "v_mfma_f32_16x16x32_bf16 v[52:55], v[240:243], v[236:239], v[52:55]\n\t" \
  "ds_read_u16_d16_hi v227, v227 offset:10528\n\t" \
  "buffer_load_dwordx4 v[236:239], v250, s[8:11], s14 offen offset:64 nt\n\t" \
  "v_lshlrev_b32_sdwa v229, 2, v228 dst_sel:DWORD dst_unused:UNUSED_PAD src0_sel:DWORD src1_sel:BYTE_3\n\t" \
  "s_waitcnt lgkmcnt(8)\n\t" \
  "v_mfma_f32_16x16x32_bf16 v[0:3], v[208:211], v[146:149], v[0:3]\n\t" \
  "ds_read_u16 v226, v226 offset:10528\n\t" \
  "v_lshlrev_b32_sdwa v228, 2, v228 dst_sel:DWORD dst_unused:UNUSED_PAD src0_sel:DWORD src1_sel:BYTE_1\n\t" \
  "v_lshlrev_b32_sdwa v231, 2, v230 dst_sel:DWORD dst_unused:UNUSED_PAD src0_sel:DWORD src1_sel:BYTE_3\n\t" \
  "v_mfma_f32_16x16x32_bf16 v[24:27], v[208:211], v[154:157], v[24:27]\n\t" \
  "ds_read_b32 v229, v229 offset:10016\n\t" \
  "v_lshlrev_b32_sdwa v230, 2, v230 dst_sel:DWORD dst_unused:UNUSED_PAD src0_sel:DWORD src1_sel:BYTE_1\n\t" \
  "v_lshlrev_b32_sdwa v233, 1, v203 dst_sel:DWORD dst_unused:UNUSED_PAD src0_sel:DWORD src1_sel:WORD_1\n\t" \
  "v_mfma_f32_16x16x32_bf16 v[48:51], v[208:211], v[162:165], v[48:51]\n\t" \
  "ds_read_b32 v228, v228 offset:10016\n\t" \
  "v_lshlrev_b32_sdwa v232, 1, v202 dst_sel:DWORD dst_unused:UNUSED_PAD src0_sel:DWORD src1_sel:WORD_1\n\t" \
  "v_lshlrev_b32_sdwa v235, 1, v201 dst_sel:DWORD dst_unused:UNUSED_PAD src0_sel:DWORD src1_sel:WORD_1\n\t" \
  "v_mfma_f32_16x16x32_bf16 v[72:75], v[208:211], v[170:173], v[72:75]\n\t" \
  "ds_read_b64_tr_b16 v[208:209], v249 offset:6224\n\t" \
  "ds_read_b32 v231, v231 offset:10016\n\t" \
  "v_lshlrev_b32_sdwa v234, 1, v200 dst_sel:DWORD dst_unused:UNUSED_PAD src0_sel:DWORD src1_sel:WORD_1\n\t" \
  "s_waitcnt lgkmcnt(10)\n\t" \
  "v_mfma_f32_16x16x32_bf16 v[4:7], v[212:215], v[146:149], v[4:7]\n\t" \
  "ds_read_b64_tr_b16 v[210:211], v249 offset:7760\n\t" \
  "v_fma_mix_f32 v221, |v179|, v221, v221 op_sel:[0,1,0] op_sel_hi:[0,1,1] clamp\n\t" \
  "v_fma_mix_f32 v220, |v178|, v220, v220 op_sel:[0,1,0] op_sel_hi:[0,1,1] clamp\n\t" \
  "v_mfma_f32_16x16x32_bf16 v[28:31], v[212:215], v[154:157], v[28:31]\n\t" \
  "ds_read_b32 v230, v230 offset:10016\n\t" \
  "v_fma_mix_f32 v223, |v177|, v223, v223 op_sel:[0,1,0] op_sel_hi:[0,1,1] clamp\n\t" \
  "v_fma_mix_f32 v222, |v176|, v222, v222 op_sel:[0,1,0] op_sel_hi:[0,1,1] clamp\n\t" \
  "s_waitcnt lgkmcnt(6)\n\t" \
  "v_mfma_f32_16x16x32_bf16 v[52:55], v[212:215], v[162:165], v[52:55]\n\t" \
  "ds_read_u16_d16_hi v233, v233 offset:10528\n\t" \
  "v_or_b32 v187, v224, v225\n\t" \
  "v_or_b32 v186, v226, v227\n\t" \
  "v_mfma_f32_16x16x32_bf16 v[76:79], v[212:215], v[170:173], v[76:79]\n\t" \
  "ds_read_b64_tr_b16 v[212:213], v249 offset:6280\n\t" \
  "v_fma_f32 v179, |v179|, v221, v179 mul:4\n\t" \
  "v_fma_f32 v178, |v178|, v220, v178 mul:4\n\t" \
  "v_mfma_f32_16x16x32_bf16 v[8:11], v[216:219], v[146:149], v[8:11]\n\t" \
  "ds_read_b64_tr_b16 v[214:215], v249 offset:7816\n\t" \
  "v_fma_f32 v177, |v177|, v223, v177 mul:4\n\t" \
  "v_fma_f32 v176, |v176|, v222, v176 mul:4\n\t" \
  "v_mfma_f32_16x16x32_bf16 v[32:35], v[216:219], v[154:157], v[32:35]\n\t" \
  "ds_read_u16 v232, v232 offset:10528\n\t" \
  "v_cvt_pk_bf16_f32 v179, v178, v179\n\t" \
  "v_cvt_pk_bf16_f32 v178, v176, v177\n\t" \
  "v_mfma_f32_16x16x32_bf16 v[56:59], v[216:219], v[162:165], v[56:59]\n\t" \
  "ds_read_u16_d16_hi v235, v235 offset:10528\n\t" \
  "v_cvt_pknorm_i16_f32 v220, |v180|, |v181|\n\t" \
  "s_setprio 1\n\t" \
  "v_cvt_pknorm_i16_f32 v222, |v182|, |v183|\n\t" \
  "v_mfma_f32_16x16x32_bf16 v[80:83], v[216:219], v[170:173], v[80:83]\n\t" \
  "ds_read_b64_tr_b16 v[216:217], v249 offset:6288\n\t" \
  "v_lshlrev_b32_sdwa v221, 2, v220 dst_sel:DWORD dst_unused:UNUSED_PAD src0_sel:DWORD src1_sel:BYTE_3\n\t" \
  "s_waitcnt lgkmcnt(7)\n\t" \
  "v_mfma_f32_16x16x32_bf16 v[12:15], v[208:211], v[146:149], v[12:15]\n\t" \
  "ds_read_b64_tr_b16 v[218:219], v249 offset:7824\n\t" \
  "v_lshlrev_b32_sdwa v220, 2, v220 dst_sel:DWORD dst_unused:UNUSED_PAD src0_sel:DWORD src1_sel:BYTE_1\n\t" \
  "v_lshlrev_b32_sdwa v223, 2, v222 dst_sel:DWORD dst_unused:UNUSED_PAD src0_sel:DWORD src1_sel:BYTE_3\n\t" \
  "v_mfma_f32_16x16x32_bf16 v[36:39], v[208:211], v[154:157], v[36:39]\n\t" \
  "ds_read_u16 v234, v234 offset:10528\n\t" \
  "v_lshlrev_b32_sdwa v222, 2, v222 dst_sel:DWORD dst_unused:UNUSED_PAD src0_sel:DWORD src1_sel:BYTE_1\n\t" \
  "v_lshlrev_b32_sdwa v225, 1, v189 dst_sel:DWORD dst_unused:UNUSED_PAD src0_sel:DWORD src1_sel:WORD_1\n\t" \
  "v_mfma_f32_16x16x32_bf16 v[60:63], v[208:211], v[162:165], v[60:63]\n\t" \
  "ds_read_b32 v221, v221 offset:10016\n\t" \
  "v_lshlrev_b32_sdwa v224, 1, v188 dst_sel:DWORD dst_unused:UNUSED_PAD src0_sel:DWORD src1_sel:WORD_1\n\t" \
  "v_lshlrev_b32_sdwa v227, 1, v191 dst_sel:DWORD dst_unused:UNUSED_PAD src0_sel:DWORD src1_sel:WORD_1\n\t" \
  "v_mfma_f32_16x16x32_bf16 v[84:87], v[208:211], v[170:173], v[84:87]\n\t" \
  "ds_read_b64_tr_b16 v[208:209], v248 offset:43048\n\t" \
  "ds_read_b32 v220, v220 offset:10016\n\t" \
  "v_lshlrev_b32_sdwa v226, 1, v190 dst_sel:DWORD dst_unused:UNUSED_PAD src0_sel:DWORD src1_sel:WORD_1\n\t" \
  "s_waitcnt lgkmcnt(8)\n\t" \
  "v_mfma_f32_16x16x32_bf16 v[16:19], v[212:215], v[146:149], v[16:19]\n\t" \
  "ds_read_b64_tr_b16 v[210:211], v248 offset:44072\n\t" \
  "ds_read_b32 v223, v223 offset:10016\n\t" \
  "v_fma_mix_f32 v229, |v195|, v229, v229 op_sel:[0,1,0] op_sel_hi:[0,1,1] clamp\n\t" \
  "v_mfma_f32_16x16x32_bf16 v[40:43], v[212:215], v[154:157], v[40:43]\n\t" \
  "ds_read_b32 v222, v222 offset:10016\n\t" \
  "v_fma_mix_f32 v228, |v194|, v228, v228 op_sel:[0,1,0] op_sel_hi:[0,1,1] clamp\n\t" \
  "v_fma_mix_f32 v231, |v193|, v231, v231 op_sel:[0,1,0] op_sel_hi:[0,1,1] clamp\n\t" \
  "v_mfma_f32_16x16x32_bf16 v[64:67], v[212:215], v[162:165], v[64:67]\n\t" \
  "ds_read_u16_d16_hi v225, v225 offset:10528\n\t" \
  "v_fma_mix_f32 v230, |v192|, v230, v230 op_sel:[0,1,0] op_sel_hi:[0,1,1] clamp\n\t" \
  "s_waitcnt lgkmcnt(7)\n\t" \
  "v_or_b32 v203, v232, v233\n\t" \
  "v_mfma_f32_16x16x32_bf16 v[88:91], v[212:215], v[170:173], v[88:91]\n\t" \
  "ds_read_b64_tr_b16 v[212:213], v248 offset:46120\n\t" \
  "v_or_b32 v202, v234, v235\n\t" \
  "v_fma_f32 v195, |v195|, v229, v195 mul:4\n\t" \
  "v_mfma_f32_16x16x32_bf16 v[20:23], v[216:219], v[146:149], v[20:23]\n\t" \
  "ds_read_b64_tr_b16 v[214:215], v248 offset:47144\n\t" \
  "v_fma_f32 v194, |v194|, v228, v194 mul:4\n\t" \
  "v_fma_f32 v193, |v193|, v231, v193 mul:4\n\t" \
  "v_mfma_f32_16x16x32_bf16 v[44:47], v[216:219], v[154:157], v[44:47]\n\t" \
  "ds_read_u16 v224, v224 offset:10528\n\t" \
  "v_fma_f32 v192, |v192|, v230, v192 mul:4\n\t" \
  "v_cvt_pk_bf16_f32 v195, v194, v195\n\t" \
  "v_mfma_f32_16x16x32_bf16 v[68:71], v[216:219], v[162:165], v[68:71]\n\t" \
  "ds_read_u16_d16_hi v227, v227 offset:10528\n\t" \
  "v_cvt_pk_bf16_f32 v194, v192, v193\n\t" \
  "v_cvt_pknorm_i16_f32 v228, |v196|, |v197|\n\t" \
  "v_mfma_f32_16x16x32_bf16 v[92:95], v[216:219], v[170:173], v[92:95]\n\t" \
  "ds_read_b64_tr_b16 v[216:217], v248 offset:45096\n\t" \
  "v_cvt_pknorm_i16_f32 v230, |v198|, |v199|\n\t" \
  "s_setprio 2\n\t" \
  "v_lshlrev_b32_sdwa v229, 2, v228 dst_sel:DWORD dst_unused:UNUSED_PAD src0_sel:DWORD src1_sel:BYTE_3\n\t" \
  "s_waitcnt lgkmcnt(8)\n\t" \
  "v_mfma_f32_16x16x32_bf16 v[144:147], v[208:211], v[96:99], 0\n\t" \
  "ds_read_b64_tr_b16 v[218:219], v248 offset:48168\n\t" \
  "v_lshlrev_b32_sdwa v228, 2, v228 dst_sel:DWORD dst_unused:UNUSED_PAD src0_sel:DWORD src1_sel:BYTE_1\n\t" \
  "v_lshlrev_b32_sdwa v231, 2, v230 dst_sel:DWORD dst_unused:UNUSED_PAD src0_sel:DWORD src1_sel:BYTE_3\n\t" \
  "v_mfma_f32_16x16x32_bf16 v[152:155], v[208:211], v[100:103], 0\n\t" \
  "ds_read_u16 v226, v226 offset:10528\n\t" \
  "v_lshlrev_b32_sdwa v230, 2, v230 dst_sel:DWORD dst_unused:UNUSED_PAD src0_sel:DWORD src1_sel:BYTE_1\n\t" \
  "v_mfma_f32_16x16x32_bf16 v[160:163], v[208:211], v[104:107], 0\n\t" \
  "ds_read_b32 v229, v229 offset:10016\n\t" \
  "v_lshlrev_b32_sdwa v233, 1, v205 dst_sel:DWORD dst_unused:UNUSED_PAD src0_sel:DWORD src1_sel:WORD_1\n\t" \
  "v_lshlrev_b32_sdwa v232, 1, v204 dst_sel:DWORD dst_unused:UNUSED_PAD src0_sel:DWORD src1_sel:WORD_1\n\t" \
  "v_mfma_f32_16x16x32_bf16 v[168:171], v[208:211], v[108:111], 0\n\t" \
  "ds_read_b64_tr_b16 v[208:209], v248 offset:43560\n\t" \
  "ds_read_b32 v228, v228 offset:10016\n\t" \
  "v_lshlrev_b32_sdwa v235, 1, v207 dst_sel:DWORD dst_unused:UNUSED_PAD src0_sel:DWORD src1_sel:WORD_1\n\t" \
  "s_waitcnt lgkmcnt(8)\n\t" \
  "v_mfma_f32_16x16x32_bf16 v[144:147], v[212:215], v[112:115], v[144:147]\n\t" \
  "ds_read_b64_tr_b16 v[210:211], v248 offset:44584\n\t" \
  "v_lshlrev_b32_sdwa v234, 1, v206 dst_sel:DWORD dst_unused:UNUSED_PAD src0_sel:DWORD src1_sel:WORD_1\n\t" \
  "v_fma_mix_f32 v221, |v181|, v221, v221 op_sel:[0,1,0] op_sel_hi:[0,1,1] clamp\n\t" \
  "v_mfma_f32_16x16x32_bf16 v[152:155], v[212:215], v[116:119], v[152:155]\n\t" \
  "ds_read_b32 v231, v231 offset:10016\n\t" \
  "ds_read_b32 v230, v230 offset:10016\n\t" \
  "v_fma_mix_f32 v220, |v180|, v220, v220 op_sel:[0,1,0] op_sel_hi:[0,1,1] clamp\n\t" \
  "v_mfma_f32_16x16x32_bf16 v[160:163], v[212:215], v[120:123], v[160:163]\n\t" \
  "ds_read_u16_d16_hi v233, v233 offset:10528\n\t" \
  "v_fma_mix_f32 v223, |v183|, v223, v223 op_sel:[0,1,0] op_sel_hi:[0,1,1] clamp\n\t" \
  "v_fma_mix_f32 v222, |v182|, v222, v222 op_sel:[0,1,0] op_sel_hi:[0,1,1] clamp\n\t" \
  "s_waitcnt lgkmcnt(7)\n\t" \
  "v_mfma_f32_16x16x32_bf16 v[168:171], v[212:215], v[124:127], v[168:171]\n\t" \
  "ds_read_b64_tr_b16 v[212:213], v248 offset:46632\n\t" \
  "v_or_b32 v188, v224, v225\n\t" \
  "v_or_b32 v189, v226, v227\n\t" \
  "v_mfma_f32_16x16x32_bf16 v[144:147], v[216:219], v[128:131], v[144:147]\n\t" \
  "ds_read_b64_tr_b16 v[214:215], v248 offset:47656\n\t" \
  "v_fma_f32 v181, |v181|, v221, v181 mul:4\n\t" \
  "v_fma_f32 v180, |v180|, v220, v180 mul:4\n\t" \
  "v_mfma_f32_16x16x32_bf16 v[152:155], v[216:219], v[132:135], v[152:155]\n\t" \
  "ds_read_u16 v232, v232 offset:10528\n\t" \
  "v_fma_f32 v183, |v183|, v223, v183 mul:4\n\t" \
  "v_fma_f32 v182, |v182|, v222, v182 mul:4\n\t" \
  "v_mfma_f32_16x16x32_bf16 v[160:163], v[216:219], v[136:139], v[160:163]\n\t" \
  "ds_read_u16_d16_hi v235, v235 offset:10528\n\t" \
  "v_cvt_pk_bf16_f32 v180, v180, v181\n\t" \
  "v_cvt_pk_bf16_f32 v181, v182, v183\n\t" \
  "v_mfma_f32_16x16x32_bf16 v[168:171], v[216:219], v[140:143], v[168:171]\n\t" \
  "ds_read_b64_tr_b16 v[216:217], v248 offset:45608\n\t" \
  "v_cvt_pknorm_i16_f32 v220, |v146|, |v147|\n\t" \
  "s_setprio 3\n\t" \
  "v_cvt_pknorm_i16_f32 v222, |v144|, |v145|\n\t" \
  "s_waitcnt lgkmcnt(8)\n\t" \
  "v_mfma_f32_16x16x32_bf16 v[148:151], v[208:211], v[96:99], 0\n\t" \
  "ds_read_b64_tr_b16 v[218:219], v248 offset:48680\n\t" \
  "v_lshlrev_b32_sdwa v221, 2, v220 dst_sel:DWORD dst_unused:UNUSED_PAD src0_sel:DWORD src1_sel:BYTE_3\n\t" \
  "s_waitcnt vmcnt(0)\n\t" \
  "v_lshlrev_b32_sdwa v220, 2, v220 dst_sel:DWORD dst_unused:UNUSED_PAD src0_sel:DWORD src1_sel:BYTE_1\n\t" \
  "v_mfma_f32_16x16x32_bf16 v[156:159], v[208:211], v[100:103], 0\n\t" \
  "s_mov_b64 exec, 3\n\t" \
  "ds_add_rtn_u32 v246, v251, v250 offset:47200\n\t" \
  "s_mov_b64 exec, -1\n\t" \
  "v_lshlrev_b32_sdwa v223, 2, v222 dst_sel:DWORD dst_unused:UNUSED_PAD src0_sel:DWORD src1_sel:BYTE_3\n\t" \
  "v_mfma_f32_16x16x32_bf16 v[164:167], v[208:211], v[104:107], 0\n\t" \
  "ds_read_u16 v234, v234 offset:10528\n\t" \
  "v_lshlrev_b32_sdwa v222, 2, v222 dst_sel:DWORD dst_unused:UNUSED_PAD src0_sel:DWORD src1_sel:BYTE_1\n\t" \
  "v_lshlrev_b32_sdwa v225, 1, v155 dst_sel:DWORD dst_unused:UNUSED_PAD src0_sel:DWORD src1_sel:WORD_1\n\t" \
  "v_mfma_f32_16x16x32_bf16 v[172:175], v[208:211], v[108:111], 0\n\t" \
  "ds_read_b32 v221, v221 offset:10016\n\t" \
  "v_lshlrev_b32_sdwa v224, 1, v154 dst_sel:DWORD dst_unused:UNUSED_PAD src0_sel:DWORD src1_sel:WORD_1\n\t" \
  "s_waitcnt lgkmcnt(2)\n\t" \
  "v_readfirstlane_b32 s4, v246\n\t" \
  "v_mfma_f32_16x16x32_bf16 v[148:151], v[212:215], v[112:115], v[148:151]\n\t" \
  "ds_read_b32 v220, v220 offset:10016\n\t" \
  "v_lshlrev_b32_sdwa v227, 1, v153 dst_sel:DWORD dst_unused:UNUSED_PAD src0_sel:DWORD src1_sel:WORD_1\n\t" \
  "s_cmpk_lt_u32 s4, 6144\n\t" \
  "s_cbranch_scc1 .Lcnx_w1_9\n.Lcnx_g1_9:\n\t" \
  "v_mfma_f32_16x16x32_bf16 v[156:159], v[212:215], v[116:119], v[156:159]\n\t" \
  "ds_read_b64_tr_b16 v[208:209], v249 offset:12304\n\t" \
  "ds_read_b32 v223, v223 offset:10016\n\t" \
  "v_lshlrev_b32_sdwa v226, 1, v152 dst_sel:DWORD dst_unused:UNUSED_PAD src0_sel:DWORD src1_sel:WORD_1\n\t" \
  "v_mfma_f32_16x16x32_bf16 v[164:167], v[212:215], v[120:123], v[164:167]\n\t" \
  "ds_read_b64_tr_b16 v[210:211], v249 offset:13840\n\t" \
  "v_fma_mix_f32 v229, |v197|, v229, v229 op_sel:[0,1,0] op_sel_hi:[0,1,1] clamp\n\t" \
  "v_fma_mix_f32 v228, |v196|, v228, v228 op_sel:[0,1,0] op_sel_hi:[0,1,1] clamp\n\t" \
  "v_mfma_f32_16x16x32_bf16 v[172:175], v[212:215], v[124:127], v[172:175]\n\t" \
  "ds_read_b64_tr_b16 v[212:213], v249 offset:12312\n\t" \
  "ds_read_b32 v222, v222 offset:10016\n\t" \
  "v_fma_mix_f32 v231, |v199|, v231, v231 op_sel:[0,1,0] op_sel_hi:[0,1,1] clamp\n\t" \
  "v_fma_mix_f32 v230, |v198|, v230, v230 op_sel:[0,1,0] op_sel_hi:[0,1,1] clamp\n\t" \
  "s_waitcnt lgkmcnt(7)\n\t" \
  "v_mfma_f32_16x16x32_bf16 v[148:151], v[216:219], v[128:131], v[148:151]\n\t" \
  "ds_read_b64_tr_b16 v[214:215], v249 offset:13848\n\t" \
  "v_or_b32 v204, v232, v233\n\t" \
  "v_or_b32 v205, v234, v235\n\t" \
  "v_mfma_f32_16x16x32_bf16 v[156:159], v[216:219], v[132:135], v[156:159]\n\t" \
  "ds_read_u16_d16_hi v225, v225 offset:10528\n\t" \
  "v_fma_f32 v197, |v197|, v229, v197 mul:4\n\t" \
  "v_fma_f32 v196, |v196|, v228, v196 mul:4\n\t" \
  "v_mfma_f32_16x16x32_bf16 v[164:167], v[216:219], v[136:139], v[164:167]\n\t" \
  "ds_read_u16 v224, v224 offset:10528\n\t" \
  "v_fma_f32 v199, |v199|, v231, v199 mul:4\n\t" \
  "v_fma_f32 v198, |v198|, v230, v198 mul:4\n\t" \
  "v_mfma_f32_16x16x32_bf16 v[172:175], v[216:219], v[140:143], v[172:175]\n\t" \
  "ds_read_b64_tr_b16 v[216:217], v249 offset:12368\n\t" \
  "v_cvt_pk_bf16_f32 v196, v196, v197\n\t" \
  "v_cvt_pk_bf16_f32 v197, v198, v199\n\t" \
  "v_mfma_f32_16x16x32_bf16 v[56:59], v[242:245], v[236:239], v[56:59]\n\t" \
  "ds_read_b64_tr_b16 v[218:219], v249 offset:13904\n\t" \
  "v_cvt_pknorm_i16_f32 v228, |v162|, |v163|\n\t" \
  "s_setprio 0\n\t" \
  "s_cmp_ge_u32 s39, 2048\n\t" \
  "s_cbranch_scc1 .Lcnx_p\n\ts_setprio 3\n.Lcnx_p:\n\t" \
  "v_cvt_pknorm_i16_f32 v230, |v160|, |v161|\n\t" \
  "v_mfma_f32_16x16x32_bf16 v[60:63], v[240:243], v[236:239], v[60:63]\n\t" \
  "ds_read_u16_d16_hi v227, v227 offset:10528\n\t" \
  "buffer_load_dwordx4 v[236:239], v250, s[8:11], s14 offen offset:128 nt\n\t" \
  "v_lshlrev_b32_sdwa v229, 2, v228 dst_sel:DWORD dst_unused:UNUSED_PAD src0_sel:DWORD src1_sel:BYTE_3\n\t" \
  "s_waitcnt lgkmcnt(8)\n\t" \
  "v_mfma_f32_16x16x32_bf16 v[0:3], v[208:211], v[178:181], v[0:3]\n\t" \
  "ds_read_u16 v226, v226 offset:10528\n\t" \
  "v_lshlrev_b32_sdwa v228, 2, v228 dst_sel:DWORD dst_unused:UNUSED_PAD src0_sel:DWORD src1_sel:BYTE_1\n\t" \
  "v_lshlrev_b32_sdwa v231, 2, v230 dst_sel:DWORD dst_unused:UNUSED_PAD src0_sel:DWORD src1_sel:BYTE_3\n\t" \
  "v_mfma_f32_16x16x32_bf16 v[24:27], v[208:211], v[186:189], v[24:27]\n\t" \
  "ds_read_b32 v229, v229 offset:10016\n\t" \
  "v_lshlrev_b32_sdwa v230, 2, v230 dst_sel:DWORD dst_unused:UNUSED_PAD src0_sel:DWORD src1_sel:BYTE_1\n\t" \
  "v_lshlrev_b32_sdwa v233, 1, v171 dst_sel:DWORD dst_unused:UNUSED_PAD src0_sel:DWORD src1_sel:WORD_1\n\t" \
  "v_mfma_f32_16x16x32_bf16 v[48:51], v[208:211], v[194:197], v[48:51]\n\t" \
  "ds_read_b32 v228, v228 offset:10016\n\t" \
  "v_lshlrev_b32_sdwa v232, 1, v170 dst_sel:DWORD dst_unused:UNUSED_PAD src0_sel:DWORD src1_sel:WORD_1\n\t" \
  "v_lshlrev_b32_sdwa v235, 1, v169 dst_sel:DWORD dst_unused:UNUSED_PAD src0_sel:DWORD src1_sel:WORD_1\n\t" \
  "v_mfma_f32_16x16x32_bf16 v[72:75], v[208:211], v[202:205], v[72:75]\n\t" \
  "ds_read_b64_tr_b16 v[208:209], v249 offset:12376\n\t" \
  "ds_read_b32 v231, v231 offset:10016\n\t" \
  "v_lshlrev_b32_sdwa v234, 1, v168 dst_sel:DWORD dst_unused:UNUSED_PAD src0_sel:DWORD src1_sel:WORD_1\n\t" \
  "s_waitcnt lgkmcnt(10)\n\t" \
  "v_mfma_f32_16x16x32_bf16 v[4:7], v[212:215], v[178:181], v[4:7]\n\t" \
  "ds_read_b64_tr_b16 v[210:211], v249 offset:13912\n\t" \
  "v_fma_mix_f32 v221, |v147|, v221, v221 op_sel:[0,1,0] op_sel_hi:[0,1,1] clamp\n\t" \
  "v_fma_mix_f32 v220, |v146|, v220, v220 op_sel:[0,1,0] op_sel_hi:[0,1,1] clamp\n\t" \
  "v_mfma_f32_16x16x32_bf16 v[28:31], v[212:215], v[186:189], v[28:31]\n\t" \
  "ds_read_b32 v230, v230 offset:10016\n\t" \
  "v_fma_mix_f32 v223, |v145|, v223, v223 op_sel:[0,1,0] op_sel_hi:[0,1,1] clamp\n\t" \
  "v_fma_mix_f32 v222, |v144|, v222, v222 op_sel:[0,1,0] op_sel_hi:[0,1,1] clamp\n\t" \
  "s_waitcnt lgkmcnt(6)\n\t" \
  "v_mfma_f32_16x16x32_bf16 v[52:55], v[212:215], v[194:197], v[52:55]\n\t" \
  "ds_read_u16_d16_hi v233, v233 offset:10528\n\t" \
  "v_or_b32 v155, v224, v225\n\t" \
  "v_or_b32 v154, v226, v227\n\t" \
  "v_mfma_f32_16x16x32_bf16 v[76:79], v[212:215], v[202:205], v[76:79]\n\t" \
  "ds_read_b64_tr_b16 v[212:213], v249 offset:12432\n\t" \
  "v_fma_f32 v147, |v147|, v221, v147 mul:4\n\t" \
  "v_fma_f32 v146, |v146|, v220, v146 mul:4\n\t" \
  "v_mfma_f32_16x16x32_bf16 v[8:11], v[216:219], v[178:181], v[8:11]\n\t" \
  "ds_read_b64_tr_b16 v[214:215], v249 offset:13968\n\t" \
  "v_fma_f32 v145, |v145|, v223, v145 mul:4\n\t" \
  "v_fma_f32 v144, |v144|, v222, v144 mul:4\n\t" \
  "v_mfma_f32_16x16x32_bf16 v[32:35], v[216:219], v[186:189], v[32:35]\n\t" \
  "ds_read_u16 v232, v232 offset:10528\n\t" \
  "v_cvt_pk_bf16_f32 v147, v146, v147\n\t" \
  "v_cvt_pk_bf16_f32 v146, v144, v145\n\t" \
  "v_mfma_f32_16x16x32_bf16 v[56:59], v[216:219], v[194:197], v[56:59]\n\t" \
  "ds_read_u16_d16_hi v235, v235 offset:10528\n\t" \
  "v_cvt_pknorm_i16_f32 v220, |v148|, |v149|\n\t" \
  "v_cvt_pknorm_i16_f32 v222, |v150|, |v151|\n\t" \
  "v_mfma_f32_16x16x32_bf16 v[80:83], v[216:219], v[202:205], v[80:83]\n\t" \
  "ds_read_b64_tr_b16 v[216:217], v249 offset:12440\n\t" \
  "v_lshlrev_b32_sdwa v221, 2, v220 dst_sel:DWORD dst_unused:UNUSED_PAD src0_sel:DWORD src1_sel:BYTE_3\n\t" \
  "v_lshlrev_b32_sdwa v220, 2, v220 dst_sel:DWORD dst_unused:UNUSED_PAD src0_sel:DWORD src1_sel:BYTE_1\n\t" \
  "s_waitcnt lgkmcnt(7)\n\t" \
  "v_mfma_f32_16x16x32_bf16 v[12:15], v[208:211], v[178:181], v[12:15]\n\t" \
  "ds_read_b64_tr_b16 v[218:219], v249 offset:13976\n\t" \
  "v_lshlrev_b32_sdwa v223, 2, v222 dst_sel:DWORD dst_unused:UNUSED_PAD src0_sel:DWORD src1_sel:BYTE_3\n\t" \
  "v_lshlrev_b32_sdwa v222, 2, v222 dst_sel:DWORD dst_unused:UNUSED_PAD src0_sel:DWORD src1_sel:BYTE_1\n\t" \
  "v_mfma_f32_16x16x32_bf16 v[36:39], v[208:211], v[186:189], v[36:39]\n\t" \
  "ds_read_u16 v234, v234 offset:10528\n\t" \
  "v_lshlrev_b32_sdwa v225, 1, v157 dst_sel:DWORD dst_unused:UNUSED_PAD src0_sel:DWORD src1_sel:WORD_1\n\t" \
  "v_mfma_f32_16x16x32_bf16 v[60:63], v[208:211], v[194:197], v[60:63]\n\t" \
  "ds_read_b32 v221, v221 offset:10016\n\t" \
  "v_lshlrev_b32_sdwa v224, 1, v156 dst_sel:DWORD dst_unused:UNUSED_PAD src0_sel:DWORD src1_sel:WORD_1\n\t" \
  "v_lshlrev_b32_sdwa v227, 1, v159 dst_sel:DWORD dst_unused:UNUSED_PAD src0_sel:DWORD src1_sel:WORD_1\n\t" \
  "v_mfma_f32_16x16x32_bf16 v[84:87], v[208:211], v[202:205], v[84:87]\n\t" \
  "ds_read_b64_tr_b16 v[208:209], v248 offset:49192\n\t" \
  "ds_read_b32 v220, v220 offset:10016\n\t" \
  "v_lshlrev_b32_sdwa v226, 1, v158 dst_sel:DWORD dst_unused:UNUSED_PAD src0_sel:DWORD src1_sel:WORD_1\n\t" \
  "s_waitcnt lgkmcnt(8)\n\t" \
  "v_mfma_f32_16x16x32_bf16 v[16:19], v[212:215], v[178:181], v[16:19]\n\t" \
  "ds_read_b64_tr_b16 v[210:211], v248 offset:50216\n\t" \
  "ds_read_b32 v223, v223 offset:10016\n\t" \
  "v_fma_mix_f32 v229, |v163|, v229, v229 op_sel:[0,1,0] op_sel_hi:[0,1,1] clamp\n\t" \
  "v_mfma_f32_16x16x32_bf16 v[40:43], v[212:215], v[186:189], v[40:43]\n\t" \
  "ds_read_b32 v222, v222 offset:10016\n\t" \
  "v_fma_mix_f32 v228, |v162|, v228, v228 op_sel:[0,1,0] op_sel_hi:[0,1,1] clamp\n\t" \
  "v_fma_mix_f32 v231, |v161|, v231, v231 op_sel:[0,1,0] op_sel_hi:[0,1,1] clamp\n\t" \
  "v_mfma_f32_16x16x32_bf16 v[64:67], v[212:215], v[194:197], v[64:67]\n\t" \
  "ds_read_u16_d16_hi v225, v225 offset:10528\n\t" \
  "v_fma_mix_f32 v230, |v160|, v230, v230 op_sel:[0,1,0] op_sel_hi:[0,1,1] clamp\n\t" \
  "s_waitcnt lgkmcnt(7)\n\t" \
  "v_or_b32 v171, v232, v233\n\t" \
  "v_mfma_f32_16x16x32_bf16 v[88:91], v[212:215], v[202:205], v[88:91]\n\t" \
  "ds_read_b64_tr_b16 v[212:213], v248 offset:52264\n\t" \
  "v_or_b32 v170, v234, v235\n\t" \
  "v_fma_f32 v163, |v163|, v229, v163 mul:4\n\t" \
  "v_mfma_f32_16x16x32_bf16 v[20:23], v[216:219], v[178:181], v[20:23]\n\t" \
  "ds_read_b64_tr_b16 v[214:215], v248 offset:53288\n\t" \
  "v_fma_f32 v162, |v162|, v228, v162 mul:4\n\t" \
  "v_fma_f32 v161, |v161|, v231, v161 mul:4\n\t" \
  "v_mfma_f32_16x16x32_bf16 v[44:47], v[216:219], v[186:189], v[44:47]\n\t" \
  "ds_read_u16 v224, v224 offset:10528\n\t" \
  "v_fma_f32 v160, |v160|, v230, v160 mul:4\n\t" \
  "v_cvt_pk_bf16_f32 v163, v162, v163\n\t" \
  "v_mfma_f32_16x16x32_bf16 v[68:71], v[216:219], v[194:197], v[68:71]\n\t" \
  "ds_read_u16_d16_hi v227, v227 offset:10528\n\t" \
  "v_cvt_pk_bf16_f32 v162, v160, v161\n\t" \
  "v_cvt_pknorm_i16_f32 v228, |v164|, |v165|\n\t" \
  "v_mfma_f32_16x16x32_bf16 v[92:95], v[216:219], v[202:205], v[92:95]\n\t" \
  "ds_read_b64_tr_b16 v[216:217], v248 offset:51240\n\t" \
  "v_cvt_pknorm_i16_f32 v230, |v166|, |v167|\n\t" \
  "v_lshlrev_b32_sdwa v229, 2, v228 dst_sel:DWORD dst_unused:UNUSED_PAD src0_sel:DWORD src1_sel:BYTE_3\n\t" \
  "s_waitcnt lgkmcnt(8)\n\t" \
  "v_mfma_f32_16x16x32_bf16 v[176:179], v[208:211], v[96:99], 0\n\t" \
  "ds_read_b64_tr_b16 v[218:219], v248 offset:54312\n\t" \
  "v_lshlrev_b32_sdwa v228, 2, v228 dst_sel:DWORD dst_unused:UNUSED_PAD src0_sel:DWORD src1_sel:BYTE_1\n\t" \
  "v_lshlrev_b32_sdwa v231, 2, v230 dst_sel:DWORD dst_unused:UNUSED_PAD src0_sel:DWORD src1_sel:BYTE_3\n\t" \
  "v_mfma_f32_16x16x32_bf16 v[184:187], v[208:211], v[100:103], 0\n\t" \
  "ds_read_u16 v226, v226 offset:10528\n\t" \
  "v_lshlrev_b32_sdwa v230, 2, v230 dst_sel:DWORD dst_unused:UNUSED_PAD src0_sel:DWORD src1_sel:BYTE_1\n\t" \
  "v_lshlrev_b32_sdwa v233, 1, v173 dst_sel:DWORD dst_unused:UNUSED_PAD src0_sel:DWORD src1_sel:WORD_1\n\t" \
  "v_mfma_f32_16x16x32_bf16 v[192:195], v[208:211], v[104:107], 0\n\t" \
  "ds_read_b32 v229, v229 offset:10016\n\t" \
  "v_lshlrev_b32_sdwa v232, 1, v172 dst_sel:DWORD dst_unused:UNUSED_PAD src0_sel:DWORD src1_sel:WORD_1\n\t" \
  "v_lshlrev_b32_sdwa v235, 1, v175 dst_sel:DWORD dst_unused:UNUSED_PAD src0_sel:DWORD src1_sel:WORD_1\n\t" \
  "v_mfma_f32_16x16x32_bf16 v[200:203], v[208:211], v[108:111], 0\n\t" \
  "ds_read_b64_tr_b16 v[208:209], v248 offset:49704\n\t" \
  "ds_read_b32 v228, v228 offset:10016\n\t" \
  "v_lshlrev_b32_sdwa v234, 1, v174 dst_sel:DWORD dst_unused:UNUSED_PAD src0_sel:DWORD src1_sel:WORD_1\n\t" \
  "s_waitcnt lgkmcnt(8)\n\t" \
  "v_mfma_f32_16x16x32_bf16 v[176:179], v[212:215], v[112:115], v[176:179]\n\t" \
  "ds_read_b64_tr_b16 v[210:211], v248 offset:50728\n\t" \
  "ds_read_b32 v231, v231 offset:10016\n\t" \
  "v_fma_mix_f32 v221, |v149|, v221, v221 op_sel:[0,1,0] op_sel_hi:[0,1,1] clamp\n\t" \
  "v_mfma_f32_16x16x32_bf16 v[184:187], v[212:215], v[116:119], v[184:187]\n\t" \
  "ds_read_b32 v230, v230 offset:10016\n\t" \
  "v_fma_mix_f32 v220, |v148|, v220, v220 op_sel:[0,1,0] op_sel_hi:[0,1,1] clamp\n\t" \
  "v_fma_mix_f32 v223, |v151|, v223, v223 op_sel:[0,1,0] op_sel_hi:[0,1,1] clamp\n\t" \
  "v_mfma_f32_16x16x32_bf16 v[192:195], v[212:215], v[120:123], v[192:195]\n\t" \
  "ds_read_u16_d16_hi v233, v233 offset:10528\n\t" \
  "v_fma_mix_f32 v222, |v150|, v222, v222 op_sel:[0,1,0] op_sel_hi:[0,1,1] clamp\n\t" \
  "s_waitcnt lgkmcnt(7)\n\t" \
  "v_or_b32 v156, v224, v225\n\t" \
  "v_mfma_f32_16x16x32_bf16 v[200:203], v[212:215], v[124:127], v[200:203]\n\t" \
  "ds_read_b64_tr_b16 v[212:213], v248 offset:52776\n\t" \
  "v_or_b32 v157, v226, v227\n\t" \
  "v_mfma_f32_16x16x32_bf16 v[176:179], v[216:219], v[128:131], v[176:179]\n\t" \
  "ds_read_b64_tr_b16 v[214:215], v248 offset:53800\n\t" \
  "v_fma_f32 v149, |v149|, v221, v149 mul:4\n\t" \
  "v_fma_f32 v148, |v148|, v220, v148 mul:4\n\t" \
  "v_mfma_f32_16x16x32_bf16 v[184:187], v[216:219], v[132:135], v[184:187]\n\t" \
  "ds_read_u16 v232, v232 offset:10528\n\t" \
  "v_fma_f32 v151, |v151|, v223, v151 mul:4\n\t" \
  "v_fma_f32 v150, |v150|, v222, v150 mul:4\n\t" \
  "v_mfma_f32_16x16x32_bf16 v[192:195], v[216:219], v[136:139], v[192:195]\n\t" \
  "ds_read_u16_d16_hi v235, v235 offset:10528\n\t" \
  "v_cvt_pk_bf16_f32 v148, v148, v149\n\t" \
  "v_cvt_pk_bf16_f32 v149, v150, v151\n\t" \
  "v_mfma_f32_16x16x32_bf16 v[200:203], v[216:219], v[140:143], v[200:203]\n\t" \
  "ds_read_b64_tr_b16 v[216:217], v248 offset:51752\n\t" \
  "v_cvt_pknorm_i16_f32 v220, |v178|, |v179|\n\t" \
  "v_cvt_pknorm_i16_f32 v222, |v176|, |v177|\n\t" \
  "s_waitcnt lgkmcnt(8)\n\t" \
  "v_mfma_f32_16x16x32_bf16 v[180:183], v[208:211], v[96:99], 0\n\t" \
  "ds_read_b64_tr_b16 v[218:219], v248 offset:54824\n\t" \
  "v_lshlrev_b32_sdwa v221, 2, v220 dst_sel:DWORD dst_unused:UNUSED_PAD src0_sel:DWORD src1_sel:BYTE_3\n\t" \
  "s_waitcnt vmcnt(0)\n\t" \
  "v_lshlrev_b32_sdwa v220, 2, v220 dst_sel:DWORD dst_unused:UNUSED_PAD src0_sel:DWORD src1_sel:BYTE_1\n\t" \
  "v_mfma_f32_16x16x32_bf16 v[188:191], v[208:211], v[100:103], 0\n\t" \
  "ds_read_u16 v234, v234 offset:10528\n\t" \
  "v_lshlrev_b32_sdwa v223, 2, v222 dst_sel:DWORD dst_unused:UNUSED_PAD src0_sel:DWORD src1_sel:BYTE_3\n\t" \
  "v_lshlrev_b32_sdwa v222, 2, v222 dst_sel:DWORD dst_unused:UNUSED_PAD src0_sel:DWORD src1_sel:BYTE_1\n\t" \
  "v_mfma_f32_16x16x32_bf16 v[196:199], v[208:211], v[104:107], 0\n\t" \
  "ds_read_b32 v221, v221 offset:10016\n\t" \
  "v_lshlrev_b32_sdwa v225, 1, v187 dst_sel:DWORD dst_unused:UNUSED_PAD src0_sel:DWORD src1_sel:WORD_1\n\t" \
  "v_lshlrev_b32_sdwa v224, 1, v186 dst_sel:DWORD dst_unused:UNUSED_PAD src0_sel:DWORD src1_sel:WORD_1\n\t" \
  "v_mfma_f32_16x16x32_bf16 v[204:207], v[208:211], v[108:111], 0\n\t" \
  "ds_read_b32 v220, v220 offset:10016\n\t" \
  "v_lshlrev_b32_sdwa v227, 1, v185 dst_sel:DWORD dst_unused:UNUSED_PAD src0_sel:DWORD src1_sel:WORD_1\n\t" \
  "v_lshlrev_b32_sdwa v226, 1, v184 dst_sel:DWORD dst_unused:UNUSED_PAD src0_sel:DWORD src1_sel:WORD_1\n\t" \
  "s_waitcnt lgkmcnt(7)\n\t" \
  "v_mfma_f32_16x16x32_bf16 v[180:183], v[212:215], v[112:115], v[180:183]\n\t" \
  "ds_read_b32 v223, v223 offset:10016\n\t" \
  "v_add_u32 v248, -81480, v248\n\t" \
  "v_add_u32 v249, -81480, v249\n\t" \
  "v_mfma_f32_16x16x32_bf16 v[188:191], v[212:215], v[116:119], v[188:191]\n\t" \
  "ds_read_b64_tr_b16 v[208:209], v249 offset:24600\n\t" \
  "ds_read_b32 v222, v222 offset:10016\n\t" \
  "v_fma_mix_f32 v229, |v165|, v229, v229 op_sel:[0,1,0] op_sel_hi:[0,1,1] clamp\n\t" \
  "v_mfma_f32_16x16x32_bf16 v[196:199], v[212:215], v[120:123], v[196:199]\n\t" \
  "ds_read_b64_tr_b16 v[210:211], v249 offset:26136\n\t" \
  "v_fma_mix_f32 v228, |v164|, v228, v228 op_sel:[0,1,0] op_sel_hi:[0,1,1] clamp\n\t" \
  "v_fma_mix_f32 v231, |v167|, v231, v231 op_sel:[0,1,0] op_sel_hi:[0,1,1] clamp\n\t" \
  "v_mfma_f32_16x16x32_bf16 v[204:207], v[212:215], v[124:127], v[204:207]\n\t" \
  "ds_read_b64_tr_b16 v[212:213], v249 offset:24608\n\t" \
  "ds_read_u16_d16_hi v225, v225 offset:10528\n\t" \
  "v_fma_mix_f32 v230, |v166|, v230, v230 op_sel:[0,1,0] op_sel_hi:[0,1,1] clamp\n\t" \
  "s_waitcnt lgkmcnt(8)\n\t" \
  "v_mfma_f32_16x16x32_bf16 v[180:183], v[216:219], v[128:131], v[180:183]\n\t" \
  "ds_read_b64_tr_b16 v[214:215], v249 offset:26144\n\t" \
  "v_or_b32 v172, v232, v233\n\t" \
  "v_or_b32 v173, v234, v235\n\t" \
  "v_mfma_f32_16x16x32_bf16 v[188:191], v[216:219], v[132:135], v[188:191]\n\t" \
  "ds_read_u16 v224, v224 offset:10528\n\t" \
  "v_fma_f32 v165, |v165|, v229, v165 mul:4\n\t" \
  "v_fma_f32 v164, |v164|, v228, v164 mul:4\n\t" \
  "v_mfma_f32_16x16x32_bf16 v[196:199], v[216:219], v[136:139], v[196:199]\n\t" \
  "ds_read_u16_d16_hi v227, v227 offset:10528\n\t" \
  "v_fma_f32 v167, |v167|, v231, v167 mul:4\n\t" \
  "v_fma_f32 v166, |v166|, v230, v166 mul:4\n\t" \
  "v_mfma_f32_16x16x32_bf16 v[204:207], v[216:219], v[140:143], v[204:207]\n\t" \
  "ds_read_b64_tr_b16 v[216:217], v249 offset:24664\n\t" \
  "v_cvt_pk_bf16_f32 v164, v164, v165\n\t" \
  "v_cvt_pk_bf16_f32 v165, v166, v167\n\t" \
  "v_mfma_f32_16x16x32_bf16 v[64:67], v[242:245], v[236:239], v[64:67]\n\t" \
  "ds_read_b64_tr_b16 v[218:219], v249 offset:26200\n\t" \
  "v_cvt_pknorm_i16_f32 v228, |v194|, |v195|\n\t" \
  "v_cvt_pknorm_i16_f32 v230, |v192|, |v193|\n\t" \
  "v_mfma_f32_16x16x32_bf16 v[68:71], v[240:243], v[236:239], v[68:71]\n\t" \
  "ds_read_u16 v226, v226 offset:10528\n\t" \
  "buffer_load_dwordx4 v[236:239], v250, s[8:11], s15 offen offset:0 nt\n\t" \
  "v_lshlrev_b32_sdwa v229, 2, v228 dst_sel:DWORD dst_unused:UNUSED_PAD src0_sel:DWORD src1_sel:BYTE_3\n\t" \
  "s_waitcnt lgkmcnt(8)\n\t" \
  "v_mfma_f32_16x16x32_bf16 v[0:3], v[208:211], v[146:149], v[0:3]\n\t" \
  "v_lshlrev_b32_sdwa v228, 2, v228 dst_sel:DWORD dst_unused:UNUSED_PAD src0_sel:DWORD src1_sel:BYTE_1\n\t" \
  "v_lshlrev_b32_sdwa v231, 2, v230 dst_sel:DWORD dst_unused:UNUSED_PAD src0_sel:DWORD src1_sel:BYTE_3\n\t" \
  "v_mfma_f32_16x16x32_bf16 v[24:27], v[208:211], v[154:157], v[24:27]\n\t" \
  "ds_read_b32 v229, v229 offset:10016\n\t" \
  "v_lshlrev_b32_sdwa v230, 2, v230 dst_sel:DWORD dst_unused:UNUSED_PAD src0_sel:DWORD src1_sel:BYTE_1\n\t" \
  "v_lshlrev_b32_sdwa v233, 1, v203 dst_sel:DWORD dst_unused:UNUSED_PAD src0_sel:DWORD src1_sel:WORD_1\n\t" \
  "v_mfma_f32_16x16x32_bf16 v[48:51], v[208:211], v[162:165], v[48:51]\n\t" \
  "ds_read_b32 v228, v228 offset:10016\n\t" \
  "v_lshlrev_b32_sdwa v232, 1, v202 dst_sel:DWORD dst_unused:UNUSED_PAD src0_sel:DWORD src1_sel:WORD_1\n\t" \
  "v_lshlrev_b32_sdwa v235, 1, v201 dst_sel:DWORD dst_unused:UNUSED_PAD src0_sel:DWORD src1_sel:WORD_1\n\t" \
  "v_mfma_f32_16x16x32_bf16 v[72:75], v[208:211], v[170:173], v[72:75]\n\t" \
  "ds_read_b64_tr_b16 v[208:209], v249 offset:24672\n\t" \
  "ds_read_b32 v231, v231 offset:10016\n\t" \
  "v_lshlrev_b32_sdwa v234, 1, v200 dst_sel:DWORD dst_unused:UNUSED_PAD src0_sel:DWORD src1_sel:WORD_1\n\t" \
  "s_waitcnt lgkmcnt(9)\n\t" \
  "v_mfma_f32_16x16x32_bf16 v[4:7], v[212:215], v[146:149], v[4:7]\n\t" \
  "ds_read_b64_tr_b16 v[210:211], v249 offset:26208\n\t" \
  "v_fma_mix_f32 v221, |v179|, v221, v221 op_sel:[0,1,0] op_sel_hi:[0,1,1] clamp\n\t" \
  "v_fma_mix_f32 v220, |v178|, v220, v220 op_sel:[0,1,0] op_sel_hi:[0,1,1] clamp\n\t" \
  "v_mfma_f32_16x16x32_bf16 v[28:31], v[212:215], v[154:157], v[28:31]\n\t" \
  "ds_read_b32 v230, v230 offset:10016\n\t" \
  "v_fma_mix_f32 v223, |v177|, v223, v223 op_sel:[0,1,0] op_sel_hi:[0,1,1] clamp\n\t" \
  "v_fma_mix_f32 v222, |v176|, v222, v222 op_sel:[0,1,0] op_sel_hi:[0,1,1] clamp\n\t" \
  "s_waitcnt lgkmcnt(6)\n\t" \
  "v_mfma_f32_16x16x32_bf16 v[52:55], v[212:215], v[162:165], v[52:55]\n\t" \
  "ds_read_u16_d16_hi v233, v233 offset:10528\n\t" \
  "v_or_b32 v187, v224, v225\n\t" \
  "v_or_b32 v186, v226, v227\n\t" \
  "v_mfma_f32_16x16x32_bf16 v[76:79], v[212:215], v[170:173], v[76:79]\n\t" \
  "ds_read_b64_tr_b16 v[212:213], v249 offset:24728\n\t" \
  "v_fma_f32 v179, |v179|, v221, v179 mul:4\n\t" \
  "v_fma_f32 v178, |v178|, v220, v178 mul:4\n\t" \
  "v_mfma_f32_16x16x32_bf16 v[8:11], v[216:219], v[146:149], v[8:11]\n\t" \
  "ds_read_b64_tr_b16 v[214:215], v249 offset:26264\n\t" \
  "v_fma_f32 v177, |v177|, v223, v177 mul:4\n\t" \
  "v_fma_f32 v176, |v176|, v222, v176 mul:4\n\t" \
  "v_mfma_f32_16x16x32_bf16 v[32:35], v[216:219], v[154:157], v[32:35]\n\t" \
  "ds_read_u16 v232, v232 offset:10528\n\t" \
  "v_cvt_pk_bf16_f32 v179, v178, v179\n\t" \
  "v_cvt_pk_bf16_f32 v178, v176, v177\n\t" \
  "v_mfma_f32_16x16x32_bf16 v[56:59], v[216:219], v[162:165], v[56:59]\n\t" \
  "ds_read_u16_d16_hi v235, v235 offset:10528\n\t" \
  "v_cvt_pknorm_i16_f32 v220, |v180|, |v181|\n\t" \
  "v_mfma_f32_16x16x32_bf16 v[80:83], v[216:219], v[170:173], v[80:83]\n\t" \
  "ds_read_b64_tr_b16 v[216:217], v249 offset:24736\n\t" \
  "v_cvt_pknorm_i16_f32 v222, |v182|, |v183|\n\t" \
  "v_lshlrev_b32_sdwa v221, 2, v220 dst_sel:DWORD dst_unused:UNUSED_PAD src0_sel:DWORD src1_sel:BYTE_3\n\t" \
  "s_waitcnt lgkmcnt(7)\n\t" \
  "v_mfma_f32_16x16x32_bf16 v[12:15], v[208:211], v[146:149], v[12:15]\n\t" \
  "ds_read_b64_tr_b16 v[218:219], v249 offset:26272\n\t" \
  "v_lshlrev_b32_sdwa v220, 2, v220 dst_sel:DWORD dst_unused:UNUSED_PAD src0_sel:DWORD src1_sel:BYTE_1\n\t" \
  "v_lshlrev_b32_sdwa v223, 2, v222 dst_sel:DWORD dst_unused:UNUSED_PAD src0_sel:DWORD src1_sel:BYTE_3\n\t" \
  "v_mfma_f32_16x16x32_bf16 v[36:39], v[208:211], v[154:157], v[36:39]\n\t" \
  "ds_read_u16 v234, v234 offset:10528\n\t" \
  "v_lshlrev_b32_sdwa v222, 2, v222 dst_sel:DWORD dst_unused:UNUSED_PAD src0_sel:DWORD src1_sel:BYTE_1\n\t" \
  "v_lshlrev_b32_sdwa v225, 1, v189 dst_sel:DWORD dst_unused:UNUSED_PAD src0_sel:DWORD src1_sel:WORD_1\n\t" \
  "v_mfma_f32_16x16x32_bf16 v[60:63], v[208:211], v[162:165], v[60:63]\n\t" \
  "ds_read_b32 v221, v221 offset:10016\n\t" \
  "v_lshlrev_b32_sdwa v224, 1, v188 dst_sel:DWORD dst_unused:UNUSED_PAD src0_sel:DWORD src1_sel:WORD_1\n\t" \
  "v_mfma_f32_16x16x32_bf16 v[84:87], v[208:211], v[170:173], v[84:87]\n\t" \
  "ds_read_b64_tr_b16 v[208:209], v248 offset:36904\n\t" \
  "ds_read_b32 v220, v220 offset:10016\n\t" \
  "v_lshlrev_b32_sdwa v227, 1, v191 dst_sel:DWORD dst_unused:UNUSED_PAD src0_sel:DWORD src1_sel:WORD_1\n\t" \
  "s_waitcnt lgkmcnt(8)\n\t" \
  "v_mfma_f32_16x16x32_bf16 v[16:19], v[212:215], v[146:149], v[16:19]\n\t" \
  "ds_read_b64_tr_b16 v[210:211], v248 offset:37928\n\t" \
  "v_lshlrev_b32_sdwa v226, 1, v190 dst_sel:DWORD dst_unused:UNUSED_PAD src0_sel:DWORD src1_sel:WORD_1\n\t" \
  "v_fma_mix_f32 v229, |v195|, v229, v229 op_sel:[0,1,0] op_sel_hi:[0,1,1] clamp\n\t" \
  "v_mfma_f32_16x16x32_bf16 v[40:43], v[212:215], v[154:157], v[40:43]\n\t" \
  "ds_read_b32 v223, v223 offset:10016\n\t" \
  "ds_read_b32 v222, v222 offset:10016\n\t" \
  "v_fma_mix_f32 v228, |v194|, v228, v228 op_sel:[0,1,0] op_sel_hi:[0,1,1] clamp\n\t" \
  "v_mfma_f32_16x16x32_bf16 v[64:67], v[212:215], v[162:165], v[64:67]\n\t" \
  "ds_read_u16_d16_hi v225, v225 offset:10528\n\t" \
  "v_fma_mix_f32 v231, |v193|, v231, v231 op_sel:[0,1,0] op_sel_hi:[0,1,1] clamp\n\t" \
  "v_fma_mix_f32 v230, |v192|, v230, v230 op_sel:[0,1,0] op_sel_hi:[0,1,1] clamp\n\t" \
  "s_waitcnt lgkmcnt(7)\n\t" \
  "v_mfma_f32_16x16x32_bf16 v[88:91], v[212:215], v[170:173], v[88:91]\n\t" \
  "ds_read_b64_tr_b16 v[212:213], v248 offset:39976\n\t" \
  "v_or_b32 v203, v232, v233\n\t" \
  "v_or_b32 v202, v234, v235\n\t" \
  "v_mfma_f32_16x16x32_bf16 v[20:23], v[216:219], v[146:149], v[20:23]\n\t" \
  "ds_read_b64_tr_b16 v[214:215], v248 offset:41000\n\t" \
  "v_fma_f32 v195, |v195|, v229, v195 mul:4\n\t" \
  "v_fma_f32 v194, |v194|, v228, v194 mul:4\n\t" \
  "v_mfma_f32_16x16x32_bf16 v[44:47], v[216:219], v[154:157], v[44:47]\n\t" \
  "ds_read_u16 v224, v224 offset:10528\n\t" \
  "v_fma_f32 v193, |v193|, v231, v193 mul:4\n\t" \
  "v_fma_f32 v192, |v192|, v230, v192 mul:4\n\t" \
  "v_mfma_f32_16x16x32_bf16 v[68:71], v[216:219], v[162:165], v[68:71]\n\t" \
  "ds_read_u16_d16_hi v227, v227 offset:10528\n\t" \
  "v_cvt_pk_bf16_f32 v195, v194, v195\n\t" \
  "v_cvt_pk_bf16_f32 v194, v192, v193\n\t" \
  "v_mfma_f32_16x16x32_bf16 v[92:95], v[216:219], v[170:173], v[92:95]\n\t" \
  "ds_read_b64_tr_b16 v[216:217], v248 offset:38952\n\t" \
  "v_cvt_pknorm_i16_f32 v228, |v196|, |v197|\n\t" \
  "v_cvt_pknorm_i16_f32 v230, |v198|, |v199|\n\t" \
  "s_waitcnt lgkmcnt(8)\n\t" \
  "v_mfma_f32_16x16x32_bf16 v[144:147], v[208:211], v[96:99], 0\n\t" \
  "ds_read_b64_tr_b16 v[218:219], v248 offset:42024\n\t" \
  "v_lshlrev_b32_sdwa v229, 2, v228 dst_sel:DWORD dst_unused:UNUSED_PAD src0_sel:DWORD src1_sel:BYTE_3\n\t" \
  "v_lshlrev_b32_sdwa v228, 2, v228 dst_sel:DWORD dst_unused:UNUSED_PAD src0_sel:DWORD src1_sel:BYTE_1\n\t" \
  "v_mfma_f32_16x16x32_bf16 v[152:155], v[208:211], v[100:103], 0\n\t" \
  "ds_read_u16 v226, v226 offset:10528\n\t" \
  "v_lshlrev_b32_sdwa v231, 2, v230 dst_sel:DWORD dst_unused:UNUSED_PAD src0_sel:DWORD src1_sel:BYTE_3\n\t" \
  "v_lshlrev_b32_sdwa v230, 2, v230 dst_sel:DWORD dst_unused:UNUSED_PAD src0_sel:DWORD src1_sel:BYTE_1\n\t" \
  "v_mfma_f32_16x16x32_bf16 v[160:163], v[208:211], v[104:107], 0\n\t" \
  "ds_read_b32 v229, v229 offset:10016\n\t" \
  "v_lshlrev_b32_sdwa v233, 1, v205 dst_sel:DWORD dst_unused:UNUSED_PAD src0_sel:DWORD src1_sel:WORD_1\n\t" \
  "v_lshlrev_b32_sdwa v232, 1, v204 dst_sel:DWORD dst_unused:UNUSED_PAD src0_sel:DWORD src1_sel:WORD_1\n\t" \
  "v_mfma_f32_16x16x32_bf16 v[168:171], v[208:211], v[108:111], 0\n\t" \
  "ds_read_b64_tr_b16 v[208:209], v248 offset:37416\n\t" \
  "ds_read_b32 v228, v228 offset:10016\n\t" \
  "v_lshlrev_b32_sdwa v235, 1, v207 dst_sel:DWORD dst_unused:UNUSED_PAD src0_sel:DWORD src1_sel:WORD_1\n\t" \
  "s_waitcnt lgkmcnt(8)\n\t" \
  "v_mfma_f32_16x16x32_bf16 v[144:147], v[212:215], v[112:115], v[144:147]\n\t" \
  "ds_read_b64_tr_b16 v[210:211], v248 offset:38440\n\t" \
  "v_lshlrev_b32_sdwa v234, 1, v206 dst_sel:DWORD dst_unused:UNUSED_PAD src0_sel:DWORD src1_sel:WORD_1\n\t" \
  "v_fma_mix_f32 v221, |v181|, v221, v221 op_sel:[0,1,0] op_sel_hi:[0,1,1] clamp\n\t" \
  "v_mfma_f32_16x16x32_bf16 v[152:155], v[212:215], v[116:119], v[152:155]\n\t" \
  "ds_read_b32 v231, v231 offset:10016\n\t" \
  "ds_read_b32 v230, v230 offset:10016\n\t" \
  "v_fma_mix_f32 v220, |v180|, v220, v220 op_sel:[0,1,0] op_sel_hi:[0,1,1] clamp\n\t" \
  "v_mfma_f32_16x16x32_bf16 v[160:163], v[212:215], v[120:123], v[160:163]\n\t" \
  "ds_read_u16_d16_hi v233, v233 offset:10528\n\t" \
  "v_fma_mix_f32 v223, |v183|, v223, v223 op_sel:[0,1,0] op_sel_hi:[0,1,1] clamp\n\t" \
  "v_fma_mix_f32 v222, |v182|, v222, v222 op_sel:[0,1,0] op_sel_hi:[0,1,1] clamp\n\t" \
  "s_waitcnt lgkmcnt(7)\n\t" \
  "v_mfma_f32_16x16x32_bf16 v[168:171], v[212:215], v[124:127], v[168:171]\n\t" \
  "ds_read_b64_tr_b16 v[212:213], v248 offset:40488\n\t" \
  "v_or_b32 v188, v224, v225\n\t" \
  "v_or_b32 v189, v226, v227\n\t" \
  "v_mfma_f32_16x16x32_bf16 v[144:147], v[216:219], v[128:131], v[144:147]\n\t" \
  "ds_read_b64_tr_b16 v[214:215], v248 offset:41512\n\t" \
  "v_fma_f32 v181, |v181|, v221, v181 mul:4\n\t" \
  "v_fma_f32 v180, |v180|, v220, v180 mul:4\n\t" \
  "v_mfma_f32_16x16x32_bf16 v[152:155], v[216:219], v[132:135], v[152:155]\n\t" \
  "ds_read_u16 v232, v232 offset:10528\n\t" \
  "v_fma_f32 v183, |v183|, v223, v183 mul:4\n\t" \
  "v_fma_f32 v182, |v182|, v222, v182 mul:4\n\t" \
  "v_mfma_f32_16x16x32_bf16 v[160:163], v[216:219], v[136:139], v[160:163]\n\t" \
  "ds_read_u16_d16_hi v235, v235 offset:10528\n\t" \
  "v_cvt_pk_bf16_f32 v180, v180, v181\n\t" \
  "v_cvt_pk_bf16_f32 v181, v182, v183\n\t" \
  "v_mfma_f32_16x16x32_bf16 v[168:171], v[216:219], v[140:143], v[168:171]\n\t" \
  "ds_read_b64_tr_b16 v[216:217], v248 offset:39464\n\t" \
  "v_cvt_pknorm_i16_f32 v220, |v146|, |v147|\n\t" \
  "s_waitcnt lgkmcnt(8)\n\t" \
  "v_mfma_f32_16x16x32_bf16 v[148:151], v[208:211], v[96:99], 0\n\t" \
  "ds_read_b64_tr_b16 v[218:219], v248 offset:42536\n\t" \
  "v_cvt_pknorm_i16_f32 v222, |v144|, |v145|\n\t" \
  "s_waitcnt vmcnt(0)\n\t" \
  "v_lshlrev_b32_sdwa v221, 2, v220 dst_sel:DWORD dst_unused:UNUSED_PAD src0_sel:DWORD src1_sel:BYTE_3\n\t" \
  "v_mfma_f32_16x16x32_bf16 v[156:159], v[208:211], v[100:103], 0\n\t" \
  "ds_read_u16 v234, v234 offset:10528\n\t" \
  "v_lshlrev_b32_sdwa v220, 2, v220 dst_sel:DWORD dst_unused:UNUSED_PAD src0_sel:DWORD src1_sel:BYTE_1\n\t" \
  "v_lshlrev_b32_sdwa v223, 2, v222 dst_sel:DWORD dst_unused:UNUSED_PAD src0_sel:DWORD src1_sel:BYTE_3\n\t" \
  "v_mfma_f32_16x16x32_bf16 v[164:167], v[208:211], v[104:107], 0\n\t" \
  "ds_read_b32 v221, v221 offset:10016\n\t" \
  "v_lshlrev_b32_sdwa v222, 2, v222 dst_sel:DWORD dst_unused:UNUSED_PAD src0_sel:DWORD src1_sel:BYTE_1\n\t" \
  "v_lshlrev_b32_sdwa v225, 1, v155 dst_sel:DWORD dst_unused:UNUSED_PAD src0_sel:DWORD src1_sel:WORD_1\n\t" \
  "v_mfma_f32_16x16x32_bf16 v[172:175], v[208:211], v[108:111], 0\n\t" \
  "ds_read_b32 v220, v220 offset:10016\n\t" \
  "v_lshlrev_b32_sdwa v224, 1, v154 dst_sel:DWORD dst_unused:UNUSED_PAD src0_sel:DWORD src1_sel:WORD_1\n\t" \
  "v_lshlrev_b32_sdwa v227, 1, v153 dst_sel:DWORD dst_unused:UNUSED_PAD src0_sel:DWORD src1_sel:WORD_1\n\t" \
  "s_waitcnt lgkmcnt(7)\n\t" \
  "v_mfma_f32_16x16x32_bf16 v[148:151], v[212:215], v[112:115], v[148:151]\n\t" \
  "ds_read_b32 v223, v223 offset:10016\n\t" \
  "v_lshlrev_b32_sdwa v226, 1, v152 dst_sel:DWORD dst_unused:UNUSED_PAD src0_sel:DWORD src1_sel:WORD_1\n\t" \
  "v_mfma_f32_16x16x32_bf16 v[156:159], v[212:215], v[116:119], v[156:159]\n\t" \
  "ds_read_b64_tr_b16 v[208:209], v249 offset:30752\n\t" \
  "ds_read_b32 v222, v222 offset:10016\n\t" \
  "v_fma_mix_f32 v229, |v197|, v229, v229 op_sel:[0,1,0] op_sel_hi:[0,1,1] clamp\n\t" \
  "v_mfma_f32_16x16x32_bf16 v[164:167], v[212:215], v[120:123], v[164:167]\n\t" \
  "ds_read_b64_tr_b16 v[210:211], v249 offset:32288\n\t" \
  "v_fma_mix_f32 v228, |v196|, v228, v228 op_sel:[0,1,0] op_sel_hi:[0,1,1] clamp\n\t" \
  "v_fma_mix_f32 v231, |v199|, v231, v231 op_sel:[0,1,0] op_sel_hi:[0,1,1] clamp\n\t" \
  "v_mfma_f32_16x16x32_bf16 v[172:175], v[212:215], v[124:127], v[172:175]\n\t" \
  "ds_read_b64_tr_b16 v[212:213], v249 offset:30760\n\t" \
  "ds_read_u16_d16_hi v225, v225 offset:10528\n\t" \
  "v_fma_mix_f32 v230, |v198|, v230, v230 op_sel:[0,1,0] op_sel_hi:[0,1,1] clamp\n\t" \
  "s_waitcnt lgkmcnt(8)\n\t" \
  "v_mfma_f32_16x16x32_bf16 v[148:151], v[216:219], v[128:131], v[148:151]\n\t" \
  "ds_read_b64_tr_b16 v[214:215], v249 offset:32296\n\t" \
  "v_or_b32 v204, v232, v233\n\t" \
  "v_or_b32 v205, v234, v235\n\t" \
  "v_mfma_f32_16x16x32_bf16 v[156:159], v[216:219], v[132:135], v[156:159]\n\t" \
  "ds_read_u16 v224, v224 offset:10528\n\t" \
  "v_fma_f32 v197, |v197|, v229, v197 mul:4\n\t" \
  "v_fma_f32 v196, |v196|, v228, v196 mul:4\n\t" \
  "v_mfma_f32_16x16x32_bf16 v[164:167], v[216:219], v[136:139], v[164:167]\n\t" \
  "ds_read_u16_d16_hi v227, v227 offset:10528\n\t" \
  "v_fma_f32 v199, |v199|, v231, v199 mul:4\n\t" \
  "v_fma_f32 v198, |v198|, v230, v198 mul:4\n\t" \
  "v_mfma_f32_16x16x32_bf16 v[172:175], v[216:219], v[140:143], v[172:175]\n\t" \
  "ds_read_b64_tr_b16 v[216:217], v249 offset:30816\n\t" \
  "v_cvt_pk_bf16_f32 v196, v196, v197\n\t" \
  "v_cvt_pk_bf16_f32 v197, v198, v199\n\t" \
  "v_mfma_f32_16x16x32_bf16 v[72:75], v[242:245], v[236:239], v[72:75]\n\t" \
  "ds_read_b64_tr_b16 v[218:219], v249 offset:32352\n\t" \
  "v_cvt_pknorm_i16_f32 v228, |v162|, |v163|\n\t" \
  "v_cvt_pknorm_i16_f32 v230, |v160|, |v161|\n\t" \
  "v_mfma_f32_16x16x32_bf16 v[76:79], v[240:243], v[236:239], v[76:79]\n\t" \
  "ds_read_u16 v226, v226 offset:10528\n\t" \
  "buffer_load_dwordx4 v[236:239], v250, s[8:11], s15 offen offset:64 nt\n\t" \
  "v_lshlrev_b32_sdwa v229, 2, v228 dst_sel:DWORD dst_unused:UNUSED_PAD src0_sel:DWORD src1_sel:BYTE_3\n\t" \
  "s_waitcnt lgkmcnt(8)\n\t" \
  "v_mfma_f32_16x16x32_bf16 v[0:3], v[208:211], v[178:181], v[0:3]\n\t" \
  "v_lshlrev_b32_sdwa v228, 2, v228 dst_sel:DWORD dst_unused:UNUSED_PAD src0_sel:DWORD src1_sel:BYTE_1\n\t" \
  "v_lshlrev_b32_sdwa v231, 2, v230 dst_sel:DWORD dst_unused:UNUSED_PAD src0_sel:DWORD src1_sel:BYTE_3\n\t" \
  "v_mfma_f32_16x16x32_bf16 v[24:27], v[208:211], v[186:189], v[24:27]\n\t" \
  "ds_read_b32 v229, v229 offset:10016\n\t" \
  "v_lshlrev_b32_sdwa v230, 2, v230 dst_sel:DWORD dst_unused:UNUSED_PAD src0_sel:DWORD src1_sel:BYTE_1\n\t" \
  "v_lshlrev_b32_sdwa v233, 1, v171 dst_sel:DWORD dst_unused:UNUSED_PAD src0_sel:DWORD src1_sel:WORD_1\n\t" \
  "v_mfma_f32_16x16x32_bf16 v[48:51], v[208:211], v[194:197], v[48:51]\n\t" \
  "ds_read_b32 v228, v228 offset:10016\n\t" \
  "v_lshlrev_b32_sdwa v232, 1, v170 dst_sel:DWORD dst_unused:UNUSED_PAD src0_sel:DWORD src1_sel:WORD_1\n\t" \
  "v_lshlrev_b32_sdwa v235, 1, v169 dst_sel:DWORD dst_unused:UNUSED_PAD src0_sel:DWORD src1_sel:WORD_1\n\t" \
  "v_mfma_f32_16x16x32_bf16 v[72:75], v[208:211], v[202:205], v[72:75]\n\t" \
  "ds_read_b64_tr_b16 v[208:209], v249 offset:30824\n\t" \
  "ds_read_b32 v231, v231 offset:10016\n\t" \
  "v_lshlrev_b32_sdwa v234, 1, v168 dst_sel:DWORD dst_unused:UNUSED_PAD src0_sel:DWORD src1_sel:WORD_1\n\t" \
  "s_waitcnt lgkmcnt(9)\n\t" \
  "v_mfma_f32_16x16x32_bf16 v[4:7], v[212:215], v[178:181], v[4:7]\n\t" \
  "ds_read_b64_tr_b16 v[210:211], v249 offset:32360\n\t" \
  "v_fma_mix_f32 v221, |v147|, v221, v221 op_sel:[0,1,0] op_sel_hi:[0,1,1] clamp\n\t" \
  "v_fma_mix_f32 v220, |v146|, v220, v220 op_sel:[0,1,0] op_sel_hi:[0,1,1] clamp\n\t" \
  "v_mfma_f32_16x16x32_bf16 v[28:31], v[212:215], v[186:189], v[28:31]\n\t" \
  "ds_read_b32 v230, v230 offset:10016\n\t" \
  "v_fma_mix_f32 v223, |v145|, v223, v223 op_sel:[0,1,0] op_sel_hi:[0,1,1] clamp\n\t" \
  "v_fma_mix_f32 v222, |v144|, v222, v222 op_sel:[0,1,0] op_sel_hi:[0,1,1] clamp\n\t" \
  "s_waitcnt lgkmcnt(6)\n\t" \
  "v_mfma_f32_16x16x32_bf16 v[52:55], v[212:215], v[194:197], v[52:55]\n\t" \
  "ds_read_u16_d16_hi v233, v233 offset:10528\n\t" \
  "v_or_b32 v155, v224, v225\n\t" \
  "v_or_b32 v154, v226, v227\n\t" \
  "v_mfma_f32_16x16x32_bf16 v[76:79], v[212:215], v[202:205], v[76:79]\n\t" \
  "ds_read_b64_tr_b16 v[212:213], v249 offset:30880\n\t" \
  "v_fma_f32 v147, |v147|, v221, v147 mul:4\n\t" \
  "v_fma_f32 v146, |v146|, v220, v146 mul:4\n\t" \
  "v_mfma_f32_16x16x32_bf16 v[8:11], v[216:219], v[178:181], v[8:11]\n\t" \
  "ds_read_b64_tr_b16 v[214:215], v249 offset:32416\n\t" \
  "v_fma_f32 v145, |v145|, v223, v145 mul:4\n\t" \
  "v_fma_f32 v144, |v144|, v222, v144 mul:4\n\t" \
  "v_mfma_f32_16x16x32_bf16 v[32:35], v[216:219], v[186:189], v[32:35]\n\t" \
  "ds_read_u16 v232, v232 offset:10528\n\t" \
  "v_cvt_pk_bf16_f32 v147, v146, v147\n\t" \
  "v_cvt_pk_bf16_f32 v146, v144, v145\n\t" \
  "v_mfma_f32_16x16x32_bf16 v[56:59], v[216:219], v[194:197], v[56:59]\n\t" \
  "ds_read_u16_d16_hi v235, v235 offset:10528\n\t" \
  "v_cvt_pknorm_i16_f32 v220, |v148|, |v149|\n\t" \
  "v_mfma_f32_16x16x32_bf16 v[80:83], v[216:219], v[202:205], v[80:83]\n\t" \
  "ds_read_b64_tr_b16 v[216:217], v249 offset:30888\n\t" \
  "v_cvt_pknorm_i16_f32 v222, |v150|, |v151|\n\t" \
  "v_lshlrev_b32_sdwa v221, 2, v220 dst_sel:DWORD dst_unused:UNUSED_PAD src0_sel:DWORD src1_sel:BYTE_3\n\t" \
  "s_waitcnt lgkmcnt(7)\n\t" \
  "v_mfma_f32_16x16x32_bf16 v[12:15], v[208:211], v[178:181], v[12:15]\n\t" \
  "ds_read_b64_tr_b16 v[218:219], v249 offset:32424\n\t" \
  "v_lshlrev_b32_sdwa v220, 2, v220 dst_sel:DWORD dst_unused:UNUSED_PAD src0_sel:DWORD src1_sel:BYTE_1\n\t" \
  "v_lshlrev_b32_sdwa v223, 2, v222 dst_sel:DWORD dst_unused:UNUSED_PAD src0_sel:DWORD src1_sel:BYTE_3\n\t" \
  "v_mfma_f32_16x16x32_bf16 v[36:39], v[208:211], v[186:189], v[36:39]\n\t" \
  "ds_read_u16 v234, v234 offset:10528\n\t" \
  "v_lshlrev_b32_sdwa v222, 2, v222 dst_sel:DWORD dst_unused:UNUSED_PAD src0_sel:DWORD src1_sel:BYTE_1\n\t" \
  "v_lshlrev_b32_sdwa v225, 1, v157 dst_sel:DWORD dst_unused:UNUSED_PAD src0_sel:DWORD src1_sel:WORD_1\n\t" \
  "v_mfma_f32_16x16x32_bf16 v[60:63], v[208:211], v[194:197], v[60:63]\n\t" \
  "ds_read_b32 v221, v221 offset:10016\n\t" \
  "v_lshlrev_b32_sdwa v224, 1, v156 dst_sel:DWORD dst_unused:UNUSED_PAD src0_sel:DWORD src1_sel:WORD_1\n\t" \
  "v_mfma_f32_16x16x32_bf16 v[84:87], v[208:211], v[202:205], v[84:87]\n\t" \
  "ds_read_b64_tr_b16 v[208:209], v248 offset:0\n\t" \
  "ds_read_b32 v220, v220 offset:10016\n\t" \
  "v_lshlrev_b32_sdwa v227, 1, v159 dst_sel:DWORD dst_unused:UNUSED_PAD src0_sel:DWORD src1_sel:WORD_1\n\t" \
  "s_waitcnt lgkmcnt(8)\n\t" \
  "v_mfma_f32_16x16x32_bf16 v[16:19], v[212:215], v[178:181], v[16:19]\n\t" \
  "ds_read_b64_tr_b16 v[210:211], v248 offset:1024\n\t" \
  "v_lshlrev_b32_sdwa v226, 1, v158 dst_sel:DWORD dst_unused:UNUSED_PAD src0_sel:DWORD src1_sel:WORD_1\n\t" \
  "v_fma_mix_f32 v229, |v163|, v229, v229 op_sel:[0,1,0] op_sel_hi:[0,1,1] clamp\n\t" \
  "v_mfma_f32_16x16x32_bf16 v[40:43], v[212:215], v[186:189], v[40:43]\n\t" \
  "ds_read_b32 v223, v223 offset:10016\n\t" \
  "ds_read_b32 v222, v222 offset:10016\n\t" \
  "v_fma_mix_f32 v228, |v162|, v228, v228 op_sel:[0,1,0] op_sel_hi:[0,1,1] clamp\n\t" \
  "v_mfma_f32_16x16x32_bf16 v[64:67], v[212:215], v[194:197], v[64:67]\n\t" \
  "ds_read_u16_d16_hi v225, v225 offset:10528\n\t" \
  "v_fma_mix_f32 v231, |v161|, v231, v231 op_sel:[0,1,0] op_sel_hi:[0,1,1] clamp\n\t" \
  "v_fma_mix_f32 v230, |v160|, v230, v230 op_sel:[0,1,0] op_sel_hi:[0,1,1] clamp\n\t" \
  "s_waitcnt lgkmcnt(7)\n\t" \
  "v_mfma_f32_16x16x32_bf16 v[88:91], v[212:215], v[202:205], v[88:91]\n\t" \
  "ds_read_b64_tr_b16 v[212:213], v248 offset:3072\n\t" \
  "v_or_b32 v171, v232, v233\n\t" \
  "v_or_b32 v170, v234, v235\n\t" \
  "v_mfma_f32_16x16x32_bf16 v[20:23], v[216:219], v[178:181], v[20:23]\n\t" \
  "ds_read_b64_tr_b16 v[214:215], v248 offset:4096\n\t" \
  "v_fma_f32 v163, |v163|, v229, v163 mul:4\n\t" \
  "v_fma_f32 v162, |v162|, v228, v162 mul:4\n\t" \
  "v_mfma_f32_16x16x32_bf16 v[44:47], v[216:219], v[186:189], v[44:47]\n\t" \
  "ds_read_u16 v224, v224 offset:10528\n\t" \
  "v_fma_f32 v161, |v161|, v231, v161 mul:4\n\t" \
  "v_fma_f32 v160, |v160|, v230, v160 mul:4\n\t" \
  "v_mfma_f32_16x16x32_bf16 v[68:71], v[216:219], v[194:197], v[68:71]\n\t" \
  "ds_read_u16_d16_hi v227, v227 offset:10528\n\t" \
  "v_cvt_pk_bf16_f32 v163, v162, v163\n\t" \
  "v_cvt_pk_bf16_f32 v162, v160, v161\n\t" \
  "v_mfma_f32_16x16x32_bf16 v[92:95], v[216:219], v[202:205], v[92:95]\n\t" \
  "ds_read_b64_tr_b16 v[216:217], v248 offset:2048\n\t" \
  "v_cvt_pknorm_i16_f32 v228, |v164|, |v165|\n\t" \
  "v_cvt_pknorm_i16_f32 v230, |v166|, |v167|\n\t" \
  "s_waitcnt lgkmcnt(8)\n\t" \
  "v_mfma_f32_16x16x32_bf16 v[176:179], v[208:211], v[96:99], 0\n\t" \
  "ds_read_b64_tr_b16 v[218:219], v248 offset:5120\n\t" \
  "v_lshlrev_b32_sdwa v229, 2, v228 dst_sel:DWORD dst_unused:UNUSED_PAD src0_sel:DWORD src1_sel:BYTE_3\n\t" \
  "v_lshlrev_b32_sdwa v228, 2, v228 dst_sel:DWORD dst_unused:UNUSED_PAD src0_sel:DWORD src1_sel:BYTE_1\n\t" \
  "v_mfma_f32_16x16x32_bf16 v[184:187], v[208:211], v[100:103], 0\n\t" \
  "ds_read_u16 v226, v226 offset:10528\n\t" \
  "v_lshlrev_b32_sdwa v231, 2, v230 dst_sel:DWORD dst_unused:UNUSED_PAD src0_sel:DWORD src1_sel:BYTE_3\n\t" \
  "v_lshlrev_b32_sdwa v230, 2, v230 dst_sel:DWORD dst_unused:UNUSED_PAD src0_sel:DWORD src1_sel:BYTE_1\n\t" \
  "v_mfma_f32_16x16x32_bf16 v[192:195], v[208:211], v[104:107], 0\n\t" \
  "ds_read_b32 v229, v229 offset:10016\n\t" \
  "v_lshlrev_b32_sdwa v233, 1, v173 dst_sel:DWORD dst_unused:UNUSED_PAD src0_sel:DWORD src1_sel:WORD_1\n\t" \
  "v_lshlrev_b32_sdwa v232, 1, v172 dst_sel:DWORD dst_unused:UNUSED_PAD src0_sel:DWORD src1_sel:WORD_1\n\t" \
  "v_mfma_f32_16x16x32_bf16 v[200:203], v[208:211], v[108:111], 0\n\t" \
  "ds_read_b64_tr_b16 v[208:209], v248 offset:512\n\t" \
  "ds_read_b32 v228, v228 offset:10016\n\t" \
  "v_lshlrev_b32_sdwa v235, 1, v175 dst_sel:DWORD dst_unused:UNUSED_PAD src0_sel:DWORD src1_sel:WORD_1\n\t" \
  "s_waitcnt lgkmcnt(8)\n\t" \
  "v_mfma_f32_16x16x32_bf16 v[176:179], v[212:215], v[112:115], v[176:179]\n\t" \
  "ds_read_b64_tr_b16 v[210:211], v248 offset:1536\n\t" \
  "v_lshlrev_b32_sdwa v234, 1, v174 dst_sel:DWORD dst_unused:UNUSED_PAD src0_sel:DWORD src1_sel:WORD_1\n\t" \
  "v_fma_mix_f32 v221, |v149|, v221, v221 op_sel:[0,1,0] op_sel_hi:[0,1,1] clamp\n\t" \
  "v_mfma_f32_16x16x32_bf16 v[184:187], v[212:215], v[116:119], v[184:187]\n\t" \
  "ds_read_b32 v231, v231 offset:10016\n\t" \
  "ds_read_b32 v230, v230 offset:10016\n\t" \
  "v_fma_mix_f32 v220, |v148|, v220, v220 op_sel:[0,1,0] op_sel_hi:[0,1,1] clamp\n\t" \
  "v_mfma_f32_16x16x32_bf16 v[192:195], v[212:215], v[120:123], v[192:195]\n\t" \
  "ds_read_u16_d16_hi v233, v233 offset:10528\n\t" \
  "v_fma_mix_f32 v223, |v151|, v223, v223 op_sel:[0,1,0] op_sel_hi:[0,1,1] clamp\n\t" \
  "v_fma_mix_f32 v222, |v150|, v222, v222 op_sel:[0,1,0] op_sel_hi:[0,1,1] clamp\n\t" \
  "s_waitcnt lgkmcnt(7)\n\t" \
  "v_mfma_f32_16x16x32_bf16 v[200:203], v[212:215], v[124:127], v[200:203]\n\t" \
  "ds_read_b64_tr_b16 v[212:213], v248 offset:3584\n\t" \
  "v_or_b32 v156, v224, v225\n\t" \
  "v_or_b32 v157, v226, v227\n\t" \
  "v_mfma_f32_16x16x32_bf16 v[176:179], v[216:219], v[128:131], v[176:179]\n\t" \
  "ds_read_b64_tr_b16 v[214:215], v248 offset:4608\n\t" \
  "v_fma_f32 v149, |v149|, v221, v149 mul:4\n\t" \
  "v_fma_f32 v148, |v148|, v220, v148 mul:4\n\t" \
  "v_mfma_f32_16x16x32_bf16 v[184:187], v[216:219], v[132:135], v[184:187]\n\t" \
  "ds_read_u16 v232, v232 offset:10528\n\t" \
  "v_fma_f32 v151, |v151|, v223, v151 mul:4\n\t" \
  "v_fma_f32 v150, |v150|, v222, v150 mul:4\n\t" \
  "v_mfma_f32_16x16x32_bf16 v[192:195], v[216:219], v[136:139], v[192:195]\n\t" \
  "ds_read_u16_d16_hi v235, v235 offset:10528\n\t" \
  "v_cvt_pk_bf16_f32 v148, v148, v149\n\t" \
  "v_cvt_pk_bf16_f32 v149, v150, v151\n\t" \
  "v_mfma_f32_16x16x32_bf16 v[200:203], v[216:219], v[140:143], v[200:203]\n\t" \
  "ds_read_b64_tr_b16 v[216:217], v248 offset:2560\n\t" \
  "v_cvt_pknorm_i16_f32 v220, |v178|, |v179|\n\t" \
  "s_waitcnt lgkmcnt(8)\n\t" \
  "v_mfma_f32_16x16x32_bf16 v[180:183], v[208:211], v[96:99], 0\n\t" \
  "ds_read_b64_tr_b16 v[218:219], v248 offset:5632\n\t" \
  "v_cvt_pknorm_i16_f32 v222, |v176|, |v177|\n\t" \
  "s_waitcnt vmcnt(0)\n\t" \
  "v_lshlrev_b32_sdwa v221, 2, v220 dst_sel:DWORD dst_unused:UNUSED_PAD src0_sel:DWORD src1_sel:BYTE_3\n\t" \
  "v_mfma_f32_16x16x32_bf16 v[188:191], v[208:211], v[100:103], 0\n\t" \
  "ds_read_u16 v234, v234 offset:10528\n\t" \
  "v_lshlrev_b32_sdwa v220, 2, v220 dst_sel:DWORD dst_unused:UNUSED_PAD src0_sel:DWORD src1_sel:BYTE_1\n\t" \
  "v_lshlrev_b32_sdwa v223, 2, v222 dst_sel:DWORD dst_unused:UNUSED_PAD src0_sel:DWORD src1_sel:BYTE_3\n\t" \
  "v_mfma_f32_16x16x32_bf16 v[196:199], v[208:211], v[104:107], 0\n\t" \
  "ds_read_b32 v221, v221 offset:10016\n\t" \
  "v_lshlrev_b32_sdwa v222, 2, v222 dst_sel:DWORD dst_unused:UNUSED_PAD src0_sel:DWORD src1_sel:BYTE_1\n\t" \
  "v_lshlrev_b32_sdwa v225, 1, v187 dst_sel:DWORD dst_unused:UNUSED_PAD src0_sel:DWORD src1_sel:WORD_1\n\t" \
  "v_mfma_f32_16x16x32_bf16 v[204:207], v[208:211], v[108:111], 0\n\t" \
  "ds_read_b32 v220, v220 offset:10016\n\t" \
  "v_lshlrev_b32_sdwa v224, 1, v186 dst_sel:DWORD dst_unused:UNUSED_PAD src0_sel:DWORD src1_sel:WORD_1\n\t" \
  "v_lshlrev_b32_sdwa v227, 1, v185 dst_sel:DWORD dst_unused:UNUSED_PAD src0_sel:DWORD src1_sel:WORD_1\n\t" \
  "s_waitcnt lgkmcnt(7)\n\t" \
  "v_mfma_f32_16x16x32_bf16 v[180:183], v[212:215], v[112:115], v[180:183]\n\t" \
  "ds_read_b32 v223, v223 offset:10016\n\t" \
  "v_lshlrev_b32_sdwa v226, 1, v184 dst_sel:DWORD dst_unused:UNUSED_PAD src0_sel:DWORD src1_sel:WORD_1\n\t" \
  "v_mfma_f32_16x16x32_bf16 v[188:191], v[212:215], v[116:119], v[188:191]\n\t" \
  "ds_read_b64_tr_b16 v[208:209], v249 offset:6152\n\t" \
  "ds_read_b32 v222, v222 offset:10016\n\t" \
  "v_fma_mix_f32 v229, |v165|, v229, v229 op_sel:[0,1,0] op_sel_hi:[0,1,1] clamp\n\t" \
  "v_mfma_f32_16x16x32_bf16 v[196:199], v[212:215], v[120:123], v[196:199]\n\t" \
  "ds_read_b64_tr_b16 v[210:211], v249 offset:7688\n\t" \
  "v_fma_mix_f32 v228, |v164|, v228, v228 op_sel:[0,1,0] op_sel_hi:[0,1,1] clamp\n\t" \
  "v_fma_mix_f32 v231, |v167|, v231, v231 op_sel:[0,1,0] op_sel_hi:[0,1,1] clamp\n\t" \
  "v_mfma_f32_16x16x32_bf16 v[204:207], v[212:215], v[124:127], v[204:207]\n\t" \
  "ds_read_b64_tr_b16 v[212:213], v249 offset:6160\n\t" \
  "ds_read_u16_d16_hi v225, v225 offset:10528\n\t" \
  "v_fma_mix_f32 v230, |v166|, v230, v230 op_sel:[0,1,0] op_sel_hi:[0,1,1] clamp\n\t" \
  "s_waitcnt lgkmcnt(8)\n\t" \
  "v_mfma_f32_16x16x32_bf16 v[180:183], v[216:219], v[128:131], v[180:183]\n\t" \
  "ds_read_b64_tr_b16 v[214:215], v249 offset:7696\n\t" \
  "v_or_b32 v172, v232, v233\n\t" \
  "v_or_b32 v173, v234, v235\n\t" \
  "v_mfma_f32_16x16x32_bf16 v[188:191], v[216:219], v[132:135], v[188:191]\n\t" \
  "ds_read_u16 v224, v224 offset:10528\n\t" \
  "v_fma_f32 v165, |v165|, v229, v165 mul:4\n\t" \
  "v_fma_f32 v164, |v164|, v228, v164 mul:4\n\t" \
  "v_mfma_f32_16x16x32_bf16 v[196:199], v[216:219], v[136:139], v[196:199]\n\t" \
  "ds_read_u16_d16_hi v227, v227 offset:10528\n\t" \
  "v_fma_f32 v167, |v167|, v231, v167 mul:4\n\t" \
  "v_fma_f32 v166, |v166|, v230, v166 mul:4\n\t" \
  "v_mfma_f32_16x16x32_bf16 v[204:207], v[216:219], v[140:143], v[204:207]\n\t" \
  "ds_read_b64_tr_b16 v[216:217], v249 offset:6216\n\t" \
  "v_cvt_pk_bf16_f32 v164, v164, v165\n\t" \
  "v_cvt_pk_bf16_f32 v165, v166, v167\n\t" \
  "v_mfma_f32_16x16x32_bf16 v[80:83], v[242:245], v[236:239], v[80:83]\n\t" \
  "ds_read_b64_tr_b16 v[218:219], v249 offset:7752\n\t" \
  "v_cvt_pknorm_i16_f32 v228, |v194|, |v195|\n\t" \
  "v_cvt_pknorm_i16_f32 v230, |v192|, |v193|\n\t" \
  "v_lshlrev_b32_sdwa v229, 2, v228 dst_sel:DWORD dst_unused:UNUSED_PAD src0_sel:DWORD src1_sel:BYTE_3\n\t" \
  "v_mfma_f32_16x16x32_bf16 v[84:87], v[240:243], v[236:239], v[84:87]\n\t" \
  "ds_read_u16 v226, v226 offset:10528\n\t" \
  "buffer_load_dwordx4 v[236:239], v250, s[8:11], s15 offen offset:128 nt\n\t" \
  "v_lshlrev_b32_sdwa v228, 2, v228 dst_sel:DWORD dst_unused:UNUSED_PAD src0_sel:DWORD src1_sel:BYTE_1\n\t" \
  "s_waitcnt lgkmcnt(8)\n\t" \
  "v_mfma_f32_16x16x32_bf16 v[0:3], v[208:211], v[146:149], v[0:3]\n\t" \
  "ds_read_b32 v229, v229 offset:10016\n\t" \
  "v_lshlrev_b32_sdwa v231, 2, v230 dst_sel:DWORD dst_unused:UNUSED_PAD src0_sel:DWORD src1_sel:BYTE_3\n\t" \
  "v_lshlrev_b32_sdwa v230, 2, v230 dst_sel:DWORD dst_unused:UNUSED_PAD src0_sel:DWORD src1_sel:BYTE_1\n\t" \
  "v_lshlrev_b32_sdwa v233, 1, v203 dst_sel:DWORD dst_unused:UNUSED_PAD src0_sel:DWORD src1_sel:WORD_1\n\t" \
  "v_mfma_f32_16x16x32_bf16 v[24:27], v[208:211], v[154:157], v[24:27]\n\t" \
  "ds_read_b32 v228, v228 offset:10016\n\t" \
  "v_lshlrev_b32_sdwa v232, 1, v202 dst_sel:DWORD dst_unused:UNUSED_PAD src0_sel:DWORD src1_sel:WORD_1\n\t" \
  "v_lshlrev_b32_sdwa v235, 1, v201 dst_sel:DWORD dst_unused:UNUSED_PAD src0_sel:DWORD src1_sel:WORD_1\n\t" \
  "v_lshlrev_b32_sdwa v234, 1, v200 dst_sel:DWORD dst_unused:UNUSED_PAD src0_sel:DWORD src1_sel:WORD_1\n\t" \
  "v_mfma_f32_16x16x32_bf16 v[48:51], v[208:211], v[162:165], v[48:51]\n\t" \
  "ds_read_b32 v231, v231 offset:10016\n\t" \
  "v_fma_mix_f32 v221, |v179|, v221, v221 op_sel:[0,1,0] op_sel_hi:[0,1,1] clamp\n\t" \
  "v_fma_mix_f32 v220, |v178|, v220, v220 op_sel:[0,1,0] op_sel_hi:[0,1,1] clamp\n\t" \
  "v_fma_mix_f32 v223, |v177|, v223, v223 op_sel:[0,1,0] op_sel_hi:[0,1,1] clamp\n\t" \
  "v_mfma_f32_16x16x32_bf16 v[72:75], v[208:211], v[170:173], v[72:75]\n\t" \
  "ds_read_b64_tr_b16 v[208:209], v249 offset:6224\n\t" \
  "ds_read_b32 v230, v230 offset:10016\n\t" \
  "v_fma_mix_f32 v222, |v176|, v222, v222 op_sel:[0,1,0] op_sel_hi:[0,1,1] clamp\n\t" \
  "s_waitcnt lgkmcnt(5)\n\t" \
  "v_or_b32 v187, v224, v225\n\t" \
  "v_or_b32 v186, v226, v227\n\t" \
  "v_mfma_f32_16x16x32_bf16 v[4:7], v[212:215], v[146:149], v[4:7]\n\t" \
  "ds_read_b64_tr_b16 v[210:211], v249 offset:7760\n\t" \
  "v_fma_f32 v179, |v179|, v221, v179 mul:4\n\t" \
  "v_fma_f32 v178, |v178|, v220, v178 mul:4\n\t" \
  "v_fma_f32 v177, |v177|, v223, v177 mul:4\n\t" \
  "v_mfma_f32_16x16x32_bf16 v[28:31], v[212:215], v[154:157], v[28:31]\n\t" \
  "ds_read_u16_d16_hi v233, v233 offset:10528\n\t" \
  "v_fma_f32 v176, |v176|, v222, v176 mul:4\n\t" \
  "v_cvt_pk_bf16_f32 v179, v178, v179\n\t" \
  "v_cvt_pk_bf16_f32 v178, v176, v177\n\t" \
  "v_mfma_f32_16x16x32_bf16 v[52:55], v[212:215], v[162:165], v[52:55]\n\t" \
  "ds_read_u16 v232, v232 offset:10528\n\t" \
  "v_cvt_pknorm_i16_f32 v220, |v180|, |v181|\n\t" \
  "v_cvt_pknorm_i16_f32 v222, |v182|, |v183|\n\t" \
  "v_lshlrev_b32_sdwa v221, 2, v220 dst_sel:DWORD dst_unused:UNUSED_PAD src0_sel:DWORD src1_sel:BYTE_3\n\t" \
  "v_mfma_f32_16x16x32_bf16 v[76:79], v[212:215], v[170:173], v[76:79]\n\t" \
  "ds_read_b64_tr_b16 v[212:213], v249 offset:6280\n\t" \
  "v_lshlrev_b32_sdwa v220, 2, v220 dst_sel:DWORD dst_unused:UNUSED_PAD src0_sel:DWORD src1_sel:BYTE_1\n\t" \
  "v_lshlrev_b32_sdwa v223, 2, v222 dst_sel:DWORD dst_unused:UNUSED_PAD src0_sel:DWORD src1_sel:BYTE_3\n\t" \
  "v_lshlrev_b32_sdwa v222, 2, v222 dst_sel:DWORD dst_unused:UNUSED_PAD src0_sel:DWORD src1_sel:BYTE_1\n\t" \
  "v_mfma_f32_16x16x32_bf16 v[8:11], v[216:219], v[146:149], v[8:11]\n\t" \
  "ds_read_b64_tr_b16 v[214:215], v249 offset:7816\n\t" \
  "ds_read_u16_d16_hi v235, v235 offset:10528\n\t" \
  "v_lshlrev_b32_sdwa v225, 1, v189 dst_sel:DWORD dst_unused:UNUSED_PAD src0_sel:DWORD src1_sel:WORD_1\n\t" \
  "v_lshlrev_b32_sdwa v224, 1, v188 dst_sel:DWORD dst_unused:UNUSED_PAD src0_sel:DWORD src1_sel:WORD_1\n\t" \
  "v_lshlrev_b32_sdwa v227, 1, v191 dst_sel:DWORD dst_unused:UNUSED_PAD src0_sel:DWORD src1_sel:WORD_1\n\t" \
  "v_mfma_f32_16x16x32_bf16 v[32:35], v[216:219], v[154:157], v[32:35]\n\t" \
  "ds_read_u16 v234, v234 offset:10528\n\t" \
  "ds_read_b32 v221, v221 offset:10016\n\t" \
  "v_lshlrev_b32_sdwa v226, 1, v190 dst_sel:DWORD dst_unused:UNUSED_PAD src0_sel:DWORD src1_sel:WORD_1\n\t" \
  "s_waitcnt lgkmcnt(8)\n\t" \
  "v_fma_mix_f32 v229, |v195|, v229, v229 op_sel:[0,1,0] op_sel_hi:[0,1,1] clamp\n\t" \
  "v_fma_mix_f32 v228, |v194|, v228, v228 op_sel:[0,1,0] op_sel_hi:[0,1,1] clamp\n\t" \
  "v_mfma_f32_16x16x32_bf16 v[56:59], v[216:219], v[162:165], v[56:59]\n\t" \
  "ds_read_b32 v220, v220 offset:10016\n\t" \
  "ds_read_b32 v223, v223 offset:10016\n\t" \
  "v_fma_mix_f32 v231, |v193|, v231, v231 op_sel:[0,1,0] op_sel_hi:[0,1,1] clamp\n\t" \
  "v_fma_mix_f32 v230, |v192|, v230, v230 op_sel:[0,1,0] op_sel_hi:[0,1,1] clamp\n\t" \
  "s_waitcnt lgkmcnt(3)\n\t" \
  "v_or_b32 v203, v232, v233\n\t" \
  "v_mfma_f32_16x16x32_bf16 v[80:83], v[216:219], v[170:173], v[80:83]\n\t" \
  "ds_read_b64_tr_b16 v[216:217], v249 offset:6288\n\t" \
  "ds_read_b32 v222, v222 offset:10016\n\t" \
  "v_or_b32 v202, v234, v235\n\t" \
  "v_fma_f32 v195, |v195|, v229, v195 mul:4\n\t" \
  "v_fma_f32 v194, |v194|, v228, v194 mul:4\n\t" \
  "v_mfma_f32_16x16x32_bf16 v[12:15], v[208:211], v[146:149], v[12:15]\n\t" \
  "ds_read_b64_tr_b16 v[218:219], v249 offset:7824\n\t" \
  "v_fma_f32 v193, |v193|, v231, v193 mul:4\n\t" \
  "s_waitcnt vmcnt(0)\n\t" \
  "v_fma_f32 v192, |v192|, v230, v192 mul:4\n\t" \
  "v_cvt_pk_bf16_f32 v195, v194, v195\n\t" \
  "v_mfma_f32_16x16x32_bf16 v[36:39], v[208:211], v[154:157], v[36:39]\n\t" \
  "ds_read_u16_d16_hi v225, v225 offset:10528\n\t" \
  "v_cvt_pk_bf16_f32 v194, v192, v193\n\t" \
  "v_cvt_pknorm_i16_f32 v228, |v196|, |v197|\n\t" \
  "v_cvt_pknorm_i16_f32 v230, |v198|, |v199|\n\t" \
  "v_mfma_f32_16x16x32_bf16 v[60:63], v[208:211], v[162:165], v[60:63]\n\t" \
  "ds_read_u16 v224, v224 offset:10528\n\t" \
  "v_lshlrev_b32_sdwa v229, 2, v228 dst_sel:DWORD dst_unused:UNUSED_PAD src0_sel:DWORD src1_sel:BYTE_3\n\t" \
  "v_lshlrev_b32_sdwa v228, 2, v228 dst_sel:DWORD dst_unused:UNUSED_PAD src0_sel:DWORD src1_sel:BYTE_1\n\t" \
  "v_lshlrev_b32_sdwa v231, 2, v230 dst_sel:DWORD dst_unused:UNUSED_PAD src0_sel:DWORD src1_sel:BYTE_3\n\t" \
  "v_mfma_f32_16x16x32_bf16 v[84:87], v[208:211], v[170:173], v[84:87]\n\t" \
  "ds_read_u16_d16_hi v227, v227 offset:10528\n\t" \
  "v_lshlrev_b32_sdwa v230, 2, v230 dst_sel:DWORD dst_unused:UNUSED_PAD src0_sel:DWORD src1_sel:BYTE_1\n\t" \
  "v_lshlrev_b32_sdwa v233, 1, v205 dst_sel:DWORD dst_unused:UNUSED_PAD src0_sel:DWORD src1_sel:WORD_1\n\t" \
  "v_lshlrev_b32_sdwa v232, 1, v204 dst_sel:DWORD dst_unused:UNUSED_PAD src0_sel:DWORD src1_sel:WORD_1\n\t" \
  "v_mfma_f32_16x16x32_bf16 v[16:19], v[212:215], v[146:149], v[16:19]\n\t" \
  "ds_read_u16 v226, v226 offset:10528\n\t" \
  "ds_read_b32 v229, v229 offset:10016\n\t" \
  "v_lshlrev_b32_sdwa v235, 1, v207 dst_sel:DWORD dst_unused:UNUSED_PAD src0_sel:DWORD src1_sel:WORD_1\n\t" \
  "v_lshlrev_b32_sdwa v234, 1, v206 dst_sel:DWORD dst_unused:UNUSED_PAD src0_sel:DWORD src1_sel:WORD_1\n\t" \
  "s_waitcnt lgkmcnt(6)\n\t" \
  "v_fma_mix_f32 v221, |v181|, v221, v221 op_sel:[0,1,0] op_sel_hi:[0,1,1] clamp\n\t" \
  "v_mfma_f32_16x16x32_bf16 v[40:43], v[212:215], v[154:157], v[40:43]\n\t" \
  "ds_read_b64_tr_b16 v[208:209], v249 offset:12304\n\t" \
  "ds_read_b32 v228, v228 offset:10016\n\t" \
  "v_fma_mix_f32 v220, |v180|, v220, v220 op_sel:[0,1,0] op_sel_hi:[0,1,1] clamp\n\t" \
  "v_fma_mix_f32 v223, |v183|, v223, v223 op_sel:[0,1,0] op_sel_hi:[0,1,1] clamp\n\t" \
  "v_fma_mix_f32 v222, |v182|, v222, v222 op_sel:[0,1,0] op_sel_hi:[0,1,1] clamp\n\t" \
  "s_waitcnt lgkmcnt(3)\n\t" \
  "v_mfma_f32_16x16x32_bf16 v[64:67], v[212:215], v[162:165], v[64:67]\n\t" \
  "ds_read_b64_tr_b16 v[210:211], v249 offset:13840\n\t" \
  "ds_read_b32 v231, v231 offset:10016\n\t" \
  "v_or_b32 v188, v224, v225\n\t" \
  "v_or_b32 v189, v226, v227\n\t" \
  "v_fma_f32 v181, |v181|, v221, v181 mul:4\n\t" \
  "v_mfma_f32_16x16x32_bf16 v[88:91], v[212:215], v[170:173], v[88:91]\n\t" \
  "ds_read_b64_tr_b16 v[212:213], v249 offset:12312\n\t" \
  "ds_read_b32 v230, v230 offset:10016\n\t" \
  "v_fma_f32 v180, |v180|, v220, v180 mul:4\n\t" \
  "v_fma_f32 v183, |v183|, v223, v183 mul:4\n\t" \
  "v_fma_f32 v182, |v182|, v222, v182 mul:4\n\t" \
  "v_mfma_f32_16x16x32_bf16 v[20:23], v[216:219], v[146:149], v[20:23]\n\t" \
  "ds_read_b64_tr_b16 v[214:215], v249 offset:13848\n\t" \
  "v_cvt_pk_bf16_f32 v180, v180, v181\n\t" \
  "v_cvt_pk_bf16_f32 v181, v182, v183\n\t" \
  "s_waitcnt lgkmcnt(1)\n\t" \
  "v_fma_mix_f32 v229, |v197|, v229, v229 op_sel:[0,1,0] op_sel_hi:[0,1,1] clamp\n\t" \
  "v_fma_mix_f32 v228, |v196|, v228, v228 op_sel:[0,1,0] op_sel_hi:[0,1,1] clamp\n\t" \
  "v_mfma_f32_16x16x32_bf16 v[44:47], v[216:219], v[154:157], v[44:47]\n\t" \
  "ds_read_u16_d16_hi v233, v233 offset:10528\n\t" \
  "ds_read_u16 v232, v232 offset:10528\n\t" \
  "ds_read_u16_d16_hi v235, v235 offset:10528\n\t" \
  "ds_read_u16 v234, v234 offset:10528\n\t" \
  "v_fma_mix_f32 v231, |v199|, v231, v231 op_sel:[0,1,0] op_sel_hi:[0,1,1] clamp\n\t" \
  "v_fma_mix_f32 v230, |v198|, v230, v230 op_sel:[0,1,0] op_sel_hi:[0,1,1] clamp\n\t" \
  "s_waitcnt lgkmcnt(0)\n\t" \
  "v_or_b32 v204, v232, v233\n\t" \
  "v_mfma_f32_16x16x32_bf16 v[68:71], v[216:219], v[162:165], v[68:71]\n\t" \
  "v_or_b32 v205, v234, v235\n\t" \
  "v_fma_f32 v197, |v197|, v229, v197 mul:4\n\t" \
  "v_fma_f32 v196, |v196|, v228, v196 mul:4\n\t" \
  "v_fma_f32 v199, |v199|, v231, v199 mul:4\n\t" \
  "v_mfma_f32_16x16x32_bf16 v[92:95], v[216:219], v[170:173], v[92:95]\n\t" \
  "ds_read_b64_tr_b16 v[216:217], v249 offset:12368\n\t" \
  "v_fma_f32 v198, |v198|, v230, v198 mul:4\n\t" \
  "v_cvt_pk_bf16_f32 v196, v196, v197\n\t" \
  "v_cvt_pk_bf16_f32 v197, v198, v199\n\t" \
  "v_mfma_f32_16x16x32_bf16 v[88:91], v[242:245], v[236:239], v[88:91]\n\t" \
  "ds_read_b64_tr_b16 v[218:219], v249 offset:13904\n\t" \
  "v_mfma_f32_16x16x32_bf16 v[92:95], v[240:243], v[236:239], v[92:95]\n\t" \
  "v_mfma_f32_16x16x32_bf16 v[0:3], v[208:211], v[178:181], v[0:3]\n\t" \
  "v_mfma_f32_16x16x32_bf16 v[24:27], v[208:211], v[186:189], v[24:27]\n\t" \
  "v_mfma_f32_16x16x32_bf16 v[48:51], v[208:211], v[194:197], v[48:51]\n\t" \
  "v_mfma_f32_16x16x32_bf16 v[72:75], v[208:211], v[202:205], v[72:75]\n\t" \
  "ds_read_b64_tr_b16 v[208:209], v249 offset:12376\n\t" \
  "v_mfma_f32_16x16x32_bf16 v[4:7], v[212:215], v[178:181], v[4:7]\n\t" \
  "ds_read_b64_tr_b16 v[210:211], v249 offset:13912\n\t" \
  "v_mfma_f32_16x16x32_bf16 v[28:31], v[212:215], v[186:189], v[28:31]\n\t" \
  "v_mfma_f32_16x16x32_bf16 v[52:55], v[212:215], v[194:197], v[52:55]\n\t" \
  "v_mfma_f32_16x16x32_bf16 v[76:79], v[212:215], v[202:205], v[76:79]\n\t" \
  "ds_read_b64_tr_b16 v[212:213], v249 offset:12432\n\t" \
  "s_waitcnt lgkmcnt(3)\n\t" \
  "v_mfma_f32_16x16x32_bf16 v[8:11], v[216:219], v[178:181], v[8:11]\n\t" \
  "ds_read_b64_tr_b16 v[214:215], v249 offset:13968\n\t" \
  "v_mfma_f32_16x16x32_bf16 v[32:35], v[216:219], v[186:189], v[32:35]\n\t" \
  "v_mfma_f32_16x16x32_bf16 v[56:59], v[216:219], v[194:197], v[56:59]\n\t" \
  "v_mfma_f32_16x16x32_bf16 v[80:83], v[216:219], v[202:205], v[80:83]\n\t" \
  "ds_read_b64_tr_b16 v[216:217], v249 offset:12440\n\t" \
  "v_cvt_pk_bf16_f32 v0, v0, v1\n\t" \
  "v_cvt_pk_bf16_f32 v1, v2, v3\n\t" \
  "v_cvt_pk_bf16_f32 v2, v4, v5\n\t" \
  "v_cvt_pk_bf16_f32 v3, v6, v7\n\t" \
  "buffer_store_dwordx4 v[0:3], v250, s[20:23], s61 offen offset:0 sc1\n\t" \
  "v_cvt_pk_bf16_f32 v24, v24, v25\n\t" \
  "v_cvt_pk_bf16_f32 v25, v26, v27\n\t" \
  "v_cvt_pk_bf16_f32 v26, v28, v29\n\t" \
  "v_cvt_pk_bf16_f32 v27, v30, v31\n\t" \
  "buffer_store_dwordx4 v[24:27], v250, s[20:23], s62 offen offset:0 sc1\n\t" \
  "v_cvt_pk_bf16_f32 v48, v48, v49\n\t" \
  "v_cvt_pk_bf16_f32 v49, v50, v51\n\t" \
  "v_cvt_pk_bf16_f32 v50, v52, v53\n\t" \
  "v_cvt_pk_bf16_f32 v51, v54, v55\n\t" \
  "buffer_store_dwordx4 v[48:51], v250, s[20:23], s14 offen offset:0 sc1\n\t" \
  "v_cvt_pk_bf16_f32 v72, v72, v73\n\t" \
  "v_cvt_pk_bf16_f32 v73, v74, v75\n\t" \
  "v_cvt_pk_bf16_f32 v74, v76, v77\n\t" \
  "v_cvt_pk_bf16_f32 v75, v78, v79\n\t" \
  "buffer_store_dwordx4 v[72:75], v250, s[20:23], s15 offen offset:0 sc1\n\t" \
  "s_waitcnt lgkmcnt(3)\n\t" \
  "v_mfma_f32_16x16x32_bf16 v[12:15], v[208:211], v[178:181], v[12:15]\n\t" \
  "ds_read_b64_tr_b16 v[218:219], v249 offset:13976\n\t" \
  "v_mfma_f32_16x16x32_bf16 v[36:39], v[208:211], v[186:189], v[36:39]\n\t" \
  "v_mfma_f32_16x16x32_bf16 v[60:63], v[208:211], v[194:197], v[60:63]\n\t" \
  "v_mfma_f32_16x16x32_bf16 v[84:87], v[208:211], v[202:205], v[84:87]\n\t" \
  "s_waitcnt lgkmcnt(2)\n\t" \
  "v_mfma_f32_16x16x32_bf16 v[16:19], v[212:215], v[178:181], v[16:19]\n\t" \
  "v_mfma_f32_16x16x32_bf16 v[40:43], v[212:215], v[186:189], v[40:43]\n\t" \
  "v_mfma_f32_16x16x32_bf16 v[64:67], v[212:215], v[194:197], v[64:67]\n\t" \
  "v_mfma_f32_16x16x32_bf16 v[88:91], v[212:215], v[202:205], v[88:91]\n\t" \
  "v_cvt_pk_bf16_f32 v8, v8, v9\n\t" \
  "v_cvt_pk_bf16_f32 v9, v10, v11\n\t" \
  "v_cvt_pk_bf16_f32 v10, v12, v13\n\t" \
  "v_cvt_pk_bf16_f32 v11, v14, v15\n\t" \
  "buffer_store_dwordx4 v[8:11], v250, s[20:23], s61 offen offset:64 sc1\n\t" \
  "v_cvt_pk_bf16_f32 v32, v32, v33\n\t" \
  "v_cvt_pk_bf16_f32 v33, v34, v35\n\t" \
  "v_cvt_pk_bf16_f32 v34, v36, v37\n\t" \
  "v_cvt_pk_bf16_f32 v35, v38, v39\n\t" \
  "buffer_store_dwordx4 v[32:35], v250, s[20:23], s62 offen offset:64 sc1\n\t" \
  "v_cvt_pk_bf16_f32 v56, v56, v57\n\t" \
  "v_cvt_pk_bf16_f32 v57, v58, v59\n\t" \
  "v_cvt_pk_bf16_f32 v58, v60, v61\n\t" \
  "v_cvt_pk_bf16_f32 v59, v62, v63\n\t" \
  "buffer_store_dwordx4 v[56:59], v250, s[20:23], s14 offen offset:64 sc1\n\t" \
  "v_cvt_pk_bf16_f32 v80, v80, v81\n\t" \
  "v_cvt_pk_bf16_f32 v81, v82, v83\n\t" \
  "v_cvt_pk_bf16_f32 v82, v84, v85\n\t" \
  "v_cvt_pk_bf16_f32 v83, v86, v87\n\t" \
  "buffer_store_dwordx4 v[80:83], v250, s[20:23], s15 offen offset:64 sc1\n\t" \
  "s_waitcnt lgkmcnt(0)\n\t" \
  "v_mfma_f32_16x16x32_bf16 v[20:23], v[216:219], v[178:181], v[20:23]\n\t" \
  "v_mfma_f32_16x16x32_bf16 v[44:47], v[216:219], v[186:189], v[44:47]\n\t" \
  "v_mfma_f32_16x16x32_bf16 v[68:71], v[216:219], v[194:197], v[68:71]\n\t" \
  "v_mfma_f32_16x16x32_bf16 v[92:95], v[216:219], v[202:205], v[92:95]\n\t" \
  "v_cvt_pk_bf16_f32 v16, v16, v17\n\t" \
  "v_cvt_pk_bf16_f32 v17, v18, v19\n\t" \
  "s_nop 2\n\t" \
  "v_cvt_pk_bf16_f32 v18, v20, v21\n\t" \
  "v_cvt_pk_bf16_f32 v19, v22, v23\n\t" \
  "buffer_store_dwordx4 v[16:19], v250, s[20:23], s61 offen offset:128 sc1\n\t" \
  "v_cvt_pk_bf16_f32 v40, v40, v41\n\t" \
  "v_cvt_pk_bf16_f32 v41, v42, v43\n\t" \
  "v_cvt_pk_bf16_f32 v42, v44, v45\n\t" \
  "v_cvt_pk_bf16_f32 v43, v46, v47\n\t" \
  "buffer_store_dwordx4 v[40:43], v250, s[20:23], s62 offen offset:128 sc1\n\t" \
  "v_cvt_pk_bf16_f32 v64, v64, v65\n\t" \
  "v_cvt_pk_bf16_f32 v65, v66, v67\n\t" \
  "v_cvt_pk_bf16_f32 v66, v68, v69\n\t" \
  "v_cvt_pk_bf16_f32 v67, v70, v71\n\t" \
  "buffer_store_dwordx4 v[64:67], v250, s[20:23], s14 offen offset:128 sc1\n\t" \
  "v_cvt_pk_bf16_f32 v88, v88, v89\n\t" \
  "v_cvt_pk_bf16_f32 v89, v90, v91\n\t" \
  "v_cvt_pk_bf16_f32 v90, v92, v93\n\t" \
  "v_cvt_pk_bf16_f32 v91, v94, v95\n\t" \
  "buffer_store_dwordx4 v[88:91], v250, s[20:23], s15 offen offset:128 sc1\n\t" \
  "s_waitcnt vmcnt(0)\n\t" \
  ".Lcnx_end:\n\t" \
  ""


__device__ __forceinline__ unsigned lds_addr(const char* p) { return (unsigned)(uintptr_t)(lds_ptr_t)p; }
__device__ __forceinline__ unsigned sg(unsigned v) { return __builtin_amdgcn_readfirstlane(v); }

constexpr double tanh_u(double y) {
    const double v = 8 * y, a = 2 * 0.79788456080286536 * (v + 0.044715 * v * v * v) / 1024;
    double m = 1;
    for (int i = 12; i > 1; --i) m = 1 + m * a / i;
    m *= a;
    for (int i = 0; i < 10; ++i) m *= m + 2;
    return m / (m + 2);
}
constexpr double h2d(int h) {
    if (h < 1024) return 0;
    double x = (1024 + (h & 1023)) * 0x1p-24;
    for (int e = 1; e < (h >> 10); ++e) x *= 2;
    return x;
}
constexpr int d2h(double x) {
    int h = 0;
    for (int s = 1 << 14; s; s >>= 1) if (h2d(h + s) <= x) h += s;
    return h;
}
constexpr unsigned ip_entry(int k) {
    if (k == 127) return 0x3c003c00u;
    const double y0 = k ? (256 * k - 0.5) / 32767 : 0, dy = ((256 * k + 255.5) / 32767 - y0) / 8;
    double t[9] = {};
    for (int i = 0; i < 9; ++i) t[i] = tanh_u(y0 + i * dy);
    const double b0 = (t[8] - t[0]) / (8 * dy), ym = y0 + 4 * dy, tm = (t[4] + (t[0] + t[8]) / 2) / 2;
    unsigned best = 0;
    double err = 1;
    for (int ha = d2h(tm - b0 * ym) - 1, i = 0; i < 4; ++i, ++ha)
        for (int hb = d2h((tm - h2d(ha)) / ym), j = 0; j < 2; ++j, ++hb) {
            double e = 0;
            for (int n = 0; n < 9; ++n) {
                const double s = h2d(ha) + h2d(hb) * (y0 + n * dy), d = (s < 1 ? s : 1) - t[n];
                e = d > e ? d : -d > e ? -d : e;
            }
            if (e < err) err = e, best = (ha < 1024 ? 0 : ha) | (hb < 1024 ? 0 : hb) << 16;
        }
    return best;
}
constexpr unsigned g_entry(int e, bool neg) {
    double y = e ? ((e & 127) + 0.5 + (e >> 7 ? 128 : 0)) * 0x1p-23 / (1 + 1.0 / 384) : 0;
    for (int i = 1; i < (e >> 7); ++i) y *= 2;
    const double t = y < 1 ? tanh_u(y) : 1;
    const unsigned u = __builtin_bit_cast(unsigned, (float)(4 * y * (neg ? t - 1 : t + 1)));
    return (u + 0x7fff + (u >> 16 & 1)) >> 16;
}
struct TanhTab { unsigned v[1024]; };
constexpr TanhTab gelu_tab(int k0) {
    TanhTab t = {};
    for (int k = k0; k < k0 + 1024 && k < 128 + GSZ / 2; ++k) {
        const int e = 2 * (k < 2048 ? k - 128 : k - 2048);
        t.v[k - k0] = k < 128 ? ip_entry(k) : g_entry(e, k >= 2048) | g_entry(e + 1, k >= 2048) << 16;
    }
    return t;
}
struct WaveTab { unsigned v192[64][4], v244[64][4], v16[64][4], s32[32]; };
struct RegTab { unsigned v240[64][4], v248[64][4]; WaveTab w[8]; };
constexpr unsigned cpos(unsigned w, unsigned px) { return (w >> 1) * 1024 + (px ^ (w & 2) << 2) * 16 + ((w ^ px >> 4) & 1) * 8; }
constexpr RegTab reg_tab() {
    RegTab t = {};
    for (unsigned l = 0; l < 64; ++l) {
        const unsigned G = l >> 4, n = l & 15, q = n >> 2, p = l & 3;
        t.v240[l][2 + (p >> 1)] = G == q ? (p & 1 ? 0x3f800000u : 0x3f80u) : 0u;
        t.v248[l][0] = WB + (l >> 2) * 32 + (p & 1) * 16 + (p >> 1) * 8;
        t.v248[l][1] = WB + (G & 1) * 3080 + ((G >> 1) * 4 + q) * 192 + p * 16;
        t.v248[l][2] = (n & 7) * 384 + (n >> 3) * 192 + G * 16;
        t.v248[l][3] = PB + l * 8;
        for (unsigned w = 0; w < 8; ++w) {
            t.w[w].v244[l][2] = XOFF + n * 400 + (G & 1) * XROW + (G >> 1) * 192 + w * 24;
            t.w[w].v244[l][3] = dw_pos(3 * w) + p * 192 + (1 - (q >> 1)) * 24 + (1 + (G & 1) - (q & 1)) * 8 + ((G ^ p) >> 1) * 4;
            const unsigned pl = PTOP + (w >> 2) * 12288, blk = (w & 3) * 16;
            t.w[w].v192[l][0] = XOFF + cpos(w, (G & 2) * 16 + (n & 8) * 2 + (G & 1) * 8 + (n & 7));
            t.w[w].v192[l][1] = t.w[w].v192[l][0] + PTOP - XOFF;
            t.w[w].v192[l][2] = pl + (cpos((G & 1) * 4 + (G & 2), blk + n) & ~8u);
            t.w[w].v192[l][3] = pl + cpos(p, blk + (l >> 2));
            t.w[w].v16[l][0] = w >= 4 ? (l < 48 ? l * 128 : OOB)
                             : w < 2  ? (12 * ((l >> 3) & 3) + ((l >> 1) & 3)) * (HID * 2) + (2 * (l & 1) + (l >> 5)) * 16 : l * 16;
            const unsigned d = dw_pos(w * 8 + (l >> 5) * 4 + p) + ((l >> 2) & 7) * 24;
            t.w[w].v16[l][1] = w < 3 ? d + (l & 1) * 192 : (l & 4 ? DW2 + 11 * 768 : 13 * 768) + p * 8;
            t.w[w].v16[l][2] = d + (~l & 1) * 192;
            t.w[w].v16[l][3] = l & 1 ? 0x07060302u : 0x05040100u;
        }
    }
    for (unsigned w = 0; w < 8; ++w) {
        const bool w1 = w < 2;
        const unsigned wpd = w1 ? 4 * HID * 2 - 1024 : 0, wl = WB + (w & 1) * (w1 ? 3072 : 3080), wc = w1 ? 64 : 6144;
        const unsigned so = (w & 1) * (w1 ? 48 * HID * 2 : 3072);
        const unsigned s[32] = {wc, so, so + wpd, so + 2 * wpd, w, w >> 2, w, w * 512,
                                0xaaaaaaaau, 0xaaaaaaaau, 0xccccccccu, 0xccccccccu,
                                wl + (w1 ? WA + 12304 : WA), wl + (w1 ? 0 : WA + 6152), wl + (w1 ? WA + 18448 : 6152),
                                wl + (w1 ? WA + 18448 + 6144 : 12304), w1 ? 12 * wc : 0, w1 ? 0 : 12 * wc,
                                0u + XOFF - WB, 3 * wc, ~0x00f0000fu, ~0xf0000f00u,
                                (w >> 2 ? PTOP - 12288u : XHI + 6 * XROW) - XOFF - 4 * 12288, 0x07060302, dw_pos(3 * w + 2) - dw_pos(3 * w), w < 4 ? ~0u : 3072, 0, 0, 0,
                                OOB, OOB, 8 * (unsigned)sizeof(WaveTab) - 2 * PB};
        for (int i = 0; i < 32; ++i) t.w[w].s32[i] = s[i];
    }
    return t;
}
struct Tables { RegTab r; TanhTab t[4]; };
static_assert(sizeof(WaveTab) == 64 * 50 && sizeof(RegTab) == 27648, "table layout");
__device__ const __attribute__((aligned(128))) Tables TABLES = {reg_tab(), {gelu_tab(0), gelu_tab(1024), gelu_tab(2048), gelu_tab(3072)}};

#define R10(r, a) r #a "0", r #a "1", r #a "2", r #a "3", r #a "4", r #a "5", r #a "6", r #a "7", r #a "8", r #a "9"
#define V10(a) R10("v", a)
#define S10(a) R10("s", a)
#define MAIN_CLOB "memory", "scc", "vcc", "v1", "v2", "v3", "v4", "v5", "v6", "v7", "v8", "v9", V10(1), V10(2), V10(3), V10(4), V10(5),  \
    V10(6), V10(7), V10(8), V10(9), V10(10), V10(11), V10(12), V10(13), V10(14), V10(15), V10(16), V10(17), V10(18), V10(19),        \
    V10(20), V10(21), V10(22), V10(23), V10(24), "v250", "v251", "v252", "v253", "v254", "s4", "s5", "s8", "s9", S10(1),       \
    S10(2), S10(3), S10(4), S10(5), "s60", "s61", "s62", "s63", "s64", "s65", "s66", "s67", "s68", "s69"

__global__ __launch_bounds__(512, 2) void convnext_kernel(const __hip_bfloat16* __restrict__ x,
                                                          const __hip_bfloat16* __restrict__ dw,
                                                          const __hip_bfloat16* __restrict__ w1,
                                                          const __hip_bfloat16* __restrict__ w2,
                                                          __hip_bfloat16* __restrict__ out) {
    __shared__ __attribute__((aligned(16))) char smem[LDS_BYTES];

    unsigned tid = threadIdx.x, lds = lds_addr(smem);
    const unsigned bid = blockIdx.x;
    asm volatile(MAIN_ASM
                 : "+{v0}"(tid), "+{s3}"(lds)
                 : "{s[0:1]}"(__builtin_amdgcn_kernarg_segment_ptr()), "{s2}"(bid), "{s[6:7]}"(TABLES.r.w)
                 : MAIN_CLOB);

}

}

extern "C" void kernel_launch(void* const* d_in, const int* in_sizes, int n_in, void* d_out, int out_size,
                              void* d_ws, size_t ws_size, hipStream_t stream) {
    (void)in_sizes; (void)n_in; (void)out_size; (void)d_ws; (void)ws_size;
    const __hip_bfloat16* x = (const __hip_bfloat16*)d_in[0];
    const __hip_bfloat16* dw = (const __hip_bfloat16*)d_in[1];
    const __hip_bfloat16* w1 = (const __hip_bfloat16*)d_in[2];
    const __hip_bfloat16* w2 = (const __hip_bfloat16*)d_in[3];
    __hip_bfloat16* out = (__hip_bfloat16*)d_out;
    convnext_kernel<<<256, 512, 0, stream>>>(x, dw, w1, w2, out);
}
```

```cpp
#include <hip/hip_runtime.h>
#include <hip/hip_bf16.h>
#include <stdint.h>

namespace {

typedef __attribute__((address_space(3))) void* lds_ptr_t;

constexpr int IMG = 512;
constexpr int CH = 96;
constexpr int HID = 384;
constexpr int ROWB = IMG * CH * 2;
constexpr int IPOFF = 13 * 768 + 32;
constexpr int GOFF = IPOFF + 512;
constexpr int GSZ = 30 * 256;
constexpr int DW2 = GOFF + GSZ;
constexpr int XROW = 8200;
constexpr int XOFF = GOFF + 65536 - 6 * XROW;
constexpr int XHI = XOFF + GSZ;
constexpr int PB = XHI + 10 * XROW;
constexpr int WB = XHI + 9 * XROW;
constexpr int WA = 3 * XROW;
constexpr int PTOP = XHI + 12 * XROW - 12288;
constexpr int CNTOFF = WB + WA + 18448 + 12288;
constexpr int LDS_BYTES = 160 * 1024;
static_assert(XOFF == 26864 && CNTOFF - PB == 47136 && DW2 + 11 * 768 + 32 <= XOFF && CNTOFF + 144 <= LDS_BYTES, "LDS map");
constexpr unsigned dw_pos(unsigned u) { return u < 12 ? u * 768 : u == 14 ? 12 * 768 : DW2 + (u - 12 - (u > 14)) * 768; }
constexpr unsigned x_pos(unsigned r) { return (r < 6 ? XOFF : XHI) + r * XROW; }
constexpr unsigned OOB = 0x40000000u;

#define MAIN_ASM \
  "s_load_dwordx2 s[8:9], s[0:1], 0x0\n\t" \
  "s_load_dwordx2 s[12:13], s[0:1], 0x8\n\t" \
  "s_load_dwordx2 s[20:21], s[0:1], 0x20\n\t" \
  "s_load_dwordx4 s[32:35], s[0:1], 0x10\n\t" \
  "v_readfirstlane_b32 s5, v0\n\t" \
  "v_and_b32 v21, 63, v0\n\t" \
  "s_mov_b32 m0, 107744\n\t" \
  "s_cmp_lt_u32 s5, 256\n\t" \
  "s_cselect_b32 s3, 16, 24\n\t" \
  "v_lshlrev_b32 v22, 4, v21\n\t" \
  "s_load_dwordx2 s[16:17], s[0:1], s3\n\t" \
  "s_mul_i32 s4, s5, 50\n\t" \
  "s_add_u32 s66, s6, s4\n\t" \
  "s_addc_u32 s67, s7, 0\n\t" \
  "global_load_dwordx4 v[244:247], v22, s[66:67] offset:1024\n\t" \
  "global_load_dwordx4 v[16:19], v22, s[66:67] offset:2048\n\t" \
  "global_load_dwordx4 v[240:243], v22, s[6:7] offset:-2048\n\t" \
  "global_load_dwordx4 v[248:251], v22, s[6:7] offset:-1024\n\t" \
  "global_load_dwordx4 v[192:195], v22, s[66:67]\n\t" \
  "s_cmp_lg_u32 s5, 192\n\t" \
  "s_cbranch_scc1 .Lcnx_nc\n\t" \
  "s_getpc_b64 s[14:15]\n\t" \
  ".Lcnx_pc:\n\t" \
  "s_lshr_b32 s3, s2, 3\n\t" \
  "s_lshl_b32 s3, s3, 11\n\t" \
  "v_lshl_add_u32 v1, v21, 5, s3\n\t" \
  "v_min_u32 v1, .Lcnx_end-.Lcnx_pc, v1\n\t" \
  "global_load_lds_dword v1, s[14:15]\n\t" \
  ".Lcnx_nc:\n\t" \
  "v_mul_u32_u24 v254, 0x147b, v0\n\t" \
  "s_bfe_u32 s27, s2, 0x40003\n\t" \
  "v_bfe_u32 v1, v0, 2, 3\n\t" \
  "s_mul_i32 s27, s27, 6144\n\t" \
  "v_lshrrev_b32 v254, 17, v254\n\t" \
  "s_sub_u32 s3, s27, 576\n\t" \
  "v_and_b32 v20, 3, v0\n\t" \
  "s_add_u32 s4, s27, 6720\n\t" \
  "v_sub_u32 v254, v0, v254\n\t" \
  "s_min_u32 s4, s4, 0x18000\n\t" \
  "v_and_b32 v2, 0x1e0, v0\n\t" \
  "s_and_b32 s24, s2, 7\n\t" \
  "v_lshl_add_u32 v254, v254, 4, s3\n\t" \
  "s_lshl_b32 s24, s24, 6\n\t" \
  "v_lshl_or_b32 v20, v20, 3, v2\n\t" \
  "s_mov_b32 s26, 0x18000\n\t" \
  "v_cmp_gt_u32 vcc, s4, v254\n\t" \
  "s_cmp_ge_u32 s2, 128\n\t" \
  "s_cselect_b32 s3, 7, 0\n\t" \
  "s_cselect_b32 s4, 0, 7\n\t" \
  "v_add_u32 v1, s3, v1\n\t" \
  "s_cselect_b32 s3, 28, 35\n\t" \
  "v_cndmask_b32 v254, 2.0, v254, vcc\n\t" \
  "s_cselect_b32 s26, s26, 0xfffe8000\n\t" \
  "v_xor_b32 v1, s4, v1\n\t" \
  "s_add_u32 s24, s24, s3\n\t" \
  "v_and_b32 v1, 7, v1\n\t" \
  "s_movk_i32 s3, 1344\n\t" \
  "s_mul_i32 s24, s24, 0x18000\n\t" \
  "v_mad_u32_u24 v1, v1, s3, v20\n\t" \
  "s_lshl4_add_u32 s25, s5, 26864\n\t" \
  "s_mov_b32 s10, 0x3000000\n\t" \
  "s_mov_b32 s11, 0x20000\n\t" \
  "s_mov_b32 s14, 9408\n\t" \
  "s_mov_b32 s15, 0x20000\n\t" \
  "s_mov_b32 s18, 73728\n\t" \
  "s_mov_b32 s19, 0x20000\n\t" \
  "s_mov_b32 s22, 0x3000000\n\t" \
  "s_mov_b32 s23, 0x20000\n\t" \
  "s_add_u32 s3, s24, s26\n\t" \
  "s_add_u32 s3, s3, s27\n\t" \
  "s_mul_i32 s4, s26, 10\n\t" \
  "v_mov_b32 v24, s3\n\t" \
  "v_mad_i32_i24 v27, v21, s4, v24\n\t" \
  "s_setreg_imm32_b32 hwreg(HW_REG_MODE, 4, 2), 0\n\t" \
  "s_setreg_imm32_b32 hwreg(HW_REG_MODE, 9, 1), 0\n\t" \
  "s_lshl_b32 s65, s26, 4\n\t" \
  "s_lshr_b32 s3, s5, 7\n\t" \
  "s_add_u32 s3, s3, 8\n\t" \
  "s_mul_i32 s3, s3, s26\n\t" \
  "s_add_u32 s29, s24, s27\n\t" \
  "s_add_u32 s29, s29, s3\n\t" \
  "s_and_b32 s3, s5, 64\n\t" \
  "s_mul_i32 s3, s3, 48\n\t" \
  "s_add_u32 s29, s29, s3\n\t" \
  "s_mul_i32 s3, s26, 9\n\t" \
  "s_add_u32 s27, s24, s65\n\t" \
  "s_mov_b32 s30, 0x01010101\n\t" \
  "s_mov_b32 s31, 0x01010101\n\t" \
  "s_add_u32 s27, s27, s3\n\t" \
  "s_mul_i32 s64, s26, 22\n\t" \
  "s_sub_u32 s64, s64, s65\n\t" \
  "s_cmp_ge_u32 s5, 256\n\t" \
  "s_cselect_b32 s3, -6, -2\n\t" \
  "s_mul_i32 s3, s3, s26\n\t" \
  "s_add_u32 s28, s29, s3\n\t" \
  "s_mov_b64 vcc, 0\n\t" \
  "s_waitcnt lgkmcnt(0)\n\t" \
  "v_add_co_u32 v24, vcc, s8, v27\n\t" \
  "v_mov_b32 v25, s9\n\t" \
  "v_addc_co_u32 v25, vcc, 0, v25, vcc\n\t" \
  "v_writelane_b32 v24, s12, 3\n\t" \
  "v_writelane_b32 v25, s13, 3\n\t" \
  "v_writelane_b32 v24, s32, 4\n\t" \
  "v_writelane_b32 v25, s33, 4\n\t" \
  "v_writelane_b32 v24, s34, 5\n\t" \
  "v_writelane_b32 v25, s35, 5\n\t" \
  "s_mov_b64 exec, 63\n\t" \
  "global_load_dword v26, v[24:25], off\n\t" \
  "s_mov_b64 exec, -1\n\t" \
  "s_mov_b64 vcc, 0\n\t" \
  "s_and_b32 s13, s13, 0xffff\n\t" \
  "s_and_b32 s9, s9, 0xffff\n\t" \
  "s_and_b32 s17, s17, 0xffff\n\t" \
  "s_cmp_ge_u32 s5, 192\n\t" \
  "s_cbranch_scc1 .Lcnx_nd\n\t" \
  "buffer_load_dwordx2 v[2:3], v1, s[12:15], 0 offen offset:0\n\t" \
  "buffer_load_dwordx2 v[4:5], v1, s[12:15], 0 offen offset:192\n\t" \
  "buffer_load_dwordx2 v[6:7], v1, s[12:15], 0 offen offset:384\n\t" \
  "buffer_load_dwordx2 v[8:9], v1, s[12:15], 0 offen offset:576\n\t" \
  "buffer_load_dwordx2 v[10:11], v1, s[12:15], 0 offen offset:768\n\t" \
  "buffer_load_dwordx2 v[12:13], v1, s[12:15], 0 offen offset:960\n\t" \
  "buffer_load_dwordx2 v[14:15], v1, s[12:15], 0 offen offset:1152\n\t" \
  ".Lcnx_nd:\n\t" \
  "s_and_b32 s3, s5, 0xc0\n\t" \
  "s_cmp_lg_u32 s3, 64\n\t" \
  "s_cbranch_scc1 .Lcnx_nw\n\t" \
  "s_lshr_b32 s3, s2, 3\n\t" \
  "s_mul_i32 s3, s3, 2304\n\t" \
  "s_mov_b32 m0, 107744\n\t" \
  "v_mul_u32_u24 v21, 36, v21\n\t" \
  "buffer_load_dword v21, s[16:19], s3 offen lds\n\t" \
  ".Lcnx_nw:\n\t" \
  "s_add_u32 m0, s25, 0\n\t" \
  "s_add_u32 s24, s24, s26\n\t" \
  "buffer_load_dwordx4 v254, s[8:11], s24 offen lds\n\t" \
  "s_add_u32 m0, s25, 8200\n\t" \
  "s_add_u32 s24, s24, s26\n\t" \
  "buffer_load_dwordx4 v254, s[8:11], s24 offen lds\n\t" \
  "s_add_u32 m0, s25, 16400\n\t" \
  "s_add_u32 s24, s24, s26\n\t" \
  "buffer_load_dwordx4 v254, s[8:11], s24 offen lds\n\t" \
  "s_add_u32 m0, s25, 24600\n\t" \
  "s_add_u32 s24, s24, s26\n\t" \
  "buffer_load_dwordx4 v254, s[8:11], s24 offen lds\n\t" \
  "s_load_dwordx16 s[32:47], s[66:67], 0xc00\n\t" \
  "s_load_dwordx16 s[48:63], s[66:67], 0xc40\n\t" \
  "s_and_b32 s21, s21, 0xffff\n\t" \
  "s_waitcnt vmcnt(5)\n\t" \
  "v_mfma_f32_32x32x16_bf16 v[144:159], v[242:245], v[240:243], 0\n\t" \
  "v_mfma_f32_32x32x16_bf16 v[160:175], v[242:245], v[240:243], 0\n\t" \
  "v_bfe_u32 v234, v251, 3, 2\n\t" \
  "v_mul_u32_u24 v234, 0x5555, v234\n\t" \
  "v_mov_b32 v252, v16\n\t" \
  "s_lshl2_add_u32 s12, s26, s29\n\t" \
  "s_lshl3_add_u32 s13, s26, s29\n\t" \
  "v_mov_b32 v253, v246\n\t" \
  "s_mov_b32 s68, 0x07060302\n\t" \
  "s_cmp_gt_u32 s5, 192\n\t" \
  "s_cbranch_scc1 .Lcnx_e2\n\t" \
  "s_cmp_eq_u32 s5, 192\n\t" \
  "s_cbranch_scc1 .Lcnx_e1\n\t" \
  "v_xor_b32 v23, 0x2b802b80, v19\n\t" \
  "s_waitcnt vmcnt(4)\n\t" \
  "v_dot2_f32_bf16 v48, v2, v19, 0\n\t" \
  "v_dot2_f32_bf16 v50, v4, v19, 0\n\t" \
  "v_dot2_f32_bf16 v52, v6, v19, 0\n\t" \
  "v_dot2_f32_bf16 v54, v8, v19, 0\n\t" \
  "v_dot2_f32_bf16 v56, v10, v19, 0\n\t" \
  "v_dot2_f32_bf16 v58, v12, v19, 0\n\t" \
  "v_dot2_f32_bf16 v60, v14, v19, 0\n\t" \
  "v_dot2_f32_bf16 v2, v2, v23, 0\n\t" \
  "v_dot2_f32_bf16 v4, v4, v23, 0\n\t" \
  "v_dot2_f32_bf16 v6, v6, v23, 0\n\t" \
  "v_dot2_f32_bf16 v8, v8, v23, 0\n\t" \
  "v_dot2_f32_bf16 v10, v10, v23, 0\n\t" \
  "v_dot2_f32_bf16 v12, v12, v23, 0\n\t" \
  "v_dot2_f32_bf16 v14, v14, v23, 0\n\t" \
  "v_perm_b32 v24, v50, 0, s68\n\t" \
  "v_perm_b32 v25, v58, v54, s68\n\t" \
  "v_perm_b32 v26, v52, v48, s68\n\t" \
  "v_perm_b32 v27, v60, v56, s68\n\t" \
  "v_perm_b32 v28, v54, v50, s68\n\t" \
  "v_perm_b32 v29, 0, v58, s68\n\t" \
  "ds_write_b64 v17, v[24:25] offset:0\n\t" \
  "ds_write_b64 v17, v[26:27] offset:8\n\t" \
  "ds_write_b64 v17, v[28:29] offset:16\n\t" \
  "v_dot2_f32_bf16 v49, v3, v19, 0\n\t" \
  "v_dot2_f32_bf16 v51, v5, v19, 0\n\t" \
  "v_dot2_f32_bf16 v53, v7, v19, 0\n\t" \
  "v_dot2_f32_bf16 v55, v9, v19, 0\n\t" \
  "v_dot2_f32_bf16 v57, v11, v19, 0\n\t" \
  "v_dot2_f32_bf16 v59, v13, v19, 0\n\t" \
  "v_dot2_f32_bf16 v61, v15, v19, 0\n\t" \
  "v_perm_b32 v30, v4, 0, s68\n\t" \
  "v_perm_b32 v31, v12, v8, s68\n\t" \
  "v_perm_b32 v32, v6, v2, s68\n\t" \
  "v_perm_b32 v33, v14, v10, s68\n\t" \
  "v_perm_b32 v34, v8, v4, s68\n\t" \
  "v_perm_b32 v35, 0, v12, s68\n\t" \
  "ds_write_b64 v18, v[30:31] offset:0\n\t" \
  "ds_write_b64 v18, v[32:33] offset:8\n\t" \
  "ds_write_b64 v18, v[34:35] offset:16\n\t" \
  "v_dot2_f32_bf16 v3, v3, v23, 0\n\t" \
  "v_dot2_f32_bf16 v5, v5, v23, 0\n\t" \
  "v_dot2_f32_bf16 v7, v7, v23, 0\n\t" \
  "v_dot2_f32_bf16 v9, v9, v23, 0\n\t" \
  "v_dot2_f32_bf16 v11, v11, v23, 0\n\t" \
  "v_dot2_f32_bf16 v13, v13, v23, 0\n\t" \
  "v_dot2_f32_bf16 v15, v15, v23, 0\n\t" \
  "v_perm_b32 v37, v51, 0, s68\n\t" \
  "v_perm_b32 v36, v59, v55, s68\n\t" \
  "v_perm_b32 v39, v53, v49, s68\n\t" \
  "v_perm_b32 v38, v61, v57, s68\n\t" \
  "v_perm_b32 v41, v55, v51, s68\n\t" \
  "v_perm_b32 v40, 0, v59, s68\n\t" \
  "ds_write_b64 v17, v[36:37] offset:384\n\t" \
  "ds_write_b64 v17, v[38:39] offset:392\n\t" \
  "ds_write_b64 v17, v[40:41] offset:400\n\t" \
  "v_perm_b32 v43, v5, 0, s68\n\t" \
  "v_perm_b32 v42, v13, v9, s68\n\t" \
  "v_perm_b32 v45, v7, v3, s68\n\t" \
  "v_perm_b32 v44, v15, v11, s68\n\t" \
  "v_perm_b32 v47, v9, v5, s68\n\t" \
  "v_perm_b32 v46, 0, v13, s68\n\t" \
  "ds_write_b64 v18, v[42:43] offset:384\n\t" \
  "ds_write_b64 v18, v[44:45] offset:392\n\t" \
  "ds_write_b64 v18, v[46:47] offset:400\n\t" \
  "s_branch .Lcnx_e3\n\t" \
  ".Lcnx_e1:\n\t" \
  "s_mov_b64 exec, 0x3ffff\n\t" \
  "ds_write_b64 v251, v[240:241] offset:47136\n\t" \
  "s_mov_b64 exec, 0xff\n\t" \
  "ds_write_b64 v17, v[240:241]\n\t" \
  "s_mov_b64 exec, -1\n\t" \
  "s_branch .Lcnx_e3\n\t" \
  ".Lcnx_e2:\n\t" \
  "s_setprio 1\n\t" \
  ".Lcnx_e3:\n\t" \
  "s_add_u32 m0, s25, 32800\n\t" \
  "s_add_u32 s24, s24, s26\n\t" \
  "buffer_load_dwordx4 v254, s[8:11], s24 offen lds\n\t" \
  "s_add_u32 m0, s25, 41000\n\t" \
  "s_add_u32 s24, s24, s26\n\t" \
  "buffer_load_dwordx4 v254, s[8:11], s24 offen lds\n\t" \
  "s_add_u32 m0, s25, 56880\n\t" \
  "s_add_u32 s24, s24, s26\n\t" \
  "buffer_load_dwordx4 v254, s[8:11], s24 offen lds\n\t" \
  "s_add_u32 m0, s25, 65080\n\t" \
  "s_add_u32 s24, s24, s26\n\t" \
  "buffer_load_dwordx4 v254, s[8:11], s24 offen lds\n\t" \
  "s_waitcnt vmcnt(6) lgkmcnt(0)\n\t" \
  "s_barrier\n\t" \
  "s_sub_u32 s66, s66, 233088\n\t" \
  "s_subb_u32 s67, s67, 0\n\t" \
  "v_add_u32 v232, s56, v247\n\t" \
  "ds_read2_b32 v[98:99], v247 offset0:0 offset1:6\n\t" \
  "ds_read2_b32 v[114:115], v247 offset0:192 offset1:198\n\t" \
  "ds_read2_b32 v[130:131], v232 offset0:0 offset1:6\n\t" \
  "ds_read2_b32 v[102:103], v247 offset0:12 offset1:18\n\t" \
  "ds_read2_b32 v[118:119], v247 offset0:204 offset1:210\n\t" \
  "ds_read2_b32 v[134:135], v232 offset0:12 offset1:18\n\t" \
  "ds_read2_b32 v[106:107], v247 offset0:24 offset1:30\n\t" \
  "ds_read2_b32 v[122:123], v247 offset0:216 offset1:222\n\t" \
  "ds_read2_b32 v[138:139], v232 offset0:24 offset1:30\n\t" \
  "ds_read2_b32 v[110:111], v247 offset0:36 offset1:42\n\t" \
  "ds_read2_b32 v[126:127], v247 offset0:228 offset1:234\n\t" \
  "ds_read2_b32 v[142:143], v232 offset0:36 offset1:42\n\t" \
  "v_add_u32 v232, 56880, v253\n\t" \
  "v_add_u32 v233, s39, v251\n\t" \
  "v_mfma_f32_32x32x16_bf16 v[176:191], v[242:245], v[240:243], 0\n\t" \
  "ds_read_b64 v[200:201], v253 offset:0\n\t" \
  "ds_read_b64 v[202:203], v253 offset:400\n\t" \
  "ds_read_b64 v[204:205], v253 offset:800\n\t" \
  "ds_read_b64 v[206:207], v253 offset:1200\n\t" \
  "ds_read_b64 v[208:209], v253 offset:8\n\t" \
  "ds_read_b64 v[210:211], v253 offset:408\n\t" \
  "ds_read_b64 v[212:213], v253 offset:808\n\t" \
  "ds_read_b64 v[214:215], v253 offset:1208\n\t" \
  "ds_read_b64 v[216:217], v253 offset:16\n\t" \
  "ds_read_b64 v[218:219], v253 offset:416\n\t" \
  "ds_read_b64 v[220:221], v253 offset:816\n\t" \
  "ds_read_b64 v[222:223], v253 offset:1216\n\t" \
  "s_add_u32 m0, s25, 73280\n\t" \
  "s_add_u32 s24, s24, s26\n\t" \
  "buffer_load_dwordx4 v254, s[8:11], s24 offen lds\n\t" \
  "s_add_u32 m0, s25, 81480\n\t" \
  "s_add_u32 s24, s24, s26\n\t" \
  "buffer_load_dwordx4 v254, s[8:11], s24 offen lds\n\t" \
  "s_add_u32 m0, s25, 89680\n\t" \
  "s_add_u32 s24, s24, s26\n\t" \
  "buffer_load_dwordx4 v254, s[8:11], s24 offen lds\n\t" \
  "s_add_u32 m0, s25, 97880\n\t" \
  "s_add_u32 s24, s24, s26\n\t" \
  "buffer_load_dwordx4 v254, s[8:11], s24 offen lds\n\t" \
  "s_waitcnt lgkmcnt(15)\n\t" \
  "v_and_b32 v96, 0xffff, v98\n\t" \
  "v_and_b32 v97, 0xffff0000, v98\n\t" \
  "v_and_b32 v98, 0xffff, v99\n\t" \
  "v_and_b32 v99, 0xffff0000, v99\n\t" \
  "v_and_b32 v112, 0xffff, v114\n\t" \
  "v_and_b32 v113, 0xffff0000, v114\n\t" \
  "v_and_b32 v114, 0xffff, v115\n\t" \
  "v_and_b32 v115, 0xffff0000, v115\n\t" \
  "v_and_b32 v128, 0xffff, v130\n\t" \
  "v_and_b32 v129, 0xffff0000, v130\n\t" \
  "v_and_b32 v130, 0xffff, v131\n\t" \
  "v_and_b32 v131, 0xffff0000, v131\n\t" \
  "s_waitcnt lgkmcnt(8)\n\t" \
  "v_smfmac_f32_16x16x64_bf16 v[144:147], v[96:99], v[200:207], v234\n\t" \
  "v_and_b32 v100, 0xffff, v102\n\t" \
  "v_and_b32 v101, 0xffff0000, v102\n\t" \
  "v_and_b32 v102, 0xffff, v103\n\t" \
  "v_and_b32 v103, 0xffff0000, v103\n\t" \
  "s_waitcnt vmcnt(8) lgkmcnt(0)\n\t" \
  "s_barrier\n\t" \
  "ds_read_b64 v[224:225], v253 offset:16400\n\t" \
  "ds_read_b64 v[226:227], v253 offset:16800\n\t" \
  "ds_read_b64 v[228:229], v253 offset:17200\n\t" \
  "ds_read_b64 v[230:231], v253 offset:17600\n\t" \
  "v_smfmac_f32_16x16x64_bf16 v[148:151], v[112:115], v[208:215], v234\n\t" \
  "ds_read_b64 v[200:201], v253 offset:16408\n\t" \
  "ds_read_b64 v[202:203], v253 offset:16808\n\t" \
  "ds_read_b64 v[204:205], v253 offset:17208\n\t" \
  "ds_read_b64 v[206:207], v253 offset:17608\n\t" \
  "v_and_b32 v116, 0xffff, v118\n\t" \
  "v_and_b32 v117, 0xffff0000, v118\n\t" \
  "v_and_b32 v118, 0xffff, v119\n\t" \
  "v_and_b32 v119, 0xffff0000, v119\n\t" \
  "v_smfmac_f32_16x16x64_bf16 v[152:155], v[128:131], v[216:223], v234\n\t" \
  "ds_read_b64 v[208:209], v253 offset:16416\n\t" \
  "ds_read_b64 v[210:211], v253 offset:16816\n\t" \
  "ds_read_b64 v[212:213], v253 offset:17216\n\t" \
  "ds_read_b64 v[214:215], v253 offset:17616\n\t" \
  "v_and_b32 v132, 0xffff, v134\n\t" \
  "v_and_b32 v133, 0xffff0000, v134\n\t" \
  "v_and_b32 v134, 0xffff, v135\n\t" \
  "v_and_b32 v135, 0xffff0000, v135\n\t" \
  "v_lshlrev_b32 v235, 1, v251\n\t" \
  "v_and_b32 v236, 0x70, v235\n\t" \
  "v_bfe_u32 v235, v250, 6, 1\n\t" \
  "v_lshl_add_u32 v235, v235, 4, v250\n\t" \
  "s_waitcnt lgkmcnt(8)\n\t" \
  "v_smfmac_f32_16x16x64_bf16 v[156:159], v[96:99], v[224:231], v234\n\t" \
  "v_and_b32 v104, 0xffff, v106\n\t" \
  "v_and_b32 v105, 0xffff0000, v106\n\t" \
  "v_and_b32 v106, 0xffff, v107\n\t" \
  "v_and_b32 v107, 0xffff0000, v107\n\t" \
  "v_add3_u32 v235, v235, v236, s61\n\t" \
  "s_cmp_eq_u32 s37, 1\n\t" \
  "s_cselect_b64 s[68:69], -1, 0\n\t" \
  "v_smfmac_f32_16x16x64_bf16 v[144:147], v[100:103], v[224:231], v234\n\t" \
  "s_waitcnt vmcnt(5) lgkmcnt(0)\n\t" \
  "s_barrier\n\t" \
  "ds_read_b64 v[216:217], v253 offset:32800\n\t" \
  "ds_read_b64 v[218:219], v253 offset:33200\n\t" \
  "ds_read_b64 v[220:221], v253 offset:33600\n\t" \
  "ds_read_b64 v[222:223], v253 offset:34000\n\t" \
  "s_add_u32 m0, s25, 0\n\t" \
  "s_add_u32 s24, s24, s26\n\t" \
  "v_smfmac_f32_16x16x64_bf16 v[160:163], v[112:115], v[200:207], v234\n\t" \
  "buffer_load_dwordx4 v254, s[8:11], s24 offen lds\n\t" \
  "ds_read_b64 v[224:225], v253 offset:32808\n\t" \
  "ds_read_b64 v[226:227], v253 offset:33208\n\t" \
  "v_and_b32 v120, 0xffff, v122\n\t" \
  "v_and_b32 v121, 0xffff0000, v122\n\t" \
  "v_and_b32 v122, 0xffff, v123\n\t" \
  "v_and_b32 v123, 0xffff0000, v123\n\t" \
  "v_smfmac_f32_16x16x64_bf16 v[148:151], v[116:119], v[200:207], v234\n\t" \
  "ds_read_b64 v[228:229], v253 offset:33608\n\t" \
  "ds_read_b64 v[230:231], v253 offset:34008\n\t" \
  "ds_read_b64 v[76:77], v235 offset:0\n\t" \
  "ds_read_b64 v[78:79], v235 offset:8\n\t" \
  "s_add_u32 m0, s25, 8200\n\t" \
  "s_add_u32 s24, s24, s26\n\t" \
  "v_smfmac_f32_16x16x64_bf16 v[164:167], v[128:131], v[208:215], v234\n\t" \
  "buffer_load_dwordx4 v254, s[8:11], s24 offen lds\n\t" \
  "ds_read_b64 v[200:201], v253 offset:32816\n\t" \
  "ds_read_b64 v[202:203], v253 offset:33216\n\t" \
  "ds_read_b64 v[84:85], v235 offset:64\n\t" \
  "v_and_b32 v136, 0xffff, v138\n\t" \
  "v_and_b32 v137, 0xffff0000, v138\n\t" \
  "v_and_b32 v138, 0xffff, v139\n\t" \
  "v_and_b32 v139, 0xffff0000, v139\n\t" \
  "v_mfma_f32_32x32x16_bf16 v[0:15], v[242:245], v[240:243], 0\n\t" \
  "v_smfmac_f32_16x16x64_bf16 v[152:155], v[132:135], v[208:215], v234\n\t" \
  "ds_read_b64 v[204:205], v253 offset:33616\n\t" \
  "ds_read_b64 v[86:87], v235 offset:72\n\t" \
  "ds_read_b64 v[92:93], v235 offset:128\n\t" \
  "s_waitcnt lgkmcnt(12)\n\t" \
  "v_smfmac_f32_16x16x64_bf16 v[168:171], v[96:99], v[216:223], v234\n\t" \
  "ds_read_b64 v[206:207], v253 offset:34016\n\t" \
  "ds_read_b64 v[94:95], v235 offset:136\n\t" \
  "v_and_b32 v108, 0xffff, v110\n\t" \
  "v_and_b32 v109, 0xffff0000, v110\n\t" \
  "v_and_b32 v110, 0xffff, v111\n\t" \
  "v_and_b32 v111, 0xffff0000, v111\n\t" \
  "v_add_u32 v235, s58, v235\n\t" \
  "s_add_u32 m0, s25, 16400\n\t" \
  "s_add_u32 s24, s24, s26\n\t" \
  "v_smfmac_f32_16x16x64_bf16 v[156:159], v[100:103], v[216:223], v234\n\t" \
  "buffer_load_dwordx4 v254, s[8:11], s24 offen lds\n\t" \
  "v_smfmac_f32_16x16x64_bf16 v[144:147], v[104:107], v[216:223], v234\n\t" \
  "s_waitcnt vmcnt(7) lgkmcnt(0)\n\t" \
  "s_barrier\n\t" \
  "v_smfmac_f32_16x16x64_bf16 v[172:175], v[112:115], v[224:231], v234\n\t" \
  "ds_read_b64 v[208:209], v232 offset:0\n\t" \
  "ds_read_b64 v[210:211], v232 offset:400\n\t" \
  "v_and_b32 v124, 0xffff, v126\n\t" \
  "v_and_b32 v125, 0xffff0000, v126\n\t" \
  "v_and_b32 v126, 0xffff, v127\n\t" \
  "v_and_b32 v127, 0xffff0000, v127\n\t" \
  "s_add_u32 m0, s25, 24600\n\t" \
  "s_add_u32 s24, s24, s26\n\t" \
  "v_smfmac_f32_16x16x64_bf16 v[160:163], v[116:119], v[224:231], v234\n\t" \
  "buffer_load_dwordx4 v254, s[8:11], s24 offen lds\n\t" \
  "ds_read_b64 v[212:213], v232 offset:800\n\t" \
  "ds_read_b64 v[214:215], v232 offset:1200\n\t" \
  "v_smfmac_f32_16x16x64_bf16 v[148:151], v[120:123], v[224:231], v234\n\t" \
  "ds_read_b64 v[216:217], v232 offset:8\n\t" \
  "ds_read_b64 v[218:219], v232 offset:408\n\t" \
  "v_smfmac_f32_16x16x64_bf16 v[176:179], v[128:131], v[200:207], v234\n\t" \
  "ds_read_b64 v[220:221], v232 offset:808\n\t" \
  "ds_read_b64 v[222:223], v232 offset:1208\n\t" \
  "v_and_b32 v140, 0xffff, v142\n\t" \
  "v_and_b32 v141, 0xffff0000, v142\n\t" \
  "v_and_b32 v142, 0xffff, v143\n\t" \
  "v_and_b32 v143, 0xffff0000, v143\n\t" \
  "v_smfmac_f32_16x16x64_bf16 v[164:167], v[132:135], v[200:207], v234\n\t" \
  "ds_read_b64 v[224:225], v232 offset:16\n\t" \
  "s_add_u32 m0, s25, 32800\n\t" \
  "s_add_u32 s24, s24, s26\n\t" \
  "v_smfmac_f32_16x16x64_bf16 v[152:155], v[136:139], v[200:207], v234\n\t" \
  "buffer_load_dwordx4 v254, s[8:11], s24 offen lds\n\t" \
  "ds_read_b64 v[226:227], v232 offset:416\n\t" \
  "s_waitcnt lgkmcnt(6)\n\t" \
  "v_smfmac_f32_16x16x64_bf16 v[180:183], v[96:99], v[208:215], v234\n\t" \
  "ds_read_b64 v[228:229], v232 offset:816\n\t" \
  "v_smfmac_f32_16x16x64_bf16 v[168:171], v[100:103], v[208:215], v234\n\t" \
  "ds_read_b64 v[230:231], v232 offset:1216\n\t" \
  "v_mfma_f32_32x32x16_bf16 v[16:31], v[242:245], v[240:243], 0\n\t" \
  "v_mov_b32 v196, 0x3f003f00\n\t" \
  "v_mov_b32 v197, 0x3f003f00\n\t" \
  "v_smfmac_f32_16x16x64_bf16 v[156:159], v[104:107], v[208:215], v234\n\t" \
  "v_mov_b32 v198, 0x3f003f00\n\t" \
  "v_mov_b32 v199, 0x3f003f00\n\t" \
  "s_add_u32 m0, s25, 41000\n\t" \
  "s_add_u32 s24, s24, s26\n\t" \
  "v_smfmac_f32_16x16x64_bf16 v[144:147], v[108:111], v[208:215], v234\n\t" \
  "buffer_load_dwordx4 v254, s[8:11], s24 offen lds\n\t" \
  "s_waitcnt vmcnt(7) lgkmcnt(0)\n\t" \
  "s_barrier\n\t" \
  "v_smfmac_f32_16x16x64_bf16 v[184:187], v[112:115], v[216:223], v234\n\t" \
  "ds_read_b64 v[200:201], v232 offset:16400\n\t" \
  "ds_read_b64 v[202:203], v232 offset:16800\n\t" \
  "v_smfmac_f32_16x16x64_bf16 v[172:175], v[116:119], v[216:223], v234\n\t" \
  "ds_read_b64 v[204:205], v232 offset:17200\n\t" \
  "ds_read_b64 v[206:207], v232 offset:17600\n\t" \
  "v_smfmac_f32_16x16x64_bf16 v[160:163], v[120:123], v[216:223], v234\n\t" \
  "ds_read_b64 v[208:209], v232 offset:16408\n\t" \
  "v_cvt_pk_bf16_f32 v144, v144, v145\n\t" \
  "v_cvt_pk_bf16_f32 v145, v146, v147\n\t" \
  "s_add_u32 m0, s25, 56880\n\t" \
  "s_add_u32 s24, s24, s26\n\t" \
  "v_smfmac_f32_16x16x64_bf16 v[148:151], v[124:127], v[216:223], v234\n\t" \
  "buffer_load_dwordx4 v254, s[8:11], s24 offen lds\n\t" \
  "ds_read_b64 v[210:211], v232 offset:16808\n\t" \
  "v_smfmac_f32_16x16x64_bf16 v[188:191], v[128:131], v[224:231], v234\n\t" \
  "ds_read_b64 v[212:213], v232 offset:17208\n\t" \
  "v_smfmac_f32_16x16x64_bf16 v[176:179], v[132:135], v[224:231], v234\n\t" \
  "ds_read_b64 v[214:215], v232 offset:17608\n\t" \
  "ds_read_b64 v[72:73], v235 offset:0\n\t" \
  "ds_read_b64 v[74:75], v235 offset:8\n\t" \
  "v_smfmac_f32_16x16x64_bf16 v[164:167], v[136:139], v[224:231], v234\n\t" \
  "ds_read_b64 v[216:217], v232 offset:16416\n\t" \
  "ds_read_b64 v[80:81], v235 offset:64\n\t" \
  "v_cvt_pk_bf16_f32 v148, v148, v149\n\t" \
  "v_cvt_pk_bf16_f32 v149, v150, v151\n\t" \
  "v_smfmac_f32_16x16x64_bf16 v[152:155], v[140:143], v[224:231], v234\n\t" \
  "ds_read_b64 v[218:219], v232 offset:16816\n\t" \
  "ds_read_b64 v[82:83], v235 offset:72\n\t" \
  "s_waitcnt lgkmcnt(10)\n\t" \
  "v_smfmac_f32_16x16x64_bf16 v[0:3], v[96:99], v[200:207], v234\n\t" \
  "ds_read_b64 v[220:221], v232 offset:17216\n\t" \
  "ds_read_b64 v[88:89], v235 offset:128\n\t" \
  "v_smfmac_f32_16x16x64_bf16 v[180:183], v[100:103], v[200:207], v234\n\t" \
  "ds_read_b64 v[222:223], v232 offset:17616\n\t" \
  "ds_read_b64 v[90:91], v235 offset:136\n\t" \
  "v_mfma_f32_32x32x16_bf16 v[32:47], v[242:245], v[240:243], 0\n\t" \
  "v_add_u32 v235, s59, v235\n\t" \
  "v_smfmac_f32_16x16x64_bf16 v[168:171], v[104:107], v[200:207], v234\n\t" \
  "v_cvt_pk_bf16_f32 v152, v152, v153\n\t" \
  "v_cvt_pk_bf16_f32 v153, v154, v155\n\t" \
  "v_smfmac_f32_16x16x64_bf16 v[156:159], v[108:111], v[200:207], v234\n\t" \
  "s_waitcnt vmcnt(6) lgkmcnt(0)\n\t" \
  "s_barrier\n\t" \
  "s_add_u32 m0, s25, 65080\n\t" \
  "s_add_u32 s24, s24, s26\n\t" \
  "v_smfmac_f32_16x16x64_bf16 v[4:7], v[112:115], v[208:215], v234\n\t" \
  "buffer_load_dwordx4 v254, s[8:11], s24 offen lds\n\t" \
  "ds_read_b64 v[224:225], v232 offset:32800\n\t" \
  "ds_read_b64 v[226:227], v232 offset:33200\n\t" \
  "s_add_u32 m0, s25, 73280\n\t" \
  "s_add_u32 s24, s24, s26\n\t" \
  "v_smfmac_f32_16x16x64_bf16 v[184:187], v[116:119], v[208:215], v234\n\t" \
  "buffer_load_dwordx4 v254, s[8:11], s24 offen lds\n\t" \
  "ds_read_b64 v[228:229], v232 offset:33600\n\t" \
  "ds_read_b64 v[230:231], v232 offset:34000\n\t" \
  "s_add_u32 m0, s25, 81480\n\t" \
  "s_add_u32 s24, s24, s26\n\t" \
  "v_smfmac_f32_16x16x64_bf16 v[172:175], v[120:123], v[208:215], v234\n\t" \
  "buffer_load_dwordx4 v254, s[8:11], s24 offen lds\n\t" \
  "ds_read_b64 v[200:201], v232 offset:32808\n\t" \
  "v_cvt_pk_bf16_f32 v156, v156, v157\n\t" \
  "v_cvt_pk_bf16_f32 v157, v158, v159\n\t" \
  "v_smfmac_f32_16x16x64_bf16 v[160:163], v[124:127], v[208:215], v234\n\t" \
  "ds_read_b64 v[202:203], v232 offset:33208\n\t" \
  "v_smfmac_f32_16x16x64_bf16 v[8:11], v[128:131], v[216:223], v234\n\t" \
  "ds_read_b64 v[204:205], v232 offset:33608\n\t" \
  "ds_read_b64 v[48:49], v235 offset:0\n\t" \
  "v_smfmac_f32_16x16x64_bf16 v[188:191], v[132:135], v[216:223], v234\n\t" \
  "ds_read_b64 v[206:207], v232 offset:34008\n\t" \
  "ds_read_b64 v[50:51], v235 offset:8\n\t" \
  "v_smfmac_f32_16x16x64_bf16 v[176:179], v[136:139], v[216:223], v234\n\t" \
  "ds_read_b64 v[208:209], v232 offset:32816\n\t" \
  "ds_read_b64 v[56:57], v235 offset:64\n\t" \
  "v_cvt_pk_bf16_f32 v160, v160, v161\n\t" \
  "v_cvt_pk_bf16_f32 v161, v162, v163\n\t" \
  "v_smfmac_f32_16x16x64_bf16 v[164:167], v[140:143], v[216:223], v234\n\t" \
  "ds_read_b64 v[210:211], v232 offset:33216\n\t" \
  "ds_read_b64 v[58:59], v235 offset:72\n\t" \
  "s_waitcnt lgkmcnt(10)\n\t" \
  "v_smfmac_f32_16x16x64_bf16 v[12:15], v[96:99], v[224:231], v234\n\t" \
  "ds_read_b64 v[212:213], v232 offset:33616\n\t" \
  "ds_read_b64 v[64:65], v235 offset:128\n\t" \
  "v_smfmac_f32_16x16x64_bf16 v[0:3], v[100:103], v[224:231], v234\n\t" \
  "ds_read_b64 v[214:215], v232 offset:34016\n\t" \
  "ds_read_b64 v[66:67], v235 offset:136\n\t" \
  "v_smfmac_f32_16x16x64_bf16 v[180:183], v[104:107], v[224:231], v234\n\t" \
  "v_cvt_pk_bf16_f32 v164, v164, v165\n\t" \
  "v_cvt_pk_bf16_f32 v165, v166, v167\n\t" \
  "v_smfmac_f32_16x16x64_bf16 v[168:171], v[108:111], v[224:231], v234\n\t" \
  "s_waitcnt vmcnt(7) lgkmcnt(0)\n\t" \
  "s_barrier\n\t" \
  "v_smfmac_f32_16x16x64_bf16 v[16:19], v[112:115], v[200:207], v234\n\t" \
  "ds_write_b64 v193, v[144:145] offset:12288\n\t" \
  "ds_read_b64 v[216:217], v253 offset:0\n\t" \
  "v_smfmac_f32_16x16x64_bf16 v[4:7], v[116:119], v[200:207], v234\n\t" \
  "ds_write_b64 v193, v[148:149] offset:16384\n\t" \
  "ds_read_b64 v[218:219], v253 offset:400\n\t" \
  "v_smfmac_f32_16x16x64_bf16 v[184:187], v[120:123], v[200:207], v234\n\t" \
  "ds_write_b64 v193, v[152:153] offset:20480\n\t" \
  "ds_read_b64 v[220:221], v253 offset:800\n\t" \
  "v_cvt_pk_bf16_f32 v168, v168, v169\n\t" \
  "v_cvt_pk_bf16_f32 v169, v170, v171\n\t" \
  "v_smfmac_f32_16x16x64_bf16 v[172:175], v[124:127], v[200:207], v234\n\t" \
  "ds_write_b64 v193, v[156:157] offset:0\n\t" \
  "ds_read_b64 v[222:223], v253 offset:1200\n\t" \
  "v_smfmac_f32_16x16x64_bf16 v[20:23], v[128:131], v[208:215], v234\n\t" \
  "ds_write_b64 v193, v[160:161] offset:4096\n\t" \
  "ds_read_b64 v[224:225], v253 offset:8\n\t" \
  "v_smfmac_f32_16x16x64_bf16 v[8:11], v[132:135], v[208:215], v234\n\t" \
  "ds_write_b64 v193, v[164:165] offset:8192\n\t" \
  "ds_read_b64 v[226:227], v253 offset:408\n\t" \
  "s_mov_b64 exec, s[68:69]\n\t" \
  "ds_read_b64 v[48:49], v235 offset:0\n\t" \
  "ds_read_b64 v[50:51], v235 offset:8\n\t" \
  "ds_read_b64 v[56:57], v235 offset:64\n\t" \
  "s_mov_b64 exec, -1\n\t" \
  "v_smfmac_f32_16x16x64_bf16 v[188:191], v[136:139], v[208:215], v234\n\t" \
  "ds_read_b64 v[228:229], v253 offset:808\n\t" \
  "ds_read_b64 v[230:231], v253 offset:1208\n\t" \
  "v_cvt_pk_bf16_f32 v172, v172, v173\n\t" \
  "v_cvt_pk_bf16_f32 v173, v174, v175\n\t" \
  "v_smfmac_f32_16x16x64_bf16 v[176:179], v[140:143], v[208:215], v234\n\t" \
  "ds_read_b64 v[200:201], v253 offset:16\n\t" \
  "ds_read_b64 v[202:203], v253 offset:416\n\t" \
  "s_waitcnt lgkmcnt(11)\n\t" \
  "v_smfmac_f32_16x16x64_bf16 v[24:27], v[96:99], v[216:223], v234\n\t" \
  "ds_read_b64 v[204:205], v253 offset:816\n\t" \
  "s_mov_b64 exec, s[68:69]\n\t" \
  "ds_read_b64 v[58:59], v235 offset:72\n\t" \
  "ds_read_b64 v[64:65], v235 offset:128\n\t" \
  "ds_read_b64 v[66:67], v235 offset:136\n\t" \
  "s_mov_b64 exec, -1\n\t" \
  "v_add_u32 v235, s60, v235\n\t" \
  "v_smfmac_f32_16x16x64_bf16 v[12:15], v[100:103], v[216:223], v234\n\t" \
  "ds_read_b64 v[206:207], v253 offset:1216\n\t" \
  "v_smfmac_f32_16x16x64_bf16 v[0:3], v[104:107], v[216:223], v234\n\t" \
  "v_cvt_pk_bf16_f32 v176, v176, v177\n\t" \
  "v_cvt_pk_bf16_f32 v177, v178, v179\n\t" \
  "v_smfmac_f32_16x16x64_bf16 v[180:183], v[108:111], v[216:223], v234\n\t" \
  "s_waitcnt vmcnt(6) lgkmcnt(0)\n\t" \
  "s_barrier\n\t" \
  "v_smfmac_f32_16x16x64_bf16 v[28:31], v[112:115], v[224:231], v234\n\t" \
  "ds_write_b64 v192, v[168:169] offset:0\n\t" \
  "ds_read_b64 v[208:209], v253 offset:16400\n\t" \
  "ds_read_b128 v[144:147], v194 offset:0\n\t" \
  "v_smfmac_f32_16x16x64_bf16 v[16:19], v[116:119], v[224:231], v234\n\t" \
  "ds_write_b64 v192, v[172:173] offset:4096\n\t" \
  "ds_read_b64 v[210:211], v253 offset:16800\n\t" \
  "ds_read_b128 v[148:151], v194 offset:4096\n\t" \
  "v_smfmac_f32_16x16x64_bf16 v[4:7], v[120:123], v[224:231], v234\n\t" \
  "ds_write_b64 v192, v[176:177] offset:8192\n\t" \
  "ds_read_b64 v[212:213], v253 offset:17200\n\t" \
  "v_cvt_pk_bf16_f32 v180, v180, v181\n\t" \
  "v_cvt_pk_bf16_f32 v181, v182, v183\n\t" \
  "ds_read_b128 v[152:155], v194 offset:8192\n\t" \
  "v_smfmac_f32_16x16x64_bf16 v[184:187], v[124:127], v[224:231], v234\n\t" \
  "ds_read_b64 v[214:215], v253 offset:17600\n\t" \
  "ds_read_b64 v[216:217], v253 offset:16408\n\t" \
  "v_add_u32 v194, -93792, v194\n\t" \
  "v_smfmac_f32_16x16x64_bf16 v[32:35], v[128:131], v[200:207], v234\n\t" \
  "ds_read_b64 v[218:219], v253 offset:16808\n\t" \
  "ds_read_b64 v[220:221], v253 offset:17208\n\t" \
  "v_smfmac_f32_16x16x64_bf16 v[20:23], v[132:135], v[200:207], v234\n\t" \
  "ds_read_b64 v[222:223], v253 offset:17608\n\t" \
  "s_waitcnt lgkmcnt(11)\n\t" \
  "v_mfma_f32_16x16x32_bf16 v[168:171], v[144:147], v[144:147], 0\n\t" \
  "ds_read_b64 v[224:225], v253 offset:16416\n\t" \
  "v_smfmac_f32_16x16x64_bf16 v[8:11], v[136:139], v[200:207], v234\n\t" \
  "ds_read_b64 v[226:227], v253 offset:16816\n\t" \
  "v_cvt_pk_bf16_f32 v184, v184, v185\n\t" \
  "v_cvt_pk_bf16_f32 v185, v186, v187\n\t" \
  "v_mfma_f32_16x16x32_bf16 v[172:175], v[196:199], v[144:147], 0\n\t" \
  "ds_read_b64 v[228:229], v253 offset:17216\n\t" \
  "v_smfmac_f32_16x16x64_bf16 v[188:191], v[140:143], v[200:207], v234\n\t" \
  "ds_read_b64 v[230:231], v253 offset:17616\n\t" \
  "s_waitcnt lgkmcnt(12)\n\t" \
  "v_mfma_f32_16x16x32_bf16 v[168:171], v[148:151], v[148:151], v[168:171]\n\t" \
  "s_waitcnt lgkmcnt(8)\n\t" \
  "v_smfmac_f32_16x16x64_bf16 v[36:39], v[96:99], v[208:215], v234\n\t" \
  "v_mfma_f32_16x16x32_bf16 v[172:175], v[196:199], v[148:151], v[172:175]\n\t" \
  "v_smfmac_f32_16x16x64_bf16 v[24:27], v[100:103], v[208:215], v234\n\t" \
  "v_mfma_f32_16x16x32_bf16 v[168:171], v[152:155], v[152:155], v[168:171]\n\t" \
  "v_smfmac_f32_16x16x64_bf16 v[12:15], v[104:107], v[208:215], v234\n\t" \
  "v_cvt_pk_bf16_f32 v188, v188, v189\n\t" \
  "v_cvt_pk_bf16_f32 v189, v190, v191\n\t" \
  "v_mfma_f32_16x16x32_bf16 v[172:175], v[196:199], v[152:155], v[172:175]\n\t" \
  "v_smfmac_f32_16x16x64_bf16 v[0:3], v[108:111], v[208:215], v234\n\t" \
  "s_waitcnt vmcnt(3) lgkmcnt(0)\n\t" \
  "s_barrier\n\t" \
  "v_lshl_add_u32 v238, v233, 1, s63\n\t" \
  "s_lshl1_add_u32 m0, s39, 10016\n\t" \
  "v_add_u32 v239, 0x2000, v238\n\t" \
  "global_load_lds_dwordx4 v238, s[6:7]\n\t" \
  "s_add_u32 m0, m0, 66048\n\t" \
  "s_cmp_eq_u32 s39, 3584\n\t" \
  "s_cselect_b32 exec_hi, 0, -1\n\t" \
  "global_load_lds_dwordx4 v239, s[6:7]\n\t" \
  "s_mov_b64 exec, -1\n\t" \
  "v_smfmac_f32_16x16x64_bf16 v[40:43], v[112:115], v[216:223], v234\n\t" \
  "ds_write_b64 v192, v[180:181] offset:12288\n\t" \
  "ds_read_b64 v[200:201], v253 offset:32800\n\t" \
  "v_smfmac_f32_16x16x64_bf16 v[28:31], v[116:119], v[216:223], v234\n\t" \
  "ds_write_b64 v192, v[184:185] offset:16384\n\t" \
  "ds_read_b64 v[202:203], v253 offset:33200\n\t" \
  "v_cndmask_b32 v168, v168, v169, s[40:41]\n\t" \
  "v_cndmask_b32 v170, v170, v171, s[40:41]\n\t" \
  "v_smfmac_f32_16x16x64_bf16 v[16:19], v[120:123], v[216:223], v234\n\t" \
  "ds_write_b64 v192, v[188:189] offset:20480\n\t" \
  "ds_read_b64 v[204:205], v253 offset:33600\n\t" \
  "v_cvt_pk_bf16_f32 v0, v0, v1\n\t" \
  "v_cvt_pk_bf16_f32 v1, v2, v3\n\t" \
  "v_cndmask_b32 v173, v168, v170, s[42:43]\n\t" \
  "v_mov_b32 v174, 0x1127c5ac\n\t" \
  "ds_read_b64_tr_b16 v[156:157], v195 offset:0\n\t" \
  "v_smfmac_f32_16x16x64_bf16 v[4:7], v[124:127], v[216:223], v234\n\t" \
  "ds_read_b64 v[206:207], v253 offset:34000\n\t" \
  "ds_read_b64 v[208:209], v253 offset:32808\n\t" \
  "s_not_b64 vcc, s[52:53]\n\t" \
  "ds_read_b64_tr_b16 v[158:159], v195 offset:4096\n\t" \
  "v_smfmac_f32_16x16x64_bf16 v[44:47], v[128:131], v[224:231], v234\n\t" \
  "ds_read_b64 v[210:211], v253 offset:33208\n\t" \
  "ds_read_b64 v[212:213], v253 offset:33608\n\t" \
  "ds_read_b64 v[52:53], v235 offset:0\n\t" \
  "ds_read_b64 v[54:55], v235 offset:8\n\t" \
  "ds_read_b64_tr_b16 v[160:161], v195 offset:2048\n\t" \
  "v_smfmac_f32_16x16x64_bf16 v[32:35], v[132:135], v[224:231], v234\n\t" \
  "ds_read_b64 v[214:215], v253 offset:34008\n\t" \
  "ds_read_b64 v[216:217], v253 offset:32816\n\t" \
  "ds_read_b64 v[60:61], v235 offset:64\n\t" \
  "ds_read_b64_tr_b16 v[162:163], v195 offset:6144\n\t" \
  "v_smfmac_f32_16x16x64_bf16 v[20:23], v[136:139], v[224:231], v234\n\t" \
  "ds_read_b64 v[218:219], v253 offset:33216\n\t" \
  "ds_read_b64 v[62:63], v235 offset:72\n\t" \
  "v_cvt_pk_bf16_f32 v4, v4, v5\n\t" \
  "v_cvt_pk_bf16_f32 v5, v6, v7\n\t" \
  "ds_read_b64_tr_b16 v[164:165], v195 offset:8192\n\t" \
  "v_smfmac_f32_16x16x64_bf16 v[8:11], v[140:143], v[224:231], v234\n\t" \
  "ds_read_b64 v[220:221], v253 offset:33616\n\t" \
  "ds_read_b64 v[68:69], v235 offset:128\n\t" \
  "ds_read_b64_tr_b16 v[166:167], v195 offset:10240\n\t" \
  "s_waitcnt lgkmcnt(15)\n\t" \
  "v_smfmac_f32_16x16x64_bf16 v[36:39], v[100:103], v[200:207], v234\n\t" \
  "ds_read_b64 v[222:223], v253 offset:34016\n\t" \
  "ds_read_b64 v[70:71], v235 offset:136\n\t" \
  "v_add_u32 v195, -93792, v195\n\t" \
  "v_smfmac_f32_16x16x64_bf16 v[24:27], v[104:107], v[200:207], v234\n\t" \
  "v_smfmac_f32_16x16x64_bf16 v[12:15], v[108:111], v[200:207], v234\n\t" \
  "v_cvt_pk_bf16_f32 v8, v8, v9\n\t" \
  "v_cvt_pk_bf16_f32 v9, v10, v11\n\t" \
  "s_waitcnt vmcnt(2) lgkmcnt(0)\n\t" \
  "s_barrier\n\t" \
  "s_cmp_lg_u32 s37, 0\n\t" \
  "s_cbranch_scc1 .Lcnx_c2\n\t" \
  "s_mov_b32 m0, s46\n\t" \
  "s_mov_b32 exec_hi, 0xffff\n\t" \
  "buffer_load_dwordx4 v252, s[16:19], s33 offen lds\n\t" \
  "s_mov_b32 exec_hi, -1\n\t" \
  "s_add_u32 s33, s33, s32\n\t" \
  "s_add_u32 m0, s46, s35\n\t" \
  "s_mov_b32 exec_hi, 0xffff\n\t" \
  "buffer_load_dwordx4 v252, s[16:19], s34 offen lds\n\t" \
  "s_mov_b32 exec_hi, -1\n\t" \
  "s_add_u32 s34, s34, s32\n\t" \
  "s_mov_b32 m0, s47\n\t" \
  "s_mov_b32 exec_hi, 0xffff\n\t" \
  "buffer_load_dwordx4 v252, s[16:19], s33 offen lds\n\t" \
  "s_mov_b32 exec_hi, -1\n\t" \
  "s_add_u32 s33, s33, s32\n\t" \
  "s_add_u32 m0, s47, s35\n\t" \
  "s_mov_b32 exec_hi, 0xffff\n\t" \
  "buffer_load_dwordx4 v252, s[16:19], s34 offen lds\n\t" \
  "s_mov_b32 exec_hi, -1\n\t" \
  "s_add_u32 s34, s34, s32\n\t" \
  ".Lcnx_c2:\n\t" \
  "v_smfmac_f32_16x16x64_bf16 v[40:43], v[116:119], v[208:215], v234\n\t" \
  "ds_write_b64 v192, v[0:1] offset:24576\n\t" \
  "ds_read_b64 v[224:225], v232 offset:0\n\t" \
  "ds_read_b128 v[180:183], v194 offset:0\n\t" \
  "ds_read_b128 v[184:187], v194 offset:4096\n\t" \
  "ds_read_b128 v[188:191], v194 offset:8192\n\t" \
  "v_smfmac_f32_16x16x64_bf16 v[28:31], v[120:123], v[208:215], v234\n\t" \
  "ds_write_b64 v192, v[4:5] offset:28672\n\t" \
  "ds_read_b64 v[226:227], v232 offset:400\n\t" \
  "ds_read_b64 v[228:229], v232 offset:800\n\t" \
  "v_smfmac_f32_16x16x64_bf16 v[16:19], v[124:127], v[208:215], v234\n\t" \
  "ds_write_b64 v192, v[8:9] offset:32768\n\t" \
  "ds_read_b64 v[230:231], v232 offset:1200\n\t" \
  "ds_read_b64 v[200:201], v232 offset:8\n\t" \
  "v_cvt_pk_bf16_f32 v12, v12, v13\n\t" \
  "v_cvt_pk_bf16_f32 v13, v14, v15\n\t" \
  "v_smfmac_f32_16x16x64_bf16 v[44:47], v[132:135], v[216:223], v234\n\t" \
  "ds_write_b64 v192, v[12:13] offset:36864\n\t" \
  "ds_read_b64 v[202:203], v232 offset:408\n\t" \
  "ds_read_b64 v[204:205], v232 offset:808\n\t" \
  "s_waitcnt lgkmcnt(11)\n\t" \
  "v_mfma_f32_16x16x32_bf16 v[176:179], v[180:183], v[180:183], 0\n\t" \
  "ds_read_b64 v[206:207], v232 offset:1208\n\t" \
  "v_mfma_f32_16x16x32_bf16 v[168:171], v[196:199], v[180:183], 0\n\t" \
  "ds_read_b64 v[208:209], v232 offset:16\n\t" \
  "v_smfmac_f32_16x16x64_bf16 v[32:35], v[136:139], v[216:223], v234\n\t" \
  "ds_read_b64 v[210:211], v232 offset:416\n\t" \
  "ds_read_b64 v[212:213], v232 offset:816\n\t" \
  "s_waitcnt lgkmcnt(14)\n\t" \
  "v_mfma_f32_16x16x32_bf16 v[176:179], v[184:187], v[184:187], v[176:179]\n\t" \
  "ds_read_b64 v[214:215], v232 offset:1216\n\t" \
  "v_mfma_f32_16x16x32_bf16 v[168:171], v[196:199], v[184:187], v[168:171]\n\t" \
  "v_smfmac_f32_16x16x64_bf16 v[20:23], v[140:143], v[216:223], v234\n\t" \
  "v_cvt_pk_bf16_f32 v16, v16, v17\n\t" \
  "v_cvt_pk_bf16_f32 v17, v18, v19\n\t" \
  "s_waitcnt lgkmcnt(14)\n\t" \
  "v_mfma_f32_16x16x32_bf16 v[176:179], v[188:191], v[188:191], v[176:179]\n\t" \
  "ds_write_b64 v192, v[16:17] offset:40960\n\t" \
  "v_mfma_f32_16x16x32_bf16 v[168:171], v[196:199], v[188:191], v[168:171]\n\t" \
  "s_waitcnt lgkmcnt(10)\n\t" \
  "v_smfmac_f32_16x16x64_bf16 v[36:39], v[104:107], v[224:231], v234\n\t" \
  "v_mov_b32 v183, 0\n\t" \
  "v_mov_b32 v187, 0\n\t" \
  "s_nop 1\n\t" \
  "v_cndmask_b32 v176, v176, v177, s[40:41]\n\t" \
  "v_cndmask_b32 v178, v178, v179, s[40:41]\n\t" \
  "v_cndmask_b32 v176, v176, v178, s[42:43]\n\t" \
  "v_smfmac_f32_16x16x64_bf16 v[24:27], v[108:111], v[224:231], v234\n\t" \
  "v_cndmask_b32_dpp v172, v168, v172, vcc row_ror:8 row_mask:0xf bank_mask:0xf\n\t" \
  "v_mul_f32 v172, 0x3daaaaab, v172\n\t" \
  "v_cndmask_b32_dpp v173, v176, v173, vcc row_ror:8 row_mask:0xf bank_mask:0xf\n\t" \
  "v_fmamk_f32 v173, v173, 0x3e2aaaab, v174\n\t" \
  "s_waitcnt vmcnt(0) lgkmcnt(0)\n\t" \
  "s_barrier\n\t" \
  "ds_read_b64 v[216:217], v232 offset:16400\n\t" \
  "ds_read_b64 v[218:219], v232 offset:16800\n\t" \
  "ds_read_b64 v[220:221], v232 offset:17200\n\t" \
  "ds_read_b64 v[222:223], v232 offset:17600\n\t" \
  "v_smfmac_f32_16x16x64_bf16 v[40:43], v[120:123], v[200:207], v234\n\t" \
  "ds_read_b64 v[224:225], v232 offset:16408\n\t" \
  "ds_read_b64 v[226:227], v232 offset:16808\n\t" \
  "v_cvt_pk_bf16_f32 v20, v20, v21\n\t" \
  "v_cvt_pk_bf16_f32 v21, v22, v23\n\t" \
  "v_fma_f32 v173, -v172, v172, v173\n\t" \
  "v_max_f32 v173, v174, v173\n\t" \
  "v_rsq_f32 v173, v173 div:2\n\t" \
  "s_mov_b64 exec, s[52:53]\n\tv_mul_f32 v173, 0x08805555, v173\n\ts_mov_b64 exec, -1\n\t" \
  "v_smfmac_f32_16x16x64_bf16 v[28:31], v[124:127], v[200:207], v234\n\t" \
  "ds_write_b64 v192, v[20:21] offset:45056\n\t" \
  "ds_read_b64 v[228:229], v232 offset:17208\n\t" \
  "s_mov_b64 vcc, s[40:41]\n\t" \
  "v_cvt_pk_bf16_f32 v175, v173, v173\n\t" \
  "v_and_b32 v173, 0xffff0000, v175\n\t" \
  "v_mul_f32 v174, -v172, v173 div:2\n\t" \
  "v_cndmask_b32_sdwa v175, v175, v173, vcc dst_sel:DWORD dst_unused:UNUSED_PAD src0_sel:WORD_1 src1_sel:DWORD\n\t" \
  "v_smfmac_f32_16x16x64_bf16 v[44:47], v[136:139], v[208:215], v234\n\t" \
  "ds_read_b64 v[230:231], v232 offset:17608\n\t" \
  "ds_read_b64 v[200:201], v232 offset:16416\n\t" \
  "v_cvt_pk_bf16_f32 v24, v24, v25\n\t" \
  "v_cvt_pk_bf16_f32 v25, v26, v27\n\t" \
  "v_and_b32 v172, 0xffff0000, v174\n\t" \
  "s_or_b64 vcc, s[52:53], s[42:43]\n\t" \
  "v_sub_f32 v174, v174, v172\n\t" \
  "v_cndmask_b32 v180, v175, v240, vcc\n\t" \
  "v_smfmac_f32_16x16x64_bf16 v[32:35], v[140:143], v[208:215], v234\n\t" \
  "ds_write_b64 v192, v[24:25] offset:56880\n\t" \
  "ds_read_b64 v[202:203], v232 offset:16816\n\t" \
  "v_perm_b32 v174, v172, v174, s55\n\t" \
  "v_cndmask_b32_dpp v184, v175, v240, vcc row_ror:8 row_mask:0xf bank_mask:0xf\n\t" \
  "s_mov_b64 vcc, s[52:53]\n\t" \
  "v_cndmask_b32 v182, v174, v240, vcc\n\t" \
  "ds_read_b64_tr_b16 v[144:145], v195 offset:0\n\t" \
  "ds_read_b64_tr_b16 v[146:147], v195 offset:4096\n\t" \
  "s_waitcnt lgkmcnt(10)\n\t" \
  "v_smfmac_f32_16x16x64_bf16 v[36:39], v[108:111], v[216:223], v234\n\t" \
  "ds_read_b64 v[204:205], v232 offset:17216\n\t" \
  "ds_read_b64 v[206:207], v232 offset:17616\n\t" \
  "v_cvt_pk_bf16_f32 v28, v28, v29\n\t" \
  "v_cvt_pk_bf16_f32 v29, v30, v31\n\t" \
  "v_cndmask_b32_dpp v186, v174, v240, vcc row_ror:8 row_mask:0xf bank_mask:0xf\n\t" \
  "s_orn2_b64 vcc, s[52:53], s[42:43]\n\t" \
  "v_cndmask_b32 v181, v175, v240, vcc\n\t" \
  "v_cndmask_b32_dpp v185, v175, v240, vcc row_ror:8 row_mask:0xf bank_mask:0xf\n\t" \
  "ds_read_b64_tr_b16 v[148:149], v195 offset:2048\n\t" \
  "ds_read_b64_tr_b16 v[150:151], v195 offset:6144\n\t" \
  "s_waitcnt lgkmcnt(9)\n\t" \
  "v_smfmac_f32_16x16x64_bf16 v[40:43], v[124:127], v[224:231], v234\n\t" \
  "ds_write_b64 v192, v[28:29] offset:60976\n\t" \
  "ds_read_b64_tr_b16 v[152:153], v195 offset:8192\n\t" \
  "ds_read_b64_tr_b16 v[154:155], v195 offset:10240\n\t" \
  "v_mfma_f32_16x16x16_bf16 v[0:3], v[196:197], v[182:183], 0\n\t" \
  "v_mfma_f32_16x16x16_bf16 v[4:7], v[196:197], v[186:187], 0\n\t" \
  "s_waitcnt lgkmcnt(5)\n\t" \
  "v_smfmac_f32_16x16x64_bf16 v[44:47], v[140:143], v[200:207], v234\n\t" \
  "v_cvt_pk_bf16_f32 v32, v32, v33\n\t" \
  "v_cvt_pk_bf16_f32 v33, v34, v35\n\t" \
  "v_mfma_f32_16x16x16_bf16 v[188:191], v[156:157], v[180:181], v[0:3]\n\t" \
  "ds_write_b64 v192, v[32:33] offset:65072\n\t" \
  "v_mfma_f32_16x16x16_bf16 v[176:179], v[158:159], v[180:181], v[0:3]\n\t" \
  "v_cvt_pk_bf16_f32 v36, v36, v37\n\t" \
  "v_cvt_pk_bf16_f32 v37, v38, v39\n\t" \
  "ds_write_b64 v193, v[36:37] offset:0\n\t" \
  "v_cvt_pk_bf16_f32 v40, v40, v41\n\t" \
  "v_cvt_pk_bf16_f32 v41, v42, v43\n\t" \
  "ds_write_b64 v193, v[40:41] offset:4096\n\t" \
  "v_mfma_f32_16x16x16_bf16 v[168:171], v[160:161], v[180:181], v[0:3]\n\t" \
  "v_cvt_pk_bf16_f32 v96, v188, v189\n\t" \
  "v_cvt_pk_bf16_f32 v97, v190, v191\n\t" \
  "v_mfma_f32_16x16x16_bf16 v[172:175], v[162:163], v[180:181], v[0:3]\n\t" \
  "v_cvt_pk_bf16_f32 v44, v44, v45\n\t" \
  "v_cvt_pk_bf16_f32 v45, v46, v47\n\t" \
  "ds_write_b64 v193, v[44:45] offset:8192\n\t" \
  "v_cvt_pk_bf16_f32 v98, v176, v177\n\t" \
  "v_cvt_pk_bf16_f32 v99, v178, v179\n\t" \
  "v_mfma_f32_16x16x16_bf16 v[188:191], v[164:165], v[180:181], v[0:3]\n\t" \
  "v_cvt_pk_bf16_f32 v112, v168, v169\n\t" \
  "v_cvt_pk_bf16_f32 v113, v170, v171\n\t" \
  "v_mfma_f32_16x16x16_bf16 v[176:179], v[166:167], v[180:181], v[0:3]\n\t" \
  "v_cvt_pk_bf16_f32 v114, v172, v173\n\t" \
  "v_cvt_pk_bf16_f32 v115, v174, v175\n\t" \
  "s_nop 2\n\t" \
  "v_cvt_pk_bf16_f32 v128, v188, v189\n\t" \
  "v_cvt_pk_bf16_f32 v129, v190, v191\n\t" \
  "s_nop 0\n\t" \
  "v_cvt_pk_bf16_f32 v130, v176, v177\n\t" \
  "v_cvt_pk_bf16_f32 v131, v178, v179\n\t" \
  "s_waitcnt vmcnt(0) lgkmcnt(0)\n\t" \
  "s_barrier\n\t" \
  "ds_read_b64_tr_b16 v[208:209], v248 offset:43048\n\t" \
  "ds_read_b64_tr_b16 v[210:211], v248 offset:44072\n\t" \
  "ds_read_b128 v[104:107], v194 offset:24576\n\t" \
  "ds_read_b128 v[120:123], v194 offset:28672\n\t" \
  "ds_read_b128 v[136:139], v194 offset:32768\n\t" \
  "v_add_u32 v194, s54, v194\n\t" \
  "ds_read_b128 v[108:111], v194 offset:49152\n\t" \
  "ds_read_b128 v[124:127], v194 offset:53248\n\t" \
  "ds_read_b128 v[140:143], v194 offset:57344\n\t" \
  "v_mfma_f32_16x16x16_bf16 v[168:171], v[144:145], v[184:185], v[4:7]\n\t" \
  "ds_read_b64_tr_b16 v[212:213], v248 offset:46120\n\t" \
  "ds_read_b64_tr_b16 v[214:215], v248 offset:47144\n\t" \
  "v_mfma_f32_16x16x16_bf16 v[172:175], v[146:147], v[184:185], v[4:7]\n\t" \
  "ds_read_b64_tr_b16 v[216:217], v248 offset:45096\n\t" \
  "ds_read_b64_tr_b16 v[218:219], v248 offset:48168\n\t" \
  "s_mov_b32 m0, s44\n\t" \
  "s_mov_b32 exec_hi, 0xffff\n\t" \
  "buffer_load_dwordx4 v252, s[16:19], s33 offen lds\n\t" \
  "s_mov_b32 exec_hi, -1\n\t" \
  "s_add_u32 s33, s33, s32\n\t" \
  "v_mfma_f32_16x16x16_bf16 v[188:191], v[148:149], v[184:185], v[4:7]\n\t" \
  "ds_read_b64_tr_b16 v[160:161], v248 offset:43560\n\t" \
  "ds_read_b64_tr_b16 v[162:163], v248 offset:44584\n\t" \
  "v_cvt_pk_bf16_f32 v100, v168, v169\n\t" \
  "v_cvt_pk_bf16_f32 v101, v170, v171\n\t" \
  "v_mfma_f32_16x16x16_bf16 v[176:179], v[150:151], v[184:185], v[4:7]\n\t" \
  "ds_read_b64_tr_b16 v[164:165], v248 offset:46632\n\t" \
  "ds_read_b64_tr_b16 v[166:167], v248 offset:47656\n\t" \
  "v_cvt_pk_bf16_f32 v102, v172, v173\n\t" \
  "v_cvt_pk_bf16_f32 v103, v174, v175\n\t" \
  "v_mfma_f32_16x16x16_bf16 v[168:171], v[152:153], v[184:185], v[4:7]\n\t" \
  "ds_read_b64_tr_b16 v[232:233], v248 offset:45608\n\t" \
  "ds_read_b64_tr_b16 v[234:235], v248 offset:48680\n\t" \
  "v_cvt_pk_bf16_f32 v116, v188, v189\n\t" \
  "v_cvt_pk_bf16_f32 v117, v190, v191\n\t" \
  "v_mfma_f32_16x16x16_bf16 v[172:175], v[154:155], v[184:185], v[4:7]\n\t" \
  "ds_read_b64_tr_b16 v[24:25], v195 offset:24576\n\t" \
  "ds_read_b64_tr_b16 v[26:27], v195 offset:28672\n\t" \
  "v_cvt_pk_bf16_f32 v118, v176, v177\n\t" \
  "v_cvt_pk_bf16_f32 v119, v178, v179\n\t" \
  "s_waitcnt lgkmcnt(15)\n\t" \
  "v_mfma_f32_16x16x32_bf16 v[144:147], v[208:211], v[96:99], 0\n\t" \
  "ds_read_b64_tr_b16 v[28:29], v195 offset:26624\n\t" \
  "ds_read_b64_tr_b16 v[30:31], v195 offset:30720\n\t" \
  "v_cvt_pk_bf16_f32 v132, v168, v169\n\t" \
  "v_cvt_pk_bf16_f32 v133, v170, v171\n\t" \
  "s_waitcnt lgkmcnt(12)\n\t" \
  "v_mfma_f32_16x16x32_bf16 v[144:147], v[212:215], v[112:115], v[144:147]\n\t" \
  "ds_read_b64_tr_b16 v[32:33], v195 offset:32768\n\t" \
  "ds_read_b64_tr_b16 v[34:35], v195 offset:34816\n\t" \
  "v_cvt_pk_bf16_f32 v134, v172, v173\n\t" \
  "v_cvt_pk_bf16_f32 v135, v174, v175\n\t" \
  "v_mfma_f32_16x16x32_bf16 v[152:155], v[208:211], v[100:103], 0\n\t" \
  "v_add_u32 v195, s54, v195\n\t" \
  "ds_read_b64_tr_b16 v[36:37], v195 offset:49152\n\t" \
  "ds_read_b64_tr_b16 v[38:39], v195 offset:53248\n\t" \
  "s_waitcnt lgkmcnt(14)\n\t" \
  "v_mfma_f32_16x16x32_bf16 v[144:147], v[216:219], v[128:131], v[144:147]\n\t" \
  "ds_read_b64_tr_b16 v[40:41], v195 offset:51200\n\t" \
  "s_add_u32 m0, s44, s35\n\t" \
  "s_mov_b32 exec_hi, 0xffff\n\t" \
  "buffer_load_dwordx4 v252, s[16:19], s34 offen lds\n\t" \
  "s_mov_b32 exec_hi, -1\n\t" \
  "s_add_u32 s34, s34, s32\n\t" \
  "v_mfma_f32_16x16x32_bf16 v[152:155], v[212:215], v[116:119], v[152:155]\n\t" \
  "ds_read_b64_tr_b16 v[42:43], v195 offset:55296\n\t" \
  "v_mfma_f32_16x16x32_bf16 v[152:155], v[216:219], v[132:135], v[152:155]\n\t" \
  "ds_read_b64_tr_b16 v[44:45], v195 offset:57344\n\t" \
  "v_mfma_f32_16x16x32_bf16 v[8:11], v[104:107], v[104:107], 0\n\t" \
  "ds_read_b64_tr_b16 v[46:47], v195 offset:59392\n\t" \
  "s_mov_b32 m0, s45\n\t" \
  "s_mov_b32 exec_hi, 0xffff\n\t" \
  "buffer_load_dwordx4 v252, s[16:19], s33 offen lds\n\t" \
  "s_mov_b32 exec_hi, -1\n\t" \
  "s_add_u32 s33, s33, s32\n\t" \
  "v_mfma_f32_16x16x32_bf16 v[12:15], v[196:199], v[104:107], 0\n\t" \
  "v_cvt_pknorm_i16_f32 v220, |v146|, |v147|\n\t" \
  "v_cvt_pknorm_i16_f32 v222, |v144|, |v145|\n\t" \
  "v_mfma_f32_16x16x32_bf16 v[8:11], v[120:123], v[120:123], v[8:11]\n\t" \
  "v_lshlrev_b32_sdwa v221, 2, v220 dst_sel:DWORD dst_unused:UNUSED_PAD src0_sel:DWORD src1_sel:BYTE_3\n\t" \
  "v_lshlrev_b32_sdwa v220, 2, v220 dst_sel:DWORD dst_unused:UNUSED_PAD src0_sel:DWORD src1_sel:BYTE_1\n\t" \
  "v_mfma_f32_16x16x32_bf16 v[12:15], v[196:199], v[120:123], v[12:15]\n\t" \
  "ds_read_b32 v221, v221 offset:10016\n\t" \
  "v_lshlrev_b32_sdwa v223, 2, v222 dst_sel:DWORD dst_unused:UNUSED_PAD src0_sel:DWORD src1_sel:BYTE_3\n\t" \
  "v_lshlrev_b32_sdwa v222, 2, v222 dst_sel:DWORD dst_unused:UNUSED_PAD src0_sel:DWORD src1_sel:BYTE_1\n\t" \
  "v_mfma_f32_16x16x32_bf16 v[8:11], v[136:139], v[136:139], v[8:11]\n\t" \
  "ds_read_b32 v220, v220 offset:10016\n\t" \
  "v_lshlrev_b32_sdwa v225, 1, v155 dst_sel:DWORD dst_unused:UNUSED_PAD src0_sel:DWORD src1_sel:WORD_1\n\t" \
  "v_lshlrev_b32_sdwa v224, 1, v154 dst_sel:DWORD dst_unused:UNUSED_PAD src0_sel:DWORD src1_sel:WORD_1\n\t" \
  "v_mfma_f32_16x16x32_bf16 v[12:15], v[196:199], v[136:139], v[12:15]\n\t" \
  "ds_read_b32 v223, v223 offset:10016\n\t" \
  "v_lshlrev_b32_sdwa v227, 1, v153 dst_sel:DWORD dst_unused:UNUSED_PAD src0_sel:DWORD src1_sel:WORD_1\n\t" \
  "v_lshlrev_b32_sdwa v226, 1, v152 dst_sel:DWORD dst_unused:UNUSED_PAD src0_sel:DWORD src1_sel:WORD_1\n\t" \
  "v_mfma_f32_16x16x32_bf16 v[16:19], v[108:111], v[108:111], 0\n\t" \
  "ds_read_b32 v222, v222 offset:10016\n\t" \
  "v_mfma_f32_16x16x32_bf16 v[20:23], v[196:199], v[108:111], 0\n\t" \
  "ds_read_u16_d16_hi v225, v225 offset:10528\n\t" \
  "s_add_u32 m0, s45, s35\n\t" \
  "s_mov_b32 exec_hi, 0xffff\n\t" \
  "buffer_load_dwordx4 v252, s[16:19], s34 offen lds\n\t" \
  "s_mov_b32 exec_hi, -1\n\t" \
  "s_add_u32 s34, s34, s32\n\t" \
  "v_cndmask_b32 v8, v8, v9, s[40:41]\n\t" \
  "v_cndmask_b32 v10, v10, v11, s[40:41]\n\t" \
  "v_mfma_f32_16x16x32_bf16 v[16:19], v[124:127], v[124:127], v[16:19]\n\t" \
  "ds_read_u16 v224, v224 offset:10528\n\t" \
  "v_cndmask_b32 v13, v8, v10, s[42:43]\n\t" \
  "v_mov_b32 v14, 0x1127c5ac\n\t" \
  "v_mfma_f32_16x16x32_bf16 v[20:23], v[196:199], v[124:127], v[20:23]\n\t" \
  "ds_read_u16_d16_hi v227, v227 offset:10528\n\t" \
  "s_not_b64 vcc, s[52:53]\n\t" \
  "v_mfma_f32_16x16x32_bf16 v[16:19], v[140:143], v[140:143], v[16:19]\n\t" \
  "ds_read_u16 v226, v226 offset:10528\n\t" \
  "s_waitcnt lgkmcnt(4)\n\t" \
  "v_fma_mix_f32 v221, |v147|, v221, v221 op_sel:[0,1,0] op_sel_hi:[0,1,1] clamp\n\t" \
  "v_fma_mix_f32 v220, |v146|, v220, v220 op_sel:[0,1,0] op_sel_hi:[0,1,1] clamp\n\t" \
  "v_mfma_f32_16x16x32_bf16 v[20:23], v[196:199], v[140:143], v[20:23]\n\t" \
  "v_fma_mix_f32 v223, |v145|, v223, v223 op_sel:[0,1,0] op_sel_hi:[0,1,1] clamp\n\t" \
  "v_fma_mix_f32 v222, |v144|, v222, v222 op_sel:[0,1,0] op_sel_hi:[0,1,1] clamp\n\t" \
  "v_mfma_f32_16x16x32_bf16 v[148:151], v[160:163], v[96:99], 0\n\t" \
  "s_waitcnt lgkmcnt(0)\n\t" \
  "v_or_b32 v155, v224, v225\n\t" \
  "v_or_b32 v154, v226, v227\n\t" \
  "v_mfma_f32_16x16x32_bf16 v[156:159], v[160:163], v[100:103], 0\n\t" \
  "v_cndmask_b32 v16, v16, v17, s[40:41]\n\t" \
  "v_cndmask_b32 v18, v18, v19, s[40:41]\n\t" \
  "v_cndmask_b32 v16, v16, v18, s[42:43]\n\t" \
  "v_cndmask_b32_dpp v12, v20, v12, vcc row_ror:8 row_mask:0xf bank_mask:0xf\n\t" \
  "v_mul_f32 v12, 0x3daaaaab, v12\n\t" \
  "v_mfma_f32_16x16x32_bf16 v[148:151], v[164:167], v[112:115], v[148:151]\n\t" \
  "v_cndmask_b32_dpp v13, v16, v13, vcc row_ror:8 row_mask:0xf bank_mask:0xf\n\t" \
  "v_fmamk_f32 v13, v13, 0x3e2aaaab, v14\n\t" \
  "v_fma_f32 v13, -v12, v12, v13\n\t" \
  "v_max_f32 v13, v14, v13\n\t" \
  "v_rsq_f32 v13, v13 div:2\n\t" \
  "v_mfma_f32_16x16x32_bf16 v[156:159], v[164:167], v[116:119], v[156:159]\n\t" \
  "s_mov_b64 exec, s[52:53]\n\tv_mul_f32 v13, 0x08805555, v13\n\ts_mov_b64 exec, -1\n\t" \
  "s_mov_b64 vcc, s[40:41]\n\t" \
  "v_cvt_pk_bf16_f32 v15, v13, v13\n\t" \
  "v_and_b32 v13, 0xffff0000, v15\n\t" \
  "v_mul_f32 v14, -v12, v13 div:2\n\t" \
  "v_cndmask_b32_sdwa v15, v15, v13, vcc dst_sel:DWORD dst_unused:UNUSED_PAD src0_sel:WORD_1 src1_sel:DWORD\n\t" \
  "v_and_b32 v12, 0xffff0000, v14\n\t" \
  "v_mfma_f32_16x16x32_bf16 v[148:151], v[232:235], v[128:131], v[148:151]\n\t" \
  "s_or_b64 vcc, s[52:53], s[42:43]\n\t" \
  "v_sub_f32 v14, v14, v12\n\t" \
  "v_cndmask_b32 v180, v15, v240, vcc\n\t" \
  "v_perm_b32 v14, v12, v14, s55\n\t" \
  "v_cndmask_b32_dpp v184, v15, v240, vcc row_ror:8 row_mask:0xf bank_mask:0xf\n\t" \
  "v_mfma_f32_16x16x32_bf16 v[156:159], v[232:235], v[132:135], v[156:159]\n\t" \
  "s_mov_b64 vcc, s[52:53]\n\t" \
  "v_cndmask_b32 v182, v14, v240, vcc\n\t" \
  "v_cndmask_b32_dpp v186, v14, v240, vcc row_ror:8 row_mask:0xf bank_mask:0xf\n\t" \
  "s_orn2_b64 vcc, s[52:53], s[42:43]\n\t" \
  "v_cndmask_b32 v181, v15, v240, vcc\n\t" \
  "v_mfma_f32_16x16x16_bf16 v[0:3], v[196:197], v[182:183], 0\n\t" \
  "v_cndmask_b32_dpp v185, v15, v240, vcc row_ror:8 row_mask:0xf bank_mask:0xf\n\t" \
  "s_mov_b64 vcc, 0\n\t" \
  "v_fma_f32 v147, |v147|, v221, v147 mul:4\n\t" \
  "v_mfma_f32_16x16x16_bf16 v[4:7], v[196:197], v[186:187], 0\n\t" \
  "v_fma_f32 v146, |v146|, v220, v146 mul:4\n\t" \
  "v_fma_f32 v145, |v145|, v223, v145 mul:4\n\t" \
  "v_mfma_f32_16x16x16_bf16 v[188:191], v[24:25], v[180:181], v[0:3]\n\t" \
  "v_fma_f32 v144, |v144|, v222, v144 mul:4\n\t" \
  "v_cvt_pk_bf16_f32 v147, v146, v147\n\t" \
  "v_mfma_f32_16x16x16_bf16 v[176:179], v[26:27], v[180:181], v[0:3]\n\t" \
  "v_cvt_pk_bf16_f32 v146, v144, v145\n\t" \
  "v_cvt_pknorm_i16_f32 v228, |v148|, |v149|\n\t" \
  "v_mfma_f32_16x16x16_bf16 v[200:203], v[36:37], v[184:185], v[4:7]\n\t" \
  "s_nop 1\n\t" \
  "v_cvt_pk_bf16_f32 v104, v188, v189\n\t" \
  "v_cvt_pk_bf16_f32 v105, v190, v191\n\t" \
  "v_mfma_f32_16x16x16_bf16 v[204:207], v[38:39], v[184:185], v[4:7]\n\t" \
  "v_cvt_pk_bf16_f32 v106, v176, v177\n\t" \
  "v_cvt_pk_bf16_f32 v107, v178, v179\n\t" \
  "v_mfma_f32_16x16x16_bf16 v[196:199], v[28:29], v[180:181], v[0:3]\n\t" \
  "v_cvt_pk_bf16_f32 v108, v200, v201\n\t" \
  "v_cvt_pk_bf16_f32 v109, v202, v203\n\t" \
  "v_mfma_f32_16x16x16_bf16 v[188:191], v[30:31], v[180:181], v[0:3]\n\t" \
  "s_nop 1\n\t" \
  "v_cvt_pk_bf16_f32 v110, v204, v205\n\t" \
  "v_cvt_pk_bf16_f32 v111, v206, v207\n\t" \
  "v_mfma_f32_16x16x32_bf16 v[160:163], v[208:211], v[104:107], 0\n\t" \
  "v_cvt_pk_bf16_f32 v120, v196, v197\n\t" \
  "v_cvt_pk_bf16_f32 v121, v198, v199\n\t" \
  "v_mfma_f32_16x16x16_bf16 v[176:179], v[40:41], v[184:185], v[4:7]\n\t" \
  "v_cvt_pk_bf16_f32 v122, v188, v189\n\t" \
  "v_cvt_pk_bf16_f32 v123, v190, v191\n\t" \
  "v_mfma_f32_16x16x16_bf16 v[200:203], v[42:43], v[184:185], v[4:7]\n\t" \
  "v_cvt_pknorm_i16_f32 v230, |v150|, |v151|\n\t" \
  "v_lshlrev_b32_sdwa v229, 2, v228 dst_sel:DWORD dst_unused:UNUSED_PAD src0_sel:DWORD src1_sel:BYTE_3\n\t" \
  "v_mfma_f32_16x16x32_bf16 v[168:171], v[208:211], v[108:111], 0\n\t" \
  "ds_read_b64_tr_b16 v[208:209], v248 offset:43560\n\t" \
  "s_nop 0\n\t" \
  "v_cvt_pk_bf16_f32 v124, v176, v177\n\t" \
  "v_cvt_pk_bf16_f32 v125, v178, v179\n\t" \
  "v_mfma_f32_16x16x16_bf16 v[204:207], v[32:33], v[180:181], v[0:3]\n\t" \
  "ds_read_b64_tr_b16 v[210:211], v248 offset:44584\n\t" \
  "v_cvt_pk_bf16_f32 v126, v200, v201\n\t" \
  "v_cvt_pk_bf16_f32 v127, v202, v203\n\t" \
  "v_mfma_f32_16x16x16_bf16 v[196:199], v[34:35], v[180:181], v[0:3]\n\t" \
  "v_lshlrev_b32_sdwa v228, 2, v228 dst_sel:DWORD dst_unused:UNUSED_PAD src0_sel:DWORD src1_sel:BYTE_1\n\t" \
  "v_lshlrev_b32_sdwa v231, 2, v230 dst_sel:DWORD dst_unused:UNUSED_PAD src0_sel:DWORD src1_sel:BYTE_3\n\t" \
  "v_mfma_f32_16x16x32_bf16 v[160:163], v[212:215], v[120:123], v[160:163]\n\t" \
  "ds_read_b32 v229, v229 offset:10016\n\t" \
  "v_cvt_pk_bf16_f32 v136, v204, v205\n\t" \
  "v_cvt_pk_bf16_f32 v137, v206, v207\n\t" \
  "v_mfma_f32_16x16x16_bf16 v[188:191], v[44:45], v[184:185], v[4:7]\n\t" \
  "ds_read_b32 v228, v228 offset:10016\n\t" \
  "v_cvt_pk_bf16_f32 v138, v196, v197\n\t" \
  "v_cvt_pk_bf16_f32 v139, v198, v199\n\t" \
  "v_mfma_f32_16x16x16_bf16 v[176:179], v[46:47], v[184:185], v[4:7]\n\t" \
  "v_lshlrev_b32_sdwa v230, 2, v230 dst_sel:DWORD dst_unused:UNUSED_PAD src0_sel:DWORD src1_sel:BYTE_1\n\t" \
  "v_lshlrev_b32_sdwa v233, 1, v157 dst_sel:DWORD dst_unused:UNUSED_PAD src0_sel:DWORD src1_sel:WORD_1\n\t" \
  "v_mfma_f32_16x16x32_bf16 v[168:171], v[212:215], v[124:127], v[168:171]\n\t" \
  "ds_read_b64_tr_b16 v[212:213], v248 offset:46632\n\t" \
  "v_cvt_pk_bf16_f32 v140, v188, v189\n\t" \
  "v_cvt_pk_bf16_f32 v141, v190, v191\n\t" \
  "v_mfma_f32_16x16x32_bf16 v[160:163], v[216:219], v[136:139], v[160:163]\n\t" \
  "ds_read_b64_tr_b16 v[214:215], v248 offset:47656\n\t" \
  "v_cvt_pk_bf16_f32 v142, v176, v177\n\t" \
  "v_cvt_pk_bf16_f32 v143, v178, v179\n\t" \
  "s_nop 1\n\t" \
  "v_mfma_f32_16x16x32_bf16 v[168:171], v[216:219], v[140:143], v[168:171]\n\t" \
  "ds_read_b64_tr_b16 v[216:217], v248 offset:45608\n\t" \
  "v_lshlrev_b32_sdwa v232, 1, v156 dst_sel:DWORD dst_unused:UNUSED_PAD src0_sel:DWORD src1_sel:WORD_1\n\t" \
  "v_lshlrev_b32_sdwa v235, 1, v159 dst_sel:DWORD dst_unused:UNUSED_PAD src0_sel:DWORD src1_sel:WORD_1\n\t" \
  "s_waitcnt lgkmcnt(5)\n\t" \
  "ds_read_b64_tr_b16 v[218:219], v248 offset:48680\n\t" \
  "s_waitcnt vmcnt(2)\n\t" \
  "s_mov_b64 exec, 3\n\t" \
  "ds_add_rtn_u32 v246, v251, v250 offset:47136\n\t" \
  "s_mov_b64 exec, -1\n\t" \
  "v_mfma_f32_16x16x32_bf16 v[164:167], v[208:211], v[104:107], 0\n\t" \
  "ds_read_b32 v231, v231 offset:10016\n\t" \
  "v_lshlrev_b32_sdwa v234, 1, v158 dst_sel:DWORD dst_unused:UNUSED_PAD src0_sel:DWORD src1_sel:WORD_1\n\t" \
  "v_mfma_f32_16x16x32_bf16 v[172:175], v[208:211], v[108:111], 0\n\t" \
  "ds_read_b64_tr_b16 v[208:209], v248 offset:49192\n\t" \
  "ds_read_b64_tr_b16 v[210:211], v248 offset:50216\n\t" \
  "s_waitcnt lgkmcnt(6)\n\t" \
  "v_mfma_f32_16x16x32_bf16 v[164:167], v[212:215], v[120:123], v[164:167]\n\t" \
  "ds_read_b32 v230, v230 offset:10016\n\t" \
  "v_mfma_f32_16x16x32_bf16 v[172:175], v[212:215], v[124:127], v[172:175]\n\t" \
  "ds_read_b64_tr_b16 v[212:213], v248 offset:52264\n\t" \
  "ds_read_b64_tr_b16 v[214:215], v248 offset:53288\n\t" \
  "s_waitcnt lgkmcnt(7)\n\t" \
  "v_mfma_f32_16x16x32_bf16 v[164:167], v[216:219], v[136:139], v[164:167]\n\t" \
  "ds_read_u16_d16_hi v233, v233 offset:10528\n\t" \
  "v_mfma_f32_16x16x32_bf16 v[172:175], v[216:219], v[140:143], v[172:175]\n\t" \
  "ds_read_b64_tr_b16 v[216:217], v248 offset:51240\n\t" \
  "v_mfma_f32_16x16x32_bf16 v[0:3], v[242:245], v[76:79], 0\n\t" \
  "v_cvt_pknorm_i16_f32 v220, |v162|, |v163|\n\t" \
  "v_cvt_pknorm_i16_f32 v222, |v160|, |v161|\n\t" \
  "s_setprio 0\n\t" \
  "ds_read_b64_tr_b16 v[218:219], v248 offset:54312\n\t" \
  "v_mfma_f32_16x16x32_bf16 v[4:7], v[240:243], v[76:79], 0\n\t" \
  "v_lshlrev_b32_sdwa v221, 2, v220 dst_sel:DWORD dst_unused:UNUSED_PAD src0_sel:DWORD src1_sel:BYTE_3\n\t" \
  "ds_read_u16 v232, v232 offset:10528\n\t" \
  "v_mfma_f32_16x16x32_bf16 v[8:11], v[242:245], v[84:87], 0\n\t" \
  "v_lshlrev_b32_sdwa v220, 2, v220 dst_sel:DWORD dst_unused:UNUSED_PAD src0_sel:DWORD src1_sel:BYTE_1\n\t" \
  "v_lshlrev_b32_sdwa v223, 2, v222 dst_sel:DWORD dst_unused:UNUSED_PAD src0_sel:DWORD src1_sel:BYTE_3\n\t" \
  "ds_read_u16_d16_hi v235, v235 offset:10528\n\t" \
  "v_mfma_f32_16x16x32_bf16 v[12:15], v[240:243], v[84:87], 0\n\t" \
  "v_lshlrev_b32_sdwa v222, 2, v222 dst_sel:DWORD dst_unused:UNUSED_PAD src0_sel:DWORD src1_sel:BYTE_1\n\t" \
  "ds_read_u16 v234, v234 offset:10528\n\t" \
  "v_mfma_f32_16x16x32_bf16 v[16:19], v[242:245], v[92:95], 0\n\t" \
  "v_lshlrev_b32_sdwa v225, 1, v171 dst_sel:DWORD dst_unused:UNUSED_PAD src0_sel:DWORD src1_sel:WORD_1\n\t" \
  "ds_read_b32 v221, v221 offset:10016\n\t" \
  "v_mfma_f32_16x16x32_bf16 v[20:23], v[240:243], v[92:95], 0\n\t" \
  "v_lshlrev_b32_sdwa v224, 1, v170 dst_sel:DWORD dst_unused:UNUSED_PAD src0_sel:DWORD src1_sel:WORD_1\n\t" \
  "v_lshlrev_b32_sdwa v227, 1, v169 dst_sel:DWORD dst_unused:UNUSED_PAD src0_sel:DWORD src1_sel:WORD_1\n\t" \
  "ds_read_b32 v220, v220 offset:10016\n\t" \
  "v_mfma_f32_16x16x32_bf16 v[24:27], v[242:245], v[72:75], 0\n\t" \
  "v_lshlrev_b32_sdwa v226, 1, v168 dst_sel:DWORD dst_unused:UNUSED_PAD src0_sel:DWORD src1_sel:WORD_1\n\t" \
  "ds_read_b32 v223, v223 offset:10016\n\t" \
  "s_waitcnt lgkmcnt(11)\n\t" \
  "v_mfma_f32_16x16x32_bf16 v[28:31], v[240:243], v[72:75], 0\n\t" \
  "v_fma_mix_f32 v229, |v149|, v229, v229 op_sel:[0,1,0] op_sel_hi:[0,1,1] clamp\n\t" \
  "v_fma_mix_f32 v228, |v148|, v228, v228 op_sel:[0,1,0] op_sel_hi:[0,1,1] clamp\n\t" \
  "ds_read_b32 v222, v222 offset:10016\n\t" \
  "v_mfma_f32_16x16x32_bf16 v[32:35], v[242:245], v[80:83], 0\n\t" \
  "v_fma_mix_f32 v231, |v151|, v231, v231 op_sel:[0,1,0] op_sel_hi:[0,1,1] clamp\n\t" \
  "ds_read_u16_d16_hi v225, v225 offset:10528\n\t" \
  "v_mfma_f32_16x16x32_bf16 v[36:39], v[240:243], v[80:83], 0\n\t" \
  "v_fma_mix_f32 v230, |v150|, v230, v230 op_sel:[0,1,0] op_sel_hi:[0,1,1] clamp\n\t" \
  "ds_read_u16 v224, v224 offset:10528\n\t" \
  "s_waitcnt lgkmcnt(6)\n\t" \
  "v_mfma_f32_16x16x32_bf16 v[40:43], v[242:245], v[88:91], 0\n\t" \
  "v_or_b32 v156, v232, v233\n\t" \
  "v_or_b32 v157, v234, v235\n\t" \
  "ds_read_u16_d16_hi v227, v227 offset:10528\n\t" \
  "v_mfma_f32_16x16x32_bf16 v[44:47], v[240:243], v[88:91], 0\n\t" \
  "v_fma_f32 v149, |v149|, v229, v149 mul:4\n\t" \
  "ds_read_u16 v226, v226 offset:10528\n\t" \
  "v_mfma_f32_16x16x32_bf16 v[72:75], v[242:245], v[52:55], 0\n\t" \
  "v_fma_f32 v148, |v148|, v228, v148 mul:4\n\t" \
  "v_fma_f32 v151, |v151|, v231, v151 mul:4\n\t" \
  "v_mfma_f32_16x16x32_bf16 v[76:79], v[240:243], v[52:55], 0\n\t" \
  "v_fma_f32 v150, |v150|, v230, v150 mul:4\n\t" \
  "v_mfma_f32_16x16x32_bf16 v[52:55], v[240:243], v[48:51], 0\n\t" \
  "v_cvt_pk_bf16_f32 v148, v148, v149\n\t" \
  "v_mfma_f32_16x16x32_bf16 v[48:51], v[242:245], v[48:51], 0\n\t" \
  "v_cvt_pk_bf16_f32 v149, v150, v151\n\t" \
  "v_cvt_pknorm_i16_f32 v228, |v164|, |v165|\n\t" \
  "v_mfma_f32_16x16x32_bf16 v[80:83], v[242:245], v[60:63], 0\n\t" \
  "v_cvt_pknorm_i16_f32 v230, |v166|, |v167|\n\t" \
  "v_mfma_f32_16x16x32_bf16 v[84:87], v[240:243], v[60:63], 0\n\t" \
  "s_add_u32 s27, s27, s26\n\t" \
  "v_lshlrev_b32_sdwa v229, 2, v228 dst_sel:DWORD dst_unused:UNUSED_PAD src0_sel:DWORD src1_sel:BYTE_3\n\t" \
  "v_lshlrev_b32_sdwa v228, 2, v228 dst_sel:DWORD dst_unused:UNUSED_PAD src0_sel:DWORD src1_sel:BYTE_1\n\t" \
  "s_mov_b64 exec, s[30:31]\n\t" \
  "buffer_load_dword v237, v254, s[8:11], s27 offen\n\t" \
  "v_mfma_f32_16x16x32_bf16 v[60:63], v[240:243], v[56:59], 0\n\t" \
  "s_mov_b64 exec, -1\n\t" \
  "v_lshlrev_b32_sdwa v231, 2, v230 dst_sel:DWORD dst_unused:UNUSED_PAD src0_sel:DWORD src1_sel:BYTE_3\n\t" \
  "ds_read_b32 v229, v229 offset:10016\n\t" \
  "v_mfma_f32_16x16x32_bf16 v[56:59], v[242:245], v[56:59], 0\n\t" \
  "v_lshlrev_b32_sdwa v230, 2, v230 dst_sel:DWORD dst_unused:UNUSED_PAD src0_sel:DWORD src1_sel:BYTE_1\n\t" \
  "ds_read_b32 v228, v228 offset:10016\n\t" \
  "v_mfma_f32_16x16x32_bf16 v[88:91], v[242:245], v[68:71], 0\n\t" \
  "v_lshlrev_b32_sdwa v233, 1, v173 dst_sel:DWORD dst_unused:UNUSED_PAD src0_sel:DWORD src1_sel:WORD_1\n\t" \
  "v_lshlrev_b32_sdwa v232, 1, v172 dst_sel:DWORD dst_unused:UNUSED_PAD src0_sel:DWORD src1_sel:WORD_1\n\t" \
  "ds_read_b32 v231, v231 offset:10016\n\t" \
  "v_mfma_f32_16x16x32_bf16 v[92:95], v[240:243], v[68:71], 0\n\t" \
  "v_lshlrev_b32_sdwa v235, 1, v175 dst_sel:DWORD dst_unused:UNUSED_PAD src0_sel:DWORD src1_sel:WORD_1\n\t" \
  "ds_read_b32 v230, v230 offset:10016\n\t" \
  "v_mfma_f32_16x16x32_bf16 v[68:71], v[240:243], v[64:67], 0\n\t" \
  "s_waitcnt lgkmcnt(8)\n\t" \
  "v_lshlrev_b32_sdwa v234, 1, v174 dst_sel:DWORD dst_unused:UNUSED_PAD src0_sel:DWORD src1_sel:WORD_1\n\t" \
  "v_fma_mix_f32 v221, |v163|, v221, v221 op_sel:[0,1,0] op_sel_hi:[0,1,1] clamp\n\t" \
  "ds_read_u16_d16_hi v233, v233 offset:10528\n\t" \
  "v_mfma_f32_16x16x32_bf16 v[64:67], v[242:245], v[64:67], 0\n\t" \
  "v_fma_mix_f32 v220, |v162|, v220, v220 op_sel:[0,1,0] op_sel_hi:[0,1,1] clamp\n\t" \
  "ds_read_u16 v232, v232 offset:10528\n\t" \
  "v_mfma_f32_16x16x32_bf16 v[176:179], v[208:211], v[96:99], 0\n\t" \
  "v_fma_mix_f32 v223, |v161|, v223, v223 op_sel:[0,1,0] op_sel_hi:[0,1,1] clamp\n\t" \
  "ds_read_u16_d16_hi v235, v235 offset:10528\n\t" \
  "v_mfma_f32_16x16x32_bf16 v[184:187], v[208:211], v[100:103], 0\n\t" \
  "s_waitcnt lgkmcnt(7)\n\t" \
  "v_fma_mix_f32 v222, |v160|, v222, v222 op_sel:[0,1,0] op_sel_hi:[0,1,1] clamp\n\t" \
  "v_or_b32 v171, v224, v225\n\t" \
  "ds_read_u16 v234, v234 offset:10528\n\t" \
  "v_mfma_f32_16x16x32_bf16 v[192:195], v[208:211], v[104:107], 0\n\t" \
  "v_or_b32 v170, v226, v227\n\t" \
  "v_mfma_f32_16x16x32_bf16 v[200:203], v[208:211], v[108:111], 0\n\t" \
  "v_nop\n\t" \
  "ds_read_b64_tr_b16 v[208:209], v248 offset:49704\n\t" \
  "v_mfma_f32_16x16x32_bf16 v[176:179], v[212:215], v[112:115], v[176:179]\n\t" \
  "v_nop\n\t" \
  "ds_read_b64_tr_b16 v[210:211], v248 offset:50728\n\t" \
  "v_mfma_f32_16x16x32_bf16 v[184:187], v[212:215], v[116:119], v[184:187]\n\t" \
  "v_mfma_f32_16x16x32_bf16 v[192:195], v[212:215], v[120:123], v[192:195]\n\t" \
  "v_mfma_f32_16x16x32_bf16 v[200:203], v[212:215], v[124:127], v[200:203]\n\t" \
  "v_nop\n\t" \
  "ds_read_b64_tr_b16 v[212:213], v248 offset:52776\n\t" \
  "v_mfma_f32_16x16x32_bf16 v[176:179], v[216:219], v[128:131], v[176:179]\n\t" \
  "v_fma_f32 v163, |v163|, v221, v163 mul:4\n\t" \
  "v_fma_f32 v162, |v162|, v220, v162 mul:4\n\t" \
  "ds_read_b64_tr_b16 v[214:215], v248 offset:53800\n\t" \
  "v_mfma_f32_16x16x32_bf16 v[184:187], v[216:219], v[132:135], v[184:187]\n\t" \
  "v_fma_f32 v161, |v161|, v223, v161 mul:4\n\t" \
  "v_fma_f32 v160, |v160|, v222, v160 mul:4\n\t" \
  "v_mfma_f32_16x16x32_bf16 v[192:195], v[216:219], v[136:139], v[192:195]\n\t" \
  "v_cvt_pk_bf16_f32 v163, v162, v163\n\t" \
  "v_cvt_pk_bf16_f32 v162, v160, v161\n\t" \
  "v_mfma_f32_16x16x32_bf16 v[200:203], v[216:219], v[140:143], v[200:203]\n\t" \
  "v_cvt_pknorm_i16_f32 v220, |v178|, |v179|\n\t" \
  "v_cvt_pknorm_i16_f32 v222, |v176|, |v177|\n\t" \
  "ds_read_b64_tr_b16 v[216:217], v248 offset:51752\n\t" \
  "s_waitcnt lgkmcnt(3)\n\t" \
  "v_mfma_f32_16x16x32_bf16 v[180:183], v[208:211], v[96:99], 0\n\t" \
  "v_lshlrev_b32_sdwa v221, 2, v220 dst_sel:DWORD dst_unused:UNUSED_PAD src0_sel:DWORD src1_sel:BYTE_3\n\t" \
  "v_lshlrev_b32_sdwa v220, 2, v220 dst_sel:DWORD dst_unused:UNUSED_PAD src0_sel:DWORD src1_sel:BYTE_1\n\t" \
  "s_waitcnt vmcnt(1)\n\t" \
  "ds_read_b64_tr_b16 v[218:219], v248 offset:54824\n\t" \
  "v_mfma_f32_16x16x32_bf16 v[188:191], v[208:211], v[100:103], 0\n\t" \
  "v_lshlrev_b32_sdwa v223, 2, v222 dst_sel:DWORD dst_unused:UNUSED_PAD src0_sel:DWORD src1_sel:BYTE_3\n\t" \
  "v_lshlrev_b32_sdwa v222, 2, v222 dst_sel:DWORD dst_unused:UNUSED_PAD src0_sel:DWORD src1_sel:BYTE_1\n\t" \
  "s_mov_b64 exec, 3\n\t" \
  "ds_add_rtn_u32 v246, v251, v250 offset:47144\n\t" \
  "v_mfma_f32_16x16x32_bf16 v[196:199], v[208:211], v[104:107], 0\n\t" \
  "s_mov_b64 exec, -1\n\t" \
  "v_lshlrev_b32_sdwa v225, 1, v187 dst_sel:DWORD dst_unused:UNUSED_PAD src0_sel:DWORD src1_sel:WORD_1\n\t" \
  "v_lshlrev_b32_sdwa v224, 1, v186 dst_sel:DWORD dst_unused:UNUSED_PAD src0_sel:DWORD src1_sel:WORD_1\n\t" \
  "ds_read_b32 v221, v221 offset:10016\n\t" \
  "v_mfma_f32_16x16x32_bf16 v[204:207], v[208:211], v[108:111], 0\n\t" \
  "s_waitcnt lgkmcnt(1)\n\t" \
  "v_lshlrev_b32_sdwa v227, 1, v185 dst_sel:DWORD dst_unused:UNUSED_PAD src0_sel:DWORD src1_sel:WORD_1\n\t" \
  "v_readfirstlane_b32 s4, v246\n\t" \
  "ds_read_b32 v220, v220 offset:10016\n\t" \
  "v_mfma_f32_16x16x32_bf16 v[180:183], v[212:215], v[112:115], v[180:183]\n\t" \
  "s_cmpk_lt_u32 s4, 3072\n\t" \
  "v_lshlrev_b32_sdwa v226, 1, v184 dst_sel:DWORD dst_unused:UNUSED_PAD src0_sel:DWORD src1_sel:WORD_1\n\t" \
  "s_cbranch_scc1 .Lcnx_w0_2\n.Lcnx_g0_2:\n\t" \
  "ds_read_b32 v223, v223 offset:10016\n\t" \
  "v_mfma_f32_16x16x32_bf16 v[188:191], v[212:215], v[116:119], v[188:191]\n\t" \
  "v_fma_mix_f32 v229, |v165|, v229, v229 op_sel:[0,1,0] op_sel_hi:[0,1,1] clamp\n\t" \
  "v_fma_mix_f32 v228, |v164|, v228, v228 op_sel:[0,1,0] op_sel_hi:[0,1,1] clamp\n\t" \
  "ds_read_b64_tr_b16 v[208:209], v249 offset:24600\n\t" \
  "ds_read_b32 v222, v222 offset:10016\n\t" \
  "v_mfma_f32_16x16x32_bf16 v[196:199], v[212:215], v[120:123], v[196:199]\n\t" \
  "v_fma_mix_f32 v231, |v167|, v231, v231 op_sel:[0,1,0] op_sel_hi:[0,1,1] clamp\n\t" \
  "v_fma_mix_f32 v230, |v166|, v230, v230 op_sel:[0,1,0] op_sel_hi:[0,1,1] clamp\n\t" \
  "ds_read_b64_tr_b16 v[210:211], v249 offset:26136\n\t" \
  "v_mfma_f32_16x16x32_bf16 v[204:207], v[212:215], v[124:127], v[204:207]\n\t" \
  "v_or_b32 v172, v232, v233\n\t" \
  "ds_read_b64_tr_b16 v[212:213], v249 offset:24608\n\t" \
  "ds_read_u16_d16_hi v225, v225 offset:10528\n\t" \
  "v_mfma_f32_16x16x32_bf16 v[180:183], v[216:219], v[128:131], v[180:183]\n\t" \
  "s_mov_b32 m0, s46\n\t" \
  "v_or_b32 v173, v234, v235\n\t" \
  "v_fma_f32 v165, |v165|, v229, v165 mul:4\n\t" \
  "s_mov_b32 exec_hi, 0xffff\n\t" \
  "buffer_load_dwordx4 v252, s[16:19], s33 offen lds\n\t" \
  "v_mfma_f32_16x16x32_bf16 v[188:191], v[216:219], v[132:135], v[188:191]\n\t" \
  "s_mov_b32 exec_hi, -1\n\t" \
  "v_fma_f32 v164, |v164|, v228, v164 mul:4\n\t" \
  "v_fma_f32 v167, |v167|, v231, v167 mul:4\n\t" \
  "s_add_u32 s33, s33, s32\n\t" \
  "ds_read_b64_tr_b16 v[214:215], v249 offset:26144\n\t" \
  "v_mfma_f32_16x16x32_bf16 v[196:199], v[216:219], v[136:139], v[196:199]\n\t" \
  "v_fma_f32 v166, |v166|, v230, v166 mul:4\n\t" \
  "v_cvt_pk_bf16_f32 v164, v164, v165\n\t" \
  "ds_read_u16 v224, v224 offset:10528\n\t" \
  "v_mfma_f32_16x16x32_bf16 v[204:207], v[216:219], v[140:143], v[204:207]\n\t" \
  "v_cvt_pk_bf16_f32 v165, v166, v167\n\t" \
  "ds_read_b64_tr_b16 v[216:217], v249 offset:24664\n\t" \
  "s_waitcnt lgkmcnt(5)\n\t" \
  "v_mfma_f32_16x16x32_bf16 v[0:3], v[208:211], v[146:149], v[0:3]\n\t" \
  "v_cvt_pknorm_i16_f32 v228, |v194|, |v195|\n\t" \
  "v_cvt_pknorm_i16_f32 v230, |v192|, |v193|\n\t" \
  "ds_read_b64_tr_b16 v[218:219], v249 offset:26200\n\t" \
  "v_mfma_f32_16x16x32_bf16 v[24:27], v[208:211], v[154:157], v[24:27]\n\t" \
  "v_lshlrev_b32_sdwa v229, 2, v228 dst_sel:DWORD dst_unused:UNUSED_PAD src0_sel:DWORD src1_sel:BYTE_3\n\t" \
  "v_lshlrev_b32_sdwa v228, 2, v228 dst_sel:DWORD dst_unused:UNUSED_PAD src0_sel:DWORD src1_sel:BYTE_1\n\t" \
  "ds_read_u16_d16_hi v227, v227 offset:10528\n\t" \
  "v_mfma_f32_16x16x32_bf16 v[48:51], v[208:211], v[162:165], v[48:51]\n\t" \
  "v_lshlrev_b32_sdwa v231, 2, v230 dst_sel:DWORD dst_unused:UNUSED_PAD src0_sel:DWORD src1_sel:BYTE_3\n\t" \
  "v_lshlrev_b32_sdwa v230, 2, v230 dst_sel:DWORD dst_unused:UNUSED_PAD src0_sel:DWORD src1_sel:BYTE_1\n\t" \
  "ds_read_u16 v226, v226 offset:10528\n\t" \
  "v_mfma_f32_16x16x32_bf16 v[72:75], v[208:211], v[170:173], v[72:75]\n\t" \
  "s_add_u32 m0, s46, s35\n\t" \
  "v_lshlrev_b32_sdwa v233, 1, v203 dst_sel:DWORD dst_unused:UNUSED_PAD src0_sel:DWORD src1_sel:WORD_1\n\t" \
  "v_lshlrev_b32_sdwa v232, 1, v202 dst_sel:DWORD dst_unused:UNUSED_PAD src0_sel:DWORD src1_sel:WORD_1\n\t" \
  "s_waitcnt lgkmcnt(5)\n\t" \
  "s_mov_b32 exec_hi, 0xffff\n\t" \
  "buffer_load_dwordx4 v252, s[16:19], s34 offen lds\n\t" \
  "v_mfma_f32_16x16x32_bf16 v[4:7], v[212:215], v[146:149], v[4:7]\n\t" \
  "s_mov_b32 exec_hi, -1\n\t" \
  "v_lshlrev_b32_sdwa v235, 1, v201 dst_sel:DWORD dst_unused:UNUSED_PAD src0_sel:DWORD src1_sel:WORD_1\n\t" \
  "s_add_u32 s34, s34, s32\n\t" \
  "ds_read_b64_tr_b16 v[208:209], v249 offset:24672\n\t" \
  "ds_read_b32 v229, v229 offset:10016\n\t" \
  "v_mfma_f32_16x16x32_bf16 v[28:31], v[212:215], v[154:157], v[28:31]\n\t" \
  "v_lshlrev_b32_sdwa v234, 1, v200 dst_sel:DWORD dst_unused:UNUSED_PAD src0_sel:DWORD src1_sel:WORD_1\n\t" \
  "ds_read_b64_tr_b16 v[210:211], v249 offset:26208\n\t" \
  "ds_read_b32 v228, v228 offset:10016\n\t" \
  "v_mfma_f32_16x16x32_bf16 v[52:55], v[212:215], v[162:165], v[52:55]\n\t" \
  "v_fma_mix_f32 v221, |v179|, v221, v221 op_sel:[0,1,0] op_sel_hi:[0,1,1] clamp\n\t" \
  "v_fma_mix_f32 v220, |v178|, v220, v220 op_sel:[0,1,0] op_sel_hi:[0,1,1] clamp\n\t" \
  "ds_read_b32 v231, v231 offset:10016\n\t" \
  "v_mfma_f32_16x16x32_bf16 v[76:79], v[212:215], v[170:173], v[76:79]\n\t" \
  "v_fma_mix_f32 v223, |v177|, v223, v223 op_sel:[0,1,0] op_sel_hi:[0,1,1] clamp\n\t" \
  "v_fma_mix_f32 v222, |v176|, v222, v222 op_sel:[0,1,0] op_sel_hi:[0,1,1] clamp\n\t" \
  "s_waitcnt lgkmcnt(5)\n\t" \
  "ds_read_b64_tr_b16 v[212:213], v249 offset:24728\n\t" \
  "ds_read_b32 v230, v230 offset:10016\n\t" \
  "v_mfma_f32_16x16x32_bf16 v[8:11], v[216:219], v[146:149], v[8:11]\n\t" \
  "v_or_b32 v187, v224, v225\n\t" \
  "v_or_b32 v186, v226, v227\n\t" \
  "ds_read_b64_tr_b16 v[214:215], v249 offset:26264\n\t" \
  "v_mfma_f32_16x16x32_bf16 v[32:35], v[216:219], v[154:157], v[32:35]\n\t" \
  "v_fma_f32 v179, |v179|, v221, v179 mul:4\n\t" \
  "v_fma_f32 v178, |v178|, v220, v178 mul:4\n\t" \
  "ds_read_u16_d16_hi v233, v233 offset:10528\n\t" \
  "v_mfma_f32_16x16x32_bf16 v[56:59], v[216:219], v[162:165], v[56:59]\n\t" \
  "v_fma_f32 v177, |v177|, v223, v177 mul:4\n\t" \
  "v_fma_f32 v176, |v176|, v222, v176 mul:4\n\t" \
  "s_add_u32 m0, s25, 0\n\t" \
  "s_add_u32 s24, s24, s26\n\t" \
  "s_sub_u32 s24, s24, s64\n\t" \
  "ds_read_u16 v232, v232 offset:10528\n\t" \
  "v_mfma_f32_16x16x32_bf16 v[80:83], v[216:219], v[170:173], v[80:83]\n\t" \
  "v_cvt_pk_bf16_f32 v179, v178, v179\n\t" \
  "v_cvt_pk_bf16_f32 v178, v176, v177\n\t" \
  "s_waitcnt lgkmcnt(7)\n\t" \
  "buffer_load_dwordx4 v254, s[8:11], s24 offen nt lds\n\t" \
  "ds_read_b64_tr_b16 v[216:217], v249 offset:24736\n\t" \
  "v_mfma_f32_16x16x32_bf16 v[12:15], v[208:211], v[146:149], v[12:15]\n\t" \
  "v_cvt_pknorm_i16_f32 v220, |v180|, |v181|\n\t" \
  "v_cvt_pknorm_i16_f32 v222, |v182|, |v183|\n\t" \
  "ds_read_b64_tr_b16 v[218:219], v249 offset:26272\n\t" \
  "v_mfma_f32_16x16x32_bf16 v[36:39], v[208:211], v[154:157], v[36:39]\n\t" \
  "v_lshlrev_b32_sdwa v221, 2, v220 dst_sel:DWORD dst_unused:UNUSED_PAD src0_sel:DWORD src1_sel:BYTE_3\n\t" \
  "v_lshlrev_b32_sdwa v220, 2, v220 dst_sel:DWORD dst_unused:UNUSED_PAD src0_sel:DWORD src1_sel:BYTE_1\n\t" \
  "ds_read_u16_d16_hi v235, v235 offset:10528\n\t" \
  "v_mfma_f32_16x16x32_bf16 v[60:63], v[208:211], v[162:165], v[60:63]\n\t" \
  "v_lshlrev_b32_sdwa v223, 2, v222 dst_sel:DWORD dst_unused:UNUSED_PAD src0_sel:DWORD src1_sel:BYTE_3\n\t" \
  "v_lshlrev_b32_sdwa v222, 2, v222 dst_sel:DWORD dst_unused:UNUSED_PAD src0_sel:DWORD src1_sel:BYTE_1\n\t" \
  "ds_read_u16 v234, v234 offset:10528\n\t" \
  "v_mfma_f32_16x16x32_bf16 v[84:87], v[208:211], v[170:173], v[84:87]\n\t" \
  "v_lshlrev_b32_sdwa v225, 1, v189 dst_sel:DWORD dst_unused:UNUSED_PAD src0_sel:DWORD src1_sel:WORD_1\n\t" \
  "v_lshlrev_b32_sdwa v224, 1, v188 dst_sel:DWORD dst_unused:UNUSED_PAD src0_sel:DWORD src1_sel:WORD_1\n\t" \
  "ds_read_b64_tr_b16 v[208:209], v248 offset:36904\n\t" \
  "s_waitcnt lgkmcnt(7)\n\t" \
  "v_mfma_f32_16x16x32_bf16 v[16:19], v[212:215], v[146:149], v[16:19]\n\t" \
  "v_lshlrev_b32_sdwa v227, 1, v191 dst_sel:DWORD dst_unused:UNUSED_PAD src0_sel:DWORD src1_sel:WORD_1\n\t" \
  "ds_read_b64_tr_b16 v[210:211], v248 offset:37928\n\t" \
  "ds_read_b32 v221, v221 offset:10016\n\t" \
  "v_mfma_f32_16x16x32_bf16 v[40:43], v[212:215], v[154:157], v[40:43]\n\t" \
  "s_add_u32 s27, s27, s26\n\t" \
  "v_lshlrev_b32_sdwa v226, 1, v190 dst_sel:DWORD dst_unused:UNUSED_PAD src0_sel:DWORD src1_sel:WORD_1\n\t" \
  "v_fma_mix_f32 v229, |v195|, v229, v229 op_sel:[0,1,0] op_sel_hi:[0,1,1] clamp\n\t" \
  "s_mov_b64 exec, s[30:31]\n\t" \
  "buffer_load_dword v237, v254, s[8:11], s27 offen\n\t" \
  "v_mfma_f32_16x16x32_bf16 v[64:67], v[212:215], v[162:165], v[64:67]\n\t" \
  "s_mov_b64 exec, -1\n\t" \
  "v_fma_mix_f32 v228, |v194|, v228, v228 op_sel:[0,1,0] op_sel_hi:[0,1,1] clamp\n\t" \
  "ds_read_b32 v220, v220 offset:10016\n\t" \
  "ds_read_b32 v223, v223 offset:10016\n\t" \
  "v_mfma_f32_16x16x32_bf16 v[88:91], v[212:215], v[170:173], v[88:91]\n\t" \
  "v_fma_mix_f32 v231, |v193|, v231, v231 op_sel:[0,1,0] op_sel_hi:[0,1,1] clamp\n\t" \
  "v_fma_mix_f32 v230, |v192|, v230, v230 op_sel:[0,1,0] op_sel_hi:[0,1,1] clamp\n\t" \
  "s_waitcnt lgkmcnt(5)\n\t" \
  "ds_read_b64_tr_b16 v[212:213], v248 offset:39976\n\t" \
  "ds_read_b32 v222, v222 offset:10016\n\t" \
  "v_mfma_f32_16x16x32_bf16 v[20:23], v[216:219], v[146:149], v[20:23]\n\t" \
  "v_or_b32 v203, v232, v233\n\t" \
  "v_or_b32 v202, v234, v235\n\t" \
  "ds_read_b64_tr_b16 v[214:215], v248 offset:41000\n\t" \
  "v_mfma_f32_16x16x32_bf16 v[44:47], v[216:219], v[154:157], v[44:47]\n\t" \
  "v_fma_f32 v195, |v195|, v229, v195 mul:4\n\t" \
  "v_fma_f32 v194, |v194|, v228, v194 mul:4\n\t" \
  "ds_read_u16_d16_hi v225, v225 offset:10528\n\t" \
  "v_mfma_f32_16x16x32_bf16 v[68:71], v[216:219], v[162:165], v[68:71]\n\t" \
  "v_fma_f32 v193, |v193|, v231, v193 mul:4\n\t" \
  "v_fma_f32 v192, |v192|, v230, v192 mul:4\n\t" \
  "ds_read_u16 v224, v224 offset:10528\n\t" \
  "v_mfma_f32_16x16x32_bf16 v[92:95], v[216:219], v[170:173], v[92:95]\n\t" \
  "v_cvt_pk_bf16_f32 v195, v194, v195\n\t" \
  "v_cvt_pk_bf16_f32 v194, v192, v193\n\t" \
  "ds_read_b64_tr_b16 v[216:217], v248 offset:38952\n\t" \
  "s_waitcnt lgkmcnt(9)\n\t" \
  "v_mfma_f32_16x16x32_bf16 v[144:147], v[208:211], v[96:99], 0\n\t" \
  "v_cvt_pknorm_i16_f32 v228, |v196|, |v197|\n\t" \
  "v_cvt_pknorm_i16_f32 v230, |v198|, |v199|\n\t" \
  "ds_read_b64_tr_b16 v[218:219], v248 offset:42024\n\t" \
  "v_mfma_f32_16x16x32_bf16 v[152:155], v[208:211], v[100:103], 0\n\t" \
  "v_lshlrev_b32_sdwa v229, 2, v228 dst_sel:DWORD dst_unused:UNUSED_PAD src0_sel:DWORD src1_sel:BYTE_3\n\t" \
  "v_lshlrev_b32_sdwa v228, 2, v228 dst_sel:DWORD dst_unused:UNUSED_PAD src0_sel:DWORD src1_sel:BYTE_1\n\t" \
  "ds_read_u16_d16_hi v227, v227 offset:10528\n\t" \
  "v_mfma_f32_16x16x32_bf16 v[160:163], v[208:211], v[104:107], 0\n\t" \
  "v_lshlrev_b32_sdwa v231, 2, v230 dst_sel:DWORD dst_unused:UNUSED_PAD src0_sel:DWORD src1_sel:BYTE_3\n\t" \
  "v_lshlrev_b32_sdwa v230, 2, v230 dst_sel:DWORD dst_unused:UNUSED_PAD src0_sel:DWORD src1_sel:BYTE_1\n\t" \
  "ds_read_u16 v226, v226 offset:10528\n\t" \
  "v_mfma_f32_16x16x32_bf16 v[168:171], v[208:211], v[108:111], 0\n\t" \
  "v_lshlrev_b32_sdwa v233, 1, v205 dst_sel:DWORD dst_unused:UNUSED_PAD src0_sel:DWORD src1_sel:WORD_1\n\t" \
  "v_lshlrev_b32_sdwa v232, 1, v204 dst_sel:DWORD dst_unused:UNUSED_PAD src0_sel:DWORD src1_sel:WORD_1\n\t" \
  "ds_read_b64_tr_b16 v[208:209], v248 offset:37416\n\t" \
  "s_waitcnt lgkmcnt(7)\n\t" \
  "v_mfma_f32_16x16x32_bf16 v[144:147], v[212:215], v[112:115], v[144:147]\n\t" \
  "v_lshlrev_b32_sdwa v235, 1, v207 dst_sel:DWORD dst_unused:UNUSED_PAD src0_sel:DWORD src1_sel:WORD_1\n\t" \
  "ds_read_b64_tr_b16 v[210:211], v248 offset:38440\n\t" \
  "ds_read_b32 v229, v229 offset:10016\n\t" \
  "v_mfma_f32_16x16x32_bf16 v[152:155], v[212:215], v[116:119], v[152:155]\n\t" \
  "v_lshlrev_b32_sdwa v234, 1, v206 dst_sel:DWORD dst_unused:UNUSED_PAD src0_sel:DWORD src1_sel:WORD_1\n\t" \
  "v_fma_mix_f32 v221, |v181|, v221, v221 op_sel:[0,1,0] op_sel_hi:[0,1,1] clamp\n\t" \
  "ds_read_b32 v228, v228 offset:10016\n\t" \
  "v_mfma_f32_16x16x32_bf16 v[160:163], v[212:215], v[120:123], v[160:163]\n\t" \
  "v_fma_mix_f32 v220, |v180|, v220, v220 op_sel:[0,1,0] op_sel_hi:[0,1,1] clamp\n\t" \
  "ds_read_b32 v231, v231 offset:10016\n\t" \
  "ds_read_b32 v230, v230 offset:10016\n\t" \
  "v_mfma_f32_16x16x32_bf16 v[168:171], v[212:215], v[124:127], v[168:171]\n\t" \
  "v_fma_mix_f32 v223, |v183|, v223, v223 op_sel:[0,1,0] op_sel_hi:[0,1,1] clamp\n\t" \
  "v_fma_mix_f32 v222, |v182|, v222, v222 op_sel:[0,1,0] op_sel_hi:[0,1,1] clamp\n\t" \
  "s_waitcnt lgkmcnt(6)\n\t" \
  "ds_read_b64_tr_b16 v[212:213], v248 offset:40488\n\t" \
  "ds_read_u16_d16_hi v233, v233 offset:10528\n\t" \
  "v_mfma_f32_16x16x32_bf16 v[144:147], v[216:219], v[128:131], v[144:147]\n\t" \
  "v_or_b32 v188, v224, v225\n\t" \
  "v_or_b32 v189, v226, v227\n\t" \
  "ds_read_b64_tr_b16 v[214:215], v248 offset:41512\n\t" \
  "v_mfma_f32_16x16x32_bf16 v[152:155], v[216:219], v[132:135], v[152:155]\n\t" \
  "v_fma_f32 v181, |v181|, v221, v181 mul:4\n\t" \
  "v_fma_f32 v180, |v180|, v220, v180 mul:4\n\t" \
  "ds_read_u16 v232, v232 offset:10528\n\t" \
  "v_mfma_f32_16x16x32_bf16 v[160:163], v[216:219], v[136:139], v[160:163]\n\t" \
  "v_fma_f32 v183, |v183|, v223, v183 mul:4\n\t" \
  "v_fma_f32 v182, |v182|, v222, v182 mul:4\n\t" \
  "ds_read_u16_d16_hi v235, v235 offset:10528\n\t" \
  "v_mfma_f32_16x16x32_bf16 v[168:171], v[216:219], v[140:143], v[168:171]\n\t" \
  "v_cvt_pk_bf16_f32 v180, v180, v181\n\t" \
  "v_cvt_pk_bf16_f32 v181, v182, v183\n\t" \
  "ds_read_b64_tr_b16 v[216:217], v248 offset:39464\n\t" \
  "s_waitcnt lgkmcnt(10)\n\t" \
  "v_mfma_f32_16x16x32_bf16 v[148:151], v[208:211], v[96:99], 0\n\t" \
  "v_cvt_pknorm_i16_f32 v220, |v146|, |v147|\n\t" \
  "v_cvt_pknorm_i16_f32 v222, |v144|, |v145|\n\t" \
  "s_waitcnt vmcnt(2)\n\t" \
  "ds_read_b64_tr_b16 v[218:219], v248 offset:42536\n\t" \
  "v_mfma_f32_16x16x32_bf16 v[156:159], v[208:211], v[100:103], 0\n\t" \
  "v_lshlrev_b32_sdwa v221, 2, v220 dst_sel:DWORD dst_unused:UNUSED_PAD src0_sel:DWORD src1_sel:BYTE_3\n\t" \
  "v_lshlrev_b32_sdwa v220, 2, v220 dst_sel:DWORD dst_unused:UNUSED_PAD src0_sel:DWORD src1_sel:BYTE_1\n\t" \
  "s_mov_b64 exec, 3\n\t" \
  "ds_add_rtn_u32 v246, v251, v250 offset:47152\n\t" \
  "v_mfma_f32_16x16x32_bf16 v[164:167], v[208:211], v[104:107], 0\n\t" \
  "s_mov_b64 exec, -1\n\t" \
  "v_lshlrev_b32_sdwa v223, 2, v222 dst_sel:DWORD dst_unused:UNUSED_PAD src0_sel:DWORD src1_sel:BYTE_3\n\t" \
  "v_lshlrev_b32_sdwa v222, 2, v222 dst_sel:DWORD dst_unused:UNUSED_PAD src0_sel:DWORD src1_sel:BYTE_1\n\t" \
  "ds_read_u16 v234, v234 offset:10528\n\t" \
  "v_mfma_f32_16x16x32_bf16 v[172:175], v[208:211], v[108:111], 0\n\t" \
  "s_waitcnt lgkmcnt(1)\n\t" \
  "v_lshlrev_b32_sdwa v225, 1, v155 dst_sel:DWORD dst_unused:UNUSED_PAD src0_sel:DWORD src1_sel:WORD_1\n\t" \
  "v_readfirstlane_b32 s4, v246\n\t" \
  "ds_read_b32 v221, v221 offset:10016\n\t" \
  "v_mfma_f32_16x16x32_bf16 v[148:151], v[212:215], v[112:115], v[148:151]\n\t" \
  "s_cmpk_lt_u32 s4, 3072\n\t" \
  "v_lshlrev_b32_sdwa v224, 1, v154 dst_sel:DWORD dst_unused:UNUSED_PAD src0_sel:DWORD src1_sel:WORD_1\n\t" \
  "s_cbranch_scc1 .Lcnx_w0_3\n.Lcnx_g0_3:\n\t" \
  "ds_read_b32 v220, v220 offset:10016\n\t" \
  "v_mfma_f32_16x16x32_bf16 v[156:159], v[212:215], v[116:119], v[156:159]\n\t" \
  "v_lshlrev_b32_sdwa v227, 1, v153 dst_sel:DWORD dst_unused:UNUSED_PAD src0_sel:DWORD src1_sel:WORD_1\n\t" \
  "v_lshlrev_b32_sdwa v226, 1, v152 dst_sel:DWORD dst_unused:UNUSED_PAD src0_sel:DWORD src1_sel:WORD_1\n\t" \
  "ds_read_b64_tr_b16 v[208:209], v249 offset:30752\n\t" \
  "v_mfma_f32_16x16x32_bf16 v[164:167], v[212:215], v[120:123], v[164:167]\n\t" \
  "v_fma_mix_f32 v229, |v197|, v229, v229 op_sel:[0,1,0] op_sel_hi:[0,1,1] clamp\n\t" \
  "v_fma_mix_f32 v228, |v196|, v228, v228 op_sel:[0,1,0] op_sel_hi:[0,1,1] clamp\n\t" \
  "ds_read_b64_tr_b16 v[210:211], v249 offset:32288\n\t" \
  "ds_read_b32 v223, v223 offset:10016\n\t" \
  "v_mfma_f32_16x16x32_bf16 v[172:175], v[212:215], v[124:127], v[172:175]\n\t" \
  "v_fma_mix_f32 v231, |v199|, v231, v231 op_sel:[0,1,0] op_sel_hi:[0,1,1] clamp\n\t" \
  "v_fma_mix_f32 v230, |v198|, v230, v230 op_sel:[0,1,0] op_sel_hi:[0,1,1] clamp\n\t" \
  "s_waitcnt lgkmcnt(5)\n\t" \
  "ds_read_b64_tr_b16 v[212:213], v249 offset:30760\n\t" \
  "ds_read_b32 v222, v222 offset:10016\n\t" \
  "v_mfma_f32_16x16x32_bf16 v[148:151], v[216:219], v[128:131], v[148:151]\n\t" \
  "s_mov_b32 m0, s47\n\t" \
  "v_or_b32 v204, v232, v233\n\t" \
  "v_or_b32 v205, v234, v235\n\t" \
  "s_mov_b32 exec_hi, 0xffff\n\t" \
  "buffer_load_dwordx4 v252, s[16:19], s33 offen lds\n\t" \
  "v_mfma_f32_16x16x32_bf16 v[156:159], v[216:219], v[132:135], v[156:159]\n\t" \
  "s_mov_b32 exec_hi, -1\n\t" \
  "v_fma_f32 v197, |v197|, v229, v197 mul:4\n\t" \
  "v_fma_f32 v196, |v196|, v228, v196 mul:4\n\t" \
  "s_add_u32 s33, s33, s32\n\t" \
  "ds_read_b64_tr_b16 v[214:215], v249 offset:32296\n\t" \
  "v_mfma_f32_16x16x32_bf16 v[164:167], v[216:219], v[136:139], v[164:167]\n\t" \
  "v_fma_f32 v199, |v199|, v231, v199 mul:4\n\t" \
  "v_fma_f32 v198, |v198|, v230, v198 mul:4\n\t" \
  "ds_read_u16_d16_hi v225, v225 offset:10528\n\t" \
  "v_mfma_f32_16x16x32_bf16 v[172:175], v[216:219], v[140:143], v[172:175]\n\t" \
  "v_cvt_pk_bf16_f32 v196, v196, v197\n\t" \
  "v_cvt_pk_bf16_f32 v197, v198, v199\n\t" \
  "ds_read_b64_tr_b16 v[216:217], v249 offset:30816\n\t" \
  "s_waitcnt lgkmcnt(6)\n\t" \
  "v_mfma_f32_16x16x32_bf16 v[0:3], v[208:211], v[178:181], v[0:3]\n\t" \
  "v_cvt_pknorm_i16_f32 v228, |v162|, |v163|\n\t" \
  "v_cvt_pknorm_i16_f32 v230, |v160|, |v161|\n\t" \
  "ds_read_b64_tr_b16 v[218:219], v249 offset:32352\n\t" \
  "v_mfma_f32_16x16x32_bf16 v[24:27], v[208:211], v[186:189], v[24:27]\n\t" \
  "v_lshlrev_b32_sdwa v229, 2, v228 dst_sel:DWORD dst_unused:UNUSED_PAD src0_sel:DWORD src1_sel:BYTE_3\n\t" \
  "v_lshlrev_b32_sdwa v228, 2, v228 dst_sel:DWORD dst_unused:UNUSED_PAD src0_sel:DWORD src1_sel:BYTE_1\n\t" \
  "ds_read_u16 v224, v224 offset:10528\n\t" \
  "v_mfma_f32_16x16x32_bf16 v[48:51], v[208:211], v[194:197], v[48:51]\n\t" \
  "v_lshlrev_b32_sdwa v231, 2, v230 dst_sel:DWORD dst_unused:UNUSED_PAD src0_sel:DWORD src1_sel:BYTE_3\n\t" \
  "v_lshlrev_b32_sdwa v230, 2, v230 dst_sel:DWORD dst_unused:UNUSED_PAD src0_sel:DWORD src1_sel:BYTE_1\n\t" \
  "ds_read_u16_d16_hi v227, v227 offset:10528\n\t" \
  "v_mfma_f32_16x16x32_bf16 v[72:75], v[208:211], v[202:205], v[72:75]\n\t" \
  "s_add_u32 m0, s47, s35\n\t" \
  "v_lshlrev_b32_sdwa v233, 1, v171 dst_sel:DWORD dst_unused:UNUSED_PAD src0_sel:DWORD src1_sel:WORD_1\n\t" \
  "v_lshlrev_b32_sdwa v232, 1, v170 dst_sel:DWORD dst_unused:UNUSED_PAD src0_sel:DWORD src1_sel:WORD_1\n\t" \
  "s_waitcnt lgkmcnt(5)\n\t" \
  "s_mov_b32 exec_hi, 0xffff\n\t" \
  "buffer_load_dwordx4 v252, s[16:19], s34 offen lds\n\t" \
  "v_mfma_f32_16x16x32_bf16 v[4:7], v[212:215], v[178:181], v[4:7]\n\t" \
  "s_mov_b32 exec_hi, -1\n\t" \
  "v_lshlrev_b32_sdwa v235, 1, v169 dst_sel:DWORD dst_unused:UNUSED_PAD src0_sel:DWORD src1_sel:WORD_1\n\t" \
  "s_add_u32 s34, s34, s32\n\t" \
  "ds_read_b64_tr_b16 v[208:209], v249 offset:30824\n\t" \
  "ds_read_u16 v226, v226 offset:10528\n\t" \
  "v_mfma_f32_16x16x32_bf16 v[28:31], v[212:215], v[186:189], v[28:31]\n\t" \
  "v_lshlrev_b32_sdwa v234, 1, v168 dst_sel:DWORD dst_unused:UNUSED_PAD src0_sel:DWORD src1_sel:WORD_1\n\t" \
  "ds_read_b64_tr_b16 v[210:211], v249 offset:32360\n\t" \
  "ds_read_b32 v229, v229 offset:10016\n\t" \
  "v_mfma_f32_16x16x32_bf16 v[52:55], v[212:215], v[194:197], v[52:55]\n\t" \
  "v_fma_mix_f32 v221, |v147|, v221, v221 op_sel:[0,1,0] op_sel_hi:[0,1,1] clamp\n\t" \
  "v_fma_mix_f32 v220, |v146|, v220, v220 op_sel:[0,1,0] op_sel_hi:[0,1,1] clamp\n\t" \
  "ds_read_b32 v228, v228 offset:10016\n\t" \
  "ds_read_b32 v231, v231 offset:10016\n\t" \
  "v_mfma_f32_16x16x32_bf16 v[76:79], v[212:215], v[202:205], v[76:79]\n\t" \
  "v_fma_mix_f32 v223, |v145|, v223, v223 op_sel:[0,1,0] op_sel_hi:[0,1,1] clamp\n\t" \
  "v_fma_mix_f32 v222, |v144|, v222, v222 op_sel:[0,1,0] op_sel_hi:[0,1,1] clamp\n\t" \
  "s_waitcnt lgkmcnt(4)\n\t" \
  "ds_read_b64_tr_b16 v[212:213], v249 offset:30880\n\t" \
  "ds_read_b32 v230, v230 offset:10016\n\t" \
  "v_mfma_f32_16x16x32_bf16 v[8:11], v[216:219], v[178:181], v[8:11]\n\t" \
  "v_or_b32 v155, v224, v225\n\t" \
  "v_or_b32 v154, v226, v227\n\t" \
  "ds_read_b64_tr_b16 v[214:215], v249 offset:32416\n\t" \
  "v_mfma_f32_16x16x32_bf16 v[32:35], v[216:219], v[186:189], v[32:35]\n\t" \
  "v_fma_f32 v147, |v147|, v221, v147 mul:4\n\t" \
  "v_fma_f32 v146, |v146|, v220, v146 mul:4\n\t" \
  "ds_read_u16_d16_hi v233, v233 offset:10528\n\t" \
  "v_mfma_f32_16x16x32_bf16 v[56:59], v[216:219], v[194:197], v[56:59]\n\t" \
  "v_fma_f32 v145, |v145|, v223, v145 mul:4\n\t" \
  "v_fma_f32 v144, |v144|, v222, v144 mul:4\n\t" \
  "s_add_u32 m0, s25, 8200\n\t" \
  "s_add_u32 s24, s24, s26\n\t" \
  "ds_read_u16 v232, v232 offset:10528\n\t" \
  "v_mfma_f32_16x16x32_bf16 v[80:83], v[216:219], v[202:205], v[80:83]\n\t" \
  "v_cvt_pk_bf16_f32 v147, v146, v147\n\t" \
  "v_cvt_pk_bf16_f32 v146, v144, v145\n\t" \
  "s_waitcnt lgkmcnt(8)\n\t" \
  "buffer_load_dwordx4 v254, s[8:11], s24 offen nt lds\n\t" \
  "ds_read_b64_tr_b16 v[216:217], v249 offset:30888\n\t" \
  "v_mfma_f32_16x16x32_bf16 v[12:15], v[208:211], v[178:181], v[12:15]\n\t" \
  "v_cvt_pknorm_i16_f32 v220, |v148|, |v149|\n\t" \
  "v_cvt_pknorm_i16_f32 v222, |v150|, |v151|\n\t" \
  "ds_read_b64_tr_b16 v[218:219], v249 offset:32424\n\t" \
  "v_mfma_f32_16x16x32_bf16 v[36:39], v[208:211], v[186:189], v[36:39]\n\t" \
  "v_lshlrev_b32_sdwa v221, 2, v220 dst_sel:DWORD dst_unused:UNUSED_PAD src0_sel:DWORD src1_sel:BYTE_3\n\t" \
  "v_lshlrev_b32_sdwa v220, 2, v220 dst_sel:DWORD dst_unused:UNUSED_PAD src0_sel:DWORD src1_sel:BYTE_1\n\t" \
  "ds_read_u16_d16_hi v235, v235 offset:10528\n\t" \
  "v_mfma_f32_16x16x32_bf16 v[60:63], v[208:211], v[194:197], v[60:63]\n\t" \
  "v_lshlrev_b32_sdwa v223, 2, v222 dst_sel:DWORD dst_unused:UNUSED_PAD src0_sel:DWORD src1_sel:BYTE_3\n\t" \
  "v_lshlrev_b32_sdwa v222, 2, v222 dst_sel:DWORD dst_unused:UNUSED_PAD src0_sel:DWORD src1_sel:BYTE_1\n\t" \
  "ds_read_u16 v234, v234 offset:10528\n\t" \
  "v_mfma_f32_16x16x32_bf16 v[84:87], v[208:211], v[202:205], v[84:87]\n\t" \
  "v_lshlrev_b32_sdwa v225, 1, v157 dst_sel:DWORD dst_unused:UNUSED_PAD src0_sel:DWORD src1_sel:WORD_1\n\t" \
  "v_lshlrev_b32_sdwa v224, 1, v156 dst_sel:DWORD dst_unused:UNUSED_PAD src0_sel:DWORD src1_sel:WORD_1\n\t" \
  "ds_read_b64_tr_b16 v[208:209], v248 offset:0\n\t" \
  "s_waitcnt lgkmcnt(7)\n\t" \
  "v_mfma_f32_16x16x32_bf16 v[16:19], v[212:215], v[178:181], v[16:19]\n\t" \
  "v_lshlrev_b32_sdwa v227, 1, v159 dst_sel:DWORD dst_unused:UNUSED_PAD src0_sel:DWORD src1_sel:WORD_1\n\t" \
  "ds_read_b64_tr_b16 v[210:211], v248 offset:1024\n\t" \
  "ds_read_b32 v221, v221 offset:10016\n\t" \
  "v_mfma_f32_16x16x32_bf16 v[40:43], v[212:215], v[186:189], v[40:43]\n\t" \
  "s_add_u32 s27, s27, s26\n\t" \
  "v_lshlrev_b32_sdwa v226, 1, v158 dst_sel:DWORD dst_unused:UNUSED_PAD src0_sel:DWORD src1_sel:WORD_1\n\t" \
  "v_fma_mix_f32 v229, |v163|, v229, v229 op_sel:[0,1,0] op_sel_hi:[0,1,1] clamp\n\t" \
  "s_mov_b64 exec, s[30:31]\n\t" \
  "buffer_load_dword v237, v254, s[8:11], s27 offen\n\t" \
  "v_mfma_f32_16x16x32_bf16 v[64:67], v[212:215], v[194:197], v[64:67]\n\t" \
  "s_mov_b64 exec, -1\n\t" \
  "v_fma_mix_f32 v228, |v162|, v228, v228 op_sel:[0,1,0] op_sel_hi:[0,1,1] clamp\n\t" \
  "ds_read_b32 v220, v220 offset:10016\n\t" \
  "ds_read_b32 v223, v223 offset:10016\n\t" \
  "v_mfma_f32_16x16x32_bf16 v[88:91], v[212:215], v[202:205], v[88:91]\n\t" \
  "v_fma_mix_f32 v231, |v161|, v231, v231 op_sel:[0,1,0] op_sel_hi:[0,1,1] clamp\n\t" \
  "v_fma_mix_f32 v230, |v160|, v230, v230 op_sel:[0,1,0] op_sel_hi:[0,1,1] clamp\n\t" \
  "s_waitcnt lgkmcnt(5)\n\t" \
  "ds_read_b64_tr_b16 v[212:213], v248 offset:3072\n\t" \
  "ds_read_b32 v222, v222 offset:10016\n\t" \
  "v_mfma_f32_16x16x32_bf16 v[20:23], v[216:219], v[178:181], v[20:23]\n\t" \
  "v_or_b32 v171, v232, v233\n\t" \
  "v_or_b32 v170, v234, v235\n\t" \
  "ds_read_b64_tr_b16 v[214:215], v248 offset:4096\n\t" \
  "v_mfma_f32_16x16x32_bf16 v[44:47], v[216:219], v[186:189], v[44:47]\n\t" \
  "v_fma_f32 v163, |v163|, v229, v163 mul:4\n\t" \
  "v_fma_f32 v162, |v162|, v228, v162 mul:4\n\t" \
  "ds_read_u16_d16_hi v225, v225 offset:10528\n\t" \
  "v_mfma_f32_16x16x32_bf16 v[68:71], v[216:219], v[194:197], v[68:71]\n\t" \
  "v_fma_f32 v161, |v161|, v231, v161 mul:4\n\t" \
  "v_fma_f32 v160, |v160|, v230, v160 mul:4\n\t" \
  "ds_read_u16 v224, v224 offset:10528\n\t" \
  "v_mfma_f32_16x16x32_bf16 v[92:95], v[216:219], v[202:205], v[92:95]\n\t" \
  "v_cvt_pk_bf16_f32 v163, v162, v163\n\t" \
  "v_cvt_pk_bf16_f32 v162, v160, v161\n\t" \
  "ds_read_b64_tr_b16 v[216:217], v248 offset:2048\n\t" \
  "s_waitcnt lgkmcnt(9)\n\t" \
  "v_mfma_f32_16x16x32_bf16 v[176:179], v[208:211], v[96:99], 0\n\t" \
  "v_cvt_pknorm_i16_f32 v228, |v164|, |v165|\n\t" \
  "v_cvt_pknorm_i16_f32 v230, |v166|, |v167|\n\t" \
  "ds_read_b64_tr_b16 v[218:219], v248 offset:5120\n\t" \
  "v_mfma_f32_16x16x32_bf16 v[184:187], v[208:211], v[100:103], 0\n\t" \
  "v_lshlrev_b32_sdwa v229, 2, v228 dst_sel:DWORD dst_unused:UNUSED_PAD src0_sel:DWORD src1_sel:BYTE_3\n\t" \
  "v_lshlrev_b32_sdwa v228, 2, v228 dst_sel:DWORD dst_unused:UNUSED_PAD src0_sel:DWORD src1_sel:BYTE_1\n\t" \
  "ds_read_u16_d16_hi v227, v227 offset:10528\n\t" \
  "v_mfma_f32_16x16x32_bf16 v[192:195], v[208:211], v[104:107], 0\n\t" \
  "v_lshlrev_b32_sdwa v231, 2, v230 dst_sel:DWORD dst_unused:UNUSED_PAD src0_sel:DWORD src1_sel:BYTE_3\n\t" \
  "v_lshlrev_b32_sdwa v230, 2, v230 dst_sel:DWORD dst_unused:UNUSED_PAD src0_sel:DWORD src1_sel:BYTE_1\n\t" \
  "ds_read_u16 v226, v226 offset:10528\n\t" \
  "v_mfma_f32_16x16x32_bf16 v[200:203], v[208:211], v[108:111], 0\n\t" \
  "v_lshlrev_b32_sdwa v233, 1, v173 dst_sel:DWORD dst_unused:UNUSED_PAD src0_sel:DWORD src1_sel:WORD_1\n\t" \
  "v_lshlrev_b32_sdwa v232, 1, v172 dst_sel:DWORD dst_unused:UNUSED_PAD src0_sel:DWORD src1_sel:WORD_1\n\t" \
  "ds_read_b64_tr_b16 v[208:209], v248 offset:512\n\t" \
  "s_waitcnt lgkmcnt(7)\n\t" \
  "v_mfma_f32_16x16x32_bf16 v[176:179], v[212:215], v[112:115], v[176:179]\n\t" \
  "v_lshlrev_b32_sdwa v235, 1, v175 dst_sel:DWORD dst_unused:UNUSED_PAD src0_sel:DWORD src1_sel:WORD_1\n\t" \
  "ds_read_b64_tr_b16 v[210:211], v248 offset:1536\n\t" \
  "ds_read_b32 v229, v229 offset:10016\n\t" \
  "v_mfma_f32_16x16x32_bf16 v[184:187], v[212:215], v[116:119], v[184:187]\n\t" \
  "v_lshlrev_b32_sdwa v234, 1, v174 dst_sel:DWORD dst_unused:UNUSED_PAD src0_sel:DWORD src1_sel:WORD_1\n\t" \
  "v_fma_mix_f32 v221, |v149|, v221, v221 op_sel:[0,1,0] op_sel_hi:[0,1,1] clamp\n\t" \
  "ds_read_b32 v228, v228 offset:10016\n\t" \
  "v_mfma_f32_16x16x32_bf16 v[192:195], v[212:215], v[120:123], v[192:195]\n\t" \
  "v_fma_mix_f32 v220, |v148|, v220, v220 op_sel:[0,1,0] op_sel_hi:[0,1,1] clamp\n\t" \
  "ds_read_b32 v231, v231 offset:10016\n\t" \
  "ds_read_b32 v230, v230 offset:10016\n\t" \
  "v_mfma_f32_16x16x32_bf16 v[200:203], v[212:215], v[124:127], v[200:203]\n\t" \
  "v_fma_mix_f32 v223, |v151|, v223, v223 op_sel:[0,1,0] op_sel_hi:[0,1,1] clamp\n\t" \
  "v_fma_mix_f32 v222, |v150|, v222, v222 op_sel:[0,1,0] op_sel_hi:[0,1,1] clamp\n\t" \
  "s_waitcnt lgkmcnt(6)\n\t" \
  "ds_read_b64_tr_b16 v[212:213], v248 offset:3584\n\t" \
  "ds_read_u16_d16_hi v233, v233 offset:10528\n\t" \
  "v_mfma_f32_16x16x32_bf16 v[176:179], v[216:219], v[128:131], v[176:179]\n\t" \
  "v_or_b32 v156, v224, v225\n\t" \
  "v_or_b32 v157, v226, v227\n\t" \
  "ds_read_b64_tr_b16 v[214:215], v248 offset:4608\n\t" \
  "v_mfma_f32_16x16x32_bf16 v[184:187], v[216:219], v[132:135], v[184:187]\n\t" \
  "v_fma_f32 v149, |v149|, v221, v149 mul:4\n\t" \
  "v_fma_f32 v148, |v148|, v220, v148 mul:4\n\t" \
  "ds_read_u16 v232, v232 offset:10528\n\t" \
  "v_mfma_f32_16x16x32_bf16 v[192:195], v[216:219], v[136:139], v[192:195]\n\t" \
  "v_fma_f32 v151, |v151|, v223, v151 mul:4\n\t" \
  "v_fma_f32 v150, |v150|, v222, v150 mul:4\n\t" \
  "ds_read_u16_d16_hi v235, v235 offset:10528\n\t" \
  "v_mfma_f32_16x16x32_bf16 v[200:203], v[216:219], v[140:143], v[200:203]\n\t" \
  "v_cvt_pk_bf16_f32 v148, v148, v149\n\t" \
  "v_cvt_pk_bf16_f32 v149, v150, v151\n\t" \
  "ds_read_b64_tr_b16 v[216:217], v248 offset:2560\n\t" \
  "s_waitcnt lgkmcnt(10)\n\t" \
  "v_mfma_f32_16x16x32_bf16 v[180:183], v[208:211], v[96:99], 0\n\t" \
  "v_cvt_pknorm_i16_f32 v220, |v178|, |v179|\n\t" \
  "v_cvt_pknorm_i16_f32 v222, |v176|, |v177|\n\t" \
  "s_waitcnt vmcnt(2)\n\t" \
  "ds_read_b64_tr_b16 v[218:219], v248 offset:5632\n\t" \
  "v_mfma_f32_16x16x32_bf16 v[188:191], v[208:211], v[100:103], 0\n\t" \
  "v_lshlrev_b32_sdwa v221, 2, v220 dst_sel:DWORD dst_unused:UNUSED_PAD src0_sel:DWORD src1_sel:BYTE_3\n\t" \
  "v_lshlrev_b32_sdwa v220, 2, v220 dst_sel:DWORD dst_unused:UNUSED_PAD src0_sel:DWORD src1_sel:BYTE_1\n\t" \
  "s_mov_b64 exec, 3\n\t" \
  "ds_add_rtn_u32 v246, v251, v250 offset:47160\n\t" \
  "v_mfma_f32_16x16x32_bf16 v[196:199], v[208:211], v[104:107], 0\n\t" \
  "s_mov_b64 exec, -1\n\t" \
  "v_lshlrev_b32_sdwa v223, 2, v222 dst_sel:DWORD dst_unused:UNUSED_PAD src0_sel:DWORD src1_sel:BYTE_3\n\t" \
  "v_lshlrev_b32_sdwa v222, 2, v222 dst_sel:DWORD dst_unused:UNUSED_PAD src0_sel:DWORD src1_sel:BYTE_1\n\t" \
  "ds_read_u16 v234, v234 offset:10528\n\t" \
  "v_mfma_f32_16x16x32_bf16 v[204:207], v[208:211], v[108:111], 0\n\t" \
  "s_waitcnt lgkmcnt(1)\n\t" \
  "v_lshlrev_b32_sdwa v225, 1, v187 dst_sel:DWORD dst_unused:UNUSED_PAD src0_sel:DWORD src1_sel:WORD_1\n\t" \
  "v_readfirstlane_b32 s4, v246\n\t" \
  "ds_read_b32 v221, v221 offset:10016\n\t" \
  "v_mfma_f32_16x16x32_bf16 v[180:183], v[212:215], v[112:115], v[180:183]\n\t" \
  "s_cmpk_lt_u32 s4, 3072\n\t" \
  "v_lshlrev_b32_sdwa v224, 1, v186 dst_sel:DWORD dst_unused:UNUSED_PAD src0_sel:DWORD src1_sel:WORD_1\n\t" \
  "s_cbranch_scc1 .Lcnx_w0_4\n.Lcnx_g0_4:\n\t" \
  "ds_read_b32 v220, v220 offset:10016\n\t" \
  "v_mfma_f32_16x16x32_bf16 v[188:191], v[212:215], v[116:119], v[188:191]\n\t" \
  "v_lshlrev_b32_sdwa v227, 1, v185 dst_sel:DWORD dst_unused:UNUSED_PAD src0_sel:DWORD src1_sel:WORD_1\n\t" \
  "v_lshlrev_b32_sdwa v226, 1, v184 dst_sel:DWORD dst_unused:UNUSED_PAD src0_sel:DWORD src1_sel:WORD_1\n\t" \
  "ds_read_b64_tr_b16 v[208:209], v249 offset:6152\n\t" \
  "v_mfma_f32_16x16x32_bf16 v[196:199], v[212:215], v[120:123], v[196:199]\n\t" \
  "v_fma_mix_f32 v229, |v165|, v229, v229 op_sel:[0,1,0] op_sel_hi:[0,1,1] clamp\n\t" \
  "v_fma_mix_f32 v228, |v164|, v228, v228 op_sel:[0,1,0] op_sel_hi:[0,1,1] clamp\n\t" \
  "ds_read_b64_tr_b16 v[210:211], v249 offset:7688\n\t" \
  "ds_read_b32 v223, v223 offset:10016\n\t" \
  "v_mfma_f32_16x16x32_bf16 v[204:207], v[212:215], v[124:127], v[204:207]\n\t" \
  "v_fma_mix_f32 v231, |v167|, v231, v231 op_sel:[0,1,0] op_sel_hi:[0,1,1] clamp\n\t" \
  "v_fma_mix_f32 v230, |v166|, v230, v230 op_sel:[0,1,0] op_sel_hi:[0,1,1] clamp\n\t" \
  "s_waitcnt lgkmcnt(5)\n\t" \
  "ds_read_b64_tr_b16 v[212:213], v249 offset:6160\n\t" \
  "ds_read_b32 v222, v222 offset:10016\n\t" \
  "v_mfma_f32_16x16x32_bf16 v[180:183], v[216:219], v[128:131], v[180:183]\n\t" \
  "s_mov_b32 m0, s44\n\t" \
  "v_or_b32 v172, v232, v233\n\t" \
  "v_or_b32 v173, v234, v235\n\t" \
  "s_mov_b32 exec_hi, 0xffff\n\t" \
  "buffer_load_dwordx4 v252, s[16:19], s33 offen lds\n\t" \
  "v_mfma_f32_16x16x32_bf16 v[188:191], v[216:219], v[132:135], v[188:191]\n\t" \
  "s_mov_b32 exec_hi, -1\n\t" \
  "v_fma_f32 v165, |v165|, v229, v165 mul:4\n\t" \
  "v_fma_f32 v164, |v164|, v228, v164 mul:4\n\t" \
  "s_add_u32 s33, s33, s32\n\t" \
  "ds_read_b64_tr_b16 v[214:215], v249 offset:7696\n\t" \
  "v_mfma_f32_16x16x32_bf16 v[196:199], v[216:219], v[136:139], v[196:199]\n\t" \
  "v_fma_f32 v167, |v167|, v231, v167 mul:4\n\t" \
  "v_fma_f32 v166, |v166|, v230, v166 mul:4\n\t" \
  "ds_read_u16_d16_hi v225, v225 offset:10528\n\t" \
  "v_mfma_f32_16x16x32_bf16 v[204:207], v[216:219], v[140:143], v[204:207]\n\t" \
  "v_cvt_pk_bf16_f32 v164, v164, v165\n\t" \
  "v_cvt_pk_bf16_f32 v165, v166, v167\n\t" \
  "ds_read_b64_tr_b16 v[216:217], v249 offset:6216\n\t" \
  "s_waitcnt lgkmcnt(6)\n\t" \
  "v_mfma_f32_16x16x32_bf16 v[0:3], v[208:211], v[146:149], v[0:3]\n\t" \
  "v_cvt_pknorm_i16_f32 v228, |v194|, |v195|\n\t" \
  "v_cvt_pknorm_i16_f32 v230, |v192|, |v193|\n\t" \
  "ds_read_b64_tr_b16 v[218:219], v249 offset:7752\n\t" \
  "v_mfma_f32_16x16x32_bf16 v[24:27], v[208:211], v[154:157], v[24:27]\n\t" \
  "v_lshlrev_b32_sdwa v229, 2, v228 dst_sel:DWORD dst_unused:UNUSED_PAD src0_sel:DWORD src1_sel:BYTE_3\n\t" \
  "v_lshlrev_b32_sdwa v228, 2, v228 dst_sel:DWORD dst_unused:UNUSED_PAD src0_sel:DWORD src1_sel:BYTE_1\n\t" \
  "ds_read_u16 v224, v224 offset:10528\n\t" \
  "v_mfma_f32_16x16x32_bf16 v[48:51], v[208:211], v[162:165], v[48:51]\n\t" \
  "v_lshlrev_b32_sdwa v231, 2, v230 dst_sel:DWORD dst_unused:UNUSED_PAD src0_sel:DWORD src1_sel:BYTE_3\n\t" \
  "v_lshlrev_b32_sdwa v230, 2, v230 dst_sel:DWORD dst_unused:UNUSED_PAD src0_sel:DWORD src1_sel:BYTE_1\n\t" \
  "ds_read_u16_d16_hi v227, v227 offset:10528\n\t" \
  "v_mfma_f32_16x16x32_bf16 v[72:75], v[208:211], v[170:173], v[72:75]\n\t" \
  "s_add_u32 m0, s44, s35\n\t" \
  "v_lshlrev_b32_sdwa v233, 1, v203 dst_sel:DWORD dst_unused:UNUSED_PAD src0_sel:DWORD src1_sel:WORD_1\n\t" \
  "v_lshlrev_b32_sdwa v232, 1, v202 dst_sel:DWORD dst_unused:UNUSED_PAD src0_sel:DWORD src1_sel:WORD_1\n\t" \
  "s_waitcnt lgkmcnt(5)\n\t" \
  "s_mov_b32 exec_hi, 0xffff\n\t" \
  "buffer_load_dwordx4 v252, s[16:19], s34 offen lds\n\t" \
  "v_mfma_f32_16x16x32_bf16 v[4:7], v[212:215], v[146:149], v[4:7]\n\t" \
  "s_mov_b32 exec_hi, -1\n\t" \
  "v_lshlrev_b32_sdwa v235, 1, v201 dst_sel:DWORD dst_unused:UNUSED_PAD src0_sel:DWORD src1_sel:WORD_1\n\t" \
  "s_add_u32 s34, s34, s32\n\t" \
  "ds_read_b64_tr_b16 v[208:209], v249 offset:6224\n\t" \
  "ds_read_u16 v226, v226 offset:10528\n\t" \
  "v_mfma_f32_16x16x32_bf16 v[28:31], v[212:215], v[154:157], v[28:31]\n\t" \
  "v_lshlrev_b32_sdwa v234, 1, v200 dst_sel:DWORD dst_unused:UNUSED_PAD src0_sel:DWORD src1_sel:WORD_1\n\t" \
  "ds_read_b64_tr_b16 v[210:211], v249 offset:7760\n\t" \
  "ds_read_b32 v229, v229 offset:10016\n\t" \
  "v_mfma_f32_16x16x32_bf16 v[52:55], v[212:215], v[162:165], v[52:55]\n\t" \
  "v_fma_mix_f32 v221, |v179|, v221, v221 op_sel:[0,1,0] op_sel_hi:[0,1,1] clamp\n\t" \
  "v_fma_mix_f32 v220, |v178|, v220, v220 op_sel:[0,1,0] op_sel_hi:[0,1,1] clamp\n\t" \
  "ds_read_b32 v228, v228 offset:10016\n\t" \
  "ds_read_b32 v231, v231 offset:10016\n\t" \
  "v_mfma_f32_16x16x32_bf16 v[76:79], v[212:215], v[170:173], v[76:79]\n\t" \
  "v_fma_mix_f32 v223, |v177|, v223, v223 op_sel:[0,1,0] op_sel_hi:[0,1,1] clamp\n\t" \
  "v_fma_mix_f32 v222, |v176|, v222, v222 op_sel:[0,1,0] op_sel_hi:[0,1,1] clamp\n\t" \
  "s_waitcnt lgkmcnt(4)\n\t" \
  "ds_read_b64_tr_b16 v[212:213], v249 offset:6280\n\t" \
  "ds_read_b32 v230, v230 offset:10016\n\t" \
  "v_mfma_f32_16x16x32_bf16 v[8:11], v[216:219], v[146:149], v[8:11]\n\t" \
  "v_or_b32 v187, v224, v225\n\t" \
  "v_or_b32 v186, v226, v227\n\t" \
  "ds_read_b64_tr_b16 v[214:215], v249 offset:7816\n\t" \
  "v_mfma_f32_16x16x32_bf16 v[32:35], v[216:219], v[154:157], v[32:35]\n\t" \
  "v_fma_f32 v179, |v179|, v221, v179 mul:4\n\t" \
  "v_fma_f32 v178, |v178|, v220, v178 mul:4\n\t" \
  "ds_read_u16_d16_hi v233, v233 offset:10528\n\t" \
  "v_mfma_f32_16x16x32_bf16 v[56:59], v[216:219], v[162:165], v[56:59]\n\t" \
  "v_fma_f32 v177, |v177|, v223, v177 mul:4\n\t" \
  "v_fma_f32 v176, |v176|, v222, v176 mul:4\n\t" \
  "s_add_u32 m0, s25, 16400\n\t" \
  "s_add_u32 s24, s24, s26\n\t" \
  "ds_read_u16 v232, v232 offset:10528\n\t" \
  "v_mfma_f32_16x16x32_bf16 v[80:83], v[216:219], v[170:173], v[80:83]\n\t" \
  "v_cvt_pk_bf16_f32 v179, v178, v179\n\t" \
  "v_cvt_pk_bf16_f32 v178, v176, v177\n\t" \
  "s_waitcnt lgkmcnt(8)\n\t" \
  "buffer_load_dwordx4 v254, s[8:11], s24 offen nt lds\n\t" \
  "ds_read_b64_tr_b16 v[216:217], v249 offset:6288\n\t" \
  "v_mfma_f32_16x16x32_bf16 v[12:15], v[208:211], v[146:149], v[12:15]\n\t" \
  "v_cvt_pknorm_i16_f32 v220, |v180|, |v181|\n\t" \
  "v_cvt_pknorm_i16_f32 v222, |v182|, |v183|\n\t" \
  "ds_read_b64_tr_b16 v[218:219], v249 offset:7824\n\t" \
  "v_mfma_f32_16x16x32_bf16 v[36:39], v[208:211], v[154:157], v[36:39]\n\t" \
  "v_lshlrev_b32_sdwa v221, 2, v220 dst_sel:DWORD dst_unused:UNUSED_PAD src0_sel:DWORD src1_sel:BYTE_3\n\t" \
  "v_lshlrev_b32_sdwa v220, 2, v220 dst_sel:DWORD dst_unused:UNUSED_PAD src0_sel:DWORD src1_sel:BYTE_1\n\t" \
  "ds_read_u16_d16_hi v235, v235 offset:10528\n\t" \
  "v_mfma_f32_16x16x32_bf16 v[60:63], v[208:211], v[162:165], v[60:63]\n\t" \
  "v_lshlrev_b32_sdwa v223, 2, v222 dst_sel:DWORD dst_unused:UNUSED_PAD src0_sel:DWORD src1_sel:BYTE_3\n\t" \
  "v_lshlrev_b32_sdwa v222, 2, v222 dst_sel:DWORD dst_unused:UNUSED_PAD src0_sel:DWORD src1_sel:BYTE_1\n\t" \
  "ds_read_u16 v234, v234 offset:10528\n\t" \
  "v_mfma_f32_16x16x32_bf16 v[84:87], v[208:211], v[170:173], v[84:87]\n\t" \
  "v_lshlrev_b32_sdwa v225, 1, v189 dst_sel:DWORD dst_unused:UNUSED_PAD src0_sel:DWORD src1_sel:WORD_1\n\t" \
  "v_lshlrev_b32_sdwa v224, 1, v188 dst_sel:DWORD dst_unused:UNUSED_PAD src0_sel:DWORD src1_sel:WORD_1\n\t" \
  "ds_read_b64_tr_b16 v[208:209], v248 offset:43048\n\t" \
  "s_waitcnt lgkmcnt(7)\n\t" \
  "v_mfma_f32_16x16x32_bf16 v[16:19], v[212:215], v[146:149], v[16:19]\n\t" \
  "v_lshlrev_b32_sdwa v227, 1, v191 dst_sel:DWORD dst_unused:UNUSED_PAD src0_sel:DWORD src1_sel:WORD_1\n\t" \
  "ds_read_b64_tr_b16 v[210:211], v248 offset:44072\n\t" \
  "ds_read_b32 v221, v221 offset:10016\n\t" \
  "v_mfma_f32_16x16x32_bf16 v[40:43], v[212:215], v[154:157], v[40:43]\n\t" \
  "s_add_u32 s27, s27, s26\n\t" \
  "v_lshlrev_b32_sdwa v226, 1, v190 dst_sel:DWORD dst_unused:UNUSED_PAD src0_sel:DWORD src1_sel:WORD_1\n\t" \
  "v_fma_mix_f32 v229, |v195|, v229, v229 op_sel:[0,1,0] op_sel_hi:[0,1,1] clamp\n\t" \
  "s_mov_b64 exec, s[30:31]\n\t" \
  "buffer_load_dword v237, v254, s[8:11], s27 offen\n\t" \
  "v_mfma_f32_16x16x32_bf16 v[64:67], v[212:215], v[162:165], v[64:67]\n\t" \
  "s_mov_b64 exec, -1\n\t" \
  "v_fma_mix_f32 v228, |v194|, v228, v228 op_sel:[0,1,0] op_sel_hi:[0,1,1] clamp\n\t" \
  "ds_read_b32 v220, v220 offset:10016\n\t" \
  "ds_read_b32 v223, v223 offset:10016\n\t" \
  "v_mfma_f32_16x16x32_bf16 v[88:91], v[212:215], v[170:173], v[88:91]\n\t" \
  "v_fma_mix_f32 v231, |v193|, v231, v231 op_sel:[0,1,0] op_sel_hi:[0,1,1] clamp\n\t" \
  "v_fma_mix_f32 v230, |v192|, v230, v230 op_sel:[0,1,0] op_sel_hi:[0,1,1] clamp\n\t" \
  "s_waitcnt lgkmcnt(5)\n\t" \
  "ds_read_b64_tr_b16 v[212:213], v248 offset:46120\n\t" \
  "ds_read_b32 v222, v222 offset:10016\n\t" \
  "v_mfma_f32_16x16x32_bf16 v[20:23], v[216:219], v[146:149], v[20:23]\n\t" \
  "v_or_b32 v203, v232, v233\n\t" \
  "v_or_b32 v202, v234, v235\n\t" \
  "ds_read_b64_tr_b16 v[214:215], v248 offset:47144\n\t" \
  "v_mfma_f32_16x16x32_bf16 v[44:47], v[216:219], v[154:157], v[44:47]\n\t" \
  "v_fma_f32 v195, |v195|, v229, v195 mul:4\n\t" \
  "v_fma_f32 v194, |v194|, v228, v194 mul:4\n\t" \
  "ds_read_u16_d16_hi v225, v225 offset:10528\n\t" \
  "v_mfma_f32_16x16x32_bf16 v[68:71], v[216:219], v[162:165], v[68:71]\n\t" \
  "v_fma_f32 v193, |v193|, v231, v193 mul:4\n\t" \
  "v_fma_f32 v192, |v192|, v230, v192 mul:4\n\t" \
  "ds_read_u16 v224, v224 offset:10528\n\t" \
  "v_mfma_f32_16x16x32_bf16 v[92:95], v[216:219], v[170:173], v[92:95]\n\t" \
  "v_cvt_pk_bf16_f32 v195, v194, v195\n\t" \
  "v_cvt_pk_bf16_f32 v194, v192, v193\n\t" \
  "ds_read_b64_tr_b16 v[216:217], v248 offset:45096\n\t" \
  "s_waitcnt lgkmcnt(9)\n\t" \
  "v_mfma_f32_16x16x32_bf16 v[144:147], v[208:211], v[96:99], 0\n\t" \
  "v_cvt_pknorm_i16_f32 v228, |v196|, |v197|\n\t" \
  "v_cvt_pknorm_i16_f32 v230, |v198|, |v199|\n\t" \
  "ds_read_b64_tr_b16 v[218:219], v248 offset:48168\n\t" \
  "v_mfma_f32_16x16x32_bf16 v[152:155], v[208:211], v[100:103], 0\n\t" \
  "v_lshlrev_b32_sdwa v229, 2, v228 dst_sel:DWORD dst_unused:UNUSED_PAD src0_sel:DWORD src1_sel:BYTE_3\n\t" \
  "v_lshlrev_b32_sdwa v228, 2, v228 dst_sel:DWORD dst_unused:UNUSED_PAD src0_sel:DWORD src1_sel:BYTE_1\n\t" \
  "ds_read_u16_d16_hi v227, v227 offset:10528\n\t" \
  "v_mfma_f32_16x16x32_bf16 v[160:163], v[208:211], v[104:107], 0\n\t" \
  "v_lshlrev_b32_sdwa v231, 2, v230 dst_sel:DWORD dst_unused:UNUSED_PAD src0_sel:DWORD src1_sel:BYTE_3\n\t" \
  "v_lshlrev_b32_sdwa v230, 2, v230 dst_sel:DWORD dst_unused:UNUSED_PAD src0_sel:DWORD src1_sel:BYTE_1\n\t" \
  "ds_read_u16 v226, v226 offset:10528\n\t" \
  "v_mfma_f32_16x16x32_bf16 v[168:171], v[208:211], v[108:111], 0\n\t" \
  "v_lshlrev_b32_sdwa v233, 1, v205 dst_sel:DWORD dst_unused:UNUSED_PAD src0_sel:DWORD src1_sel:WORD_1\n\t" \
  "v_lshlrev_b32_sdwa v232, 1, v204 dst_sel:DWORD dst_unused:UNUSED_PAD src0_sel:DWORD src1_sel:WORD_1\n\t" \
  "ds_read_b64_tr_b16 v[208:209], v248 offset:43560\n\t" \
  "s_waitcnt lgkmcnt(7)\n\t" \
  "v_mfma_f32_16x16x32_bf16 v[144:147], v[212:215], v[112:115], v[144:147]\n\t" \
  "v_lshlrev_b32_sdwa v235, 1, v207 dst_sel:DWORD dst_unused:UNUSED_PAD src0_sel:DWORD src1_sel:WORD_1\n\t" \
  "ds_read_b64_tr_b16 v[210:211], v248 offset:44584\n\t" \
  "ds_read_b32 v229, v229 offset:10016\n\t" \
  "v_mfma_f32_16x16x32_bf16 v[152:155], v[212:215], v[116:119], v[152:155]\n\t" \
  "v_lshlrev_b32_sdwa v234, 1, v206 dst_sel:DWORD dst_unused:UNUSED_PAD src0_sel:DWORD src1_sel:WORD_1\n\t" \
  "v_fma_mix_f32 v221, |v181|, v221, v221 op_sel:[0,1,0] op_sel_hi:[0,1,1] clamp\n\t" \
  "ds_read_b32 v228, v228 offset:10016\n\t" \
  "v_mfma_f32_16x16x32_bf16 v[160:163], v[212:215], v[120:123], v[160:163]\n\t" \
  "v_fma_mix_f32 v220, |v180|, v220, v220 op_sel:[0,1,0] op_sel_hi:[0,1,1] clamp\n\t" \
  "ds_read_b32 v231, v231 offset:10016\n\t" \
  "ds_read_b32 v230, v230 offset:10016\n\t" \
  "v_mfma_f32_16x16x32_bf16 v[168:171], v[212:215], v[124:127], v[168:171]\n\t" \
  "v_fma_mix_f32 v223, |v183|, v223, v223 op_sel:[0,1,0] op_sel_hi:[0,1,1] clamp\n\t" \
  "v_fma_mix_f32 v222, |v182|, v222, v222 op_sel:[0,1,0] op_sel_hi:[0,1,1] clamp\n\t" \
  "s_waitcnt lgkmcnt(6)\n\t" \
  "ds_read_b64_tr_b16 v[212:213], v248 offset:46632\n\t" \
  "ds_read_u16_d16_hi v233, v233 offset:10528\n\t" \
  "v_mfma_f32_16x16x32_bf16 v[144:147], v[216:219], v[128:131], v[144:147]\n\t" \
  "v_or_b32 v188, v224, v225\n\t" \
  "v_or_b32 v189, v226, v227\n\t" \
  "ds_read_b64_tr_b16 v[214:215], v248 offset:47656\n\t" \
  "v_mfma_f32_16x16x32_bf16 v[152:155], v[216:219], v[132:135], v[152:155]\n\t" \
  "v_fma_f32 v181, |v181|, v221, v181 mul:4\n\t" \
  "v_fma_f32 v180, |v180|, v220, v180 mul:4\n\t" \
  "ds_read_u16 v232, v232 offset:10528\n\t" \
  "v_mfma_f32_16x16x32_bf16 v[160:163], v[216:219], v[136:139], v[160:163]\n\t" \
  "v_fma_f32 v183, |v183|, v223, v183 mul:4\n\t" \
  "v_fma_f32 v182, |v182|, v222, v182 mul:4\n\t" \
  "ds_read_u16_d16_hi v235, v235 offset:10528\n\t" \
  "v_mfma_f32_16x16x32_bf16 v[168:171], v[216:219], v[140:143], v[168:171]\n\t" \
  "v_cvt_pk_bf16_f32 v180, v180, v181\n\t" \
  "v_cvt_pk_bf16_f32 v181, v182, v183\n\t" \
  "ds_read_b64_tr_b16 v[216:217], v248 offset:45608\n\t" \
  "s_waitcnt lgkmcnt(10)\n\t" \
  "v_mfma_f32_16x16x32_bf16 v[148:151], v[208:211], v[96:99], 0\n\t" \
  "v_cvt_pknorm_i16_f32 v220, |v146|, |v147|\n\t" \
  "v_cvt_pknorm_i16_f32 v222, |v144|, |v145|\n\t" \
  "s_waitcnt vmcnt(2)\n\t" \
  "ds_read_b64_tr_b16 v[218:219], v248 offset:48680\n\t" \
  "v_mfma_f32_16x16x32_bf16 v[156:159], v[208:211], v[100:103], 0\n\t" \
  "v_lshlrev_b32_sdwa v221, 2, v220 dst_sel:DWORD dst_unused:UNUSED_PAD src0_sel:DWORD src1_sel:BYTE_3\n\t" \
  "v_lshlrev_b32_sdwa v220, 2, v220 dst_sel:DWORD dst_unused:UNUSED_PAD src0_sel:DWORD src1_sel:BYTE_1\n\t" \
  "s_mov_b64 exec, 3\n\t" \
  "ds_add_rtn_u32 v246, v251, v250 offset:47168\n\t" \
  "v_mfma_f32_16x16x32_bf16 v[164:167], v[208:211], v[104:107], 0\n\t" \
  "s_mov_b64 exec, -1\n\t" \
  "v_lshlrev_b32_sdwa v223, 2, v222 dst_sel:DWORD dst_unused:UNUSED_PAD src0_sel:DWORD src1_sel:BYTE_3\n\t" \
  "v_lshlrev_b32_sdwa v222, 2, v222 dst_sel:DWORD dst_unused:UNUSED_PAD src0_sel:DWORD src1_sel:BYTE_1\n\t" \
  "ds_read_u16 v234, v234 offset:10528\n\t" \
  "v_mfma_f32_16x16x32_bf16 v[172:175], v[208:211], v[108:111], 0\n\t" \
  "s_waitcnt lgkmcnt(1)\n\t" \
  "v_lshlrev_b32_sdwa v225, 1, v155 dst_sel:DWORD dst_unused:UNUSED_PAD src0_sel:DWORD src1_sel:WORD_1\n\t" \
  "v_readfirstlane_b32 s4, v246\n\t" \
  "ds_read_b32 v221, v221 offset:10016\n\t" \
  "v_mfma_f32_16x16x32_bf16 v[148:151], v[212:215], v[112:115], v[148:151]\n\t" \
  "s_cmpk_lt_u32 s4, 3072\n\t" \
  "v_lshlrev_b32_sdwa v224, 1, v154 dst_sel:DWORD dst_unused:UNUSED_PAD src0_sel:DWORD src1_sel:WORD_1\n\t" \
  "s_cbranch_scc1 .Lcnx_w0_5\n.Lcnx_g0_5:\n\t" \
  "ds_read_b32 v220, v220 offset:10016\n\t" \
  "v_mfma_f32_16x16x32_bf16 v[156:159], v[212:215], v[116:119], v[156:159]\n\t" \
  "v_lshlrev_b32_sdwa v227, 1, v153 dst_sel:DWORD dst_unused:UNUSED_PAD src0_sel:DWORD src1_sel:WORD_1\n\t" \
  "v_lshlrev_b32_sdwa v226, 1, v152 dst_sel:DWORD dst_unused:UNUSED_PAD src0_sel:DWORD src1_sel:WORD_1\n\t" \
  "ds_read_b64_tr_b16 v[208:209], v249 offset:12304\n\t" \
  "v_mfma_f32_16x16x32_bf16 v[164:167], v[212:215], v[120:123], v[164:167]\n\t" \
  "v_fma_mix_f32 v229, |v197|, v229, v229 op_sel:[0,1,0] op_sel_hi:[0,1,1] clamp\n\t" \
  "v_fma_mix_f32 v228, |v196|, v228, v228 op_sel:[0,1,0] op_sel_hi:[0,1,1] clamp\n\t" \
  "ds_read_b64_tr_b16 v[210:211], v249 offset:13840\n\t" \
  "ds_read_b32 v223, v223 offset:10016\n\t" \
  "v_mfma_f32_16x16x32_bf16 v[172:175], v[212:215], v[124:127], v[172:175]\n\t" \
  "v_fma_mix_f32 v231, |v199|, v231, v231 op_sel:[0,1,0] op_sel_hi:[0,1,1] clamp\n\t" \
  "v_fma_mix_f32 v230, |v198|, v230, v230 op_sel:[0,1,0] op_sel_hi:[0,1,1] clamp\n\t" \
  "s_waitcnt lgkmcnt(5)\n\t" \
  "ds_read_b64_tr_b16 v[212:213], v249 offset:12312\n\t" \
  "ds_read_b32 v222, v222 offset:10016\n\t" \
  "v_mfma_f32_16x16x32_bf16 v[148:151], v[216:219], v[128:131], v[148:151]\n\t" \
  "s_mov_b32 m0, s45\n\t" \
  "v_or_b32 v204, v232, v233\n\t" \
  "v_or_b32 v205, v234, v235\n\t" \
  "s_mov_b32 exec_hi, 0xffff\n\t" \
  "buffer_load_dwordx4 v252, s[16:19], s33 offen lds\n\t" \
  "v_mfma_f32_16x16x32_bf16 v[156:159], v[216:219], v[132:135], v[156:159]\n\t" \
  "s_mov_b32 exec_hi, -1\n\t" \
  "v_fma_f32 v197, |v197|, v229, v197 mul:4\n\t" \
  "v_fma_f32 v196, |v196|, v228, v196 mul:4\n\t" \
  "s_add_u32 s33, s33, s32\n\t" \
  "ds_read_b64_tr_b16 v[214:215], v249 offset:13848\n\t" \
  "v_mfma_f32_16x16x32_bf16 v[164:167], v[216:219], v[136:139], v[164:167]\n\t" \
  "v_fma_f32 v199, |v199|, v231, v199 mul:4\n\t" \
  "v_fma_f32 v198, |v198|, v230, v198 mul:4\n\t" \
  "ds_read_u16_d16_hi v225, v225 offset:10528\n\t" \
  "v_mfma_f32_16x16x32_bf16 v[172:175], v[216:219], v[140:143], v[172:175]\n\t" \
  "v_cvt_pk_bf16_f32 v196, v196, v197\n\t" \
  "v_cvt_pk_bf16_f32 v197, v198, v199\n\t" \
  "ds_read_b64_tr_b16 v[216:217], v249 offset:12368\n\t" \
  "s_waitcnt lgkmcnt(6)\n\t" \
  "v_mfma_f32_16x16x32_bf16 v[0:3], v[208:211], v[178:181], v[0:3]\n\t" \
  "v_cvt_pknorm_i16_f32 v228, |v162|, |v163|\n\t" \
  "v_cvt_pknorm_i16_f32 v230, |v160|, |v161|\n\t" \
  "ds_read_b64_tr_b16 v[218:219], v249 offset:13904\n\t" \
  "v_mfma_f32_16x16x32_bf16 v[24:27], v[208:211], v[186:189], v[24:27]\n\t" \
  "v_lshlrev_b32_sdwa v229, 2, v228 dst_sel:DWORD dst_unused:UNUSED_PAD src0_sel:DWORD src1_sel:BYTE_3\n\t" \
  "v_lshlrev_b32_sdwa v228, 2, v228 dst_sel:DWORD dst_unused:UNUSED_PAD src0_sel:DWORD src1_sel:BYTE_1\n\t" \
  "ds_read_u16 v224, v224 offset:10528\n\t" \
  "v_mfma_f32_16x16x32_bf16 v[48:51], v[208:211], v[194:197], v[48:51]\n\t" \
  "v_lshlrev_b32_sdwa v231, 2, v230 dst_sel:DWORD dst_unused:UNUSED_PAD src0_sel:DWORD src1_sel:BYTE_3\n\t" \
  "v_lshlrev_b32_sdwa v230, 2, v230 dst_sel:DWORD dst_unused:UNUSED_PAD src0_sel:DWORD src1_sel:BYTE_1\n\t" \
  "ds_read_u16_d16_hi v227, v227 offset:10528\n\t" \
  "v_mfma_f32_16x16x32_bf16 v[72:75], v[208:211], v[202:205], v[72:75]\n\t" \
  "s_add_u32 m0, s45, s35\n\t" \
  "v_lshlrev_b32_sdwa v233, 1, v171 dst_sel:DWORD dst_unused:UNUSED_PAD src0_sel:DWORD src1_sel:WORD_1\n\t" \
  "v_lshlrev_b32_sdwa v232, 1, v170 dst_sel:DWORD dst_unused:UNUSED_PAD src0_sel:DWORD src1_sel:WORD_1\n\t" \
  "s_waitcnt lgkmcnt(5)\n\t" \
  "s_mov_b32 exec_hi, 0xffff\n\t" \
  "buffer_load_dwordx4 v252, s[16:19], s34 offen lds\n\t" \
  "v_mfma_f32_16x16x32_bf16 v[4:7], v[212:215], v[178:181], v[4:7]\n\t" \
  "s_mov_b32 exec_hi, -1\n\t" \
  "v_lshlrev_b32_sdwa v235, 1, v169 dst_sel:DWORD dst_unused:UNUSED_PAD src0_sel:DWORD src1_sel:WORD_1\n\t" \
  "s_add_u32 s34, s34, s32\n\t" \
  "ds_read_b64_tr_b16 v[208:209], v249 offset:12376\n\t" \
  "ds_read_u16 v226, v226 offset:10528\n\t" \
  "v_mfma_f32_16x16x32_bf16 v[28:31], v[212:215], v[186:189], v[28:31]\n\t" \
  "v_lshlrev_b32_sdwa v234, 1, v168 dst_sel:DWORD dst_unused:UNUSED_PAD src0_sel:DWORD src1_sel:WORD_1\n\t" \
  "ds_read_b64_tr_b16 v[210:211], v249 offset:13912\n\t" \
  "ds_read_b32 v229, v229 offset:10016\n\t" \
  "v_mfma_f32_16x16x32_bf16 v[52:55], v[212:215], v[194:197], v[52:55]\n\t" \
  "v_fma_mix_f32 v221, |v147|, v221, v221 op_sel:[0,1,0] op_sel_hi:[0,1,1] clamp\n\t" \
  "v_fma_mix_f32 v220, |v146|, v220, v220 op_sel:[0,1,0] op_sel_hi:[0,1,1] clamp\n\t" \
  "ds_read_b32 v228, v228 offset:10016\n\t" \
  "ds_read_b32 v231, v231 offset:10016\n\t" \
  "v_mfma_f32_16x16x32_bf16 v[76:79], v[212:215], v[202:205], v[76:79]\n\t" \
  "v_fma_mix_f32 v223, |v145|, v223, v223 op_sel:[0,1,0] op_sel_hi:[0,1,1] clamp\n\t" \
  "v_fma_mix_f32 v222, |v144|, v222, v222 op_sel:[0,1,0] op_sel_hi:[0,1,1] clamp\n\t" \
  "s_waitcnt lgkmcnt(4)\n\t" \
  "ds_read_b64_tr_b16 v[212:213], v249 offset:12432\n\t" \
  "ds_read_b32 v230, v230 offset:10016\n\t" \
  "v_mfma_f32_16x16x32_bf16 v[8:11], v[216:219], v[178:181], v[8:11]\n\t" \
  "v_or_b32 v155, v224, v225\n\t" \
  "v_or_b32 v154, v226, v227\n\t" \
  "ds_read_b64_tr_b16 v[214:215], v249 offset:13968\n\t" \
  "v_mfma_f32_16x16x32_bf16 v[32:35], v[216:219], v[186:189], v[32:35]\n\t" \
  "v_fma_f32 v147, |v147|, v221, v147 mul:4\n\t" \
  "v_fma_f32 v146, |v146|, v220, v146 mul:4\n\t" \
  "ds_read_u16_d16_hi v233, v233 offset:10528\n\t" \
  "v_mfma_f32_16x16x32_bf16 v[56:59], v[216:219], v[194:197], v[56:59]\n\t" \
  "v_fma_f32 v145, |v145|, v223, v145 mul:4\n\t" \
  "v_fma_f32 v144, |v144|, v222, v144 mul:4\n\t" \
  "s_add_u32 m0, s25, 24600\n\t" \
  "s_add_u32 s24, s24, s26\n\t" \
  "ds_read_u16 v232, v232 offset:10528\n\t" \
  "v_mfma_f32_16x16x32_bf16 v[80:83], v[216:219], v[202:205], v[80:83]\n\t" \
  "v_cvt_pk_bf16_f32 v147, v146, v147\n\t" \
  "v_cvt_pk_bf16_f32 v146, v144, v145\n\t" \
  "s_waitcnt lgkmcnt(8)\n\t" \
  "buffer_load_dwordx4 v254, s[8:11], s24 offen lds\n\t" \
  "ds_read_b64_tr_b16 v[216:217], v249 offset:12440\n\t" \
  "v_mfma_f32_16x16x32_bf16 v[12:15], v[208:211], v[178:181], v[12:15]\n\t" \
  "v_cvt_pknorm_i16_f32 v220, |v148|, |v149|\n\t" \
  "v_cvt_pknorm_i16_f32 v222, |v150|, |v151|\n\t" \
  "ds_read_b64_tr_b16 v[218:219], v249 offset:13976\n\t" \
  "v_mfma_f32_16x16x32_bf16 v[36:39], v[208:211], v[186:189], v[36:39]\n\t" \
  "v_lshlrev_b32_sdwa v221, 2, v220 dst_sel:DWORD dst_unused:UNUSED_PAD src0_sel:DWORD src1_sel:BYTE_3\n\t" \
  "v_lshlrev_b32_sdwa v220, 2, v220 dst_sel:DWORD dst_unused:UNUSED_PAD src0_sel:DWORD src1_sel:BYTE_1\n\t" \
  "ds_read_u16_d16_hi v235, v235 offset:10528\n\t" \
  "v_mfma_f32_16x16x32_bf16 v[60:63], v[208:211], v[194:197], v[60:63]\n\t" \
  "v_lshlrev_b32_sdwa v223, 2, v222 dst_sel:DWORD dst_unused:UNUSED_PAD src0_sel:DWORD src1_sel:BYTE_3\n\t" \
  "v_lshlrev_b32_sdwa v222, 2, v222 dst_sel:DWORD dst_unused:UNUSED_PAD src0_sel:DWORD src1_sel:BYTE_1\n\t" \
  "ds_read_u16 v234, v234 offset:10528\n\t" \
  "v_mfma_f32_16x16x32_bf16 v[84:87], v[208:211], v[202:205], v[84:87]\n\t" \
  "v_lshlrev_b32_sdwa v225, 1, v157 dst_sel:DWORD dst_unused:UNUSED_PAD src0_sel:DWORD src1_sel:WORD_1\n\t" \
  "v_lshlrev_b32_sdwa v224, 1, v156 dst_sel:DWORD dst_unused:UNUSED_PAD src0_sel:DWORD src1_sel:WORD_1\n\t" \
  "ds_read_b64_tr_b16 v[208:209], v248 offset:49192\n\t" \
  "s_waitcnt lgkmcnt(7)\n\t" \
  "v_mfma_f32_16x16x32_bf16 v[16:19], v[212:215], v[178:181], v[16:19]\n\t" \
  "v_lshlrev_b32_sdwa v227, 1, v159 dst_sel:DWORD dst_unused:UNUSED_PAD src0_sel:DWORD src1_sel:WORD_1\n\t" \
  "ds_read_b64_tr_b16 v[210:211], v248 offset:50216\n\t" \
  "ds_read_b32 v221, v221 offset:10016\n\t" \
  "v_mfma_f32_16x16x32_bf16 v[40:43], v[212:215], v[186:189], v[40:43]\n\t" \
  "s_add_u32 s27, s27, s26\n\t" \
  "v_lshlrev_b32_sdwa v226, 1, v158 dst_sel:DWORD dst_unused:UNUSED_PAD src0_sel:DWORD src1_sel:WORD_1\n\t" \
  "v_fma_mix_f32 v229, |v163|, v229, v229 op_sel:[0,1,0] op_sel_hi:[0,1,1] clamp\n\t" \
  "s_mov_b64 exec, s[30:31]\n\t" \
  "buffer_load_dword v237, v254, s[8:11], s27 offen\n\t" \
  "v_mfma_f32_16x16x32_bf16 v[64:67], v[212:215], v[194:197], v[64:67]\n\t" \
  "s_mov_b64 exec, -1\n\t" \
  "v_fma_mix_f32 v228, |v162|, v228, v228 op_sel:[0,1,0] op_sel_hi:[0,1,1] clamp\n\t" \
  "ds_read_b32 v220, v220 offset:10016\n\t" \
  "ds_read_b32 v223, v223 offset:10016\n\t" \
  "v_mfma_f32_16x16x32_bf16 v[88:91], v[212:215], v[202:205], v[88:91]\n\t" \
  "v_fma_mix_f32 v231, |v161|, v231, v231 op_sel:[0,1,0] op_sel_hi:[0,1,1] clamp\n\t" \
  "v_fma_mix_f32 v230, |v160|, v230, v230 op_sel:[0,1,0] op_sel_hi:[0,1,1] clamp\n\t" \
  "s_waitcnt lgkmcnt(5)\n\t" \
  "ds_read_b64_tr_b16 v[212:213], v248 offset:52264\n\t" \
  "ds_read_b32 v222, v222 offset:10016\n\t" \
  "v_mfma_f32_16x16x32_bf16 v[20:23], v[216:219], v[178:181], v[20:23]\n\t" \
  "v_or_b32 v171, v232, v233\n\t" \
  "v_or_b32 v170, v234, v235\n\t" \
  "ds_read_b64_tr_b16 v[214:215], v248 offset:53288\n\t" \
  "v_mfma_f32_16x16x32_bf16 v[44:47], v[216:219], v[186:189], v[44:47]\n\t" \
  "v_fma_f32 v163, |v163|, v229, v163 mul:4\n\t" \
  "v_fma_f32 v162, |v162|, v228, v162 mul:4\n\t" \
  "ds_read_u16_d16_hi v225, v225 offset:10528\n\t" \
  "v_mfma_f32_16x16x32_bf16 v[68:71], v[216:219], v[194:197], v[68:71]\n\t" \
  "v_fma_f32 v161, |v161|, v231, v161 mul:4\n\t" \
  "v_fma_f32 v160, |v160|, v230, v160 mul:4\n\t" \
  "ds_read_u16 v224, v224 offset:10528\n\t" \
  "v_mfma_f32_16x16x32_bf16 v[92:95], v[216:219], v[202:205], v[92:95]\n\t" \
  "v_cvt_pk_bf16_f32 v163, v162, v163\n\t" \
  "v_cvt_pk_bf16_f32 v162, v160, v161\n\t" \
  "ds_read_b64_tr_b16 v[216:217], v248 offset:51240\n\t" \
  "s_waitcnt lgkmcnt(9)\n\t" \
  "v_mfma_f32_16x16x32_bf16 v[176:179], v[208:211], v[96:99], 0\n\t" \
  "v_cvt_pknorm_i16_f32 v228, |v164|, |v165|\n\t" \
  "v_cvt_pknorm_i16_f32 v230, |v166|, |v167|\n\t" \
  "ds_read_b64_tr_b16 v[218:219], v248 offset:54312\n\t" \
  "v_mfma_f32_16x16x32_bf16 v[184:187], v[208:211], v[100:103], 0\n\t" \
  "v_lshlrev_b32_sdwa v229, 2, v228 dst_sel:DWORD dst_unused:UNUSED_PAD src0_sel:DWORD src1_sel:BYTE_3\n\t" \
  "v_lshlrev_b32_sdwa v228, 2, v228 dst_sel:DWORD dst_unused:UNUSED_PAD src0_sel:DWORD src1_sel:BYTE_1\n\t" \
  "ds_read_u16_d16_hi v227, v227 offset:10528\n\t" \
  "v_mfma_f32_16x16x32_bf16 v[192:195], v[208:211], v[104:107], 0\n\t" \
  "v_lshlrev_b32_sdwa v231, 2, v230 dst_sel:DWORD dst_unused:UNUSED_PAD src0_sel:DWORD src1_sel:BYTE_3\n\t" \
  "v_lshlrev_b32_sdwa v230, 2, v230 dst_sel:DWORD dst_unused:UNUSED_PAD src0_sel:DWORD src1_sel:BYTE_1\n\t" \
  "ds_read_u16 v226, v226 offset:10528\n\t" \
  "v_mfma_f32_16x16x32_bf16 v[200:203], v[208:211], v[108:111], 0\n\t" \
  "v_lshlrev_b32_sdwa v233, 1, v173 dst_sel:DWORD dst_unused:UNUSED_PAD src0_sel:DWORD src1_sel:WORD_1\n\t" \
  "v_lshlrev_b32_sdwa v232, 1, v172 dst_sel:DWORD dst_unused:UNUSED_PAD src0_sel:DWORD src1_sel:WORD_1\n\t" \
  "ds_read_b64_tr_b16 v[208:209], v248 offset:49704\n\t" \
  "s_waitcnt lgkmcnt(7)\n\t" \
  "v_mfma_f32_16x16x32_bf16 v[176:179], v[212:215], v[112:115], v[176:179]\n\t" \
  "v_lshlrev_b32_sdwa v235, 1, v175 dst_sel:DWORD dst_unused:UNUSED_PAD src0_sel:DWORD src1_sel:WORD_1\n\t" \
  "ds_read_b64_tr_b16 v[210:211], v248 offset:50728\n\t" \
  "ds_read_b32 v229, v229 offset:10016\n\t" \
  "v_mfma_f32_16x16x32_bf16 v[184:187], v[212:215], v[116:119], v[184:187]\n\t" \
  "v_lshlrev_b32_sdwa v234, 1, v174 dst_sel:DWORD dst_unused:UNUSED_PAD src0_sel:DWORD src1_sel:WORD_1\n\t" \
  "v_fma_mix_f32 v221, |v149|, v221, v221 op_sel:[0,1,0] op_sel_hi:[0,1,1] clamp\n\t" \
  "ds_read_b32 v228, v228 offset:10016\n\t" \
  "v_mfma_f32_16x16x32_bf16 v[192:195], v[212:215], v[120:123], v[192:195]\n\t" \
  "v_fma_mix_f32 v220, |v148|, v220, v220 op_sel:[0,1,0] op_sel_hi:[0,1,1] clamp\n\t" \
  "ds_read_b32 v231, v231 offset:10016\n\t" \
  "ds_read_b32 v230, v230 offset:10016\n\t" \
  "v_mfma_f32_16x16x32_bf16 v[200:203], v[212:215], v[124:127], v[200:203]\n\t" \
  "v_fma_mix_f32 v223, |v151|, v223, v223 op_sel:[0,1,0] op_sel_hi:[0,1,1] clamp\n\t" \
  "v_fma_mix_f32 v222, |v150|, v222, v222 op_sel:[0,1,0] op_sel_hi:[0,1,1] clamp\n\t" \
  "s_waitcnt lgkmcnt(6)\n\t" \
  "ds_read_b64_tr_b16 v[212:213], v248 offset:52776\n\t" \
  "ds_read_u16_d16_hi v233, v233 offset:10528\n\t" \
  "v_mfma_f32_16x16x32_bf16 v[176:179], v[216:219], v[128:131], v[176:179]\n\t" \
  "v_or_b32 v156, v224, v225\n\t" \
  "v_or_b32 v157, v226, v227\n\t" \
  "ds_read_b64_tr_b16 v[214:215], v248 offset:53800\n\t" \
  "v_mfma_f32_16x16x32_bf16 v[184:187], v[216:219], v[132:135], v[184:187]\n\t" \
  "v_fma_f32 v149, |v149|, v221, v149 mul:4\n\t" \
  "v_fma_f32 v148, |v148|, v220, v148 mul:4\n\t" \
  "ds_read_u16 v232, v232 offset:10528\n\t" \
  "v_mfma_f32_16x16x32_bf16 v[192:195], v[216:219], v[136:139], v[192:195]\n\t" \
  "v_fma_f32 v151, |v151|, v223, v151 mul:4\n\t" \
  "v_fma_f32 v150, |v150|, v222, v150 mul:4\n\t" \
  "ds_read_u16_d16_hi v235, v235 offset:10528\n\t" \
  "v_mfma_f32_16x16x32_bf16 v[200:203], v[216:219], v[140:143], v[200:203]\n\t" \
  "v_cvt_pk_bf16_f32 v148, v148, v149\n\t" \
  "v_cvt_pk_bf16_f32 v149, v150, v151\n\t" \
  "ds_read_b64_tr_b16 v[216:217], v248 offset:51752\n\t" \
  "s_waitcnt lgkmcnt(10)\n\t" \
  "v_mfma_f32_16x16x32_bf16 v[180:183], v[208:211], v[96:99], 0\n\t" \
  "v_cvt_pknorm_i16_f32 v220, |v178|, |v179|\n\t" \
  "v_cvt_pknorm_i16_f32 v222, |v176|, |v177|\n\t" \
  "s_waitcnt vmcnt(2)\n\t" \
  "ds_read_b64_tr_b16 v[218:219], v248 offset:54824\n\t" \
  "v_mfma_f32_16x16x32_bf16 v[188:191], v[208:211], v[100:103], 0\n\t" \
  "v_lshlrev_b32_sdwa v221, 2, v220 dst_sel:DWORD dst_unused:UNUSED_PAD src0_sel:DWORD src1_sel:BYTE_3\n\t" \
  "v_lshlrev_b32_sdwa v220, 2, v220 dst_sel:DWORD dst_unused:UNUSED_PAD src0_sel:DWORD src1_sel:BYTE_1\n\t" \
  "s_mov_b64 exec, 3\n\t" \
  "ds_add_rtn_u32 v246, v251, v250 offset:47176\n\t" \
  "v_mfma_f32_16x16x32_bf16 v[196:199], v[208:211], v[104:107], 0\n\t" \
  "s_mov_b64 exec, -1\n\t" \
  "v_lshlrev_b32_sdwa v223, 2, v222 dst_sel:DWORD dst_unused:UNUSED_PAD src0_sel:DWORD src1_sel:BYTE_3\n\t" \
  "v_lshlrev_b32_sdwa v222, 2, v222 dst_sel:DWORD dst_unused:UNUSED_PAD src0_sel:DWORD src1_sel:BYTE_1\n\t" \
  "ds_read_u16 v234, v234 offset:10528\n\t" \
  "v_mfma_f32_16x16x32_bf16 v[204:207], v[208:211], v[108:111], 0\n\t" \
  "s_waitcnt lgkmcnt(1)\n\t" \
  "v_lshlrev_b32_sdwa v225, 1, v187 dst_sel:DWORD dst_unused:UNUSED_PAD src0_sel:DWORD src1_sel:WORD_1\n\t" \
  "v_readfirstlane_b32 s4, v246\n\t" \
  "ds_read_b32 v221, v221 offset:10016\n\t" \
  "v_mfma_f32_16x16x32_bf16 v[180:183], v[212:215], v[112:115], v[180:183]\n\t" \
  "s_cmpk_lt_u32 s4, 3072\n\t" \
  "v_lshlrev_b32_sdwa v224, 1, v186 dst_sel:DWORD dst_unused:UNUSED_PAD src0_sel:DWORD src1_sel:WORD_1\n\t" \
  "s_cbranch_scc1 .Lcnx_w0_6\n.Lcnx_g0_6:\n\t" \
  "ds_read_b32 v220, v220 offset:10016\n\t" \
  "v_mfma_f32_16x16x32_bf16 v[188:191], v[212:215], v[116:119], v[188:191]\n\t" \
  "v_lshlrev_b32_sdwa v227, 1, v185 dst_sel:DWORD dst_unused:UNUSED_PAD src0_sel:DWORD src1_sel:WORD_1\n\t" \
  "v_lshlrev_b32_sdwa v226, 1, v184 dst_sel:DWORD dst_unused:UNUSED_PAD src0_sel:DWORD src1_sel:WORD_1\n\t" \
  "ds_read_b64_tr_b16 v[208:209], v249 offset:24600\n\t" \
  "v_mfma_f32_16x16x32_bf16 v[196:199], v[212:215], v[120:123], v[196:199]\n\t" \
  "v_fma_mix_f32 v229, |v165|, v229, v229 op_sel:[0,1,0] op_sel_hi:[0,1,1] clamp\n\t" \
  "v_fma_mix_f32 v228, |v164|, v228, v228 op_sel:[0,1,0] op_sel_hi:[0,1,1] clamp\n\t" \
  "ds_read_b64_tr_b16 v[210:211], v249 offset:26136\n\t" \
  "ds_read_b32 v223, v223 offset:10016\n\t" \
  "v_mfma_f32_16x16x32_bf16 v[204:207], v[212:215], v[124:127], v[204:207]\n\t" \
  "v_fma_mix_f32 v231, |v167|, v231, v231 op_sel:[0,1,0] op_sel_hi:[0,1,1] clamp\n\t" \
  "v_fma_mix_f32 v230, |v166|, v230, v230 op_sel:[0,1,0] op_sel_hi:[0,1,1] clamp\n\t" \
  "s_waitcnt lgkmcnt(5)\n\t" \
  "ds_read_b64_tr_b16 v[212:213], v249 offset:24608\n\t" \
  "ds_read_b32 v222, v222 offset:10016\n\t" \
  "v_mfma_f32_16x16x32_bf16 v[180:183], v[216:219], v[128:131], v[180:183]\n\t" \
  "s_mov_b32 m0, s46\n\t" \
  "v_or_b32 v172, v232, v233\n\t" \
  "v_or_b32 v173, v234, v235\n\t" \
  "s_mov_b32 exec_hi, 0xffff\n\t" \
  "buffer_load_dwordx4 v252, s[16:19], s33 offen lds\n\t" \
  "v_mfma_f32_16x16x32_bf16 v[188:191], v[216:219], v[132:135], v[188:191]\n\t" \
  "s_mov_b32 exec_hi, -1\n\t" \
  "v_fma_f32 v165, |v165|, v229, v165 mul:4\n\t" \
  "v_fma_f32 v164, |v164|, v228, v164 mul:4\n\t" \
  "s_add_u32 s33, s33, s32\n\t" \
  "ds_read_b64_tr_b16 v[214:215], v249 offset:26144\n\t" \
  "v_mfma_f32_16x16x32_bf16 v[196:199], v[216:219], v[136:139], v[196:199]\n\t" \
  "v_fma_f32 v167, |v167|, v231, v167 mul:4\n\t" \
  "v_fma_f32 v166, |v166|, v230, v166 mul:4\n\t" \
  "ds_read_u16_d16_hi v225, v225 offset:10528\n\t" \
  "v_mfma_f32_16x16x32_bf16 v[204:207], v[216:219], v[140:143], v[204:207]\n\t" \
  "v_cvt_pk_bf16_f32 v164, v164, v165\n\t" \
  "v_cvt_pk_bf16_f32 v165, v166, v167\n\t" \
  "ds_read_b64_tr_b16 v[216:217], v249 offset:24664\n\t" \
  "s_waitcnt lgkmcnt(6)\n\t" \
  "v_mfma_f32_16x16x32_bf16 v[0:3], v[208:211], v[146:149], v[0:3]\n\t" \
  "v_cvt_pknorm_i16_f32 v228, |v194|, |v195|\n\t" \
  "v_cvt_pknorm_i16_f32 v230, |v192|, |v193|\n\t" \
  "ds_read_b64_tr_b16 v[218:219], v249 offset:26200\n\t" \
  "v_mfma_f32_16x16x32_bf16 v[24:27], v[208:211], v[154:157], v[24:27]\n\t" \
  "v_lshlrev_b32_sdwa v229, 2, v228 dst_sel:DWORD dst_unused:UNUSED_PAD src0_sel:DWORD src1_sel:BYTE_3\n\t" \
  "v_lshlrev_b32_sdwa v228, 2, v228 dst_sel:DWORD dst_unused:UNUSED_PAD src0_sel:DWORD src1_sel:BYTE_1\n\t" \
  "ds_read_u16 v224, v224 offset:10528\n\t" \
  "v_mfma_f32_16x16x32_bf16 v[48:51], v[208:211], v[162:165], v[48:51]\n\t" \
  "v_lshlrev_b32_sdwa v231, 2, v230 dst_sel:DWORD dst_unused:UNUSED_PAD src0_sel:DWORD src1_sel:BYTE_3\n\t" \
  "v_lshlrev_b32_sdwa v230, 2, v230 dst_sel:DWORD dst_unused:UNUSED_PAD src0_sel:DWORD src1_sel:BYTE_1\n\t" \
  "ds_read_u16_d16_hi v227, v227 offset:10528\n\t" \
  "v_mfma_f32_16x16x32_bf16 v[72:75], v[208:211], v[170:173], v[72:75]\n\t" \
  "s_add_u32 m0, s46, s35\n\t" \
  "v_lshlrev_b32_sdwa v233, 1, v203 dst_sel:DWORD dst_unused:UNUSED_PAD src0_sel:DWORD src1_sel:WORD_1\n\t" \
  "v_lshlrev_b32_sdwa v232, 1, v202 dst_sel:DWORD dst_unused:UNUSED_PAD src0_sel:DWORD src1_sel:WORD_1\n\t" \
  "s_waitcnt lgkmcnt(5)\n\t" \
  "s_mov_b32 exec_hi, 0xffff\n\t" \
  "buffer_load_dwordx4 v252, s[16:19], s34 offen lds\n\t" \
  "v_mfma_f32_16x16x32_bf16 v[4:7], v[212:215], v[146:149], v[4:7]\n\t" \
  "s_mov_b32 exec_hi, -1\n\t" \
  "v_lshlrev_b32_sdwa v235, 1, v201 dst_sel:DWORD dst_unused:UNUSED_PAD src0_sel:DWORD src1_sel:WORD_1\n\t" \
  "s_add_u32 s34, s34, s32\n\t" \
  "ds_read_b64_tr_b16 v[208:209], v249 offset:24672\n\t" \
  "ds_read_u16 v226, v226 offset:10528\n\t" \
  "v_mfma_f32_16x16x32_bf16 v[28:31], v[212:215], v[154:157], v[28:31]\n\t" \
  "v_lshlrev_b32_sdwa v234, 1, v200 dst_sel:DWORD dst_unused:UNUSED_PAD src0_sel:DWORD src1_sel:WORD_1\n\t" \
  "ds_read_b64_tr_b16 v[210:211], v249 offset:26208\n\t" \
  "ds_read_b32 v229, v229 offset:10016\n\t" \
  "v_mfma_f32_16x16x32_bf16 v[52:55], v[212:215], v[162:165], v[52:55]\n\t" \
  "v_fma_mix_f32 v221, |v179|, v221, v221 op_sel:[0,1,0] op_sel_hi:[0,1,1] clamp\n\t" \
  "v_fma_mix_f32 v220, |v178|, v220, v220 op_sel:[0,1,0] op_sel_hi:[0,1,1] clamp\n\t" \
  "ds_read_b32 v228, v228 offset:10016\n\t" \
  "ds_read_b32 v231, v231 offset:10016\n\t" \
  "v_mfma_f32_16x16x32_bf16 v[76:79], v[212:215], v[170:173], v[76:79]\n\t" \
  "v_fma_mix_f32 v223, |v177|, v223, v223 op_sel:[0,1,0] op_sel_hi:[0,1,1] clamp\n\t" \
  "v_fma_mix_f32 v222, |v176|, v222, v222 op_sel:[0,1,0] op_sel_hi:[0,1,1] clamp\n\t" \
  "s_waitcnt lgkmcnt(4)\n\t" \
  "ds_read_b64_tr_b16 v[212:213], v249 offset:24728\n\t" \
  "ds_read_b32 v230, v230 offset:10016\n\t" \
  "v_mfma_f32_16x16x32_bf16 v[8:11], v[216:219], v[146:149], v[8:11]\n\t" \
  "v_or_b32 v187, v224, v225\n\t" \
  "v_or_b32 v186, v226, v227\n\t" \
  "ds_read_b64_tr_b16 v[214:215], v249 offset:26264\n\t" \
  "v_mfma_f32_16x16x32_bf16 v[32:35], v[216:219], v[154:157], v[32:35]\n\t" \
  "v_fma_f32 v179, |v179|, v221, v179 mul:4\n\t" \
  "v_fma_f32 v178, |v178|, v220, v178 mul:4\n\t" \
  "ds_read_u16_d16_hi v233, v233 offset:10528\n\t" \
  "v_mfma_f32_16x16x32_bf16 v[56:59], v[216:219], v[162:165], v[56:59]\n\t" \
  "v_fma_f32 v177, |v177|, v223, v177 mul:4\n\t" \
  "v_fma_f32 v176, |v176|, v222, v176 mul:4\n\t" \
  "s_add_u32 m0, s25, 32800\n\t" \
  "s_add_u32 s24, s24, s26\n\t" \
  "ds_read_u16 v232, v232 offset:10528\n\t" \
  "v_mfma_f32_16x16x32_bf16 v[80:83], v[216:219], v[170:173], v[80:83]\n\t" \
  "v_cvt_pk_bf16_f32 v179, v178, v179\n\t" \
  "v_cvt_pk_bf16_f32 v178, v176, v177\n\t" \
  "s_waitcnt lgkmcnt(8)\n\t" \
  "buffer_load_dwordx4 v254, s[8:11], s24 offen lds\n\t" \
  "ds_read_b64_tr_b16 v[216:217], v249 offset:24736\n\t" \
  "v_mfma_f32_16x16x32_bf16 v[12:15], v[208:211], v[146:149], v[12:15]\n\t" \
  "v_cvt_pknorm_i16_f32 v220, |v180|, |v181|\n\t" \
  "v_cvt_pknorm_i16_f32 v222, |v182|, |v183|\n\t" \
  "ds_read_b64_tr_b16 v[218:219], v249 offset:26272\n\t" \
  "v_mfma_f32_16x16x32_bf16 v[36:39], v[208:211], v[154:157], v[36:39]\n\t" \
  "v_lshlrev_b32_sdwa v221, 2, v220 dst_sel:DWORD dst_unused:UNUSED_PAD src0_sel:DWORD src1_sel:BYTE_3\n\t" \
  "v_lshlrev_b32_sdwa v220, 2, v220 dst_sel:DWORD dst_unused:UNUSED_PAD src0_sel:DWORD src1_sel:BYTE_1\n\t" \
  "ds_read_u16_d16_hi v235, v235 offset:10528\n\t" \
  "v_mfma_f32_16x16x32_bf16 v[60:63], v[208:211], v[162:165], v[60:63]\n\t" \
  "v_lshlrev_b32_sdwa v223, 2, v222 dst_sel:DWORD dst_unused:UNUSED_PAD src0_sel:DWORD src1_sel:BYTE_3\n\t" \
  "v_lshlrev_b32_sdwa v222, 2, v222 dst_sel:DWORD dst_unused:UNUSED_PAD src0_sel:DWORD src1_sel:BYTE_1\n\t" \
  "ds_read_u16 v234, v234 offset:10528\n\t" \
  "v_mfma_f32_16x16x32_bf16 v[84:87], v[208:211], v[170:173], v[84:87]\n\t" \
  "v_lshlrev_b32_sdwa v225, 1, v189 dst_sel:DWORD dst_unused:UNUSED_PAD src0_sel:DWORD src1_sel:WORD_1\n\t" \
  "v_lshlrev_b32_sdwa v224, 1, v188 dst_sel:DWORD dst_unused:UNUSED_PAD src0_sel:DWORD src1_sel:WORD_1\n\t" \
  "ds_read_b64_tr_b16 v[208:209], v248 offset:36904\n\t" \
  "s_waitcnt lgkmcnt(7)\n\t" \
  "v_mfma_f32_16x16x32_bf16 v[16:19], v[212:215], v[146:149], v[16:19]\n\t" \
  "v_lshlrev_b32_sdwa v227, 1, v191 dst_sel:DWORD dst_unused:UNUSED_PAD src0_sel:DWORD src1_sel:WORD_1\n\t" \
  "ds_read_b64_tr_b16 v[210:211], v248 offset:37928\n\t" \
  "ds_read_b32 v221, v221 offset:10016\n\t" \
  "v_mfma_f32_16x16x32_bf16 v[40:43], v[212:215], v[154:157], v[40:43]\n\t" \
  "s_add_u32 s27, s27, s26\n\t" \
  "v_lshlrev_b32_sdwa v226, 1, v190 dst_sel:DWORD dst_unused:UNUSED_PAD src0_sel:DWORD src1_sel:WORD_1\n\t" \
  "v_fma_mix_f32 v229, |v195|, v229, v229 op_sel:[0,1,0] op_sel_hi:[0,1,1] clamp\n\t" \
  "s_mov_b64 exec, s[30:31]\n\t" \
  "buffer_load_dword v237, v254, s[8:11], s27 offen\n\t" \
  "v_mfma_f32_16x16x32_bf16 v[64:67], v[212:215], v[162:165], v[64:67]\n\t" \
  "s_mov_b64 exec, -1\n\t" \
  "v_fma_mix_f32 v228, |v194|, v228, v228 op_sel:[0,1,0] op_sel_hi:[0,1,1] clamp\n\t" \
  "ds_read_b32 v220, v220 offset:10016\n\t" \
  "ds_read_b32 v223, v223 offset:10016\n\t" \
  "v_mfma_f32_16x16x32_bf16 v[88:91], v[212:215], v[170:173], v[88:91]\n\t" \
  "v_fma_mix_f32 v231, |v193|, v231, v231 op_sel:[0,1,0] op_sel_hi:[0,1,1] clamp\n\t" \
  "v_fma_mix_f32 v230, |v192|, v230, v230 op_sel:[0,1,0] op_sel_hi:[0,1,1] clamp\n\t" \
  "s_waitcnt lgkmcnt(5)\n\t" \
  "ds_read_b64_tr_b16 v[212:213], v248 offset:39976\n\t" \
  "ds_read_b32 v222, v222 offset:10016\n\t" \
  "v_mfma_f32_16x16x32_bf16 v[20:23], v[216:219], v[146:149], v[20:23]\n\t" \
  "v_or_b32 v203, v232, v233\n\t" \
  "v_or_b32 v202, v234, v235\n\t" \
  "ds_read_b64_tr_b16 v[214:215], v248 offset:41000\n\t" \
  "v_mfma_f32_16x16x32_bf16 v[44:47], v[216:219], v[154:157], v[44:47]\n\t" \
  "v_fma_f32 v195, |v195|, v229, v195 mul:4\n\t" \
  "v_fma_f32 v194, |v194|, v228, v194 mul:4\n\t" \
  "ds_read_u16_d16_hi v225, v225 offset:10528\n\t" \
  "v_mfma_f32_16x16x32_bf16 v[68:71], v[216:219], v[162:165], v[68:71]\n\t" \
  "v_fma_f32 v193, |v193|, v231, v193 mul:4\n\t" \
  "v_fma_f32 v192, |v192|, v230, v192 mul:4\n\t" \
  "ds_read_u16 v224, v224 offset:10528\n\t" \
  "v_mfma_f32_16x16x32_bf16 v[92:95], v[216:219], v[170:173], v[92:95]\n\t" \
  "v_cvt_pk_bf16_f32 v195, v194, v195\n\t" \
  "v_cvt_pk_bf16_f32 v194, v192, v193\n\t" \
  "ds_read_b64_tr_b16 v[216:217], v248 offset:38952\n\t" \
  "s_waitcnt lgkmcnt(9)\n\t" \
  "v_mfma_f32_16x16x32_bf16 v[144:147], v[208:211], v[96:99], 0\n\t" \
  "v_cvt_pknorm_i16_f32 v228, |v196|, |v197|\n\t" \
  "v_cvt_pknorm_i16_f32 v230, |v198|, |v199|\n\t" \
  "ds_read_b64_tr_b16 v[218:219], v248 offset:42024\n\t" \
  "v_mfma_f32_16x16x32_bf16 v[152:155], v[208:211], v[100:103], 0\n\t" \
  "v_lshlrev_b32_sdwa v229, 2, v228 dst_sel:DWORD dst_unused:UNUSED_PAD src0_sel:DWORD src1_sel:BYTE_3\n\t" \
  "v_lshlrev_b32_sdwa v228, 2, v228 dst_sel:DWORD dst_unused:UNUSED_PAD src0_sel:DWORD src1_sel:BYTE_1\n\t" \
  "ds_read_u16_d16_hi v227, v227 offset:10528\n\t" \
  "v_mfma_f32_16x16x32_bf16 v[160:163], v[208:211], v[104:107], 0\n\t" \
  "v_lshlrev_b32_sdwa v231, 2, v230 dst_sel:DWORD dst_unused:UNUSED_PAD src0_sel:DWORD src1_sel:BYTE_3\n\t" \
  "v_lshlrev_b32_sdwa v230, 2, v230 dst_sel:DWORD dst_unused:UNUSED_PAD src0_sel:DWORD src1_sel:BYTE_1\n\t" \
  "ds_read_u16 v226, v226 offset:10528\n\t" \
  "v_mfma_f32_16x16x32_bf16 v[168:171], v[208:211], v[108:111], 0\n\t" \
  "v_lshlrev_b32_sdwa v233, 1, v205 dst_sel:DWORD dst_unused:UNUSED_PAD src0_sel:DWORD src1_sel:WORD_1\n\t" \
  "v_lshlrev_b32_sdwa v232, 1, v204 dst_sel:DWORD dst_unused:UNUSED_PAD src0_sel:DWORD src1_sel:WORD_1\n\t" \
  "ds_read_b64_tr_b16 v[208:209], v248 offset:37416\n\t" \
  "s_waitcnt lgkmcnt(7)\n\t" \
  "v_mfma_f32_16x16x32_bf16 v[144:147], v[212:215], v[112:115], v[144:147]\n\t" \
  "v_lshlrev_b32_sdwa v235, 1, v207 dst_sel:DWORD dst_unused:UNUSED_PAD src0_sel:DWORD src1_sel:WORD_1\n\t" \
  "ds_read_b64_tr_b16 v[210:211], v248 offset:38440\n\t" \
  "ds_read_b32 v229, v229 offset:10016\n\t" \
  "v_mfma_f32_16x16x32_bf16 v[152:155], v[212:215], v[116:119], v[152:155]\n\t" \
  "v_lshlrev_b32_sdwa v234, 1, v206 dst_sel:DWORD dst_unused:UNUSED_PAD src0_sel:DWORD src1_sel:WORD_1\n\t" \
  "v_fma_mix_f32 v221, |v181|, v221, v221 op_sel:[0,1,0] op_sel_hi:[0,1,1] clamp\n\t" \
  "ds_read_b32 v228, v228 offset:10016\n\t" \
  "v_mfma_f32_16x16x32_bf16 v[160:163], v[212:215], v[120:123], v[160:163]\n\t" \
  "v_fma_mix_f32 v220, |v180|, v220, v220 op_sel:[0,1,0] op_sel_hi:[0,1,1] clamp\n\t" \
  "ds_read_b32 v231, v231 offset:10016\n\t" \
  "ds_read_b32 v230, v230 offset:10016\n\t" \
  "v_mfma_f32_16x16x32_bf16 v[168:171], v[212:215], v[124:127], v[168:171]\n\t" \
  "v_fma_mix_f32 v223, |v183|, v223, v223 op_sel:[0,1,0] op_sel_hi:[0,1,1] clamp\n\t" \
  "v_fma_mix_f32 v222, |v182|, v222, v222 op_sel:[0,1,0] op_sel_hi:[0,1,1] clamp\n\t" \
  "s_waitcnt lgkmcnt(6)\n\t" \
  "ds_read_b64_tr_b16 v[212:213], v248 offset:40488\n\t" \
  "ds_read_u16_d16_hi v233, v233 offset:10528\n\t" \
  "v_mfma_f32_16x16x32_bf16 v[144:147], v[216:219], v[128:131], v[144:147]\n\t" \
  "v_or_b32 v188, v224, v225\n\t" \
  "v_or_b32 v189, v226, v227\n\t" \
  "ds_read_b64_tr_b16 v[214:215], v248 offset:41512\n\t" \
  "v_mfma_f32_16x16x32_bf16 v[152:155], v[216:219], v[132:135], v[152:155]\n\t" \
  "v_fma_f32 v181, |v181|, v221, v181 mul:4\n\t" \
  "v_fma_f32 v180, |v180|, v220, v180 mul:4\n\t" \
  "ds_read_u16 v232, v232 offset:10528\n\t" \
  "v_mfma_f32_16x16x32_bf16 v[160:163], v[216:219], v[136:139], v[160:163]\n\t" \
  "v_fma_f32 v183, |v183|, v223, v183 mul:4\n\t" \
  "v_fma_f32 v182, |v182|, v222, v182 mul:4\n\t" \
  "ds_read_u16_d16_hi v235, v235 offset:10528\n\t" \
  "v_mfma_f32_16x16x32_bf16 v[168:171], v[216:219], v[140:143], v[168:171]\n\t" \
  "v_cvt_pk_bf16_f32 v180, v180, v181\n\t" \
  "v_cvt_pk_bf16_f32 v181, v182, v183\n\t" \
  "ds_read_b64_tr_b16 v[216:217], v248 offset:39464\n\t" \
  "s_waitcnt lgkmcnt(10)\n\t" \
  "v_mfma_f32_16x16x32_bf16 v[148:151], v[208:211], v[96:99], 0\n\t" \
  "v_cvt_pknorm_i16_f32 v220, |v146|, |v147|\n\t" \
  "v_cvt_pknorm_i16_f32 v222, |v144|, |v145|\n\t" \
  "s_waitcnt vmcnt(2)\n\t" \
  "ds_read_b64_tr_b16 v[218:219], v248 offset:42536\n\t" \
  "v_mfma_f32_16x16x32_bf16 v[156:159], v[208:211], v[100:103], 0\n\t" \
  "v_lshlrev_b32_sdwa v221, 2, v220 dst_sel:DWORD dst_unused:UNUSED_PAD src0_sel:DWORD src1_sel:BYTE_3\n\t" \
  "v_lshlrev_b32_sdwa v220, 2, v220 dst_sel:DWORD dst_unused:UNUSED_PAD src0_sel:DWORD src1_sel:BYTE_1\n\t" \
  "s_mov_b64 exec, 3\n\t" \
  "ds_add_rtn_u32 v246, v251, v250 offset:47184\n\t" \
  "v_mfma_f32_16x16x32_bf16 v[164:167], v[208:211], v[104:107], 0\n\t" \
  "s_mov_b64 exec, -1\n\t" \
  "v_lshlrev_b32_sdwa v223, 2, v222 dst_sel:DWORD dst_unused:UNUSED_PAD src0_sel:DWORD src1_sel:BYTE_3\n\t" \
  "v_lshlrev_b32_sdwa v222, 2, v222 dst_sel:DWORD dst_unused:UNUSED_PAD src0_sel:DWORD src1_sel:BYTE_1\n\t" \
  "ds_read_u16 v234, v234 offset:10528\n\t" \
  "v_mfma_f32_16x16x32_bf16 v[172:175], v[208:211], v[108:111], 0\n\t" \
  "s_waitcnt lgkmcnt(1)\n\t" \
  "v_lshlrev_b32_sdwa v225, 1, v155 dst_sel:DWORD dst_unused:UNUSED_PAD src0_sel:DWORD src1_sel:WORD_1\n\t" \
  "v_readfirstlane_b32 s4, v246\n\t" \
  "ds_read_b32 v221, v221 offset:10016\n\t" \
  "v_mfma_f32_16x16x32_bf16 v[148:151], v[212:215], v[112:115], v[148:151]\n\t" \
  "s_cmpk_lt_u32 s4, 3072\n\t" \
  "v_lshlrev_b32_sdwa v224, 1, v154 dst_sel:DWORD dst_unused:UNUSED_PAD src0_sel:DWORD src1_sel:WORD_1\n\t" \
  "s_cbranch_scc1 .Lcnx_w0_7\n.Lcnx_g0_7:\n\t" \
  "ds_read_b32 v220, v220 offset:10016\n\t" \
  "v_mfma_f32_16x16x32_bf16 v[156:159], v[212:215], v[116:119], v[156:159]\n\t" \
  "v_lshlrev_b32_sdwa v227, 1, v153 dst_sel:DWORD dst_unused:UNUSED_PAD src0_sel:DWORD src1_sel:WORD_1\n\t" \
  "v_lshlrev_b32_sdwa v226, 1, v152 dst_sel:DWORD dst_unused:UNUSED_PAD src0_sel:DWORD src1_sel:WORD_1\n\t" \
  "ds_read_b64_tr_b16 v[208:209], v249 offset:30752\n\t" \
  "v_mfma_f32_16x16x32_bf16 v[164:167], v[212:215], v[120:123], v[164:167]\n\t" \
  "v_fma_mix_f32 v229, |v197|, v229, v229 op_sel:[0,1,0] op_sel_hi:[0,1,1] clamp\n\t" \
  "v_fma_mix_f32 v228, |v196|, v228, v228 op_sel:[0,1,0] op_sel_hi:[0,1,1] clamp\n\t" \
  "ds_read_b64_tr_b16 v[210:211], v249 offset:32288\n\t" \
  "ds_read_b32 v223, v223 offset:10016\n\t" \
  "v_mfma_f32_16x16x32_bf16 v[172:175], v[212:215], v[124:127], v[172:175]\n\t" \
  "v_fma_mix_f32 v231, |v199|, v231, v231 op_sel:[0,1,0] op_sel_hi:[0,1,1] clamp\n\t" \
  "v_fma_mix_f32 v230, |v198|, v230, v230 op_sel:[0,1,0] op_sel_hi:[0,1,1] clamp\n\t" \
  "s_waitcnt lgkmcnt(5)\n\t" \
  "ds_read_b64_tr_b16 v[212:213], v249 offset:30760\n\t" \
  "ds_read_b32 v222, v222 offset:10016\n\t" \
  "v_mfma_f32_16x16x32_bf16 v[148:151], v[216:219], v[128:131], v[148:151]\n\t" \
  "s_mov_b32 m0, s47\n\t" \
  "v_or_b32 v204, v232, v233\n\t" \
  "v_or_b32 v205, v234, v235\n\t" \
  "s_mov_b32 exec_hi, 0xffff\n\t" \
  "buffer_load_dwordx4 v252, s[16:19], s33 offen lds\n\t" \
  "v_mfma_f32_16x16x32_bf16 v[156:159], v[216:219], v[132:135], v[156:159]\n\t" \
  "s_mov_b32 exec_hi, -1\n\t" \
  "v_fma_f32 v197, |v197|, v229, v197 mul:4\n\t" \
  "v_fma_f32 v196, |v196|, v228, v196 mul:4\n\t" \
  "s_add_u32 s33, s33, s32\n\t" \
  "ds_read_b64_tr_b16 v[214:215], v249 offset:32296\n\t" \
  "v_mfma_f32_16x16x32_bf16 v[164:167], v[216:219], v[136:139], v[164:167]\n\t" \
  "v_fma_f32 v199, |v199|, v231, v199 mul:4\n\t" \
  "v_fma_f32 v198, |v198|, v230, v198 mul:4\n\t" \
  "ds_read_u16_d16_hi v225, v225 offset:10528\n\t" \
  "v_mfma_f32_16x16x32_bf16 v[172:175], v[216:219], v[140:143], v[172:175]\n\t" \
  "v_cvt_pk_bf16_f32 v196, v196, v197\n\t" \
  "v_cvt_pk_bf16_f32 v197, v198, v199\n\t" \
  "ds_read_b64_tr_b16 v[216:217], v249 offset:30816\n\t" \
  "s_waitcnt lgkmcnt(6)\n\t" \
  "v_mfma_f32_16x16x32_bf16 v[0:3], v[208:211], v[178:181], v[0:3]\n\t" \
  "v_cvt_pknorm_i16_f32 v228, |v162|, |v163|\n\t" \
  "v_cvt_pknorm_i16_f32 v230, |v160|, |v161|\n\t" \
  "ds_read_b64_tr_b16 v[218:219], v249 offset:32352\n\t" \
  "v_mfma_f32_16x16x32_bf16 v[24:27], v[208:211], v[186:189], v[24:27]\n\t" \
  "v_lshlrev_b32_sdwa v229, 2, v228 dst_sel:DWORD dst_unused:UNUSED_PAD src0_sel:DWORD src1_sel:BYTE_3\n\t" \
  "v_lshlrev_b32_sdwa v228, 2, v228 dst_sel:DWORD dst_unused:UNUSED_PAD src0_sel:DWORD src1_sel:BYTE_1\n\t" \
  "ds_read_u16 v224, v224 offset:10528\n\t" \
  "v_mfma_f32_16x16x32_bf16 v[48:51], v[208:211], v[194:197], v[48:51]\n\t" \
  "v_lshlrev_b32_sdwa v231, 2, v230 dst_sel:DWORD dst_unused:UNUSED_PAD src0_sel:DWORD src1_sel:BYTE_3\n\t" \
  "v_lshlrev_b32_sdwa v230, 2, v230 dst_sel:DWORD dst_unused:UNUSED_PAD src0_sel:DWORD src1_sel:BYTE_1\n\t" \
  "ds_read_u16_d16_hi v227, v227 offset:10528\n\t" \
  "v_mfma_f32_16x16x32_bf16 v[72:75], v[208:211], v[202:205], v[72:75]\n\t" \
  "s_add_u32 m0, s47, s35\n\t" \
  "v_lshlrev_b32_sdwa v233, 1, v171 dst_sel:DWORD dst_unused:UNUSED_PAD src0_sel:DWORD src1_sel:WORD_1\n\t" \
  "v_lshlrev_b32_sdwa v232, 1, v170 dst_sel:DWORD dst_unused:UNUSED_PAD src0_sel:DWORD src1_sel:WORD_1\n\t" \
  "s_waitcnt lgkmcnt(5)\n\t" \
  "s_mov_b32 exec_hi, 0xffff\n\t" \
  "buffer_load_dwordx4 v252, s[16:19], s34 offen lds\n\t" \
  "v_mfma_f32_16x16x32_bf16 v[4:7], v[212:215], v[178:181], v[4:7]\n\t" \
  "s_mov_b32 exec_hi, -1\n\t" \
  "v_lshlrev_b32_sdwa v235, 1, v169 dst_sel:DWORD dst_unused:UNUSED_PAD src0_sel:DWORD src1_sel:WORD_1\n\t" \
  "s_add_u32 s34, s34, s32\n\t" \
  "ds_read_b64_tr_b16 v[208:209], v249 offset:30824\n\t" \
  "ds_read_u16 v226, v226 offset:10528\n\t" \
  "v_mfma_f32_16x16x32_bf16 v[28:31], v[212:215], v[186:189], v[28:31]\n\t" \
  "v_lshlrev_b32_sdwa v234, 1, v168 dst_sel:DWORD dst_unused:UNUSED_PAD src0_sel:DWORD src1_sel:WORD_1\n\t" \
  "ds_read_b64_tr_b16 v[210:211], v249 offset:32360\n\t" \
  "ds_read_b32 v229, v229 offset:10016\n\t" \
  "v_mfma_f32_16x16x32_bf16 v[52:55], v[212:215], v[194:197], v[52:55]\n\t" \
  "v_fma_mix_f32 v221, |v147|, v221, v221 op_sel:[0,1,0] op_sel_hi:[0,1,1] clamp\n\t" \
  "v_fma_mix_f32 v220, |v146|, v220, v220 op_sel:[0,1,0] op_sel_hi:[0,1,1] clamp\n\t" \
  "ds_read_b32 v228, v228 offset:10016\n\t" \
  "ds_read_b32 v231, v231 offset:10016\n\t" \
  "v_mfma_f32_16x16x32_bf16 v[76:79], v[212:215], v[202:205], v[76:79]\n\t" \
  "v_fma_mix_f32 v223, |v145|, v223, v223 op_sel:[0,1,0] op_sel_hi:[0,1,1] clamp\n\t" \
  "v_fma_mix_f32 v222, |v144|, v222, v222 op_sel:[0,1,0] op_sel_hi:[0,1,1] clamp\n\t" \
  "s_waitcnt lgkmcnt(4)\n\t" \
  "ds_read_b64_tr_b16 v[212:213], v249 offset:30880\n\t" \
  "ds_read_b32 v230, v230 offset:10016\n\t" \
  "v_mfma_f32_16x16x32_bf16 v[8:11], v[216:219], v[178:181], v[8:11]\n\t" \
  "v_or_b32 v155, v224, v225\n\t" \
  "v_or_b32 v154, v226, v227\n\t" \
  "ds_read_b64_tr_b16 v[214:215], v249 offset:32416\n\t" \
  "v_mfma_f32_16x16x32_bf16 v[32:35], v[216:219], v[186:189], v[32:35]\n\t" \
  "v_fma_f32 v147, |v147|, v221, v147 mul:4\n\t" \
  "v_fma_f32 v146, |v146|, v220, v146 mul:4\n\t" \
  "ds_read_u16_d16_hi v233, v233 offset:10528\n\t" \
  "v_mfma_f32_16x16x32_bf16 v[56:59], v[216:219], v[194:197], v[56:59]\n\t" \
  "v_fma_f32 v145, |v145|, v223, v145 mul:4\n\t" \
  "v_fma_f32 v144, |v144|, v222, v144 mul:4\n\t" \
  "s_add_u32 m0, s25, 41000\n\t" \
  "s_add_u32 s24, s24, s26\n\t" \
  "ds_read_u16 v232, v232 offset:10528\n\t" \
  "v_mfma_f32_16x16x32_bf16 v[80:83], v[216:219], v[202:205], v[80:83]\n\t" \
  "v_cvt_pk_bf16_f32 v147, v146, v147\n\t" \
  "v_cvt_pk_bf16_f32 v146, v144, v145\n\t" \
  "s_waitcnt lgkmcnt(8)\n\t" \
  "buffer_load_dwordx4 v254, s[8:11], s24 offen lds\n\t" \
  "ds_read_b64_tr_b16 v[216:217], v249 offset:30888\n\t" \
  "v_mfma_f32_16x16x32_bf16 v[12:15], v[208:211], v[178:181], v[12:15]\n\t" \
  "v_cvt_pknorm_i16_f32 v220, |v148|, |v149|\n\t" \
  "v_cvt_pknorm_i16_f32 v222, |v150|, |v151|\n\t" \
  "ds_read_b64_tr_b16 v[218:219], v249 offset:32424\n\t" \
  "v_mfma_f32_16x16x32_bf16 v[36:39], v[208:211], v[186:189], v[36:39]\n\t" \
  "v_lshlrev_b32_sdwa v221, 2, v220 dst_sel:DWORD dst_unused:UNUSED_PAD src0_sel:DWORD src1_sel:BYTE_3\n\t" \
  "v_lshlrev_b32_sdwa v220, 2, v220 dst_sel:DWORD dst_unused:UNUSED_PAD src0_sel:DWORD src1_sel:BYTE_1\n\t" \
  "ds_read_u16_d16_hi v235, v235 offset:10528\n\t" \
  "v_mfma_f32_16x16x32_bf16 v[60:63], v[208:211], v[194:197], v[60:63]\n\t" \
  "v_lshlrev_b32_sdwa v223, 2, v222 dst_sel:DWORD dst_unused:UNUSED_PAD src0_sel:DWORD src1_sel:BYTE_3\n\t" \
  "v_lshlrev_b32_sdwa v222, 2, v222 dst_sel:DWORD dst_unused:UNUSED_PAD src0_sel:DWORD src1_sel:BYTE_1\n\t" \
  "ds_read_u16 v234, v234 offset:10528\n\t" \
  "v_mfma_f32_16x16x32_bf16 v[84:87], v[208:211], v[202:205], v[84:87]\n\t" \
  "v_lshlrev_b32_sdwa v225, 1, v157 dst_sel:DWORD dst_unused:UNUSED_PAD src0_sel:DWORD src1_sel:WORD_1\n\t" \
  "v_lshlrev_b32_sdwa v224, 1, v156 dst_sel:DWORD dst_unused:UNUSED_PAD src0_sel:DWORD src1_sel:WORD_1\n\t" \
  "ds_read_b64_tr_b16 v[208:209], v248 offset:0\n\t" \
  "s_waitcnt lgkmcnt(7)\n\t" \
  "v_mfma_f32_16x16x32_bf16 v[16:19], v[212:215], v[178:181], v[16:19]\n\t" \
  "v_lshlrev_b32_sdwa v227, 1, v159 dst_sel:DWORD dst_unused:UNUSED_PAD src0_sel:DWORD src1_sel:WORD_1\n\t" \
  "ds_read_b64_tr_b16 v[210:211], v248 offset:1024\n\t" \
  "ds_read_b32 v221, v221 offset:10016\n\t" \
  "v_mfma_f32_16x16x32_bf16 v[40:43], v[212:215], v[186:189], v[40:43]\n\t" \
  "s_add_u32 s27, s27, s26\n\t" \
  "v_lshlrev_b32_sdwa v226, 1, v158 dst_sel:DWORD dst_unused:UNUSED_PAD src0_sel:DWORD src1_sel:WORD_1\n\t" \
  "v_fma_mix_f32 v229, |v163|, v229, v229 op_sel:[0,1,0] op_sel_hi:[0,1,1] clamp\n\t" \
  "s_mov_b64 exec, s[30:31]\n\t" \
  "buffer_load_dword v237, v254, s[8:11], s27 offen\n\t" \
  "v_mfma_f32_16x16x32_bf16 v[64:67], v[212:215], v[194:197], v[64:67]\n\t" \
  "s_mov_b64 exec, -1\n\t" \
  "v_fma_mix_f32 v228, |v162|, v228, v228 op_sel:[0,1,0] op_sel_hi:[0,1,1] clamp\n\t" \
  "ds_read_b32 v220, v220 offset:10016\n\t" \
  "ds_read_b32 v223, v223 offset:10016\n\t" \
  "v_mfma_f32_16x16x32_bf16 v[88:91], v[212:215], v[202:205], v[88:91]\n\t" \
  "v_fma_mix_f32 v231, |v161|, v231, v231 op_sel:[0,1,0] op_sel_hi:[0,1,1] clamp\n\t" \
  "v_fma_mix_f32 v230, |v160|, v230, v230 op_sel:[0,1,0] op_sel_hi:[0,1,1] clamp\n\t" \
  "s_waitcnt lgkmcnt(5)\n\t" \
  "ds_read_b64_tr_b16 v[212:213], v248 offset:3072\n\t" \
  "ds_read_b32 v222, v222 offset:10016\n\t" \
  "v_mfma_f32_16x16x32_bf16 v[20:23], v[216:219], v[178:181], v[20:23]\n\t" \
  "v_or_b32 v171, v232, v233\n\t" \
  "v_or_b32 v170, v234, v235\n\t" \
  "ds_read_b64_tr_b16 v[214:215], v248 offset:4096\n\t" \
  "v_mfma_f32_16x16x32_bf16 v[44:47], v[216:219], v[186:189], v[44:47]\n\t" \
  "v_fma_f32 v163, |v163|, v229, v163 mul:4\n\t" \
  "v_fma_f32 v162, |v162|, v228, v162 mul:4\n\t" \
  "ds_read_u16_d16_hi v225, v225 offset:10528\n\t" \
  "v_mfma_f32_16x16x32_bf16 v[68:71], v[216:219], v[194:197], v[68:71]\n\t" \
  "v_fma_f32 v161, |v161|, v231, v161 mul:4\n\t" \
  "v_fma_f32 v160, |v160|, v230, v160 mul:4\n\t" \
  "ds_read_u16 v224, v224 offset:10528\n\t" \
  "v_mfma_f32_16x16x32_bf16 v[92:95], v[216:219], v[202:205], v[92:95]\n\t" \
  "v_cvt_pk_bf16_f32 v163, v162, v163\n\t" \
  "v_cvt_pk_bf16_f32 v162, v160, v161\n\t" \
  "ds_read_b64_tr_b16 v[216:217], v248 offset:2048\n\t" \
  "s_waitcnt lgkmcnt(9)\n\t" \
  "v_mfma_f32_16x16x32_bf16 v[176:179], v[208:211], v[96:99], 0\n\t" \
  "v_cvt_pknorm_i16_f32 v228, |v164|, |v165|\n\t" \
  "v_cvt_pknorm_i16_f32 v230, |v166|, |v167|\n\t" \
  "ds_read_b64_tr_b16 v[218:219], v248 offset:5120\n\t" \
  "v_mfma_f32_16x16x32_bf16 v[184:187], v[208:211], v[100:103], 0\n\t" \
  "v_lshlrev_b32_sdwa v229, 2, v228 dst_sel:DWORD dst_unused:UNUSED_PAD src0_sel:DWORD src1_sel:BYTE_3\n\t" \
  "v_lshlrev_b32_sdwa v228, 2, v228 dst_sel:DWORD dst_unused:UNUSED_PAD src0_sel:DWORD src1_sel:BYTE_1\n\t" \
  "ds_read_u16_d16_hi v227, v227 offset:10528\n\t" \
  "v_mfma_f32_16x16x32_bf16 v[192:195], v[208:211], v[104:107], 0\n\t" \
  "v_lshlrev_b32_sdwa v231, 2, v230 dst_sel:DWORD dst_unused:UNUSED_PAD src0_sel:DWORD src1_sel:BYTE_3\n\t" \
  "v_lshlrev_b32_sdwa v230, 2, v230 dst_sel:DWORD dst_unused:UNUSED_PAD src0_sel:DWORD src1_sel:BYTE_1\n\t" \
  "ds_read_u16 v226, v226 offset:10528\n\t" \
  "v_mfma_f32_16x16x32_bf16 v[200:203], v[208:211], v[108:111], 0\n\t" \
  "v_lshlrev_b32_sdwa v233, 1, v173 dst_sel:DWORD dst_unused:UNUSED_PAD src0_sel:DWORD src1_sel:WORD_1\n\t" \
  "v_lshlrev_b32_sdwa v232, 1, v172 dst_sel:DWORD dst_unused:UNUSED_PAD src0_sel:DWORD src1_sel:WORD_1\n\t" \
  "ds_read_b64_tr_b16 v[208:209], v248 offset:512\n\t" \
  "s_waitcnt lgkmcnt(7)\n\t" \
  "v_mfma_f32_16x16x32_bf16 v[176:179], v[212:215], v[112:115], v[176:179]\n\t" \
  "v_lshlrev_b32_sdwa v235, 1, v175 dst_sel:DWORD dst_unused:UNUSED_PAD src0_sel:DWORD src1_sel:WORD_1\n\t" \
  "ds_read_b64_tr_b16 v[210:211], v248 offset:1536\n\t" \
  "ds_read_b32 v229, v229 offset:10016\n\t" \
  "v_mfma_f32_16x16x32_bf16 v[184:187], v[212:215], v[116:119], v[184:187]\n\t" \
  "v_lshlrev_b32_sdwa v234, 1, v174 dst_sel:DWORD dst_unused:UNUSED_PAD src0_sel:DWORD src1_sel:WORD_1\n\t" \
  "v_fma_mix_f32 v221, |v149|, v221, v221 op_sel:[0,1,0] op_sel_hi:[0,1,1] clamp\n\t" \
  "ds_read_b32 v228, v228 offset:10016\n\t" \
  "v_mfma_f32_16x16x32_bf16 v[192:195], v[212:215], v[120:123], v[192:195]\n\t" \
  "v_fma_mix_f32 v220, |v148|, v220, v220 op_sel:[0,1,0] op_sel_hi:[0,1,1] clamp\n\t" \
  "ds_read_b32 v231, v231 offset:10016\n\t" \
  "ds_read_b32 v230, v230 offset:10016\n\t" \
  "v_mfma_f32_16x16x32_bf16 v[200:203], v[212:215], v[124:127], v[200:203]\n\t" \
  "v_fma_mix_f32 v223, |v151|, v223, v223 op_sel:[0,1,0] op_sel_hi:[0,1,1] clamp\n\t" \
  "v_fma_mix_f32 v222, |v150|, v222, v222 op_sel:[0,1,0] op_sel_hi:[0,1,1] clamp\n\t" \
  "s_waitcnt lgkmcnt(6)\n\t" \
  "ds_read_b64_tr_b16 v[212:213], v248 offset:3584\n\t" \
  "ds_read_u16_d16_hi v233, v233 offset:10528\n\t" \
  "v_mfma_f32_16x16x32_bf16 v[176:179], v[216:219], v[128:131], v[176:179]\n\t" \
  "v_or_b32 v156, v224, v225\n\t" \
  "v_or_b32 v157, v226, v227\n\t" \
  "ds_read_b64_tr_b16 v[214:215], v248 offset:4608\n\t" \
  "v_mfma_f32_16x16x32_bf16 v[184:187], v[216:219], v[132:135], v[184:187]\n\t" \
  "v_fma_f32 v149, |v149|, v221, v149 mul:4\n\t" \
  "v_fma_f32 v148, |v148|, v220, v148 mul:4\n\t" \
  "ds_read_u16 v232, v232 offset:10528\n\t" \
  "v_mfma_f32_16x16x32_bf16 v[192:195], v[216:219], v[136:139], v[192:195]\n\t" \
  "v_fma_f32 v151, |v151|, v223, v151 mul:4\n\t" \
  "v_fma_f32 v150, |v150|, v222, v150 mul:4\n\t" \
  "ds_read_u16_d16_hi v235, v235 offset:10528\n\t" \
  "v_mfma_f32_16x16x32_bf16 v[200:203], v[216:219], v[140:143], v[200:203]\n\t" \
  "v_cvt_pk_bf16_f32 v148, v148, v149\n\t" \
  "v_cvt_pk_bf16_f32 v149, v150, v151\n\t" \
  "ds_read_b64_tr_b16 v[216:217], v248 offset:2560\n\t" \
  "s_waitcnt lgkmcnt(10)\n\t" \
  "v_mfma_f32_16x16x32_bf16 v[180:183], v[208:211], v[96:99], 0\n\t" \
  "v_cvt_pknorm_i16_f32 v220, |v178|, |v179|\n\t" \
  "v_cvt_pknorm_i16_f32 v222, |v176|, |v177|\n\t" \
  "s_waitcnt vmcnt(2)\n\t" \
  "ds_read_b64_tr_b16 v[218:219], v248 offset:5632\n\t" \
  "v_mfma_f32_16x16x32_bf16 v[188:191], v[208:211], v[100:103], 0\n\t" \
  "v_lshlrev_b32_sdwa v221, 2, v220 dst_sel:DWORD dst_unused:UNUSED_PAD src0_sel:DWORD src1_sel:BYTE_3\n\t" \
  "v_lshlrev_b32_sdwa v220, 2, v220 dst_sel:DWORD dst_unused:UNUSED_PAD src0_sel:DWORD src1_sel:BYTE_1\n\t" \
  "s_mov_b64 exec, 3\n\t" \
  "ds_add_rtn_u32 v246, v251, v250 offset:47192\n\t" \
  "v_mfma_f32_16x16x32_bf16 v[196:199], v[208:211], v[104:107], 0\n\t" \
  "s_mov_b64 exec, -1\n\t" \
  "v_lshlrev_b32_sdwa v223, 2, v222 dst_sel:DWORD dst_unused:UNUSED_PAD src0_sel:DWORD src1_sel:BYTE_3\n\t" \
  "v_lshlrev_b32_sdwa v222, 2, v222 dst_sel:DWORD dst_unused:UNUSED_PAD src0_sel:DWORD src1_sel:BYTE_1\n\t" \
  "ds_read_u16 v234, v234 offset:10528\n\t" \
  "v_mfma_f32_16x16x32_bf16 v[204:207], v[208:211], v[108:111], 0\n\t" \
  "s_waitcnt lgkmcnt(1)\n\t" \
  "v_lshlrev_b32_sdwa v225, 1, v187 dst_sel:DWORD dst_unused:UNUSED_PAD src0_sel:DWORD src1_sel:WORD_1\n\t" \
  "v_readfirstlane_b32 s4, v246\n\t" \
  "ds_read_b32 v221, v221 offset:10016\n\t" \
  "v_mfma_f32_16x16x32_bf16 v[180:183], v[212:215], v[112:115], v[180:183]\n\t" \
  "s_cmpk_lt_u32 s4, 3072\n\t" \
  "v_lshlrev_b32_sdwa v224, 1, v186 dst_sel:DWORD dst_unused:UNUSED_PAD src0_sel:DWORD src1_sel:WORD_1\n\t" \
  "s_cbranch_scc1 .Lcnx_w0_8\n.Lcnx_g0_8:\n\t" \
  "ds_read_b32 v220, v220 offset:10016\n\t" \
  "v_mfma_f32_16x16x32_bf16 v[188:191], v[212:215], v[116:119], v[188:191]\n\t" \
  "v_lshlrev_b32_sdwa v227, 1, v185 dst_sel:DWORD dst_unused:UNUSED_PAD src0_sel:DWORD src1_sel:WORD_1\n\t" \
  "v_lshlrev_b32_sdwa v226, 1, v184 dst_sel:DWORD dst_unused:UNUSED_PAD src0_sel:DWORD src1_sel:WORD_1\n\t" \
  "ds_read_b64_tr_b16 v[208:209], v249 offset:6152\n\t" \
  "v_mfma_f32_16x16x32_bf16 v[196:199], v[212:215], v[120:123], v[196:199]\n\t" \
  "v_fma_mix_f32 v229, |v165|, v229, v229 op_sel:[0,1,0] op_sel_hi:[0,1,1] clamp\n\t" \
  "v_fma_mix_f32 v228, |v164|, v228, v228 op_sel:[0,1,0] op_sel_hi:[0,1,1] clamp\n\t" \
  "ds_read_b64_tr_b16 v[210:211], v249 offset:7688\n\t" \
  "ds_read_b32 v223, v223 offset:10016\n\t" \
  "v_mfma_f32_16x16x32_bf16 v[204:207], v[212:215], v[124:127], v[204:207]\n\t" \
  "v_fma_mix_f32 v231, |v167|, v231, v231 op_sel:[0,1,0] op_sel_hi:[0,1,1] clamp\n\t" \
  "v_fma_mix_f32 v230, |v166|, v230, v230 op_sel:[0,1,0] op_sel_hi:[0,1,1] clamp\n\t" \
  "s_waitcnt lgkmcnt(5)\n\t" \
  "ds_read_b64_tr_b16 v[212:213], v249 offset:6160\n\t" \
  "ds_read_b32 v222, v222 offset:10016\n\t" \
  "v_mfma_f32_16x16x32_bf16 v[180:183], v[216:219], v[128:131], v[180:183]\n\t" \
  "s_mov_b32 m0, s44\n\t" \
  "v_or_b32 v172, v232, v233\n\t" \
  "v_or_b32 v173, v234, v235\n\t" \
  "s_mov_b32 exec_hi, 0xffff\n\t" \
  "buffer_load_dwordx4 v252, s[16:19], s33 offen lds\n\t" \
  "v_mfma_f32_16x16x32_bf16 v[188:191], v[216:219], v[132:135], v[188:191]\n\t" \
  "s_mov_b32 exec_hi, -1\n\t" \
  "v_fma_f32 v165, |v165|, v229, v165 mul:4\n\t" \
  "v_fma_f32 v164, |v164|, v228, v164 mul:4\n\t" \
  "s_add_u32 s33, s33, s32\n\t" \
  "ds_read_b64_tr_b16 v[214:215], v249 offset:7696\n\t" \
  "v_mfma_f32_16x16x32_bf16 v[196:199], v[216:219], v[136:139], v[196:199]\n\t" \
  "v_fma_f32 v167, |v167|, v231, v167 mul:4\n\t" \
  "v_fma_f32 v166, |v166|, v230, v166 mul:4\n\t" \
  "ds_read_u16_d16_hi v225, v225 offset:10528\n\t" \
  "v_mfma_f32_16x16x32_bf16 v[204:207], v[216:219], v[140:143], v[204:207]\n\t" \
  "v_cvt_pk_bf16_f32 v164, v164, v165\n\t" \
  "v_cvt_pk_bf16_f32 v165, v166, v167\n\t" \
  "ds_read_b64_tr_b16 v[216:217], v249 offset:6216\n\t" \
  "s_waitcnt lgkmcnt(6)\n\t" \
  "v_mfma_f32_16x16x32_bf16 v[0:3], v[208:211], v[146:149], v[0:3]\n\t" \
  "v_cvt_pknorm_i16_f32 v228, |v194|, |v195|\n\t" \
  "v_cvt_pknorm_i16_f32 v230, |v192|, |v193|\n\t" \
  "ds_read_b64_tr_b16 v[218:219], v249 offset:7752\n\t" \
  "v_mfma_f32_16x16x32_bf16 v[24:27], v[208:211], v[154:157], v[24:27]\n\t" \
  "v_lshlrev_b32_sdwa v229, 2, v228 dst_sel:DWORD dst_unused:UNUSED_PAD src0_sel:DWORD src1_sel:BYTE_3\n\t" \
  "v_lshlrev_b32_sdwa v228, 2, v228 dst_sel:DWORD dst_unused:UNUSED_PAD src0_sel:DWORD src1_sel:BYTE_1\n\t" \
  "ds_read_u16 v224, v224 offset:10528\n\t" \
  "v_mfma_f32_16x16x32_bf16 v[48:51], v[208:211], v[162:165], v[48:51]\n\t" \
  "v_lshlrev_b32_sdwa v231, 2, v230 dst_sel:DWORD dst_unused:UNUSED_PAD src0_sel:DWORD src1_sel:BYTE_3\n\t" \
  "v_lshlrev_b32_sdwa v230, 2, v230 dst_sel:DWORD dst_unused:UNUSED_PAD src0_sel:DWORD src1_sel:BYTE_1\n\t" \
  "ds_read_u16_d16_hi v227, v227 offset:10528\n\t" \
  "v_mfma_f32_16x16x32_bf16 v[72:75], v[208:211], v[170:173], v[72:75]\n\t" \
  "s_add_u32 m0, s44, s35\n\t" \
  "v_lshlrev_b32_sdwa v233, 1, v203 dst_sel:DWORD dst_unused:UNUSED_PAD src0_sel:DWORD src1_sel:WORD_1\n\t" \
  "v_lshlrev_b32_sdwa v232, 1, v202 dst_sel:DWORD dst_unused:UNUSED_PAD src0_sel:DWORD src1_sel:WORD_1\n\t" \
  "s_waitcnt lgkmcnt(5)\n\t" \
  "s_mov_b32 exec_hi, 0xffff\n\t" \
  "buffer_load_dwordx4 v252, s[16:19], s34 offen lds\n\t" \
  "v_mfma_f32_16x16x32_bf16 v[4:7], v[212:215], v[146:149], v[4:7]\n\t" \
  "s_mov_b32 exec_hi, -1\n\t" \
  "v_lshlrev_b32_sdwa v235, 1, v201 dst_sel:DWORD dst_unused:UNUSED_PAD src0_sel:DWORD src1_sel:WORD_1\n\t" \
  "s_add_u32 s34, s34, s32\n\t" \
  "ds_read_b64_tr_b16 v[208:209], v249 offset:6224\n\t" \
  "ds_read_u16 v226, v226 offset:10528\n\t" \
  "v_mfma_f32_16x16x32_bf16 v[28:31], v[212:215], v[154:157], v[28:31]\n\t" \
  "v_lshlrev_b32_sdwa v234, 1, v200 dst_sel:DWORD dst_unused:UNUSED_PAD src0_sel:DWORD src1_sel:WORD_1\n\t" \
  "ds_read_b64_tr_b16 v[210:211], v249 offset:7760\n\t" \
  "ds_read_b32 v229, v229 offset:10016\n\t" \
  "v_mfma_f32_16x16x32_bf16 v[52:55], v[212:215], v[162:165], v[52:55]\n\t" \
  "v_fma_mix_f32 v221, |v179|, v221, v221 op_sel:[0,1,0] op_sel_hi:[0,1,1] clamp\n\t" \
  "v_fma_mix_f32 v220, |v178|, v220, v220 op_sel:[0,1,0] op_sel_hi:[0,1,1] clamp\n\t" \
  "ds_read_b32 v228, v228 offset:10016\n\t" \
  "ds_read_b32 v231, v231 offset:10016\n\t" \
  "v_mfma_f32_16x16x32_bf16 v[76:79], v[212:215], v[170:173], v[76:79]\n\t" \
  "v_fma_mix_f32 v223, |v177|, v223, v223 op_sel:[0,1,0] op_sel_hi:[0,1,1] clamp\n\t" \
  "v_fma_mix_f32 v222, |v176|, v222, v222 op_sel:[0,1,0] op_sel_hi:[0,1,1] clamp\n\t" \
  "s_waitcnt lgkmcnt(4)\n\t" \
  "ds_read_b64_tr_b16 v[212:213], v249 offset:6280\n\t" \
  "ds_read_b32 v230, v230 offset:10016\n\t" \
  "v_mfma_f32_16x16x32_bf16 v[8:11], v[216:219], v[146:149], v[8:11]\n\t" \
  "v_or_b32 v187, v224, v225\n\t" \
  "v_or_b32 v186, v226, v227\n\t" \
  "ds_read_b64_tr_b16 v[214:215], v249 offset:7816\n\t" \
  "v_mfma_f32_16x16x32_bf16 v[32:35], v[216:219], v[154:157], v[32:35]\n\t" \
  "v_fma_f32 v179, |v179|, v221, v179 mul:4\n\t" \
  "v_fma_f32 v178, |v178|, v220, v178 mul:4\n\t" \
  "ds_read_u16_d16_hi v233, v233 offset:10528\n\t" \
  "v_mfma_f32_16x16x32_bf16 v[56:59], v[216:219], v[162:165], v[56:59]\n\t" \
  "v_fma_f32 v177, |v177|, v223, v177 mul:4\n\t" \
  "v_fma_f32 v176, |v176|, v222, v176 mul:4\n\t" \
  "s_add_u32 m0, s25, 56880\n\t" \
  "s_add_u32 s24, s24, s26\n\t" \
  "ds_read_u16 v232, v232 offset:10528\n\t" \
  "v_mfma_f32_16x16x32_bf16 v[80:83], v[216:219], v[170:173], v[80:83]\n\t" \
  "v_cvt_pk_bf16_f32 v179, v178, v179\n\t" \
  "v_cvt_pk_bf16_f32 v178, v176, v177\n\t" \
  "s_waitcnt lgkmcnt(8)\n\t" \
  "buffer_load_dwordx4 v254, s[8:11], s24 offen lds\n\t" \
  "ds_read_b64_tr_b16 v[216:217], v249 offset:6288\n\t" \
  "v_mfma_f32_16x16x32_bf16 v[12:15], v[208:211], v[146:149], v[12:15]\n\t" \
  "v_cvt_pknorm_i16_f32 v220, |v180|, |v181|\n\t" \
  "v_cvt_pknorm_i16_f32 v222, |v182|, |v183|\n\t" \
  "ds_read_b64_tr_b16 v[218:219], v249 offset:7824\n\t" \
  "v_mfma_f32_16x16x32_bf16 v[36:39], v[208:211], v[154:157], v[36:39]\n\t" \
  "v_lshlrev_b32_sdwa v221, 2, v220 dst_sel:DWORD dst_unused:UNUSED_PAD src0_sel:DWORD src1_sel:BYTE_3\n\t" \
  "v_lshlrev_b32_sdwa v220, 2, v220 dst_sel:DWORD dst_unused:UNUSED_PAD src0_sel:DWORD src1_sel:BYTE_1\n\t" \
  "ds_read_u16_d16_hi v235, v235 offset:10528\n\t" \
  "v_mfma_f32_16x16x32_bf16 v[60:63], v[208:211], v[162:165], v[60:63]\n\t" \
  "v_lshlrev_b32_sdwa v223, 2, v222 dst_sel:DWORD dst_unused:UNUSED_PAD src0_sel:DWORD src1_sel:BYTE_3\n\t" \
  "v_lshlrev_b32_sdwa v222, 2, v222 dst_sel:DWORD dst_unused:UNUSED_PAD src0_sel:DWORD src1_sel:BYTE_1\n\t" \
  "ds_read_u16 v234, v234 offset:10528\n\t" \
  "v_mfma_f32_16x16x32_bf16 v[84:87], v[208:211], v[170:173], v[84:87]\n\t" \
  "v_lshlrev_b32_sdwa v225, 1, v189 dst_sel:DWORD dst_unused:UNUSED_PAD src0_sel:DWORD src1_sel:WORD_1\n\t" \
  "v_lshlrev_b32_sdwa v224, 1, v188 dst_sel:DWORD dst_unused:UNUSED_PAD src0_sel:DWORD src1_sel:WORD_1\n\t" \
  "ds_read_b64_tr_b16 v[208:209], v248 offset:43048\n\t" \
  "s_waitcnt lgkmcnt(7)\n\t" \
  "v_mfma_f32_16x16x32_bf16 v[16:19], v[212:215], v[146:149], v[16:19]\n\t" \
  "v_lshlrev_b32_sdwa v227, 1, v191 dst_sel:DWORD dst_unused:UNUSED_PAD src0_sel:DWORD src1_sel:WORD_1\n\t" \
  "ds_read_b64_tr_b16 v[210:211], v248 offset:44072\n\t" \
  "ds_read_b32 v221, v221 offset:10016\n\t" \
  "v_mfma_f32_16x16x32_bf16 v[40:43], v[212:215], v[154:157], v[40:43]\n\t" \
  "s_add_u32 s27, s27, s26\n\t" \
  "v_lshlrev_b32_sdwa v226, 1, v190 dst_sel:DWORD dst_unused:UNUSED_PAD src0_sel:DWORD src1_sel:WORD_1\n\t" \
  "v_fma_mix_f32 v229, |v195|, v229, v229 op_sel:[0,1,0] op_sel_hi:[0,1,1] clamp\n\t" \
  "s_mov_b64 exec, s[30:31]\n\t" \
  "buffer_load_dword v237, v254, s[8:11], s27 offen\n\t" \
  "v_mfma_f32_16x16x32_bf16 v[64:67], v[212:215], v[162:165], v[64:67]\n\t" \
  "s_mov_b64 exec, -1\n\t" \
  "v_fma_mix_f32 v228, |v194|, v228, v228 op_sel:[0,1,0] op_sel_hi:[0,1,1] clamp\n\t" \
  "ds_read_b32 v220, v220 offset:10016\n\t" \
  "ds_read_b32 v223, v223 offset:10016\n\t" \
  "v_mfma_f32_16x16x32_bf16 v[88:91], v[212:215], v[170:173], v[88:91]\n\t" \
  "v_fma_mix_f32 v231, |v193|, v231, v231 op_sel:[0,1,0] op_sel_hi:[0,1,1] clamp\n\t" \
  "v_fma_mix_f32 v230, |v192|, v230, v230 op_sel:[0,1,0] op_sel_hi:[0,1,1] clamp\n\t" \
  "s_waitcnt lgkmcnt(5)\n\t" \
  "ds_read_b64_tr_b16 v[212:213], v248 offset:46120\n\t" \
  "ds_read_b32 v222, v222 offset:10016\n\t" \
  "v_mfma_f32_16x16x32_bf16 v[20:23], v[216:219], v[146:149], v[20:23]\n\t" \
  "v_or_b32 v203, v232, v233\n\t" \
  "v_or_b32 v202, v234, v235\n\t" \
  "ds_read_b64_tr_b16 v[214:215], v248 offset:47144\n\t" \
  "v_mfma_f32_16x16x32_bf16 v[44:47], v[216:219], v[154:157], v[44:47]\n\t" \
  "v_fma_f32 v195, |v195|, v229, v195 mul:4\n\t" \
  "v_fma_f32 v194, |v194|, v228, v194 mul:4\n\t" \
  "ds_read_u16_d16_hi v225, v225 offset:10528\n\t" \
  "v_mfma_f32_16x16x32_bf16 v[68:71], v[216:219], v[162:165], v[68:71]\n\t" \
  "v_fma_f32 v193, |v193|, v231, v193 mul:4\n\t" \
  "v_fma_f32 v192, |v192|, v230, v192 mul:4\n\t" \
  "ds_read_u16 v224, v224 offset:10528\n\t" \
  "v_mfma_f32_16x16x32_bf16 v[92:95], v[216:219], v[170:173], v[92:95]\n\t" \
  "v_cvt_pk_bf16_f32 v195, v194, v195\n\t" \
  "v_cvt_pk_bf16_f32 v194, v192, v193\n\t" \
  "ds_read_b64_tr_b16 v[216:217], v248 offset:45096\n\t" \
  "s_waitcnt lgkmcnt(9)\n\t" \
  "v_mfma_f32_16x16x32_bf16 v[144:147], v[208:211], v[96:99], 0\n\t" \
  "v_cvt_pknorm_i16_f32 v228, |v196|, |v197|\n\t" \
  "v_cvt_pknorm_i16_f32 v230, |v198|, |v199|\n\t" \
  "ds_read_b64_tr_b16 v[218:219], v248 offset:48168\n\t" \
  "v_mfma_f32_16x16x32_bf16 v[152:155], v[208:211], v[100:103], 0\n\t" \
  "v_lshlrev_b32_sdwa v229, 2, v228 dst_sel:DWORD dst_unused:UNUSED_PAD src0_sel:DWORD src1_sel:BYTE_3\n\t" \
  "v_lshlrev_b32_sdwa v228, 2, v228 dst_sel:DWORD dst_unused:UNUSED_PAD src0_sel:DWORD src1_sel:BYTE_1\n\t" \
  "ds_read_u16_d16_hi v227, v227 offset:10528\n\t" \
  "v_mfma_f32_16x16x32_bf16 v[160:163], v[208:211], v[104:107], 0\n\t" \
  "v_lshlrev_b32_sdwa v231, 2, v230 dst_sel:DWORD dst_unused:UNUSED_PAD src0_sel:DWORD src1_sel:BYTE_3\n\t" \
  "v_lshlrev_b32_sdwa v230, 2, v230 dst_sel:DWORD dst_unused:UNUSED_PAD src0_sel:DWORD src1_sel:BYTE_1\n\t" \
  "ds_read_u16 v226, v226 offset:10528\n\t" \
  "v_mfma_f32_16x16x32_bf16 v[168:171], v[208:211], v[108:111], 0\n\t" \
  "v_lshlrev_b32_sdwa v233, 1, v205 dst_sel:DWORD dst_unused:UNUSED_PAD src0_sel:DWORD src1_sel:WORD_1\n\t" \
  "v_lshlrev_b32_sdwa v232, 1, v204 dst_sel:DWORD dst_unused:UNUSED_PAD src0_sel:DWORD src1_sel:WORD_1\n\t" \
  "ds_read_b64_tr_b16 v[208:209], v248 offset:43560\n\t" \
  "s_waitcnt lgkmcnt(7)\n\t" \
  "v_mfma_f32_16x16x32_bf16 v[144:147], v[212:215], v[112:115], v[144:147]\n\t" \
  "v_lshlrev_b32_sdwa v235, 1, v207 dst_sel:DWORD dst_unused:UNUSED_PAD src0_sel:DWORD src1_sel:WORD_1\n\t" \
  "ds_read_b64_tr_b16 v[210:211], v248 offset:44584\n\t" \
  "ds_read_b32 v229, v229 offset:10016\n\t" \
  "v_mfma_f32_16x16x32_bf16 v[152:155], v[212:215], v[116:119], v[152:155]\n\t" \
  "v_lshlrev_b32_sdwa v234, 1, v206 dst_sel:DWORD dst_unused:UNUSED_PAD src0_sel:DWORD src1_sel:WORD_1\n\t" \
  "v_fma_mix_f32 v221, |v181|, v221, v221 op_sel:[0,1,0] op_sel_hi:[0,1,1] clamp\n\t" \
  "ds_read_b32 v228, v228 offset:10016\n\t" \
  "v_mfma_f32_16x16x32_bf16 v[160:163], v[212:215], v[120:123], v[160:163]\n\t" \
  "v_fma_mix_f32 v220, |v180|, v220, v220 op_sel:[0,1,0] op_sel_hi:[0,1,1] clamp\n\t" \
  "ds_read_b32 v231, v231 offset:10016\n\t" \
  "ds_read_b32 v230, v230 offset:10016\n\t" \
  "v_mfma_f32_16x16x32_bf16 v[168:171], v[212:215], v[124:127], v[168:171]\n\t" \
  "v_fma_mix_f32 v223, |v183|, v223, v223 op_sel:[0,1,0] op_sel_hi:[0,1,1] clamp\n\t" \
  "v_fma_mix_f32 v222, |v182|, v222, v222 op_sel:[0,1,0] op_sel_hi:[0,1,1] clamp\n\t" \
  "s_waitcnt lgkmcnt(6)\n\t" \
  "ds_read_b64_tr_b16 v[212:213], v248 offset:46632\n\t" \
  "ds_read_u16_d16_hi v233, v233 offset:10528\n\t" \
  "v_mfma_f32_16x16x32_bf16 v[144:147], v[216:219], v[128:131], v[144:147]\n\t" \
  "v_or_b32 v188, v224, v225\n\t" \
  "v_or_b32 v189, v226, v227\n\t" \
  "ds_read_b64_tr_b16 v[214:215], v248 offset:47656\n\t" \
  "v_mfma_f32_16x16x32_bf16 v[152:155], v[216:219], v[132:135], v[152:155]\n\t" \
  "v_fma_f32 v181, |v181|, v221, v181 mul:4\n\t" \
  "v_fma_f32 v180, |v180|, v220, v180 mul:4\n\t" \
  "ds_read_u16 v232, v232 offset:10528\n\t" \
  "v_mfma_f32_16x16x32_bf16 v[160:163], v[216:219], v[136:139], v[160:163]\n\t" \
  "v_fma_f32 v183, |v183|, v223, v183 mul:4\n\t" \
  "v_fma_f32 v182, |v182|, v222, v182 mul:4\n\t" \
  "ds_read_u16_d16_hi v235, v235 offset:10528\n\t" \
  "v_mfma_f32_16x16x32_bf16 v[168:171], v[216:219], v[140:143], v[168:171]\n\t" \
  "v_cvt_pk_bf16_f32 v180, v180, v181\n\t" \
  "v_cvt_pk_bf16_f32 v181, v182, v183\n\t" \
  "ds_read_b64_tr_b16 v[216:217], v248 offset:45608\n\t" \
  "s_waitcnt lgkmcnt(10)\n\t" \
  "v_mfma_f32_16x16x32_bf16 v[148:151], v[208:211], v[96:99], 0\n\t" \
  "v_cvt_pknorm_i16_f32 v220, |v146|, |v147|\n\t" \
  "v_cvt_pknorm_i16_f32 v222, |v144|, |v145|\n\t" \
  "s_waitcnt vmcnt(2)\n\t" \
  "ds_read_b64_tr_b16 v[218:219], v248 offset:48680\n\t" \
  "v_mfma_f32_16x16x32_bf16 v[156:159], v[208:211], v[100:103], 0\n\t" \
  "v_lshlrev_b32_sdwa v221, 2, v220 dst_sel:DWORD dst_unused:UNUSED_PAD src0_sel:DWORD src1_sel:BYTE_3\n\t" \
  "v_lshlrev_b32_sdwa v220, 2, v220 dst_sel:DWORD dst_unused:UNUSED_PAD src0_sel:DWORD src1_sel:BYTE_1\n\t" \
  "s_mov_b64 exec, 3\n\t" \
  "ds_add_rtn_u32 v246, v251, v250 offset:47200\n\t" \
  "v_mfma_f32_16x16x32_bf16 v[164:167], v[208:211], v[104:107], 0\n\t" \
  "s_mov_b64 exec, -1\n\t" \
  "v_lshlrev_b32_sdwa v223, 2, v222 dst_sel:DWORD dst_unused:UNUSED_PAD src0_sel:DWORD src1_sel:BYTE_3\n\t" \
  "v_lshlrev_b32_sdwa v222, 2, v222 dst_sel:DWORD dst_unused:UNUSED_PAD src0_sel:DWORD src1_sel:BYTE_1\n\t" \
  "ds_read_u16 v234, v234 offset:10528\n\t" \
  "v_mfma_f32_16x16x32_bf16 v[172:175], v[208:211], v[108:111], 0\n\t" \
  "s_waitcnt lgkmcnt(1)\n\t" \
  "v_lshlrev_b32_sdwa v225, 1, v155 dst_sel:DWORD dst_unused:UNUSED_PAD src0_sel:DWORD src1_sel:WORD_1\n\t" \
  "v_readfirstlane_b32 s4, v246\n\t" \
  "ds_read_b32 v221, v221 offset:10016\n\t" \
  "v_mfma_f32_16x16x32_bf16 v[148:151], v[212:215], v[112:115], v[148:151]\n\t" \
  "s_cmpk_lt_u32 s4, 3072\n\t" \
  "v_lshlrev_b32_sdwa v224, 1, v154 dst_sel:DWORD dst_unused:UNUSED_PAD src0_sel:DWORD src1_sel:WORD_1\n\t" \
  "s_cbranch_scc1 .Lcnx_w0_9\n.Lcnx_g0_9:\n\t" \
  "ds_read_b32 v220, v220 offset:10016\n\t" \
  "v_mfma_f32_16x16x32_bf16 v[156:159], v[212:215], v[116:119], v[156:159]\n\t" \
  "v_lshlrev_b32_sdwa v227, 1, v153 dst_sel:DWORD dst_unused:UNUSED_PAD src0_sel:DWORD src1_sel:WORD_1\n\t" \
  "v_lshlrev_b32_sdwa v226, 1, v152 dst_sel:DWORD dst_unused:UNUSED_PAD src0_sel:DWORD src1_sel:WORD_1\n\t" \
  "ds_read_b64_tr_b16 v[208:209], v249 offset:12304\n\t" \
  "v_mfma_f32_16x16x32_bf16 v[164:167], v[212:215], v[120:123], v[164:167]\n\t" \
  "v_fma_mix_f32 v229, |v197|, v229, v229 op_sel:[0,1,0] op_sel_hi:[0,1,1] clamp\n\t" \
  "v_fma_mix_f32 v228, |v196|, v228, v228 op_sel:[0,1,0] op_sel_hi:[0,1,1] clamp\n\t" \
  "ds_read_b64_tr_b16 v[210:211], v249 offset:13840\n\t" \
  "ds_read_b32 v223, v223 offset:10016\n\t" \
  "v_mfma_f32_16x16x32_bf16 v[172:175], v[212:215], v[124:127], v[172:175]\n\t" \
  "v_fma_mix_f32 v231, |v199|, v231, v231 op_sel:[0,1,0] op_sel_hi:[0,1,1] clamp\n\t" \
  "v_fma_mix_f32 v230, |v198|, v230, v230 op_sel:[0,1,0] op_sel_hi:[0,1,1] clamp\n\t" \
  "s_waitcnt lgkmcnt(5)\n\t" \
  "ds_read_b64_tr_b16 v[212:213], v249 offset:12312\n\t" \
  "ds_read_b32 v222, v222 offset:10016\n\t" \
  "v_mfma_f32_16x16x32_bf16 v[148:151], v[216:219], v[128:131], v[148:151]\n\t" \
  "s_mov_b32 m0, s45\n\t" \
  "v_or_b32 v204, v232, v233\n\t" \
  "v_or_b32 v205, v234, v235\n\t" \
  "s_mov_b32 exec_hi, 0xffff\n\t" \
  "buffer_load_dwordx4 v252, s[16:19], s33 offen lds\n\t" \
  "v_mfma_f32_16x16x32_bf16 v[156:159], v[216:219], v[132:135], v[156:159]\n\t" \
  "s_mov_b32 exec_hi, -1\n\t" \
  "v_fma_f32 v197, |v197|, v229, v197 mul:4\n\t" \
  "v_fma_f32 v196, |v196|, v228, v196 mul:4\n\t" \
  "s_add_u32 s33, s33, s48\n\t" \
  "ds_read_b64_tr_b16 v[214:215], v249 offset:13848\n\t" \
  "v_mfma_f32_16x16x32_bf16 v[164:167], v[216:219], v[136:139], v[164:167]\n\t" \
  "v_fma_f32 v199, |v199|, v231, v199 mul:4\n\t" \
  "v_fma_f32 v198, |v198|, v230, v198 mul:4\n\t" \
  "ds_read_u16_d16_hi v225, v225 offset:10528\n\t" \
  "v_mfma_f32_16x16x32_bf16 v[172:175], v[216:219], v[140:143], v[172:175]\n\t" \
  "v_cvt_pk_bf16_f32 v196, v196, v197\n\t" \
  "v_cvt_pk_bf16_f32 v197, v198, v199\n\t" \
  "ds_read_b64_tr_b16 v[216:217], v249 offset:12368\n\t" \
  "s_waitcnt lgkmcnt(6)\n\t" \
  "v_mfma_f32_16x16x32_bf16 v[0:3], v[208:211], v[178:181], v[0:3]\n\t" \
  "v_cvt_pknorm_i16_f32 v228, |v162|, |v163|\n\t" \
  "v_cvt_pknorm_i16_f32 v230, |v160|, |v161|\n\t" \
  "ds_read_b64_tr_b16 v[218:219], v249 offset:13904\n\t" \
  "v_mfma_f32_16x16x32_bf16 v[24:27], v[208:211], v[186:189], v[24:27]\n\t" \
  "v_lshlrev_b32_sdwa v229, 2, v228 dst_sel:DWORD dst_unused:UNUSED_PAD src0_sel:DWORD src1_sel:BYTE_3\n\t" \
  "v_lshlrev_b32_sdwa v228, 2, v228 dst_sel:DWORD dst_unused:UNUSED_PAD src0_sel:DWORD src1_sel:BYTE_1\n\t" \
  "ds_read_u16 v224, v224 offset:10528\n\t" \
  "v_mfma_f32_16x16x32_bf16 v[48:51], v[208:211], v[194:197], v[48:51]\n\t" \
  "v_lshlrev_b32_sdwa v231, 2, v230 dst_sel:DWORD dst_unused:UNUSED_PAD src0_sel:DWORD src1_sel:BYTE_3\n\t" \
  "v_lshlrev_b32_sdwa v230, 2, v230 dst_sel:DWORD dst_unused:UNUSED_PAD src0_sel:DWORD src1_sel:BYTE_1\n\t" \
  "ds_read_u16_d16_hi v227, v227 offset:10528\n\t" \
  "v_mfma_f32_16x16x32_bf16 v[72:75], v[208:211], v[202:205], v[72:75]\n\t" \
  "s_add_u32 m0, s45, s35\n\t" \
  "v_lshlrev_b32_sdwa v233, 1, v171 dst_sel:DWORD dst_unused:UNUSED_PAD src0_sel:DWORD src1_sel:WORD_1\n\t" \
  "v_lshlrev_b32_sdwa v232, 1, v170 dst_sel:DWORD dst_unused:UNUSED_PAD src0_sel:DWORD src1_sel:WORD_1\n\t" \
  "s_waitcnt lgkmcnt(5)\n\t" \
  "s_mov_b32 exec_hi, 0xffff\n\t" \
  "buffer_load_dwordx4 v252, s[16:19], s34 offen lds\n\t" \
  "v_mfma_f32_16x16x32_bf16 v[4:7], v[212:215], v[178:181], v[4:7]\n\t" \
  "s_mov_b32 exec_hi, -1\n\t" \
  "v_lshlrev_b32_sdwa v235, 1, v169 dst_sel:DWORD dst_unused:UNUSED_PAD src0_sel:DWORD src1_sel:WORD_1\n\t" \
  "s_add_u32 s34, s34, s48\n\t" \
  "ds_read_b64_tr_b16 v[208:209], v249 offset:12376\n\t" \
  "ds_read_u16 v226, v226 offset:10528\n\t" \
  "v_mfma_f32_16x16x32_bf16 v[28:31], v[212:215], v[186:189], v[28:31]\n\t" \
  "v_lshlrev_b32_sdwa v234, 1, v168 dst_sel:DWORD dst_unused:UNUSED_PAD src0_sel:DWORD src1_sel:WORD_1\n\t" \
  "ds_read_b64_tr_b16 v[210:211], v249 offset:13912\n\t" \
  "ds_read_b32 v229, v229 offset:10016\n\t" \
  "v_mfma_f32_16x16x32_bf16 v[52:55], v[212:215], v[194:197], v[52:55]\n\t" \
  "v_fma_mix_f32 v221, |v147|, v221, v221 op_sel:[0,1,0] op_sel_hi:[0,1,1] clamp\n\t" \
  "v_fma_mix_f32 v220, |v146|, v220, v220 op_sel:[0,1,0] op_sel_hi:[0,1,1] clamp\n\t" \
  "ds_read_b32 v228, v228 offset:10016\n\t" \
  "ds_read_b32 v231, v231 offset:10016\n\t" \
  "v_mfma_f32_16x16x32_bf16 v[76:79], v[212:215], v[202:205], v[76:79]\n\t" \
  "v_fma_mix_f32 v223, |v145|, v223, v223 op_sel:[0,1,0] op_sel_hi:[0,1,1] clamp\n\t" \
  "v_fma_mix_f32 v222, |v144|, v222, v222 op_sel:[0,1,0] op_sel_hi:[0,1,1] clamp\n\t" \
  "s_waitcnt lgkmcnt(4)\n\t" \
  "ds_read_b64_tr_b16 v[212:213], v249 offset:12432\n\t" \
  "ds_read_b32 v230, v230 offset:10016\n\t" \
  "v_mfma_f32_16x16x32_bf16 v[8:11], v[216:219], v[178:181], v[8:11]\n\t" \
  "v_or_b32 v155, v224, v225\n\t" \
  "v_or_b32 v154, v226, v227\n\t" \
  "ds_read_b64_tr_b16 v[214:215], v249 offset:13968\n\t" \
  "v_mfma_f32_16x16x32_bf16 v[32:35], v[216:219], v[186:189], v[32:35]\n\t" \
  "v_fma_f32 v147, |v147|, v221, v147 mul:4\n\t" \
  "v_fma_f32 v146, |v146|, v220, v146 mul:4\n\t" \
  "ds_read_u16_d16_hi v233, v233 offset:10528\n\t" \
  "v_mfma_f32_16x16x32_bf16 v[56:59], v[216:219], v[194:197], v[56:59]\n\t" \
  "v_fma_f32 v145, |v145|, v223, v145 mul:4\n\t" \
  "v_fma_f32 v144, |v144|, v222, v144 mul:4\n\t" \
  "s_add_u32 m0, s25, 65080\n\t" \
  "s_add_u32 s24, s24, s26\n\t" \
  "ds_read_u16 v232, v232 offset:10528\n\t" \
  "v_mfma_f32_16x16x32_bf16 v[80:83], v[216:219], v[202:205], v[80:83]\n\t" \
  "v_cvt_pk_bf16_f32 v147, v146, v147\n\t" \
  "v_cvt_pk_bf16_f32 v146, v144, v145\n\t" \
  "s_waitcnt lgkmcnt(8)\n\t" \
  "buffer_load_dwordx4 v254, s[8:11], s24 offen lds\n\t" \
  "ds_read_b64_tr_b16 v[216:217], v249 offset:12440\n\t" \
  "v_mfma_f32_16x16x32_bf16 v[12:15], v[208:211], v[178:181], v[12:15]\n\t" \
  "v_cvt_pknorm_i16_f32 v220, |v148|, |v149|\n\t" \
  "v_cvt_pknorm_i16_f32 v222, |v150|, |v151|\n\t" \
  "ds_read_b64_tr_b16 v[218:219], v249 offset:13976\n\t" \
  "v_mfma_f32_16x16x32_bf16 v[36:39], v[208:211], v[186:189], v[36:39]\n\t" \
  "v_lshlrev_b32_sdwa v221, 2, v220 dst_sel:DWORD dst_unused:UNUSED_PAD src0_sel:DWORD src1_sel:BYTE_3\n\t" \
  "v_lshlrev_b32_sdwa v220, 2, v220 dst_sel:DWORD dst_unused:UNUSED_PAD src0_sel:DWORD src1_sel:BYTE_1\n\t" \
  "ds_read_u16_d16_hi v235, v235 offset:10528\n\t" \
  "v_mfma_f32_16x16x32_bf16 v[60:63], v[208:211], v[194:197], v[60:63]\n\t" \
  "v_lshlrev_b32_sdwa v223, 2, v222 dst_sel:DWORD dst_unused:UNUSED_PAD src0_sel:DWORD src1_sel:BYTE_3\n\t" \
  "v_lshlrev_b32_sdwa v222, 2, v222 dst_sel:DWORD dst_unused:UNUSED_PAD src0_sel:DWORD src1_sel:BYTE_1\n\t" \
  "ds_read_u16 v234, v234 offset:10528\n\t" \
  "v_mfma_f32_16x16x32_bf16 v[84:87], v[208:211], v[202:205], v[84:87]\n\t" \
  "v_lshlrev_b32_sdwa v225, 1, v157 dst_sel:DWORD dst_unused:UNUSED_PAD src0_sel:DWORD src1_sel:WORD_1\n\t" \
  "v_lshlrev_b32_sdwa v224, 1, v156 dst_sel:DWORD dst_unused:UNUSED_PAD src0_sel:DWORD src1_sel:WORD_1\n\t" \
  "ds_read_b64_tr_b16 v[208:209], v248 offset:49192\n\t" \
  "s_waitcnt lgkmcnt(7)\n\t" \
  "v_mfma_f32_16x16x32_bf16 v[16:19], v[212:215], v[178:181], v[16:19]\n\t" \
  "v_lshlrev_b32_sdwa v227, 1, v159 dst_sel:DWORD dst_unused:UNUSED_PAD src0_sel:DWORD src1_sel:WORD_1\n\t" \
  "ds_read_b64_tr_b16 v[210:211], v248 offset:50216\n\t" \
  "ds_read_b32 v221, v221 offset:10016\n\t" \
  "v_mfma_f32_16x16x32_bf16 v[40:43], v[212:215], v[186:189], v[40:43]\n\t" \
  "s_add_u32 s27, s27, s26\n\t" \
  "v_lshlrev_b32_sdwa v226, 1, v158 dst_sel:DWORD dst_unused:UNUSED_PAD src0_sel:DWORD src1_sel:WORD_1\n\t" \
  "v_fma_mix_f32 v229, |v163|, v229, v229 op_sel:[0,1,0] op_sel_hi:[0,1,1] clamp\n\t" \
  "s_mov_b64 exec, s[30:31]\n\t" \
  "buffer_load_dword v237, v254, s[8:11], s27 offen\n\t" \
  "v_mfma_f32_16x16x32_bf16 v[64:67], v[212:215], v[194:197], v[64:67]\n\t" \
  "s_mov_b64 exec, -1\n\t" \
  "v_fma_mix_f32 v228, |v162|, v228, v228 op_sel:[0,1,0] op_sel_hi:[0,1,1] clamp\n\t" \
  "ds_read_b32 v220, v220 offset:10016\n\t" \
  "ds_read_b32 v223, v223 offset:10016\n\t" \
  "v_mfma_f32_16x16x32_bf16 v[88:91], v[212:215], v[202:205], v[88:91]\n\t" \
  "v_fma_mix_f32 v231, |v161|, v231, v231 op_sel:[0,1,0] op_sel_hi:[0,1,1] clamp\n\t" \
  "v_fma_mix_f32 v230, |v160|, v230, v230 op_sel:[0,1,0] op_sel_hi:[0,1,1] clamp\n\t" \
  "s_waitcnt lgkmcnt(5)\n\t" \
  "ds_read_b64_tr_b16 v[212:213], v248 offset:52264\n\t" \
  "ds_read_b32 v222, v222 offset:10016\n\t" \
  "v_mfma_f32_16x16x32_bf16 v[20:23], v[216:219], v[178:181], v[20:23]\n\t" \
  "v_or_b32 v171, v232, v233\n\t" \
  "v_or_b32 v170, v234, v235\n\t" \
  "ds_read_b64_tr_b16 v[214:215], v248 offset:53288\n\t" \
  "v_mfma_f32_16x16x32_bf16 v[44:47], v[216:219], v[186:189], v[44:47]\n\t" \
  "v_fma_f32 v163, |v163|, v229, v163 mul:4\n\t" \
  "v_fma_f32 v162, |v162|, v228, v162 mul:4\n\t" \
  "ds_read_u16_d16_hi v225, v225 offset:10528\n\t" \
  "v_mfma_f32_16x16x32_bf16 v[68:71], v[216:219], v[194:197], v[68:71]\n\t" \
  "v_fma_f32 v161, |v161|, v231, v161 mul:4\n\t" \
  "v_fma_f32 v160, |v160|, v230, v160 mul:4\n\t" \
  "ds_read_u16 v224, v224 offset:10528\n\t" \
  "v_mfma_f32_16x16x32_bf16 v[92:95], v[216:219], v[202:205], v[92:95]\n\t" \
  "v_cvt_pk_bf16_f32 v163, v162, v163\n\t" \
  "v_cvt_pk_bf16_f32 v162, v160, v161\n\t" \
  "ds_read_b64_tr_b16 v[216:217], v248 offset:51240\n\t" \
  "s_waitcnt lgkmcnt(9)\n\t" \
  "v_mfma_f32_16x16x32_bf16 v[176:179], v[208:211], v[96:99], 0\n\t" \
  "v_cvt_pknorm_i16_f32 v228, |v164|, |v165|\n\t" \
  "v_cvt_pknorm_i16_f32 v230, |v166|, |v167|\n\t" \
  "ds_read_b64_tr_b16 v[218:219], v248 offset:54312\n\t" \
  "v_mfma_f32_16x16x32_bf16 v[184:187], v[208:211], v[100:103], 0\n\t" \
  "v_lshlrev_b32_sdwa v229, 2, v228 dst_sel:DWORD dst_unused:UNUSED_PAD src0_sel:DWORD src1_sel:BYTE_3\n\t" \
  "v_lshlrev_b32_sdwa v228, 2, v228 dst_sel:DWORD dst_unused:UNUSED_PAD src0_sel:DWORD src1_sel:BYTE_1\n\t" \
  "ds_read_u16_d16_hi v227, v227 offset:10528\n\t" \
  "v_mfma_f32_16x16x32_bf16 v[192:195], v[208:211], v[104:107], 0\n\t" \
  "v_lshlrev_b32_sdwa v231, 2, v230 dst_sel:DWORD dst_unused:UNUSED_PAD src0_sel:DWORD src1_sel:BYTE_3\n\t" \
  "v_lshlrev_b32_sdwa v230, 2, v230 dst_sel:DWORD dst_unused:UNUSED_PAD src0_sel:DWORD src1_sel:BYTE_1\n\t" \
  "ds_read_u16 v226, v226 offset:10528\n\t" \
  "v_mfma_f32_16x16x32_bf16 v[200:203], v[208:211], v[108:111], 0\n\t" \
  "v_lshlrev_b32_sdwa v233, 1, v173 dst_sel:DWORD dst_unused:UNUSED_PAD src0_sel:DWORD src1_sel:WORD_1\n\t" \
  "v_lshlrev_b32_sdwa v232, 1, v172 dst_sel:DWORD dst_unused:UNUSED_PAD src0_sel:DWORD src1_sel:WORD_1\n\t" \
  "ds_read_b64_tr_b16 v[208:209], v248 offset:49704\n\t" \
  "s_waitcnt lgkmcnt(7)\n\t" \
  "v_mfma_f32_16x16x32_bf16 v[176:179], v[212:215], v[112:115], v[176:179]\n\t" \
  "v_lshlrev_b32_sdwa v235, 1, v175 dst_sel:DWORD dst_unused:UNUSED_PAD src0_sel:DWORD src1_sel:WORD_1\n\t" \
  "ds_read_b64_tr_b16 v[210:211], v248 offset:50728\n\t" \
  "ds_read_b32 v229, v229 offset:10016\n\t" \
  "v_mfma_f32_16x16x32_bf16 v[184:187], v[212:215], v[116:119], v[184:187]\n\t" \
  "v_lshlrev_b32_sdwa v234, 1, v174 dst_sel:DWORD dst_unused:UNUSED_PAD src0_sel:DWORD src1_sel:WORD_1\n\t" \
  "v_fma_mix_f32 v221, |v149|, v221, v221 op_sel:[0,1,0] op_sel_hi:[0,1,1] clamp\n\t" \
  "ds_read_b32 v228, v228 offset:10016\n\t" \
  "v_mfma_f32_16x16x32_bf16 v[192:195], v[212:215], v[120:123], v[192:195]\n\t" \
  "v_fma_mix_f32 v220, |v148|, v220, v220 op_sel:[0,1,0] op_sel_hi:[0,1,1] clamp\n\t" \
  "ds_read_b32 v231, v231 offset:10016\n\t" \
  "ds_read_b32 v230, v230 offset:10016\n\t" \
  "v_mfma_f32_16x16x32_bf16 v[200:203], v[212:215], v[124:127], v[200:203]\n\t" \
  "v_fma_mix_f32 v223, |v151|, v223, v223 op_sel:[0,1,0] op_sel_hi:[0,1,1] clamp\n\t" \
  "v_fma_mix_f32 v222, |v150|, v222, v222 op_sel:[0,1,0] op_sel_hi:[0,1,1] clamp\n\t" \
  "s_waitcnt lgkmcnt(6)\n\t" \
  "ds_read_b64_tr_b16 v[212:213], v248 offset:52776\n\t" \
  "ds_read_u16_d16_hi v233, v233 offset:10528\n\t" \
  "v_mfma_f32_16x16x32_bf16 v[176:179], v[216:219], v[128:131], v[176:179]\n\t" \
  "v_or_b32 v156, v224, v225\n\t" \
  "v_or_b32 v157, v226, v227\n\t" \
  "ds_read_b64_tr_b16 v[214:215], v248 offset:53800\n\t" \
  "v_mfma_f32_16x16x32_bf16 v[184:187], v[216:219], v[132:135], v[184:187]\n\t" \
  "v_fma_f32 v149, |v149|, v221, v149 mul:4\n\t" \
  "v_fma_f32 v148, |v148|, v220, v148 mul:4\n\t" \
  "ds_read_u16 v232, v232 offset:10528\n\t" \
  "v_mfma_f32_16x16x32_bf16 v[192:195], v[216:219], v[136:139], v[192:195]\n\t" \
  "v_fma_f32 v151, |v151|, v223, v151 mul:4\n\t" \
  "v_fma_f32 v150, |v150|, v222, v150 mul:4\n\t" \
  "ds_read_u16_d16_hi v235, v235 offset:10528\n\t" \
  "v_mfma_f32_16x16x32_bf16 v[200:203], v[216:219], v[140:143], v[200:203]\n\t" \
  "v_cvt_pk_bf16_f32 v148, v148, v149\n\t" \
  "v_cvt_pk_bf16_f32 v149, v150, v151\n\t" \
  "ds_read_b64_tr_b16 v[216:217], v248 offset:51752\n\t" \
  "s_waitcnt lgkmcnt(10)\n\t" \
  "v_mfma_f32_16x16x32_bf16 v[180:183], v[208:211], v[96:99], 0\n\t" \
  "v_cvt_pknorm_i16_f32 v220, |v178|, |v179|\n\t" \
  "v_cvt_pknorm_i16_f32 v222, |v176|, |v177|\n\t" \
  "s_waitcnt vmcnt(2)\n\t" \
  "ds_read_b64_tr_b16 v[218:219], v248 offset:54824\n\t" \
  "v_mfma_f32_16x16x32_bf16 v[188:191], v[208:211], v[100:103], 0\n\t" \
  "v_lshlrev_b32_sdwa v221, 2, v220 dst_sel:DWORD dst_unused:UNUSED_PAD src0_sel:DWORD src1_sel:BYTE_3\n\t" \
  "v_lshlrev_b32_sdwa v220, 2, v220 dst_sel:DWORD dst_unused:UNUSED_PAD src0_sel:DWORD src1_sel:BYTE_1\n\t" \
  "s_mov_b64 exec, 3\n\t" \
  "ds_add_rtn_u32 v246, v251, v250 offset:47208\n\t" \
  "v_mfma_f32_16x16x32_bf16 v[196:199], v[208:211], v[104:107], 0\n\t" \
  "s_mov_b64 exec, -1\n\t" \
  "v_lshlrev_b32_sdwa v223, 2, v222 dst_sel:DWORD dst_unused:UNUSED_PAD src0_sel:DWORD src1_sel:BYTE_3\n\t" \
  "v_lshlrev_b32_sdwa v222, 2, v222 dst_sel:DWORD dst_unused:UNUSED_PAD src0_sel:DWORD src1_sel:BYTE_1\n\t" \
  "ds_read_u16 v234, v234 offset:10528\n\t" \
  "v_mfma_f32_16x16x32_bf16 v[204:207], v[208:211], v[108:111], 0\n\t" \
  "s_waitcnt lgkmcnt(1)\n\t" \
  "v_lshlrev_b32_sdwa v225, 1, v187 dst_sel:DWORD dst_unused:UNUSED_PAD src0_sel:DWORD src1_sel:WORD_1\n\t" \
  "v_readfirstlane_b32 s4, v246\n\t" \
  "ds_read_b32 v221, v221 offset:10016\n\t" \
  "v_mfma_f32_16x16x32_bf16 v[180:183], v[212:215], v[112:115], v[180:183]\n\t" \
  "s_cmpk_lt_u32 s4, 3072\n\t" \
  "v_lshlrev_b32_sdwa v224, 1, v186 dst_sel:DWORD dst_unused:UNUSED_PAD src0_sel:DWORD src1_sel:WORD_1\n\t" \
  "s_cbranch_scc1 .Lcnx_w0_10\n.Lcnx_g0_10:\n\t" \
  "ds_read_b32 v220, v220 offset:10016\n\t" \
  "v_mfma_f32_16x16x32_bf16 v[188:191], v[212:215], v[116:119], v[188:191]\n\t" \
  "v_lshlrev_b32_sdwa v227, 1, v185 dst_sel:DWORD dst_unused:UNUSED_PAD src0_sel:DWORD src1_sel:WORD_1\n\t" \
  "v_lshlrev_b32_sdwa v226, 1, v184 dst_sel:DWORD dst_unused:UNUSED_PAD src0_sel:DWORD src1_sel:WORD_1\n\t" \
  "ds_read_b64_tr_b16 v[208:209], v249 offset:24600\n\t" \
  "v_mfma_f32_16x16x32_bf16 v[196:199], v[212:215], v[120:123], v[196:199]\n\t" \
  "v_fma_mix_f32 v229, |v165|, v229, v229 op_sel:[0,1,0] op_sel_hi:[0,1,1] clamp\n\t" \
  "v_fma_mix_f32 v228, |v164|, v228, v228 op_sel:[0,1,0] op_sel_hi:[0,1,1] clamp\n\t" \
  "ds_read_b64_tr_b16 v[210:211], v249 offset:26136\n\t" \
  "ds_read_b32 v223, v223 offset:10016\n\t" \
  "v_mfma_f32_16x16x32_bf16 v[204:207], v[212:215], v[124:127], v[204:207]\n\t" \
  "v_fma_mix_f32 v231, |v167|, v231, v231 op_sel:[0,1,0] op_sel_hi:[0,1,1] clamp\n\t" \
  "v_fma_mix_f32 v230, |v166|, v230, v230 op_sel:[0,1,0] op_sel_hi:[0,1,1] clamp\n\t" \
  "s_waitcnt lgkmcnt(5)\n\t" \
  "ds_read_b64_tr_b16 v[212:213], v249 offset:24608\n\t" \
  "ds_read_b32 v222, v222 offset:10016\n\t" \
  "v_mfma_f32_16x16x32_bf16 v[180:183], v[216:219], v[128:131], v[180:183]\n\t" \
  "s_mov_b32 m0, s46\n\t" \
  "v_or_b32 v172, v232, v233\n\t" \
  "v_or_b32 v173, v234, v235\n\t" \
  "s_mov_b32 exec_hi, 0xffff\n\t" \
  "buffer_load_dwordx4 v252, s[16:19], s33 offen lds\n\t" \
  "v_mfma_f32_16x16x32_bf16 v[188:191], v[216:219], v[132:135], v[188:191]\n\t" \
  "s_mov_b32 exec_hi, -1\n\t" \
  "v_fma_f32 v165, |v165|, v229, v165 mul:4\n\t" \
  "v_fma_f32 v164, |v164|, v228, v164 mul:4\n\t" \
  "s_add_u32 s33, s33, s32\n\t" \
  "ds_read_b64_tr_b16 v[214:215], v249 offset:26144\n\t" \
  "v_mfma_f32_16x16x32_bf16 v[196:199], v[216:219], v[136:139], v[196:199]\n\t" \
  "v_fma_f32 v167, |v167|, v231, v167 mul:4\n\t" \
  "v_fma_f32 v166, |v166|, v230, v166 mul:4\n\t" \
  "ds_read_u16_d16_hi v225, v225 offset:10528\n\t" \
  "v_mfma_f32_16x16x32_bf16 v[204:207], v[216:219], v[140:143], v[204:207]\n\t" \
  "v_cvt_pk_bf16_f32 v164, v164, v165\n\t" \
  "v_cvt_pk_bf16_f32 v165, v166, v167\n\t" \
  "ds_read_b64_tr_b16 v[216:217], v249 offset:24664\n\t" \
  "s_waitcnt lgkmcnt(6)\n\t" \
  "v_mfma_f32_16x16x32_bf16 v[0:3], v[208:211], v[146:149], v[0:3]\n\t" \
  "v_cvt_pknorm_i16_f32 v228, |v194|, |v195|\n\t" \
  "v_cvt_pknorm_i16_f32 v230, |v192|, |v193|\n\t" \
  "ds_read_b64_tr_b16 v[218:219], v249 offset:26200\n\t" \
  "v_mfma_f32_16x16x32_bf16 v[24:27], v[208:211], v[154:157], v[24:27]\n\t" \
  "v_lshlrev_b32_sdwa v229, 2, v228 dst_sel:DWORD dst_unused:UNUSED_PAD src0_sel:DWORD src1_sel:BYTE_3\n\t" \
  "v_lshlrev_b32_sdwa v228, 2, v228 dst_sel:DWORD dst_unused:UNUSED_PAD src0_sel:DWORD src1_sel:BYTE_1\n\t" \
  "ds_read_u16 v224, v224 offset:10528\n\t" \
  "v_mfma_f32_16x16x32_bf16 v[48:51], v[208:211], v[162:165], v[48:51]\n\t" \
  "v_lshlrev_b32_sdwa v231, 2, v230 dst_sel:DWORD dst_unused:UNUSED_PAD src0_sel:DWORD src1_sel:BYTE_3\n\t" \
  "v_lshlrev_b32_sdwa v230, 2, v230 dst_sel:DWORD dst_unused:UNUSED_PAD src0_sel:DWORD src1_sel:BYTE_1\n\t" \
  "ds_read_u16_d16_hi v227, v227 offset:10528\n\t" \
  "v_mfma_f32_16x16x32_bf16 v[72:75], v[208:211], v[170:173], v[72:75]\n\t" \
  "s_add_u32 m0, s46, s35\n\t" \
  "v_lshlrev_b32_sdwa v233, 1, v203 dst_sel:DWORD dst_unused:UNUSED_PAD src0_sel:DWORD src1_sel:WORD_1\n\t" \
  "v_lshlrev_b32_sdwa v232, 1, v202 dst_sel:DWORD dst_unused:UNUSED_PAD src0_sel:DWORD src1_sel:WORD_1\n\t" \
  "s_waitcnt lgkmcnt(5)\n\t" \
  "s_mov_b32 exec_hi, 0xffff\n\t" \
  "buffer_load_dwordx4 v252, s[16:19], s34 offen lds\n\t" \
  "v_mfma_f32_16x16x32_bf16 v[4:7], v[212:215], v[146:149], v[4:7]\n\t" \
  "s_mov_b32 exec_hi, -1\n\t" \
  "v_lshlrev_b32_sdwa v235, 1, v201 dst_sel:DWORD dst_unused:UNUSED_PAD src0_sel:DWORD src1_sel:WORD_1\n\t" \
  "s_add_u32 s34, s34, s32\n\t" \
  "ds_read_b64_tr_b16 v[208:209], v249 offset:24672\n\t" \
  "ds_read_u16 v226, v226 offset:10528\n\t" \
  "v_mfma_f32_16x16x32_bf16 v[28:31], v[212:215], v[154:157], v[28:31]\n\t" \
  "v_lshlrev_b32_sdwa v234, 1, v200 dst_sel:DWORD dst_unused:UNUSED_PAD src0_sel:DWORD src1_sel:WORD_1\n\t" \
  "ds_read_b64_tr_b16 v[210:211], v249 offset:26208\n\t" \
  "ds_read_b32 v229, v229 offset:10016\n\t" \
  "v_mfma_f32_16x16x32_bf16 v[52:55], v[212:215], v[162:165], v[52:55]\n\t" \
  "v_fma_mix_f32 v221, |v179|, v221, v221 op_sel:[0,1,0] op_sel_hi:[0,1,1] clamp\n\t" \
  "v_fma_mix_f32 v220, |v178|, v220, v220 op_sel:[0,1,0] op_sel_hi:[0,1,1] clamp\n\t" \
  "ds_read_b32 v228, v228 offset:10016\n\t" \
  "ds_read_b32 v231, v231 offset:10016\n\t" \
  "v_mfma_f32_16x16x32_bf16 v[76:79], v[212:215], v[170:173], v[76:79]\n\t" \
  "v_fma_mix_f32 v223, |v177|, v223, v223 op_sel:[0,1,0] op_sel_hi:[0,1,1] clamp\n\t" \
  "v_fma_mix_f32 v222, |v176|, v222, v222 op_sel:[0,1,0] op_sel_hi:[0,1,1] clamp\n\t" \
  "s_waitcnt lgkmcnt(4)\n\t" \
  "ds_read_b64_tr_b16 v[212:213], v249 offset:24728\n\t" \
  "ds_read_b32 v230, v230 offset:10016\n\t" \
  "v_mfma_f32_16x16x32_bf16 v[8:11], v[216:219], v[146:149], v[8:11]\n\t" \
  "v_or_b32 v187, v224, v225\n\t" \
  "v_or_b32 v186, v226, v227\n\t" \
  "ds_read_b64_tr_b16 v[214:215], v249 offset:26264\n\t" \
  "v_mfma_f32_16x16x32_bf16 v[32:35], v[216:219], v[154:157], v[32:35]\n\t" \
  "v_fma_f32 v179, |v179|, v221, v179 mul:4\n\t" \
  "v_fma_f32 v178, |v178|, v220, v178 mul:4\n\t" \
  "ds_read_u16_d16_hi v233, v233 offset:10528\n\t" \
  "v_mfma_f32_16x16x32_bf16 v[56:59], v[216:219], v[162:165], v[56:59]\n\t" \
  "v_fma_f32 v177, |v177|, v223, v177 mul:4\n\t" \
  "v_fma_f32 v176, |v176|, v222, v176 mul:4\n\t" \
  "s_add_u32 m0, s25, 73280\n\t" \
  "s_add_u32 s24, s24, s26\n\t" \
  "ds_read_u16 v232, v232 offset:10528\n\t" \
  "v_mfma_f32_16x16x32_bf16 v[80:83], v[216:219], v[170:173], v[80:83]\n\t" \
  "v_cvt_pk_bf16_f32 v179, v178, v179\n\t" \
  "v_cvt_pk_bf16_f32 v178, v176, v177\n\t" \
  "s_waitcnt lgkmcnt(8)\n\t" \
  "buffer_load_dwordx4 v254, s[8:11], s24 offen lds\n\t" \
  "ds_read_b64_tr_b16 v[216:217], v249 offset:24736\n\t" \
  "v_mfma_f32_16x16x32_bf16 v[12:15], v[208:211], v[146:149], v[12:15]\n\t" \
  "v_cvt_pknorm_i16_f32 v220, |v180|, |v181|\n\t" \
  "v_cvt_pknorm_i16_f32 v222, |v182|, |v183|\n\t" \
  "ds_read_b64_tr_b16 v[218:219], v249 offset:26272\n\t" \
  "v_mfma_f32_16x16x32_bf16 v[36:39], v[208:211], v[154:157], v[36:39]\n\t" \
  "v_lshlrev_b32_sdwa v221, 2, v220 dst_sel:DWORD dst_unused:UNUSED_PAD src0_sel:DWORD src1_sel:BYTE_3\n\t" \
  "v_lshlrev_b32_sdwa v220, 2, v220 dst_sel:DWORD dst_unused:UNUSED_PAD src0_sel:DWORD src1_sel:BYTE_1\n\t" \
  "ds_read_u16_d16_hi v235, v235 offset:10528\n\t" \
  "v_mfma_f32_16x16x32_bf16 v[60:63], v[208:211], v[162:165], v[60:63]\n\t" \
  "v_lshlrev_b32_sdwa v223, 2, v222 dst_sel:DWORD dst_unused:UNUSED_PAD src0_sel:DWORD src1_sel:BYTE_3\n\t" \
  "v_lshlrev_b32_sdwa v222, 2, v222 dst_sel:DWORD dst_unused:UNUSED_PAD src0_sel:DWORD src1_sel:BYTE_1\n\t" \
  "ds_read_u16 v234, v234 offset:10528\n\t" \
  "v_mfma_f32_16x16x32_bf16 v[84:87], v[208:211], v[170:173], v[84:87]\n\t" \
  "v_lshlrev_b32_sdwa v225, 1, v189 dst_sel:DWORD dst_unused:UNUSED_PAD src0_sel:DWORD src1_sel:WORD_1\n\t" \
  "v_lshlrev_b32_sdwa v224, 1, v188 dst_sel:DWORD dst_unused:UNUSED_PAD src0_sel:DWORD src1_sel:WORD_1\n\t" \
  "ds_read_b64_tr_b16 v[208:209], v248 offset:36904\n\t" \
  "s_waitcnt lgkmcnt(7)\n\t" \
  "v_mfma_f32_16x16x32_bf16 v[16:19], v[212:215], v[146:149], v[16:19]\n\t" \
  "v_lshlrev_b32_sdwa v227, 1, v191 dst_sel:DWORD dst_unused:UNUSED_PAD src0_sel:DWORD src1_sel:WORD_1\n\t" \
  "ds_read_b64_tr_b16 v[210:211], v248 offset:37928\n\t" \
  "ds_read_b32 v221, v221 offset:10016\n\t" \
  "v_mfma_f32_16x16x32_bf16 v[40:43], v[212:215], v[154:157], v[40:43]\n\t" \
  "s_add_u32 s27, s27, s26\n\t" \
  "v_lshlrev_b32_sdwa v226, 1, v190 dst_sel:DWORD dst_unused:UNUSED_PAD src0_sel:DWORD src1_sel:WORD_1\n\t" \
  "v_fma_mix_f32 v229, |v195|, v229, v229 op_sel:[0,1,0] op_sel_hi:[0,1,1] clamp\n\t" \
  "s_mov_b64 exec, s[30:31]\n\t" \
  "buffer_load_dword v237, v254, s[8:11], s27 offen\n\t" \
  "v_mfma_f32_16x16x32_bf16 v[64:67], v[212:215], v[162:165], v[64:67]\n\t" \
  "s_mov_b64 exec, -1\n\t" \
  "v_fma_mix_f32 v228, |v194|, v228, v228 op_sel:[0,1,0] op_sel_hi:[0,1,1] clamp\n\t" \
  "ds_read_b32 v220, v220 offset:10016\n\t" \
  "ds_read_b32 v223, v223 offset:10016\n\t" \
  "v_mfma_f32_16x16x32_bf16 v[88:91], v[212:215], v[170:173], v[88:91]\n\t" \
  "v_fma_mix_f32 v231, |v193|, v231, v231 op_sel:[0,1,0] op_sel_hi:[0,1,1] clamp\n\t" \
  "v_fma_mix_f32 v230, |v192|, v230, v230 op_sel:[0,1,0] op_sel_hi:[0,1,1] clamp\n\t" \
  "s_waitcnt lgkmcnt(5)\n\t" \
  "ds_read_b64_tr_b16 v[212:213], v248 offset:39976\n\t" \
  "ds_read_b32 v222, v222 offset:10016\n\t" \
  "v_mfma_f32_16x16x32_bf16 v[20:23], v[216:219], v[146:149], v[20:23]\n\t" \
  "v_or_b32 v203, v232, v233\n\t" \
  "v_or_b32 v202, v234, v235\n\t" \
  "ds_read_b64_tr_b16 v[214:215], v248 offset:41000\n\t" \
  "v_mfma_f32_16x16x32_bf16 v[44:47], v[216:219], v[154:157], v[44:47]\n\t" \
  "v_fma_f32 v195, |v195|, v229, v195 mul:4\n\t" \
  "v_fma_f32 v194, |v194|, v228, v194 mul:4\n\t" \
  "ds_read_u16_d16_hi v225, v225 offset:10528\n\t" \
  "v_mfma_f32_16x16x32_bf16 v[68:71], v[216:219], v[162:165], v[68:71]\n\t" \
  "v_fma_f32 v193, |v193|, v231, v193 mul:4\n\t" \
  "v_fma_f32 v192, |v192|, v230, v192 mul:4\n\t" \
  "ds_read_u16 v224, v224 offset:10528\n\t" \
  "v_mfma_f32_16x16x32_bf16 v[92:95], v[216:219], v[170:173], v[92:95]\n\t" \
  "v_cvt_pk_bf16_f32 v195, v194, v195\n\t" \
  "v_cvt_pk_bf16_f32 v194, v192, v193\n\t" \
  "ds_read_b64_tr_b16 v[216:217], v248 offset:38952\n\t" \
  "s_waitcnt lgkmcnt(9)\n\t" \
  "v_mfma_f32_16x16x32_bf16 v[144:147], v[208:211], v[96:99], 0\n\t" \
  "v_cvt_pknorm_i16_f32 v228, |v196|, |v197|\n\t" \
  "v_cvt_pknorm_i16_f32 v230, |v198|, |v199|\n\t" \
  "ds_read_b64_tr_b16 v[218:219], v248 offset:42024\n\t" \
  "v_mfma_f32_16x16x32_bf16 v[152:155], v[208:211], v[100:103], 0\n\t" \
  "v_lshlrev_b32_sdwa v229, 2, v228 dst_sel:DWORD dst_unused:UNUSED_PAD src0_sel:DWORD src1_sel:BYTE_3\n\t" \
  "v_lshlrev_b32_sdwa v228, 2, v228 dst_sel:DWORD dst_unused:UNUSED_PAD src0_sel:DWORD src1_sel:BYTE_1\n\t" \
  "ds_read_u16_d16_hi v227, v227 offset:10528\n\t" \
  "v_mfma_f32_16x16x32_bf16 v[160:163], v[208:211], v[104:107], 0\n\t" \
  "s_cmp_eq_u32 s38, 4\n\t" \
  "v_lshlrev_b32_sdwa v231, 2, v230 dst_sel:DWORD dst_unused:UNUSED_PAD src0_sel:DWORD src1_sel:BYTE_3\n\t" \
  "s_cbranch_scc1 .Lcnx_t0\n.Lcnx_u0:\n\t" \
  "ds_read_u16 v226, v226 offset:10528\n\t" \
  "v_mfma_f32_16x16x32_bf16 v[168:171], v[208:211], v[108:111], 0\n\t" \
  "v_lshlrev_b32_sdwa v230, 2, v230 dst_sel:DWORD dst_unused:UNUSED_PAD src0_sel:DWORD src1_sel:BYTE_1\n\t" \
  "v_lshlrev_b32_sdwa v233, 1, v205 dst_sel:DWORD dst_unused:UNUSED_PAD src0_sel:DWORD src1_sel:WORD_1\n\t" \
  "ds_read_b64_tr_b16 v[208:209], v248 offset:37416\n\t" \
  "s_waitcnt lgkmcnt(7)\n\t" \
  "v_mfma_f32_16x16x32_bf16 v[144:147], v[212:215], v[112:115], v[144:147]\n\t" \
  "v_lshlrev_b32_sdwa v232, 1, v204 dst_sel:DWORD dst_unused:UNUSED_PAD src0_sel:DWORD src1_sel:WORD_1\n\t" \
  "v_lshlrev_b32_sdwa v235, 1, v207 dst_sel:DWORD dst_unused:UNUSED_PAD src0_sel:DWORD src1_sel:WORD_1\n\t" \
  "ds_read_b64_tr_b16 v[210:211], v248 offset:38440\n\t" \
  "v_mfma_f32_16x16x32_bf16 v[152:155], v[212:215], v[116:119], v[152:155]\n\t" \
  "v_lshlrev_b32_sdwa v234, 1, v206 dst_sel:DWORD dst_unused:UNUSED_PAD src0_sel:DWORD src1_sel:WORD_1\n\t" \
  "v_fma_mix_f32 v221, |v181|, v221, v221 op_sel:[0,1,0] op_sel_hi:[0,1,1] clamp\n\t" \
  "ds_read_b32 v229, v229 offset:10016\n\t" \
  "ds_read_b32 v228, v228 offset:10016\n\t" \
  "v_mfma_f32_16x16x32_bf16 v[160:163], v[212:215], v[120:123], v[160:163]\n\t" \
  "v_fma_mix_f32 v220, |v180|, v220, v220 op_sel:[0,1,0] op_sel_hi:[0,1,1] clamp\n\t" \
  "ds_read_b32 v231, v231 offset:10016\n\t" \
  "ds_read_b32 v230, v230 offset:10016\n\t" \
  "v_mfma_f32_16x16x32_bf16 v[168:171], v[212:215], v[124:127], v[168:171]\n\t" \
  "v_fma_mix_f32 v223, |v183|, v223, v223 op_sel:[0,1,0] op_sel_hi:[0,1,1] clamp\n\t" \
  "v_fma_mix_f32 v222, |v182|, v222, v222 op_sel:[0,1,0] op_sel_hi:[0,1,1] clamp\n\t" \
  "s_waitcnt lgkmcnt(6)\n\t" \
  "ds_read_b64_tr_b16 v[212:213], v248 offset:40488\n\t" \
  "ds_read_u16_d16_hi v233, v233 offset:10528\n\t" \
  "v_mfma_f32_16x16x32_bf16 v[144:147], v[216:219], v[128:131], v[144:147]\n\t" \
  "v_or_b32 v188, v224, v225\n\t" \
  "v_or_b32 v189, v226, v227\n\t" \
  "ds_read_b64_tr_b16 v[214:215], v248 offset:41512\n\t" \
  "v_mfma_f32_16x16x32_bf16 v[152:155], v[216:219], v[132:135], v[152:155]\n\t" \
  "v_fma_f32 v181, |v181|, v221, v181 mul:4\n\t" \
  "v_fma_f32 v180, |v180|, v220, v180 mul:4\n\t" \
  "ds_read_u16 v232, v232 offset:10528\n\t" \
  "v_mfma_f32_16x16x32_bf16 v[160:163], v[216:219], v[136:139], v[160:163]\n\t" \
  "v_fma_f32 v183, |v183|, v223, v183 mul:4\n\t" \
  "v_fma_f32 v182, |v182|, v222, v182 mul:4\n\t" \
  "ds_read_u16_d16_hi v235, v235 offset:10528\n\t" \
  "v_mfma_f32_16x16x32_bf16 v[168:171], v[216:219], v[140:143], v[168:171]\n\t" \
  "v_cvt_pk_bf16_f32 v180, v180, v181\n\t" \
  "v_cvt_pk_bf16_f32 v181, v182, v183\n\t" \
  "ds_read_b64_tr_b16 v[216:217], v248 offset:39464\n\t" \
  "s_waitcnt lgkmcnt(10)\n\t" \
  "v_mfma_f32_16x16x32_bf16 v[148:151], v[208:211], v[96:99], 0\n\t" \
  "v_cvt_pknorm_i16_f32 v220, |v146|, |v147|\n\t" \
  "v_cvt_pknorm_i16_f32 v222, |v144|, |v145|\n\t" \
  "s_waitcnt vmcnt(2)\n\t" \
  "ds_read_b64_tr_b16 v[218:219], v248 offset:42536\n\t" \
  "v_mfma_f32_16x16x32_bf16 v[156:159], v[208:211], v[100:103], 0\n\t" \
  "v_lshlrev_b32_sdwa v221, 2, v220 dst_sel:DWORD dst_unused:UNUSED_PAD src0_sel:DWORD src1_sel:BYTE_3\n\t" \
  "v_lshlrev_b32_sdwa v220, 2, v220 dst_sel:DWORD dst_unused:UNUSED_PAD src0_sel:DWORD src1_sel:BYTE_1\n\t" \
  "s_mov_b64 exec, 3\n\t" \
  "ds_add_rtn_u32 v246, v251, v250 offset:47216\n\t" \
  "v_mfma_f32_16x16x32_bf16 v[164:167], v[208:211], v[104:107], 0\n\t" \
  "s_mov_b64 exec, -1\n\t" \
  "v_lshlrev_b32_sdwa v223, 2, v222 dst_sel:DWORD dst_unused:UNUSED_PAD src0_sel:DWORD src1_sel:BYTE_3\n\t" \
  "v_lshlrev_b32_sdwa v222, 2, v222 dst_sel:DWORD dst_unused:UNUSED_PAD src0_sel:DWORD src1_sel:BYTE_1\n\t" \
  "ds_read_u16 v234, v234 offset:10528\n\t" \
  "v_mfma_f32_16x16x32_bf16 v[172:175], v[208:211], v[108:111], 0\n\t" \
  "s_waitcnt lgkmcnt(1)\n\t" \
  "v_lshlrev_b32_sdwa v225, 1, v155 dst_sel:DWORD dst_unused:UNUSED_PAD src0_sel:DWORD src1_sel:WORD_1\n\t" \
  "v_readfirstlane_b32 s4, v246\n\t" \
  "ds_read_b32 v221, v221 offset:10016\n\t" \
  "v_mfma_f32_16x16x32_bf16 v[148:151], v[212:215], v[112:115], v[148:151]\n\t" \
  "s_cmpk_lt_u32 s4, 3072\n\t" \
  "v_lshlrev_b32_sdwa v224, 1, v154 dst_sel:DWORD dst_unused:UNUSED_PAD src0_sel:DWORD src1_sel:WORD_1\n\t" \
  "s_cbranch_scc1 .Lcnx_w0_11\n.Lcnx_g0_11:\n\t" \
  "ds_read_b32 v220, v220 offset:10016\n\t" \
  "v_mfma_f32_16x16x32_bf16 v[156:159], v[212:215], v[116:119], v[156:159]\n\t" \
  "v_lshlrev_b32_sdwa v227, 1, v153 dst_sel:DWORD dst_unused:UNUSED_PAD src0_sel:DWORD src1_sel:WORD_1\n\t" \
  "v_lshlrev_b32_sdwa v226, 1, v152 dst_sel:DWORD dst_unused:UNUSED_PAD src0_sel:DWORD src1_sel:WORD_1\n\t" \
  "ds_read_b64_tr_b16 v[208:209], v249 offset:30752\n\t" \
  "v_mfma_f32_16x16x32_bf16 v[164:167], v[212:215], v[120:123], v[164:167]\n\t" \
  "v_fma_mix_f32 v229, |v197|, v229, v229 op_sel:[0,1,0] op_sel_hi:[0,1,1] clamp\n\t" \
  "v_fma_mix_f32 v228, |v196|, v228, v228 op_sel:[0,1,0] op_sel_hi:[0,1,1] clamp\n\t" \
  "ds_read_b64_tr_b16 v[210:211], v249 offset:32288\n\t" \
  "ds_read_b32 v223, v223 offset:10016\n\t" \
  "v_mfma_f32_16x16x32_bf16 v[172:175], v[212:215], v[124:127], v[172:175]\n\t" \
  "v_fma_mix_f32 v231, |v199|, v231, v231 op_sel:[0,1,0] op_sel_hi:[0,1,1] clamp\n\t" \
  "v_fma_mix_f32 v230, |v198|, v230, v230 op_sel:[0,1,0] op_sel_hi:[0,1,1] clamp\n\t" \
  "s_waitcnt lgkmcnt(5)\n\t" \
  "ds_read_b64_tr_b16 v[212:213], v249 offset:30760\n\t" \
  "ds_read_b32 v222, v222 offset:10016\n\t" \
  "v_mfma_f32_16x16x32_bf16 v[148:151], v[216:219], v[128:131], v[148:151]\n\t" \
  "s_mov_b32 m0, s47\n\t" \
  "v_or_b32 v204, v232, v233\n\t" \
  "v_or_b32 v205, v234, v235\n\t" \
  "s_mov_b32 exec_hi, 0xffff\n\t" \
  "buffer_load_dwordx4 v252, s[16:19], s33 offen lds\n\t" \
  "v_mfma_f32_16x16x32_bf16 v[156:159], v[216:219], v[132:135], v[156:159]\n\t" \
  "s_mov_b32 exec_hi, -1\n\t" \
  "v_fma_f32 v197, |v197|, v229, v197 mul:4\n\t" \
  "v_fma_f32 v196, |v196|, v228, v196 mul:4\n\t" \
  "s_add_u32 s33, s33, s49\n\t" \
  "ds_read_b64_tr_b16 v[214:215], v249 offset:32296\n\t" \
  "v_mfma_f32_16x16x32_bf16 v[164:167], v[216:219], v[136:139], v[164:167]\n\t" \
  "v_fma_f32 v199, |v199|, v231, v199 mul:4\n\t" \
  "v_fma_f32 v198, |v198|, v230, v198 mul:4\n\t" \
  "ds_read_u16_d16_hi v225, v225 offset:10528\n\t" \
  "v_mfma_f32_16x16x32_bf16 v[172:175], v[216:219], v[140:143], v[172:175]\n\t" \
  "v_cvt_pk_bf16_f32 v196, v196, v197\n\t" \
  "v_cvt_pk_bf16_f32 v197, v198, v199\n\t" \
  "ds_read_b64_tr_b16 v[216:217], v249 offset:30816\n\t" \
  "s_waitcnt lgkmcnt(6)\n\t" \
  "v_mfma_f32_16x16x32_bf16 v[0:3], v[208:211], v[178:181], v[0:3]\n\t" \
  "v_cvt_pknorm_i16_f32 v228, |v162|, |v163|\n\t" \
  "v_cvt_pknorm_i16_f32 v230, |v160|, |v161|\n\t" \
  "ds_read_b64_tr_b16 v[218:219], v249 offset:32352\n\t" \
  "v_mfma_f32_16x16x32_bf16 v[24:27], v[208:211], v[186:189], v[24:27]\n\t" \
  "v_lshlrev_b32_sdwa v229, 2, v228 dst_sel:DWORD dst_unused:UNUSED_PAD src0_sel:DWORD src1_sel:BYTE_3\n\t" \
  "v_lshlrev_b32_sdwa v228, 2, v228 dst_sel:DWORD dst_unused:UNUSED_PAD src0_sel:DWORD src1_sel:BYTE_1\n\t" \
  "ds_read_u16 v224, v224 offset:10528\n\t" \
  "v_mfma_f32_16x16x32_bf16 v[48:51], v[208:211], v[194:197], v[48:51]\n\t" \
  "v_lshlrev_b32_sdwa v231, 2, v230 dst_sel:DWORD dst_unused:UNUSED_PAD src0_sel:DWORD src1_sel:BYTE_3\n\t" \
  "v_lshlrev_b32_sdwa v230, 2, v230 dst_sel:DWORD dst_unused:UNUSED_PAD src0_sel:DWORD src1_sel:BYTE_1\n\t" \
  "ds_read_u16_d16_hi v227, v227 offset:10528\n\t" \
  "v_mfma_f32_16x16x32_bf16 v[72:75], v[208:211], v[202:205], v[72:75]\n\t" \
  "s_add_u32 m0, s47, s35\n\t" \
  "v_lshlrev_b32_sdwa v233, 1, v171 dst_sel:DWORD dst_unused:UNUSED_PAD src0_sel:DWORD src1_sel:WORD_1\n\t" \
  "v_lshlrev_b32_sdwa v232, 1, v170 dst_sel:DWORD dst_unused:UNUSED_PAD src0_sel:DWORD src1_sel:WORD_1\n\t" \
  "s_waitcnt lgkmcnt(5)\n\t" \
  "s_mov_b32 exec_hi, 0xffff\n\t" \
  "buffer_load_dwordx4 v252, s[16:19], s34 offen lds\n\t" \
  "v_mfma_f32_16x16x32_bf16 v[4:7], v[212:215], v[178:181], v[4:7]\n\t" \
  "s_mov_b32 exec_hi, -1\n\t" \
  "v_lshlrev_b32_sdwa v235, 1, v169 dst_sel:DWORD dst_unused:UNUSED_PAD src0_sel:DWORD src1_sel:WORD_1\n\t" \
  "s_add_u32 s34, s34, s49\n\t" \
  "ds_read_b64_tr_b16 v[208:209], v249 offset:30824\n\t" \
  "ds_read_u16 v226, v226 offset:10528\n\t" \
  "v_mfma_f32_16x16x32_bf16 v[28:31], v[212:215], v[186:189], v[28:31]\n\t" \
  "v_lshlrev_b32_sdwa v234, 1, v168 dst_sel:DWORD dst_unused:UNUSED_PAD src0_sel:DWORD src1_sel:WORD_1\n\t" \
  "ds_read_b64_tr_b16 v[210:211], v249 offset:32360\n\t" \
  "ds_read_b32 v229, v229 offset:10016\n\t" \
  "v_mfma_f32_16x16x32_bf16 v[52:55], v[212:215], v[194:197], v[52:55]\n\t" \
  "v_fma_mix_f32 v221, |v147|, v221, v221 op_sel:[0,1,0] op_sel_hi:[0,1,1] clamp\n\t" \
  "v_fma_mix_f32 v220, |v146|, v220, v220 op_sel:[0,1,0] op_sel_hi:[0,1,1] clamp\n\t" \
  "ds_read_b32 v228, v228 offset:10016\n\t" \
  "ds_read_b32 v231, v231 offset:10016\n\t" \
  "v_mfma_f32_16x16x32_bf16 v[76:79], v[212:215], v[202:205], v[76:79]\n\t" \
  "v_fma_mix_f32 v223, |v145|, v223, v223 op_sel:[0,1,0] op_sel_hi:[0,1,1] clamp\n\t" \
  "v_fma_mix_f32 v222, |v144|, v222, v222 op_sel:[0,1,0] op_sel_hi:[0,1,1] clamp\n\t" \
  "s_waitcnt lgkmcnt(4)\n\t" \
  "ds_read_b64_tr_b16 v[212:213], v249 offset:30880\n\t" \
  "ds_read_b32 v230, v230 offset:10016\n\t" \
  "v_mfma_f32_16x16x32_bf16 v[8:11], v[216:219], v[178:181], v[8:11]\n\t" \
  "v_or_b32 v155, v224, v225\n\t" \
  "v_or_b32 v154, v226, v227\n\t" \
  "ds_read_b64_tr_b16 v[214:215], v249 offset:32416\n\t" \
  "v_mfma_f32_16x16x32_bf16 v[32:35], v[216:219], v[186:189], v[32:35]\n\t" \
  "v_fma_f32 v147, |v147|, v221, v147 mul:4\n\t" \
  "v_fma_f32 v146, |v146|, v220, v146 mul:4\n\t" \
  "ds_read_u16_d16_hi v233, v233 offset:10528\n\t" \
  "v_mfma_f32_16x16x32_bf16 v[56:59], v[216:219], v[194:197], v[56:59]\n\t" \
  "v_fma_f32 v145, |v145|, v223, v145 mul:4\n\t" \
  "v_fma_f32 v144, |v144|, v222, v144 mul:4\n\t" \
  "ds_read_u16 v232, v232 offset:10528\n\t" \
  "v_mfma_f32_16x16x32_bf16 v[80:83], v[216:219], v[202:205], v[80:83]\n\t" \
  "v_cvt_pk_bf16_f32 v147, v146, v147\n\t" \
  "v_cvt_pk_bf16_f32 v146, v144, v145\n\t" \
  "ds_read_b64_tr_b16 v[216:217], v249 offset:30888\n\t" \
  "s_waitcnt lgkmcnt(9)\n\t" \
  "v_mfma_f32_16x16x32_bf16 v[12:15], v[208:211], v[178:181], v[12:15]\n\t" \
  "v_cvt_pknorm_i16_f32 v220, |v148|, |v149|\n\t" \
  "v_cvt_pknorm_i16_f32 v222, |v150|, |v151|\n\t" \
  "ds_read_b64_tr_b16 v[218:219], v249 offset:32424\n\t" \
  "v_mfma_f32_16x16x32_bf16 v[36:39], v[208:211], v[186:189], v[36:39]\n\t" \
  "v_lshlrev_b32_sdwa v221, 2, v220 dst_sel:DWORD dst_unused:UNUSED_PAD src0_sel:DWORD src1_sel:BYTE_3\n\t" \
  "v_lshlrev_b32_sdwa v220, 2, v220 dst_sel:DWORD dst_unused:UNUSED_PAD src0_sel:DWORD src1_sel:BYTE_1\n\t" \
  "ds_read_u16_d16_hi v235, v235 offset:10528\n\t" \
  "v_mfma_f32_16x16x32_bf16 v[60:63], v[208:211], v[194:197], v[60:63]\n\t" \
  "v_lshlrev_b32_sdwa v223, 2, v222 dst_sel:DWORD dst_unused:UNUSED_PAD src0_sel:DWORD src1_sel:BYTE_3\n\t" \
  "v_lshlrev_b32_sdwa v222, 2, v222 dst_sel:DWORD dst_unused:UNUSED_PAD src0_sel:DWORD src1_sel:BYTE_1\n\t" \
  "ds_read_u16 v234, v234 offset:10528\n\t" \
  "v_mfma_f32_16x16x32_bf16 v[84:87], v[208:211], v[202:205], v[84:87]\n\t" \
  "v_lshlrev_b32_sdwa v225, 1, v157 dst_sel:DWORD dst_unused:UNUSED_PAD src0_sel:DWORD src1_sel:WORD_1\n\t" \
  "v_lshlrev_b32_sdwa v224, 1, v156 dst_sel:DWORD dst_unused:UNUSED_PAD src0_sel:DWORD src1_sel:WORD_1\n\t" \
  "ds_read_b64_tr_b16 v[208:209], v248 offset:0\n\t" \
  "s_waitcnt lgkmcnt(7)\n\t" \
  "v_mfma_f32_16x16x32_bf16 v[16:19], v[212:215], v[178:181], v[16:19]\n\t" \
  "v_lshlrev_b32_sdwa v227, 1, v159 dst_sel:DWORD dst_unused:UNUSED_PAD src0_sel:DWORD src1_sel:WORD_1\n\t" \
  "ds_read_b64_tr_b16 v[210:211], v248 offset:1024\n\t" \
  "ds_read_b32 v221, v221 offset:10016\n\t" \
  "v_mfma_f32_16x16x32_bf16 v[40:43], v[212:215], v[186:189], v[40:43]\n\t" \
  "s_add_u32 s27, s27, s26\n\t" \
  "v_lshlrev_b32_sdwa v226, 1, v158 dst_sel:DWORD dst_unused:UNUSED_PAD src0_sel:DWORD src1_sel:WORD_1\n\t" \
  "v_fma_mix_f32 v229, |v163|, v229, v229 op_sel:[0,1,0] op_sel_hi:[0,1,1] clamp\n\t" \
  "s_mov_b64 exec, s[30:31]\n\t" \
  "buffer_load_dword v237, v254, s[8:11], s27 offen\n\t" \
  "v_mfma_f32_16x16x32_bf16 v[64:67], v[212:215], v[194:197], v[64:67]\n\t" \
  "s_mov_b64 exec, -1\n\t" \
  "v_fma_mix_f32 v228, |v162|, v228, v228 op_sel:[0,1,0] op_sel_hi:[0,1,1] clamp\n\t" \
  "ds_read_b32 v220, v220 offset:10016\n\t" \
  "ds_read_b32 v223, v223 offset:10016\n\t" \
  "v_mfma_f32_16x16x32_bf16 v[88:91], v[212:215], v[202:205], v[88:91]\n\t" \
  "v_fma_mix_f32 v231, |v161|, v231, v231 op_sel:[0,1,0] op_sel_hi:[0,1,1] clamp\n\t" \
  "v_fma_mix_f32 v230, |v160|, v230, v230 op_sel:[0,1,0] op_sel_hi:[0,1,1] clamp\n\t" \
  "s_waitcnt lgkmcnt(5)\n\t" \
  "ds_read_b64_tr_b16 v[212:213], v248 offset:3072\n\t" \
  "ds_read_b32 v222, v222 offset:10016\n\t" \
  "v_mfma_f32_16x16x32_bf16 v[20:23], v[216:219], v[178:181], v[20:23]\n\t" \
  "v_or_b32 v171, v232, v233\n\t" \
  "v_or_b32 v170, v234, v235\n\t" \
  "ds_read_b64_tr_b16 v[214:215], v248 offset:4096\n\t" \
  "v_mfma_f32_16x16x32_bf16 v[44:47], v[216:219], v[186:189], v[44:47]\n\t" \
  "v_fma_f32 v163, |v163|, v229, v163 mul:4\n\t" \
  "v_fma_f32 v162, |v162|, v228, v162 mul:4\n\t" \
  "ds_read_u16_d16_hi v225, v225 offset:10528\n\t" \
  "v_mfma_f32_16x16x32_bf16 v[68:71], v[216:219], v[194:197], v[68:71]\n\t" \
  "v_fma_f32 v161, |v161|, v231, v161 mul:4\n\t" \
  "v_fma_f32 v160, |v160|, v230, v160 mul:4\n\t" \
  "ds_read_u16 v224, v224 offset:10528\n\t" \
  "v_mfma_f32_16x16x32_bf16 v[92:95], v[216:219], v[202:205], v[92:95]\n\t" \
  "v_cvt_pk_bf16_f32 v163, v162, v163\n\t" \
  "v_cvt_pk_bf16_f32 v162, v160, v161\n\t" \
  "ds_read_b64_tr_b16 v[216:217], v248 offset:2048\n\t" \
  "s_waitcnt lgkmcnt(9)\n\t" \
  "v_mfma_f32_16x16x32_bf16 v[176:179], v[208:211], v[96:99], 0\n\t" \
  "v_cvt_pknorm_i16_f32 v228, |v164|, |v165|\n\t" \
  "v_cvt_pknorm_i16_f32 v230, |v166|, |v167|\n\t" \
  "ds_read_b64_tr_b16 v[218:219], v248 offset:5120\n\t" \
  "v_mfma_f32_16x16x32_bf16 v[184:187], v[208:211], v[100:103], 0\n\t" \
  "v_lshlrev_b32_sdwa v229, 2, v228 dst_sel:DWORD dst_unused:UNUSED_PAD src0_sel:DWORD src1_sel:BYTE_3\n\t" \
  "v_lshlrev_b32_sdwa v228, 2, v228 dst_sel:DWORD dst_unused:UNUSED_PAD src0_sel:DWORD src1_sel:BYTE_1\n\t" \
  "ds_read_u16_d16_hi v227, v227 offset:10528\n\t" \
  "v_mfma_f32_16x16x32_bf16 v[192:195], v[208:211], v[104:107], 0\n\t" \
  "v_lshlrev_b32_sdwa v231, 2, v230 dst_sel:DWORD dst_unused:UNUSED_PAD src0_sel:DWORD src1_sel:BYTE_3\n\t" \
  "v_lshlrev_b32_sdwa v230, 2, v230 dst_sel:DWORD dst_unused:UNUSED_PAD src0_sel:DWORD src1_sel:BYTE_1\n\t" \
  "ds_read_u16 v226, v226 offset:10528\n\t" \
  "v_mfma_f32_16x16x32_bf16 v[200:203], v[208:211], v[108:111], 0\n\t" \
  "v_lshlrev_b32_sdwa v233, 1, v173 dst_sel:DWORD dst_unused:UNUSED_PAD src0_sel:DWORD src1_sel:WORD_1\n\t" \
  "v_lshlrev_b32_sdwa v232, 1, v172 dst_sel:DWORD dst_unused:UNUSED_PAD src0_sel:DWORD src1_sel:WORD_1\n\t" \
  "ds_read_b64_tr_b16 v[208:209], v248 offset:512\n\t" \
  "s_waitcnt lgkmcnt(7)\n\t" \
  "v_mfma_f32_16x16x32_bf16 v[176:179], v[212:215], v[112:115], v[176:179]\n\t" \
  "v_lshlrev_b32_sdwa v235, 1, v175 dst_sel:DWORD dst_unused:UNUSED_PAD src0_sel:DWORD src1_sel:WORD_1\n\t" \
  "ds_read_b64_tr_b16 v[210:211], v248 offset:1536\n\t" \
  "ds_read_b32 v229, v229 offset:10016\n\t" \
  "v_mfma_f32_16x16x32_bf16 v[184:187], v[212:215], v[116:119], v[184:187]\n\t" \
  "v_lshlrev_b32_sdwa v234, 1, v174 dst_sel:DWORD dst_unused:UNUSED_PAD src0_sel:DWORD src1_sel:WORD_1\n\t" \
  "v_fma_mix_f32 v221, |v149|, v221, v221 op_sel:[0,1,0] op_sel_hi:[0,1,1] clamp\n\t" \
  "ds_read_b32 v228, v228 offset:10016\n\t" \
  "v_mfma_f32_16x16x32_bf16 v[192:195], v[212:215], v[120:123], v[192:195]\n\t" \
  "v_fma_mix_f32 v220, |v148|, v220, v220 op_sel:[0,1,0] op_sel_hi:[0,1,1] clamp\n\t" \
  "ds_read_b32 v231, v231 offset:10016\n\t" \
  "ds_read_b32 v230, v230 offset:10016\n\t" \
  "v_mfma_f32_16x16x32_bf16 v[200:203], v[212:215], v[124:127], v[200:203]\n\t" \
  "v_fma_mix_f32 v223, |v151|, v223, v223 op_sel:[0,1,0] op_sel_hi:[0,1,1] clamp\n\t" \
  "v_fma_mix_f32 v222, |v150|, v222, v222 op_sel:[0,1,0] op_sel_hi:[0,1,1] clamp\n\t" \
  "s_waitcnt lgkmcnt(6)\n\t" \
  "ds_read_b64_tr_b16 v[212:213], v248 offset:3584\n\t" \
  "ds_read_u16_d16_hi v233, v233 offset:10528\n\t" \
  "v_mfma_f32_16x16x32_bf16 v[176:179], v[216:219], v[128:131], v[176:179]\n\t" \
  "v_or_b32 v156, v224, v225\n\t" \
  "v_or_b32 v157, v226, v227\n\t" \
  "ds_read_b64_tr_b16 v[214:215], v248 offset:4608\n\t" \
  "v_mfma_f32_16x16x32_bf16 v[184:187], v[216:219], v[132:135], v[184:187]\n\t" \
  "v_fma_f32 v149, |v149|, v221, v149 mul:4\n\t" \
  "v_fma_f32 v148, |v148|, v220, v148 mul:4\n\t" \
  "ds_read_u16 v232, v232 offset:10528\n\t" \
  "v_mfma_f32_16x16x32_bf16 v[192:195], v[216:219], v[136:139], v[192:195]\n\t" \
  "v_fma_f32 v151, |v151|, v223, v151 mul:4\n\t" \
  "v_fma_f32 v150, |v150|, v222, v150 mul:4\n\t" \
  "ds_read_u16_d16_hi v235, v235 offset:10528\n\t" \
  "v_mfma_f32_16x16x32_bf16 v[200:203], v[216:219], v[140:143], v[200:203]\n\t" \
  "v_cvt_pk_bf16_f32 v148, v148, v149\n\t" \
  "v_cvt_pk_bf16_f32 v149, v150, v151\n\t" \
  "ds_read_b64_tr_b16 v[216:217], v248 offset:2560\n\t" \
  "s_waitcnt lgkmcnt(10)\n\t" \
  "v_mfma_f32_16x16x32_bf16 v[180:183], v[208:211], v[96:99], 0\n\t" \
  "v_cvt_pknorm_i16_f32 v220, |v178|, |v179|\n\t" \
  "v_cvt_pknorm_i16_f32 v222, |v176|, |v177|\n\t" \
  "s_waitcnt vmcnt(1)\n\t" \
  "ds_read_b64_tr_b16 v[218:219], v248 offset:5632\n\t" \
  "v_mfma_f32_16x16x32_bf16 v[188:191], v[208:211], v[100:103], 0\n\t" \
  "v_lshlrev_b32_sdwa v221, 2, v220 dst_sel:DWORD dst_unused:UNUSED_PAD src0_sel:DWORD src1_sel:BYTE_3\n\t" \
  "v_lshlrev_b32_sdwa v220, 2, v220 dst_sel:DWORD dst_unused:UNUSED_PAD src0_sel:DWORD src1_sel:BYTE_1\n\t" \
  "s_mov_b64 exec, 3\n\t" \
  "ds_add_rtn_u32 v246, v251, v250 offset:47224\n\t" \
  "v_mfma_f32_16x16x32_bf16 v[196:199], v[208:211], v[104:107], 0\n\t" \
  "s_mov_b64 exec, -1\n\t" \
  "v_lshlrev_b32_sdwa v223, 2, v222 dst_sel:DWORD dst_unused:UNUSED_PAD src0_sel:DWORD src1_sel:BYTE_3\n\t" \
  "v_lshlrev_b32_sdwa v222, 2, v222 dst_sel:DWORD dst_unused:UNUSED_PAD src0_sel:DWORD src1_sel:BYTE_1\n\t" \
  "ds_read_u16 v234, v234 offset:10528\n\t" \
  "v_mfma_f32_16x16x32_bf16 v[204:207], v[208:211], v[108:111], 0\n\t" \
  "s_waitcnt lgkmcnt(1)\n\t" \
  "v_lshlrev_b32_sdwa v225, 1, v187 dst_sel:DWORD dst_unused:UNUSED_PAD src0_sel:DWORD src1_sel:WORD_1\n\t" \
  "v_readfirstlane_b32 s4, v246\n\t" \
  "ds_read_b32 v221, v221 offset:10016\n\t" \
  "v_mfma_f32_16x16x32_bf16 v[180:183], v[212:215], v[112:115], v[180:183]\n\t" \
  "s_cmpk_lt_u32 s4, 3072\n\t" \
  "v_lshlrev_b32_sdwa v224, 1, v186 dst_sel:DWORD dst_unused:UNUSED_PAD src0_sel:DWORD src1_sel:WORD_1\n\t" \
  "s_cbranch_scc1 .Lcnx_w0_12\n.Lcnx_g0_12:\n\t" \
  "ds_read_b32 v220, v220 offset:10016\n\t" \
  "v_mfma_f32_16x16x32_bf16 v[188:191], v[212:215], v[116:119], v[188:191]\n\t" \
  "v_lshlrev_b32_sdwa v227, 1, v185 dst_sel:DWORD dst_unused:UNUSED_PAD src0_sel:DWORD src1_sel:WORD_1\n\t" \
  "v_lshlrev_b32_sdwa v226, 1, v184 dst_sel:DWORD dst_unused:UNUSED_PAD src0_sel:DWORD src1_sel:WORD_1\n\t" \
  "ds_read_b64_tr_b16 v[208:209], v249 offset:6152\n\t" \
  "v_mfma_f32_16x16x32_bf16 v[196:199], v[212:215], v[120:123], v[196:199]\n\t" \
  "v_fma_mix_f32 v229, |v165|, v229, v229 op_sel:[0,1,0] op_sel_hi:[0,1,1] clamp\n\t" \
  "v_fma_mix_f32 v228, |v164|, v228, v228 op_sel:[0,1,0] op_sel_hi:[0,1,1] clamp\n\t" \
  "ds_read_b64_tr_b16 v[210:211], v249 offset:7688\n\t" \
  "ds_read_b32 v223, v223 offset:10016\n\t" \
  "v_mfma_f32_16x16x32_bf16 v[204:207], v[212:215], v[124:127], v[204:207]\n\t" \
  "v_fma_mix_f32 v231, |v167|, v231, v231 op_sel:[0,1,0] op_sel_hi:[0,1,1] clamp\n\t" \
  "v_fma_mix_f32 v230, |v166|, v230, v230 op_sel:[0,1,0] op_sel_hi:[0,1,1] clamp\n\t" \
  "s_waitcnt lgkmcnt(5)\n\t" \
  "ds_read_b64_tr_b16 v[212:213], v249 offset:6160\n\t" \
  "ds_read_b32 v222, v222 offset:10016\n\t" \
  "v_mfma_f32_16x16x32_bf16 v[180:183], v[216:219], v[128:131], v[180:183]\n\t" \
  "v_or_b32 v172, v232, v233\n\t" \
  "v_or_b32 v173, v234, v235\n\t" \
  "ds_read_b64_tr_b16 v[214:215], v249 offset:7696\n\t" \
  "v_mfma_f32_16x16x32_bf16 v[188:191], v[216:219], v[132:135], v[188:191]\n\t" \
  "v_fma_f32 v165, |v165|, v229, v165 mul:4\n\t" \
  "v_fma_f32 v164, |v164|, v228, v164 mul:4\n\t" \
  "ds_read_u16_d16_hi v225, v225 offset:10528\n\t" \
  "v_mfma_f32_16x16x32_bf16 v[196:199], v[216:219], v[136:139], v[196:199]\n\t" \
  "v_fma_f32 v167, |v167|, v231, v167 mul:4\n\t" \
  "v_fma_f32 v166, |v166|, v230, v166 mul:4\n\t" \
  "ds_read_u16 v224, v224 offset:10528\n\t" \
  "v_mfma_f32_16x16x32_bf16 v[204:207], v[216:219], v[140:143], v[204:207]\n\t" \
  "v_cvt_pk_bf16_f32 v164, v164, v165\n\t" \
  "v_cvt_pk_bf16_f32 v165, v166, v167\n\t" \
  "ds_read_b64_tr_b16 v[216:217], v249 offset:6216\n\t" \
  "s_waitcnt lgkmcnt(7)\n\t" \
  "v_mfma_f32_16x16x32_bf16 v[0:3], v[208:211], v[146:149], v[0:3]\n\t" \
  "ds_read_b64_tr_b16 v[218:219], v249 offset:7752\n\t" \
  "v_cvt_pknorm_i16_f32 v228, |v194|, |v195|\n\t" \
  "s_setprio 3\n\t" \
  "v_cvt_pknorm_i16_f32 v230, |v192|, |v193|\n\t" \
  "v_lshlrev_b32_sdwa v229, 2, v228 dst_sel:DWORD dst_unused:UNUSED_PAD src0_sel:DWORD src1_sel:BYTE_3\n\t" \
  "v_lshlrev_b32_sdwa v228, 2, v228 dst_sel:DWORD dst_unused:UNUSED_PAD src0_sel:DWORD src1_sel:BYTE_1\n\t" \
  "v_mfma_f32_16x16x32_bf16 v[24:27], v[208:211], v[154:157], v[24:27]\n\t" \
  "ds_read_u16_d16_hi v227, v227 offset:10528\n\t" \
  "v_lshlrev_b32_sdwa v231, 2, v230 dst_sel:DWORD dst_unused:UNUSED_PAD src0_sel:DWORD src1_sel:BYTE_3\n\t" \
  "v_lshlrev_b32_sdwa v230, 2, v230 dst_sel:DWORD dst_unused:UNUSED_PAD src0_sel:DWORD src1_sel:BYTE_1\n\t" \
  "v_lshlrev_b32_sdwa v233, 1, v203 dst_sel:DWORD dst_unused:UNUSED_PAD src0_sel:DWORD src1_sel:WORD_1\n\t" \
  "v_lshlrev_b32_sdwa v232, 1, v202 dst_sel:DWORD dst_unused:UNUSED_PAD src0_sel:DWORD src1_sel:WORD_1\n\t" \
  "v_mfma_f32_16x16x32_bf16 v[48:51], v[208:211], v[162:165], v[48:51]\n\t" \
  "ds_read_u16 v226, v226 offset:10528\n\t" \
  "ds_read_b32 v229, v229 offset:10016\n\t" \
  "v_lshlrev_b32_sdwa v235, 1, v201 dst_sel:DWORD dst_unused:UNUSED_PAD src0_sel:DWORD src1_sel:WORD_1\n\t" \
  "v_lshlrev_b32_sdwa v234, 1, v200 dst_sel:DWORD dst_unused:UNUSED_PAD src0_sel:DWORD src1_sel:WORD_1\n\t" \
  "s_waitcnt lgkmcnt(8)\n\t" \
  "v_fma_mix_f32 v221, |v179|, v221, v221 op_sel:[0,1,0] op_sel_hi:[0,1,1] clamp\n\t" \
  "v_fma_mix_f32 v220, |v178|, v220, v220 op_sel:[0,1,0] op_sel_hi:[0,1,1] clamp\n\t" \
  "v_mfma_f32_16x16x32_bf16 v[72:75], v[208:211], v[170:173], v[72:75]\n\t" \
  "ds_read_b64_tr_b16 v[208:209], v249 offset:6224\n\t" \
  "ds_read_b32 v228, v228 offset:10016\n\t" \
  "v_fma_mix_f32 v223, |v177|, v223, v223 op_sel:[0,1,0] op_sel_hi:[0,1,1] clamp\n\t" \
  "v_fma_mix_f32 v222, |v176|, v222, v222 op_sel:[0,1,0] op_sel_hi:[0,1,1] clamp\n\t" \
  "s_waitcnt lgkmcnt(3)\n\t" \
  "v_or_b32 v187, v224, v225\n\t" \
  "v_or_b32 v186, v226, v227\n\t" \
  "v_mfma_f32_16x16x32_bf16 v[4:7], v[212:215], v[146:149], v[4:7]\n\t" \
  "ds_read_b64_tr_b16 v[210:211], v249 offset:7760\n\t" \
  "ds_read_b32 v231, v231 offset:10016\n\t" \
  "v_fma_f32 v179, |v179|, v221, v179 mul:4\n\t" \
  "v_fma_f32 v178, |v178|, v220, v178 mul:4\n\t" \
  "v_fma_f32 v177, |v177|, v223, v177 mul:4\n\t" \
  "v_fma_f32 v176, |v176|, v222, v176 mul:4\n\t" \
  "v_mfma_f32_16x16x32_bf16 v[28:31], v[212:215], v[154:157], v[28:31]\n\t" \
  "s_add_u32 s27, s27, s26\n\t" \
  "s_mov_b64 exec, s[30:31]\n\t" \
  "buffer_load_dword v237, v254, s[8:11], s27 offen\n\t" \
  "s_mov_b64 exec, -1\n\t" \
  "v_cvt_pk_bf16_f32 v179, v178, v179\n\t" \
  "v_mfma_f32_16x16x32_bf16 v[52:55], v[212:215], v[162:165], v[52:55]\n\t" \
  "ds_read_b32 v230, v230 offset:10016\n\t" \
  "v_cvt_pk_bf16_f32 v178, v176, v177\n\t" \
  "s_setprio 2\n\t" \
  "v_cvt_pknorm_i16_f32 v220, |v180|, |v181|\n\t" \
  "v_cvt_pknorm_i16_f32 v222, |v182|, |v183|\n\t" \
  "v_lshlrev_b32_sdwa v221, 2, v220 dst_sel:DWORD dst_unused:UNUSED_PAD src0_sel:DWORD src1_sel:BYTE_3\n\t" \
  "v_mfma_f32_16x16x32_bf16 v[76:79], v[212:215], v[170:173], v[76:79]\n\t" \
  "ds_read_b64_tr_b16 v[212:213], v249 offset:6280\n\t" \
  "ds_read_u16_d16_hi v233, v233 offset:10528\n\t" \
  "v_lshlrev_b32_sdwa v220, 2, v220 dst_sel:DWORD dst_unused:UNUSED_PAD src0_sel:DWORD src1_sel:BYTE_1\n\t" \
  "v_lshlrev_b32_sdwa v223, 2, v222 dst_sel:DWORD dst_unused:UNUSED_PAD src0_sel:DWORD src1_sel:BYTE_3\n\t" \
  "v_lshlrev_b32_sdwa v222, 2, v222 dst_sel:DWORD dst_unused:UNUSED_PAD src0_sel:DWORD src1_sel:BYTE_1\n\t" \
  "v_mfma_f32_16x16x32_bf16 v[8:11], v[216:219], v[146:149], v[8:11]\n\t" \
  "ds_read_b64_tr_b16 v[214:215], v249 offset:7816\n\t" \
  "ds_read_u16 v232, v232 offset:10528\n\t" \
  "v_lshlrev_b32_sdwa v225, 1, v189 dst_sel:DWORD dst_unused:UNUSED_PAD src0_sel:DWORD src1_sel:WORD_1\n\t" \
  "v_lshlrev_b32_sdwa v224, 1, v188 dst_sel:DWORD dst_unused:UNUSED_PAD src0_sel:DWORD src1_sel:WORD_1\n\t" \
  "v_lshlrev_b32_sdwa v227, 1, v191 dst_sel:DWORD dst_unused:UNUSED_PAD src0_sel:DWORD src1_sel:WORD_1\n\t" \
  "v_lshlrev_b32_sdwa v226, 1, v190 dst_sel:DWORD dst_unused:UNUSED_PAD src0_sel:DWORD src1_sel:WORD_1\n\t" \
  "s_waitcnt lgkmcnt(4)\n\t" \
  "v_mfma_f32_16x16x32_bf16 v[32:35], v[216:219], v[154:157], v[32:35]\n\t" \
  "ds_read_u16_d16_hi v235, v235 offset:10528\n\t" \
  "ds_read_u16 v234, v234 offset:10528\n\t" \
  "v_fma_mix_f32 v229, |v195|, v229, v229 op_sel:[0,1,0] op_sel_hi:[0,1,1] clamp\n\t" \
  "v_fma_mix_f32 v228, |v194|, v228, v228 op_sel:[0,1,0] op_sel_hi:[0,1,1] clamp\n\t" \
  "v_fma_mix_f32 v231, |v193|, v231, v231 op_sel:[0,1,0] op_sel_hi:[0,1,1] clamp\n\t" \
  "v_mfma_f32_16x16x32_bf16 v[56:59], v[216:219], v[162:165], v[56:59]\n\t" \
  "ds_read_b32 v221, v221 offset:10016\n\t" \
  "ds_read_b32 v220, v220 offset:10016\n\t" \
  "v_fma_mix_f32 v230, |v192|, v230, v230 op_sel:[0,1,0] op_sel_hi:[0,1,1] clamp\n\t" \
  "s_waitcnt lgkmcnt(2)\n\t" \
  "v_or_b32 v203, v232, v233\n\t" \
  "v_or_b32 v202, v234, v235\n\t" \
  "v_fma_f32 v195, |v195|, v229, v195 mul:4\n\t" \
  "v_mfma_f32_16x16x32_bf16 v[80:83], v[216:219], v[170:173], v[80:83]\n\t" \
  "ds_read_b64_tr_b16 v[216:217], v249 offset:6288\n\t" \
  "ds_read_b32 v223, v223 offset:10016\n\t" \
  "v_fma_f32 v194, |v194|, v228, v194 mul:4\n\t" \
  "v_fma_f32 v193, |v193|, v231, v193 mul:4\n\t" \
  "v_fma_f32 v192, |v192|, v230, v192 mul:4\n\t" \
  "v_mfma_f32_16x16x32_bf16 v[12:15], v[208:211], v[146:149], v[12:15]\n\t" \
  "ds_read_b64_tr_b16 v[218:219], v249 offset:7824\n\t" \
  "ds_read_b32 v222, v222 offset:10016\n\t" \
  "v_cvt_pk_bf16_f32 v195, v194, v195\n\t" \
  "v_cvt_pk_bf16_f32 v194, v192, v193\n\t" \
  "v_cvt_pknorm_i16_f32 v228, |v196|, |v197|\n\t" \
  "v_cvt_pknorm_i16_f32 v230, |v198|, |v199|\n\t" \
  "v_mfma_f32_16x16x32_bf16 v[36:39], v[208:211], v[154:157], v[36:39]\n\t" \
  "s_mov_b64 exec, 3\n\t" \
  "ds_add_rtn_u32 v246, v251, v250 offset:47232\n\t" \
  "s_mov_b64 exec, -1\n\t" \
  "v_lshlrev_b32_sdwa v229, 2, v228 dst_sel:DWORD dst_unused:UNUSED_PAD src0_sel:DWORD src1_sel:BYTE_3\n\t" \
  "v_mfma_f32_16x16x32_bf16 v[60:63], v[208:211], v[162:165], v[60:63]\n\t" \
  "ds_read_u16_d16_hi v225, v225 offset:10528\n\t" \
  "v_lshlrev_b32_sdwa v228, 2, v228 dst_sel:DWORD dst_unused:UNUSED_PAD src0_sel:DWORD src1_sel:BYTE_1\n\t" \
  "s_setprio 1\n\t" \
  "v_lshlrev_b32_sdwa v231, 2, v230 dst_sel:DWORD dst_unused:UNUSED_PAD src0_sel:DWORD src1_sel:BYTE_3\n\t" \
  "v_lshlrev_b32_sdwa v230, 2, v230 dst_sel:DWORD dst_unused:UNUSED_PAD src0_sel:DWORD src1_sel:BYTE_1\n\t" \
  "v_mfma_f32_16x16x32_bf16 v[84:87], v[208:211], v[170:173], v[84:87]\n\t" \
  "ds_read_u16 v224, v224 offset:10528\n\t" \
  "ds_read_u16_d16_hi v227, v227 offset:10528\n\t" \
  "v_lshlrev_b32_sdwa v233, 1, v205 dst_sel:DWORD dst_unused:UNUSED_PAD src0_sel:DWORD src1_sel:WORD_1\n\t" \
  "v_lshlrev_b32_sdwa v232, 1, v204 dst_sel:DWORD dst_unused:UNUSED_PAD src0_sel:DWORD src1_sel:WORD_1\n\t" \
  "v_lshlrev_b32_sdwa v235, 1, v207 dst_sel:DWORD dst_unused:UNUSED_PAD src0_sel:DWORD src1_sel:WORD_1\n\t" \
  "s_waitcnt lgkmcnt(3)\n\t" \
  "v_readfirstlane_b32 s4, v246\n\t" \
  "v_mfma_f32_16x16x32_bf16 v[16:19], v[212:215], v[146:149], v[16:19]\n\t" \
  "ds_read_u16 v226, v226 offset:10528\n\t" \
  "ds_read_b32 v229, v229 offset:10016\n\t" \
  "v_lshlrev_b32_sdwa v234, 1, v206 dst_sel:DWORD dst_unused:UNUSED_PAD src0_sel:DWORD src1_sel:WORD_1\n\t" \
  "s_cmpk_lt_u32 s4, 3072\n\t" \
  "s_cbranch_scc1 .Lcnx_w0_13\n.Lcnx_g0_13:\n\t" \
  "v_mfma_f32_16x16x32_bf16 v[40:43], v[212:215], v[154:157], v[40:43]\n\t" \
  "ds_read_b64_tr_b16 v[208:209], v249 offset:12304\n\t" \
  "ds_read_b32 v228, v228 offset:10016\n\t" \
  "v_fma_mix_f32 v221, |v181|, v221, v221 op_sel:[0,1,0] op_sel_hi:[0,1,1] clamp\n\t" \
  "v_fma_mix_f32 v220, |v180|, v220, v220 op_sel:[0,1,0] op_sel_hi:[0,1,1] clamp\n\t" \
  "v_fma_mix_f32 v223, |v183|, v223, v223 op_sel:[0,1,0] op_sel_hi:[0,1,1] clamp\n\t" \
  "v_mfma_f32_16x16x32_bf16 v[64:67], v[212:215], v[162:165], v[64:67]\n\t" \
  "ds_read_b64_tr_b16 v[210:211], v249 offset:13840\n\t" \
  "ds_read_b32 v231, v231 offset:10016\n\t" \
  "v_fma_mix_f32 v222, |v182|, v222, v222 op_sel:[0,1,0] op_sel_hi:[0,1,1] clamp\n\t" \
  "s_setprio 0\n\t" \
  "s_waitcnt lgkmcnt(5)\n\t" \
  "v_or_b32 v188, v224, v225\n\t" \
  "v_or_b32 v189, v226, v227\n\t" \
  "v_fma_f32 v181, |v181|, v221, v181 mul:4\n\t" \
  "v_mfma_f32_16x16x32_bf16 v[88:91], v[212:215], v[170:173], v[88:91]\n\t" \
  "ds_read_b64_tr_b16 v[212:213], v249 offset:12312\n\t" \
  "ds_read_b32 v230, v230 offset:10016\n\t" \
  "v_fma_f32 v180, |v180|, v220, v180 mul:4\n\t" \
  "v_fma_f32 v183, |v183|, v223, v183 mul:4\n\t" \
  "v_fma_f32 v182, |v182|, v222, v182 mul:4\n\t" \
  "v_mfma_f32_16x16x32_bf16 v[20:23], v[216:219], v[146:149], v[20:23]\n\t" \
  "ds_read_b64_tr_b16 v[214:215], v249 offset:13848\n\t" \
  "v_cvt_pk_bf16_f32 v180, v180, v181\n\t" \
  "v_cvt_pk_bf16_f32 v181, v182, v183\n\t" \
  "s_waitcnt lgkmcnt(1)\n\t" \
  "v_fma_mix_f32 v229, |v197|, v229, v229 op_sel:[0,1,0] op_sel_hi:[0,1,1] clamp\n\t" \
  "v_fma_mix_f32 v228, |v196|, v228, v228 op_sel:[0,1,0] op_sel_hi:[0,1,1] clamp\n\t" \
  "v_mfma_f32_16x16x32_bf16 v[44:47], v[216:219], v[154:157], v[44:47]\n\t" \
  "ds_read_u16_d16_hi v233, v233 offset:10528\n\t" \
  "ds_read_u16 v232, v232 offset:10528\n\t" \
  "ds_read_u16_d16_hi v235, v235 offset:10528\n\t" \
  "ds_read_u16 v234, v234 offset:10528\n\t" \
  "v_fma_mix_f32 v231, |v199|, v231, v231 op_sel:[0,1,0] op_sel_hi:[0,1,1] clamp\n\t" \
  "v_fma_mix_f32 v230, |v198|, v230, v230 op_sel:[0,1,0] op_sel_hi:[0,1,1] clamp\n\t" \
  "s_waitcnt lgkmcnt(0)\n\t" \
  "v_or_b32 v204, v232, v233\n\t" \
  "v_mfma_f32_16x16x32_bf16 v[68:71], v[216:219], v[162:165], v[68:71]\n\t" \
  "v_or_b32 v205, v234, v235\n\t" \
  "v_fma_f32 v197, |v197|, v229, v197 mul:4\n\t" \
  "v_fma_f32 v196, |v196|, v228, v196 mul:4\n\t" \
  "v_fma_f32 v199, |v199|, v231, v199 mul:4\n\t" \
  "v_mfma_f32_16x16x32_bf16 v[92:95], v[216:219], v[170:173], v[92:95]\n\t" \
  "ds_read_b64_tr_b16 v[216:217], v249 offset:12368\n\t" \
  "v_fma_f32 v198, |v198|, v230, v198 mul:4\n\t" \
  "v_cvt_pk_bf16_f32 v196, v196, v197\n\t" \
  "v_cvt_pk_bf16_f32 v197, v198, v199\n\t" \
  "v_mfma_f32_32x32x16_bf16 v[144:159], v[242:245], v[240:243], 0\n\t" \
  "v_mfma_f32_32x32x16_bf16 v[160:175], v[242:245], v[240:243], 0\n\t" \
  "v_mfma_f32_16x16x32_bf16 v[0:3], v[208:211], v[178:181], v[0:3]\n\t" \
  "ds_read_b64_tr_b16 v[218:219], v249 offset:13904\n\t" \
  "v_add_u32 v232, s56, v247\n\t" \
  "v_mfma_f32_16x16x32_bf16 v[24:27], v[208:211], v[186:189], v[24:27]\n\t" \
  "ds_read2_b32 v[98:99], v247 offset0:0 offset1:6\n\t" \
  "v_mfma_f32_16x16x32_bf16 v[48:51], v[208:211], v[194:197], v[48:51]\n\t" \
  "ds_read2_b32 v[114:115], v247 offset0:192 offset1:198\n\t" \
  "v_mfma_f32_16x16x32_bf16 v[72:75], v[208:211], v[202:205], v[72:75]\n\t" \
  "ds_read_b64_tr_b16 v[208:209], v249 offset:12376\n\t" \
  "ds_read2_b32 v[130:131], v232 offset0:0 offset1:6\n\t" \
  "v_mfma_f32_16x16x32_bf16 v[4:7], v[212:215], v[178:181], v[4:7]\n\t" \
  "ds_read_b64_tr_b16 v[210:211], v249 offset:13912\n\t" \
  "ds_read2_b32 v[102:103], v247 offset0:12 offset1:18\n\t" \
  "v_mfma_f32_16x16x32_bf16 v[28:31], v[212:215], v[186:189], v[28:31]\n\t" \
  "ds_read2_b32 v[118:119], v247 offset0:204 offset1:210\n\t" \
  "ds_read2_b32 v[134:135], v232 offset0:12 offset1:18\n\t" \
  "s_add_u32 s27, s27, s26\n\t" \
  "s_mov_b64 exec, s[30:31]\n\t" \
  "buffer_load_dword v237, v254, s[8:11], s27 offen\n\t" \
  "s_mov_b64 exec, -1\n\t" \
  "v_bfe_u32 v234, v251, 3, 2\n\t" \
  "v_mul_u32_u24 v234, 0x5555, v234\n\t" \
  "v_mfma_f32_16x16x32_bf16 v[52:55], v[212:215], v[194:197], v[52:55]\n\t" \
  "ds_read2_b32 v[106:107], v247 offset0:24 offset1:30\n\t" \
  "ds_read2_b32 v[122:123], v247 offset0:216 offset1:222\n\t" \
  "v_mfma_f32_16x16x32_bf16 v[76:79], v[212:215], v[202:205], v[76:79]\n\t" \
  "ds_read_b64_tr_b16 v[212:213], v249 offset:12432\n\t" \
  "ds_read2_b32 v[138:139], v232 offset0:24 offset1:30\n\t" \
  "s_waitcnt lgkmcnt(12)\n\t" \
  "v_mfma_f32_16x16x32_bf16 v[8:11], v[216:219], v[178:181], v[8:11]\n\t" \
  "ds_read_b64_tr_b16 v[214:215], v249 offset:13968\n\t" \
  "v_mfma_f32_16x16x32_bf16 v[32:35], v[216:219], v[186:189], v[32:35]\n\t" \
  "ds_read2_b32 v[110:111], v247 offset0:36 offset1:42\n\t" \
  "v_mfma_f32_16x16x32_bf16 v[56:59], v[216:219], v[194:197], v[56:59]\n\t" \
  "ds_read2_b32 v[126:127], v247 offset0:228 offset1:234\n\t" \
  "v_mfma_f32_16x16x32_bf16 v[80:83], v[216:219], v[202:205], v[80:83]\n\t" \
  "ds_read_b64_tr_b16 v[216:217], v249 offset:12440\n\t" \
  "v_cvt_pk_bf16_f32 v48, v48, v49\n\t" \
  "v_cvt_pk_bf16_f32 v49, v50, v51\n\t" \
  "v_cvt_pk_bf16_f32 v50, v52, v53\n\t" \
  "v_cvt_pk_bf16_f32 v51, v54, v55\n\t" \
  "v_cvt_pk_bf16_f32 v52, v0, v1\n\t" \
  "v_cvt_pk_bf16_f32 v53, v2, v3\n\t" \
  "v_cvt_pk_bf16_f32 v54, v4, v5\n\t" \
  "v_cvt_pk_bf16_f32 v55, v6, v7\n\t" \
  "v_cvt_pk_bf16_f32 v72, v72, v73\n\t" \
  "v_cvt_pk_bf16_f32 v73, v74, v75\n\t" \
  "v_cvt_pk_bf16_f32 v74, v76, v77\n\t" \
  "v_cvt_pk_bf16_f32 v75, v78, v79\n\t" \
  "v_cvt_pk_bf16_f32 v76, v24, v25\n\t" \
  "v_cvt_pk_bf16_f32 v77, v26, v27\n\t" \
  "v_cvt_pk_bf16_f32 v78, v28, v29\n\t" \
  "v_cvt_pk_bf16_f32 v79, v30, v31\n\t" \
  "s_waitcnt lgkmcnt(11)\n\t" \
  "v_mfma_f32_16x16x32_bf16 v[12:15], v[208:211], v[178:181], v[12:15]\n\t" \
  "ds_read_b64_tr_b16 v[218:219], v249 offset:13976\n\t" \
  "v_mfma_f32_16x16x32_bf16 v[36:39], v[208:211], v[186:189], v[36:39]\n\t" \
  "ds_read2_b32 v[142:143], v232 offset0:36 offset1:42\n\t" \
  "v_mfma_f32_16x16x32_bf16 v[60:63], v[208:211], v[194:197], v[60:63]\n\t" \
  "v_mfma_f32_16x16x32_bf16 v[84:87], v[208:211], v[202:205], v[84:87]\n\t" \
  "s_waitcnt lgkmcnt(5)\n\t" \
  "v_mfma_f32_16x16x32_bf16 v[16:19], v[212:215], v[178:181], v[16:19]\n\t" \
  "v_mfma_f32_16x16x32_bf16 v[40:43], v[212:215], v[186:189], v[40:43]\n\t" \
  "v_mfma_f32_16x16x32_bf16 v[64:67], v[212:215], v[194:197], v[64:67]\n\t" \
  "v_mfma_f32_16x16x32_bf16 v[88:91], v[212:215], v[202:205], v[88:91]\n\t" \
  "v_cvt_pk_bf16_f32 v56, v56, v57\n\t" \
  "v_cvt_pk_bf16_f32 v57, v58, v59\n\t" \
  "v_cvt_pk_bf16_f32 v58, v60, v61\n\t" \
  "v_cvt_pk_bf16_f32 v59, v62, v63\n\t" \
  "v_cvt_pk_bf16_f32 v60, v8, v9\n\t" \
  "v_cvt_pk_bf16_f32 v61, v10, v11\n\t" \
  "v_cvt_pk_bf16_f32 v62, v12, v13\n\t" \
  "v_cvt_pk_bf16_f32 v63, v14, v15\n\t" \
  "v_cvt_pk_bf16_f32 v80, v80, v81\n\t" \
  "v_cvt_pk_bf16_f32 v81, v82, v83\n\t" \
  "v_cvt_pk_bf16_f32 v82, v84, v85\n\t" \
  "v_cvt_pk_bf16_f32 v83, v86, v87\n\t" \
  "v_cvt_pk_bf16_f32 v84, v32, v33\n\t" \
  "v_cvt_pk_bf16_f32 v85, v34, v35\n\t" \
  "v_cvt_pk_bf16_f32 v86, v36, v37\n\t" \
  "v_cvt_pk_bf16_f32 v87, v38, v39\n\t" \
  "s_waitcnt lgkmcnt(1)\n\t" \
  "v_mfma_f32_16x16x32_bf16 v[20:23], v[216:219], v[178:181], v[20:23]\n\t" \
  "v_mfma_f32_16x16x32_bf16 v[44:47], v[216:219], v[186:189], v[44:47]\n\t" \
  "v_mfma_f32_16x16x32_bf16 v[68:71], v[216:219], v[194:197], v[68:71]\n\t" \
  "v_mfma_f32_16x16x32_bf16 v[92:95], v[216:219], v[202:205], v[92:95]\n\t" \
  "v_cvt_pk_bf16_f32 v64, v64, v65\n\t" \
  "v_cvt_pk_bf16_f32 v65, v66, v67\n\t" \
  "s_nop 4\n\t" \
  "v_cvt_pk_bf16_f32 v66, v68, v69\n\t" \
  "v_cvt_pk_bf16_f32 v67, v70, v71\n\t" \
  "v_cvt_pk_bf16_f32 v68, v16, v17\n\t" \
  "v_cvt_pk_bf16_f32 v69, v18, v19\n\t" \
  "v_cvt_pk_bf16_f32 v70, v20, v21\n\t" \
  "v_cvt_pk_bf16_f32 v71, v22, v23\n\t" \
  "v_cvt_pk_bf16_f32 v88, v88, v89\n\t" \
  "v_cvt_pk_bf16_f32 v89, v90, v91\n\t" \
  "v_cvt_pk_bf16_f32 v90, v92, v93\n\t" \
  "v_cvt_pk_bf16_f32 v91, v94, v95\n\t" \
  "v_cvt_pk_bf16_f32 v92, v40, v41\n\t" \
  "v_cvt_pk_bf16_f32 v93, v42, v43\n\t" \
  "v_cvt_pk_bf16_f32 v94, v44, v45\n\t" \
  "v_cvt_pk_bf16_f32 v95, v46, v47\n\t" \
  "s_and_b32 s69, s69, 0xffff\n\t" \
  "s_add_u32 s63, s28, s65\n\t" \
  "s_add_u32 s62, s29, s65\n\t" \
  "s_add_u32 s14, s12, s65\n\t" \
  "s_add_u32 s15, s13, s65\n\t" \
  "v_lshlrev_b32 v198, 1, v251\n\t" \
  "global_load_dwordx4 v[192:195], v198, s[66:67]\n\t" \
  "s_branch .Lcnx_mid\n\t" \
  ".Lcnx_w0_2:\n\t" \
  "ds_read_b32 v246, v251 offset:47144\n\t" \
  "s_waitcnt lgkmcnt(0)\n\t" \
  "v_readfirstlane_b32 s4, v246\n\t" \
  "s_cmp_lt_u32 s4, 3072\n\t" \
  "s_cbranch_scc1 .Lcnx_w0_2\n\t" \
  "s_branch .Lcnx_g0_2\n\t" \
  ".Lcnx_w0_3:\n\t" \
  "ds_read_b32 v246, v251 offset:47152\n\t" \
  "s_waitcnt lgkmcnt(0)\n\t" \
  "v_readfirstlane_b32 s4, v246\n\t" \
  "s_cmp_lt_u32 s4, 3072\n\t" \
  "s_cbranch_scc1 .Lcnx_w0_3\n\t" \
  "s_branch .Lcnx_g0_3\n\t" \
  ".Lcnx_w0_4:\n\t" \
  "ds_read_b32 v246, v251 offset:47160\n\t" \
  "s_waitcnt lgkmcnt(0)\n\t" \
  "v_readfirstlane_b32 s4, v246\n\t" \
  "s_cmp_lt_u32 s4, 3072\n\t" \
  "s_cbranch_scc1 .Lcnx_w0_4\n\t" \
  "s_branch .Lcnx_g0_4\n\t" \
  ".Lcnx_w0_5:\n\t" \
  "ds_read_b32 v246, v251 offset:47168\n\t" \
  "s_waitcnt lgkmcnt(0)\n\t" \
  "v_readfirstlane_b32 s4, v246\n\t" \
  "s_cmp_lt_u32 s4, 3072\n\t" \
  "s_cbranch_scc1 .Lcnx_w0_5\n\t" \
  "s_branch .Lcnx_g0_5\n\t" \
  ".Lcnx_w0_6:\n\t" \
  "ds_read_b32 v246, v251 offset:47176\n\t" \
  "s_waitcnt lgkmcnt(0)\n\t" \
  "v_readfirstlane_b32 s4, v246\n\t" \
  "s_cmp_lt_u32 s4, 3072\n\t" \
  "s_cbranch_scc1 .Lcnx_w0_6\n\t" \
  "s_branch .Lcnx_g0_6\n\t" \
  ".Lcnx_w0_7:\n\t" \
  "ds_read_b32 v246, v251 offset:47184\n\t" \
  "s_waitcnt lgkmcnt(0)\n\t" \
  "v_readfirstlane_b32 s4, v246\n\t" \
  "s_cmp_lt_u32 s4, 3072\n\t" \
  "s_cbranch_scc1 .Lcnx_w0_7\n\t" \
  "s_branch .Lcnx_g0_7\n\t" \
  ".Lcnx_w0_8:\n\t" \
  "ds_read_b32 v246, v251 offset:47192\n\t" \
  "s_waitcnt lgkmcnt(0)\n\t" \
  "v_readfirstlane_b32 s4, v246\n\t" \
  "s_cmp_lt_u32 s4, 3072\n\t" \
  "s_cbranch_scc1 .Lcnx_w0_8\n\t" \
  "s_branch .Lcnx_g0_8\n\t" \
  ".Lcnx_w0_9:\n\t" \
  "ds_read_b32 v246, v251 offset:47200\n\t" \
  "s_waitcnt lgkmcnt(0)\n\t" \
  "v_readfirstlane_b32 s4, v246\n\t" \
  "s_cmp_lt_u32 s4, 3072\n\t" \
  "s_cbranch_scc1 .Lcnx_w0_9\n\t" \
  "s_branch .Lcnx_g0_9\n\t" \
  ".Lcnx_w0_10:\n\t" \
  "ds_read_b32 v246, v251 offset:47208\n\t" \
  "s_waitcnt lgkmcnt(0)\n\t" \
  "v_readfirstlane_b32 s4, v246\n\t" \
  "s_cmp_lt_u32 s4, 3072\n\t" \
  "s_cbranch_scc1 .Lcnx_w0_10\n\t" \
  "s_branch .Lcnx_g0_10\n\t" \
  ".Lcnx_t0:\n\t" \
  "v_add_u32 v246, -116544, v251\n\t" \
  "v_lshrrev_b32 v246, 3, v246\n\t" \
  "v_mul_i32_i24 v246, s26, v246\n\t" \
  "v_add_u32 v246, s28, v246\n\t" \
  "s_mov_b32 m0, 107744\n\t" \
  "s_mov_b64 exec, 0xffff\n\t" \
  "buffer_load_dword v246, s[20:23], 0 offen lds\n\t" \
  "s_mov_b64 exec, -1\n\t" \
  "s_branch .Lcnx_u0\n\t" \
  ".Lcnx_w0_11:\n\t" \
  "ds_read_b32 v246, v251 offset:47216\n\t" \
  "s_waitcnt lgkmcnt(0)\n\t" \
  "v_readfirstlane_b32 s4, v246\n\t" \
  "s_cmp_lt_u32 s4, 3072\n\t" \
  "s_cbranch_scc1 .Lcnx_w0_11\n\t" \
  "s_branch .Lcnx_g0_11\n\t" \
  ".Lcnx_w0_12:\n\t" \
  "ds_read_b32 v246, v251 offset:47224\n\t" \
  "s_waitcnt lgkmcnt(0)\n\t" \
  "v_readfirstlane_b32 s4, v246\n\t" \
  "s_cmp_lt_u32 s4, 3072\n\t" \
  "s_cbranch_scc1 .Lcnx_w0_12\n\t" \
  "s_branch .Lcnx_g0_12\n\t" \
  ".Lcnx_w0_13:\n\t" \
  "ds_read_b32 v246, v251 offset:47232\n\t" \
  "s_waitcnt lgkmcnt(0)\n\t" \
  "v_readfirstlane_b32 s4, v246\n\t" \
  "s_cmp_lt_u32 s4, 3072\n\t" \
  "s_cbranch_scc1 .Lcnx_w0_13\n\t" \
  "s_branch .Lcnx_g0_13\n\t" \
  ".Lcnx_w1_2:\n\t" \
  "ds_read_b32 v246, v251 offset:47144\n\t" \
  "s_waitcnt lgkmcnt(0)\n\t" \
  "v_readfirstlane_b32 s4, v246\n\t" \
  "s_cmp_lt_u32 s4, 6144\n\t" \
  "s_cbranch_scc1 .Lcnx_w1_2\n\t" \
  "s_branch .Lcnx_g1_2\n\t" \
  ".Lcnx_w1_3:\n\t" \
  "ds_read_b32 v246, v251 offset:47152\n\t" \
  "s_waitcnt lgkmcnt(0)\n\t" \
  "v_readfirstlane_b32 s4, v246\n\t" \
  "s_cmp_lt_u32 s4, 6144\n\t" \
  "s_cbranch_scc1 .Lcnx_w1_3\n\t" \
  "s_branch .Lcnx_g1_3\n\t" \
  ".Lcnx_w1_4:\n\t" \
  "ds_read_b32 v246, v251 offset:47160\n\t" \
  "s_waitcnt lgkmcnt(0)\n\t" \
  "v_readfirstlane_b32 s4, v246\n\t" \
  "s_cmp_lt_u32 s4, 6144\n\t" \
  "s_cbranch_scc1 .Lcnx_w1_4\n\t" \
  "s_branch .Lcnx_g1_4\n\t" \
  ".Lcnx_w1_5:\n\t" \
  "ds_read_b32 v246, v251 offset:47168\n\t" \
  "s_waitcnt lgkmcnt(0)\n\t" \
  "v_readfirstlane_b32 s4, v246\n\t" \
  "s_cmp_lt_u32 s4, 6144\n\t" \
  "s_cbranch_scc1 .Lcnx_w1_5\n\t" \
  "s_branch .Lcnx_g1_5\n\t" \
  ".Lcnx_w1_6:\n\t" \
  "ds_read_b32 v246, v251 offset:47176\n\t" \
  "s_waitcnt lgkmcnt(0)\n\t" \
  "v_readfirstlane_b32 s4, v246\n\t" \
  "s_cmp_lt_u32 s4, 6144\n\t" \
  "s_cbranch_scc1 .Lcnx_w1_6\n\t" \
  "s_branch .Lcnx_g1_6\n\t" \
  ".Lcnx_w1_7:\n\t" \
  "ds_read_b32 v246, v251 offset:47184\n\t" \
  "s_waitcnt lgkmcnt(0)\n\t" \
  "v_readfirstlane_b32 s4, v246\n\t" \
  "s_cmp_lt_u32 s4, 6144\n\t" \
  "s_cbranch_scc1 .Lcnx_w1_7\n\t" \
  "s_branch .Lcnx_g1_7\n\t" \
  ".Lcnx_w1_8:\n\t" \
  "ds_read_b32 v246, v251 offset:47192\n\t" \
  "s_waitcnt lgkmcnt(0)\n\t" \
  "v_readfirstlane_b32 s4, v246\n\t" \
  "s_cmp_lt_u32 s4, 6144\n\t" \
  "s_cbranch_scc1 .Lcnx_w1_8\n\t" \
  "s_branch .Lcnx_g1_8\n\t" \
  ".Lcnx_w1_9:\n\t" \
  "ds_read_b32 v246, v251 offset:47200\n\t" \
  "s_waitcnt lgkmcnt(0)\n\t" \
  "v_readfirstlane_b32 s4, v246\n\t" \
  "s_cmp_lt_u32 s4, 6144\n\t" \
  "s_cbranch_scc1 .Lcnx_w1_9\n\t" \
  "s_branch .Lcnx_g1_9\n\t" \
  ".Lcnx_mid:\n\t" \
  "v_add_u32 v232, 56880, v253\n\t" \
  "v_add_u32 v233, s39, v251\n\t" \
  "v_mfma_f32_32x32x16_bf16 v[176:191], v[242:245], v[240:243], 0\n\t" \
  "ds_read_b64 v[200:201], v253 offset:0\n\t" \
  "ds_read_b64 v[202:203], v253 offset:400\n\t" \
  "ds_read_b64 v[204:205], v253 offset:800\n\t" \
  "ds_read_b64 v[206:207], v253 offset:1200\n\t" \
  "ds_read_b64 v[208:209], v253 offset:8\n\t" \
  "ds_read_b64 v[210:211], v253 offset:408\n\t" \
  "ds_read_b64 v[212:213], v253 offset:808\n\t" \
  "ds_read_b64 v[214:215], v253 offset:1208\n\t" \
  "ds_read_b64 v[216:217], v253 offset:16\n\t" \
  "ds_read_b64 v[218:219], v253 offset:416\n\t" \
  "ds_read_b64 v[220:221], v253 offset:816\n\t" \
  "ds_read_b64 v[222:223], v253 offset:1216\n\t" \
  "s_waitcnt lgkmcnt(15)\n\t" \
  "v_and_b32 v96, 0xffff, v98\n\t" \
  "v_and_b32 v97, 0xffff0000, v98\n\t" \
  "v_and_b32 v98, 0xffff, v99\n\t" \
  "v_and_b32 v99, 0xffff0000, v99\n\t" \
  "v_and_b32 v112, 0xffff, v114\n\t" \
  "v_and_b32 v113, 0xffff0000, v114\n\t" \
  "v_and_b32 v114, 0xffff, v115\n\t" \
  "v_and_b32 v115, 0xffff0000, v115\n\t" \
  "v_and_b32 v128, 0xffff, v130\n\t" \
  "v_and_b32 v129, 0xffff0000, v130\n\t" \
  "v_and_b32 v130, 0xffff, v131\n\t" \
  "v_and_b32 v131, 0xffff0000, v131\n\t" \
  "s_waitcnt lgkmcnt(8)\n\t" \
  "v_smfmac_f32_16x16x64_bf16 v[144:147], v[96:99], v[200:207], v234\n\t" \
  "v_and_b32 v100, 0xffff, v102\n\t" \
  "v_and_b32 v101, 0xffff0000, v102\n\t" \
  "v_and_b32 v102, 0xffff, v103\n\t" \
  "v_and_b32 v103, 0xffff0000, v103\n\t" \
  "s_waitcnt vmcnt(9) lgkmcnt(0)\n\t" \
  "s_barrier\n\t" \
  "ds_read_b64 v[224:225], v253 offset:16400\n\t" \
  "ds_read_b64 v[226:227], v253 offset:16800\n\t" \
  "ds_read_b64 v[228:229], v253 offset:17200\n\t" \
  "ds_read_b64 v[230:231], v253 offset:17600\n\t" \
  "s_add_u32 m0, s25, 81480\n\t" \
  "s_add_u32 s24, s24, s26\n\t" \
  "v_smfmac_f32_16x16x64_bf16 v[148:151], v[112:115], v[208:215], v234\n\t" \
  "buffer_load_dwordx4 v254, s[8:11], s24 offen lds\n\t" \
  "ds_read_b64 v[200:201], v253 offset:16408\n\t" \
  "ds_read_b64 v[202:203], v253 offset:16808\n\t" \
  "ds_read_b64 v[204:205], v253 offset:17208\n\t" \
  "ds_read_b64 v[206:207], v253 offset:17608\n\t" \
  "v_and_b32 v116, 0xffff, v118\n\t" \
  "v_and_b32 v117, 0xffff0000, v118\n\t" \
  "v_and_b32 v118, 0xffff, v119\n\t" \
  "v_and_b32 v119, 0xffff0000, v119\n\t" \
  "s_add_u32 m0, s25, 89680\n\t" \
  "s_add_u32 s24, s24, s26\n\t" \
  "v_smfmac_f32_16x16x64_bf16 v[152:155], v[128:131], v[216:223], v234\n\t" \
  "buffer_load_dwordx4 v254, s[8:11], s24 offen lds\n\t" \
  "ds_read_b64 v[208:209], v253 offset:16416\n\t" \
  "ds_read_b64 v[210:211], v253 offset:16816\n\t" \
  "ds_read_b64 v[212:213], v253 offset:17216\n\t" \
  "ds_read_b64 v[214:215], v253 offset:17616\n\t" \
  "v_and_b32 v132, 0xffff, v134\n\t" \
  "v_and_b32 v133, 0xffff0000, v134\n\t" \
  "v_and_b32 v134, 0xffff, v135\n\t" \
  "v_and_b32 v135, 0xffff0000, v135\n\t" \
  "v_lshlrev_b32 v235, 1, v251\n\t" \
  "v_and_b32 v236, 0x70, v235\n\t" \
  "v_bfe_u32 v235, v250, 6, 1\n\t" \
  "s_waitcnt lgkmcnt(8)\n\t" \
  "s_add_u32 m0, s25, 97880\n\t" \
  "s_add_u32 s24, s24, s26\n\t" \
  "v_smfmac_f32_16x16x64_bf16 v[156:159], v[96:99], v[224:231], v234\n\t" \
  "buffer_load_dwordx4 v254, s[8:11], s24 offen lds\n\t" \
  "v_and_b32 v104, 0xffff, v106\n\t" \
  "v_and_b32 v105, 0xffff0000, v106\n\t" \
  "v_and_b32 v106, 0xffff, v107\n\t" \
  "v_and_b32 v107, 0xffff0000, v107\n\t" \
  "v_lshl_add_u32 v235, v235, 4, v250\n\t" \
  "v_add3_u32 v235, v235, v236, s61\n\t" \
  "v_smfmac_f32_16x16x64_bf16 v[144:147], v[100:103], v[224:231], v234\n\t" \
  "s_waitcnt vmcnt(9) lgkmcnt(0)\n\t" \
  "s_barrier\n\t" \
  "ds_read_b64 v[216:217], v253 offset:32800\n\t" \
  "ds_read_b64 v[218:219], v253 offset:33200\n\t" \
  "ds_read_b64 v[220:221], v253 offset:33600\n\t" \
  "ds_read_b64 v[222:223], v253 offset:34000\n\t" \
  "s_add_u32 m0, s25, 0\n\t" \
  "s_add_u32 s24, s24, s26\n\t" \
  "v_smfmac_f32_16x16x64_bf16 v[160:163], v[112:115], v[200:207], v234\n\t" \
  "buffer_load_dwordx4 v254, s[8:11], s24 offen lds\n\t" \
  "ds_read_b64 v[224:225], v253 offset:32808\n\t" \
  "ds_read_b64 v[226:227], v253 offset:33208\n\t" \
  "v_and_b32 v120, 0xffff, v122\n\t" \
  "v_and_b32 v121, 0xffff0000, v122\n\t" \
  "v_and_b32 v122, 0xffff, v123\n\t" \
  "v_and_b32 v123, 0xffff0000, v123\n\t" \
  "v_smfmac_f32_16x16x64_bf16 v[148:151], v[116:119], v[200:207], v234\n\t" \
  "ds_read_b64 v[228:229], v253 offset:33608\n\t" \
  "ds_read_b64 v[230:231], v253 offset:34008\n\t" \
  "ds_read_b64 v[36:37], v235 offset:0\n\t" \
  "ds_read_b64 v[38:39], v235 offset:8\n\t" \
  "s_add_u32 m0, s25, 8200\n\t" \
  "s_add_u32 s24, s24, s26\n\t" \
  "v_smfmac_f32_16x16x64_bf16 v[164:167], v[128:131], v[208:215], v234\n\t" \
  "buffer_load_dwordx4 v254, s[8:11], s24 offen lds\n\t" \
  "ds_read_b64 v[200:201], v253 offset:32816\n\t" \
  "ds_read_b64 v[202:203], v253 offset:33216\n\t" \
  "ds_read_b64 v[40:41], v235 offset:64\n\t" \
  "v_and_b32 v136, 0xffff, v138\n\t" \
  "v_and_b32 v137, 0xffff0000, v138\n\t" \
  "v_and_b32 v138, 0xffff, v139\n\t" \
  "v_and_b32 v139, 0xffff0000, v139\n\t" \
  "v_mfma_f32_32x32x16_bf16 v[0:15], v[242:245], v[240:243], 0\n\t" \
  "v_smfmac_f32_16x16x64_bf16 v[152:155], v[132:135], v[208:215], v234\n\t" \
  "ds_read_b64 v[204:205], v253 offset:33616\n\t" \
  "ds_read_b64 v[42:43], v235 offset:72\n\t" \
  "ds_read_b64 v[44:45], v235 offset:128\n\t" \
  "s_waitcnt lgkmcnt(12)\n\t" \
  "v_smfmac_f32_16x16x64_bf16 v[168:171], v[96:99], v[216:223], v234\n\t" \
  "ds_read_b64 v[206:207], v253 offset:34016\n\t" \
  "ds_read_b64 v[46:47], v235 offset:136\n\t" \
  "v_and_b32 v108, 0xffff, v110\n\t" \
  "v_and_b32 v109, 0xffff0000, v110\n\t" \
  "v_and_b32 v110, 0xffff, v111\n\t" \
  "v_and_b32 v111, 0xffff0000, v111\n\t" \
  "v_add_u32 v235, s58, v235\n\t" \
  "v_add_u32 v235, s59, v235\n\t" \
  "v_add_u32 v235, s60, v235\n\t" \
  "s_add_u32 m0, s25, 16400\n\t" \
  "s_add_u32 s24, s24, s26\n\t" \
  "v_smfmac_f32_16x16x64_bf16 v[156:159], v[100:103], v[216:223], v234\n\t" \
  "buffer_load_dwordx4 v254, s[8:11], s24 offen lds\n\t" \
  "v_smfmac_f32_16x16x64_bf16 v[144:147], v[104:107], v[216:223], v234\n\t" \
  "s_waitcnt vmcnt(11) lgkmcnt(0)\n\t" \
  "s_barrier\n\t" \
  "v_smfmac_f32_16x16x64_bf16 v[172:175], v[112:115], v[224:231], v234\n\t" \
  "ds_read_b64 v[208:209], v232 offset:0\n\t" \
  "ds_read_b64 v[210:211], v232 offset:400\n\t" \
  "v_and_b32 v124, 0xffff, v126\n\t" \
  "v_and_b32 v125, 0xffff0000, v126\n\t" \
  "v_and_b32 v126, 0xffff, v127\n\t" \
  "v_and_b32 v127, 0xffff0000, v127\n\t" \
  "s_add_u32 m0, s25, 24600\n\t" \
  "s_add_u32 s24, s24, s26\n\t" \
  "v_smfmac_f32_16x16x64_bf16 v[160:163], v[116:119], v[224:231], v234\n\t" \
  "buffer_load_dwordx4 v254, s[8:11], s24 offen lds\n\t" \
  "ds_read_b64 v[212:213], v232 offset:800\n\t" \
  "ds_read_b64 v[214:215], v232 offset:1200\n\t" \
  "v_smfmac_f32_16x16x64_bf16 v[148:151], v[120:123], v[224:231], v234\n\t" \
  "ds_read_b64 v[216:217], v232 offset:8\n\t" \
  "ds_read_b64 v[218:219], v232 offset:408\n\t" \
  "v_smfmac_f32_16x16x64_bf16 v[176:179], v[128:131], v[200:207], v234\n\t" \
  "ds_read_b64 v[220:221], v232 offset:808\n\t" \
  "ds_read_b64 v[222:223], v232 offset:1208\n\t" \
  "v_and_b32 v140, 0xffff, v142\n\t" \
  "v_and_b32 v141, 0xffff0000, v142\n\t" \
  "v_and_b32 v142, 0xffff, v143\n\t" \
  "v_and_b32 v143, 0xffff0000, v143\n\t" \
  "v_smfmac_f32_16x16x64_bf16 v[164:167], v[132:135], v[200:207], v234\n\t" \
  "ds_read_b64 v[224:225], v232 offset:16\n\t" \
  "s_add_u32 m0, s25, 32800\n\t" \
  "s_add_u32 s24, s24, s26\n\t" \
  "v_smfmac_f32_16x16x64_bf16 v[152:155], v[136:139], v[200:207], v234\n\t" \
  "buffer_load_dwordx4 v254, s[8:11], s24 offen lds\n\t" \
  "ds_read_b64 v[226:227], v232 offset:416\n\t" \
  "s_waitcnt lgkmcnt(6)\n\t" \
  "v_smfmac_f32_16x16x64_bf16 v[180:183], v[96:99], v[208:215], v234\n\t" \
  "ds_read_b64 v[228:229], v232 offset:816\n\t" \
  "v_smfmac_f32_16x16x64_bf16 v[168:171], v[100:103], v[208:215], v234\n\t" \
  "ds_read_b64 v[230:231], v232 offset:1216\n\t" \
  "v_mfma_f32_32x32x16_bf16 v[16:31], v[242:245], v[240:243], 0\n\t" \
  "v_mov_b32 v196, 0x3f003f00\n\t" \
  "v_mov_b32 v197, 0x3f003f00\n\t" \
  "v_smfmac_f32_16x16x64_bf16 v[156:159], v[104:107], v[208:215], v234\n\t" \
  "v_mov_b32 v198, 0x3f003f00\n\t" \
  "v_mov_b32 v199, 0x3f003f00\n\t" \
  "s_add_u32 m0, s25, 41000\n\t" \
  "s_add_u32 s24, s24, s26\n\t" \
  "v_smfmac_f32_16x16x64_bf16 v[144:147], v[108:111], v[208:215], v234\n\t" \
  "buffer_load_dwordx4 v254, s[8:11], s24 offen lds\n\t" \
  "s_waitcnt vmcnt(8) lgkmcnt(0)\n\t" \
  "s_barrier\n\t" \
  "v_smfmac_f32_16x16x64_bf16 v[184:187], v[112:115], v[216:223], v234\n\t" \
  "ds_read_b64 v[200:201], v232 offset:16400\n\t" \
  "ds_read_b64 v[202:203], v232 offset:16800\n\t" \
  "v_smfmac_f32_16x16x64_bf16 v[172:175], v[116:119], v[216:223], v234\n\t" \
  "ds_read_b64 v[204:205], v232 offset:17200\n\t" \
  "ds_read_b64 v[206:207], v232 offset:17600\n\t" \
  "v_smfmac_f32_16x16x64_bf16 v[160:163], v[120:123], v[216:223], v234\n\t" \
  "ds_read_b64 v[208:209], v232 offset:16408\n\t" \
  "v_cvt_pk_bf16_f32 v144, v144, v145\n\t" \
  "v_cvt_pk_bf16_f32 v145, v146, v147\n\t" \
  "s_add_u32 m0, s25, 56880\n\t" \
  "s_add_u32 s24, s24, s26\n\t" \
  "v_smfmac_f32_16x16x64_bf16 v[148:151], v[124:127], v[216:223], v234\n\t" \
  "buffer_load_dwordx4 v254, s[8:11], s24 offen lds\n\t" \
  "ds_read_b64 v[210:211], v232 offset:16808\n\t" \
  "v_smfmac_f32_16x16x64_bf16 v[188:191], v[128:131], v[224:231], v234\n\t" \
  "ds_read_b64 v[212:213], v232 offset:17208\n\t" \
  "v_smfmac_f32_16x16x64_bf16 v[176:179], v[132:135], v[224:231], v234\n\t" \
  "ds_read_b64 v[214:215], v232 offset:17608\n\t" \
  "v_smfmac_f32_16x16x64_bf16 v[164:167], v[136:139], v[224:231], v234\n\t" \
  "ds_read_b64 v[216:217], v232 offset:16416\n\t" \
  "v_cvt_pk_bf16_f32 v148, v148, v149\n\t" \
  "v_cvt_pk_bf16_f32 v149, v150, v151\n\t" \
  "s_add_u32 m0, s25, 65080\n\t" \
  "s_add_u32 s24, s24, s26\n\t" \
  "v_smfmac_f32_16x16x64_bf16 v[152:155], v[140:143], v[224:231], v234\n\t" \
  "buffer_load_dwordx4 v254, s[8:11], s24 offen lds\n\t" \
  "ds_read_b64 v[218:219], v232 offset:16816\n\t" \
  "s_waitcnt lgkmcnt(6)\n\t" \
  "v_smfmac_f32_16x16x64_bf16 v[0:3], v[96:99], v[200:207], v234\n\t" \
  "ds_read_b64 v[220:221], v232 offset:17216\n\t" \
  "v_smfmac_f32_16x16x64_bf16 v[180:183], v[100:103], v[200:207], v234\n\t" \
  "ds_read_b64 v[222:223], v232 offset:17616\n\t" \
  "v_mfma_f32_16x16x32_bf16 v[32:35], v[242:245], v[240:243], 0\n\t" \
  "v_smfmac_f32_16x16x64_bf16 v[168:171], v[104:107], v[200:207], v234\n\t" \
  "v_cvt_pk_bf16_f32 v152, v152, v153\n\t" \
  "v_cvt_pk_bf16_f32 v153, v154, v155\n\t" \
  "v_smfmac_f32_16x16x64_bf16 v[156:159], v[108:111], v[200:207], v234\n\t" \
  "s_waitcnt vmcnt(8) lgkmcnt(0)\n\t" \
  "s_barrier\n\t" \
  "s_add_u32 m0, s25, 73280\n\t" \
  "s_add_u32 s24, s24, s26\n\t" \
  "v_smfmac_f32_16x16x64_bf16 v[4:7], v[112:115], v[208:215], v234\n\t" \
  "buffer_load_dwordx4 v254, s[8:11], s24 offen lds\n\t" \
  "ds_read_b64 v[224:225], v232 offset:32800\n\t" \
  "ds_read_b64 v[226:227], v232 offset:33200\n\t" \
  "v_smfmac_f32_16x16x64_bf16 v[184:187], v[116:119], v[208:215], v234\n\t" \
  "ds_read_b64 v[228:229], v232 offset:33600\n\t" \
  "ds_read_b64 v[230:231], v232 offset:34000\n\t" \
  "s_add_u32 m0, s25, 81480\n\t" \
  "s_add_u32 s24, s24, s26\n\t" \
  "v_smfmac_f32_16x16x64_bf16 v[172:175], v[120:123], v[208:215], v234\n\t" \
  "buffer_load_dwordx4 v254, s[8:11], s24 offen lds\n\t" \
  "ds_read_b64 v[200:201], v232 offset:32808\n\t" \
  "v_cvt_pk_bf16_f32 v156, v156, v157\n\t" \
  "v_cvt_pk_bf16_f32 v157, v158, v159\n\t" \
  "v_smfmac_f32_16x16x64_bf16 v[160:163], v[124:127], v[208:215], v234\n\t" \
  "ds_read_b64 v[202:203], v232 offset:33208\n\t" \
  "v_smfmac_f32_16x16x64_bf16 v[8:11], v[128:131], v[216:223], v234\n\t" \
  "buffer_store_dwordx4 v[72:75], v250, s[20:23], s13 offen offset:0 sc1\n\t" \
  "ds_read_b64 v[204:205], v232 offset:33608\n\t" \
  "v_smfmac_f32_16x16x64_bf16 v[188:191], v[132:135], v[216:223], v234\n\t" \
  "ds_read_b64 v[206:207], v232 offset:34008\n\t" \
  "v_smfmac_f32_16x16x64_bf16 v[176:179], v[136:139], v[216:223], v234\n\t" \
  "ds_read_b64 v[208:209], v232 offset:32816\n\t" \
  "v_cvt_pk_bf16_f32 v160, v160, v161\n\t" \
  "v_cvt_pk_bf16_f32 v161, v162, v163\n\t" \
  "v_smfmac_f32_16x16x64_bf16 v[164:167], v[140:143], v[216:223], v234\n\t" \
  "ds_read_b64 v[210:211], v232 offset:33216\n\t" \
  "s_waitcnt lgkmcnt(6)\n\t" \
  "v_smfmac_f32_16x16x64_bf16 v[12:15], v[96:99], v[224:231], v234\n\t" \
  "ds_read_b64 v[212:213], v232 offset:33616\n\t" \
  "v_smfmac_f32_16x16x64_bf16 v[0:3], v[100:103], v[224:231], v234\n\t" \
  "ds_read_b64 v[214:215], v232 offset:34016\n\t" \
  "v_smfmac_f32_16x16x64_bf16 v[180:183], v[104:107], v[224:231], v234\n\t" \
  "s_nop 0\n\t" \
  "v_cvt_pk_bf16_f32 v164, v164, v165\n\t" \
  "v_cvt_pk_bf16_f32 v165, v166, v167\n\t" \
  "v_smfmac_f32_16x16x64_bf16 v[168:171], v[108:111], v[224:231], v234\n\t" \
  "s_waitcnt vmcnt(9) lgkmcnt(0)\n\t" \
  "s_barrier\n\t" \
  "v_smfmac_f32_16x16x64_bf16 v[16:19], v[112:115], v[200:207], v234\n\t" \
  "ds_write_b64 v193, v[144:145] offset:12288\n\t" \
  "ds_read_b64 v[216:217], v253 offset:0\n\t" \
  "v_smfmac_f32_16x16x64_bf16 v[4:7], v[116:119], v[200:207], v234\n\t" \
  "ds_write_b64 v193, v[148:149] offset:16384\n\t" \
  "ds_read_b64 v[218:219], v253 offset:400\n\t" \
  "v_smfmac_f32_16x16x64_bf16 v[184:187], v[120:123], v[200:207], v234\n\t" \
  "buffer_store_dwordx4 v[80:83], v250, s[20:23], s13 offen offset:64 sc1\n\t" \
  "ds_read_b64 v[220:221], v253 offset:800\n\t" \
  "ds_read_b64 v[222:223], v253 offset:1200\n\t" \
  "v_cvt_pk_bf16_f32 v168, v168, v169\n\t" \
  "v_cvt_pk_bf16_f32 v169, v170, v171\n\t" \
  "v_smfmac_f32_16x16x64_bf16 v[172:175], v[124:127], v[200:207], v234\n\t" \
  "ds_write_b64 v193, v[152:153] offset:20480\n\t" \
  "ds_read_b64 v[224:225], v253 offset:8\n\t" \
  "v_smfmac_f32_16x16x64_bf16 v[20:23], v[128:131], v[208:215], v234\n\t" \
  "ds_write_b64 v193, v[156:157] offset:0\n\t" \
  "ds_read_b64 v[226:227], v253 offset:408\n\t" \
  "v_smfmac_f32_16x16x64_bf16 v[8:11], v[132:135], v[208:215], v234\n\t" \
  "ds_write_b64 v193, v[160:161] offset:4096\n\t" \
  "ds_read_b64 v[228:229], v253 offset:808\n\t" \
  "v_smfmac_f32_16x16x64_bf16 v[188:191], v[136:139], v[208:215], v234\n\t" \
  "ds_write_b64 v193, v[164:165] offset:8192\n\t" \
  "ds_read_b64 v[230:231], v253 offset:1208\n\t" \
  "v_mfma_f32_16x16x32_bf16 v[72:75], v[242:245], v[240:243], 0\n\t" \
  "v_cvt_pk_bf16_f32 v172, v172, v173\n\t" \
  "v_cvt_pk_bf16_f32 v173, v174, v175\n\t" \
  "v_smfmac_f32_16x16x64_bf16 v[176:179], v[140:143], v[208:215], v234\n\t" \
  "ds_read_b64 v[200:201], v253 offset:16\n\t" \
  "ds_read_b64 v[202:203], v253 offset:416\n\t" \
  "s_waitcnt lgkmcnt(10)\n\t" \
  "v_smfmac_f32_16x16x64_bf16 v[24:27], v[96:99], v[216:223], v234\n\t" \
  "ds_read_b64 v[204:205], v253 offset:816\n\t" \
  "v_smfmac_f32_16x16x64_bf16 v[12:15], v[100:103], v[216:223], v234\n\t" \
  "ds_read_b64 v[206:207], v253 offset:1216\n\t" \
  "v_smfmac_f32_16x16x64_bf16 v[0:3], v[104:107], v[216:223], v234\n\t" \
  "v_mfma_f32_16x16x32_bf16 v[80:83], v[242:245], v[240:243], 0\n\t" \
  "v_cvt_pk_bf16_f32 v176, v176, v177\n\t" \
  "v_cvt_pk_bf16_f32 v177, v178, v179\n\t" \
  "v_smfmac_f32_16x16x64_bf16 v[180:183], v[108:111], v[216:223], v234\n\t" \
  "buffer_store_dwordx4 v[88:91], v250, s[20:23], s13 offen offset:128 sc1\n\t" \
  "s_waitcnt vmcnt(9) lgkmcnt(0)\n\t" \
  "s_barrier\n\t" \
  "v_smfmac_f32_16x16x64_bf16 v[28:31], v[112:115], v[224:231], v234\n\t" \
  "ds_write_b64 v192, v[168:169] offset:0\n\t" \
  "ds_read_b64 v[208:209], v253 offset:16400\n\t" \
  "ds_read_b128 v[144:147], v194 offset:0\n\t" \
  "v_smfmac_f32_16x16x64_bf16 v[16:19], v[116:119], v[224:231], v234\n\t" \
  "ds_write_b64 v192, v[172:173] offset:4096\n\t" \
  "ds_read_b64 v[210:211], v253 offset:16800\n\t" \
  "ds_read_b128 v[148:151], v194 offset:4096\n\t" \
  "v_smfmac_f32_16x16x64_bf16 v[4:7], v[120:123], v[224:231], v234\n\t" \
  "ds_write_b64 v192, v[176:177] offset:8192\n\t" \
  "ds_read_b64 v[212:213], v253 offset:17200\n\t" \
  "v_mfma_f32_16x16x32_bf16 v[88:91], v[242:245], v[240:243], 0\n\t" \
  "v_cvt_pk_bf16_f32 v180, v180, v181\n\t" \
  "v_cvt_pk_bf16_f32 v181, v182, v183\n\t" \
  "ds_read_b128 v[152:155], v194 offset:8192\n\t" \
  "v_smfmac_f32_16x16x64_bf16 v[184:187], v[124:127], v[224:231], v234\n\t" \
  "ds_read_b64 v[214:215], v253 offset:17600\n\t" \
  "ds_read_b64 v[216:217], v253 offset:16408\n\t" \
  "v_add_u32 v194, -93792, v194\n\t" \
  "v_smfmac_f32_16x16x64_bf16 v[32:35], v[128:131], v[200:207], v234\n\t" \
  "ds_read_b64 v[218:219], v253 offset:16808\n\t" \
  "ds_read_b64 v[220:221], v253 offset:17208\n\t" \
  "v_smfmac_f32_16x16x64_bf16 v[20:23], v[132:135], v[200:207], v234\n\t" \
  "ds_read_b64 v[222:223], v253 offset:17608\n\t" \
  "s_waitcnt lgkmcnt(11)\n\t" \
  "v_mfma_f32_16x16x32_bf16 v[168:171], v[144:147], v[144:147], 0\n\t" \
  "ds_read_b64 v[224:225], v253 offset:16416\n\t" \
  "v_smfmac_f32_16x16x64_bf16 v[8:11], v[136:139], v[200:207], v234\n\t" \
  "ds_read_b64 v[226:227], v253 offset:16816\n\t" \
  "v_cvt_pk_bf16_f32 v184, v184, v185\n\t" \
  "v_cvt_pk_bf16_f32 v185, v186, v187\n\t" \
  "v_mfma_f32_16x16x32_bf16 v[172:175], v[196:199], v[144:147], 0\n\t" \
  "ds_read_b64 v[228:229], v253 offset:17216\n\t" \
  "v_smfmac_f32_16x16x64_bf16 v[188:191], v[140:143], v[200:207], v234\n\t" \
  "ds_read_b64 v[230:231], v253 offset:17616\n\t" \
  "s_waitcnt lgkmcnt(12)\n\t" \
  "v_mfma_f32_16x16x32_bf16 v[168:171], v[148:151], v[148:151], v[168:171]\n\t" \
  "s_waitcnt lgkmcnt(8)\n\t" \
  "v_smfmac_f32_16x16x64_bf16 v[72:75], v[96:99], v[208:215], v234\n\t" \
  "v_mfma_f32_16x16x32_bf16 v[172:175], v[196:199], v[148:151], v[172:175]\n\t" \
  "v_smfmac_f32_16x16x64_bf16 v[24:27], v[100:103], v[208:215], v234\n\t" \
  "v_mfma_f32_16x16x32_bf16 v[168:171], v[152:155], v[152:155], v[168:171]\n\t" \
  "buffer_store_dwordx4 v[76:79], v250, s[20:23], s29 offen offset:0 sc1\n\t" \
  "v_smfmac_f32_16x16x64_bf16 v[12:15], v[104:107], v[208:215], v234\n\t" \
  "v_cvt_pk_bf16_f32 v188, v188, v189\n\t" \
  "v_cvt_pk_bf16_f32 v189, v190, v191\n\t" \
  "v_mfma_f32_16x16x32_bf16 v[172:175], v[196:199], v[152:155], v[172:175]\n\t" \
  "v_smfmac_f32_16x16x64_bf16 v[0:3], v[108:111], v[208:215], v234\n\t" \
  "s_waitcnt vmcnt(7) lgkmcnt(0)\n\t" \
  "s_barrier\n\t" \
  "v_smfmac_f32_16x16x64_bf16 v[80:83], v[112:115], v[216:223], v234\n\t" \
  "ds_write_b64 v192, v[180:181] offset:12288\n\t" \
  "ds_read_b64 v[200:201], v253 offset:32800\n\t" \
  "v_smfmac_f32_16x16x64_bf16 v[28:31], v[116:119], v[216:223], v234\n\t" \
  "ds_write_b64 v192, v[184:185] offset:16384\n\t" \
  "ds_read_b64 v[202:203], v253 offset:33200\n\t" \
  "v_cndmask_b32 v168, v168, v169, s[40:41]\n\t" \
  "v_cndmask_b32 v170, v170, v171, s[40:41]\n\t" \
  "v_smfmac_f32_16x16x64_bf16 v[16:19], v[120:123], v[216:223], v234\n\t" \
  "ds_write_b64 v192, v[188:189] offset:20480\n\t" \
  "ds_read_b64 v[204:205], v253 offset:33600\n\t" \
  "v_cvt_pk_bf16_f32 v0, v0, v1\n\t" \
  "v_cvt_pk_bf16_f32 v1, v2, v3\n\t" \
  "v_cndmask_b32 v173, v168, v170, s[42:43]\n\t" \
  "v_mov_b32 v174, 0x1127c5ac\n\t" \
  "ds_read_b64_tr_b16 v[156:157], v195 offset:0\n\t" \
  "v_smfmac_f32_16x16x64_bf16 v[4:7], v[124:127], v[216:223], v234\n\t" \
  "ds_read_b64 v[206:207], v253 offset:34000\n\t" \
  "ds_read_b64 v[208:209], v253 offset:32808\n\t" \
  "s_not_b64 vcc, s[52:53]\n\t" \
  "ds_read_b64_tr_b16 v[158:159], v195 offset:4096\n\t" \
  "v_smfmac_f32_16x16x64_bf16 v[88:91], v[128:131], v[224:231], v234\n\t" \
  "ds_read_b64 v[210:211], v253 offset:33208\n\t" \
  "ds_read_b64 v[212:213], v253 offset:33608\n\t" \
  "ds_read_b64 v[76:77], v235 offset:0\n\t" \
  "ds_read_b64 v[78:79], v235 offset:8\n\t" \
  "ds_read_b64_tr_b16 v[160:161], v195 offset:2048\n\t" \
  "v_smfmac_f32_16x16x64_bf16 v[32:35], v[132:135], v[224:231], v234\n\t" \
  "buffer_store_dwordx4 v[84:87], v250, s[20:23], s29 offen offset:64 sc1\n\t" \
  "ds_read_b64 v[214:215], v253 offset:34008\n\t" \
  "ds_read_b64 v[216:217], v253 offset:32816\n\t" \
  "ds_read_b64 v[84:85], v235 offset:64\n\t" \
  "ds_read_b64_tr_b16 v[162:163], v195 offset:6144\n\t" \
  "v_smfmac_f32_16x16x64_bf16 v[20:23], v[136:139], v[224:231], v234\n\t" \
  "ds_read_b64 v[218:219], v253 offset:33216\n\t" \
  "ds_read_b64 v[86:87], v235 offset:72\n\t" \
  "v_cvt_pk_bf16_f32 v4, v4, v5\n\t" \
  "v_cvt_pk_bf16_f32 v5, v6, v7\n\t" \
  "ds_read_b64_tr_b16 v[164:165], v195 offset:8192\n\t" \
  "v_smfmac_f32_16x16x64_bf16 v[8:11], v[140:143], v[224:231], v234\n\t" \
  "ds_read_b64 v[220:221], v253 offset:33616\n\t" \
  "ds_read_b64 v[236:237], v235 offset:128\n\t" \
  "ds_read_b64_tr_b16 v[166:167], v195 offset:10240\n\t" \
  "s_waitcnt lgkmcnt(15)\n\t" \
  "v_smfmac_f32_16x16x64_bf16 v[72:75], v[100:103], v[200:207], v234\n\t" \
  "ds_read_b64 v[222:223], v253 offset:34016\n\t" \
  "ds_read_b64 v[238:239], v235 offset:136\n\t" \
  "v_add_u32 v195, -93792, v195\n\t" \
  "v_smfmac_f32_16x16x64_bf16 v[24:27], v[104:107], v[200:207], v234\n\t" \
  "v_smfmac_f32_16x16x64_bf16 v[12:15], v[108:111], v[200:207], v234\n\t" \
  "v_cvt_pk_bf16_f32 v8, v8, v9\n\t" \
  "v_cvt_pk_bf16_f32 v9, v10, v11\n\t" \
  "s_waitcnt vmcnt(7) lgkmcnt(0)\n\t" \
  "s_barrier\n\t" \
  "v_smfmac_f32_16x16x64_bf16 v[80:83], v[116:119], v[208:215], v234\n\t" \
  "ds_write_b64 v192, v[0:1] offset:24576\n\t" \
  "ds_read_b64 v[224:225], v232 offset:0\n\t" \
  "ds_read_b128 v[180:183], v194 offset:0\n\t" \
  "ds_read_b128 v[184:187], v194 offset:4096\n\t" \
  "ds_read_b128 v[188:191], v194 offset:8192\n\t" \
  "v_smfmac_f32_16x16x64_bf16 v[28:31], v[120:123], v[208:215], v234\n\t" \
  "ds_write_b64 v192, v[4:5] offset:28672\n\t" \
  "ds_read_b64 v[226:227], v232 offset:400\n\t" \
  "ds_read_b64 v[228:229], v232 offset:800\n\t" \
  "v_smfmac_f32_16x16x64_bf16 v[16:19], v[124:127], v[208:215], v234\n\t" \
  "ds_write_b64 v192, v[8:9] offset:32768\n\t" \
  "ds_read_b64 v[230:231], v232 offset:1200\n\t" \
  "ds_read_b64 v[200:201], v232 offset:8\n\t" \
  "v_cvt_pk_bf16_f32 v12, v12, v13\n\t" \
  "v_cvt_pk_bf16_f32 v13, v14, v15\n\t" \
  "v_smfmac_f32_16x16x64_bf16 v[88:91], v[132:135], v[216:223], v234\n\t" \
  "ds_write_b64 v192, v[12:13] offset:36864\n\t" \
  "ds_read_b64 v[202:203], v232 offset:408\n\t" \
  "ds_read_b64 v[204:205], v232 offset:808\n\t" \
  "s_waitcnt lgkmcnt(11)\n\t" \
  "v_mfma_f32_16x16x32_bf16 v[176:179], v[180:183], v[180:183], 0\n\t" \
  "ds_read_b64 v[206:207], v232 offset:1208\n\t" \
  "v_mfma_f32_16x16x32_bf16 v[168:171], v[196:199], v[180:183], 0\n\t" \
  "ds_read_b64 v[208:209], v232 offset:16\n\t" \
  "v_smfmac_f32_16x16x64_bf16 v[32:35], v[136:139], v[216:223], v234\n\t" \
  "ds_read_b64 v[210:211], v232 offset:416\n\t" \
  "ds_read_b64 v[212:213], v232 offset:816\n\t" \
  "s_waitcnt lgkmcnt(14)\n\t" \
  "v_mfma_f32_16x16x32_bf16 v[176:179], v[184:187], v[184:187], v[176:179]\n\t" \
  "buffer_store_dwordx4 v[92:95], v250, s[20:23], s29 offen offset:128 sc1\n\t" \
  "v_mfma_f32_16x16x32_bf16 v[168:171], v[196:199], v[184:187], v[168:171]\n\t" \
  "ds_read_b64 v[214:215], v232 offset:1216\n\t" \
  "v_smfmac_f32_16x16x64_bf16 v[20:23], v[140:143], v[216:223], v234\n\t" \
  "v_cvt_pk_bf16_f32 v16, v16, v17\n\t" \
  "v_cvt_pk_bf16_f32 v17, v18, v19\n\t" \
  "s_waitcnt lgkmcnt(14)\n\t" \
  "v_mfma_f32_16x16x32_bf16 v[176:179], v[188:191], v[188:191], v[176:179]\n\t" \
  "ds_write_b64 v192, v[16:17] offset:40960\n\t" \
  "v_mfma_f32_16x16x32_bf16 v[168:171], v[196:199], v[188:191], v[168:171]\n\t" \
  "s_waitcnt lgkmcnt(10)\n\t" \
  "v_smfmac_f32_16x16x64_bf16 v[72:75], v[104:107], v[224:231], v234\n\t" \
  "v_mov_b32 v183, 0\n\t" \
  "v_mov_b32 v187, 0\n\t" \
  "s_nop 1\n\t" \
  "v_cndmask_b32 v176, v176, v177, s[40:41]\n\t" \
  "v_cndmask_b32 v178, v178, v179, s[40:41]\n\t" \
  "v_cndmask_b32 v176, v176, v178, s[42:43]\n\t" \
  "v_smfmac_f32_16x16x64_bf16 v[24:27], v[108:111], v[224:231], v234\n\t" \
  "v_cndmask_b32_dpp v172, v168, v172, vcc row_ror:8 row_mask:0xf bank_mask:0xf\n\t" \
  "v_mul_f32 v172, 0x3daaaaab, v172\n\t" \
  "v_cndmask_b32_dpp v173, v176, v173, vcc row_ror:8 row_mask:0xf bank_mask:0xf\n\t" \
  "v_fmamk_f32 v173, v173, 0x3e2aaaab, v174\n\t" \
  "s_waitcnt vmcnt(6) lgkmcnt(0)\n\t" \
  "s_barrier\n\t" \
  "ds_read_b64 v[216:217], v232 offset:16400\n\t" \
  "ds_read_b64 v[218:219], v232 offset:16800\n\t" \
  "ds_read_b64 v[220:221], v232 offset:17200\n\t" \
  "ds_read_b64 v[222:223], v232 offset:17600\n\t" \
  "v_smfmac_f32_16x16x64_bf16 v[80:83], v[120:123], v[200:207], v234\n\t" \
  "ds_read_b64 v[224:225], v232 offset:16408\n\t" \
  "ds_read_b64 v[226:227], v232 offset:16808\n\t" \
  "v_cvt_pk_bf16_f32 v20, v20, v21\n\t" \
  "v_cvt_pk_bf16_f32 v21, v22, v23\n\t" \
  "v_fma_f32 v173, -v172, v172, v173\n\t" \
  "v_max_f32 v173, v174, v173\n\t" \
  "v_rsq_f32 v173, v173 div:2\n\t" \
  "s_mov_b64 exec, s[52:53]\n\tv_mul_f32 v173, 0x08805555, v173\n\ts_mov_b64 exec, -1\n\t" \
  "v_smfmac_f32_16x16x64_bf16 v[28:31], v[124:127], v[200:207], v234\n\t" \
  "ds_write_b64 v192, v[20:21] offset:45056\n\t" \
  "ds_read_b64 v[228:229], v232 offset:17208\n\t" \
  "s_mov_b64 vcc, s[40:41]\n\t" \
  "v_cvt_pk_bf16_f32 v175, v173, v173\n\t" \
  "v_and_b32 v173, 0xffff0000, v175\n\t" \
  "v_mul_f32 v174, -v172, v173 div:2\n\t" \
  "v_cndmask_b32_sdwa v175, v175, v173, vcc dst_sel:DWORD dst_unused:UNUSED_PAD src0_sel:WORD_1 src1_sel:DWORD\n\t" \
  "v_smfmac_f32_16x16x64_bf16 v[88:91], v[136:139], v[208:215], v234\n\t" \
  "ds_read_b64 v[230:231], v232 offset:17608\n\t" \
  "ds_read_b64 v[200:201], v232 offset:16416\n\t" \
  "v_cvt_pk_bf16_f32 v24, v24, v25\n\t" \
  "v_cvt_pk_bf16_f32 v25, v26, v27\n\t" \
  "v_and_b32 v172, 0xffff0000, v174\n\t" \
  "s_or_b64 vcc, s[52:53], s[42:43]\n\t" \
  "v_sub_f32 v174, v174, v172\n\t" \
  "v_cndmask_b32 v180, v175, v240, vcc\n\t" \
  "v_smfmac_f32_16x16x64_bf16 v[32:35], v[140:143], v[208:215], v234\n\t" \
  "ds_write_b64 v192, v[24:25] offset:56880\n\t" \
  "ds_read_b64 v[202:203], v232 offset:16816\n\t" \
  "v_perm_b32 v174, v172, v174, s55\n\t" \
  "v_cndmask_b32_dpp v184, v175, v240, vcc row_ror:8 row_mask:0xf bank_mask:0xf\n\t" \
  "s_mov_b64 vcc, s[52:53]\n\t" \
  "v_cndmask_b32 v182, v174, v240, vcc\n\t" \
  "ds_read_b64_tr_b16 v[144:145], v195 offset:0\n\t" \
  "ds_read_b64_tr_b16 v[146:147], v195 offset:4096\n\t" \
  "s_waitcnt lgkmcnt(10)\n\t" \
  "v_smfmac_f32_16x16x64_bf16 v[72:75], v[108:111], v[216:223], v234\n\t" \
  "ds_read_b64 v[204:205], v232 offset:17216\n\t" \
  "ds_read_b64 v[206:207], v232 offset:17616\n\t" \
  "v_cvt_pk_bf16_f32 v28, v28, v29\n\t" \
  "v_cvt_pk_bf16_f32 v29, v30, v31\n\t" \
  "v_cndmask_b32_dpp v186, v174, v240, vcc row_ror:8 row_mask:0xf bank_mask:0xf\n\t" \
  "s_orn2_b64 vcc, s[52:53], s[42:43]\n\t" \
  "v_cndmask_b32 v181, v175, v240, vcc\n\t" \
  "v_cndmask_b32_dpp v185, v175, v240, vcc row_ror:8 row_mask:0xf bank_mask:0xf\n\t" \
  "ds_read_b64_tr_b16 v[148:149], v195 offset:2048\n\t" \
  "ds_read_b64_tr_b16 v[150:151], v195 offset:6144\n\t" \
  "s_waitcnt lgkmcnt(9)\n\t" \
  "v_smfmac_f32_16x16x64_bf16 v[80:83], v[124:127], v[224:231], v234\n\t" \
  "ds_write_b64 v192, v[28:29] offset:60976\n\t" \
  "ds_read_b64_tr_b16 v[152:153], v195 offset:8192\n\t" \
  "ds_read_b64_tr_b16 v[154:155], v195 offset:10240\n\t" \
  "v_mfma_f32_16x16x16_bf16 v[0:3], v[196:197], v[182:183], 0\n\t" \
  "v_mfma_f32_16x16x16_bf16 v[4:7], v[196:197], v[186:187], 0\n\t" \
  "s_waitcnt lgkmcnt(5)\n\t" \
  "v_smfmac_f32_16x16x64_bf16 v[88:91], v[140:143], v[200:207], v234\n\t" \
  "v_cvt_pk_bf16_f32 v32, v32, v33\n\t" \
  "v_cvt_pk_bf16_f32 v33, v34, v35\n\t" \
  "v_mfma_f32_16x16x16_bf16 v[188:191], v[156:157], v[180:181], v[0:3]\n\t" \
  "ds_write_b64 v192, v[32:33] offset:65072\n\t" \
  "v_mfma_f32_16x16x16_bf16 v[176:179], v[158:159], v[180:181], v[0:3]\n\t" \
  "v_cvt_pk_bf16_f32 v72, v72, v73\n\t" \
  "v_cvt_pk_bf16_f32 v73, v74, v75\n\t" \
  "ds_write_b64 v193, v[72:73] offset:0\n\t" \
  "v_cvt_pk_bf16_f32 v80, v80, v81\n\t" \
  "v_cvt_pk_bf16_f32 v81, v82, v83\n\t" \
  "ds_write_b64 v193, v[80:81] offset:4096\n\t" \
  "v_mfma_f32_16x16x16_bf16 v[168:171], v[160:161], v[180:181], v[0:3]\n\t" \
  "v_cvt_pk_bf16_f32 v96, v188, v189\n\t" \
  "v_cvt_pk_bf16_f32 v97, v190, v191\n\t" \
  "v_mfma_f32_16x16x16_bf16 v[172:175], v[162:163], v[180:181], v[0:3]\n\t" \
  "v_cvt_pk_bf16_f32 v88, v88, v89\n\t" \
  "v_cvt_pk_bf16_f32 v89, v90, v91\n\t" \
  "ds_write_b64 v193, v[88:89] offset:8192\n\t" \
  "v_cvt_pk_bf16_f32 v98, v176, v177\n\t" \
  "v_cvt_pk_bf16_f32 v99, v178, v179\n\t" \
  "v_mfma_f32_16x16x16_bf16 v[188:191], v[164:165], v[180:181], v[0:3]\n\t" \
  "v_cvt_pk_bf16_f32 v112, v168, v169\n\t" \
  "v_cvt_pk_bf16_f32 v113, v170, v171\n\t" \
  "v_mfma_f32_16x16x16_bf16 v[176:179], v[166:167], v[180:181], v[0:3]\n\t" \
  "v_cvt_pk_bf16_f32 v114, v172, v173\n\t" \
  "v_cvt_pk_bf16_f32 v115, v174, v175\n\t" \
  "s_nop 2\n\t" \
  "v_cvt_pk_bf16_f32 v128, v188, v189\n\t" \
  "v_cvt_pk_bf16_f32 v129, v190, v191\n\t" \
  "s_nop 0\n\t" \
  "v_cvt_pk_bf16_f32 v130, v176, v177\n\t" \
  "v_cvt_pk_bf16_f32 v131, v178, v179\n\t" \
  "s_waitcnt lgkmcnt(0)\n\t" \
  "s_barrier\n\t" \
  "ds_read_b64_tr_b16 v[208:209], v248 offset:43048\n\t" \
  "ds_read_b64_tr_b16 v[210:211], v248 offset:44072\n\t" \
  "ds_read_b128 v[104:107], v194 offset:24576\n\t" \
  "ds_read_b128 v[120:123], v194 offset:28672\n\t" \
  "ds_read_b128 v[136:139], v194 offset:32768\n\t" \
  "v_add_u32 v194, s54, v194\n\t" \
  "ds_read_b128 v[108:111], v194 offset:49152\n\t" \
  "ds_read_b128 v[124:127], v194 offset:53248\n\t" \
  "ds_read_b128 v[140:143], v194 offset:57344\n\t" \
  "v_mfma_f32_16x16x16_bf16 v[168:171], v[144:145], v[184:185], v[4:7]\n\t" \
  "ds_read_b64_tr_b16 v[212:213], v248 offset:46120\n\t" \
  "ds_read_b64_tr_b16 v[214:215], v248 offset:47144\n\t" \
  "v_mfma_f32_16x16x16_bf16 v[172:175], v[146:147], v[184:185], v[4:7]\n\t" \
  "ds_read_b64_tr_b16 v[216:217], v248 offset:45096\n\t" \
  "ds_read_b64_tr_b16 v[218:219], v248 offset:48168\n\t" \
  "s_mov_b32 m0, s44\n\t" \
  "s_mov_b32 exec_hi, 0xffff\n\t" \
  "buffer_load_dwordx4 v252, s[16:19], s33 offen lds\n\t" \
  "s_mov_b32 exec_hi, -1\n\t" \
  "s_add_u32 s33, s33, s32\n\t" \
  "v_mfma_f32_16x16x16_bf16 v[188:191], v[148:149], v[184:185], v[4:7]\n\t" \
  "ds_read_b64_tr_b16 v[160:161], v248 offset:43560\n\t" \
  "ds_read_b64_tr_b16 v[162:163], v248 offset:44584\n\t" \
  "v_cvt_pk_bf16_f32 v100, v168, v169\n\t" \
  "v_cvt_pk_bf16_f32 v101, v170, v171\n\t" \
  "v_mfma_f32_16x16x16_bf16 v[176:179], v[150:151], v[184:185], v[4:7]\n\t" \
  "ds_read_b64_tr_b16 v[164:165], v248 offset:46632\n\t" \
  "ds_read_b64_tr_b16 v[166:167], v248 offset:47656\n\t" \
  "v_cvt_pk_bf16_f32 v102, v172, v173\n\t" \
  "v_cvt_pk_bf16_f32 v103, v174, v175\n\t" \
  "v_mfma_f32_16x16x16_bf16 v[168:171], v[152:153], v[184:185], v[4:7]\n\t" \
  "ds_read_b64_tr_b16 v[232:233], v248 offset:45608\n\t" \
  "ds_read_b64_tr_b16 v[234:235], v248 offset:48680\n\t" \
  "v_cvt_pk_bf16_f32 v116, v188, v189\n\t" \
  "v_cvt_pk_bf16_f32 v117, v190, v191\n\t" \
  "v_mfma_f32_16x16x16_bf16 v[172:175], v[154:155], v[184:185], v[4:7]\n\t" \
  "ds_read_b64_tr_b16 v[24:25], v195 offset:24576\n\t" \
  "ds_read_b64_tr_b16 v[26:27], v195 offset:28672\n\t" \
  "v_cvt_pk_bf16_f32 v118, v176, v177\n\t" \
  "v_cvt_pk_bf16_f32 v119, v178, v179\n\t" \
  "s_waitcnt lgkmcnt(15)\n\t" \
  "v_mfma_f32_16x16x32_bf16 v[144:147], v[208:211], v[96:99], 0\n\t" \
  "ds_read_b64_tr_b16 v[28:29], v195 offset:26624\n\t" \
  "ds_read_b64_tr_b16 v[30:31], v195 offset:30720\n\t" \
  "v_cvt_pk_bf16_f32 v132, v168, v169\n\t" \
  "v_cvt_pk_bf16_f32 v133, v170, v171\n\t" \
  "s_waitcnt lgkmcnt(12)\n\t" \
  "v_mfma_f32_16x16x32_bf16 v[144:147], v[212:215], v[112:115], v[144:147]\n\t" \
  "ds_read_b64_tr_b16 v[32:33], v195 offset:32768\n\t" \
  "ds_read_b64_tr_b16 v[34:35], v195 offset:34816\n\t" \
  "v_cvt_pk_bf16_f32 v134, v172, v173\n\t" \
  "v_cvt_pk_bf16_f32 v135, v174, v175\n\t" \
  "v_mfma_f32_16x16x32_bf16 v[152:155], v[208:211], v[100:103], 0\n\t" \
  "v_add_u32 v195, s54, v195\n\t" \
  "ds_read_b64_tr_b16 v[72:73], v195 offset:49152\n\t" \
  "ds_read_b64_tr_b16 v[74:75], v195 offset:53248\n\t" \
  "s_waitcnt lgkmcnt(14)\n\t" \
  "v_mfma_f32_16x16x32_bf16 v[144:147], v[216:219], v[128:131], v[144:147]\n\t" \
  "ds_read_b64_tr_b16 v[80:81], v195 offset:51200\n\t" \
  "s_add_u32 m0, s44, s35\n\t" \
  "s_mov_b32 exec_hi, 0xffff\n\t" \
  "buffer_load_dwordx4 v252, s[16:19], s34 offen lds\n\t" \
  "s_mov_b32 exec_hi, -1\n\t" \
  "s_add_u32 s34, s34, s32\n\t" \
  "v_mfma_f32_16x16x32_bf16 v[152:155], v[212:215], v[116:119], v[152:155]\n\t" \
  "ds_read_b64_tr_b16 v[82:83], v195 offset:55296\n\t" \
  "v_mfma_f32_16x16x32_bf16 v[152:155], v[216:219], v[132:135], v[152:155]\n\t" \
  "ds_read_b64_tr_b16 v[88:89], v195 offset:57344\n\t" \
  "v_mfma_f32_16x16x32_bf16 v[8:11], v[104:107], v[104:107], 0\n\t" \
  "ds_read_b64_tr_b16 v[90:91], v195 offset:59392\n\t" \
  "s_mov_b32 m0, s45\n\t" \
  "s_mov_b32 exec_hi, 0xffff\n\t" \
  "buffer_load_dwordx4 v252, s[16:19], s33 offen lds\n\t" \
  "s_mov_b32 exec_hi, -1\n\t" \
  "s_add_u32 s33, s33, s32\n\t" \
  "v_mfma_f32_16x16x32_bf16 v[12:15], v[196:199], v[104:107], 0\n\t" \
  "v_cvt_pknorm_i16_f32 v220, |v146|, |v147|\n\t" \
  "v_cvt_pknorm_i16_f32 v222, |v144|, |v145|\n\t" \
  "v_mfma_f32_16x16x32_bf16 v[8:11], v[120:123], v[120:123], v[8:11]\n\t" \
  "v_lshlrev_b32_sdwa v221, 2, v220 dst_sel:DWORD dst_unused:UNUSED_PAD src0_sel:DWORD src1_sel:BYTE_3\n\t" \
  "v_lshlrev_b32_sdwa v220, 2, v220 dst_sel:DWORD dst_unused:UNUSED_PAD src0_sel:DWORD src1_sel:BYTE_1\n\t" \
  "v_mfma_f32_16x16x32_bf16 v[12:15], v[196:199], v[120:123], v[12:15]\n\t" \
  "ds_read_b32 v221, v221 offset:10016\n\t" \
  "v_lshlrev_b32_sdwa v223, 2, v222 dst_sel:DWORD dst_unused:UNUSED_PAD src0_sel:DWORD src1_sel:BYTE_3\n\t" \
  "v_lshlrev_b32_sdwa v222, 2, v222 dst_sel:DWORD dst_unused:UNUSED_PAD src0_sel:DWORD src1_sel:BYTE_1\n\t" \
  "v_mfma_f32_16x16x32_bf16 v[8:11], v[136:139], v[136:139], v[8:11]\n\t" \
  "ds_read_b32 v220, v220 offset:10016\n\t" \
  "v_lshlrev_b32_sdwa v225, 1, v155 dst_sel:DWORD dst_unused:UNUSED_PAD src0_sel:DWORD src1_sel:WORD_1\n\t" \
  "v_lshlrev_b32_sdwa v224, 1, v154 dst_sel:DWORD dst_unused:UNUSED_PAD src0_sel:DWORD src1_sel:WORD_1\n\t" \
  "v_mfma_f32_16x16x32_bf16 v[12:15], v[196:199], v[136:139], v[12:15]\n\t" \
  "ds_read_b32 v223, v223 offset:10016\n\t" \
  "v_lshlrev_b32_sdwa v227, 1, v153 dst_sel:DWORD dst_unused:UNUSED_PAD src0_sel:DWORD src1_sel:WORD_1\n\t" \
  "v_lshlrev_b32_sdwa v226, 1, v152 dst_sel:DWORD dst_unused:UNUSED_PAD src0_sel:DWORD src1_sel:WORD_1\n\t" \
  "v_mfma_f32_16x16x32_bf16 v[16:19], v[108:111], v[108:111], 0\n\t" \
  "ds_read_b32 v222, v222 offset:10016\n\t" \
  "v_mfma_f32_16x16x32_bf16 v[20:23], v[196:199], v[108:111], 0\n\t" \
  "ds_read_u16_d16_hi v225, v225 offset:10528\n\t" \
  "s_add_u32 m0, s45, s35\n\t" \
  "s_mov_b32 exec_hi, 0xffff\n\t" \
  "buffer_load_dwordx4 v252, s[16:19], s34 offen lds\n\t" \
  "s_mov_b32 exec_hi, -1\n\t" \
  "s_add_u32 s34, s34, s32\n\t" \
  "v_cndmask_b32 v8, v8, v9, s[40:41]\n\t" \
  "v_cndmask_b32 v10, v10, v11, s[40:41]\n\t" \
  "v_mfma_f32_16x16x32_bf16 v[16:19], v[124:127], v[124:127], v[16:19]\n\t" \
  "ds_read_u16 v224, v224 offset:10528\n\t" \
  "v_cndmask_b32 v13, v8, v10, s[42:43]\n\t" \
  "v_mov_b32 v14, 0x1127c5ac\n\t" \
  "v_mfma_f32_16x16x32_bf16 v[20:23], v[196:199], v[124:127], v[20:23]\n\t" \
  "ds_read_u16_d16_hi v227, v227 offset:10528\n\t" \
  "s_not_b64 vcc, s[52:53]\n\t" \
  "v_mfma_f32_16x16x32_bf16 v[16:19], v[140:143], v[140:143], v[16:19]\n\t" \
  "ds_read_u16 v226, v226 offset:10528\n\t" \
  "s_waitcnt lgkmcnt(4)\n\t" \
  "v_fma_mix_f32 v221, |v147|, v221, v221 op_sel:[0,1,0] op_sel_hi:[0,1,1] clamp\n\t" \
  "v_fma_mix_f32 v220, |v146|, v220, v220 op_sel:[0,1,0] op_sel_hi:[0,1,1] clamp\n\t" \
  "v_mfma_f32_16x16x32_bf16 v[20:23], v[196:199], v[140:143], v[20:23]\n\t" \
  "buffer_store_dwordx4 v[48:51], v250, s[20:23], s12 offen offset:0 sc1\n\t" \
  "v_fma_mix_f32 v223, |v145|, v223, v223 op_sel:[0,1,0] op_sel_hi:[0,1,1] clamp\n\t" \
  "v_fma_mix_f32 v222, |v144|, v222, v222 op_sel:[0,1,0] op_sel_hi:[0,1,1] clamp\n\t" \
  "v_mfma_f32_16x16x32_bf16 v[148:151], v[160:163], v[96:99], 0\n\t" \
  "s_waitcnt lgkmcnt(0)\n\t" \
  "v_or_b32 v155, v224, v225\n\t" \
  "v_or_b32 v154, v226, v227\n\t" \
  "v_mfma_f32_16x16x32_bf16 v[156:159], v[160:163], v[100:103], 0\n\t" \
  "v_cndmask_b32 v16, v16, v17, s[40:41]\n\t" \
  "v_cndmask_b32 v18, v18, v19, s[40:41]\n\t" \
  "v_cndmask_b32 v16, v16, v18, s[42:43]\n\t" \
  "v_cndmask_b32_dpp v12, v20, v12, vcc row_ror:8 row_mask:0xf bank_mask:0xf\n\t" \
  "v_mul_f32 v12, 0x3daaaaab, v12\n\t" \
  "v_mfma_f32_16x16x32_bf16 v[148:151], v[164:167], v[112:115], v[148:151]\n\t" \
  "v_cndmask_b32_dpp v13, v16, v13, vcc row_ror:8 row_mask:0xf bank_mask:0xf\n\t" \
  "v_fmamk_f32 v13, v13, 0x3e2aaaab, v14\n\t" \
  "v_fma_f32 v13, -v12, v12, v13\n\t" \
  "v_max_f32 v13, v14, v13\n\t" \
  "v_rsq_f32 v13, v13 div:2\n\t" \
  "v_mfma_f32_16x16x32_bf16 v[156:159], v[164:167], v[116:119], v[156:159]\n\t" \
  "s_mov_b64 exec, s[52:53]\n\tv_mul_f32 v13, 0x08805555, v13\n\ts_mov_b64 exec, -1\n\t" \
  "s_mov_b64 vcc, s[40:41]\n\t" \
  "v_cvt_pk_bf16_f32 v15, v13, v13\n\t" \
  "v_and_b32 v13, 0xffff0000, v15\n\t" \
  "v_mul_f32 v14, -v12, v13 div:2\n\t" \
  "v_cndmask_b32_sdwa v15, v15, v13, vcc dst_sel:DWORD dst_unused:UNUSED_PAD src0_sel:WORD_1 src1_sel:DWORD\n\t" \
  "v_and_b32 v12, 0xffff0000, v14\n\t" \
  "v_mfma_f32_16x16x32_bf16 v[148:151], v[232:235], v[128:131], v[148:151]\n\t" \
  "s_or_b64 vcc, s[52:53], s[42:43]\n\t" \
  "v_sub_f32 v14, v14, v12\n\t" \
  "v_cndmask_b32 v180, v15, v240, vcc\n\t" \
  "v_perm_b32 v14, v12, v14, s55\n\t" \
  "v_cndmask_b32_dpp v184, v15, v240, vcc row_ror:8 row_mask:0xf bank_mask:0xf\n\t" \
  "v_mfma_f32_16x16x32_bf16 v[156:159], v[232:235], v[132:135], v[156:159]\n\t" \
  "s_mov_b64 vcc, s[52:53]\n\t" \
  "v_cndmask_b32 v182, v14, v240, vcc\n\t" \
  "v_cndmask_b32_dpp v186, v14, v240, vcc row_ror:8 row_mask:0xf bank_mask:0xf\n\t" \
  "s_orn2_b64 vcc, s[52:53], s[42:43]\n\t" \
  "v_cndmask_b32 v181, v15, v240, vcc\n\t" \
  "v_mfma_f32_16x16x16_bf16 v[0:3], v[196:197], v[182:183], 0\n\t" \
  "v_cndmask_b32_dpp v185, v15, v240, vcc row_ror:8 row_mask:0xf bank_mask:0xf\n\t" \
  "s_mov_b64 vcc, 0\n\t" \
  "v_fma_f32 v147, |v147|, v221, v147 mul:4\n\t" \
  "v_mfma_f32_16x16x16_bf16 v[4:7], v[196:197], v[186:187], 0\n\t" \
  "v_fma_f32 v146, |v146|, v220, v146 mul:4\n\t" \
  "v_fma_f32 v145, |v145|, v223, v145 mul:4\n\t" \
  "v_mfma_f32_16x16x16_bf16 v[188:191], v[24:25], v[180:181], v[0:3]\n\t" \
  "buffer_store_dwordx4 v[56:59], v250, s[20:23], s12 offen offset:64 sc1\n\t" \
  "v_fma_f32 v144, |v144|, v222, v144 mul:4\n\t" \
  "v_cvt_pk_bf16_f32 v147, v146, v147\n\t" \
  "v_mfma_f32_16x16x16_bf16 v[176:179], v[26:27], v[180:181], v[0:3]\n\t" \
  "v_cvt_pk_bf16_f32 v146, v144, v145\n\t" \
  "v_cvt_pknorm_i16_f32 v228, |v148|, |v149|\n\t" \
  "v_mfma_f32_16x16x16_bf16 v[200:203], v[72:73], v[184:185], v[4:7]\n\t" \
  "s_nop 0\n\t" \
  "v_cvt_pk_bf16_f32 v104, v188, v189\n\t" \
  "v_cvt_pk_bf16_f32 v105, v190, v191\n\t" \
  "v_mfma_f32_16x16x16_bf16 v[204:207], v[74:75], v[184:185], v[4:7]\n\t" \
  "s_nop 0\n\t" \
  "v_cvt_pk_bf16_f32 v106, v176, v177\n\t" \
  "v_cvt_pk_bf16_f32 v107, v178, v179\n\t" \
  "v_mfma_f32_16x16x16_bf16 v[196:199], v[28:29], v[180:181], v[0:3]\n\t" \
  "v_cvt_pk_bf16_f32 v108, v200, v201\n\t" \
  "v_cvt_pk_bf16_f32 v109, v202, v203\n\t" \
  "v_mfma_f32_16x16x16_bf16 v[188:191], v[30:31], v[180:181], v[0:3]\n\t" \
  "s_nop 0\n\t" \
  "v_cvt_pk_bf16_f32 v110, v204, v205\n\t" \
  "v_cvt_pk_bf16_f32 v111, v206, v207\n\t" \
  "v_mfma_f32_16x16x32_bf16 v[160:163], v[208:211], v[104:107], 0\n\t" \
  "s_nop 0\n\t" \
  "v_cvt_pk_bf16_f32 v120, v196, v197\n\t" \
  "v_cvt_pk_bf16_f32 v121, v198, v199\n\t" \
  "v_mfma_f32_16x16x16_bf16 v[176:179], v[80:81], v[184:185], v[4:7]\n\t" \
  "v_cvt_pk_bf16_f32 v122, v188, v189\n\t" \
  "v_cvt_pk_bf16_f32 v123, v190, v191\n\t" \
  "v_mfma_f32_16x16x16_bf16 v[200:203], v[82:83], v[184:185], v[4:7]\n\t" \
  "v_cvt_pknorm_i16_f32 v230, |v150|, |v151|\n\t" \
  "v_lshlrev_b32_sdwa v229, 2, v228 dst_sel:DWORD dst_unused:UNUSED_PAD src0_sel:DWORD src1_sel:BYTE_3\n\t" \
  "v_mfma_f32_16x16x32_bf16 v[168:171], v[208:211], v[108:111], 0\n\t" \
  "buffer_store_dwordx4 v[64:67], v250, s[20:23], s12 offen offset:128 sc1\n\t" \
  "ds_read_b64_tr_b16 v[208:209], v248 offset:43560\n\t" \
  "v_cvt_pk_bf16_f32 v124, v176, v177\n\t" \
  "v_cvt_pk_bf16_f32 v125, v178, v179\n\t" \
  "v_mfma_f32_16x16x16_bf16 v[204:207], v[32:33], v[180:181], v[0:3]\n\t" \
  "ds_read_b64_tr_b16 v[210:211], v248 offset:44584\n\t" \
  "v_cvt_pk_bf16_f32 v126, v200, v201\n\t" \
  "v_cvt_pk_bf16_f32 v127, v202, v203\n\t" \
  "v_mfma_f32_16x16x16_bf16 v[196:199], v[34:35], v[180:181], v[0:3]\n\t" \
  "v_lshlrev_b32_sdwa v228, 2, v228 dst_sel:DWORD dst_unused:UNUSED_PAD src0_sel:DWORD src1_sel:BYTE_1\n\t" \
  "v_lshlrev_b32_sdwa v231, 2, v230 dst_sel:DWORD dst_unused:UNUSED_PAD src0_sel:DWORD src1_sel:BYTE_3\n\t" \
  "v_mfma_f32_16x16x32_bf16 v[160:163], v[212:215], v[120:123], v[160:163]\n\t" \
  "ds_read_b32 v229, v229 offset:10016\n\t" \
  "v_cvt_pk_bf16_f32 v136, v204, v205\n\t" \
  "v_cvt_pk_bf16_f32 v137, v206, v207\n\t" \
  "v_mfma_f32_16x16x16_bf16 v[188:191], v[88:89], v[184:185], v[4:7]\n\t" \
  "ds_read_b32 v228, v228 offset:10016\n\t" \
  "v_cvt_pk_bf16_f32 v138, v196, v197\n\t" \
  "v_cvt_pk_bf16_f32 v139, v198, v199\n\t" \
  "v_mfma_f32_16x16x16_bf16 v[176:179], v[90:91], v[184:185], v[4:7]\n\t" \
  "v_lshlrev_b32_sdwa v230, 2, v230 dst_sel:DWORD dst_unused:UNUSED_PAD src0_sel:DWORD src1_sel:BYTE_1\n\t" \
  "v_lshlrev_b32_sdwa v233, 1, v157 dst_sel:DWORD dst_unused:UNUSED_PAD src0_sel:DWORD src1_sel:WORD_1\n\t" \
  "v_mfma_f32_16x16x32_bf16 v[168:171], v[212:215], v[124:127], v[168:171]\n\t" \
  "ds_read_b64_tr_b16 v[212:213], v248 offset:46632\n\t" \
  "v_cvt_pk_bf16_f32 v140, v188, v189\n\t" \
  "v_cvt_pk_bf16_f32 v141, v190, v191\n\t" \
  "v_mfma_f32_16x16x32_bf16 v[160:163], v[216:219], v[136:139], v[160:163]\n\t" \
  "ds_read_b64_tr_b16 v[214:215], v248 offset:47656\n\t" \
  "v_cvt_pk_bf16_f32 v142, v176, v177\n\t" \
  "v_cvt_pk_bf16_f32 v143, v178, v179\n\t" \
  "s_nop 1\n\t" \
  "v_mfma_f32_16x16x32_bf16 v[168:171], v[216:219], v[140:143], v[168:171]\n\t" \
  "ds_read_b64_tr_b16 v[216:217], v248 offset:45608\n\t" \
  "v_lshlrev_b32_sdwa v232, 1, v156 dst_sel:DWORD dst_unused:UNUSED_PAD src0_sel:DWORD src1_sel:WORD_1\n\t" \
  "v_lshlrev_b32_sdwa v235, 1, v159 dst_sel:DWORD dst_unused:UNUSED_PAD src0_sel:DWORD src1_sel:WORD_1\n\t" \
  "s_waitcnt lgkmcnt(5)\n\t" \
  "ds_read_b64_tr_b16 v[218:219], v248 offset:48680\n\t" \
  "s_waitcnt vmcnt(5)\n\t" \
  "s_mov_b64 exec, 3\n\t" \
  "ds_add_rtn_u32 v246, v251, v250 offset:47136\n\t" \
  "s_mov_b64 exec, -1\n\t" \
  "v_mfma_f32_16x16x32_bf16 v[164:167], v[208:211], v[104:107], 0\n\t" \
  "ds_read_b32 v231, v231 offset:10016\n\t" \
  "v_lshlrev_b32_sdwa v234, 1, v158 dst_sel:DWORD dst_unused:UNUSED_PAD src0_sel:DWORD src1_sel:WORD_1\n\t" \
  "v_mfma_f32_16x16x32_bf16 v[172:175], v[208:211], v[108:111], 0\n\t" \
  "ds_read_b64_tr_b16 v[208:209], v248 offset:49192\n\t" \
  "ds_read_b64_tr_b16 v[210:211], v248 offset:50216\n\t" \
  "s_waitcnt lgkmcnt(6)\n\t" \
  "v_mfma_f32_16x16x32_bf16 v[164:167], v[212:215], v[120:123], v[164:167]\n\t" \
  "ds_read_b32 v230, v230 offset:10016\n\t" \
  "v_mfma_f32_16x16x32_bf16 v[172:175], v[212:215], v[124:127], v[172:175]\n\t" \
  "ds_read_b64_tr_b16 v[212:213], v248 offset:52264\n\t" \
  "ds_read_b64_tr_b16 v[214:215], v248 offset:53288\n\t" \
  "s_waitcnt lgkmcnt(7)\n\t" \
  "v_mfma_f32_16x16x32_bf16 v[164:167], v[216:219], v[136:139], v[164:167]\n\t" \
  "ds_read_u16_d16_hi v233, v233 offset:10528\n\t" \
  "v_mfma_f32_16x16x32_bf16 v[172:175], v[216:219], v[140:143], v[172:175]\n\t" \
  "ds_read_b64_tr_b16 v[216:217], v248 offset:51240\n\t" \
  "v_mfma_f32_16x16x32_bf16 v[0:3], v[242:245], v[36:39], 0\n\t" \
  "v_cvt_pknorm_i16_f32 v220, |v162|, |v163|\n\t" \
  "v_cvt_pknorm_i16_f32 v222, |v160|, |v161|\n\t" \
  "buffer_store_dwordx4 v[52:55], v250, s[20:23], s28 offen offset:0 sc1\n\t" \
  "ds_read_b64_tr_b16 v[218:219], v248 offset:54312\n\t" \
  "v_mfma_f32_16x16x32_bf16 v[4:7], v[240:243], v[36:39], 0\n\t" \
  "v_lshlrev_b32_sdwa v221, 2, v220 dst_sel:DWORD dst_unused:UNUSED_PAD src0_sel:DWORD src1_sel:BYTE_3\n\t" \
  "v_lshlrev_b32_sdwa v220, 2, v220 dst_sel:DWORD dst_unused:UNUSED_PAD src0_sel:DWORD src1_sel:BYTE_1\n\t" \
  "s_setprio 0\n\t" \
  "ds_read_u16 v232, v232 offset:10528\n\t" \
  "v_mfma_f32_16x16x32_bf16 v[8:11], v[242:245], v[40:43], 0\n\t" \
  "v_lshlrev_b32_sdwa v223, 2, v222 dst_sel:DWORD dst_unused:UNUSED_PAD src0_sel:DWORD src1_sel:BYTE_3\n\t" \
  "v_lshlrev_b32_sdwa v222, 2, v222 dst_sel:DWORD dst_unused:UNUSED_PAD src0_sel:DWORD src1_sel:BYTE_1\n\t" \
  "ds_read_u16_d16_hi v235, v235 offset:10528\n\t" \
  "v_mfma_f32_16x16x32_bf16 v[12:15], v[240:243], v[40:43], 0\n\t" \
  "v_lshlrev_b32_sdwa v225, 1, v171 dst_sel:DWORD dst_unused:UNUSED_PAD src0_sel:DWORD src1_sel:WORD_1\n\t" \
  "v_lshlrev_b32_sdwa v224, 1, v170 dst_sel:DWORD dst_unused:UNUSED_PAD src0_sel:DWORD src1_sel:WORD_1\n\t" \
  "ds_read_u16 v234, v234 offset:10528\n\t" \
  "v_mfma_f32_16x16x32_bf16 v[16:19], v[242:245], v[44:47], 0\n\t" \
  "v_lshlrev_b32_sdwa v227, 1, v169 dst_sel:DWORD dst_unused:UNUSED_PAD src0_sel:DWORD src1_sel:WORD_1\n\t" \
  "v_lshlrev_b32_sdwa v226, 1, v168 dst_sel:DWORD dst_unused:UNUSED_PAD src0_sel:DWORD src1_sel:WORD_1\n\t" \
  "buffer_store_dwordx4 v[60:63], v250, s[20:23], s28 offen offset:64 sc1\n\t" \
  "ds_read_b32 v221, v221 offset:10016\n\t" \
  "v_mfma_f32_16x16x32_bf16 v[20:23], v[240:243], v[44:47], 0\n\t" \
  "s_waitcnt lgkmcnt(9)\n\t" \
  "v_fma_mix_f32 v229, |v149|, v229, v229 op_sel:[0,1,0] op_sel_hi:[0,1,1] clamp\n\t" \
  "ds_read_b32 v220, v220 offset:10016\n\t" \
  "ds_read_b32 v223, v223 offset:10016\n\t" \
  "v_mfma_f32_16x16x32_bf16 v[72:75], v[242:245], v[76:79], 0\n\t" \
  "v_fma_mix_f32 v228, |v148|, v228, v228 op_sel:[0,1,0] op_sel_hi:[0,1,1] clamp\n\t" \
  "v_fma_mix_f32 v231, |v151|, v231, v231 op_sel:[0,1,0] op_sel_hi:[0,1,1] clamp\n\t" \
  "ds_read_b32 v222, v222 offset:10016\n\t" \
  "v_mfma_f32_16x16x32_bf16 v[76:79], v[240:243], v[76:79], 0\n\t" \
  "s_waitcnt lgkmcnt(4)\n\t" \
  "v_fma_mix_f32 v230, |v150|, v230, v230 op_sel:[0,1,0] op_sel_hi:[0,1,1] clamp\n\t" \
  "v_or_b32 v156, v232, v233\n\t" \
  "ds_read_u16_d16_hi v225, v225 offset:10528\n\t" \
  "v_mfma_f32_16x16x32_bf16 v[80:83], v[242:245], v[84:87], 0\n\t" \
  "v_or_b32 v157, v234, v235\n\t" \
  "v_fma_f32 v149, |v149|, v229, v149 mul:4\n\t" \
  "buffer_store_dwordx4 v[68:71], v250, s[20:23], s28 offen offset:128 sc1\n\t" \
  "ds_read_u16 v224, v224 offset:10528\n\t" \
  "v_mfma_f32_16x16x32_bf16 v[84:87], v[240:243], v[84:87], 0\n\t" \
  "v_fma_f32 v148, |v148|, v228, v148 mul:4\n\t" \
  "v_fma_f32 v151, |v151|, v231, v151 mul:4\n\t" \
  "ds_read_u16_d16_hi v227, v227 offset:10528\n\t" \
  "v_mfma_f32_16x16x32_bf16 v[88:91], v[242:245], v[236:239], 0\n\t" \
  "v_fma_f32 v150, |v150|, v230, v150 mul:4\n\t" \
  "v_cvt_pk_bf16_f32 v148, v148, v149\n\t" \
  "ds_read_u16 v226, v226 offset:10528\n\t" \
  "v_mfma_f32_16x16x32_bf16 v[92:95], v[240:243], v[236:239], 0\n\t" \
  "v_cvt_pk_bf16_f32 v149, v150, v151\n\t" \
  "v_cvt_pknorm_i16_f32 v228, |v164|, |v165|\n\t" \
  "v_mfma_f32_16x16x32_bf16 v[176:179], v[208:211], v[96:99], 0\n\t" \
  "v_cvt_pknorm_i16_f32 v230, |v166|, |v167|\n\t" \
  "v_lshlrev_b32_sdwa v229, 2, v228 dst_sel:DWORD dst_unused:UNUSED_PAD src0_sel:DWORD src1_sel:BYTE_3\n\t" \
  "v_mfma_f32_16x16x32_bf16 v[184:187], v[208:211], v[100:103], 0\n\t" \
  "v_lshlrev_b32_sdwa v228, 2, v228 dst_sel:DWORD dst_unused:UNUSED_PAD src0_sel:DWORD src1_sel:BYTE_1\n\t" \
  "v_lshlrev_b32_sdwa v231, 2, v230 dst_sel:DWORD dst_unused:UNUSED_PAD src0_sel:DWORD src1_sel:BYTE_3\n\t" \
  "v_mfma_f32_16x16x32_bf16 v[192:195], v[208:211], v[104:107], 0\n\t" \
  "v_lshlrev_b32_sdwa v230, 2, v230 dst_sel:DWORD dst_unused:UNUSED_PAD src0_sel:DWORD src1_sel:BYTE_1\n\t" \
  "ds_read_b32 v229, v229 offset:10016\n\t" \
  "v_mfma_f32_16x16x32_bf16 v[200:203], v[208:211], v[108:111], 0\n\t" \
  "v_lshlrev_b32_sdwa v233, 1, v173 dst_sel:DWORD dst_unused:UNUSED_PAD src0_sel:DWORD src1_sel:WORD_1\n\t" \
  "v_lshlrev_b32_sdwa v232, 1, v172 dst_sel:DWORD dst_unused:UNUSED_PAD src0_sel:DWORD src1_sel:WORD_1\n\t" \
  "ds_read_b64_tr_b16 v[208:209], v248 offset:49704\n\t" \
  "v_mfma_f32_16x16x32_bf16 v[176:179], v[212:215], v[112:115], v[176:179]\n\t" \
  "v_lshlrev_b32_sdwa v235, 1, v175 dst_sel:DWORD dst_unused:UNUSED_PAD src0_sel:DWORD src1_sel:WORD_1\n\t" \
  "v_lshlrev_b32_sdwa v234, 1, v174 dst_sel:DWORD dst_unused:UNUSED_PAD src0_sel:DWORD src1_sel:WORD_1\n\t" \
  "ds_read_b64_tr_b16 v[210:211], v248 offset:50728\n\t" \
  "v_mfma_f32_16x16x32_bf16 v[184:187], v[212:215], v[116:119], v[184:187]\n\t" \
  "s_waitcnt lgkmcnt(7)\n\t" \
  "v_fma_mix_f32 v221, |v163|, v221, v221 op_sel:[0,1,0] op_sel_hi:[0,1,1] clamp\n\t" \
  "ds_read_b32 v228, v228 offset:10016\n\t" \
  "ds_read_b32 v231, v231 offset:10016\n\t" \
  "v_mfma_f32_16x16x32_bf16 v[192:195], v[212:215], v[120:123], v[192:195]\n\t" \
  "v_fma_mix_f32 v220, |v162|, v220, v220 op_sel:[0,1,0] op_sel_hi:[0,1,1] clamp\n\t" \
  "v_fma_mix_f32 v223, |v161|, v223, v223 op_sel:[0,1,0] op_sel_hi:[0,1,1] clamp\n\t" \
  "ds_read_b32 v230, v230 offset:10016\n\t" \
  "v_mfma_f32_16x16x32_bf16 v[200:203], v[212:215], v[124:127], v[200:203]\n\t" \
  "v_fma_mix_f32 v222, |v160|, v222, v222 op_sel:[0,1,0] op_sel_hi:[0,1,1] clamp\n\t" \
  "s_waitcnt lgkmcnt(6)\n\t" \
  "ds_read_b64_tr_b16 v[212:213], v248 offset:52776\n\t" \
  "ds_read_u16_d16_hi v233, v233 offset:10528\n\t" \
  "v_mfma_f32_16x16x32_bf16 v[176:179], v[216:219], v[128:131], v[176:179]\n\t" \
  "v_or_b32 v171, v224, v225\n\t" \
  "v_or_b32 v170, v226, v227\n\t" \
  "ds_read_b64_tr_b16 v[214:215], v248 offset:53800\n\t" \
  "v_mfma_f32_16x16x32_bf16 v[184:187], v[216:219], v[132:135], v[184:187]\n\t" \
  "v_fma_f32 v163, |v163|, v221, v163 mul:4\n\t" \
  "v_fma_f32 v162, |v162|, v220, v162 mul:4\n\t" \
  "ds_read_u16 v232, v232 offset:10528\n\t" \
  "v_mfma_f32_16x16x32_bf16 v[192:195], v[216:219], v[136:139], v[192:195]\n\t" \
  "v_fma_f32 v161, |v161|, v223, v161 mul:4\n\t" \
  "v_fma_f32 v160, |v160|, v222, v160 mul:4\n\t" \
  "ds_read_u16_d16_hi v235, v235 offset:10528\n\t" \
  "v_mfma_f32_16x16x32_bf16 v[200:203], v[216:219], v[140:143], v[200:203]\n\t" \
  "v_cvt_pk_bf16_f32 v163, v162, v163\n\t" \
  "v_cvt_pk_bf16_f32 v162, v160, v161\n\t" \
  "ds_read_b64_tr_b16 v[216:217], v248 offset:51752\n\t" \
  "s_waitcnt lgkmcnt(9)\n\t" \
  "v_mfma_f32_16x16x32_bf16 v[180:183], v[208:211], v[96:99], 0\n\t" \
  "v_cvt_pknorm_i16_f32 v220, |v178|, |v179|\n\t" \
  "v_cvt_pknorm_i16_f32 v222, |v176|, |v177|\n\t" \
  "s_waitcnt vmcnt(6)\n\t" \
  "ds_read_b64_tr_b16 v[218:219], v248 offset:54824\n\t" \
  "v_mfma_f32_16x16x32_bf16 v[188:191], v[208:211], v[100:103], 0\n\t" \
  "v_lshlrev_b32_sdwa v221, 2, v220 dst_sel:DWORD dst_unused:UNUSED_PAD src0_sel:DWORD src1_sel:BYTE_3\n\t" \
  "v_lshlrev_b32_sdwa v220, 2, v220 dst_sel:DWORD dst_unused:UNUSED_PAD src0_sel:DWORD src1_sel:BYTE_1\n\t" \
  "s_mov_b64 exec, 3\n\t" \
  "ds_add_rtn_u32 v246, v251, v250 offset:47144\n\t" \
  "v_mfma_f32_16x16x32_bf16 v[196:199], v[208:211], v[104:107], 0\n\t" \
  "s_mov_b64 exec, -1\n\t" \
  "v_lshlrev_b32_sdwa v223, 2, v222 dst_sel:DWORD dst_unused:UNUSED_PAD src0_sel:DWORD src1_sel:BYTE_3\n\t" \
  "v_lshlrev_b32_sdwa v222, 2, v222 dst_sel:DWORD dst_unused:UNUSED_PAD src0_sel:DWORD src1_sel:BYTE_1\n\t" \
  "ds_read_u16 v234, v234 offset:10528\n\t" \
  "v_mfma_f32_16x16x32_bf16 v[204:207], v[208:211], v[108:111], 0\n\t" \
  "s_waitcnt lgkmcnt(1)\n\t" \
  "v_lshlrev_b32_sdwa v225, 1, v187 dst_sel:DWORD dst_unused:UNUSED_PAD src0_sel:DWORD src1_sel:WORD_1\n\t" \
  "v_readfirstlane_b32 s4, v246\n\t" \
  "ds_read_b32 v221, v221 offset:10016\n\t" \
  "v_mfma_f32_16x16x32_bf16 v[180:183], v[212:215], v[112:115], v[180:183]\n\t" \
  "s_cmpk_lt_u32 s4, 6144\n\t" \
  "v_lshlrev_b32_sdwa v224, 1, v186 dst_sel:DWORD dst_unused:UNUSED_PAD src0_sel:DWORD src1_sel:WORD_1\n\t" \
  "s_cbranch_scc1 .Lcnx_w1_2\n.Lcnx_g1_2:\n\t" \
  "ds_read_b32 v220, v220 offset:10016\n\t" \
  "v_mfma_f32_16x16x32_bf16 v[188:191], v[212:215], v[116:119], v[188:191]\n\t" \
  "v_lshlrev_b32_sdwa v227, 1, v185 dst_sel:DWORD dst_unused:UNUSED_PAD src0_sel:DWORD src1_sel:WORD_1\n\t" \
  "v_lshlrev_b32_sdwa v226, 1, v184 dst_sel:DWORD dst_unused:UNUSED_PAD src0_sel:DWORD src1_sel:WORD_1\n\t" \
  "ds_read_b64_tr_b16 v[208:209], v249 offset:24600\n\t" \
  "v_mfma_f32_16x16x32_bf16 v[196:199], v[212:215], v[120:123], v[196:199]\n\t" \
  "v_fma_mix_f32 v229, |v165|, v229, v229 op_sel:[0,1,0] op_sel_hi:[0,1,1] clamp\n\t" \
  "v_fma_mix_f32 v228, |v164|, v228, v228 op_sel:[0,1,0] op_sel_hi:[0,1,1] clamp\n\t" \
  "ds_read_b64_tr_b16 v[210:211], v249 offset:26136\n\t" \
  "ds_read_b32 v223, v223 offset:10016\n\t" \
  "v_mfma_f32_16x16x32_bf16 v[204:207], v[212:215], v[124:127], v[204:207]\n\t" \
  "v_fma_mix_f32 v231, |v167|, v231, v231 op_sel:[0,1,0] op_sel_hi:[0,1,1] clamp\n\t" \
  "v_fma_mix_f32 v230, |v166|, v230, v230 op_sel:[0,1,0] op_sel_hi:[0,1,1] clamp\n\t" \
  "s_waitcnt lgkmcnt(5)\n\t" \
  "ds_read_b64_tr_b16 v[212:213], v249 offset:24608\n\t" \
  "ds_read_b32 v222, v222 offset:10016\n\t" \
  "v_mfma_f32_16x16x32_bf16 v[180:183], v[216:219], v[128:131], v[180:183]\n\t" \
  "s_mov_b32 m0, s46\n\t" \
  "v_or_b32 v172, v232, v233\n\t" \
  "v_or_b32 v173, v234, v235\n\t" \
  "s_mov_b32 exec_hi, 0xffff\n\t" \
  "buffer_load_dwordx4 v252, s[16:19], s33 offen lds\n\t" \
  "v_mfma_f32_16x16x32_bf16 v[188:191], v[216:219], v[132:135], v[188:191]\n\t" \
  "s_mov_b32 exec_hi, -1\n\t" \
  "v_fma_f32 v165, |v165|, v229, v165 mul:4\n\t" \
  "v_fma_f32 v164, |v164|, v228, v164 mul:4\n\t" \
  "s_add_u32 s33, s33, s32\n\t" \
  "ds_read_b64_tr_b16 v[214:215], v249 offset:26144\n\t" \
  "v_mfma_f32_16x16x32_bf16 v[196:199], v[216:219], v[136:139], v[196:199]\n\t" \
  "v_fma_f32 v167, |v167|, v231, v167 mul:4\n\t" \
  "v_fma_f32 v166, |v166|, v230, v166 mul:4\n\t" \
  "ds_read_u16_d16_hi v225, v225 offset:10528\n\t" \
  "v_mfma_f32_16x16x32_bf16 v[204:207], v[216:219], v[140:143], v[204:207]\n\t" \
  "v_cvt_pk_bf16_f32 v164, v164, v165\n\t" \
  "v_cvt_pk_bf16_f32 v165, v166, v167\n\t" \
  "ds_read_b64_tr_b16 v[216:217], v249 offset:24664\n\t" \
  "s_waitcnt lgkmcnt(6)\n\t" \
  "v_mfma_f32_16x16x32_bf16 v[0:3], v[208:211], v[146:149], v[0:3]\n\t" \
  "v_cvt_pknorm_i16_f32 v228, |v194|, |v195|\n\t" \
  "v_cvt_pknorm_i16_f32 v230, |v192|, |v193|\n\t" \
  "ds_read_b64_tr_b16 v[218:219], v249 offset:26200\n\t" \
  "v_mfma_f32_16x16x32_bf16 v[24:27], v[208:211], v[154:157], 0\n\t" \
  "v_lshlrev_b32_sdwa v229, 2, v228 dst_sel:DWORD dst_unused:UNUSED_PAD src0_sel:DWORD src1_sel:BYTE_3\n\t" \
  "v_lshlrev_b32_sdwa v228, 2, v228 dst_sel:DWORD dst_unused:UNUSED_PAD src0_sel:DWORD src1_sel:BYTE_1\n\t" \
  "ds_read_u16 v224, v224 offset:10528\n\t" \
  "v_mfma_f32_16x16x32_bf16 v[48:51], v[208:211], v[162:165], 0\n\t" \
  "v_lshlrev_b32_sdwa v231, 2, v230 dst_sel:DWORD dst_unused:UNUSED_PAD src0_sel:DWORD src1_sel:BYTE_3\n\t" \
  "v_lshlrev_b32_sdwa v230, 2, v230 dst_sel:DWORD dst_unused:UNUSED_PAD src0_sel:DWORD src1_sel:BYTE_1\n\t" \
  "ds_read_u16_d16_hi v227, v227 offset:10528\n\t" \
  "v_mfma_f32_16x16x32_bf16 v[72:75], v[208:211], v[170:173], v[72:75]\n\t" \
  "s_add_u32 m0, s46, s35\n\t" \
  "v_lshlrev_b32_sdwa v233, 1, v203 dst_sel:DWORD dst_unused:UNUSED_PAD src0_sel:DWORD src1_sel:WORD_1\n\t" \
  "v_lshlrev_b32_sdwa v232, 1, v202 dst_sel:DWORD dst_unused:UNUSED_PAD src0_sel:DWORD src1_sel:WORD_1\n\t" \
  "s_waitcnt lgkmcnt(5)\n\t" \
  "s_mov_b32 exec_hi, 0xffff\n\t" \
  "buffer_load_dwordx4 v252, s[16:19], s34 offen lds\n\t" \
  "v_mfma_f32_16x16x32_bf16 v[4:7], v[212:215], v[146:149], v[4:7]\n\t" \
  "s_mov_b32 exec_hi, -1\n\t" \
  "v_lshlrev_b32_sdwa v235, 1, v201 dst_sel:DWORD dst_unused:UNUSED_PAD src0_sel:DWORD src1_sel:WORD_1\n\t" \
  "s_add_u32 s34, s34, s32\n\t" \
  "ds_read_b64_tr_b16 v[208:209], v249 offset:24672\n\t" \
  "ds_read_u16 v226, v226 offset:10528\n\t" \
  "v_mfma_f32_16x16x32_bf16 v[28:31], v[212:215], v[154:157], 0\n\t" \
  "v_lshlrev_b32_sdwa v234, 1, v200 dst_sel:DWORD dst_unused:UNUSED_PAD src0_sel:DWORD src1_sel:WORD_1\n\t" \
  "ds_read_b64_tr_b16 v[210:211], v249 offset:26208\n\t" \
  "ds_read_b32 v229, v229 offset:10016\n\t" \
  "v_mfma_f32_16x16x32_bf16 v[52:55], v[212:215], v[162:165], 0\n\t" \
  "v_fma_mix_f32 v221, |v179|, v221, v221 op_sel:[0,1,0] op_sel_hi:[0,1,1] clamp\n\t" \
  "v_fma_mix_f32 v220, |v178|, v220, v220 op_sel:[0,1,0] op_sel_hi:[0,1,1] clamp\n\t" \
  "ds_read_b32 v228, v228 offset:10016\n\t" \
  "ds_read_b32 v231, v231 offset:10016\n\t" \
  "v_mfma_f32_16x16x32_bf16 v[76:79], v[212:215], v[170:173], v[76:79]\n\t" \
  "v_fma_mix_f32 v223, |v177|, v223, v223 op_sel:[0,1,0] op_sel_hi:[0,1,1] clamp\n\t" \
  "v_fma_mix_f32 v222, |v176|, v222, v222 op_sel:[0,1,0] op_sel_hi:[0,1,1] clamp\n\t" \
  "s_waitcnt lgkmcnt(4)\n\t" \
  "ds_read_b64_tr_b16 v[212:213], v249 offset:24728\n\t" \
  "ds_read_b32 v230, v230 offset:10016\n\t" \
  "v_mfma_f32_16x16x32_bf16 v[8:11], v[216:219], v[146:149], v[8:11]\n\t" \
  "v_or_b32 v187, v224, v225\n\t" \
  "v_or_b32 v186, v226, v227\n\t" \
  "ds_read_b64_tr_b16 v[214:215], v249 offset:26264\n\t" \
  "v_mfma_f32_16x16x32_bf16 v[32:35], v[216:219], v[154:157], 0\n\t" \
  "v_fma_f32 v179, |v179|, v221, v179 mul:4\n\t" \
  "v_fma_f32 v178, |v178|, v220, v178 mul:4\n\t" \
  "ds_read_u16_d16_hi v233, v233 offset:10528\n\t" \
  "v_mfma_f32_16x16x32_bf16 v[56:59], v[216:219], v[162:165], 0\n\t" \
  "v_fma_f32 v177, |v177|, v223, v177 mul:4\n\t" \
  "v_fma_f32 v176, |v176|, v222, v176 mul:4\n\t" \
  "ds_read_u16 v232, v232 offset:10528\n\t" \
  "v_mfma_f32_16x16x32_bf16 v[80:83], v[216:219], v[170:173], v[80:83]\n\t" \
  "v_cvt_pk_bf16_f32 v179, v178, v179\n\t" \
  "v_cvt_pk_bf16_f32 v178, v176, v177\n\t" \
  "ds_read_b64_tr_b16 v[216:217], v249 offset:24736\n\t" \
  "s_waitcnt lgkmcnt(9)\n\t" \
  "v_mfma_f32_16x16x32_bf16 v[12:15], v[208:211], v[146:149], v[12:15]\n\t" \
  "v_cvt_pknorm_i16_f32 v220, |v180|, |v181|\n\t" \
  "v_cvt_pknorm_i16_f32 v222, |v182|, |v183|\n\t" \
  "ds_read_b64_tr_b16 v[218:219], v249 offset:26272\n\t" \
  "v_mfma_f32_16x16x32_bf16 v[36:39], v[208:211], v[154:157], 0\n\t" \
  "v_lshlrev_b32_sdwa v221, 2, v220 dst_sel:DWORD dst_unused:UNUSED_PAD src0_sel:DWORD src1_sel:BYTE_3\n\t" \
  "v_lshlrev_b32_sdwa v220, 2, v220 dst_sel:DWORD dst_unused:UNUSED_PAD src0_sel:DWORD src1_sel:BYTE_1\n\t" \
  "ds_read_u16_d16_hi v235, v235 offset:10528\n\t" \
  "v_mfma_f32_16x16x32_bf16 v[60:63], v[208:211], v[162:165], 0\n\t" \
  "v_lshlrev_b32_sdwa v223, 2, v222 dst_sel:DWORD dst_unused:UNUSED_PAD src0_sel:DWORD src1_sel:BYTE_3\n\t" \
  "v_lshlrev_b32_sdwa v222, 2, v222 dst_sel:DWORD dst_unused:UNUSED_PAD src0_sel:DWORD src1_sel:BYTE_1\n\t" \
  "ds_read_u16 v234, v234 offset:10528\n\t" \
  "v_mfma_f32_16x16x32_bf16 v[84:87], v[208:211], v[170:173], v[84:87]\n\t" \
  "v_lshlrev_b32_sdwa v225, 1, v189 dst_sel:DWORD dst_unused:UNUSED_PAD src0_sel:DWORD src1_sel:WORD_1\n\t" \
  "v_lshlrev_b32_sdwa v224, 1, v188 dst_sel:DWORD dst_unused:UNUSED_PAD src0_sel:DWORD src1_sel:WORD_1\n\t" \
  "ds_read_b64_tr_b16 v[208:209], v248 offset:36904\n\t" \
  "s_waitcnt lgkmcnt(7)\n\t" \
  "v_mfma_f32_16x16x32_bf16 v[16:19], v[212:215], v[146:149], v[16:19]\n\t" \
  "v_lshlrev_b32_sdwa v227, 1, v191 dst_sel:DWORD dst_unused:UNUSED_PAD src0_sel:DWORD src1_sel:WORD_1\n\t" \
  "ds_read_b64_tr_b16 v[210:211], v248 offset:37928\n\t" \
  "ds_read_b32 v221, v221 offset:10016\n\t" \
  "v_mfma_f32_16x16x32_bf16 v[40:43], v[212:215], v[154:157], 0\n\t" \
  "v_lshlrev_b32_sdwa v226, 1, v190 dst_sel:DWORD dst_unused:UNUSED_PAD src0_sel:DWORD src1_sel:WORD_1\n\t" \
  "v_fma_mix_f32 v229, |v195|, v229, v229 op_sel:[0,1,0] op_sel_hi:[0,1,1] clamp\n\t" \
  "ds_read_b32 v220, v220 offset:10016\n\t" \
  "v_mfma_f32_16x16x32_bf16 v[64:67], v[212:215], v[162:165], 0\n\t" \
  "v_fma_mix_f32 v228, |v194|, v228, v228 op_sel:[0,1,0] op_sel_hi:[0,1,1] clamp\n\t" \
  "ds_read_b32 v223, v223 offset:10016\n\t" \
  "ds_read_b32 v222, v222 offset:10016\n\t" \
  "v_mfma_f32_16x16x32_bf16 v[88:91], v[212:215], v[170:173], v[88:91]\n\t" \
  "v_fma_mix_f32 v231, |v193|, v231, v231 op_sel:[0,1,0] op_sel_hi:[0,1,1] clamp\n\t" \
  "v_fma_mix_f32 v230, |v192|, v230, v230 op_sel:[0,1,0] op_sel_hi:[0,1,1] clamp\n\t" \
  "s_waitcnt lgkmcnt(6)\n\t" \
  "ds_read_b64_tr_b16 v[212:213], v248 offset:39976\n\t" \
  "ds_read_u16_d16_hi v225, v225 offset:10528\n\t" \
  "v_mfma_f32_16x16x32_bf16 v[20:23], v[216:219], v[146:149], v[20:23]\n\t" \
  "v_or_b32 v203, v232, v233\n\t" \
  "v_or_b32 v202, v234, v235\n\t" \
  "ds_read_b64_tr_b16 v[214:215], v248 offset:41000\n\t" \
  "v_mfma_f32_16x16x32_bf16 v[44:47], v[216:219], v[154:157], 0\n\t" \
  "v_fma_f32 v195, |v195|, v229, v195 mul:4\n\t" \
  "v_fma_f32 v194, |v194|, v228, v194 mul:4\n\t" \
  "ds_read_u16 v224, v224 offset:10528\n\t" \
  "v_mfma_f32_16x16x32_bf16 v[68:71], v[216:219], v[162:165], 0\n\t" \
  "v_fma_f32 v193, |v193|, v231, v193 mul:4\n\t" \
  "v_fma_f32 v192, |v192|, v230, v192 mul:4\n\t" \
  "ds_read_u16_d16_hi v227, v227 offset:10528\n\t" \
  "v_mfma_f32_16x16x32_bf16 v[92:95], v[216:219], v[170:173], v[92:95]\n\t" \
  "v_cvt_pk_bf16_f32 v195, v194, v195\n\t" \
  "v_cvt_pk_bf16_f32 v194, v192, v193\n\t" \
  "ds_read_b64_tr_b16 v[216:217], v248 offset:38952\n\t" \
  "s_waitcnt lgkmcnt(10)\n\t" \
  "v_mfma_f32_16x16x32_bf16 v[144:147], v[208:211], v[96:99], 0\n\t" \
  "v_cvt_pknorm_i16_f32 v228, |v196|, |v197|\n\t" \
  "v_cvt_pknorm_i16_f32 v230, |v198|, |v199|\n\t" \
  "ds_read_b64_tr_b16 v[218:219], v248 offset:42024\n\t" \
  "v_mfma_f32_16x16x32_bf16 v[152:155], v[208:211], v[100:103], 0\n\t" \
  "v_lshlrev_b32_sdwa v229, 2, v228 dst_sel:DWORD dst_unused:UNUSED_PAD src0_sel:DWORD src1_sel:BYTE_3\n\t" \
  "v_lshlrev_b32_sdwa v228, 2, v228 dst_sel:DWORD dst_unused:UNUSED_PAD src0_sel:DWORD src1_sel:BYTE_1\n\t" \
  "ds_read_u16 v226, v226 offset:10528\n\t" \
  "v_mfma_f32_16x16x32_bf16 v[160:163], v[208:211], v[104:107], 0\n\t" \
  "v_lshlrev_b32_sdwa v231, 2, v230 dst_sel:DWORD dst_unused:UNUSED_PAD src0_sel:DWORD src1_sel:BYTE_3\n\t" \
  "v_lshlrev_b32_sdwa v230, 2, v230 dst_sel:DWORD dst_unused:UNUSED_PAD src0_sel:DWORD src1_sel:BYTE_1\n\t" \
  "ds_read_b32 v229, v229 offset:10016\n\t" \
  "v_mfma_f32_16x16x32_bf16 v[168:171], v[208:211], v[108:111], 0\n\t" \
  "v_lshlrev_b32_sdwa v233, 1, v205 dst_sel:DWORD dst_unused:UNUSED_PAD src0_sel:DWORD src1_sel:WORD_1\n\t" \
  "v_lshlrev_b32_sdwa v232, 1, v204 dst_sel:DWORD dst_unused:UNUSED_PAD src0_sel:DWORD src1_sel:WORD_1\n\t" \
  "ds_read_b64_tr_b16 v[208:209], v248 offset:37416\n\t" \
  "s_waitcnt lgkmcnt(7)\n\t" \
  "v_mfma_f32_16x16x32_bf16 v[144:147], v[212:215], v[112:115], v[144:147]\n\t" \
  "v_lshlrev_b32_sdwa v235, 1, v207 dst_sel:DWORD dst_unused:UNUSED_PAD src0_sel:DWORD src1_sel:WORD_1\n\t" \
  "v_lshlrev_b32_sdwa v234, 1, v206 dst_sel:DWORD dst_unused:UNUSED_PAD src0_sel:DWORD src1_sel:WORD_1\n\t" \
  "ds_read_b64_tr_b16 v[210:211], v248 offset:38440\n\t" \
  "v_mfma_f32_16x16x32_bf16 v[152:155], v[212:215], v[116:119], v[152:155]\n\t" \
  "v_fma_mix_f32 v221, |v181|, v221, v221 op_sel:[0,1,0] op_sel_hi:[0,1,1] clamp\n\t" \
  "ds_read_b32 v228, v228 offset:10016\n\t" \
  "ds_read_b32 v231, v231 offset:10016\n\t" \
  "v_mfma_f32_16x16x32_bf16 v[160:163], v[212:215], v[120:123], v[160:163]\n\t" \
  "v_fma_mix_f32 v220, |v180|, v220, v220 op_sel:[0,1,0] op_sel_hi:[0,1,1] clamp\n\t" \
  "v_fma_mix_f32 v223, |v183|, v223, v223 op_sel:[0,1,0] op_sel_hi:[0,1,1] clamp\n\t" \
  "ds_read_b32 v230, v230 offset:10016\n\t" \
  "v_mfma_f32_16x16x32_bf16 v[168:171], v[212:215], v[124:127], v[168:171]\n\t" \
  "v_fma_mix_f32 v222, |v182|, v222, v222 op_sel:[0,1,0] op_sel_hi:[0,1,1] clamp\n\t" \
  "s_waitcnt lgkmcnt(6)\n\t" \
  "ds_read_b64_tr_b16 v[212:213], v248 offset:40488\n\t" \
  "ds_read_u16_d16_hi v233, v233 offset:10528\n\t" \
  "v_mfma_f32_16x16x32_bf16 v[144:147], v[216:219], v[128:131], v[144:147]\n\t" \
  "v_or_b32 v188, v224, v225\n\t" \
  "v_or_b32 v189, v226, v227\n\t" \
  "ds_read_b64_tr_b16 v[214:215], v248 offset:41512\n\t" \
  "v_mfma_f32_16x16x32_bf16 v[152:155], v[216:219], v[132:135], v[152:155]\n\t" \
  "v_fma_f32 v181, |v181|, v221, v181 mul:4\n\t" \
  "v_fma_f32 v180, |v180|, v220, v180 mul:4\n\t" \
  "ds_read_u16 v232, v232 offset:10528\n\t" \
  "v_mfma_f32_16x16x32_bf16 v[160:163], v[216:219], v[136:139], v[160:163]\n\t" \
  "v_fma_f32 v183, |v183|, v223, v183 mul:4\n\t" \
  "v_fma_f32 v182, |v182|, v222, v182 mul:4\n\t" \
  "ds_read_u16_d16_hi v235, v235 offset:10528\n\t" \
  "v_mfma_f32_16x16x32_bf16 v[168:171], v[216:219], v[140:143], v[168:171]\n\t" \
  "v_cvt_pk_bf16_f32 v180, v180, v181\n\t" \
  "v_cvt_pk_bf16_f32 v181, v182, v183\n\t" \
  "ds_read_b64_tr_b16 v[216:217], v248 offset:39464\n\t" \
  "s_waitcnt lgkmcnt(9)\n\t" \
  "v_mfma_f32_16x16x32_bf16 v[148:151], v[208:211], v[96:99], 0\n\t" \
  "v_cvt_pknorm_i16_f32 v220, |v146|, |v147|\n\t" \
  "v_cvt_pknorm_i16_f32 v222, |v144|, |v145|\n\t" \
  "s_waitcnt vmcnt(0)\n\t" \
  "ds_read_b64_tr_b16 v[218:219], v248 offset:42536\n\t" \
  "v_mfma_f32_16x16x32_bf16 v[156:159], v[208:211], v[100:103], 0\n\t" \
  "v_lshlrev_b32_sdwa v221, 2, v220 dst_sel:DWORD dst_unused:UNUSED_PAD src0_sel:DWORD src1_sel:BYTE_3\n\t" \
  "v_lshlrev_b32_sdwa v220, 2, v220 dst_sel:DWORD dst_unused:UNUSED_PAD src0_sel:DWORD src1_sel:BYTE_1\n\t" \
  "s_mov_b64 exec, 3\n\t" \
  "ds_add_rtn_u32 v246, v251, v250 offset:47152\n\t" \
  "v_mfma_f32_16x16x32_bf16 v[164:167], v[208:211], v[104:107], 0\n\t" \
  "s_mov_b64 exec, -1\n\t" \
  "v_lshlrev_b32_sdwa v223, 2, v222 dst_sel:DWORD dst_unused:UNUSED_PAD src0_sel:DWORD src1_sel:BYTE_3\n\t" \
  "v_lshlrev_b32_sdwa v222, 2, v222 dst_sel:DWORD dst_unused:UNUSED_PAD src0_sel:DWORD src1_sel:BYTE_1\n\t" \
  "ds_read_u16 v234, v234 offset:10528\n\t" \
  "v_mfma_f32_16x16x32_bf16 v[172:175], v[208:211], v[108:111], 0\n\t" \
  "s_waitcnt lgkmcnt(1)\n\t" \
  "v_lshlrev_b32_sdwa v225, 1, v155 dst_sel:DWORD dst_unused:UNUSED_PAD src0_sel:DWORD src1_sel:WORD_1\n\t" \
  "v_readfirstlane_b32 s4, v246\n\t" \
  "ds_read_b32 v221, v221 offset:10016\n\t" \
  "v_mfma_f32_16x16x32_bf16 v[148:151], v[212:215], v[112:115], v[148:151]\n\t" \
  "s_cmpk_lt_u32 s4, 6144\n\t" \
  "v_lshlrev_b32_sdwa v224, 1, v154 dst_sel:DWORD dst_unused:UNUSED_PAD src0_sel:DWORD src1_sel:WORD_1\n\t" \
  "s_cbranch_scc1 .Lcnx_w1_3\n.Lcnx_g1_3:\n\t" \
  "ds_read_b32 v220, v220 offset:10016\n\t" \
  "v_mfma_f32_16x16x32_bf16 v[156:159], v[212:215], v[116:119], v[156:159]\n\t" \
  "v_lshlrev_b32_sdwa v227, 1, v153 dst_sel:DWORD dst_unused:UNUSED_PAD src0_sel:DWORD src1_sel:WORD_1\n\t" \
  "v_lshlrev_b32_sdwa v226, 1, v152 dst_sel:DWORD dst_unused:UNUSED_PAD src0_sel:DWORD src1_sel:WORD_1\n\t" \
  "ds_read_b64_tr_b16 v[208:209], v249 offset:30752\n\t" \
  "v_mfma_f32_16x16x32_bf16 v[164:167], v[212:215], v[120:123], v[164:167]\n\t" \
  "v_fma_mix_f32 v229, |v197|, v229, v229 op_sel:[0,1,0] op_sel_hi:[0,1,1] clamp\n\t" \
  "v_fma_mix_f32 v228, |v196|, v228, v228 op_sel:[0,1,0] op_sel_hi:[0,1,1] clamp\n\t" \
  "ds_read_b64_tr_b16 v[210:211], v249 offset:32288\n\t" \
  "ds_read_b32 v223, v223 offset:10016\n\t" \
  "v_mfma_f32_16x16x32_bf16 v[172:175], v[212:215], v[124:127], v[172:175]\n\t" \
  "v_fma_mix_f32 v231, |v199|, v231, v231 op_sel:[0,1,0] op_sel_hi:[0,1,1] clamp\n\t" \
  "v_fma_mix_f32 v230, |v198|, v230, v230 op_sel:[0,1,0] op_sel_hi:[0,1,1] clamp\n\t" \
  "s_waitcnt lgkmcnt(5)\n\t" \
  "ds_read_b64_tr_b16 v[212:213], v249 offset:30760\n\t" \
  "ds_read_b32 v222, v222 offset:10016\n\t" \
  "v_mfma_f32_16x16x32_bf16 v[148:151], v[216:219], v[128:131], v[148:151]\n\t" \
  "s_mov_b32 m0, s47\n\t" \
  "v_or_b32 v204, v232, v233\n\t" \
  "v_or_b32 v205, v234, v235\n\t" \
  "s_mov_b32 exec_hi, 0xffff\n\t" \
  "buffer_load_dwordx4 v252, s[16:19], s33 offen lds\n\t" \
  "v_mfma_f32_16x16x32_bf16 v[156:159], v[216:219], v[132:135], v[156:159]\n\t" \
  "s_mov_b32 exec_hi, -1\n\t" \
  "v_fma_f32 v197, |v197|, v229, v197 mul:4\n\t" \
  "v_fma_f32 v196, |v196|, v228, v196 mul:4\n\t" \
  "s_add_u32 s33, s33, s32\n\t" \
  "ds_read_b64_tr_b16 v[214:215], v249 offset:32296\n\t" \
  "v_mfma_f32_16x16x32_bf16 v[164:167], v[216:219], v[136:139], v[164:167]\n\t" \
  "v_fma_f32 v199, |v199|, v231, v199 mul:4\n\t" \
  "v_fma_f32 v198, |v198|, v230, v198 mul:4\n\t" \
  "ds_read_u16_d16_hi v225, v225 offset:10528\n\t" \
  "v_mfma_f32_16x16x32_bf16 v[172:175], v[216:219], v[140:143], v[172:175]\n\t" \
  "v_cvt_pk_bf16_f32 v196, v196, v197\n\t" \
  "v_cvt_pk_bf16_f32 v197, v198, v199\n\t" \
  "ds_read_b64_tr_b16 v[216:217], v249 offset:30816\n\t" \
  "s_waitcnt lgkmcnt(6)\n\t" \
  "v_mfma_f32_16x16x32_bf16 v[0:3], v[208:211], v[178:181], v[0:3]\n\t" \
  "v_cvt_pknorm_i16_f32 v228, |v162|, |v163|\n\t" \
  "v_cvt_pknorm_i16_f32 v230, |v160|, |v161|\n\t" \
  "ds_read_b64_tr_b16 v[218:219], v249 offset:32352\n\t" \
  "v_mfma_f32_16x16x32_bf16 v[24:27], v[208:211], v[186:189], v[24:27]\n\t" \
  "v_lshlrev_b32_sdwa v229, 2, v228 dst_sel:DWORD dst_unused:UNUSED_PAD src0_sel:DWORD src1_sel:BYTE_3\n\t" \
  "v_lshlrev_b32_sdwa v228, 2, v228 dst_sel:DWORD dst_unused:UNUSED_PAD src0_sel:DWORD src1_sel:BYTE_1\n\t" \
  "ds_read_u16 v224, v224 offset:10528\n\t" \
  "v_mfma_f32_16x16x32_bf16 v[48:51], v[208:211], v[194:197], v[48:51]\n\t" \
  "v_lshlrev_b32_sdwa v231, 2, v230 dst_sel:DWORD dst_unused:UNUSED_PAD src0_sel:DWORD src1_sel:BYTE_3\n\t" \
  "v_lshlrev_b32_sdwa v230, 2, v230 dst_sel:DWORD dst_unused:UNUSED_PAD src0_sel:DWORD src1_sel:BYTE_1\n\t" \
  "ds_read_u16_d16_hi v227, v227 offset:10528\n\t" \
  "v_mfma_f32_16x16x32_bf16 v[72:75], v[208:211], v[202:205], v[72:75]\n\t" \
  "s_add_u32 m0, s47, s35\n\t" \
  "v_lshlrev_b32_sdwa v233, 1, v171 dst_sel:DWORD dst_unused:UNUSED_PAD src0_sel:DWORD src1_sel:WORD_1\n\t" \
  "v_lshlrev_b32_sdwa v232, 1, v170 dst_sel:DWORD dst_unused:UNUSED_PAD src0_sel:DWORD src1_sel:WORD_1\n\t" \
  "s_waitcnt lgkmcnt(5)\n\t" \
  "s_mov_b32 exec_hi, 0xffff\n\t" \
  "buffer_load_dwordx4 v252, s[16:19], s34 offen lds\n\t" \
  "v_mfma_f32_16x16x32_bf16 v[4:7], v[212:215], v[178:181], v[4:7]\n\t" \
  "s_mov_b32 exec_hi, -1\n\t" \
  "v_lshlrev_b32_sdwa v235, 1, v169 dst_sel:DWORD dst_unused:UNUSED_PAD src0_sel:DWORD src1_sel:WORD_1\n\t" \
  "s_add_u32 s34, s34, s32\n\t" \
  "ds_read_b64_tr_b16 v[208:209], v249 offset:30824\n\t" \
  "ds_read_u16 v226, v226 offset:10528\n\t" \
  "v_mfma_f32_16x16x32_bf16 v[28:31], v[212:215], v[186:189], v[28:31]\n\t" \
  "v_lshlrev_b32_sdwa v234, 1, v168 dst_sel:DWORD dst_unused:UNUSED_PAD src0_sel:DWORD src1_sel:WORD_1\n\t" \
  "ds_read_b64_tr_b16 v[210:211], v249 offset:32360\n\t" \
  "ds_read_b32 v229, v229 offset:10016\n\t" \
  "v_mfma_f32_16x16x32_bf16 v[52:55], v[212:215], v[194:197], v[52:55]\n\t" \
  "v_fma_mix_f32 v221, |v147|, v221, v221 op_sel:[0,1,0] op_sel_hi:[0,1,1] clamp\n\t" \
  "v_fma_mix_f32 v220, |v146|, v220, v220 op_sel:[0,1,0] op_sel_hi:[0,1,1] clamp\n\t" \
  "ds_read_b32 v228, v228 offset:10016\n\t" \
  "ds_read_b32 v231, v231 offset:10016\n\t" \
  "v_mfma_f32_16x16x32_bf16 v[76:79], v[212:215], v[202:205], v[76:79]\n\t" \
  "v_fma_mix_f32 v223, |v145|, v223, v223 op_sel:[0,1,0] op_sel_hi:[0,1,1] clamp\n\t" \
  "v_fma_mix_f32 v222, |v144|, v222, v222 op_sel:[0,1,0] op_sel_hi:[0,1,1] clamp\n\t" \
  "s_waitcnt lgkmcnt(4)\n\t" \
  "ds_read_b64_tr_b16 v[212:213], v249 offset:30880\n\t" \
  "ds_read_b32 v230, v230 offset:10016\n\t" \
  "v_mfma_f32_16x16x32_bf16 v[8:11], v[216:219], v[178:181], v[8:11]\n\t" \
  "v_or_b32 v155, v224, v225\n\t" \
  "v_or_b32 v154, v226, v227\n\t" \
  "ds_read_b64_tr_b16 v[214:215], v249 offset:32416\n\t" \
  "v_mfma_f32_16x16x32_bf16 v[32:35], v[216:219], v[186:189], v[32:35]\n\t" \
  "v_fma_f32 v147, |v147|, v221, v147 mul:4\n\t" \
  "v_fma_f32 v146, |v146|, v220, v146 mul:4\n\t" \
  "ds_read_u16_d16_hi v233, v233 offset:10528\n\t" \
  "v_mfma_f32_16x16x32_bf16 v[56:59], v[216:219], v[194:197], v[56:59]\n\t" \
  "v_fma_f32 v145, |v145|, v223, v145 mul:4\n\t" \
  "v_fma_f32 v144, |v144|, v222, v144 mul:4\n\t" \
  "ds_read_u16 v232, v232 offset:10528\n\t" \
  "v_mfma_f32_16x16x32_bf16 v[80:83], v[216:219], v[202:205], v[80:83]\n\t" \
  "v_cvt_pk_bf16_f32 v147, v146, v147\n\t" \
  "v_cvt_pk_bf16_f32 v146, v144, v145\n\t" \
  "ds_read_b64_tr_b16 v[216:217], v249 offset:30888\n\t" \
  "s_waitcnt lgkmcnt(9)\n\t" \
  "v_mfma_f32_16x16x32_bf16 v[12:15], v[208:211], v[178:181], v[12:15]\n\t" \
  "v_cvt_pknorm_i16_f32 v220, |v148|, |v149|\n\t" \
  "v_cvt_pknorm_i16_f32 v222, |v150|, |v151|\n\t" \
  "ds_read_b64_tr_b16 v[218:219], v249 offset:32424\n\t" \
  "v_mfma_f32_16x16x32_bf16 v[36:39], v[208:211], v[186:189], v[36:39]\n\t" \
  "v_lshlrev_b32_sdwa v221, 2, v220 dst_sel:DWORD dst_unused:UNUSED_PAD src0_sel:DWORD src1_sel:BYTE_3\n\t" \
  "v_lshlrev_b32_sdwa v220, 2, v220 dst_sel:DWORD dst_unused:UNUSED_PAD src0_sel:DWORD src1_sel:BYTE_1\n\t" \
  "ds_read_u16_d16_hi v235, v235 offset:10528\n\t" \
  "v_mfma_f32_16x16x32_bf16 v[60:63], v[208:211], v[194:197], v[60:63]\n\t" \
  "v_lshlrev_b32_sdwa v223, 2, v222 dst_sel:DWORD dst_unused:UNUSED_PAD src0_sel:DWORD src1_sel:BYTE_3\n\t" \
  "v_lshlrev_b32_sdwa v222, 2, v222 dst_sel:DWORD dst_unused:UNUSED_PAD src0_sel:DWORD src1_sel:BYTE_1\n\t" \
  "ds_read_u16 v234, v234 offset:10528\n\t" \
  "v_mfma_f32_16x16x32_bf16 v[84:87], v[208:211], v[202:205], v[84:87]\n\t" \
  "s_add_u32 m0, s44, s50\n\t" \
  "v_lshlrev_b32_sdwa v225, 1, v157 dst_sel:DWORD dst_unused:UNUSED_PAD src0_sel:DWORD src1_sel:WORD_1\n\t" \
  "v_lshlrev_b32_sdwa v224, 1, v156 dst_sel:DWORD dst_unused:UNUSED_PAD src0_sel:DWORD src1_sel:WORD_1\n\t" \
  "s_add_u32 s3, s33, s51\n\t" \
  "s_waitcnt lgkmcnt(6)\n\t" \
  "s_mov_b32 exec_hi, 0xffff\n\t" \
  "buffer_load_dwordx4 v252, s[16:19], s3 offen lds\n\t" \
  "v_mfma_f32_16x16x32_bf16 v[16:19], v[212:215], v[178:181], v[16:19]\n\t" \
  "s_mov_b64 exec, -1\n\t" \
  "v_lshlrev_b32_sdwa v227, 1, v159 dst_sel:DWORD dst_unused:UNUSED_PAD src0_sel:DWORD src1_sel:WORD_1\n\t" \
  "ds_read_b64_tr_b16 v[208:209], v248 offset:0\n\t" \
  "ds_read_b32 v221, v221 offset:10016\n\t" \
  "v_mfma_f32_16x16x32_bf16 v[40:43], v[212:215], v[186:189], v[40:43]\n\t" \
  "v_lshlrev_b32_sdwa v226, 1, v158 dst_sel:DWORD dst_unused:UNUSED_PAD src0_sel:DWORD src1_sel:WORD_1\n\t" \
  "ds_read_b64_tr_b16 v[210:211], v248 offset:1024\n\t" \
  "ds_read_b32 v220, v220 offset:10016\n\t" \
  "v_mfma_f32_16x16x32_bf16 v[64:67], v[212:215], v[194:197], v[64:67]\n\t" \
  "v_fma_mix_f32 v229, |v163|, v229, v229 op_sel:[0,1,0] op_sel_hi:[0,1,1] clamp\n\t" \
  "v_fma_mix_f32 v228, |v162|, v228, v228 op_sel:[0,1,0] op_sel_hi:[0,1,1] clamp\n\t" \
  "ds_read_b32 v223, v223 offset:10016\n\t" \
  "v_mfma_f32_16x16x32_bf16 v[88:91], v[212:215], v[202:205], v[88:91]\n\t" \
  "v_fma_mix_f32 v231, |v161|, v231, v231 op_sel:[0,1,0] op_sel_hi:[0,1,1] clamp\n\t" \
  "v_fma_mix_f32 v230, |v160|, v230, v230 op_sel:[0,1,0] op_sel_hi:[0,1,1] clamp\n\t" \
  "s_waitcnt lgkmcnt(5)\n\t" \
  "ds_read_b64_tr_b16 v[212:213], v248 offset:3072\n\t" \
  "ds_read_b32 v222, v222 offset:10016\n\t" \
  "v_mfma_f32_16x16x32_bf16 v[20:23], v[216:219], v[178:181], v[20:23]\n\t" \
  "v_or_b32 v171, v232, v233\n\t" \
  "v_or_b32 v170, v234, v235\n\t" \
  "ds_read_b64_tr_b16 v[214:215], v248 offset:4096\n\t" \
  "v_mfma_f32_16x16x32_bf16 v[44:47], v[216:219], v[186:189], v[44:47]\n\t" \
  "v_fma_f32 v163, |v163|, v229, v163 mul:4\n\t" \
  "v_fma_f32 v162, |v162|, v228, v162 mul:4\n\t" \
  "ds_read_u16_d16_hi v225, v225 offset:10528\n\t" \
  "v_mfma_f32_16x16x32_bf16 v[68:71], v[216:219], v[194:197], v[68:71]\n\t" \
  "v_fma_f32 v161, |v161|, v231, v161 mul:4\n\t" \
  "v_fma_f32 v160, |v160|, v230, v160 mul:4\n\t" \
  "ds_read_u16 v224, v224 offset:10528\n\t" \
  "v_mfma_f32_16x16x32_bf16 v[92:95], v[216:219], v[202:205], v[92:95]\n\t" \
  "s_add_u32 m0, s44, s36\n\t" \
  "v_cvt_pk_bf16_f32 v163, v162, v163\n\t" \
  "v_cvt_pk_bf16_f32 v162, v160, v161\n\t" \
  "s_add_u32 s3, s34, s51\n\t" \
  "s_waitcnt lgkmcnt(7)\n\t" \
  "s_mov_b32 exec_hi, 0xffff\n\t" \
  "buffer_load_dwordx4 v252, s[16:19], s3 offen lds\n\t" \
  "v_mfma_f32_16x16x32_bf16 v[176:179], v[208:211], v[96:99], 0\n\t" \
  "s_mov_b64 exec, -1\n\t" \
  "v_cvt_pknorm_i16_f32 v228, |v164|, |v165|\n\t" \
  "v_cvt_pknorm_i16_f32 v230, |v166|, |v167|\n\t" \
  "ds_read_b64_tr_b16 v[216:217], v248 offset:2048\n\t" \
  "v_mfma_f32_16x16x32_bf16 v[184:187], v[208:211], v[100:103], 0\n\t" \
  "v_lshlrev_b32_sdwa v229, 2, v228 dst_sel:DWORD dst_unused:UNUSED_PAD src0_sel:DWORD src1_sel:BYTE_3\n\t" \
  "v_lshlrev_b32_sdwa v228, 2, v228 dst_sel:DWORD dst_unused:UNUSED_PAD src0_sel:DWORD src1_sel:BYTE_1\n\t" \
  "ds_read_b64_tr_b16 v[218:219], v248 offset:5120\n\t" \
  "v_mfma_f32_16x16x32_bf16 v[192:195], v[208:211], v[104:107], 0\n\t" \
  "v_lshlrev_b32_sdwa v231, 2, v230 dst_sel:DWORD dst_unused:UNUSED_PAD src0_sel:DWORD src1_sel:BYTE_3\n\t" \
  "v_lshlrev_b32_sdwa v230, 2, v230 dst_sel:DWORD dst_unused:UNUSED_PAD src0_sel:DWORD src1_sel:BYTE_1\n\t" \
  "ds_read_u16_d16_hi v227, v227 offset:10528\n\t" \
  "v_mfma_f32_16x16x32_bf16 v[200:203], v[208:211], v[108:111], 0\n\t" \
  "v_lshlrev_b32_sdwa v233, 1, v173 dst_sel:DWORD dst_unused:UNUSED_PAD src0_sel:DWORD src1_sel:WORD_1\n\t" \
  "s_waitcnt lgkmcnt(5)\n\t" \
  "ds_read_b64_tr_b16 v[208:209], v248 offset:512\n\t" \
  "ds_read_u16 v226, v226 offset:10528\n\t" \
  "v_mfma_f32_16x16x32_bf16 v[176:179], v[212:215], v[112:115], v[176:179]\n\t" \
  "v_lshlrev_b32_sdwa v232, 1, v172 dst_sel:DWORD dst_unused:UNUSED_PAD src0_sel:DWORD src1_sel:WORD_1\n\t" \
  "v_lshlrev_b32_sdwa v235, 1, v175 dst_sel:DWORD dst_unused:UNUSED_PAD src0_sel:DWORD src1_sel:WORD_1\n\t" \
  "ds_read_b64_tr_b16 v[210:211], v248 offset:1536\n\t" \
  "v_mfma_f32_16x16x32_bf16 v[184:187], v[212:215], v[116:119], v[184:187]\n\t" \
  "v_lshlrev_b32_sdwa v234, 1, v174 dst_sel:DWORD dst_unused:UNUSED_PAD src0_sel:DWORD src1_sel:WORD_1\n\t" \
  "v_fma_mix_f32 v221, |v149|, v221, v221 op_sel:[0,1,0] op_sel_hi:[0,1,1] clamp\n\t" \
  "ds_read_b32 v229, v229 offset:10016\n\t" \
  "ds_read_b32 v228, v228 offset:10016\n\t" \
  "v_mfma_f32_16x16x32_bf16 v[192:195], v[212:215], v[120:123], v[192:195]\n\t" \
  "v_fma_mix_f32 v220, |v148|, v220, v220 op_sel:[0,1,0] op_sel_hi:[0,1,1] clamp\n\t" \
  "ds_read_b32 v231, v231 offset:10016\n\t" \
  "ds_read_b32 v230, v230 offset:10016\n\t" \
  "v_mfma_f32_16x16x32_bf16 v[200:203], v[212:215], v[124:127], v[200:203]\n\t" \
  "v_fma_mix_f32 v223, |v151|, v223, v223 op_sel:[0,1,0] op_sel_hi:[0,1,1] clamp\n\t" \
  "v_fma_mix_f32 v222, |v150|, v222, v222 op_sel:[0,1,0] op_sel_hi:[0,1,1] clamp\n\t" \
  "s_waitcnt lgkmcnt(5)\n\t" \
  "ds_read_b64_tr_b16 v[212:213], v248 offset:3584\n\t" \
  "ds_read_u16_d16_hi v233, v233 offset:10528\n\t" \
  "v_mfma_f32_16x16x32_bf16 v[176:179], v[216:219], v[128:131], v[176:179]\n\t" \
  "v_or_b32 v156, v224, v225\n\t" \
  "v_or_b32 v157, v226, v227\n\t" \
  "ds_read_b64_tr_b16 v[214:215], v248 offset:4608\n\t" \
  "v_mfma_f32_16x16x32_bf16 v[184:187], v[216:219], v[132:135], v[184:187]\n\t" \
  "v_fma_f32 v149, |v149|, v221, v149 mul:4\n\t" \
  "v_fma_f32 v148, |v148|, v220, v148 mul:4\n\t" \
  "ds_read_u16 v232, v232 offset:10528\n\t" \
  "v_mfma_f32_16x16x32_bf16 v[192:195], v[216:219], v[136:139], v[192:195]\n\t" \
  "v_fma_f32 v151, |v151|, v223, v151 mul:4\n\t" \
  "v_fma_f32 v150, |v150|, v222, v150 mul:4\n\t" \
  "ds_read_u16_d16_hi v235, v235 offset:10528\n\t" \
  "v_mfma_f32_16x16x32_bf16 v[200:203], v[216:219], v[140:143], v[200:203]\n\t" \
  "v_cvt_pk_bf16_f32 v148, v148, v149\n\t" \
  "v_cvt_pk_bf16_f32 v149, v150, v151\n\t" \
  "ds_read_b64_tr_b16 v[216:217], v248 offset:2560\n\t" \
  "s_waitcnt lgkmcnt(10)\n\t" \
  "v_mfma_f32_16x16x32_bf16 v[180:183], v[208:211], v[96:99], 0\n\t" \
  "v_cvt_pknorm_i16_f32 v220, |v178|, |v179|\n\t" \
  "v_cvt_pknorm_i16_f32 v222, |v176|, |v177|\n\t" \
  "s_waitcnt vmcnt(2)\n\t" \
  "ds_read_b64_tr_b16 v[218:219], v248 offset:5632\n\t" \
  "v_mfma_f32_16x16x32_bf16 v[188:191], v[208:211], v[100:103], 0\n\t" \
  "v_lshlrev_b32_sdwa v221, 2, v220 dst_sel:DWORD dst_unused:UNUSED_PAD src0_sel:DWORD src1_sel:BYTE_3\n\t" \
  "v_lshlrev_b32_sdwa v220, 2, v220 dst_sel:DWORD dst_unused:UNUSED_PAD src0_sel:DWORD src1_sel:BYTE_1\n\t" \
  "s_mov_b64 exec, 3\n\t" \
  "ds_add_rtn_u32 v246, v251, v250 offset:47160\n\t" \
  "v_mfma_f32_16x16x32_bf16 v[196:199], v[208:211], v[104:107], 0\n\t" \
  "s_mov_b64 exec, -1\n\t" \
  "v_lshlrev_b32_sdwa v223, 2, v222 dst_sel:DWORD dst_unused:UNUSED_PAD src0_sel:DWORD src1_sel:BYTE_3\n\t" \
  "v_lshlrev_b32_sdwa v222, 2, v222 dst_sel:DWORD dst_unused:UNUSED_PAD src0_sel:DWORD src1_sel:BYTE_1\n\t" \
  "ds_read_u16 v234, v234 offset:10528\n\t" \
  "v_mfma_f32_16x16x32_bf16 v[204:207], v[208:211], v[108:111], 0\n\t" \
  "s_waitcnt lgkmcnt(1)\n\t" \
  "v_lshlrev_b32_sdwa v225, 1, v187 dst_sel:DWORD dst_unused:UNUSED_PAD src0_sel:DWORD src1_sel:WORD_1\n\t" \
  "v_readfirstlane_b32 s4, v246\n\t" \
  "ds_read_b32 v221, v221 offset:10016\n\t" \
  "v_mfma_f32_16x16x32_bf16 v[180:183], v[212:215], v[112:115], v[180:183]\n\t" \
  "s_cmpk_lt_u32 s4, 6144\n\t" \
  "v_lshlrev_b32_sdwa v224, 1, v186 dst_sel:DWORD dst_unused:UNUSED_PAD src0_sel:DWORD src1_sel:WORD_1\n\t" \
  "s_cbranch_scc1 .Lcnx_w1_4\n.Lcnx_g1_4:\n\t" \
  "ds_read_b32 v220, v220 offset:10016\n\t" \
  "v_mfma_f32_16x16x32_bf16 v[188:191], v[212:215], v[116:119], v[188:191]\n\t" \
  "v_lshlrev_b32_sdwa v227, 1, v185 dst_sel:DWORD dst_unused:UNUSED_PAD src0_sel:DWORD src1_sel:WORD_1\n\t" \
  "v_lshlrev_b32_sdwa v226, 1, v184 dst_sel:DWORD dst_unused:UNUSED_PAD src0_sel:DWORD src1_sel:WORD_1\n\t" \
  "ds_read_b64_tr_b16 v[208:209], v249 offset:6152\n\t" \
  "v_mfma_f32_16x16x32_bf16 v[196:199], v[212:215], v[120:123], v[196:199]\n\t" \
  "v_fma_mix_f32 v229, |v165|, v229, v229 op_sel:[0,1,0] op_sel_hi:[0,1,1] clamp\n\t" \
  "v_fma_mix_f32 v228, |v164|, v228, v228 op_sel:[0,1,0] op_sel_hi:[0,1,1] clamp\n\t" \
  "ds_read_b64_tr_b16 v[210:211], v249 offset:7688\n\t" \
  "ds_read_b32 v223, v223 offset:10016\n\t" \
  "v_mfma_f32_16x16x32_bf16 v[204:207], v[212:215], v[124:127], v[204:207]\n\t" \
  "v_fma_mix_f32 v231, |v167|, v231, v231 op_sel:[0,1,0] op_sel_hi:[0,1,1] clamp\n\t" \
  "v_fma_mix_f32 v230, |v166|, v230, v230 op_sel:[0,1,0] op_sel_hi:[0,1,1] clamp\n\t" \
  "s_waitcnt lgkmcnt(5)\n\t" \
  "ds_read_b64_tr_b16 v[212:213], v249 offset:6160\n\t" \
  "ds_read_b32 v222, v222 offset:10016\n\t" \
  "v_mfma_f32_16x16x32_bf16 v[180:183], v[216:219], v[128:131], v[180:183]\n\t" \
  "s_mov_b32 m0, s44\n\t" \
  "v_or_b32 v172, v232, v233\n\t" \
  "v_or_b32 v173, v234, v235\n\t" \
  "s_mov_b32 exec_hi, 0xffff\n\t" \
  "buffer_load_dwordx4 v252, s[16:19], s33 offen lds\n\t" \
  "v_mfma_f32_16x16x32_bf16 v[188:191], v[216:219], v[132:135], v[188:191]\n\t" \
  "s_mov_b32 exec_hi, -1\n\t" \
  "v_fma_f32 v165, |v165|, v229, v165 mul:4\n\t" \
  "v_fma_f32 v164, |v164|, v228, v164 mul:4\n\t" \
  "s_add_u32 s33, s33, s32\n\t" \
  "ds_read_b64_tr_b16 v[214:215], v249 offset:7696\n\t" \
  "v_mfma_f32_16x16x32_bf16 v[196:199], v[216:219], v[136:139], v[196:199]\n\t" \
  "v_fma_f32 v167, |v167|, v231, v167 mul:4\n\t" \
  "v_fma_f32 v166, |v166|, v230, v166 mul:4\n\t" \
  "ds_read_u16_d16_hi v225, v225 offset:10528\n\t" \
  "v_mfma_f32_16x16x32_bf16 v[204:207], v[216:219], v[140:143], v[204:207]\n\t" \
  "v_cvt_pk_bf16_f32 v164, v164, v165\n\t" \
  "v_cvt_pk_bf16_f32 v165, v166, v167\n\t" \
  "ds_read_b64_tr_b16 v[216:217], v249 offset:6216\n\t" \
  "s_waitcnt lgkmcnt(6)\n\t" \
  "v_mfma_f32_16x16x32_bf16 v[0:3], v[208:211], v[146:149], v[0:3]\n\t" \
  "v_cvt_pknorm_i16_f32 v228, |v194|, |v195|\n\t" \
  "v_cvt_pknorm_i16_f32 v230, |v192|, |v193|\n\t" \
  "ds_read_b64_tr_b16 v[218:219], v249 offset:7752\n\t" \
  "v_mfma_f32_16x16x32_bf16 v[24:27], v[208:211], v[154:157], v[24:27]\n\t" \
  "v_lshlrev_b32_sdwa v229, 2, v228 dst_sel:DWORD dst_unused:UNUSED_PAD src0_sel:DWORD src1_sel:BYTE_3\n\t" \
  "v_lshlrev_b32_sdwa v228, 2, v228 dst_sel:DWORD dst_unused:UNUSED_PAD src0_sel:DWORD src1_sel:BYTE_1\n\t" \
  "ds_read_u16 v224, v224 offset:10528\n\t" \
  "v_mfma_f32_16x16x32_bf16 v[48:51], v[208:211], v[162:165], v[48:51]\n\t" \
  "v_lshlrev_b32_sdwa v231, 2, v230 dst_sel:DWORD dst_unused:UNUSED_PAD src0_sel:DWORD src1_sel:BYTE_3\n\t" \
  "v_lshlrev_b32_sdwa v230, 2, v230 dst_sel:DWORD dst_unused:UNUSED_PAD src0_sel:DWORD src1_sel:BYTE_1\n\t" \
  "ds_read_u16_d16_hi v227, v227 offset:10528\n\t" \
  "v_mfma_f32_16x16x32_bf16 v[72:75], v[208:211], v[170:173], v[72:75]\n\t" \
  "s_add_u32 m0, s44, s35\n\t" \
  "v_lshlrev_b32_sdwa v233, 1, v203 dst_sel:DWORD dst_unused:UNUSED_PAD src0_sel:DWORD src1_sel:WORD_1\n\t" \
  "v_lshlrev_b32_sdwa v232, 1, v202 dst_sel:DWORD dst_unused:UNUSED_PAD src0_sel:DWORD src1_sel:WORD_1\n\t" \
  "s_waitcnt lgkmcnt(5)\n\t" \
  "s_mov_b32 exec_hi, 0xffff\n\t" \
  "buffer_load_dwordx4 v252, s[16:19], s34 offen lds\n\t" \
  "v_mfma_f32_16x16x32_bf16 v[4:7], v[212:215], v[146:149], v[4:7]\n\t" \
  "s_mov_b32 exec_hi, -1\n\t" \
  "v_lshlrev_b32_sdwa v235, 1, v201 dst_sel:DWORD dst_unused:UNUSED_PAD src0_sel:DWORD src1_sel:WORD_1\n\t" \
  "s_add_u32 s34, s34, s32\n\t" \
  "ds_read_b64_tr_b16 v[208:209], v249 offset:6224\n\t" \
  "ds_read_u16 v226, v226 offset:10528\n\t" \
  "v_mfma_f32_16x16x32_bf16 v[28:31], v[212:215], v[154:157], v[28:31]\n\t" \
  "v_lshlrev_b32_sdwa v234, 1, v200 dst_sel:DWORD dst_unused:UNUSED_PAD src0_sel:DWORD src1_sel:WORD_1\n\t" \
  "ds_read_b64_tr_b16 v[210:211], v249 offset:7760\n\t" \
  "ds_read_b32 v229, v229 offset:10016\n\t" \
  "v_mfma_f32_16x16x32_bf16 v[52:55], v[212:215], v[162:165], v[52:55]\n\t" \
  "v_fma_mix_f32 v221, |v179|, v221, v221 op_sel:[0,1,0] op_sel_hi:[0,1,1] clamp\n\t" \
  "v_fma_mix_f32 v220, |v178|, v220, v220 op_sel:[0,1,0] op_sel_hi:[0,1,1] clamp\n\t" \
  "ds_read_b32 v228, v228 offset:10016\n\t" \
  "ds_read_b32 v231, v231 offset:10016\n\t" \
  "v_mfma_f32_16x16x32_bf16 v[76:79], v[212:215], v[170:173], v[76:79]\n\t" \
  "v_fma_mix_f32 v223, |v177|, v223, v223 op_sel:[0,1,0] op_sel_hi:[0,1,1] clamp\n\t" \
  "v_fma_mix_f32 v222, |v176|, v222, v222 op_sel:[0,1,0] op_sel_hi:[0,1,1] clamp\n\t" \
  "s_waitcnt lgkmcnt(4)\n\t" \
  "ds_read_b64_tr_b16 v[212:213], v249 offset:6280\n\t" \
  "ds_read_b32 v230, v230 offset:10016\n\t" \
  "v_mfma_f32_16x16x32_bf16 v[8:11], v[216:219], v[146:149], v[8:11]\n\t" \
  "v_or_b32 v187, v224, v225\n\t" \
  "v_or_b32 v186, v226, v227\n\t" \
  "ds_read_b64_tr_b16 v[214:215], v249 offset:7816\n\t" \
  "v_mfma_f32_16x16x32_bf16 v[32:35], v[216:219], v[154:157], v[32:35]\n\t" \
  "v_fma_f32 v179, |v179|, v221, v179 mul:4\n\t" \
  "v_fma_f32 v178, |v178|, v220, v178 mul:4\n\t" \
  "buffer_load_dwordx4 v[236:239], v250, s[8:11], s62 offen offset:0 nt\n\t" \
  "ds_read_u16_d16_hi v233, v233 offset:10528\n\t" \
  "v_mfma_f32_16x16x32_bf16 v[56:59], v[216:219], v[162:165], v[56:59]\n\t" \
  "v_fma_f32 v177, |v177|, v223, v177 mul:4\n\t" \
  "v_fma_f32 v176, |v176|, v222, v176 mul:4\n\t" \
  "ds_read_u16 v232, v232 offset:10528\n\t" \
  "v_mfma_f32_16x16x32_bf16 v[80:83], v[216:219], v[170:173], v[80:83]\n\t" \
  "v_cvt_pk_bf16_f32 v179, v178, v179\n\t" \
  "v_cvt_pk_bf16_f32 v178, v176, v177\n\t" \
  "ds_read_b64_tr_b16 v[216:217], v249 offset:6288\n\t" \
  "s_waitcnt lgkmcnt(9)\n\t" \
  "v_mfma_f32_16x16x32_bf16 v[12:15], v[208:211], v[146:149], v[12:15]\n\t" \
  "v_cvt_pknorm_i16_f32 v220, |v180|, |v181|\n\t" \
  "v_cvt_pknorm_i16_f32 v222, |v182|, |v183|\n\t" \
  "ds_read_b64_tr_b16 v[218:219], v249 offset:7824\n\t" \
  "v_mfma_f32_16x16x32_bf16 v[36:39], v[208:211], v[154:157], v[36:39]\n\t" \
  "v_lshlrev_b32_sdwa v221, 2, v220 dst_sel:DWORD dst_unused:UNUSED_PAD src0_sel:DWORD src1_sel:BYTE_3\n\t" \
  "v_lshlrev_b32_sdwa v220, 2, v220 dst_sel:DWORD dst_unused:UNUSED_PAD src0_sel:DWORD src1_sel:BYTE_1\n\t" \
  "ds_read_u16_d16_hi v235, v235 offset:10528\n\t" \
  "v_mfma_f32_16x16x32_bf16 v[60:63], v[208:211], v[162:165], v[60:63]\n\t" \
  "v_lshlrev_b32_sdwa v223, 2, v222 dst_sel:DWORD dst_unused:UNUSED_PAD src0_sel:DWORD src1_sel:BYTE_3\n\t" \
  "v_lshlrev_b32_sdwa v222, 2, v222 dst_sel:DWORD dst_unused:UNUSED_PAD src0_sel:DWORD src1_sel:BYTE_1\n\t" \
  "ds_read_u16 v234, v234 offset:10528\n\t" \
  "v_mfma_f32_16x16x32_bf16 v[84:87], v[208:211], v[170:173], v[84:87]\n\t" \
  "s_add_u32 m0, s45, s50\n\t" \
  "v_lshlrev_b32_sdwa v225, 1, v189 dst_sel:DWORD dst_unused:UNUSED_PAD src0_sel:DWORD src1_sel:WORD_1\n\t" \
  "v_lshlrev_b32_sdwa v224, 1, v188 dst_sel:DWORD dst_unused:UNUSED_PAD src0_sel:DWORD src1_sel:WORD_1\n\t" \
  "s_add_u32 s3, s33, s51\n\t" \
  "s_waitcnt lgkmcnt(6)\n\t" \
  "s_mov_b32 exec_hi, 0xffff\n\t" \
  "buffer_load_dwordx4 v252, s[16:19], s3 offen lds\n\t" \
  "v_mfma_f32_16x16x32_bf16 v[16:19], v[212:215], v[146:149], v[16:19]\n\t" \
  "s_mov_b64 exec, -1\n\t" \
  "v_lshlrev_b32_sdwa v227, 1, v191 dst_sel:DWORD dst_unused:UNUSED_PAD src0_sel:DWORD src1_sel:WORD_1\n\t" \
  "ds_read_b64_tr_b16 v[208:209], v248 offset:43048\n\t" \
  "ds_read_b32 v221, v221 offset:10016\n\t" \
  "v_mfma_f32_16x16x32_bf16 v[40:43], v[212:215], v[154:157], v[40:43]\n\t" \
  "v_lshlrev_b32_sdwa v226, 1, v190 dst_sel:DWORD dst_unused:UNUSED_PAD src0_sel:DWORD src1_sel:WORD_1\n\t" \
  "ds_read_b64_tr_b16 v[210:211], v248 offset:44072\n\t" \
  "ds_read_b32 v220, v220 offset:10016\n\t" \
  "v_mfma_f32_16x16x32_bf16 v[64:67], v[212:215], v[162:165], v[64:67]\n\t" \
  "v_fma_mix_f32 v229, |v195|, v229, v229 op_sel:[0,1,0] op_sel_hi:[0,1,1] clamp\n\t" \
  "v_fma_mix_f32 v228, |v194|, v228, v228 op_sel:[0,1,0] op_sel_hi:[0,1,1] clamp\n\t" \
  "ds_read_b32 v223, v223 offset:10016\n\t" \
  "v_mfma_f32_16x16x32_bf16 v[88:91], v[212:215], v[170:173], v[88:91]\n\t" \
  "v_fma_mix_f32 v231, |v193|, v231, v231 op_sel:[0,1,0] op_sel_hi:[0,1,1] clamp\n\t" \
  "v_fma_mix_f32 v230, |v192|, v230, v230 op_sel:[0,1,0] op_sel_hi:[0,1,1] clamp\n\t" \
  "s_waitcnt lgkmcnt(5)\n\t" \
  "ds_read_b64_tr_b16 v[212:213], v248 offset:46120\n\t" \
  "ds_read_b32 v222, v222 offset:10016\n\t" \
  "v_mfma_f32_16x16x32_bf16 v[20:23], v[216:219], v[146:149], v[20:23]\n\t" \
  "v_or_b32 v203, v232, v233\n\t" \
  "v_or_b32 v202, v234, v235\n\t" \
  "ds_read_b64_tr_b16 v[214:215], v248 offset:47144\n\t" \
  "v_mfma_f32_16x16x32_bf16 v[44:47], v[216:219], v[154:157], v[44:47]\n\t" \
  "v_fma_f32 v195, |v195|, v229, v195 mul:4\n\t" \
  "v_fma_f32 v194, |v194|, v228, v194 mul:4\n\t" \
  "ds_read_u16_d16_hi v225, v225 offset:10528\n\t" \
  "v_mfma_f32_16x16x32_bf16 v[68:71], v[216:219], v[162:165], v[68:71]\n\t" \
  "v_fma_f32 v193, |v193|, v231, v193 mul:4\n\t" \
  "v_fma_f32 v192, |v192|, v230, v192 mul:4\n\t" \
  "ds_read_u16 v224, v224 offset:10528\n\t" \
  "v_mfma_f32_16x16x32_bf16 v[92:95], v[216:219], v[170:173], v[92:95]\n\t" \
  "s_add_u32 m0, s45, s36\n\t" \
  "v_cvt_pk_bf16_f32 v195, v194, v195\n\t" \
  "v_cvt_pk_bf16_f32 v194, v192, v193\n\t" \
  "s_add_u32 s3, s34, s51\n\t" \
  "s_waitcnt lgkmcnt(7)\n\t" \
  "s_mov_b32 exec_hi, 0xffff\n\t" \
  "buffer_load_dwordx4 v252, s[16:19], s3 offen lds\n\t" \
  "v_mfma_f32_16x16x32_bf16 v[144:147], v[208:211], v[96:99], 0\n\t" \
  "s_mov_b64 exec, -1\n\t" \
  "v_cvt_pknorm_i16_f32 v228, |v196|, |v197|\n\t" \
  "v_cvt_pknorm_i16_f32 v230, |v198|, |v199|\n\t" \
  "ds_read_b64_tr_b16 v[216:217], v248 offset:45096\n\t" \
  "v_mfma_f32_16x16x32_bf16 v[152:155], v[208:211], v[100:103], 0\n\t" \
  "v_lshlrev_b32_sdwa v229, 2, v228 dst_sel:DWORD dst_unused:UNUSED_PAD src0_sel:DWORD src1_sel:BYTE_3\n\t" \
  "v_lshlrev_b32_sdwa v228, 2, v228 dst_sel:DWORD dst_unused:UNUSED_PAD src0_sel:DWORD src1_sel:BYTE_1\n\t" \
  "ds_read_b64_tr_b16 v[218:219], v248 offset:48168\n\t" \
  "v_mfma_f32_16x16x32_bf16 v[160:163], v[208:211], v[104:107], 0\n\t" \
  "v_lshlrev_b32_sdwa v231, 2, v230 dst_sel:DWORD dst_unused:UNUSED_PAD src0_sel:DWORD src1_sel:BYTE_3\n\t" \
  "v_lshlrev_b32_sdwa v230, 2, v230 dst_sel:DWORD dst_unused:UNUSED_PAD src0_sel:DWORD src1_sel:BYTE_1\n\t" \
  "ds_read_u16_d16_hi v227, v227 offset:10528\n\t" \
  "v_mfma_f32_16x16x32_bf16 v[168:171], v[208:211], v[108:111], 0\n\t" \
  "v_lshlrev_b32_sdwa v233, 1, v205 dst_sel:DWORD dst_unused:UNUSED_PAD src0_sel:DWORD src1_sel:WORD_1\n\t" \
  "s_waitcnt lgkmcnt(5)\n\t" \
  "ds_read_b64_tr_b16 v[208:209], v248 offset:43560\n\t" \
  "ds_read_u16 v226, v226 offset:10528\n\t" \
  "v_mfma_f32_16x16x32_bf16 v[144:147], v[212:215], v[112:115], v[144:147]\n\t" \
  "v_lshlrev_b32_sdwa v232, 1, v204 dst_sel:DWORD dst_unused:UNUSED_PAD src0_sel:DWORD src1_sel:WORD_1\n\t" \
  "v_lshlrev_b32_sdwa v235, 1, v207 dst_sel:DWORD dst_unused:UNUSED_PAD src0_sel:DWORD src1_sel:WORD_1\n\t" \
  "ds_read_b64_tr_b16 v[210:211], v248 offset:44584\n\t" \
  "v_mfma_f32_16x16x32_bf16 v[152:155], v[212:215], v[116:119], v[152:155]\n\t" \
  "v_lshlrev_b32_sdwa v234, 1, v206 dst_sel:DWORD dst_unused:UNUSED_PAD src0_sel:DWORD src1_sel:WORD_1\n\t" \
  "v_fma_mix_f32 v221, |v181|, v221, v221 op_sel:[0,1,0] op_sel_hi:[0,1,1] clamp\n\t" \
  "ds_read_b32 v229, v229 offset:10016\n\t" \
  "ds_read_b32 v228, v228 offset:10016\n\t" \
  "v_mfma_f32_16x16x32_bf16 v[160:163], v[212:215], v[120:123], v[160:163]\n\t" \
  "v_fma_mix_f32 v220, |v180|, v220, v220 op_sel:[0,1,0] op_sel_hi:[0,1,1] clamp\n\t" \
  "ds_read_b32 v231, v231 offset:10016\n\t" \
  "ds_read_b32 v230, v230 offset:10016\n\t" \
  "v_mfma_f32_16x16x32_bf16 v[168:171], v[212:215], v[124:127], v[168:171]\n\t" \
  "v_fma_mix_f32 v223, |v183|, v223, v223 op_sel:[0,1,0] op_sel_hi:[0,1,1] clamp\n\t" \
  "v_fma_mix_f32 v222, |v182|, v222, v222 op_sel:[0,1,0] op_sel_hi:[0,1,1] clamp\n\t" \
  "s_waitcnt lgkmcnt(5)\n\t" \
  "ds_read_b64_tr_b16 v[212:213], v248 offset:46632\n\t" \
  "ds_read_u16_d16_hi v233, v233 offset:10528\n\t" \
  "v_mfma_f32_16x16x32_bf16 v[144:147], v[216:219], v[128:131], v[144:147]\n\t" \
  "v_or_b32 v188, v224, v225\n\t" \
  "v_or_b32 v189, v226, v227\n\t" \
  "ds_read_b64_tr_b16 v[214:215], v248 offset:47656\n\t" \
  "v_mfma_f32_16x16x32_bf16 v[152:155], v[216:219], v[132:135], v[152:155]\n\t" \
  "v_fma_f32 v181, |v181|, v221, v181 mul:4\n\t" \
  "v_fma_f32 v180, |v180|, v220, v180 mul:4\n\t" \
  "ds_read_u16 v232, v232 offset:10528\n\t" \
  "v_mfma_f32_16x16x32_bf16 v[160:163], v[216:219], v[136:139], v[160:163]\n\t" \
  "v_fma_f32 v183, |v183|, v223, v183 mul:4\n\t" \
  "v_fma_f32 v182, |v182|, v222, v182 mul:4\n\t" \
  "ds_read_u16_d16_hi v235, v235 offset:10528\n\t" \
  "v_mfma_f32_16x16x32_bf16 v[168:171], v[216:219], v[140:143], v[168:171]\n\t" \
  "v_cvt_pk_bf16_f32 v180, v180, v181\n\t" \
  "v_cvt_pk_bf16_f32 v181, v182, v183\n\t" \
  "ds_read_b64_tr_b16 v[216:217], v248 offset:45608\n\t" \
  "s_waitcnt lgkmcnt(10)\n\t" \
  "v_mfma_f32_16x16x32_bf16 v[148:151], v[208:211], v[96:99], 0\n\t" \
  "v_cvt_pknorm_i16_f32 v220, |v146|, |v147|\n\t" \
  "v_cvt_pknorm_i16_f32 v222, |v144|, |v145|\n\t" \
  "s_waitcnt vmcnt(2)\n\t" \
  "ds_read_b64_tr_b16 v[218:219], v248 offset:48680\n\t" \
  "v_mfma_f32_16x16x32_bf16 v[156:159], v[208:211], v[100:103], 0\n\t" \
  "v_lshlrev_b32_sdwa v221, 2, v220 dst_sel:DWORD dst_unused:UNUSED_PAD src0_sel:DWORD src1_sel:BYTE_3\n\t" \
  "v_lshlrev_b32_sdwa v220, 2, v220 dst_sel:DWORD dst_unused:UNUSED_PAD src0_sel:DWORD src1_sel:BYTE_1\n\t" \
  "s_mov_b64 exec, 3\n\t" \
  "ds_add_rtn_u32 v246, v251, v250 offset:47168\n\t" \
  "v_mfma_f32_16x16x32_bf16 v[164:167], v[208:211], v[104:107], 0\n\t" \
  "s_mov_b64 exec, -1\n\t" \
  "v_lshlrev_b32_sdwa v223, 2, v222 dst_sel:DWORD dst_unused:UNUSED_PAD src0_sel:DWORD src1_sel:BYTE_3\n\t" \
  "v_lshlrev_b32_sdwa v222, 2, v222 dst_sel:DWORD dst_unused:UNUSED_PAD src0_sel:DWORD src1_sel:BYTE_1\n\t" \
  "ds_read_u16 v234, v234 offset:10528\n\t" \
  "v_mfma_f32_16x16x32_bf16 v[172:175], v[208:211], v[108:111], 0\n\t" \
  "s_waitcnt lgkmcnt(1)\n\t" \
  "v_lshlrev_b32_sdwa v225, 1, v155 dst_sel:DWORD dst_unused:UNUSED_PAD src0_sel:DWORD src1_sel:WORD_1\n\t" \
  "v_readfirstlane_b32 s4, v246\n\t" \
  "ds_read_b32 v221, v221 offset:10016\n\t" \
  "v_mfma_f32_16x16x32_bf16 v[148:151], v[212:215], v[112:115], v[148:151]\n\t" \
  "s_cmpk_lt_u32 s4, 6144\n\t" \
  "v_lshlrev_b32_sdwa v224, 1, v154 dst_sel:DWORD dst_unused:UNUSED_PAD src0_sel:DWORD src1_sel:WORD_1\n\t" \
  "s_cbranch_scc1 .Lcnx_w1_5\n.Lcnx_g1_5:\n\t" \
  "ds_read_b32 v220, v220 offset:10016\n\t" \
  "v_mfma_f32_16x16x32_bf16 v[156:159], v[212:215], v[116:119], v[156:159]\n\t" \
  "v_lshlrev_b32_sdwa v227, 1, v153 dst_sel:DWORD dst_unused:UNUSED_PAD src0_sel:DWORD src1_sel:WORD_1\n\t" \
  "v_lshlrev_b32_sdwa v226, 1, v152 dst_sel:DWORD dst_unused:UNUSED_PAD src0_sel:DWORD src1_sel:WORD_1\n\t" \
  "ds_read_b64_tr_b16 v[208:209], v249 offset:12304\n\t" \
  "v_mfma_f32_16x16x32_bf16 v[164:167], v[212:215], v[120:123], v[164:167]\n\t" \
  "v_fma_mix_f32 v229, |v197|, v229, v229 op_sel:[0,1,0] op_sel_hi:[0,1,1] clamp\n\t" \
  "v_fma_mix_f32 v228, |v196|, v228, v228 op_sel:[0,1,0] op_sel_hi:[0,1,1] clamp\n\t" \
  "ds_read_b64_tr_b16 v[210:211], v249 offset:13840\n\t" \
  "ds_read_b32 v223, v223 offset:10016\n\t" \
  "v_mfma_f32_16x16x32_bf16 v[172:175], v[212:215], v[124:127], v[172:175]\n\t" \
  "v_fma_mix_f32 v231, |v199|, v231, v231 op_sel:[0,1,0] op_sel_hi:[0,1,1] clamp\n\t" \
  "v_fma_mix_f32 v230, |v198|, v230, v230 op_sel:[0,1,0] op_sel_hi:[0,1,1] clamp\n\t" \
  "s_waitcnt lgkmcnt(5)\n\t" \
  "ds_read_b64_tr_b16 v[212:213], v249 offset:12312\n\t" \
  "ds_read_b32 v222, v222 offset:10016\n\t" \
  "v_mfma_f32_16x16x32_bf16 v[148:151], v[216:219], v[128:131], v[148:151]\n\t" \
  "s_mov_b32 m0, s45\n\t" \
  "v_or_b32 v204, v232, v233\n\t" \
  "v_or_b32 v205, v234, v235\n\t" \
  "s_mov_b32 exec_hi, 0xffff\n\t" \
  "buffer_load_dwordx4 v252, s[16:19], s33 offen lds\n\t" \
  "v_mfma_f32_16x16x32_bf16 v[156:159], v[216:219], v[132:135], v[156:159]\n\t" \
  "s_mov_b32 exec_hi, -1\n\t" \
  "v_fma_f32 v197, |v197|, v229, v197 mul:4\n\t" \
  "v_fma_f32 v196, |v196|, v228, v196 mul:4\n\t" \
  "s_add_u32 s33, s33, s32\n\t" \
  "ds_read_b64_tr_b16 v[214:215], v249 offset:13848\n\t" \
  "v_mfma_f32_16x16x32_bf16 v[164:167], v[216:219], v[136:139], v[164:167]\n\t" \
  "v_fma_f32 v199, |v199|, v231, v199 mul:4\n\t" \
  "v_fma_f32 v198, |v198|, v230, v198 mul:4\n\t" \
  "ds_read_u16_d16_hi v225, v225 offset:10528\n\t" \
  "v_mfma_f32_16x16x32_bf16 v[172:175], v[216:219], v[140:143], v[172:175]\n\t" \
  "v_cvt_pk_bf16_f32 v196, v196, v197\n\t" \
  "v_cvt_pk_bf16_f32 v197, v198, v199\n\t" \
  "ds_read_b64_tr_b16 v[216:217], v249 offset:12368\n\t" \
  "v_mfma_f32_16x16x32_bf16 v[24:27], v[242:245], v[236:239], v[24:27]\n\t" \
  "v_cvt_pknorm_i16_f32 v228, |v162|, |v163|\n\t" \
  "v_cvt_pknorm_i16_f32 v230, |v160|, |v161|\n\t" \
  "ds_read_b64_tr_b16 v[218:219], v249 offset:13904\n\t" \
  "v_mfma_f32_16x16x32_bf16 v[28:31], v[240:243], v[236:239], v[28:31]\n\t" \
  "v_lshlrev_b32_sdwa v229, 2, v228 dst_sel:DWORD dst_unused:UNUSED_PAD src0_sel:DWORD src1_sel:BYTE_3\n\t" \
  "v_lshlrev_b32_sdwa v228, 2, v228 dst_sel:DWORD dst_unused:UNUSED_PAD src0_sel:DWORD src1_sel:BYTE_1\n\t" \
  "ds_read_u16 v224, v224 offset:10528\n\t" \
  "s_waitcnt lgkmcnt(8)\n\t" \
  "v_mfma_f32_16x16x32_bf16 v[0:3], v[208:211], v[178:181], v[0:3]\n\t" \
  "v_lshlrev_b32_sdwa v231, 2, v230 dst_sel:DWORD dst_unused:UNUSED_PAD src0_sel:DWORD src1_sel:BYTE_3\n\t" \
  "v_lshlrev_b32_sdwa v230, 2, v230 dst_sel:DWORD dst_unused:UNUSED_PAD src0_sel:DWORD src1_sel:BYTE_1\n\t" \
  "ds_read_u16_d16_hi v227, v227 offset:10528\n\t" \
  "v_mfma_f32_16x16x32_bf16 v[24:27], v[208:211], v[186:189], v[24:27]\n\t" \
  "s_add_u32 m0, s45, s35\n\t" \
  "v_lshlrev_b32_sdwa v233, 1, v171 dst_sel:DWORD dst_unused:UNUSED_PAD src0_sel:DWORD src1_sel:WORD_1\n\t" \
  "v_lshlrev_b32_sdwa v232, 1, v170 dst_sel:DWORD dst_unused:UNUSED_PAD src0_sel:DWORD src1_sel:WORD_1\n\t" \
  "s_mov_b32 exec_hi, 0xffff\n\t" \
  "buffer_load_dwordx4 v252, s[16:19], s34 offen lds\n\t" \
  "v_mfma_f32_16x16x32_bf16 v[48:51], v[208:211], v[194:197], v[48:51]\n\t" \
  "s_mov_b32 exec_hi, -1\n\t" \
  "v_lshlrev_b32_sdwa v235, 1, v169 dst_sel:DWORD dst_unused:UNUSED_PAD src0_sel:DWORD src1_sel:WORD_1\n\t" \
  "s_add_u32 s34, s34, s32\n\t" \
  "ds_read_u16 v226, v226 offset:10528\n\t" \
  "ds_read_b32 v229, v229 offset:10016\n\t" \
  "v_mfma_f32_16x16x32_bf16 v[72:75], v[208:211], v[202:205], v[72:75]\n\t" \
  "v_lshlrev_b32_sdwa v234, 1, v168 dst_sel:DWORD dst_unused:UNUSED_PAD src0_sel:DWORD src1_sel:WORD_1\n\t" \
  "s_waitcnt lgkmcnt(7)\n\t" \
  "ds_read_b64_tr_b16 v[208:209], v249 offset:12376\n\t" \
  "ds_read_b32 v228, v228 offset:10016\n\t" \
  "v_mfma_f32_16x16x32_bf16 v[4:7], v[212:215], v[178:181], v[4:7]\n\t" \
  "v_fma_mix_f32 v221, |v147|, v221, v221 op_sel:[0,1,0] op_sel_hi:[0,1,1] clamp\n\t" \
  "ds_read_b64_tr_b16 v[210:211], v249 offset:13912\n\t" \
  "ds_read_b32 v231, v231 offset:10016\n\t" \
  "v_mfma_f32_16x16x32_bf16 v[28:31], v[212:215], v[186:189], v[28:31]\n\t" \
  "v_fma_mix_f32 v220, |v146|, v220, v220 op_sel:[0,1,0] op_sel_hi:[0,1,1] clamp\n\t" \
  "v_fma_mix_f32 v223, |v145|, v223, v223 op_sel:[0,1,0] op_sel_hi:[0,1,1] clamp\n\t" \
  "ds_read_b32 v230, v230 offset:10016\n\t" \
  "v_mfma_f32_16x16x32_bf16 v[52:55], v[212:215], v[194:197], v[52:55]\n\t" \
  "s_waitcnt lgkmcnt(6)\n\t" \
  "v_fma_mix_f32 v222, |v144|, v222, v222 op_sel:[0,1,0] op_sel_hi:[0,1,1] clamp\n\t" \
  "v_or_b32 v155, v224, v225\n\t" \
  "ds_read_u16_d16_hi v233, v233 offset:10528\n\t" \
  "v_mfma_f32_16x16x32_bf16 v[76:79], v[212:215], v[202:205], v[76:79]\n\t" \
  "v_or_b32 v154, v226, v227\n\t" \
  "v_fma_f32 v147, |v147|, v221, v147 mul:4\n\t" \
  "buffer_load_dwordx4 v[236:239], v250, s[8:11], s62 offen offset:64 nt\n\t" \
  "ds_read_b64_tr_b16 v[212:213], v249 offset:12432\n\t" \
  "v_mfma_f32_16x16x32_bf16 v[8:11], v[216:219], v[178:181], v[8:11]\n\t" \
  "v_fma_f32 v146, |v146|, v220, v146 mul:4\n\t" \
  "v_fma_f32 v145, |v145|, v223, v145 mul:4\n\t" \
  "ds_read_b64_tr_b16 v[214:215], v249 offset:13968\n\t" \
  "v_mfma_f32_16x16x32_bf16 v[32:35], v[216:219], v[186:189], v[32:35]\n\t" \
  "v_fma_f32 v144, |v144|, v222, v144 mul:4\n\t" \
  "v_cvt_pk_bf16_f32 v147, v146, v147\n\t" \
  "ds_read_u16 v232, v232 offset:10528\n\t" \
  "v_mfma_f32_16x16x32_bf16 v[56:59], v[216:219], v[194:197], v[56:59]\n\t" \
  "v_cvt_pk_bf16_f32 v146, v144, v145\n\t" \
  "v_cvt_pknorm_i16_f32 v220, |v148|, |v149|\n\t" \
  "ds_read_u16_d16_hi v235, v235 offset:10528\n\t" \
  "v_mfma_f32_16x16x32_bf16 v[80:83], v[216:219], v[202:205], v[80:83]\n\t" \
  "v_cvt_pknorm_i16_f32 v222, |v150|, |v151|\n\t" \
  "v_lshlrev_b32_sdwa v221, 2, v220 dst_sel:DWORD dst_unused:UNUSED_PAD src0_sel:DWORD src1_sel:BYTE_3\n\t" \
  "ds_read_b64_tr_b16 v[216:217], v249 offset:12440\n\t" \
  "s_waitcnt lgkmcnt(8)\n\t" \
  "v_mfma_f32_16x16x32_bf16 v[12:15], v[208:211], v[178:181], v[12:15]\n\t" \
  "v_lshlrev_b32_sdwa v220, 2, v220 dst_sel:DWORD dst_unused:UNUSED_PAD src0_sel:DWORD src1_sel:BYTE_1\n\t" \
  "v_lshlrev_b32_sdwa v223, 2, v222 dst_sel:DWORD dst_unused:UNUSED_PAD src0_sel:DWORD src1_sel:BYTE_3\n\t" \
  "ds_read_b64_tr_b16 v[218:219], v249 offset:13976\n\t" \
  "v_mfma_f32_16x16x32_bf16 v[36:39], v[208:211], v[186:189], v[36:39]\n\t" \
  "s_add_u32 m0, s46, s50\n\t" \
  "v_lshlrev_b32_sdwa v222, 2, v222 dst_sel:DWORD dst_unused:UNUSED_PAD src0_sel:DWORD src1_sel:BYTE_1\n\t" \
  "v_lshlrev_b32_sdwa v225, 1, v157 dst_sel:DWORD dst_unused:UNUSED_PAD src0_sel:DWORD src1_sel:WORD_1\n\t" \
  "s_add_u32 s3, s33, s51\n\t" \
  "s_mov_b64 exec, s[68:69]\n\t" \
  "buffer_load_dwordx4 v252, s[16:19], s3 offen lds\n\t" \
  "v_mfma_f32_16x16x32_bf16 v[60:63], v[208:211], v[194:197], v[60:63]\n\t" \
  "s_mov_b64 exec, -1\n\t" \
  "v_lshlrev_b32_sdwa v224, 1, v156 dst_sel:DWORD dst_unused:UNUSED_PAD src0_sel:DWORD src1_sel:WORD_1\n\t" \
  "v_lshlrev_b32_sdwa v227, 1, v159 dst_sel:DWORD dst_unused:UNUSED_PAD src0_sel:DWORD src1_sel:WORD_1\n\t" \
  "ds_read_u16 v234, v234 offset:10528\n\t" \
  "v_mfma_f32_16x16x32_bf16 v[84:87], v[208:211], v[202:205], v[84:87]\n\t" \
  "v_lshlrev_b32_sdwa v226, 1, v158 dst_sel:DWORD dst_unused:UNUSED_PAD src0_sel:DWORD src1_sel:WORD_1\n\t" \
  "s_waitcnt lgkmcnt(5)\n\t" \
  "ds_read_b64_tr_b16 v[208:209], v248 offset:49192\n\t" \
  "ds_read_b32 v221, v221 offset:10016\n\t" \
  "v_mfma_f32_16x16x32_bf16 v[16:19], v[212:215], v[178:181], v[16:19]\n\t" \
  "v_fma_mix_f32 v229, |v163|, v229, v229 op_sel:[0,1,0] op_sel_hi:[0,1,1] clamp\n\t" \
  "ds_read_b64_tr_b16 v[210:211], v248 offset:50216\n\t" \
  "ds_read_b32 v220, v220 offset:10016\n\t" \
  "v_mfma_f32_16x16x32_bf16 v[40:43], v[212:215], v[186:189], v[40:43]\n\t" \
  "v_fma_mix_f32 v228, |v162|, v228, v228 op_sel:[0,1,0] op_sel_hi:[0,1,1] clamp\n\t" \
  "v_fma_mix_f32 v231, |v161|, v231, v231 op_sel:[0,1,0] op_sel_hi:[0,1,1] clamp\n\t" \
  "ds_read_b32 v223, v223 offset:10016\n\t" \
  "ds_read_b32 v222, v222 offset:10016\n\t" \
  "v_mfma_f32_16x16x32_bf16 v[64:67], v[212:215], v[194:197], v[64:67]\n\t" \
  "s_waitcnt lgkmcnt(6)\n\t" \
  "v_fma_mix_f32 v230, |v160|, v230, v230 op_sel:[0,1,0] op_sel_hi:[0,1,1] clamp\n\t" \
  "v_or_b32 v171, v232, v233\n\t" \
  "ds_read_u16_d16_hi v225, v225 offset:10528\n\t" \
  "v_mfma_f32_16x16x32_bf16 v[88:91], v[212:215], v[202:205], v[88:91]\n\t" \
  "v_or_b32 v170, v234, v235\n\t" \
  "v_fma_f32 v163, |v163|, v229, v163 mul:4\n\t" \
  "ds_read_b64_tr_b16 v[212:213], v248 offset:52264\n\t" \
  "v_mfma_f32_16x16x32_bf16 v[20:23], v[216:219], v[178:181], v[20:23]\n\t" \
  "v_fma_f32 v162, |v162|, v228, v162 mul:4\n\t" \
  "v_fma_f32 v161, |v161|, v231, v161 mul:4\n\t" \
  "ds_read_b64_tr_b16 v[214:215], v248 offset:53288\n\t" \
  "v_mfma_f32_16x16x32_bf16 v[44:47], v[216:219], v[186:189], v[44:47]\n\t" \
  "s_add_u32 m0, s46, s36\n\t" \
  "v_fma_f32 v160, |v160|, v230, v160 mul:4\n\t" \
  "v_cvt_pk_bf16_f32 v163, v162, v163\n\t" \
  "s_add_u32 s3, s34, s51\n\t" \
  "s_mov_b64 exec, s[68:69]\n\t" \
  "buffer_load_dwordx4 v252, s[16:19], s3 offen lds\n\t" \
  "v_mfma_f32_16x16x32_bf16 v[68:71], v[216:219], v[194:197], v[68:71]\n\t" \
  "s_mov_b64 exec, -1\n\t" \
  "v_cvt_pk_bf16_f32 v162, v160, v161\n\t" \
  "v_cvt_pknorm_i16_f32 v228, |v164|, |v165|\n\t" \
  "ds_read_u16 v224, v224 offset:10528\n\t" \
  "v_mfma_f32_16x16x32_bf16 v[92:95], v[216:219], v[202:205], v[92:95]\n\t" \
  "v_cvt_pknorm_i16_f32 v230, |v166|, |v167|\n\t" \
  "v_lshlrev_b32_sdwa v229, 2, v228 dst_sel:DWORD dst_unused:UNUSED_PAD src0_sel:DWORD src1_sel:BYTE_3\n\t" \
  "ds_read_b64_tr_b16 v[216:217], v248 offset:51240\n\t" \
  "s_waitcnt lgkmcnt(8)\n\t" \
  "v_mfma_f32_16x16x32_bf16 v[176:179], v[208:211], v[96:99], 0\n\t" \
  "v_lshlrev_b32_sdwa v228, 2, v228 dst_sel:DWORD dst_unused:UNUSED_PAD src0_sel:DWORD src1_sel:BYTE_1\n\t" \
  "v_lshlrev_b32_sdwa v231, 2, v230 dst_sel:DWORD dst_unused:UNUSED_PAD src0_sel:DWORD src1_sel:BYTE_3\n\t" \
  "ds_read_b64_tr_b16 v[218:219], v248 offset:54312\n\t" \
  "v_mfma_f32_16x16x32_bf16 v[184:187], v[208:211], v[100:103], 0\n\t" \
  "v_lshlrev_b32_sdwa v230, 2, v230 dst_sel:DWORD dst_unused:UNUSED_PAD src0_sel:DWORD src1_sel:BYTE_1\n\t" \
  "ds_read_u16_d16_hi v227, v227 offset:10528\n\t" \
  "v_mfma_f32_16x16x32_bf16 v[192:195], v[208:211], v[104:107], 0\n\t" \
  "v_lshlrev_b32_sdwa v233, 1, v173 dst_sel:DWORD dst_unused:UNUSED_PAD src0_sel:DWORD src1_sel:WORD_1\n\t" \
  "v_lshlrev_b32_sdwa v232, 1, v172 dst_sel:DWORD dst_unused:UNUSED_PAD src0_sel:DWORD src1_sel:WORD_1\n\t" \
  "ds_read_u16 v226, v226 offset:10528\n\t" \
  "v_mfma_f32_16x16x32_bf16 v[200:203], v[208:211], v[108:111], 0\n\t" \
  "v_lshlrev_b32_sdwa v235, 1, v175 dst_sel:DWORD dst_unused:UNUSED_PAD src0_sel:DWORD src1_sel:WORD_1\n\t" \
  "s_waitcnt lgkmcnt(5)\n\t" \
  "ds_read_b64_tr_b16 v[208:209], v248 offset:49704\n\t" \
  "ds_read_b32 v229, v229 offset:10016\n\t" \
  "v_mfma_f32_16x16x32_bf16 v[176:179], v[212:215], v[112:115], v[176:179]\n\t" \
  "v_lshlrev_b32_sdwa v234, 1, v174 dst_sel:DWORD dst_unused:UNUSED_PAD src0_sel:DWORD src1_sel:WORD_1\n\t" \
  "ds_read_b64_tr_b16 v[210:211], v248 offset:50728\n\t" \
  "ds_read_b32 v228, v228 offset:10016\n\t" \
  "v_mfma_f32_16x16x32_bf16 v[184:187], v[212:215], v[116:119], v[184:187]\n\t" \
  "v_fma_mix_f32 v221, |v149|, v221, v221 op_sel:[0,1,0] op_sel_hi:[0,1,1] clamp\n\t" \
  "v_fma_mix_f32 v220, |v148|, v220, v220 op_sel:[0,1,0] op_sel_hi:[0,1,1] clamp\n\t" \
  "ds_read_b32 v231, v231 offset:10016\n\t" \
  "v_mfma_f32_16x16x32_bf16 v[192:195], v[212:215], v[120:123], v[192:195]\n\t" \
  "v_fma_mix_f32 v223, |v151|, v223, v223 op_sel:[0,1,0] op_sel_hi:[0,1,1] clamp\n\t" \
  "v_fma_mix_f32 v222, |v150|, v222, v222 op_sel:[0,1,0] op_sel_hi:[0,1,1] clamp\n\t" \
  "ds_read_b32 v230, v230 offset:10016\n\t" \
  "s_waitcnt lgkmcnt(6)\n\t" \
  "v_mfma_f32_16x16x32_bf16 v[200:203], v[212:215], v[124:127], v[200:203]\n\t" \
  "v_or_b32 v156, v224, v225\n\t" \
  "v_or_b32 v157, v226, v227\n\t" \
  "ds_read_b64_tr_b16 v[212:213], v248 offset:52776\n\t" \
  "ds_read_u16_d16_hi v233, v233 offset:10528\n\t" \
  "v_mfma_f32_16x16x32_bf16 v[176:179], v[216:219], v[128:131], v[176:179]\n\t" \
  "v_fma_f32 v149, |v149|, v221, v149 mul:4\n\t" \
  "v_fma_f32 v148, |v148|, v220, v148 mul:4\n\t" \
  "ds_read_b64_tr_b16 v[214:215], v248 offset:53800\n\t" \
  "v_mfma_f32_16x16x32_bf16 v[184:187], v[216:219], v[132:135], v[184:187]\n\t" \
  "v_fma_f32 v151, |v151|, v223, v151 mul:4\n\t" \
  "v_fma_f32 v150, |v150|, v222, v150 mul:4\n\t" \
  "ds_read_u16 v232, v232 offset:10528\n\t" \
  "v_mfma_f32_16x16x32_bf16 v[192:195], v[216:219], v[136:139], v[192:195]\n\t" \
  "v_cvt_pk_bf16_f32 v148, v148, v149\n\t" \
  "v_cvt_pk_bf16_f32 v149, v150, v151\n\t" \
  "ds_read_u16_d16_hi v235, v235 offset:10528\n\t" \
  "v_mfma_f32_16x16x32_bf16 v[200:203], v[216:219], v[140:143], v[200:203]\n\t" \
  "v_cvt_pknorm_i16_f32 v220, |v178|, |v179|\n\t" \
  "v_cvt_pknorm_i16_f32 v222, |v176|, |v177|\n\t" \
  "ds_read_b64_tr_b16 v[216:217], v248 offset:51752\n\t" \
  "s_waitcnt lgkmcnt(9)\n\t" \
  "v_mfma_f32_16x16x32_bf16 v[180:183], v[208:211], v[96:99], 0\n\t" \
  "v_lshlrev_b32_sdwa v221, 2, v220 dst_sel:DWORD dst_unused:UNUSED_PAD src0_sel:DWORD src1_sel:BYTE_3\n\t" \
  "v_lshlrev_b32_sdwa v220, 2, v220 dst_sel:DWORD dst_unused:UNUSED_PAD src0_sel:DWORD src1_sel:BYTE_1\n\t" \
  "s_waitcnt vmcnt(2)\n\t" \
  "ds_read_b64_tr_b16 v[218:219], v248 offset:54824\n\t" \
  "v_mfma_f32_16x16x32_bf16 v[188:191], v[208:211], v[100:103], 0\n\t" \
  "v_lshlrev_b32_sdwa v223, 2, v222 dst_sel:DWORD dst_unused:UNUSED_PAD src0_sel:DWORD src1_sel:BYTE_3\n\t" \
  "v_lshlrev_b32_sdwa v222, 2, v222 dst_sel:DWORD dst_unused:UNUSED_PAD src0_sel:DWORD src1_sel:BYTE_1\n\t" \
  "s_mov_b64 exec, 3\n\t" \
  "ds_add_rtn_u32 v246, v251, v250 offset:47176\n\t" \
  "v_mfma_f32_16x16x32_bf16 v[196:199], v[208:211], v[104:107], 0\n\t" \
  "s_mov_b64 exec, -1\n\t" \
  "v_lshlrev_b32_sdwa v225, 1, v187 dst_sel:DWORD dst_unused:UNUSED_PAD src0_sel:DWORD src1_sel:WORD_1\n\t" \
  "ds_read_u16 v234, v234 offset:10528\n\t" \
  "v_mfma_f32_16x16x32_bf16 v[204:207], v[208:211], v[108:111], 0\n\t" \
  "s_waitcnt lgkmcnt(1)\n\t" \
  "v_lshlrev_b32_sdwa v224, 1, v186 dst_sel:DWORD dst_unused:UNUSED_PAD src0_sel:DWORD src1_sel:WORD_1\n\t" \
  "v_readfirstlane_b32 s4, v246\n\t" \
  "ds_read_b32 v221, v221 offset:10016\n\t" \
  "v_mfma_f32_16x16x32_bf16 v[180:183], v[212:215], v[112:115], v[180:183]\n\t" \
  "s_cmpk_lt_u32 s4, 6144\n\t" \
  "v_lshlrev_b32_sdwa v227, 1, v185 dst_sel:DWORD dst_unused:UNUSED_PAD src0_sel:DWORD src1_sel:WORD_1\n\t" \
  "s_cbranch_scc1 .Lcnx_w1_6\n.Lcnx_g1_6:\n\t" \
  "ds_read_b32 v220, v220 offset:10016\n\t" \
  "v_mfma_f32_16x16x32_bf16 v[188:191], v[212:215], v[116:119], v[188:191]\n\t" \
  "v_lshlrev_b32_sdwa v226, 1, v184 dst_sel:DWORD dst_unused:UNUSED_PAD src0_sel:DWORD src1_sel:WORD_1\n\t" \
  "ds_read_b64_tr_b16 v[208:209], v249 offset:24600\n\t" \
  "ds_read_b32 v223, v223 offset:10016\n\t" \
  "v_mfma_f32_16x16x32_bf16 v[196:199], v[212:215], v[120:123], v[196:199]\n\t" \
  "v_fma_mix_f32 v229, |v165|, v229, v229 op_sel:[0,1,0] op_sel_hi:[0,1,1] clamp\n\t" \
  "v_fma_mix_f32 v228, |v164|, v228, v228 op_sel:[0,1,0] op_sel_hi:[0,1,1] clamp\n\t" \
  "ds_read_b64_tr_b16 v[210:211], v249 offset:26136\n\t" \
  "v_mfma_f32_16x16x32_bf16 v[204:207], v[212:215], v[124:127], v[204:207]\n\t" \
  "v_fma_mix_f32 v231, |v167|, v231, v231 op_sel:[0,1,0] op_sel_hi:[0,1,1] clamp\n\t" \
  "v_fma_mix_f32 v230, |v166|, v230, v230 op_sel:[0,1,0] op_sel_hi:[0,1,1] clamp\n\t" \
  "s_waitcnt lgkmcnt(5)\n\t" \
  "ds_read_b64_tr_b16 v[212:213], v249 offset:24608\n\t" \
  "ds_read_b32 v222, v222 offset:10016\n\t" \
  "v_mfma_f32_16x16x32_bf16 v[180:183], v[216:219], v[128:131], v[180:183]\n\t" \
  "s_mov_b32 m0, s46\n\t" \
  "v_or_b32 v172, v232, v233\n\t" \
  "v_or_b32 v173, v234, v235\n\t" \
  "s_mov_b32 exec_hi, 0xffff\n\t" \
  "buffer_load_dwordx4 v252, s[16:19], s33 offen lds\n\t" \
  "v_mfma_f32_16x16x32_bf16 v[188:191], v[216:219], v[132:135], v[188:191]\n\t" \
  "s_mov_b32 exec_hi, -1\n\t" \
  "v_fma_f32 v165, |v165|, v229, v165 mul:4\n\t" \
  "v_fma_f32 v164, |v164|, v228, v164 mul:4\n\t" \
  "s_add_u32 s33, s33, s32\n\t" \
  "ds_read_b64_tr_b16 v[214:215], v249 offset:26144\n\t" \
  "v_mfma_f32_16x16x32_bf16 v[196:199], v[216:219], v[136:139], v[196:199]\n\t" \
  "v_fma_f32 v167, |v167|, v231, v167 mul:4\n\t" \
  "v_fma_f32 v166, |v166|, v230, v166 mul:4\n\t" \
  "ds_read_u16_d16_hi v225, v225 offset:10528\n\t" \
  "v_mfma_f32_16x16x32_bf16 v[204:207], v[216:219], v[140:143], v[204:207]\n\t" \
  "v_cvt_pk_bf16_f32 v164, v164, v165\n\t" \
  "v_cvt_pk_bf16_f32 v165, v166, v167\n\t" \
  "ds_read_b64_tr_b16 v[216:217], v249 offset:24664\n\t" \
  "v_mfma_f32_16x16x32_bf16 v[32:35], v[242:245], v[236:239], v[32:35]\n\t" \
  "v_cvt_pknorm_i16_f32 v228, |v194|, |v195|\n\t" \
  "v_cvt_pknorm_i16_f32 v230, |v192|, |v193|\n\t" \
  "ds_read_b64_tr_b16 v[218:219], v249 offset:26200\n\t" \
  "v_mfma_f32_16x16x32_bf16 v[36:39], v[240:243], v[236:239], v[36:39]\n\t" \
  "v_lshlrev_b32_sdwa v229, 2, v228 dst_sel:DWORD dst_unused:UNUSED_PAD src0_sel:DWORD src1_sel:BYTE_3\n\t" \
  "v_lshlrev_b32_sdwa v228, 2, v228 dst_sel:DWORD dst_unused:UNUSED_PAD src0_sel:DWORD src1_sel:BYTE_1\n\t" \
  "ds_read_u16 v224, v224 offset:10528\n\t" \
  "s_waitcnt lgkmcnt(7)\n\t" \
  "v_mfma_f32_16x16x32_bf16 v[0:3], v[208:211], v[146:149], v[0:3]\n\t" \
  "v_lshlrev_b32_sdwa v231, 2, v230 dst_sel:DWORD dst_unused:UNUSED_PAD src0_sel:DWORD src1_sel:BYTE_3\n\t" \
  "v_lshlrev_b32_sdwa v230, 2, v230 dst_sel:DWORD dst_unused:UNUSED_PAD src0_sel:DWORD src1_sel:BYTE_1\n\t" \
  "ds_read_u16_d16_hi v227, v227 offset:10528\n\t" \
  "v_mfma_f32_16x16x32_bf16 v[24:27], v[208:211], v[154:157], v[24:27]\n\t" \
  "s_add_u32 m0, s46, s35\n\t" \
  "v_lshlrev_b32_sdwa v233, 1, v203 dst_sel:DWORD dst_unused:UNUSED_PAD src0_sel:DWORD src1_sel:WORD_1\n\t" \
  "v_lshlrev_b32_sdwa v232, 1, v202 dst_sel:DWORD dst_unused:UNUSED_PAD src0_sel:DWORD src1_sel:WORD_1\n\t" \
  "s_mov_b32 exec_hi, 0xffff\n\t" \
  "buffer_load_dwordx4 v252, s[16:19], s34 offen lds\n\t" \
  "v_mfma_f32_16x16x32_bf16 v[48:51], v[208:211], v[162:165], v[48:51]\n\t" \
  "s_mov_b32 exec_hi, -1\n\t" \
  "v_lshlrev_b32_sdwa v235, 1, v201 dst_sel:DWORD dst_unused:UNUSED_PAD src0_sel:DWORD src1_sel:WORD_1\n\t" \
  "s_add_u32 s34, s34, s32\n\t" \
  "ds_read_u16 v226, v226 offset:10528\n\t" \
  "ds_read_b32 v229, v229 offset:10016\n\t" \
  "v_mfma_f32_16x16x32_bf16 v[72:75], v[208:211], v[170:173], v[72:75]\n\t" \
  "v_lshlrev_b32_sdwa v234, 1, v200 dst_sel:DWORD dst_unused:UNUSED_PAD src0_sel:DWORD src1_sel:WORD_1\n\t" \
  "s_waitcnt lgkmcnt(7)\n\t" \
  "ds_read_b64_tr_b16 v[208:209], v249 offset:24672\n\t" \
  "ds_read_b32 v228, v228 offset:10016\n\t" \
  "v_mfma_f32_16x16x32_bf16 v[4:7], v[212:215], v[146:149], v[4:7]\n\t" \
  "v_fma_mix_f32 v221, |v179|, v221, v221 op_sel:[0,1,0] op_sel_hi:[0,1,1] clamp\n\t" \
  "ds_read_b64_tr_b16 v[210:211], v249 offset:26208\n\t" \
  "ds_read_b32 v231, v231 offset:10016\n\t" \
  "v_mfma_f32_16x16x32_bf16 v[28:31], v[212:215], v[154:157], v[28:31]\n\t" \
  "v_fma_mix_f32 v220, |v178|, v220, v220 op_sel:[0,1,0] op_sel_hi:[0,1,1] clamp\n\t" \
  "v_fma_mix_f32 v223, |v177|, v223, v223 op_sel:[0,1,0] op_sel_hi:[0,1,1] clamp\n\t" \
  "ds_read_b32 v230, v230 offset:10016\n\t" \
  "v_mfma_f32_16x16x32_bf16 v[52:55], v[212:215], v[162:165], v[52:55]\n\t" \
  "s_waitcnt lgkmcnt(6)\n\t" \
  "v_fma_mix_f32 v222, |v176|, v222, v222 op_sel:[0,1,0] op_sel_hi:[0,1,1] clamp\n\t" \
  "v_or_b32 v187, v224, v225\n\t" \
  "ds_read_u16_d16_hi v233, v233 offset:10528\n\t" \
  "v_mfma_f32_16x16x32_bf16 v[76:79], v[212:215], v[170:173], v[76:79]\n\t" \
  "v_or_b32 v186, v226, v227\n\t" \
  "v_fma_f32 v179, |v179|, v221, v179 mul:4\n\t" \
  "buffer_load_dwordx4 v[236:239], v250, s[8:11], s62 offen offset:128 nt\n\t" \
  "ds_read_b64_tr_b16 v[212:213], v249 offset:24728\n\t" \
  "v_mfma_f32_16x16x32_bf16 v[8:11], v[216:219], v[146:149], v[8:11]\n\t" \
  "v_fma_f32 v178, |v178|, v220, v178 mul:4\n\t" \
  "v_fma_f32 v177, |v177|, v223, v177 mul:4\n\t" \
  "ds_read_b64_tr_b16 v[214:215], v249 offset:26264\n\t" \
  "v_mfma_f32_16x16x32_bf16 v[32:35], v[216:219], v[154:157], v[32:35]\n\t" \
  "v_fma_f32 v176, |v176|, v222, v176 mul:4\n\t" \
  "v_cvt_pk_bf16_f32 v179, v178, v179\n\t" \
  "ds_read_u16 v232, v232 offset:10528\n\t" \
  "v_mfma_f32_16x16x32_bf16 v[56:59], v[216:219], v[162:165], v[56:59]\n\t" \
  "v_cvt_pk_bf16_f32 v178, v176, v177\n\t" \
  "v_cvt_pknorm_i16_f32 v220, |v180|, |v181|\n\t" \
  "ds_read_u16_d16_hi v235, v235 offset:10528\n\t" \
  "v_mfma_f32_16x16x32_bf16 v[80:83], v[216:219], v[170:173], v[80:83]\n\t" \
  "v_cvt_pknorm_i16_f32 v222, |v182|, |v183|\n\t" \
  "v_lshlrev_b32_sdwa v221, 2, v220 dst_sel:DWORD dst_unused:UNUSED_PAD src0_sel:DWORD src1_sel:BYTE_3\n\t" \
  "ds_read_b64_tr_b16 v[216:217], v249 offset:24736\n\t" \
  "s_waitcnt lgkmcnt(8)\n\t" \
  "v_mfma_f32_16x16x32_bf16 v[12:15], v[208:211], v[146:149], v[12:15]\n\t" \
  "v_lshlrev_b32_sdwa v220, 2, v220 dst_sel:DWORD dst_unused:UNUSED_PAD src0_sel:DWORD src1_sel:BYTE_1\n\t" \
  "v_lshlrev_b32_sdwa v223, 2, v222 dst_sel:DWORD dst_unused:UNUSED_PAD src0_sel:DWORD src1_sel:BYTE_3\n\t" \
  "ds_read_b64_tr_b16 v[218:219], v249 offset:26272\n\t" \
  "v_mfma_f32_16x16x32_bf16 v[36:39], v[208:211], v[154:157], v[36:39]\n\t" \
  "s_add_u32 m0, s47, s50\n\t" \
  "v_lshlrev_b32_sdwa v222, 2, v222 dst_sel:DWORD dst_unused:UNUSED_PAD src0_sel:DWORD src1_sel:BYTE_1\n\t" \
  "v_lshlrev_b32_sdwa v225, 1, v189 dst_sel:DWORD dst_unused:UNUSED_PAD src0_sel:DWORD src1_sel:WORD_1\n\t" \
  "s_add_u32 s3, s33, s51\n\t" \
  "s_mov_b64 exec, s[68:69]\n\t" \
  "buffer_load_dwordx4 v252, s[16:19], s3 offen lds\n\t" \
  "v_mfma_f32_16x16x32_bf16 v[60:63], v[208:211], v[162:165], v[60:63]\n\t" \
  "s_mov_b64 exec, -1\n\t" \
  "v_lshlrev_b32_sdwa v224, 1, v188 dst_sel:DWORD dst_unused:UNUSED_PAD src0_sel:DWORD src1_sel:WORD_1\n\t" \
  "v_lshlrev_b32_sdwa v227, 1, v191 dst_sel:DWORD dst_unused:UNUSED_PAD src0_sel:DWORD src1_sel:WORD_1\n\t" \
  "ds_read_u16 v234, v234 offset:10528\n\t" \
  "v_mfma_f32_16x16x32_bf16 v[84:87], v[208:211], v[170:173], v[84:87]\n\t" \
  "v_lshlrev_b32_sdwa v226, 1, v190 dst_sel:DWORD dst_unused:UNUSED_PAD src0_sel:DWORD src1_sel:WORD_1\n\t" \
  "s_waitcnt lgkmcnt(5)\n\t" \
  "ds_read_b64_tr_b16 v[208:209], v248 offset:36904\n\t" \
  "ds_read_b32 v221, v221 offset:10016\n\t" \
  "v_mfma_f32_16x16x32_bf16 v[16:19], v[212:215], v[146:149], v[16:19]\n\t" \
  "v_fma_mix_f32 v229, |v195|, v229, v229 op_sel:[0,1,0] op_sel_hi:[0,1,1] clamp\n\t" \
  "ds_read_b64_tr_b16 v[210:211], v248 offset:37928\n\t" \
  "ds_read_b32 v220, v220 offset:10016\n\t" \
  "v_mfma_f32_16x16x32_bf16 v[40:43], v[212:215], v[154:157], v[40:43]\n\t" \
  "v_fma_mix_f32 v228, |v194|, v228, v228 op_sel:[0,1,0] op_sel_hi:[0,1,1] clamp\n\t" \
  "v_fma_mix_f32 v231, |v193|, v231, v231 op_sel:[0,1,0] op_sel_hi:[0,1,1] clamp\n\t" \
  "ds_read_b32 v223, v223 offset:10016\n\t" \
  "ds_read_b32 v222, v222 offset:10016\n\t" \
  "v_mfma_f32_16x16x32_bf16 v[64:67], v[212:215], v[162:165], v[64:67]\n\t" \
  "s_waitcnt lgkmcnt(6)\n\t" \
  "v_fma_mix_f32 v230, |v192|, v230, v230 op_sel:[0,1,0] op_sel_hi:[0,1,1] clamp\n\t" \
  "v_or_b32 v203, v232, v233\n\t" \
  "ds_read_u16_d16_hi v225, v225 offset:10528\n\t" \
  "v_mfma_f32_16x16x32_bf16 v[88:91], v[212:215], v[170:173], v[88:91]\n\t" \
  "v_or_b32 v202, v234, v235\n\t" \
  "v_fma_f32 v195, |v195|, v229, v195 mul:4\n\t" \
  "ds_read_b64_tr_b16 v[212:213], v248 offset:39976\n\t" \
  "v_mfma_f32_16x16x32_bf16 v[20:23], v[216:219], v[146:149], v[20:23]\n\t" \
  "v_fma_f32 v194, |v194|, v228, v194 mul:4\n\t" \
  "v_fma_f32 v193, |v193|, v231, v193 mul:4\n\t" \
  "ds_read_b64_tr_b16 v[214:215], v248 offset:41000\n\t" \
  "v_mfma_f32_16x16x32_bf16 v[44:47], v[216:219], v[154:157], v[44:47]\n\t" \
  "s_add_u32 m0, s47, s36\n\t" \
  "v_fma_f32 v192, |v192|, v230, v192 mul:4\n\t" \
  "v_cvt_pk_bf16_f32 v195, v194, v195\n\t" \
  "s_add_u32 s3, s34, s51\n\t" \
  "s_mov_b64 exec, s[68:69]\n\t" \
  "buffer_load_dwordx4 v252, s[16:19], s3 offen lds\n\t" \
  "v_mfma_f32_16x16x32_bf16 v[68:71], v[216:219], v[162:165], v[68:71]\n\t" \
  "s_mov_b64 exec, -1\n\t" \
  "v_cvt_pk_bf16_f32 v194, v192, v193\n\t" \
  "v_cvt_pknorm_i16_f32 v228, |v196|, |v197|\n\t" \
  "ds_read_u16 v224, v224 offset:10528\n\t" \
  "v_mfma_f32_16x16x32_bf16 v[92:95], v[216:219], v[170:173], v[92:95]\n\t" \
  "v_cvt_pknorm_i16_f32 v230, |v198|, |v199|\n\t" \
  "v_lshlrev_b32_sdwa v229, 2, v228 dst_sel:DWORD dst_unused:UNUSED_PAD src0_sel:DWORD src1_sel:BYTE_3\n\t" \
  "ds_read_b64_tr_b16 v[216:217], v248 offset:38952\n\t" \
  "s_waitcnt lgkmcnt(8)\n\t" \
  "v_mfma_f32_16x16x32_bf16 v[144:147], v[208:211], v[96:99], 0\n\t" \
  "v_lshlrev_b32_sdwa v228, 2, v228 dst_sel:DWORD dst_unused:UNUSED_PAD src0_sel:DWORD src1_sel:BYTE_1\n\t" \
  "v_lshlrev_b32_sdwa v231, 2, v230 dst_sel:DWORD dst_unused:UNUSED_PAD src0_sel:DWORD src1_sel:BYTE_3\n\t" \
  "ds_read_b64_tr_b16 v[218:219], v248 offset:42024\n\t" \
  "v_mfma_f32_16x16x32_bf16 v[152:155], v[208:211], v[100:103], 0\n\t" \
  "v_lshlrev_b32_sdwa v230, 2, v230 dst_sel:DWORD dst_unused:UNUSED_PAD src0_sel:DWORD src1_sel:BYTE_1\n\t" \
  "ds_read_u16_d16_hi v227, v227 offset:10528\n\t" \
  "v_mfma_f32_16x16x32_bf16 v[160:163], v[208:211], v[104:107], 0\n\t" \
  "v_lshlrev_b32_sdwa v233, 1, v205 dst_sel:DWORD dst_unused:UNUSED_PAD src0_sel:DWORD src1_sel:WORD_1\n\t" \
  "v_lshlrev_b32_sdwa v232, 1, v204 dst_sel:DWORD dst_unused:UNUSED_PAD src0_sel:DWORD src1_sel:WORD_1\n\t" \
  "ds_read_u16 v226, v226 offset:10528\n\t" \
  "v_mfma_f32_16x16x32_bf16 v[168:171], v[208:211], v[108:111], 0\n\t" \
  "v_lshlrev_b32_sdwa v235, 1, v207 dst_sel:DWORD dst_unused:UNUSED_PAD src0_sel:DWORD src1_sel:WORD_1\n\t" \
  "s_waitcnt lgkmcnt(5)\n\t" \
  "ds_read_b64_tr_b16 v[208:209], v248 offset:37416\n\t" \
  "ds_read_b32 v229, v229 offset:10016\n\t" \
  "v_mfma_f32_16x16x32_bf16 v[144:147], v[212:215], v[112:115], v[144:147]\n\t" \
  "v_lshlrev_b32_sdwa v234, 1, v206 dst_sel:DWORD dst_unused:UNUSED_PAD src0_sel:DWORD src1_sel:WORD_1\n\t" \
  "ds_read_b64_tr_b16 v[210:211], v248 offset:38440\n\t" \
  "ds_read_b32 v228, v228 offset:10016\n\t" \
  "v_mfma_f32_16x16x32_bf16 v[152:155], v[212:215], v[116:119], v[152:155]\n\t" \
  "v_fma_mix_f32 v221, |v181|, v221, v221 op_sel:[0,1,0] op_sel_hi:[0,1,1] clamp\n\t" \
  "v_fma_mix_f32 v220, |v180|, v220, v220 op_sel:[0,1,0] op_sel_hi:[0,1,1] clamp\n\t" \
  "ds_read_b32 v231, v231 offset:10016\n\t" \
  "v_mfma_f32_16x16x32_bf16 v[160:163], v[212:215], v[120:123], v[160:163]\n\t" \
  "v_fma_mix_f32 v223, |v183|, v223, v223 op_sel:[0,1,0] op_sel_hi:[0,1,1] clamp\n\t" \
  "v_fma_mix_f32 v222, |v182|, v222, v222 op_sel:[0,1,0] op_sel_hi:[0,1,1] clamp\n\t" \
  "ds_read_b32 v230, v230 offset:10016\n\t" \
  "s_waitcnt lgkmcnt(6)\n\t" \
  "v_mfma_f32_16x16x32_bf16 v[168:171], v[212:215], v[124:127], v[168:171]\n\t" \
  "v_or_b32 v188, v224, v225\n\t" \
  "v_or_b32 v189, v226, v227\n\t" \
  "ds_read_b64_tr_b16 v[212:213], v248 offset:40488\n\t" \
  "ds_read_u16_d16_hi v233, v233 offset:10528\n\t" \
  "v_mfma_f32_16x16x32_bf16 v[144:147], v[216:219], v[128:131], v[144:147]\n\t" \
  "v_fma_f32 v181, |v181|, v221, v181 mul:4\n\t" \
  "v_fma_f32 v180, |v180|, v220, v180 mul:4\n\t" \
  "ds_read_b64_tr_b16 v[214:215], v248 offset:41512\n\t" \
  "v_mfma_f32_16x16x32_bf16 v[152:155], v[216:219], v[132:135], v[152:155]\n\t" \
  "v_fma_f32 v183, |v183|, v223, v183 mul:4\n\t" \
  "v_fma_f32 v182, |v182|, v222, v182 mul:4\n\t" \
  "ds_read_u16 v232, v232 offset:10528\n\t" \
  "v_mfma_f32_16x16x32_bf16 v[160:163], v[216:219], v[136:139], v[160:163]\n\t" \
  "v_cvt_pk_bf16_f32 v180, v180, v181\n\t" \
  "v_cvt_pk_bf16_f32 v181, v182, v183\n\t" \
  "ds_read_u16_d16_hi v235, v235 offset:10528\n\t" \
  "v_mfma_f32_16x16x32_bf16 v[168:171], v[216:219], v[140:143], v[168:171]\n\t" \
  "v_cvt_pknorm_i16_f32 v220, |v146|, |v147|\n\t" \
  "v_cvt_pknorm_i16_f32 v222, |v144|, |v145|\n\t" \
  "ds_read_b64_tr_b16 v[216:217], v248 offset:39464\n\t" \
  "s_waitcnt lgkmcnt(9)\n\t" \
  "v_mfma_f32_16x16x32_bf16 v[148:151], v[208:211], v[96:99], 0\n\t" \
  "v_lshlrev_b32_sdwa v221, 2, v220 dst_sel:DWORD dst_unused:UNUSED_PAD src0_sel:DWORD src1_sel:BYTE_3\n\t" \
  "v_lshlrev_b32_sdwa v220, 2, v220 dst_sel:DWORD dst_unused:UNUSED_PAD src0_sel:DWORD src1_sel:BYTE_1\n\t" \
  "s_waitcnt vmcnt(2)\n\t" \
  "ds_read_b64_tr_b16 v[218:219], v248 offset:42536\n\t" \
  "v_mfma_f32_16x16x32_bf16 v[156:159], v[208:211], v[100:103], 0\n\t" \
  "v_lshlrev_b32_sdwa v223, 2, v222 dst_sel:DWORD dst_unused:UNUSED_PAD src0_sel:DWORD src1_sel:BYTE_3\n\t" \
  "v_lshlrev_b32_sdwa v222, 2, v222 dst_sel:DWORD dst_unused:UNUSED_PAD src0_sel:DWORD src1_sel:BYTE_1\n\t" \
  "s_mov_b64 exec, 3\n\t" \
  "ds_add_rtn_u32 v246, v251, v250 offset:47184\n\t" \
  "v_mfma_f32_16x16x32_bf16 v[164:167], v[208:211], v[104:107], 0\n\t" \
  "s_mov_b64 exec, -1\n\t" \
  "v_lshlrev_b32_sdwa v225, 1, v155 dst_sel:DWORD dst_unused:UNUSED_PAD src0_sel:DWORD src1_sel:WORD_1\n\t" \
  "ds_read_u16 v234, v234 offset:10528\n\t" \
  "v_mfma_f32_16x16x32_bf16 v[172:175], v[208:211], v[108:111], 0\n\t" \
  "s_waitcnt lgkmcnt(1)\n\t" \
  "v_lshlrev_b32_sdwa v224, 1, v154 dst_sel:DWORD dst_unused:UNUSED_PAD src0_sel:DWORD src1_sel:WORD_1\n\t" \
  "v_readfirstlane_b32 s4, v246\n\t" \
  "ds_read_b32 v221, v221 offset:10016\n\t" \
  "v_mfma_f32_16x16x32_bf16 v[148:151], v[212:215], v[112:115], v[148:151]\n\t" \
  "s_cmpk_lt_u32 s4, 6144\n\t" \
  "v_lshlrev_b32_sdwa v227, 1, v153 dst_sel:DWORD dst_unused:UNUSED_PAD src0_sel:DWORD src1_sel:WORD_1\n\t" \
  "s_cbranch_scc1 .Lcnx_w1_7\n.Lcnx_g1_7:\n\t" \
  "ds_read_b32 v220, v220 offset:10016\n\t" \
  "v_mfma_f32_16x16x32_bf16 v[156:159], v[212:215], v[116:119], v[156:159]\n\t" \
  "v_lshlrev_b32_sdwa v226, 1, v152 dst_sel:DWORD dst_unused:UNUSED_PAD src0_sel:DWORD src1_sel:WORD_1\n\t" \
  "ds_read_b64_tr_b16 v[208:209], v249 offset:30752\n\t" \
  "ds_read_b32 v223, v223 offset:10016\n\t" \
  "v_mfma_f32_16x16x32_bf16 v[164:167], v[212:215], v[120:123], v[164:167]\n\t" \
  "v_fma_mix_f32 v229, |v197|, v229, v229 op_sel:[0,1,0] op_sel_hi:[0,1,1] clamp\n\t" \
  "v_fma_mix_f32 v228, |v196|, v228, v228 op_sel:[0,1,0] op_sel_hi:[0,1,1] clamp\n\t" \
  "ds_read_b64_tr_b16 v[210:211], v249 offset:32288\n\t" \
  "v_mfma_f32_16x16x32_bf16 v[172:175], v[212:215], v[124:127], v[172:175]\n\t" \
  "v_fma_mix_f32 v231, |v199|, v231, v231 op_sel:[0,1,0] op_sel_hi:[0,1,1] clamp\n\t" \
  "v_fma_mix_f32 v230, |v198|, v230, v230 op_sel:[0,1,0] op_sel_hi:[0,1,1] clamp\n\t" \
  "s_waitcnt lgkmcnt(5)\n\t" \
  "ds_read_b64_tr_b16 v[212:213], v249 offset:30760\n\t" \
  "ds_read_b32 v222, v222 offset:10016\n\t" \
  "v_mfma_f32_16x16x32_bf16 v[148:151], v[216:219], v[128:131], v[148:151]\n\t" \
  "s_mov_b32 m0, s47\n\t" \
  "v_or_b32 v204, v232, v233\n\t" \
  "v_or_b32 v205, v234, v235\n\t" \
  "s_mov_b32 exec_hi, 0xffff\n\t" \
  "buffer_load_dwordx4 v252, s[16:19], s33 offen lds\n\t" \
  "v_mfma_f32_16x16x32_bf16 v[156:159], v[216:219], v[132:135], v[156:159]\n\t" \
  "s_mov_b32 exec_hi, -1\n\t" \
  "v_fma_f32 v197, |v197|, v229, v197 mul:4\n\t" \
  "v_fma_f32 v196, |v196|, v228, v196 mul:4\n\t" \
  "s_add_u32 s33, s33, s32\n\t" \
  "ds_read_b64_tr_b16 v[214:215], v249 offset:32296\n\t" \
  "v_mfma_f32_16x16x32_bf16 v[164:167], v[216:219], v[136:139], v[164:167]\n\t" \
  "v_fma_f32 v199, |v199|, v231, v199 mul:4\n\t" \
  "v_fma_f32 v198, |v198|, v230, v198 mul:4\n\t" \
  "ds_read_u16_d16_hi v225, v225 offset:10528\n\t" \
  "v_mfma_f32_16x16x32_bf16 v[172:175], v[216:219], v[140:143], v[172:175]\n\t" \
  "v_cvt_pk_bf16_f32 v196, v196, v197\n\t" \
  "v_cvt_pk_bf16_f32 v197, v198, v199\n\t" \
  "ds_read_b64_tr_b16 v[216:217], v249 offset:30816\n\t" \
  "v_mfma_f32_16x16x32_bf16 v[40:43], v[242:245], v[236:239], v[40:43]\n\t" \
  "v_cvt_pknorm_i16_f32 v228, |v162|, |v163|\n\t" \
  "v_cvt_pknorm_i16_f32 v230, |v160|, |v161|\n\t" \
  "ds_read_b64_tr_b16 v[218:219], v249 offset:32352\n\t" \
  "v_mfma_f32_16x16x32_bf16 v[44:47], v[240:243], v[236:239], v[44:47]\n\t" \
  "v_lshlrev_b32_sdwa v229, 2, v228 dst_sel:DWORD dst_unused:UNUSED_PAD src0_sel:DWORD src1_sel:BYTE_3\n\t" \
  "v_lshlrev_b32_sdwa v228, 2, v228 dst_sel:DWORD dst_unused:UNUSED_PAD src0_sel:DWORD src1_sel:BYTE_1\n\t" \
  "ds_read_u16 v224, v224 offset:10528\n\t" \
  "s_waitcnt lgkmcnt(7)\n\t" \
  "v_mfma_f32_16x16x32_bf16 v[0:3], v[208:211], v[178:181], v[0:3]\n\t" \
  "v_lshlrev_b32_sdwa v231, 2, v230 dst_sel:DWORD dst_unused:UNUSED_PAD src0_sel:DWORD src1_sel:BYTE_3\n\t" \
  "v_lshlrev_b32_sdwa v230, 2, v230 dst_sel:DWORD dst_unused:UNUSED_PAD src0_sel:DWORD src1_sel:BYTE_1\n\t" \
  "ds_read_u16_d16_hi v227, v227 offset:10528\n\t" \
  "v_mfma_f32_16x16x32_bf16 v[24:27], v[208:211], v[186:189], v[24:27]\n\t" \
  "s_add_u32 m0, s47, s35\n\t" \
  "v_lshlrev_b32_sdwa v233, 1, v171 dst_sel:DWORD dst_unused:UNUSED_PAD src0_sel:DWORD src1_sel:WORD_1\n\t" \
  "v_lshlrev_b32_sdwa v232, 1, v170 dst_sel:DWORD dst_unused:UNUSED_PAD src0_sel:DWORD src1_sel:WORD_1\n\t" \
  "s_mov_b32 exec_hi, 0xffff\n\t" \
  "buffer_load_dwordx4 v252, s[16:19], s34 offen lds\n\t" \
  "v_mfma_f32_16x16x32_bf16 v[48:51], v[208:211], v[194:197], v[48:51]\n\t" \
  "s_mov_b32 exec_hi, -1\n\t" \
  "v_lshlrev_b32_sdwa v235, 1, v169 dst_sel:DWORD dst_unused:UNUSED_PAD src0_sel:DWORD src1_sel:WORD_1\n\t" \
  "s_add_u32 s34, s34, s32\n\t" \
  "ds_read_u16 v226, v226 offset:10528\n\t" \
  "ds_read_b32 v229, v229 offset:10016\n\t" \
  "v_mfma_f32_16x16x32_bf16 v[72:75], v[208:211], v[202:205], v[72:75]\n\t" \
  "v_lshlrev_b32_sdwa v234, 1, v168 dst_sel:DWORD dst_unused:UNUSED_PAD src0_sel:DWORD src1_sel:WORD_1\n\t" \
  "s_waitcnt lgkmcnt(7)\n\t" \
  "ds_read_b64_tr_b16 v[208:209], v249 offset:30824\n\t" \
  "ds_read_b32 v228, v228 offset:10016\n\t" \
  "v_mfma_f32_16x16x32_bf16 v[4:7], v[212:215], v[178:181], v[4:7]\n\t" \
  "v_fma_mix_f32 v221, |v147|, v221, v221 op_sel:[0,1,0] op_sel_hi:[0,1,1] clamp\n\t" \
  "ds_read_b64_tr_b16 v[210:211], v249 offset:32360\n\t" \
  "ds_read_b32 v231, v231 offset:10016\n\t" \
  "v_mfma_f32_16x16x32_bf16 v[28:31], v[212:215], v[186:189], v[28:31]\n\t" \
  "v_fma_mix_f32 v220, |v146|, v220, v220 op_sel:[0,1,0] op_sel_hi:[0,1,1] clamp\n\t" \
  "v_fma_mix_f32 v223, |v145|, v223, v223 op_sel:[0,1,0] op_sel_hi:[0,1,1] clamp\n\t" \
  "ds_read_b32 v230, v230 offset:10016\n\t" \
  "v_mfma_f32_16x16x32_bf16 v[52:55], v[212:215], v[194:197], v[52:55]\n\t" \
  "s_waitcnt lgkmcnt(6)\n\t" \
  "v_fma_mix_f32 v222, |v144|, v222, v222 op_sel:[0,1,0] op_sel_hi:[0,1,1] clamp\n\t" \
  "v_or_b32 v155, v224, v225\n\t" \
  "ds_read_u16_d16_hi v233, v233 offset:10528\n\t" \
  "v_mfma_f32_16x16x32_bf16 v[76:79], v[212:215], v[202:205], v[76:79]\n\t" \
  "v_or_b32 v154, v226, v227\n\t" \
  "v_fma_f32 v147, |v147|, v221, v147 mul:4\n\t" \
  "buffer_load_dwordx4 v[236:239], v250, s[8:11], s14 offen offset:0 nt\n\t" \
  "ds_read_b64_tr_b16 v[212:213], v249 offset:30880\n\t" \
  "v_mfma_f32_16x16x32_bf16 v[8:11], v[216:219], v[178:181], v[8:11]\n\t" \
  "v_fma_f32 v146, |v146|, v220, v146 mul:4\n\t" \
  "v_fma_f32 v145, |v145|, v223, v145 mul:4\n\t" \
  "ds_read_b64_tr_b16 v[214:215], v249 offset:32416\n\t" \
  "v_mfma_f32_16x16x32_bf16 v[32:35], v[216:219], v[186:189], v[32:35]\n\t" \
  "v_fma_f32 v144, |v144|, v222, v144 mul:4\n\t" \
  "v_cvt_pk_bf16_f32 v147, v146, v147\n\t" \
  "ds_read_u16 v232, v232 offset:10528\n\t" \
  "v_mfma_f32_16x16x32_bf16 v[56:59], v[216:219], v[194:197], v[56:59]\n\t" \
  "v_cvt_pk_bf16_f32 v146, v144, v145\n\t" \
  "v_cvt_pknorm_i16_f32 v220, |v148|, |v149|\n\t" \
  "ds_read_u16_d16_hi v235, v235 offset:10528\n\t" \
  "v_mfma_f32_16x16x32_bf16 v[80:83], v[216:219], v[202:205], v[80:83]\n\t" \
  "v_cvt_pknorm_i16_f32 v222, |v150|, |v151|\n\t" \
  "v_lshlrev_b32_sdwa v221, 2, v220 dst_sel:DWORD dst_unused:UNUSED_PAD src0_sel:DWORD src1_sel:BYTE_3\n\t" \
  "ds_read_b64_tr_b16 v[216:217], v249 offset:30888\n\t" \
  "s_waitcnt lgkmcnt(8)\n\t" \
  "v_mfma_f32_16x16x32_bf16 v[12:15], v[208:211], v[178:181], v[12:15]\n\t" \
  "v_lshlrev_b32_sdwa v220, 2, v220 dst_sel:DWORD dst_unused:UNUSED_PAD src0_sel:DWORD src1_sel:BYTE_1\n\t" \
  "v_lshlrev_b32_sdwa v223, 2, v222 dst_sel:DWORD dst_unused:UNUSED_PAD src0_sel:DWORD src1_sel:BYTE_3\n\t" \
  "ds_read_b64_tr_b16 v[218:219], v249 offset:32424\n\t" \
  "v_mfma_f32_16x16x32_bf16 v[36:39], v[208:211], v[186:189], v[36:39]\n\t" \
  "v_lshlrev_b32_sdwa v222, 2, v222 dst_sel:DWORD dst_unused:UNUSED_PAD src0_sel:DWORD src1_sel:BYTE_1\n\t" \
  "v_lshlrev_b32_sdwa v225, 1, v157 dst_sel:DWORD dst_unused:UNUSED_PAD src0_sel:DWORD src1_sel:WORD_1\n\t" \
  "ds_read_u16 v234, v234 offset:10528\n\t" \
  "v_mfma_f32_16x16x32_bf16 v[60:63], v[208:211], v[194:197], v[60:63]\n\t" \
  "v_lshlrev_b32_sdwa v224, 1, v156 dst_sel:DWORD dst_unused:UNUSED_PAD src0_sel:DWORD src1_sel:WORD_1\n\t" \
  "v_lshlrev_b32_sdwa v227, 1, v159 dst_sel:DWORD dst_unused:UNUSED_PAD src0_sel:DWORD src1_sel:WORD_1\n\t" \
  "ds_read_b32 v221, v221 offset:10016\n\t" \
  "v_mfma_f32_16x16x32_bf16 v[84:87], v[208:211], v[202:205], v[84:87]\n\t" \
  "v_lshlrev_b32_sdwa v226, 1, v158 dst_sel:DWORD dst_unused:UNUSED_PAD src0_sel:DWORD src1_sel:WORD_1\n\t" \
  "s_waitcnt lgkmcnt(6)\n\t" \
  "ds_read_b64_tr_b16 v[208:209], v248 offset:0\n\t" \
  "ds_read_b32 v220, v220 offset:10016\n\t" \
  "v_mfma_f32_16x16x32_bf16 v[16:19], v[212:215], v[178:181], v[16:19]\n\t" \
  "v_fma_mix_f32 v229, |v163|, v229, v229 op_sel:[0,1,0] op_sel_hi:[0,1,1] clamp\n\t" \
  "ds_read_b64_tr_b16 v[210:211], v248 offset:1024\n\t" \
  "ds_read_b32 v223, v223 offset:10016\n\t" \
  "v_mfma_f32_16x16x32_bf16 v[40:43], v[212:215], v[186:189], v[40:43]\n\t" \
  "v_fma_mix_f32 v228, |v162|, v228, v228 op_sel:[0,1,0] op_sel_hi:[0,1,1] clamp\n\t" \
  "v_fma_mix_f32 v231, |v161|, v231, v231 op_sel:[0,1,0] op_sel_hi:[0,1,1] clamp\n\t" \
  "ds_read_b32 v222, v222 offset:10016\n\t" \
  "v_mfma_f32_16x16x32_bf16 v[64:67], v[212:215], v[194:197], v[64:67]\n\t" \
  "s_waitcnt lgkmcnt(6)\n\t" \
  "v_fma_mix_f32 v230, |v160|, v230, v230 op_sel:[0,1,0] op_sel_hi:[0,1,1] clamp\n\t" \
  "v_or_b32 v171, v232, v233\n\t" \
  "ds_read_u16_d16_hi v225, v225 offset:10528\n\t" \
  "v_mfma_f32_16x16x32_bf16 v[88:91], v[212:215], v[202:205], v[88:91]\n\t" \
  "v_or_b32 v170, v234, v235\n\t" \
  "v_fma_f32 v163, |v163|, v229, v163 mul:4\n\t" \
  "ds_read_b64_tr_b16 v[212:213], v248 offset:3072\n\t" \
  "v_mfma_f32_16x16x32_bf16 v[20:23], v[216:219], v[178:181], v[20:23]\n\t" \
  "v_fma_f32 v162, |v162|, v228, v162 mul:4\n\t" \
  "v_fma_f32 v161, |v161|, v231, v161 mul:4\n\t" \
  "ds_read_b64_tr_b16 v[214:215], v248 offset:4096\n\t" \
  "v_mfma_f32_16x16x32_bf16 v[44:47], v[216:219], v[186:189], v[44:47]\n\t" \
  "v_fma_f32 v160, |v160|, v230, v160 mul:4\n\t" \
  "v_cvt_pk_bf16_f32 v163, v162, v163\n\t" \
  "ds_read_u16 v224, v224 offset:10528\n\t" \
  "v_mfma_f32_16x16x32_bf16 v[68:71], v[216:219], v[194:197], v[68:71]\n\t" \
  "v_cvt_pk_bf16_f32 v162, v160, v161\n\t" \
  "v_cvt_pknorm_i16_f32 v228, |v164|, |v165|\n\t" \
  "ds_read_u16_d16_hi v227, v227 offset:10528\n\t" \
  "v_mfma_f32_16x16x32_bf16 v[92:95], v[216:219], v[202:205], v[92:95]\n\t" \
  "v_cvt_pknorm_i16_f32 v230, |v166|, |v167|\n\t" \
  "v_lshlrev_b32_sdwa v229, 2, v228 dst_sel:DWORD dst_unused:UNUSED_PAD src0_sel:DWORD src1_sel:BYTE_3\n\t" \
  "ds_read_b64_tr_b16 v[216:217], v248 offset:2048\n\t" \
  "s_waitcnt lgkmcnt(8)\n\t" \
  "v_mfma_f32_16x16x32_bf16 v[176:179], v[208:211], v[96:99], 0\n\t" \
  "v_lshlrev_b32_sdwa v228, 2, v228 dst_sel:DWORD dst_unused:UNUSED_PAD src0_sel:DWORD src1_sel:BYTE_1\n\t" \
  "v_lshlrev_b32_sdwa v231, 2, v230 dst_sel:DWORD dst_unused:UNUSED_PAD src0_sel:DWORD src1_sel:BYTE_3\n\t" \
  "ds_read_b64_tr_b16 v[218:219], v248 offset:5120\n\t" \
  "v_mfma_f32_16x16x32_bf16 v[184:187], v[208:211], v[100:103], 0\n\t" \
  "v_lshlrev_b32_sdwa v230, 2, v230 dst_sel:DWORD dst_unused:UNUSED_PAD src0_sel:DWORD src1_sel:BYTE_1\n\t" \
  "ds_read_u16 v226, v226 offset:10528\n\t" \
  "v_mfma_f32_16x16x32_bf16 v[192:195], v[208:211], v[104:107], 0\n\t" \
  "v_lshlrev_b32_sdwa v233, 1, v173 dst_sel:DWORD dst_unused:UNUSED_PAD src0_sel:DWORD src1_sel:WORD_1\n\t" \
  "v_lshlrev_b32_sdwa v232, 1, v172 dst_sel:DWORD dst_unused:UNUSED_PAD src0_sel:DWORD src1_sel:WORD_1\n\t" \
  "ds_read_b32 v229, v229 offset:10016\n\t" \
  "v_mfma_f32_16x16x32_bf16 v[200:203], v[208:211], v[108:111], 0\n\t" \
  "v_lshlrev_b32_sdwa v235, 1, v175 dst_sel:DWORD dst_unused:UNUSED_PAD src0_sel:DWORD src1_sel:WORD_1\n\t" \
  "s_waitcnt lgkmcnt(6)\n\t" \
  "ds_read_b64_tr_b16 v[208:209], v248 offset:512\n\t" \
  "ds_read_b32 v228, v228 offset:10016\n\t" \
  "v_mfma_f32_16x16x32_bf16 v[176:179], v[212:215], v[112:115], v[176:179]\n\t" \
  "v_lshlrev_b32_sdwa v234, 1, v174 dst_sel:DWORD dst_unused:UNUSED_PAD src0_sel:DWORD src1_sel:WORD_1\n\t" \
  "v_fma_mix_f32 v221, |v149|, v221, v221 op_sel:[0,1,0] op_sel_hi:[0,1,1] clamp\n\t" \
  "ds_read_b64_tr_b16 v[210:211], v248 offset:1536\n\t" \
  "v_mfma_f32_16x16x32_bf16 v[184:187], v[212:215], v[116:119], v[184:187]\n\t" \
  "v_fma_mix_f32 v220, |v148|, v220, v220 op_sel:[0,1,0] op_sel_hi:[0,1,1] clamp\n\t" \
  "ds_read_b32 v231, v231 offset:10016\n\t" \
  "ds_read_b32 v230, v230 offset:10016\n\t" \
  "v_mfma_f32_16x16x32_bf16 v[192:195], v[212:215], v[120:123], v[192:195]\n\t" \
  "v_fma_mix_f32 v223, |v151|, v223, v223 op_sel:[0,1,0] op_sel_hi:[0,1,1] clamp\n\t" \
  "v_fma_mix_f32 v222, |v150|, v222, v222 op_sel:[0,1,0] op_sel_hi:[0,1,1] clamp\n\t" \
  "ds_read_u16_d16_hi v233, v233 offset:10528\n\t" \
  "s_waitcnt lgkmcnt(7)\n\t" \
  "v_mfma_f32_16x16x32_bf16 v[200:203], v[212:215], v[124:127], v[200:203]\n\t" \
  "v_or_b32 v156, v224, v225\n\t" \
  "v_or_b32 v157, v226, v227\n\t" \
  "ds_read_b64_tr_b16 v[212:213], v248 offset:3584\n\t" \
  "v_mfma_f32_16x16x32_bf16 v[176:179], v[216:219], v[128:131], v[176:179]\n\t" \
  "v_fma_f32 v149, |v149|, v221, v149 mul:4\n\t" \
  "v_fma_f32 v148, |v148|, v220, v148 mul:4\n\t" \
  "ds_read_b64_tr_b16 v[214:215], v248 offset:4608\n\t" \
  "v_mfma_f32_16x16x32_bf16 v[184:187], v[216:219], v[132:135], v[184:187]\n\t" \
  "v_fma_f32 v151, |v151|, v223, v151 mul:4\n\t" \
  "v_fma_f32 v150, |v150|, v222, v150 mul:4\n\t" \
  "ds_read_u16 v232, v232 offset:10528\n\t" \
  "v_mfma_f32_16x16x32_bf16 v[192:195], v[216:219], v[136:139], v[192:195]\n\t" \
  "v_cvt_pk_bf16_f32 v148, v148, v149\n\t" \
  "v_cvt_pk_bf16_f32 v149, v150, v151\n\t" \
  "ds_read_u16_d16_hi v235, v235 offset:10528\n\t" \
  "v_mfma_f32_16x16x32_bf16 v[200:203], v[216:219], v[140:143], v[200:203]\n\t" \
  "v_cvt_pknorm_i16_f32 v220, |v178|, |v179|\n\t" \
  "v_cvt_pknorm_i16_f32 v222, |v176|, |v177|\n\t" \
  "ds_read_b64_tr_b16 v[216:217], v248 offset:2560\n\t" \
  "s_waitcnt lgkmcnt(8)\n\t" \
  "v_mfma_f32_16x16x32_bf16 v[180:183], v[208:211], v[96:99], 0\n\t" \
  "v_lshlrev_b32_sdwa v221, 2, v220 dst_sel:DWORD dst_unused:UNUSED_PAD src0_sel:DWORD src1_sel:BYTE_3\n\t" \
  "v_lshlrev_b32_sdwa v220, 2, v220 dst_sel:DWORD dst_unused:UNUSED_PAD src0_sel:DWORD src1_sel:BYTE_1\n\t" \
  "s_waitcnt vmcnt(0)\n\t" \
  "ds_read_b64_tr_b16 v[218:219], v248 offset:5632\n\t" \
  "v_mfma_f32_16x16x32_bf16 v[188:191], v[208:211], v[100:103], 0\n\t" \
  "v_lshlrev_b32_sdwa v223, 2, v222 dst_sel:DWORD dst_unused:UNUSED_PAD src0_sel:DWORD src1_sel:BYTE_3\n\t" \
  "v_lshlrev_b32_sdwa v222, 2, v222 dst_sel:DWORD dst_unused:UNUSED_PAD src0_sel:DWORD src1_sel:BYTE_1\n\t" \
  "s_mov_b64 exec, 3\n\t" \
  "ds_add_rtn_u32 v246, v251, v250 offset:47192\n\t" \
  "v_mfma_f32_16x16x32_bf16 v[196:199], v[208:211], v[104:107], 0\n\t" \
  "s_mov_b64 exec, -1\n\t" \
  "v_lshlrev_b32_sdwa v225, 1, v187 dst_sel:DWORD dst_unused:UNUSED_PAD src0_sel:DWORD src1_sel:WORD_1\n\t" \
  "ds_read_u16 v234, v234 offset:10528\n\t" \
  "v_mfma_f32_16x16x32_bf16 v[204:207], v[208:211], v[108:111], 0\n\t" \
  "s_waitcnt lgkmcnt(1)\n\t" \
  "v_lshlrev_b32_sdwa v224, 1, v186 dst_sel:DWORD dst_unused:UNUSED_PAD src0_sel:DWORD src1_sel:WORD_1\n\t" \
  "v_readfirstlane_b32 s4, v246\n\t" \
  "ds_read_b32 v221, v221 offset:10016\n\t" \
  "v_mfma_f32_16x16x32_bf16 v[180:183], v[212:215], v[112:115], v[180:183]\n\t" \
  "s_cmpk_lt_u32 s4, 6144\n\t" \
  "v_lshlrev_b32_sdwa v227, 1, v185 dst_sel:DWORD dst_unused:UNUSED_PAD src0_sel:DWORD src1_sel:WORD_1\n\t" \
  "s_cbranch_scc1 .Lcnx_w1_8\n.Lcnx_g1_8:\n\t" \
  "ds_read_b32 v220, v220 offset:10016\n\t" \
  "v_mfma_f32_16x16x32_bf16 v[188:191], v[212:215], v[116:119], v[188:191]\n\t" \
  "v_lshlrev_b32_sdwa v226, 1, v184 dst_sel:DWORD dst_unused:UNUSED_PAD src0_sel:DWORD src1_sel:WORD_1\n\t" \
  "ds_read_b64_tr_b16 v[208:209], v249 offset:6152\n\t" \
  "ds_read_b32 v223, v223 offset:10016\n\t" \
  "v_mfma_f32_16x16x32_bf16 v[196:199], v[212:215], v[120:123], v[196:199]\n\t" \
  "v_fma_mix_f32 v229, |v165|, v229, v229 op_sel:[0,1,0] op_sel_hi:[0,1,1] clamp\n\t" \
  "v_fma_mix_f32 v228, |v164|, v228, v228 op_sel:[0,1,0] op_sel_hi:[0,1,1] clamp\n\t" \
  "ds_read_b64_tr_b16 v[210:211], v249 offset:7688\n\t" \
  "v_mfma_f32_16x16x32_bf16 v[204:207], v[212:215], v[124:127], v[204:207]\n\t" \
  "v_fma_mix_f32 v231, |v167|, v231, v231 op_sel:[0,1,0] op_sel_hi:[0,1,1] clamp\n\t" \
  "v_fma_mix_f32 v230, |v166|, v230, v230 op_sel:[0,1,0] op_sel_hi:[0,1,1] clamp\n\t" \
  "s_waitcnt lgkmcnt(5)\n\t" \
  "ds_read_b64_tr_b16 v[212:213], v249 offset:6160\n\t" \
  "ds_read_b32 v222, v222 offset:10016\n\t" \
  "v_mfma_f32_16x16x32_bf16 v[180:183], v[216:219], v[128:131], v[180:183]\n\t" \
  "v_or_b32 v172, v232, v233\n\t" \
  "v_or_b32 v173, v234, v235\n\t" \
  "ds_read_b64_tr_b16 v[214:215], v249 offset:7696\n\t" \
  "v_mfma_f32_16x16x32_bf16 v[188:191], v[216:219], v[132:135], v[188:191]\n\t" \
  "v_fma_f32 v165, |v165|, v229, v165 mul:4\n\t" \
  "v_fma_f32 v164, |v164|, v228, v164 mul:4\n\t" \
  "ds_read_u16_d16_hi v225, v225 offset:10528\n\t" \
  "v_mfma_f32_16x16x32_bf16 v[196:199], v[216:219], v[136:139], v[196:199]\n\t" \
  "v_fma_f32 v167, |v167|, v231, v167 mul:4\n\t" \
  "v_fma_f32 v166, |v166|, v230, v166 mul:4\n\t" \
  "ds_read_u16 v224, v224 offset:10528\n\t" \
  "v_mfma_f32_16x16x32_bf16 v[204:207], v[216:219], v[140:143], v[204:207]\n\t" \
  "v_cvt_pk_bf16_f32 v164, v164, v165\n\t" \
  "v_cvt_pk_bf16_f32 v165, v166, v167\n\t" \
  "ds_read_b64_tr_b16 v[216:217], v249 offset:6216\n\t" \
  "v_mfma_f32_16x16x32_bf16 v[48:51], v[242:245], v[236:239], v[48:51]\n\t" \
  "v_cvt_pknorm_i16_f32 v228, |v194|, |v195|\n\t" \
  "v_cvt_pknorm_i16_f32 v230, |v192|, |v193|\n\t" \
  "ds_read_b64_tr_b16 v[218:219], v249 offset:7752\n\t" \
  "v_mfma_f32_16x16x32_bf16 v[52:55], v[240:243], v[236:239], v[52:55]\n\t" \
  "v_lshlrev_b32_sdwa v229, 2, v228 dst_sel:DWORD dst_unused:UNUSED_PAD src0_sel:DWORD src1_sel:BYTE_3\n\t" \
  "v_lshlrev_b32_sdwa v228, 2, v228 dst_sel:DWORD dst_unused:UNUSED_PAD src0_sel:DWORD src1_sel:BYTE_1\n\t" \
  "ds_read_u16_d16_hi v227, v227 offset:10528\n\t" \
  "s_waitcnt lgkmcnt(8)\n\t" \
  "v_mfma_f32_16x16x32_bf16 v[0:3], v[208:211], v[146:149], v[0:3]\n\t" \
  "v_lshlrev_b32_sdwa v231, 2, v230 dst_sel:DWORD dst_unused:UNUSED_PAD src0_sel:DWORD src1_sel:BYTE_3\n\t" \
  "v_lshlrev_b32_sdwa v230, 2, v230 dst_sel:DWORD dst_unused:UNUSED_PAD src0_sel:DWORD src1_sel:BYTE_1\n\t" \
  "ds_read_u16 v226, v226 offset:10528\n\t" \
  "v_mfma_f32_16x16x32_bf16 v[24:27], v[208:211], v[154:157], v[24:27]\n\t" \
  "v_lshlrev_b32_sdwa v233, 1, v203 dst_sel:DWORD dst_unused:UNUSED_PAD src0_sel:DWORD src1_sel:WORD_1\n\t" \
  "v_lshlrev_b32_sdwa v232, 1, v202 dst_sel:DWORD dst_unused:UNUSED_PAD src0_sel:DWORD src1_sel:WORD_1\n\t" \
  "ds_read_b32 v229, v229 offset:10016\n\t" \
  "v_mfma_f32_16x16x32_bf16 v[48:51], v[208:211], v[162:165], v[48:51]\n\t" \
  "v_lshlrev_b32_sdwa v235, 1, v201 dst_sel:DWORD dst_unused:UNUSED_PAD src0_sel:DWORD src1_sel:WORD_1\n\t" \
  "ds_read_b32 v228, v228 offset:10016\n\t" \
  "v_mfma_f32_16x16x32_bf16 v[72:75], v[208:211], v[170:173], v[72:75]\n\t" \
  "v_lshlrev_b32_sdwa v234, 1, v200 dst_sel:DWORD dst_unused:UNUSED_PAD src0_sel:DWORD src1_sel:WORD_1\n\t" \
  "s_waitcnt lgkmcnt(8)\n\t" \
  "ds_read_b64_tr_b16 v[208:209], v249 offset:6224\n\t" \
  "ds_read_b32 v231, v231 offset:10016\n\t" \
  "v_mfma_f32_16x16x32_bf16 v[4:7], v[212:215], v[146:149], v[4:7]\n\t" \
  "v_fma_mix_f32 v221, |v179|, v221, v221 op_sel:[0,1,0] op_sel_hi:[0,1,1] clamp\n\t" \
  "v_fma_mix_f32 v220, |v178|, v220, v220 op_sel:[0,1,0] op_sel_hi:[0,1,1] clamp\n\t" \
  "ds_read_b64_tr_b16 v[210:211], v249 offset:7760\n\t" \
  "v_mfma_f32_16x16x32_bf16 v[28:31], v[212:215], v[154:157], v[28:31]\n\t" \
  "v_fma_mix_f32 v223, |v177|, v223, v223 op_sel:[0,1,0] op_sel_hi:[0,1,1] clamp\n\t" \
  "v_fma_mix_f32 v222, |v176|, v222, v222 op_sel:[0,1,0] op_sel_hi:[0,1,1] clamp\n\t" \
  "ds_read_b32 v230, v230 offset:10016\n\t" \
  "s_waitcnt lgkmcnt(6)\n\t" \
  "v_mfma_f32_16x16x32_bf16 v[52:55], v[212:215], v[162:165], v[52:55]\n\t" \
  "v_or_b32 v187, v224, v225\n\t" \
  "v_or_b32 v186, v226, v227\n\t" \
  "ds_read_u16_d16_hi v233, v233 offset:10528\n\t" \
  "v_mfma_f32_16x16x32_bf16 v[76:79], v[212:215], v[170:173], v[76:79]\n\t" \
  "v_fma_f32 v179, |v179|, v221, v179 mul:4\n\t" \
  "v_fma_f32 v178, |v178|, v220, v178 mul:4\n\t" \
  "buffer_load_dwordx4 v[236:239], v250, s[8:11], s14 offen offset:64 nt\n\t" \
  "ds_read_b64_tr_b16 v[212:213], v249 offset:6280\n\t" \
  "v_mfma_f32_16x16x32_bf16 v[8:11], v[216:219], v[146:149], v[8:11]\n\t" \
  "v_fma_f32 v177, |v177|, v223, v177 mul:4\n\t" \
  "v_fma_f32 v176, |v176|, v222, v176 mul:4\n\t" \
  "ds_read_b64_tr_b16 v[214:215], v249 offset:7816\n\t" \
  "v_mfma_f32_16x16x32_bf16 v[32:35], v[216:219], v[154:157], v[32:35]\n\t" \
  "v_cvt_pk_bf16_f32 v179, v178, v179\n\t" \
  "v_cvt_pk_bf16_f32 v178, v176, v177\n\t" \
  "ds_read_u16 v232, v232 offset:10528\n\t" \
  "v_mfma_f32_16x16x32_bf16 v[56:59], v[216:219], v[162:165], v[56:59]\n\t" \
  "v_cvt_pknorm_i16_f32 v220, |v180|, |v181|\n\t" \
  "v_cvt_pknorm_i16_f32 v222, |v182|, |v183|\n\t" \
  "ds_read_u16_d16_hi v235, v235 offset:10528\n\t" \
  "v_mfma_f32_16x16x32_bf16 v[80:83], v[216:219], v[170:173], v[80:83]\n\t" \
  "v_lshlrev_b32_sdwa v221, 2, v220 dst_sel:DWORD dst_unused:UNUSED_PAD src0_sel:DWORD src1_sel:BYTE_3\n\t" \
  "ds_read_b64_tr_b16 v[216:217], v249 offset:6288\n\t" \
  "s_waitcnt lgkmcnt(7)\n\t" \
  "v_mfma_f32_16x16x32_bf16 v[12:15], v[208:211], v[146:149], v[12:15]\n\t" \
  "v_lshlrev_b32_sdwa v220, 2, v220 dst_sel:DWORD dst_unused:UNUSED_PAD src0_sel:DWORD src1_sel:BYTE_1\n\t" \
  "v_lshlrev_b32_sdwa v223, 2, v222 dst_sel:DWORD dst_unused:UNUSED_PAD src0_sel:DWORD src1_sel:BYTE_3\n\t" \
  "ds_read_b64_tr_b16 v[218:219], v249 offset:7824\n\t" \
  "v_mfma_f32_16x16x32_bf16 v[36:39], v[208:211], v[154:157], v[36:39]\n\t" \
  "v_lshlrev_b32_sdwa v222, 2, v222 dst_sel:DWORD dst_unused:UNUSED_PAD src0_sel:DWORD src1_sel:BYTE_1\n\t" \
  "v_lshlrev_b32_sdwa v225, 1, v189 dst_sel:DWORD dst_unused:UNUSED_PAD src0_sel:DWORD src1_sel:WORD_1\n\t" \
  "ds_read_u16 v234, v234 offset:10528\n\t" \
  "v_mfma_f32_16x16x32_bf16 v[60:63], v[208:211], v[162:165], v[60:63]\n\t" \
  "v_lshlrev_b32_sdwa v224, 1, v188 dst_sel:DWORD dst_unused:UNUSED_PAD src0_sel:DWORD src1_sel:WORD_1\n\t" \
  "v_lshlrev_b32_sdwa v227, 1, v191 dst_sel:DWORD dst_unused:UNUSED_PAD src0_sel:DWORD src1_sel:WORD_1\n\t" \
  "ds_read_b32 v221, v221 offset:10016\n\t" \
  "v_mfma_f32_16x16x32_bf16 v[84:87], v[208:211], v[170:173], v[84:87]\n\t" \
  "v_lshlrev_b32_sdwa v226, 1, v190 dst_sel:DWORD dst_unused:UNUSED_PAD src0_sel:DWORD src1_sel:WORD_1\n\t" \
  "s_waitcnt lgkmcnt(6)\n\t" \
  "ds_read_b64_tr_b16 v[208:209], v248 offset:43048\n\t" \
  "ds_read_b32 v220, v220 offset:10016\n\t" \
  "v_mfma_f32_16x16x32_bf16 v[16:19], v[212:215], v[146:149], v[16:19]\n\t" \
  "v_fma_mix_f32 v229, |v195|, v229, v229 op_sel:[0,1,0] op_sel_hi:[0,1,1] clamp\n\t" \
  "ds_read_b64_tr_b16 v[210:211], v248 offset:44072\n\t" \
  "ds_read_b32 v223, v223 offset:10016\n\t" \
  "v_mfma_f32_16x16x32_bf16 v[40:43], v[212:215], v[154:157], v[40:43]\n\t" \
  "v_fma_mix_f32 v228, |v194|, v228, v228 op_sel:[0,1,0] op_sel_hi:[0,1,1] clamp\n\t" \
  "v_fma_mix_f32 v231, |v193|, v231, v231 op_sel:[0,1,0] op_sel_hi:[0,1,1] clamp\n\t" \
  "ds_read_b32 v222, v222 offset:10016\n\t" \
  "v_mfma_f32_16x16x32_bf16 v[64:67], v[212:215], v[162:165], v[64:67]\n\t" \
  "s_waitcnt lgkmcnt(6)\n\t" \
  "v_fma_mix_f32 v230, |v192|, v230, v230 op_sel:[0,1,0] op_sel_hi:[0,1,1] clamp\n\t" \
  "v_or_b32 v203, v232, v233\n\t" \
  "ds_read_u16_d16_hi v225, v225 offset:10528\n\t" \
  "v_mfma_f32_16x16x32_bf16 v[88:91], v[212:215], v[170:173], v[88:91]\n\t" \
  "v_or_b32 v202, v234, v235\n\t" \
  "v_fma_f32 v195, |v195|, v229, v195 mul:4\n\t" \
  "ds_read_b64_tr_b16 v[212:213], v248 offset:46120\n\t" \
  "v_mfma_f32_16x16x32_bf16 v[20:23], v[216:219], v[146:149], v[20:23]\n\t" \
  "v_fma_f32 v194, |v194|, v228, v194 mul:4\n\t" \
  "v_fma_f32 v193, |v193|, v231, v193 mul:4\n\t" \
  "ds_read_b64_tr_b16 v[214:215], v248 offset:47144\n\t" \
  "v_mfma_f32_16x16x32_bf16 v[44:47], v[216:219], v[154:157], v[44:47]\n\t" \
  "v_fma_f32 v192, |v192|, v230, v192 mul:4\n\t" \
  "v_cvt_pk_bf16_f32 v195, v194, v195\n\t" \
  "ds_read_u16 v224, v224 offset:10528\n\t" \
  "v_mfma_f32_16x16x32_bf16 v[68:71], v[216:219], v[162:165], v[68:71]\n\t" \
  "v_cvt_pk_bf16_f32 v194, v192, v193\n\t" \
  "v_cvt_pknorm_i16_f32 v228, |v196|, |v197|\n\t" \
  "ds_read_u16_d16_hi v227, v227 offset:10528\n\t" \
  "v_mfma_f32_16x16x32_bf16 v[92:95], v[216:219], v[170:173], v[92:95]\n\t" \
  "v_cvt_pknorm_i16_f32 v230, |v198|, |v199|\n\t" \
  "v_lshlrev_b32_sdwa v229, 2, v228 dst_sel:DWORD dst_unused:UNUSED_PAD src0_sel:DWORD src1_sel:BYTE_3\n\t" \
  "ds_read_b64_tr_b16 v[216:217], v248 offset:45096\n\t" \
  "s_waitcnt lgkmcnt(8)\n\t" \
  "v_mfma_f32_16x16x32_bf16 v[144:147], v[208:211], v[96:99], 0\n\t" \
  "v_lshlrev_b32_sdwa v228, 2, v228 dst_sel:DWORD dst_unused:UNUSED_PAD src0_sel:DWORD src1_sel:BYTE_1\n\t" \
  "v_lshlrev_b32_sdwa v231, 2, v230 dst_sel:DWORD dst_unused:UNUSED_PAD src0_sel:DWORD src1_sel:BYTE_3\n\t" \
  "ds_read_b64_tr_b16 v[218:219], v248 offset:48168\n\t" \
  "v_mfma_f32_16x16x32_bf16 v[152:155], v[208:211], v[100:103], 0\n\t" \
  "v_lshlrev_b32_sdwa v230, 2, v230 dst_sel:DWORD dst_unused:UNUSED_PAD src0_sel:DWORD src1_sel:BYTE_1\n\t" \
  "ds_read_u16 v226, v226 offset:10528\n\t" \
  "v_mfma_f32_16x16x32_bf16 v[160:163], v[208:211], v[104:107], 0\n\t" \
  "v_lshlrev_b32_sdwa v233, 1, v205 dst_sel:DWORD dst_unused:UNUSED_PAD src0_sel:DWORD src1_sel:WORD_1\n\t" \
  "v_lshlrev_b32_sdwa v232, 1, v204 dst_sel:DWORD dst_unused:UNUSED_PAD src0_sel:DWORD src1_sel:WORD_1\n\t" \
  "ds_read_b32 v229, v229 offset:10016\n\t" \
  "v_mfma_f32_16x16x32_bf16 v[168:171], v[208:211], v[108:111], 0\n\t" \
  "v_lshlrev_b32_sdwa v235, 1, v207 dst_sel:DWORD dst_unused:UNUSED_PAD src0_sel:DWORD src1_sel:WORD_1\n\t" \
  "s_waitcnt lgkmcnt(6)\n\t" \
  "ds_read_b64_tr_b16 v[208:209], v248 offset:43560\n\t" \
  "ds_read_b32 v228, v228 offset:10016\n\t" \
  "v_mfma_f32_16x16x32_bf16 v[144:147], v[212:215], v[112:115], v[144:147]\n\t" \
  "v_lshlrev_b32_sdwa v234, 1, v206 dst_sel:DWORD dst_unused:UNUSED_PAD src0_sel:DWORD src1_sel:WORD_1\n\t" \
  "v_fma_mix_f32 v221, |v181|, v221, v221 op_sel:[0,1,0] op_sel_hi:[0,1,1] clamp\n\t" \
  "ds_read_b64_tr_b16 v[210:211], v248 offset:44584\n\t" \
  "v_mfma_f32_16x16x32_bf16 v[152:155], v[212:215], v[116:119], v[152:155]\n\t" \
  "v_fma_mix_f32 v220, |v180|, v220, v220 op_sel:[0,1,0] op_sel_hi:[0,1,1] clamp\n\t" \
  "ds_read_b32 v231, v231 offset:10016\n\t" \
  "ds_read_b32 v230, v230 offset:10016\n\t" \
  "v_mfma_f32_16x16x32_bf16 v[160:163], v[212:215], v[120:123], v[160:163]\n\t" \
  "v_fma_mix_f32 v223, |v183|, v223, v223 op_sel:[0,1,0] op_sel_hi:[0,1,1] clamp\n\t" \
  "v_fma_mix_f32 v222, |v182|, v222, v222 op_sel:[0,1,0] op_sel_hi:[0,1,1] clamp\n\t" \
  "ds_read_u16_d16_hi v233, v233 offset:10528\n\t" \
  "s_waitcnt lgkmcnt(7)\n\t" \
  "v_mfma_f32_16x16x32_bf16 v[168:171], v[212:215], v[124:127], v[168:171]\n\t" \
  "v_or_b32 v188, v224, v225\n\t" \
  "v_or_b32 v189, v226, v227\n\t" \
  "ds_read_b64_tr_b16 v[212:213], v248 offset:46632\n\t" \
  "v_mfma_f32_16x16x32_bf16 v[144:147], v[216:219], v[128:131], v[144:147]\n\t" \
  "v_fma_f32 v181, |v181|, v221, v181 mul:4\n\t" \
  "v_fma_f32 v180, |v180|, v220, v180 mul:4\n\t" \
  "ds_read_b64_tr_b16 v[214:215], v248 offset:47656\n\t" \
  "v_mfma_f32_16x16x32_bf16 v[152:155], v[216:219], v[132:135], v[152:155]\n\t" \
  "v_fma_f32 v183, |v183|, v223, v183 mul:4\n\t" \
  "v_fma_f32 v182, |v182|, v222, v182 mul:4\n\t" \
  "ds_read_u16 v232, v232 offset:10528\n\t" \
  "v_mfma_f32_16x16x32_bf16 v[160:163], v[216:219], v[136:139], v[160:163]\n\t" \
  "v_cvt_pk_bf16_f32 v180, v180, v181\n\t" \
  "v_cvt_pk_bf16_f32 v181, v182, v183\n\t" \
  "ds_read_u16_d16_hi v235, v235 offset:10528\n\t" \
  "v_mfma_f32_16x16x32_bf16 v[168:171], v[216:219], v[140:143], v[168:171]\n\t" \
  "v_cvt_pknorm_i16_f32 v220, |v146|, |v147|\n\t" \
  "v_cvt_pknorm_i16_f32 v222, |v144|, |v145|\n\t" \
  "ds_read_b64_tr_b16 v[216:217], v248 offset:45608\n\t" \
  "s_waitcnt lgkmcnt(8)\n\t" \
  "v_mfma_f32_16x16x32_bf16 v[148:151], v[208:211], v[96:99], 0\n\t" \
  "v_lshlrev_b32_sdwa v221, 2, v220 dst_sel:DWORD dst_unused:UNUSED_PAD src0_sel:DWORD src1_sel:BYTE_3\n\t" \
  "v_lshlrev_b32_sdwa v220, 2, v220 dst_sel:DWORD dst_unused:UNUSED_PAD src0_sel:DWORD src1_sel:BYTE_1\n\t" \
  "s_waitcnt vmcnt(0)\n\t" \
  "ds_read_b64_tr_b16 v[218:219], v248 offset:48680\n\t" \
  "v_mfma_f32_16x16x32_bf16 v[156:159], v[208:211], v[100:103], 0\n\t" \
  "v_lshlrev_b32_sdwa v223, 2, v222 dst_sel:DWORD dst_unused:UNUSED_PAD src0_sel:DWORD src1_sel:BYTE_3\n\t" \
  "v_lshlrev_b32_sdwa v222, 2, v222 dst_sel:DWORD dst_unused:UNUSED_PAD src0_sel:DWORD src1_sel:BYTE_1\n\t" \
  "s_mov_b64 exec, 3\n\t" \
  "ds_add_rtn_u32 v246, v251, v250 offset:47200\n\t" \
  "v_mfma_f32_16x16x32_bf16 v[164:167], v[208:211], v[104:107], 0\n\t" \
  "s_mov_b64 exec, -1\n\t" \
  "v_lshlrev_b32_sdwa v225, 1, v155 dst_sel:DWORD dst_unused:UNUSED_PAD src0_sel:DWORD src1_sel:WORD_1\n\t" \
  "ds_read_u16 v234, v234 offset:10528\n\t" \
  "v_mfma_f32_16x16x32_bf16 v[172:175], v[208:211], v[108:111], 0\n\t" \
  "s_waitcnt lgkmcnt(1)\n\t" \
  "v_lshlrev_b32_sdwa v224, 1, v154 dst_sel:DWORD dst_unused:UNUSED_PAD src0_sel:DWORD src1_sel:WORD_1\n\t" \
  "v_readfirstlane_b32 s4, v246\n\t" \
  "ds_read_b32 v221, v221 offset:10016\n\t" \
  "v_mfma_f32_16x16x32_bf16 v[148:151], v[212:215], v[112:115], v[148:151]\n\t" \
  "s_cmpk_lt_u32 s4, 6144\n\t" \
  "v_lshlrev_b32_sdwa v227, 1, v153 dst_sel:DWORD dst_unused:UNUSED_PAD src0_sel:DWORD src1_sel:WORD_1\n\t" \
  "s_cbranch_scc1 .Lcnx_w1_9\n.Lcnx_g1_9:\n\t" \
  "ds_read_b32 v220, v220 offset:10016\n\t" \
  "v_mfma_f32_16x16x32_bf16 v[156:159], v[212:215], v[116:119], v[156:159]\n\t" \
  "v_lshlrev_b32_sdwa v226, 1, v152 dst_sel:DWORD dst_unused:UNUSED_PAD src0_sel:DWORD src1_sel:WORD_1\n\t" \
  "ds_read_b64_tr_b16 v[208:209], v249 offset:12304\n\t" \
  "ds_read_b32 v223, v223 offset:10016\n\t" \
  "v_mfma_f32_16x16x32_bf16 v[164:167], v[212:215], v[120:123], v[164:167]\n\t" \
  "v_fma_mix_f32 v229, |v197|, v229, v229 op_sel:[0,1,0] op_sel_hi:[0,1,1] clamp\n\t" \
  "v_fma_mix_f32 v228, |v196|, v228, v228 op_sel:[0,1,0] op_sel_hi:[0,1,1] clamp\n\t" \
  "ds_read_b64_tr_b16 v[210:211], v249 offset:13840\n\t" \
  "v_mfma_f32_16x16x32_bf16 v[172:175], v[212:215], v[124:127], v[172:175]\n\t" \
  "v_fma_mix_f32 v231, |v199|, v231, v231 op_sel:[0,1,0] op_sel_hi:[0,1,1] clamp\n\t" \
  "v_fma_mix_f32 v230, |v198|, v230, v230 op_sel:[0,1,0] op_sel_hi:[0,1,1] clamp\n\t" \
  "s_waitcnt lgkmcnt(5)\n\t" \
  "ds_read_b64_tr_b16 v[212:213], v249 offset:12312\n\t" \
  "ds_read_b32 v222, v222 offset:10016\n\t" \
  "v_mfma_f32_16x16x32_bf16 v[148:151], v[216:219], v[128:131], v[148:151]\n\t" \
  "v_or_b32 v204, v232, v233\n\t" \
  "v_or_b32 v205, v234, v235\n\t" \
  "ds_read_b64_tr_b16 v[214:215], v249 offset:13848\n\t" \
  "v_mfma_f32_16x16x32_bf16 v[156:159], v[216:219], v[132:135], v[156:159]\n\t" \
  "v_fma_f32 v197, |v197|, v229, v197 mul:4\n\t" \
  "v_fma_f32 v196, |v196|, v228, v196 mul:4\n\t" \
  "ds_read_u16_d16_hi v225, v225 offset:10528\n\t" \
  "v_mfma_f32_16x16x32_bf16 v[164:167], v[216:219], v[136:139], v[164:167]\n\t" \
  "v_fma_f32 v199, |v199|, v231, v199 mul:4\n\t" \
  "v_fma_f32 v198, |v198|, v230, v198 mul:4\n\t" \
  "ds_read_u16 v224, v224 offset:10528\n\t" \
  "v_mfma_f32_16x16x32_bf16 v[172:175], v[216:219], v[140:143], v[172:175]\n\t" \
  "v_cvt_pk_bf16_f32 v196, v196, v197\n\t" \
  "v_cvt_pk_bf16_f32 v197, v198, v199\n\t" \
  "ds_read_b64_tr_b16 v[216:217], v249 offset:12368\n\t" \
  "v_mfma_f32_16x16x32_bf16 v[56:59], v[242:245], v[236:239], v[56:59]\n\t" \
  "ds_read_b64_tr_b16 v[218:219], v249 offset:13904\n\t" \
  "v_cvt_pknorm_i16_f32 v228, |v162|, |v163|\n\t" \
  "s_setprio 0\n\t" \
  "s_cmp_ge_u32 s39, 2048\n\t" \
  "s_cbranch_scc1 .Lcnx_p\n\ts_setprio 3\n.Lcnx_p:\n\t" \
  "v_cvt_pknorm_i16_f32 v230, |v160|, |v161|\n\t" \
  "v_mfma_f32_16x16x32_bf16 v[60:63], v[240:243], v[236:239], v[60:63]\n\t" \
  "ds_read_u16_d16_hi v227, v227 offset:10528\n\t" \
  "v_lshlrev_b32_sdwa v229, 2, v228 dst_sel:DWORD dst_unused:UNUSED_PAD src0_sel:DWORD src1_sel:BYTE_3\n\t" \
  "v_lshlrev_b32_sdwa v228, 2, v228 dst_sel:DWORD dst_unused:UNUSED_PAD src0_sel:DWORD src1_sel:BYTE_1\n\t" \
  "s_waitcnt lgkmcnt(8)\n\t" \
  "v_mfma_f32_16x16x32_bf16 v[0:3], v[208:211], v[178:181], v[0:3]\n\t" \
  "ds_read_u16 v226, v226 offset:10528\n\t" \
  "v_lshlrev_b32_sdwa v231, 2, v230 dst_sel:DWORD dst_unused:UNUSED_PAD src0_sel:DWORD src1_sel:BYTE_3\n\t" \
  "v_lshlrev_b32_sdwa v230, 2, v230 dst_sel:DWORD dst_unused:UNUSED_PAD src0_sel:DWORD src1_sel:BYTE_1\n\t" \
  "v_mfma_f32_16x16x32_bf16 v[24:27], v[208:211], v[186:189], v[24:27]\n\t" \
  "ds_read_b32 v229, v229 offset:10016\n\t" \
  "v_lshlrev_b32_sdwa v233, 1, v171 dst_sel:DWORD dst_unused:UNUSED_PAD src0_sel:DWORD src1_sel:WORD_1\n\t" \
  "v_lshlrev_b32_sdwa v232, 1, v170 dst_sel:DWORD dst_unused:UNUSED_PAD src0_sel:DWORD src1_sel:WORD_1\n\t" \
  "v_mfma_f32_16x16x32_bf16 v[48:51], v[208:211], v[194:197], v[48:51]\n\t" \
  "ds_read_b32 v228, v228 offset:10016\n\t" \
  "v_lshlrev_b32_sdwa v235, 1, v169 dst_sel:DWORD dst_unused:UNUSED_PAD src0_sel:DWORD src1_sel:WORD_1\n\t" \
  "v_lshlrev_b32_sdwa v234, 1, v168 dst_sel:DWORD dst_unused:UNUSED_PAD src0_sel:DWORD src1_sel:WORD_1\n\t" \
  "v_mfma_f32_16x16x32_bf16 v[72:75], v[208:211], v[202:205], v[72:75]\n\t" \
  "ds_read_b64_tr_b16 v[208:209], v249 offset:12376\n\t" \
  "ds_read_b32 v231, v231 offset:10016\n\t" \
  "s_waitcnt lgkmcnt(11)\n\t" \
  "v_fma_mix_f32 v221, |v147|, v221, v221 op_sel:[0,1,0] op_sel_hi:[0,1,1] clamp\n\t" \
  "s_waitcnt lgkmcnt(10)\n\t" \
  "v_mfma_f32_16x16x32_bf16 v[4:7], v[212:215], v[178:181], v[4:7]\n\t" \
  "ds_read_b64_tr_b16 v[210:211], v249 offset:13912\n\t" \
  "ds_read_b32 v230, v230 offset:10016\n\t" \
  "v_fma_mix_f32 v220, |v146|, v220, v220 op_sel:[0,1,0] op_sel_hi:[0,1,1] clamp\n\t" \
  "v_mfma_f32_16x16x32_bf16 v[28:31], v[212:215], v[186:189], v[28:31]\n\t" \
  "ds_read_u16_d16_hi v233, v233 offset:10528\n\t" \
  "v_fma_mix_f32 v223, |v145|, v223, v223 op_sel:[0,1,0] op_sel_hi:[0,1,1] clamp\n\t" \
  "v_fma_mix_f32 v222, |v144|, v222, v222 op_sel:[0,1,0] op_sel_hi:[0,1,1] clamp\n\t" \
  "s_waitcnt lgkmcnt(7)\n\t" \
  "v_mfma_f32_16x16x32_bf16 v[52:55], v[212:215], v[194:197], v[52:55]\n\t" \
  "ds_read_u16 v232, v232 offset:10528\n\t" \
  "v_or_b32 v155, v224, v225\n\t" \
  "v_or_b32 v154, v226, v227\n\t" \
  "v_mfma_f32_16x16x32_bf16 v[76:79], v[212:215], v[202:205], v[76:79]\n\t" \
  "ds_read_b64_tr_b16 v[212:213], v249 offset:12432\n\t" \
  "buffer_load_dwordx4 v[236:239], v250, s[8:11], s14 offen offset:128 nt\n\t" \
  "v_fma_f32 v147, |v147|, v221, v147 mul:4\n\t" \
  "v_mfma_f32_16x16x32_bf16 v[8:11], v[216:219], v[178:181], v[8:11]\n\t" \
  "ds_read_b64_tr_b16 v[214:215], v249 offset:13968\n\t" \
  "v_fma_f32 v146, |v146|, v220, v146 mul:4\n\t" \
  "v_fma_f32 v145, |v145|, v223, v145 mul:4\n\t" \
  "v_mfma_f32_16x16x32_bf16 v[32:35], v[216:219], v[186:189], v[32:35]\n\t" \
  "ds_read_u16_d16_hi v235, v235 offset:10528\n\t" \
  "v_fma_f32 v144, |v144|, v222, v144 mul:4\n\t" \
  "v_cvt_pk_bf16_f32 v147, v146, v147\n\t" \
  "v_mfma_f32_16x16x32_bf16 v[56:59], v[216:219], v[194:197], v[56:59]\n\t" \
  "ds_read_u16 v234, v234 offset:10528\n\t" \
  "v_cvt_pk_bf16_f32 v146, v144, v145\n\t" \
  "v_cvt_pknorm_i16_f32 v220, |v148|, |v149|\n\t" \
  "v_mfma_f32_16x16x32_bf16 v[80:83], v[216:219], v[202:205], v[80:83]\n\t" \
  "ds_read_b64_tr_b16 v[216:217], v249 offset:12440\n\t" \
  "v_cvt_pknorm_i16_f32 v222, |v150|, |v151|\n\t" \
  "v_lshlrev_b32_sdwa v221, 2, v220 dst_sel:DWORD dst_unused:UNUSED_PAD src0_sel:DWORD src1_sel:BYTE_3\n\t" \
  "s_waitcnt lgkmcnt(8)\n\t" \
  "v_mfma_f32_16x16x32_bf16 v[12:15], v[208:211], v[178:181], v[12:15]\n\t" \
  "ds_read_b64_tr_b16 v[218:219], v249 offset:13976\n\t" \
  "v_lshlrev_b32_sdwa v220, 2, v220 dst_sel:DWORD dst_unused:UNUSED_PAD src0_sel:DWORD src1_sel:BYTE_1\n\t" \
  "v_lshlrev_b32_sdwa v223, 2, v222 dst_sel:DWORD dst_unused:UNUSED_PAD src0_sel:DWORD src1_sel:BYTE_3\n\t" \
  "v_mfma_f32_16x16x32_bf16 v[36:39], v[208:211], v[186:189], v[36:39]\n\t" \
  "ds_read_b32 v221, v221 offset:10016\n\t" \
  "v_lshlrev_b32_sdwa v222, 2, v222 dst_sel:DWORD dst_unused:UNUSED_PAD src0_sel:DWORD src1_sel:BYTE_1\n\t" \
  "v_lshlrev_b32_sdwa v225, 1, v157 dst_sel:DWORD dst_unused:UNUSED_PAD src0_sel:DWORD src1_sel:WORD_1\n\t" \
  "v_mfma_f32_16x16x32_bf16 v[60:63], v[208:211], v[194:197], v[60:63]\n\t" \
  "ds_read_b32 v220, v220 offset:10016\n\t" \
  "v_lshlrev_b32_sdwa v224, 1, v156 dst_sel:DWORD dst_unused:UNUSED_PAD src0_sel:DWORD src1_sel:WORD_1\n\t" \
  "v_lshlrev_b32_sdwa v227, 1, v159 dst_sel:DWORD dst_unused:UNUSED_PAD src0_sel:DWORD src1_sel:WORD_1\n\t" \
  "v_mfma_f32_16x16x32_bf16 v[84:87], v[208:211], v[202:205], v[84:87]\n\t" \
  "ds_read_b64_tr_b16 v[208:209], v248 offset:49192\n\t" \
  "ds_read_b32 v223, v223 offset:10016\n\t" \
  "v_lshlrev_b32_sdwa v226, 1, v158 dst_sel:DWORD dst_unused:UNUSED_PAD src0_sel:DWORD src1_sel:WORD_1\n\t" \
  "s_waitcnt lgkmcnt(8)\n\t" \
  "v_mfma_f32_16x16x32_bf16 v[16:19], v[212:215], v[178:181], v[16:19]\n\t" \
  "ds_read_b64_tr_b16 v[210:211], v248 offset:50216\n\t" \
  "v_fma_mix_f32 v229, |v163|, v229, v229 op_sel:[0,1,0] op_sel_hi:[0,1,1] clamp\n\t" \
  "v_fma_mix_f32 v228, |v162|, v228, v228 op_sel:[0,1,0] op_sel_hi:[0,1,1] clamp\n\t" \
  "v_mfma_f32_16x16x32_bf16 v[40:43], v[212:215], v[186:189], v[40:43]\n\t" \
  "ds_read_b32 v222, v222 offset:10016\n\t" \
  "v_fma_mix_f32 v231, |v161|, v231, v231 op_sel:[0,1,0] op_sel_hi:[0,1,1] clamp\n\t" \
  "v_fma_mix_f32 v230, |v160|, v230, v230 op_sel:[0,1,0] op_sel_hi:[0,1,1] clamp\n\t" \
  "s_waitcnt lgkmcnt(8)\n\t" \
  "v_mfma_f32_16x16x32_bf16 v[64:67], v[212:215], v[194:197], v[64:67]\n\t" \
  "ds_read_u16_d16_hi v225, v225 offset:10528\n\t" \
  "v_or_b32 v171, v232, v233\n\t" \
  "v_or_b32 v170, v234, v235\n\t" \
  "v_mfma_f32_16x16x32_bf16 v[88:91], v[212:215], v[202:205], v[88:91]\n\t" \
  "ds_read_b64_tr_b16 v[212:213], v248 offset:52264\n\t" \
  "v_fma_f32 v163, |v163|, v229, v163 mul:4\n\t" \
  "v_fma_f32 v162, |v162|, v228, v162 mul:4\n\t" \
  "s_waitcnt lgkmcnt(8)\n\t" \
  "v_mfma_f32_16x16x32_bf16 v[20:23], v[216:219], v[178:181], v[20:23]\n\t" \
  "ds_read_b64_tr_b16 v[214:215], v248 offset:53288\n\t" \
  "v_fma_f32 v161, |v161|, v231, v161 mul:4\n\t" \
  "v_fma_f32 v160, |v160|, v230, v160 mul:4\n\t" \
  "v_mfma_f32_16x16x32_bf16 v[44:47], v[216:219], v[186:189], v[44:47]\n\t" \
  "ds_read_u16 v224, v224 offset:10528\n\t" \
  "v_cvt_pk_bf16_f32 v163, v162, v163\n\t" \
  "v_cvt_pk_bf16_f32 v162, v160, v161\n\t" \
  "v_mfma_f32_16x16x32_bf16 v[68:71], v[216:219], v[194:197], v[68:71]\n\t" \
  "ds_read_u16_d16_hi v227, v227 offset:10528\n\t" \
  "v_cvt_pknorm_i16_f32 v228, |v164|, |v165|\n\t" \
  "v_cvt_pknorm_i16_f32 v230, |v166|, |v167|\n\t" \
  "v_mfma_f32_16x16x32_bf16 v[92:95], v[216:219], v[202:205], v[92:95]\n\t" \
  "ds_read_b64_tr_b16 v[216:217], v248 offset:51240\n\t" \
  "v_lshlrev_b32_sdwa v229, 2, v228 dst_sel:DWORD dst_unused:UNUSED_PAD src0_sel:DWORD src1_sel:BYTE_3\n\t" \
  "s_waitcnt lgkmcnt(7)\n\t" \
  "v_mfma_f32_16x16x32_bf16 v[176:179], v[208:211], v[96:99], 0\n\t" \
  "ds_read_b64_tr_b16 v[218:219], v248 offset:54312\n\t" \
  "v_lshlrev_b32_sdwa v228, 2, v228 dst_sel:DWORD dst_unused:UNUSED_PAD src0_sel:DWORD src1_sel:BYTE_1\n\t" \
  "v_lshlrev_b32_sdwa v231, 2, v230 dst_sel:DWORD dst_unused:UNUSED_PAD src0_sel:DWORD src1_sel:BYTE_3\n\t" \
  "v_mfma_f32_16x16x32_bf16 v[184:187], v[208:211], v[100:103], 0\n\t" \
  "ds_read_u16 v226, v226 offset:10528\n\t" \
  "v_lshlrev_b32_sdwa v230, 2, v230 dst_sel:DWORD dst_unused:UNUSED_PAD src0_sel:DWORD src1_sel:BYTE_1\n\t" \
  "v_lshlrev_b32_sdwa v233, 1, v173 dst_sel:DWORD dst_unused:UNUSED_PAD src0_sel:DWORD src1_sel:WORD_1\n\t" \
  "v_mfma_f32_16x16x32_bf16 v[192:195], v[208:211], v[104:107], 0\n\t" \
  "ds_read_b32 v229, v229 offset:10016\n\t" \
  "v_lshlrev_b32_sdwa v232, 1, v172 dst_sel:DWORD dst_unused:UNUSED_PAD src0_sel:DWORD src1_sel:WORD_1\n\t" \
  "v_lshlrev_b32_sdwa v235, 1, v175 dst_sel:DWORD dst_unused:UNUSED_PAD src0_sel:DWORD src1_sel:WORD_1\n\t" \
  "v_mfma_f32_16x16x32_bf16 v[200:203], v[208:211], v[108:111], 0\n\t" \
  "ds_read_b64_tr_b16 v[208:209], v248 offset:49704\n\t" \
  "ds_read_b32 v228, v228 offset:10016\n\t" \
  "v_lshlrev_b32_sdwa v234, 1, v174 dst_sel:DWORD dst_unused:UNUSED_PAD src0_sel:DWORD src1_sel:WORD_1\n\t" \
  "s_waitcnt lgkmcnt(8)\n\t" \
  "v_mfma_f32_16x16x32_bf16 v[176:179], v[212:215], v[112:115], v[176:179]\n\t" \
  "ds_read_b64_tr_b16 v[210:211], v248 offset:50728\n\t" \
  "ds_read_b32 v231, v231 offset:10016\n\t" \
  "v_fma_mix_f32 v221, |v149|, v221, v221 op_sel:[0,1,0] op_sel_hi:[0,1,1] clamp\n\t" \
  "v_mfma_f32_16x16x32_bf16 v[184:187], v[212:215], v[116:119], v[184:187]\n\t" \
  "ds_read_b32 v230, v230 offset:10016\n\t" \
  "v_fma_mix_f32 v220, |v148|, v220, v220 op_sel:[0,1,0] op_sel_hi:[0,1,1] clamp\n\t" \
  "v_fma_mix_f32 v223, |v151|, v223, v223 op_sel:[0,1,0] op_sel_hi:[0,1,1] clamp\n\t" \
  "v_mfma_f32_16x16x32_bf16 v[192:195], v[212:215], v[120:123], v[192:195]\n\t" \
  "ds_read_u16_d16_hi v233, v233 offset:10528\n\t" \
  "v_fma_mix_f32 v222, |v150|, v222, v222 op_sel:[0,1,0] op_sel_hi:[0,1,1] clamp\n\t" \
  "s_waitcnt lgkmcnt(7)\n\t" \
  "v_or_b32 v156, v224, v225\n\t" \
  "v_mfma_f32_16x16x32_bf16 v[200:203], v[212:215], v[124:127], v[200:203]\n\t" \
  "ds_read_b64_tr_b16 v[212:213], v248 offset:52776\n\t" \
  "v_or_b32 v157, v226, v227\n\t" \
  "v_mfma_f32_16x16x32_bf16 v[176:179], v[216:219], v[128:131], v[176:179]\n\t" \
  "ds_read_b64_tr_b16 v[214:215], v248 offset:53800\n\t" \
  "v_fma_f32 v149, |v149|, v221, v149 mul:4\n\t" \
  "v_fma_f32 v148, |v148|, v220, v148 mul:4\n\t" \
  "v_mfma_f32_16x16x32_bf16 v[184:187], v[216:219], v[132:135], v[184:187]\n\t" \
  "ds_read_u16 v232, v232 offset:10528\n\t" \
  "v_fma_f32 v151, |v151|, v223, v151 mul:4\n\t" \
  "v_fma_f32 v150, |v150|, v222, v150 mul:4\n\t" \
  "v_mfma_f32_16x16x32_bf16 v[192:195], v[216:219], v[136:139], v[192:195]\n\t" \
  "ds_read_u16_d16_hi v235, v235 offset:10528\n\t" \
  "v_cvt_pk_bf16_f32 v148, v148, v149\n\t" \
  "v_cvt_pk_bf16_f32 v149, v150, v151\n\t" \
  "v_mfma_f32_16x16x32_bf16 v[200:203], v[216:219], v[140:143], v[200:203]\n\t" \
  "ds_read_b64_tr_b16 v[216:217], v248 offset:51752\n\t" \
  "v_cvt_pknorm_i16_f32 v220, |v178|, |v179|\n\t" \
  "v_cvt_pknorm_i16_f32 v222, |v176|, |v177|\n\t" \
  "s_waitcnt lgkmcnt(8)\n\t" \
  "v_mfma_f32_16x16x32_bf16 v[180:183], v[208:211], v[96:99], 0\n\t" \
  "ds_read_b64_tr_b16 v[218:219], v248 offset:54824\n\t" \
  "v_lshlrev_b32_sdwa v221, 2, v220 dst_sel:DWORD dst_unused:UNUSED_PAD src0_sel:DWORD src1_sel:BYTE_3\n\t" \
  "s_waitcnt vmcnt(0)\n\t" \
  "v_lshlrev_b32_sdwa v220, 2, v220 dst_sel:DWORD dst_unused:UNUSED_PAD src0_sel:DWORD src1_sel:BYTE_1\n\t" \
  "v_mfma_f32_16x16x32_bf16 v[188:191], v[208:211], v[100:103], 0\n\t" \
  "ds_read_u16 v234, v234 offset:10528\n\t" \
  "v_lshlrev_b32_sdwa v223, 2, v222 dst_sel:DWORD dst_unused:UNUSED_PAD src0_sel:DWORD src1_sel:BYTE_3\n\t" \
  "v_lshlrev_b32_sdwa v222, 2, v222 dst_sel:DWORD dst_unused:UNUSED_PAD src0_sel:DWORD src1_sel:BYTE_1\n\t" \
  "v_mfma_f32_16x16x32_bf16 v[196:199], v[208:211], v[104:107], 0\n\t" \
  "ds_read_b32 v221, v221 offset:10016\n\t" \
  "v_lshlrev_b32_sdwa v225, 1, v187 dst_sel:DWORD dst_unused:UNUSED_PAD src0_sel:DWORD src1_sel:WORD_1\n\t" \
  "v_lshlrev_b32_sdwa v224, 1, v186 dst_sel:DWORD dst_unused:UNUSED_PAD src0_sel:DWORD src1_sel:WORD_1\n\t" \
  "v_mfma_f32_16x16x32_bf16 v[204:207], v[208:211], v[108:111], 0\n\t" \
  "ds_read_b32 v220, v220 offset:10016\n\t" \
  "v_lshlrev_b32_sdwa v227, 1, v185 dst_sel:DWORD dst_unused:UNUSED_PAD src0_sel:DWORD src1_sel:WORD_1\n\t" \
  "v_lshlrev_b32_sdwa v226, 1, v184 dst_sel:DWORD dst_unused:UNUSED_PAD src0_sel:DWORD src1_sel:WORD_1\n\t" \
  "s_waitcnt lgkmcnt(7)\n\t" \
  "v_mfma_f32_16x16x32_bf16 v[180:183], v[212:215], v[112:115], v[180:183]\n\t" \
  "ds_read_b32 v223, v223 offset:10016\n\t" \
  "v_add_u32 v248, -81480, v248\n\t" \
  "v_add_u32 v249, -81480, v249\n\t" \
  "v_mfma_f32_16x16x32_bf16 v[188:191], v[212:215], v[116:119], v[188:191]\n\t" \
  "ds_read_b64_tr_b16 v[208:209], v249 offset:24600\n\t" \
  "ds_read_b32 v222, v222 offset:10016\n\t" \
  "v_fma_mix_f32 v229, |v165|, v229, v229 op_sel:[0,1,0] op_sel_hi:[0,1,1] clamp\n\t" \
  "v_mfma_f32_16x16x32_bf16 v[196:199], v[212:215], v[120:123], v[196:199]\n\t" \
  "ds_read_b64_tr_b16 v[210:211], v249 offset:26136\n\t" \
  "v_fma_mix_f32 v228, |v164|, v228, v228 op_sel:[0,1,0] op_sel_hi:[0,1,1] clamp\n\t" \
  "v_fma_mix_f32 v231, |v167|, v231, v231 op_sel:[0,1,0] op_sel_hi:[0,1,1] clamp\n\t" \
  "v_mfma_f32_16x16x32_bf16 v[204:207], v[212:215], v[124:127], v[204:207]\n\t" \
  "ds_read_b64_tr_b16 v[212:213], v249 offset:24608\n\t" \
  "ds_read_u16_d16_hi v225, v225 offset:10528\n\t" \
  "v_fma_mix_f32 v230, |v166|, v230, v230 op_sel:[0,1,0] op_sel_hi:[0,1,1] clamp\n\t" \
  "s_waitcnt lgkmcnt(8)\n\t" \
  "v_mfma_f32_16x16x32_bf16 v[180:183], v[216:219], v[128:131], v[180:183]\n\t" \
  "ds_read_b64_tr_b16 v[214:215], v249 offset:26144\n\t" \
  "v_or_b32 v172, v232, v233\n\t" \
  "v_or_b32 v173, v234, v235\n\t" \
  "v_mfma_f32_16x16x32_bf16 v[188:191], v[216:219], v[132:135], v[188:191]\n\t" \
  "ds_read_u16 v224, v224 offset:10528\n\t" \
  "v_fma_f32 v165, |v165|, v229, v165 mul:4\n\t" \
  "v_fma_f32 v164, |v164|, v228, v164 mul:4\n\t" \
  "v_mfma_f32_16x16x32_bf16 v[196:199], v[216:219], v[136:139], v[196:199]\n\t" \
  "ds_read_u16_d16_hi v227, v227 offset:10528\n\t" \
  "v_fma_f32 v167, |v167|, v231, v167 mul:4\n\t" \
  "v_fma_f32 v166, |v166|, v230, v166 mul:4\n\t" \
  "v_mfma_f32_16x16x32_bf16 v[204:207], v[216:219], v[140:143], v[204:207]\n\t" \
  "ds_read_b64_tr_b16 v[216:217], v249 offset:24664\n\t" \
  "v_cvt_pk_bf16_f32 v164, v164, v165\n\t" \
  "v_cvt_pk_bf16_f32 v165, v166, v167\n\t" \
  "v_mfma_f32_16x16x32_bf16 v[64:67], v[242:245], v[236:239], v[64:67]\n\t" \
  "ds_read_b64_tr_b16 v[218:219], v249 offset:26200\n\t" \
  "v_cvt_pknorm_i16_f32 v228, |v194|, |v195|\n\t" \
  "v_cvt_pknorm_i16_f32 v230, |v192|, |v193|\n\t" \
  "v_mfma_f32_16x16x32_bf16 v[68:71], v[240:243], v[236:239], v[68:71]\n\t" \
  "ds_read_u16 v226, v226 offset:10528\n\t" \
  "v_lshlrev_b32_sdwa v229, 2, v228 dst_sel:DWORD dst_unused:UNUSED_PAD src0_sel:DWORD src1_sel:BYTE_3\n\t" \
  "v_lshlrev_b32_sdwa v228, 2, v228 dst_sel:DWORD dst_unused:UNUSED_PAD src0_sel:DWORD src1_sel:BYTE_1\n\t" \
  "s_waitcnt lgkmcnt(8)\n\t" \
  "v_mfma_f32_16x16x32_bf16 v[0:3], v[208:211], v[146:149], v[0:3]\n\t" \
  "ds_read_b32 v229, v229 offset:10016\n\t" \
  "v_lshlrev_b32_sdwa v231, 2, v230 dst_sel:DWORD dst_unused:UNUSED_PAD src0_sel:DWORD src1_sel:BYTE_3\n\t" \
  "v_lshlrev_b32_sdwa v230, 2, v230 dst_sel:DWORD dst_unused:UNUSED_PAD src0_sel:DWORD src1_sel:BYTE_1\n\t" \
  "v_mfma_f32_16x16x32_bf16 v[24:27], v[208:211], v[154:157], v[24:27]\n\t" \
  "ds_read_b32 v228, v228 offset:10016\n\t" \
  "v_lshlrev_b32_sdwa v233, 1, v203 dst_sel:DWORD dst_unused:UNUSED_PAD src0_sel:DWORD src1_sel:WORD_1\n\t" \
  "v_lshlrev_b32_sdwa v232, 1, v202 dst_sel:DWORD dst_unused:UNUSED_PAD src0_sel:DWORD src1_sel:WORD_1\n\t" \
  "v_mfma_f32_16x16x32_bf16 v[48:51], v[208:211], v[162:165], v[48:51]\n\t" \
  "ds_read_b32 v231, v231 offset:10016\n\t" \
  "v_lshlrev_b32_sdwa v235, 1, v201 dst_sel:DWORD dst_unused:UNUSED_PAD src0_sel:DWORD src1_sel:WORD_1\n\t" \
  "v_mfma_f32_16x16x32_bf16 v[72:75], v[208:211], v[170:173], v[72:75]\n\t" \
  "ds_read_b64_tr_b16 v[208:209], v249 offset:24672\n\t" \
  "v_lshlrev_b32_sdwa v234, 1, v200 dst_sel:DWORD dst_unused:UNUSED_PAD src0_sel:DWORD src1_sel:WORD_1\n\t" \
  "v_fma_mix_f32 v221, |v179|, v221, v221 op_sel:[0,1,0] op_sel_hi:[0,1,1] clamp\n\t" \
  "s_waitcnt lgkmcnt(9)\n\t" \
  "v_mfma_f32_16x16x32_bf16 v[4:7], v[212:215], v[146:149], v[4:7]\n\t" \
  "ds_read_b64_tr_b16 v[210:211], v249 offset:26208\n\t" \
  "ds_read_b32 v230, v230 offset:10016\n\t" \
  "v_fma_mix_f32 v220, |v178|, v220, v220 op_sel:[0,1,0] op_sel_hi:[0,1,1] clamp\n\t" \
  "v_mfma_f32_16x16x32_bf16 v[28:31], v[212:215], v[154:157], v[28:31]\n\t" \
  "ds_read_u16_d16_hi v233, v233 offset:10528\n\t" \
  "v_fma_mix_f32 v223, |v177|, v223, v223 op_sel:[0,1,0] op_sel_hi:[0,1,1] clamp\n\t" \
  "v_fma_mix_f32 v222, |v176|, v222, v222 op_sel:[0,1,0] op_sel_hi:[0,1,1] clamp\n\t" \
  "s_waitcnt lgkmcnt(7)\n\t" \
  "v_mfma_f32_16x16x32_bf16 v[52:55], v[212:215], v[162:165], v[52:55]\n\t" \
  "ds_read_u16 v232, v232 offset:10528\n\t" \
  "v_or_b32 v187, v224, v225\n\t" \
  "v_or_b32 v186, v226, v227\n\t" \
  "v_mfma_f32_16x16x32_bf16 v[76:79], v[212:215], v[170:173], v[76:79]\n\t" \
  "ds_read_b64_tr_b16 v[212:213], v249 offset:24728\n\t" \
  "v_fma_f32 v179, |v179|, v221, v179 mul:4\n\t" \
  "v_fma_f32 v178, |v178|, v220, v178 mul:4\n\t" \
  "v_mfma_f32_16x16x32_bf16 v[8:11], v[216:219], v[146:149], v[8:11]\n\t" \
  "ds_read_b64_tr_b16 v[214:215], v249 offset:26264\n\t" \
  "v_fma_f32 v177, |v177|, v223, v177 mul:4\n\t" \
  "v_fma_f32 v176, |v176|, v222, v176 mul:4\n\t" \
  "v_mfma_f32_16x16x32_bf16 v[32:35], v[216:219], v[154:157], v[32:35]\n\t" \
  "ds_read_u16_d16_hi v235, v235 offset:10528\n\t" \
  "v_cvt_pk_bf16_f32 v179, v178, v179\n\t" \
  "v_cvt_pk_bf16_f32 v178, v176, v177\n\t" \
  "v_mfma_f32_16x16x32_bf16 v[56:59], v[216:219], v[162:165], v[56:59]\n\t" \
  "ds_read_u16 v234, v234 offset:10528\n\t" \
  "v_cvt_pknorm_i16_f32 v220, |v180|, |v181|\n\t" \
  "v_mfma_f32_16x16x32_bf16 v[80:83], v[216:219], v[170:173], v[80:83]\n\t" \
  "ds_read_b64_tr_b16 v[216:217], v249 offset:24736\n\t" \
  "v_cvt_pknorm_i16_f32 v222, |v182|, |v183|\n\t" \
  "v_lshlrev_b32_sdwa v221, 2, v220 dst_sel:DWORD dst_unused:UNUSED_PAD src0_sel:DWORD src1_sel:BYTE_3\n\t" \
  "s_waitcnt lgkmcnt(8)\n\t" \
  "v_mfma_f32_16x16x32_bf16 v[12:15], v[208:211], v[146:149], v[12:15]\n\t" \
  "ds_read_b64_tr_b16 v[218:219], v249 offset:26272\n\t" \
  "v_lshlrev_b32_sdwa v220, 2, v220 dst_sel:DWORD dst_unused:UNUSED_PAD src0_sel:DWORD src1_sel:BYTE_1\n\t" \
  "v_lshlrev_b32_sdwa v223, 2, v222 dst_sel:DWORD dst_unused:UNUSED_PAD src0_sel:DWORD src1_sel:BYTE_3\n\t" \
  "v_mfma_f32_16x16x32_bf16 v[36:39], v[208:211], v[154:157], v[36:39]\n\t" \
  "ds_read_b32 v221, v221 offset:10016\n\t" \
  "v_lshlrev_b32_sdwa v222, 2, v222 dst_sel:DWORD dst_unused:UNUSED_PAD src0_sel:DWORD src1_sel:BYTE_1\n\t" \
  "v_lshlrev_b32_sdwa v225, 1, v189 dst_sel:DWORD dst_unused:UNUSED_PAD src0_sel:DWORD src1_sel:WORD_1\n\t" \
  "v_mfma_f32_16x16x32_bf16 v[60:63], v[208:211], v[162:165], v[60:63]\n\t" \
  "ds_read_b32 v220, v220 offset:10016\n\t" \
  "v_lshlrev_b32_sdwa v224, 1, v188 dst_sel:DWORD dst_unused:UNUSED_PAD src0_sel:DWORD src1_sel:WORD_1\n\t" \
  "v_mfma_f32_16x16x32_bf16 v[84:87], v[208:211], v[170:173], v[84:87]\n\t" \
  "ds_read_b64_tr_b16 v[208:209], v248 offset:36904\n\t" \
  "v_lshlrev_b32_sdwa v227, 1, v191 dst_sel:DWORD dst_unused:UNUSED_PAD src0_sel:DWORD src1_sel:WORD_1\n\t" \
  "v_lshlrev_b32_sdwa v226, 1, v190 dst_sel:DWORD dst_unused:UNUSED_PAD src0_sel:DWORD src1_sel:WORD_1\n\t" \
  "s_waitcnt lgkmcnt(7)\n\t" \
  "v_mfma_f32_16x16x32_bf16 v[16:19], v[212:215], v[146:149], v[16:19]\n\t" \
  "ds_read_b64_tr_b16 v[210:211], v248 offset:37928\n\t" \
  "ds_read_b32 v223, v223 offset:10016\n\t" \
  "v_fma_mix_f32 v229, |v195|, v229, v229 op_sel:[0,1,0] op_sel_hi:[0,1,1] clamp\n\t" \
  "v_mfma_f32_16x16x32_bf16 v[40:43], v[212:215], v[154:157], v[40:43]\n\t" \
  "ds_read_b32 v222, v222 offset:10016\n\t" \
  "v_fma_mix_f32 v228, |v194|, v228, v228 op_sel:[0,1,0] op_sel_hi:[0,1,1] clamp\n\t" \
  "v_fma_mix_f32 v231, |v193|, v231, v231 op_sel:[0,1,0] op_sel_hi:[0,1,1] clamp\n\t" \
  "v_mfma_f32_16x16x32_bf16 v[64:67], v[212:215], v[162:165], v[64:67]\n\t" \
  "ds_read_u16_d16_hi v225, v225 offset:10528\n\t" \
  "v_fma_mix_f32 v230, |v192|, v230, v230 op_sel:[0,1,0] op_sel_hi:[0,1,1] clamp\n\t" \
  "s_waitcnt lgkmcnt(9)\n\t" \
  "v_or_b32 v203, v232, v233\n\t" \
  "v_mfma_f32_16x16x32_bf16 v[88:91], v[212:215], v[170:173], v[88:91]\n\t" \
  "ds_read_b64_tr_b16 v[212:213], v248 offset:39976\n\t" \
  "v_or_b32 v202, v234, v235\n\t" \
  "v_fma_f32 v195, |v195|, v229, v195 mul:4\n\t" \
  "s_waitcnt lgkmcnt(8)\n\t" \
  "v_mfma_f32_16x16x32_bf16 v[20:23], v[216:219], v[146:149], v[20:23]\n\t" \
  "ds_read_b64_tr_b16 v[214:215], v248 offset:41000\n\t" \
  "v_fma_f32 v194, |v194|, v228, v194 mul:4\n\t" \
  "v_fma_f32 v193, |v193|, v231, v193 mul:4\n\t" \
  "v_mfma_f32_16x16x32_bf16 v[44:47], v[216:219], v[154:157], v[44:47]\n\t" \
  "ds_read_u16 v224, v224 offset:10528\n\t" \
  "v_fma_f32 v192, |v192|, v230, v192 mul:4\n\t" \
  "v_cvt_pk_bf16_f32 v195, v194, v195\n\t" \
  "v_mfma_f32_16x16x32_bf16 v[68:71], v[216:219], v[162:165], v[68:71]\n\t" \
  "ds_read_u16_d16_hi v227, v227 offset:10528\n\t" \
  "v_cvt_pk_bf16_f32 v194, v192, v193\n\t" \
  "v_cvt_pknorm_i16_f32 v228, |v196|, |v197|\n\t" \
  "v_mfma_f32_16x16x32_bf16 v[92:95], v[216:219], v[170:173], v[92:95]\n\t" \
  "ds_read_b64_tr_b16 v[216:217], v248 offset:38952\n\t" \
  "v_cvt_pknorm_i16_f32 v230, |v198|, |v199|\n\t" \
  "s_waitcnt lgkmcnt(8)\n\t" \
  "v_mfma_f32_16x16x32_bf16 v[144:147], v[208:211], v[96:99], 0\n\t" \
  "ds_read_b64_tr_b16 v[218:219], v248 offset:42024\n\t" \
  "v_lshlrev_b32_sdwa v229, 2, v228 dst_sel:DWORD dst_unused:UNUSED_PAD src0_sel:DWORD src1_sel:BYTE_3\n\t" \
  "v_lshlrev_b32_sdwa v228, 2, v228 dst_sel:DWORD dst_unused:UNUSED_PAD src0_sel:DWORD src1_sel:BYTE_1\n\t" \
  "v_mfma_f32_16x16x32_bf16 v[152:155], v[208:211], v[100:103], 0\n\t" \
  "ds_read_u16 v226, v226 offset:10528\n\t" \
  "v_lshlrev_b32_sdwa v231, 2, v230 dst_sel:DWORD dst_unused:UNUSED_PAD src0_sel:DWORD src1_sel:BYTE_3\n\t" \
  "v_lshlrev_b32_sdwa v230, 2, v230 dst_sel:DWORD dst_unused:UNUSED_PAD src0_sel:DWORD src1_sel:BYTE_1\n\t" \
  "v_mfma_f32_16x16x32_bf16 v[160:163], v[208:211], v[104:107], 0\n\t" \
  "ds_read_b32 v229, v229 offset:10016\n\t" \
  "v_lshlrev_b32_sdwa v233, 1, v205 dst_sel:DWORD dst_unused:UNUSED_PAD src0_sel:DWORD src1_sel:WORD_1\n\t" \
  "v_lshlrev_b32_sdwa v232, 1, v204 dst_sel:DWORD dst_unused:UNUSED_PAD src0_sel:DWORD src1_sel:WORD_1\n\t" \
  "v_mfma_f32_16x16x32_bf16 v[168:171], v[208:211], v[108:111], 0\n\t" \
  "ds_read_b64_tr_b16 v[208:209], v248 offset:37416\n\t" \
  "ds_read_b32 v228, v228 offset:10016\n\t" \
  "v_lshlrev_b32_sdwa v235, 1, v207 dst_sel:DWORD dst_unused:UNUSED_PAD src0_sel:DWORD src1_sel:WORD_1\n\t" \
  "s_waitcnt lgkmcnt(8)\n\t" \
  "v_mfma_f32_16x16x32_bf16 v[144:147], v[212:215], v[112:115], v[144:147]\n\t" \
  "ds_read_b64_tr_b16 v[210:211], v248 offset:38440\n\t" \
  "v_lshlrev_b32_sdwa v234, 1, v206 dst_sel:DWORD dst_unused:UNUSED_PAD src0_sel:DWORD src1_sel:WORD_1\n\t" \
  "v_fma_mix_f32 v221, |v181|, v221, v221 op_sel:[0,1,0] op_sel_hi:[0,1,1] clamp\n\t" \
  "v_mfma_f32_16x16x32_bf16 v[152:155], v[212:215], v[116:119], v[152:155]\n\t" \
  "ds_read_b32 v231, v231 offset:10016\n\t" \
  "ds_read_b32 v230, v230 offset:10016\n\t" \
  "v_fma_mix_f32 v220, |v180|, v220, v220 op_sel:[0,1,0] op_sel_hi:[0,1,1] clamp\n\t" \
  "v_mfma_f32_16x16x32_bf16 v[160:163], v[212:215], v[120:123], v[160:163]\n\t" \
  "ds_read_u16_d16_hi v233, v233 offset:10528\n\t" \
  "v_fma_mix_f32 v223, |v183|, v223, v223 op_sel:[0,1,0] op_sel_hi:[0,1,1] clamp\n\t" \
  "v_fma_mix_f32 v222, |v182|, v222, v222 op_sel:[0,1,0] op_sel_hi:[0,1,1] clamp\n\t" \
  "s_waitcnt lgkmcnt(7)\n\t" \
  "v_mfma_f32_16x16x32_bf16 v[168:171], v[212:215], v[124:127], v[168:171]\n\t" \
  "ds_read_b64_tr_b16 v[212:213], v248 offset:40488\n\t" \
  "v_or_b32 v188, v224, v225\n\t" \
  "v_or_b32 v189, v226, v227\n\t" \
  "v_mfma_f32_16x16x32_bf16 v[144:147], v[216:219], v[128:131], v[144:147]\n\t" \
  "ds_read_b64_tr_b16 v[214:215], v248 offset:41512\n\t" \
  "v_fma_f32 v181, |v181|, v221, v181 mul:4\n\t" \
  "v_fma_f32 v180, |v180|, v220, v180 mul:4\n\t" \
  "v_mfma_f32_16x16x32_bf16 v[152:155], v[216:219], v[132:135], v[152:155]\n\t" \
  "ds_read_u16 v232, v232 offset:10528\n\t" \
  "v_fma_f32 v183, |v183|, v223, v183 mul:4\n\t" \
  "v_fma_f32 v182, |v182|, v222, v182 mul:4\n\t" \
  "v_mfma_f32_16x16x32_bf16 v[160:163], v[216:219], v[136:139], v[160:163]\n\t" \
  "ds_read_u16_d16_hi v235, v235 offset:10528\n\t" \
  "v_cvt_pk_bf16_f32 v180, v180, v181\n\t" \
  "v_cvt_pk_bf16_f32 v181, v182, v183\n\t" \
  "v_mfma_f32_16x16x32_bf16 v[168:171], v[216:219], v[140:143], v[168:171]\n\t" \
  "ds_read_b64_tr_b16 v[216:217], v248 offset:39464\n\t" \
  "v_cvt_pknorm_i16_f32 v220, |v146|, |v147|\n\t" \
  "s_waitcnt lgkmcnt(8)\n\t" \
  "v_mfma_f32_16x16x32_bf16 v[148:151], v[208:211], v[96:99], 0\n\t" \
  "ds_read_b64_tr_b16 v[218:219], v248 offset:42536\n\t" \
  "v_cvt_pknorm_i16_f32 v222, |v144|, |v145|\n\t" \
  "v_lshlrev_b32_sdwa v221, 2, v220 dst_sel:DWORD dst_unused:UNUSED_PAD src0_sel:DWORD src1_sel:BYTE_3\n\t" \
  "v_mfma_f32_16x16x32_bf16 v[156:159], v[208:211], v[100:103], 0\n\t" \
  "ds_read_u16 v234, v234 offset:10528\n\t" \
  "v_lshlrev_b32_sdwa v220, 2, v220 dst_sel:DWORD dst_unused:UNUSED_PAD src0_sel:DWORD src1_sel:BYTE_1\n\t" \
  "v_lshlrev_b32_sdwa v223, 2, v222 dst_sel:DWORD dst_unused:UNUSED_PAD src0_sel:DWORD src1_sel:BYTE_3\n\t" \
  "v_mfma_f32_16x16x32_bf16 v[164:167], v[208:211], v[104:107], 0\n\t" \
  "ds_read_b32 v221, v221 offset:10016\n\t" \
  "v_lshlrev_b32_sdwa v222, 2, v222 dst_sel:DWORD dst_unused:UNUSED_PAD src0_sel:DWORD src1_sel:BYTE_1\n\t" \
  "v_lshlrev_b32_sdwa v225, 1, v155 dst_sel:DWORD dst_unused:UNUSED_PAD src0_sel:DWORD src1_sel:WORD_1\n\t" \
  "v_mfma_f32_16x16x32_bf16 v[172:175], v[208:211], v[108:111], 0\n\t" \
  "ds_read_b32 v220, v220 offset:10016\n\t" \
  "v_lshlrev_b32_sdwa v224, 1, v154 dst_sel:DWORD dst_unused:UNUSED_PAD src0_sel:DWORD src1_sel:WORD_1\n\t" \
  "v_lshlrev_b32_sdwa v227, 1, v153 dst_sel:DWORD dst_unused:UNUSED_PAD src0_sel:DWORD src1_sel:WORD_1\n\t" \
  "s_waitcnt lgkmcnt(7)\n\t" \
  "v_mfma_f32_16x16x32_bf16 v[148:151], v[212:215], v[112:115], v[148:151]\n\t" \
  "ds_read_b32 v223, v223 offset:10016\n\t" \
  "v_lshlrev_b32_sdwa v226, 1, v152 dst_sel:DWORD dst_unused:UNUSED_PAD src0_sel:DWORD src1_sel:WORD_1\n\t" \
  "v_mfma_f32_16x16x32_bf16 v[156:159], v[212:215], v[116:119], v[156:159]\n\t" \
  "ds_read_b64_tr_b16 v[208:209], v249 offset:30752\n\t" \
  "ds_read_b32 v222, v222 offset:10016\n\t" \
  "v_fma_mix_f32 v229, |v197|, v229, v229 op_sel:[0,1,0] op_sel_hi:[0,1,1] clamp\n\t" \
  "v_mfma_f32_16x16x32_bf16 v[164:167], v[212:215], v[120:123], v[164:167]\n\t" \
  "ds_read_b64_tr_b16 v[210:211], v249 offset:32288\n\t" \
  "v_fma_mix_f32 v228, |v196|, v228, v228 op_sel:[0,1,0] op_sel_hi:[0,1,1] clamp\n\t" \
  "v_fma_mix_f32 v231, |v199|, v231, v231 op_sel:[0,1,0] op_sel_hi:[0,1,1] clamp\n\t" \
  "v_mfma_f32_16x16x32_bf16 v[172:175], v[212:215], v[124:127], v[172:175]\n\t" \
  "ds_read_b64_tr_b16 v[212:213], v249 offset:30760\n\t" \
  "ds_read_u16_d16_hi v225, v225 offset:10528\n\t" \
  "v_fma_mix_f32 v230, |v198|, v230, v230 op_sel:[0,1,0] op_sel_hi:[0,1,1] clamp\n\t" \
  "s_waitcnt lgkmcnt(8)\n\t" \
  "v_mfma_f32_16x16x32_bf16 v[148:151], v[216:219], v[128:131], v[148:151]\n\t" \
  "ds_read_b64_tr_b16 v[214:215], v249 offset:32296\n\t" \
  "v_or_b32 v204, v232, v233\n\t" \
  "v_or_b32 v205, v234, v235\n\t" \
  "v_mfma_f32_16x16x32_bf16 v[156:159], v[216:219], v[132:135], v[156:159]\n\t" \
  "ds_read_u16 v224, v224 offset:10528\n\t" \
  "v_fma_f32 v197, |v197|, v229, v197 mul:4\n\t" \
  "v_fma_f32 v196, |v196|, v228, v196 mul:4\n\t" \
  "v_mfma_f32_16x16x32_bf16 v[164:167], v[216:219], v[136:139], v[164:167]\n\t" \
  "ds_read_u16_d16_hi v227, v227 offset:10528\n\t" \
  "v_fma_f32 v199, |v199|, v231, v199 mul:4\n\t" \
  "v_fma_f32 v198, |v198|, v230, v198 mul:4\n\t" \
  "v_mfma_f32_16x16x32_bf16 v[172:175], v[216:219], v[140:143], v[172:175]\n\t" \
  "ds_read_b64_tr_b16 v[216:217], v249 offset:30816\n\t" \
  "v_cvt_pk_bf16_f32 v196, v196, v197\n\t" \
  "v_cvt_pk_bf16_f32 v197, v198, v199\n\t" \
  "s_waitcnt lgkmcnt(6)\n\t" \
  "v_mfma_f32_16x16x32_bf16 v[0:3], v[208:211], v[178:181], v[0:3]\n\t" \
  "ds_read_b64_tr_b16 v[218:219], v249 offset:32352\n\t" \
  "v_cvt_pknorm_i16_f32 v228, |v162|, |v163|\n\t" \
  "v_cvt_pknorm_i16_f32 v230, |v160|, |v161|\n\t" \
  "v_mfma_f32_16x16x32_bf16 v[24:27], v[208:211], v[186:189], v[24:27]\n\t" \
  "ds_read_u16 v226, v226 offset:10528\n\t" \
  "v_lshlrev_b32_sdwa v229, 2, v228 dst_sel:DWORD dst_unused:UNUSED_PAD src0_sel:DWORD src1_sel:BYTE_3\n\t" \
  "v_lshlrev_b32_sdwa v228, 2, v228 dst_sel:DWORD dst_unused:UNUSED_PAD src0_sel:DWORD src1_sel:BYTE_1\n\t" \
  "v_mfma_f32_16x16x32_bf16 v[48:51], v[208:211], v[194:197], v[48:51]\n\t" \
  "ds_read_b32 v229, v229 offset:10016\n\t" \
  "v_lshlrev_b32_sdwa v231, 2, v230 dst_sel:DWORD dst_unused:UNUSED_PAD src0_sel:DWORD src1_sel:BYTE_3\n\t" \
  "v_lshlrev_b32_sdwa v230, 2, v230 dst_sel:DWORD dst_unused:UNUSED_PAD src0_sel:DWORD src1_sel:BYTE_1\n\t" \
  "v_mfma_f32_16x16x32_bf16 v[72:75], v[208:211], v[202:205], v[72:75]\n\t" \
  "ds_read_b64_tr_b16 v[208:209], v249 offset:30824\n\t" \
  "v_lshlrev_b32_sdwa v233, 1, v171 dst_sel:DWORD dst_unused:UNUSED_PAD src0_sel:DWORD src1_sel:WORD_1\n\t" \
  "v_lshlrev_b32_sdwa v232, 1, v170 dst_sel:DWORD dst_unused:UNUSED_PAD src0_sel:DWORD src1_sel:WORD_1\n\t" \
  "s_waitcnt lgkmcnt(7)\n\t" \
  "v_mfma_f32_16x16x32_bf16 v[4:7], v[212:215], v[178:181], v[4:7]\n\t" \
  "ds_read_b64_tr_b16 v[210:211], v249 offset:32360\n\t" \
  "v_lshlrev_b32_sdwa v235, 1, v169 dst_sel:DWORD dst_unused:UNUSED_PAD src0_sel:DWORD src1_sel:WORD_1\n\t" \
  "v_lshlrev_b32_sdwa v234, 1, v168 dst_sel:DWORD dst_unused:UNUSED_PAD src0_sel:DWORD src1_sel:WORD_1\n\t" \
  "v_mfma_f32_16x16x32_bf16 v[28:31], v[212:215], v[186:189], v[28:31]\n\t" \
  "ds_read_b32 v228, v228 offset:10016\n\t" \
  "ds_read_b32 v231, v231 offset:10016\n\t" \
  "v_fma_mix_f32 v221, |v147|, v221, v221 op_sel:[0,1,0] op_sel_hi:[0,1,1] clamp\n\t" \
  "v_mfma_f32_16x16x32_bf16 v[52:55], v[212:215], v[194:197], v[52:55]\n\t" \
  "ds_read_b32 v230, v230 offset:10016\n\t" \
  "v_fma_mix_f32 v220, |v146|, v220, v220 op_sel:[0,1,0] op_sel_hi:[0,1,1] clamp\n\t" \
  "v_fma_mix_f32 v223, |v145|, v223, v223 op_sel:[0,1,0] op_sel_hi:[0,1,1] clamp\n\t" \
  "v_mfma_f32_16x16x32_bf16 v[76:79], v[212:215], v[202:205], v[76:79]\n\t" \
  "ds_read_b64_tr_b16 v[212:213], v249 offset:30880\n\t" \
  "ds_read_u16_d16_hi v233, v233 offset:10528\n\t" \
  "v_fma_mix_f32 v222, |v144|, v222, v222 op_sel:[0,1,0] op_sel_hi:[0,1,1] clamp\n\t" \
  "s_waitcnt lgkmcnt(8)\n\t" \
  "v_mfma_f32_16x16x32_bf16 v[8:11], v[216:219], v[178:181], v[8:11]\n\t" \
  "ds_read_b64_tr_b16 v[214:215], v249 offset:32416\n\t" \
  "v_or_b32 v155, v224, v225\n\t" \
  "v_or_b32 v154, v226, v227\n\t" \
  "v_mfma_f32_16x16x32_bf16 v[32:35], v[216:219], v[186:189], v[32:35]\n\t" \
  "ds_read_u16 v232, v232 offset:10528\n\t" \
  "v_fma_f32 v147, |v147|, v221, v147 mul:4\n\t" \
  "v_fma_f32 v146, |v146|, v220, v146 mul:4\n\t" \
  "v_mfma_f32_16x16x32_bf16 v[56:59], v[216:219], v[194:197], v[56:59]\n\t" \
  "ds_read_u16_d16_hi v235, v235 offset:10528\n\t" \
  "v_fma_f32 v145, |v145|, v223, v145 mul:4\n\t" \
  "v_fma_f32 v144, |v144|, v222, v144 mul:4\n\t" \
  "v_mfma_f32_16x16x32_bf16 v[80:83], v[216:219], v[202:205], v[80:83]\n\t" \
  "ds_read_b64_tr_b16 v[216:217], v249 offset:30888\n\t" \
  "v_cvt_pk_bf16_f32 v147, v146, v147\n\t" \
  "v_cvt_pk_bf16_f32 v146, v144, v145\n\t" \
  "s_waitcnt lgkmcnt(9)\n\t" \
  "v_mfma_f32_16x16x32_bf16 v[12:15], v[208:211], v[178:181], v[12:15]\n\t" \
  "ds_read_b64_tr_b16 v[218:219], v249 offset:32424\n\t" \
  "v_cvt_pknorm_i16_f32 v220, |v148|, |v149|\n\t" \
  "v_cvt_pknorm_i16_f32 v222, |v150|, |v151|\n\t" \
  "v_mfma_f32_16x16x32_bf16 v[36:39], v[208:211], v[186:189], v[36:39]\n\t" \
  "ds_read_u16 v234, v234 offset:10528\n\t" \
  "v_lshlrev_b32_sdwa v221, 2, v220 dst_sel:DWORD dst_unused:UNUSED_PAD src0_sel:DWORD src1_sel:BYTE_3\n\t" \
  "v_lshlrev_b32_sdwa v220, 2, v220 dst_sel:DWORD dst_unused:UNUSED_PAD src0_sel:DWORD src1_sel:BYTE_1\n\t" \
  "v_mfma_f32_16x16x32_bf16 v[60:63], v[208:211], v[194:197], v[60:63]\n\t" \
  "ds_read_b32 v221, v221 offset:10016\n\t" \
  "v_lshlrev_b32_sdwa v223, 2, v222 dst_sel:DWORD dst_unused:UNUSED_PAD src0_sel:DWORD src1_sel:BYTE_3\n\t" \
  "v_lshlrev_b32_sdwa v222, 2, v222 dst_sel:DWORD dst_unused:UNUSED_PAD src0_sel:DWORD src1_sel:BYTE_1\n\t" \
  "v_mfma_f32_16x16x32_bf16 v[84:87], v[208:211], v[202:205], v[84:87]\n\t" \
  "ds_read_b64_tr_b16 v[208:209], v248 offset:0\n\t" \
  "v_lshlrev_b32_sdwa v225, 1, v157 dst_sel:DWORD dst_unused:UNUSED_PAD src0_sel:DWORD src1_sel:WORD_1\n\t" \
  "v_lshlrev_b32_sdwa v224, 1, v156 dst_sel:DWORD dst_unused:UNUSED_PAD src0_sel:DWORD src1_sel:WORD_1\n\t" \
  "s_waitcnt lgkmcnt(7)\n\t" \
  "v_mfma_f32_16x16x32_bf16 v[16:19], v[212:215], v[178:181], v[16:19]\n\t" \
  "ds_read_b64_tr_b16 v[210:211], v248 offset:1024\n\t" \
  "v_lshlrev_b32_sdwa v227, 1, v159 dst_sel:DWORD dst_unused:UNUSED_PAD src0_sel:DWORD src1_sel:WORD_1\n\t" \
  "v_lshlrev_b32_sdwa v226, 1, v158 dst_sel:DWORD dst_unused:UNUSED_PAD src0_sel:DWORD src1_sel:WORD_1\n\t" \
  "v_mfma_f32_16x16x32_bf16 v[40:43], v[212:215], v[186:189], v[40:43]\n\t" \
  "ds_read_b32 v220, v220 offset:10016\n\t" \
  "ds_read_b32 v223, v223 offset:10016\n\t" \
  "v_fma_mix_f32 v229, |v163|, v229, v229 op_sel:[0,1,0] op_sel_hi:[0,1,1] clamp\n\t" \
  "v_mfma_f32_16x16x32_bf16 v[64:67], v[212:215], v[194:197], v[64:67]\n\t" \
  "ds_read_b32 v222, v222 offset:10016\n\t" \
  "v_fma_mix_f32 v228, |v162|, v228, v228 op_sel:[0,1,0] op_sel_hi:[0,1,1] clamp\n\t" \
  "v_fma_mix_f32 v231, |v161|, v231, v231 op_sel:[0,1,0] op_sel_hi:[0,1,1] clamp\n\t" \
  "v_mfma_f32_16x16x32_bf16 v[88:91], v[212:215], v[202:205], v[88:91]\n\t" \
  "ds_read_b64_tr_b16 v[212:213], v248 offset:3072\n\t" \
  "ds_read_u16_d16_hi v225, v225 offset:10528\n\t" \
  "v_fma_mix_f32 v230, |v160|, v230, v230 op_sel:[0,1,0] op_sel_hi:[0,1,1] clamp\n\t" \
  "s_waitcnt lgkmcnt(8)\n\t" \
  "v_mfma_f32_16x16x32_bf16 v[20:23], v[216:219], v[178:181], v[20:23]\n\t" \
  "ds_read_b64_tr_b16 v[214:215], v248 offset:4096\n\t" \
  "v_or_b32 v171, v232, v233\n\t" \
  "v_or_b32 v170, v234, v235\n\t" \
  "v_mfma_f32_16x16x32_bf16 v[44:47], v[216:219], v[186:189], v[44:47]\n\t" \
  "ds_read_u16 v224, v224 offset:10528\n\t" \
  "v_fma_f32 v163, |v163|, v229, v163 mul:4\n\t" \
  "v_fma_f32 v162, |v162|, v228, v162 mul:4\n\t" \
  "v_mfma_f32_16x16x32_bf16 v[68:71], v[216:219], v[194:197], v[68:71]\n\t" \
  "ds_read_u16_d16_hi v227, v227 offset:10528\n\t" \
  "v_fma_f32 v161, |v161|, v231, v161 mul:4\n\t" \
  "v_fma_f32 v160, |v160|, v230, v160 mul:4\n\t" \
  "v_mfma_f32_16x16x32_bf16 v[92:95], v[216:219], v[202:205], v[92:95]\n\t" \
  "ds_read_b64_tr_b16 v[216:217], v248 offset:2048\n\t" \
  "v_cvt_pk_bf16_f32 v163, v162, v163\n\t" \
  "v_cvt_pk_bf16_f32 v162, v160, v161\n\t" \
  "s_waitcnt lgkmcnt(9)\n\t" \
  "v_mfma_f32_16x16x32_bf16 v[176:179], v[208:211], v[96:99], 0\n\t" \
  "ds_read_b64_tr_b16 v[218:219], v248 offset:5120\n\t" \
  "v_cvt_pknorm_i16_f32 v228, |v164|, |v165|\n\t" \
  "v_cvt_pknorm_i16_f32 v230, |v166|, |v167|\n\t" \
  "v_mfma_f32_16x16x32_bf16 v[184:187], v[208:211], v[100:103], 0\n\t" \
  "ds_read_u16 v226, v226 offset:10528\n\t" \
  "v_lshlrev_b32_sdwa v229, 2, v228 dst_sel:DWORD dst_unused:UNUSED_PAD src0_sel:DWORD src1_sel:BYTE_3\n\t" \
  "v_lshlrev_b32_sdwa v228, 2, v228 dst_sel:DWORD dst_unused:UNUSED_PAD src0_sel:DWORD src1_sel:BYTE_1\n\t" \
  "v_mfma_f32_16x16x32_bf16 v[192:195], v[208:211], v[104:107], 0\n\t" \
  "ds_read_b32 v229, v229 offset:10016\n\t" \
  "v_lshlrev_b32_sdwa v231, 2, v230 dst_sel:DWORD dst_unused:UNUSED_PAD src0_sel:DWORD src1_sel:BYTE_3\n\t" \
  "v_lshlrev_b32_sdwa v230, 2, v230 dst_sel:DWORD dst_unused:UNUSED_PAD src0_sel:DWORD src1_sel:BYTE_1\n\t" \
  "v_mfma_f32_16x16x32_bf16 v[200:203], v[208:211], v[108:111], 0\n\t" \
  "ds_read_b64_tr_b16 v[208:209], v248 offset:512\n\t" \
  "v_lshlrev_b32_sdwa v233, 1, v173 dst_sel:DWORD dst_unused:UNUSED_PAD src0_sel:DWORD src1_sel:WORD_1\n\t" \
  "v_lshlrev_b32_sdwa v232, 1, v172 dst_sel:DWORD dst_unused:UNUSED_PAD src0_sel:DWORD src1_sel:WORD_1\n\t" \
  "s_waitcnt lgkmcnt(7)\n\t" \
  "v_mfma_f32_16x16x32_bf16 v[176:179], v[212:215], v[112:115], v[176:179]\n\t" \
  "ds_read_b64_tr_b16 v[210:211], v248 offset:1536\n\t" \
  "v_lshlrev_b32_sdwa v235, 1, v175 dst_sel:DWORD dst_unused:UNUSED_PAD src0_sel:DWORD src1_sel:WORD_1\n\t" \
  "v_lshlrev_b32_sdwa v234, 1, v174 dst_sel:DWORD dst_unused:UNUSED_PAD src0_sel:DWORD src1_sel:WORD_1\n\t" \
  "v_mfma_f32_16x16x32_bf16 v[184:187], v[212:215], v[116:119], v[184:187]\n\t" \
  "ds_read_b32 v228, v228 offset:10016\n\t" \
  "ds_read_b32 v231, v231 offset:10016\n\t" \
  "v_fma_mix_f32 v221, |v149|, v221, v221 op_sel:[0,1,0] op_sel_hi:[0,1,1] clamp\n\t" \
  "v_mfma_f32_16x16x32_bf16 v[192:195], v[212:215], v[120:123], v[192:195]\n\t" \
  "ds_read_b32 v230, v230 offset:10016\n\t" \
  "v_fma_mix_f32 v220, |v148|, v220, v220 op_sel:[0,1,0] op_sel_hi:[0,1,1] clamp\n\t" \
  "v_fma_mix_f32 v223, |v151|, v223, v223 op_sel:[0,1,0] op_sel_hi:[0,1,1] clamp\n\t" \
  "v_mfma_f32_16x16x32_bf16 v[200:203], v[212:215], v[124:127], v[200:203]\n\t" \
  "ds_read_b64_tr_b16 v[212:213], v248 offset:3584\n\t" \
  "ds_read_u16_d16_hi v233, v233 offset:10528\n\t" \
  "v_fma_mix_f32 v222, |v150|, v222, v222 op_sel:[0,1,0] op_sel_hi:[0,1,1] clamp\n\t" \
  "s_waitcnt lgkmcnt(8)\n\t" \
  "v_mfma_f32_16x16x32_bf16 v[176:179], v[216:219], v[128:131], v[176:179]\n\t" \
  "ds_read_b64_tr_b16 v[214:215], v248 offset:4608\n\t" \
  "v_or_b32 v156, v224, v225\n\t" \
  "v_or_b32 v157, v226, v227\n\t" \
  "v_mfma_f32_16x16x32_bf16 v[184:187], v[216:219], v[132:135], v[184:187]\n\t" \
  "ds_read_u16 v232, v232 offset:10528\n\t" \
  "v_fma_f32 v149, |v149|, v221, v149 mul:4\n\t" \
  "v_fma_f32 v148, |v148|, v220, v148 mul:4\n\t" \
  "v_mfma_f32_16x16x32_bf16 v[192:195], v[216:219], v[136:139], v[192:195]\n\t" \
  "ds_read_u16_d16_hi v235, v235 offset:10528\n\t" \
  "v_fma_f32 v151, |v151|, v223, v151 mul:4\n\t" \
  "v_fma_f32 v150, |v150|, v222, v150 mul:4\n\t" \
  "v_mfma_f32_16x16x32_bf16 v[200:203], v[216:219], v[140:143], v[200:203]\n\t" \
  "ds_read_b64_tr_b16 v[216:217], v248 offset:2560\n\t" \
  "v_cvt_pk_bf16_f32 v148, v148, v149\n\t" \
  "v_cvt_pk_bf16_f32 v149, v150, v151\n\t" \
  "s_waitcnt lgkmcnt(9)\n\t" \
  "v_mfma_f32_16x16x32_bf16 v[180:183], v[208:211], v[96:99], 0\n\t" \
  "ds_read_b64_tr_b16 v[218:219], v248 offset:5632\n\t" \
  "v_cvt_pknorm_i16_f32 v220, |v178|, |v179|\n\t" \
  "v_cvt_pknorm_i16_f32 v222, |v176|, |v177|\n\t" \
  "v_mfma_f32_16x16x32_bf16 v[188:191], v[208:211], v[100:103], 0\n\t" \
  "ds_read_u16 v234, v234 offset:10528\n\t" \
  "v_lshlrev_b32_sdwa v221, 2, v220 dst_sel:DWORD dst_unused:UNUSED_PAD src0_sel:DWORD src1_sel:BYTE_3\n\t" \
  "v_lshlrev_b32_sdwa v220, 2, v220 dst_sel:DWORD dst_unused:UNUSED_PAD src0_sel:DWORD src1_sel:BYTE_1\n\t" \
  "v_mfma_f32_16x16x32_bf16 v[196:199], v[208:211], v[104:107], 0\n\t" \
  "ds_read_b32 v221, v221 offset:10016\n\t" \
  "v_lshlrev_b32_sdwa v223, 2, v222 dst_sel:DWORD dst_unused:UNUSED_PAD src0_sel:DWORD src1_sel:BYTE_3\n\t" \
  "v_lshlrev_b32_sdwa v222, 2, v222 dst_sel:DWORD dst_unused:UNUSED_PAD src0_sel:DWORD src1_sel:BYTE_1\n\t" \
  "v_mfma_f32_16x16x32_bf16 v[204:207], v[208:211], v[108:111], 0\n\t" \
  "ds_read_b32 v220, v220 offset:10016\n\t" \
  "v_lshlrev_b32_sdwa v225, 1, v187 dst_sel:DWORD dst_unused:UNUSED_PAD src0_sel:DWORD src1_sel:WORD_1\n\t" \
  "v_lshlrev_b32_sdwa v224, 1, v186 dst_sel:DWORD dst_unused:UNUSED_PAD src0_sel:DWORD src1_sel:WORD_1\n\t" \
  "s_waitcnt lgkmcnt(7)\n\t" \
  "v_mfma_f32_16x16x32_bf16 v[180:183], v[212:215], v[112:115], v[180:183]\n\t" \
  "ds_read_b32 v223, v223 offset:10016\n\t" \
  "v_lshlrev_b32_sdwa v227, 1, v185 dst_sel:DWORD dst_unused:UNUSED_PAD src0_sel:DWORD src1_sel:WORD_1\n\t" \
  "v_lshlrev_b32_sdwa v226, 1, v184 dst_sel:DWORD dst_unused:UNUSED_PAD src0_sel:DWORD src1_sel:WORD_1\n\t" \
  "v_mfma_f32_16x16x32_bf16 v[188:191], v[212:215], v[116:119], v[188:191]\n\t" \
  "ds_read_b64_tr_b16 v[208:209], v249 offset:6152\n\t" \
  "ds_read_b32 v222, v222 offset:10016\n\t" \
  "v_fma_mix_f32 v229, |v165|, v229, v229 op_sel:[0,1,0] op_sel_hi:[0,1,1] clamp\n\t" \
  "v_mfma_f32_16x16x32_bf16 v[196:199], v[212:215], v[120:123], v[196:199]\n\t" \
  "ds_read_b64_tr_b16 v[210:211], v249 offset:7688\n\t" \
  "v_fma_mix_f32 v228, |v164|, v228, v228 op_sel:[0,1,0] op_sel_hi:[0,1,1] clamp\n\t" \
  "v_fma_mix_f32 v231, |v167|, v231, v231 op_sel:[0,1,0] op_sel_hi:[0,1,1] clamp\n\t" \
  "v_mfma_f32_16x16x32_bf16 v[204:207], v[212:215], v[124:127], v[204:207]\n\t" \
  "ds_read_b64_tr_b16 v[212:213], v249 offset:6160\n\t" \
  "ds_read_u16_d16_hi v225, v225 offset:10528\n\t" \
  "v_fma_mix_f32 v230, |v166|, v230, v230 op_sel:[0,1,0] op_sel_hi:[0,1,1] clamp\n\t" \
  "s_waitcnt lgkmcnt(8)\n\t" \
  "v_mfma_f32_16x16x32_bf16 v[180:183], v[216:219], v[128:131], v[180:183]\n\t" \
  "ds_read_b64_tr_b16 v[214:215], v249 offset:7696\n\t" \
  "v_or_b32 v172, v232, v233\n\t" \
  "v_or_b32 v173, v234, v235\n\t" \
  "v_mfma_f32_16x16x32_bf16 v[188:191], v[216:219], v[132:135], v[188:191]\n\t" \
  "ds_read_u16 v224, v224 offset:10528\n\t" \
  "v_fma_f32 v165, |v165|, v229, v165 mul:4\n\t" \
  "v_fma_f32 v164, |v164|, v228, v164 mul:4\n\t" \
  "v_mfma_f32_16x16x32_bf16 v[196:199], v[216:219], v[136:139], v[196:199]\n\t" \
  "ds_read_u16_d16_hi v227, v227 offset:10528\n\t" \
  "v_fma_f32 v167, |v167|, v231, v167 mul:4\n\t" \
  "v_fma_f32 v166, |v166|, v230, v166 mul:4\n\t" \
  "v_mfma_f32_16x16x32_bf16 v[204:207], v[216:219], v[140:143], v[204:207]\n\t" \
  "ds_read_b64_tr_b16 v[216:217], v249 offset:6216\n\t" \
  "v_cvt_pk_bf16_f32 v164, v164, v165\n\t" \
  "v_cvt_pk_bf16_f32 v165, v166, v167\n\t" \
  "s_waitcnt lgkmcnt(6)\n\t" \
  "v_mfma_f32_16x16x32_bf16 v[0:3], v[208:211], v[146:149], v[0:3]\n\t" \
  "ds_read_b64_tr_b16 v[218:219], v249 offset:7752\n\t" \
  "v_cvt_pknorm_i16_f32 v228, |v194|, |v195|\n\t" \
  "v_cvt_pknorm_i16_f32 v230, |v192|, |v193|\n\t" \
  "v_lshlrev_b32_sdwa v229, 2, v228 dst_sel:DWORD dst_unused:UNUSED_PAD src0_sel:DWORD src1_sel:BYTE_3\n\t" \
  "v_mfma_f32_16x16x32_bf16 v[24:27], v[208:211], v[154:157], v[24:27]\n\t" \
  "ds_read_u16 v226, v226 offset:10528\n\t" \
  "v_lshlrev_b32_sdwa v228, 2, v228 dst_sel:DWORD dst_unused:UNUSED_PAD src0_sel:DWORD src1_sel:BYTE_1\n\t" \
  "v_lshlrev_b32_sdwa v231, 2, v230 dst_sel:DWORD dst_unused:UNUSED_PAD src0_sel:DWORD src1_sel:BYTE_3\n\t" \
  "v_lshlrev_b32_sdwa v230, 2, v230 dst_sel:DWORD dst_unused:UNUSED_PAD src0_sel:DWORD src1_sel:BYTE_1\n\t" \
  "v_mfma_f32_16x16x32_bf16 v[48:51], v[208:211], v[162:165], v[48:51]\n\t" \
  "ds_read_b32 v229, v229 offset:10016\n\t" \
  "v_lshlrev_b32_sdwa v233, 1, v203 dst_sel:DWORD dst_unused:UNUSED_PAD src0_sel:DWORD src1_sel:WORD_1\n\t" \
  "v_lshlrev_b32_sdwa v232, 1, v202 dst_sel:DWORD dst_unused:UNUSED_PAD src0_sel:DWORD src1_sel:WORD_1\n\t" \
  "v_lshlrev_b32_sdwa v235, 1, v201 dst_sel:DWORD dst_unused:UNUSED_PAD src0_sel:DWORD src1_sel:WORD_1\n\t" \
  "v_mfma_f32_16x16x32_bf16 v[72:75], v[208:211], v[170:173], v[72:75]\n\t" \
  "ds_read_b64_tr_b16 v[208:209], v249 offset:6224\n\t" \
  "ds_read_b32 v228, v228 offset:10016\n\t" \
  "v_lshlrev_b32_sdwa v234, 1, v200 dst_sel:DWORD dst_unused:UNUSED_PAD src0_sel:DWORD src1_sel:WORD_1\n\t" \
  "v_fma_mix_f32 v221, |v179|, v221, v221 op_sel:[0,1,0] op_sel_hi:[0,1,1] clamp\n\t" \
  "v_fma_mix_f32 v220, |v178|, v220, v220 op_sel:[0,1,0] op_sel_hi:[0,1,1] clamp\n\t" \
  "s_waitcnt lgkmcnt(8)\n\t" \
  "v_mfma_f32_16x16x32_bf16 v[4:7], v[212:215], v[146:149], v[4:7]\n\t" \
  "ds_read_b64_tr_b16 v[210:211], v249 offset:7760\n\t" \
  "ds_read_b32 v231, v231 offset:10016\n\t" \
  "v_fma_mix_f32 v223, |v177|, v223, v223 op_sel:[0,1,0] op_sel_hi:[0,1,1] clamp\n\t" \
  "v_fma_mix_f32 v222, |v176|, v222, v222 op_sel:[0,1,0] op_sel_hi:[0,1,1] clamp\n\t" \
  "s_waitcnt lgkmcnt(5)\n\t" \
  "v_or_b32 v187, v224, v225\n\t" \
  "v_mfma_f32_16x16x32_bf16 v[28:31], v[212:215], v[154:157], v[28:31]\n\t" \
  "ds_read_b32 v230, v230 offset:10016\n\t" \
  "v_or_b32 v186, v226, v227\n\t" \
  "v_fma_f32 v179, |v179|, v221, v179 mul:4\n\t" \
  "v_fma_f32 v178, |v178|, v220, v178 mul:4\n\t" \
  "v_mfma_f32_16x16x32_bf16 v[52:55], v[212:215], v[162:165], v[52:55]\n\t" \
  "ds_read_u16_d16_hi v233, v233 offset:10528\n\t" \
  "v_fma_f32 v177, |v177|, v223, v177 mul:4\n\t" \
  "v_fma_f32 v176, |v176|, v222, v176 mul:4\n\t" \
  "v_cvt_pk_bf16_f32 v179, v178, v179\n\t" \
  "v_mfma_f32_16x16x32_bf16 v[76:79], v[212:215], v[170:173], v[76:79]\n\t" \
  "ds_read_b64_tr_b16 v[212:213], v249 offset:6280\n\t" \
  "v_cvt_pk_bf16_f32 v178, v176, v177\n\t" \
  "v_cvt_pknorm_i16_f32 v220, |v180|, |v181|\n\t" \
  "v_cvt_pknorm_i16_f32 v222, |v182|, |v183|\n\t" \
  "v_mfma_f32_16x16x32_bf16 v[8:11], v[216:219], v[146:149], v[8:11]\n\t" \
  "ds_read_b64_tr_b16 v[214:215], v249 offset:7816\n\t" \
  "v_lshlrev_b32_sdwa v221, 2, v220 dst_sel:DWORD dst_unused:UNUSED_PAD src0_sel:DWORD src1_sel:BYTE_3\n\t" \
  "v_lshlrev_b32_sdwa v220, 2, v220 dst_sel:DWORD dst_unused:UNUSED_PAD src0_sel:DWORD src1_sel:BYTE_1\n\t" \
  "v_lshlrev_b32_sdwa v223, 2, v222 dst_sel:DWORD dst_unused:UNUSED_PAD src0_sel:DWORD src1_sel:BYTE_3\n\t" \
  "v_mfma_f32_16x16x32_bf16 v[32:35], v[216:219], v[154:157], v[32:35]\n\t" \
  "ds_read_u16 v232, v232 offset:10528\n\t" \
  "v_lshlrev_b32_sdwa v222, 2, v222 dst_sel:DWORD dst_unused:UNUSED_PAD src0_sel:DWORD src1_sel:BYTE_1\n\t" \
  "v_lshlrev_b32_sdwa v225, 1, v189 dst_sel:DWORD dst_unused:UNUSED_PAD src0_sel:DWORD src1_sel:WORD_1\n\t" \
  "v_lshlrev_b32_sdwa v224, 1, v188 dst_sel:DWORD dst_unused:UNUSED_PAD src0_sel:DWORD src1_sel:WORD_1\n\t" \
  "v_mfma_f32_16x16x32_bf16 v[56:59], v[216:219], v[162:165], v[56:59]\n\t" \
  "ds_read_u16_d16_hi v235, v235 offset:10528\n\t" \
  "ds_read_u16 v234, v234 offset:10528\n\t" \
  "v_lshlrev_b32_sdwa v227, 1, v191 dst_sel:DWORD dst_unused:UNUSED_PAD src0_sel:DWORD src1_sel:WORD_1\n\t" \
  "v_lshlrev_b32_sdwa v226, 1, v190 dst_sel:DWORD dst_unused:UNUSED_PAD src0_sel:DWORD src1_sel:WORD_1\n\t" \
  "s_waitcnt lgkmcnt(6)\n\t" \
  "v_fma_mix_f32 v229, |v195|, v229, v229 op_sel:[0,1,0] op_sel_hi:[0,1,1] clamp\n\t" \
  "v_mfma_f32_16x16x32_bf16 v[80:83], v[216:219], v[170:173], v[80:83]\n\t" \
  "ds_read_b64_tr_b16 v[216:217], v249 offset:6288\n\t" \
  "ds_read_b32 v221, v221 offset:10016\n\t" \
  "v_fma_mix_f32 v228, |v194|, v228, v228 op_sel:[0,1,0] op_sel_hi:[0,1,1] clamp\n\t" \
  "v_fma_mix_f32 v231, |v193|, v231, v231 op_sel:[0,1,0] op_sel_hi:[0,1,1] clamp\n\t" \
  "v_fma_mix_f32 v230, |v192|, v230, v230 op_sel:[0,1,0] op_sel_hi:[0,1,1] clamp\n\t" \
  "s_waitcnt lgkmcnt(2)\n\t" \
  "v_mfma_f32_16x16x32_bf16 v[12:15], v[208:211], v[146:149], v[12:15]\n\t" \
  "ds_read_b64_tr_b16 v[218:219], v249 offset:7824\n\t" \
  "ds_read_b32 v220, v220 offset:10016\n\t" \
  "v_or_b32 v203, v232, v233\n\t" \
  "v_or_b32 v202, v234, v235\n\t" \
  "v_fma_f32 v195, |v195|, v229, v195 mul:4\n\t" \
  "v_fma_f32 v194, |v194|, v228, v194 mul:4\n\t" \
  "v_mfma_f32_16x16x32_bf16 v[36:39], v[208:211], v[154:157], v[36:39]\n\t" \
  "ds_read_b32 v223, v223 offset:10016\n\t" \
  "ds_read_b32 v222, v222 offset:10016\n\t" \
  "v_fma_f32 v193, |v193|, v231, v193 mul:4\n\t" \
  "v_fma_f32 v192, |v192|, v230, v192 mul:4\n\t" \
  "v_cvt_pk_bf16_f32 v195, v194, v195\n\t" \
  "v_mfma_f32_16x16x32_bf16 v[60:63], v[208:211], v[162:165], v[60:63]\n\t" \
  "ds_read_u16_d16_hi v225, v225 offset:10528\n\t" \
  "v_cvt_pk_bf16_f32 v194, v192, v193\n\t" \
  "v_cvt_pknorm_i16_f32 v228, |v196|, |v197|\n\t" \
  "v_cvt_pknorm_i16_f32 v230, |v198|, |v199|\n\t" \
  "v_lshlrev_b32_sdwa v229, 2, v228 dst_sel:DWORD dst_unused:UNUSED_PAD src0_sel:DWORD src1_sel:BYTE_3\n\t" \
  "v_mfma_f32_16x16x32_bf16 v[84:87], v[208:211], v[170:173], v[84:87]\n\t" \
  "ds_read_u16 v224, v224 offset:10528\n\t" \
  "v_lshlrev_b32_sdwa v228, 2, v228 dst_sel:DWORD dst_unused:UNUSED_PAD src0_sel:DWORD src1_sel:BYTE_1\n\t" \
  "v_lshlrev_b32_sdwa v231, 2, v230 dst_sel:DWORD dst_unused:UNUSED_PAD src0_sel:DWORD src1_sel:BYTE_3\n\t" \
  "v_lshlrev_b32_sdwa v230, 2, v230 dst_sel:DWORD dst_unused:UNUSED_PAD src0_sel:DWORD src1_sel:BYTE_1\n\t" \
  "v_mfma_f32_16x16x32_bf16 v[16:19], v[212:215], v[146:149], v[16:19]\n\t" \
  "ds_read_u16_d16_hi v227, v227 offset:10528\n\t" \
  "v_lshlrev_b32_sdwa v233, 1, v205 dst_sel:DWORD dst_unused:UNUSED_PAD src0_sel:DWORD src1_sel:WORD_1\n\t" \
  "v_lshlrev_b32_sdwa v232, 1, v204 dst_sel:DWORD dst_unused:UNUSED_PAD src0_sel:DWORD src1_sel:WORD_1\n\t" \
  "v_lshlrev_b32_sdwa v235, 1, v207 dst_sel:DWORD dst_unused:UNUSED_PAD src0_sel:DWORD src1_sel:WORD_1\n\t" \
  "v_lshlrev_b32_sdwa v234, 1, v206 dst_sel:DWORD dst_unused:UNUSED_PAD src0_sel:DWORD src1_sel:WORD_1\n\t" \
  "s_waitcnt lgkmcnt(3)\n\t" \
  "v_mfma_f32_16x16x32_bf16 v[40:43], v[212:215], v[154:157], v[40:43]\n\t" \
  "ds_read_b64_tr_b16 v[208:209], v249 offset:12304\n\t" \
  "ds_read_u16 v226, v226 offset:10528\n\t" \
  "v_fma_mix_f32 v221, |v181|, v221, v221 op_sel:[0,1,0] op_sel_hi:[0,1,1] clamp\n\t" \
  "v_fma_mix_f32 v220, |v180|, v220, v220 op_sel:[0,1,0] op_sel_hi:[0,1,1] clamp\n\t" \
  "v_fma_mix_f32 v223, |v183|, v223, v223 op_sel:[0,1,0] op_sel_hi:[0,1,1] clamp\n\t" \
  "v_mfma_f32_16x16x32_bf16 v[64:67], v[212:215], v[162:165], v[64:67]\n\t" \
  "ds_read_b64_tr_b16 v[210:211], v249 offset:13840\n\t" \
  "ds_read_b32 v229, v229 offset:10016\n\t" \
  "v_fma_mix_f32 v222, |v182|, v222, v222 op_sel:[0,1,0] op_sel_hi:[0,1,1] clamp\n\t" \
  "s_waitcnt lgkmcnt(2)\n\t" \
  "v_or_b32 v188, v224, v225\n\t" \
  "v_or_b32 v189, v226, v227\n\t" \
  "v_fma_f32 v181, |v181|, v221, v181 mul:4\n\t" \
  "v_mfma_f32_16x16x32_bf16 v[88:91], v[212:215], v[170:173], v[88:91]\n\t" \
  "ds_read_b64_tr_b16 v[212:213], v249 offset:12312\n\t" \
  "ds_read_b32 v228, v228 offset:10016\n\t" \
  "v_fma_f32 v180, |v180|, v220, v180 mul:4\n\t" \
  "v_fma_f32 v183, |v183|, v223, v183 mul:4\n\t" \
  "v_fma_f32 v182, |v182|, v222, v182 mul:4\n\t" \
  "v_mfma_f32_16x16x32_bf16 v[20:23], v[216:219], v[146:149], v[20:23]\n\t" \
  "ds_read_b64_tr_b16 v[214:215], v249 offset:13848\n\t" \
  "ds_read_b32 v231, v231 offset:10016\n\t" \
  "v_cvt_pk_bf16_f32 v180, v180, v181\n\t" \
  "v_cvt_pk_bf16_f32 v181, v182, v183\n\t" \
  "ds_read_b32 v230, v230 offset:10016\n\t" \
  "s_waitcnt lgkmcnt(0)\n\t" \
  "v_fma_mix_f32 v229, |v197|, v229, v229 op_sel:[0,1,0] op_sel_hi:[0,1,1] clamp\n\t" \
  "v_fma_mix_f32 v228, |v196|, v228, v228 op_sel:[0,1,0] op_sel_hi:[0,1,1] clamp\n\t" \
  "v_mfma_f32_16x16x32_bf16 v[44:47], v[216:219], v[154:157], v[44:47]\n\t" \
  "ds_read_u16_d16_hi v233, v233 offset:10528\n\t" \
  "ds_read_u16 v232, v232 offset:10528\n\t" \
  "ds_read_u16_d16_hi v235, v235 offset:10528\n\t" \
  "ds_read_u16 v234, v234 offset:10528\n\t" \
  "v_fma_mix_f32 v231, |v199|, v231, v231 op_sel:[0,1,0] op_sel_hi:[0,1,1] clamp\n\t" \
  "v_fma_mix_f32 v230, |v198|, v230, v230 op_sel:[0,1,0] op_sel_hi:[0,1,1] clamp\n\t" \
  "s_waitcnt lgkmcnt(0)\n\t" \
  "v_or_b32 v204, v232, v233\n\t" \
  "v_mfma_f32_16x16x32_bf16 v[68:71], v[216:219], v[162:165], v[68:71]\n\t" \
  "v_or_b32 v205, v234, v235\n\t" \
  "v_fma_f32 v197, |v197|, v229, v197 mul:4\n\t" \
  "v_fma_f32 v196, |v196|, v228, v196 mul:4\n\t" \
  "v_fma_f32 v199, |v199|, v231, v199 mul:4\n\t" \
  "v_mfma_f32_16x16x32_bf16 v[92:95], v[216:219], v[170:173], v[92:95]\n\t" \
  "ds_read_b64_tr_b16 v[216:217], v249 offset:12368\n\t" \
  "v_fma_f32 v198, |v198|, v230, v198 mul:4\n\t" \
  "v_cvt_pk_bf16_f32 v196, v196, v197\n\t" \
  "v_cvt_pk_bf16_f32 v197, v198, v199\n\t" \
  "v_mfma_f32_16x16x32_bf16 v[0:3], v[208:211], v[178:181], v[0:3]\n\t" \
  "ds_read_b64_tr_b16 v[218:219], v249 offset:13904\n\t" \
  "v_mfma_f32_16x16x32_bf16 v[24:27], v[208:211], v[186:189], v[24:27]\n\t" \
  "v_mfma_f32_16x16x32_bf16 v[48:51], v[208:211], v[194:197], v[48:51]\n\t" \
  "v_mfma_f32_16x16x32_bf16 v[72:75], v[208:211], v[202:205], v[72:75]\n\t" \
  "ds_read_b64_tr_b16 v[208:209], v249 offset:12376\n\t" \
  "v_mfma_f32_16x16x32_bf16 v[4:7], v[212:215], v[178:181], v[4:7]\n\t" \
  "ds_read_b64_tr_b16 v[210:211], v249 offset:13912\n\t" \
  "v_mfma_f32_16x16x32_bf16 v[28:31], v[212:215], v[186:189], v[28:31]\n\t" \
  "v_mfma_f32_16x16x32_bf16 v[52:55], v[212:215], v[194:197], v[52:55]\n\t" \
  "v_mfma_f32_16x16x32_bf16 v[76:79], v[212:215], v[202:205], v[76:79]\n\t" \
  "ds_read_b64_tr_b16 v[212:213], v249 offset:12432\n\t" \
  "s_waitcnt lgkmcnt(3)\n\t" \
  "v_mfma_f32_16x16x32_bf16 v[8:11], v[216:219], v[178:181], v[8:11]\n\t" \
  "ds_read_b64_tr_b16 v[214:215], v249 offset:13968\n\t" \
  "v_mfma_f32_16x16x32_bf16 v[32:35], v[216:219], v[186:189], v[32:35]\n\t" \
  "v_mfma_f32_16x16x32_bf16 v[56:59], v[216:219], v[194:197], v[56:59]\n\t" \
  "v_mfma_f32_16x16x32_bf16 v[80:83], v[216:219], v[202:205], v[80:83]\n\t" \
  "ds_read_b64_tr_b16 v[216:217], v249 offset:12440\n\t" \
  "v_cvt_pk_bf16_f32 v0, v0, v1\n\t" \
  "v_cvt_pk_bf16_f32 v1, v2, v3\n\t" \
  "v_cvt_pk_bf16_f32 v2, v4, v5\n\t" \
  "v_cvt_pk_bf16_f32 v3, v6, v7\n\t" \
  "buffer_store_dwordx4 v[0:3], v250, s[20:23], s63 offen offset:0 sc1\n\t" \
  "v_cvt_pk_bf16_f32 v24, v24, v25\n\t" \
  "v_cvt_pk_bf16_f32 v25, v26, v27\n\t" \
  "v_cvt_pk_bf16_f32 v26, v28, v29\n\t" \
  "v_cvt_pk_bf16_f32 v27, v30, v31\n\t" \
  "buffer_store_dwordx4 v[24:27], v250, s[20:23], s62 offen offset:0 sc1\n\t" \
  "v_cvt_pk_bf16_f32 v48, v48, v49\n\t" \
  "v_cvt_pk_bf16_f32 v49, v50, v51\n\t" \
  "v_cvt_pk_bf16_f32 v50, v52, v53\n\t" \
  "v_cvt_pk_bf16_f32 v51, v54, v55\n\t" \
  "buffer_store_dwordx4 v[48:51], v250, s[20:23], s14 offen offset:0 sc1\n\t" \
  "v_cvt_pk_bf16_f32 v72, v72, v73\n\t" \
  "v_cvt_pk_bf16_f32 v73, v74, v75\n\t" \
  "v_cvt_pk_bf16_f32 v74, v76, v77\n\t" \
  "v_cvt_pk_bf16_f32 v75, v78, v79\n\t" \
  "buffer_store_dwordx4 v[72:75], v250, s[20:23], s15 offen offset:0 sc1\n\t" \
  "s_waitcnt lgkmcnt(3)\n\t" \
  "v_mfma_f32_16x16x32_bf16 v[12:15], v[208:211], v[178:181], v[12:15]\n\t" \
  "ds_read_b64_tr_b16 v[218:219], v249 offset:13976\n\t" \
  "v_mfma_f32_16x16x32_bf16 v[36:39], v[208:211], v[186:189], v[36:39]\n\t" \
  "v_mfma_f32_16x16x32_bf16 v[60:63], v[208:211], v[194:197], v[60:63]\n\t" \
  "v_mfma_f32_16x16x32_bf16 v[84:87], v[208:211], v[202:205], v[84:87]\n\t" \
  "s_waitcnt lgkmcnt(2)\n\t" \
  "v_mfma_f32_16x16x32_bf16 v[16:19], v[212:215], v[178:181], v[16:19]\n\t" \
  "v_mfma_f32_16x16x32_bf16 v[40:43], v[212:215], v[186:189], v[40:43]\n\t" \
  "v_mfma_f32_16x16x32_bf16 v[64:67], v[212:215], v[194:197], v[64:67]\n\t" \
  "v_mfma_f32_16x16x32_bf16 v[88:91], v[212:215], v[202:205], v[88:91]\n\t" \
  "v_cvt_pk_bf16_f32 v8, v8, v9\n\t" \
  "v_cvt_pk_bf16_f32 v9, v10, v11\n\t" \
  "v_cvt_pk_bf16_f32 v10, v12, v13\n\t" \
  "v_cvt_pk_bf16_f32 v11, v14, v15\n\t" \
  "buffer_store_dwordx4 v[8:11], v250, s[20:23], s63 offen offset:64 sc1\n\t" \
  "v_cvt_pk_bf16_f32 v32, v32, v33\n\t" \
  "v_cvt_pk_bf16_f32 v33, v34, v35\n\t" \
  "v_cvt_pk_bf16_f32 v34, v36, v37\n\t" \
  "v_cvt_pk_bf16_f32 v35, v38, v39\n\t" \
  "buffer_store_dwordx4 v[32:35], v250, s[20:23], s62 offen offset:64 sc1\n\t" \
  "v_cvt_pk_bf16_f32 v56, v56, v57\n\t" \
  "v_cvt_pk_bf16_f32 v57, v58, v59\n\t" \
  "v_cvt_pk_bf16_f32 v58, v60, v61\n\t" \
  "v_cvt_pk_bf16_f32 v59, v62, v63\n\t" \
  "buffer_store_dwordx4 v[56:59], v250, s[20:23], s14 offen offset:64 sc1\n\t" \
  "v_cvt_pk_bf16_f32 v80, v80, v81\n\t" \
  "v_cvt_pk_bf16_f32 v81, v82, v83\n\t" \
  "v_cvt_pk_bf16_f32 v82, v84, v85\n\t" \
  "v_cvt_pk_bf16_f32 v83, v86, v87\n\t" \
  "buffer_store_dwordx4 v[80:83], v250, s[20:23], s15 offen offset:64 sc1\n\t" \
  "s_waitcnt lgkmcnt(0)\n\t" \
  "v_mfma_f32_16x16x32_bf16 v[20:23], v[216:219], v[178:181], v[20:23]\n\t" \
  "v_mfma_f32_16x16x32_bf16 v[44:47], v[216:219], v[186:189], v[44:47]\n\t" \
  "v_mfma_f32_16x16x32_bf16 v[68:71], v[216:219], v[194:197], v[68:71]\n\t" \
  "v_mfma_f32_16x16x32_bf16 v[92:95], v[216:219], v[202:205], v[92:95]\n\t" \
  "v_cvt_pk_bf16_f32 v16, v16, v17\n\t" \
  "v_cvt_pk_bf16_f32 v17, v18, v19\n\t" \
  "s_nop 2\n\t" \
  "v_cvt_pk_bf16_f32 v18, v20, v21\n\t" \
  "v_cvt_pk_bf16_f32 v19, v22, v23\n\t" \
  "buffer_store_dwordx4 v[16:19], v250, s[20:23], s63 offen offset:128 sc1\n\t" \
  "v_cvt_pk_bf16_f32 v40, v40, v41\n\t" \
  "v_cvt_pk_bf16_f32 v41, v42, v43\n\t" \
  "v_cvt_pk_bf16_f32 v42, v44, v45\n\t" \
  "v_cvt_pk_bf16_f32 v43, v46, v47\n\t" \
  "buffer_store_dwordx4 v[40:43], v250, s[20:23], s62 offen offset:128 sc1\n\t" \
  "v_cvt_pk_bf16_f32 v64, v64, v65\n\t" \
  "v_cvt_pk_bf16_f32 v65, v66, v67\n\t" \
  "v_cvt_pk_bf16_f32 v66, v68, v69\n\t" \
  "v_cvt_pk_bf16_f32 v67, v70, v71\n\t" \
  "buffer_store_dwordx4 v[64:67], v250, s[20:23], s14 offen offset:128 sc1\n\t" \
  "v_cvt_pk_bf16_f32 v88, v88, v89\n\t" \
  "v_cvt_pk_bf16_f32 v89, v90, v91\n\t" \
  "v_cvt_pk_bf16_f32 v90, v92, v93\n\t" \
  "v_cvt_pk_bf16_f32 v91, v94, v95\n\t" \
  "buffer_store_dwordx4 v[88:91], v250, s[20:23], s15 offen offset:128 sc1\n\t" \
  "s_waitcnt vmcnt(0)\n\t" \
  ".Lcnx_end:\n\t" \
  ""


__device__ __forceinline__ unsigned lds_addr(const char* p) { return (unsigned)(uintptr_t)(lds_ptr_t)p; }
__device__ __forceinline__ unsigned sg(unsigned v) { return __builtin_amdgcn_readfirstlane(v); }

constexpr double tanh_u(double y) {
    const double v = 8 * y, a = 2 * 0.79788456080286536 * (v + 0.044715 * v * v * v) / 1024;
    double m = 1;
    for (int i = 12; i > 1; --i) m = 1 + m * a / i;
    m *= a;
    for (int i = 0; i < 10; ++i) m *= m + 2;
    return m / (m + 2);
}
constexpr double h2d(int h) {
    if (h < 1024) return 0;
    double x = (1024 + (h & 1023)) * 0x1p-24;
    for (int e = 1; e < (h >> 10); ++e) x *= 2;
    return x;
}
constexpr int d2h(double x) {
    int h = 0;
    for (int s = 1 << 14; s; s >>= 1) if (h2d(h + s) <= x) h += s;
    return h;
}
constexpr unsigned ip_entry(int k) {
    if (k == 127) return 0x3c003c00u;
    const double y0 = k ? (256 * k - 0.5) / 32767 : 0, dy = ((256 * k + 255.5) / 32767 - y0) / 8;
    double t[9] = {};
    for (int i = 0; i < 9; ++i) t[i] = tanh_u(y0 + i * dy);
    const double b0 = (t[8] - t[0]) / (8 * dy), ym = y0 + 4 * dy, tm = (t[4] + (t[0] + t[8]) / 2) / 2;
    unsigned best = 0;
    double err = 1;
    for (int ha = d2h(tm - b0 * ym) - 1, i = 0; i < 4; ++i, ++ha)
        for (int hb = d2h((tm - h2d(ha)) / ym), j = 0; j < 2; ++j, ++hb) {
            double e = 0;
            for (int n = 0; n < 9; ++n) {
                const double s = h2d(ha) + h2d(hb) * (y0 + n * dy), d = (s < 1 ? s : 1) - t[n];
                e = d > e ? d : -d > e ? -d : e;
            }
            if (e < err) err = e, best = (ha < 1024 ? 0 : ha) | (hb < 1024 ? 0 : hb) << 16;
        }
    return best;
}
constexpr unsigned g_entry(int e, bool neg) {
    double y = e ? ((e & 127) + 0.5 + (e >> 7 ? 128 : 0)) * 0x1p-23 / (1 + 1.0 / 384) : 0;
    for (int i = 1; i < (e >> 7); ++i) y *= 2;
    const double t = y < 1 ? tanh_u(y) : 1;
    const unsigned u = __builtin_bit_cast(unsigned, (float)(4 * y * (neg ? t - 1 : t + 1)));
    return (u + 0x7fff + (u >> 16 & 1)) >> 16;
}
struct TanhTab { unsigned v[1024]; };
constexpr TanhTab gelu_tab(int k0) {
    TanhTab t = {};
    for (int k = k0; k < k0 + 1024 && k < 128 + GSZ / 2; ++k) {
        const int e = 2 * (k < 2048 ? k - 128 : k - 2048);
        t.v[k - k0] = k < 128 ? ip_entry(k) : g_entry(e, k >= 2048) | g_entry(e + 1, k >= 2048) << 16;
    }
    return t;
}
struct WaveTab { unsigned v192[64][4], v244[64][4], v16[64][4], s32[32]; };
struct RegTab { unsigned v240[64][4], v248[64][4]; WaveTab w[8]; };
constexpr unsigned cpos(unsigned w, unsigned px) { return (w >> 1) * 1024 + (px ^ (w & 2) << 2) * 16 + ((w ^ px >> 4) & 1) * 8; }
constexpr RegTab reg_tab() {
    RegTab t = {};
    for (unsigned l = 0; l < 64; ++l) {
        const unsigned G = l >> 4, n = l & 15, q = n >> 2, p = l & 3;
        t.v240[l][2 + (p >> 1)] = G == q ? (p & 1 ? 0x3f800000u : 0x3f80u) : 0u;
        t.v248[l][0] = WB + (l >> 2) * 32 + (p & 1) * 16 + (p >> 1) * 8;
        t.v248[l][1] = WB + (G & 1) * 3080 + ((G >> 1) * 4 + q) * 192 + p * 16;
        t.v248[l][2] = (n & 7) * 384 + (n >> 3) * 192 + G * 16;
        t.v248[l][3] = PB + l * 8;
        for (unsigned w = 0; w < 8; ++w) {
            t.w[w].v244[l][2] = XOFF + n * 400 + (G & 1) * XROW + (G >> 1) * 192 + w * 24;
            t.w[w].v244[l][3] = dw_pos(3 * w) + p * 192 + (1 - (q >> 1)) * 24 + (1 + (G & 1) - (q & 1)) * 8 + ((G ^ p) >> 1) * 4;
            const unsigned pl = PTOP + (w >> 2) * 12288, blk = (w & 3) * 16;
            t.w[w].v192[l][0] = XOFF + cpos(w, (G & 2) * 16 + (n & 8) * 2 + (G & 1) * 8 + (n & 7));
            t.w[w].v192[l][1] = t.w[w].v192[l][0] + PTOP - XOFF;
            t.w[w].v192[l][2] = pl + (cpos((G & 1) * 4 + (G & 2), blk + n) & ~8u);
            t.w[w].v192[l][3] = pl + cpos(p, blk + (l >> 2));
            const unsigned u = (w & 3) * 48 + l, j = u & 63;
            t.w[w].v16[l][0] = w < 4 ? (4 * (u >> 6) + 12 * ((j >> 3) & 3) + ((j >> 1) & 3)) * (HID * 2) + (2 * (j & 1) + (j >> 5)) * 16 : u * 16;
            const unsigned d = dw_pos(w * 8 + (l >> 5) * 4 + p) + ((l >> 2) & 7) * 24;
            t.w[w].v16[l][1] = w < 3 ? d + (l & 1) * 192 : (l & 4 ? DW2 + 11 * 768 : 13 * 768) + p * 8;
            t.w[w].v16[l][2] = d + (~l & 1) * 192;
            t.w[w].v16[l][3] = l & 1 ? 0x2b800000u : 0x2b80u;
        }
    }
    for (unsigned w = 0; w < 8; ++w) {
        const bool w1 = w < 4;
        const unsigned dm = w1 ? 3072 : 3080, wl = WB + (w & 3) * 768, wc = w1 ? 64 : 6144;
        const unsigned rp[4] = {x_pos(3 + (w >> 1 ^ 2)), x_pos(7 + (w >> 1)), x_pos((11 + (w >> 1)) % 12), x_pos(3 + (w >> 1))};
        const unsigned s[32] = {wc, 0, w1 ? 48u * HID * 2 : 3072, dm, dm + XOFF - WB, w >> 2, w, w * 512,
                                0xaaaaaaaau, 0xaaaaaaaau, 0xccccccccu, 0xccccccccu,
                                wl + (w1 ? WA + 12304 : WA), wl + (w1 ? 0 : WA + 6152), wl + (w1 ? WA + 18448 : 6152),
                                wl + (w1 ? WA + 18448 + 6144 : 12304), w1 ? -11 * wc : wc, w1 ? wc : -11 * wc,
                                0u + XOFF - WB, 4 * wc, ~0x00f0000fu, ~0xf0000f00u,
                                (w >> 2 ? PTOP - 12288u : XHI + 6 * XROW) - XOFF - 4 * 12288, 0x07060302, dw_pos(3 * w + 2) - dw_pos(3 * w), 0,
                                rp[1] - rp[0], rp[2] - rp[1], rp[3] - rp[2], rp[0] + 592 + (w & 1) * 3200, 0, 8 * (unsigned)sizeof(WaveTab) - 2 * PB};
        for (int i = 0; i < 32; ++i) t.w[w].s32[i] = s[i];
    }
    return t;
}
struct Tables { RegTab r; TanhTab t[4]; };
static_assert(sizeof(WaveTab) == 64 * 50 && sizeof(RegTab) == 27648, "table layout");
__device__ const __attribute__((aligned(128))) Tables TABLES = {reg_tab(), {gelu_tab(0), gelu_tab(1024), gelu_tab(2048), gelu_tab(3072)}};

#define R10(r, a) r #a "0", r #a "1", r #a "2", r #a "3", r #a "4", r #a "5", r #a "6", r #a "7", r #a "8", r #a "9"
#define V10(a) R10("v", a)
#define S10(a) R10("s", a)
#define MAIN_CLOB "memory", "scc", "vcc", "v1", "v2", "v3", "v4", "v5", "v6", "v7", "v8", "v9", V10(1), V10(2), V10(3), V10(4), V10(5),  \
    V10(6), V10(7), V10(8), V10(9), V10(10), V10(11), V10(12), V10(13), V10(14), V10(15), V10(16), V10(17), V10(18), V10(19),        \
    V10(20), V10(21), V10(22), V10(23), V10(24), "v250", "v251", "v252", "v253", "v254", "s4", "s5", "s8", "s9", S10(1),       \
    S10(2), S10(3), S10(4), S10(5), "s60", "s61", "s62", "s63", "s64", "s65", "s66", "s67", "s68", "s69"

__global__ __launch_bounds__(512, 2) void convnext_kernel(const __hip_bfloat16* __restrict__ x,
                                                          const __hip_bfloat16* __restrict__ dw,
                                                          const __hip_bfloat16* __restrict__ w1,
                                                          const __hip_bfloat16* __restrict__ w2,
                                                          __hip_bfloat16* __restrict__ out) {
    __shared__ __attribute__((aligned(16))) char smem[LDS_BYTES];

    unsigned tid = threadIdx.x, lds = lds_addr(smem);
    const unsigned bid = blockIdx.x;
    asm volatile(MAIN_ASM
                 : "+{v0}"(tid), "+{s3}"(lds)
                 : "{s[0:1]}"(__builtin_amdgcn_kernarg_segment_ptr()), "{s2}"(bid), "{s[6:7]}"(TABLES.r.w)
                 : MAIN_CLOB);

}

}

extern "C" void kernel_launch(void* const* d_in, const int* in_sizes, int n_in, void* d_out, int out_size,
                              void* d_ws, size_t ws_size, hipStream_t stream) {
    (void)in_sizes; (void)n_in; (void)out_size; (void)d_ws; (void)ws_size;
    const __hip_bfloat16* x = (const __hip_bfloat16*)d_in[0];
    const __hip_bfloat16* dw = (const __hip_bfloat16*)d_in[1];
    const __hip_bfloat16* w1 = (const __hip_bfloat16*)d_in[2];
    const __hip_bfloat16* w2 = (const __hip_bfloat16*)d_in[3];
    __hip_bfloat16* out = (__hip_bfloat16*)d_out;
    convnext_kernel<<<256, 512, 0, stream>>>(x, dw, w1, w2, out);
}
```

```cpp
#include <hip/hip_runtime.h>
#include <hip/hip_bf16.h>
#include <stdint.h>

namespace {

typedef __attribute__((address_space(3))) void* lds_ptr_t;

constexpr int IMG = 512;
constexpr int CH = 96;
constexpr int HID = 384;
constexpr int ROWB = IMG * CH * 2;
constexpr int IPOFF = 13 * 768 + 32;
constexpr int GOFF = IPOFF + 512;
constexpr int GSZ = 30 * 256;
constexpr int DW2 = GOFF + GSZ;
constexpr int XROW = 8200;
constexpr int XOFF = GOFF + 65536 - 6 * XROW;
constexpr int XHI = XOFF + GSZ;
constexpr int PB = XHI + 10 * XROW;
constexpr int WB = XHI + 9 * XROW;
constexpr int WA = 3 * XROW;
constexpr int PTOP = XHI + 12 * XROW - 12288;
constexpr int CNTOFF = WB + WA + 18448 + 12288;
constexpr int LDS_BYTES = 160 * 1024;
static_assert(XOFF == 26864 && CNTOFF - PB == 47136 && DW2 + 11 * 768 + 32 <= XOFF && CNTOFF + 144 <= LDS_BYTES, "LDS map");
constexpr unsigned dw_pos(unsigned u) { return u < 12 ? u * 768 : u == 14 ? 12 * 768 : DW2 + (u - 12 - (u > 14)) * 768; }
constexpr unsigned x_pos(unsigned r) { return (r < 6 ? XOFF : XHI) + r * XROW; }
constexpr unsigned OOB = 0x40000000u;

#define MAIN_ASM \
  "s_load_dwordx2 s[8:9], s[0:1], 0x0\n\t" \
  "s_load_dwordx2 s[12:13], s[0:1], 0x8\n\t" \
  "s_load_dwordx2 s[20:21], s[0:1], 0x20\n\t" \
  "s_load_dwordx4 s[32:35], s[0:1], 0x10\n\t" \
  "v_readfirstlane_b32 s5, v0\n\t" \
  "v_and_b32 v21, 63, v0\n\t" \
  "s_mov_b32 m0, 107744\n\t" \
  "s_cmp_lt_u32 s5, 256\n\t" \
  "s_cselect_b32 s3, 16, 24\n\t" \
  "v_lshlrev_b32 v22, 4, v21\n\t" \
  "s_load_dwordx2 s[16:17], s[0:1], s3\n\t" \
  "s_mul_i32 s4, s5, 50\n\t" \
  "s_add_u32 s66, s6, s4\n\t" \
  "s_addc_u32 s67, s7, 0\n\t" \
  "global_load_dwordx4 v[244:247], v22, s[66:67] offset:1024\n\t" \
  "global_load_dwordx4 v[16:19], v22, s[66:67] offset:2048\n\t" \
  "global_load_dwordx4 v[240:243], v22, s[6:7] offset:-2048\n\t" \
  "global_load_dwordx4 v[248:251], v22, s[6:7] offset:-1024\n\t" \
  "global_load_dwordx4 v[192:195], v22, s[66:67]\n\t" \
  "s_cmp_lg_u32 s5, 192\n\t" \
  "s_cbranch_scc1 .Lcnx_nc\n\t" \
  "s_getpc_b64 s[14:15]\n\t" \
  ".Lcnx_pc:\n\t" \
  "s_lshr_b32 s3, s2, 3\n\t" \
  "s_lshl_b32 s3, s3, 11\n\t" \
  "v_lshl_add_u32 v1, v21, 5, s3\n\t" \
  "v_min_u32 v1, .Lcnx_end-.Lcnx_pc, v1\n\t" \
  "global_load_lds_dword v1, s[14:15]\n\t" \
  ".Lcnx_nc:\n\t" \
  "v_mul_u32_u24 v254, 0x147b, v0\n\t" \
  "s_bfe_u32 s27, s2, 0x40003\n\t" \
  "v_bfe_u32 v1, v0, 2, 3\n\t" \
  "s_mul_i32 s27, s27, 6144\n\t" \
  "v_lshrrev_b32 v254, 17, v254\n\t" \
  "s_sub_u32 s3, s27, 576\n\t" \
  "v_and_b32 v20, 3, v0\n\t" \
  "s_add_u32 s4, s27, 6720\n\t" \
  "v_sub_u32 v254, v0, v254\n\t" \
  "s_min_u32 s4, s4, 0x18000\n\t" \
  "v_and_b32 v2, 0x1e0, v0\n\t" \
  "s_and_b32 s24, s2, 7\n\t" \
  "v_lshl_add_u32 v254, v254, 4, s3\n\t" \
  "s_lshl_b32 s24, s24, 6\n\t" \
  "v_lshl_or_b32 v20, v20, 3, v2\n\t" \
  "s_mov_b32 s26, 0x18000\n\t" \
  "v_cmp_gt_u32 vcc, s4, v254\n\t" \
  "s_cmp_ge_u32 s2, 128\n\t" \
  "s_cselect_b32 s3, 7, 0\n\t" \
  "s_cselect_b32 s4, 0, 7\n\t" \
  "v_add_u32 v1, s3, v1\n\t" \
  "s_cselect_b32 s3, 28, 35\n\t" \
  "v_cndmask_b32 v254, 2.0, v254, vcc\n\t" \
  "s_cselect_b32 s26, s26, 0xfffe8000\n\t" \
  "v_xor_b32 v1, s4, v1\n\t" \
  "s_add_u32 s24, s24, s3\n\t" \
  "v_and_b32 v1, 7, v1\n\t" \
  "s_movk_i32 s3, 1344\n\t" \
  "s_mul_i32 s24, s24, 0x18000\n\t" \
  "v_mad_u32_u24 v1, v1, s3, v20\n\t" \
  "s_lshl4_add_u32 s25, s5, 26864\n\t" \
  "s_mov_b32 s10, 0x3000000\n\t" \
  "s_mov_b32 s11, 0x20000\n\t" \
  "s_mov_b32 s14, 9408\n\t" \
  "s_mov_b32 s15, 0x20000\n\t" \
  "s_mov_b32 s18, 73728\n\t" \
  "s_mov_b32 s19, 0x20000\n\t" \
  "s_mov_b32 s22, 0x3000000\n\t" \
  "s_mov_b32 s23, 0x20000\n\t" \
  "s_add_u32 s3, s24, s26\n\t" \
  "s_add_u32 s3, s3, s27\n\t" \
  "s_mul_i32 s4, s26, 10\n\t" \
  "v_mov_b32 v24, s3\n\t" \
  "v_mad_i32_i24 v27, v21, s4, v24\n\t" \
  "s_setreg_imm32_b32 hwreg(HW_REG_MODE, 4, 2), 0\n\t" \
  "s_setreg_imm32_b32 hwreg(HW_REG_MODE, 9, 1), 0\n\t" \
  "s_lshl_b32 s65, s26, 4\n\t" \
  "s_lshr_b32 s3, s5, 7\n\t" \
  "s_add_u32 s3, s3, 8\n\t" \
  "s_mul_i32 s3, s3, s26\n\t" \
  "s_add_u32 s29, s24, s27\n\t" \
  "s_add_u32 s29, s29, s3\n\t" \
  "s_and_b32 s3, s5, 64\n\t" \
  "s_mul_i32 s3, s3, 48\n\t" \
  "s_add_u32 s29, s29, s3\n\t" \
  "s_mul_i32 s3, s26, 9\n\t" \
  "s_add_u32 s27, s24, s65\n\t" \
  "s_mov_b32 s30, 0x01010101\n\t" \
  "s_mov_b32 s31, 0x01010101\n\t" \
  "s_add_u32 s27, s27, s3\n\t" \
  "s_mul_i32 s64, s26, 22\n\t" \
  "s_sub_u32 s64, s64, s65\n\t" \
  "s_cmp_ge_u32 s5, 256\n\t" \
  "s_cselect_b32 s3, -6, -2\n\t" \
  "s_mul_i32 s3, s3, s26\n\t" \
  "s_add_u32 s28, s29, s3\n\t" \
  "s_mov_b64 vcc, 0\n\t" \
  "s_waitcnt lgkmcnt(0)\n\t" \
  "v_add_co_u32 v24, vcc, s8, v27\n\t" \
  "v_mov_b32 v25, s9\n\t" \
  "v_addc_co_u32 v25, vcc, 0, v25, vcc\n\t" \
  "v_writelane_b32 v24, s12, 3\n\t" \
  "v_writelane_b32 v25, s13, 3\n\t" \
  "v_writelane_b32 v24, s32, 4\n\t" \
  "v_writelane_b32 v25, s33, 4\n\t" \
  "v_writelane_b32 v24, s34, 5\n\t" \
  "v_writelane_b32 v25, s35, 5\n\t" \
  "s_mov_b64 exec, 63\n\t" \
  "global_load_dword v26, v[24:25], off\n\t" \
  "s_mov_b64 exec, -1\n\t" \
  "s_mov_b64 vcc, 0\n\t" \
  "s_and_b32 s13, s13, 0xffff\n\t" \
  "s_and_b32 s9, s9, 0xffff\n\t" \
  "s_and_b32 s17, s17, 0xffff\n\t" \
  "s_cmp_ge_u32 s5, 192\n\t" \
  "s_cbranch_scc1 .Lcnx_nd\n\t" \
  "buffer_load_dwordx2 v[2:3], v1, s[12:15], 0 offen offset:0\n\t" \
  "buffer_load_dwordx2 v[4:5], v1, s[12:15], 0 offen offset:192\n\t" \
  "buffer_load_dwordx2 v[6:7], v1, s[12:15], 0 offen offset:384\n\t" \
  "buffer_load_dwordx2 v[8:9], v1, s[12:15], 0 offen offset:576\n\t" \
  "buffer_load_dwordx2 v[10:11], v1, s[12:15], 0 offen offset:768\n\t" \
  "buffer_load_dwordx2 v[12:13], v1, s[12:15], 0 offen offset:960\n\t" \
  "buffer_load_dwordx2 v[14:15], v1, s[12:15], 0 offen offset:1152\n\t" \
  ".Lcnx_nd:\n\t" \
  "s_and_b32 s3, s5, 0xc0\n\t" \
  "s_cmp_lg_u32 s3, 64\n\t" \
  "s_cbranch_scc1 .Lcnx_nw\n\t" \
  "s_lshr_b32 s3, s2, 3\n\t" \
  "s_mul_i32 s3, s3, 2304\n\t" \
  "s_mov_b32 m0, 107744\n\t" \
  "v_mul_u32_u24 v21, 36, v21\n\t" \
  "buffer_load_dword v21, s[16:19], s3 offen lds\n\t" \
  ".Lcnx_nw:\n\t" \
  "s_add_u32 m0, s25, 0\n\t" \
  "s_add_u32 s24, s24, s26\n\t" \
  "buffer_load_dwordx4 v254, s[8:11], s24 offen lds\n\t" \
  "s_add_u32 m0, s25, 8200\n\t" \
  "s_add_u32 s24, s24, s26\n\t" \
  "buffer_load_dwordx4 v254, s[8:11], s24 offen lds\n\t" \
  "s_add_u32 m0, s25, 16400\n\t" \
  "s_add_u32 s24, s24, s26\n\t" \
  "buffer_load_dwordx4 v254, s[8:11], s24 offen lds\n\t" \
  "s_add_u32 m0, s25, 24600\n\t" \
  "s_add_u32 s24, s24, s26\n\t" \
  "buffer_load_dwordx4 v254, s[8:11], s24 offen lds\n\t" \
  "s_load_dwordx16 s[32:47], s[66:67], 0xc00\n\t" \
  "s_load_dwordx16 s[48:63], s[66:67], 0xc40\n\t" \
  "s_and_b32 s21, s21, 0xffff\n\t" \
  "s_waitcnt vmcnt(5)\n\t" \
  "v_mfma_f32_32x32x16_bf16 v[144:159], v[242:245], v[240:243], 0\n\t" \
  "v_mfma_f32_32x32x16_bf16 v[160:175], v[242:245], v[240:243], 0\n\t" \
  "v_bfe_u32 v234, v251, 3, 2\n\t" \
  "v_mul_u32_u24 v234, 0x5555, v234\n\t" \
  "v_mov_b32 v252, v16\n\t" \
  "s_lshl2_add_u32 s12, s26, s29\n\t" \
  "s_lshl3_add_u32 s13, s26, s29\n\t" \
  "v_mov_b32 v253, v246\n\t" \
  "s_mov_b32 s68, 0x07060302\n\t" \
  "s_cmp_gt_u32 s5, 192\n\t" \
  "s_cbranch_scc1 .Lcnx_e2\n\t" \
  "s_cmp_eq_u32 s5, 192\n\t" \
  "s_cbranch_scc1 .Lcnx_e1\n\t" \
  "v_xor_b32 v23, 0x2b802b80, v19\n\t" \
  "s_waitcnt vmcnt(4)\n\t" \
  "v_dot2_f32_bf16 v48, v2, v19, 0\n\t" \
  "v_dot2_f32_bf16 v50, v4, v19, 0\n\t" \
  "v_dot2_f32_bf16 v52, v6, v19, 0\n\t" \
  "v_dot2_f32_bf16 v54, v8, v19, 0\n\t" \
  "v_dot2_f32_bf16 v56, v10, v19, 0\n\t" \
  "v_dot2_f32_bf16 v58, v12, v19, 0\n\t" \
  "v_dot2_f32_bf16 v60, v14, v19, 0\n\t" \
  "v_dot2_f32_bf16 v2, v2, v23, 0\n\t" \
  "v_dot2_f32_bf16 v4, v4, v23, 0\n\t" \
  "v_dot2_f32_bf16 v6, v6, v23, 0\n\t" \
  "v_dot2_f32_bf16 v8, v8, v23, 0\n\t" \
  "v_dot2_f32_bf16 v10, v10, v23, 0\n\t" \
  "v_dot2_f32_bf16 v12, v12, v23, 0\n\t" \
  "v_dot2_f32_bf16 v14, v14, v23, 0\n\t" \
  "v_perm_b32 v24, v50, 0, s68\n\t" \
  "v_perm_b32 v25, v58, v54, s68\n\t" \
  "v_perm_b32 v26, v52, v48, s68\n\t" \
  "v_perm_b32 v27, v60, v56, s68\n\t" \
  "v_perm_b32 v28, v54, v50, s68\n\t" \
  "v_perm_b32 v29, 0, v58, s68\n\t" \
  "ds_write_b64 v17, v[24:25] offset:0\n\t" \
  "ds_write_b64 v17, v[26:27] offset:8\n\t" \
  "ds_write_b64 v17, v[28:29] offset:16\n\t" \
  "v_dot2_f32_bf16 v49, v3, v19, 0\n\t" \
  "v_dot2_f32_bf16 v51, v5, v19, 0\n\t" \
  "v_dot2_f32_bf16 v53, v7, v19, 0\n\t" \
  "v_dot2_f32_bf16 v55, v9, v19, 0\n\t" \
  "v_dot2_f32_bf16 v57, v11, v19, 0\n\t" \
  "v_dot2_f32_bf16 v59, v13, v19, 0\n\t" \
  "v_dot2_f32_bf16 v61, v15, v19, 0\n\t" \
  "v_perm_b32 v30, v4, 0, s68\n\t" \
  "v_perm_b32 v31, v12, v8, s68\n\t" \
  "v_perm_b32 v32, v6, v2, s68\n\t" \
  "v_perm_b32 v33, v14, v10, s68\n\t" \
  "v_perm_b32 v34, v8, v4, s68\n\t" \
  "v_perm_b32 v35, 0, v12, s68\n\t" \
  "ds_write_b64 v18, v[30:31] offset:0\n\t" \
  "ds_write_b64 v18, v[32:33] offset:8\n\t" \
  "ds_write_b64 v18, v[34:35] offset:16\n\t" \
  "v_dot2_f32_bf16 v3, v3, v23, 0\n\t" \
  "v_dot2_f32_bf16 v5, v5, v23, 0\n\t" \
  "v_dot2_f32_bf16 v7, v7, v23, 0\n\t" \
  "v_dot2_f32_bf16 v9, v9, v23, 0\n\t" \
  "v_dot2_f32_bf16 v11, v11, v23, 0\n\t" \
  "v_dot2_f32_bf16 v13, v13, v23, 0\n\t" \
  "v_dot2_f32_bf16 v15, v15, v23, 0\n\t" \
  "v_perm_b32 v37, v51, 0, s68\n\t" \
  "v_perm_b32 v36, v59, v55, s68\n\t" \
  "v_perm_b32 v39, v53, v49, s68\n\t" \
  "v_perm_b32 v38, v61, v57, s68\n\t" \
  "v_perm_b32 v41, v55, v51, s68\n\t" \
  "v_perm_b32 v40, 0, v59, s68\n\t" \
  "ds_write_b64 v17, v[36:37] offset:384\n\t" \
  "ds_write_b64 v17, v[38:39] offset:392\n\t" \
  "ds_write_b64 v17, v[40:41] offset:400\n\t" \
  "v_perm_b32 v43, v5, 0, s68\n\t" \
  "v_perm_b32 v42, v13, v9, s68\n\t" \
  "v_perm_b32 v45, v7, v3, s68\n\t" \
  "v_perm_b32 v44, v15, v11, s68\n\t" \
  "v_perm_b32 v47, v9, v5, s68\n\t" \
  "v_perm_b32 v46, 0, v13, s68\n\t" \
  "ds_write_b64 v18, v[42:43] offset:384\n\t" \
  "ds_write_b64 v18, v[44:45] offset:392\n\t" \
  "ds_write_b64 v18, v[46:47] offset:400\n\t" \
  "s_branch .Lcnx_e3\n\t" \
  ".Lcnx_e1:\n\t" \
  "s_mov_b64 exec, 0x3ffff\n\t" \
  "ds_write_b64 v251, v[240:241] offset:47136\n\t" \
  "s_mov_b64 exec, 0xff\n\t" \
  "ds_write_b64 v17, v[240:241]\n\t" \
  "s_mov_b64 exec, -1\n\t" \
  "s_branch .Lcnx_e3\n\t" \
  ".Lcnx_e2:\n\t" \
  "s_setprio 1\n\t" \
  ".Lcnx_e3:\n\t" \
  "s_add_u32 m0, s25, 32800\n\t" \
  "s_add_u32 s24, s24, s26\n\t" \
  "buffer_load_dwordx4 v254, s[8:11], s24 offen lds\n\t" \
  "s_add_u32 m0, s25, 41000\n\t" \
  "s_add_u32 s24, s24, s26\n\t" \
  "buffer_load_dwordx4 v254, s[8:11], s24 offen lds\n\t" \
  "s_add_u32 m0, s25, 56880\n\t" \
  "s_add_u32 s24, s24, s26\n\t" \
  "buffer_load_dwordx4 v254, s[8:11], s24 offen lds\n\t" \
  "s_add_u32 m0, s25, 65080\n\t" \
  "s_add_u32 s24, s24, s26\n\t" \
  "buffer_load_dwordx4 v254, s[8:11], s24 offen lds\n\t" \
  "s_waitcnt vmcnt(6) lgkmcnt(0)\n\t" \
  "s_barrier\n\t" \
  "s_sub_u32 s66, s66, 233088\n\t" \
  "s_subb_u32 s67, s67, 0\n\t" \
  "v_add_u32 v232, s56, v247\n\t" \
  "ds_read2_b32 v[98:99], v247 offset0:0 offset1:6\n\t" \
  "ds_read2_b32 v[114:115], v247 offset0:192 offset1:198\n\t" \
  "ds_read2_b32 v[130:131], v232 offset0:0 offset1:6\n\t" \
  "ds_read2_b32 v[102:103], v247 offset0:12 offset1:18\n\t" \
  "ds_read2_b32 v[118:119], v247 offset0:204 offset1:210\n\t" \
  "ds_read2_b32 v[134:135], v232 offset0:12 offset1:18\n\t" \
  "ds_read2_b32 v[106:107], v247 offset0:24 offset1:30\n\t" \
  "ds_read2_b32 v[122:123], v247 offset0:216 offset1:222\n\t" \
  "ds_read2_b32 v[138:139], v232 offset0:24 offset1:30\n\t" \
  "ds_read2_b32 v[110:111], v247 offset0:36 offset1:42\n\t" \
  "ds_read2_b32 v[126:127], v247 offset0:228 offset1:234\n\t" \
  "ds_read2_b32 v[142:143], v232 offset0:36 offset1:42\n\t" \
  "v_add_u32 v232, 56880, v253\n\t" \
  "v_add_u32 v233, s39, v251\n\t" \
  "v_mfma_f32_32x32x16_bf16 v[176:191], v[242:245], v[240:243], 0\n\t" \
  "ds_read_b64 v[200:201], v253 offset:0\n\t" \
  "ds_read_b64 v[202:203], v253 offset:400\n\t" \
  "ds_read_b64 v[204:205], v253 offset:800\n\t" \
  "ds_read_b64 v[206:207], v253 offset:1200\n\t" \
  "ds_read_b64 v[208:209], v253 offset:8\n\t" \
  "ds_read_b64 v[210:211], v253 offset:408\n\t" \
  "ds_read_b64 v[212:213], v253 offset:808\n\t" \
  "ds_read_b64 v[214:215], v253 offset:1208\n\t" \
  "ds_read_b64 v[216:217], v253 offset:16\n\t" \
  "ds_read_b64 v[218:219], v253 offset:416\n\t" \
  "ds_read_b64 v[220:221], v253 offset:816\n\t" \
  "ds_read_b64 v[222:223], v253 offset:1216\n\t" \
  "s_add_u32 m0, s25, 73280\n\t" \
  "s_add_u32 s24, s24, s26\n\t" \
  "buffer_load_dwordx4 v254, s[8:11], s24 offen lds\n\t" \
  "s_add_u32 m0, s25, 81480\n\t" \
  "s_add_u32 s24, s24, s26\n\t" \
  "buffer_load_dwordx4 v254, s[8:11], s24 offen lds\n\t" \
  "s_add_u32 m0, s25, 89680\n\t" \
  "s_add_u32 s24, s24, s26\n\t" \
  "buffer_load_dwordx4 v254, s[8:11], s24 offen lds\n\t" \
  "s_add_u32 m0, s25, 97880\n\t" \
  "s_add_u32 s24, s24, s26\n\t" \
  "buffer_load_dwordx4 v254, s[8:11], s24 offen lds\n\t" \
  "s_waitcnt lgkmcnt(15)\n\t" \
  "v_and_b32 v96, 0xffff, v98\n\t" \
  "v_and_b32 v97, 0xffff0000, v98\n\t" \
  "v_and_b32 v98, 0xffff, v99\n\t" \
  "v_and_b32 v99, 0xffff0000, v99\n\t" \
  "v_and_b32 v112, 0xffff, v114\n\t" \
  "v_and_b32 v113, 0xffff0000, v114\n\t" \
  "v_and_b32 v114, 0xffff, v115\n\t" \
  "v_and_b32 v115, 0xffff0000, v115\n\t" \
  "v_and_b32 v128, 0xffff, v130\n\t" \
  "v_and_b32 v129, 0xffff0000, v130\n\t" \
  "v_and_b32 v130, 0xffff, v131\n\t" \
  "v_and_b32 v131, 0xffff0000, v131\n\t" \
  "s_waitcnt lgkmcnt(8)\n\t" \
  "v_smfmac_f32_16x16x64_bf16 v[144:147], v[96:99], v[200:207], v234\n\t" \
  "v_and_b32 v100, 0xffff, v102\n\t" \
  "v_and_b32 v101, 0xffff0000, v102\n\t" \
  "v_and_b32 v102, 0xffff, v103\n\t" \
  "v_and_b32 v103, 0xffff0000, v103\n\t" \
  "s_waitcnt vmcnt(8) lgkmcnt(0)\n\t" \
  "s_barrier\n\t" \
  "ds_read_b64 v[224:225], v253 offset:16400\n\t" \
  "ds_read_b64 v[226:227], v253 offset:16800\n\t" \
  "ds_read_b64 v[228:229], v253 offset:17200\n\t" \
  "ds_read_b64 v[230:231], v253 offset:17600\n\t" \
  "v_smfmac_f32_16x16x64_bf16 v[148:151], v[112:115], v[208:215], v234\n\t" \
  "ds_read_b64 v[200:201], v253 offset:16408\n\t" \
  "ds_read_b64 v[202:203], v253 offset:16808\n\t" \
  "ds_read_b64 v[204:205], v253 offset:17208\n\t" \
  "ds_read_b64 v[206:207], v253 offset:17608\n\t" \
  "v_and_b32 v116, 0xffff, v118\n\t" \
  "v_and_b32 v117, 0xffff0000, v118\n\t" \
  "v_and_b32 v118, 0xffff, v119\n\t" \
  "v_and_b32 v119, 0xffff0000, v119\n\t" \
  "v_smfmac_f32_16x16x64_bf16 v[152:155], v[128:131], v[216:223], v234\n\t" \
  "ds_read_b64 v[208:209], v253 offset:16416\n\t" \
  "ds_read_b64 v[210:211], v253 offset:16816\n\t" \
  "ds_read_b64 v[212:213], v253 offset:17216\n\t" \
  "ds_read_b64 v[214:215], v253 offset:17616\n\t" \
  "v_and_b32 v132, 0xffff, v134\n\t" \
  "v_and_b32 v133, 0xffff0000, v134\n\t" \
  "v_and_b32 v134, 0xffff, v135\n\t" \
  "v_and_b32 v135, 0xffff0000, v135\n\t" \
  "v_lshlrev_b32 v235, 1, v251\n\t" \
  "v_and_b32 v236, 0x70, v235\n\t" \
  "v_bfe_u32 v235, v250, 6, 1\n\t" \
  "v_lshl_add_u32 v235, v235, 4, v250\n\t" \
  "s_waitcnt lgkmcnt(8)\n\t" \
  "v_smfmac_f32_16x16x64_bf16 v[156:159], v[96:99], v[224:231], v234\n\t" \
  "v_and_b32 v104, 0xffff, v106\n\t" \
  "v_and_b32 v105, 0xffff0000, v106\n\t" \
  "v_and_b32 v106, 0xffff, v107\n\t" \
  "v_and_b32 v107, 0xffff0000, v107\n\t" \
  "v_add3_u32 v235, v235, v236, s61\n\t" \
  "s_cmp_eq_u32 s37, 1\n\t" \
  "s_cselect_b64 s[68:69], -1, 0\n\t" \
  "v_smfmac_f32_16x16x64_bf16 v[144:147], v[100:103], v[224:231], v234\n\t" \
  "s_waitcnt vmcnt(5) lgkmcnt(0)\n\t" \
  "s_barrier\n\t" \
  "ds_read_b64 v[216:217], v253 offset:32800\n\t" \
  "ds_read_b64 v[218:219], v253 offset:33200\n\t" \
  "ds_read_b64 v[220:221], v253 offset:33600\n\t" \
  "ds_read_b64 v[222:223], v253 offset:34000\n\t" \
  "s_add_u32 m0, s25, 0\n\t" \
  "s_add_u32 s24, s24, s26\n\t" \
  "v_smfmac_f32_16x16x64_bf16 v[160:163], v[112:115], v[200:207], v234\n\t" \
  "buffer_load_dwordx4 v254, s[8:11], s24 offen lds\n\t" \
  "ds_read_b64 v[224:225], v253 offset:32808\n\t" \
  "ds_read_b64 v[226:227], v253 offset:33208\n\t" \
  "v_and_b32 v120, 0xffff, v122\n\t" \
  "v_and_b32 v121, 0xffff0000, v122\n\t" \
  "v_and_b32 v122, 0xffff, v123\n\t" \
  "v_and_b32 v123, 0xffff0000, v123\n\t" \
  "v_smfmac_f32_16x16x64_bf16 v[148:151], v[116:119], v[200:207], v234\n\t" \
  "ds_read_b64 v[228:229], v253 offset:33608\n\t" \
  "ds_read_b64 v[230:231], v253 offset:34008\n\t" \
  "ds_read_b64 v[76:77], v235 offset:0\n\t" \
  "ds_read_b64 v[78:79], v235 offset:8\n\t" \
  "s_add_u32 m0, s25, 8200\n\t" \
  "s_add_u32 s24, s24, s26\n\t" \
  "v_smfmac_f32_16x16x64_bf16 v[164:167], v[128:131], v[208:215], v234\n\t" \
  "buffer_load_dwordx4 v254, s[8:11], s24 offen lds\n\t" \
  "ds_read_b64 v[200:201], v253 offset:32816\n\t" \
  "ds_read_b64 v[202:203], v253 offset:33216\n\t" \
  "ds_read_b64 v[84:85], v235 offset:64\n\t" \
  "v_and_b32 v136, 0xffff, v138\n\t" \
  "v_and_b32 v137, 0xffff0000, v138\n\t" \
  "v_and_b32 v138, 0xffff, v139\n\t" \
  "v_and_b32 v139, 0xffff0000, v139\n\t" \
  "v_mfma_f32_32x32x16_bf16 v[0:15], v[242:245], v[240:243], 0\n\t" \
  "v_smfmac_f32_16x16x64_bf16 v[152:155], v[132:135], v[208:215], v234\n\t" \
  "ds_read_b64 v[204:205], v253 offset:33616\n\t" \
  "ds_read_b64 v[86:87], v235 offset:72\n\t" \
  "ds_read_b64 v[92:93], v235 offset:128\n\t" \
  "s_waitcnt lgkmcnt(12)\n\t" \
  "v_smfmac_f32_16x16x64_bf16 v[168:171], v[96:99], v[216:223], v234\n\t" \
  "ds_read_b64 v[206:207], v253 offset:34016\n\t" \
  "ds_read_b64 v[94:95], v235 offset:136\n\t" \
  "v_and_b32 v108, 0xffff, v110\n\t" \
  "v_and_b32 v109, 0xffff0000, v110\n\t" \
  "v_and_b32 v110, 0xffff, v111\n\t" \
  "v_and_b32 v111, 0xffff0000, v111\n\t" \
  "v_add_u32 v235, s58, v235\n\t" \
  "s_add_u32 m0, s25, 16400\n\t" \
  "s_add_u32 s24, s24, s26\n\t" \
  "v_smfmac_f32_16x16x64_bf16 v[156:159], v[100:103], v[216:223], v234\n\t" \
  "buffer_load_dwordx4 v254, s[8:11], s24 offen lds\n\t" \
  "v_smfmac_f32_16x16x64_bf16 v[144:147], v[104:107], v[216:223], v234\n\t" \
  "s_waitcnt vmcnt(7) lgkmcnt(0)\n\t" \
  "s_barrier\n\t" \
  "v_smfmac_f32_16x16x64_bf16 v[172:175], v[112:115], v[224:231], v234\n\t" \
  "ds_read_b64 v[208:209], v232 offset:0\n\t" \
  "ds_read_b64 v[210:211], v232 offset:400\n\t" \
  "v_and_b32 v124, 0xffff, v126\n\t" \
  "v_and_b32 v125, 0xffff0000, v126\n\t" \
  "v_and_b32 v126, 0xffff, v127\n\t" \
  "v_and_b32 v127, 0xffff0000, v127\n\t" \
  "s_add_u32 m0, s25, 24600\n\t" \
  "s_add_u32 s24, s24, s26\n\t" \
  "v_smfmac_f32_16x16x64_bf16 v[160:163], v[116:119], v[224:231], v234\n\t" \
  "buffer_load_dwordx4 v254, s[8:11], s24 offen lds\n\t" \
  "ds_read_b64 v[212:213], v232 offset:800\n\t" \
  "ds_read_b64 v[214:215], v232 offset:1200\n\t" \
  "v_smfmac_f32_16x16x64_bf16 v[148:151], v[120:123], v[224:231], v234\n\t" \
  "ds_read_b64 v[216:217], v232 offset:8\n\t" \
  "ds_read_b64 v[218:219], v232 offset:408\n\t" \
  "v_smfmac_f32_16x16x64_bf16 v[176:179], v[128:131], v[200:207], v234\n\t" \
  "ds_read_b64 v[220:221], v232 offset:808\n\t" \
  "ds_read_b64 v[222:223], v232 offset:1208\n\t" \
  "v_and_b32 v140, 0xffff, v142\n\t" \
  "v_and_b32 v141, 0xffff0000, v142\n\t" \
  "v_and_b32 v142, 0xffff, v143\n\t" \
  "v_and_b32 v143, 0xffff0000, v143\n\t" \
  "v_smfmac_f32_16x16x64_bf16 v[164:167], v[132:135], v[200:207], v234\n\t" \
  "ds_read_b64 v[224:225], v232 offset:16\n\t" \
  "s_add_u32 m0, s25, 32800\n\t" \
  "s_add_u32 s24, s24, s26\n\t" \
  "v_smfmac_f32_16x16x64_bf16 v[152:155], v[136:139], v[200:207], v234\n\t" \
  "buffer_load_dwordx4 v254, s[8:11], s24 offen lds\n\t" \
  "ds_read_b64 v[226:227], v232 offset:416\n\t" \
  "s_waitcnt lgkmcnt(6)\n\t" \
  "v_smfmac_f32_16x16x64_bf16 v[180:183], v[96:99], v[208:215], v234\n\t" \
  "ds_read_b64 v[228:229], v232 offset:816\n\t" \
  "v_smfmac_f32_16x16x64_bf16 v[168:171], v[100:103], v[208:215], v234\n\t" \
  "ds_read_b64 v[230:231], v232 offset:1216\n\t" \
  "v_mfma_f32_32x32x16_bf16 v[16:31], v[242:245], v[240:243], 0\n\t" \
  "v_mov_b32 v196, 0x3f003f00\n\t" \
  "v_mov_b32 v197, 0x3f003f00\n\t" \
  "v_smfmac_f32_16x16x64_bf16 v[156:159], v[104:107], v[208:215], v234\n\t" \
  "v_mov_b32 v198, 0x3f003f00\n\t" \
  "v_mov_b32 v199, 0x3f003f00\n\t" \
  "s_add_u32 m0, s25, 41000\n\t" \
  "s_add_u32 s24, s24, s26\n\t" \
  "v_smfmac_f32_16x16x64_bf16 v[144:147], v[108:111], v[208:215], v234\n\t" \
  "buffer_load_dwordx4 v254, s[8:11], s24 offen lds\n\t" \
  "s_waitcnt vmcnt(7) lgkmcnt(0)\n\t" \
  "s_barrier\n\t" \
  "v_smfmac_f32_16x16x64_bf16 v[184:187], v[112:115], v[216:223], v234\n\t" \
  "ds_read_b64 v[200:201], v232 offset:16400\n\t" \
  "ds_read_b64 v[202:203], v232 offset:16800\n\t" \
  "v_smfmac_f32_16x16x64_bf16 v[172:175], v[116:119], v[216:223], v234\n\t" \
  "ds_read_b64 v[204:205], v232 offset:17200\n\t" \
  "ds_read_b64 v[206:207], v232 offset:17600\n\t" \
  "v_smfmac_f32_16x16x64_bf16 v[160:163], v[120:123], v[216:223], v234\n\t" \
  "ds_read_b64 v[208:209], v232 offset:16408\n\t" \
  "v_cvt_pk_bf16_f32 v144, v144, v145\n\t" \
  "v_cvt_pk_bf16_f32 v145, v146, v147\n\t" \
  "s_add_u32 m0, s25, 56880\n\t" \
  "s_add_u32 s24, s24, s26\n\t" \
  "v_smfmac_f32_16x16x64_bf16 v[148:151], v[124:127], v[216:223], v234\n\t" \
  "buffer_load_dwordx4 v254, s[8:11], s24 offen lds\n\t" \
  "ds_read_b64 v[210:211], v232 offset:16808\n\t" \
  "v_smfmac_f32_16x16x64_bf16 v[188:191], v[128:131], v[224:231], v234\n\t" \
  "ds_read_b64 v[212:213], v232 offset:17208\n\t" \
  "v_smfmac_f32_16x16x64_bf16 v[176:179], v[132:135], v[224:231], v234\n\t" \
  "ds_read_b64 v[214:215], v232 offset:17608\n\t" \
  "ds_read_b64 v[72:73], v235 offset:0\n\t" \
  "ds_read_b64 v[74:75], v235 offset:8\n\t" \
  "v_smfmac_f32_16x16x64_bf16 v[164:167], v[136:139], v[224:231], v234\n\t" \
  "ds_read_b64 v[216:217], v232 offset:16416\n\t" \
  "ds_read_b64 v[80:81], v235 offset:64\n\t" \
  "v_cvt_pk_bf16_f32 v148, v148, v149\n\t" \
  "v_cvt_pk_bf16_f32 v149, v150, v151\n\t" \
  "v_smfmac_f32_16x16x64_bf16 v[152:155], v[140:143], v[224:231], v234\n\t" \
  "ds_read_b64 v[218:219], v232 offset:16816\n\t" \
  "ds_read_b64 v[82:83], v235 offset:72\n\t" \
  "s_waitcnt lgkmcnt(10)\n\t" \
  "v_smfmac_f32_16x16x64_bf16 v[0:3], v[96:99], v[200:207], v234\n\t" \
  "ds_read_b64 v[220:221], v232 offset:17216\n\t" \
  "ds_read_b64 v[88:89], v235 offset:128\n\t" \
  "v_smfmac_f32_16x16x64_bf16 v[180:183], v[100:103], v[200:207], v234\n\t" \
  "ds_read_b64 v[222:223], v232 offset:17616\n\t" \
  "ds_read_b64 v[90:91], v235 offset:136\n\t" \
  "v_mfma_f32_32x32x16_bf16 v[32:47], v[242:245], v[240:243], 0\n\t" \
  "v_add_u32 v235, s59, v235\n\t" \
  "v_smfmac_f32_16x16x64_bf16 v[168:171], v[104:107], v[200:207], v234\n\t" \
  "v_cvt_pk_bf16_f32 v152, v152, v153\n\t" \
  "v_cvt_pk_bf16_f32 v153, v154, v155\n\t" \
  "v_smfmac_f32_16x16x64_bf16 v[156:159], v[108:111], v[200:207], v234\n\t" \
  "s_waitcnt vmcnt(6) lgkmcnt(0)\n\t" \
  "s_barrier\n\t" \
  "s_add_u32 m0, s25, 65080\n\t" \
  "s_add_u32 s24, s24, s26\n\t" \
  "v_smfmac_f32_16x16x64_bf16 v[4:7], v[112:115], v[208:215], v234\n\t" \
  "buffer_load_dwordx4 v254, s[8:11], s24 offen lds\n\t" \
  "ds_read_b64 v[224:225], v232 offset:32800\n\t" \
  "ds_read_b64 v[226:227], v232 offset:33200\n\t" \
  "s_add_u32 m0, s25, 73280\n\t" \
  "s_add_u32 s24, s24, s26\n\t" \
  "v_smfmac_f32_16x16x64_bf16 v[184:187], v[116:119], v[208:215], v234\n\t" \
  "buffer_load_dwordx4 v254, s[8:11], s24 offen lds\n\t" \
  "ds_read_b64 v[228:229], v232 offset:33600\n\t" \
  "ds_read_b64 v[230:231], v232 offset:34000\n\t" \
  "s_add_u32 m0, s25, 81480\n\t" \
  "s_add_u32 s24, s24, s26\n\t" \
  "v_smfmac_f32_16x16x64_bf16 v[172:175], v[120:123], v[208:215], v234\n\t" \
  "buffer_load_dwordx4 v254, s[8:11], s24 offen lds\n\t" \
  "ds_read_b64 v[200:201], v232 offset:32808\n\t" \
  "v_cvt_pk_bf16_f32 v156, v156, v157\n\t" \
  "v_cvt_pk_bf16_f32 v157, v158, v159\n\t" \
  "v_smfmac_f32_16x16x64_bf16 v[160:163], v[124:127], v[208:215], v234\n\t" \
  "ds_read_b64 v[202:203], v232 offset:33208\n\t" \
  "v_smfmac_f32_16x16x64_bf16 v[8:11], v[128:131], v[216:223], v234\n\t" \
  "ds_read_b64 v[204:205], v232 offset:33608\n\t" \
  "ds_read_b64 v[48:49], v235 offset:0\n\t" \
  "v_smfmac_f32_16x16x64_bf16 v[188:191], v[132:135], v[216:223], v234\n\t" \
  "ds_read_b64 v[206:207], v232 offset:34008\n\t" \
  "ds_read_b64 v[50:51], v235 offset:8\n\t" \
  "v_smfmac_f32_16x16x64_bf16 v[176:179], v[136:139], v[216:223], v234\n\t" \
  "ds_read_b64 v[208:209], v232 offset:32816\n\t" \
  "ds_read_b64 v[56:57], v235 offset:64\n\t" \
  "v_cvt_pk_bf16_f32 v160, v160, v161\n\t" \
  "v_cvt_pk_bf16_f32 v161, v162, v163\n\t" \
  "v_smfmac_f32_16x16x64_bf16 v[164:167], v[140:143], v[216:223], v234\n\t" \
  "ds_read_b64 v[210:211], v232 offset:33216\n\t" \
  "ds_read_b64 v[58:59], v235 offset:72\n\t" \
  "s_waitcnt lgkmcnt(10)\n\t" \
  "v_smfmac_f32_16x16x64_bf16 v[12:15], v[96:99], v[224:231], v234\n\t" \
  "ds_read_b64 v[212:213], v232 offset:33616\n\t" \
  "ds_read_b64 v[64:65], v235 offset:128\n\t" \
  "v_smfmac_f32_16x16x64_bf16 v[0:3], v[100:103], v[224:231], v234\n\t" \
  "ds_read_b64 v[214:215], v232 offset:34016\n\t" \
  "ds_read_b64 v[66:67], v235 offset:136\n\t" \
  "v_smfmac_f32_16x16x64_bf16 v[180:183], v[104:107], v[224:231], v234\n\t" \
  "v_cvt_pk_bf16_f32 v164, v164, v165\n\t" \
  "v_cvt_pk_bf16_f32 v165, v166, v167\n\t" \
  "v_smfmac_f32_16x16x64_bf16 v[168:171], v[108:111], v[224:231], v234\n\t" \
  "s_waitcnt vmcnt(7) lgkmcnt(0)\n\t" \
  "s_barrier\n\t" \
  "v_smfmac_f32_16x16x64_bf16 v[16:19], v[112:115], v[200:207], v234\n\t" \
  "ds_write_b64 v193, v[144:145] offset:12288\n\t" \
  "ds_read_b64 v[216:217], v253 offset:0\n\t" \
  "v_smfmac_f32_16x16x64_bf16 v[4:7], v[116:119], v[200:207], v234\n\t" \
  "ds_write_b64 v193, v[148:149] offset:16384\n\t" \
  "ds_read_b64 v[218:219], v253 offset:400\n\t" \
  "v_smfmac_f32_16x16x64_bf16 v[184:187], v[120:123], v[200:207], v234\n\t" \
  "ds_write_b64 v193, v[152:153] offset:20480\n\t" \
  "ds_read_b64 v[220:221], v253 offset:800\n\t" \
  "v_cvt_pk_bf16_f32 v168, v168, v169\n\t" \
  "v_cvt_pk_bf16_f32 v169, v170, v171\n\t" \
  "v_smfmac_f32_16x16x64_bf16 v[172:175], v[124:127], v[200:207], v234\n\t" \
  "ds_write_b64 v193, v[156:157] offset:0\n\t" \
  "ds_read_b64 v[222:223], v253 offset:1200\n\t" \
  "v_smfmac_f32_16x16x64_bf16 v[20:23], v[128:131], v[208:215], v234\n\t" \
  "ds_write_b64 v193, v[160:161] offset:4096\n\t" \
  "ds_read_b64 v[224:225], v253 offset:8\n\t" \
  "v_smfmac_f32_16x16x64_bf16 v[8:11], v[132:135], v[208:215], v234\n\t" \
  "ds_write_b64 v193, v[164:165] offset:8192\n\t" \
  "ds_read_b64 v[226:227], v253 offset:408\n\t" \
  "s_mov_b64 exec, s[68:69]\n\t" \
  "ds_read_b64 v[48:49], v235 offset:0\n\t" \
  "ds_read_b64 v[50:51], v235 offset:8\n\t" \
  "ds_read_b64 v[56:57], v235 offset:64\n\t" \
  "s_mov_b64 exec, -1\n\t" \
  "v_smfmac_f32_16x16x64_bf16 v[188:191], v[136:139], v[208:215], v234\n\t" \
  "ds_read_b64 v[228:229], v253 offset:808\n\t" \
  "ds_read_b64 v[230:231], v253 offset:1208\n\t" \
  "v_cvt_pk_bf16_f32 v172, v172, v173\n\t" \
  "v_cvt_pk_bf16_f32 v173, v174, v175\n\t" \
  "v_smfmac_f32_16x16x64_bf16 v[176:179], v[140:143], v[208:215], v234\n\t" \
  "ds_read_b64 v[200:201], v253 offset:16\n\t" \
  "ds_read_b64 v[202:203], v253 offset:416\n\t" \
  "s_waitcnt lgkmcnt(11)\n\t" \
  "v_smfmac_f32_16x16x64_bf16 v[24:27], v[96:99], v[216:223], v234\n\t" \
  "ds_read_b64 v[204:205], v253 offset:816\n\t" \
  "s_mov_b64 exec, s[68:69]\n\t" \
  "ds_read_b64 v[58:59], v235 offset:72\n\t" \
  "ds_read_b64 v[64:65], v235 offset:128\n\t" \
  "ds_read_b64 v[66:67], v235 offset:136\n\t" \
  "s_mov_b64 exec, -1\n\t" \
  "v_add_u32 v235, s60, v235\n\t" \
  "v_smfmac_f32_16x16x64_bf16 v[12:15], v[100:103], v[216:223], v234\n\t" \
  "ds_read_b64 v[206:207], v253 offset:1216\n\t" \
  "v_smfmac_f32_16x16x64_bf16 v[0:3], v[104:107], v[216:223], v234\n\t" \
  "v_cvt_pk_bf16_f32 v176, v176, v177\n\t" \
  "v_cvt_pk_bf16_f32 v177, v178, v179\n\t" \
  "v_smfmac_f32_16x16x64_bf16 v[180:183], v[108:111], v[216:223], v234\n\t" \
  "s_waitcnt vmcnt(6) lgkmcnt(0)\n\t" \
  "s_barrier\n\t" \
  "v_smfmac_f32_16x16x64_bf16 v[28:31], v[112:115], v[224:231], v234\n\t" \
  "ds_write_b64 v192, v[168:169] offset:0\n\t" \
  "ds_read_b64 v[208:209], v253 offset:16400\n\t" \
  "ds_read_b128 v[144:147], v194 offset:0\n\t" \
  "v_smfmac_f32_16x16x64_bf16 v[16:19], v[116:119], v[224:231], v234\n\t" \
  "ds_write_b64 v192, v[172:173] offset:4096\n\t" \
  "ds_read_b64 v[210:211], v253 offset:16800\n\t" \
  "ds_read_b128 v[148:151], v194 offset:4096\n\t" \
  "v_smfmac_f32_16x16x64_bf16 v[4:7], v[120:123], v[224:231], v234\n\t" \
  "ds_write_b64 v192, v[176:177] offset:8192\n\t" \
  "ds_read_b64 v[212:213], v253 offset:17200\n\t" \
  "v_cvt_pk_bf16_f32 v180, v180, v181\n\t" \
  "v_cvt_pk_bf16_f32 v181, v182, v183\n\t" \
  "ds_read_b128 v[152:155], v194 offset:8192\n\t" \
  "v_smfmac_f32_16x16x64_bf16 v[184:187], v[124:127], v[224:231], v234\n\t" \
  "ds_read_b64 v[214:215], v253 offset:17600\n\t" \
  "ds_read_b64 v[216:217], v253 offset:16408\n\t" \
  "v_add_u32 v194, -93792, v194\n\t" \
  "v_smfmac_f32_16x16x64_bf16 v[32:35], v[128:131], v[200:207], v234\n\t" \
  "ds_read_b64 v[218:219], v253 offset:16808\n\t" \
  "ds_read_b64 v[220:221], v253 offset:17208\n\t" \
  "v_smfmac_f32_16x16x64_bf16 v[20:23], v[132:135], v[200:207], v234\n\t" \
  "ds_read_b64 v[222:223], v253 offset:17608\n\t" \
  "s_waitcnt lgkmcnt(11)\n\t" \
  "v_mfma_f32_16x16x32_bf16 v[168:171], v[144:147], v[144:147], 0\n\t" \
  "ds_read_b64 v[224:225], v253 offset:16416\n\t" \
  "v_smfmac_f32_16x16x64_bf16 v[8:11], v[136:139], v[200:207], v234\n\t" \
  "ds_read_b64 v[226:227], v253 offset:16816\n\t" \
  "v_cvt_pk_bf16_f32 v184, v184, v185\n\t" \
  "v_cvt_pk_bf16_f32 v185, v186, v187\n\t" \
  "v_mfma_f32_16x16x32_bf16 v[172:175], v[196:199], v[144:147], 0\n\t" \
  "ds_read_b64 v[228:229], v253 offset:17216\n\t" \
  "v_smfmac_f32_16x16x64_bf16 v[188:191], v[140:143], v[200:207], v234\n\t" \
  "ds_read_b64 v[230:231], v253 offset:17616\n\t" \
  "s_waitcnt lgkmcnt(12)\n\t" \
  "v_mfma_f32_16x16x32_bf16 v[168:171], v[148:151], v[148:151], v[168:171]\n\t" \
  "s_waitcnt lgkmcnt(8)\n\t" \
  "v_smfmac_f32_16x16x64_bf16 v[36:39], v[96:99], v[208:215], v234\n\t" \
  "v_mfma_f32_16x16x32_bf16 v[172:175], v[196:199], v[148:151], v[172:175]\n\t" \
  "v_smfmac_f32_16x16x64_bf16 v[24:27], v[100:103], v[208:215], v234\n\t" \
  "v_mfma_f32_16x16x32_bf16 v[168:171], v[152:155], v[152:155], v[168:171]\n\t" \
  "v_smfmac_f32_16x16x64_bf16 v[12:15], v[104:107], v[208:215], v234\n\t" \
  "v_cvt_pk_bf16_f32 v188, v188, v189\n\t" \
  "v_cvt_pk_bf16_f32 v189, v190, v191\n\t" \
  "v_mfma_f32_16x16x32_bf16 v[172:175], v[196:199], v[152:155], v[172:175]\n\t" \
  "v_smfmac_f32_16x16x64_bf16 v[0:3], v[108:111], v[208:215], v234\n\t" \
  "s_waitcnt vmcnt(3) lgkmcnt(0)\n\t" \
  "s_barrier\n\t" \
  "v_lshl_add_u32 v238, v233, 1, s63\n\t" \
  "s_lshl1_add_u32 m0, s39, 10016\n\t" \
  "v_add_u32 v239, 0x2000, v238\n\t" \
  "global_load_lds_dwordx4 v238, s[6:7]\n\t" \
  "s_add_u32 m0, m0, 66048\n\t" \
  "s_cmp_eq_u32 s39, 3584\n\t" \
  "s_cselect_b32 exec_hi, 0, -1\n\t" \
  "global_load_lds_dwordx4 v239, s[6:7]\n\t" \
  "s_mov_b64 exec, -1\n\t" \
  "v_smfmac_f32_16x16x64_bf16 v[40:43], v[112:115], v[216:223], v234\n\t" \
  "ds_write_b64 v192, v[180:181] offset:12288\n\t" \
  "ds_read_b64 v[200:201], v253 offset:32800\n\t" \
  "v_smfmac_f32_16x16x64_bf16 v[28:31], v[116:119], v[216:223], v234\n\t" \
  "ds_write_b64 v192, v[184:185] offset:16384\n\t" \
  "ds_read_b64 v[202:203], v253 offset:33200\n\t" \
  "v_cndmask_b32 v168, v168, v169, s[40:41]\n\t" \
  "v_cndmask_b32 v170, v170, v171, s[40:41]\n\t" \
  "v_smfmac_f32_16x16x64_bf16 v[16:19], v[120:123], v[216:223], v234\n\t" \
  "ds_write_b64 v192, v[188:189] offset:20480\n\t" \
  "ds_read_b64 v[204:205], v253 offset:33600\n\t" \
  "v_cvt_pk_bf16_f32 v0, v0, v1\n\t" \
  "v_cvt_pk_bf16_f32 v1, v2, v3\n\t" \
  "v_cndmask_b32 v173, v168, v170, s[42:43]\n\t" \
  "v_mov_b32 v174, 0x1127c5ac\n\t" \
  "ds_read_b64_tr_b16 v[156:157], v195 offset:0\n\t" \
  "v_smfmac_f32_16x16x64_bf16 v[4:7], v[124:127], v[216:223], v234\n\t" \
  "ds_read_b64 v[206:207], v253 offset:34000\n\t" \
  "ds_read_b64 v[208:209], v253 offset:32808\n\t" \
  "s_not_b64 vcc, s[52:53]\n\t" \
  "ds_read_b64_tr_b16 v[158:159], v195 offset:4096\n\t" \
  "v_smfmac_f32_16x16x64_bf16 v[44:47], v[128:131], v[224:231], v234\n\t" \
  "ds_read_b64 v[210:211], v253 offset:33208\n\t" \
  "ds_read_b64 v[212:213], v253 offset:33608\n\t" \
  "ds_read_b64 v[52:53], v235 offset:0\n\t" \
  "ds_read_b64 v[54:55], v235 offset:8\n\t" \
  "ds_read_b64_tr_b16 v[160:161], v195 offset:2048\n\t" \
  "v_smfmac_f32_16x16x64_bf16 v[32:35], v[132:135], v[224:231], v234\n\t" \
  "ds_read_b64 v[214:215], v253 offset:34008\n\t" \
  "ds_read_b64 v[216:217], v253 offset:32816\n\t" \
  "ds_read_b64 v[60:61], v235 offset:64\n\t" \
  "ds_read_b64_tr_b16 v[162:163], v195 offset:6144\n\t" \
  "v_smfmac_f32_16x16x64_bf16 v[20:23], v[136:139], v[224:231], v234\n\t" \
  "ds_read_b64 v[218:219], v253 offset:33216\n\t" \
  "ds_read_b64 v[62:63], v235 offset:72\n\t" \
  "v_cvt_pk_bf16_f32 v4, v4, v5\n\t" \
  "v_cvt_pk_bf16_f32 v5, v6, v7\n\t" \
  "ds_read_b64_tr_b16 v[164:165], v195 offset:8192\n\t" \
  "v_smfmac_f32_16x16x64_bf16 v[8:11], v[140:143], v[224:231], v234\n\t" \
  "ds_read_b64 v[220:221], v253 offset:33616\n\t" \
  "ds_read_b64 v[68:69], v235 offset:128\n\t" \
  "ds_read_b64_tr_b16 v[166:167], v195 offset:10240\n\t" \
  "s_waitcnt lgkmcnt(15)\n\t" \
  "v_smfmac_f32_16x16x64_bf16 v[36:39], v[100:103], v[200:207], v234\n\t" \
  "ds_read_b64 v[222:223], v253 offset:34016\n\t" \
  "ds_read_b64 v[70:71], v235 offset:136\n\t" \
  "v_add_u32 v195, -93792, v195\n\t" \
  "v_smfmac_f32_16x16x64_bf16 v[24:27], v[104:107], v[200:207], v234\n\t" \
  "v_smfmac_f32_16x16x64_bf16 v[12:15], v[108:111], v[200:207], v234\n\t" \
  "v_cvt_pk_bf16_f32 v8, v8, v9\n\t" \
  "v_cvt_pk_bf16_f32 v9, v10, v11\n\t" \
  "s_waitcnt vmcnt(2) lgkmcnt(0)\n\t" \
  "s_barrier\n\t" \
  "s_cmp_lg_u32 s37, 0\n\t" \
  "s_cbranch_scc1 .Lcnx_c2\n\t" \
  "s_mov_b32 m0, s46\n\t" \
  "s_mov_b32 exec_hi, 0xffff\n\t" \
  "buffer_load_dwordx4 v252, s[16:19], s33 offen lds\n\t" \
  "s_mov_b32 exec_hi, -1\n\t" \
  "s_add_u32 s33, s33, s32\n\t" \
  "s_add_u32 m0, s46, s35\n\t" \
  "s_mov_b32 exec_hi, 0xffff\n\t" \
  "buffer_load_dwordx4 v252, s[16:19], s34 offen lds\n\t" \
  "s_mov_b32 exec_hi, -1\n\t" \
  "s_add_u32 s34, s34, s32\n\t" \
  "s_mov_b32 m0, s47\n\t" \
  "s_mov_b32 exec_hi, 0xffff\n\t" \
  "buffer_load_dwordx4 v252, s[16:19], s33 offen lds\n\t" \
  "s_mov_b32 exec_hi, -1\n\t" \
  "s_add_u32 s33, s33, s32\n\t" \
  "s_add_u32 m0, s47, s35\n\t" \
  "s_mov_b32 exec_hi, 0xffff\n\t" \
  "buffer_load_dwordx4 v252, s[16:19], s34 offen lds\n\t" \
  "s_mov_b32 exec_hi, -1\n\t" \
  "s_add_u32 s34, s34, s32\n\t" \
  ".Lcnx_c2:\n\t" \
  "v_smfmac_f32_16x16x64_bf16 v[40:43], v[116:119], v[208:215], v234\n\t" \
  "ds_write_b64 v192, v[0:1] offset:24576\n\t" \
  "ds_read_b64 v[224:225], v232 offset:0\n\t" \
  "ds_read_b128 v[180:183], v194 offset:0\n\t" \
  "ds_read_b128 v[184:187], v194 offset:4096\n\t" \
  "ds_read_b128 v[188:191], v194 offset:8192\n\t" \
  "v_smfmac_f32_16x16x64_bf16 v[28:31], v[120:123], v[208:215], v234\n\t" \
  "ds_write_b64 v192, v[4:5] offset:28672\n\t" \
  "ds_read_b64 v[226:227], v232 offset:400\n\t" \
  "ds_read_b64 v[228:229], v232 offset:800\n\t" \
  "v_smfmac_f32_16x16x64_bf16 v[16:19], v[124:127], v[208:215], v234\n\t" \
  "ds_write_b64 v192, v[8:9] offset:32768\n\t" \
  "ds_read_b64 v[230:231], v232 offset:1200\n\t" \
  "ds_read_b64 v[200:201], v232 offset:8\n\t" \
  "v_cvt_pk_bf16_f32 v12, v12, v13\n\t" \
  "v_cvt_pk_bf16_f32 v13, v14, v15\n\t" \
  "v_smfmac_f32_16x16x64_bf16 v[44:47], v[132:135], v[216:223], v234\n\t" \
  "ds_write_b64 v192, v[12:13] offset:36864\n\t" \
  "ds_read_b64 v[202:203], v232 offset:408\n\t" \
  "ds_read_b64 v[204:205], v232 offset:808\n\t" \
  "s_waitcnt lgkmcnt(11)\n\t" \
  "v_mfma_f32_16x16x32_bf16 v[176:179], v[180:183], v[180:183], 0\n\t" \
  "ds_read_b64 v[206:207], v232 offset:1208\n\t" \
  "v_mfma_f32_16x16x32_bf16 v[168:171], v[196:199], v[180:183], 0\n\t" \
  "ds_read_b64 v[208:209], v232 offset:16\n\t" \
  "v_smfmac_f32_16x16x64_bf16 v[32:35], v[136:139], v[216:223], v234\n\t" \
  "ds_read_b64 v[210:211], v232 offset:416\n\t" \
  "ds_read_b64 v[212:213], v232 offset:816\n\t" \
  "s_waitcnt lgkmcnt(14)\n\t" \
  "v_mfma_f32_16x16x32_bf16 v[176:179], v[184:187], v[184:187], v[176:179]\n\t" \
  "ds_read_b64 v[214:215], v232 offset:1216\n\t" \
  "v_mfma_f32_16x16x32_bf16 v[168:171], v[196:199], v[184:187], v[168:171]\n\t" \
  "v_smfmac_f32_16x16x64_bf16 v[20:23], v[140:143], v[216:223], v234\n\t" \
  "v_cvt_pk_bf16_f32 v16, v16, v17\n\t" \
  "v_cvt_pk_bf16_f32 v17, v18, v19\n\t" \
  "s_waitcnt lgkmcnt(14)\n\t" \
  "v_mfma_f32_16x16x32_bf16 v[176:179], v[188:191], v[188:191], v[176:179]\n\t" \
  "ds_write_b64 v192, v[16:17] offset:40960\n\t" \
  "v_mfma_f32_16x16x32_bf16 v[168:171], v[196:199], v[188:191], v[168:171]\n\t" \
  "s_waitcnt lgkmcnt(10)\n\t" \
  "v_smfmac_f32_16x16x64_bf16 v[36:39], v[104:107], v[224:231], v234\n\t" \
  "v_mov_b32 v183, 0\n\t" \
  "v_mov_b32 v187, 0\n\t" \
  "s_nop 1\n\t" \
  "v_cndmask_b32 v176, v176, v177, s[40:41]\n\t" \
  "v_cndmask_b32 v178, v178, v179, s[40:41]\n\t" \
  "v_cndmask_b32 v176, v176, v178, s[42:43]\n\t" \
  "v_smfmac_f32_16x16x64_bf16 v[24:27], v[108:111], v[224:231], v234\n\t" \
  "v_cndmask_b32_dpp v172, v168, v172, vcc row_ror:8 row_mask:0xf bank_mask:0xf\n\t" \
  "v_mul_f32 v172, 0x3daaaaab, v172\n\t" \
  "v_cndmask_b32_dpp v173, v176, v173, vcc row_ror:8 row_mask:0xf bank_mask:0xf\n\t" \
  "v_fmamk_f32 v173, v173, 0x3e2aaaab, v174\n\t" \
  "s_waitcnt vmcnt(0) lgkmcnt(0)\n\t" \
  "s_barrier\n\t" \
  "ds_read_b64 v[216:217], v232 offset:16400\n\t" \
  "ds_read_b64 v[218:219], v232 offset:16800\n\t" \
  "ds_read_b64 v[220:221], v232 offset:17200\n\t" \
  "ds_read_b64 v[222:223], v232 offset:17600\n\t" \
  "v_smfmac_f32_16x16x64_bf16 v[40:43], v[120:123], v[200:207], v234\n\t" \
  "ds_read_b64 v[224:225], v232 offset:16408\n\t" \
  "ds_read_b64 v[226:227], v232 offset:16808\n\t" \
  "v_cvt_pk_bf16_f32 v20, v20, v21\n\t" \
  "v_cvt_pk_bf16_f32 v21, v22, v23\n\t" \
  "v_fma_f32 v173, -v172, v172, v173\n\t" \
  "v_max_f32 v173, v174, v173\n\t" \
  "v_rsq_f32 v173, v173 div:2\n\t" \
  "s_mov_b64 exec, s[52:53]\n\tv_mul_f32 v173, 0x08805555, v173\n\ts_mov_b64 exec, -1\n\t" \
  "v_smfmac_f32_16x16x64_bf16 v[28:31], v[124:127], v[200:207], v234\n\t" \
  "ds_write_b64 v192, v[20:21] offset:45056\n\t" \
  "ds_read_b64 v[228:229], v232 offset:17208\n\t" \
  "s_mov_b64 vcc, s[40:41]\n\t" \
  "v_cvt_pk_bf16_f32 v175, v173, v173\n\t" \
  "v_and_b32 v173, 0xffff0000, v175\n\t" \
  "v_mul_f32 v174, -v172, v173 div:2\n\t" \
  "v_cndmask_b32_sdwa v175, v175, v173, vcc dst_sel:DWORD dst_unused:UNUSED_PAD src0_sel:WORD_1 src1_sel:DWORD\n\t" \
  "v_smfmac_f32_16x16x64_bf16 v[44:47], v[136:139], v[208:215], v234\n\t" \
  "ds_read_b64 v[230:231], v232 offset:17608\n\t" \
  "ds_read_b64 v[200:201], v232 offset:16416\n\t" \
  "v_cvt_pk_bf16_f32 v24, v24, v25\n\t" \
  "v_cvt_pk_bf16_f32 v25, v26, v27\n\t" \
  "v_and_b32 v172, 0xffff0000, v174\n\t" \
  "s_or_b64 vcc, s[52:53], s[42:43]\n\t" \
  "v_sub_f32 v174, v174, v172\n\t" \
  "v_cndmask_b32 v180, v175, v240, vcc\n\t" \
  "v_smfmac_f32_16x16x64_bf16 v[32:35], v[140:143], v[208:215], v234\n\t" \
  "ds_write_b64 v192, v[24:25] offset:56880\n\t" \
  "ds_read_b64 v[202:203], v232 offset:16816\n\t" \
  "v_perm_b32 v174, v172, v174, s55\n\t" \
  "v_cndmask_b32_dpp v184, v175, v240, vcc row_ror:8 row_mask:0xf bank_mask:0xf\n\t" \
  "s_mov_b64 vcc, s[52:53]\n\t" \
  "v_cndmask_b32 v182, v174, v240, vcc\n\t" \
  "ds_read_b64_tr_b16 v[144:145], v195 offset:0\n\t" \
  "ds_read_b64_tr_b16 v[146:147], v195 offset:4096\n\t" \
  "s_waitcnt lgkmcnt(10)\n\t" \
  "v_smfmac_f32_16x16x64_bf16 v[36:39], v[108:111], v[216:223], v234\n\t" \
  "ds_read_b64 v[204:205], v232 offset:17216\n\t" \
  "ds_read_b64 v[206:207], v232 offset:17616\n\t" \
  "v_cvt_pk_bf16_f32 v28, v28, v29\n\t" \
  "v_cvt_pk_bf16_f32 v29, v30, v31\n\t" \
  "v_cndmask_b32_dpp v186, v174, v240, vcc row_ror:8 row_mask:0xf bank_mask:0xf\n\t" \
  "s_orn2_b64 vcc, s[52:53], s[42:43]\n\t" \
  "v_cndmask_b32 v181, v175, v240, vcc\n\t" \
  "v_cndmask_b32_dpp v185, v175, v240, vcc row_ror:8 row_mask:0xf bank_mask:0xf\n\t" \
  "ds_read_b64_tr_b16 v[148:149], v195 offset:2048\n\t" \
  "ds_read_b64_tr_b16 v[150:151], v195 offset:6144\n\t" \
  "s_waitcnt lgkmcnt(9)\n\t" \
  "v_smfmac_f32_16x16x64_bf16 v[40:43], v[124:127], v[224:231], v234\n\t" \
  "ds_write_b64 v192, v[28:29] offset:60976\n\t" \
  "ds_read_b64_tr_b16 v[152:153], v195 offset:8192\n\t" \
  "ds_read_b64_tr_b16 v[154:155], v195 offset:10240\n\t" \
  "v_mfma_f32_16x16x16_bf16 v[0:3], v[196:197], v[182:183], 0\n\t" \
  "v_mfma_f32_16x16x16_bf16 v[4:7], v[196:197], v[186:187], 0\n\t" \
  "s_waitcnt lgkmcnt(5)\n\t" \
  "v_smfmac_f32_16x16x64_bf16 v[44:47], v[140:143], v[200:207], v234\n\t" \
  "v_cvt_pk_bf16_f32 v32, v32, v33\n\t" \
  "v_cvt_pk_bf16_f32 v33, v34, v35\n\t" \
  "v_mfma_f32_16x16x16_bf16 v[188:191], v[156:157], v[180:181], v[0:3]\n\t" \
  "ds_write_b64 v192, v[32:33] offset:65072\n\t" \
  "v_mfma_f32_16x16x16_bf16 v[176:179], v[158:159], v[180:181], v[0:3]\n\t" \
  "v_cvt_pk_bf16_f32 v36, v36, v37\n\t" \
  "v_cvt_pk_bf16_f32 v37, v38, v39\n\t" \
  "ds_write_b64 v193, v[36:37] offset:0\n\t" \
  "v_cvt_pk_bf16_f32 v40, v40, v41\n\t" \
  "v_cvt_pk_bf16_f32 v41, v42, v43\n\t" \
  "ds_write_b64 v193, v[40:41] offset:4096\n\t" \
  "v_mfma_f32_16x16x16_bf16 v[168:171], v[160:161], v[180:181], v[0:3]\n\t" \
  "v_cvt_pk_bf16_f32 v96, v188, v189\n\t" \
  "v_cvt_pk_bf16_f32 v97, v190, v191\n\t" \
  "v_mfma_f32_16x16x16_bf16 v[172:175], v[162:163], v[180:181], v[0:3]\n\t" \
  "v_cvt_pk_bf16_f32 v44, v44, v45\n\t" \
  "v_cvt_pk_bf16_f32 v45, v46, v47\n\t" \
  "ds_write_b64 v193, v[44:45] offset:8192\n\t" \
  "v_cvt_pk_bf16_f32 v98, v176, v177\n\t" \
  "v_cvt_pk_bf16_f32 v99, v178, v179\n\t" \
  "v_mfma_f32_16x16x16_bf16 v[188:191], v[164:165], v[180:181], v[0:3]\n\t" \
  "v_cvt_pk_bf16_f32 v112, v168, v169\n\t" \
  "v_cvt_pk_bf16_f32 v113, v170, v171\n\t" \
  "v_mfma_f32_16x16x16_bf16 v[176:179], v[166:167], v[180:181], v[0:3]\n\t" \
  "v_cvt_pk_bf16_f32 v114, v172, v173\n\t" \
  "v_cvt_pk_bf16_f32 v115, v174, v175\n\t" \
  "s_nop 2\n\t" \
  "v_cvt_pk_bf16_f32 v128, v188, v189\n\t" \
  "v_cvt_pk_bf16_f32 v129, v190, v191\n\t" \
  "s_nop 0\n\t" \
  "v_cvt_pk_bf16_f32 v130, v176, v177\n\t" \
  "v_cvt_pk_bf16_f32 v131, v178, v179\n\t" \
  "s_waitcnt vmcnt(0) lgkmcnt(0)\n\t" \
  "s_barrier\n\t" \
  "ds_read_b64_tr_b16 v[208:209], v248 offset:43048\n\t" \
  "ds_read_b64_tr_b16 v[210:211], v248 offset:44072\n\t" \
  "ds_read_b128 v[104:107], v194 offset:24576\n\t" \
  "ds_read_b128 v[120:123], v194 offset:28672\n\t" \
  "ds_read_b128 v[136:139], v194 offset:32768\n\t" \
  "v_add_u32 v194, s54, v194\n\t" \
  "ds_read_b128 v[108:111], v194 offset:49152\n\t" \
  "ds_read_b128 v[124:127], v194 offset:53248\n\t" \
  "ds_read_b128 v[140:143], v194 offset:57344\n\t" \
  "v_mfma_f32_16x16x16_bf16 v[168:171], v[144:145], v[184:185], v[4:7]\n\t" \
  "ds_read_b64_tr_b16 v[212:213], v248 offset:46120\n\t" \
  "ds_read_b64_tr_b16 v[214:215], v248 offset:47144\n\t" \
  "v_mfma_f32_16x16x16_bf16 v[172:175], v[146:147], v[184:185], v[4:7]\n\t" \
  "ds_read_b64_tr_b16 v[216:217], v248 offset:45096\n\t" \
  "ds_read_b64_tr_b16 v[218:219], v248 offset:48168\n\t" \
  "s_mov_b32 m0, s44\n\t" \
  "s_mov_b32 exec_hi, 0xffff\n\t" \
  "buffer_load_dwordx4 v252, s[16:19], s33 offen lds\n\t" \
  "s_mov_b32 exec_hi, -1\n\t" \
  "s_add_u32 s33, s33, s32\n\t" \
  "v_mfma_f32_16x16x16_bf16 v[188:191], v[148:149], v[184:185], v[4:7]\n\t" \
  "ds_read_b64_tr_b16 v[160:161], v248 offset:43560\n\t" \
  "ds_read_b64_tr_b16 v[162:163], v248 offset:44584\n\t" \
  "v_cvt_pk_bf16_f32 v100, v168, v169\n\t" \
  "v_cvt_pk_bf16_f32 v101, v170, v171\n\t" \
  "v_mfma_f32_16x16x16_bf16 v[176:179], v[150:151], v[184:185], v[4:7]\n\t" \
  "ds_read_b64_tr_b16 v[164:165], v248 offset:46632\n\t" \
  "ds_read_b64_tr_b16 v[166:167], v248 offset:47656\n\t" \
  "v_cvt_pk_bf16_f32 v102, v172, v173\n\t" \
  "v_cvt_pk_bf16_f32 v103, v174, v175\n\t" \
  "v_mfma_f32_16x16x16_bf16 v[168:171], v[152:153], v[184:185], v[4:7]\n\t" \
  "ds_read_b64_tr_b16 v[232:233], v248 offset:45608\n\t" \
  "ds_read_b64_tr_b16 v[234:235], v248 offset:48680\n\t" \
  "v_cvt_pk_bf16_f32 v116, v188, v189\n\t" \
  "v_cvt_pk_bf16_f32 v117, v190, v191\n\t" \
  "v_mfma_f32_16x16x16_bf16 v[172:175], v[154:155], v[184:185], v[4:7]\n\t" \
  "ds_read_b64_tr_b16 v[24:25], v195 offset:24576\n\t" \
  "ds_read_b64_tr_b16 v[26:27], v195 offset:28672\n\t" \
  "v_cvt_pk_bf16_f32 v118, v176, v177\n\t" \
  "v_cvt_pk_bf16_f32 v119, v178, v179\n\t" \
  "s_waitcnt lgkmcnt(15)\n\t" \
  "v_mfma_f32_16x16x32_bf16 v[144:147], v[208:211], v[96:99], 0\n\t" \
  "ds_read_b64_tr_b16 v[28:29], v195 offset:26624\n\t" \
  "ds_read_b64_tr_b16 v[30:31], v195 offset:30720\n\t" \
  "v_cvt_pk_bf16_f32 v132, v168, v169\n\t" \
  "v_cvt_pk_bf16_f32 v133, v170, v171\n\t" \
  "s_waitcnt lgkmcnt(12)\n\t" \
  "v_mfma_f32_16x16x32_bf16 v[144:147], v[212:215], v[112:115], v[144:147]\n\t" \
  "ds_read_b64_tr_b16 v[32:33], v195 offset:32768\n\t" \
  "ds_read_b64_tr_b16 v[34:35], v195 offset:34816\n\t" \
  "v_cvt_pk_bf16_f32 v134, v172, v173\n\t" \
  "v_cvt_pk_bf16_f32 v135, v174, v175\n\t" \
  "v_mfma_f32_16x16x32_bf16 v[152:155], v[208:211], v[100:103], 0\n\t" \
  "v_add_u32 v195, s54, v195\n\t" \
  "ds_read_b64_tr_b16 v[36:37], v195 offset:49152\n\t" \
  "ds_read_b64_tr_b16 v[38:39], v195 offset:53248\n\t" \
  "s_waitcnt lgkmcnt(14)\n\t" \
  "v_mfma_f32_16x16x32_bf16 v[144:147], v[216:219], v[128:131], v[144:147]\n\t" \
  "ds_read_b64_tr_b16 v[40:41], v195 offset:51200\n\t" \
  "s_add_u32 m0, s44, s35\n\t" \
  "s_mov_b32 exec_hi, 0xffff\n\t" \
  "buffer_load_dwordx4 v252, s[16:19], s34 offen lds\n\t" \
  "s_mov_b32 exec_hi, -1\n\t" \
  "s_add_u32 s34, s34, s32\n\t" \
  "v_mfma_f32_16x16x32_bf16 v[152:155], v[212:215], v[116:119], v[152:155]\n\t" \
  "ds_read_b64_tr_b16 v[42:43], v195 offset:55296\n\t" \
  "v_mfma_f32_16x16x32_bf16 v[152:155], v[216:219], v[132:135], v[152:155]\n\t" \
  "ds_read_b64_tr_b16 v[44:45], v195 offset:57344\n\t" \
  "v_mfma_f32_16x16x32_bf16 v[8:11], v[104:107], v[104:107], 0\n\t" \
  "ds_read_b64_tr_b16 v[46:47], v195 offset:59392\n\t" \
  "s_mov_b32 m0, s45\n\t" \
  "s_mov_b32 exec_hi, 0xffff\n\t" \
  "buffer_load_dwordx4 v252, s[16:19], s33 offen lds\n\t" \
  "s_mov_b32 exec_hi, -1\n\t" \
  "s_add_u32 s33, s33, s32\n\t" \
  "v_mfma_f32_16x16x32_bf16 v[12:15], v[196:199], v[104:107], 0\n\t" \
  "v_cvt_pknorm_i16_f32 v220, |v146|, |v147|\n\t" \
  "v_cvt_pknorm_i16_f32 v222, |v144|, |v145|\n\t" \
  "v_mfma_f32_16x16x32_bf16 v[8:11], v[120:123], v[120:123], v[8:11]\n\t" \
  "v_lshlrev_b32_sdwa v221, 2, v220 dst_sel:DWORD dst_unused:UNUSED_PAD src0_sel:DWORD src1_sel:BYTE_3\n\t" \
  "v_lshlrev_b32_sdwa v220, 2, v220 dst_sel:DWORD dst_unused:UNUSED_PAD src0_sel:DWORD src1_sel:BYTE_1\n\t" \
  "v_mfma_f32_16x16x32_bf16 v[12:15], v[196:199], v[120:123], v[12:15]\n\t" \
  "ds_read_b32 v221, v221 offset:10016\n\t" \
  "v_lshlrev_b32_sdwa v223, 2, v222 dst_sel:DWORD dst_unused:UNUSED_PAD src0_sel:DWORD src1_sel:BYTE_3\n\t" \
  "v_lshlrev_b32_sdwa v222, 2, v222 dst_sel:DWORD dst_unused:UNUSED_PAD src0_sel:DWORD src1_sel:BYTE_1\n\t" \
  "v_mfma_f32_16x16x32_bf16 v[8:11], v[136:139], v[136:139], v[8:11]\n\t" \
  "ds_read_b32 v220, v220 offset:10016\n\t" \
  "v_lshlrev_b32_sdwa v225, 1, v155 dst_sel:DWORD dst_unused:UNUSED_PAD src0_sel:DWORD src1_sel:WORD_1\n\t" \
  "v_lshlrev_b32_sdwa v224, 1, v154 dst_sel:DWORD dst_unused:UNUSED_PAD src0_sel:DWORD src1_sel:WORD_1\n\t" \
  "v_mfma_f32_16x16x32_bf16 v[12:15], v[196:199], v[136:139], v[12:15]\n\t" \
  "ds_read_b32 v223, v223 offset:10016\n\t" \
  "v_lshlrev_b32_sdwa v227, 1, v153 dst_sel:DWORD dst_unused:UNUSED_PAD src0_sel:DWORD src1_sel:WORD_1\n\t" \
  "v_lshlrev_b32_sdwa v226, 1, v152 dst_sel:DWORD dst_unused:UNUSED_PAD src0_sel:DWORD src1_sel:WORD_1\n\t" \
  "v_mfma_f32_16x16x32_bf16 v[16:19], v[108:111], v[108:111], 0\n\t" \
  "ds_read_b32 v222, v222 offset:10016\n\t" \
  "v_mfma_f32_16x16x32_bf16 v[20:23], v[196:199], v[108:111], 0\n\t" \
  "ds_read_u16_d16_hi v225, v225 offset:10528\n\t" \
  "s_add_u32 m0, s45, s35\n\t" \
  "s_mov_b32 exec_hi, 0xffff\n\t" \
  "buffer_load_dwordx4 v252, s[16:19], s34 offen lds\n\t" \
  "s_mov_b32 exec_hi, -1\n\t" \
  "s_add_u32 s34, s34, s32\n\t" \
  "v_cndmask_b32 v8, v8, v9, s[40:41]\n\t" \
  "v_cndmask_b32 v10, v10, v11, s[40:41]\n\t" \
  "v_mfma_f32_16x16x32_bf16 v[16:19], v[124:127], v[124:127], v[16:19]\n\t" \
  "ds_read_u16 v224, v224 offset:10528\n\t" \
  "v_cndmask_b32 v13, v8, v10, s[42:43]\n\t" \
  "v_mov_b32 v14, 0x1127c5ac\n\t" \
  "v_mfma_f32_16x16x32_bf16 v[20:23], v[196:199], v[124:127], v[20:23]\n\t" \
  "ds_read_u16_d16_hi v227, v227 offset:10528\n\t" \
  "s_not_b64 vcc, s[52:53]\n\t" \
  "v_mfma_f32_16x16x32_bf16 v[16:19], v[140:143], v[140:143], v[16:19]\n\t" \
  "ds_read_u16 v226, v226 offset:10528\n\t" \
  "s_waitcnt lgkmcnt(4)\n\t" \
  "v_fma_mix_f32 v221, |v147|, v221, v221 op_sel:[0,1,0] op_sel_hi:[0,1,1] clamp\n\t" \
  "v_fma_mix_f32 v220, |v146|, v220, v220 op_sel:[0,1,0] op_sel_hi:[0,1,1] clamp\n\t" \
  "v_mfma_f32_16x16x32_bf16 v[20:23], v[196:199], v[140:143], v[20:23]\n\t" \
  "v_fma_mix_f32 v223, |v145|, v223, v223 op_sel:[0,1,0] op_sel_hi:[0,1,1] clamp\n\t" \
  "v_fma_mix_f32 v222, |v144|, v222, v222 op_sel:[0,1,0] op_sel_hi:[0,1,1] clamp\n\t" \
  "v_mfma_f32_16x16x32_bf16 v[148:151], v[160:163], v[96:99], 0\n\t" \
  "s_waitcnt lgkmcnt(0)\n\t" \
  "v_or_b32 v155, v224, v225\n\t" \
  "v_or_b32 v154, v226, v227\n\t" \
  "v_mfma_f32_16x16x32_bf16 v[156:159], v[160:163], v[100:103], 0\n\t" \
  "v_cndmask_b32 v16, v16, v17, s[40:41]\n\t" \
  "v_cndmask_b32 v18, v18, v19, s[40:41]\n\t" \
  "v_cndmask_b32 v16, v16, v18, s[42:43]\n\t" \
  "v_cndmask_b32_dpp v12, v20, v12, vcc row_ror:8 row_mask:0xf bank_mask:0xf\n\t" \
  "v_mul_f32 v12, 0x3daaaaab, v12\n\t" \
  "v_mfma_f32_16x16x32_bf16 v[148:151], v[164:167], v[112:115], v[148:151]\n\t" \
  "v_cndmask_b32_dpp v13, v16, v13, vcc row_ror:8 row_mask:0xf bank_mask:0xf\n\t" \
  "v_fmamk_f32 v13, v13, 0x3e2aaaab, v14\n\t" \
  "v_fma_f32 v13, -v12, v12, v13\n\t" \
  "v_max_f32 v13, v14, v13\n\t" \
  "v_rsq_f32 v13, v13 div:2\n\t" \
  "v_mfma_f32_16x16x32_bf16 v[156:159], v[164:167], v[116:119], v[156:159]\n\t" \
  "s_mov_b64 exec, s[52:53]\n\tv_mul_f32 v13, 0x08805555, v13\n\ts_mov_b64 exec, -1\n\t" \
  "s_mov_b64 vcc, s[40:41]\n\t" \
  "v_cvt_pk_bf16_f32 v15, v13, v13\n\t" \
  "v_and_b32 v13, 0xffff0000, v15\n\t" \
  "v_mul_f32 v14, -v12, v13 div:2\n\t" \
  "v_cndmask_b32_sdwa v15, v15, v13, vcc dst_sel:DWORD dst_unused:UNUSED_PAD src0_sel:WORD_1 src1_sel:DWORD\n\t" \
  "v_and_b32 v12, 0xffff0000, v14\n\t" \
  "v_mfma_f32_16x16x32_bf16 v[148:151], v[232:235], v[128:131], v[148:151]\n\t" \
  "s_or_b64 vcc, s[52:53], s[42:43]\n\t" \
  "v_sub_f32 v14, v14, v12\n\t" \
  "v_cndmask_b32 v180, v15, v240, vcc\n\t" \
  "v_perm_b32 v14, v12, v14, s55\n\t" \
  "v_cndmask_b32_dpp v184, v15, v240, vcc row_ror:8 row_mask:0xf bank_mask:0xf\n\t" \
  "v_mfma_f32_16x16x32_bf16 v[156:159], v[232:235], v[132:135], v[156:159]\n\t" \
  "s_mov_b64 vcc, s[52:53]\n\t" \
  "v_cndmask_b32 v182, v14, v240, vcc\n\t" \
  "v_cndmask_b32_dpp v186, v14, v240, vcc row_ror:8 row_mask:0xf bank_mask:0xf\n\t" \
  "s_orn2_b64 vcc, s[52:53], s[42:43]\n\t" \
  "v_cndmask_b32 v181, v15, v240, vcc\n\t" \
  "v_mfma_f32_16x16x16_bf16 v[0:3], v[196:197], v[182:183], 0\n\t" \
  "v_cndmask_b32_dpp v185, v15, v240, vcc row_ror:8 row_mask:0xf bank_mask:0xf\n\t" \
  "s_mov_b64 vcc, 0\n\t" \
  "v_fma_f32 v147, |v147|, v221, v147 mul:4\n\t" \
  "v_mfma_f32_16x16x16_bf16 v[4:7], v[196:197], v[186:187], 0\n\t" \
  "v_fma_f32 v146, |v146|, v220, v146 mul:4\n\t" \
  "v_fma_f32 v145, |v145|, v223, v145 mul:4\n\t" \
  "v_mfma_f32_16x16x16_bf16 v[188:191], v[24:25], v[180:181], v[0:3]\n\t" \
  "v_fma_f32 v144, |v144|, v222, v144 mul:4\n\t" \
  "v_cvt_pk_bf16_f32 v147, v146, v147\n\t" \
  "v_mfma_f32_16x16x16_bf16 v[176:179], v[26:27], v[180:181], v[0:3]\n\t" \
  "v_cvt_pk_bf16_f32 v146, v144, v145\n\t" \
  "v_cvt_pknorm_i16_f32 v228, |v148|, |v149|\n\t" \
  "v_mfma_f32_16x16x16_bf16 v[200:203], v[36:37], v[184:185], v[4:7]\n\t" \
  "s_nop 1\n\t" \
  "v_cvt_pk_bf16_f32 v104, v188, v189\n\t" \
  "v_cvt_pk_bf16_f32 v105, v190, v191\n\t" \
  "v_mfma_f32_16x16x16_bf16 v[204:207], v[38:39], v[184:185], v[4:7]\n\t" \
  "v_cvt_pk_bf16_f32 v106, v176, v177\n\t" \
  "v_cvt_pk_bf16_f32 v107, v178, v179\n\t" \
  "v_mfma_f32_16x16x16_bf16 v[196:199], v[28:29], v[180:181], v[0:3]\n\t" \
  "v_cvt_pk_bf16_f32 v108, v200, v201\n\t" \
  "v_cvt_pk_bf16_f32 v109, v202, v203\n\t" \
  "v_mfma_f32_16x16x16_bf16 v[188:191], v[30:31], v[180:181], v[0:3]\n\t" \
  "s_nop 1\n\t" \
  "v_cvt_pk_bf16_f32 v110, v204, v205\n\t" \
  "v_cvt_pk_bf16_f32 v111, v206, v207\n\t" \
  "v_mfma_f32_16x16x32_bf16 v[160:163], v[208:211], v[104:107], 0\n\t" \
  "v_cvt_pk_bf16_f32 v120, v196, v197\n\t" \
  "v_cvt_pk_bf16_f32 v121, v198, v199\n\t" \
  "v_mfma_f32_16x16x16_bf16 v[176:179], v[40:41], v[184:185], v[4:7]\n\t" \
  "v_cvt_pk_bf16_f32 v122, v188, v189\n\t" \
  "v_cvt_pk_bf16_f32 v123, v190, v191\n\t" \
  "v_mfma_f32_16x16x16_bf16 v[200:203], v[42:43], v[184:185], v[4:7]\n\t" \
  "v_cvt_pknorm_i16_f32 v230, |v150|, |v151|\n\t" \
  "v_lshlrev_b32_sdwa v229, 2, v228 dst_sel:DWORD dst_unused:UNUSED_PAD src0_sel:DWORD src1_sel:BYTE_3\n\t" \
  "v_mfma_f32_16x16x32_bf16 v[168:171], v[208:211], v[108:111], 0\n\t" \
  "ds_read_b64_tr_b16 v[208:209], v248 offset:43560\n\t" \
  "s_nop 0\n\t" \
  "v_cvt_pk_bf16_f32 v124, v176, v177\n\t" \
  "v_cvt_pk_bf16_f32 v125, v178, v179\n\t" \
  "v_mfma_f32_16x16x16_bf16 v[204:207], v[32:33], v[180:181], v[0:3]\n\t" \
  "ds_read_b64_tr_b16 v[210:211], v248 offset:44584\n\t" \
  "v_cvt_pk_bf16_f32 v126, v200, v201\n\t" \
  "v_cvt_pk_bf16_f32 v127, v202, v203\n\t" \
  "v_mfma_f32_16x16x16_bf16 v[196:199], v[34:35], v[180:181], v[0:3]\n\t" \
  "v_lshlrev_b32_sdwa v228, 2, v228 dst_sel:DWORD dst_unused:UNUSED_PAD src0_sel:DWORD src1_sel:BYTE_1\n\t" \
  "v_lshlrev_b32_sdwa v231, 2, v230 dst_sel:DWORD dst_unused:UNUSED_PAD src0_sel:DWORD src1_sel:BYTE_3\n\t" \
  "v_mfma_f32_16x16x32_bf16 v[160:163], v[212:215], v[120:123], v[160:163]\n\t" \
  "ds_read_b32 v229, v229 offset:10016\n\t" \
  "v_cvt_pk_bf16_f32 v136, v204, v205\n\t" \
  "v_cvt_pk_bf16_f32 v137, v206, v207\n\t" \
  "v_mfma_f32_16x16x16_bf16 v[188:191], v[44:45], v[184:185], v[4:7]\n\t" \
  "ds_read_b32 v228, v228 offset:10016\n\t" \
  "v_cvt_pk_bf16_f32 v138, v196, v197\n\t" \
  "v_cvt_pk_bf16_f32 v139, v198, v199\n\t" \
  "v_mfma_f32_16x16x16_bf16 v[176:179], v[46:47], v[184:185], v[4:7]\n\t" \
  "v_lshlrev_b32_sdwa v230, 2, v230 dst_sel:DWORD dst_unused:UNUSED_PAD src0_sel:DWORD src1_sel:BYTE_1\n\t" \
  "v_lshlrev_b32_sdwa v233, 1, v157 dst_sel:DWORD dst_unused:UNUSED_PAD src0_sel:DWORD src1_sel:WORD_1\n\t" \
  "v_mfma_f32_16x16x32_bf16 v[168:171], v[212:215], v[124:127], v[168:171]\n\t" \
  "ds_read_b64_tr_b16 v[212:213], v248 offset:46632\n\t" \
  "v_cvt_pk_bf16_f32 v140, v188, v189\n\t" \
  "v_cvt_pk_bf16_f32 v141, v190, v191\n\t" \
  "v_mfma_f32_16x16x32_bf16 v[160:163], v[216:219], v[136:139], v[160:163]\n\t" \
  "ds_read_b64_tr_b16 v[214:215], v248 offset:47656\n\t" \
  "v_cvt_pk_bf16_f32 v142, v176, v177\n\t" \
  "v_cvt_pk_bf16_f32 v143, v178, v179\n\t" \
  "s_nop 1\n\t" \
  "v_mfma_f32_16x16x32_bf16 v[168:171], v[216:219], v[140:143], v[168:171]\n\t" \
  "ds_read_b64_tr_b16 v[216:217], v248 offset:45608\n\t" \
  "v_lshlrev_b32_sdwa v232, 1, v156 dst_sel:DWORD dst_unused:UNUSED_PAD src0_sel:DWORD src1_sel:WORD_1\n\t" \
  "v_lshlrev_b32_sdwa v235, 1, v159 dst_sel:DWORD dst_unused:UNUSED_PAD src0_sel:DWORD src1_sel:WORD_1\n\t" \
  "s_waitcnt lgkmcnt(5)\n\t" \
  "ds_read_b64_tr_b16 v[218:219], v248 offset:48680\n\t" \
  "s_waitcnt vmcnt(2)\n\t" \
  "s_mov_b64 exec, 3\n\t" \
  "ds_add_rtn_u32 v246, v251, v250 offset:47136\n\t" \
  "s_mov_b64 exec, -1\n\t" \
  "v_mfma_f32_16x16x32_bf16 v[164:167], v[208:211], v[104:107], 0\n\t" \
  "ds_read_b32 v231, v231 offset:10016\n\t" \
  "v_lshlrev_b32_sdwa v234, 1, v158 dst_sel:DWORD dst_unused:UNUSED_PAD src0_sel:DWORD src1_sel:WORD_1\n\t" \
  "v_mfma_f32_16x16x32_bf16 v[172:175], v[208:211], v[108:111], 0\n\t" \
  "ds_read_b64_tr_b16 v[208:209], v248 offset:49192\n\t" \
  "ds_read_b64_tr_b16 v[210:211], v248 offset:50216\n\t" \
  "s_waitcnt lgkmcnt(6)\n\t" \
  "v_mfma_f32_16x16x32_bf16 v[164:167], v[212:215], v[120:123], v[164:167]\n\t" \
  "ds_read_b32 v230, v230 offset:10016\n\t" \
  "v_mfma_f32_16x16x32_bf16 v[172:175], v[212:215], v[124:127], v[172:175]\n\t" \
  "ds_read_b64_tr_b16 v[212:213], v248 offset:52264\n\t" \
  "ds_read_b64_tr_b16 v[214:215], v248 offset:53288\n\t" \
  "s_waitcnt lgkmcnt(7)\n\t" \
  "v_mfma_f32_16x16x32_bf16 v[164:167], v[216:219], v[136:139], v[164:167]\n\t" \
  "ds_read_u16_d16_hi v233, v233 offset:10528\n\t" \
  "v_mfma_f32_16x16x32_bf16 v[172:175], v[216:219], v[140:143], v[172:175]\n\t" \
  "ds_read_b64_tr_b16 v[216:217], v248 offset:51240\n\t" \
  "v_mfma_f32_4x4x4_16b_bf16 v[0:3], v[242:243], v[76:77], 0\n\t" \
  "v_mfma_f32_4x4x4_16b_bf16 v[4:7], v[242:243], v[78:79], 0\n\t" \
  "v_cvt_pknorm_i16_f32 v220, |v162|, |v163|\n\t" \
  "v_cvt_pknorm_i16_f32 v222, |v160|, |v161|\n\t" \
  "s_setprio 0\n\t" \
  "ds_read_b64_tr_b16 v[218:219], v248 offset:54312\n\t" \
  "v_mfma_f32_4x4x4_16b_bf16 v[8:11], v[242:243], v[84:85], 0\n\t" \
  "v_mfma_f32_4x4x4_16b_bf16 v[12:15], v[242:243], v[86:87], 0\n\t" \
  "v_lshlrev_b32_sdwa v221, 2, v220 dst_sel:DWORD dst_unused:UNUSED_PAD src0_sel:DWORD src1_sel:BYTE_3\n\t" \
  "v_lshlrev_b32_sdwa v220, 2, v220 dst_sel:DWORD dst_unused:UNUSED_PAD src0_sel:DWORD src1_sel:BYTE_1\n\t" \
  "ds_read_u16 v232, v232 offset:10528\n\t" \
  "v_mfma_f32_4x4x4_16b_bf16 v[16:19], v[242:243], v[92:93], 0\n\t" \
  "v_mfma_f32_4x4x4_16b_bf16 v[20:23], v[242:243], v[94:95], 0\n\t" \
  "v_lshlrev_b32_sdwa v223, 2, v222 dst_sel:DWORD dst_unused:UNUSED_PAD src0_sel:DWORD src1_sel:BYTE_3\n\t" \
  "v_lshlrev_b32_sdwa v222, 2, v222 dst_sel:DWORD dst_unused:UNUSED_PAD src0_sel:DWORD src1_sel:BYTE_1\n\t" \
  "ds_read_u16_d16_hi v235, v235 offset:10528\n\t" \
  "v_mfma_f32_4x4x4_16b_bf16 v[24:27], v[242:243], v[72:73], 0\n\t" \
  "v_mfma_f32_4x4x4_16b_bf16 v[28:31], v[242:243], v[74:75], 0\n\t" \
  "v_lshlrev_b32_sdwa v225, 1, v171 dst_sel:DWORD dst_unused:UNUSED_PAD src0_sel:DWORD src1_sel:WORD_1\n\t" \
  "v_lshlrev_b32_sdwa v224, 1, v170 dst_sel:DWORD dst_unused:UNUSED_PAD src0_sel:DWORD src1_sel:WORD_1\n\t" \
  "ds_read_u16 v234, v234 offset:10528\n\t" \
  "v_mfma_f32_4x4x4_16b_bf16 v[32:35], v[242:243], v[80:81], 0\n\t" \
  "v_mfma_f32_4x4x4_16b_bf16 v[36:39], v[242:243], v[82:83], 0\n\t" \
  "v_lshlrev_b32_sdwa v227, 1, v169 dst_sel:DWORD dst_unused:UNUSED_PAD src0_sel:DWORD src1_sel:WORD_1\n\t" \
  "v_lshlrev_b32_sdwa v226, 1, v168 dst_sel:DWORD dst_unused:UNUSED_PAD src0_sel:DWORD src1_sel:WORD_1\n\t" \
  "ds_read_b32 v221, v221 offset:10016\n\t" \
  "v_mfma_f32_4x4x4_16b_bf16 v[40:43], v[242:243], v[88:89], 0\n\t" \
  "v_mfma_f32_4x4x4_16b_bf16 v[44:47], v[242:243], v[90:91], 0\n\t" \
  "s_waitcnt lgkmcnt(9)\n\t" \
  "v_fma_mix_f32 v229, |v149|, v229, v229 op_sel:[0,1,0] op_sel_hi:[0,1,1] clamp\n\t" \
  "ds_read_b32 v220, v220 offset:10016\n\t" \
  "ds_read_b32 v223, v223 offset:10016\n\t" \
  "v_mfma_f32_4x4x4_16b_bf16 v[72:75], v[242:243], v[52:53], 0\n\t" \
  "v_mfma_f32_4x4x4_16b_bf16 v[76:79], v[242:243], v[54:55], 0\n\t" \
  "v_fma_mix_f32 v228, |v148|, v228, v228 op_sel:[0,1,0] op_sel_hi:[0,1,1] clamp\n\t" \
  "v_fma_mix_f32 v231, |v151|, v231, v231 op_sel:[0,1,0] op_sel_hi:[0,1,1] clamp\n\t" \
  "ds_read_b32 v222, v222 offset:10016\n\t" \
  "v_mfma_f32_4x4x4_16b_bf16 v[52:55], v[242:243], v[50:51], 0\n\t" \
  "v_mfma_f32_4x4x4_16b_bf16 v[48:51], v[242:243], v[48:49], 0\n\t" \
  "s_waitcnt lgkmcnt(4)\n\t" \
  "v_fma_mix_f32 v230, |v150|, v230, v230 op_sel:[0,1,0] op_sel_hi:[0,1,1] clamp\n\t" \
  "v_or_b32 v156, v232, v233\n\t" \
  "ds_read_u16_d16_hi v225, v225 offset:10528\n\t" \
  "v_mfma_f32_4x4x4_16b_bf16 v[80:83], v[242:243], v[60:61], 0\n\t" \
  "v_mfma_f32_4x4x4_16b_bf16 v[84:87], v[242:243], v[62:63], 0\n\t" \
  "v_or_b32 v157, v234, v235\n\t" \
  "v_fma_f32 v149, |v149|, v229, v149 mul:4\n\t" \
  "ds_read_u16 v224, v224 offset:10528\n\t" \
  "v_mfma_f32_4x4x4_16b_bf16 v[60:63], v[242:243], v[58:59], 0\n\t" \
  "v_mfma_f32_4x4x4_16b_bf16 v[56:59], v[242:243], v[56:57], 0\n\t" \
  "v_fma_f32 v148, |v148|, v228, v148 mul:4\n\t" \
  "v_fma_f32 v151, |v151|, v231, v151 mul:4\n\t" \
  "ds_read_u16_d16_hi v227, v227 offset:10528\n\t" \
  "v_mfma_f32_4x4x4_16b_bf16 v[88:91], v[242:243], v[68:69], 0\n\t" \
  "v_mfma_f32_4x4x4_16b_bf16 v[92:95], v[242:243], v[70:71], 0\n\t" \
  "v_fma_f32 v150, |v150|, v230, v150 mul:4\n\t" \
  "v_cvt_pk_bf16_f32 v148, v148, v149\n\t" \
  "ds_read_u16 v226, v226 offset:10528\n\t" \
  "v_mfma_f32_4x4x4_16b_bf16 v[68:71], v[242:243], v[66:67], 0\n\t" \
  "v_mfma_f32_4x4x4_16b_bf16 v[64:67], v[242:243], v[64:65], 0\n\t" \
  "v_cvt_pk_bf16_f32 v149, v150, v151\n\t" \
  "v_cvt_pknorm_i16_f32 v228, |v164|, |v165|\n\t" \
  "v_mfma_f32_16x16x32_bf16 v[176:179], v[208:211], v[96:99], 0\n\t" \
  "v_cvt_pknorm_i16_f32 v230, |v166|, |v167|\n\t" \
  "v_lshlrev_b32_sdwa v229, 2, v228 dst_sel:DWORD dst_unused:UNUSED_PAD src0_sel:DWORD src1_sel:BYTE_3\n\t" \
  "v_mfma_f32_16x16x32_bf16 v[184:187], v[208:211], v[100:103], 0\n\t" \
  "v_lshlrev_b32_sdwa v228, 2, v228 dst_sel:DWORD dst_unused:UNUSED_PAD src0_sel:DWORD src1_sel:BYTE_1\n\t" \
  "v_lshlrev_b32_sdwa v231, 2, v230 dst_sel:DWORD dst_unused:UNUSED_PAD src0_sel:DWORD src1_sel:BYTE_3\n\t" \
  "v_mfma_f32_16x16x32_bf16 v[192:195], v[208:211], v[104:107], 0\n\t" \
  "v_lshlrev_b32_sdwa v230, 2, v230 dst_sel:DWORD dst_unused:UNUSED_PAD src0_sel:DWORD src1_sel:BYTE_1\n\t" \
  "ds_read_b32 v229, v229 offset:10016\n\t" \
  "v_mfma_f32_16x16x32_bf16 v[200:203], v[208:211], v[108:111], 0\n\t" \
  "v_lshlrev_b32_sdwa v233, 1, v173 dst_sel:DWORD dst_unused:UNUSED_PAD src0_sel:DWORD src1_sel:WORD_1\n\t" \
  "v_lshlrev_b32_sdwa v232, 1, v172 dst_sel:DWORD dst_unused:UNUSED_PAD src0_sel:DWORD src1_sel:WORD_1\n\t" \
  "ds_read_b64_tr_b16 v[208:209], v248 offset:49704\n\t" \
  "v_mfma_f32_16x16x32_bf16 v[176:179], v[212:215], v[112:115], v[176:179]\n\t" \
  "v_lshlrev_b32_sdwa v235, 1, v175 dst_sel:DWORD dst_unused:UNUSED_PAD src0_sel:DWORD src1_sel:WORD_1\n\t" \
  "v_lshlrev_b32_sdwa v234, 1, v174 dst_sel:DWORD dst_unused:UNUSED_PAD src0_sel:DWORD src1_sel:WORD_1\n\t" \
  "ds_read_b64_tr_b16 v[210:211], v248 offset:50728\n\t" \
  "s_waitcnt lgkmcnt(7)\n\t" \
  "v_mfma_f32_16x16x32_bf16 v[184:187], v[212:215], v[116:119], v[184:187]\n\t" \
  "s_add_u32 s27, s27, s26\n\t" \
  "v_fma_mix_f32 v221, |v163|, v221, v221 op_sel:[0,1,0] op_sel_hi:[0,1,1] clamp\n\t" \
  "v_fma_mix_f32 v220, |v162|, v220, v220 op_sel:[0,1,0] op_sel_hi:[0,1,1] clamp\n\t" \
  "s_mov_b64 exec, s[30:31]\n\t" \
  "buffer_load_dword v237, v254, s[8:11], s27 offen\n\t" \
  "v_mfma_f32_16x16x32_bf16 v[192:195], v[212:215], v[120:123], v[192:195]\n\t" \
  "s_mov_b64 exec, -1\n\t" \
  "v_fma_mix_f32 v223, |v161|, v223, v223 op_sel:[0,1,0] op_sel_hi:[0,1,1] clamp\n\t" \
  "ds_read_b32 v228, v228 offset:10016\n\t" \
  "ds_read_b32 v231, v231 offset:10016\n\t" \
  "v_mfma_f32_16x16x32_bf16 v[200:203], v[212:215], v[124:127], v[200:203]\n\t" \
  "v_fma_mix_f32 v222, |v160|, v222, v222 op_sel:[0,1,0] op_sel_hi:[0,1,1] clamp\n\t" \
  "s_waitcnt lgkmcnt(5)\n\t" \
  "ds_read_b64_tr_b16 v[212:213], v248 offset:52776\n\t" \
  "ds_read_b32 v230, v230 offset:10016\n\t" \
  "v_mfma_f32_16x16x32_bf16 v[176:179], v[216:219], v[128:131], v[176:179]\n\t" \
  "v_or_b32 v171, v224, v225\n\t" \
  "v_or_b32 v170, v226, v227\n\t" \
  "ds_read_b64_tr_b16 v[214:215], v248 offset:53800\n\t" \
  "v_mfma_f32_16x16x32_bf16 v[184:187], v[216:219], v[132:135], v[184:187]\n\t" \
  "v_fma_f32 v163, |v163|, v221, v163 mul:4\n\t" \
  "v_fma_f32 v162, |v162|, v220, v162 mul:4\n\t" \
  "ds_read_u16_d16_hi v233, v233 offset:10528\n\t" \
  "v_mfma_f32_16x16x32_bf16 v[192:195], v[216:219], v[136:139], v[192:195]\n\t" \
  "v_fma_f32 v161, |v161|, v223, v161 mul:4\n\t" \
  "v_fma_f32 v160, |v160|, v222, v160 mul:4\n\t" \
  "ds_read_u16 v232, v232 offset:10528\n\t" \
  "v_mfma_f32_16x16x32_bf16 v[200:203], v[216:219], v[140:143], v[200:203]\n\t" \
  "v_cvt_pk_bf16_f32 v163, v162, v163\n\t" \
  "v_cvt_pk_bf16_f32 v162, v160, v161\n\t" \
  "ds_read_b64_tr_b16 v[216:217], v248 offset:51752\n\t" \
  "s_waitcnt lgkmcnt(8)\n\t" \
  "v_mfma_f32_16x16x32_bf16 v[180:183], v[208:211], v[96:99], 0\n\t" \
  "v_cvt_pknorm_i16_f32 v220, |v178|, |v179|\n\t" \
  "v_cvt_pknorm_i16_f32 v222, |v176|, |v177|\n\t" \
  "s_waitcnt vmcnt(1)\n\t" \
  "ds_read_b64_tr_b16 v[218:219], v248 offset:54824\n\t" \
  "v_mfma_f32_16x16x32_bf16 v[188:191], v[208:211], v[100:103], 0\n\t" \
  "v_lshlrev_b32_sdwa v221, 2, v220 dst_sel:DWORD dst_unused:UNUSED_PAD src0_sel:DWORD src1_sel:BYTE_3\n\t" \
  "v_lshlrev_b32_sdwa v220, 2, v220 dst_sel:DWORD dst_unused:UNUSED_PAD src0_sel:DWORD src1_sel:BYTE_1\n\t" \
  "s_mov_b64 exec, 3\n\t" \
  "ds_add_rtn_u32 v246, v251, v250 offset:47144\n\t" \
  "v_mfma_f32_16x16x32_bf16 v[196:199], v[208:211], v[104:107], 0\n\t" \
  "s_mov_b64 exec, -1\n\t" \
  "v_lshlrev_b32_sdwa v223, 2, v222 dst_sel:DWORD dst_unused:UNUSED_PAD src0_sel:DWORD src1_sel:BYTE_3\n\t" \
  "v_lshlrev_b32_sdwa v222, 2, v222 dst_sel:DWORD dst_unused:UNUSED_PAD src0_sel:DWORD src1_sel:BYTE_1\n\t" \
  "ds_read_u16_d16_hi v235, v235 offset:10528\n\t" \
  "v_mfma_f32_16x16x32_bf16 v[204:207], v[208:211], v[108:111], 0\n\t" \
  "s_waitcnt lgkmcnt(1)\n\t" \
  "v_lshlrev_b32_sdwa v225, 1, v187 dst_sel:DWORD dst_unused:UNUSED_PAD src0_sel:DWORD src1_sel:WORD_1\n\t" \
  "v_readfirstlane_b32 s4, v246\n\t" \
  "ds_read_u16 v234, v234 offset:10528\n\t" \
  "v_mfma_f32_16x16x32_bf16 v[180:183], v[212:215], v[112:115], v[180:183]\n\t" \
  "s_cmpk_lt_u32 s4, 3072\n\t" \
  "v_lshlrev_b32_sdwa v224, 1, v186 dst_sel:DWORD dst_unused:UNUSED_PAD src0_sel:DWORD src1_sel:WORD_1\n\t" \
  "s_cbranch_scc1 .Lcnx_w0_2\n.Lcnx_g0_2:\n\t" \
  "ds_read_b32 v221, v221 offset:10016\n\t" \
  "v_mfma_f32_16x16x32_bf16 v[188:191], v[212:215], v[116:119], v[188:191]\n\t" \
  "v_lshlrev_b32_sdwa v227, 1, v185 dst_sel:DWORD dst_unused:UNUSED_PAD src0_sel:DWORD src1_sel:WORD_1\n\t" \
  "v_lshlrev_b32_sdwa v226, 1, v184 dst_sel:DWORD dst_unused:UNUSED_PAD src0_sel:DWORD src1_sel:WORD_1\n\t" \
  "ds_read_b64_tr_b16 v[208:209], v249 offset:24600\n\t" \
  "ds_read_b32 v220, v220 offset:10016\n\t" \
  "v_mfma_f32_16x16x32_bf16 v[196:199], v[212:215], v[120:123], v[196:199]\n\t" \
  "v_fma_mix_f32 v229, |v165|, v229, v229 op_sel:[0,1,0] op_sel_hi:[0,1,1] clamp\n\t" \
  "v_fma_mix_f32 v228, |v164|, v228, v228 op_sel:[0,1,0] op_sel_hi:[0,1,1] clamp\n\t" \
  "ds_read_b64_tr_b16 v[210:211], v249 offset:26136\n\t" \
  "ds_read_b32 v223, v223 offset:10016\n\t" \
  "v_mfma_f32_16x16x32_bf16 v[204:207], v[212:215], v[124:127], v[204:207]\n\t" \
  "v_fma_mix_f32 v231, |v167|, v231, v231 op_sel:[0,1,0] op_sel_hi:[0,1,1] clamp\n\t" \
  "v_fma_mix_f32 v230, |v166|, v230, v230 op_sel:[0,1,0] op_sel_hi:[0,1,1] clamp\n\t" \
  "s_waitcnt lgkmcnt(5)\n\t" \
  "ds_read_b64_tr_b16 v[212:213], v249 offset:24608\n\t" \
  "ds_read_b32 v222, v222 offset:10016\n\t" \
  "v_mfma_f32_16x16x32_bf16 v[180:183], v[216:219], v[128:131], v[180:183]\n\t" \
  "s_mov_b32 m0, s46\n\t" \
  "v_or_b32 v172, v232, v233\n\t" \
  "v_or_b32 v173, v234, v235\n\t" \
  "s_mov_b32 exec_hi, 0xffff\n\t" \
  "buffer_load_dwordx4 v252, s[16:19], s33 offen lds\n\t" \
  "v_mfma_f32_16x16x32_bf16 v[188:191], v[216:219], v[132:135], v[188:191]\n\t" \
  "s_mov_b32 exec_hi, -1\n\t" \
  "v_fma_f32 v165, |v165|, v229, v165 mul:4\n\t" \
  "v_fma_f32 v164, |v164|, v228, v164 mul:4\n\t" \
  "s_add_u32 s33, s33, s32\n\t" \
  "ds_read_b64_tr_b16 v[214:215], v249 offset:26144\n\t" \
  "v_mfma_f32_16x16x32_bf16 v[196:199], v[216:219], v[136:139], v[196:199]\n\t" \
  "v_fma_f32 v167, |v167|, v231, v167 mul:4\n\t" \
  "v_fma_f32 v166, |v166|, v230, v166 mul:4\n\t" \
  "ds_read_u16_d16_hi v225, v225 offset:10528\n\t" \
  "v_mfma_f32_16x16x32_bf16 v[204:207], v[216:219], v[140:143], v[204:207]\n\t" \
  "v_cvt_pk_bf16_f32 v164, v164, v165\n\t" \
  "v_cvt_pk_bf16_f32 v165, v166, v167\n\t" \
  "ds_read_b64_tr_b16 v[216:217], v249 offset:24664\n\t" \
  "s_waitcnt lgkmcnt(6)\n\t" \
  "v_mfma_f32_16x16x32_bf16 v[0:3], v[208:211], v[146:149], v[0:3]\n\t" \
  "v_cvt_pknorm_i16_f32 v228, |v194|, |v195|\n\t" \
  "v_cvt_pknorm_i16_f32 v230, |v192|, |v193|\n\t" \
  "ds_read_b64_tr_b16 v[218:219], v249 offset:26200\n\t" \
  "v_mfma_f32_16x16x32_bf16 v[24:27], v[208:211], v[154:157], v[24:27]\n\t" \
  "v_lshlrev_b32_sdwa v229, 2, v228 dst_sel:DWORD dst_unused:UNUSED_PAD src0_sel:DWORD src1_sel:BYTE_3\n\t" \
  "v_lshlrev_b32_sdwa v228, 2, v228 dst_sel:DWORD dst_unused:UNUSED_PAD src0_sel:DWORD src1_sel:BYTE_1\n\t" \
  "ds_read_u16 v224, v224 offset:10528\n\t" \
  "v_mfma_f32_16x16x32_bf16 v[48:51], v[208:211], v[162:165], v[48:51]\n\t" \
  "v_lshlrev_b32_sdwa v231, 2, v230 dst_sel:DWORD dst_unused:UNUSED_PAD src0_sel:DWORD src1_sel:BYTE_3\n\t" \
  "v_lshlrev_b32_sdwa v230, 2, v230 dst_sel:DWORD dst_unused:UNUSED_PAD src0_sel:DWORD src1_sel:BYTE_1\n\t" \
  "ds_read_u16_d16_hi v227, v227 offset:10528\n\t" \
  "v_mfma_f32_16x16x32_bf16 v[72:75], v[208:211], v[170:173], v[72:75]\n\t" \
  "s_add_u32 m0, s46, s35\n\t" \
  "v_lshlrev_b32_sdwa v233, 1, v203 dst_sel:DWORD dst_unused:UNUSED_PAD src0_sel:DWORD src1_sel:WORD_1\n\t" \
  "v_lshlrev_b32_sdwa v232, 1, v202 dst_sel:DWORD dst_unused:UNUSED_PAD src0_sel:DWORD src1_sel:WORD_1\n\t" \
  "s_waitcnt lgkmcnt(5)\n\t" \
  "s_mov_b32 exec_hi, 0xffff\n\t" \
  "buffer_load_dwordx4 v252, s[16:19], s34 offen lds\n\t" \
  "v_mfma_f32_16x16x32_bf16 v[4:7], v[212:215], v[146:149], v[4:7]\n\t" \
  "s_mov_b32 exec_hi, -1\n\t" \
  "v_lshlrev_b32_sdwa v235, 1, v201 dst_sel:DWORD dst_unused:UNUSED_PAD src0_sel:DWORD src1_sel:WORD_1\n\t" \
  "s_add_u32 s34, s34, s32\n\t" \
  "ds_read_b64_tr_b16 v[208:209], v249 offset:24672\n\t" \
  "ds_read_u16 v226, v226 offset:10528\n\t" \
  "v_mfma_f32_16x16x32_bf16 v[28:31], v[212:215], v[154:157], v[28:31]\n\t" \
  "v_lshlrev_b32_sdwa v234, 1, v200 dst_sel:DWORD dst_unused:UNUSED_PAD src0_sel:DWORD src1_sel:WORD_1\n\t" \
  "ds_read_b64_tr_b16 v[210:211], v249 offset:26208\n\t" \
  "ds_read_b32 v229, v229 offset:10016\n\t" \
  "v_mfma_f32_16x16x32_bf16 v[52:55], v[212:215], v[162:165], v[52:55]\n\t" \
  "v_fma_mix_f32 v221, |v179|, v221, v221 op_sel:[0,1,0] op_sel_hi:[0,1,1] clamp\n\t" \
  "v_fma_mix_f32 v220, |v178|, v220, v220 op_sel:[0,1,0] op_sel_hi:[0,1,1] clamp\n\t" \
  "ds_read_b32 v228, v228 offset:10016\n\t" \
  "ds_read_b32 v231, v231 offset:10016\n\t" \
  "v_mfma_f32_16x16x32_bf16 v[76:79], v[212:215], v[170:173], v[76:79]\n\t" \
  "v_fma_mix_f32 v223, |v177|, v223, v223 op_sel:[0,1,0] op_sel_hi:[0,1,1] clamp\n\t" \
  "v_fma_mix_f32 v222, |v176|, v222, v222 op_sel:[0,1,0] op_sel_hi:[0,1,1] clamp\n\t" \
  "s_waitcnt lgkmcnt(4)\n\t" \
  "ds_read_b64_tr_b16 v[212:213], v249 offset:24728\n\t" \
  "ds_read_b32 v230, v230 offset:10016\n\t" \
  "v_mfma_f32_16x16x32_bf16 v[8:11], v[216:219], v[146:149], v[8:11]\n\t" \
  "v_or_b32 v187, v224, v225\n\t" \
  "v_or_b32 v186, v226, v227\n\t" \
  "ds_read_b64_tr_b16 v[214:215], v249 offset:26264\n\t" \
  "v_mfma_f32_16x16x32_bf16 v[32:35], v[216:219], v[154:157], v[32:35]\n\t" \
  "v_fma_f32 v179, |v179|, v221, v179 mul:4\n\t" \
  "v_fma_f32 v178, |v178|, v220, v178 mul:4\n\t" \
  "ds_read_u16_d16_hi v233, v233 offset:10528\n\t" \
  "v_mfma_f32_16x16x32_bf16 v[56:59], v[216:219], v[162:165], v[56:59]\n\t" \
  "v_fma_f32 v177, |v177|, v223, v177 mul:4\n\t" \
  "v_fma_f32 v176, |v176|, v222, v176 mul:4\n\t" \
  "s_add_u32 m0, s25, 0\n\t" \
  "s_add_u32 s24, s24, s26\n\t" \
  "s_sub_u32 s24, s24, s64\n\t" \
  "ds_read_u16 v232, v232 offset:10528\n\t" \
  "v_mfma_f32_16x16x32_bf16 v[80:83], v[216:219], v[170:173], v[80:83]\n\t" \
  "v_cvt_pk_bf16_f32 v179, v178, v179\n\t" \
  "v_cvt_pk_bf16_f32 v178, v176, v177\n\t" \
  "s_waitcnt lgkmcnt(8)\n\t" \
  "buffer_load_dwordx4 v254, s[8:11], s24 offen nt lds\n\t" \
  "ds_read_b64_tr_b16 v[216:217], v249 offset:24736\n\t" \
  "v_mfma_f32_16x16x32_bf16 v[12:15], v[208:211], v[146:149], v[12:15]\n\t" \
  "v_cvt_pknorm_i16_f32 v220, |v180|, |v181|\n\t" \
  "v_cvt_pknorm_i16_f32 v222, |v182|, |v183|\n\t" \
  "ds_read_b64_tr_b16 v[218:219], v249 offset:26272\n\t" \
  "v_mfma_f32_16x16x32_bf16 v[36:39], v[208:211], v[154:157], v[36:39]\n\t" \
  "v_lshlrev_b32_sdwa v221, 2, v220 dst_sel:DWORD dst_unused:UNUSED_PAD src0_sel:DWORD src1_sel:BYTE_3\n\t" \
  "v_lshlrev_b32_sdwa v220, 2, v220 dst_sel:DWORD dst_unused:UNUSED_PAD src0_sel:DWORD src1_sel:BYTE_1\n\t" \
  "ds_read_u16_d16_hi v235, v235 offset:10528\n\t" \
  "v_mfma_f32_16x16x32_bf16 v[60:63], v[208:211], v[162:165], v[60:63]\n\t" \
  "v_lshlrev_b32_sdwa v223, 2, v222 dst_sel:DWORD dst_unused:UNUSED_PAD src0_sel:DWORD src1_sel:BYTE_3\n\t" \
  "v_lshlrev_b32_sdwa v222, 2, v222 dst_sel:DWORD dst_unused:UNUSED_PAD src0_sel:DWORD src1_sel:BYTE_1\n\t" \
  "ds_read_u16 v234, v234 offset:10528\n\t" \
  "v_mfma_f32_16x16x32_bf16 v[84:87], v[208:211], v[170:173], v[84:87]\n\t" \
  "v_lshlrev_b32_sdwa v225, 1, v189 dst_sel:DWORD dst_unused:UNUSED_PAD src0_sel:DWORD src1_sel:WORD_1\n\t" \
  "v_lshlrev_b32_sdwa v224, 1, v188 dst_sel:DWORD dst_unused:UNUSED_PAD src0_sel:DWORD src1_sel:WORD_1\n\t" \
  "ds_read_b64_tr_b16 v[208:209], v248 offset:36904\n\t" \
  "s_waitcnt lgkmcnt(7)\n\t" \
  "v_mfma_f32_16x16x32_bf16 v[16:19], v[212:215], v[146:149], v[16:19]\n\t" \
  "v_lshlrev_b32_sdwa v227, 1, v191 dst_sel:DWORD dst_unused:UNUSED_PAD src0_sel:DWORD src1_sel:WORD_1\n\t" \
  "ds_read_b64_tr_b16 v[210:211], v248 offset:37928\n\t" \
  "ds_read_b32 v221, v221 offset:10016\n\t" \
  "v_mfma_f32_16x16x32_bf16 v[40:43], v[212:215], v[154:157], v[40:43]\n\t" \
  "s_add_u32 s27, s27, s26\n\t" \
  "v_lshlrev_b32_sdwa v226, 1, v190 dst_sel:DWORD dst_unused:UNUSED_PAD src0_sel:DWORD src1_sel:WORD_1\n\t" \
  "v_fma_mix_f32 v229, |v195|, v229, v229 op_sel:[0,1,0] op_sel_hi:[0,1,1] clamp\n\t" \
  "s_mov_b64 exec, s[30:31]\n\t" \
  "buffer_load_dword v237, v254, s[8:11], s27 offen\n\t" \
  "v_mfma_f32_16x16x32_bf16 v[64:67], v[212:215], v[162:165], v[64:67]\n\t" \
  "s_mov_b64 exec, -1\n\t" \
  "v_fma_mix_f32 v228, |v194|, v228, v228 op_sel:[0,1,0] op_sel_hi:[0,1,1] clamp\n\t" \
  "ds_read_b32 v220, v220 offset:10016\n\t" \
  "ds_read_b32 v223, v223 offset:10016\n\t" \
  "v_mfma_f32_16x16x32_bf16 v[88:91], v[212:215], v[170:173], v[88:91]\n\t" \
  "v_fma_mix_f32 v231, |v193|, v231, v231 op_sel:[0,1,0] op_sel_hi:[0,1,1] clamp\n\t" \
  "v_fma_mix_f32 v230, |v192|, v230, v230 op_sel:[0,1,0] op_sel_hi:[0,1,1] clamp\n\t" \
  "s_waitcnt lgkmcnt(5)\n\t" \
  "ds_read_b64_tr_b16 v[212:213], v248 offset:39976\n\t" \
  "ds_read_b32 v222, v222 offset:10016\n\t" \
  "v_mfma_f32_16x16x32_bf16 v[20:23], v[216:219], v[146:149], v[20:23]\n\t" \
  "v_or_b32 v203, v232, v233\n\t" \
  "v_or_b32 v202, v234, v235\n\t" \
  "ds_read_b64_tr_b16 v[214:215], v248 offset:41000\n\t" \
  "v_mfma_f32_16x16x32_bf16 v[44:47], v[216:219], v[154:157], v[44:47]\n\t" \
  "v_fma_f32 v195, |v195|, v229, v195 mul:4\n\t" \
  "v_fma_f32 v194, |v194|, v228, v194 mul:4\n\t" \
  "ds_read_u16_d16_hi v225, v225 offset:10528\n\t" \
  "v_mfma_f32_16x16x32_bf16 v[68:71], v[216:219], v[162:165], v[68:71]\n\t" \
  "v_fma_f32 v193, |v193|, v231, v193 mul:4\n\t" \
  "v_fma_f32 v192, |v192|, v230, v192 mul:4\n\t" \
  "ds_read_u16 v224, v224 offset:10528\n\t" \
  "v_mfma_f32_16x16x32_bf16 v[92:95], v[216:219], v[170:173], v[92:95]\n\t" \
  "v_cvt_pk_bf16_f32 v195, v194, v195\n\t" \
  "v_cvt_pk_bf16_f32 v194, v192, v193\n\t" \
  "ds_read_b64_tr_b16 v[216:217], v248 offset:38952\n\t" \
  "s_waitcnt lgkmcnt(9)\n\t" \
  "v_mfma_f32_16x16x32_bf16 v[144:147], v[208:211], v[96:99], 0\n\t" \
  "v_cvt_pknorm_i16_f32 v228, |v196|, |v197|\n\t" \
  "v_cvt_pknorm_i16_f32 v230, |v198|, |v199|\n\t" \
  "ds_read_b64_tr_b16 v[218:219], v248 offset:42024\n\t" \
  "v_mfma_f32_16x16x32_bf16 v[152:155], v[208:211], v[100:103], 0\n\t" \
  "v_lshlrev_b32_sdwa v229, 2, v228 dst_sel:DWORD dst_unused:UNUSED_PAD src0_sel:DWORD src1_sel:BYTE_3\n\t" \
  "v_lshlrev_b32_sdwa v228, 2, v228 dst_sel:DWORD dst_unused:UNUSED_PAD src0_sel:DWORD src1_sel:BYTE_1\n\t" \
  "ds_read_u16_d16_hi v227, v227 offset:10528\n\t" \
  "v_mfma_f32_16x16x32_bf16 v[160:163], v[208:211], v[104:107], 0\n\t" \
  "v_lshlrev_b32_sdwa v231, 2, v230 dst_sel:DWORD dst_unused:UNUSED_PAD src0_sel:DWORD src1_sel:BYTE_3\n\t" \
  "v_lshlrev_b32_sdwa v230, 2, v230 dst_sel:DWORD dst_unused:UNUSED_PAD src0_sel:DWORD src1_sel:BYTE_1\n\t" \
  "ds_read_u16 v226, v226 offset:10528\n\t" \
  "v_mfma_f32_16x16x32_bf16 v[168:171], v[208:211], v[108:111], 0\n\t" \
  "v_lshlrev_b32_sdwa v233, 1, v205 dst_sel:DWORD dst_unused:UNUSED_PAD src0_sel:DWORD src1_sel:WORD_1\n\t" \
  "v_lshlrev_b32_sdwa v232, 1, v204 dst_sel:DWORD dst_unused:UNUSED_PAD src0_sel:DWORD src1_sel:WORD_1\n\t" \
  "ds_read_b64_tr_b16 v[208:209], v248 offset:37416\n\t" \
  "s_waitcnt lgkmcnt(7)\n\t" \
  "v_mfma_f32_16x16x32_bf16 v[144:147], v[212:215], v[112:115], v[144:147]\n\t" \
  "v_lshlrev_b32_sdwa v235, 1, v207 dst_sel:DWORD dst_unused:UNUSED_PAD src0_sel:DWORD src1_sel:WORD_1\n\t" \
  "ds_read_b64_tr_b16 v[210:211], v248 offset:38440\n\t" \
  "ds_read_b32 v229, v229 offset:10016\n\t" \
  "v_mfma_f32_16x16x32_bf16 v[152:155], v[212:215], v[116:119], v[152:155]\n\t" \
  "v_lshlrev_b32_sdwa v234, 1, v206 dst_sel:DWORD dst_unused:UNUSED_PAD src0_sel:DWORD src1_sel:WORD_1\n\t" \
  "v_fma_mix_f32 v221, |v181|, v221, v221 op_sel:[0,1,0] op_sel_hi:[0,1,1] clamp\n\t" \
  "ds_read_b32 v228, v228 offset:10016\n\t" \
  "v_mfma_f32_16x16x32_bf16 v[160:163], v[212:215], v[120:123], v[160:163]\n\t" \
  "v_fma_mix_f32 v220, |v180|, v220, v220 op_sel:[0,1,0] op_sel_hi:[0,1,1] clamp\n\t" \
  "ds_read_b32 v231, v231 offset:10016\n\t" \
  "ds_read_b32 v230, v230 offset:10016\n\t" \
  "v_mfma_f32_16x16x32_bf16 v[168:171], v[212:215], v[124:127], v[168:171]\n\t" \
  "v_fma_mix_f32 v223, |v183|, v223, v223 op_sel:[0,1,0] op_sel_hi:[0,1,1] clamp\n\t" \
  "v_fma_mix_f32 v222, |v182|, v222, v222 op_sel:[0,1,0] op_sel_hi:[0,1,1] clamp\n\t" \
  "s_waitcnt lgkmcnt(6)\n\t" \
  "ds_read_b64_tr_b16 v[212:213], v248 offset:40488\n\t" \
  "ds_read_u16_d16_hi v233, v233 offset:10528\n\t" \
  "v_mfma_f32_16x16x32_bf16 v[144:147], v[216:219], v[128:131], v[144:147]\n\t" \
  "v_or_b32 v188, v224, v225\n\t" \
  "v_or_b32 v189, v226, v227\n\t" \
  "ds_read_b64_tr_b16 v[214:215], v248 offset:41512\n\t" \
  "v_mfma_f32_16x16x32_bf16 v[152:155], v[216:219], v[132:135], v[152:155]\n\t" \
  "v_fma_f32 v181, |v181|, v221, v181 mul:4\n\t" \
  "v_fma_f32 v180, |v180|, v220, v180 mul:4\n\t" \
  "ds_read_u16 v232, v232 offset:10528\n\t" \
  "v_mfma_f32_16x16x32_bf16 v[160:163], v[216:219], v[136:139], v[160:163]\n\t" \
  "v_fma_f32 v183, |v183|, v223, v183 mul:4\n\t" \
  "v_fma_f32 v182, |v182|, v222, v182 mul:4\n\t" \
  "ds_read_u16_d16_hi v235, v235 offset:10528\n\t" \
  "v_mfma_f32_16x16x32_bf16 v[168:171], v[216:219], v[140:143], v[168:171]\n\t" \
  "v_cvt_pk_bf16_f32 v180, v180, v181\n\t" \
  "v_cvt_pk_bf16_f32 v181, v182, v183\n\t" \
  "ds_read_b64_tr_b16 v[216:217], v248 offset:39464\n\t" \
  "s_waitcnt lgkmcnt(10)\n\t" \
  "v_mfma_f32_16x16x32_bf16 v[148:151], v[208:211], v[96:99], 0\n\t" \
  "v_cvt_pknorm_i16_f32 v220, |v146|, |v147|\n\t" \
  "v_cvt_pknorm_i16_f32 v222, |v144|, |v145|\n\t" \
  "s_waitcnt vmcnt(2)\n\t" \
  "ds_read_b64_tr_b16 v[218:219], v248 offset:42536\n\t" \
  "v_mfma_f32_16x16x32_bf16 v[156:159], v[208:211], v[100:103], 0\n\t" \
  "v_lshlrev_b32_sdwa v221, 2, v220 dst_sel:DWORD dst_unused:UNUSED_PAD src0_sel:DWORD src1_sel:BYTE_3\n\t" \
  "v_lshlrev_b32_sdwa v220, 2, v220 dst_sel:DWORD dst_unused:UNUSED_PAD src0_sel:DWORD src1_sel:BYTE_1\n\t" \
  "s_mov_b64 exec, 3\n\t" \
  "ds_add_rtn_u32 v246, v251, v250 offset:47152\n\t" \
  "v_mfma_f32_16x16x32_bf16 v[164:167], v[208:211], v[104:107], 0\n\t" \
  "s_mov_b64 exec, -1\n\t" \
  "v_lshlrev_b32_sdwa v223, 2, v222 dst_sel:DWORD dst_unused:UNUSED_PAD src0_sel:DWORD src1_sel:BYTE_3\n\t" \
  "v_lshlrev_b32_sdwa v222, 2, v222 dst_sel:DWORD dst_unused:UNUSED_PAD src0_sel:DWORD src1_sel:BYTE_1\n\t" \
  "ds_read_u16 v234, v234 offset:10528\n\t" \
  "v_mfma_f32_16x16x32_bf16 v[172:175], v[208:211], v[108:111], 0\n\t" \
  "s_waitcnt lgkmcnt(1)\n\t" \
  "v_lshlrev_b32_sdwa v225, 1, v155 dst_sel:DWORD dst_unused:UNUSED_PAD src0_sel:DWORD src1_sel:WORD_1\n\t" \
  "v_readfirstlane_b32 s4, v246\n\t" \
  "ds_read_b32 v221, v221 offset:10016\n\t" \
  "v_mfma_f32_16x16x32_bf16 v[148:151], v[212:215], v[112:115], v[148:151]\n\t" \
  "s_cmpk_lt_u32 s4, 3072\n\t" \
  "v_lshlrev_b32_sdwa v224, 1, v154 dst_sel:DWORD dst_unused:UNUSED_PAD src0_sel:DWORD src1_sel:WORD_1\n\t" \
  "s_cbranch_scc1 .Lcnx_w0_3\n.Lcnx_g0_3:\n\t" \
  "ds_read_b32 v220, v220 offset:10016\n\t" \
  "v_mfma_f32_16x16x32_bf16 v[156:159], v[212:215], v[116:119], v[156:159]\n\t" \
  "v_lshlrev_b32_sdwa v227, 1, v153 dst_sel:DWORD dst_unused:UNUSED_PAD src0_sel:DWORD src1_sel:WORD_1\n\t" \
  "v_lshlrev_b32_sdwa v226, 1, v152 dst_sel:DWORD dst_unused:UNUSED_PAD src0_sel:DWORD src1_sel:WORD_1\n\t" \
  "ds_read_b64_tr_b16 v[208:209], v249 offset:30752\n\t" \
  "v_mfma_f32_16x16x32_bf16 v[164:167], v[212:215], v[120:123], v[164:167]\n\t" \
  "v_fma_mix_f32 v229, |v197|, v229, v229 op_sel:[0,1,0] op_sel_hi:[0,1,1] clamp\n\t" \
  "v_fma_mix_f32 v228, |v196|, v228, v228 op_sel:[0,1,0] op_sel_hi:[0,1,1] clamp\n\t" \
  "ds_read_b64_tr_b16 v[210:211], v249 offset:32288\n\t" \
  "ds_read_b32 v223, v223 offset:10016\n\t" \
  "v_mfma_f32_16x16x32_bf16 v[172:175], v[212:215], v[124:127], v[172:175]\n\t" \
  "v_fma_mix_f32 v231, |v199|, v231, v231 op_sel:[0,1,0] op_sel_hi:[0,1,1] clamp\n\t" \
  "v_fma_mix_f32 v230, |v198|, v230, v230 op_sel:[0,1,0] op_sel_hi:[0,1,1] clamp\n\t" \
  "s_waitcnt lgkmcnt(5)\n\t" \
  "ds_read_b64_tr_b16 v[212:213], v249 offset:30760\n\t" \
  "ds_read_b32 v222, v222 offset:10016\n\t" \
  "v_mfma_f32_16x16x32_bf16 v[148:151], v[216:219], v[128:131], v[148:151]\n\t" \
  "s_mov_b32 m0, s47\n\t" \
  "v_or_b32 v204, v232, v233\n\t" \
  "v_or_b32 v205, v234, v235\n\t" \
  "s_mov_b32 exec_hi, 0xffff\n\t" \
  "buffer_load_dwordx4 v252, s[16:19], s33 offen lds\n\t" \
  "v_mfma_f32_16x16x32_bf16 v[156:159], v[216:219], v[132:135], v[156:159]\n\t" \
  "s_mov_b32 exec_hi, -1\n\t" \
  "v_fma_f32 v197, |v197|, v229, v197 mul:4\n\t" \
  "v_fma_f32 v196, |v196|, v228, v196 mul:4\n\t" \
  "s_add_u32 s33, s33, s32\n\t" \
  "ds_read_b64_tr_b16 v[214:215], v249 offset:32296\n\t" \
  "v_mfma_f32_16x16x32_bf16 v[164:167], v[216:219], v[136:139], v[164:167]\n\t" \
  "v_fma_f32 v199, |v199|, v231, v199 mul:4\n\t" \
  "v_fma_f32 v198, |v198|, v230, v198 mul:4\n\t" \
  "ds_read_u16_d16_hi v225, v225 offset:10528\n\t" \
  "v_mfma_f32_16x16x32_bf16 v[172:175], v[216:219], v[140:143], v[172:175]\n\t" \
  "v_cvt_pk_bf16_f32 v196, v196, v197\n\t" \
  "v_cvt_pk_bf16_f32 v197, v198, v199\n\t" \
  "ds_read_b64_tr_b16 v[216:217], v249 offset:30816\n\t" \
  "s_waitcnt lgkmcnt(6)\n\t" \
  "v_mfma_f32_16x16x32_bf16 v[0:3], v[208:211], v[178:181], v[0:3]\n\t" \
  "v_cvt_pknorm_i16_f32 v228, |v162|, |v163|\n\t" \
  "v_cvt_pknorm_i16_f32 v230, |v160|, |v161|\n\t" \
  "ds_read_b64_tr_b16 v[218:219], v249 offset:32352\n\t" \
  "v_mfma_f32_16x16x32_bf16 v[24:27], v[208:211], v[186:189], v[24:27]\n\t" \
  "v_lshlrev_b32_sdwa v229, 2, v228 dst_sel:DWORD dst_unused:UNUSED_PAD src0_sel:DWORD src1_sel:BYTE_3\n\t" \
  "v_lshlrev_b32_sdwa v228, 2, v228 dst_sel:DWORD dst_unused:UNUSED_PAD src0_sel:DWORD src1_sel:BYTE_1\n\t" \
  "ds_read_u16 v224, v224 offset:10528\n\t" \
  "v_mfma_f32_16x16x32_bf16 v[48:51], v[208:211], v[194:197], v[48:51]\n\t" \
  "v_lshlrev_b32_sdwa v231, 2, v230 dst_sel:DWORD dst_unused:UNUSED_PAD src0_sel:DWORD src1_sel:BYTE_3\n\t" \
  "v_lshlrev_b32_sdwa v230, 2, v230 dst_sel:DWORD dst_unused:UNUSED_PAD src0_sel:DWORD src1_sel:BYTE_1\n\t" \
  "ds_read_u16_d16_hi v227, v227 offset:10528\n\t" \
  "v_mfma_f32_16x16x32_bf16 v[72:75], v[208:211], v[202:205], v[72:75]\n\t" \
  "s_add_u32 m0, s47, s35\n\t" \
  "v_lshlrev_b32_sdwa v233, 1, v171 dst_sel:DWORD dst_unused:UNUSED_PAD src0_sel:DWORD src1_sel:WORD_1\n\t" \
  "v_lshlrev_b32_sdwa v232, 1, v170 dst_sel:DWORD dst_unused:UNUSED_PAD src0_sel:DWORD src1_sel:WORD_1\n\t" \
  "s_waitcnt lgkmcnt(5)\n\t" \
  "s_mov_b32 exec_hi, 0xffff\n\t" \
  "buffer_load_dwordx4 v252, s[16:19], s34 offen lds\n\t" \
  "v_mfma_f32_16x16x32_bf16 v[4:7], v[212:215], v[178:181], v[4:7]\n\t" \
  "s_mov_b32 exec_hi, -1\n\t" \
  "v_lshlrev_b32_sdwa v235, 1, v169 dst_sel:DWORD dst_unused:UNUSED_PAD src0_sel:DWORD src1_sel:WORD_1\n\t" \
  "s_add_u32 s34, s34, s32\n\t" \
  "ds_read_b64_tr_b16 v[208:209], v249 offset:30824\n\t" \
  "ds_read_u16 v226, v226 offset:10528\n\t" \
  "v_mfma_f32_16x16x32_bf16 v[28:31], v[212:215], v[186:189], v[28:31]\n\t" \
  "v_lshlrev_b32_sdwa v234, 1, v168 dst_sel:DWORD dst_unused:UNUSED_PAD src0_sel:DWORD src1_sel:WORD_1\n\t" \
  "ds_read_b64_tr_b16 v[210:211], v249 offset:32360\n\t" \
  "ds_read_b32 v229, v229 offset:10016\n\t" \
  "v_mfma_f32_16x16x32_bf16 v[52:55], v[212:215], v[194:197], v[52:55]\n\t" \
  "v_fma_mix_f32 v221, |v147|, v221, v221 op_sel:[0,1,0] op_sel_hi:[0,1,1] clamp\n\t" \
  "v_fma_mix_f32 v220, |v146|, v220, v220 op_sel:[0,1,0] op_sel_hi:[0,1,1] clamp\n\t" \
  "ds_read_b32 v228, v228 offset:10016\n\t" \
  "ds_read_b32 v231, v231 offset:10016\n\t" \
  "v_mfma_f32_16x16x32_bf16 v[76:79], v[212:215], v[202:205], v[76:79]\n\t" \
  "v_fma_mix_f32 v223, |v145|, v223, v223 op_sel:[0,1,0] op_sel_hi:[0,1,1] clamp\n\t" \
  "v_fma_mix_f32 v222, |v144|, v222, v222 op_sel:[0,1,0] op_sel_hi:[0,1,1] clamp\n\t" \
  "s_waitcnt lgkmcnt(4)\n\t" \
  "ds_read_b64_tr_b16 v[212:213], v249 offset:30880\n\t" \
  "ds_read_b32 v230, v230 offset:10016\n\t" \
  "v_mfma_f32_16x16x32_bf16 v[8:11], v[216:219], v[178:181], v[8:11]\n\t" \
  "v_or_b32 v155, v224, v225\n\t" \
  "v_or_b32 v154, v226, v227\n\t" \
  "ds_read_b64_tr_b16 v[214:215], v249 offset:32416\n\t" \
  "v_mfma_f32_16x16x32_bf16 v[32:35], v[216:219], v[186:189], v[32:35]\n\t" \
  "v_fma_f32 v147, |v147|, v221, v147 mul:4\n\t" \
  "v_fma_f32 v146, |v146|, v220, v146 mul:4\n\t" \
  "ds_read_u16_d16_hi v233, v233 offset:10528\n\t" \
  "v_mfma_f32_16x16x32_bf16 v[56:59], v[216:219], v[194:197], v[56:59]\n\t" \
  "v_fma_f32 v145, |v145|, v223, v145 mul:4\n\t" \
  "v_fma_f32 v144, |v144|, v222, v144 mul:4\n\t" \
  "s_add_u32 m0, s25, 8200\n\t" \
  "s_add_u32 s24, s24, s26\n\t" \
  "ds_read_u16 v232, v232 offset:10528\n\t" \
  "v_mfma_f32_16x16x32_bf16 v[80:83], v[216:219], v[202:205], v[80:83]\n\t" \
  "v_cvt_pk_bf16_f32 v147, v146, v147\n\t" \
  "v_cvt_pk_bf16_f32 v146, v144, v145\n\t" \
  "s_waitcnt lgkmcnt(8)\n\t" \
  "buffer_load_dwordx4 v254, s[8:11], s24 offen nt lds\n\t" \
  "ds_read_b64_tr_b16 v[216:217], v249 offset:30888\n\t" \
  "v_mfma_f32_16x16x32_bf16 v[12:15], v[208:211], v[178:181], v[12:15]\n\t" \
  "v_cvt_pknorm_i16_f32 v220, |v148|, |v149|\n\t" \
  "v_cvt_pknorm_i16_f32 v222, |v150|, |v151|\n\t" \
  "ds_read_b64_tr_b16 v[218:219], v249 offset:32424\n\t" \
  "v_mfma_f32_16x16x32_bf16 v[36:39], v[208:211], v[186:189], v[36:39]\n\t" \
  "v_lshlrev_b32_sdwa v221, 2, v220 dst_sel:DWORD dst_unused:UNUSED_PAD src0_sel:DWORD src1_sel:BYTE_3\n\t" \
  "v_lshlrev_b32_sdwa v220, 2, v220 dst_sel:DWORD dst_unused:UNUSED_PAD src0_sel:DWORD src1_sel:BYTE_1\n\t" \
  "ds_read_u16_d16_hi v235, v235 offset:10528\n\t" \
  "v_mfma_f32_16x16x32_bf16 v[60:63], v[208:211], v[194:197], v[60:63]\n\t" \
  "v_lshlrev_b32_sdwa v223, 2, v222 dst_sel:DWORD dst_unused:UNUSED_PAD src0_sel:DWORD src1_sel:BYTE_3\n\t" \
  "v_lshlrev_b32_sdwa v222, 2, v222 dst_sel:DWORD dst_unused:UNUSED_PAD src0_sel:DWORD src1_sel:BYTE_1\n\t" \
  "ds_read_u16 v234, v234 offset:10528\n\t" \
  "v_mfma_f32_16x16x32_bf16 v[84:87], v[208:211], v[202:205], v[84:87]\n\t" \
  "v_lshlrev_b32_sdwa v225, 1, v157 dst_sel:DWORD dst_unused:UNUSED_PAD src0_sel:DWORD src1_sel:WORD_1\n\t" \
  "v_lshlrev_b32_sdwa v224, 1, v156 dst_sel:DWORD dst_unused:UNUSED_PAD src0_sel:DWORD src1_sel:WORD_1\n\t" \
  "ds_read_b64_tr_b16 v[208:209], v248 offset:0\n\t" \
  "s_waitcnt lgkmcnt(7)\n\t" \
  "v_mfma_f32_16x16x32_bf16 v[16:19], v[212:215], v[178:181], v[16:19]\n\t" \
  "v_lshlrev_b32_sdwa v227, 1, v159 dst_sel:DWORD dst_unused:UNUSED_PAD src0_sel:DWORD src1_sel:WORD_1\n\t" \
  "ds_read_b64_tr_b16 v[210:211], v248 offset:1024\n\t" \
  "ds_read_b32 v221, v221 offset:10016\n\t" \
  "v_mfma_f32_16x16x32_bf16 v[40:43], v[212:215], v[186:189], v[40:43]\n\t" \
  "s_add_u32 s27, s27, s26\n\t" \
  "v_lshlrev_b32_sdwa v226, 1, v158 dst_sel:DWORD dst_unused:UNUSED_PAD src0_sel:DWORD src1_sel:WORD_1\n\t" \
  "v_fma_mix_f32 v229, |v163|, v229, v229 op_sel:[0,1,0] op_sel_hi:[0,1,1] clamp\n\t" \
  "s_mov_b64 exec, s[30:31]\n\t" \
  "buffer_load_dword v237, v254, s[8:11], s27 offen\n\t" \
  "v_mfma_f32_16x16x32_bf16 v[64:67], v[212:215], v[194:197], v[64:67]\n\t" \
  "s_mov_b64 exec, -1\n\t" \
  "v_fma_mix_f32 v228, |v162|, v228, v228 op_sel:[0,1,0] op_sel_hi:[0,1,1] clamp\n\t" \
  "ds_read_b32 v220, v220 offset:10016\n\t" \
  "ds_read_b32 v223, v223 offset:10016\n\t" \
  "v_mfma_f32_16x16x32_bf16 v[88:91], v[212:215], v[202:205], v[88:91]\n\t" \
  "v_fma_mix_f32 v231, |v161|, v231, v231 op_sel:[0,1,0] op_sel_hi:[0,1,1] clamp\n\t" \
  "v_fma_mix_f32 v230, |v160|, v230, v230 op_sel:[0,1,0] op_sel_hi:[0,1,1] clamp\n\t" \
  "s_waitcnt lgkmcnt(5)\n\t" \
  "ds_read_b64_tr_b16 v[212:213], v248 offset:3072\n\t" \
  "ds_read_b32 v222, v222 offset:10016\n\t" \
  "v_mfma_f32_16x16x32_bf16 v[20:23], v[216:219], v[178:181], v[20:23]\n\t" \
  "v_or_b32 v171, v232, v233\n\t" \
  "v_or_b32 v170, v234, v235\n\t" \
  "ds_read_b64_tr_b16 v[214:215], v248 offset:4096\n\t" \
  "v_mfma_f32_16x16x32_bf16 v[44:47], v[216:219], v[186:189], v[44:47]\n\t" \
  "v_fma_f32 v163, |v163|, v229, v163 mul:4\n\t" \
  "v_fma_f32 v162, |v162|, v228, v162 mul:4\n\t" \
  "ds_read_u16_d16_hi v225, v225 offset:10528\n\t" \
  "v_mfma_f32_16x16x32_bf16 v[68:71], v[216:219], v[194:197], v[68:71]\n\t" \
  "v_fma_f32 v161, |v161|, v231, v161 mul:4\n\t" \
  "v_fma_f32 v160, |v160|, v230, v160 mul:4\n\t" \
  "ds_read_u16 v224, v224 offset:10528\n\t" \
  "v_mfma_f32_16x16x32_bf16 v[92:95], v[216:219], v[202:205], v[92:95]\n\t" \
  "v_cvt_pk_bf16_f32 v163, v162, v163\n\t" \
  "v_cvt_pk_bf16_f32 v162, v160, v161\n\t" \
  "ds_read_b64_tr_b16 v[216:217], v248 offset:2048\n\t" \
  "s_waitcnt lgkmcnt(9)\n\t" \
  "v_mfma_f32_16x16x32_bf16 v[176:179], v[208:211], v[96:99], 0\n\t" \
  "v_cvt_pknorm_i16_f32 v228, |v164|, |v165|\n\t" \
  "v_cvt_pknorm_i16_f32 v230, |v166|, |v167|\n\t" \
  "ds_read_b64_tr_b16 v[218:219], v248 offset:5120\n\t" \
  "v_mfma_f32_16x16x32_bf16 v[184:187], v[208:211], v[100:103], 0\n\t" \
  "v_lshlrev_b32_sdwa v229, 2, v228 dst_sel:DWORD dst_unused:UNUSED_PAD src0_sel:DWORD src1_sel:BYTE_3\n\t" \
  "v_lshlrev_b32_sdwa v228, 2, v228 dst_sel:DWORD dst_unused:UNUSED_PAD src0_sel:DWORD src1_sel:BYTE_1\n\t" \
  "ds_read_u16_d16_hi v227, v227 offset:10528\n\t" \
  "v_mfma_f32_16x16x32_bf16 v[192:195], v[208:211], v[104:107], 0\n\t" \
  "v_lshlrev_b32_sdwa v231, 2, v230 dst_sel:DWORD dst_unused:UNUSED_PAD src0_sel:DWORD src1_sel:BYTE_3\n\t" \
  "v_lshlrev_b32_sdwa v230, 2, v230 dst_sel:DWORD dst_unused:UNUSED_PAD src0_sel:DWORD src1_sel:BYTE_1\n\t" \
  "ds_read_u16 v226, v226 offset:10528\n\t" \
  "v_mfma_f32_16x16x32_bf16 v[200:203], v[208:211], v[108:111], 0\n\t" \
  "v_lshlrev_b32_sdwa v233, 1, v173 dst_sel:DWORD dst_unused:UNUSED_PAD src0_sel:DWORD src1_sel:WORD_1\n\t" \
  "v_lshlrev_b32_sdwa v232, 1, v172 dst_sel:DWORD dst_unused:UNUSED_PAD src0_sel:DWORD src1_sel:WORD_1\n\t" \
  "ds_read_b64_tr_b16 v[208:209], v248 offset:512\n\t" \
  "s_waitcnt lgkmcnt(7)\n\t" \
  "v_mfma_f32_16x16x32_bf16 v[176:179], v[212:215], v[112:115], v[176:179]\n\t" \
  "v_lshlrev_b32_sdwa v235, 1, v175 dst_sel:DWORD dst_unused:UNUSED_PAD src0_sel:DWORD src1_sel:WORD_1\n\t" \
  "ds_read_b64_tr_b16 v[210:211], v248 offset:1536\n\t" \
  "ds_read_b32 v229, v229 offset:10016\n\t" \
  "v_mfma_f32_16x16x32_bf16 v[184:187], v[212:215], v[116:119], v[184:187]\n\t" \
  "v_lshlrev_b32_sdwa v234, 1, v174 dst_sel:DWORD dst_unused:UNUSED_PAD src0_sel:DWORD src1_sel:WORD_1\n\t" \
  "v_fma_mix_f32 v221, |v149|, v221, v221 op_sel:[0,1,0] op_sel_hi:[0,1,1] clamp\n\t" \
  "ds_read_b32 v228, v228 offset:10016\n\t" \
  "v_mfma_f32_16x16x32_bf16 v[192:195], v[212:215], v[120:123], v[192:195]\n\t" \
  "v_fma_mix_f32 v220, |v148|, v220, v220 op_sel:[0,1,0] op_sel_hi:[0,1,1] clamp\n\t" \
  "ds_read_b32 v231, v231 offset:10016\n\t" \
  "ds_read_b32 v230, v230 offset:10016\n\t" \
  "v_mfma_f32_16x16x32_bf16 v[200:203], v[212:215], v[124:127], v[200:203]\n\t" \
  "v_fma_mix_f32 v223, |v151|, v223, v223 op_sel:[0,1,0] op_sel_hi:[0,1,1] clamp\n\t" \
  "v_fma_mix_f32 v222, |v150|, v222, v222 op_sel:[0,1,0] op_sel_hi:[0,1,1] clamp\n\t" \
  "s_waitcnt lgkmcnt(6)\n\t" \
  "ds_read_b64_tr_b16 v[212:213], v248 offset:3584\n\t" \
  "ds_read_u16_d16_hi v233, v233 offset:10528\n\t" \
  "v_mfma_f32_16x16x32_bf16 v[176:179], v[216:219], v[128:131], v[176:179]\n\t" \
  "v_or_b32 v156, v224, v225\n\t" \
  "v_or_b32 v157, v226, v227\n\t" \
  "ds_read_b64_tr_b16 v[214:215], v248 offset:4608\n\t" \
  "v_mfma_f32_16x16x32_bf16 v[184:187], v[216:219], v[132:135], v[184:187]\n\t" \
  "v_fma_f32 v149, |v149|, v221, v149 mul:4\n\t" \
  "v_fma_f32 v148, |v148|, v220, v148 mul:4\n\t" \
  "ds_read_u16 v232, v232 offset:10528\n\t" \
  "v_mfma_f32_16x16x32_bf16 v[192:195], v[216:219], v[136:139], v[192:195]\n\t" \
  "v_fma_f32 v151, |v151|, v223, v151 mul:4\n\t" \
  "v_fma_f32 v150, |v150|, v222, v150 mul:4\n\t" \
  "ds_read_u16_d16_hi v235, v235 offset:10528\n\t" \
  "v_mfma_f32_16x16x32_bf16 v[200:203], v[216:219], v[140:143], v[200:203]\n\t" \
  "v_cvt_pk_bf16_f32 v148, v148, v149\n\t" \
  "v_cvt_pk_bf16_f32 v149, v150, v151\n\t" \
  "ds_read_b64_tr_b16 v[216:217], v248 offset:2560\n\t" \
  "s_waitcnt lgkmcnt(10)\n\t" \
  "v_mfma_f32_16x16x32_bf16 v[180:183], v[208:211], v[96:99], 0\n\t" \
  "v_cvt_pknorm_i16_f32 v220, |v178|, |v179|\n\t" \
  "v_cvt_pknorm_i16_f32 v222, |v176|, |v177|\n\t" \
  "s_waitcnt vmcnt(2)\n\t" \
  "ds_read_b64_tr_b16 v[218:219], v248 offset:5632\n\t" \
  "v_mfma_f32_16x16x32_bf16 v[188:191], v[208:211], v[100:103], 0\n\t" \
  "v_lshlrev_b32_sdwa v221, 2, v220 dst_sel:DWORD dst_unused:UNUSED_PAD src0_sel:DWORD src1_sel:BYTE_3\n\t" \
  "v_lshlrev_b32_sdwa v220, 2, v220 dst_sel:DWORD dst_unused:UNUSED_PAD src0_sel:DWORD src1_sel:BYTE_1\n\t" \
  "s_mov_b64 exec, 3\n\t" \
  "ds_add_rtn_u32 v246, v251, v250 offset:47160\n\t" \
  "v_mfma_f32_16x16x32_bf16 v[196:199], v[208:211], v[104:107], 0\n\t" \
  "s_mov_b64 exec, -1\n\t" \
  "v_lshlrev_b32_sdwa v223, 2, v222 dst_sel:DWORD dst_unused:UNUSED_PAD src0_sel:DWORD src1_sel:BYTE_3\n\t" \
  "v_lshlrev_b32_sdwa v222, 2, v222 dst_sel:DWORD dst_unused:UNUSED_PAD src0_sel:DWORD src1_sel:BYTE_1\n\t" \
  "ds_read_u16 v234, v234 offset:10528\n\t" \
  "v_mfma_f32_16x16x32_bf16 v[204:207], v[208:211], v[108:111], 0\n\t" \
  "s_waitcnt lgkmcnt(1)\n\t" \
  "v_lshlrev_b32_sdwa v225, 1, v187 dst_sel:DWORD dst_unused:UNUSED_PAD src0_sel:DWORD src1_sel:WORD_1\n\t" \
  "v_readfirstlane_b32 s4, v246\n\t" \
  "ds_read_b32 v221, v221 offset:10016\n\t" \
  "v_mfma_f32_16x16x32_bf16 v[180:183], v[212:215], v[112:115], v[180:183]\n\t" \
  "s_cmpk_lt_u32 s4, 3072\n\t" \
  "v_lshlrev_b32_sdwa v224, 1, v186 dst_sel:DWORD dst_unused:UNUSED_PAD src0_sel:DWORD src1_sel:WORD_1\n\t" \
  "s_cbranch_scc1 .Lcnx_w0_4\n.Lcnx_g0_4:\n\t" \
  "ds_read_b32 v220, v220 offset:10016\n\t" \
  "v_mfma_f32_16x16x32_bf16 v[188:191], v[212:215], v[116:119], v[188:191]\n\t" \
  "v_lshlrev_b32_sdwa v227, 1, v185 dst_sel:DWORD dst_unused:UNUSED_PAD src0_sel:DWORD src1_sel:WORD_1\n\t" \
  "v_lshlrev_b32_sdwa v226, 1, v184 dst_sel:DWORD dst_unused:UNUSED_PAD src0_sel:DWORD src1_sel:WORD_1\n\t" \
  "ds_read_b64_tr_b16 v[208:209], v249 offset:6152\n\t" \
  "v_mfma_f32_16x16x32_bf16 v[196:199], v[212:215], v[120:123], v[196:199]\n\t" \
  "v_fma_mix_f32 v229, |v165|, v229, v229 op_sel:[0,1,0] op_sel_hi:[0,1,1] clamp\n\t" \
  "v_fma_mix_f32 v228, |v164|, v228, v228 op_sel:[0,1,0] op_sel_hi:[0,1,1] clamp\n\t" \
  "ds_read_b64_tr_b16 v[210:211], v249 offset:7688\n\t" \
  "ds_read_b32 v223, v223 offset:10016\n\t" \
  "v_mfma_f32_16x16x32_bf16 v[204:207], v[212:215], v[124:127], v[204:207]\n\t" \
  "v_fma_mix_f32 v231, |v167|, v231, v231 op_sel:[0,1,0] op_sel_hi:[0,1,1] clamp\n\t" \
  "v_fma_mix_f32 v230, |v166|, v230, v230 op_sel:[0,1,0] op_sel_hi:[0,1,1] clamp\n\t" \
  "s_waitcnt lgkmcnt(5)\n\t" \
  "ds_read_b64_tr_b16 v[212:213], v249 offset:6160\n\t" \
  "ds_read_b32 v222, v222 offset:10016\n\t" \
  "v_mfma_f32_16x16x32_bf16 v[180:183], v[216:219], v[128:131], v[180:183]\n\t" \
  "s_mov_b32 m0, s44\n\t" \
  "v_or_b32 v172, v232, v233\n\t" \
  "v_or_b32 v173, v234, v235\n\t" \
  "s_mov_b32 exec_hi, 0xffff\n\t" \
  "buffer_load_dwordx4 v252, s[16:19], s33 offen lds\n\t" \
  "v_mfma_f32_16x16x32_bf16 v[188:191], v[216:219], v[132:135], v[188:191]\n\t" \
  "s_mov_b32 exec_hi, -1\n\t" \
  "v_fma_f32 v165, |v165|, v229, v165 mul:4\n\t" \
  "v_fma_f32 v164, |v164|, v228, v164 mul:4\n\t" \
  "s_add_u32 s33, s33, s32\n\t" \
  "ds_read_b64_tr_b16 v[214:215], v249 offset:7696\n\t" \
  "v_mfma_f32_16x16x32_bf16 v[196:199], v[216:219], v[136:139], v[196:199]\n\t" \
  "v_fma_f32 v167, |v167|, v231, v167 mul:4\n\t" \
  "v_fma_f32 v166, |v166|, v230, v166 mul:4\n\t" \
  "ds_read_u16_d16_hi v225, v225 offset:10528\n\t" \
  "v_mfma_f32_16x16x32_bf16 v[204:207], v[216:219], v[140:143], v[204:207]\n\t" \
  "v_cvt_pk_bf16_f32 v164, v164, v165\n\t" \
  "v_cvt_pk_bf16_f32 v165, v166, v167\n\t" \
  "ds_read_b64_tr_b16 v[216:217], v249 offset:6216\n\t" \
  "s_waitcnt lgkmcnt(6)\n\t" \
  "v_mfma_f32_16x16x32_bf16 v[0:3], v[208:211], v[146:149], v[0:3]\n\t" \
  "v_cvt_pknorm_i16_f32 v228, |v194|, |v195|\n\t" \
  "v_cvt_pknorm_i16_f32 v230, |v192|, |v193|\n\t" \
  "ds_read_b64_tr_b16 v[218:219], v249 offset:7752\n\t" \
  "v_mfma_f32_16x16x32_bf16 v[24:27], v[208:211], v[154:157], v[24:27]\n\t" \
  "v_lshlrev_b32_sdwa v229, 2, v228 dst_sel:DWORD dst_unused:UNUSED_PAD src0_sel:DWORD src1_sel:BYTE_3\n\t" \
  "v_lshlrev_b32_sdwa v228, 2, v228 dst_sel:DWORD dst_unused:UNUSED_PAD src0_sel:DWORD src1_sel:BYTE_1\n\t" \
  "ds_read_u16 v224, v224 offset:10528\n\t" \
  "v_mfma_f32_16x16x32_bf16 v[48:51], v[208:211], v[162:165], v[48:51]\n\t" \
  "v_lshlrev_b32_sdwa v231, 2, v230 dst_sel:DWORD dst_unused:UNUSED_PAD src0_sel:DWORD src1_sel:BYTE_3\n\t" \
  "v_lshlrev_b32_sdwa v230, 2, v230 dst_sel:DWORD dst_unused:UNUSED_PAD src0_sel:DWORD src1_sel:BYTE_1\n\t" \
  "ds_read_u16_d16_hi v227, v227 offset:10528\n\t" \
  "v_mfma_f32_16x16x32_bf16 v[72:75], v[208:211], v[170:173], v[72:75]\n\t" \
  "s_add_u32 m0, s44, s35\n\t" \
  "v_lshlrev_b32_sdwa v233, 1, v203 dst_sel:DWORD dst_unused:UNUSED_PAD src0_sel:DWORD src1_sel:WORD_1\n\t" \
  "v_lshlrev_b32_sdwa v232, 1, v202 dst_sel:DWORD dst_unused:UNUSED_PAD src0_sel:DWORD src1_sel:WORD_1\n\t" \
  "s_waitcnt lgkmcnt(5)\n\t" \
  "s_mov_b32 exec_hi, 0xffff\n\t" \
  "buffer_load_dwordx4 v252, s[16:19], s34 offen lds\n\t" \
  "v_mfma_f32_16x16x32_bf16 v[4:7], v[212:215], v[146:149], v[4:7]\n\t" \
  "s_mov_b32 exec_hi, -1\n\t" \
  "v_lshlrev_b32_sdwa v235, 1, v201 dst_sel:DWORD dst_unused:UNUSED_PAD src0_sel:DWORD src1_sel:WORD_1\n\t" \
  "s_add_u32 s34, s34, s32\n\t" \
  "ds_read_b64_tr_b16 v[208:209], v249 offset:6224\n\t" \
  "ds_read_u16 v226, v226 offset:10528\n\t" \
  "v_mfma_f32_16x16x32_bf16 v[28:31], v[212:215], v[154:157], v[28:31]\n\t" \
  "v_lshlrev_b32_sdwa v234, 1, v200 dst_sel:DWORD dst_unused:UNUSED_PAD src0_sel:DWORD src1_sel:WORD_1\n\t" \
  "ds_read_b64_tr_b16 v[210:211], v249 offset:7760\n\t" \
  "ds_read_b32 v229, v229 offset:10016\n\t" \
  "v_mfma_f32_16x16x32_bf16 v[52:55], v[212:215], v[162:165], v[52:55]\n\t" \
  "v_fma_mix_f32 v221, |v179|, v221, v221 op_sel:[0,1,0] op_sel_hi:[0,1,1] clamp\n\t" \
  "v_fma_mix_f32 v220, |v178|, v220, v220 op_sel:[0,1,0] op_sel_hi:[0,1,1] clamp\n\t" \
  "ds_read_b32 v228, v228 offset:10016\n\t" \
  "ds_read_b32 v231, v231 offset:10016\n\t" \
  "v_mfma_f32_16x16x32_bf16 v[76:79], v[212:215], v[170:173], v[76:79]\n\t" \
  "v_fma_mix_f32 v223, |v177|, v223, v223 op_sel:[0,1,0] op_sel_hi:[0,1,1] clamp\n\t" \
  "v_fma_mix_f32 v222, |v176|, v222, v222 op_sel:[0,1,0] op_sel_hi:[0,1,1] clamp\n\t" \
  "s_waitcnt lgkmcnt(4)\n\t" \
  "ds_read_b64_tr_b16 v[212:213], v249 offset:6280\n\t" \
  "ds_read_b32 v230, v230 offset:10016\n\t" \
  "v_mfma_f32_16x16x32_bf16 v[8:11], v[216:219], v[146:149], v[8:11]\n\t" \
  "v_or_b32 v187, v224, v225\n\t" \
  "v_or_b32 v186, v226, v227\n\t" \
  "ds_read_b64_tr_b16 v[214:215], v249 offset:7816\n\t" \
  "v_mfma_f32_16x16x32_bf16 v[32:35], v[216:219], v[154:157], v[32:35]\n\t" \
  "v_fma_f32 v179, |v179|, v221, v179 mul:4\n\t" \
  "v_fma_f32 v178, |v178|, v220, v178 mul:4\n\t" \
  "ds_read_u16_d16_hi v233, v233 offset:10528\n\t" \
  "v_mfma_f32_16x16x32_bf16 v[56:59], v[216:219], v[162:165], v[56:59]\n\t" \
  "v_fma_f32 v177, |v177|, v223, v177 mul:4\n\t" \
  "v_fma_f32 v176, |v176|, v222, v176 mul:4\n\t" \
  "s_add_u32 m0, s25, 16400\n\t" \
  "s_add_u32 s24, s24, s26\n\t" \
  "ds_read_u16 v232, v232 offset:10528\n\t" \
  "v_mfma_f32_16x16x32_bf16 v[80:83], v[216:219], v[170:173], v[80:83]\n\t" \
  "v_cvt_pk_bf16_f32 v179, v178, v179\n\t" \
  "v_cvt_pk_bf16_f32 v178, v176, v177\n\t" \
  "s_waitcnt lgkmcnt(8)\n\t" \
  "buffer_load_dwordx4 v254, s[8:11], s24 offen nt lds\n\t" \
  "ds_read_b64_tr_b16 v[216:217], v249 offset:6288\n\t" \
  "v_mfma_f32_16x16x32_bf16 v[12:15], v[208:211], v[146:149], v[12:15]\n\t" \
  "v_cvt_pknorm_i16_f32 v220, |v180|, |v181|\n\t" \
  "v_cvt_pknorm_i16_f32 v222, |v182|, |v183|\n\t" \
  "ds_read_b64_tr_b16 v[218:219], v249 offset:7824\n\t" \
  "v_mfma_f32_16x16x32_bf16 v[36:39], v[208:211], v[154:157], v[36:39]\n\t" \
  "v_lshlrev_b32_sdwa v221, 2, v220 dst_sel:DWORD dst_unused:UNUSED_PAD src0_sel:DWORD src1_sel:BYTE_3\n\t" \
  "v_lshlrev_b32_sdwa v220, 2, v220 dst_sel:DWORD dst_unused:UNUSED_PAD src0_sel:DWORD src1_sel:BYTE_1\n\t" \
  "ds_read_u16_d16_hi v235, v235 offset:10528\n\t" \
  "v_mfma_f32_16x16x32_bf16 v[60:63], v[208:211], v[162:165], v[60:63]\n\t" \
  "v_lshlrev_b32_sdwa v223, 2, v222 dst_sel:DWORD dst_unused:UNUSED_PAD src0_sel:DWORD src1_sel:BYTE_3\n\t" \
  "v_lshlrev_b32_sdwa v222, 2, v222 dst_sel:DWORD dst_unused:UNUSED_PAD src0_sel:DWORD src1_sel:BYTE_1\n\t" \
  "ds_read_u16 v234, v234 offset:10528\n\t" \
  "v_mfma_f32_16x16x32_bf16 v[84:87], v[208:211], v[170:173], v[84:87]\n\t" \
  "v_lshlrev_b32_sdwa v225, 1, v189 dst_sel:DWORD dst_unused:UNUSED_PAD src0_sel:DWORD src1_sel:WORD_1\n\t" \
  "v_lshlrev_b32_sdwa v224, 1, v188 dst_sel:DWORD dst_unused:UNUSED_PAD src0_sel:DWORD src1_sel:WORD_1\n\t" \
  "ds_read_b64_tr_b16 v[208:209], v248 offset:43048\n\t" \
  "s_waitcnt lgkmcnt(7)\n\t" \
  "v_mfma_f32_16x16x32_bf16 v[16:19], v[212:215], v[146:149], v[16:19]\n\t" \
  "v_lshlrev_b32_sdwa v227, 1, v191 dst_sel:DWORD dst_unused:UNUSED_PAD src0_sel:DWORD src1_sel:WORD_1\n\t" \
  "ds_read_b64_tr_b16 v[210:211], v248 offset:44072\n\t" \
  "ds_read_b32 v221, v221 offset:10016\n\t" \
  "v_mfma_f32_16x16x32_bf16 v[40:43], v[212:215], v[154:157], v[40:43]\n\t" \
  "s_add_u32 s27, s27, s26\n\t" \
  "v_lshlrev_b32_sdwa v226, 1, v190 dst_sel:DWORD dst_unused:UNUSED_PAD src0_sel:DWORD src1_sel:WORD_1\n\t" \
  "v_fma_mix_f32 v229, |v195|, v229, v229 op_sel:[0,1,0] op_sel_hi:[0,1,1] clamp\n\t" \
  "s_mov_b64 exec, s[30:31]\n\t" \
  "buffer_load_dword v237, v254, s[8:11], s27 offen\n\t" \
  "v_mfma_f32_16x16x32_bf16 v[64:67], v[212:215], v[162:165], v[64:67]\n\t" \
  "s_mov_b64 exec, -1\n\t" \
  "v_fma_mix_f32 v228, |v194|, v228, v228 op_sel:[0,1,0] op_sel_hi:[0,1,1] clamp\n\t" \
  "ds_read_b32 v220, v220 offset:10016\n\t" \
  "ds_read_b32 v223, v223 offset:10016\n\t" \
  "v_mfma_f32_16x16x32_bf16 v[88:91], v[212:215], v[170:173], v[88:91]\n\t" \
  "v_fma_mix_f32 v231, |v193|, v231, v231 op_sel:[0,1,0] op_sel_hi:[0,1,1] clamp\n\t" \
  "v_fma_mix_f32 v230, |v192|, v230, v230 op_sel:[0,1,0] op_sel_hi:[0,1,1] clamp\n\t" \
  "s_waitcnt lgkmcnt(5)\n\t" \
  "ds_read_b64_tr_b16 v[212:213], v248 offset:46120\n\t" \
  "ds_read_b32 v222, v222 offset:10016\n\t" \
  "v_mfma_f32_16x16x32_bf16 v[20:23], v[216:219], v[146:149], v[20:23]\n\t" \
  "v_or_b32 v203, v232, v233\n\t" \
  "v_or_b32 v202, v234, v235\n\t" \
  "ds_read_b64_tr_b16 v[214:215], v248 offset:47144\n\t" \
  "v_mfma_f32_16x16x32_bf16 v[44:47], v[216:219], v[154:157], v[44:47]\n\t" \
  "v_fma_f32 v195, |v195|, v229, v195 mul:4\n\t" \
  "v_fma_f32 v194, |v194|, v228, v194 mul:4\n\t" \
  "ds_read_u16_d16_hi v225, v225 offset:10528\n\t" \
  "v_mfma_f32_16x16x32_bf16 v[68:71], v[216:219], v[162:165], v[68:71]\n\t" \
  "v_fma_f32 v193, |v193|, v231, v193 mul:4\n\t" \
  "v_fma_f32 v192, |v192|, v230, v192 mul:4\n\t" \
  "ds_read_u16 v224, v224 offset:10528\n\t" \
  "v_mfma_f32_16x16x32_bf16 v[92:95], v[216:219], v[170:173], v[92:95]\n\t" \
  "v_cvt_pk_bf16_f32 v195, v194, v195\n\t" \
  "v_cvt_pk_bf16_f32 v194, v192, v193\n\t" \
  "ds_read_b64_tr_b16 v[216:217], v248 offset:45096\n\t" \
  "s_waitcnt lgkmcnt(9)\n\t" \
  "v_mfma_f32_16x16x32_bf16 v[144:147], v[208:211], v[96:99], 0\n\t" \
  "v_cvt_pknorm_i16_f32 v228, |v196|, |v197|\n\t" \
  "v_cvt_pknorm_i16_f32 v230, |v198|, |v199|\n\t" \
  "ds_read_b64_tr_b16 v[218:219], v248 offset:48168\n\t" \
  "v_mfma_f32_16x16x32_bf16 v[152:155], v[208:211], v[100:103], 0\n\t" \
  "v_lshlrev_b32_sdwa v229, 2, v228 dst_sel:DWORD dst_unused:UNUSED_PAD src0_sel:DWORD src1_sel:BYTE_3\n\t" \
  "v_lshlrev_b32_sdwa v228, 2, v228 dst_sel:DWORD dst_unused:UNUSED_PAD src0_sel:DWORD src1_sel:BYTE_1\n\t" \
  "ds_read_u16_d16_hi v227, v227 offset:10528\n\t" \
  "v_mfma_f32_16x16x32_bf16 v[160:163], v[208:211], v[104:107], 0\n\t" \
  "v_lshlrev_b32_sdwa v231, 2, v230 dst_sel:DWORD dst_unused:UNUSED_PAD src0_sel:DWORD src1_sel:BYTE_3\n\t" \
  "v_lshlrev_b32_sdwa v230, 2, v230 dst_sel:DWORD dst_unused:UNUSED_PAD src0_sel:DWORD src1_sel:BYTE_1\n\t" \
  "ds_read_u16 v226, v226 offset:10528\n\t" \
  "v_mfma_f32_16x16x32_bf16 v[168:171], v[208:211], v[108:111], 0\n\t" \
  "v_lshlrev_b32_sdwa v233, 1, v205 dst_sel:DWORD dst_unused:UNUSED_PAD src0_sel:DWORD src1_sel:WORD_1\n\t" \
  "v_lshlrev_b32_sdwa v232, 1, v204 dst_sel:DWORD dst_unused:UNUSED_PAD src0_sel:DWORD src1_sel:WORD_1\n\t" \
  "ds_read_b64_tr_b16 v[208:209], v248 offset:43560\n\t" \
  "s_waitcnt lgkmcnt(7)\n\t" \
  "v_mfma_f32_16x16x32_bf16 v[144:147], v[212:215], v[112:115], v[144:147]\n\t" \
  "v_lshlrev_b32_sdwa v235, 1, v207 dst_sel:DWORD dst_unused:UNUSED_PAD src0_sel:DWORD src1_sel:WORD_1\n\t" \
  "ds_read_b64_tr_b16 v[210:211], v248 offset:44584\n\t" \
  "ds_read_b32 v229, v229 offset:10016\n\t" \
  "v_mfma_f32_16x16x32_bf16 v[152:155], v[212:215], v[116:119], v[152:155]\n\t" \
  "v_lshlrev_b32_sdwa v234, 1, v206 dst_sel:DWORD dst_unused:UNUSED_PAD src0_sel:DWORD src1_sel:WORD_1\n\t" \
  "v_fma_mix_f32 v221, |v181|, v221, v221 op_sel:[0,1,0] op_sel_hi:[0,1,1] clamp\n\t" \
  "ds_read_b32 v228, v228 offset:10016\n\t" \
  "v_mfma_f32_16x16x32_bf16 v[160:163], v[212:215], v[120:123], v[160:163]\n\t" \
  "v_fma_mix_f32 v220, |v180|, v220, v220 op_sel:[0,1,0] op_sel_hi:[0,1,1] clamp\n\t" \
  "ds_read_b32 v231, v231 offset:10016\n\t" \
  "ds_read_b32 v230, v230 offset:10016\n\t" \
  "v_mfma_f32_16x16x32_bf16 v[168:171], v[212:215], v[124:127], v[168:171]\n\t" \
  "v_fma_mix_f32 v223, |v183|, v223, v223 op_sel:[0,1,0] op_sel_hi:[0,1,1] clamp\n\t" \
  "v_fma_mix_f32 v222, |v182|, v222, v222 op_sel:[0,1,0] op_sel_hi:[0,1,1] clamp\n\t" \
  "s_waitcnt lgkmcnt(6)\n\t" \
  "ds_read_b64_tr_b16 v[212:213], v248 offset:46632\n\t" \
  "ds_read_u16_d16_hi v233, v233 offset:10528\n\t" \
  "v_mfma_f32_16x16x32_bf16 v[144:147], v[216:219], v[128:131], v[144:147]\n\t" \
  "v_or_b32 v188, v224, v225\n\t" \
  "v_or_b32 v189, v226, v227\n\t" \
  "ds_read_b64_tr_b16 v[214:215], v248 offset:47656\n\t" \
  "v_mfma_f32_16x16x32_bf16 v[152:155], v[216:219], v[132:135], v[152:155]\n\t" \
  "v_fma_f32 v181, |v181|, v221, v181 mul:4\n\t" \
  "v_fma_f32 v180, |v180|, v220, v180 mul:4\n\t" \
  "ds_read_u16 v232, v232 offset:10528\n\t" \
  "v_mfma_f32_16x16x32_bf16 v[160:163], v[216:219], v[136:139], v[160:163]\n\t" \
  "v_fma_f32 v183, |v183|, v223, v183 mul:4\n\t" \
  "v_fma_f32 v182, |v182|, v222, v182 mul:4\n\t" \
  "ds_read_u16_d16_hi v235, v235 offset:10528\n\t" \
  "v_mfma_f32_16x16x32_bf16 v[168:171], v[216:219], v[140:143], v[168:171]\n\t" \
  "v_cvt_pk_bf16_f32 v180, v180, v181\n\t" \
  "v_cvt_pk_bf16_f32 v181, v182, v183\n\t" \
  "ds_read_b64_tr_b16 v[216:217], v248 offset:45608\n\t" \
  "s_waitcnt lgkmcnt(10)\n\t" \
  "v_mfma_f32_16x16x32_bf16 v[148:151], v[208:211], v[96:99], 0\n\t" \
  "v_cvt_pknorm_i16_f32 v220, |v146|, |v147|\n\t" \
  "v_cvt_pknorm_i16_f32 v222, |v144|, |v145|\n\t" \
  "s_waitcnt vmcnt(2)\n\t" \
  "ds_read_b64_tr_b16 v[218:219], v248 offset:48680\n\t" \
  "v_mfma_f32_16x16x32_bf16 v[156:159], v[208:211], v[100:103], 0\n\t" \
  "v_lshlrev_b32_sdwa v221, 2, v220 dst_sel:DWORD dst_unused:UNUSED_PAD src0_sel:DWORD src1_sel:BYTE_3\n\t" \
  "v_lshlrev_b32_sdwa v220, 2, v220 dst_sel:DWORD dst_unused:UNUSED_PAD src0_sel:DWORD src1_sel:BYTE_1\n\t" \
  "s_mov_b64 exec, 3\n\t" \
  "ds_add_rtn_u32 v246, v251, v250 offset:47168\n\t" \
  "v_mfma_f32_16x16x32_bf16 v[164:167], v[208:211], v[104:107], 0\n\t" \
  "s_mov_b64 exec, -1\n\t" \
  "v_lshlrev_b32_sdwa v223, 2, v222 dst_sel:DWORD dst_unused:UNUSED_PAD src0_sel:DWORD src1_sel:BYTE_3\n\t" \
  "v_lshlrev_b32_sdwa v222, 2, v222 dst_sel:DWORD dst_unused:UNUSED_PAD src0_sel:DWORD src1_sel:BYTE_1\n\t" \
  "ds_read_u16 v234, v234 offset:10528\n\t" \
  "v_mfma_f32_16x16x32_bf16 v[172:175], v[208:211], v[108:111], 0\n\t" \
  "s_waitcnt lgkmcnt(1)\n\t" \
  "v_lshlrev_b32_sdwa v225, 1, v155 dst_sel:DWORD dst_unused:UNUSED_PAD src0_sel:DWORD src1_sel:WORD_1\n\t" \
  "v_readfirstlane_b32 s4, v246\n\t" \
  "ds_read_b32 v221, v221 offset:10016\n\t" \
  "v_mfma_f32_16x16x32_bf16 v[148:151], v[212:215], v[112:115], v[148:151]\n\t" \
  "s_cmpk_lt_u32 s4, 3072\n\t" \
  "v_lshlrev_b32_sdwa v224, 1, v154 dst_sel:DWORD dst_unused:UNUSED_PAD src0_sel:DWORD src1_sel:WORD_1\n\t" \
  "s_cbranch_scc1 .Lcnx_w0_5\n.Lcnx_g0_5:\n\t" \
  "ds_read_b32 v220, v220 offset:10016\n\t" \
  "v_mfma_f32_16x16x32_bf16 v[156:159], v[212:215], v[116:119], v[156:159]\n\t" \
  "v_lshlrev_b32_sdwa v227, 1, v153 dst_sel:DWORD dst_unused:UNUSED_PAD src0_sel:DWORD src1_sel:WORD_1\n\t" \
  "v_lshlrev_b32_sdwa v226, 1, v152 dst_sel:DWORD dst_unused:UNUSED_PAD src0_sel:DWORD src1_sel:WORD_1\n\t" \
  "ds_read_b64_tr_b16 v[208:209], v249 offset:12304\n\t" \
  "v_mfma_f32_16x16x32_bf16 v[164:167], v[212:215], v[120:123], v[164:167]\n\t" \
  "v_fma_mix_f32 v229, |v197|, v229, v229 op_sel:[0,1,0] op_sel_hi:[0,1,1] clamp\n\t" \
  "v_fma_mix_f32 v228, |v196|, v228, v228 op_sel:[0,1,0] op_sel_hi:[0,1,1] clamp\n\t" \
  "ds_read_b64_tr_b16 v[210:211], v249 offset:13840\n\t" \
  "ds_read_b32 v223, v223 offset:10016\n\t" \
  "v_mfma_f32_16x16x32_bf16 v[172:175], v[212:215], v[124:127], v[172:175]\n\t" \
  "v_fma_mix_f32 v231, |v199|, v231, v231 op_sel:[0,1,0] op_sel_hi:[0,1,1] clamp\n\t" \
  "v_fma_mix_f32 v230, |v198|, v230, v230 op_sel:[0,1,0] op_sel_hi:[0,1,1] clamp\n\t" \
  "s_waitcnt lgkmcnt(5)\n\t" \
  "ds_read_b64_tr_b16 v[212:213], v249 offset:12312\n\t" \
  "ds_read_b32 v222, v222 offset:10016\n\t" \
  "v_mfma_f32_16x16x32_bf16 v[148:151], v[216:219], v[128:131], v[148:151]\n\t" \
  "s_mov_b32 m0, s45\n\t" \
  "v_or_b32 v204, v232, v233\n\t" \
  "v_or_b32 v205, v234, v235\n\t" \
  "s_mov_b32 exec_hi, 0xffff\n\t" \
  "buffer_load_dwordx4 v252, s[16:19], s33 offen lds\n\t" \
  "v_mfma_f32_16x16x32_bf16 v[156:159], v[216:219], v[132:135], v[156:159]\n\t" \
  "s_mov_b32 exec_hi, -1\n\t" \
  "v_fma_f32 v197, |v197|, v229, v197 mul:4\n\t" \
  "v_fma_f32 v196, |v196|, v228, v196 mul:4\n\t" \
  "s_add_u32 s33, s33, s32\n\t" \
  "ds_read_b64_tr_b16 v[214:215], v249 offset:13848\n\t" \
  "v_mfma_f32_16x16x32_bf16 v[164:167], v[216:219], v[136:139], v[164:167]\n\t" \
  "v_fma_f32 v199, |v199|, v231, v199 mul:4\n\t" \
  "v_fma_f32 v198, |v198|, v230, v198 mul:4\n\t" \
  "ds_read_u16_d16_hi v225, v225 offset:10528\n\t" \
  "v_mfma_f32_16x16x32_bf16 v[172:175], v[216:219], v[140:143], v[172:175]\n\t" \
  "v_cvt_pk_bf16_f32 v196, v196, v197\n\t" \
  "v_cvt_pk_bf16_f32 v197, v198, v199\n\t" \
  "ds_read_b64_tr_b16 v[216:217], v249 offset:12368\n\t" \
  "s_waitcnt lgkmcnt(6)\n\t" \
  "v_mfma_f32_16x16x32_bf16 v[0:3], v[208:211], v[178:181], v[0:3]\n\t" \
  "v_cvt_pknorm_i16_f32 v228, |v162|, |v163|\n\t" \
  "v_cvt_pknorm_i16_f32 v230, |v160|, |v161|\n\t" \
  "ds_read_b64_tr_b16 v[218:219], v249 offset:13904\n\t" \
  "v_mfma_f32_16x16x32_bf16 v[24:27], v[208:211], v[186:189], v[24:27]\n\t" \
  "v_lshlrev_b32_sdwa v229, 2, v228 dst_sel:DWORD dst_unused:UNUSED_PAD src0_sel:DWORD src1_sel:BYTE_3\n\t" \
  "v_lshlrev_b32_sdwa v228, 2, v228 dst_sel:DWORD dst_unused:UNUSED_PAD src0_sel:DWORD src1_sel:BYTE_1\n\t" \
  "ds_read_u16 v224, v224 offset:10528\n\t" \
  "v_mfma_f32_16x16x32_bf16 v[48:51], v[208:211], v[194:197], v[48:51]\n\t" \
  "v_lshlrev_b32_sdwa v231, 2, v230 dst_sel:DWORD dst_unused:UNUSED_PAD src0_sel:DWORD src1_sel:BYTE_3\n\t" \
  "v_lshlrev_b32_sdwa v230, 2, v230 dst_sel:DWORD dst_unused:UNUSED_PAD src0_sel:DWORD src1_sel:BYTE_1\n\t" \
  "ds_read_u16_d16_hi v227, v227 offset:10528\n\t" \
  "v_mfma_f32_16x16x32_bf16 v[72:75], v[208:211], v[202:205], v[72:75]\n\t" \
  "s_add_u32 m0, s45, s35\n\t" \
  "v_lshlrev_b32_sdwa v233, 1, v171 dst_sel:DWORD dst_unused:UNUSED_PAD src0_sel:DWORD src1_sel:WORD_1\n\t" \
  "v_lshlrev_b32_sdwa v232, 1, v170 dst_sel:DWORD dst_unused:UNUSED_PAD src0_sel:DWORD src1_sel:WORD_1\n\t" \
  "s_waitcnt lgkmcnt(5)\n\t" \
  "s_mov_b32 exec_hi, 0xffff\n\t" \
  "buffer_load_dwordx4 v252, s[16:19], s34 offen lds\n\t" \
  "v_mfma_f32_16x16x32_bf16 v[4:7], v[212:215], v[178:181], v[4:7]\n\t" \
  "s_mov_b32 exec_hi, -1\n\t" \
  "v_lshlrev_b32_sdwa v235, 1, v169 dst_sel:DWORD dst_unused:UNUSED_PAD src0_sel:DWORD src1_sel:WORD_1\n\t" \
  "s_add_u32 s34, s34, s32\n\t" \
  "ds_read_b64_tr_b16 v[208:209], v249 offset:12376\n\t" \
  "ds_read_u16 v226, v226 offset:10528\n\t" \
  "v_mfma_f32_16x16x32_bf16 v[28:31], v[212:215], v[186:189], v[28:31]\n\t" \
  "v_lshlrev_b32_sdwa v234, 1, v168 dst_sel:DWORD dst_unused:UNUSED_PAD src0_sel:DWORD src1_sel:WORD_1\n\t" \
  "ds_read_b64_tr_b16 v[210:211], v249 offset:13912\n\t" \
  "ds_read_b32 v229, v229 offset:10016\n\t" \
  "v_mfma_f32_16x16x32_bf16 v[52:55], v[212:215], v[194:197], v[52:55]\n\t" \
  "v_fma_mix_f32 v221, |v147|, v221, v221 op_sel:[0,1,0] op_sel_hi:[0,1,1] clamp\n\t" \
  "v_fma_mix_f32 v220, |v146|, v220, v220 op_sel:[0,1,0] op_sel_hi:[0,1,1] clamp\n\t" \
  "ds_read_b32 v228, v228 offset:10016\n\t" \
  "ds_read_b32 v231, v231 offset:10016\n\t" \
  "v_mfma_f32_16x16x32_bf16 v[76:79], v[212:215], v[202:205], v[76:79]\n\t" \
  "v_fma_mix_f32 v223, |v145|, v223, v223 op_sel:[0,1,0] op_sel_hi:[0,1,1] clamp\n\t" \
  "v_fma_mix_f32 v222, |v144|, v222, v222 op_sel:[0,1,0] op_sel_hi:[0,1,1] clamp\n\t" \
  "s_waitcnt lgkmcnt(4)\n\t" \
  "ds_read_b64_tr_b16 v[212:213], v249 offset:12432\n\t" \
  "ds_read_b32 v230, v230 offset:10016\n\t" \
  "v_mfma_f32_16x16x32_bf16 v[8:11], v[216:219], v[178:181], v[8:11]\n\t" \
  "v_or_b32 v155, v224, v225\n\t" \
  "v_or_b32 v154, v226, v227\n\t" \
  "ds_read_b64_tr_b16 v[214:215], v249 offset:13968\n\t" \
  "v_mfma_f32_16x16x32_bf16 v[32:35], v[216:219], v[186:189], v[32:35]\n\t" \
  "v_fma_f32 v147, |v147|, v221, v147 mul:4\n\t" \
  "v_fma_f32 v146, |v146|, v220, v146 mul:4\n\t" \
  "ds_read_u16_d16_hi v233, v233 offset:10528\n\t" \
  "v_mfma_f32_16x16x32_bf16 v[56:59], v[216:219], v[194:197], v[56:59]\n\t" \
  "v_fma_f32 v145, |v145|, v223, v145 mul:4\n\t" \
  "v_fma_f32 v144, |v144|, v222, v144 mul:4\n\t" \
  "s_add_u32 m0, s25, 24600\n\t" \
  "s_add_u32 s24, s24, s26\n\t" \
  "ds_read_u16 v232, v232 offset:10528\n\t" \
  "v_mfma_f32_16x16x32_bf16 v[80:83], v[216:219], v[202:205], v[80:83]\n\t" \
  "v_cvt_pk_bf16_f32 v147, v146, v147\n\t" \
  "v_cvt_pk_bf16_f32 v146, v144, v145\n\t" \
  "s_waitcnt lgkmcnt(8)\n\t" \
  "buffer_load_dwordx4 v254, s[8:11], s24 offen lds\n\t" \
  "ds_read_b64_tr_b16 v[216:217], v249 offset:12440\n\t" \
  "v_mfma_f32_16x16x32_bf16 v[12:15], v[208:211], v[178:181], v[12:15]\n\t" \
  "v_cvt_pknorm_i16_f32 v220, |v148|, |v149|\n\t" \
  "v_cvt_pknorm_i16_f32 v222, |v150|, |v151|\n\t" \
  "ds_read_b64_tr_b16 v[218:219], v249 offset:13976\n\t" \
  "v_mfma_f32_16x16x32_bf16 v[36:39], v[208:211], v[186:189], v[36:39]\n\t" \
  "v_lshlrev_b32_sdwa v221, 2, v220 dst_sel:DWORD dst_unused:UNUSED_PAD src0_sel:DWORD src1_sel:BYTE_3\n\t" \
  "v_lshlrev_b32_sdwa v220, 2, v220 dst_sel:DWORD dst_unused:UNUSED_PAD src0_sel:DWORD src1_sel:BYTE_1\n\t" \
  "ds_read_u16_d16_hi v235, v235 offset:10528\n\t" \
  "v_mfma_f32_16x16x32_bf16 v[60:63], v[208:211], v[194:197], v[60:63]\n\t" \
  "v_lshlrev_b32_sdwa v223, 2, v222 dst_sel:DWORD dst_unused:UNUSED_PAD src0_sel:DWORD src1_sel:BYTE_3\n\t" \
  "v_lshlrev_b32_sdwa v222, 2, v222 dst_sel:DWORD dst_unused:UNUSED_PAD src0_sel:DWORD src1_sel:BYTE_1\n\t" \
  "ds_read_u16 v234, v234 offset:10528\n\t" \
  "v_mfma_f32_16x16x32_bf16 v[84:87], v[208:211], v[202:205], v[84:87]\n\t" \
  "v_lshlrev_b32_sdwa v225, 1, v157 dst_sel:DWORD dst_unused:UNUSED_PAD src0_sel:DWORD src1_sel:WORD_1\n\t" \
  "v_lshlrev_b32_sdwa v224, 1, v156 dst_sel:DWORD dst_unused:UNUSED_PAD src0_sel:DWORD src1_sel:WORD_1\n\t" \
  "ds_read_b64_tr_b16 v[208:209], v248 offset:49192\n\t" \
  "s_waitcnt lgkmcnt(7)\n\t" \
  "v_mfma_f32_16x16x32_bf16 v[16:19], v[212:215], v[178:181], v[16:19]\n\t" \
  "v_lshlrev_b32_sdwa v227, 1, v159 dst_sel:DWORD dst_unused:UNUSED_PAD src0_sel:DWORD src1_sel:WORD_1\n\t" \
  "ds_read_b64_tr_b16 v[210:211], v248 offset:50216\n\t" \
  "ds_read_b32 v221, v221 offset:10016\n\t" \
  "v_mfma_f32_16x16x32_bf16 v[40:43], v[212:215], v[186:189], v[40:43]\n\t" \
  "s_add_u32 s27, s27, s26\n\t" \
  "v_lshlrev_b32_sdwa v226, 1, v158 dst_sel:DWORD dst_unused:UNUSED_PAD src0_sel:DWORD src1_sel:WORD_1\n\t" \
  "v_fma_mix_f32 v229, |v163|, v229, v229 op_sel:[0,1,0] op_sel_hi:[0,1,1] clamp\n\t" \
  "s_mov_b64 exec, s[30:31]\n\t" \
  "buffer_load_dword v237, v254, s[8:11], s27 offen\n\t" \
  "v_mfma_f32_16x16x32_bf16 v[64:67], v[212:215], v[194:197], v[64:67]\n\t" \
  "s_mov_b64 exec, -1\n\t" \
  "v_fma_mix_f32 v228, |v162|, v228, v228 op_sel:[0,1,0] op_sel_hi:[0,1,1] clamp\n\t" \
  "ds_read_b32 v220, v220 offset:10016\n\t" \
  "ds_read_b32 v223, v223 offset:10016\n\t" \
  "v_mfma_f32_16x16x32_bf16 v[88:91], v[212:215], v[202:205], v[88:91]\n\t" \
  "v_fma_mix_f32 v231, |v161|, v231, v231 op_sel:[0,1,0] op_sel_hi:[0,1,1] clamp\n\t" \
  "v_fma_mix_f32 v230, |v160|, v230, v230 op_sel:[0,1,0] op_sel_hi:[0,1,1] clamp\n\t" \
  "s_waitcnt lgkmcnt(5)\n\t" \
  "ds_read_b64_tr_b16 v[212:213], v248 offset:52264\n\t" \
  "ds_read_b32 v222, v222 offset:10016\n\t" \
  "v_mfma_f32_16x16x32_bf16 v[20:23], v[216:219], v[178:181], v[20:23]\n\t" \
  "v_or_b32 v171, v232, v233\n\t" \
  "v_or_b32 v170, v234, v235\n\t" \
  "ds_read_b64_tr_b16 v[214:215], v248 offset:53288\n\t" \
  "v_mfma_f32_16x16x32_bf16 v[44:47], v[216:219], v[186:189], v[44:47]\n\t" \
  "v_fma_f32 v163, |v163|, v229, v163 mul:4\n\t" \
  "v_fma_f32 v162, |v162|, v228, v162 mul:4\n\t" \
  "ds_read_u16_d16_hi v225, v225 offset:10528\n\t" \
  "v_mfma_f32_16x16x32_bf16 v[68:71], v[216:219], v[194:197], v[68:71]\n\t" \
  "v_fma_f32 v161, |v161|, v231, v161 mul:4\n\t" \
  "v_fma_f32 v160, |v160|, v230, v160 mul:4\n\t" \
  "ds_read_u16 v224, v224 offset:10528\n\t" \
  "v_mfma_f32_16x16x32_bf16 v[92:95], v[216:219], v[202:205], v[92:95]\n\t" \
  "v_cvt_pk_bf16_f32 v163, v162, v163\n\t" \
  "v_cvt_pk_bf16_f32 v162, v160, v161\n\t" \
  "ds_read_b64_tr_b16 v[216:217], v248 offset:51240\n\t" \
  "s_waitcnt lgkmcnt(9)\n\t" \
  "v_mfma_f32_16x16x32_bf16 v[176:179], v[208:211], v[96:99], 0\n\t" \
  "v_cvt_pknorm_i16_f32 v228, |v164|, |v165|\n\t" \
  "v_cvt_pknorm_i16_f32 v230, |v166|, |v167|\n\t" \
  "ds_read_b64_tr_b16 v[218:219], v248 offset:54312\n\t" \
  "v_mfma_f32_16x16x32_bf16 v[184:187], v[208:211], v[100:103], 0\n\t" \
  "v_lshlrev_b32_sdwa v229, 2, v228 dst_sel:DWORD dst_unused:UNUSED_PAD src0_sel:DWORD src1_sel:BYTE_3\n\t" \
  "v_lshlrev_b32_sdwa v228, 2, v228 dst_sel:DWORD dst_unused:UNUSED_PAD src0_sel:DWORD src1_sel:BYTE_1\n\t" \
  "ds_read_u16_d16_hi v227, v227 offset:10528\n\t" \
  "v_mfma_f32_16x16x32_bf16 v[192:195], v[208:211], v[104:107], 0\n\t" \
  "v_lshlrev_b32_sdwa v231, 2, v230 dst_sel:DWORD dst_unused:UNUSED_PAD src0_sel:DWORD src1_sel:BYTE_3\n\t" \
  "v_lshlrev_b32_sdwa v230, 2, v230 dst_sel:DWORD dst_unused:UNUSED_PAD src0_sel:DWORD src1_sel:BYTE_1\n\t" \
  "ds_read_u16 v226, v226 offset:10528\n\t" \
  "v_mfma_f32_16x16x32_bf16 v[200:203], v[208:211], v[108:111], 0\n\t" \
  "v_lshlrev_b32_sdwa v233, 1, v173 dst_sel:DWORD dst_unused:UNUSED_PAD src0_sel:DWORD src1_sel:WORD_1\n\t" \
  "v_lshlrev_b32_sdwa v232, 1, v172 dst_sel:DWORD dst_unused:UNUSED_PAD src0_sel:DWORD src1_sel:WORD_1\n\t" \
  "ds_read_b64_tr_b16 v[208:209], v248 offset:49704\n\t" \
  "s_waitcnt lgkmcnt(7)\n\t" \
  "v_mfma_f32_16x16x32_bf16 v[176:179], v[212:215], v[112:115], v[176:179]\n\t" \
  "v_lshlrev_b32_sdwa v235, 1, v175 dst_sel:DWORD dst_unused:UNUSED_PAD src0_sel:DWORD src1_sel:WORD_1\n\t" \
  "ds_read_b64_tr_b16 v[210:211], v248 offset:50728\n\t" \
  "ds_read_b32 v229, v229 offset:10016\n\t" \
  "v_mfma_f32_16x16x32_bf16 v[184:187], v[212:215], v[116:119], v[184:187]\n\t" \
  "v_lshlrev_b32_sdwa v234, 1, v174 dst_sel:DWORD dst_unused:UNUSED_PAD src0_sel:DWORD src1_sel:WORD_1\n\t" \
  "v_fma_mix_f32 v221, |v149|, v221, v221 op_sel:[0,1,0] op_sel_hi:[0,1,1] clamp\n\t" \
  "ds_read_b32 v228, v228 offset:10016\n\t" \
  "v_mfma_f32_16x16x32_bf16 v[192:195], v[212:215], v[120:123], v[192:195]\n\t" \
  "v_fma_mix_f32 v220, |v148|, v220, v220 op_sel:[0,1,0] op_sel_hi:[0,1,1] clamp\n\t" \
  "ds_read_b32 v231, v231 offset:10016\n\t" \
  "ds_read_b32 v230, v230 offset:10016\n\t" \
  "v_mfma_f32_16x16x32_bf16 v[200:203], v[212:215], v[124:127], v[200:203]\n\t" \
  "v_fma_mix_f32 v223, |v151|, v223, v223 op_sel:[0,1,0] op_sel_hi:[0,1,1] clamp\n\t" \
  "v_fma_mix_f32 v222, |v150|, v222, v222 op_sel:[0,1,0] op_sel_hi:[0,1,1] clamp\n\t" \
  "s_waitcnt lgkmcnt(6)\n\t" \
  "ds_read_b64_tr_b16 v[212:213], v248 offset:52776\n\t" \
  "ds_read_u16_d16_hi v233, v233 offset:10528\n\t" \
  "v_mfma_f32_16x16x32_bf16 v[176:179], v[216:219], v[128:131], v[176:179]\n\t" \
  "v_or_b32 v156, v224, v225\n\t" \
  "v_or_b32 v157, v226, v227\n\t" \
  "ds_read_b64_tr_b16 v[214:215], v248 offset:53800\n\t" \
  "v_mfma_f32_16x16x32_bf16 v[184:187], v[216:219], v[132:135], v[184:187]\n\t" \
  "v_fma_f32 v149, |v149|, v221, v149 mul:4\n\t" \
  "v_fma_f32 v148, |v148|, v220, v148 mul:4\n\t" \
  "ds_read_u16 v232, v232 offset:10528\n\t" \
  "v_mfma_f32_16x16x32_bf16 v[192:195], v[216:219], v[136:139], v[192:195]\n\t" \
  "v_fma_f32 v151, |v151|, v223, v151 mul:4\n\t" \
  "v_fma_f32 v150, |v150|, v222, v150 mul:4\n\t" \
  "ds_read_u16_d16_hi v235, v235 offset:10528\n\t" \
  "v_mfma_f32_16x16x32_bf16 v[200:203], v[216:219], v[140:143], v[200:203]\n\t" \
  "v_cvt_pk_bf16_f32 v148, v148, v149\n\t" \
  "v_cvt_pk_bf16_f32 v149, v150, v151\n\t" \
  "ds_read_b64_tr_b16 v[216:217], v248 offset:51752\n\t" \
  "s_waitcnt lgkmcnt(10)\n\t" \
  "v_mfma_f32_16x16x32_bf16 v[180:183], v[208:211], v[96:99], 0\n\t" \
  "v_cvt_pknorm_i16_f32 v220, |v178|, |v179|\n\t" \
  "v_cvt_pknorm_i16_f32 v222, |v176|, |v177|\n\t" \
  "s_waitcnt vmcnt(2)\n\t" \
  "ds_read_b64_tr_b16 v[218:219], v248 offset:54824\n\t" \
  "v_mfma_f32_16x16x32_bf16 v[188:191], v[208:211], v[100:103], 0\n\t" \
  "v_lshlrev_b32_sdwa v221, 2, v220 dst_sel:DWORD dst_unused:UNUSED_PAD src0_sel:DWORD src1_sel:BYTE_3\n\t" \
  "v_lshlrev_b32_sdwa v220, 2, v220 dst_sel:DWORD dst_unused:UNUSED_PAD src0_sel:DWORD src1_sel:BYTE_1\n\t" \
  "s_mov_b64 exec, 3\n\t" \
  "ds_add_rtn_u32 v246, v251, v250 offset:47176\n\t" \
  "v_mfma_f32_16x16x32_bf16 v[196:199], v[208:211], v[104:107], 0\n\t" \
  "s_mov_b64 exec, -1\n\t" \
  "v_lshlrev_b32_sdwa v223, 2, v222 dst_sel:DWORD dst_unused:UNUSED_PAD src0_sel:DWORD src1_sel:BYTE_3\n\t" \
  "v_lshlrev_b32_sdwa v222, 2, v222 dst_sel:DWORD dst_unused:UNUSED_PAD src0_sel:DWORD src1_sel:BYTE_1\n\t" \
  "ds_read_u16 v234, v234 offset:10528\n\t" \
  "v_mfma_f32_16x16x32_bf16 v[204:207], v[208:211], v[108:111], 0\n\t" \
  "s_waitcnt lgkmcnt(1)\n\t" \
  "v_lshlrev_b32_sdwa v225, 1, v187 dst_sel:DWORD dst_unused:UNUSED_PAD src0_sel:DWORD src1_sel:WORD_1\n\t" \
  "v_readfirstlane_b32 s4, v246\n\t" \
  "ds_read_b32 v221, v221 offset:10016\n\t" \
  "v_mfma_f32_16x16x32_bf16 v[180:183], v[212:215], v[112:115], v[180:183]\n\t" \
  "s_cmpk_lt_u32 s4, 3072\n\t" \
  "v_lshlrev_b32_sdwa v224, 1, v186 dst_sel:DWORD dst_unused:UNUSED_PAD src0_sel:DWORD src1_sel:WORD_1\n\t" \
  "s_cbranch_scc1 .Lcnx_w0_6\n.Lcnx_g0_6:\n\t" \
  "ds_read_b32 v220, v220 offset:10016\n\t" \
  "v_mfma_f32_16x16x32_bf16 v[188:191], v[212:215], v[116:119], v[188:191]\n\t" \
  "v_lshlrev_b32_sdwa v227, 1, v185 dst_sel:DWORD dst_unused:UNUSED_PAD src0_sel:DWORD src1_sel:WORD_1\n\t" \
  "v_lshlrev_b32_sdwa v226, 1, v184 dst_sel:DWORD dst_unused:UNUSED_PAD src0_sel:DWORD src1_sel:WORD_1\n\t" \
  "ds_read_b64_tr_b16 v[208:209], v249 offset:24600\n\t" \
  "v_mfma_f32_16x16x32_bf16 v[196:199], v[212:215], v[120:123], v[196:199]\n\t" \
  "v_fma_mix_f32 v229, |v165|, v229, v229 op_sel:[0,1,0] op_sel_hi:[0,1,1] clamp\n\t" \
  "v_fma_mix_f32 v228, |v164|, v228, v228 op_sel:[0,1,0] op_sel_hi:[0,1,1] clamp\n\t" \
  "ds_read_b64_tr_b16 v[210:211], v249 offset:26136\n\t" \
  "ds_read_b32 v223, v223 offset:10016\n\t" \
  "v_mfma_f32_16x16x32_bf16 v[204:207], v[212:215], v[124:127], v[204:207]\n\t" \
  "v_fma_mix_f32 v231, |v167|, v231, v231 op_sel:[0,1,0] op_sel_hi:[0,1,1] clamp\n\t" \
  "v_fma_mix_f32 v230, |v166|, v230, v230 op_sel:[0,1,0] op_sel_hi:[0,1,1] clamp\n\t" \
  "s_waitcnt lgkmcnt(5)\n\t" \
  "ds_read_b64_tr_b16 v[212:213], v249 offset:24608\n\t" \
  "ds_read_b32 v222, v222 offset:10016\n\t" \
  "v_mfma_f32_16x16x32_bf16 v[180:183], v[216:219], v[128:131], v[180:183]\n\t" \
  "s_mov_b32 m0, s46\n\t" \
  "v_or_b32 v172, v232, v233\n\t" \
  "v_or_b32 v173, v234, v235\n\t" \
  "s_mov_b32 exec_hi, 0xffff\n\t" \
  "buffer_load_dwordx4 v252, s[16:19], s33 offen lds\n\t" \
  "v_mfma_f32_16x16x32_bf16 v[188:191], v[216:219], v[132:135], v[188:191]\n\t" \
  "s_mov_b32 exec_hi, -1\n\t" \
  "v_fma_f32 v165, |v165|, v229, v165 mul:4\n\t" \
  "v_fma_f32 v164, |v164|, v228, v164 mul:4\n\t" \
  "s_add_u32 s33, s33, s32\n\t" \
  "ds_read_b64_tr_b16 v[214:215], v249 offset:26144\n\t" \
  "v_mfma_f32_16x16x32_bf16 v[196:199], v[216:219], v[136:139], v[196:199]\n\t" \
  "v_fma_f32 v167, |v167|, v231, v167 mul:4\n\t" \
  "v_fma_f32 v166, |v166|, v230, v166 mul:4\n\t" \
  "ds_read_u16_d16_hi v225, v225 offset:10528\n\t" \
  "v_mfma_f32_16x16x32_bf16 v[204:207], v[216:219], v[140:143], v[204:207]\n\t" \
  "v_cvt_pk_bf16_f32 v164, v164, v165\n\t" \
  "v_cvt_pk_bf16_f32 v165, v166, v167\n\t" \
  "ds_read_b64_tr_b16 v[216:217], v249 offset:24664\n\t" \
  "s_waitcnt lgkmcnt(6)\n\t" \
  "v_mfma_f32_16x16x32_bf16 v[0:3], v[208:211], v[146:149], v[0:3]\n\t" \
  "v_cvt_pknorm_i16_f32 v228, |v194|, |v195|\n\t" \
  "v_cvt_pknorm_i16_f32 v230, |v192|, |v193|\n\t" \
  "ds_read_b64_tr_b16 v[218:219], v249 offset:26200\n\t" \
  "v_mfma_f32_16x16x32_bf16 v[24:27], v[208:211], v[154:157], v[24:27]\n\t" \
  "v_lshlrev_b32_sdwa v229, 2, v228 dst_sel:DWORD dst_unused:UNUSED_PAD src0_sel:DWORD src1_sel:BYTE_3\n\t" \
  "v_lshlrev_b32_sdwa v228, 2, v228 dst_sel:DWORD dst_unused:UNUSED_PAD src0_sel:DWORD src1_sel:BYTE_1\n\t" \
  "ds_read_u16 v224, v224 offset:10528\n\t" \
  "v_mfma_f32_16x16x32_bf16 v[48:51], v[208:211], v[162:165], v[48:51]\n\t" \
  "v_lshlrev_b32_sdwa v231, 2, v230 dst_sel:DWORD dst_unused:UNUSED_PAD src0_sel:DWORD src1_sel:BYTE_3\n\t" \
  "v_lshlrev_b32_sdwa v230, 2, v230 dst_sel:DWORD dst_unused:UNUSED_PAD src0_sel:DWORD src1_sel:BYTE_1\n\t" \
  "ds_read_u16_d16_hi v227, v227 offset:10528\n\t" \
  "v_mfma_f32_16x16x32_bf16 v[72:75], v[208:211], v[170:173], v[72:75]\n\t" \
  "s_add_u32 m0, s46, s35\n\t" \
  "v_lshlrev_b32_sdwa v233, 1, v203 dst_sel:DWORD dst_unused:UNUSED_PAD src0_sel:DWORD src1_sel:WORD_1\n\t" \
  "v_lshlrev_b32_sdwa v232, 1, v202 dst_sel:DWORD dst_unused:UNUSED_PAD src0_sel:DWORD src1_sel:WORD_1\n\t" \
  "s_waitcnt lgkmcnt(5)\n\t" \
  "s_mov_b32 exec_hi, 0xffff\n\t" \
  "buffer_load_dwordx4 v252, s[16:19], s34 offen lds\n\t" \
  "v_mfma_f32_16x16x32_bf16 v[4:7], v[212:215], v[146:149], v[4:7]\n\t" \
  "s_mov_b32 exec_hi, -1\n\t" \
  "v_lshlrev_b32_sdwa v235, 1, v201 dst_sel:DWORD dst_unused:UNUSED_PAD src0_sel:DWORD src1_sel:WORD_1\n\t" \
  "s_add_u32 s34, s34, s32\n\t" \
  "ds_read_b64_tr_b16 v[208:209], v249 offset:24672\n\t" \
  "ds_read_u16 v226, v226 offset:10528\n\t" \
  "v_mfma_f32_16x16x32_bf16 v[28:31], v[212:215], v[154:157], v[28:31]\n\t" \
  "v_lshlrev_b32_sdwa v234, 1, v200 dst_sel:DWORD dst_unused:UNUSED_PAD src0_sel:DWORD src1_sel:WORD_1\n\t" \
  "ds_read_b64_tr_b16 v[210:211], v249 offset:26208\n\t" \
  "ds_read_b32 v229, v229 offset:10016\n\t" \
  "v_mfma_f32_16x16x32_bf16 v[52:55], v[212:215], v[162:165], v[52:55]\n\t" \
  "v_fma_mix_f32 v221, |v179|, v221, v221 op_sel:[0,1,0] op_sel_hi:[0,1,1] clamp\n\t" \
  "v_fma_mix_f32 v220, |v178|, v220, v220 op_sel:[0,1,0] op_sel_hi:[0,1,1] clamp\n\t" \
  "ds_read_b32 v228, v228 offset:10016\n\t" \
  "ds_read_b32 v231, v231 offset:10016\n\t" \
  "v_mfma_f32_16x16x32_bf16 v[76:79], v[212:215], v[170:173], v[76:79]\n\t" \
  "v_fma_mix_f32 v223, |v177|, v223, v223 op_sel:[0,1,0] op_sel_hi:[0,1,1] clamp\n\t" \
  "v_fma_mix_f32 v222, |v176|, v222, v222 op_sel:[0,1,0] op_sel_hi:[0,1,1] clamp\n\t" \
  "s_waitcnt lgkmcnt(4)\n\t" \
  "ds_read_b64_tr_b16 v[212:213], v249 offset:24728\n\t" \
  "ds_read_b32 v230, v230 offset:10016\n\t" \
  "v_mfma_f32_16x16x32_bf16 v[8:11], v[216:219], v[146:149], v[8:11]\n\t" \
  "v_or_b32 v187, v224, v225\n\t" \
  "v_or_b32 v186, v226, v227\n\t" \
  "ds_read_b64_tr_b16 v[214:215], v249 offset:26264\n\t" \
  "v_mfma_f32_16x16x32_bf16 v[32:35], v[216:219], v[154:157], v[32:35]\n\t" \
  "v_fma_f32 v179, |v179|, v221, v179 mul:4\n\t" \
  "v_fma_f32 v178, |v178|, v220, v178 mul:4\n\t" \
  "ds_read_u16_d16_hi v233, v233 offset:10528\n\t" \
  "v_mfma_f32_16x16x32_bf16 v[56:59], v[216:219], v[162:165], v[56:59]\n\t" \
  "v_fma_f32 v177, |v177|, v223, v177 mul:4\n\t" \
  "v_fma_f32 v176, |v176|, v222, v176 mul:4\n\t" \
  "s_add_u32 m0, s25, 32800\n\t" \
  "s_add_u32 s24, s24, s26\n\t" \
  "ds_read_u16 v232, v232 offset:10528\n\t" \
  "v_mfma_f32_16x16x32_bf16 v[80:83], v[216:219], v[170:173], v[80:83]\n\t" \
  "v_cvt_pk_bf16_f32 v179, v178, v179\n\t" \
  "v_cvt_pk_bf16_f32 v178, v176, v177\n\t" \
  "s_waitcnt lgkmcnt(8)\n\t" \
  "buffer_load_dwordx4 v254, s[8:11], s24 offen lds\n\t" \
  "ds_read_b64_tr_b16 v[216:217], v249 offset:24736\n\t" \
  "v_mfma_f32_16x16x32_bf16 v[12:15], v[208:211], v[146:149], v[12:15]\n\t" \
  "v_cvt_pknorm_i16_f32 v220, |v180|, |v181|\n\t" \
  "v_cvt_pknorm_i16_f32 v222, |v182|, |v183|\n\t" \
  "ds_read_b64_tr_b16 v[218:219], v249 offset:26272\n\t" \
  "v_mfma_f32_16x16x32_bf16 v[36:39], v[208:211], v[154:157], v[36:39]\n\t" \
  "v_lshlrev_b32_sdwa v221, 2, v220 dst_sel:DWORD dst_unused:UNUSED_PAD src0_sel:DWORD src1_sel:BYTE_3\n\t" \
  "v_lshlrev_b32_sdwa v220, 2, v220 dst_sel:DWORD dst_unused:UNUSED_PAD src0_sel:DWORD src1_sel:BYTE_1\n\t" \
  "ds_read_u16_d16_hi v235, v235 offset:10528\n\t" \
  "v_mfma_f32_16x16x32_bf16 v[60:63], v[208:211], v[162:165], v[60:63]\n\t" \
  "v_lshlrev_b32_sdwa v223, 2, v222 dst_sel:DWORD dst_unused:UNUSED_PAD src0_sel:DWORD src1_sel:BYTE_3\n\t" \
  "v_lshlrev_b32_sdwa v222, 2, v222 dst_sel:DWORD dst_unused:UNUSED_PAD src0_sel:DWORD src1_sel:BYTE_1\n\t" \
  "ds_read_u16 v234, v234 offset:10528\n\t" \
  "v_mfma_f32_16x16x32_bf16 v[84:87], v[208:211], v[170:173], v[84:87]\n\t" \
  "v_lshlrev_b32_sdwa v225, 1, v189 dst_sel:DWORD dst_unused:UNUSED_PAD src0_sel:DWORD src1_sel:WORD_1\n\t" \
  "v_lshlrev_b32_sdwa v224, 1, v188 dst_sel:DWORD dst_unused:UNUSED_PAD src0_sel:DWORD src1_sel:WORD_1\n\t" \
  "ds_read_b64_tr_b16 v[208:209], v248 offset:36904\n\t" \
  "s_waitcnt lgkmcnt(7)\n\t" \
  "v_mfma_f32_16x16x32_bf16 v[16:19], v[212:215], v[146:149], v[16:19]\n\t" \
  "v_lshlrev_b32_sdwa v227, 1, v191 dst_sel:DWORD dst_unused:UNUSED_PAD src0_sel:DWORD src1_sel:WORD_1\n\t" \
  "ds_read_b64_tr_b16 v[210:211], v248 offset:37928\n\t" \
  "ds_read_b32 v221, v221 offset:10016\n\t" \
  "v_mfma_f32_16x16x32_bf16 v[40:43], v[212:215], v[154:157], v[40:43]\n\t" \
  "s_add_u32 s27, s27, s26\n\t" \
  "v_lshlrev_b32_sdwa v226, 1, v190 dst_sel:DWORD dst_unused:UNUSED_PAD src0_sel:DWORD src1_sel:WORD_1\n\t" \
  "v_fma_mix_f32 v229, |v195|, v229, v229 op_sel:[0,1,0] op_sel_hi:[0,1,1] clamp\n\t" \
  "s_mov_b64 exec, s[30:31]\n\t" \
  "buffer_load_dword v237, v254, s[8:11], s27 offen\n\t" \
  "v_mfma_f32_16x16x32_bf16 v[64:67], v[212:215], v[162:165], v[64:67]\n\t" \
  "s_mov_b64 exec, -1\n\t" \
  "v_fma_mix_f32 v228, |v194|, v228, v228 op_sel:[0,1,0] op_sel_hi:[0,1,1] clamp\n\t" \
  "ds_read_b32 v220, v220 offset:10016\n\t" \
  "ds_read_b32 v223, v223 offset:10016\n\t" \
  "v_mfma_f32_16x16x32_bf16 v[88:91], v[212:215], v[170:173], v[88:91]\n\t" \
  "v_fma_mix_f32 v231, |v193|, v231, v231 op_sel:[0,1,0] op_sel_hi:[0,1,1] clamp\n\t" \
  "v_fma_mix_f32 v230, |v192|, v230, v230 op_sel:[0,1,0] op_sel_hi:[0,1,1] clamp\n\t" \
  "s_waitcnt lgkmcnt(5)\n\t" \
  "ds_read_b64_tr_b16 v[212:213], v248 offset:39976\n\t" \
  "ds_read_b32 v222, v222 offset:10016\n\t" \
  "v_mfma_f32_16x16x32_bf16 v[20:23], v[216:219], v[146:149], v[20:23]\n\t" \
  "v_or_b32 v203, v232, v233\n\t" \
  "v_or_b32 v202, v234, v235\n\t" \
  "ds_read_b64_tr_b16 v[214:215], v248 offset:41000\n\t" \
  "v_mfma_f32_16x16x32_bf16 v[44:47], v[216:219], v[154:157], v[44:47]\n\t" \
  "v_fma_f32 v195, |v195|, v229, v195 mul:4\n\t" \
  "v_fma_f32 v194, |v194|, v228, v194 mul:4\n\t" \
  "ds_read_u16_d16_hi v225, v225 offset:10528\n\t" \
  "v_mfma_f32_16x16x32_bf16 v[68:71], v[216:219], v[162:165], v[68:71]\n\t" \
  "v_fma_f32 v193, |v193|, v231, v193 mul:4\n\t" \
  "v_fma_f32 v192, |v192|, v230, v192 mul:4\n\t" \
  "ds_read_u16 v224, v224 offset:10528\n\t" \
  "v_mfma_f32_16x16x32_bf16 v[92:95], v[216:219], v[170:173], v[92:95]\n\t" \
  "v_cvt_pk_bf16_f32 v195, v194, v195\n\t" \
  "v_cvt_pk_bf16_f32 v194, v192, v193\n\t" \
  "ds_read_b64_tr_b16 v[216:217], v248 offset:38952\n\t" \
  "s_waitcnt lgkmcnt(9)\n\t" \
  "v_mfma_f32_16x16x32_bf16 v[144:147], v[208:211], v[96:99], 0\n\t" \
  "v_cvt_pknorm_i16_f32 v228, |v196|, |v197|\n\t" \
  "v_cvt_pknorm_i16_f32 v230, |v198|, |v199|\n\t" \
  "ds_read_b64_tr_b16 v[218:219], v248 offset:42024\n\t" \
  "v_mfma_f32_16x16x32_bf16 v[152:155], v[208:211], v[100:103], 0\n\t" \
  "v_lshlrev_b32_sdwa v229, 2, v228 dst_sel:DWORD dst_unused:UNUSED_PAD src0_sel:DWORD src1_sel:BYTE_3\n\t" \
  "v_lshlrev_b32_sdwa v228, 2, v228 dst_sel:DWORD dst_unused:UNUSED_PAD src0_sel:DWORD src1_sel:BYTE_1\n\t" \
  "ds_read_u16_d16_hi v227, v227 offset:10528\n\t" \
  "v_mfma_f32_16x16x32_bf16 v[160:163], v[208:211], v[104:107], 0\n\t" \
  "v_lshlrev_b32_sdwa v231, 2, v230 dst_sel:DWORD dst_unused:UNUSED_PAD src0_sel:DWORD src1_sel:BYTE_3\n\t" \
  "v_lshlrev_b32_sdwa v230, 2, v230 dst_sel:DWORD dst_unused:UNUSED_PAD src0_sel:DWORD src1_sel:BYTE_1\n\t" \
  "ds_read_u16 v226, v226 offset:10528\n\t" \
  "v_mfma_f32_16x16x32_bf16 v[168:171], v[208:211], v[108:111], 0\n\t" \
  "v_lshlrev_b32_sdwa v233, 1, v205 dst_sel:DWORD dst_unused:UNUSED_PAD src0_sel:DWORD src1_sel:WORD_1\n\t" \
  "v_lshlrev_b32_sdwa v232, 1, v204 dst_sel:DWORD dst_unused:UNUSED_PAD src0_sel:DWORD src1_sel:WORD_1\n\t" \
  "ds_read_b64_tr_b16 v[208:209], v248 offset:37416\n\t" \
  "s_waitcnt lgkmcnt(7)\n\t" \
  "v_mfma_f32_16x16x32_bf16 v[144:147], v[212:215], v[112:115], v[144:147]\n\t" \
  "v_lshlrev_b32_sdwa v235, 1, v207 dst_sel:DWORD dst_unused:UNUSED_PAD src0_sel:DWORD src1_sel:WORD_1\n\t" \
  "ds_read_b64_tr_b16 v[210:211], v248 offset:38440\n\t" \
  "ds_read_b32 v229, v229 offset:10016\n\t" \
  "v_mfma_f32_16x16x32_bf16 v[152:155], v[212:215], v[116:119], v[152:155]\n\t" \
  "v_lshlrev_b32_sdwa v234, 1, v206 dst_sel:DWORD dst_unused:UNUSED_PAD src0_sel:DWORD src1_sel:WORD_1\n\t" \
  "v_fma_mix_f32 v221, |v181|, v221, v221 op_sel:[0,1,0] op_sel_hi:[0,1,1] clamp\n\t" \
  "ds_read_b32 v228, v228 offset:10016\n\t" \
  "v_mfma_f32_16x16x32_bf16 v[160:163], v[212:215], v[120:123], v[160:163]\n\t" \
  "v_fma_mix_f32 v220, |v180|, v220, v220 op_sel:[0,1,0] op_sel_hi:[0,1,1] clamp\n\t" \
  "ds_read_b32 v231, v231 offset:10016\n\t" \
  "ds_read_b32 v230, v230 offset:10016\n\t" \
  "v_mfma_f32_16x16x32_bf16 v[168:171], v[212:215], v[124:127], v[168:171]\n\t" \
  "v_fma_mix_f32 v223, |v183|, v223, v223 op_sel:[0,1,0] op_sel_hi:[0,1,1] clamp\n\t" \
  "v_fma_mix_f32 v222, |v182|, v222, v222 op_sel:[0,1,0] op_sel_hi:[0,1,1] clamp\n\t" \
  "s_waitcnt lgkmcnt(6)\n\t" \
  "ds_read_b64_tr_b16 v[212:213], v248 offset:40488\n\t" \
  "ds_read_u16_d16_hi v233, v233 offset:10528\n\t" \
  "v_mfma_f32_16x16x32_bf16 v[144:147], v[216:219], v[128:131], v[144:147]\n\t" \
  "v_or_b32 v188, v224, v225\n\t" \
  "v_or_b32 v189, v226, v227\n\t" \
  "ds_read_b64_tr_b16 v[214:215], v248 offset:41512\n\t" \
  "v_mfma_f32_16x16x32_bf16 v[152:155], v[216:219], v[132:135], v[152:155]\n\t" \
  "v_fma_f32 v181, |v181|, v221, v181 mul:4\n\t" \
  "v_fma_f32 v180, |v180|, v220, v180 mul:4\n\t" \
  "ds_read_u16 v232, v232 offset:10528\n\t" \
  "v_mfma_f32_16x16x32_bf16 v[160:163], v[216:219], v[136:139], v[160:163]\n\t" \
  "v_fma_f32 v183, |v183|, v223, v183 mul:4\n\t" \
  "v_fma_f32 v182, |v182|, v222, v182 mul:4\n\t" \
  "ds_read_u16_d16_hi v235, v235 offset:10528\n\t" \
  "v_mfma_f32_16x16x32_bf16 v[168:171], v[216:219], v[140:143], v[168:171]\n\t" \
  "v_cvt_pk_bf16_f32 v180, v180, v181\n\t" \
  "v_cvt_pk_bf16_f32 v181, v182, v183\n\t" \
  "ds_read_b64_tr_b16 v[216:217], v248 offset:39464\n\t" \
  "s_waitcnt lgkmcnt(10)\n\t" \
  "v_mfma_f32_16x16x32_bf16 v[148:151], v[208:211], v[96:99], 0\n\t" \
  "v_cvt_pknorm_i16_f32 v220, |v146|, |v147|\n\t" \
  "v_cvt_pknorm_i16_f32 v222, |v144|, |v145|\n\t" \
  "s_waitcnt vmcnt(2)\n\t" \
  "ds_read_b64_tr_b16 v[218:219], v248 offset:42536\n\t" \
  "v_mfma_f32_16x16x32_bf16 v[156:159], v[208:211], v[100:103], 0\n\t" \
  "v_lshlrev_b32_sdwa v221, 2, v220 dst_sel:DWORD dst_unused:UNUSED_PAD src0_sel:DWORD src1_sel:BYTE_3\n\t" \
  "v_lshlrev_b32_sdwa v220, 2, v220 dst_sel:DWORD dst_unused:UNUSED_PAD src0_sel:DWORD src1_sel:BYTE_1\n\t" \
  "s_mov_b64 exec, 3\n\t" \
  "ds_add_rtn_u32 v246, v251, v250 offset:47184\n\t" \
  "v_mfma_f32_16x16x32_bf16 v[164:167], v[208:211], v[104:107], 0\n\t" \
  "s_mov_b64 exec, -1\n\t" \
  "v_lshlrev_b32_sdwa v223, 2, v222 dst_sel:DWORD dst_unused:UNUSED_PAD src0_sel:DWORD src1_sel:BYTE_3\n\t" \
  "v_lshlrev_b32_sdwa v222, 2, v222 dst_sel:DWORD dst_unused:UNUSED_PAD src0_sel:DWORD src1_sel:BYTE_1\n\t" \
  "ds_read_u16 v234, v234 offset:10528\n\t" \
  "v_mfma_f32_16x16x32_bf16 v[172:175], v[208:211], v[108:111], 0\n\t" \
  "s_waitcnt lgkmcnt(1)\n\t" \
  "v_lshlrev_b32_sdwa v225, 1, v155 dst_sel:DWORD dst_unused:UNUSED_PAD src0_sel:DWORD src1_sel:WORD_1\n\t" \
  "v_readfirstlane_b32 s4, v246\n\t" \
  "ds_read_b32 v221, v221 offset:10016\n\t" \
  "v_mfma_f32_16x16x32_bf16 v[148:151], v[212:215], v[112:115], v[148:151]\n\t" \
  "s_cmpk_lt_u32 s4, 3072\n\t" \
  "v_lshlrev_b32_sdwa v224, 1, v154 dst_sel:DWORD dst_unused:UNUSED_PAD src0_sel:DWORD src1_sel:WORD_1\n\t" \
  "s_cbranch_scc1 .Lcnx_w0_7\n.Lcnx_g0_7:\n\t" \
  "ds_read_b32 v220, v220 offset:10016\n\t" \
  "v_mfma_f32_16x16x32_bf16 v[156:159], v[212:215], v[116:119], v[156:159]\n\t" \
  "v_lshlrev_b32_sdwa v227, 1, v153 dst_sel:DWORD dst_unused:UNUSED_PAD src0_sel:DWORD src1_sel:WORD_1\n\t" \
  "v_lshlrev_b32_sdwa v226, 1, v152 dst_sel:DWORD dst_unused:UNUSED_PAD src0_sel:DWORD src1_sel:WORD_1\n\t" \
  "ds_read_b64_tr_b16 v[208:209], v249 offset:30752\n\t" \
  "v_mfma_f32_16x16x32_bf16 v[164:167], v[212:215], v[120:123], v[164:167]\n\t" \
  "v_fma_mix_f32 v229, |v197|, v229, v229 op_sel:[0,1,0] op_sel_hi:[0,1,1] clamp\n\t" \
  "v_fma_mix_f32 v228, |v196|, v228, v228 op_sel:[0,1,0] op_sel_hi:[0,1,1] clamp\n\t" \
  "ds_read_b64_tr_b16 v[210:211], v249 offset:32288\n\t" \
  "ds_read_b32 v223, v223 offset:10016\n\t" \
  "v_mfma_f32_16x16x32_bf16 v[172:175], v[212:215], v[124:127], v[172:175]\n\t" \
  "v_fma_mix_f32 v231, |v199|, v231, v231 op_sel:[0,1,0] op_sel_hi:[0,1,1] clamp\n\t" \
  "v_fma_mix_f32 v230, |v198|, v230, v230 op_sel:[0,1,0] op_sel_hi:[0,1,1] clamp\n\t" \
  "s_waitcnt lgkmcnt(5)\n\t" \
  "ds_read_b64_tr_b16 v[212:213], v249 offset:30760\n\t" \
  "ds_read_b32 v222, v222 offset:10016\n\t" \
  "v_mfma_f32_16x16x32_bf16 v[148:151], v[216:219], v[128:131], v[148:151]\n\t" \
  "s_mov_b32 m0, s47\n\t" \
  "v_or_b32 v204, v232, v233\n\t" \
  "v_or_b32 v205, v234, v235\n\t" \
  "s_mov_b32 exec_hi, 0xffff\n\t" \
  "buffer_load_dwordx4 v252, s[16:19], s33 offen lds\n\t" \
  "v_mfma_f32_16x16x32_bf16 v[156:159], v[216:219], v[132:135], v[156:159]\n\t" \
  "s_mov_b32 exec_hi, -1\n\t" \
  "v_fma_f32 v197, |v197|, v229, v197 mul:4\n\t" \
  "v_fma_f32 v196, |v196|, v228, v196 mul:4\n\t" \
  "s_add_u32 s33, s33, s32\n\t" \
  "ds_read_b64_tr_b16 v[214:215], v249 offset:32296\n\t" \
  "v_mfma_f32_16x16x32_bf16 v[164:167], v[216:219], v[136:139], v[164:167]\n\t" \
  "v_fma_f32 v199, |v199|, v231, v199 mul:4\n\t" \
  "v_fma_f32 v198, |v198|, v230, v198 mul:4\n\t" \
  "ds_read_u16_d16_hi v225, v225 offset:10528\n\t" \
  "v_mfma_f32_16x16x32_bf16 v[172:175], v[216:219], v[140:143], v[172:175]\n\t" \
  "v_cvt_pk_bf16_f32 v196, v196, v197\n\t" \
  "v_cvt_pk_bf16_f32 v197, v198, v199\n\t" \
  "ds_read_b64_tr_b16 v[216:217], v249 offset:30816\n\t" \
  "s_waitcnt lgkmcnt(6)\n\t" \
  "v_mfma_f32_16x16x32_bf16 v[0:3], v[208:211], v[178:181], v[0:3]\n\t" \
  "v_cvt_pknorm_i16_f32 v228, |v162|, |v163|\n\t" \
  "v_cvt_pknorm_i16_f32 v230, |v160|, |v161|\n\t" \
  "ds_read_b64_tr_b16 v[218:219], v249 offset:32352\n\t" \
  "v_mfma_f32_16x16x32_bf16 v[24:27], v[208:211], v[186:189], v[24:27]\n\t" \
  "v_lshlrev_b32_sdwa v229, 2, v228 dst_sel:DWORD dst_unused:UNUSED_PAD src0_sel:DWORD src1_sel:BYTE_3\n\t" \
  "v_lshlrev_b32_sdwa v228, 2, v228 dst_sel:DWORD dst_unused:UNUSED_PAD src0_sel:DWORD src1_sel:BYTE_1\n\t" \
  "ds_read_u16 v224, v224 offset:10528\n\t" \
  "v_mfma_f32_16x16x32_bf16 v[48:51], v[208:211], v[194:197], v[48:51]\n\t" \
  "v_lshlrev_b32_sdwa v231, 2, v230 dst_sel:DWORD dst_unused:UNUSED_PAD src0_sel:DWORD src1_sel:BYTE_3\n\t" \
  "v_lshlrev_b32_sdwa v230, 2, v230 dst_sel:DWORD dst_unused:UNUSED_PAD src0_sel:DWORD src1_sel:BYTE_1\n\t" \
  "ds_read_u16_d16_hi v227, v227 offset:10528\n\t" \
  "v_mfma_f32_16x16x32_bf16 v[72:75], v[208:211], v[202:205], v[72:75]\n\t" \
  "s_add_u32 m0, s47, s35\n\t" \
  "v_lshlrev_b32_sdwa v233, 1, v171 dst_sel:DWORD dst_unused:UNUSED_PAD src0_sel:DWORD src1_sel:WORD_1\n\t" \
  "v_lshlrev_b32_sdwa v232, 1, v170 dst_sel:DWORD dst_unused:UNUSED_PAD src0_sel:DWORD src1_sel:WORD_1\n\t" \
  "s_waitcnt lgkmcnt(5)\n\t" \
  "s_mov_b32 exec_hi, 0xffff\n\t" \
  "buffer_load_dwordx4 v252, s[16:19], s34 offen lds\n\t" \
  "v_mfma_f32_16x16x32_bf16 v[4:7], v[212:215], v[178:181], v[4:7]\n\t" \
  "s_mov_b32 exec_hi, -1\n\t" \
  "v_lshlrev_b32_sdwa v235, 1, v169 dst_sel:DWORD dst_unused:UNUSED_PAD src0_sel:DWORD src1_sel:WORD_1\n\t" \
  "s_add_u32 s34, s34, s32\n\t" \
  "ds_read_b64_tr_b16 v[208:209], v249 offset:30824\n\t" \
  "ds_read_u16 v226, v226 offset:10528\n\t" \
  "v_mfma_f32_16x16x32_bf16 v[28:31], v[212:215], v[186:189], v[28:31]\n\t" \
  "v_lshlrev_b32_sdwa v234, 1, v168 dst_sel:DWORD dst_unused:UNUSED_PAD src0_sel:DWORD src1_sel:WORD_1\n\t" \
  "ds_read_b64_tr_b16 v[210:211], v249 offset:32360\n\t" \
  "ds_read_b32 v229, v229 offset:10016\n\t" \
  "v_mfma_f32_16x16x32_bf16 v[52:55], v[212:215], v[194:197], v[52:55]\n\t" \
  "v_fma_mix_f32 v221, |v147|, v221, v221 op_sel:[0,1,0] op_sel_hi:[0,1,1] clamp\n\t" \
  "v_fma_mix_f32 v220, |v146|, v220, v220 op_sel:[0,1,0] op_sel_hi:[0,1,1] clamp\n\t" \
  "ds_read_b32 v228, v228 offset:10016\n\t" \
  "ds_read_b32 v231, v231 offset:10016\n\t" \
  "v_mfma_f32_16x16x32_bf16 v[76:79], v[212:215], v[202:205], v[76:79]\n\t" \
  "v_fma_mix_f32 v223, |v145|, v223, v223 op_sel:[0,1,0] op_sel_hi:[0,1,1] clamp\n\t" \
  "v_fma_mix_f32 v222, |v144|, v222, v222 op_sel:[0,1,0] op_sel_hi:[0,1,1] clamp\n\t" \
  "s_waitcnt lgkmcnt(4)\n\t" \
  "ds_read_b64_tr_b16 v[212:213], v249 offset:30880\n\t" \
  "ds_read_b32 v230, v230 offset:10016\n\t" \
  "v_mfma_f32_16x16x32_bf16 v[8:11], v[216:219], v[178:181], v[8:11]\n\t" \
  "v_or_b32 v155, v224, v225\n\t" \
  "v_or_b32 v154, v226, v227\n\t" \
  "ds_read_b64_tr_b16 v[214:215], v249 offset:32416\n\t" \
  "v_mfma_f32_16x16x32_bf16 v[32:35], v[216:219], v[186:189], v[32:35]\n\t" \
  "v_fma_f32 v147, |v147|, v221, v147 mul:4\n\t" \
  "v_fma_f32 v146, |v146|, v220, v146 mul:4\n\t" \
  "ds_read_u16_d16_hi v233, v233 offset:10528\n\t" \
  "v_mfma_f32_16x16x32_bf16 v[56:59], v[216:219], v[194:197], v[56:59]\n\t" \
  "v_fma_f32 v145, |v145|, v223, v145 mul:4\n\t" \
  "v_fma_f32 v144, |v144|, v222, v144 mul:4\n\t" \
  "s_add_u32 m0, s25, 41000\n\t" \
  "s_add_u32 s24, s24, s26\n\t" \
  "ds_read_u16 v232, v232 offset:10528\n\t" \
  "v_mfma_f32_16x16x32_bf16 v[80:83], v[216:219], v[202:205], v[80:83]\n\t" \
  "v_cvt_pk_bf16_f32 v147, v146, v147\n\t" \
  "v_cvt_pk_bf16_f32 v146, v144, v145\n\t" \
  "s_waitcnt lgkmcnt(8)\n\t" \
  "buffer_load_dwordx4 v254, s[8:11], s24 offen lds\n\t" \
  "ds_read_b64_tr_b16 v[216:217], v249 offset:30888\n\t" \
  "v_mfma_f32_16x16x32_bf16 v[12:15], v[208:211], v[178:181], v[12:15]\n\t" \
  "v_cvt_pknorm_i16_f32 v220, |v148|, |v149|\n\t" \
  "v_cvt_pknorm_i16_f32 v222, |v150|, |v151|\n\t" \
  "ds_read_b64_tr_b16 v[218:219], v249 offset:32424\n\t" \
  "v_mfma_f32_16x16x32_bf16 v[36:39], v[208:211], v[186:189], v[36:39]\n\t" \
  "v_lshlrev_b32_sdwa v221, 2, v220 dst_sel:DWORD dst_unused:UNUSED_PAD src0_sel:DWORD src1_sel:BYTE_3\n\t" \
  "v_lshlrev_b32_sdwa v220, 2, v220 dst_sel:DWORD dst_unused:UNUSED_PAD src0_sel:DWORD src1_sel:BYTE_1\n\t" \
  "ds_read_u16_d16_hi v235, v235 offset:10528\n\t" \
  "v_mfma_f32_16x16x32_bf16 v[60:63], v[208:211], v[194:197], v[60:63]\n\t" \
  "v_lshlrev_b32_sdwa v223, 2, v222 dst_sel:DWORD dst_unused:UNUSED_PAD src0_sel:DWORD src1_sel:BYTE_3\n\t" \
  "v_lshlrev_b32_sdwa v222, 2, v222 dst_sel:DWORD dst_unused:UNUSED_PAD src0_sel:DWORD src1_sel:BYTE_1\n\t" \
  "ds_read_u16 v234, v234 offset:10528\n\t" \
  "v_mfma_f32_16x16x32_bf16 v[84:87], v[208:211], v[202:205], v[84:87]\n\t" \
  "v_lshlrev_b32_sdwa v225, 1, v157 dst_sel:DWORD dst_unused:UNUSED_PAD src0_sel:DWORD src1_sel:WORD_1\n\t" \
  "v_lshlrev_b32_sdwa v224, 1, v156 dst_sel:DWORD dst_unused:UNUSED_PAD src0_sel:DWORD src1_sel:WORD_1\n\t" \
  "ds_read_b64_tr_b16 v[208:209], v248 offset:0\n\t" \
  "s_waitcnt lgkmcnt(7)\n\t" \
  "v_mfma_f32_16x16x32_bf16 v[16:19], v[212:215], v[178:181], v[16:19]\n\t" \
  "v_lshlrev_b32_sdwa v227, 1, v159 dst_sel:DWORD dst_unused:UNUSED_PAD src0_sel:DWORD src1_sel:WORD_1\n\t" \
  "ds_read_b64_tr_b16 v[210:211], v248 offset:1024\n\t" \
  "ds_read_b32 v221, v221 offset:10016\n\t" \
  "v_mfma_f32_16x16x32_bf16 v[40:43], v[212:215], v[186:189], v[40:43]\n\t" \
  "s_add_u32 s27, s27, s26\n\t" \
  "v_lshlrev_b32_sdwa v226, 1, v158 dst_sel:DWORD dst_unused:UNUSED_PAD src0_sel:DWORD src1_sel:WORD_1\n\t" \
  "v_fma_mix_f32 v229, |v163|, v229, v229 op_sel:[0,1,0] op_sel_hi:[0,1,1] clamp\n\t" \
  "s_mov_b64 exec, s[30:31]\n\t" \
  "buffer_load_dword v237, v254, s[8:11], s27 offen\n\t" \
  "v_mfma_f32_16x16x32_bf16 v[64:67], v[212:215], v[194:197], v[64:67]\n\t" \
  "s_mov_b64 exec, -1\n\t" \
  "v_fma_mix_f32 v228, |v162|, v228, v228 op_sel:[0,1,0] op_sel_hi:[0,1,1] clamp\n\t" \
  "ds_read_b32 v220, v220 offset:10016\n\t" \
  "ds_read_b32 v223, v223 offset:10016\n\t" \
  "v_mfma_f32_16x16x32_bf16 v[88:91], v[212:215], v[202:205], v[88:91]\n\t" \
  "v_fma_mix_f32 v231, |v161|, v231, v231 op_sel:[0,1,0] op_sel_hi:[0,1,1] clamp\n\t" \
  "v_fma_mix_f32 v230, |v160|, v230, v230 op_sel:[0,1,0] op_sel_hi:[0,1,1] clamp\n\t" \
  "s_waitcnt lgkmcnt(5)\n\t" \
  "ds_read_b64_tr_b16 v[212:213], v248 offset:3072\n\t" \
  "ds_read_b32 v222, v222 offset:10016\n\t" \
  "v_mfma_f32_16x16x32_bf16 v[20:23], v[216:219], v[178:181], v[20:23]\n\t" \
  "v_or_b32 v171, v232, v233\n\t" \
  "v_or_b32 v170, v234, v235\n\t" \
  "ds_read_b64_tr_b16 v[214:215], v248 offset:4096\n\t" \
  "v_mfma_f32_16x16x32_bf16 v[44:47], v[216:219], v[186:189], v[44:47]\n\t" \
  "v_fma_f32 v163, |v163|, v229, v163 mul:4\n\t" \
  "v_fma_f32 v162, |v162|, v228, v162 mul:4\n\t" \
  "ds_read_u16_d16_hi v225, v225 offset:10528\n\t" \
  "v_mfma_f32_16x16x32_bf16 v[68:71], v[216:219], v[194:197], v[68:71]\n\t" \
  "v_fma_f32 v161, |v161|, v231, v161 mul:4\n\t" \
  "v_fma_f32 v160, |v160|, v230, v160 mul:4\n\t" \
  "ds_read_u16 v224, v224 offset:10528\n\t" \
  "v_mfma_f32_16x16x32_bf16 v[92:95], v[216:219], v[202:205], v[92:95]\n\t" \
  "v_cvt_pk_bf16_f32 v163, v162, v163\n\t" \
  "v_cvt_pk_bf16_f32 v162, v160, v161\n\t" \
  "ds_read_b64_tr_b16 v[216:217], v248 offset:2048\n\t" \
  "s_waitcnt lgkmcnt(9)\n\t" \
  "v_mfma_f32_16x16x32_bf16 v[176:179], v[208:211], v[96:99], 0\n\t" \
  "v_cvt_pknorm_i16_f32 v228, |v164|, |v165|\n\t" \
  "v_cvt_pknorm_i16_f32 v230, |v166|, |v167|\n\t" \
  "ds_read_b64_tr_b16 v[218:219], v248 offset:5120\n\t" \
  "v_mfma_f32_16x16x32_bf16 v[184:187], v[208:211], v[100:103], 0\n\t" \
  "v_lshlrev_b32_sdwa v229, 2, v228 dst_sel:DWORD dst_unused:UNUSED_PAD src0_sel:DWORD src1_sel:BYTE_3\n\t" \
  "v_lshlrev_b32_sdwa v228, 2, v228 dst_sel:DWORD dst_unused:UNUSED_PAD src0_sel:DWORD src1_sel:BYTE_1\n\t" \
  "ds_read_u16_d16_hi v227, v227 offset:10528\n\t" \
  "v_mfma_f32_16x16x32_bf16 v[192:195], v[208:211], v[104:107], 0\n\t" \
  "v_lshlrev_b32_sdwa v231, 2, v230 dst_sel:DWORD dst_unused:UNUSED_PAD src0_sel:DWORD src1_sel:BYTE_3\n\t" \
  "v_lshlrev_b32_sdwa v230, 2, v230 dst_sel:DWORD dst_unused:UNUSED_PAD src0_sel:DWORD src1_sel:BYTE_1\n\t" \
  "ds_read_u16 v226, v226 offset:10528\n\t" \
  "v_mfma_f32_16x16x32_bf16 v[200:203], v[208:211], v[108:111], 0\n\t" \
  "v_lshlrev_b32_sdwa v233, 1, v173 dst_sel:DWORD dst_unused:UNUSED_PAD src0_sel:DWORD src1_sel:WORD_1\n\t" \
  "v_lshlrev_b32_sdwa v232, 1, v172 dst_sel:DWORD dst_unused:UNUSED_PAD src0_sel:DWORD src1_sel:WORD_1\n\t" \
  "ds_read_b64_tr_b16 v[208:209], v248 offset:512\n\t" \
  "s_waitcnt lgkmcnt(7)\n\t" \
  "v_mfma_f32_16x16x32_bf16 v[176:179], v[212:215], v[112:115], v[176:179]\n\t" \
  "v_lshlrev_b32_sdwa v235, 1, v175 dst_sel:DWORD dst_unused:UNUSED_PAD src0_sel:DWORD src1_sel:WORD_1\n\t" \
  "ds_read_b64_tr_b16 v[210:211], v248 offset:1536\n\t" \
  "ds_read_b32 v229, v229 offset:10016\n\t" \
  "v_mfma_f32_16x16x32_bf16 v[184:187], v[212:215], v[116:119], v[184:187]\n\t" \
  "v_lshlrev_b32_sdwa v234, 1, v174 dst_sel:DWORD dst_unused:UNUSED_PAD src0_sel:DWORD src1_sel:WORD_1\n\t" \
  "v_fma_mix_f32 v221, |v149|, v221, v221 op_sel:[0,1,0] op_sel_hi:[0,1,1] clamp\n\t" \
  "ds_read_b32 v228, v228 offset:10016\n\t" \
  "v_mfma_f32_16x16x32_bf16 v[192:195], v[212:215], v[120:123], v[192:195]\n\t" \
  "v_fma_mix_f32 v220, |v148|, v220, v220 op_sel:[0,1,0] op_sel_hi:[0,1,1] clamp\n\t" \
  "ds_read_b32 v231, v231 offset:10016\n\t" \
  "ds_read_b32 v230, v230 offset:10016\n\t" \
  "v_mfma_f32_16x16x32_bf16 v[200:203], v[212:215], v[124:127], v[200:203]\n\t" \
  "v_fma_mix_f32 v223, |v151|, v223, v223 op_sel:[0,1,0] op_sel_hi:[0,1,1] clamp\n\t" \
  "v_fma_mix_f32 v222, |v150|, v222, v222 op_sel:[0,1,0] op_sel_hi:[0,1,1] clamp\n\t" \
  "s_waitcnt lgkmcnt(6)\n\t" \
  "ds_read_b64_tr_b16 v[212:213], v248 offset:3584\n\t" \
  "ds_read_u16_d16_hi v233, v233 offset:10528\n\t" \
  "v_mfma_f32_16x16x32_bf16 v[176:179], v[216:219], v[128:131], v[176:179]\n\t" \
  "v_or_b32 v156, v224, v225\n\t" \
  "v_or_b32 v157, v226, v227\n\t" \
  "ds_read_b64_tr_b16 v[214:215], v248 offset:4608\n\t" \
  "v_mfma_f32_16x16x32_bf16 v[184:187], v[216:219], v[132:135], v[184:187]\n\t" \
  "v_fma_f32 v149, |v149|, v221, v149 mul:4\n\t" \
  "v_fma_f32 v148, |v148|, v220, v148 mul:4\n\t" \
  "ds_read_u16 v232, v232 offset:10528\n\t" \
  "v_mfma_f32_16x16x32_bf16 v[192:195], v[216:219], v[136:139], v[192:195]\n\t" \
  "v_fma_f32 v151, |v151|, v223, v151 mul:4\n\t" \
  "v_fma_f32 v150, |v150|, v222, v150 mul:4\n\t" \
  "ds_read_u16_d16_hi v235, v235 offset:10528\n\t" \
  "v_mfma_f32_16x16x32_bf16 v[200:203], v[216:219], v[140:143], v[200:203]\n\t" \
  "v_cvt_pk_bf16_f32 v148, v148, v149\n\t" \
  "v_cvt_pk_bf16_f32 v149, v150, v151\n\t" \
  "ds_read_b64_tr_b16 v[216:217], v248 offset:2560\n\t" \
  "s_waitcnt lgkmcnt(10)\n\t" \
  "v_mfma_f32_16x16x32_bf16 v[180:183], v[208:211], v[96:99], 0\n\t" \
  "v_cvt_pknorm_i16_f32 v220, |v178|, |v179|\n\t" \
  "v_cvt_pknorm_i16_f32 v222, |v176|, |v177|\n\t" \
  "s_waitcnt vmcnt(2)\n\t" \
  "ds_read_b64_tr_b16 v[218:219], v248 offset:5632\n\t" \
  "v_mfma_f32_16x16x32_bf16 v[188:191], v[208:211], v[100:103], 0\n\t" \
  "v_lshlrev_b32_sdwa v221, 2, v220 dst_sel:DWORD dst_unused:UNUSED_PAD src0_sel:DWORD src1_sel:BYTE_3\n\t" \
  "v_lshlrev_b32_sdwa v220, 2, v220 dst_sel:DWORD dst_unused:UNUSED_PAD src0_sel:DWORD src1_sel:BYTE_1\n\t" \
  "s_mov_b64 exec, 3\n\t" \
  "ds_add_rtn_u32 v246, v251, v250 offset:47192\n\t" \
  "v_mfma_f32_16x16x32_bf16 v[196:199], v[208:211], v[104:107], 0\n\t" \
  "s_mov_b64 exec, -1\n\t" \
  "v_lshlrev_b32_sdwa v223, 2, v222 dst_sel:DWORD dst_unused:UNUSED_PAD src0_sel:DWORD src1_sel:BYTE_3\n\t" \
  "v_lshlrev_b32_sdwa v222, 2, v222 dst_sel:DWORD dst_unused:UNUSED_PAD src0_sel:DWORD src1_sel:BYTE_1\n\t" \
  "ds_read_u16 v234, v234 offset:10528\n\t" \
  "v_mfma_f32_16x16x32_bf16 v[204:207], v[208:211], v[108:111], 0\n\t" \
  "s_waitcnt lgkmcnt(1)\n\t" \
  "v_lshlrev_b32_sdwa v225, 1, v187 dst_sel:DWORD dst_unused:UNUSED_PAD src0_sel:DWORD src1_sel:WORD_1\n\t" \
  "v_readfirstlane_b32 s4, v246\n\t" \
  "ds_read_b32 v221, v221 offset:10016\n\t" \
  "v_mfma_f32_16x16x32_bf16 v[180:183], v[212:215], v[112:115], v[180:183]\n\t" \
  "s_cmpk_lt_u32 s4, 3072\n\t" \
  "v_lshlrev_b32_sdwa v224, 1, v186 dst_sel:DWORD dst_unused:UNUSED_PAD src0_sel:DWORD src1_sel:WORD_1\n\t" \
  "s_cbranch_scc1 .Lcnx_w0_8\n.Lcnx_g0_8:\n\t" \
  "ds_read_b32 v220, v220 offset:10016\n\t" \
  "v_mfma_f32_16x16x32_bf16 v[188:191], v[212:215], v[116:119], v[188:191]\n\t" \
  "v_lshlrev_b32_sdwa v227, 1, v185 dst_sel:DWORD dst_unused:UNUSED_PAD src0_sel:DWORD src1_sel:WORD_1\n\t" \
  "v_lshlrev_b32_sdwa v226, 1, v184 dst_sel:DWORD dst_unused:UNUSED_PAD src0_sel:DWORD src1_sel:WORD_1\n\t" \
  "ds_read_b64_tr_b16 v[208:209], v249 offset:6152\n\t" \
  "v_mfma_f32_16x16x32_bf16 v[196:199], v[212:215], v[120:123], v[196:199]\n\t" \
  "v_fma_mix_f32 v229, |v165|, v229, v229 op_sel:[0,1,0] op_sel_hi:[0,1,1] clamp\n\t" \
  "v_fma_mix_f32 v228, |v164|, v228, v228 op_sel:[0,1,0] op_sel_hi:[0,1,1] clamp\n\t" \
  "ds_read_b64_tr_b16 v[210:211], v249 offset:7688\n\t" \
  "ds_read_b32 v223, v223 offset:10016\n\t" \
  "v_mfma_f32_16x16x32_bf16 v[204:207], v[212:215], v[124:127], v[204:207]\n\t" \
  "v_fma_mix_f32 v231, |v167|, v231, v231 op_sel:[0,1,0] op_sel_hi:[0,1,1] clamp\n\t" \
  "v_fma_mix_f32 v230, |v166|, v230, v230 op_sel:[0,1,0] op_sel_hi:[0,1,1] clamp\n\t" \
  "s_waitcnt lgkmcnt(5)\n\t" \
  "ds_read_b64_tr_b16 v[212:213], v249 offset:6160\n\t" \
  "ds_read_b32 v222, v222 offset:10016\n\t" \
  "v_mfma_f32_16x16x32_bf16 v[180:183], v[216:219], v[128:131], v[180:183]\n\t" \
  "s_mov_b32 m0, s44\n\t" \
  "v_or_b32 v172, v232, v233\n\t" \
  "v_or_b32 v173, v234, v235\n\t" \
  "s_mov_b32 exec_hi, 0xffff\n\t" \
  "buffer_load_dwordx4 v252, s[16:19], s33 offen lds\n\t" \
  "v_mfma_f32_16x16x32_bf16 v[188:191], v[216:219], v[132:135], v[188:191]\n\t" \
  "s_mov_b32 exec_hi, -1\n\t" \
  "v_fma_f32 v165, |v165|, v229, v165 mul:4\n\t" \
  "v_fma_f32 v164, |v164|, v228, v164 mul:4\n\t" \
  "s_add_u32 s33, s33, s32\n\t" \
  "ds_read_b64_tr_b16 v[214:215], v249 offset:7696\n\t" \
  "v_mfma_f32_16x16x32_bf16 v[196:199], v[216:219], v[136:139], v[196:199]\n\t" \
  "v_fma_f32 v167, |v167|, v231, v167 mul:4\n\t" \
  "v_fma_f32 v166, |v166|, v230, v166 mul:4\n\t" \
  "ds_read_u16_d16_hi v225, v225 offset:10528\n\t" \
  "v_mfma_f32_16x16x32_bf16 v[204:207], v[216:219], v[140:143], v[204:207]\n\t" \
  "v_cvt_pk_bf16_f32 v164, v164, v165\n\t" \
  "v_cvt_pk_bf16_f32 v165, v166, v167\n\t" \
  "ds_read_b64_tr_b16 v[216:217], v249 offset:6216\n\t" \
  "s_waitcnt lgkmcnt(6)\n\t" \
  "v_mfma_f32_16x16x32_bf16 v[0:3], v[208:211], v[146:149], v[0:3]\n\t" \
  "v_cvt_pknorm_i16_f32 v228, |v194|, |v195|\n\t" \
  "v_cvt_pknorm_i16_f32 v230, |v192|, |v193|\n\t" \
  "ds_read_b64_tr_b16 v[218:219], v249 offset:7752\n\t" \
  "v_mfma_f32_16x16x32_bf16 v[24:27], v[208:211], v[154:157], v[24:27]\n\t" \
  "v_lshlrev_b32_sdwa v229, 2, v228 dst_sel:DWORD dst_unused:UNUSED_PAD src0_sel:DWORD src1_sel:BYTE_3\n\t" \
  "v_lshlrev_b32_sdwa v228, 2, v228 dst_sel:DWORD dst_unused:UNUSED_PAD src0_sel:DWORD src1_sel:BYTE_1\n\t" \
  "ds_read_u16 v224, v224 offset:10528\n\t" \
  "v_mfma_f32_16x16x32_bf16 v[48:51], v[208:211], v[162:165], v[48:51]\n\t" \
  "v_lshlrev_b32_sdwa v231, 2, v230 dst_sel:DWORD dst_unused:UNUSED_PAD src0_sel:DWORD src1_sel:BYTE_3\n\t" \
  "v_lshlrev_b32_sdwa v230, 2, v230 dst_sel:DWORD dst_unused:UNUSED_PAD src0_sel:DWORD src1_sel:BYTE_1\n\t" \
  "ds_read_u16_d16_hi v227, v227 offset:10528\n\t" \
  "v_mfma_f32_16x16x32_bf16 v[72:75], v[208:211], v[170:173], v[72:75]\n\t" \
  "s_add_u32 m0, s44, s35\n\t" \
  "v_lshlrev_b32_sdwa v233, 1, v203 dst_sel:DWORD dst_unused:UNUSED_PAD src0_sel:DWORD src1_sel:WORD_1\n\t" \
  "v_lshlrev_b32_sdwa v232, 1, v202 dst_sel:DWORD dst_unused:UNUSED_PAD src0_sel:DWORD src1_sel:WORD_1\n\t" \
  "s_waitcnt lgkmcnt(5)\n\t" \
  "s_mov_b32 exec_hi, 0xffff\n\t" \
  "buffer_load_dwordx4 v252, s[16:19], s34 offen lds\n\t" \
  "v_mfma_f32_16x16x32_bf16 v[4:7], v[212:215], v[146:149], v[4:7]\n\t" \
  "s_mov_b32 exec_hi, -1\n\t" \
  "v_lshlrev_b32_sdwa v235, 1, v201 dst_sel:DWORD dst_unused:UNUSED_PAD src0_sel:DWORD src1_sel:WORD_1\n\t" \
  "s_add_u32 s34, s34, s32\n\t" \
  "ds_read_b64_tr_b16 v[208:209], v249 offset:6224\n\t" \
  "ds_read_u16 v226, v226 offset:10528\n\t" \
  "v_mfma_f32_16x16x32_bf16 v[28:31], v[212:215], v[154:157], v[28:31]\n\t" \
  "v_lshlrev_b32_sdwa v234, 1, v200 dst_sel:DWORD dst_unused:UNUSED_PAD src0_sel:DWORD src1_sel:WORD_1\n\t" \
  "ds_read_b64_tr_b16 v[210:211], v249 offset:7760\n\t" \
  "ds_read_b32 v229, v229 offset:10016\n\t" \
  "v_mfma_f32_16x16x32_bf16 v[52:55], v[212:215], v[162:165], v[52:55]\n\t" \
  "v_fma_mix_f32 v221, |v179|, v221, v221 op_sel:[0,1,0] op_sel_hi:[0,1,1] clamp\n\t" \
  "v_fma_mix_f32 v220, |v178|, v220, v220 op_sel:[0,1,0] op_sel_hi:[0,1,1] clamp\n\t" \
  "ds_read_b32 v228, v228 offset:10016\n\t" \
  "ds_read_b32 v231, v231 offset:10016\n\t" \
  "v_mfma_f32_16x16x32_bf16 v[76:79], v[212:215], v[170:173], v[76:79]\n\t" \
  "v_fma_mix_f32 v223, |v177|, v223, v223 op_sel:[0,1,0] op_sel_hi:[0,1,1] clamp\n\t" \
  "v_fma_mix_f32 v222, |v176|, v222, v222 op_sel:[0,1,0] op_sel_hi:[0,1,1] clamp\n\t" \
  "s_waitcnt lgkmcnt(4)\n\t" \
  "ds_read_b64_tr_b16 v[212:213], v249 offset:6280\n\t" \
  "ds_read_b32 v230, v230 offset:10016\n\t" \
  "v_mfma_f32_16x16x32_bf16 v[8:11], v[216:219], v[146:149], v[8:11]\n\t" \
  "v_or_b32 v187, v224, v225\n\t" \
  "v_or_b32 v186, v226, v227\n\t" \
  "ds_read_b64_tr_b16 v[214:215], v249 offset:7816\n\t" \
  "v_mfma_f32_16x16x32_bf16 v[32:35], v[216:219], v[154:157], v[32:35]\n\t" \
  "v_fma_f32 v179, |v179|, v221, v179 mul:4\n\t" \
  "v_fma_f32 v178, |v178|, v220, v178 mul:4\n\t" \
  "ds_read_u16_d16_hi v233, v233 offset:10528\n\t" \
  "v_mfma_f32_16x16x32_bf16 v[56:59], v[216:219], v[162:165], v[56:59]\n\t" \
  "v_fma_f32 v177, |v177|, v223, v177 mul:4\n\t" \
  "v_fma_f32 v176, |v176|, v222, v176 mul:4\n\t" \
  "s_add_u32 m0, s25, 56880\n\t" \
  "s_add_u32 s24, s24, s26\n\t" \
  "ds_read_u16 v232, v232 offset:10528\n\t" \
  "v_mfma_f32_16x16x32_bf16 v[80:83], v[216:219], v[170:173], v[80:83]\n\t" \
  "v_cvt_pk_bf16_f32 v179, v178, v179\n\t" \
  "v_cvt_pk_bf16_f32 v178, v176, v177\n\t" \
  "s_waitcnt lgkmcnt(8)\n\t" \
  "buffer_load_dwordx4 v254, s[8:11], s24 offen lds\n\t" \
  "ds_read_b64_tr_b16 v[216:217], v249 offset:6288\n\t" \
  "v_mfma_f32_16x16x32_bf16 v[12:15], v[208:211], v[146:149], v[12:15]\n\t" \
  "v_cvt_pknorm_i16_f32 v220, |v180|, |v181|\n\t" \
  "v_cvt_pknorm_i16_f32 v222, |v182|, |v183|\n\t" \
  "ds_read_b64_tr_b16 v[218:219], v249 offset:7824\n\t" \
  "v_mfma_f32_16x16x32_bf16 v[36:39], v[208:211], v[154:157], v[36:39]\n\t" \
  "v_lshlrev_b32_sdwa v221, 2, v220 dst_sel:DWORD dst_unused:UNUSED_PAD src0_sel:DWORD src1_sel:BYTE_3\n\t" \
  "v_lshlrev_b32_sdwa v220, 2, v220 dst_sel:DWORD dst_unused:UNUSED_PAD src0_sel:DWORD src1_sel:BYTE_1\n\t" \
  "ds_read_u16_d16_hi v235, v235 offset:10528\n\t" \
  "v_mfma_f32_16x16x32_bf16 v[60:63], v[208:211], v[162:165], v[60:63]\n\t" \
  "v_lshlrev_b32_sdwa v223, 2, v222 dst_sel:DWORD dst_unused:UNUSED_PAD src0_sel:DWORD src1_sel:BYTE_3\n\t" \
  "v_lshlrev_b32_sdwa v222, 2, v222 dst_sel:DWORD dst_unused:UNUSED_PAD src0_sel:DWORD src1_sel:BYTE_1\n\t" \
  "ds_read_u16 v234, v234 offset:10528\n\t" \
  "v_mfma_f32_16x16x32_bf16 v[84:87], v[208:211], v[170:173], v[84:87]\n\t" \
  "v_lshlrev_b32_sdwa v225, 1, v189 dst_sel:DWORD dst_unused:UNUSED_PAD src0_sel:DWORD src1_sel:WORD_1\n\t" \
  "v_lshlrev_b32_sdwa v224, 1, v188 dst_sel:DWORD dst_unused:UNUSED_PAD src0_sel:DWORD src1_sel:WORD_1\n\t" \
  "ds_read_b64_tr_b16 v[208:209], v248 offset:43048\n\t" \
  "s_waitcnt lgkmcnt(7)\n\t" \
  "v_mfma_f32_16x16x32_bf16 v[16:19], v[212:215], v[146:149], v[16:19]\n\t" \
  "v_lshlrev_b32_sdwa v227, 1, v191 dst_sel:DWORD dst_unused:UNUSED_PAD src0_sel:DWORD src1_sel:WORD_1\n\t" \
  "ds_read_b64_tr_b16 v[210:211], v248 offset:44072\n\t" \
  "ds_read_b32 v221, v221 offset:10016\n\t" \
  "v_mfma_f32_16x16x32_bf16 v[40:43], v[212:215], v[154:157], v[40:43]\n\t" \
  "s_add_u32 s27, s27, s26\n\t" \
  "v_lshlrev_b32_sdwa v226, 1, v190 dst_sel:DWORD dst_unused:UNUSED_PAD src0_sel:DWORD src1_sel:WORD_1\n\t" \
  "v_fma_mix_f32 v229, |v195|, v229, v229 op_sel:[0,1,0] op_sel_hi:[0,1,1] clamp\n\t" \
  "s_mov_b64 exec, s[30:31]\n\t" \
  "buffer_load_dword v237, v254, s[8:11], s27 offen\n\t" \
  "v_mfma_f32_16x16x32_bf16 v[64:67], v[212:215], v[162:165], v[64:67]\n\t" \
  "s_mov_b64 exec, -1\n\t" \
  "v_fma_mix_f32 v228, |v194|, v228, v228 op_sel:[0,1,0] op_sel_hi:[0,1,1] clamp\n\t" \
  "ds_read_b32 v220, v220 offset:10016\n\t" \
  "ds_read_b32 v223, v223 offset:10016\n\t" \
  "v_mfma_f32_16x16x32_bf16 v[88:91], v[212:215], v[170:173], v[88:91]\n\t" \
  "v_fma_mix_f32 v231, |v193|, v231, v231 op_sel:[0,1,0] op_sel_hi:[0,1,1] clamp\n\t" \
  "v_fma_mix_f32 v230, |v192|, v230, v230 op_sel:[0,1,0] op_sel_hi:[0,1,1] clamp\n\t" \
  "s_waitcnt lgkmcnt(5)\n\t" \
  "ds_read_b64_tr_b16 v[212:213], v248 offset:46120\n\t" \
  "ds_read_b32 v222, v222 offset:10016\n\t" \
  "v_mfma_f32_16x16x32_bf16 v[20:23], v[216:219], v[146:149], v[20:23]\n\t" \
  "v_or_b32 v203, v232, v233\n\t" \
  "v_or_b32 v202, v234, v235\n\t" \
  "ds_read_b64_tr_b16 v[214:215], v248 offset:47144\n\t" \
  "v_mfma_f32_16x16x32_bf16 v[44:47], v[216:219], v[154:157], v[44:47]\n\t" \
  "v_fma_f32 v195, |v195|, v229, v195 mul:4\n\t" \
  "v_fma_f32 v194, |v194|, v228, v194 mul:4\n\t" \
  "ds_read_u16_d16_hi v225, v225 offset:10528\n\t" \
  "v_mfma_f32_16x16x32_bf16 v[68:71], v[216:219], v[162:165], v[68:71]\n\t" \
  "v_fma_f32 v193, |v193|, v231, v193 mul:4\n\t" \
  "v_fma_f32 v192, |v192|, v230, v192 mul:4\n\t" \
  "ds_read_u16 v224, v224 offset:10528\n\t" \
  "v_mfma_f32_16x16x32_bf16 v[92:95], v[216:219], v[170:173], v[92:95]\n\t" \
  "v_cvt_pk_bf16_f32 v195, v194, v195\n\t" \
  "v_cvt_pk_bf16_f32 v194, v192, v193\n\t" \
  "ds_read_b64_tr_b16 v[216:217], v248 offset:45096\n\t" \
  "s_waitcnt lgkmcnt(9)\n\t" \
  "v_mfma_f32_16x16x32_bf16 v[144:147], v[208:211], v[96:99], 0\n\t" \
  "v_cvt_pknorm_i16_f32 v228, |v196|, |v197|\n\t" \
  "v_cvt_pknorm_i16_f32 v230, |v198|, |v199|\n\t" \
  "ds_read_b64_tr_b16 v[218:219], v248 offset:48168\n\t" \
  "v_mfma_f32_16x16x32_bf16 v[152:155], v[208:211], v[100:103], 0\n\t" \
  "v_lshlrev_b32_sdwa v229, 2, v228 dst_sel:DWORD dst_unused:UNUSED_PAD src0_sel:DWORD src1_sel:BYTE_3\n\t" \
  "v_lshlrev_b32_sdwa v228, 2, v228 dst_sel:DWORD dst_unused:UNUSED_PAD src0_sel:DWORD src1_sel:BYTE_1\n\t" \
  "ds_read_u16_d16_hi v227, v227 offset:10528\n\t" \
  "v_mfma_f32_16x16x32_bf16 v[160:163], v[208:211], v[104:107], 0\n\t" \
  "v_lshlrev_b32_sdwa v231, 2, v230 dst_sel:DWORD dst_unused:UNUSED_PAD src0_sel:DWORD src1_sel:BYTE_3\n\t" \
  "v_lshlrev_b32_sdwa v230, 2, v230 dst_sel:DWORD dst_unused:UNUSED_PAD src0_sel:DWORD src1_sel:BYTE_1\n\t" \
  "ds_read_u16 v226, v226 offset:10528\n\t" \
  "v_mfma_f32_16x16x32_bf16 v[168:171], v[208:211], v[108:111], 0\n\t" \
  "v_lshlrev_b32_sdwa v233, 1, v205 dst_sel:DWORD dst_unused:UNUSED_PAD src0_sel:DWORD src1_sel:WORD_1\n\t" \
  "v_lshlrev_b32_sdwa v232, 1, v204 dst_sel:DWORD dst_unused:UNUSED_PAD src0_sel:DWORD src1_sel:WORD_1\n\t" \
  "ds_read_b64_tr_b16 v[208:209], v248 offset:43560\n\t" \
  "s_waitcnt lgkmcnt(7)\n\t" \
  "v_mfma_f32_16x16x32_bf16 v[144:147], v[212:215], v[112:115], v[144:147]\n\t" \
  "v_lshlrev_b32_sdwa v235, 1, v207 dst_sel:DWORD dst_unused:UNUSED_PAD src0_sel:DWORD src1_sel:WORD_1\n\t" \
  "ds_read_b64_tr_b16 v[210:211], v248 offset:44584\n\t" \
  "ds_read_b32 v229, v229 offset:10016\n\t" \
  "v_mfma_f32_16x16x32_bf16 v[152:155], v[212:215], v[116:119], v[152:155]\n\t" \
  "v_lshlrev_b32_sdwa v234, 1, v206 dst_sel:DWORD dst_unused:UNUSED_PAD src0_sel:DWORD src1_sel:WORD_1\n\t" \
  "v_fma_mix_f32 v221, |v181|, v221, v221 op_sel:[0,1,0] op_sel_hi:[0,1,1] clamp\n\t" \
  "ds_read_b32 v228, v228 offset:10016\n\t" \
  "v_mfma_f32_16x16x32_bf16 v[160:163], v[212:215], v[120:123], v[160:163]\n\t" \
  "v_fma_mix_f32 v220, |v180|, v220, v220 op_sel:[0,1,0] op_sel_hi:[0,1,1] clamp\n\t" \
  "ds_read_b32 v231, v231 offset:10016\n\t" \
  "ds_read_b32 v230, v230 offset:10016\n\t" \
  "v_mfma_f32_16x16x32_bf16 v[168:171], v[212:215], v[124:127], v[168:171]\n\t" \
  "v_fma_mix_f32 v223, |v183|, v223, v223 op_sel:[0,1,0] op_sel_hi:[0,1,1] clamp\n\t" \
  "v_fma_mix_f32 v222, |v182|, v222, v222 op_sel:[0,1,0] op_sel_hi:[0,1,1] clamp\n\t" \
  "s_waitcnt lgkmcnt(6)\n\t" \
  "ds_read_b64_tr_b16 v[212:213], v248 offset:46632\n\t" \
  "ds_read_u16_d16_hi v233, v233 offset:10528\n\t" \
  "v_mfma_f32_16x16x32_bf16 v[144:147], v[216:219], v[128:131], v[144:147]\n\t" \
  "v_or_b32 v188, v224, v225\n\t" \
  "v_or_b32 v189, v226, v227\n\t" \
  "ds_read_b64_tr_b16 v[214:215], v248 offset:47656\n\t" \
  "v_mfma_f32_16x16x32_bf16 v[152:155], v[216:219], v[132:135], v[152:155]\n\t" \
  "v_fma_f32 v181, |v181|, v221, v181 mul:4\n\t" \
  "v_fma_f32 v180, |v180|, v220, v180 mul:4\n\t" \
  "ds_read_u16 v232, v232 offset:10528\n\t" \
  "v_mfma_f32_16x16x32_bf16 v[160:163], v[216:219], v[136:139], v[160:163]\n\t" \
  "v_fma_f32 v183, |v183|, v223, v183 mul:4\n\t" \
  "v_fma_f32 v182, |v182|, v222, v182 mul:4\n\t" \
  "ds_read_u16_d16_hi v235, v235 offset:10528\n\t" \
  "v_mfma_f32_16x16x32_bf16 v[168:171], v[216:219], v[140:143], v[168:171]\n\t" \
  "v_cvt_pk_bf16_f32 v180, v180, v181\n\t" \
  "v_cvt_pk_bf16_f32 v181, v182, v183\n\t" \
  "ds_read_b64_tr_b16 v[216:217], v248 offset:45608\n\t" \
  "s_waitcnt lgkmcnt(10)\n\t" \
  "v_mfma_f32_16x16x32_bf16 v[148:151], v[208:211], v[96:99], 0\n\t" \
  "v_cvt_pknorm_i16_f32 v220, |v146|, |v147|\n\t" \
  "v_cvt_pknorm_i16_f32 v222, |v144|, |v145|\n\t" \
  "s_waitcnt vmcnt(2)\n\t" \
  "ds_read_b64_tr_b16 v[218:219], v248 offset:48680\n\t" \
  "v_mfma_f32_16x16x32_bf16 v[156:159], v[208:211], v[100:103], 0\n\t" \
  "v_lshlrev_b32_sdwa v221, 2, v220 dst_sel:DWORD dst_unused:UNUSED_PAD src0_sel:DWORD src1_sel:BYTE_3\n\t" \
  "v_lshlrev_b32_sdwa v220, 2, v220 dst_sel:DWORD dst_unused:UNUSED_PAD src0_sel:DWORD src1_sel:BYTE_1\n\t" \
  "s_mov_b64 exec, 3\n\t" \
  "ds_add_rtn_u32 v246, v251, v250 offset:47200\n\t" \
  "v_mfma_f32_16x16x32_bf16 v[164:167], v[208:211], v[104:107], 0\n\t" \
  "s_mov_b64 exec, -1\n\t" \
  "v_lshlrev_b32_sdwa v223, 2, v222 dst_sel:DWORD dst_unused:UNUSED_PAD src0_sel:DWORD src1_sel:BYTE_3\n\t" \
  "v_lshlrev_b32_sdwa v222, 2, v222 dst_sel:DWORD dst_unused:UNUSED_PAD src0_sel:DWORD src1_sel:BYTE_1\n\t" \
  "ds_read_u16 v234, v234 offset:10528\n\t" \
  "v_mfma_f32_16x16x32_bf16 v[172:175], v[208:211], v[108:111], 0\n\t" \
  "s_waitcnt lgkmcnt(1)\n\t" \
  "v_lshlrev_b32_sdwa v225, 1, v155 dst_sel:DWORD dst_unused:UNUSED_PAD src0_sel:DWORD src1_sel:WORD_1\n\t" \
  "v_readfirstlane_b32 s4, v246\n\t" \
  "ds_read_b32 v221, v221 offset:10016\n\t" \
  "v_mfma_f32_16x16x32_bf16 v[148:151], v[212:215], v[112:115], v[148:151]\n\t" \
  "s_cmpk_lt_u32 s4, 3072\n\t" \
  "v_lshlrev_b32_sdwa v224, 1, v154 dst_sel:DWORD dst_unused:UNUSED_PAD src0_sel:DWORD src1_sel:WORD_1\n\t" \
  "s_cbranch_scc1 .Lcnx_w0_9\n.Lcnx_g0_9:\n\t" \
  "ds_read_b32 v220, v220 offset:10016\n\t" \
  "v_mfma_f32_16x16x32_bf16 v[156:159], v[212:215], v[116:119], v[156:159]\n\t" \
  "v_lshlrev_b32_sdwa v227, 1, v153 dst_sel:DWORD dst_unused:UNUSED_PAD src0_sel:DWORD src1_sel:WORD_1\n\t" \
  "v_lshlrev_b32_sdwa v226, 1, v152 dst_sel:DWORD dst_unused:UNUSED_PAD src0_sel:DWORD src1_sel:WORD_1\n\t" \
  "ds_read_b64_tr_b16 v[208:209], v249 offset:12304\n\t" \
  "v_mfma_f32_16x16x32_bf16 v[164:167], v[212:215], v[120:123], v[164:167]\n\t" \
  "v_fma_mix_f32 v229, |v197|, v229, v229 op_sel:[0,1,0] op_sel_hi:[0,1,1] clamp\n\t" \
  "v_fma_mix_f32 v228, |v196|, v228, v228 op_sel:[0,1,0] op_sel_hi:[0,1,1] clamp\n\t" \
  "ds_read_b64_tr_b16 v[210:211], v249 offset:13840\n\t" \
  "ds_read_b32 v223, v223 offset:10016\n\t" \
  "v_mfma_f32_16x16x32_bf16 v[172:175], v[212:215], v[124:127], v[172:175]\n\t" \
  "v_fma_mix_f32 v231, |v199|, v231, v231 op_sel:[0,1,0] op_sel_hi:[0,1,1] clamp\n\t" \
  "v_fma_mix_f32 v230, |v198|, v230, v230 op_sel:[0,1,0] op_sel_hi:[0,1,1] clamp\n\t" \
  "s_waitcnt lgkmcnt(5)\n\t" \
  "ds_read_b64_tr_b16 v[212:213], v249 offset:12312\n\t" \
  "ds_read_b32 v222, v222 offset:10016\n\t" \
  "v_mfma_f32_16x16x32_bf16 v[148:151], v[216:219], v[128:131], v[148:151]\n\t" \
  "s_mov_b32 m0, s45\n\t" \
  "v_or_b32 v204, v232, v233\n\t" \
  "v_or_b32 v205, v234, v235\n\t" \
  "s_mov_b32 exec_hi, 0xffff\n\t" \
  "buffer_load_dwordx4 v252, s[16:19], s33 offen lds\n\t" \
  "v_mfma_f32_16x16x32_bf16 v[156:159], v[216:219], v[132:135], v[156:159]\n\t" \
  "s_mov_b32 exec_hi, -1\n\t" \
  "v_fma_f32 v197, |v197|, v229, v197 mul:4\n\t" \
  "v_fma_f32 v196, |v196|, v228, v196 mul:4\n\t" \
  "s_add_u32 s33, s33, s48\n\t" \
  "ds_read_b64_tr_b16 v[214:215], v249 offset:13848\n\t" \
  "v_mfma_f32_16x16x32_bf16 v[164:167], v[216:219], v[136:139], v[164:167]\n\t" \
  "v_fma_f32 v199, |v199|, v231, v199 mul:4\n\t" \
  "v_fma_f32 v198, |v198|, v230, v198 mul:4\n\t" \
  "ds_read_u16_d16_hi v225, v225 offset:10528\n\t" \
  "v_mfma_f32_16x16x32_bf16 v[172:175], v[216:219], v[140:143], v[172:175]\n\t" \
  "v_cvt_pk_bf16_f32 v196, v196, v197\n\t" \
  "v_cvt_pk_bf16_f32 v197, v198, v199\n\t" \
  "ds_read_b64_tr_b16 v[216:217], v249 offset:12368\n\t" \
  "s_waitcnt lgkmcnt(6)\n\t" \
  "v_mfma_f32_16x16x32_bf16 v[0:3], v[208:211], v[178:181], v[0:3]\n\t" \
  "v_cvt_pknorm_i16_f32 v228, |v162|, |v163|\n\t" \
  "v_cvt_pknorm_i16_f32 v230, |v160|, |v161|\n\t" \
  "ds_read_b64_tr_b16 v[218:219], v249 offset:13904\n\t" \
  "v_mfma_f32_16x16x32_bf16 v[24:27], v[208:211], v[186:189], v[24:27]\n\t" \
  "v_lshlrev_b32_sdwa v229, 2, v228 dst_sel:DWORD dst_unused:UNUSED_PAD src0_sel:DWORD src1_sel:BYTE_3\n\t" \
  "v_lshlrev_b32_sdwa v228, 2, v228 dst_sel:DWORD dst_unused:UNUSED_PAD src0_sel:DWORD src1_sel:BYTE_1\n\t" \
  "ds_read_u16 v224, v224 offset:10528\n\t" \
  "v_mfma_f32_16x16x32_bf16 v[48:51], v[208:211], v[194:197], v[48:51]\n\t" \
  "v_lshlrev_b32_sdwa v231, 2, v230 dst_sel:DWORD dst_unused:UNUSED_PAD src0_sel:DWORD src1_sel:BYTE_3\n\t" \
  "v_lshlrev_b32_sdwa v230, 2, v230 dst_sel:DWORD dst_unused:UNUSED_PAD src0_sel:DWORD src1_sel:BYTE_1\n\t" \
  "ds_read_u16_d16_hi v227, v227 offset:10528\n\t" \
  "v_mfma_f32_16x16x32_bf16 v[72:75], v[208:211], v[202:205], v[72:75]\n\t" \
  "s_add_u32 m0, s45, s35\n\t" \
  "v_lshlrev_b32_sdwa v233, 1, v171 dst_sel:DWORD dst_unused:UNUSED_PAD src0_sel:DWORD src1_sel:WORD_1\n\t" \
  "v_lshlrev_b32_sdwa v232, 1, v170 dst_sel:DWORD dst_unused:UNUSED_PAD src0_sel:DWORD src1_sel:WORD_1\n\t" \
  "s_waitcnt lgkmcnt(5)\n\t" \
  "s_mov_b32 exec_hi, 0xffff\n\t" \
  "buffer_load_dwordx4 v252, s[16:19], s34 offen lds\n\t" \
  "v_mfma_f32_16x16x32_bf16 v[4:7], v[212:215], v[178:181], v[4:7]\n\t" \
  "s_mov_b32 exec_hi, -1\n\t" \
  "v_lshlrev_b32_sdwa v235, 1, v169 dst_sel:DWORD dst_unused:UNUSED_PAD src0_sel:DWORD src1_sel:WORD_1\n\t" \
  "s_add_u32 s34, s34, s48\n\t" \
  "ds_read_b64_tr_b16 v[208:209], v249 offset:12376\n\t" \
  "ds_read_u16 v226, v226 offset:10528\n\t" \
  "v_mfma_f32_16x16x32_bf16 v[28:31], v[212:215], v[186:189], v[28:31]\n\t" \
  "v_lshlrev_b32_sdwa v234, 1, v168 dst_sel:DWORD dst_unused:UNUSED_PAD src0_sel:DWORD src1_sel:WORD_1\n\t" \
  "ds_read_b64_tr_b16 v[210:211], v249 offset:13912\n\t" \
  "ds_read_b32 v229, v229 offset:10016\n\t" \
  "v_mfma_f32_16x16x32_bf16 v[52:55], v[212:215], v[194:197], v[52:55]\n\t" \
  "v_fma_mix_f32 v221, |v147|, v221, v221 op_sel:[0,1,0] op_sel_hi:[0,1,1] clamp\n\t" \
  "v_fma_mix_f32 v220, |v146|, v220, v220 op_sel:[0,1,0] op_sel_hi:[0,1,1] clamp\n\t" \
  "ds_read_b32 v228, v228 offset:10016\n\t" \
  "ds_read_b32 v231, v231 offset:10016\n\t" \
  "v_mfma_f32_16x16x32_bf16 v[76:79], v[212:215], v[202:205], v[76:79]\n\t" \
  "v_fma_mix_f32 v223, |v145|, v223, v223 op_sel:[0,1,0] op_sel_hi:[0,1,1] clamp\n\t" \
  "v_fma_mix_f32 v222, |v144|, v222, v222 op_sel:[0,1,0] op_sel_hi:[0,1,1] clamp\n\t" \
  "s_waitcnt lgkmcnt(4)\n\t" \
  "ds_read_b64_tr_b16 v[212:213], v249 offset:12432\n\t" \
  "ds_read_b32 v230, v230 offset:10016\n\t" \
  "v_mfma_f32_16x16x32_bf16 v[8:11], v[216:219], v[178:181], v[8:11]\n\t" \
  "v_or_b32 v155, v224, v225\n\t" \
  "v_or_b32 v154, v226, v227\n\t" \
  "ds_read_b64_tr_b16 v[214:215], v249 offset:13968\n\t" \
  "v_mfma_f32_16x16x32_bf16 v[32:35], v[216:219], v[186:189], v[32:35]\n\t" \
  "v_fma_f32 v147, |v147|, v221, v147 mul:4\n\t" \
  "v_fma_f32 v146, |v146|, v220, v146 mul:4\n\t" \
  "ds_read_u16_d16_hi v233, v233 offset:10528\n\t" \
  "v_mfma_f32_16x16x32_bf16 v[56:59], v[216:219], v[194:197], v[56:59]\n\t" \
  "v_fma_f32 v145, |v145|, v223, v145 mul:4\n\t" \
  "v_fma_f32 v144, |v144|, v222, v144 mul:4\n\t" \
  "s_add_u32 m0, s25, 65080\n\t" \
  "s_add_u32 s24, s24, s26\n\t" \
  "ds_read_u16 v232, v232 offset:10528\n\t" \
  "v_mfma_f32_16x16x32_bf16 v[80:83], v[216:219], v[202:205], v[80:83]\n\t" \
  "v_cvt_pk_bf16_f32 v147, v146, v147\n\t" \
  "v_cvt_pk_bf16_f32 v146, v144, v145\n\t" \
  "s_waitcnt lgkmcnt(8)\n\t" \
  "buffer_load_dwordx4 v254, s[8:11], s24 offen lds\n\t" \
  "ds_read_b64_tr_b16 v[216:217], v249 offset:12440\n\t" \
  "v_mfma_f32_16x16x32_bf16 v[12:15], v[208:211], v[178:181], v[12:15]\n\t" \
  "v_cvt_pknorm_i16_f32 v220, |v148|, |v149|\n\t" \
  "v_cvt_pknorm_i16_f32 v222, |v150|, |v151|\n\t" \
  "ds_read_b64_tr_b16 v[218:219], v249 offset:13976\n\t" \
  "v_mfma_f32_16x16x32_bf16 v[36:39], v[208:211], v[186:189], v[36:39]\n\t" \
  "v_lshlrev_b32_sdwa v221, 2, v220 dst_sel:DWORD dst_unused:UNUSED_PAD src0_sel:DWORD src1_sel:BYTE_3\n\t" \
  "v_lshlrev_b32_sdwa v220, 2, v220 dst_sel:DWORD dst_unused:UNUSED_PAD src0_sel:DWORD src1_sel:BYTE_1\n\t" \
  "ds_read_u16_d16_hi v235, v235 offset:10528\n\t" \
  "v_mfma_f32_16x16x32_bf16 v[60:63], v[208:211], v[194:197], v[60:63]\n\t" \
  "v_lshlrev_b32_sdwa v223, 2, v222 dst_sel:DWORD dst_unused:UNUSED_PAD src0_sel:DWORD src1_sel:BYTE_3\n\t" \
  "v_lshlrev_b32_sdwa v222, 2, v222 dst_sel:DWORD dst_unused:UNUSED_PAD src0_sel:DWORD src1_sel:BYTE_1\n\t" \
  "ds_read_u16 v234, v234 offset:10528\n\t" \
  "v_mfma_f32_16x16x32_bf16 v[84:87], v[208:211], v[202:205], v[84:87]\n\t" \
  "v_lshlrev_b32_sdwa v225, 1, v157 dst_sel:DWORD dst_unused:UNUSED_PAD src0_sel:DWORD src1_sel:WORD_1\n\t" \
  "v_lshlrev_b32_sdwa v224, 1, v156 dst_sel:DWORD dst_unused:UNUSED_PAD src0_sel:DWORD src1_sel:WORD_1\n\t" \
  "ds_read_b64_tr_b16 v[208:209], v248 offset:49192\n\t" \
  "s_waitcnt lgkmcnt(7)\n\t" \
  "v_mfma_f32_16x16x32_bf16 v[16:19], v[212:215], v[178:181], v[16:19]\n\t" \
  "v_lshlrev_b32_sdwa v227, 1, v159 dst_sel:DWORD dst_unused:UNUSED_PAD src0_sel:DWORD src1_sel:WORD_1\n\t" \
  "ds_read_b64_tr_b16 v[210:211], v248 offset:50216\n\t" \
  "ds_read_b32 v221, v221 offset:10016\n\t" \
  "v_mfma_f32_16x16x32_bf16 v[40:43], v[212:215], v[186:189], v[40:43]\n\t" \
  "s_add_u32 s27, s27, s26\n\t" \
  "v_lshlrev_b32_sdwa v226, 1, v158 dst_sel:DWORD dst_unused:UNUSED_PAD src0_sel:DWORD src1_sel:WORD_1\n\t" \
  "v_fma_mix_f32 v229, |v163|, v229, v229 op_sel:[0,1,0] op_sel_hi:[0,1,1] clamp\n\t" \
  "s_mov_b64 exec, s[30:31]\n\t" \
  "buffer_load_dword v237, v254, s[8:11], s27 offen\n\t" \
  "v_mfma_f32_16x16x32_bf16 v[64:67], v[212:215], v[194:197], v[64:67]\n\t" \
  "s_mov_b64 exec, -1\n\t" \
  "v_fma_mix_f32 v228, |v162|, v228, v228 op_sel:[0,1,0] op_sel_hi:[0,1,1] clamp\n\t" \
  "ds_read_b32 v220, v220 offset:10016\n\t" \
  "ds_read_b32 v223, v223 offset:10016\n\t" \
  "v_mfma_f32_16x16x32_bf16 v[88:91], v[212:215], v[202:205], v[88:91]\n\t" \
  "v_fma_mix_f32 v231, |v161|, v231, v231 op_sel:[0,1,0] op_sel_hi:[0,1,1] clamp\n\t" \
  "v_fma_mix_f32 v230, |v160|, v230, v230 op_sel:[0,1,0] op_sel_hi:[0,1,1] clamp\n\t" \
  "s_waitcnt lgkmcnt(5)\n\t" \
  "ds_read_b64_tr_b16 v[212:213], v248 offset:52264\n\t" \
  "ds_read_b32 v222, v222 offset:10016\n\t" \
  "v_mfma_f32_16x16x32_bf16 v[20:23], v[216:219], v[178:181], v[20:23]\n\t" \
  "v_or_b32 v171, v232, v233\n\t" \
  "v_or_b32 v170, v234, v235\n\t" \
  "ds_read_b64_tr_b16 v[214:215], v248 offset:53288\n\t" \
  "v_mfma_f32_16x16x32_bf16 v[44:47], v[216:219], v[186:189], v[44:47]\n\t" \
  "v_fma_f32 v163, |v163|, v229, v163 mul:4\n\t" \
  "v_fma_f32 v162, |v162|, v228, v162 mul:4\n\t" \
  "ds_read_u16_d16_hi v225, v225 offset:10528\n\t" \
  "v_mfma_f32_16x16x32_bf16 v[68:71], v[216:219], v[194:197], v[68:71]\n\t" \
  "v_fma_f32 v161, |v161|, v231, v161 mul:4\n\t" \
  "v_fma_f32 v160, |v160|, v230, v160 mul:4\n\t" \
  "ds_read_u16 v224, v224 offset:10528\n\t" \
  "v_mfma_f32_16x16x32_bf16 v[92:95], v[216:219], v[202:205], v[92:95]\n\t" \
  "v_cvt_pk_bf16_f32 v163, v162, v163\n\t" \
  "v_cvt_pk_bf16_f32 v162, v160, v161\n\t" \
  "ds_read_b64_tr_b16 v[216:217], v248 offset:51240\n\t" \
  "s_waitcnt lgkmcnt(9)\n\t" \
  "v_mfma_f32_16x16x32_bf16 v[176:179], v[208:211], v[96:99], 0\n\t" \
  "v_cvt_pknorm_i16_f32 v228, |v164|, |v165|\n\t" \
  "v_cvt_pknorm_i16_f32 v230, |v166|, |v167|\n\t" \
  "ds_read_b64_tr_b16 v[218:219], v248 offset:54312\n\t" \
  "v_mfma_f32_16x16x32_bf16 v[184:187], v[208:211], v[100:103], 0\n\t" \
  "v_lshlrev_b32_sdwa v229, 2, v228 dst_sel:DWORD dst_unused:UNUSED_PAD src0_sel:DWORD src1_sel:BYTE_3\n\t" \
  "v_lshlrev_b32_sdwa v228, 2, v228 dst_sel:DWORD dst_unused:UNUSED_PAD src0_sel:DWORD src1_sel:BYTE_1\n\t" \
  "ds_read_u16_d16_hi v227, v227 offset:10528\n\t" \
  "v_mfma_f32_16x16x32_bf16 v[192:195], v[208:211], v[104:107], 0\n\t" \
  "v_lshlrev_b32_sdwa v231, 2, v230 dst_sel:DWORD dst_unused:UNUSED_PAD src0_sel:DWORD src1_sel:BYTE_3\n\t" \
  "v_lshlrev_b32_sdwa v230, 2, v230 dst_sel:DWORD dst_unused:UNUSED_PAD src0_sel:DWORD src1_sel:BYTE_1\n\t" \
  "ds_read_u16 v226, v226 offset:10528\n\t" \
  "v_mfma_f32_16x16x32_bf16 v[200:203], v[208:211], v[108:111], 0\n\t" \
  "v_lshlrev_b32_sdwa v233, 1, v173 dst_sel:DWORD dst_unused:UNUSED_PAD src0_sel:DWORD src1_sel:WORD_1\n\t" \
  "v_lshlrev_b32_sdwa v232, 1, v172 dst_sel:DWORD dst_unused:UNUSED_PAD src0_sel:DWORD src1_sel:WORD_1\n\t" \
  "ds_read_b64_tr_b16 v[208:209], v248 offset:49704\n\t" \
  "s_waitcnt lgkmcnt(7)\n\t" \
  "v_mfma_f32_16x16x32_bf16 v[176:179], v[212:215], v[112:115], v[176:179]\n\t" \
  "v_lshlrev_b32_sdwa v235, 1, v175 dst_sel:DWORD dst_unused:UNUSED_PAD src0_sel:DWORD src1_sel:WORD_1\n\t" \
  "ds_read_b64_tr_b16 v[210:211], v248 offset:50728\n\t" \
  "ds_read_b32 v229, v229 offset:10016\n\t" \
  "v_mfma_f32_16x16x32_bf16 v[184:187], v[212:215], v[116:119], v[184:187]\n\t" \
  "v_lshlrev_b32_sdwa v234, 1, v174 dst_sel:DWORD dst_unused:UNUSED_PAD src0_sel:DWORD src1_sel:WORD_1\n\t" \
  "v_fma_mix_f32 v221, |v149|, v221, v221 op_sel:[0,1,0] op_sel_hi:[0,1,1] clamp\n\t" \
  "ds_read_b32 v228, v228 offset:10016\n\t" \
  "v_mfma_f32_16x16x32_bf16 v[192:195], v[212:215], v[120:123], v[192:195]\n\t" \
  "v_fma_mix_f32 v220, |v148|, v220, v220 op_sel:[0,1,0] op_sel_hi:[0,1,1] clamp\n\t" \
  "ds_read_b32 v231, v231 offset:10016\n\t" \
  "ds_read_b32 v230, v230 offset:10016\n\t" \
  "v_mfma_f32_16x16x32_bf16 v[200:203], v[212:215], v[124:127], v[200:203]\n\t" \
  "v_fma_mix_f32 v223, |v151|, v223, v223 op_sel:[0,1,0] op_sel_hi:[0,1,1] clamp\n\t" \
  "v_fma_mix_f32 v222, |v150|, v222, v222 op_sel:[0,1,0] op_sel_hi:[0,1,1] clamp\n\t" \
  "s_waitcnt lgkmcnt(6)\n\t" \
  "ds_read_b64_tr_b16 v[212:213], v248 offset:52776\n\t" \
  "ds_read_u16_d16_hi v233, v233 offset:10528\n\t" \
  "v_mfma_f32_16x16x32_bf16 v[176:179], v[216:219], v[128:131], v[176:179]\n\t" \
  "v_or_b32 v156, v224, v225\n\t" \
  "v_or_b32 v157, v226, v227\n\t" \
  "ds_read_b64_tr_b16 v[214:215], v248 offset:53800\n\t" \
  "v_mfma_f32_16x16x32_bf16 v[184:187], v[216:219], v[132:135], v[184:187]\n\t" \
  "v_fma_f32 v149, |v149|, v221, v149 mul:4\n\t" \
  "v_fma_f32 v148, |v148|, v220, v148 mul:4\n\t" \
  "ds_read_u16 v232, v232 offset:10528\n\t" \
  "v_mfma_f32_16x16x32_bf16 v[192:195], v[216:219], v[136:139], v[192:195]\n\t" \
  "v_fma_f32 v151, |v151|, v223, v151 mul:4\n\t" \
  "v_fma_f32 v150, |v150|, v222, v150 mul:4\n\t" \
  "ds_read_u16_d16_hi v235, v235 offset:10528\n\t" \
  "v_mfma_f32_16x16x32_bf16 v[200:203], v[216:219], v[140:143], v[200:203]\n\t" \
  "v_cvt_pk_bf16_f32 v148, v148, v149\n\t" \
  "v_cvt_pk_bf16_f32 v149, v150, v151\n\t" \
  "ds_read_b64_tr_b16 v[216:217], v248 offset:51752\n\t" \
  "s_waitcnt lgkmcnt(10)\n\t" \
  "v_mfma_f32_16x16x32_bf16 v[180:183], v[208:211], v[96:99], 0\n\t" \
  "v_cvt_pknorm_i16_f32 v220, |v178|, |v179|\n\t" \
  "v_cvt_pknorm_i16_f32 v222, |v176|, |v177|\n\t" \
  "s_waitcnt vmcnt(2)\n\t" \
  "ds_read_b64_tr_b16 v[218:219], v248 offset:54824\n\t" \
  "v_mfma_f32_16x16x32_bf16 v[188:191], v[208:211], v[100:103], 0\n\t" \
  "v_lshlrev_b32_sdwa v221, 2, v220 dst_sel:DWORD dst_unused:UNUSED_PAD src0_sel:DWORD src1_sel:BYTE_3\n\t" \
  "v_lshlrev_b32_sdwa v220, 2, v220 dst_sel:DWORD dst_unused:UNUSED_PAD src0_sel:DWORD src1_sel:BYTE_1\n\t" \
  "s_mov_b64 exec, 3\n\t" \
  "ds_add_rtn_u32 v246, v251, v250 offset:47208\n\t" \
  "v_mfma_f32_16x16x32_bf16 v[196:199], v[208:211], v[104:107], 0\n\t" \
  "s_mov_b64 exec, -1\n\t" \
  "v_lshlrev_b32_sdwa v223, 2, v222 dst_sel:DWORD dst_unused:UNUSED_PAD src0_sel:DWORD src1_sel:BYTE_3\n\t" \
  "v_lshlrev_b32_sdwa v222, 2, v222 dst_sel:DWORD dst_unused:UNUSED_PAD src0_sel:DWORD src1_sel:BYTE_1\n\t" \
  "ds_read_u16 v234, v234 offset:10528\n\t" \
  "v_mfma_f32_16x16x32_bf16 v[204:207], v[208:211], v[108:111], 0\n\t" \
  "s_waitcnt lgkmcnt(1)\n\t" \
  "v_lshlrev_b32_sdwa v225, 1, v187 dst_sel:DWORD dst_unused:UNUSED_PAD src0_sel:DWORD src1_sel:WORD_1\n\t" \
  "v_readfirstlane_b32 s4, v246\n\t" \
  "ds_read_b32 v221, v221 offset:10016\n\t" \
  "v_mfma_f32_16x16x32_bf16 v[180:183], v[212:215], v[112:115], v[180:183]\n\t" \
  "s_cmpk_lt_u32 s4, 3072\n\t" \
  "v_lshlrev_b32_sdwa v224, 1, v186 dst_sel:DWORD dst_unused:UNUSED_PAD src0_sel:DWORD src1_sel:WORD_1\n\t" \
  "s_cbranch_scc1 .Lcnx_w0_10\n.Lcnx_g0_10:\n\t" \
  "ds_read_b32 v220, v220 offset:10016\n\t" \
  "v_mfma_f32_16x16x32_bf16 v[188:191], v[212:215], v[116:119], v[188:191]\n\t" \
  "v_lshlrev_b32_sdwa v227, 1, v185 dst_sel:DWORD dst_unused:UNUSED_PAD src0_sel:DWORD src1_sel:WORD_1\n\t" \
  "v_lshlrev_b32_sdwa v226, 1, v184 dst_sel:DWORD dst_unused:UNUSED_PAD src0_sel:DWORD src1_sel:WORD_1\n\t" \
  "ds_read_b64_tr_b16 v[208:209], v249 offset:24600\n\t" \
  "v_mfma_f32_16x16x32_bf16 v[196:199], v[212:215], v[120:123], v[196:199]\n\t" \
  "v_fma_mix_f32 v229, |v165|, v229, v229 op_sel:[0,1,0] op_sel_hi:[0,1,1] clamp\n\t" \
  "v_fma_mix_f32 v228, |v164|, v228, v228 op_sel:[0,1,0] op_sel_hi:[0,1,1] clamp\n\t" \
  "ds_read_b64_tr_b16 v[210:211], v249 offset:26136\n\t" \
  "ds_read_b32 v223, v223 offset:10016\n\t" \
  "v_mfma_f32_16x16x32_bf16 v[204:207], v[212:215], v[124:127], v[204:207]\n\t" \
  "v_fma_mix_f32 v231, |v167|, v231, v231 op_sel:[0,1,0] op_sel_hi:[0,1,1] clamp\n\t" \
  "v_fma_mix_f32 v230, |v166|, v230, v230 op_sel:[0,1,0] op_sel_hi:[0,1,1] clamp\n\t" \
  "s_waitcnt lgkmcnt(5)\n\t" \
  "ds_read_b64_tr_b16 v[212:213], v249 offset:24608\n\t" \
  "ds_read_b32 v222, v222 offset:10016\n\t" \
  "v_mfma_f32_16x16x32_bf16 v[180:183], v[216:219], v[128:131], v[180:183]\n\t" \
  "s_mov_b32 m0, s46\n\t" \
  "v_or_b32 v172, v232, v233\n\t" \
  "v_or_b32 v173, v234, v235\n\t" \
  "s_mov_b32 exec_hi, 0xffff\n\t" \
  "buffer_load_dwordx4 v252, s[16:19], s33 offen lds\n\t" \
  "v_mfma_f32_16x16x32_bf16 v[188:191], v[216:219], v[132:135], v[188:191]\n\t" \
  "s_mov_b32 exec_hi, -1\n\t" \
  "v_fma_f32 v165, |v165|, v229, v165 mul:4\n\t" \
  "v_fma_f32 v164, |v164|, v228, v164 mul:4\n\t" \
  "s_add_u32 s33, s33, s32\n\t" \
  "ds_read_b64_tr_b16 v[214:215], v249 offset:26144\n\t" \
  "v_mfma_f32_16x16x32_bf16 v[196:199], v[216:219], v[136:139], v[196:199]\n\t" \
  "v_fma_f32 v167, |v167|, v231, v167 mul:4\n\t" \
  "v_fma_f32 v166, |v166|, v230, v166 mul:4\n\t" \
  "ds_read_u16_d16_hi v225, v225 offset:10528\n\t" \
  "v_mfma_f32_16x16x32_bf16 v[204:207], v[216:219], v[140:143], v[204:207]\n\t" \
  "v_cvt_pk_bf16_f32 v164, v164, v165\n\t" \
  "v_cvt_pk_bf16_f32 v165, v166, v167\n\t" \
  "ds_read_b64_tr_b16 v[216:217], v249 offset:24664\n\t" \
  "s_waitcnt lgkmcnt(6)\n\t" \
  "v_mfma_f32_16x16x32_bf16 v[0:3], v[208:211], v[146:149], v[0:3]\n\t" \
  "v_cvt_pknorm_i16_f32 v228, |v194|, |v195|\n\t" \
  "v_cvt_pknorm_i16_f32 v230, |v192|, |v193|\n\t" \
  "ds_read_b64_tr_b16 v[218:219], v249 offset:26200\n\t" \
  "v_mfma_f32_16x16x32_bf16 v[24:27], v[208:211], v[154:157], v[24:27]\n\t" \
  "v_lshlrev_b32_sdwa v229, 2, v228 dst_sel:DWORD dst_unused:UNUSED_PAD src0_sel:DWORD src1_sel:BYTE_3\n\t" \
  "v_lshlrev_b32_sdwa v228, 2, v228 dst_sel:DWORD dst_unused:UNUSED_PAD src0_sel:DWORD src1_sel:BYTE_1\n\t" \
  "ds_read_u16 v224, v224 offset:10528\n\t" \
  "v_mfma_f32_16x16x32_bf16 v[48:51], v[208:211], v[162:165], v[48:51]\n\t" \
  "v_lshlrev_b32_sdwa v231, 2, v230 dst_sel:DWORD dst_unused:UNUSED_PAD src0_sel:DWORD src1_sel:BYTE_3\n\t" \
  "v_lshlrev_b32_sdwa v230, 2, v230 dst_sel:DWORD dst_unused:UNUSED_PAD src0_sel:DWORD src1_sel:BYTE_1\n\t" \
  "ds_read_u16_d16_hi v227, v227 offset:10528\n\t" \
  "v_mfma_f32_16x16x32_bf16 v[72:75], v[208:211], v[170:173], v[72:75]\n\t" \
  "s_add_u32 m0, s46, s35\n\t" \
  "v_lshlrev_b32_sdwa v233, 1, v203 dst_sel:DWORD dst_unused:UNUSED_PAD src0_sel:DWORD src1_sel:WORD_1\n\t" \
  "v_lshlrev_b32_sdwa v232, 1, v202 dst_sel:DWORD dst_unused:UNUSED_PAD src0_sel:DWORD src1_sel:WORD_1\n\t" \
  "s_waitcnt lgkmcnt(5)\n\t" \
  "s_mov_b32 exec_hi, 0xffff\n\t" \
  "buffer_load_dwordx4 v252, s[16:19], s34 offen lds\n\t" \
  "v_mfma_f32_16x16x32_bf16 v[4:7], v[212:215], v[146:149], v[4:7]\n\t" \
  "s_mov_b32 exec_hi, -1\n\t" \
  "v_lshlrev_b32_sdwa v235, 1, v201 dst_sel:DWORD dst_unused:UNUSED_PAD src0_sel:DWORD src1_sel:WORD_1\n\t" \
  "s_add_u32 s34, s34, s32\n\t" \
  "ds_read_b64_tr_b16 v[208:209], v249 offset:24672\n\t" \
  "ds_read_u16 v226, v226 offset:10528\n\t" \
  "v_mfma_f32_16x16x32_bf16 v[28:31], v[212:215], v[154:157], v[28:31]\n\t" \
  "v_lshlrev_b32_sdwa v234, 1, v200 dst_sel:DWORD dst_unused:UNUSED_PAD src0_sel:DWORD src1_sel:WORD_1\n\t" \
  "ds_read_b64_tr_b16 v[210:211], v249 offset:26208\n\t" \
  "ds_read_b32 v229, v229 offset:10016\n\t" \
  "v_mfma_f32_16x16x32_bf16 v[52:55], v[212:215], v[162:165], v[52:55]\n\t" \
  "v_fma_mix_f32 v221, |v179|, v221, v221 op_sel:[0,1,0] op_sel_hi:[0,1,1] clamp\n\t" \
  "v_fma_mix_f32 v220, |v178|, v220, v220 op_sel:[0,1,0] op_sel_hi:[0,1,1] clamp\n\t" \
  "ds_read_b32 v228, v228 offset:10016\n\t" \
  "ds_read_b32 v231, v231 offset:10016\n\t" \
  "v_mfma_f32_16x16x32_bf16 v[76:79], v[212:215], v[170:173], v[76:79]\n\t" \
  "v_fma_mix_f32 v223, |v177|, v223, v223 op_sel:[0,1,0] op_sel_hi:[0,1,1] clamp\n\t" \
  "v_fma_mix_f32 v222, |v176|, v222, v222 op_sel:[0,1,0] op_sel_hi:[0,1,1] clamp\n\t" \
  "s_waitcnt lgkmcnt(4)\n\t" \
  "ds_read_b64_tr_b16 v[212:213], v249 offset:24728\n\t" \
  "ds_read_b32 v230, v230 offset:10016\n\t" \
  "v_mfma_f32_16x16x32_bf16 v[8:11], v[216:219], v[146:149], v[8:11]\n\t" \
  "v_or_b32 v187, v224, v225\n\t" \
  "v_or_b32 v186, v226, v227\n\t" \
  "ds_read_b64_tr_b16 v[214:215], v249 offset:26264\n\t" \
  "v_mfma_f32_16x16x32_bf16 v[32:35], v[216:219], v[154:157], v[32:35]\n\t" \
  "v_fma_f32 v179, |v179|, v221, v179 mul:4\n\t" \
  "v_fma_f32 v178, |v178|, v220, v178 mul:4\n\t" \
  "ds_read_u16_d16_hi v233, v233 offset:10528\n\t" \
  "v_mfma_f32_16x16x32_bf16 v[56:59], v[216:219], v[162:165], v[56:59]\n\t" \
  "v_fma_f32 v177, |v177|, v223, v177 mul:4\n\t" \
  "v_fma_f32 v176, |v176|, v222, v176 mul:4\n\t" \
  "s_add_u32 m0, s25, 73280\n\t" \
  "s_add_u32 s24, s24, s26\n\t" \
  "ds_read_u16 v232, v232 offset:10528\n\t" \
  "v_mfma_f32_16x16x32_bf16 v[80:83], v[216:219], v[170:173], v[80:83]\n\t" \
  "v_cvt_pk_bf16_f32 v179, v178, v179\n\t" \
  "v_cvt_pk_bf16_f32 v178, v176, v177\n\t" \
  "s_waitcnt lgkmcnt(8)\n\t" \
  "buffer_load_dwordx4 v254, s[8:11], s24 offen lds\n\t" \
  "ds_read_b64_tr_b16 v[216:217], v249 offset:24736\n\t" \
  "v_mfma_f32_16x16x32_bf16 v[12:15], v[208:211], v[146:149], v[12:15]\n\t" \
  "v_cvt_pknorm_i16_f32 v220, |v180|, |v181|\n\t" \
  "v_cvt_pknorm_i16_f32 v222, |v182|, |v183|\n\t" \
  "ds_read_b64_tr_b16 v[218:219], v249 offset:26272\n\t" \
  "v_mfma_f32_16x16x32_bf16 v[36:39], v[208:211], v[154:157], v[36:39]\n\t" \
  "v_lshlrev_b32_sdwa v221, 2, v220 dst_sel:DWORD dst_unused:UNUSED_PAD src0_sel:DWORD src1_sel:BYTE_3\n\t" \
  "v_lshlrev_b32_sdwa v220, 2, v220 dst_sel:DWORD dst_unused:UNUSED_PAD src0_sel:DWORD src1_sel:BYTE_1\n\t" \
  "ds_read_u16_d16_hi v235, v235 offset:10528\n\t" \
  "v_mfma_f32_16x16x32_bf16 v[60:63], v[208:211], v[162:165], v[60:63]\n\t" \
  "v_lshlrev_b32_sdwa v223, 2, v222 dst_sel:DWORD dst_unused:UNUSED_PAD src0_sel:DWORD src1_sel:BYTE_3\n\t" \
  "v_lshlrev_b32_sdwa v222, 2, v222 dst_sel:DWORD dst_unused:UNUSED_PAD src0_sel:DWORD src1_sel:BYTE_1\n\t" \
  "ds_read_u16 v234, v234 offset:10528\n\t" \
  "v_mfma_f32_16x16x32_bf16 v[84:87], v[208:211], v[170:173], v[84:87]\n\t" \
  "v_lshlrev_b32_sdwa v225, 1, v189 dst_sel:DWORD dst_unused:UNUSED_PAD src0_sel:DWORD src1_sel:WORD_1\n\t" \
  "v_lshlrev_b32_sdwa v224, 1, v188 dst_sel:DWORD dst_unused:UNUSED_PAD src0_sel:DWORD src1_sel:WORD_1\n\t" \
  "ds_read_b64_tr_b16 v[208:209], v248 offset:36904\n\t" \
  "s_waitcnt lgkmcnt(7)\n\t" \
  "v_mfma_f32_16x16x32_bf16 v[16:19], v[212:215], v[146:149], v[16:19]\n\t" \
  "v_lshlrev_b32_sdwa v227, 1, v191 dst_sel:DWORD dst_unused:UNUSED_PAD src0_sel:DWORD src1_sel:WORD_1\n\t" \
  "ds_read_b64_tr_b16 v[210:211], v248 offset:37928\n\t" \
  "ds_read_b32 v221, v221 offset:10016\n\t" \
  "v_mfma_f32_16x16x32_bf16 v[40:43], v[212:215], v[154:157], v[40:43]\n\t" \
  "s_add_u32 s27, s27, s26\n\t" \
  "v_lshlrev_b32_sdwa v226, 1, v190 dst_sel:DWORD dst_unused:UNUSED_PAD src0_sel:DWORD src1_sel:WORD_1\n\t" \
  "v_fma_mix_f32 v229, |v195|, v229, v229 op_sel:[0,1,0] op_sel_hi:[0,1,1] clamp\n\t" \
  "s_mov_b64 exec, s[30:31]\n\t" \
  "buffer_load_dword v237, v254, s[8:11], s27 offen\n\t" \
  "v_mfma_f32_16x16x32_bf16 v[64:67], v[212:215], v[162:165], v[64:67]\n\t" \
  "s_mov_b64 exec, -1\n\t" \
  "v_fma_mix_f32 v228, |v194|, v228, v228 op_sel:[0,1,0] op_sel_hi:[0,1,1] clamp\n\t" \
  "ds_read_b32 v220, v220 offset:10016\n\t" \
  "ds_read_b32 v223, v223 offset:10016\n\t" \
  "v_mfma_f32_16x16x32_bf16 v[88:91], v[212:215], v[170:173], v[88:91]\n\t" \
  "v_fma_mix_f32 v231, |v193|, v231, v231 op_sel:[0,1,0] op_sel_hi:[0,1,1] clamp\n\t" \
  "v_fma_mix_f32 v230, |v192|, v230, v230 op_sel:[0,1,0] op_sel_hi:[0,1,1] clamp\n\t" \
  "s_waitcnt lgkmcnt(5)\n\t" \
  "ds_read_b64_tr_b16 v[212:213], v248 offset:39976\n\t" \
  "ds_read_b32 v222, v222 offset:10016\n\t" \
  "v_mfma_f32_16x16x32_bf16 v[20:23], v[216:219], v[146:149], v[20:23]\n\t" \
  "v_or_b32 v203, v232, v233\n\t" \
  "v_or_b32 v202, v234, v235\n\t" \
  "ds_read_b64_tr_b16 v[214:215], v248 offset:41000\n\t" \
  "v_mfma_f32_16x16x32_bf16 v[44:47], v[216:219], v[154:157], v[44:47]\n\t" \
  "v_fma_f32 v195, |v195|, v229, v195 mul:4\n\t" \
  "v_fma_f32 v194, |v194|, v228, v194 mul:4\n\t" \
  "ds_read_u16_d16_hi v225, v225 offset:10528\n\t" \
  "v_mfma_f32_16x16x32_bf16 v[68:71], v[216:219], v[162:165], v[68:71]\n\t" \
  "v_fma_f32 v193, |v193|, v231, v193 mul:4\n\t" \
  "v_fma_f32 v192, |v192|, v230, v192 mul:4\n\t" \
  "ds_read_u16 v224, v224 offset:10528\n\t" \
  "v_mfma_f32_16x16x32_bf16 v[92:95], v[216:219], v[170:173], v[92:95]\n\t" \
  "v_cvt_pk_bf16_f32 v195, v194, v195\n\t" \
  "v_cvt_pk_bf16_f32 v194, v192, v193\n\t" \
  "ds_read_b64_tr_b16 v[216:217], v248 offset:38952\n\t" \
  "s_waitcnt lgkmcnt(9)\n\t" \
  "v_mfma_f32_16x16x32_bf16 v[144:147], v[208:211], v[96:99], 0\n\t" \
  "v_cvt_pknorm_i16_f32 v228, |v196|, |v197|\n\t" \
  "v_cvt_pknorm_i16_f32 v230, |v198|, |v199|\n\t" \
  "ds_read_b64_tr_b16 v[218:219], v248 offset:42024\n\t" \
  "v_mfma_f32_16x16x32_bf16 v[152:155], v[208:211], v[100:103], 0\n\t" \
  "v_lshlrev_b32_sdwa v229, 2, v228 dst_sel:DWORD dst_unused:UNUSED_PAD src0_sel:DWORD src1_sel:BYTE_3\n\t" \
  "v_lshlrev_b32_sdwa v228, 2, v228 dst_sel:DWORD dst_unused:UNUSED_PAD src0_sel:DWORD src1_sel:BYTE_1\n\t" \
  "ds_read_u16_d16_hi v227, v227 offset:10528\n\t" \
  "v_mfma_f32_16x16x32_bf16 v[160:163], v[208:211], v[104:107], 0\n\t" \
  "s_cmp_eq_u32 s38, 4\n\t" \
  "v_lshlrev_b32_sdwa v231, 2, v230 dst_sel:DWORD dst_unused:UNUSED_PAD src0_sel:DWORD src1_sel:BYTE_3\n\t" \
  "s_cbranch_scc1 .Lcnx_t0\n.Lcnx_u0:\n\t" \
  "ds_read_u16 v226, v226 offset:10528\n\t" \
  "v_mfma_f32_16x16x32_bf16 v[168:171], v[208:211], v[108:111], 0\n\t" \
  "v_lshlrev_b32_sdwa v230, 2, v230 dst_sel:DWORD dst_unused:UNUSED_PAD src0_sel:DWORD src1_sel:BYTE_1\n\t" \
  "v_lshlrev_b32_sdwa v233, 1, v205 dst_sel:DWORD dst_unused:UNUSED_PAD src0_sel:DWORD src1_sel:WORD_1\n\t" \
  "ds_read_b64_tr_b16 v[208:209], v248 offset:37416\n\t" \
  "s_waitcnt lgkmcnt(7)\n\t" \
  "v_mfma_f32_16x16x32_bf16 v[144:147], v[212:215], v[112:115], v[144:147]\n\t" \
  "v_lshlrev_b32_sdwa v232, 1, v204 dst_sel:DWORD dst_unused:UNUSED_PAD src0_sel:DWORD src1_sel:WORD_1\n\t" \
  "v_lshlrev_b32_sdwa v235, 1, v207 dst_sel:DWORD dst_unused:UNUSED_PAD src0_sel:DWORD src1_sel:WORD_1\n\t" \
  "ds_read_b64_tr_b16 v[210:211], v248 offset:38440\n\t" \
  "v_mfma_f32_16x16x32_bf16 v[152:155], v[212:215], v[116:119], v[152:155]\n\t" \
  "v_lshlrev_b32_sdwa v234, 1, v206 dst_sel:DWORD dst_unused:UNUSED_PAD src0_sel:DWORD src1_sel:WORD_1\n\t" \
  "v_fma_mix_f32 v221, |v181|, v221, v221 op_sel:[0,1,0] op_sel_hi:[0,1,1] clamp\n\t" \
  "ds_read_b32 v229, v229 offset:10016\n\t" \
  "ds_read_b32 v228, v228 offset:10016\n\t" \
  "v_mfma_f32_16x16x32_bf16 v[160:163], v[212:215], v[120:123], v[160:163]\n\t" \
  "v_fma_mix_f32 v220, |v180|, v220, v220 op_sel:[0,1,0] op_sel_hi:[0,1,1] clamp\n\t" \
  "ds_read_b32 v231, v231 offset:10016\n\t" \
  "ds_read_b32 v230, v230 offset:10016\n\t" \
  "v_mfma_f32_16x16x32_bf16 v[168:171], v[212:215], v[124:127], v[168:171]\n\t" \
  "v_fma_mix_f32 v223, |v183|, v223, v223 op_sel:[0,1,0] op_sel_hi:[0,1,1] clamp\n\t" \
  "v_fma_mix_f32 v222, |v182|, v222, v222 op_sel:[0,1,0] op_sel_hi:[0,1,1] clamp\n\t" \
  "s_waitcnt lgkmcnt(6)\n\t" \
  "ds_read_b64_tr_b16 v[212:213], v248 offset:40488\n\t" \
  "ds_read_u16_d16_hi v233, v233 offset:10528\n\t" \
  "v_mfma_f32_16x16x32_bf16 v[144:147], v[216:219], v[128:131], v[144:147]\n\t" \
  "v_or_b32 v188, v224, v225\n\t" \
  "v_or_b32 v189, v226, v227\n\t" \
  "ds_read_b64_tr_b16 v[214:215], v248 offset:41512\n\t" \
  "v_mfma_f32_16x16x32_bf16 v[152:155], v[216:219], v[132:135], v[152:155]\n\t" \
  "v_fma_f32 v181, |v181|, v221, v181 mul:4\n\t" \
  "v_fma_f32 v180, |v180|, v220, v180 mul:4\n\t" \
  "ds_read_u16 v232, v232 offset:10528\n\t" \
  "v_mfma_f32_16x16x32_bf16 v[160:163], v[216:219], v[136:139], v[160:163]\n\t" \
  "v_fma_f32 v183, |v183|, v223, v183 mul:4\n\t" \
  "v_fma_f32 v182, |v182|, v222, v182 mul:4\n\t" \
  "ds_read_u16_d16_hi v235, v235 offset:10528\n\t" \
  "v_mfma_f32_16x16x32_bf16 v[168:171], v[216:219], v[140:143], v[168:171]\n\t" \
  "v_cvt_pk_bf16_f32 v180, v180, v181\n\t" \
  "v_cvt_pk_bf16_f32 v181, v182, v183\n\t" \
  "ds_read_b64_tr_b16 v[216:217], v248 offset:39464\n\t" \
  "s_waitcnt lgkmcnt(10)\n\t" \
  "v_mfma_f32_16x16x32_bf16 v[148:151], v[208:211], v[96:99], 0\n\t" \
  "v_cvt_pknorm_i16_f32 v220, |v146|, |v147|\n\t" \
  "v_cvt_pknorm_i16_f32 v222, |v144|, |v145|\n\t" \
  "s_waitcnt vmcnt(2)\n\t" \
  "ds_read_b64_tr_b16 v[218:219], v248 offset:42536\n\t" \
  "v_mfma_f32_16x16x32_bf16 v[156:159], v[208:211], v[100:103], 0\n\t" \
  "v_lshlrev_b32_sdwa v221, 2, v220 dst_sel:DWORD dst_unused:UNUSED_PAD src0_sel:DWORD src1_sel:BYTE_3\n\t" \
  "v_lshlrev_b32_sdwa v220, 2, v220 dst_sel:DWORD dst_unused:UNUSED_PAD src0_sel:DWORD src1_sel:BYTE_1\n\t" \
  "s_mov_b64 exec, 3\n\t" \
  "ds_add_rtn_u32 v246, v251, v250 offset:47216\n\t" \
  "v_mfma_f32_16x16x32_bf16 v[164:167], v[208:211], v[104:107], 0\n\t" \
  "s_mov_b64 exec, -1\n\t" \
  "v_lshlrev_b32_sdwa v223, 2, v222 dst_sel:DWORD dst_unused:UNUSED_PAD src0_sel:DWORD src1_sel:BYTE_3\n\t" \
  "v_lshlrev_b32_sdwa v222, 2, v222 dst_sel:DWORD dst_unused:UNUSED_PAD src0_sel:DWORD src1_sel:BYTE_1\n\t" \
  "ds_read_u16 v234, v234 offset:10528\n\t" \
  "v_mfma_f32_16x16x32_bf16 v[172:175], v[208:211], v[108:111], 0\n\t" \
  "s_waitcnt lgkmcnt(1)\n\t" \
  "v_lshlrev_b32_sdwa v225, 1, v155 dst_sel:DWORD dst_unused:UNUSED_PAD src0_sel:DWORD src1_sel:WORD_1\n\t" \
  "v_readfirstlane_b32 s4, v246\n\t" \
  "ds_read_b32 v221, v221 offset:10016\n\t" \
  "v_mfma_f32_16x16x32_bf16 v[148:151], v[212:215], v[112:115], v[148:151]\n\t" \
  "s_cmpk_lt_u32 s4, 3072\n\t" \
  "v_lshlrev_b32_sdwa v224, 1, v154 dst_sel:DWORD dst_unused:UNUSED_PAD src0_sel:DWORD src1_sel:WORD_1\n\t" \
  "s_cbranch_scc1 .Lcnx_w0_11\n.Lcnx_g0_11:\n\t" \
  "ds_read_b32 v220, v220 offset:10016\n\t" \
  "v_mfma_f32_16x16x32_bf16 v[156:159], v[212:215], v[116:119], v[156:159]\n\t" \
  "v_lshlrev_b32_sdwa v227, 1, v153 dst_sel:DWORD dst_unused:UNUSED_PAD src0_sel:DWORD src1_sel:WORD_1\n\t" \
  "v_lshlrev_b32_sdwa v226, 1, v152 dst_sel:DWORD dst_unused:UNUSED_PAD src0_sel:DWORD src1_sel:WORD_1\n\t" \
  "ds_read_b64_tr_b16 v[208:209], v249 offset:30752\n\t" \
  "v_mfma_f32_16x16x32_bf16 v[164:167], v[212:215], v[120:123], v[164:167]\n\t" \
  "v_fma_mix_f32 v229, |v197|, v229, v229 op_sel:[0,1,0] op_sel_hi:[0,1,1] clamp\n\t" \
  "v_fma_mix_f32 v228, |v196|, v228, v228 op_sel:[0,1,0] op_sel_hi:[0,1,1] clamp\n\t" \
  "ds_read_b64_tr_b16 v[210:211], v249 offset:32288\n\t" \
  "ds_read_b32 v223, v223 offset:10016\n\t" \
  "v_mfma_f32_16x16x32_bf16 v[172:175], v[212:215], v[124:127], v[172:175]\n\t" \
  "v_fma_mix_f32 v231, |v199|, v231, v231 op_sel:[0,1,0] op_sel_hi:[0,1,1] clamp\n\t" \
  "v_fma_mix_f32 v230, |v198|, v230, v230 op_sel:[0,1,0] op_sel_hi:[0,1,1] clamp\n\t" \
  "s_waitcnt lgkmcnt(5)\n\t" \
  "ds_read_b64_tr_b16 v[212:213], v249 offset:30760\n\t" \
  "ds_read_b32 v222, v222 offset:10016\n\t" \
  "v_mfma_f32_16x16x32_bf16 v[148:151], v[216:219], v[128:131], v[148:151]\n\t" \
  "s_mov_b32 m0, s47\n\t" \
  "v_or_b32 v204, v232, v233\n\t" \
  "v_or_b32 v205, v234, v235\n\t" \
  "s_mov_b32 exec_hi, 0xffff\n\t" \
  "buffer_load_dwordx4 v252, s[16:19], s33 offen lds\n\t" \
  "v_mfma_f32_16x16x32_bf16 v[156:159], v[216:219], v[132:135], v[156:159]\n\t" \
  "s_mov_b32 exec_hi, -1\n\t" \
  "v_fma_f32 v197, |v197|, v229, v197 mul:4\n\t" \
  "v_fma_f32 v196, |v196|, v228, v196 mul:4\n\t" \
  "s_add_u32 s33, s33, s49\n\t" \
  "ds_read_b64_tr_b16 v[214:215], v249 offset:32296\n\t" \
  "v_mfma_f32_16x16x32_bf16 v[164:167], v[216:219], v[136:139], v[164:167]\n\t" \
  "v_fma_f32 v199, |v199|, v231, v199 mul:4\n\t" \
  "v_fma_f32 v198, |v198|, v230, v198 mul:4\n\t" \
  "ds_read_u16_d16_hi v225, v225 offset:10528\n\t" \
  "v_mfma_f32_16x16x32_bf16 v[172:175], v[216:219], v[140:143], v[172:175]\n\t" \
  "v_cvt_pk_bf16_f32 v196, v196, v197\n\t" \
  "v_cvt_pk_bf16_f32 v197, v198, v199\n\t" \
  "ds_read_b64_tr_b16 v[216:217], v249 offset:30816\n\t" \
  "s_waitcnt lgkmcnt(6)\n\t" \
  "v_mfma_f32_16x16x32_bf16 v[0:3], v[208:211], v[178:181], v[0:3]\n\t" \
  "v_cvt_pknorm_i16_f32 v228, |v162|, |v163|\n\t" \
  "v_cvt_pknorm_i16_f32 v230, |v160|, |v161|\n\t" \
  "ds_read_b64_tr_b16 v[218:219], v249 offset:32352\n\t" \
  "v_mfma_f32_16x16x32_bf16 v[24:27], v[208:211], v[186:189], v[24:27]\n\t" \
  "v_lshlrev_b32_sdwa v229, 2, v228 dst_sel:DWORD dst_unused:UNUSED_PAD src0_sel:DWORD src1_sel:BYTE_3\n\t" \
  "v_lshlrev_b32_sdwa v228, 2, v228 dst_sel:DWORD dst_unused:UNUSED_PAD src0_sel:DWORD src1_sel:BYTE_1\n\t" \
  "ds_read_u16 v224, v224 offset:10528\n\t" \
  "v_mfma_f32_16x16x32_bf16 v[48:51], v[208:211], v[194:197], v[48:51]\n\t" \
  "v_lshlrev_b32_sdwa v231, 2, v230 dst_sel:DWORD dst_unused:UNUSED_PAD src0_sel:DWORD src1_sel:BYTE_3\n\t" \
  "v_lshlrev_b32_sdwa v230, 2, v230 dst_sel:DWORD dst_unused:UNUSED_PAD src0_sel:DWORD src1_sel:BYTE_1\n\t" \
  "ds_read_u16_d16_hi v227, v227 offset:10528\n\t" \
  "v_mfma_f32_16x16x32_bf16 v[72:75], v[208:211], v[202:205], v[72:75]\n\t" \
  "s_add_u32 m0, s47, s35\n\t" \
  "v_lshlrev_b32_sdwa v233, 1, v171 dst_sel:DWORD dst_unused:UNUSED_PAD src0_sel:DWORD src1_sel:WORD_1\n\t" \
  "v_lshlrev_b32_sdwa v232, 1, v170 dst_sel:DWORD dst_unused:UNUSED_PAD src0_sel:DWORD src1_sel:WORD_1\n\t" \
  "s_waitcnt lgkmcnt(5)\n\t" \
  "s_mov_b32 exec_hi, 0xffff\n\t" \
  "buffer_load_dwordx4 v252, s[16:19], s34 offen lds\n\t" \
  "v_mfma_f32_16x16x32_bf16 v[4:7], v[212:215], v[178:181], v[4:7]\n\t" \
  "s_mov_b32 exec_hi, -1\n\t" \
  "v_lshlrev_b32_sdwa v235, 1, v169 dst_sel:DWORD dst_unused:UNUSED_PAD src0_sel:DWORD src1_sel:WORD_1\n\t" \
  "s_add_u32 s34, s34, s49\n\t" \
  "ds_read_b64_tr_b16 v[208:209], v249 offset:30824\n\t" \
  "ds_read_u16 v226, v226 offset:10528\n\t" \
  "v_mfma_f32_16x16x32_bf16 v[28:31], v[212:215], v[186:189], v[28:31]\n\t" \
  "v_lshlrev_b32_sdwa v234, 1, v168 dst_sel:DWORD dst_unused:UNUSED_PAD src0_sel:DWORD src1_sel:WORD_1\n\t" \
  "ds_read_b64_tr_b16 v[210:211], v249 offset:32360\n\t" \
  "ds_read_b32 v229, v229 offset:10016\n\t" \
  "v_mfma_f32_16x16x32_bf16 v[52:55], v[212:215], v[194:197], v[52:55]\n\t" \
  "v_fma_mix_f32 v221, |v147|, v221, v221 op_sel:[0,1,0] op_sel_hi:[0,1,1] clamp\n\t" \
  "v_fma_mix_f32 v220, |v146|, v220, v220 op_sel:[0,1,0] op_sel_hi:[0,1,1] clamp\n\t" \
  "ds_read_b32 v228, v228 offset:10016\n\t" \
  "ds_read_b32 v231, v231 offset:10016\n\t" \
  "v_mfma_f32_16x16x32_bf16 v[76:79], v[212:215], v[202:205], v[76:79]\n\t" \
  "v_fma_mix_f32 v223, |v145|, v223, v223 op_sel:[0,1,0] op_sel_hi:[0,1,1] clamp\n\t" \
  "v_fma_mix_f32 v222, |v144|, v222, v222 op_sel:[0,1,0] op_sel_hi:[0,1,1] clamp\n\t" \
  "s_waitcnt lgkmcnt(4)\n\t" \
  "ds_read_b64_tr_b16 v[212:213], v249 offset:30880\n\t" \
  "ds_read_b32 v230, v230 offset:10016\n\t" \
  "v_mfma_f32_16x16x32_bf16 v[8:11], v[216:219], v[178:181], v[8:11]\n\t" \
  "v_or_b32 v155, v224, v225\n\t" \
  "v_or_b32 v154, v226, v227\n\t" \
  "ds_read_b64_tr_b16 v[214:215], v249 offset:32416\n\t" \
  "v_mfma_f32_16x16x32_bf16 v[32:35], v[216:219], v[186:189], v[32:35]\n\t" \
  "v_fma_f32 v147, |v147|, v221, v147 mul:4\n\t" \
  "v_fma_f32 v146, |v146|, v220, v146 mul:4\n\t" \
  "ds_read_u16_d16_hi v233, v233 offset:10528\n\t" \
  "v_mfma_f32_16x16x32_bf16 v[56:59], v[216:219], v[194:197], v[56:59]\n\t" \
  "v_fma_f32 v145, |v145|, v223, v145 mul:4\n\t" \
  "v_fma_f32 v144, |v144|, v222, v144 mul:4\n\t" \
  "ds_read_u16 v232, v232 offset:10528\n\t" \
  "v_mfma_f32_16x16x32_bf16 v[80:83], v[216:219], v[202:205], v[80:83]\n\t" \
  "v_cvt_pk_bf16_f32 v147, v146, v147\n\t" \
  "v_cvt_pk_bf16_f32 v146, v144, v145\n\t" \
  "ds_read_b64_tr_b16 v[216:217], v249 offset:30888\n\t" \
  "s_waitcnt lgkmcnt(9)\n\t" \
  "v_mfma_f32_16x16x32_bf16 v[12:15], v[208:211], v[178:181], v[12:15]\n\t" \
  "v_cvt_pknorm_i16_f32 v220, |v148|, |v149|\n\t" \
  "v_cvt_pknorm_i16_f32 v222, |v150|, |v151|\n\t" \
  "ds_read_b64_tr_b16 v[218:219], v249 offset:32424\n\t" \
  "v_mfma_f32_16x16x32_bf16 v[36:39], v[208:211], v[186:189], v[36:39]\n\t" \
  "v_lshlrev_b32_sdwa v221, 2, v220 dst_sel:DWORD dst_unused:UNUSED_PAD src0_sel:DWORD src1_sel:BYTE_3\n\t" \
  "v_lshlrev_b32_sdwa v220, 2, v220 dst_sel:DWORD dst_unused:UNUSED_PAD src0_sel:DWORD src1_sel:BYTE_1\n\t" \
  "ds_read_u16_d16_hi v235, v235 offset:10528\n\t" \
  "v_mfma_f32_16x16x32_bf16 v[60:63], v[208:211], v[194:197], v[60:63]\n\t" \
  "v_lshlrev_b32_sdwa v223, 2, v222 dst_sel:DWORD dst_unused:UNUSED_PAD src0_sel:DWORD src1_sel:BYTE_3\n\t" \
  "v_lshlrev_b32_sdwa v222, 2, v222 dst_sel:DWORD dst_unused:UNUSED_PAD src0_sel:DWORD src1_sel:BYTE_1\n\t" \
  "ds_read_u16 v234, v234 offset:10528\n\t" \
  "v_mfma_f32_16x16x32_bf16 v[84:87], v[208:211], v[202:205], v[84:87]\n\t" \
  "v_lshlrev_b32_sdwa v225, 1, v157 dst_sel:DWORD dst_unused:UNUSED_PAD src0_sel:DWORD src1_sel:WORD_1\n\t" \
  "v_lshlrev_b32_sdwa v224, 1, v156 dst_sel:DWORD dst_unused:UNUSED_PAD src0_sel:DWORD src1_sel:WORD_1\n\t" \
  "ds_read_b64_tr_b16 v[208:209], v248 offset:0\n\t" \
  "s_waitcnt lgkmcnt(7)\n\t" \
  "v_mfma_f32_16x16x32_bf16 v[16:19], v[212:215], v[178:181], v[16:19]\n\t" \
  "v_lshlrev_b32_sdwa v227, 1, v159 dst_sel:DWORD dst_unused:UNUSED_PAD src0_sel:DWORD src1_sel:WORD_1\n\t" \
  "ds_read_b64_tr_b16 v[210:211], v248 offset:1024\n\t" \
  "ds_read_b32 v221, v221 offset:10016\n\t" \
  "v_mfma_f32_16x16x32_bf16 v[40:43], v[212:215], v[186:189], v[40:43]\n\t" \
  "s_add_u32 s27, s27, s26\n\t" \
  "v_lshlrev_b32_sdwa v226, 1, v158 dst_sel:DWORD dst_unused:UNUSED_PAD src0_sel:DWORD src1_sel:WORD_1\n\t" \
  "v_fma_mix_f32 v229, |v163|, v229, v229 op_sel:[0,1,0] op_sel_hi:[0,1,1] clamp\n\t" \
  "s_mov_b64 exec, s[30:31]\n\t" \
  "buffer_load_dword v237, v254, s[8:11], s27 offen\n\t" \
  "v_mfma_f32_16x16x32_bf16 v[64:67], v[212:215], v[194:197], v[64:67]\n\t" \
  "s_mov_b64 exec, -1\n\t" \
  "v_fma_mix_f32 v228, |v162|, v228, v228 op_sel:[0,1,0] op_sel_hi:[0,1,1] clamp\n\t" \
  "ds_read_b32 v220, v220 offset:10016\n\t" \
  "ds_read_b32 v223, v223 offset:10016\n\t" \
  "v_mfma_f32_16x16x32_bf16 v[88:91], v[212:215], v[202:205], v[88:91]\n\t" \
  "v_fma_mix_f32 v231, |v161|, v231, v231 op_sel:[0,1,0] op_sel_hi:[0,1,1] clamp\n\t" \
  "v_fma_mix_f32 v230, |v160|, v230, v230 op_sel:[0,1,0] op_sel_hi:[0,1,1] clamp\n\t" \
  "s_waitcnt lgkmcnt(5)\n\t" \
  "ds_read_b64_tr_b16 v[212:213], v248 offset:3072\n\t" \
  "ds_read_b32 v222, v222 offset:10016\n\t" \
  "v_mfma_f32_16x16x32_bf16 v[20:23], v[216:219], v[178:181], v[20:23]\n\t" \
  "v_or_b32 v171, v232, v233\n\t" \
  "v_or_b32 v170, v234, v235\n\t" \
  "ds_read_b64_tr_b16 v[214:215], v248 offset:4096\n\t" \
  "v_mfma_f32_16x16x32_bf16 v[44:47], v[216:219], v[186:189], v[44:47]\n\t" \
  "v_fma_f32 v163, |v163|, v229, v163 mul:4\n\t" \
  "v_fma_f32 v162, |v162|, v228, v162 mul:4\n\t" \
  "ds_read_u16_d16_hi v225, v225 offset:10528\n\t" \
  "v_mfma_f32_16x16x32_bf16 v[68:71], v[216:219], v[194:197], v[68:71]\n\t" \
  "v_fma_f32 v161, |v161|, v231, v161 mul:4\n\t" \
  "v_fma_f32 v160, |v160|, v230, v160 mul:4\n\t" \
  "ds_read_u16 v224, v224 offset:10528\n\t" \
  "v_mfma_f32_16x16x32_bf16 v[92:95], v[216:219], v[202:205], v[92:95]\n\t" \
  "v_cvt_pk_bf16_f32 v163, v162, v163\n\t" \
  "v_cvt_pk_bf16_f32 v162, v160, v161\n\t" \
  "ds_read_b64_tr_b16 v[216:217], v248 offset:2048\n\t" \
  "s_waitcnt lgkmcnt(9)\n\t" \
  "v_mfma_f32_16x16x32_bf16 v[176:179], v[208:211], v[96:99], 0\n\t" \
  "v_cvt_pknorm_i16_f32 v228, |v164|, |v165|\n\t" \
  "v_cvt_pknorm_i16_f32 v230, |v166|, |v167|\n\t" \
  "ds_read_b64_tr_b16 v[218:219], v248 offset:5120\n\t" \
  "v_mfma_f32_16x16x32_bf16 v[184:187], v[208:211], v[100:103], 0\n\t" \
  "v_lshlrev_b32_sdwa v229, 2, v228 dst_sel:DWORD dst_unused:UNUSED_PAD src0_sel:DWORD src1_sel:BYTE_3\n\t" \
  "v_lshlrev_b32_sdwa v228, 2, v228 dst_sel:DWORD dst_unused:UNUSED_PAD src0_sel:DWORD src1_sel:BYTE_1\n\t" \
  "ds_read_u16_d16_hi v227, v227 offset:10528\n\t" \
  "v_mfma_f32_16x16x32_bf16 v[192:195], v[208:211], v[104:107], 0\n\t" \
  "v_lshlrev_b32_sdwa v231, 2, v230 dst_sel:DWORD dst_unused:UNUSED_PAD src0_sel:DWORD src1_sel:BYTE_3\n\t" \
  "v_lshlrev_b32_sdwa v230, 2, v230 dst_sel:DWORD dst_unused:UNUSED_PAD src0_sel:DWORD src1_sel:BYTE_1\n\t" \
  "ds_read_u16 v226, v226 offset:10528\n\t" \
  "v_mfma_f32_16x16x32_bf16 v[200:203], v[208:211], v[108:111], 0\n\t" \
  "v_lshlrev_b32_sdwa v233, 1, v173 dst_sel:DWORD dst_unused:UNUSED_PAD src0_sel:DWORD src1_sel:WORD_1\n\t" \
  "v_lshlrev_b32_sdwa v232, 1, v172 dst_sel:DWORD dst_unused:UNUSED_PAD src0_sel:DWORD src1_sel:WORD_1\n\t" \
  "ds_read_b64_tr_b16 v[208:209], v248 offset:512\n\t" \
  "s_waitcnt lgkmcnt(7)\n\t" \
  "v_mfma_f32_16x16x32_bf16 v[176:179], v[212:215], v[112:115], v[176:179]\n\t" \
  "v_lshlrev_b32_sdwa v235, 1, v175 dst_sel:DWORD dst_unused:UNUSED_PAD src0_sel:DWORD src1_sel:WORD_1\n\t" \
  "ds_read_b64_tr_b16 v[210:211], v248 offset:1536\n\t" \
  "ds_read_b32 v229, v229 offset:10016\n\t" \
  "v_mfma_f32_16x16x32_bf16 v[184:187], v[212:215], v[116:119], v[184:187]\n\t" \
  "v_lshlrev_b32_sdwa v234, 1, v174 dst_sel:DWORD dst_unused:UNUSED_PAD src0_sel:DWORD src1_sel:WORD_1\n\t" \
  "v_fma_mix_f32 v221, |v149|, v221, v221 op_sel:[0,1,0] op_sel_hi:[0,1,1] clamp\n\t" \
  "ds_read_b32 v228, v228 offset:10016\n\t" \
  "v_mfma_f32_16x16x32_bf16 v[192:195], v[212:215], v[120:123], v[192:195]\n\t" \
  "v_fma_mix_f32 v220, |v148|, v220, v220 op_sel:[0,1,0] op_sel_hi:[0,1,1] clamp\n\t" \
  "ds_read_b32 v231, v231 offset:10016\n\t" \
  "ds_read_b32 v230, v230 offset:10016\n\t" \
  "v_mfma_f32_16x16x32_bf16 v[200:203], v[212:215], v[124:127], v[200:203]\n\t" \
  "v_fma_mix_f32 v223, |v151|, v223, v223 op_sel:[0,1,0] op_sel_hi:[0,1,1] clamp\n\t" \
  "v_fma_mix_f32 v222, |v150|, v222, v222 op_sel:[0,1,0] op_sel_hi:[0,1,1] clamp\n\t" \
  "s_waitcnt lgkmcnt(6)\n\t" \
  "ds_read_b64_tr_b16 v[212:213], v248 offset:3584\n\t" \
  "ds_read_u16_d16_hi v233, v233 offset:10528\n\t" \
  "v_mfma_f32_16x16x32_bf16 v[176:179], v[216:219], v[128:131], v[176:179]\n\t" \
  "v_or_b32 v156, v224, v225\n\t" \
  "v_or_b32 v157, v226, v227\n\t" \
  "ds_read_b64_tr_b16 v[214:215], v248 offset:4608\n\t" \
  "v_mfma_f32_16x16x32_bf16 v[184:187], v[216:219], v[132:135], v[184:187]\n\t" \
  "v_fma_f32 v149, |v149|, v221, v149 mul:4\n\t" \
  "v_fma_f32 v148, |v148|, v220, v148 mul:4\n\t" \
  "ds_read_u16 v232, v232 offset:10528\n\t" \
  "v_mfma_f32_16x16x32_bf16 v[192:195], v[216:219], v[136:139], v[192:195]\n\t" \
  "v_fma_f32 v151, |v151|, v223, v151 mul:4\n\t" \
  "v_fma_f32 v150, |v150|, v222, v150 mul:4\n\t" \
  "ds_read_u16_d16_hi v235, v235 offset:10528\n\t" \
  "v_mfma_f32_16x16x32_bf16 v[200:203], v[216:219], v[140:143], v[200:203]\n\t" \
  "v_cvt_pk_bf16_f32 v148, v148, v149\n\t" \
  "v_cvt_pk_bf16_f32 v149, v150, v151\n\t" \
  "ds_read_b64_tr_b16 v[216:217], v248 offset:2560\n\t" \
  "s_waitcnt lgkmcnt(10)\n\t" \
  "v_mfma_f32_16x16x32_bf16 v[180:183], v[208:211], v[96:99], 0\n\t" \
  "v_cvt_pknorm_i16_f32 v220, |v178|, |v179|\n\t" \
  "v_cvt_pknorm_i16_f32 v222, |v176|, |v177|\n\t" \
  "s_waitcnt vmcnt(1)\n\t" \
  "ds_read_b64_tr_b16 v[218:219], v248 offset:5632\n\t" \
  "v_mfma_f32_16x16x32_bf16 v[188:191], v[208:211], v[100:103], 0\n\t" \
  "v_lshlrev_b32_sdwa v221, 2, v220 dst_sel:DWORD dst_unused:UNUSED_PAD src0_sel:DWORD src1_sel:BYTE_3\n\t" \
  "v_lshlrev_b32_sdwa v220, 2, v220 dst_sel:DWORD dst_unused:UNUSED_PAD src0_sel:DWORD src1_sel:BYTE_1\n\t" \
  "s_mov_b64 exec, 3\n\t" \
  "ds_add_rtn_u32 v246, v251, v250 offset:47224\n\t" \
  "v_mfma_f32_16x16x32_bf16 v[196:199], v[208:211], v[104:107], 0\n\t" \
  "s_mov_b64 exec, -1\n\t" \
  "v_lshlrev_b32_sdwa v223, 2, v222 dst_sel:DWORD dst_unused:UNUSED_PAD src0_sel:DWORD src1_sel:BYTE_3\n\t" \
  "v_lshlrev_b32_sdwa v222, 2, v222 dst_sel:DWORD dst_unused:UNUSED_PAD src0_sel:DWORD src1_sel:BYTE_1\n\t" \
  "ds_read_u16 v234, v234 offset:10528\n\t" \
  "v_mfma_f32_16x16x32_bf16 v[204:207], v[208:211], v[108:111], 0\n\t" \
  "s_waitcnt lgkmcnt(1)\n\t" \
  "v_lshlrev_b32_sdwa v225, 1, v187 dst_sel:DWORD dst_unused:UNUSED_PAD src0_sel:DWORD src1_sel:WORD_1\n\t" \
  "v_readfirstlane_b32 s4, v246\n\t" \
  "ds_read_b32 v221, v221 offset:10016\n\t" \
  "v_mfma_f32_16x16x32_bf16 v[180:183], v[212:215], v[112:115], v[180:183]\n\t" \
  "s_cmpk_lt_u32 s4, 3072\n\t" \
  "v_lshlrev_b32_sdwa v224, 1, v186 dst_sel:DWORD dst_unused:UNUSED_PAD src0_sel:DWORD src1_sel:WORD_1\n\t" \
  "s_cbranch_scc1 .Lcnx_w0_12\n.Lcnx_g0_12:\n\t" \
  "ds_read_b32 v220, v220 offset:10016\n\t" \
  "v_mfma_f32_16x16x32_bf16 v[188:191], v[212:215], v[116:119], v[188:191]\n\t" \
  "v_lshlrev_b32_sdwa v227, 1, v185 dst_sel:DWORD dst_unused:UNUSED_PAD src0_sel:DWORD src1_sel:WORD_1\n\t" \
  "v_lshlrev_b32_sdwa v226, 1, v184 dst_sel:DWORD dst_unused:UNUSED_PAD src0_sel:DWORD src1_sel:WORD_1\n\t" \
  "ds_read_b64_tr_b16 v[208:209], v249 offset:6152\n\t" \
  "v_mfma_f32_16x16x32_bf16 v[196:199], v[212:215], v[120:123], v[196:199]\n\t" \
  "v_fma_mix_f32 v229, |v165|, v229, v229 op_sel:[0,1,0] op_sel_hi:[0,1,1] clamp\n\t" \
  "v_fma_mix_f32 v228, |v164|, v228, v228 op_sel:[0,1,0] op_sel_hi:[0,1,1] clamp\n\t" \
  "ds_read_b64_tr_b16 v[210:211], v249 offset:7688\n\t" \
  "ds_read_b32 v223, v223 offset:10016\n\t" \
  "v_mfma_f32_16x16x32_bf16 v[204:207], v[212:215], v[124:127], v[204:207]\n\t" \
  "v_fma_mix_f32 v231, |v167|, v231, v231 op_sel:[0,1,0] op_sel_hi:[0,1,1] clamp\n\t" \
  "v_fma_mix_f32 v230, |v166|, v230, v230 op_sel:[0,1,0] op_sel_hi:[0,1,1] clamp\n\t" \
  "s_waitcnt lgkmcnt(5)\n\t" \
  "ds_read_b64_tr_b16 v[212:213], v249 offset:6160\n\t" \
  "ds_read_b32 v222, v222 offset:10016\n\t" \
  "v_mfma_f32_16x16x32_bf16 v[180:183], v[216:219], v[128:131], v[180:183]\n\t" \
  "v_or_b32 v172, v232, v233\n\t" \
  "v_or_b32 v173, v234, v235\n\t" \
  "ds_read_b64_tr_b16 v[214:215], v249 offset:7696\n\t" \
  "v_mfma_f32_16x16x32_bf16 v[188:191], v[216:219], v[132:135], v[188:191]\n\t" \
  "v_fma_f32 v165, |v165|, v229, v165 mul:4\n\t" \
  "v_fma_f32 v164, |v164|, v228, v164 mul:4\n\t" \
  "ds_read_u16_d16_hi v225, v225 offset:10528\n\t" \
  "v_mfma_f32_16x16x32_bf16 v[196:199], v[216:219], v[136:139], v[196:199]\n\t" \
  "v_fma_f32 v167, |v167|, v231, v167 mul:4\n\t" \
  "v_fma_f32 v166, |v166|, v230, v166 mul:4\n\t" \
  "ds_read_u16 v224, v224 offset:10528\n\t" \
  "v_mfma_f32_16x16x32_bf16 v[204:207], v[216:219], v[140:143], v[204:207]\n\t" \
  "v_cvt_pk_bf16_f32 v164, v164, v165\n\t" \
  "v_cvt_pk_bf16_f32 v165, v166, v167\n\t" \
  "ds_read_b64_tr_b16 v[216:217], v249 offset:6216\n\t" \
  "s_waitcnt lgkmcnt(7)\n\t" \
  "v_mfma_f32_16x16x32_bf16 v[0:3], v[208:211], v[146:149], v[0:3]\n\t" \
  "ds_read_b64_tr_b16 v[218:219], v249 offset:7752\n\t" \
  "v_cvt_pknorm_i16_f32 v228, |v194|, |v195|\n\t" \
  "s_setprio 3\n\t" \
  "v_cvt_pknorm_i16_f32 v230, |v192|, |v193|\n\t" \
  "v_lshlrev_b32_sdwa v229, 2, v228 dst_sel:DWORD dst_unused:UNUSED_PAD src0_sel:DWORD src1_sel:BYTE_3\n\t" \
  "v_lshlrev_b32_sdwa v228, 2, v228 dst_sel:DWORD dst_unused:UNUSED_PAD src0_sel:DWORD src1_sel:BYTE_1\n\t" \
  "v_mfma_f32_16x16x32_bf16 v[24:27], v[208:211], v[154:157], v[24:27]\n\t" \
  "ds_read_u16_d16_hi v227, v227 offset:10528\n\t" \
  "v_lshlrev_b32_sdwa v231, 2, v230 dst_sel:DWORD dst_unused:UNUSED_PAD src0_sel:DWORD src1_sel:BYTE_3\n\t" \
  "v_lshlrev_b32_sdwa v230, 2, v230 dst_sel:DWORD dst_unused:UNUSED_PAD src0_sel:DWORD src1_sel:BYTE_1\n\t" \
  "v_lshlrev_b32_sdwa v233, 1, v203 dst_sel:DWORD dst_unused:UNUSED_PAD src0_sel:DWORD src1_sel:WORD_1\n\t" \
  "v_lshlrev_b32_sdwa v232, 1, v202 dst_sel:DWORD dst_unused:UNUSED_PAD src0_sel:DWORD src1_sel:WORD_1\n\t" \
  "v_mfma_f32_16x16x32_bf16 v[48:51], v[208:211], v[162:165], v[48:51]\n\t" \
  "ds_read_u16 v226, v226 offset:10528\n\t" \
  "ds_read_b32 v229, v229 offset:10016\n\t" \
  "v_lshlrev_b32_sdwa v235, 1, v201 dst_sel:DWORD dst_unused:UNUSED_PAD src0_sel:DWORD src1_sel:WORD_1\n\t" \
  "v_lshlrev_b32_sdwa v234, 1, v200 dst_sel:DWORD dst_unused:UNUSED_PAD src0_sel:DWORD src1_sel:WORD_1\n\t" \
  "s_waitcnt lgkmcnt(8)\n\t" \
  "v_fma_mix_f32 v221, |v179|, v221, v221 op_sel:[0,1,0] op_sel_hi:[0,1,1] clamp\n\t" \
  "v_fma_mix_f32 v220, |v178|, v220, v220 op_sel:[0,1,0] op_sel_hi:[0,1,1] clamp\n\t" \
  "v_mfma_f32_16x16x32_bf16 v[72:75], v[208:211], v[170:173], v[72:75]\n\t" \
  "ds_read_b64_tr_b16 v[208:209], v249 offset:6224\n\t" \
  "ds_read_b32 v228, v228 offset:10016\n\t" \
  "v_fma_mix_f32 v223, |v177|, v223, v223 op_sel:[0,1,0] op_sel_hi:[0,1,1] clamp\n\t" \
  "v_fma_mix_f32 v222, |v176|, v222, v222 op_sel:[0,1,0] op_sel_hi:[0,1,1] clamp\n\t" \
  "s_waitcnt lgkmcnt(3)\n\t" \
  "v_or_b32 v187, v224, v225\n\t" \
  "v_or_b32 v186, v226, v227\n\t" \
  "v_mfma_f32_16x16x32_bf16 v[4:7], v[212:215], v[146:149], v[4:7]\n\t" \
  "ds_read_b64_tr_b16 v[210:211], v249 offset:7760\n\t" \
  "ds_read_b32 v231, v231 offset:10016\n\t" \
  "v_fma_f32 v179, |v179|, v221, v179 mul:4\n\t" \
  "v_fma_f32 v178, |v178|, v220, v178 mul:4\n\t" \
  "v_fma_f32 v177, |v177|, v223, v177 mul:4\n\t" \
  "v_fma_f32 v176, |v176|, v222, v176 mul:4\n\t" \
  "v_mfma_f32_16x16x32_bf16 v[28:31], v[212:215], v[154:157], v[28:31]\n\t" \
  "s_add_u32 s27, s27, s26\n\t" \
  "s_mov_b64 exec, s[30:31]\n\t" \
  "buffer_load_dword v237, v254, s[8:11], s27 offen\n\t" \
  "s_mov_b64 exec, -1\n\t" \
  "v_cvt_pk_bf16_f32 v179, v178, v179\n\t" \
  "v_mfma_f32_16x16x32_bf16 v[52:55], v[212:215], v[162:165], v[52:55]\n\t" \
  "ds_read_b32 v230, v230 offset:10016\n\t" \
  "v_cvt_pk_bf16_f32 v178, v176, v177\n\t" \
  "s_setprio 2\n\t" \
  "v_cvt_pknorm_i16_f32 v220, |v180|, |v181|\n\t" \
  "v_cvt_pknorm_i16_f32 v222, |v182|, |v183|\n\t" \
  "v_lshlrev_b32_sdwa v221, 2, v220 dst_sel:DWORD dst_unused:UNUSED_PAD src0_sel:DWORD src1_sel:BYTE_3\n\t" \
  "v_mfma_f32_16x16x32_bf16 v[76:79], v[212:215], v[170:173], v[76:79]\n\t" \
  "ds_read_b64_tr_b16 v[212:213], v249 offset:6280\n\t" \
  "ds_read_u16_d16_hi v233, v233 offset:10528\n\t" \
  "v_lshlrev_b32_sdwa v220, 2, v220 dst_sel:DWORD dst_unused:UNUSED_PAD src0_sel:DWORD src1_sel:BYTE_1\n\t" \
  "v_lshlrev_b32_sdwa v223, 2, v222 dst_sel:DWORD dst_unused:UNUSED_PAD src0_sel:DWORD src1_sel:BYTE_3\n\t" \
  "v_lshlrev_b32_sdwa v222, 2, v222 dst_sel:DWORD dst_unused:UNUSED_PAD src0_sel:DWORD src1_sel:BYTE_1\n\t" \
  "v_mfma_f32_16x16x32_bf16 v[8:11], v[216:219], v[146:149], v[8:11]\n\t" \
  "ds_read_b64_tr_b16 v[214:215], v249 offset:7816\n\t" \
  "ds_read_u16 v232, v232 offset:10528\n\t" \
  "v_lshlrev_b32_sdwa v225, 1, v189 dst_sel:DWORD dst_unused:UNUSED_PAD src0_sel:DWORD src1_sel:WORD_1\n\t" \
  "v_lshlrev_b32_sdwa v224, 1, v188 dst_sel:DWORD dst_unused:UNUSED_PAD src0_sel:DWORD src1_sel:WORD_1\n\t" \
  "v_lshlrev_b32_sdwa v227, 1, v191 dst_sel:DWORD dst_unused:UNUSED_PAD src0_sel:DWORD src1_sel:WORD_1\n\t" \
  "v_lshlrev_b32_sdwa v226, 1, v190 dst_sel:DWORD dst_unused:UNUSED_PAD src0_sel:DWORD src1_sel:WORD_1\n\t" \
  "s_waitcnt lgkmcnt(4)\n\t" \
  "v_mfma_f32_16x16x32_bf16 v[32:35], v[216:219], v[154:157], v[32:35]\n\t" \
  "ds_read_u16_d16_hi v235, v235 offset:10528\n\t" \
  "ds_read_u16 v234, v234 offset:10528\n\t" \
  "v_fma_mix_f32 v229, |v195|, v229, v229 op_sel:[0,1,0] op_sel_hi:[0,1,1] clamp\n\t" \
  "v_fma_mix_f32 v228, |v194|, v228, v228 op_sel:[0,1,0] op_sel_hi:[0,1,1] clamp\n\t" \
  "v_fma_mix_f32 v231, |v193|, v231, v231 op_sel:[0,1,0] op_sel_hi:[0,1,1] clamp\n\t" \
  "v_mfma_f32_16x16x32_bf16 v[56:59], v[216:219], v[162:165], v[56:59]\n\t" \
  "ds_read_b32 v221, v221 offset:10016\n\t" \
  "ds_read_b32 v220, v220 offset:10016\n\t" \
  "v_fma_mix_f32 v230, |v192|, v230, v230 op_sel:[0,1,0] op_sel_hi:[0,1,1] clamp\n\t" \
  "s_waitcnt lgkmcnt(2)\n\t" \
  "v_or_b32 v203, v232, v233\n\t" \
  "v_or_b32 v202, v234, v235\n\t" \
  "v_fma_f32 v195, |v195|, v229, v195 mul:4\n\t" \
  "v_mfma_f32_16x16x32_bf16 v[80:83], v[216:219], v[170:173], v[80:83]\n\t" \
  "ds_read_b64_tr_b16 v[216:217], v249 offset:6288\n\t" \
  "ds_read_b32 v223, v223 offset:10016\n\t" \
  "v_fma_f32 v194, |v194|, v228, v194 mul:4\n\t" \
  "v_fma_f32 v193, |v193|, v231, v193 mul:4\n\t" \
  "v_fma_f32 v192, |v192|, v230, v192 mul:4\n\t" \
  "v_mfma_f32_16x16x32_bf16 v[12:15], v[208:211], v[146:149], v[12:15]\n\t" \
  "ds_read_b64_tr_b16 v[218:219], v249 offset:7824\n\t" \
  "ds_read_b32 v222, v222 offset:10016\n\t" \
  "v_cvt_pk_bf16_f32 v195, v194, v195\n\t" \
  "v_cvt_pk_bf16_f32 v194, v192, v193\n\t" \
  "v_cvt_pknorm_i16_f32 v228, |v196|, |v197|\n\t" \
  "v_cvt_pknorm_i16_f32 v230, |v198|, |v199|\n\t" \
  "v_mfma_f32_16x16x32_bf16 v[36:39], v[208:211], v[154:157], v[36:39]\n\t" \
  "s_mov_b64 exec, 3\n\t" \
  "ds_add_rtn_u32 v246, v251, v250 offset:47232\n\t" \
  "s_mov_b64 exec, -1\n\t" \
  "v_lshlrev_b32_sdwa v229, 2, v228 dst_sel:DWORD dst_unused:UNUSED_PAD src0_sel:DWORD src1_sel:BYTE_3\n\t" \
  "v_mfma_f32_16x16x32_bf16 v[60:63], v[208:211], v[162:165], v[60:63]\n\t" \
  "ds_read_u16_d16_hi v225, v225 offset:10528\n\t" \
  "v_lshlrev_b32_sdwa v228, 2, v228 dst_sel:DWORD dst_unused:UNUSED_PAD src0_sel:DWORD src1_sel:BYTE_1\n\t" \
  "s_setprio 1\n\t" \
  "v_lshlrev_b32_sdwa v231, 2, v230 dst_sel:DWORD dst_unused:UNUSED_PAD src0_sel:DWORD src1_sel:BYTE_3\n\t" \
  "v_lshlrev_b32_sdwa v230, 2, v230 dst_sel:DWORD dst_unused:UNUSED_PAD src0_sel:DWORD src1_sel:BYTE_1\n\t" \
  "v_mfma_f32_16x16x32_bf16 v[84:87], v[208:211], v[170:173], v[84:87]\n\t" \
  "ds_read_u16 v224, v224 offset:10528\n\t" \
  "ds_read_u16_d16_hi v227, v227 offset:10528\n\t" \
  "v_lshlrev_b32_sdwa v233, 1, v205 dst_sel:DWORD dst_unused:UNUSED_PAD src0_sel:DWORD src1_sel:WORD_1\n\t" \
  "v_lshlrev_b32_sdwa v232, 1, v204 dst_sel:DWORD dst_unused:UNUSED_PAD src0_sel:DWORD src1_sel:WORD_1\n\t" \
  "v_lshlrev_b32_sdwa v235, 1, v207 dst_sel:DWORD dst_unused:UNUSED_PAD src0_sel:DWORD src1_sel:WORD_1\n\t" \
  "s_waitcnt lgkmcnt(3)\n\t" \
  "v_readfirstlane_b32 s4, v246\n\t" \
  "v_mfma_f32_16x16x32_bf16 v[16:19], v[212:215], v[146:149], v[16:19]\n\t" \
  "ds_read_u16 v226, v226 offset:10528\n\t" \
  "ds_read_b32 v229, v229 offset:10016\n\t" \
  "v_lshlrev_b32_sdwa v234, 1, v206 dst_sel:DWORD dst_unused:UNUSED_PAD src0_sel:DWORD src1_sel:WORD_1\n\t" \
  "s_cmpk_lt_u32 s4, 3072\n\t" \
  "s_cbranch_scc1 .Lcnx_w0_13\n.Lcnx_g0_13:\n\t" \
  "v_mfma_f32_16x16x32_bf16 v[40:43], v[212:215], v[154:157], v[40:43]\n\t" \
  "ds_read_b64_tr_b16 v[208:209], v249 offset:12304\n\t" \
  "ds_read_b32 v228, v228 offset:10016\n\t" \
  "v_fma_mix_f32 v221, |v181|, v221, v221 op_sel:[0,1,0] op_sel_hi:[0,1,1] clamp\n\t" \
  "v_fma_mix_f32 v220, |v180|, v220, v220 op_sel:[0,1,0] op_sel_hi:[0,1,1] clamp\n\t" \
  "v_fma_mix_f32 v223, |v183|, v223, v223 op_sel:[0,1,0] op_sel_hi:[0,1,1] clamp\n\t" \
  "v_mfma_f32_16x16x32_bf16 v[64:67], v[212:215], v[162:165], v[64:67]\n\t" \
  "ds_read_b64_tr_b16 v[210:211], v249 offset:13840\n\t" \
  "ds_read_b32 v231, v231 offset:10016\n\t" \
  "v_fma_mix_f32 v222, |v182|, v222, v222 op_sel:[0,1,0] op_sel_hi:[0,1,1] clamp\n\t" \
  "s_setprio 0\n\t" \
  "s_waitcnt lgkmcnt(5)\n\t" \
  "v_or_b32 v188, v224, v225\n\t" \
  "v_or_b32 v189, v226, v227\n\t" \
  "v_fma_f32 v181, |v181|, v221, v181 mul:4\n\t" \
  "v_mfma_f32_16x16x32_bf16 v[88:91], v[212:215], v[170:173], v[88:91]\n\t" \
  "ds_read_b64_tr_b16 v[212:213], v249 offset:12312\n\t" \
  "ds_read_b32 v230, v230 offset:10016\n\t" \
  "v_fma_f32 v180, |v180|, v220, v180 mul:4\n\t" \
  "v_fma_f32 v183, |v183|, v223, v183 mul:4\n\t" \
  "v_fma_f32 v182, |v182|, v222, v182 mul:4\n\t" \
  "v_mfma_f32_16x16x32_bf16 v[20:23], v[216:219], v[146:149], v[20:23]\n\t" \
  "ds_read_b64_tr_b16 v[214:215], v249 offset:13848\n\t" \
  "v_cvt_pk_bf16_f32 v180, v180, v181\n\t" \
  "v_cvt_pk_bf16_f32 v181, v182, v183\n\t" \
  "s_waitcnt lgkmcnt(1)\n\t" \
  "v_fma_mix_f32 v229, |v197|, v229, v229 op_sel:[0,1,0] op_sel_hi:[0,1,1] clamp\n\t" \
  "v_fma_mix_f32 v228, |v196|, v228, v228 op_sel:[0,1,0] op_sel_hi:[0,1,1] clamp\n\t" \
  "v_mfma_f32_16x16x32_bf16 v[44:47], v[216:219], v[154:157], v[44:47]\n\t" \
  "ds_read_u16_d16_hi v233, v233 offset:10528\n\t" \
  "ds_read_u16 v232, v232 offset:10528\n\t" \
  "ds_read_u16_d16_hi v235, v235 offset:10528\n\t" \
  "ds_read_u16 v234, v234 offset:10528\n\t" \
  "v_fma_mix_f32 v231, |v199|, v231, v231 op_sel:[0,1,0] op_sel_hi:[0,1,1] clamp\n\t" \
  "v_fma_mix_f32 v230, |v198|, v230, v230 op_sel:[0,1,0] op_sel_hi:[0,1,1] clamp\n\t" \
  "s_waitcnt lgkmcnt(0)\n\t" \
  "v_or_b32 v204, v232, v233\n\t" \
  "v_mfma_f32_16x16x32_bf16 v[68:71], v[216:219], v[162:165], v[68:71]\n\t" \
  "v_or_b32 v205, v234, v235\n\t" \
  "v_fma_f32 v197, |v197|, v229, v197 mul:4\n\t" \
  "v_fma_f32 v196, |v196|, v228, v196 mul:4\n\t" \
  "v_fma_f32 v199, |v199|, v231, v199 mul:4\n\t" \
  "v_mfma_f32_16x16x32_bf16 v[92:95], v[216:219], v[170:173], v[92:95]\n\t" \
  "ds_read_b64_tr_b16 v[216:217], v249 offset:12368\n\t" \
  "v_fma_f32 v198, |v198|, v230, v198 mul:4\n\t" \
  "v_cvt_pk_bf16_f32 v196, v196, v197\n\t" \
  "v_cvt_pk_bf16_f32 v197, v198, v199\n\t" \
  "v_mfma_f32_32x32x16_bf16 v[144:159], v[242:245], v[240:243], 0\n\t" \
  "v_mfma_f32_32x32x16_bf16 v[160:175], v[242:245], v[240:243], 0\n\t" \
  "v_mfma_f32_16x16x32_bf16 v[0:3], v[208:211], v[178:181], v[0:3]\n\t" \
  "ds_read_b64_tr_b16 v[218:219], v249 offset:13904\n\t" \
  "v_add_u32 v232, s56, v247\n\t" \
  "v_mfma_f32_16x16x32_bf16 v[24:27], v[208:211], v[186:189], v[24:27]\n\t" \
  "ds_read2_b32 v[98:99], v247 offset0:0 offset1:6\n\t" \
  "v_mfma_f32_16x16x32_bf16 v[48:51], v[208:211], v[194:197], v[48:51]\n\t" \
  "ds_read2_b32 v[114:115], v247 offset0:192 offset1:198\n\t" \
  "v_mfma_f32_16x16x32_bf16 v[72:75], v[208:211], v[202:205], v[72:75]\n\t" \
  "ds_read_b64_tr_b16 v[208:209], v249 offset:12376\n\t" \
  "ds_read2_b32 v[130:131], v232 offset0:0 offset1:6\n\t" \
  "v_mfma_f32_16x16x32_bf16 v[4:7], v[212:215], v[178:181], v[4:7]\n\t" \
  "ds_read_b64_tr_b16 v[210:211], v249 offset:13912\n\t" \
  "ds_read2_b32 v[102:103], v247 offset0:12 offset1:18\n\t" \
  "v_mfma_f32_16x16x32_bf16 v[28:31], v[212:215], v[186:189], v[28:31]\n\t" \
  "ds_read2_b32 v[118:119], v247 offset0:204 offset1:210\n\t" \
  "ds_read2_b32 v[134:135], v232 offset0:12 offset1:18\n\t" \
  "s_add_u32 s27, s27, s26\n\t" \
  "s_mov_b64 exec, s[30:31]\n\t" \
  "buffer_load_dword v237, v254, s[8:11], s27 offen\n\t" \
  "s_mov_b64 exec, -1\n\t" \
  "v_bfe_u32 v234, v251, 3, 2\n\t" \
  "v_mul_u32_u24 v234, 0x5555, v234\n\t" \
  "v_mfma_f32_16x16x32_bf16 v[52:55], v[212:215], v[194:197], v[52:55]\n\t" \
  "ds_read2_b32 v[106:107], v247 offset0:24 offset1:30\n\t" \
  "ds_read2_b32 v[122:123], v247 offset0:216 offset1:222\n\t" \
  "v_mfma_f32_16x16x32_bf16 v[76:79], v[212:215], v[202:205], v[76:79]\n\t" \
  "ds_read_b64_tr_b16 v[212:213], v249 offset:12432\n\t" \
  "ds_read2_b32 v[138:139], v232 offset0:24 offset1:30\n\t" \
  "s_waitcnt lgkmcnt(12)\n\t" \
  "v_mfma_f32_16x16x32_bf16 v[8:11], v[216:219], v[178:181], v[8:11]\n\t" \
  "ds_read_b64_tr_b16 v[214:215], v249 offset:13968\n\t" \
  "v_mfma_f32_16x16x32_bf16 v[32:35], v[216:219], v[186:189], v[32:35]\n\t" \
  "ds_read2_b32 v[110:111], v247 offset0:36 offset1:42\n\t" \
  "v_mfma_f32_16x16x32_bf16 v[56:59], v[216:219], v[194:197], v[56:59]\n\t" \
  "ds_read2_b32 v[126:127], v247 offset0:228 offset1:234\n\t" \
  "v_mfma_f32_16x16x32_bf16 v[80:83], v[216:219], v[202:205], v[80:83]\n\t" \
  "ds_read_b64_tr_b16 v[216:217], v249 offset:12440\n\t" \
  "v_cvt_pk_bf16_f32 v48, v48, v49\n\t" \
  "v_cvt_pk_bf16_f32 v49, v50, v51\n\t" \
  "v_cvt_pk_bf16_f32 v50, v52, v53\n\t" \
  "v_cvt_pk_bf16_f32 v51, v54, v55\n\t" \
  "v_cvt_pk_bf16_f32 v52, v0, v1\n\t" \
  "v_cvt_pk_bf16_f32 v53, v2, v3\n\t" \
  "v_cvt_pk_bf16_f32 v54, v4, v5\n\t" \
  "v_cvt_pk_bf16_f32 v55, v6, v7\n\t" \
  "v_cvt_pk_bf16_f32 v72, v72, v73\n\t" \
  "v_cvt_pk_bf16_f32 v73, v74, v75\n\t" \
  "v_cvt_pk_bf16_f32 v74, v76, v77\n\t" \
  "v_cvt_pk_bf16_f32 v75, v78, v79\n\t" \
  "v_cvt_pk_bf16_f32 v76, v24, v25\n\t" \
  "v_cvt_pk_bf16_f32 v77, v26, v27\n\t" \
  "v_cvt_pk_bf16_f32 v78, v28, v29\n\t" \
  "v_cvt_pk_bf16_f32 v79, v30, v31\n\t" \
  "buffer_store_dwordx4 v[72:75], v250, s[20:23], s13 offen offset:0 sc1\n\t" \
  "s_waitcnt lgkmcnt(11)\n\t" \
  "v_mfma_f32_16x16x32_bf16 v[12:15], v[208:211], v[178:181], v[12:15]\n\t" \
  "ds_read_b64_tr_b16 v[218:219], v249 offset:13976\n\t" \
  "v_mfma_f32_16x16x32_bf16 v[36:39], v[208:211], v[186:189], v[36:39]\n\t" \
  "ds_read2_b32 v[142:143], v232 offset0:36 offset1:42\n\t" \
  "v_mfma_f32_16x16x32_bf16 v[60:63], v[208:211], v[194:197], v[60:63]\n\t" \
  "v_mfma_f32_16x16x32_bf16 v[84:87], v[208:211], v[202:205], v[84:87]\n\t" \
  "s_waitcnt lgkmcnt(5)\n\t" \
  "v_mfma_f32_16x16x32_bf16 v[16:19], v[212:215], v[178:181], v[16:19]\n\t" \
  "v_mfma_f32_16x16x32_bf16 v[40:43], v[212:215], v[186:189], v[40:43]\n\t" \
  "v_mfma_f32_16x16x32_bf16 v[64:67], v[212:215], v[194:197], v[64:67]\n\t" \
  "v_mfma_f32_16x16x32_bf16 v[88:91], v[212:215], v[202:205], v[88:91]\n\t" \
  "v_cvt_pk_bf16_f32 v56, v56, v57\n\t" \
  "v_cvt_pk_bf16_f32 v57, v58, v59\n\t" \
  "v_cvt_pk_bf16_f32 v58, v60, v61\n\t" \
  "v_cvt_pk_bf16_f32 v59, v62, v63\n\t" \
  "v_cvt_pk_bf16_f32 v60, v8, v9\n\t" \
  "v_cvt_pk_bf16_f32 v61, v10, v11\n\t" \
  "v_cvt_pk_bf16_f32 v62, v12, v13\n\t" \
  "v_cvt_pk_bf16_f32 v63, v14, v15\n\t" \
  "v_cvt_pk_bf16_f32 v80, v80, v81\n\t" \
  "v_cvt_pk_bf16_f32 v81, v82, v83\n\t" \
  "v_cvt_pk_bf16_f32 v82, v84, v85\n\t" \
  "v_cvt_pk_bf16_f32 v83, v86, v87\n\t" \
  "v_cvt_pk_bf16_f32 v84, v32, v33\n\t" \
  "v_cvt_pk_bf16_f32 v85, v34, v35\n\t" \
  "v_cvt_pk_bf16_f32 v86, v36, v37\n\t" \
  "v_cvt_pk_bf16_f32 v87, v38, v39\n\t" \
  "buffer_store_dwordx4 v[80:83], v250, s[20:23], s13 offen offset:64 sc1\n\t" \
  "s_waitcnt lgkmcnt(1)\n\t" \
  "v_mfma_f32_16x16x32_bf16 v[20:23], v[216:219], v[178:181], v[20:23]\n\t" \
  "v_mfma_f32_16x16x32_bf16 v[44:47], v[216:219], v[186:189], v[44:47]\n\t" \
  "v_mfma_f32_16x16x32_bf16 v[68:71], v[216:219], v[194:197], v[68:71]\n\t" \
  "v_mfma_f32_16x16x32_bf16 v[92:95], v[216:219], v[202:205], v[92:95]\n\t" \
  "v_cvt_pk_bf16_f32 v64, v64, v65\n\t" \
  "v_cvt_pk_bf16_f32 v65, v66, v67\n\t" \
  "s_nop 4\n\t" \
  "v_cvt_pk_bf16_f32 v66, v68, v69\n\t" \
  "v_cvt_pk_bf16_f32 v67, v70, v71\n\t" \
  "v_cvt_pk_bf16_f32 v68, v16, v17\n\t" \
  "v_cvt_pk_bf16_f32 v69, v18, v19\n\t" \
  "v_cvt_pk_bf16_f32 v70, v20, v21\n\t" \
  "v_cvt_pk_bf16_f32 v71, v22, v23\n\t" \
  "v_cvt_pk_bf16_f32 v88, v88, v89\n\t" \
  "v_cvt_pk_bf16_f32 v89, v90, v91\n\t" \
  "v_cvt_pk_bf16_f32 v90, v92, v93\n\t" \
  "v_cvt_pk_bf16_f32 v91, v94, v95\n\t" \
  "v_cvt_pk_bf16_f32 v92, v40, v41\n\t" \
  "v_cvt_pk_bf16_f32 v93, v42, v43\n\t" \
  "v_cvt_pk_bf16_f32 v94, v44, v45\n\t" \
  "v_cvt_pk_bf16_f32 v95, v46, v47\n\t" \
  "buffer_store_dwordx4 v[88:91], v250, s[20:23], s13 offen offset:128 sc1\n\t" \
  "s_add_u32 s63, s28, s65\n\t" \
  "s_add_u32 s62, s29, s65\n\t" \
  "s_add_u32 s14, s12, s65\n\t" \
  "s_add_u32 s15, s13, s65\n\t" \
  "v_lshlrev_b32 v198, 1, v251\n\t" \
  "global_load_dwordx4 v[192:195], v198, s[66:67]\n\t" \
  "s_branch .Lcnx_mid\n\t" \
  ".Lcnx_w0_2:\n\t" \
  "ds_read_b32 v246, v251 offset:47144\n\t" \
  "s_waitcnt lgkmcnt(0)\n\t" \
  "v_readfirstlane_b32 s4, v246\n\t" \
  "s_cmp_lt_u32 s4, 3072\n\t" \
  "s_cbranch_scc1 .Lcnx_w0_2\n\t" \
  "s_branch .Lcnx_g0_2\n\t" \
  ".Lcnx_w0_3:\n\t" \
  "ds_read_b32 v246, v251 offset:47152\n\t" \
  "s_waitcnt lgkmcnt(0)\n\t" \
  "v_readfirstlane_b32 s4, v246\n\t" \
  "s_cmp_lt_u32 s4, 3072\n\t" \
  "s_cbranch_scc1 .Lcnx_w0_3\n\t" \
  "s_branch .Lcnx_g0_3\n\t" \
  ".Lcnx_w0_4:\n\t" \
  "ds_read_b32 v246, v251 offset:47160\n\t" \
  "s_waitcnt lgkmcnt(0)\n\t" \
  "v_readfirstlane_b32 s4, v246\n\t" \
  "s_cmp_lt_u32 s4, 3072\n\t" \
  "s_cbranch_scc1 .Lcnx_w0_4\n\t" \
  "s_branch .Lcnx_g0_4\n\t" \
  ".Lcnx_w0_5:\n\t" \
  "ds_read_b32 v246, v251 offset:47168\n\t" \
  "s_waitcnt lgkmcnt(0)\n\t" \
  "v_readfirstlane_b32 s4, v246\n\t" \
  "s_cmp_lt_u32 s4, 3072\n\t" \
  "s_cbranch_scc1 .Lcnx_w0_5\n\t" \
  "s_branch .Lcnx_g0_5\n\t" \
  ".Lcnx_w0_6:\n\t" \
  "ds_read_b32 v246, v251 offset:47176\n\t" \
  "s_waitcnt lgkmcnt(0)\n\t" \
  "v_readfirstlane_b32 s4, v246\n\t" \
  "s_cmp_lt_u32 s4, 3072\n\t" \
  "s_cbranch_scc1 .Lcnx_w0_6\n\t" \
  "s_branch .Lcnx_g0_6\n\t" \
  ".Lcnx_w0_7:\n\t" \
  "ds_read_b32 v246, v251 offset:47184\n\t" \
  "s_waitcnt lgkmcnt(0)\n\t" \
  "v_readfirstlane_b32 s4, v246\n\t" \
  "s_cmp_lt_u32 s4, 3072\n\t" \
  "s_cbranch_scc1 .Lcnx_w0_7\n\t" \
  "s_branch .Lcnx_g0_7\n\t" \
  ".Lcnx_w0_8:\n\t" \
  "ds_read_b32 v246, v251 offset:47192\n\t" \
  "s_waitcnt lgkmcnt(0)\n\t" \
  "v_readfirstlane_b32 s4, v246\n\t" \
  "s_cmp_lt_u32 s4, 3072\n\t" \
  "s_cbranch_scc1 .Lcnx_w0_8\n\t" \
  "s_branch .Lcnx_g0_8\n\t" \
  ".Lcnx_w0_9:\n\t" \
  "ds_read_b32 v246, v251 offset:47200\n\t" \
  "s_waitcnt lgkmcnt(0)\n\t" \
  "v_readfirstlane_b32 s4, v246\n\t" \
  "s_cmp_lt_u32 s4, 3072\n\t" \
  "s_cbranch_scc1 .Lcnx_w0_9\n\t" \
  "s_branch .Lcnx_g0_9\n\t" \
  ".Lcnx_w0_10:\n\t" \
  "ds_read_b32 v246, v251 offset:47208\n\t" \
  "s_waitcnt lgkmcnt(0)\n\t" \
  "v_readfirstlane_b32 s4, v246\n\t" \
  "s_cmp_lt_u32 s4, 3072\n\t" \
  "s_cbranch_scc1 .Lcnx_w0_10\n\t" \
  "s_branch .Lcnx_g0_10\n\t" \
  ".Lcnx_t0:\n\t" \
  "v_add_u32 v246, -116544, v251\n\t" \
  "v_lshrrev_b32 v246, 3, v246\n\t" \
  "v_mul_i32_i24 v246, s26, v246\n\t" \
  "v_add_u32 v246, s28, v246\n\t" \
  "s_mov_b32 m0, 107744\n\t" \
  "s_mov_b64 exec, 0xffff\n\t" \
  "buffer_load_dword v246, s[20:23], 0 offen lds\n\t" \
  "s_mov_b64 exec, -1\n\t" \
  "s_branch .Lcnx_u0\n\t" \
  ".Lcnx_w0_11:\n\t" \
  "ds_read_b32 v246, v251 offset:47216\n\t" \
  "s_waitcnt lgkmcnt(0)\n\t" \
  "v_readfirstlane_b32 s4, v246\n\t" \
  "s_cmp_lt_u32 s4, 3072\n\t" \
  "s_cbranch_scc1 .Lcnx_w0_11\n\t" \
  "s_branch .Lcnx_g0_11\n\t" \
  ".Lcnx_w0_12:\n\t" \
  "ds_read_b32 v246, v251 offset:47224\n\t" \
  "s_waitcnt lgkmcnt(0)\n\t" \
  "v_readfirstlane_b32 s4, v246\n\t" \
  "s_cmp_lt_u32 s4, 3072\n\t" \
  "s_cbranch_scc1 .Lcnx_w0_12\n\t" \
  "s_branch .Lcnx_g0_12\n\t" \
  ".Lcnx_w0_13:\n\t" \
  "ds_read_b32 v246, v251 offset:47232\n\t" \
  "s_waitcnt lgkmcnt(0)\n\t" \
  "v_readfirstlane_b32 s4, v246\n\t" \
  "s_cmp_lt_u32 s4, 3072\n\t" \
  "s_cbranch_scc1 .Lcnx_w0_13\n\t" \
  "s_branch .Lcnx_g0_13\n\t" \
  ".Lcnx_w1_2:\n\t" \
  "ds_read_b32 v246, v251 offset:47144\n\t" \
  "s_waitcnt lgkmcnt(0)\n\t" \
  "v_readfirstlane_b32 s4, v246\n\t" \
  "s_cmp_lt_u32 s4, 6144\n\t" \
  "s_cbranch_scc1 .Lcnx_w1_2\n\t" \
  "s_branch .Lcnx_g1_2\n\t" \
  ".Lcnx_w1_3:\n\t" \
  "ds_read_b32 v246, v251 offset:47152\n\t" \
  "s_waitcnt lgkmcnt(0)\n\t" \
  "v_readfirstlane_b32 s4, v246\n\t" \
  "s_cmp_lt_u32 s4, 6144\n\t" \
  "s_cbranch_scc1 .Lcnx_w1_3\n\t" \
  "s_branch .Lcnx_g1_3\n\t" \
  ".Lcnx_w1_4:\n\t" \
  "ds_read_b32 v246, v251 offset:47160\n\t" \
  "s_waitcnt lgkmcnt(0)\n\t" \
  "v_readfirstlane_b32 s4, v246\n\t" \
  "s_cmp_lt_u32 s4, 6144\n\t" \
  "s_cbranch_scc1 .Lcnx_w1_4\n\t" \
  "s_branch .Lcnx_g1_4\n\t" \
  ".Lcnx_w1_5:\n\t" \
  "ds_read_b32 v246, v251 offset:47168\n\t" \
  "s_waitcnt lgkmcnt(0)\n\t" \
  "v_readfirstlane_b32 s4, v246\n\t" \
  "s_cmp_lt_u32 s4, 6144\n\t" \
  "s_cbranch_scc1 .Lcnx_w1_5\n\t" \
  "s_branch .Lcnx_g1_5\n\t" \
  ".Lcnx_w1_6:\n\t" \
  "ds_read_b32 v246, v251 offset:47176\n\t" \
  "s_waitcnt lgkmcnt(0)\n\t" \
  "v_readfirstlane_b32 s4, v246\n\t" \
  "s_cmp_lt_u32 s4, 6144\n\t" \
  "s_cbranch_scc1 .Lcnx_w1_6\n\t" \
  "s_branch .Lcnx_g1_6\n\t" \
  ".Lcnx_w1_7:\n\t" \
  "ds_read_b32 v246, v251 offset:47184\n\t" \
  "s_waitcnt lgkmcnt(0)\n\t" \
  "v_readfirstlane_b32 s4, v246\n\t" \
  "s_cmp_lt_u32 s4, 6144\n\t" \
  "s_cbranch_scc1 .Lcnx_w1_7\n\t" \
  "s_branch .Lcnx_g1_7\n\t" \
  ".Lcnx_w1_8:\n\t" \
  "ds_read_b32 v246, v251 offset:47192\n\t" \
  "s_waitcnt lgkmcnt(0)\n\t" \
  "v_readfirstlane_b32 s4, v246\n\t" \
  "s_cmp_lt_u32 s4, 6144\n\t" \
  "s_cbranch_scc1 .Lcnx_w1_8\n\t" \
  "s_branch .Lcnx_g1_8\n\t" \
  ".Lcnx_w1_9:\n\t" \
  "ds_read_b32 v246, v251 offset:47200\n\t" \
  "s_waitcnt lgkmcnt(0)\n\t" \
  "v_readfirstlane_b32 s4, v246\n\t" \
  "s_cmp_lt_u32 s4, 6144\n\t" \
  "s_cbranch_scc1 .Lcnx_w1_9\n\t" \
  "s_branch .Lcnx_g1_9\n\t" \
  ".Lcnx_mid:\n\t" \
  "v_add_u32 v232, 56880, v253\n\t" \
  "v_add_u32 v233, s39, v251\n\t" \
  "v_mfma_f32_32x32x16_bf16 v[176:191], v[242:245], v[240:243], 0\n\t" \
  "ds_read_b64 v[200:201], v253 offset:0\n\t" \
  "ds_read_b64 v[202:203], v253 offset:400\n\t" \
  "ds_read_b64 v[204:205], v253 offset:800\n\t" \
  "ds_read_b64 v[206:207], v253 offset:1200\n\t" \
  "ds_read_b64 v[208:209], v253 offset:8\n\t" \
  "ds_read_b64 v[210:211], v253 offset:408\n\t" \
  "ds_read_b64 v[212:213], v253 offset:808\n\t" \
  "ds_read_b64 v[214:215], v253 offset:1208\n\t" \
  "ds_read_b64 v[216:217], v253 offset:16\n\t" \
  "ds_read_b64 v[218:219], v253 offset:416\n\t" \
  "ds_read_b64 v[220:221], v253 offset:816\n\t" \
  "ds_read_b64 v[222:223], v253 offset:1216\n\t" \
  "s_waitcnt lgkmcnt(15)\n\t" \
  "v_and_b32 v96, 0xffff, v98\n\t" \
  "v_and_b32 v97, 0xffff0000, v98\n\t" \
  "v_and_b32 v98, 0xffff, v99\n\t" \
  "v_and_b32 v99, 0xffff0000, v99\n\t" \
  "v_and_b32 v112, 0xffff, v114\n\t" \
  "v_and_b32 v113, 0xffff0000, v114\n\t" \
  "v_and_b32 v114, 0xffff, v115\n\t" \
  "v_and_b32 v115, 0xffff0000, v115\n\t" \
  "v_and_b32 v128, 0xffff, v130\n\t" \
  "v_and_b32 v129, 0xffff0000, v130\n\t" \
  "v_and_b32 v130, 0xffff, v131\n\t" \
  "v_and_b32 v131, 0xffff0000, v131\n\t" \
  "s_waitcnt lgkmcnt(8)\n\t" \
  "v_smfmac_f32_16x16x64_bf16 v[144:147], v[96:99], v[200:207], v234\n\t" \
  "v_and_b32 v100, 0xffff, v102\n\t" \
  "v_and_b32 v101, 0xffff0000, v102\n\t" \
  "v_and_b32 v102, 0xffff, v103\n\t" \
  "v_and_b32 v103, 0xffff0000, v103\n\t" \
  "s_waitcnt vmcnt(12) lgkmcnt(0)\n\t" \
  "s_barrier\n\t" \
  "ds_read_b64 v[224:225], v253 offset:16400\n\t" \
  "ds_read_b64 v[226:227], v253 offset:16800\n\t" \
  "ds_read_b64 v[228:229], v253 offset:17200\n\t" \
  "ds_read_b64 v[230:231], v253 offset:17600\n\t" \
  "s_add_u32 m0, s25, 81480\n\t" \
  "s_add_u32 s24, s24, s26\n\t" \
  "v_smfmac_f32_16x16x64_bf16 v[148:151], v[112:115], v[208:215], v234\n\t" \
  "buffer_load_dwordx4 v254, s[8:11], s24 offen lds\n\t" \
  "ds_read_b64 v[200:201], v253 offset:16408\n\t" \
  "ds_read_b64 v[202:203], v253 offset:16808\n\t" \
  "ds_read_b64 v[204:205], v253 offset:17208\n\t" \
  "ds_read_b64 v[206:207], v253 offset:17608\n\t" \
  "v_and_b32 v116, 0xffff, v118\n\t" \
  "v_and_b32 v117, 0xffff0000, v118\n\t" \
  "v_and_b32 v118, 0xffff, v119\n\t" \
  "v_and_b32 v119, 0xffff0000, v119\n\t" \
  "s_add_u32 m0, s25, 89680\n\t" \
  "s_add_u32 s24, s24, s26\n\t" \
  "v_smfmac_f32_16x16x64_bf16 v[152:155], v[128:131], v[216:223], v234\n\t" \
  "buffer_load_dwordx4 v254, s[8:11], s24 offen lds\n\t" \
  "ds_read_b64 v[208:209], v253 offset:16416\n\t" \
  "ds_read_b64 v[210:211], v253 offset:16816\n\t" \
  "ds_read_b64 v[212:213], v253 offset:17216\n\t" \
  "ds_read_b64 v[214:215], v253 offset:17616\n\t" \
  "v_and_b32 v132, 0xffff, v134\n\t" \
  "v_and_b32 v133, 0xffff0000, v134\n\t" \
  "v_and_b32 v134, 0xffff, v135\n\t" \
  "v_and_b32 v135, 0xffff0000, v135\n\t" \
  "v_lshlrev_b32 v235, 1, v251\n\t" \
  "v_and_b32 v236, 0x70, v235\n\t" \
  "v_bfe_u32 v235, v250, 6, 1\n\t" \
  "s_waitcnt lgkmcnt(8)\n\t" \
  "s_add_u32 m0, s25, 97880\n\t" \
  "s_add_u32 s24, s24, s26\n\t" \
  "v_smfmac_f32_16x16x64_bf16 v[156:159], v[96:99], v[224:231], v234\n\t" \
  "buffer_load_dwordx4 v254, s[8:11], s24 offen lds\n\t" \
  "v_and_b32 v104, 0xffff, v106\n\t" \
  "v_and_b32 v105, 0xffff0000, v106\n\t" \
  "v_and_b32 v106, 0xffff, v107\n\t" \
  "v_and_b32 v107, 0xffff0000, v107\n\t" \
  "v_lshl_add_u32 v235, v235, 4, v250\n\t" \
  "v_add3_u32 v235, v235, v236, s61\n\t" \
  "v_smfmac_f32_16x16x64_bf16 v[144:147], v[100:103], v[224:231], v234\n\t" \
  "s_waitcnt vmcnt(12) lgkmcnt(0)\n\t" \
  "s_barrier\n\t" \
  "ds_read_b64 v[216:217], v253 offset:32800\n\t" \
  "ds_read_b64 v[218:219], v253 offset:33200\n\t" \
  "ds_read_b64 v[220:221], v253 offset:33600\n\t" \
  "ds_read_b64 v[222:223], v253 offset:34000\n\t" \
  "s_add_u32 m0, s25, 0\n\t" \
  "s_add_u32 s24, s24, s26\n\t" \
  "v_smfmac_f32_16x16x64_bf16 v[160:163], v[112:115], v[200:207], v234\n\t" \
  "buffer_load_dwordx4 v254, s[8:11], s24 offen lds\n\t" \
  "ds_read_b64 v[224:225], v253 offset:32808\n\t" \
  "ds_read_b64 v[226:227], v253 offset:33208\n\t" \
  "v_and_b32 v120, 0xffff, v122\n\t" \
  "v_and_b32 v121, 0xffff0000, v122\n\t" \
  "v_and_b32 v122, 0xffff, v123\n\t" \
  "v_and_b32 v123, 0xffff0000, v123\n\t" \
  "v_smfmac_f32_16x16x64_bf16 v[148:151], v[116:119], v[200:207], v234\n\t" \
  "ds_read_b64 v[228:229], v253 offset:33608\n\t" \
  "ds_read_b64 v[230:231], v253 offset:34008\n\t" \
  "ds_read_b64 v[36:37], v235 offset:0\n\t" \
  "ds_read_b64 v[38:39], v235 offset:8\n\t" \
  "s_add_u32 m0, s25, 8200\n\t" \
  "s_add_u32 s24, s24, s26\n\t" \
  "v_smfmac_f32_16x16x64_bf16 v[164:167], v[128:131], v[208:215], v234\n\t" \
  "buffer_load_dwordx4 v254, s[8:11], s24 offen lds\n\t" \
  "ds_read_b64 v[200:201], v253 offset:32816\n\t" \
  "ds_read_b64 v[202:203], v253 offset:33216\n\t" \
  "ds_read_b64 v[40:41], v235 offset:64\n\t" \
  "v_and_b32 v136, 0xffff, v138\n\t" \
  "v_and_b32 v137, 0xffff0000, v138\n\t" \
  "v_and_b32 v138, 0xffff, v139\n\t" \
  "v_and_b32 v139, 0xffff0000, v139\n\t" \
  "v_mfma_f32_32x32x16_bf16 v[0:15], v[242:245], v[240:243], 0\n\t" \
  "v_smfmac_f32_16x16x64_bf16 v[152:155], v[132:135], v[208:215], v234\n\t" \
  "ds_read_b64 v[204:205], v253 offset:33616\n\t" \
  "ds_read_b64 v[42:43], v235 offset:72\n\t" \
  "ds_read_b64 v[44:45], v235 offset:128\n\t" \
  "s_waitcnt lgkmcnt(12)\n\t" \
  "v_smfmac_f32_16x16x64_bf16 v[168:171], v[96:99], v[216:223], v234\n\t" \
  "ds_read_b64 v[206:207], v253 offset:34016\n\t" \
  "ds_read_b64 v[46:47], v235 offset:136\n\t" \
  "v_and_b32 v108, 0xffff, v110\n\t" \
  "v_and_b32 v109, 0xffff0000, v110\n\t" \
  "v_and_b32 v110, 0xffff, v111\n\t" \
  "v_and_b32 v111, 0xffff0000, v111\n\t" \
  "v_add_u32 v235, s58, v235\n\t" \
  "s_add_u32 m0, s25, 16400\n\t" \
  "s_add_u32 s24, s24, s26\n\t" \
  "v_smfmac_f32_16x16x64_bf16 v[156:159], v[100:103], v[216:223], v234\n\t" \
  "buffer_load_dwordx4 v254, s[8:11], s24 offen lds\n\t" \
  "v_smfmac_f32_16x16x64_bf16 v[144:147], v[104:107], v[216:223], v234\n\t" \
  "s_waitcnt vmcnt(4) lgkmcnt(0)\n\t" \
  "s_barrier\n\t" \
  "v_smfmac_f32_16x16x64_bf16 v[172:175], v[112:115], v[224:231], v234\n\t" \
  "ds_read_b64 v[208:209], v232 offset:0\n\t" \
  "ds_read_b64 v[210:211], v232 offset:400\n\t" \
  "v_and_b32 v124, 0xffff, v126\n\t" \
  "v_and_b32 v125, 0xffff0000, v126\n\t" \
  "v_and_b32 v126, 0xffff, v127\n\t" \
  "v_and_b32 v127, 0xffff0000, v127\n\t" \
  "s_add_u32 m0, s25, 24600\n\t" \
  "s_add_u32 s24, s24, s26\n\t" \
  "v_smfmac_f32_16x16x64_bf16 v[160:163], v[116:119], v[224:231], v234\n\t" \
  "buffer_load_dwordx4 v254, s[8:11], s24 offen lds\n\t" \
  "ds_read_b64 v[212:213], v232 offset:800\n\t" \
  "ds_read_b64 v[214:215], v232 offset:1200\n\t" \
  "v_smfmac_f32_16x16x64_bf16 v[148:151], v[120:123], v[224:231], v234\n\t" \
  "ds_read_b64 v[216:217], v232 offset:8\n\t" \
  "ds_read_b64 v[218:219], v232 offset:408\n\t" \
  "v_smfmac_f32_16x16x64_bf16 v[176:179], v[128:131], v[200:207], v234\n\t" \
  "ds_read_b64 v[220:221], v232 offset:808\n\t" \
  "ds_read_b64 v[222:223], v232 offset:1208\n\t" \
  "v_and_b32 v140, 0xffff, v142\n\t" \
  "v_and_b32 v141, 0xffff0000, v142\n\t" \
  "v_and_b32 v142, 0xffff, v143\n\t" \
  "v_and_b32 v143, 0xffff0000, v143\n\t" \
  "v_smfmac_f32_16x16x64_bf16 v[164:167], v[132:135], v[200:207], v234\n\t" \
  "ds_read_b64 v[224:225], v232 offset:16\n\t" \
  "ds_read_b64 v[72:73], v235 offset:0\n\t" \
  "s_add_u32 m0, s25, 32800\n\t" \
  "s_add_u32 s24, s24, s26\n\t" \
  "v_smfmac_f32_16x16x64_bf16 v[152:155], v[136:139], v[200:207], v234\n\t" \
  "buffer_load_dwordx4 v254, s[8:11], s24 offen lds\n\t" \
  "ds_read_b64 v[226:227], v232 offset:416\n\t" \
  "ds_read_b64 v[74:75], v235 offset:8\n\t" \
  "s_waitcnt lgkmcnt(8)\n\t" \
  "v_smfmac_f32_16x16x64_bf16 v[180:183], v[96:99], v[208:215], v234\n\t" \
  "ds_read_b64 v[228:229], v232 offset:816\n\t" \
  "ds_read_b64 v[80:81], v235 offset:64\n\t" \
  "v_smfmac_f32_16x16x64_bf16 v[168:171], v[100:103], v[208:215], v234\n\t" \
  "ds_read_b64 v[230:231], v232 offset:1216\n\t" \
  "ds_read_b64 v[82:83], v235 offset:72\n\t" \
  "v_mfma_f32_32x32x16_bf16 v[16:31], v[242:245], v[240:243], 0\n\t" \
  "v_mov_b32 v196, 0x3f003f00\n\t" \
  "v_mov_b32 v197, 0x3f003f00\n\t" \
  "v_smfmac_f32_16x16x64_bf16 v[156:159], v[104:107], v[208:215], v234\n\t" \
  "ds_read_b64 v[88:89], v235 offset:128\n\t" \
  "v_mov_b32 v198, 0x3f003f00\n\t" \
  "v_mov_b32 v199, 0x3f003f00\n\t" \
  "s_add_u32 m0, s25, 41000\n\t" \
  "s_add_u32 s24, s24, s26\n\t" \
  "v_smfmac_f32_16x16x64_bf16 v[144:147], v[108:111], v[208:215], v234\n\t" \
  "buffer_load_dwordx4 v254, s[8:11], s24 offen lds\n\t" \
  "ds_read_b64 v[90:91], v235 offset:136\n\t" \
  "v_add_u32 v235, s59, v235\n\t" \
  "v_add_u32 v235, s60, v235\n\t" \
  "s_waitcnt vmcnt(8) lgkmcnt(0)\n\t" \
  "s_barrier\n\t" \
  "v_smfmac_f32_16x16x64_bf16 v[184:187], v[112:115], v[216:223], v234\n\t" \
  "ds_read_b64 v[200:201], v232 offset:16400\n\t" \
  "ds_read_b64 v[202:203], v232 offset:16800\n\t" \
  "v_smfmac_f32_16x16x64_bf16 v[172:175], v[116:119], v[216:223], v234\n\t" \
  "ds_read_b64 v[204:205], v232 offset:17200\n\t" \
  "ds_read_b64 v[206:207], v232 offset:17600\n\t" \
  "v_smfmac_f32_16x16x64_bf16 v[160:163], v[120:123], v[216:223], v234\n\t" \
  "ds_read_b64 v[208:209], v232 offset:16408\n\t" \
  "v_cvt_pk_bf16_f32 v144, v144, v145\n\t" \
  "v_cvt_pk_bf16_f32 v145, v146, v147\n\t" \
  "s_add_u32 m0, s25, 56880\n\t" \
  "s_add_u32 s24, s24, s26\n\t" \
  "v_smfmac_f32_16x16x64_bf16 v[148:151], v[124:127], v[216:223], v234\n\t" \
  "buffer_load_dwordx4 v254, s[8:11], s24 offen lds\n\t" \
  "ds_read_b64 v[210:211], v232 offset:16808\n\t" \
  "v_smfmac_f32_16x16x64_bf16 v[188:191], v[128:131], v[224:231], v234\n\t" \
  "ds_read_b64 v[212:213], v232 offset:17208\n\t" \
  "v_smfmac_f32_16x16x64_bf16 v[176:179], v[132:135], v[224:231], v234\n\t" \
  "ds_read_b64 v[214:215], v232 offset:17608\n\t" \
  "v_smfmac_f32_16x16x64_bf16 v[164:167], v[136:139], v[224:231], v234\n\t" \
  "ds_read_b64 v[216:217], v232 offset:16416\n\t" \
  "v_cvt_pk_bf16_f32 v148, v148, v149\n\t" \
  "v_cvt_pk_bf16_f32 v149, v150, v151\n\t" \
  "s_add_u32 m0, s25, 65080\n\t" \
  "s_add_u32 s24, s24, s26\n\t" \
  "v_smfmac_f32_16x16x64_bf16 v[152:155], v[140:143], v[224:231], v234\n\t" \
  "buffer_load_dwordx4 v254, s[8:11], s24 offen lds\n\t" \
  "ds_read_b64 v[218:219], v232 offset:16816\n\t" \
  "s_waitcnt lgkmcnt(6)\n\t" \
  "v_smfmac_f32_16x16x64_bf16 v[0:3], v[96:99], v[200:207], v234\n\t" \
  "ds_read_b64 v[220:221], v232 offset:17216\n\t" \
  "v_smfmac_f32_16x16x64_bf16 v[180:183], v[100:103], v[200:207], v234\n\t" \
  "ds_read_b64 v[222:223], v232 offset:17616\n\t" \
  "v_mfma_f32_16x16x32_bf16 v[32:35], v[242:245], v[240:243], 0\n\t" \
  "v_smfmac_f32_16x16x64_bf16 v[168:171], v[104:107], v[200:207], v234\n\t" \
  "v_cvt_pk_bf16_f32 v152, v152, v153\n\t" \
  "v_cvt_pk_bf16_f32 v153, v154, v155\n\t" \
  "v_smfmac_f32_16x16x64_bf16 v[156:159], v[108:111], v[200:207], v234\n\t" \
  "s_waitcnt vmcnt(8) lgkmcnt(0)\n\t" \
  "s_barrier\n\t" \
  "s_add_u32 m0, s25, 73280\n\t" \
  "s_add_u32 s24, s24, s26\n\t" \
  "v_smfmac_f32_16x16x64_bf16 v[4:7], v[112:115], v[208:215], v234\n\t" \
  "buffer_load_dwordx4 v254, s[8:11], s24 offen lds\n\t" \
  "ds_read_b64 v[224:225], v232 offset:32800\n\t" \
  "ds_read_b64 v[226:227], v232 offset:33200\n\t" \
  "v_smfmac_f32_16x16x64_bf16 v[184:187], v[116:119], v[208:215], v234\n\t" \
  "ds_read_b64 v[228:229], v232 offset:33600\n\t" \
  "ds_read_b64 v[230:231], v232 offset:34000\n\t" \
  "s_add_u32 m0, s25, 81480\n\t" \
  "s_add_u32 s24, s24, s26\n\t" \
  "v_smfmac_f32_16x16x64_bf16 v[172:175], v[120:123], v[208:215], v234\n\t" \
  "buffer_load_dwordx4 v254, s[8:11], s24 offen lds\n\t" \
  "ds_read_b64 v[200:201], v232 offset:32808\n\t" \
  "v_cvt_pk_bf16_f32 v156, v156, v157\n\t" \
  "v_cvt_pk_bf16_f32 v157, v158, v159\n\t" \
  "v_smfmac_f32_16x16x64_bf16 v[160:163], v[124:127], v[208:215], v234\n\t" \
  "ds_read_b64 v[202:203], v232 offset:33208\n\t" \
  "v_smfmac_f32_16x16x64_bf16 v[8:11], v[128:131], v[216:223], v234\n\t" \
  "buffer_store_dwordx4 v[76:79], v250, s[20:23], s29 offen offset:0 sc1\n\t" \
  "ds_read_b64 v[204:205], v232 offset:33608\n\t" \
  "v_smfmac_f32_16x16x64_bf16 v[188:191], v[132:135], v[216:223], v234\n\t" \
  "ds_read_b64 v[206:207], v232 offset:34008\n\t" \
  "v_smfmac_f32_16x16x64_bf16 v[176:179], v[136:139], v[216:223], v234\n\t" \
  "ds_read_b64 v[208:209], v232 offset:32816\n\t" \
  "v_cvt_pk_bf16_f32 v160, v160, v161\n\t" \
  "v_cvt_pk_bf16_f32 v161, v162, v163\n\t" \
  "v_smfmac_f32_16x16x64_bf16 v[164:167], v[140:143], v[216:223], v234\n\t" \
  "ds_read_b64 v[210:211], v232 offset:33216\n\t" \
  "s_waitcnt lgkmcnt(6)\n\t" \
  "v_smfmac_f32_16x16x64_bf16 v[12:15], v[96:99], v[224:231], v234\n\t" \
  "ds_read_b64 v[212:213], v232 offset:33616\n\t" \
  "v_smfmac_f32_16x16x64_bf16 v[0:3], v[100:103], v[224:231], v234\n\t" \
  "ds_read_b64 v[214:215], v232 offset:34016\n\t" \
  "v_smfmac_f32_16x16x64_bf16 v[180:183], v[104:107], v[224:231], v234\n\t" \
  "s_nop 0\n\t" \
  "v_cvt_pk_bf16_f32 v164, v164, v165\n\t" \
  "v_cvt_pk_bf16_f32 v165, v166, v167\n\t" \
  "v_smfmac_f32_16x16x64_bf16 v[168:171], v[108:111], v[224:231], v234\n\t" \
  "s_waitcnt vmcnt(9) lgkmcnt(0)\n\t" \
  "s_barrier\n\t" \
  "v_smfmac_f32_16x16x64_bf16 v[16:19], v[112:115], v[200:207], v234\n\t" \
  "ds_write_b64 v193, v[144:145] offset:12288\n\t" \
  "ds_read_b64 v[216:217], v253 offset:0\n\t" \
  "v_smfmac_f32_16x16x64_bf16 v[4:7], v[116:119], v[200:207], v234\n\t" \
  "ds_write_b64 v193, v[148:149] offset:16384\n\t" \
  "ds_read_b64 v[218:219], v253 offset:400\n\t" \
  "v_smfmac_f32_16x16x64_bf16 v[184:187], v[120:123], v[200:207], v234\n\t" \
  "buffer_store_dwordx4 v[84:87], v250, s[20:23], s29 offen offset:64 sc1\n\t" \
  "ds_read_b64 v[220:221], v253 offset:800\n\t" \
  "ds_read_b64 v[222:223], v253 offset:1200\n\t" \
  "v_cvt_pk_bf16_f32 v168, v168, v169\n\t" \
  "v_cvt_pk_bf16_f32 v169, v170, v171\n\t" \
  "v_smfmac_f32_16x16x64_bf16 v[172:175], v[124:127], v[200:207], v234\n\t" \
  "ds_write_b64 v193, v[152:153] offset:20480\n\t" \
  "ds_read_b64 v[224:225], v253 offset:8\n\t" \
  "v_smfmac_f32_16x16x64_bf16 v[20:23], v[128:131], v[208:215], v234\n\t" \
  "ds_write_b64 v193, v[156:157] offset:0\n\t" \
  "ds_read_b64 v[226:227], v253 offset:408\n\t" \
  "v_smfmac_f32_16x16x64_bf16 v[8:11], v[132:135], v[208:215], v234\n\t" \
  "ds_write_b64 v193, v[160:161] offset:4096\n\t" \
  "ds_read_b64 v[228:229], v253 offset:808\n\t" \
  "v_smfmac_f32_16x16x64_bf16 v[188:191], v[136:139], v[208:215], v234\n\t" \
  "ds_write_b64 v193, v[164:165] offset:8192\n\t" \
  "ds_read_b64 v[230:231], v253 offset:1208\n\t" \
  "v_mfma_f32_16x16x32_bf16 v[76:79], v[242:245], v[240:243], 0\n\t" \
  "v_cvt_pk_bf16_f32 v172, v172, v173\n\t" \
  "v_cvt_pk_bf16_f32 v173, v174, v175\n\t" \
  "v_smfmac_f32_16x16x64_bf16 v[176:179], v[140:143], v[208:215], v234\n\t" \
  "ds_read_b64 v[200:201], v253 offset:16\n\t" \
  "ds_read_b64 v[202:203], v253 offset:416\n\t" \
  "s_waitcnt lgkmcnt(10)\n\t" \
  "v_smfmac_f32_16x16x64_bf16 v[24:27], v[96:99], v[216:223], v234\n\t" \
  "ds_read_b64 v[204:205], v253 offset:816\n\t" \
  "v_smfmac_f32_16x16x64_bf16 v[12:15], v[100:103], v[216:223], v234\n\t" \
  "ds_read_b64 v[206:207], v253 offset:1216\n\t" \
  "v_smfmac_f32_16x16x64_bf16 v[0:3], v[104:107], v[216:223], v234\n\t" \
  "v_mfma_f32_16x16x32_bf16 v[84:87], v[242:245], v[240:243], 0\n\t" \
  "v_cvt_pk_bf16_f32 v176, v176, v177\n\t" \
  "v_cvt_pk_bf16_f32 v177, v178, v179\n\t" \
  "v_smfmac_f32_16x16x64_bf16 v[180:183], v[108:111], v[216:223], v234\n\t" \
  "buffer_store_dwordx4 v[92:95], v250, s[20:23], s29 offen offset:128 sc1\n\t" \
  "s_waitcnt vmcnt(9) lgkmcnt(0)\n\t" \
  "s_barrier\n\t" \
  "v_smfmac_f32_16x16x64_bf16 v[28:31], v[112:115], v[224:231], v234\n\t" \
  "ds_write_b64 v192, v[168:169] offset:0\n\t" \
  "ds_read_b64 v[208:209], v253 offset:16400\n\t" \
  "ds_read_b128 v[144:147], v194 offset:0\n\t" \
  "v_smfmac_f32_16x16x64_bf16 v[16:19], v[116:119], v[224:231], v234\n\t" \
  "ds_write_b64 v192, v[172:173] offset:4096\n\t" \
  "ds_read_b64 v[210:211], v253 offset:16800\n\t" \
  "ds_read_b128 v[148:151], v194 offset:4096\n\t" \
  "v_smfmac_f32_16x16x64_bf16 v[4:7], v[120:123], v[224:231], v234\n\t" \
  "ds_write_b64 v192, v[176:177] offset:8192\n\t" \
  "ds_read_b64 v[212:213], v253 offset:17200\n\t" \
  "v_mfma_f32_16x16x32_bf16 v[92:95], v[242:245], v[240:243], 0\n\t" \
  "v_cvt_pk_bf16_f32 v180, v180, v181\n\t" \
  "v_cvt_pk_bf16_f32 v181, v182, v183\n\t" \
  "ds_read_b128 v[152:155], v194 offset:8192\n\t" \
  "v_smfmac_f32_16x16x64_bf16 v[184:187], v[124:127], v[224:231], v234\n\t" \
  "ds_read_b64 v[214:215], v253 offset:17600\n\t" \
  "ds_read_b64 v[216:217], v253 offset:16408\n\t" \
  "v_add_u32 v194, -93792, v194\n\t" \
  "v_smfmac_f32_16x16x64_bf16 v[32:35], v[128:131], v[200:207], v234\n\t" \
  "ds_read_b64 v[218:219], v253 offset:16808\n\t" \
  "ds_read_b64 v[220:221], v253 offset:17208\n\t" \
  "v_smfmac_f32_16x16x64_bf16 v[20:23], v[132:135], v[200:207], v234\n\t" \
  "ds_read_b64 v[222:223], v253 offset:17608\n\t" \
  "s_waitcnt lgkmcnt(11)\n\t" \
  "v_mfma_f32_16x16x32_bf16 v[168:171], v[144:147], v[144:147], 0\n\t" \
  "ds_read_b64 v[224:225], v253 offset:16416\n\t" \
  "v_smfmac_f32_16x16x64_bf16 v[8:11], v[136:139], v[200:207], v234\n\t" \
  "ds_read_b64 v[226:227], v253 offset:16816\n\t" \
  "v_cvt_pk_bf16_f32 v184, v184, v185\n\t" \
  "v_cvt_pk_bf16_f32 v185, v186, v187\n\t" \
  "v_mfma_f32_16x16x32_bf16 v[172:175], v[196:199], v[144:147], 0\n\t" \
  "ds_read_b64 v[228:229], v253 offset:17216\n\t" \
  "v_smfmac_f32_16x16x64_bf16 v[188:191], v[140:143], v[200:207], v234\n\t" \
  "ds_read_b64 v[230:231], v253 offset:17616\n\t" \
  "s_waitcnt lgkmcnt(12)\n\t" \
  "v_mfma_f32_16x16x32_bf16 v[168:171], v[148:151], v[148:151], v[168:171]\n\t" \
  "s_waitcnt lgkmcnt(8)\n\t" \
  "v_smfmac_f32_16x16x64_bf16 v[76:79], v[96:99], v[208:215], v234\n\t" \
  "v_mfma_f32_16x16x32_bf16 v[172:175], v[196:199], v[148:151], v[172:175]\n\t" \
  "v_smfmac_f32_16x16x64_bf16 v[24:27], v[100:103], v[208:215], v234\n\t" \
  "v_mfma_f32_16x16x32_bf16 v[168:171], v[152:155], v[152:155], v[168:171]\n\t" \
  "buffer_store_dwordx4 v[48:51], v250, s[20:23], s12 offen offset:0 sc1\n\t" \
  "v_smfmac_f32_16x16x64_bf16 v[12:15], v[104:107], v[208:215], v234\n\t" \
  "v_cvt_pk_bf16_f32 v188, v188, v189\n\t" \
  "v_cvt_pk_bf16_f32 v189, v190, v191\n\t" \
  "v_mfma_f32_16x16x32_bf16 v[172:175], v[196:199], v[152:155], v[172:175]\n\t" \
  "v_smfmac_f32_16x16x64_bf16 v[0:3], v[108:111], v[208:215], v234\n\t" \
  "s_waitcnt vmcnt(7) lgkmcnt(0)\n\t" \
  "s_barrier\n\t" \
  "v_smfmac_f32_16x16x64_bf16 v[84:87], v[112:115], v[216:223], v234\n\t" \
  "ds_write_b64 v192, v[180:181] offset:12288\n\t" \
  "ds_read_b64 v[200:201], v253 offset:32800\n\t" \
  "v_smfmac_f32_16x16x64_bf16 v[28:31], v[116:119], v[216:223], v234\n\t" \
  "ds_write_b64 v192, v[184:185] offset:16384\n\t" \
  "ds_read_b64 v[202:203], v253 offset:33200\n\t" \
  "v_cndmask_b32 v168, v168, v169, s[40:41]\n\t" \
  "v_cndmask_b32 v170, v170, v171, s[40:41]\n\t" \
  "v_smfmac_f32_16x16x64_bf16 v[16:19], v[120:123], v[216:223], v234\n\t" \
  "ds_write_b64 v192, v[188:189] offset:20480\n\t" \
  "ds_read_b64 v[204:205], v253 offset:33600\n\t" \
  "v_cvt_pk_bf16_f32 v0, v0, v1\n\t" \
  "v_cvt_pk_bf16_f32 v1, v2, v3\n\t" \
  "v_cndmask_b32 v173, v168, v170, s[42:43]\n\t" \
  "v_mov_b32 v174, 0x1127c5ac\n\t" \
  "ds_read_b64_tr_b16 v[156:157], v195 offset:0\n\t" \
  "v_smfmac_f32_16x16x64_bf16 v[4:7], v[124:127], v[216:223], v234\n\t" \
  "ds_read_b64 v[206:207], v253 offset:34000\n\t" \
  "ds_read_b64 v[208:209], v253 offset:32808\n\t" \
  "s_not_b64 vcc, s[52:53]\n\t" \
  "ds_read_b64_tr_b16 v[158:159], v195 offset:4096\n\t" \
  "v_smfmac_f32_16x16x64_bf16 v[92:95], v[128:131], v[224:231], v234\n\t" \
  "ds_read_b64 v[210:211], v253 offset:33208\n\t" \
  "ds_read_b64 v[212:213], v253 offset:33608\n\t" \
  "ds_read_b64 v[48:49], v235 offset:0\n\t" \
  "ds_read_b64 v[50:51], v235 offset:8\n\t" \
  "ds_read_b64_tr_b16 v[160:161], v195 offset:2048\n\t" \
  "v_smfmac_f32_16x16x64_bf16 v[32:35], v[132:135], v[224:231], v234\n\t" \
  "buffer_store_dwordx4 v[56:59], v250, s[20:23], s12 offen offset:64 sc1\n\t" \
  "ds_read_b64 v[214:215], v253 offset:34008\n\t" \
  "ds_read_b64 v[216:217], v253 offset:32816\n\t" \
  "ds_read_b64 v[56:57], v235 offset:64\n\t" \
  "ds_read_b64_tr_b16 v[162:163], v195 offset:6144\n\t" \
  "v_smfmac_f32_16x16x64_bf16 v[20:23], v[136:139], v[224:231], v234\n\t" \
  "ds_read_b64 v[218:219], v253 offset:33216\n\t" \
  "ds_read_b64 v[58:59], v235 offset:72\n\t" \
  "v_cvt_pk_bf16_f32 v4, v4, v5\n\t" \
  "v_cvt_pk_bf16_f32 v5, v6, v7\n\t" \
  "ds_read_b64_tr_b16 v[164:165], v195 offset:8192\n\t" \
  "v_smfmac_f32_16x16x64_bf16 v[8:11], v[140:143], v[224:231], v234\n\t" \
  "ds_read_b64 v[220:221], v253 offset:33616\n\t" \
  "ds_read_b64 v[236:237], v235 offset:128\n\t" \
  "ds_read_b64_tr_b16 v[166:167], v195 offset:10240\n\t" \
  "s_waitcnt lgkmcnt(15)\n\t" \
  "v_smfmac_f32_16x16x64_bf16 v[76:79], v[100:103], v[200:207], v234\n\t" \
  "ds_read_b64 v[222:223], v253 offset:34016\n\t" \
  "ds_read_b64 v[238:239], v235 offset:136\n\t" \
  "v_add_u32 v195, -93792, v195\n\t" \
  "v_smfmac_f32_16x16x64_bf16 v[24:27], v[104:107], v[200:207], v234\n\t" \
  "v_smfmac_f32_16x16x64_bf16 v[12:15], v[108:111], v[200:207], v234\n\t" \
  "v_cvt_pk_bf16_f32 v8, v8, v9\n\t" \
  "v_cvt_pk_bf16_f32 v9, v10, v11\n\t" \
  "s_waitcnt vmcnt(7) lgkmcnt(0)\n\t" \
  "s_barrier\n\t" \
  "v_smfmac_f32_16x16x64_bf16 v[84:87], v[116:119], v[208:215], v234\n\t" \
  "ds_write_b64 v192, v[0:1] offset:24576\n\t" \
  "ds_read_b64 v[224:225], v232 offset:0\n\t" \
  "ds_read_b128 v[180:183], v194 offset:0\n\t" \
  "ds_read_b128 v[184:187], v194 offset:4096\n\t" \
  "ds_read_b128 v[188:191], v194 offset:8192\n\t" \
  "v_smfmac_f32_16x16x64_bf16 v[28:31], v[120:123], v[208:215], v234\n\t" \
  "ds_write_b64 v192, v[4:5] offset:28672\n\t" \
  "ds_read_b64 v[226:227], v232 offset:400\n\t" \
  "ds_read_b64 v[228:229], v232 offset:800\n\t" \
  "v_smfmac_f32_16x16x64_bf16 v[16:19], v[124:127], v[208:215], v234\n\t" \
  "ds_write_b64 v192, v[8:9] offset:32768\n\t" \
  "ds_read_b64 v[230:231], v232 offset:1200\n\t" \
  "ds_read_b64 v[200:201], v232 offset:8\n\t" \
  "v_cvt_pk_bf16_f32 v12, v12, v13\n\t" \
  "v_cvt_pk_bf16_f32 v13, v14, v15\n\t" \
  "v_smfmac_f32_16x16x64_bf16 v[92:95], v[132:135], v[216:223], v234\n\t" \
  "ds_write_b64 v192, v[12:13] offset:36864\n\t" \
  "ds_read_b64 v[202:203], v232 offset:408\n\t" \
  "ds_read_b64 v[204:205], v232 offset:808\n\t" \
  "s_waitcnt lgkmcnt(11)\n\t" \
  "v_mfma_f32_16x16x32_bf16 v[176:179], v[180:183], v[180:183], 0\n\t" \
  "ds_read_b64 v[206:207], v232 offset:1208\n\t" \
  "v_mfma_f32_16x16x32_bf16 v[168:171], v[196:199], v[180:183], 0\n\t" \
  "ds_read_b64 v[208:209], v232 offset:16\n\t" \
  "v_smfmac_f32_16x16x64_bf16 v[32:35], v[136:139], v[216:223], v234\n\t" \
  "ds_read_b64 v[210:211], v232 offset:416\n\t" \
  "ds_read_b64 v[212:213], v232 offset:816\n\t" \
  "s_waitcnt lgkmcnt(14)\n\t" \
  "v_mfma_f32_16x16x32_bf16 v[176:179], v[184:187], v[184:187], v[176:179]\n\t" \
  "buffer_store_dwordx4 v[64:67], v250, s[20:23], s12 offen offset:128 sc1\n\t" \
  "v_mfma_f32_16x16x32_bf16 v[168:171], v[196:199], v[184:187], v[168:171]\n\t" \
  "ds_read_b64 v[214:215], v232 offset:1216\n\t" \
  "v_smfmac_f32_16x16x64_bf16 v[20:23], v[140:143], v[216:223], v234\n\t" \
  "v_cvt_pk_bf16_f32 v16, v16, v17\n\t" \
  "v_cvt_pk_bf16_f32 v17, v18, v19\n\t" \
  "s_waitcnt lgkmcnt(14)\n\t" \
  "v_mfma_f32_16x16x32_bf16 v[176:179], v[188:191], v[188:191], v[176:179]\n\t" \
  "ds_write_b64 v192, v[16:17] offset:40960\n\t" \
  "v_mfma_f32_16x16x32_bf16 v[168:171], v[196:199], v[188:191], v[168:171]\n\t" \
  "s_waitcnt lgkmcnt(10)\n\t" \
  "v_smfmac_f32_16x16x64_bf16 v[76:79], v[104:107], v[224:231], v234\n\t" \
  "v_mov_b32 v183, 0\n\t" \
  "v_mov_b32 v187, 0\n\t" \
  "s_nop 1\n\t" \
  "v_cndmask_b32 v176, v176, v177, s[40:41]\n\t" \
  "v_cndmask_b32 v178, v178, v179, s[40:41]\n\t" \
  "v_cndmask_b32 v176, v176, v178, s[42:43]\n\t" \
  "v_smfmac_f32_16x16x64_bf16 v[24:27], v[108:111], v[224:231], v234\n\t" \
  "v_cndmask_b32_dpp v172, v168, v172, vcc row_ror:8 row_mask:0xf bank_mask:0xf\n\t" \
  "v_mul_f32 v172, 0x3daaaaab, v172\n\t" \
  "v_cndmask_b32_dpp v173, v176, v173, vcc row_ror:8 row_mask:0xf bank_mask:0xf\n\t" \
  "v_fmamk_f32 v173, v173, 0x3e2aaaab, v174\n\t" \
  "s_waitcnt vmcnt(6) lgkmcnt(0)\n\t" \
  "s_barrier\n\t" \
  "ds_read_b64 v[216:217], v232 offset:16400\n\t" \
  "ds_read_b64 v[218:219], v232 offset:16800\n\t" \
  "ds_read_b64 v[220:221], v232 offset:17200\n\t" \
  "ds_read_b64 v[222:223], v232 offset:17600\n\t" \
  "v_smfmac_f32_16x16x64_bf16 v[84:87], v[120:123], v[200:207], v234\n\t" \
  "ds_read_b64 v[224:225], v232 offset:16408\n\t" \
  "ds_read_b64 v[226:227], v232 offset:16808\n\t" \
  "v_cvt_pk_bf16_f32 v20, v20, v21\n\t" \
  "v_cvt_pk_bf16_f32 v21, v22, v23\n\t" \
  "v_fma_f32 v173, -v172, v172, v173\n\t" \
  "v_max_f32 v173, v174, v173\n\t" \
  "v_rsq_f32 v173, v173 div:2\n\t" \
  "s_mov_b64 exec, s[52:53]\n\tv_mul_f32 v173, 0x08805555, v173\n\ts_mov_b64 exec, -1\n\t" \
  "v_smfmac_f32_16x16x64_bf16 v[28:31], v[124:127], v[200:207], v234\n\t" \
  "ds_write_b64 v192, v[20:21] offset:45056\n\t" \
  "ds_read_b64 v[228:229], v232 offset:17208\n\t" \
  "s_mov_b64 vcc, s[40:41]\n\t" \
  "v_cvt_pk_bf16_f32 v175, v173, v173\n\t" \
  "v_and_b32 v173, 0xffff0000, v175\n\t" \
  "v_mul_f32 v174, -v172, v173 div:2\n\t" \
  "v_cndmask_b32_sdwa v175, v175, v173, vcc dst_sel:DWORD dst_unused:UNUSED_PAD src0_sel:WORD_1 src1_sel:DWORD\n\t" \
  "v_smfmac_f32_16x16x64_bf16 v[92:95], v[136:139], v[208:215], v234\n\t" \
  "ds_read_b64 v[230:231], v232 offset:17608\n\t" \
  "ds_read_b64 v[200:201], v232 offset:16416\n\t" \
  "v_cvt_pk_bf16_f32 v24, v24, v25\n\t" \
  "v_cvt_pk_bf16_f32 v25, v26, v27\n\t" \
  "v_and_b32 v172, 0xffff0000, v174\n\t" \
  "s_or_b64 vcc, s[52:53], s[42:43]\n\t" \
  "v_sub_f32 v174, v174, v172\n\t" \
  "v_cndmask_b32 v180, v175, v240, vcc\n\t" \
  "v_smfmac_f32_16x16x64_bf16 v[32:35], v[140:143], v[208:215], v234\n\t" \
  "ds_write_b64 v192, v[24:25] offset:56880\n\t" \
  "ds_read_b64 v[202:203], v232 offset:16816\n\t" \
  "v_perm_b32 v174, v172, v174, s55\n\t" \
  "v_cndmask_b32_dpp v184, v175, v240, vcc row_ror:8 row_mask:0xf bank_mask:0xf\n\t" \
  "s_mov_b64 vcc, s[52:53]\n\t" \
  "v_cndmask_b32 v182, v174, v240, vcc\n\t" \
  "ds_read_b64_tr_b16 v[144:145], v195 offset:0\n\t" \
  "ds_read_b64_tr_b16 v[146:147], v195 offset:4096\n\t" \
  "s_waitcnt lgkmcnt(10)\n\t" \
  "v_smfmac_f32_16x16x64_bf16 v[76:79], v[108:111], v[216:223], v234\n\t" \
  "ds_read_b64 v[204:205], v232 offset:17216\n\t" \
  "ds_read_b64 v[206:207], v232 offset:17616\n\t" \
  "v_cvt_pk_bf16_f32 v28, v28, v29\n\t" \
  "v_cvt_pk_bf16_f32 v29, v30, v31\n\t" \
  "v_cndmask_b32_dpp v186, v174, v240, vcc row_ror:8 row_mask:0xf bank_mask:0xf\n\t" \
  "s_orn2_b64 vcc, s[52:53], s[42:43]\n\t" \
  "v_cndmask_b32 v181, v175, v240, vcc\n\t" \
  "v_cndmask_b32_dpp v185, v175, v240, vcc row_ror:8 row_mask:0xf bank_mask:0xf\n\t" \
  "ds_read_b64_tr_b16 v[148:149], v195 offset:2048\n\t" \
  "ds_read_b64_tr_b16 v[150:151], v195 offset:6144\n\t" \
  "s_waitcnt lgkmcnt(9)\n\t" \
  "v_smfmac_f32_16x16x64_bf16 v[84:87], v[124:127], v[224:231], v234\n\t" \
  "ds_write_b64 v192, v[28:29] offset:60976\n\t" \
  "ds_read_b64_tr_b16 v[152:153], v195 offset:8192\n\t" \
  "ds_read_b64_tr_b16 v[154:155], v195 offset:10240\n\t" \
  "v_mfma_f32_16x16x16_bf16 v[0:3], v[196:197], v[182:183], 0\n\t" \
  "v_mfma_f32_16x16x16_bf16 v[4:7], v[196:197], v[186:187], 0\n\t" \
  "s_waitcnt lgkmcnt(5)\n\t" \
  "v_smfmac_f32_16x16x64_bf16 v[92:95], v[140:143], v[200:207], v234\n\t" \
  "v_cvt_pk_bf16_f32 v32, v32, v33\n\t" \
  "v_cvt_pk_bf16_f32 v33, v34, v35\n\t" \
  "v_mfma_f32_16x16x16_bf16 v[188:191], v[156:157], v[180:181], v[0:3]\n\t" \
  "ds_write_b64 v192, v[32:33] offset:65072\n\t" \
  "v_mfma_f32_16x16x16_bf16 v[176:179], v[158:159], v[180:181], v[0:3]\n\t" \
  "v_cvt_pk_bf16_f32 v76, v76, v77\n\t" \
  "v_cvt_pk_bf16_f32 v77, v78, v79\n\t" \
  "ds_write_b64 v193, v[76:77] offset:0\n\t" \
  "v_cvt_pk_bf16_f32 v84, v84, v85\n\t" \
  "v_cvt_pk_bf16_f32 v85, v86, v87\n\t" \
  "ds_write_b64 v193, v[84:85] offset:4096\n\t" \
  "v_mfma_f32_16x16x16_bf16 v[168:171], v[160:161], v[180:181], v[0:3]\n\t" \
  "v_cvt_pk_bf16_f32 v96, v188, v189\n\t" \
  "v_cvt_pk_bf16_f32 v97, v190, v191\n\t" \
  "v_mfma_f32_16x16x16_bf16 v[172:175], v[162:163], v[180:181], v[0:3]\n\t" \
  "v_cvt_pk_bf16_f32 v92, v92, v93\n\t" \
  "v_cvt_pk_bf16_f32 v93, v94, v95\n\t" \
  "ds_write_b64 v193, v[92:93] offset:8192\n\t" \
  "v_cvt_pk_bf16_f32 v98, v176, v177\n\t" \
  "v_cvt_pk_bf16_f32 v99, v178, v179\n\t" \
  "v_mfma_f32_16x16x16_bf16 v[188:191], v[164:165], v[180:181], v[0:3]\n\t" \
  "v_cvt_pk_bf16_f32 v112, v168, v169\n\t" \
  "v_cvt_pk_bf16_f32 v113, v170, v171\n\t" \
  "v_mfma_f32_16x16x16_bf16 v[176:179], v[166:167], v[180:181], v[0:3]\n\t" \
  "v_cvt_pk_bf16_f32 v114, v172, v173\n\t" \
  "v_cvt_pk_bf16_f32 v115, v174, v175\n\t" \
  "s_nop 2\n\t" \
  "v_cvt_pk_bf16_f32 v128, v188, v189\n\t" \
  "v_cvt_pk_bf16_f32 v129, v190, v191\n\t" \
  "s_nop 0\n\t" \
  "v_cvt_pk_bf16_f32 v130, v176, v177\n\t" \
  "v_cvt_pk_bf16_f32 v131, v178, v179\n\t" \
  "s_and_b32 s69, s69, 0xffff\n\t" \
  "s_waitcnt lgkmcnt(0)\n\t" \
  "s_barrier\n\t" \
  "ds_read_b64_tr_b16 v[208:209], v248 offset:43048\n\t" \
  "ds_read_b64_tr_b16 v[210:211], v248 offset:44072\n\t" \
  "ds_read_b128 v[104:107], v194 offset:24576\n\t" \
  "ds_read_b128 v[120:123], v194 offset:28672\n\t" \
  "ds_read_b128 v[136:139], v194 offset:32768\n\t" \
  "v_add_u32 v194, s54, v194\n\t" \
  "ds_read_b128 v[108:111], v194 offset:49152\n\t" \
  "ds_read_b128 v[124:127], v194 offset:53248\n\t" \
  "ds_read_b128 v[140:143], v194 offset:57344\n\t" \
  "v_mfma_f32_16x16x16_bf16 v[168:171], v[144:145], v[184:185], v[4:7]\n\t" \
  "ds_read_b64_tr_b16 v[212:213], v248 offset:46120\n\t" \
  "ds_read_b64_tr_b16 v[214:215], v248 offset:47144\n\t" \
  "v_mfma_f32_16x16x16_bf16 v[172:175], v[146:147], v[184:185], v[4:7]\n\t" \
  "ds_read_b64_tr_b16 v[216:217], v248 offset:45096\n\t" \
  "ds_read_b64_tr_b16 v[218:219], v248 offset:48168\n\t" \
  "s_mov_b32 m0, s44\n\t" \
  "s_mov_b32 exec_hi, 0xffff\n\t" \
  "buffer_load_dwordx4 v252, s[16:19], s33 offen lds\n\t" \
  "s_mov_b32 exec_hi, -1\n\t" \
  "s_add_u32 s33, s33, s32\n\t" \
  "v_mfma_f32_16x16x16_bf16 v[188:191], v[148:149], v[184:185], v[4:7]\n\t" \
  "ds_read_b64_tr_b16 v[160:161], v248 offset:43560\n\t" \
  "ds_read_b64_tr_b16 v[162:163], v248 offset:44584\n\t" \
  "v_cvt_pk_bf16_f32 v100, v168, v169\n\t" \
  "v_cvt_pk_bf16_f32 v101, v170, v171\n\t" \
  "v_mfma_f32_16x16x16_bf16 v[176:179], v[150:151], v[184:185], v[4:7]\n\t" \
  "ds_read_b64_tr_b16 v[164:165], v248 offset:46632\n\t" \
  "ds_read_b64_tr_b16 v[166:167], v248 offset:47656\n\t" \
  "v_cvt_pk_bf16_f32 v102, v172, v173\n\t" \
  "v_cvt_pk_bf16_f32 v103, v174, v175\n\t" \
  "v_mfma_f32_16x16x16_bf16 v[168:171], v[152:153], v[184:185], v[4:7]\n\t" \
  "ds_read_b64_tr_b16 v[232:233], v248 offset:45608\n\t" \
  "ds_read_b64_tr_b16 v[234:235], v248 offset:48680\n\t" \
  "v_cvt_pk_bf16_f32 v116, v188, v189\n\t" \
  "v_cvt_pk_bf16_f32 v117, v190, v191\n\t" \
  "v_mfma_f32_16x16x16_bf16 v[172:175], v[154:155], v[184:185], v[4:7]\n\t" \
  "ds_read_b64_tr_b16 v[24:25], v195 offset:24576\n\t" \
  "ds_read_b64_tr_b16 v[26:27], v195 offset:28672\n\t" \
  "v_cvt_pk_bf16_f32 v118, v176, v177\n\t" \
  "v_cvt_pk_bf16_f32 v119, v178, v179\n\t" \
  "s_waitcnt lgkmcnt(15)\n\t" \
  "v_mfma_f32_16x16x32_bf16 v[144:147], v[208:211], v[96:99], 0\n\t" \
  "ds_read_b64_tr_b16 v[28:29], v195 offset:26624\n\t" \
  "ds_read_b64_tr_b16 v[30:31], v195 offset:30720\n\t" \
  "v_cvt_pk_bf16_f32 v132, v168, v169\n\t" \
  "v_cvt_pk_bf16_f32 v133, v170, v171\n\t" \
  "s_waitcnt lgkmcnt(12)\n\t" \
  "v_mfma_f32_16x16x32_bf16 v[144:147], v[212:215], v[112:115], v[144:147]\n\t" \
  "ds_read_b64_tr_b16 v[32:33], v195 offset:32768\n\t" \
  "ds_read_b64_tr_b16 v[34:35], v195 offset:34816\n\t" \
  "v_cvt_pk_bf16_f32 v134, v172, v173\n\t" \
  "v_cvt_pk_bf16_f32 v135, v174, v175\n\t" \
  "v_mfma_f32_16x16x32_bf16 v[152:155], v[208:211], v[100:103], 0\n\t" \
  "v_add_u32 v195, s54, v195\n\t" \
  "ds_read_b64_tr_b16 v[76:77], v195 offset:49152\n\t" \
  "ds_read_b64_tr_b16 v[78:79], v195 offset:53248\n\t" \
  "s_waitcnt lgkmcnt(14)\n\t" \
  "v_mfma_f32_16x16x32_bf16 v[144:147], v[216:219], v[128:131], v[144:147]\n\t" \
  "ds_read_b64_tr_b16 v[84:85], v195 offset:51200\n\t" \
  "s_add_u32 m0, s44, s35\n\t" \
  "s_mov_b32 exec_hi, 0xffff\n\t" \
  "buffer_load_dwordx4 v252, s[16:19], s34 offen lds\n\t" \
  "s_mov_b32 exec_hi, -1\n\t" \
  "s_add_u32 s34, s34, s32\n\t" \
  "v_mfma_f32_16x16x32_bf16 v[152:155], v[212:215], v[116:119], v[152:155]\n\t" \
  "ds_read_b64_tr_b16 v[86:87], v195 offset:55296\n\t" \
  "v_mfma_f32_16x16x32_bf16 v[152:155], v[216:219], v[132:135], v[152:155]\n\t" \
  "ds_read_b64_tr_b16 v[92:93], v195 offset:57344\n\t" \
  "v_mfma_f32_16x16x32_bf16 v[8:11], v[104:107], v[104:107], 0\n\t" \
  "ds_read_b64_tr_b16 v[94:95], v195 offset:59392\n\t" \
  "s_mov_b32 m0, s45\n\t" \
  "s_mov_b32 exec_hi, 0xffff\n\t" \
  "buffer_load_dwordx4 v252, s[16:19], s33 offen lds\n\t" \
  "s_mov_b32 exec_hi, -1\n\t" \
  "s_add_u32 s33, s33, s32\n\t" \
  "v_mfma_f32_16x16x32_bf16 v[12:15], v[196:199], v[104:107], 0\n\t" \
  "v_cvt_pknorm_i16_f32 v220, |v146|, |v147|\n\t" \
  "v_cvt_pknorm_i16_f32 v222, |v144|, |v145|\n\t" \
  "v_mfma_f32_16x16x32_bf16 v[8:11], v[120:123], v[120:123], v[8:11]\n\t" \
  "v_lshlrev_b32_sdwa v221, 2, v220 dst_sel:DWORD dst_unused:UNUSED_PAD src0_sel:DWORD src1_sel:BYTE_3\n\t" \
  "v_lshlrev_b32_sdwa v220, 2, v220 dst_sel:DWORD dst_unused:UNUSED_PAD src0_sel:DWORD src1_sel:BYTE_1\n\t" \
  "v_mfma_f32_16x16x32_bf16 v[12:15], v[196:199], v[120:123], v[12:15]\n\t" \
  "ds_read_b32 v221, v221 offset:10016\n\t" \
  "v_lshlrev_b32_sdwa v223, 2, v222 dst_sel:DWORD dst_unused:UNUSED_PAD src0_sel:DWORD src1_sel:BYTE_3\n\t" \
  "v_lshlrev_b32_sdwa v222, 2, v222 dst_sel:DWORD dst_unused:UNUSED_PAD src0_sel:DWORD src1_sel:BYTE_1\n\t" \
  "v_mfma_f32_16x16x32_bf16 v[8:11], v[136:139], v[136:139], v[8:11]\n\t" \
  "ds_read_b32 v220, v220 offset:10016\n\t" \
  "v_lshlrev_b32_sdwa v225, 1, v155 dst_sel:DWORD dst_unused:UNUSED_PAD src0_sel:DWORD src1_sel:WORD_1\n\t" \
  "v_lshlrev_b32_sdwa v224, 1, v154 dst_sel:DWORD dst_unused:UNUSED_PAD src0_sel:DWORD src1_sel:WORD_1\n\t" \
  "v_mfma_f32_16x16x32_bf16 v[12:15], v[196:199], v[136:139], v[12:15]\n\t" \
  "ds_read_b32 v223, v223 offset:10016\n\t" \
  "v_lshlrev_b32_sdwa v227, 1, v153 dst_sel:DWORD dst_unused:UNUSED_PAD src0_sel:DWORD src1_sel:WORD_1\n\t" \
  "v_lshlrev_b32_sdwa v226, 1, v152 dst_sel:DWORD dst_unused:UNUSED_PAD src0_sel:DWORD src1_sel:WORD_1\n\t" \
  "v_mfma_f32_16x16x32_bf16 v[16:19], v[108:111], v[108:111], 0\n\t" \
  "ds_read_b32 v222, v222 offset:10016\n\t" \
  "v_mfma_f32_16x16x32_bf16 v[20:23], v[196:199], v[108:111], 0\n\t" \
  "ds_read_u16_d16_hi v225, v225 offset:10528\n\t" \
  "s_add_u32 m0, s45, s35\n\t" \
  "s_mov_b32 exec_hi, 0xffff\n\t" \
  "buffer_load_dwordx4 v252, s[16:19], s34 offen lds\n\t" \
  "s_mov_b32 exec_hi, -1\n\t" \
  "s_add_u32 s34, s34, s32\n\t" \
  "v_cndmask_b32 v8, v8, v9, s[40:41]\n\t" \
  "v_cndmask_b32 v10, v10, v11, s[40:41]\n\t" \
  "v_mfma_f32_16x16x32_bf16 v[16:19], v[124:127], v[124:127], v[16:19]\n\t" \
  "ds_read_u16 v224, v224 offset:10528\n\t" \
  "v_cndmask_b32 v13, v8, v10, s[42:43]\n\t" \
  "v_mov_b32 v14, 0x1127c5ac\n\t" \
  "v_mfma_f32_16x16x32_bf16 v[20:23], v[196:199], v[124:127], v[20:23]\n\t" \
  "ds_read_u16_d16_hi v227, v227 offset:10528\n\t" \
  "s_not_b64 vcc, s[52:53]\n\t" \
  "v_mfma_f32_16x16x32_bf16 v[16:19], v[140:143], v[140:143], v[16:19]\n\t" \
  "ds_read_u16 v226, v226 offset:10528\n\t" \
  "s_waitcnt lgkmcnt(4)\n\t" \
  "v_fma_mix_f32 v221, |v147|, v221, v221 op_sel:[0,1,0] op_sel_hi:[0,1,1] clamp\n\t" \
  "v_fma_mix_f32 v220, |v146|, v220, v220 op_sel:[0,1,0] op_sel_hi:[0,1,1] clamp\n\t" \
  "v_mfma_f32_16x16x32_bf16 v[20:23], v[196:199], v[140:143], v[20:23]\n\t" \
  "buffer_store_dwordx4 v[52:55], v250, s[20:23], s28 offen offset:0 sc1\n\t" \
  "v_fma_mix_f32 v223, |v145|, v223, v223 op_sel:[0,1,0] op_sel_hi:[0,1,1] clamp\n\t" \
  "v_fma_mix_f32 v222, |v144|, v222, v222 op_sel:[0,1,0] op_sel_hi:[0,1,1] clamp\n\t" \
  "v_mfma_f32_16x16x32_bf16 v[148:151], v[160:163], v[96:99], 0\n\t" \
  "s_waitcnt lgkmcnt(0)\n\t" \
  "v_or_b32 v155, v224, v225\n\t" \
  "v_or_b32 v154, v226, v227\n\t" \
  "v_mfma_f32_16x16x32_bf16 v[156:159], v[160:163], v[100:103], 0\n\t" \
  "v_cndmask_b32 v16, v16, v17, s[40:41]\n\t" \
  "v_cndmask_b32 v18, v18, v19, s[40:41]\n\t" \
  "v_cndmask_b32 v16, v16, v18, s[42:43]\n\t" \
  "v_cndmask_b32_dpp v12, v20, v12, vcc row_ror:8 row_mask:0xf bank_mask:0xf\n\t" \
  "v_mul_f32 v12, 0x3daaaaab, v12\n\t" \
  "v_mfma_f32_16x16x32_bf16 v[148:151], v[164:167], v[112:115], v[148:151]\n\t" \
  "v_cndmask_b32_dpp v13, v16, v13, vcc row_ror:8 row_mask:0xf bank_mask:0xf\n\t" \
  "v_fmamk_f32 v13, v13, 0x3e2aaaab, v14\n\t" \
  "v_fma_f32 v13, -v12, v12, v13\n\t" \
  "v_max_f32 v13, v14, v13\n\t" \
  "v_rsq_f32 v13, v13 div:2\n\t" \
  "v_mfma_f32_16x16x32_bf16 v[156:159], v[164:167], v[116:119], v[156:159]\n\t" \
  "s_mov_b64 exec, s[52:53]\n\tv_mul_f32 v13, 0x08805555, v13\n\ts_mov_b64 exec, -1\n\t" \
  "s_mov_b64 vcc, s[40:41]\n\t" \
  "v_cvt_pk_bf16_f32 v15, v13, v13\n\t" \
  "v_and_b32 v13, 0xffff0000, v15\n\t" \
  "v_mul_f32 v14, -v12, v13 div:2\n\t" \
  "v_cndmask_b32_sdwa v15, v15, v13, vcc dst_sel:DWORD dst_unused:UNUSED_PAD src0_sel:WORD_1 src1_sel:DWORD\n\t" \
  "v_and_b32 v12, 0xffff0000, v14\n\t" \
  "v_mfma_f32_16x16x32_bf16 v[148:151], v[232:235], v[128:131], v[148:151]\n\t" \
  "s_or_b64 vcc, s[52:53], s[42:43]\n\t" \
  "v_sub_f32 v14, v14, v12\n\t" \
  "v_cndmask_b32 v180, v15, v240, vcc\n\t" \
  "v_perm_b32 v14, v12, v14, s55\n\t" \
  "v_cndmask_b32_dpp v184, v15, v240, vcc row_ror:8 row_mask:0xf bank_mask:0xf\n\t" \
  "v_mfma_f32_16x16x32_bf16 v[156:159], v[232:235], v[132:135], v[156:159]\n\t" \
  "s_mov_b64 vcc, s[52:53]\n\t" \
  "v_cndmask_b32 v182, v14, v240, vcc\n\t" \
  "v_cndmask_b32_dpp v186, v14, v240, vcc row_ror:8 row_mask:0xf bank_mask:0xf\n\t" \
  "s_orn2_b64 vcc, s[52:53], s[42:43]\n\t" \
  "v_cndmask_b32 v181, v15, v240, vcc\n\t" \
  "v_mfma_f32_16x16x16_bf16 v[0:3], v[196:197], v[182:183], 0\n\t" \
  "v_cndmask_b32_dpp v185, v15, v240, vcc row_ror:8 row_mask:0xf bank_mask:0xf\n\t" \
  "s_mov_b64 vcc, 0\n\t" \
  "v_fma_f32 v147, |v147|, v221, v147 mul:4\n\t" \
  "v_mfma_f32_16x16x16_bf16 v[4:7], v[196:197], v[186:187], 0\n\t" \
  "v_fma_f32 v146, |v146|, v220, v146 mul:4\n\t" \
  "v_fma_f32 v145, |v145|, v223, v145 mul:4\n\t" \
  "v_mfma_f32_16x16x16_bf16 v[188:191], v[24:25], v[180:181], v[0:3]\n\t" \
  "v_fma_f32 v144, |v144|, v222, v144 mul:4\n\t" \
  "v_cvt_pk_bf16_f32 v147, v146, v147\n\t" \
  "v_mfma_f32_16x16x16_bf16 v[176:179], v[26:27], v[180:181], v[0:3]\n\t" \
  "v_cvt_pk_bf16_f32 v146, v144, v145\n\t" \
  "v_cvt_pknorm_i16_f32 v228, |v148|, |v149|\n\t" \
  "v_mfma_f32_16x16x16_bf16 v[200:203], v[76:77], v[184:185], v[4:7]\n\t" \
  "s_nop 1\n\t" \
  "v_cvt_pk_bf16_f32 v104, v188, v189\n\t" \
  "v_cvt_pk_bf16_f32 v105, v190, v191\n\t" \
  "v_mfma_f32_16x16x16_bf16 v[204:207], v[78:79], v[184:185], v[4:7]\n\t" \
  "v_cvt_pk_bf16_f32 v106, v176, v177\n\t" \
  "v_cvt_pk_bf16_f32 v107, v178, v179\n\t" \
  "v_mfma_f32_16x16x16_bf16 v[196:199], v[28:29], v[180:181], v[0:3]\n\t" \
  "buffer_store_dwordx4 v[60:63], v250, s[20:23], s28 offen offset:64 sc1\n\t" \
  "v_cvt_pk_bf16_f32 v108, v200, v201\n\t" \
  "v_cvt_pk_bf16_f32 v109, v202, v203\n\t" \
  "v_mfma_f32_16x16x16_bf16 v[188:191], v[30:31], v[180:181], v[0:3]\n\t" \
  "s_nop 0\n\t" \
  "v_cvt_pk_bf16_f32 v110, v204, v205\n\t" \
  "v_cvt_pk_bf16_f32 v111, v206, v207\n\t" \
  "v_mfma_f32_16x16x32_bf16 v[160:163], v[208:211], v[104:107], 0\n\t" \
  "v_cvt_pk_bf16_f32 v120, v196, v197\n\t" \
  "v_cvt_pk_bf16_f32 v121, v198, v199\n\t" \
  "v_mfma_f32_16x16x16_bf16 v[176:179], v[84:85], v[184:185], v[4:7]\n\t" \
  "s_nop 0\n\t" \
  "v_cvt_pk_bf16_f32 v122, v188, v189\n\t" \
  "v_cvt_pk_bf16_f32 v123, v190, v191\n\t" \
  "v_mfma_f32_16x16x16_bf16 v[200:203], v[86:87], v[184:185], v[4:7]\n\t" \
  "v_cvt_pknorm_i16_f32 v230, |v150|, |v151|\n\t" \
  "v_lshlrev_b32_sdwa v229, 2, v228 dst_sel:DWORD dst_unused:UNUSED_PAD src0_sel:DWORD src1_sel:BYTE_3\n\t" \
  "v_mfma_f32_16x16x32_bf16 v[168:171], v[208:211], v[108:111], 0\n\t" \
  "ds_read_b64_tr_b16 v[208:209], v248 offset:43560\n\t" \
  "v_cvt_pk_bf16_f32 v124, v176, v177\n\t" \
  "v_cvt_pk_bf16_f32 v125, v178, v179\n\t" \
  "v_mfma_f32_16x16x16_bf16 v[204:207], v[32:33], v[180:181], v[0:3]\n\t" \
  "ds_read_b64_tr_b16 v[210:211], v248 offset:44584\n\t" \
  "v_cvt_pk_bf16_f32 v126, v200, v201\n\t" \
  "v_cvt_pk_bf16_f32 v127, v202, v203\n\t" \
  "v_mfma_f32_16x16x16_bf16 v[196:199], v[34:35], v[180:181], v[0:3]\n\t" \
  "v_lshlrev_b32_sdwa v228, 2, v228 dst_sel:DWORD dst_unused:UNUSED_PAD src0_sel:DWORD src1_sel:BYTE_1\n\t" \
  "v_lshlrev_b32_sdwa v231, 2, v230 dst_sel:DWORD dst_unused:UNUSED_PAD src0_sel:DWORD src1_sel:BYTE_3\n\t" \
  "v_mfma_f32_16x16x32_bf16 v[160:163], v[212:215], v[120:123], v[160:163]\n\t" \
  "ds_read_b32 v229, v229 offset:10016\n\t" \
  "v_cvt_pk_bf16_f32 v136, v204, v205\n\t" \
  "v_cvt_pk_bf16_f32 v137, v206, v207\n\t" \
  "v_mfma_f32_16x16x16_bf16 v[188:191], v[92:93], v[184:185], v[4:7]\n\t" \
  "ds_read_b32 v228, v228 offset:10016\n\t" \
  "v_cvt_pk_bf16_f32 v138, v196, v197\n\t" \
  "v_cvt_pk_bf16_f32 v139, v198, v199\n\t" \
  "v_mfma_f32_16x16x16_bf16 v[176:179], v[94:95], v[184:185], v[4:7]\n\t" \
  "v_lshlrev_b32_sdwa v230, 2, v230 dst_sel:DWORD dst_unused:UNUSED_PAD src0_sel:DWORD src1_sel:BYTE_1\n\t" \
  "v_lshlrev_b32_sdwa v233, 1, v157 dst_sel:DWORD dst_unused:UNUSED_PAD src0_sel:DWORD src1_sel:WORD_1\n\t" \
  "v_mfma_f32_16x16x32_bf16 v[168:171], v[212:215], v[124:127], v[168:171]\n\t" \
  "ds_read_b64_tr_b16 v[212:213], v248 offset:46632\n\t" \
  "v_cvt_pk_bf16_f32 v140, v188, v189\n\t" \
  "v_cvt_pk_bf16_f32 v141, v190, v191\n\t" \
  "v_mfma_f32_16x16x32_bf16 v[160:163], v[216:219], v[136:139], v[160:163]\n\t" \
  "ds_read_b64_tr_b16 v[214:215], v248 offset:47656\n\t" \
  "v_cvt_pk_bf16_f32 v142, v176, v177\n\t" \
  "v_cvt_pk_bf16_f32 v143, v178, v179\n\t" \
  "s_nop 1\n\t" \
  "v_mfma_f32_16x16x32_bf16 v[168:171], v[216:219], v[140:143], v[168:171]\n\t" \
  "ds_read_b64_tr_b16 v[216:217], v248 offset:45608\n\t" \
  "v_lshlrev_b32_sdwa v232, 1, v156 dst_sel:DWORD dst_unused:UNUSED_PAD src0_sel:DWORD src1_sel:WORD_1\n\t" \
  "v_lshlrev_b32_sdwa v235, 1, v159 dst_sel:DWORD dst_unused:UNUSED_PAD src0_sel:DWORD src1_sel:WORD_1\n\t" \
  "s_waitcnt lgkmcnt(5)\n\t" \
  "ds_read_b64_tr_b16 v[218:219], v248 offset:48680\n\t" \
  "s_waitcnt vmcnt(4)\n\t" \
  "s_mov_b64 exec, 3\n\t" \
  "ds_add_rtn_u32 v246, v251, v250 offset:47136\n\t" \
  "s_mov_b64 exec, -1\n\t" \
  "v_mfma_f32_16x16x32_bf16 v[164:167], v[208:211], v[104:107], 0\n\t" \
  "ds_read_b32 v231, v231 offset:10016\n\t" \
  "v_lshlrev_b32_sdwa v234, 1, v158 dst_sel:DWORD dst_unused:UNUSED_PAD src0_sel:DWORD src1_sel:WORD_1\n\t" \
  "v_mfma_f32_16x16x32_bf16 v[172:175], v[208:211], v[108:111], 0\n\t" \
  "ds_read_b64_tr_b16 v[208:209], v248 offset:49192\n\t" \
  "ds_read_b64_tr_b16 v[210:211], v248 offset:50216\n\t" \
  "s_waitcnt lgkmcnt(6)\n\t" \
  "v_mfma_f32_16x16x32_bf16 v[164:167], v[212:215], v[120:123], v[164:167]\n\t" \
  "ds_read_b32 v230, v230 offset:10016\n\t" \
  "v_mfma_f32_16x16x32_bf16 v[172:175], v[212:215], v[124:127], v[172:175]\n\t" \
  "ds_read_b64_tr_b16 v[212:213], v248 offset:52264\n\t" \
  "ds_read_b64_tr_b16 v[214:215], v248 offset:53288\n\t" \
  "s_waitcnt lgkmcnt(7)\n\t" \
  "v_mfma_f32_16x16x32_bf16 v[164:167], v[216:219], v[136:139], v[164:167]\n\t" \
  "ds_read_u16_d16_hi v233, v233 offset:10528\n\t" \
  "v_mfma_f32_16x16x32_bf16 v[172:175], v[216:219], v[140:143], v[172:175]\n\t" \
  "ds_read_b64_tr_b16 v[216:217], v248 offset:51240\n\t" \
  "v_mfma_f32_4x4x4_16b_bf16 v[0:3], v[242:243], v[36:37], 0\n\t" \
  "v_mfma_f32_4x4x4_16b_bf16 v[4:7], v[242:243], v[38:39], 0\n\t" \
  "v_cvt_pknorm_i16_f32 v220, |v162|, |v163|\n\t" \
  "v_cvt_pknorm_i16_f32 v222, |v160|, |v161|\n\t" \
  "buffer_store_dwordx4 v[68:71], v250, s[20:23], s28 offen offset:128 sc1\n\t" \
  "ds_read_b64_tr_b16 v[218:219], v248 offset:54312\n\t" \
  "v_mfma_f32_4x4x4_16b_bf16 v[8:11], v[242:243], v[40:41], 0\n\t" \
  "v_mfma_f32_4x4x4_16b_bf16 v[12:15], v[242:243], v[42:43], 0\n\t" \
  "v_lshlrev_b32_sdwa v221, 2, v220 dst_sel:DWORD dst_unused:UNUSED_PAD src0_sel:DWORD src1_sel:BYTE_3\n\t" \
  "v_lshlrev_b32_sdwa v220, 2, v220 dst_sel:DWORD dst_unused:UNUSED_PAD src0_sel:DWORD src1_sel:BYTE_1\n\t" \
  "s_setprio 0\n\t" \
  "ds_read_u16 v232, v232 offset:10528\n\t" \
  "v_mfma_f32_4x4x4_16b_bf16 v[16:19], v[242:243], v[44:45], 0\n\t" \
  "v_mfma_f32_4x4x4_16b_bf16 v[20:23], v[242:243], v[46:47], 0\n\t" \
  "v_lshlrev_b32_sdwa v223, 2, v222 dst_sel:DWORD dst_unused:UNUSED_PAD src0_sel:DWORD src1_sel:BYTE_3\n\t" \
  "v_lshlrev_b32_sdwa v222, 2, v222 dst_sel:DWORD dst_unused:UNUSED_PAD src0_sel:DWORD src1_sel:BYTE_1\n\t" \
  "ds_read_u16_d16_hi v235, v235 offset:10528\n\t" \
  "v_mfma_f32_4x4x4_16b_bf16 v[24:27], v[242:243], v[72:73], 0\n\t" \
  "v_mfma_f32_4x4x4_16b_bf16 v[28:31], v[242:243], v[74:75], 0\n\t" \
  "v_lshlrev_b32_sdwa v225, 1, v171 dst_sel:DWORD dst_unused:UNUSED_PAD src0_sel:DWORD src1_sel:WORD_1\n\t" \
  "v_lshlrev_b32_sdwa v224, 1, v170 dst_sel:DWORD dst_unused:UNUSED_PAD src0_sel:DWORD src1_sel:WORD_1\n\t" \
  "ds_read_u16 v234, v234 offset:10528\n\t" \
  "v_mfma_f32_4x4x4_16b_bf16 v[32:35], v[242:243], v[80:81], 0\n\t" \
  "v_mfma_f32_4x4x4_16b_bf16 v[36:39], v[242:243], v[82:83], 0\n\t" \
  "v_lshlrev_b32_sdwa v227, 1, v169 dst_sel:DWORD dst_unused:UNUSED_PAD src0_sel:DWORD src1_sel:WORD_1\n\t" \
  "v_lshlrev_b32_sdwa v226, 1, v168 dst_sel:DWORD dst_unused:UNUSED_PAD src0_sel:DWORD src1_sel:WORD_1\n\t" \
  "ds_read_b32 v221, v221 offset:10016\n\t" \
  "s_waitcnt lgkmcnt(9)\n\t" \
  "v_mfma_f32_4x4x4_16b_bf16 v[40:43], v[242:243], v[88:89], 0\n\t" \
  "v_mfma_f32_4x4x4_16b_bf16 v[44:47], v[242:243], v[90:91], 0\n\t" \
  "v_fma_mix_f32 v229, |v149|, v229, v229 op_sel:[0,1,0] op_sel_hi:[0,1,1] clamp\n\t" \
  "v_fma_mix_f32 v228, |v148|, v228, v228 op_sel:[0,1,0] op_sel_hi:[0,1,1] clamp\n\t" \
  "ds_read_b32 v220, v220 offset:10016\n\t" \
  "ds_read_b32 v223, v223 offset:10016\n\t" \
  "v_mfma_f32_4x4x4_16b_bf16 v[72:75], v[242:243], v[48:49], 0\n\t" \
  "v_mfma_f32_4x4x4_16b_bf16 v[76:79], v[242:243], v[50:51], 0\n\t" \
  "v_fma_mix_f32 v231, |v151|, v231, v231 op_sel:[0,1,0] op_sel_hi:[0,1,1] clamp\n\t" \
  "v_fma_mix_f32 v230, |v150|, v230, v230 op_sel:[0,1,0] op_sel_hi:[0,1,1] clamp\n\t" \
  "ds_read_b32 v222, v222 offset:10016\n\t" \
  "s_waitcnt lgkmcnt(4)\n\t" \
  "v_mfma_f32_4x4x4_16b_bf16 v[80:83], v[242:243], v[56:57], 0\n\t" \
  "v_mfma_f32_4x4x4_16b_bf16 v[84:87], v[242:243], v[58:59], 0\n\t" \
  "v_or_b32 v156, v232, v233\n\t" \
  "v_or_b32 v157, v234, v235\n\t" \
  "ds_read_u16_d16_hi v225, v225 offset:10528\n\t" \
  "v_mfma_f32_4x4x4_16b_bf16 v[88:91], v[242:243], v[236:237], 0\n\t" \
  "v_mfma_f32_4x4x4_16b_bf16 v[92:95], v[242:243], v[238:239], 0\n\t" \
  "v_fma_f32 v149, |v149|, v229, v149 mul:4\n\t" \
  "v_fma_f32 v148, |v148|, v228, v148 mul:4\n\t" \
  "ds_read_u16 v224, v224 offset:10528\n\t" \
  "v_mfma_f32_16x16x32_bf16 v[176:179], v[208:211], v[96:99], 0\n\t" \
  "v_fma_f32 v151, |v151|, v231, v151 mul:4\n\t" \
  "v_fma_f32 v150, |v150|, v230, v150 mul:4\n\t" \
  "ds_read_u16_d16_hi v227, v227 offset:10528\n\t" \
  "v_mfma_f32_16x16x32_bf16 v[184:187], v[208:211], v[100:103], 0\n\t" \
  "v_cvt_pk_bf16_f32 v148, v148, v149\n\t" \
  "v_cvt_pk_bf16_f32 v149, v150, v151\n\t" \
  "ds_read_u16 v226, v226 offset:10528\n\t" \
  "v_mfma_f32_16x16x32_bf16 v[192:195], v[208:211], v[104:107], 0\n\t" \
  "v_cvt_pknorm_i16_f32 v228, |v164|, |v165|\n\t" \
  "v_cvt_pknorm_i16_f32 v230, |v166|, |v167|\n\t" \
  "v_mfma_f32_16x16x32_bf16 v[200:203], v[208:211], v[108:111], 0\n\t" \
  "v_lshlrev_b32_sdwa v229, 2, v228 dst_sel:DWORD dst_unused:UNUSED_PAD src0_sel:DWORD src1_sel:BYTE_3\n\t" \
  "v_lshlrev_b32_sdwa v228, 2, v228 dst_sel:DWORD dst_unused:UNUSED_PAD src0_sel:DWORD src1_sel:BYTE_1\n\t" \
  "ds_read_b64_tr_b16 v[208:209], v248 offset:49704\n\t" \
  "v_mfma_f32_16x16x32_bf16 v[176:179], v[212:215], v[112:115], v[176:179]\n\t" \
  "v_lshlrev_b32_sdwa v231, 2, v230 dst_sel:DWORD dst_unused:UNUSED_PAD src0_sel:DWORD src1_sel:BYTE_3\n\t" \
  "v_lshlrev_b32_sdwa v230, 2, v230 dst_sel:DWORD dst_unused:UNUSED_PAD src0_sel:DWORD src1_sel:BYTE_1\n\t" \
  "ds_read_b64_tr_b16 v[210:211], v248 offset:50728\n\t" \
  "v_mfma_f32_16x16x32_bf16 v[184:187], v[212:215], v[116:119], v[184:187]\n\t" \
  "v_lshlrev_b32_sdwa v233, 1, v173 dst_sel:DWORD dst_unused:UNUSED_PAD src0_sel:DWORD src1_sel:WORD_1\n\t" \
  "v_lshlrev_b32_sdwa v232, 1, v172 dst_sel:DWORD dst_unused:UNUSED_PAD src0_sel:DWORD src1_sel:WORD_1\n\t" \
  "ds_read_b32 v229, v229 offset:10016\n\t" \
  "v_mfma_f32_16x16x32_bf16 v[192:195], v[212:215], v[120:123], v[192:195]\n\t" \
  "v_lshlrev_b32_sdwa v235, 1, v175 dst_sel:DWORD dst_unused:UNUSED_PAD src0_sel:DWORD src1_sel:WORD_1\n\t" \
  "v_lshlrev_b32_sdwa v234, 1, v174 dst_sel:DWORD dst_unused:UNUSED_PAD src0_sel:DWORD src1_sel:WORD_1\n\t" \
  "ds_read_b32 v228, v228 offset:10016\n\t" \
  "s_waitcnt lgkmcnt(8)\n\t" \
  "v_mfma_f32_16x16x32_bf16 v[200:203], v[212:215], v[124:127], v[200:203]\n\t" \
  "v_fma_mix_f32 v221, |v163|, v221, v221 op_sel:[0,1,0] op_sel_hi:[0,1,1] clamp\n\t" \
  "v_fma_mix_f32 v220, |v162|, v220, v220 op_sel:[0,1,0] op_sel_hi:[0,1,1] clamp\n\t" \
  "ds_read_b64_tr_b16 v[212:213], v248 offset:52776\n\t" \
  "ds_read_b32 v231, v231 offset:10016\n\t" \
  "v_mfma_f32_16x16x32_bf16 v[176:179], v[216:219], v[128:131], v[176:179]\n\t" \
  "v_fma_mix_f32 v223, |v161|, v223, v223 op_sel:[0,1,0] op_sel_hi:[0,1,1] clamp\n\t" \
  "v_fma_mix_f32 v222, |v160|, v222, v222 op_sel:[0,1,0] op_sel_hi:[0,1,1] clamp\n\t" \
  "s_waitcnt lgkmcnt(6)\n\t" \
  "ds_read_b64_tr_b16 v[214:215], v248 offset:53800\n\t" \
  "ds_read_b32 v230, v230 offset:10016\n\t" \
  "v_mfma_f32_16x16x32_bf16 v[184:187], v[216:219], v[132:135], v[184:187]\n\t" \
  "v_or_b32 v171, v224, v225\n\t" \
  "v_or_b32 v170, v226, v227\n\t" \
  "ds_read_u16_d16_hi v233, v233 offset:10528\n\t" \
  "v_mfma_f32_16x16x32_bf16 v[192:195], v[216:219], v[136:139], v[192:195]\n\t" \
  "v_fma_f32 v163, |v163|, v221, v163 mul:4\n\t" \
  "v_fma_f32 v162, |v162|, v220, v162 mul:4\n\t" \
  "ds_read_u16 v232, v232 offset:10528\n\t" \
  "v_mfma_f32_16x16x32_bf16 v[200:203], v[216:219], v[140:143], v[200:203]\n\t" \
  "v_fma_f32 v161, |v161|, v223, v161 mul:4\n\t" \
  "v_fma_f32 v160, |v160|, v222, v160 mul:4\n\t" \
  "ds_read_b64_tr_b16 v[216:217], v248 offset:51752\n\t" \
  "s_waitcnt lgkmcnt(9)\n\t" \
  "v_mfma_f32_16x16x32_bf16 v[180:183], v[208:211], v[96:99], 0\n\t" \
  "v_cvt_pk_bf16_f32 v163, v162, v163\n\t" \
  "v_cvt_pk_bf16_f32 v162, v160, v161\n\t" \
  "s_waitcnt vmcnt(3)\n\t" \
  "ds_read_b64_tr_b16 v[218:219], v248 offset:54824\n\t" \
  "v_mfma_f32_16x16x32_bf16 v[188:191], v[208:211], v[100:103], 0\n\t" \
  "v_cvt_pknorm_i16_f32 v220, |v178|, |v179|\n\t" \
  "v_cvt_pknorm_i16_f32 v222, |v176|, |v177|\n\t" \
  "s_mov_b64 exec, 3\n\t" \
  "ds_add_rtn_u32 v246, v251, v250 offset:47144\n\t" \
  "v_mfma_f32_16x16x32_bf16 v[196:199], v[208:211], v[104:107], 0\n\t" \
  "s_mov_b64 exec, -1\n\t" \
  "v_lshlrev_b32_sdwa v221, 2, v220 dst_sel:DWORD dst_unused:UNUSED_PAD src0_sel:DWORD src1_sel:BYTE_3\n\t" \
  "v_lshlrev_b32_sdwa v220, 2, v220 dst_sel:DWORD dst_unused:UNUSED_PAD src0_sel:DWORD src1_sel:BYTE_1\n\t" \
  "ds_read_u16_d16_hi v235, v235 offset:10528\n\t" \
  "v_mfma_f32_16x16x32_bf16 v[204:207], v[208:211], v[108:111], 0\n\t" \
  "s_waitcnt lgkmcnt(1)\n\t" \
  "v_lshlrev_b32_sdwa v223, 2, v222 dst_sel:DWORD dst_unused:UNUSED_PAD src0_sel:DWORD src1_sel:BYTE_3\n\t" \
  "v_readfirstlane_b32 s4, v246\n\t" \
  "ds_read_u16 v234, v234 offset:10528\n\t" \
  "v_mfma_f32_16x16x32_bf16 v[180:183], v[212:215], v[112:115], v[180:183]\n\t" \
  "s_cmpk_lt_u32 s4, 6144\n\t" \
  "v_lshlrev_b32_sdwa v222, 2, v222 dst_sel:DWORD dst_unused:UNUSED_PAD src0_sel:DWORD src1_sel:BYTE_1\n\t" \
  "s_cbranch_scc1 .Lcnx_w1_2\n.Lcnx_g1_2:\n\t" \
  "ds_read_b32 v221, v221 offset:10016\n\t" \
  "v_mfma_f32_16x16x32_bf16 v[188:191], v[212:215], v[116:119], v[188:191]\n\t" \
  "v_lshlrev_b32_sdwa v225, 1, v187 dst_sel:DWORD dst_unused:UNUSED_PAD src0_sel:DWORD src1_sel:WORD_1\n\t" \
  "v_lshlrev_b32_sdwa v224, 1, v186 dst_sel:DWORD dst_unused:UNUSED_PAD src0_sel:DWORD src1_sel:WORD_1\n\t" \
  "ds_read_b64_tr_b16 v[208:209], v249 offset:24600\n\t" \
  "v_mfma_f32_16x16x32_bf16 v[196:199], v[212:215], v[120:123], v[196:199]\n\t" \
  "v_lshlrev_b32_sdwa v227, 1, v185 dst_sel:DWORD dst_unused:UNUSED_PAD src0_sel:DWORD src1_sel:WORD_1\n\t" \
  "v_lshlrev_b32_sdwa v226, 1, v184 dst_sel:DWORD dst_unused:UNUSED_PAD src0_sel:DWORD src1_sel:WORD_1\n\t" \
  "ds_read_b64_tr_b16 v[210:211], v249 offset:26136\n\t" \
  "v_mfma_f32_16x16x32_bf16 v[204:207], v[212:215], v[124:127], v[204:207]\n\t" \
  "v_fma_mix_f32 v229, |v165|, v229, v229 op_sel:[0,1,0] op_sel_hi:[0,1,1] clamp\n\t" \
  "v_fma_mix_f32 v228, |v164|, v228, v228 op_sel:[0,1,0] op_sel_hi:[0,1,1] clamp\n\t" \
  "ds_read_b64_tr_b16 v[212:213], v249 offset:24608\n\t" \
  "ds_read_b32 v220, v220 offset:10016\n\t" \
  "v_mfma_f32_16x16x32_bf16 v[180:183], v[216:219], v[128:131], v[180:183]\n\t" \
  "s_mov_b32 m0, s46\n\t" \
  "v_fma_mix_f32 v231, |v167|, v231, v231 op_sel:[0,1,0] op_sel_hi:[0,1,1] clamp\n\t" \
  "v_fma_mix_f32 v230, |v166|, v230, v230 op_sel:[0,1,0] op_sel_hi:[0,1,1] clamp\n\t" \
  "s_waitcnt lgkmcnt(5)\n\t" \
  "s_mov_b32 exec_hi, 0xffff\n\t" \
  "buffer_load_dwordx4 v252, s[16:19], s33 offen lds\n\t" \
  "v_mfma_f32_16x16x32_bf16 v[188:191], v[216:219], v[132:135], v[188:191]\n\t" \
  "s_mov_b32 exec_hi, -1\n\t" \
  "v_or_b32 v172, v232, v233\n\t" \
  "v_or_b32 v173, v234, v235\n\t" \
  "v_fma_f32 v165, |v165|, v229, v165 mul:4\n\t" \
  "s_add_u32 s33, s33, s32\n\t" \
  "ds_read_b64_tr_b16 v[214:215], v249 offset:26144\n\t" \
  "ds_read_b32 v223, v223 offset:10016\n\t" \
  "v_mfma_f32_16x16x32_bf16 v[196:199], v[216:219], v[136:139], v[196:199]\n\t" \
  "v_fma_f32 v164, |v164|, v228, v164 mul:4\n\t" \
  "v_fma_f32 v167, |v167|, v231, v167 mul:4\n\t" \
  "ds_read_b32 v222, v222 offset:10016\n\t" \
  "v_mfma_f32_16x16x32_bf16 v[204:207], v[216:219], v[140:143], v[204:207]\n\t" \
  "v_fma_f32 v166, |v166|, v230, v166 mul:4\n\t" \
  "v_cvt_pk_bf16_f32 v164, v164, v165\n\t" \
  "v_cvt_pk_bf16_f32 v165, v166, v167\n\t" \
  "s_waitcnt lgkmcnt(5)\n\t" \
  "ds_read_b64_tr_b16 v[216:217], v249 offset:24664\n\t" \
  "ds_read_u16_d16_hi v225, v225 offset:10528\n\t" \
  "v_mfma_f32_16x16x32_bf16 v[0:3], v[208:211], v[146:149], v[0:3]\n\t" \
  "v_cvt_pknorm_i16_f32 v228, |v194|, |v195|\n\t" \
  "v_cvt_pknorm_i16_f32 v230, |v192|, |v193|\n\t" \
  "ds_read_b64_tr_b16 v[218:219], v249 offset:26200\n\t" \
  "v_mfma_f32_16x16x32_bf16 v[24:27], v[208:211], v[154:157], v[24:27]\n\t" \
  "v_lshlrev_b32_sdwa v229, 2, v228 dst_sel:DWORD dst_unused:UNUSED_PAD src0_sel:DWORD src1_sel:BYTE_3\n\t" \
  "v_lshlrev_b32_sdwa v228, 2, v228 dst_sel:DWORD dst_unused:UNUSED_PAD src0_sel:DWORD src1_sel:BYTE_1\n\t" \
  "ds_read_u16 v224, v224 offset:10528\n\t" \
  "v_mfma_f32_16x16x32_bf16 v[48:51], v[208:211], v[162:165], 0\n\t" \
  "v_lshlrev_b32_sdwa v231, 2, v230 dst_sel:DWORD dst_unused:UNUSED_PAD src0_sel:DWORD src1_sel:BYTE_3\n\t" \
  "v_lshlrev_b32_sdwa v230, 2, v230 dst_sel:DWORD dst_unused:UNUSED_PAD src0_sel:DWORD src1_sel:BYTE_1\n\t" \
  "ds_read_u16_d16_hi v227, v227 offset:10528\n\t" \
  "v_mfma_f32_16x16x32_bf16 v[72:75], v[208:211], v[170:173], v[72:75]\n\t" \
  "s_add_u32 m0, s46, s35\n\t" \
  "v_lshlrev_b32_sdwa v233, 1, v203 dst_sel:DWORD dst_unused:UNUSED_PAD src0_sel:DWORD src1_sel:WORD_1\n\t" \
  "v_lshlrev_b32_sdwa v232, 1, v202 dst_sel:DWORD dst_unused:UNUSED_PAD src0_sel:DWORD src1_sel:WORD_1\n\t" \
  "s_waitcnt lgkmcnt(7)\n\t" \
  "s_mov_b32 exec_hi, 0xffff\n\t" \
  "buffer_load_dwordx4 v252, s[16:19], s34 offen lds\n\t" \
  "v_mfma_f32_16x16x32_bf16 v[4:7], v[212:215], v[146:149], v[4:7]\n\t" \
  "s_mov_b32 exec_hi, -1\n\t" \
  "v_lshlrev_b32_sdwa v235, 1, v201 dst_sel:DWORD dst_unused:UNUSED_PAD src0_sel:DWORD src1_sel:WORD_1\n\t" \
  "s_add_u32 s34, s34, s32\n\t" \
  "ds_read_b64_tr_b16 v[208:209], v249 offset:24672\n\t" \
  "ds_read_u16 v226, v226 offset:10528\n\t" \
  "v_mfma_f32_16x16x32_bf16 v[28:31], v[212:215], v[154:157], v[28:31]\n\t" \
  "v_lshlrev_b32_sdwa v234, 1, v200 dst_sel:DWORD dst_unused:UNUSED_PAD src0_sel:DWORD src1_sel:WORD_1\n\t" \
  "s_waitcnt lgkmcnt(7)\n\t" \
  "ds_read_b64_tr_b16 v[210:211], v249 offset:26208\n\t" \
  "ds_read_b32 v229, v229 offset:10016\n\t" \
  "v_mfma_f32_16x16x32_bf16 v[52:55], v[212:215], v[162:165], 0\n\t" \
  "v_fma_mix_f32 v221, |v179|, v221, v221 op_sel:[0,1,0] op_sel_hi:[0,1,1] clamp\n\t" \
  "v_fma_mix_f32 v220, |v178|, v220, v220 op_sel:[0,1,0] op_sel_hi:[0,1,1] clamp\n\t" \
  "ds_read_b32 v228, v228 offset:10016\n\t" \
  "ds_read_b32 v231, v231 offset:10016\n\t" \
  "v_mfma_f32_16x16x32_bf16 v[76:79], v[212:215], v[170:173], v[76:79]\n\t" \
  "v_fma_mix_f32 v223, |v177|, v223, v223 op_sel:[0,1,0] op_sel_hi:[0,1,1] clamp\n\t" \
  "v_fma_mix_f32 v222, |v176|, v222, v222 op_sel:[0,1,0] op_sel_hi:[0,1,1] clamp\n\t" \
  "s_waitcnt lgkmcnt(4)\n\t" \
  "ds_read_b64_tr_b16 v[212:213], v249 offset:24728\n\t" \
  "ds_read_b32 v230, v230 offset:10016\n\t" \
  "v_mfma_f32_16x16x32_bf16 v[8:11], v[216:219], v[146:149], v[8:11]\n\t" \
  "v_or_b32 v187, v224, v225\n\t" \
  "v_or_b32 v186, v226, v227\n\t" \
  "ds_read_b64_tr_b16 v[214:215], v249 offset:26264\n\t" \
  "v_mfma_f32_16x16x32_bf16 v[32:35], v[216:219], v[154:157], v[32:35]\n\t" \
  "v_fma_f32 v179, |v179|, v221, v179 mul:4\n\t" \
  "v_fma_f32 v178, |v178|, v220, v178 mul:4\n\t" \
  "ds_read_u16_d16_hi v233, v233 offset:10528\n\t" \
  "v_mfma_f32_16x16x32_bf16 v[56:59], v[216:219], v[162:165], 0\n\t" \
  "v_fma_f32 v177, |v177|, v223, v177 mul:4\n\t" \
  "v_fma_f32 v176, |v176|, v222, v176 mul:4\n\t" \
  "ds_read_u16 v232, v232 offset:10528\n\t" \
  "v_mfma_f32_16x16x32_bf16 v[80:83], v[216:219], v[170:173], v[80:83]\n\t" \
  "v_cvt_pk_bf16_f32 v179, v178, v179\n\t" \
  "v_cvt_pk_bf16_f32 v178, v176, v177\n\t" \
  "ds_read_b64_tr_b16 v[216:217], v249 offset:24736\n\t" \
  "s_waitcnt lgkmcnt(9)\n\t" \
  "v_mfma_f32_16x16x32_bf16 v[12:15], v[208:211], v[146:149], v[12:15]\n\t" \
  "v_cvt_pknorm_i16_f32 v220, |v180|, |v181|\n\t" \
  "v_cvt_pknorm_i16_f32 v222, |v182|, |v183|\n\t" \
  "ds_read_b64_tr_b16 v[218:219], v249 offset:26272\n\t" \
  "v_mfma_f32_16x16x32_bf16 v[36:39], v[208:211], v[154:157], v[36:39]\n\t" \
  "v_lshlrev_b32_sdwa v221, 2, v220 dst_sel:DWORD dst_unused:UNUSED_PAD src0_sel:DWORD src1_sel:BYTE_3\n\t" \
  "v_lshlrev_b32_sdwa v220, 2, v220 dst_sel:DWORD dst_unused:UNUSED_PAD src0_sel:DWORD src1_sel:BYTE_1\n\t" \
  "ds_read_u16_d16_hi v235, v235 offset:10528\n\t" \
  "v_mfma_f32_16x16x32_bf16 v[60:63], v[208:211], v[162:165], 0\n\t" \
  "v_lshlrev_b32_sdwa v223, 2, v222 dst_sel:DWORD dst_unused:UNUSED_PAD src0_sel:DWORD src1_sel:BYTE_3\n\t" \
  "v_lshlrev_b32_sdwa v222, 2, v222 dst_sel:DWORD dst_unused:UNUSED_PAD src0_sel:DWORD src1_sel:BYTE_1\n\t" \
  "ds_read_u16 v234, v234 offset:10528\n\t" \
  "v_mfma_f32_16x16x32_bf16 v[84:87], v[208:211], v[170:173], v[84:87]\n\t" \
  "v_lshlrev_b32_sdwa v225, 1, v189 dst_sel:DWORD dst_unused:UNUSED_PAD src0_sel:DWORD src1_sel:WORD_1\n\t" \
  "v_lshlrev_b32_sdwa v224, 1, v188 dst_sel:DWORD dst_unused:UNUSED_PAD src0_sel:DWORD src1_sel:WORD_1\n\t" \
  "ds_read_b64_tr_b16 v[208:209], v248 offset:36904\n\t" \
  "s_waitcnt lgkmcnt(7)\n\t" \
  "v_mfma_f32_16x16x32_bf16 v[16:19], v[212:215], v[146:149], v[16:19]\n\t" \
  "v_lshlrev_b32_sdwa v227, 1, v191 dst_sel:DWORD dst_unused:UNUSED_PAD src0_sel:DWORD src1_sel:WORD_1\n\t" \
  "ds_read_b64_tr_b16 v[210:211], v248 offset:37928\n\t" \
  "ds_read_b32 v221, v221 offset:10016\n\t" \
  "v_mfma_f32_16x16x32_bf16 v[40:43], v[212:215], v[154:157], v[40:43]\n\t" \
  "v_lshlrev_b32_sdwa v226, 1, v190 dst_sel:DWORD dst_unused:UNUSED_PAD src0_sel:DWORD src1_sel:WORD_1\n\t" \
  "v_fma_mix_f32 v229, |v195|, v229, v229 op_sel:[0,1,0] op_sel_hi:[0,1,1] clamp\n\t" \
  "ds_read_b32 v220, v220 offset:10016\n\t" \
  "v_mfma_f32_16x16x32_bf16 v[64:67], v[212:215], v[162:165], 0\n\t" \
  "v_fma_mix_f32 v228, |v194|, v228, v228 op_sel:[0,1,0] op_sel_hi:[0,1,1] clamp\n\t" \
  "ds_read_b32 v223, v223 offset:10016\n\t" \
  "ds_read_b32 v222, v222 offset:10016\n\t" \
  "v_mfma_f32_16x16x32_bf16 v[88:91], v[212:215], v[170:173], v[88:91]\n\t" \
  "v_fma_mix_f32 v231, |v193|, v231, v231 op_sel:[0,1,0] op_sel_hi:[0,1,1] clamp\n\t" \
  "v_fma_mix_f32 v230, |v192|, v230, v230 op_sel:[0,1,0] op_sel_hi:[0,1,1] clamp\n\t" \
  "s_waitcnt lgkmcnt(6)\n\t" \
  "ds_read_b64_tr_b16 v[212:213], v248 offset:39976\n\t" \
  "ds_read_u16_d16_hi v225, v225 offset:10528\n\t" \
  "v_mfma_f32_16x16x32_bf16 v[20:23], v[216:219], v[146:149], v[20:23]\n\t" \
  "v_or_b32 v203, v232, v233\n\t" \
  "v_or_b32 v202, v234, v235\n\t" \
  "ds_read_b64_tr_b16 v[214:215], v248 offset:41000\n\t" \
  "v_mfma_f32_16x16x32_bf16 v[44:47], v[216:219], v[154:157], v[44:47]\n\t" \
  "v_fma_f32 v195, |v195|, v229, v195 mul:4\n\t" \
  "v_fma_f32 v194, |v194|, v228, v194 mul:4\n\t" \
  "ds_read_u16 v224, v224 offset:10528\n\t" \
  "v_mfma_f32_16x16x32_bf16 v[68:71], v[216:219], v[162:165], 0\n\t" \
  "v_fma_f32 v193, |v193|, v231, v193 mul:4\n\t" \
  "v_fma_f32 v192, |v192|, v230, v192 mul:4\n\t" \
  "ds_read_u16_d16_hi v227, v227 offset:10528\n\t" \
  "v_mfma_f32_16x16x32_bf16 v[92:95], v[216:219], v[170:173], v[92:95]\n\t" \
  "v_cvt_pk_bf16_f32 v195, v194, v195\n\t" \
  "v_cvt_pk_bf16_f32 v194, v192, v193\n\t" \
  "ds_read_b64_tr_b16 v[216:217], v248 offset:38952\n\t" \
  "s_waitcnt lgkmcnt(10)\n\t" \
  "v_mfma_f32_16x16x32_bf16 v[144:147], v[208:211], v[96:99], 0\n\t" \
  "v_cvt_pknorm_i16_f32 v228, |v196|, |v197|\n\t" \
  "v_cvt_pknorm_i16_f32 v230, |v198|, |v199|\n\t" \
  "ds_read_b64_tr_b16 v[218:219], v248 offset:42024\n\t" \
  "v_mfma_f32_16x16x32_bf16 v[152:155], v[208:211], v[100:103], 0\n\t" \
  "v_lshlrev_b32_sdwa v229, 2, v228 dst_sel:DWORD dst_unused:UNUSED_PAD src0_sel:DWORD src1_sel:BYTE_3\n\t" \
  "v_lshlrev_b32_sdwa v228, 2, v228 dst_sel:DWORD dst_unused:UNUSED_PAD src0_sel:DWORD src1_sel:BYTE_1\n\t" \
  "ds_read_u16 v226, v226 offset:10528\n\t" \
  "v_mfma_f32_16x16x32_bf16 v[160:163], v[208:211], v[104:107], 0\n\t" \
  "v_lshlrev_b32_sdwa v231, 2, v230 dst_sel:DWORD dst_unused:UNUSED_PAD src0_sel:DWORD src1_sel:BYTE_3\n\t" \
  "v_lshlrev_b32_sdwa v230, 2, v230 dst_sel:DWORD dst_unused:UNUSED_PAD src0_sel:DWORD src1_sel:BYTE_1\n\t" \
  "ds_read_b32 v229, v229 offset:10016\n\t" \
  "v_mfma_f32_16x16x32_bf16 v[168:171], v[208:211], v[108:111], 0\n\t" \
  "v_lshlrev_b32_sdwa v233, 1, v205 dst_sel:DWORD dst_unused:UNUSED_PAD src0_sel:DWORD src1_sel:WORD_1\n\t" \
  "v_lshlrev_b32_sdwa v232, 1, v204 dst_sel:DWORD dst_unused:UNUSED_PAD src0_sel:DWORD src1_sel:WORD_1\n\t" \
  "ds_read_b64_tr_b16 v[208:209], v248 offset:37416\n\t" \
  "s_waitcnt lgkmcnt(7)\n\t" \
  "v_mfma_f32_16x16x32_bf16 v[144:147], v[212:215], v[112:115], v[144:147]\n\t" \
  "v_lshlrev_b32_sdwa v235, 1, v207 dst_sel:DWORD dst_unused:UNUSED_PAD src0_sel:DWORD src1_sel:WORD_1\n\t" \
  "v_lshlrev_b32_sdwa v234, 1, v206 dst_sel:DWORD dst_unused:UNUSED_PAD src0_sel:DWORD src1_sel:WORD_1\n\t" \
  "ds_read_b64_tr_b16 v[210:211], v248 offset:38440\n\t" \
  "v_mfma_f32_16x16x32_bf16 v[152:155], v[212:215], v[116:119], v[152:155]\n\t" \
  "v_fma_mix_f32 v221, |v181|, v221, v221 op_sel:[0,1,0] op_sel_hi:[0,1,1] clamp\n\t" \
  "ds_read_b32 v228, v228 offset:10016\n\t" \
  "ds_read_b32 v231, v231 offset:10016\n\t" \
  "v_mfma_f32_16x16x32_bf16 v[160:163], v[212:215], v[120:123], v[160:163]\n\t" \
  "v_fma_mix_f32 v220, |v180|, v220, v220 op_sel:[0,1,0] op_sel_hi:[0,1,1] clamp\n\t" \
  "v_fma_mix_f32 v223, |v183|, v223, v223 op_sel:[0,1,0] op_sel_hi:[0,1,1] clamp\n\t" \
  "ds_read_b32 v230, v230 offset:10016\n\t" \
  "v_mfma_f32_16x16x32_bf16 v[168:171], v[212:215], v[124:127], v[168:171]\n\t" \
  "v_fma_mix_f32 v222, |v182|, v222, v222 op_sel:[0,1,0] op_sel_hi:[0,1,1] clamp\n\t" \
  "s_waitcnt lgkmcnt(6)\n\t" \
  "ds_read_b64_tr_b16 v[212:213], v248 offset:40488\n\t" \
  "ds_read_u16_d16_hi v233, v233 offset:10528\n\t" \
  "v_mfma_f32_16x16x32_bf16 v[144:147], v[216:219], v[128:131], v[144:147]\n\t" \
  "v_or_b32 v188, v224, v225\n\t" \
  "v_or_b32 v189, v226, v227\n\t" \
  "ds_read_b64_tr_b16 v[214:215], v248 offset:41512\n\t" \
  "v_mfma_f32_16x16x32_bf16 v[152:155], v[216:219], v[132:135], v[152:155]\n\t" \
  "v_fma_f32 v181, |v181|, v221, v181 mul:4\n\t" \
  "v_fma_f32 v180, |v180|, v220, v180 mul:4\n\t" \
  "ds_read_u16 v232, v232 offset:10528\n\t" \
  "v_mfma_f32_16x16x32_bf16 v[160:163], v[216:219], v[136:139], v[160:163]\n\t" \
  "v_fma_f32 v183, |v183|, v223, v183 mul:4\n\t" \
  "v_fma_f32 v182, |v182|, v222, v182 mul:4\n\t" \
  "ds_read_u16_d16_hi v235, v235 offset:10528\n\t" \
  "v_mfma_f32_16x16x32_bf16 v[168:171], v[216:219], v[140:143], v[168:171]\n\t" \
  "v_cvt_pk_bf16_f32 v180, v180, v181\n\t" \
  "v_cvt_pk_bf16_f32 v181, v182, v183\n\t" \
  "ds_read_b64_tr_b16 v[216:217], v248 offset:39464\n\t" \
  "s_waitcnt lgkmcnt(9)\n\t" \
  "v_mfma_f32_16x16x32_bf16 v[148:151], v[208:211], v[96:99], 0\n\t" \
  "v_cvt_pknorm_i16_f32 v220, |v146|, |v147|\n\t" \
  "v_cvt_pknorm_i16_f32 v222, |v144|, |v145|\n\t" \
  "s_waitcnt vmcnt(0)\n\t" \
  "ds_read_b64_tr_b16 v[218:219], v248 offset:42536\n\t" \
  "v_mfma_f32_16x16x32_bf16 v[156:159], v[208:211], v[100:103], 0\n\t" \
  "v_lshlrev_b32_sdwa v221, 2, v220 dst_sel:DWORD dst_unused:UNUSED_PAD src0_sel:DWORD src1_sel:BYTE_3\n\t" \
  "v_lshlrev_b32_sdwa v220, 2, v220 dst_sel:DWORD dst_unused:UNUSED_PAD src0_sel:DWORD src1_sel:BYTE_1\n\t" \
  "s_mov_b64 exec, 3\n\t" \
  "ds_add_rtn_u32 v246, v251, v250 offset:47152\n\t" \
  "v_mfma_f32_16x16x32_bf16 v[164:167], v[208:211], v[104:107], 0\n\t" \
  "s_mov_b64 exec, -1\n\t" \
  "v_lshlrev_b32_sdwa v223, 2, v222 dst_sel:DWORD dst_unused:UNUSED_PAD src0_sel:DWORD src1_sel:BYTE_3\n\t" \
  "v_lshlrev_b32_sdwa v222, 2, v222 dst_sel:DWORD dst_unused:UNUSED_PAD src0_sel:DWORD src1_sel:BYTE_1\n\t" \
  "ds_read_u16 v234, v234 offset:10528\n\t" \
  "v_mfma_f32_16x16x32_bf16 v[172:175], v[208:211], v[108:111], 0\n\t" \
  "s_waitcnt lgkmcnt(1)\n\t" \
  "v_lshlrev_b32_sdwa v225, 1, v155 dst_sel:DWORD dst_unused:UNUSED_PAD src0_sel:DWORD src1_sel:WORD_1\n\t" \
  "v_readfirstlane_b32 s4, v246\n\t" \
  "ds_read_b32 v221, v221 offset:10016\n\t" \
  "v_mfma_f32_16x16x32_bf16 v[148:151], v[212:215], v[112:115], v[148:151]\n\t" \
  "s_cmpk_lt_u32 s4, 6144\n\t" \
  "v_lshlrev_b32_sdwa v224, 1, v154 dst_sel:DWORD dst_unused:UNUSED_PAD src0_sel:DWORD src1_sel:WORD_1\n\t" \
  "s_cbranch_scc1 .Lcnx_w1_3\n.Lcnx_g1_3:\n\t" \
  "ds_read_b32 v220, v220 offset:10016\n\t" \
  "v_mfma_f32_16x16x32_bf16 v[156:159], v[212:215], v[116:119], v[156:159]\n\t" \
  "v_lshlrev_b32_sdwa v227, 1, v153 dst_sel:DWORD dst_unused:UNUSED_PAD src0_sel:DWORD src1_sel:WORD_1\n\t" \
  "v_lshlrev_b32_sdwa v226, 1, v152 dst_sel:DWORD dst_unused:UNUSED_PAD src0_sel:DWORD src1_sel:WORD_1\n\t" \
  "ds_read_b64_tr_b16 v[208:209], v249 offset:30752\n\t" \
  "v_mfma_f32_16x16x32_bf16 v[164:167], v[212:215], v[120:123], v[164:167]\n\t" \
  "v_fma_mix_f32 v229, |v197|, v229, v229 op_sel:[0,1,0] op_sel_hi:[0,1,1] clamp\n\t" \
  "v_fma_mix_f32 v228, |v196|, v228, v228 op_sel:[0,1,0] op_sel_hi:[0,1,1] clamp\n\t" \
  "ds_read_b64_tr_b16 v[210:211], v249 offset:32288\n\t" \
  "ds_read_b32 v223, v223 offset:10016\n\t" \
  "v_mfma_f32_16x16x32_bf16 v[172:175], v[212:215], v[124:127], v[172:175]\n\t" \
  "v_fma_mix_f32 v231, |v199|, v231, v231 op_sel:[0,1,0] op_sel_hi:[0,1,1] clamp\n\t" \
  "v_fma_mix_f32 v230, |v198|, v230, v230 op_sel:[0,1,0] op_sel_hi:[0,1,1] clamp\n\t" \
  "s_waitcnt lgkmcnt(5)\n\t" \
  "ds_read_b64_tr_b16 v[212:213], v249 offset:30760\n\t" \
  "ds_read_b32 v222, v222 offset:10016\n\t" \
  "v_mfma_f32_16x16x32_bf16 v[148:151], v[216:219], v[128:131], v[148:151]\n\t" \
  "s_mov_b32 m0, s47\n\t" \
  "v_or_b32 v204, v232, v233\n\t" \
  "v_or_b32 v205, v234, v235\n\t" \
  "s_mov_b32 exec_hi, 0xffff\n\t" \
  "buffer_load_dwordx4 v252, s[16:19], s33 offen lds\n\t" \
  "v_mfma_f32_16x16x32_bf16 v[156:159], v[216:219], v[132:135], v[156:159]\n\t" \
  "s_mov_b32 exec_hi, -1\n\t" \
  "v_fma_f32 v197, |v197|, v229, v197 mul:4\n\t" \
  "v_fma_f32 v196, |v196|, v228, v196 mul:4\n\t" \
  "s_add_u32 s33, s33, s32\n\t" \
  "ds_read_b64_tr_b16 v[214:215], v249 offset:32296\n\t" \
  "v_mfma_f32_16x16x32_bf16 v[164:167], v[216:219], v[136:139], v[164:167]\n\t" \
  "v_fma_f32 v199, |v199|, v231, v199 mul:4\n\t" \
  "v_fma_f32 v198, |v198|, v230, v198 mul:4\n\t" \
  "ds_read_u16_d16_hi v225, v225 offset:10528\n\t" \
  "v_mfma_f32_16x16x32_bf16 v[172:175], v[216:219], v[140:143], v[172:175]\n\t" \
  "v_cvt_pk_bf16_f32 v196, v196, v197\n\t" \
  "v_cvt_pk_bf16_f32 v197, v198, v199\n\t" \
  "ds_read_b64_tr_b16 v[216:217], v249 offset:30816\n\t" \
  "s_waitcnt lgkmcnt(6)\n\t" \
  "v_mfma_f32_16x16x32_bf16 v[0:3], v[208:211], v[178:181], v[0:3]\n\t" \
  "v_cvt_pknorm_i16_f32 v228, |v162|, |v163|\n\t" \
  "v_cvt_pknorm_i16_f32 v230, |v160|, |v161|\n\t" \
  "ds_read_b64_tr_b16 v[218:219], v249 offset:32352\n\t" \
  "v_mfma_f32_16x16x32_bf16 v[24:27], v[208:211], v[186:189], v[24:27]\n\t" \
  "v_lshlrev_b32_sdwa v229, 2, v228 dst_sel:DWORD dst_unused:UNUSED_PAD src0_sel:DWORD src1_sel:BYTE_3\n\t" \
  "v_lshlrev_b32_sdwa v228, 2, v228 dst_sel:DWORD dst_unused:UNUSED_PAD src0_sel:DWORD src1_sel:BYTE_1\n\t" \
  "ds_read_u16 v224, v224 offset:10528\n\t" \
  "v_mfma_f32_16x16x32_bf16 v[48:51], v[208:211], v[194:197], v[48:51]\n\t" \
  "v_lshlrev_b32_sdwa v231, 2, v230 dst_sel:DWORD dst_unused:UNUSED_PAD src0_sel:DWORD src1_sel:BYTE_3\n\t" \
  "v_lshlrev_b32_sdwa v230, 2, v230 dst_sel:DWORD dst_unused:UNUSED_PAD src0_sel:DWORD src1_sel:BYTE_1\n\t" \
  "ds_read_u16_d16_hi v227, v227 offset:10528\n\t" \
  "v_mfma_f32_16x16x32_bf16 v[72:75], v[208:211], v[202:205], v[72:75]\n\t" \
  "s_add_u32 m0, s47, s35\n\t" \
  "v_lshlrev_b32_sdwa v233, 1, v171 dst_sel:DWORD dst_unused:UNUSED_PAD src0_sel:DWORD src1_sel:WORD_1\n\t" \
  "v_lshlrev_b32_sdwa v232, 1, v170 dst_sel:DWORD dst_unused:UNUSED_PAD src0_sel:DWORD src1_sel:WORD_1\n\t" \
  "s_waitcnt lgkmcnt(5)\n\t" \
  "s_mov_b32 exec_hi, 0xffff\n\t" \
  "buffer_load_dwordx4 v252, s[16:19], s34 offen lds\n\t" \
  "v_mfma_f32_16x16x32_bf16 v[4:7], v[212:215], v[178:181], v[4:7]\n\t" \
  "s_mov_b32 exec_hi, -1\n\t" \
  "v_lshlrev_b32_sdwa v235, 1, v169 dst_sel:DWORD dst_unused:UNUSED_PAD src0_sel:DWORD src1_sel:WORD_1\n\t" \
  "s_add_u32 s34, s34, s32\n\t" \
  "ds_read_b64_tr_b16 v[208:209], v249 offset:30824\n\t" \
  "ds_read_u16 v226, v226 offset:10528\n\t" \
  "v_mfma_f32_16x16x32_bf16 v[28:31], v[212:215], v[186:189], v[28:31]\n\t" \
  "v_lshlrev_b32_sdwa v234, 1, v168 dst_sel:DWORD dst_unused:UNUSED_PAD src0_sel:DWORD src1_sel:WORD_1\n\t" \
  "ds_read_b64_tr_b16 v[210:211], v249 offset:32360\n\t" \
  "ds_read_b32 v229, v229 offset:10016\n\t" \
  "v_mfma_f32_16x16x32_bf16 v[52:55], v[212:215], v[194:197], v[52:55]\n\t" \
  "v_fma_mix_f32 v221, |v147|, v221, v221 op_sel:[0,1,0] op_sel_hi:[0,1,1] clamp\n\t" \
  "v_fma_mix_f32 v220, |v146|, v220, v220 op_sel:[0,1,0] op_sel_hi:[0,1,1] clamp\n\t" \
  "ds_read_b32 v228, v228 offset:10016\n\t" \
  "ds_read_b32 v231, v231 offset:10016\n\t" \
  "v_mfma_f32_16x16x32_bf16 v[76:79], v[212:215], v[202:205], v[76:79]\n\t" \
  "v_fma_mix_f32 v223, |v145|, v223, v223 op_sel:[0,1,0] op_sel_hi:[0,1,1] clamp\n\t" \
  "v_fma_mix_f32 v222, |v144|, v222, v222 op_sel:[0,1,0] op_sel_hi:[0,1,1] clamp\n\t" \
  "s_waitcnt lgkmcnt(4)\n\t" \
  "ds_read_b64_tr_b16 v[212:213], v249 offset:30880\n\t" \
  "ds_read_b32 v230, v230 offset:10016\n\t" \
  "v_mfma_f32_16x16x32_bf16 v[8:11], v[216:219], v[178:181], v[8:11]\n\t" \
  "v_or_b32 v155, v224, v225\n\t" \
  "v_or_b32 v154, v226, v227\n\t" \
  "ds_read_b64_tr_b16 v[214:215], v249 offset:32416\n\t" \
  "v_mfma_f32_16x16x32_bf16 v[32:35], v[216:219], v[186:189], v[32:35]\n\t" \
  "v_fma_f32 v147, |v147|, v221, v147 mul:4\n\t" \
  "v_fma_f32 v146, |v146|, v220, v146 mul:4\n\t" \
  "ds_read_u16_d16_hi v233, v233 offset:10528\n\t" \
  "v_mfma_f32_16x16x32_bf16 v[56:59], v[216:219], v[194:197], v[56:59]\n\t" \
  "v_fma_f32 v145, |v145|, v223, v145 mul:4\n\t" \
  "v_fma_f32 v144, |v144|, v222, v144 mul:4\n\t" \
  "ds_read_u16 v232, v232 offset:10528\n\t" \
  "v_mfma_f32_16x16x32_bf16 v[80:83], v[216:219], v[202:205], v[80:83]\n\t" \
  "v_cvt_pk_bf16_f32 v147, v146, v147\n\t" \
  "v_cvt_pk_bf16_f32 v146, v144, v145\n\t" \
  "ds_read_b64_tr_b16 v[216:217], v249 offset:30888\n\t" \
  "s_waitcnt lgkmcnt(9)\n\t" \
  "v_mfma_f32_16x16x32_bf16 v[12:15], v[208:211], v[178:181], v[12:15]\n\t" \
  "v_cvt_pknorm_i16_f32 v220, |v148|, |v149|\n\t" \
  "v_cvt_pknorm_i16_f32 v222, |v150|, |v151|\n\t" \
  "ds_read_b64_tr_b16 v[218:219], v249 offset:32424\n\t" \
  "v_mfma_f32_16x16x32_bf16 v[36:39], v[208:211], v[186:189], v[36:39]\n\t" \
  "v_lshlrev_b32_sdwa v221, 2, v220 dst_sel:DWORD dst_unused:UNUSED_PAD src0_sel:DWORD src1_sel:BYTE_3\n\t" \
  "v_lshlrev_b32_sdwa v220, 2, v220 dst_sel:DWORD dst_unused:UNUSED_PAD src0_sel:DWORD src1_sel:BYTE_1\n\t" \
  "ds_read_u16_d16_hi v235, v235 offset:10528\n\t" \
  "v_mfma_f32_16x16x32_bf16 v[60:63], v[208:211], v[194:197], v[60:63]\n\t" \
  "v_lshlrev_b32_sdwa v223, 2, v222 dst_sel:DWORD dst_unused:UNUSED_PAD src0_sel:DWORD src1_sel:BYTE_3\n\t" \
  "v_lshlrev_b32_sdwa v222, 2, v222 dst_sel:DWORD dst_unused:UNUSED_PAD src0_sel:DWORD src1_sel:BYTE_1\n\t" \
  "ds_read_u16 v234, v234 offset:10528\n\t" \
  "v_mfma_f32_16x16x32_bf16 v[84:87], v[208:211], v[202:205], v[84:87]\n\t" \
  "s_add_u32 m0, s44, s50\n\t" \
  "v_lshlrev_b32_sdwa v225, 1, v157 dst_sel:DWORD dst_unused:UNUSED_PAD src0_sel:DWORD src1_sel:WORD_1\n\t" \
  "v_lshlrev_b32_sdwa v224, 1, v156 dst_sel:DWORD dst_unused:UNUSED_PAD src0_sel:DWORD src1_sel:WORD_1\n\t" \
  "s_add_u32 s3, s33, s51\n\t" \
  "s_waitcnt lgkmcnt(6)\n\t" \
  "s_mov_b32 exec_hi, 0xffff\n\t" \
  "buffer_load_dwordx4 v252, s[16:19], s3 offen lds\n\t" \
  "v_mfma_f32_16x16x32_bf16 v[16:19], v[212:215], v[178:181], v[16:19]\n\t" \
  "s_mov_b64 exec, -1\n\t" \
  "v_lshlrev_b32_sdwa v227, 1, v159 dst_sel:DWORD dst_unused:UNUSED_PAD src0_sel:DWORD src1_sel:WORD_1\n\t" \
  "ds_read_b64_tr_b16 v[208:209], v248 offset:0\n\t" \
  "ds_read_b32 v221, v221 offset:10016\n\t" \
  "v_mfma_f32_16x16x32_bf16 v[40:43], v[212:215], v[186:189], v[40:43]\n\t" \
  "v_lshlrev_b32_sdwa v226, 1, v158 dst_sel:DWORD dst_unused:UNUSED_PAD src0_sel:DWORD src1_sel:WORD_1\n\t" \
  "ds_read_b64_tr_b16 v[210:211], v248 offset:1024\n\t" \
  "ds_read_b32 v220, v220 offset:10016\n\t" \
  "v_mfma_f32_16x16x32_bf16 v[64:67], v[212:215], v[194:197], v[64:67]\n\t" \
  "v_fma_mix_f32 v229, |v163|, v229, v229 op_sel:[0,1,0] op_sel_hi:[0,1,1] clamp\n\t" \
  "v_fma_mix_f32 v228, |v162|, v228, v228 op_sel:[0,1,0] op_sel_hi:[0,1,1] clamp\n\t" \
  "ds_read_b32 v223, v223 offset:10016\n\t" \
  "v_mfma_f32_16x16x32_bf16 v[88:91], v[212:215], v[202:205], v[88:91]\n\t" \
  "v_fma_mix_f32 v231, |v161|, v231, v231 op_sel:[0,1,0] op_sel_hi:[0,1,1] clamp\n\t" \
  "v_fma_mix_f32 v230, |v160|, v230, v230 op_sel:[0,1,0] op_sel_hi:[0,1,1] clamp\n\t" \
  "s_waitcnt lgkmcnt(5)\n\t" \
  "ds_read_b64_tr_b16 v[212:213], v248 offset:3072\n\t" \
  "ds_read_b32 v222, v222 offset:10016\n\t" \
  "v_mfma_f32_16x16x32_bf16 v[20:23], v[216:219], v[178:181], v[20:23]\n\t" \
  "v_or_b32 v171, v232, v233\n\t" \
  "v_or_b32 v170, v234, v235\n\t" \
  "ds_read_b64_tr_b16 v[214:215], v248 offset:4096\n\t" \
  "v_mfma_f32_16x16x32_bf16 v[44:47], v[216:219], v[186:189], v[44:47]\n\t" \
  "v_fma_f32 v163, |v163|, v229, v163 mul:4\n\t" \
  "v_fma_f32 v162, |v162|, v228, v162 mul:4\n\t" \
  "ds_read_u16_d16_hi v225, v225 offset:10528\n\t" \
  "v_mfma_f32_16x16x32_bf16 v[68:71], v[216:219], v[194:197], v[68:71]\n\t" \
  "v_fma_f32 v161, |v161|, v231, v161 mul:4\n\t" \
  "v_fma_f32 v160, |v160|, v230, v160 mul:4\n\t" \
  "ds_read_u16 v224, v224 offset:10528\n\t" \
  "v_mfma_f32_16x16x32_bf16 v[92:95], v[216:219], v[202:205], v[92:95]\n\t" \
  "s_add_u32 m0, s44, s36\n\t" \
  "v_cvt_pk_bf16_f32 v163, v162, v163\n\t" \
  "v_cvt_pk_bf16_f32 v162, v160, v161\n\t" \
  "s_add_u32 s3, s34, s51\n\t" \
  "s_waitcnt lgkmcnt(7)\n\t" \
  "s_mov_b32 exec_hi, 0xffff\n\t" \
  "buffer_load_dwordx4 v252, s[16:19], s3 offen lds\n\t" \
  "v_mfma_f32_16x16x32_bf16 v[176:179], v[208:211], v[96:99], 0\n\t" \
  "s_mov_b64 exec, -1\n\t" \
  "v_cvt_pknorm_i16_f32 v228, |v164|, |v165|\n\t" \
  "v_cvt_pknorm_i16_f32 v230, |v166|, |v167|\n\t" \
  "ds_read_b64_tr_b16 v[216:217], v248 offset:2048\n\t" \
  "v_mfma_f32_16x16x32_bf16 v[184:187], v[208:211], v[100:103], 0\n\t" \
  "v_lshlrev_b32_sdwa v229, 2, v228 dst_sel:DWORD dst_unused:UNUSED_PAD src0_sel:DWORD src1_sel:BYTE_3\n\t" \
  "v_lshlrev_b32_sdwa v228, 2, v228 dst_sel:DWORD dst_unused:UNUSED_PAD src0_sel:DWORD src1_sel:BYTE_1\n\t" \
  "ds_read_b64_tr_b16 v[218:219], v248 offset:5120\n\t" \
  "v_mfma_f32_16x16x32_bf16 v[192:195], v[208:211], v[104:107], 0\n\t" \
  "v_lshlrev_b32_sdwa v231, 2, v230 dst_sel:DWORD dst_unused:UNUSED_PAD src0_sel:DWORD src1_sel:BYTE_3\n\t" \
  "v_lshlrev_b32_sdwa v230, 2, v230 dst_sel:DWORD dst_unused:UNUSED_PAD src0_sel:DWORD src1_sel:BYTE_1\n\t" \
  "ds_read_u16_d16_hi v227, v227 offset:10528\n\t" \
  "v_mfma_f32_16x16x32_bf16 v[200:203], v[208:211], v[108:111], 0\n\t" \
  "v_lshlrev_b32_sdwa v233, 1, v173 dst_sel:DWORD dst_unused:UNUSED_PAD src0_sel:DWORD src1_sel:WORD_1\n\t" \
  "s_waitcnt lgkmcnt(5)\n\t" \
  "ds_read_b64_tr_b16 v[208:209], v248 offset:512\n\t" \
  "ds_read_u16 v226, v226 offset:10528\n\t" \
  "v_mfma_f32_16x16x32_bf16 v[176:179], v[212:215], v[112:115], v[176:179]\n\t" \
  "v_lshlrev_b32_sdwa v232, 1, v172 dst_sel:DWORD dst_unused:UNUSED_PAD src0_sel:DWORD src1_sel:WORD_1\n\t" \
  "v_lshlrev_b32_sdwa v235, 1, v175 dst_sel:DWORD dst_unused:UNUSED_PAD src0_sel:DWORD src1_sel:WORD_1\n\t" \
  "ds_read_b64_tr_b16 v[210:211], v248 offset:1536\n\t" \
  "v_mfma_f32_16x16x32_bf16 v[184:187], v[212:215], v[116:119], v[184:187]\n\t" \
  "v_lshlrev_b32_sdwa v234, 1, v174 dst_sel:DWORD dst_unused:UNUSED_PAD src0_sel:DWORD src1_sel:WORD_1\n\t" \
  "v_fma_mix_f32 v221, |v149|, v221, v221 op_sel:[0,1,0] op_sel_hi:[0,1,1] clamp\n\t" \
  "ds_read_b32 v229, v229 offset:10016\n\t" \
  "ds_read_b32 v228, v228 offset:10016\n\t" \
  "v_mfma_f32_16x16x32_bf16 v[192:195], v[212:215], v[120:123], v[192:195]\n\t" \
  "v_fma_mix_f32 v220, |v148|, v220, v220 op_sel:[0,1,0] op_sel_hi:[0,1,1] clamp\n\t" \
  "ds_read_b32 v231, v231 offset:10016\n\t" \
  "ds_read_b32 v230, v230 offset:10016\n\t" \
  "v_mfma_f32_16x16x32_bf16 v[200:203], v[212:215], v[124:127], v[200:203]\n\t" \
  "v_fma_mix_f32 v223, |v151|, v223, v223 op_sel:[0,1,0] op_sel_hi:[0,1,1] clamp\n\t" \
  "v_fma_mix_f32 v222, |v150|, v222, v222 op_sel:[0,1,0] op_sel_hi:[0,1,1] clamp\n\t" \
  "s_waitcnt lgkmcnt(5)\n\t" \
  "ds_read_b64_tr_b16 v[212:213], v248 offset:3584\n\t" \
  "ds_read_u16_d16_hi v233, v233 offset:10528\n\t" \
  "v_mfma_f32_16x16x32_bf16 v[176:179], v[216:219], v[128:131], v[176:179]\n\t" \
  "v_or_b32 v156, v224, v225\n\t" \
  "v_or_b32 v157, v226, v227\n\t" \
  "ds_read_b64_tr_b16 v[214:215], v248 offset:4608\n\t" \
  "v_mfma_f32_16x16x32_bf16 v[184:187], v[216:219], v[132:135], v[184:187]\n\t" \
  "v_fma_f32 v149, |v149|, v221, v149 mul:4\n\t" \
  "v_fma_f32 v148, |v148|, v220, v148 mul:4\n\t" \
  "ds_read_u16 v232, v232 offset:10528\n\t" \
  "v_mfma_f32_16x16x32_bf16 v[192:195], v[216:219], v[136:139], v[192:195]\n\t" \
  "v_fma_f32 v151, |v151|, v223, v151 mul:4\n\t" \
  "v_fma_f32 v150, |v150|, v222, v150 mul:4\n\t" \
  "ds_read_u16_d16_hi v235, v235 offset:10528\n\t" \
  "v_mfma_f32_16x16x32_bf16 v[200:203], v[216:219], v[140:143], v[200:203]\n\t" \
  "v_cvt_pk_bf16_f32 v148, v148, v149\n\t" \
  "v_cvt_pk_bf16_f32 v149, v150, v151\n\t" \
  "ds_read_b64_tr_b16 v[216:217], v248 offset:2560\n\t" \
  "s_waitcnt lgkmcnt(10)\n\t" \
  "v_mfma_f32_16x16x32_bf16 v[180:183], v[208:211], v[96:99], 0\n\t" \
  "v_cvt_pknorm_i16_f32 v220, |v178|, |v179|\n\t" \
  "v_cvt_pknorm_i16_f32 v222, |v176|, |v177|\n\t" \
  "s_waitcnt vmcnt(2)\n\t" \
  "ds_read_b64_tr_b16 v[218:219], v248 offset:5632\n\t" \
  "v_mfma_f32_16x16x32_bf16 v[188:191], v[208:211], v[100:103], 0\n\t" \
  "v_lshlrev_b32_sdwa v221, 2, v220 dst_sel:DWORD dst_unused:UNUSED_PAD src0_sel:DWORD src1_sel:BYTE_3\n\t" \
  "v_lshlrev_b32_sdwa v220, 2, v220 dst_sel:DWORD dst_unused:UNUSED_PAD src0_sel:DWORD src1_sel:BYTE_1\n\t" \
  "s_mov_b64 exec, 3\n\t" \
  "ds_add_rtn_u32 v246, v251, v250 offset:47160\n\t" \
  "v_mfma_f32_16x16x32_bf16 v[196:199], v[208:211], v[104:107], 0\n\t" \
  "s_mov_b64 exec, -1\n\t" \
  "v_lshlrev_b32_sdwa v223, 2, v222 dst_sel:DWORD dst_unused:UNUSED_PAD src0_sel:DWORD src1_sel:BYTE_3\n\t" \
  "v_lshlrev_b32_sdwa v222, 2, v222 dst_sel:DWORD dst_unused:UNUSED_PAD src0_sel:DWORD src1_sel:BYTE_1\n\t" \
  "ds_read_u16 v234, v234 offset:10528\n\t" \
  "v_mfma_f32_16x16x32_bf16 v[204:207], v[208:211], v[108:111], 0\n\t" \
  "s_waitcnt lgkmcnt(1)\n\t" \
  "v_lshlrev_b32_sdwa v225, 1, v187 dst_sel:DWORD dst_unused:UNUSED_PAD src0_sel:DWORD src1_sel:WORD_1\n\t" \
  "v_readfirstlane_b32 s4, v246\n\t" \
  "ds_read_b32 v221, v221 offset:10016\n\t" \
  "v_mfma_f32_16x16x32_bf16 v[180:183], v[212:215], v[112:115], v[180:183]\n\t" \
  "s_cmpk_lt_u32 s4, 6144\n\t" \
  "v_lshlrev_b32_sdwa v224, 1, v186 dst_sel:DWORD dst_unused:UNUSED_PAD src0_sel:DWORD src1_sel:WORD_1\n\t" \
  "s_cbranch_scc1 .Lcnx_w1_4\n.Lcnx_g1_4:\n\t" \
  "ds_read_b32 v220, v220 offset:10016\n\t" \
  "v_mfma_f32_16x16x32_bf16 v[188:191], v[212:215], v[116:119], v[188:191]\n\t" \
  "v_lshlrev_b32_sdwa v227, 1, v185 dst_sel:DWORD dst_unused:UNUSED_PAD src0_sel:DWORD src1_sel:WORD_1\n\t" \
  "v_lshlrev_b32_sdwa v226, 1, v184 dst_sel:DWORD dst_unused:UNUSED_PAD src0_sel:DWORD src1_sel:WORD_1\n\t" \
  "ds_read_b64_tr_b16 v[208:209], v249 offset:6152\n\t" \
  "v_mfma_f32_16x16x32_bf16 v[196:199], v[212:215], v[120:123], v[196:199]\n\t" \
  "v_fma_mix_f32 v229, |v165|, v229, v229 op_sel:[0,1,0] op_sel_hi:[0,1,1] clamp\n\t" \
  "v_fma_mix_f32 v228, |v164|, v228, v228 op_sel:[0,1,0] op_sel_hi:[0,1,1] clamp\n\t" \
  "ds_read_b64_tr_b16 v[210:211], v249 offset:7688\n\t" \
  "ds_read_b32 v223, v223 offset:10016\n\t" \
  "v_mfma_f32_16x16x32_bf16 v[204:207], v[212:215], v[124:127], v[204:207]\n\t" \
  "v_fma_mix_f32 v231, |v167|, v231, v231 op_sel:[0,1,0] op_sel_hi:[0,1,1] clamp\n\t" \
  "v_fma_mix_f32 v230, |v166|, v230, v230 op_sel:[0,1,0] op_sel_hi:[0,1,1] clamp\n\t" \
  "s_waitcnt lgkmcnt(5)\n\t" \
  "ds_read_b64_tr_b16 v[212:213], v249 offset:6160\n\t" \
  "ds_read_b32 v222, v222 offset:10016\n\t" \
  "v_mfma_f32_16x16x32_bf16 v[180:183], v[216:219], v[128:131], v[180:183]\n\t" \
  "s_mov_b32 m0, s44\n\t" \
  "v_or_b32 v172, v232, v233\n\t" \
  "v_or_b32 v173, v234, v235\n\t" \
  "s_mov_b32 exec_hi, 0xffff\n\t" \
  "buffer_load_dwordx4 v252, s[16:19], s33 offen lds\n\t" \
  "v_mfma_f32_16x16x32_bf16 v[188:191], v[216:219], v[132:135], v[188:191]\n\t" \
  "s_mov_b32 exec_hi, -1\n\t" \
  "v_fma_f32 v165, |v165|, v229, v165 mul:4\n\t" \
  "v_fma_f32 v164, |v164|, v228, v164 mul:4\n\t" \
  "s_add_u32 s33, s33, s32\n\t" \
  "ds_read_b64_tr_b16 v[214:215], v249 offset:7696\n\t" \
  "v_mfma_f32_16x16x32_bf16 v[196:199], v[216:219], v[136:139], v[196:199]\n\t" \
  "v_fma_f32 v167, |v167|, v231, v167 mul:4\n\t" \
  "v_fma_f32 v166, |v166|, v230, v166 mul:4\n\t" \
  "ds_read_u16_d16_hi v225, v225 offset:10528\n\t" \
  "v_mfma_f32_16x16x32_bf16 v[204:207], v[216:219], v[140:143], v[204:207]\n\t" \
  "v_cvt_pk_bf16_f32 v164, v164, v165\n\t" \
  "v_cvt_pk_bf16_f32 v165, v166, v167\n\t" \
  "ds_read_b64_tr_b16 v[216:217], v249 offset:6216\n\t" \
  "s_waitcnt lgkmcnt(6)\n\t" \
  "v_mfma_f32_16x16x32_bf16 v[0:3], v[208:211], v[146:149], v[0:3]\n\t" \
  "v_cvt_pknorm_i16_f32 v228, |v194|, |v195|\n\t" \
  "v_cvt_pknorm_i16_f32 v230, |v192|, |v193|\n\t" \
  "ds_read_b64_tr_b16 v[218:219], v249 offset:7752\n\t" \
  "v_mfma_f32_16x16x32_bf16 v[24:27], v[208:211], v[154:157], v[24:27]\n\t" \
  "v_lshlrev_b32_sdwa v229, 2, v228 dst_sel:DWORD dst_unused:UNUSED_PAD src0_sel:DWORD src1_sel:BYTE_3\n\t" \
  "v_lshlrev_b32_sdwa v228, 2, v228 dst_sel:DWORD dst_unused:UNUSED_PAD src0_sel:DWORD src1_sel:BYTE_1\n\t" \
  "ds_read_u16 v224, v224 offset:10528\n\t" \
  "v_mfma_f32_16x16x32_bf16 v[48:51], v[208:211], v[162:165], v[48:51]\n\t" \
  "v_lshlrev_b32_sdwa v231, 2, v230 dst_sel:DWORD dst_unused:UNUSED_PAD src0_sel:DWORD src1_sel:BYTE_3\n\t" \
  "v_lshlrev_b32_sdwa v230, 2, v230 dst_sel:DWORD dst_unused:UNUSED_PAD src0_sel:DWORD src1_sel:BYTE_1\n\t" \
  "ds_read_u16_d16_hi v227, v227 offset:10528\n\t" \
  "v_mfma_f32_16x16x32_bf16 v[72:75], v[208:211], v[170:173], v[72:75]\n\t" \
  "s_add_u32 m0, s44, s35\n\t" \
  "v_lshlrev_b32_sdwa v233, 1, v203 dst_sel:DWORD dst_unused:UNUSED_PAD src0_sel:DWORD src1_sel:WORD_1\n\t" \
  "v_lshlrev_b32_sdwa v232, 1, v202 dst_sel:DWORD dst_unused:UNUSED_PAD src0_sel:DWORD src1_sel:WORD_1\n\t" \
  "s_waitcnt lgkmcnt(5)\n\t" \
  "s_mov_b32 exec_hi, 0xffff\n\t" \
  "buffer_load_dwordx4 v252, s[16:19], s34 offen lds\n\t" \
  "v_mfma_f32_16x16x32_bf16 v[4:7], v[212:215], v[146:149], v[4:7]\n\t" \
  "s_mov_b32 exec_hi, -1\n\t" \
  "v_lshlrev_b32_sdwa v235, 1, v201 dst_sel:DWORD dst_unused:UNUSED_PAD src0_sel:DWORD src1_sel:WORD_1\n\t" \
  "s_add_u32 s34, s34, s32\n\t" \
  "ds_read_b64_tr_b16 v[208:209], v249 offset:6224\n\t" \
  "ds_read_u16 v226, v226 offset:10528\n\t" \
  "v_mfma_f32_16x16x32_bf16 v[28:31], v[212:215], v[154:157], v[28:31]\n\t" \
  "v_lshlrev_b32_sdwa v234, 1, v200 dst_sel:DWORD dst_unused:UNUSED_PAD src0_sel:DWORD src1_sel:WORD_1\n\t" \
  "ds_read_b64_tr_b16 v[210:211], v249 offset:7760\n\t" \
  "ds_read_b32 v229, v229 offset:10016\n\t" \
  "v_mfma_f32_16x16x32_bf16 v[52:55], v[212:215], v[162:165], v[52:55]\n\t" \
  "v_fma_mix_f32 v221, |v179|, v221, v221 op_sel:[0,1,0] op_sel_hi:[0,1,1] clamp\n\t" \
  "v_fma_mix_f32 v220, |v178|, v220, v220 op_sel:[0,1,0] op_sel_hi:[0,1,1] clamp\n\t" \
  "ds_read_b32 v228, v228 offset:10016\n\t" \
  "ds_read_b32 v231, v231 offset:10016\n\t" \
  "v_mfma_f32_16x16x32_bf16 v[76:79], v[212:215], v[170:173], v[76:79]\n\t" \
  "v_fma_mix_f32 v223, |v177|, v223, v223 op_sel:[0,1,0] op_sel_hi:[0,1,1] clamp\n\t" \
  "v_fma_mix_f32 v222, |v176|, v222, v222 op_sel:[0,1,0] op_sel_hi:[0,1,1] clamp\n\t" \
  "s_waitcnt lgkmcnt(4)\n\t" \
  "ds_read_b64_tr_b16 v[212:213], v249 offset:6280\n\t" \
  "ds_read_b32 v230, v230 offset:10016\n\t" \
  "v_mfma_f32_16x16x32_bf16 v[8:11], v[216:219], v[146:149], v[8:11]\n\t" \
  "v_or_b32 v187, v224, v225\n\t" \
  "v_or_b32 v186, v226, v227\n\t" \
  "ds_read_b64_tr_b16 v[214:215], v249 offset:7816\n\t" \
  "v_mfma_f32_16x16x32_bf16 v[32:35], v[216:219], v[154:157], v[32:35]\n\t" \
  "v_fma_f32 v179, |v179|, v221, v179 mul:4\n\t" \
  "v_fma_f32 v178, |v178|, v220, v178 mul:4\n\t" \
  "ds_read_u16_d16_hi v233, v233 offset:10528\n\t" \
  "v_mfma_f32_16x16x32_bf16 v[56:59], v[216:219], v[162:165], v[56:59]\n\t" \
  "v_fma_f32 v177, |v177|, v223, v177 mul:4\n\t" \
  "v_fma_f32 v176, |v176|, v222, v176 mul:4\n\t" \
  "ds_read_u16 v232, v232 offset:10528\n\t" \
  "v_mfma_f32_16x16x32_bf16 v[80:83], v[216:219], v[170:173], v[80:83]\n\t" \
  "v_cvt_pk_bf16_f32 v179, v178, v179\n\t" \
  "v_cvt_pk_bf16_f32 v178, v176, v177\n\t" \
  "ds_read_b64_tr_b16 v[216:217], v249 offset:6288\n\t" \
  "s_waitcnt lgkmcnt(9)\n\t" \
  "v_mfma_f32_16x16x32_bf16 v[12:15], v[208:211], v[146:149], v[12:15]\n\t" \
  "v_cvt_pknorm_i16_f32 v220, |v180|, |v181|\n\t" \
  "v_cvt_pknorm_i16_f32 v222, |v182|, |v183|\n\t" \
  "ds_read_b64_tr_b16 v[218:219], v249 offset:7824\n\t" \
  "v_mfma_f32_16x16x32_bf16 v[36:39], v[208:211], v[154:157], v[36:39]\n\t" \
  "v_lshlrev_b32_sdwa v221, 2, v220 dst_sel:DWORD dst_unused:UNUSED_PAD src0_sel:DWORD src1_sel:BYTE_3\n\t" \
  "v_lshlrev_b32_sdwa v220, 2, v220 dst_sel:DWORD dst_unused:UNUSED_PAD src0_sel:DWORD src1_sel:BYTE_1\n\t" \
  "ds_read_u16_d16_hi v235, v235 offset:10528\n\t" \
  "v_mfma_f32_16x16x32_bf16 v[60:63], v[208:211], v[162:165], v[60:63]\n\t" \
  "v_lshlrev_b32_sdwa v223, 2, v222 dst_sel:DWORD dst_unused:UNUSED_PAD src0_sel:DWORD src1_sel:BYTE_3\n\t" \
  "v_lshlrev_b32_sdwa v222, 2, v222 dst_sel:DWORD dst_unused:UNUSED_PAD src0_sel:DWORD src1_sel:BYTE_1\n\t" \
  "ds_read_u16 v234, v234 offset:10528\n\t" \
  "v_mfma_f32_16x16x32_bf16 v[84:87], v[208:211], v[170:173], v[84:87]\n\t" \
  "s_add_u32 m0, s45, s50\n\t" \
  "v_lshlrev_b32_sdwa v225, 1, v189 dst_sel:DWORD dst_unused:UNUSED_PAD src0_sel:DWORD src1_sel:WORD_1\n\t" \
  "v_lshlrev_b32_sdwa v224, 1, v188 dst_sel:DWORD dst_unused:UNUSED_PAD src0_sel:DWORD src1_sel:WORD_1\n\t" \
  "s_add_u32 s3, s33, s51\n\t" \
  "s_waitcnt lgkmcnt(6)\n\t" \
  "s_mov_b32 exec_hi, 0xffff\n\t" \
  "buffer_load_dwordx4 v252, s[16:19], s3 offen lds\n\t" \
  "v_mfma_f32_16x16x32_bf16 v[16:19], v[212:215], v[146:149], v[16:19]\n\t" \
  "s_mov_b64 exec, -1\n\t" \
  "v_lshlrev_b32_sdwa v227, 1, v191 dst_sel:DWORD dst_unused:UNUSED_PAD src0_sel:DWORD src1_sel:WORD_1\n\t" \
  "ds_read_b64_tr_b16 v[208:209], v248 offset:43048\n\t" \
  "ds_read_b32 v221, v221 offset:10016\n\t" \
  "v_mfma_f32_16x16x32_bf16 v[40:43], v[212:215], v[154:157], v[40:43]\n\t" \
  "v_lshlrev_b32_sdwa v226, 1, v190 dst_sel:DWORD dst_unused:UNUSED_PAD src0_sel:DWORD src1_sel:WORD_1\n\t" \
  "ds_read_b64_tr_b16 v[210:211], v248 offset:44072\n\t" \
  "ds_read_b32 v220, v220 offset:10016\n\t" \
  "v_mfma_f32_16x16x32_bf16 v[64:67], v[212:215], v[162:165], v[64:67]\n\t" \
  "v_fma_mix_f32 v229, |v195|, v229, v229 op_sel:[0,1,0] op_sel_hi:[0,1,1] clamp\n\t" \
  "v_fma_mix_f32 v228, |v194|, v228, v228 op_sel:[0,1,0] op_sel_hi:[0,1,1] clamp\n\t" \
  "ds_read_b32 v223, v223 offset:10016\n\t" \
  "v_mfma_f32_16x16x32_bf16 v[88:91], v[212:215], v[170:173], v[88:91]\n\t" \
  "v_fma_mix_f32 v231, |v193|, v231, v231 op_sel:[0,1,0] op_sel_hi:[0,1,1] clamp\n\t" \
  "v_fma_mix_f32 v230, |v192|, v230, v230 op_sel:[0,1,0] op_sel_hi:[0,1,1] clamp\n\t" \
  "s_waitcnt lgkmcnt(5)\n\t" \
  "ds_read_b64_tr_b16 v[212:213], v248 offset:46120\n\t" \
  "ds_read_b32 v222, v222 offset:10016\n\t" \
  "v_mfma_f32_16x16x32_bf16 v[20:23], v[216:219], v[146:149], v[20:23]\n\t" \
  "v_or_b32 v203, v232, v233\n\t" \
  "v_or_b32 v202, v234, v235\n\t" \
  "ds_read_b64_tr_b16 v[214:215], v248 offset:47144\n\t" \
  "v_mfma_f32_16x16x32_bf16 v[44:47], v[216:219], v[154:157], v[44:47]\n\t" \
  "v_fma_f32 v195, |v195|, v229, v195 mul:4\n\t" \
  "v_fma_f32 v194, |v194|, v228, v194 mul:4\n\t" \
  "ds_read_u16_d16_hi v225, v225 offset:10528\n\t" \
  "v_mfma_f32_16x16x32_bf16 v[68:71], v[216:219], v[162:165], v[68:71]\n\t" \
  "v_fma_f32 v193, |v193|, v231, v193 mul:4\n\t" \
  "v_fma_f32 v192, |v192|, v230, v192 mul:4\n\t" \
  "ds_read_u16 v224, v224 offset:10528\n\t" \
  "v_mfma_f32_16x16x32_bf16 v[92:95], v[216:219], v[170:173], v[92:95]\n\t" \
  "s_add_u32 m0, s45, s36\n\t" \
  "v_cvt_pk_bf16_f32 v195, v194, v195\n\t" \
  "v_cvt_pk_bf16_f32 v194, v192, v193\n\t" \
  "s_add_u32 s3, s34, s51\n\t" \
  "s_waitcnt lgkmcnt(7)\n\t" \
  "s_mov_b32 exec_hi, 0xffff\n\t" \
  "buffer_load_dwordx4 v252, s[16:19], s3 offen lds\n\t" \
  "v_mfma_f32_16x16x32_bf16 v[144:147], v[208:211], v[96:99], 0\n\t" \
  "s_mov_b64 exec, -1\n\t" \
  "v_cvt_pknorm_i16_f32 v228, |v196|, |v197|\n\t" \
  "v_cvt_pknorm_i16_f32 v230, |v198|, |v199|\n\t" \
  "ds_read_b64_tr_b16 v[216:217], v248 offset:45096\n\t" \
  "v_mfma_f32_16x16x32_bf16 v[152:155], v[208:211], v[100:103], 0\n\t" \
  "v_lshlrev_b32_sdwa v229, 2, v228 dst_sel:DWORD dst_unused:UNUSED_PAD src0_sel:DWORD src1_sel:BYTE_3\n\t" \
  "v_lshlrev_b32_sdwa v228, 2, v228 dst_sel:DWORD dst_unused:UNUSED_PAD src0_sel:DWORD src1_sel:BYTE_1\n\t" \
  "ds_read_b64_tr_b16 v[218:219], v248 offset:48168\n\t" \
  "v_mfma_f32_16x16x32_bf16 v[160:163], v[208:211], v[104:107], 0\n\t" \
  "v_lshlrev_b32_sdwa v231, 2, v230 dst_sel:DWORD dst_unused:UNUSED_PAD src0_sel:DWORD src1_sel:BYTE_3\n\t" \
  "v_lshlrev_b32_sdwa v230, 2, v230 dst_sel:DWORD dst_unused:UNUSED_PAD src0_sel:DWORD src1_sel:BYTE_1\n\t" \
  "ds_read_u16_d16_hi v227, v227 offset:10528\n\t" \
  "v_mfma_f32_16x16x32_bf16 v[168:171], v[208:211], v[108:111], 0\n\t" \
  "v_lshlrev_b32_sdwa v233, 1, v205 dst_sel:DWORD dst_unused:UNUSED_PAD src0_sel:DWORD src1_sel:WORD_1\n\t" \
  "s_waitcnt lgkmcnt(5)\n\t" \
  "ds_read_b64_tr_b16 v[208:209], v248 offset:43560\n\t" \
  "ds_read_u16 v226, v226 offset:10528\n\t" \
  "v_mfma_f32_16x16x32_bf16 v[144:147], v[212:215], v[112:115], v[144:147]\n\t" \
  "v_lshlrev_b32_sdwa v232, 1, v204 dst_sel:DWORD dst_unused:UNUSED_PAD src0_sel:DWORD src1_sel:WORD_1\n\t" \
  "v_lshlrev_b32_sdwa v235, 1, v207 dst_sel:DWORD dst_unused:UNUSED_PAD src0_sel:DWORD src1_sel:WORD_1\n\t" \
  "ds_read_b64_tr_b16 v[210:211], v248 offset:44584\n\t" \
  "v_mfma_f32_16x16x32_bf16 v[152:155], v[212:215], v[116:119], v[152:155]\n\t" \
  "v_lshlrev_b32_sdwa v234, 1, v206 dst_sel:DWORD dst_unused:UNUSED_PAD src0_sel:DWORD src1_sel:WORD_1\n\t" \
  "v_fma_mix_f32 v221, |v181|, v221, v221 op_sel:[0,1,0] op_sel_hi:[0,1,1] clamp\n\t" \
  "ds_read_b32 v229, v229 offset:10016\n\t" \
  "ds_read_b32 v228, v228 offset:10016\n\t" \
  "v_mfma_f32_16x16x32_bf16 v[160:163], v[212:215], v[120:123], v[160:163]\n\t" \
  "v_fma_mix_f32 v220, |v180|, v220, v220 op_sel:[0,1,0] op_sel_hi:[0,1,1] clamp\n\t" \
  "ds_read_b32 v231, v231 offset:10016\n\t" \
  "ds_read_b32 v230, v230 offset:10016\n\t" \
  "v_mfma_f32_16x16x32_bf16 v[168:171], v[212:215], v[124:127], v[168:171]\n\t" \
  "v_fma_mix_f32 v223, |v183|, v223, v223 op_sel:[0,1,0] op_sel_hi:[0,1,1] clamp\n\t" \
  "v_fma_mix_f32 v222, |v182|, v222, v222 op_sel:[0,1,0] op_sel_hi:[0,1,1] clamp\n\t" \
  "s_waitcnt lgkmcnt(5)\n\t" \
  "ds_read_b64_tr_b16 v[212:213], v248 offset:46632\n\t" \
  "ds_read_u16_d16_hi v233, v233 offset:10528\n\t" \
  "v_mfma_f32_16x16x32_bf16 v[144:147], v[216:219], v[128:131], v[144:147]\n\t" \
  "v_or_b32 v188, v224, v225\n\t" \
  "v_or_b32 v189, v226, v227\n\t" \
  "ds_read_b64_tr_b16 v[214:215], v248 offset:47656\n\t" \
  "v_mfma_f32_16x16x32_bf16 v[152:155], v[216:219], v[132:135], v[152:155]\n\t" \
  "v_fma_f32 v181, |v181|, v221, v181 mul:4\n\t" \
  "v_fma_f32 v180, |v180|, v220, v180 mul:4\n\t" \
  "ds_read_u16 v232, v232 offset:10528\n\t" \
  "v_mfma_f32_16x16x32_bf16 v[160:163], v[216:219], v[136:139], v[160:163]\n\t" \
  "v_fma_f32 v183, |v183|, v223, v183 mul:4\n\t" \
  "v_fma_f32 v182, |v182|, v222, v182 mul:4\n\t" \
  "ds_read_u16_d16_hi v235, v235 offset:10528\n\t" \
  "v_mfma_f32_16x16x32_bf16 v[168:171], v[216:219], v[140:143], v[168:171]\n\t" \
  "v_cvt_pk_bf16_f32 v180, v180, v181\n\t" \
  "v_cvt_pk_bf16_f32 v181, v182, v183\n\t" \
  "ds_read_b64_tr_b16 v[216:217], v248 offset:45608\n\t" \
  "s_waitcnt lgkmcnt(10)\n\t" \
  "v_mfma_f32_16x16x32_bf16 v[148:151], v[208:211], v[96:99], 0\n\t" \
  "v_cvt_pknorm_i16_f32 v220, |v146|, |v147|\n\t" \
  "v_cvt_pknorm_i16_f32 v222, |v144|, |v145|\n\t" \
  "s_waitcnt vmcnt(2)\n\t" \
  "ds_read_b64_tr_b16 v[218:219], v248 offset:48680\n\t" \
  "v_mfma_f32_16x16x32_bf16 v[156:159], v[208:211], v[100:103], 0\n\t" \
  "v_lshlrev_b32_sdwa v221, 2, v220 dst_sel:DWORD dst_unused:UNUSED_PAD src0_sel:DWORD src1_sel:BYTE_3\n\t" \
  "v_lshlrev_b32_sdwa v220, 2, v220 dst_sel:DWORD dst_unused:UNUSED_PAD src0_sel:DWORD src1_sel:BYTE_1\n\t" \
  "s_mov_b64 exec, 3\n\t" \
  "ds_add_rtn_u32 v246, v251, v250 offset:47168\n\t" \
  "v_mfma_f32_16x16x32_bf16 v[164:167], v[208:211], v[104:107], 0\n\t" \
  "s_mov_b64 exec, -1\n\t" \
  "v_lshlrev_b32_sdwa v223, 2, v222 dst_sel:DWORD dst_unused:UNUSED_PAD src0_sel:DWORD src1_sel:BYTE_3\n\t" \
  "v_lshlrev_b32_sdwa v222, 2, v222 dst_sel:DWORD dst_unused:UNUSED_PAD src0_sel:DWORD src1_sel:BYTE_1\n\t" \
  "ds_read_u16 v234, v234 offset:10528\n\t" \
  "v_mfma_f32_16x16x32_bf16 v[172:175], v[208:211], v[108:111], 0\n\t" \
  "s_waitcnt lgkmcnt(1)\n\t" \
  "v_lshlrev_b32_sdwa v225, 1, v155 dst_sel:DWORD dst_unused:UNUSED_PAD src0_sel:DWORD src1_sel:WORD_1\n\t" \
  "v_readfirstlane_b32 s4, v246\n\t" \
  "ds_read_b32 v221, v221 offset:10016\n\t" \
  "v_mfma_f32_16x16x32_bf16 v[148:151], v[212:215], v[112:115], v[148:151]\n\t" \
  "s_cmpk_lt_u32 s4, 6144\n\t" \
  "v_lshlrev_b32_sdwa v224, 1, v154 dst_sel:DWORD dst_unused:UNUSED_PAD src0_sel:DWORD src1_sel:WORD_1\n\t" \
  "s_cbranch_scc1 .Lcnx_w1_5\n.Lcnx_g1_5:\n\t" \
  "ds_read_b32 v220, v220 offset:10016\n\t" \
  "v_mfma_f32_16x16x32_bf16 v[156:159], v[212:215], v[116:119], v[156:159]\n\t" \
  "v_lshlrev_b32_sdwa v227, 1, v153 dst_sel:DWORD dst_unused:UNUSED_PAD src0_sel:DWORD src1_sel:WORD_1\n\t" \
  "v_lshlrev_b32_sdwa v226, 1, v152 dst_sel:DWORD dst_unused:UNUSED_PAD src0_sel:DWORD src1_sel:WORD_1\n\t" \
  "ds_read_b64_tr_b16 v[208:209], v249 offset:12304\n\t" \
  "v_mfma_f32_16x16x32_bf16 v[164:167], v[212:215], v[120:123], v[164:167]\n\t" \
  "v_fma_mix_f32 v229, |v197|, v229, v229 op_sel:[0,1,0] op_sel_hi:[0,1,1] clamp\n\t" \
  "v_fma_mix_f32 v228, |v196|, v228, v228 op_sel:[0,1,0] op_sel_hi:[0,1,1] clamp\n\t" \
  "ds_read_b64_tr_b16 v[210:211], v249 offset:13840\n\t" \
  "ds_read_b32 v223, v223 offset:10016\n\t" \
  "v_mfma_f32_16x16x32_bf16 v[172:175], v[212:215], v[124:127], v[172:175]\n\t" \
  "v_fma_mix_f32 v231, |v199|, v231, v231 op_sel:[0,1,0] op_sel_hi:[0,1,1] clamp\n\t" \
  "v_fma_mix_f32 v230, |v198|, v230, v230 op_sel:[0,1,0] op_sel_hi:[0,1,1] clamp\n\t" \
  "s_waitcnt lgkmcnt(5)\n\t" \
  "ds_read_b64_tr_b16 v[212:213], v249 offset:12312\n\t" \
  "ds_read_b32 v222, v222 offset:10016\n\t" \
  "v_mfma_f32_16x16x32_bf16 v[148:151], v[216:219], v[128:131], v[148:151]\n\t" \
  "s_mov_b32 m0, s45\n\t" \
  "v_or_b32 v204, v232, v233\n\t" \
  "v_or_b32 v205, v234, v235\n\t" \
  "s_mov_b32 exec_hi, 0xffff\n\t" \
  "buffer_load_dwordx4 v252, s[16:19], s33 offen lds\n\t" \
  "v_mfma_f32_16x16x32_bf16 v[156:159], v[216:219], v[132:135], v[156:159]\n\t" \
  "s_mov_b32 exec_hi, -1\n\t" \
  "v_fma_f32 v197, |v197|, v229, v197 mul:4\n\t" \
  "v_fma_f32 v196, |v196|, v228, v196 mul:4\n\t" \
  "s_add_u32 s33, s33, s32\n\t" \
  "ds_read_b64_tr_b16 v[214:215], v249 offset:13848\n\t" \
  "v_mfma_f32_16x16x32_bf16 v[164:167], v[216:219], v[136:139], v[164:167]\n\t" \
  "v_fma_f32 v199, |v199|, v231, v199 mul:4\n\t" \
  "v_fma_f32 v198, |v198|, v230, v198 mul:4\n\t" \
  "ds_read_u16_d16_hi v225, v225 offset:10528\n\t" \
  "v_mfma_f32_16x16x32_bf16 v[172:175], v[216:219], v[140:143], v[172:175]\n\t" \
  "v_cvt_pk_bf16_f32 v196, v196, v197\n\t" \
  "v_cvt_pk_bf16_f32 v197, v198, v199\n\t" \
  "ds_read_b64_tr_b16 v[216:217], v249 offset:12368\n\t" \
  "s_waitcnt lgkmcnt(6)\n\t" \
  "v_mfma_f32_16x16x32_bf16 v[0:3], v[208:211], v[178:181], v[0:3]\n\t" \
  "v_cvt_pknorm_i16_f32 v228, |v162|, |v163|\n\t" \
  "v_cvt_pknorm_i16_f32 v230, |v160|, |v161|\n\t" \
  "ds_read_b64_tr_b16 v[218:219], v249 offset:13904\n\t" \
  "v_mfma_f32_16x16x32_bf16 v[24:27], v[208:211], v[186:189], v[24:27]\n\t" \
  "v_lshlrev_b32_sdwa v229, 2, v228 dst_sel:DWORD dst_unused:UNUSED_PAD src0_sel:DWORD src1_sel:BYTE_3\n\t" \
  "v_lshlrev_b32_sdwa v228, 2, v228 dst_sel:DWORD dst_unused:UNUSED_PAD src0_sel:DWORD src1_sel:BYTE_1\n\t" \
  "ds_read_u16 v224, v224 offset:10528\n\t" \
  "v_mfma_f32_16x16x32_bf16 v[48:51], v[208:211], v[194:197], v[48:51]\n\t" \
  "v_lshlrev_b32_sdwa v231, 2, v230 dst_sel:DWORD dst_unused:UNUSED_PAD src0_sel:DWORD src1_sel:BYTE_3\n\t" \
  "v_lshlrev_b32_sdwa v230, 2, v230 dst_sel:DWORD dst_unused:UNUSED_PAD src0_sel:DWORD src1_sel:BYTE_1\n\t" \
  "ds_read_u16_d16_hi v227, v227 offset:10528\n\t" \
  "v_mfma_f32_16x16x32_bf16 v[72:75], v[208:211], v[202:205], v[72:75]\n\t" \
  "s_add_u32 m0, s45, s35\n\t" \
  "v_lshlrev_b32_sdwa v233, 1, v171 dst_sel:DWORD dst_unused:UNUSED_PAD src0_sel:DWORD src1_sel:WORD_1\n\t" \
  "v_lshlrev_b32_sdwa v232, 1, v170 dst_sel:DWORD dst_unused:UNUSED_PAD src0_sel:DWORD src1_sel:WORD_1\n\t" \
  "s_waitcnt lgkmcnt(5)\n\t" \
  "s_mov_b32 exec_hi, 0xffff\n\t" \
  "buffer_load_dwordx4 v252, s[16:19], s34 offen lds\n\t" \
  "v_mfma_f32_16x16x32_bf16 v[4:7], v[212:215], v[178:181], v[4:7]\n\t" \
  "s_mov_b32 exec_hi, -1\n\t" \
  "v_lshlrev_b32_sdwa v235, 1, v169 dst_sel:DWORD dst_unused:UNUSED_PAD src0_sel:DWORD src1_sel:WORD_1\n\t" \
  "s_add_u32 s34, s34, s32\n\t" \
  "ds_read_b64_tr_b16 v[208:209], v249 offset:12376\n\t" \
  "ds_read_u16 v226, v226 offset:10528\n\t" \
  "v_mfma_f32_16x16x32_bf16 v[28:31], v[212:215], v[186:189], v[28:31]\n\t" \
  "v_lshlrev_b32_sdwa v234, 1, v168 dst_sel:DWORD dst_unused:UNUSED_PAD src0_sel:DWORD src1_sel:WORD_1\n\t" \
  "ds_read_b64_tr_b16 v[210:211], v249 offset:13912\n\t" \
  "ds_read_b32 v229, v229 offset:10016\n\t" \
  "v_mfma_f32_16x16x32_bf16 v[52:55], v[212:215], v[194:197], v[52:55]\n\t" \
  "v_fma_mix_f32 v221, |v147|, v221, v221 op_sel:[0,1,0] op_sel_hi:[0,1,1] clamp\n\t" \
  "v_fma_mix_f32 v220, |v146|, v220, v220 op_sel:[0,1,0] op_sel_hi:[0,1,1] clamp\n\t" \
  "ds_read_b32 v228, v228 offset:10016\n\t" \
  "ds_read_b32 v231, v231 offset:10016\n\t" \
  "v_mfma_f32_16x16x32_bf16 v[76:79], v[212:215], v[202:205], v[76:79]\n\t" \
  "v_fma_mix_f32 v223, |v145|, v223, v223 op_sel:[0,1,0] op_sel_hi:[0,1,1] clamp\n\t" \
  "v_fma_mix_f32 v222, |v144|, v222, v222 op_sel:[0,1,0] op_sel_hi:[0,1,1] clamp\n\t" \
  "s_waitcnt lgkmcnt(4)\n\t" \
  "ds_read_b64_tr_b16 v[212:213], v249 offset:12432\n\t" \
  "ds_read_b32 v230, v230 offset:10016\n\t" \
  "v_mfma_f32_16x16x32_bf16 v[8:11], v[216:219], v[178:181], v[8:11]\n\t" \
  "v_or_b32 v155, v224, v225\n\t" \
  "v_or_b32 v154, v226, v227\n\t" \
  "ds_read_b64_tr_b16 v[214:215], v249 offset:13968\n\t" \
  "v_mfma_f32_16x16x32_bf16 v[32:35], v[216:219], v[186:189], v[32:35]\n\t" \
  "v_fma_f32 v147, |v147|, v221, v147 mul:4\n\t" \
  "v_fma_f32 v146, |v146|, v220, v146 mul:4\n\t" \
  "ds_read_u16_d16_hi v233, v233 offset:10528\n\t" \
  "v_mfma_f32_16x16x32_bf16 v[56:59], v[216:219], v[194:197], v[56:59]\n\t" \
  "v_fma_f32 v145, |v145|, v223, v145 mul:4\n\t" \
  "v_fma_f32 v144, |v144|, v222, v144 mul:4\n\t" \
  "ds_read_u16 v232, v232 offset:10528\n\t" \
  "v_mfma_f32_16x16x32_bf16 v[80:83], v[216:219], v[202:205], v[80:83]\n\t" \
  "v_cvt_pk_bf16_f32 v147, v146, v147\n\t" \
  "v_cvt_pk_bf16_f32 v146, v144, v145\n\t" \
  "ds_read_b64_tr_b16 v[216:217], v249 offset:12440\n\t" \
  "s_waitcnt lgkmcnt(9)\n\t" \
  "v_mfma_f32_16x16x32_bf16 v[12:15], v[208:211], v[178:181], v[12:15]\n\t" \
  "v_cvt_pknorm_i16_f32 v220, |v148|, |v149|\n\t" \
  "v_cvt_pknorm_i16_f32 v222, |v150|, |v151|\n\t" \
  "ds_read_b64_tr_b16 v[218:219], v249 offset:13976\n\t" \
  "v_mfma_f32_16x16x32_bf16 v[36:39], v[208:211], v[186:189], v[36:39]\n\t" \
  "v_lshlrev_b32_sdwa v221, 2, v220 dst_sel:DWORD dst_unused:UNUSED_PAD src0_sel:DWORD src1_sel:BYTE_3\n\t" \
  "v_lshlrev_b32_sdwa v220, 2, v220 dst_sel:DWORD dst_unused:UNUSED_PAD src0_sel:DWORD src1_sel:BYTE_1\n\t" \
  "ds_read_u16_d16_hi v235, v235 offset:10528\n\t" \
  "v_mfma_f32_16x16x32_bf16 v[60:63], v[208:211], v[194:197], v[60:63]\n\t" \
  "v_lshlrev_b32_sdwa v223, 2, v222 dst_sel:DWORD dst_unused:UNUSED_PAD src0_sel:DWORD src1_sel:BYTE_3\n\t" \
  "v_lshlrev_b32_sdwa v222, 2, v222 dst_sel:DWORD dst_unused:UNUSED_PAD src0_sel:DWORD src1_sel:BYTE_1\n\t" \
  "ds_read_u16 v234, v234 offset:10528\n\t" \
  "v_mfma_f32_16x16x32_bf16 v[84:87], v[208:211], v[202:205], v[84:87]\n\t" \
  "s_add_u32 m0, s46, s50\n\t" \
  "v_lshlrev_b32_sdwa v225, 1, v157 dst_sel:DWORD dst_unused:UNUSED_PAD src0_sel:DWORD src1_sel:WORD_1\n\t" \
  "v_lshlrev_b32_sdwa v224, 1, v156 dst_sel:DWORD dst_unused:UNUSED_PAD src0_sel:DWORD src1_sel:WORD_1\n\t" \
  "s_add_u32 s3, s33, s51\n\t" \
  "s_waitcnt lgkmcnt(6)\n\t" \
  "s_mov_b64 exec, s[68:69]\n\t" \
  "buffer_load_dwordx4 v252, s[16:19], s3 offen lds\n\t" \
  "v_mfma_f32_16x16x32_bf16 v[16:19], v[212:215], v[178:181], v[16:19]\n\t" \
  "s_mov_b64 exec, -1\n\t" \
  "v_lshlrev_b32_sdwa v227, 1, v159 dst_sel:DWORD dst_unused:UNUSED_PAD src0_sel:DWORD src1_sel:WORD_1\n\t" \
  "ds_read_b64_tr_b16 v[208:209], v248 offset:49192\n\t" \
  "ds_read_b32 v221, v221 offset:10016\n\t" \
  "v_mfma_f32_16x16x32_bf16 v[40:43], v[212:215], v[186:189], v[40:43]\n\t" \
  "v_lshlrev_b32_sdwa v226, 1, v158 dst_sel:DWORD dst_unused:UNUSED_PAD src0_sel:DWORD src1_sel:WORD_1\n\t" \
  "ds_read_b64_tr_b16 v[210:211], v248 offset:50216\n\t" \
  "ds_read_b32 v220, v220 offset:10016\n\t" \
  "v_mfma_f32_16x16x32_bf16 v[64:67], v[212:215], v[194:197], v[64:67]\n\t" \
  "v_fma_mix_f32 v229, |v163|, v229, v229 op_sel:[0,1,0] op_sel_hi:[0,1,1] clamp\n\t" \
  "v_fma_mix_f32 v228, |v162|, v228, v228 op_sel:[0,1,0] op_sel_hi:[0,1,1] clamp\n\t" \
  "ds_read_b32 v223, v223 offset:10016\n\t" \
  "v_mfma_f32_16x16x32_bf16 v[88:91], v[212:215], v[202:205], v[88:91]\n\t" \
  "v_fma_mix_f32 v231, |v161|, v231, v231 op_sel:[0,1,0] op_sel_hi:[0,1,1] clamp\n\t" \
  "v_fma_mix_f32 v230, |v160|, v230, v230 op_sel:[0,1,0] op_sel_hi:[0,1,1] clamp\n\t" \
  "s_waitcnt lgkmcnt(5)\n\t" \
  "ds_read_b64_tr_b16 v[212:213], v248 offset:52264\n\t" \
  "ds_read_b32 v222, v222 offset:10016\n\t" \
  "v_mfma_f32_16x16x32_bf16 v[20:23], v[216:219], v[178:181], v[20:23]\n\t" \
  "v_or_b32 v171, v232, v233\n\t" \
  "v_or_b32 v170, v234, v235\n\t" \
  "ds_read_b64_tr_b16 v[214:215], v248 offset:53288\n\t" \
  "v_mfma_f32_16x16x32_bf16 v[44:47], v[216:219], v[186:189], v[44:47]\n\t" \
  "v_fma_f32 v163, |v163|, v229, v163 mul:4\n\t" \
  "v_fma_f32 v162, |v162|, v228, v162 mul:4\n\t" \
  "ds_read_u16_d16_hi v225, v225 offset:10528\n\t" \
  "v_mfma_f32_16x16x32_bf16 v[68:71], v[216:219], v[194:197], v[68:71]\n\t" \
  "v_fma_f32 v161, |v161|, v231, v161 mul:4\n\t" \
  "v_fma_f32 v160, |v160|, v230, v160 mul:4\n\t" \
  "ds_read_u16 v224, v224 offset:10528\n\t" \
  "v_mfma_f32_16x16x32_bf16 v[92:95], v[216:219], v[202:205], v[92:95]\n\t" \
  "s_add_u32 m0, s46, s36\n\t" \
  "v_cvt_pk_bf16_f32 v163, v162, v163\n\t" \
  "v_cvt_pk_bf16_f32 v162, v160, v161\n\t" \
  "s_add_u32 s3, s34, s51\n\t" \
  "s_waitcnt lgkmcnt(7)\n\t" \
  "s_mov_b64 exec, s[68:69]\n\t" \
  "buffer_load_dwordx4 v252, s[16:19], s3 offen lds\n\t" \
  "v_mfma_f32_16x16x32_bf16 v[176:179], v[208:211], v[96:99], 0\n\t" \
  "s_mov_b64 exec, -1\n\t" \
  "v_cvt_pknorm_i16_f32 v228, |v164|, |v165|\n\t" \
  "v_cvt_pknorm_i16_f32 v230, |v166|, |v167|\n\t" \
  "ds_read_b64_tr_b16 v[216:217], v248 offset:51240\n\t" \
  "v_mfma_f32_16x16x32_bf16 v[184:187], v[208:211], v[100:103], 0\n\t" \
  "v_lshlrev_b32_sdwa v229, 2, v228 dst_sel:DWORD dst_unused:UNUSED_PAD src0_sel:DWORD src1_sel:BYTE_3\n\t" \
  "v_lshlrev_b32_sdwa v228, 2, v228 dst_sel:DWORD dst_unused:UNUSED_PAD src0_sel:DWORD src1_sel:BYTE_1\n\t" \
  "ds_read_b64_tr_b16 v[218:219], v248 offset:54312\n\t" \
  "v_mfma_f32_16x16x32_bf16 v[192:195], v[208:211], v[104:107], 0\n\t" \
  "v_lshlrev_b32_sdwa v231, 2, v230 dst_sel:DWORD dst_unused:UNUSED_PAD src0_sel:DWORD src1_sel:BYTE_3\n\t" \
  "v_lshlrev_b32_sdwa v230, 2, v230 dst_sel:DWORD dst_unused:UNUSED_PAD src0_sel:DWORD src1_sel:BYTE_1\n\t" \
  "ds_read_u16_d16_hi v227, v227 offset:10528\n\t" \
  "v_mfma_f32_16x16x32_bf16 v[200:203], v[208:211], v[108:111], 0\n\t" \
  "v_lshlrev_b32_sdwa v233, 1, v173 dst_sel:DWORD dst_unused:UNUSED_PAD src0_sel:DWORD src1_sel:WORD_1\n\t" \
  "s_waitcnt lgkmcnt(5)\n\t" \
  "ds_read_b64_tr_b16 v[208:209], v248 offset:49704\n\t" \
  "ds_read_u16 v226, v226 offset:10528\n\t" \
  "v_mfma_f32_16x16x32_bf16 v[176:179], v[212:215], v[112:115], v[176:179]\n\t" \
  "v_lshlrev_b32_sdwa v232, 1, v172 dst_sel:DWORD dst_unused:UNUSED_PAD src0_sel:DWORD src1_sel:WORD_1\n\t" \
  "v_lshlrev_b32_sdwa v235, 1, v175 dst_sel:DWORD dst_unused:UNUSED_PAD src0_sel:DWORD src1_sel:WORD_1\n\t" \
  "ds_read_b64_tr_b16 v[210:211], v248 offset:50728\n\t" \
  "v_mfma_f32_16x16x32_bf16 v[184:187], v[212:215], v[116:119], v[184:187]\n\t" \
  "v_lshlrev_b32_sdwa v234, 1, v174 dst_sel:DWORD dst_unused:UNUSED_PAD src0_sel:DWORD src1_sel:WORD_1\n\t" \
  "v_fma_mix_f32 v221, |v149|, v221, v221 op_sel:[0,1,0] op_sel_hi:[0,1,1] clamp\n\t" \
  "ds_read_b32 v229, v229 offset:10016\n\t" \
  "ds_read_b32 v228, v228 offset:10016\n\t" \
  "v_mfma_f32_16x16x32_bf16 v[192:195], v[212:215], v[120:123], v[192:195]\n\t" \
  "v_fma_mix_f32 v220, |v148|, v220, v220 op_sel:[0,1,0] op_sel_hi:[0,1,1] clamp\n\t" \
  "ds_read_b32 v231, v231 offset:10016\n\t" \
  "ds_read_b32 v230, v230 offset:10016\n\t" \
  "v_mfma_f32_16x16x32_bf16 v[200:203], v[212:215], v[124:127], v[200:203]\n\t" \
  "v_fma_mix_f32 v223, |v151|, v223, v223 op_sel:[0,1,0] op_sel_hi:[0,1,1] clamp\n\t" \
  "v_fma_mix_f32 v222, |v150|, v222, v222 op_sel:[0,1,0] op_sel_hi:[0,1,1] clamp\n\t" \
  "s_waitcnt lgkmcnt(5)\n\t" \
  "ds_read_b64_tr_b16 v[212:213], v248 offset:52776\n\t" \
  "ds_read_u16_d16_hi v233, v233 offset:10528\n\t" \
  "v_mfma_f32_16x16x32_bf16 v[176:179], v[216:219], v[128:131], v[176:179]\n\t" \
  "v_or_b32 v156, v224, v225\n\t" \
  "v_or_b32 v157, v226, v227\n\t" \
  "ds_read_b64_tr_b16 v[214:215], v248 offset:53800\n\t" \
  "v_mfma_f32_16x16x32_bf16 v[184:187], v[216:219], v[132:135], v[184:187]\n\t" \
  "v_fma_f32 v149, |v149|, v221, v149 mul:4\n\t" \
  "v_fma_f32 v148, |v148|, v220, v148 mul:4\n\t" \
  "ds_read_u16 v232, v232 offset:10528\n\t" \
  "v_mfma_f32_16x16x32_bf16 v[192:195], v[216:219], v[136:139], v[192:195]\n\t" \
  "v_fma_f32 v151, |v151|, v223, v151 mul:4\n\t" \
  "v_fma_f32 v150, |v150|, v222, v150 mul:4\n\t" \
  "ds_read_u16_d16_hi v235, v235 offset:10528\n\t" \
  "v_mfma_f32_16x16x32_bf16 v[200:203], v[216:219], v[140:143], v[200:203]\n\t" \
  "v_cvt_pk_bf16_f32 v148, v148, v149\n\t" \
  "v_cvt_pk_bf16_f32 v149, v150, v151\n\t" \
  "ds_read_b64_tr_b16 v[216:217], v248 offset:51752\n\t" \
  "s_waitcnt lgkmcnt(10)\n\t" \
  "v_mfma_f32_16x16x32_bf16 v[180:183], v[208:211], v[96:99], 0\n\t" \
  "v_cvt_pknorm_i16_f32 v220, |v178|, |v179|\n\t" \
  "v_cvt_pknorm_i16_f32 v222, |v176|, |v177|\n\t" \
  "s_waitcnt vmcnt(2)\n\t" \
  "ds_read_b64_tr_b16 v[218:219], v248 offset:54824\n\t" \
  "v_mfma_f32_16x16x32_bf16 v[188:191], v[208:211], v[100:103], 0\n\t" \
  "v_lshlrev_b32_sdwa v221, 2, v220 dst_sel:DWORD dst_unused:UNUSED_PAD src0_sel:DWORD src1_sel:BYTE_3\n\t" \
  "v_lshlrev_b32_sdwa v220, 2, v220 dst_sel:DWORD dst_unused:UNUSED_PAD src0_sel:DWORD src1_sel:BYTE_1\n\t" \
  "s_mov_b64 exec, 3\n\t" \
  "ds_add_rtn_u32 v246, v251, v250 offset:47176\n\t" \
  "v_mfma_f32_16x16x32_bf16 v[196:199], v[208:211], v[104:107], 0\n\t" \
  "s_mov_b64 exec, -1\n\t" \
  "v_lshlrev_b32_sdwa v223, 2, v222 dst_sel:DWORD dst_unused:UNUSED_PAD src0_sel:DWORD src1_sel:BYTE_3\n\t" \
  "v_lshlrev_b32_sdwa v222, 2, v222 dst_sel:DWORD dst_unused:UNUSED_PAD src0_sel:DWORD src1_sel:BYTE_1\n\t" \
  "ds_read_u16 v234, v234 offset:10528\n\t" \
  "v_mfma_f32_16x16x32_bf16 v[204:207], v[208:211], v[108:111], 0\n\t" \
  "s_waitcnt lgkmcnt(1)\n\t" \
  "v_lshlrev_b32_sdwa v225, 1, v187 dst_sel:DWORD dst_unused:UNUSED_PAD src0_sel:DWORD src1_sel:WORD_1\n\t" \
  "v_readfirstlane_b32 s4, v246\n\t" \
  "ds_read_b32 v221, v221 offset:10016\n\t" \
  "v_mfma_f32_16x16x32_bf16 v[180:183], v[212:215], v[112:115], v[180:183]\n\t" \
  "s_cmpk_lt_u32 s4, 6144\n\t" \
  "v_lshlrev_b32_sdwa v224, 1, v186 dst_sel:DWORD dst_unused:UNUSED_PAD src0_sel:DWORD src1_sel:WORD_1\n\t" \
  "s_cbranch_scc1 .Lcnx_w1_6\n.Lcnx_g1_6:\n\t" \
  "ds_read_b32 v220, v220 offset:10016\n\t" \
  "v_mfma_f32_16x16x32_bf16 v[188:191], v[212:215], v[116:119], v[188:191]\n\t" \
  "v_lshlrev_b32_sdwa v227, 1, v185 dst_sel:DWORD dst_unused:UNUSED_PAD src0_sel:DWORD src1_sel:WORD_1\n\t" \
  "v_lshlrev_b32_sdwa v226, 1, v184 dst_sel:DWORD dst_unused:UNUSED_PAD src0_sel:DWORD src1_sel:WORD_1\n\t" \
  "ds_read_b64_tr_b16 v[208:209], v249 offset:24600\n\t" \
  "v_mfma_f32_16x16x32_bf16 v[196:199], v[212:215], v[120:123], v[196:199]\n\t" \
  "v_fma_mix_f32 v229, |v165|, v229, v229 op_sel:[0,1,0] op_sel_hi:[0,1,1] clamp\n\t" \
  "v_fma_mix_f32 v228, |v164|, v228, v228 op_sel:[0,1,0] op_sel_hi:[0,1,1] clamp\n\t" \
  "ds_read_b64_tr_b16 v[210:211], v249 offset:26136\n\t" \
  "ds_read_b32 v223, v223 offset:10016\n\t" \
  "v_mfma_f32_16x16x32_bf16 v[204:207], v[212:215], v[124:127], v[204:207]\n\t" \
  "v_fma_mix_f32 v231, |v167|, v231, v231 op_sel:[0,1,0] op_sel_hi:[0,1,1] clamp\n\t" \
  "v_fma_mix_f32 v230, |v166|, v230, v230 op_sel:[0,1,0] op_sel_hi:[0,1,1] clamp\n\t" \
  "s_waitcnt lgkmcnt(5)\n\t" \
  "ds_read_b64_tr_b16 v[212:213], v249 offset:24608\n\t" \
  "ds_read_b32 v222, v222 offset:10016\n\t" \
  "v_mfma_f32_16x16x32_bf16 v[180:183], v[216:219], v[128:131], v[180:183]\n\t" \
  "s_mov_b32 m0, s46\n\t" \
  "v_or_b32 v172, v232, v233\n\t" \
  "v_or_b32 v173, v234, v235\n\t" \
  "s_mov_b32 exec_hi, 0xffff\n\t" \
  "buffer_load_dwordx4 v252, s[16:19], s33 offen lds\n\t" \
  "v_mfma_f32_16x16x32_bf16 v[188:191], v[216:219], v[132:135], v[188:191]\n\t" \
  "s_mov_b32 exec_hi, -1\n\t" \
  "v_fma_f32 v165, |v165|, v229, v165 mul:4\n\t" \
  "v_fma_f32 v164, |v164|, v228, v164 mul:4\n\t" \
  "s_add_u32 s33, s33, s32\n\t" \
  "ds_read_b64_tr_b16 v[214:215], v249 offset:26144\n\t" \
  "v_mfma_f32_16x16x32_bf16 v[196:199], v[216:219], v[136:139], v[196:199]\n\t" \
  "v_fma_f32 v167, |v167|, v231, v167 mul:4\n\t" \
  "v_fma_f32 v166, |v166|, v230, v166 mul:4\n\t" \
  "ds_read_u16_d16_hi v225, v225 offset:10528\n\t" \
  "v_mfma_f32_16x16x32_bf16 v[204:207], v[216:219], v[140:143], v[204:207]\n\t" \
  "v_cvt_pk_bf16_f32 v164, v164, v165\n\t" \
  "v_cvt_pk_bf16_f32 v165, v166, v167\n\t" \
  "ds_read_b64_tr_b16 v[216:217], v249 offset:24664\n\t" \
  "s_waitcnt lgkmcnt(6)\n\t" \
  "v_mfma_f32_16x16x32_bf16 v[0:3], v[208:211], v[146:149], v[0:3]\n\t" \
  "v_cvt_pknorm_i16_f32 v228, |v194|, |v195|\n\t" \
  "v_cvt_pknorm_i16_f32 v230, |v192|, |v193|\n\t" \
  "ds_read_b64_tr_b16 v[218:219], v249 offset:26200\n\t" \
  "v_mfma_f32_16x16x32_bf16 v[24:27], v[208:211], v[154:157], v[24:27]\n\t" \
  "v_lshlrev_b32_sdwa v229, 2, v228 dst_sel:DWORD dst_unused:UNUSED_PAD src0_sel:DWORD src1_sel:BYTE_3\n\t" \
  "v_lshlrev_b32_sdwa v228, 2, v228 dst_sel:DWORD dst_unused:UNUSED_PAD src0_sel:DWORD src1_sel:BYTE_1\n\t" \
  "ds_read_u16 v224, v224 offset:10528\n\t" \
  "v_mfma_f32_16x16x32_bf16 v[48:51], v[208:211], v[162:165], v[48:51]\n\t" \
  "v_lshlrev_b32_sdwa v231, 2, v230 dst_sel:DWORD dst_unused:UNUSED_PAD src0_sel:DWORD src1_sel:BYTE_3\n\t" \
  "v_lshlrev_b32_sdwa v230, 2, v230 dst_sel:DWORD dst_unused:UNUSED_PAD src0_sel:DWORD src1_sel:BYTE_1\n\t" \
  "ds_read_u16_d16_hi v227, v227 offset:10528\n\t" \
  "v_mfma_f32_16x16x32_bf16 v[72:75], v[208:211], v[170:173], v[72:75]\n\t" \
  "s_add_u32 m0, s46, s35\n\t" \
  "v_lshlrev_b32_sdwa v233, 1, v203 dst_sel:DWORD dst_unused:UNUSED_PAD src0_sel:DWORD src1_sel:WORD_1\n\t" \
  "v_lshlrev_b32_sdwa v232, 1, v202 dst_sel:DWORD dst_unused:UNUSED_PAD src0_sel:DWORD src1_sel:WORD_1\n\t" \
  "s_waitcnt lgkmcnt(5)\n\t" \
  "s_mov_b32 exec_hi, 0xffff\n\t" \
  "buffer_load_dwordx4 v252, s[16:19], s34 offen lds\n\t" \
  "v_mfma_f32_16x16x32_bf16 v[4:7], v[212:215], v[146:149], v[4:7]\n\t" \
  "s_mov_b32 exec_hi, -1\n\t" \
  "v_lshlrev_b32_sdwa v235, 1, v201 dst_sel:DWORD dst_unused:UNUSED_PAD src0_sel:DWORD src1_sel:WORD_1\n\t" \
  "s_add_u32 s34, s34, s32\n\t" \
  "ds_read_b64_tr_b16 v[208:209], v249 offset:24672\n\t" \
  "ds_read_u16 v226, v226 offset:10528\n\t" \
  "v_mfma_f32_16x16x32_bf16 v[28:31], v[212:215], v[154:157], v[28:31]\n\t" \
  "v_lshlrev_b32_sdwa v234, 1, v200 dst_sel:DWORD dst_unused:UNUSED_PAD src0_sel:DWORD src1_sel:WORD_1\n\t" \
  "ds_read_b64_tr_b16 v[210:211], v249 offset:26208\n\t" \
  "ds_read_b32 v229, v229 offset:10016\n\t" \
  "v_mfma_f32_16x16x32_bf16 v[52:55], v[212:215], v[162:165], v[52:55]\n\t" \
  "v_fma_mix_f32 v221, |v179|, v221, v221 op_sel:[0,1,0] op_sel_hi:[0,1,1] clamp\n\t" \
  "v_fma_mix_f32 v220, |v178|, v220, v220 op_sel:[0,1,0] op_sel_hi:[0,1,1] clamp\n\t" \
  "ds_read_b32 v228, v228 offset:10016\n\t" \
  "ds_read_b32 v231, v231 offset:10016\n\t" \
  "v_mfma_f32_16x16x32_bf16 v[76:79], v[212:215], v[170:173], v[76:79]\n\t" \
  "v_fma_mix_f32 v223, |v177|, v223, v223 op_sel:[0,1,0] op_sel_hi:[0,1,1] clamp\n\t" \
  "v_fma_mix_f32 v222, |v176|, v222, v222 op_sel:[0,1,0] op_sel_hi:[0,1,1] clamp\n\t" \
  "s_waitcnt lgkmcnt(4)\n\t" \
  "ds_read_b64_tr_b16 v[212:213], v249 offset:24728\n\t" \
  "ds_read_b32 v230, v230 offset:10016\n\t" \
  "v_mfma_f32_16x16x32_bf16 v[8:11], v[216:219], v[146:149], v[8:11]\n\t" \
  "v_or_b32 v187, v224, v225\n\t" \
  "v_or_b32 v186, v226, v227\n\t" \
  "ds_read_b64_tr_b16 v[214:215], v249 offset:26264\n\t" \
  "v_mfma_f32_16x16x32_bf16 v[32:35], v[216:219], v[154:157], v[32:35]\n\t" \
  "v_fma_f32 v179, |v179|, v221, v179 mul:4\n\t" \
  "v_fma_f32 v178, |v178|, v220, v178 mul:4\n\t" \
  "ds_read_u16_d16_hi v233, v233 offset:10528\n\t" \
  "v_mfma_f32_16x16x32_bf16 v[56:59], v[216:219], v[162:165], v[56:59]\n\t" \
  "v_fma_f32 v177, |v177|, v223, v177 mul:4\n\t" \
  "v_fma_f32 v176, |v176|, v222, v176 mul:4\n\t" \
  "ds_read_u16 v232, v232 offset:10528\n\t" \
  "v_mfma_f32_16x16x32_bf16 v[80:83], v[216:219], v[170:173], v[80:83]\n\t" \
  "v_cvt_pk_bf16_f32 v179, v178, v179\n\t" \
  "v_cvt_pk_bf16_f32 v178, v176, v177\n\t" \
  "ds_read_b64_tr_b16 v[216:217], v249 offset:24736\n\t" \
  "s_waitcnt lgkmcnt(9)\n\t" \
  "v_mfma_f32_16x16x32_bf16 v[12:15], v[208:211], v[146:149], v[12:15]\n\t" \
  "v_cvt_pknorm_i16_f32 v220, |v180|, |v181|\n\t" \
  "v_cvt_pknorm_i16_f32 v222, |v182|, |v183|\n\t" \
  "ds_read_b64_tr_b16 v[218:219], v249 offset:26272\n\t" \
  "v_mfma_f32_16x16x32_bf16 v[36:39], v[208:211], v[154:157], v[36:39]\n\t" \
  "v_lshlrev_b32_sdwa v221, 2, v220 dst_sel:DWORD dst_unused:UNUSED_PAD src0_sel:DWORD src1_sel:BYTE_3\n\t" \
  "v_lshlrev_b32_sdwa v220, 2, v220 dst_sel:DWORD dst_unused:UNUSED_PAD src0_sel:DWORD src1_sel:BYTE_1\n\t" \
  "ds_read_u16_d16_hi v235, v235 offset:10528\n\t" \
  "v_mfma_f32_16x16x32_bf16 v[60:63], v[208:211], v[162:165], v[60:63]\n\t" \
  "v_lshlrev_b32_sdwa v223, 2, v222 dst_sel:DWORD dst_unused:UNUSED_PAD src0_sel:DWORD src1_sel:BYTE_3\n\t" \
  "v_lshlrev_b32_sdwa v222, 2, v222 dst_sel:DWORD dst_unused:UNUSED_PAD src0_sel:DWORD src1_sel:BYTE_1\n\t" \
  "ds_read_u16 v234, v234 offset:10528\n\t" \
  "v_mfma_f32_16x16x32_bf16 v[84:87], v[208:211], v[170:173], v[84:87]\n\t" \
  "s_add_u32 m0, s47, s50\n\t" \
  "v_lshlrev_b32_sdwa v225, 1, v189 dst_sel:DWORD dst_unused:UNUSED_PAD src0_sel:DWORD src1_sel:WORD_1\n\t" \
  "v_lshlrev_b32_sdwa v224, 1, v188 dst_sel:DWORD dst_unused:UNUSED_PAD src0_sel:DWORD src1_sel:WORD_1\n\t" \
  "s_add_u32 s3, s33, s51\n\t" \
  "s_waitcnt lgkmcnt(6)\n\t" \
  "s_mov_b64 exec, s[68:69]\n\t" \
  "buffer_load_dwordx4 v252, s[16:19], s3 offen lds\n\t" \
  "v_mfma_f32_16x16x32_bf16 v[16:19], v[212:215], v[146:149], v[16:19]\n\t" \
  "s_mov_b64 exec, -1\n\t" \
  "v_lshlrev_b32_sdwa v227, 1, v191 dst_sel:DWORD dst_unused:UNUSED_PAD src0_sel:DWORD src1_sel:WORD_1\n\t" \
  "ds_read_b64_tr_b16 v[208:209], v248 offset:36904\n\t" \
  "ds_read_b32 v221, v221 offset:10016\n\t" \
  "v_mfma_f32_16x16x32_bf16 v[40:43], v[212:215], v[154:157], v[40:43]\n\t" \
  "v_lshlrev_b32_sdwa v226, 1, v190 dst_sel:DWORD dst_unused:UNUSED_PAD src0_sel:DWORD src1_sel:WORD_1\n\t" \
  "ds_read_b64_tr_b16 v[210:211], v248 offset:37928\n\t" \
  "ds_read_b32 v220, v220 offset:10016\n\t" \
  "v_mfma_f32_16x16x32_bf16 v[64:67], v[212:215], v[162:165], v[64:67]\n\t" \
  "v_fma_mix_f32 v229, |v195|, v229, v229 op_sel:[0,1,0] op_sel_hi:[0,1,1] clamp\n\t" \
  "v_fma_mix_f32 v228, |v194|, v228, v228 op_sel:[0,1,0] op_sel_hi:[0,1,1] clamp\n\t" \
  "ds_read_b32 v223, v223 offset:10016\n\t" \
  "v_mfma_f32_16x16x32_bf16 v[88:91], v[212:215], v[170:173], v[88:91]\n\t" \
  "v_fma_mix_f32 v231, |v193|, v231, v231 op_sel:[0,1,0] op_sel_hi:[0,1,1] clamp\n\t" \
  "v_fma_mix_f32 v230, |v192|, v230, v230 op_sel:[0,1,0] op_sel_hi:[0,1,1] clamp\n\t" \
  "s_waitcnt lgkmcnt(5)\n\t" \
  "ds_read_b64_tr_b16 v[212:213], v248 offset:39976\n\t" \
  "ds_read_b32 v222, v222 offset:10016\n\t" \
  "v_mfma_f32_16x16x32_bf16 v[20:23], v[216:219], v[146:149], v[20:23]\n\t" \
  "v_or_b32 v203, v232, v233\n\t" \
  "v_or_b32 v202, v234, v235\n\t" \
  "ds_read_b64_tr_b16 v[214:215], v248 offset:41000\n\t" \
  "v_mfma_f32_16x16x32_bf16 v[44:47], v[216:219], v[154:157], v[44:47]\n\t" \
  "v_fma_f32 v195, |v195|, v229, v195 mul:4\n\t" \
  "v_fma_f32 v194, |v194|, v228, v194 mul:4\n\t" \
  "ds_read_u16_d16_hi v225, v225 offset:10528\n\t" \
  "v_mfma_f32_16x16x32_bf16 v[68:71], v[216:219], v[162:165], v[68:71]\n\t" \
  "v_fma_f32 v193, |v193|, v231, v193 mul:4\n\t" \
  "v_fma_f32 v192, |v192|, v230, v192 mul:4\n\t" \
  "ds_read_u16 v224, v224 offset:10528\n\t" \
  "v_mfma_f32_16x16x32_bf16 v[92:95], v[216:219], v[170:173], v[92:95]\n\t" \
  "s_add_u32 m0, s47, s36\n\t" \
  "v_cvt_pk_bf16_f32 v195, v194, v195\n\t" \
  "v_cvt_pk_bf16_f32 v194, v192, v193\n\t" \
  "s_add_u32 s3, s34, s51\n\t" \
  "s_waitcnt lgkmcnt(7)\n\t" \
  "s_mov_b64 exec, s[68:69]\n\t" \
  "buffer_load_dwordx4 v252, s[16:19], s3 offen lds\n\t" \
  "v_mfma_f32_16x16x32_bf16 v[144:147], v[208:211], v[96:99], 0\n\t" \
  "s_mov_b64 exec, -1\n\t" \
  "v_cvt_pknorm_i16_f32 v228, |v196|, |v197|\n\t" \
  "v_cvt_pknorm_i16_f32 v230, |v198|, |v199|\n\t" \
  "ds_read_b64_tr_b16 v[216:217], v248 offset:38952\n\t" \
  "v_mfma_f32_16x16x32_bf16 v[152:155], v[208:211], v[100:103], 0\n\t" \
  "v_lshlrev_b32_sdwa v229, 2, v228 dst_sel:DWORD dst_unused:UNUSED_PAD src0_sel:DWORD src1_sel:BYTE_3\n\t" \
  "v_lshlrev_b32_sdwa v228, 2, v228 dst_sel:DWORD dst_unused:UNUSED_PAD src0_sel:DWORD src1_sel:BYTE_1\n\t" \
  "ds_read_b64_tr_b16 v[218:219], v248 offset:42024\n\t" \
  "v_mfma_f32_16x16x32_bf16 v[160:163], v[208:211], v[104:107], 0\n\t" \
  "v_lshlrev_b32_sdwa v231, 2, v230 dst_sel:DWORD dst_unused:UNUSED_PAD src0_sel:DWORD src1_sel:BYTE_3\n\t" \
  "v_lshlrev_b32_sdwa v230, 2, v230 dst_sel:DWORD dst_unused:UNUSED_PAD src0_sel:DWORD src1_sel:BYTE_1\n\t" \
  "ds_read_u16_d16_hi v227, v227 offset:10528\n\t" \
  "v_mfma_f32_16x16x32_bf16 v[168:171], v[208:211], v[108:111], 0\n\t" \
  "v_lshlrev_b32_sdwa v233, 1, v205 dst_sel:DWORD dst_unused:UNUSED_PAD src0_sel:DWORD src1_sel:WORD_1\n\t" \
  "s_waitcnt lgkmcnt(5)\n\t" \
  "ds_read_b64_tr_b16 v[208:209], v248 offset:37416\n\t" \
  "ds_read_u16 v226, v226 offset:10528\n\t" \
  "v_mfma_f32_16x16x32_bf16 v[144:147], v[212:215], v[112:115], v[144:147]\n\t" \
  "v_lshlrev_b32_sdwa v232, 1, v204 dst_sel:DWORD dst_unused:UNUSED_PAD src0_sel:DWORD src1_sel:WORD_1\n\t" \
  "v_lshlrev_b32_sdwa v235, 1, v207 dst_sel:DWORD dst_unused:UNUSED_PAD src0_sel:DWORD src1_sel:WORD_1\n\t" \
  "ds_read_b64_tr_b16 v[210:211], v248 offset:38440\n\t" \
  "v_mfma_f32_16x16x32_bf16 v[152:155], v[212:215], v[116:119], v[152:155]\n\t" \
  "v_lshlrev_b32_sdwa v234, 1, v206 dst_sel:DWORD dst_unused:UNUSED_PAD src0_sel:DWORD src1_sel:WORD_1\n\t" \
  "v_fma_mix_f32 v221, |v181|, v221, v221 op_sel:[0,1,0] op_sel_hi:[0,1,1] clamp\n\t" \
  "ds_read_b32 v229, v229 offset:10016\n\t" \
  "ds_read_b32 v228, v228 offset:10016\n\t" \
  "v_mfma_f32_16x16x32_bf16 v[160:163], v[212:215], v[120:123], v[160:163]\n\t" \
  "v_fma_mix_f32 v220, |v180|, v220, v220 op_sel:[0,1,0] op_sel_hi:[0,1,1] clamp\n\t" \
  "ds_read_b32 v231, v231 offset:10016\n\t" \
  "ds_read_b32 v230, v230 offset:10016\n\t" \
  "v_mfma_f32_16x16x32_bf16 v[168:171], v[212:215], v[124:127], v[168:171]\n\t" \
  "v_fma_mix_f32 v223, |v183|, v223, v223 op_sel:[0,1,0] op_sel_hi:[0,1,1] clamp\n\t" \
  "v_fma_mix_f32 v222, |v182|, v222, v222 op_sel:[0,1,0] op_sel_hi:[0,1,1] clamp\n\t" \
  "s_waitcnt lgkmcnt(5)\n\t" \
  "ds_read_b64_tr_b16 v[212:213], v248 offset:40488\n\t" \
  "ds_read_u16_d16_hi v233, v233 offset:10528\n\t" \
  "v_mfma_f32_16x16x32_bf16 v[144:147], v[216:219], v[128:131], v[144:147]\n\t" \
  "v_or_b32 v188, v224, v225\n\t" \
  "v_or_b32 v189, v226, v227\n\t" \
  "ds_read_b64_tr_b16 v[214:215], v248 offset:41512\n\t" \
  "v_mfma_f32_16x16x32_bf16 v[152:155], v[216:219], v[132:135], v[152:155]\n\t" \
  "v_fma_f32 v181, |v181|, v221, v181 mul:4\n\t" \
  "v_fma_f32 v180, |v180|, v220, v180 mul:4\n\t" \
  "ds_read_u16 v232, v232 offset:10528\n\t" \
  "v_mfma_f32_16x16x32_bf16 v[160:163], v[216:219], v[136:139], v[160:163]\n\t" \
  "v_fma_f32 v183, |v183|, v223, v183 mul:4\n\t" \
  "v_fma_f32 v182, |v182|, v222, v182 mul:4\n\t" \
  "ds_read_u16_d16_hi v235, v235 offset:10528\n\t" \
  "v_mfma_f32_16x16x32_bf16 v[168:171], v[216:219], v[140:143], v[168:171]\n\t" \
  "v_cvt_pk_bf16_f32 v180, v180, v181\n\t" \
  "v_cvt_pk_bf16_f32 v181, v182, v183\n\t" \
  "ds_read_b64_tr_b16 v[216:217], v248 offset:39464\n\t" \
  "s_waitcnt lgkmcnt(10)\n\t" \
  "v_mfma_f32_16x16x32_bf16 v[148:151], v[208:211], v[96:99], 0\n\t" \
  "v_cvt_pknorm_i16_f32 v220, |v146|, |v147|\n\t" \
  "v_cvt_pknorm_i16_f32 v222, |v144|, |v145|\n\t" \
  "s_waitcnt vmcnt(2)\n\t" \
  "ds_read_b64_tr_b16 v[218:219], v248 offset:42536\n\t" \
  "v_mfma_f32_16x16x32_bf16 v[156:159], v[208:211], v[100:103], 0\n\t" \
  "v_lshlrev_b32_sdwa v221, 2, v220 dst_sel:DWORD dst_unused:UNUSED_PAD src0_sel:DWORD src1_sel:BYTE_3\n\t" \
  "v_lshlrev_b32_sdwa v220, 2, v220 dst_sel:DWORD dst_unused:UNUSED_PAD src0_sel:DWORD src1_sel:BYTE_1\n\t" \
  "s_mov_b64 exec, 3\n\t" \
  "ds_add_rtn_u32 v246, v251, v250 offset:47184\n\t" \
  "v_mfma_f32_16x16x32_bf16 v[164:167], v[208:211], v[104:107], 0\n\t" \
  "s_mov_b64 exec, -1\n\t" \
  "v_lshlrev_b32_sdwa v223, 2, v222 dst_sel:DWORD dst_unused:UNUSED_PAD src0_sel:DWORD src1_sel:BYTE_3\n\t" \
  "v_lshlrev_b32_sdwa v222, 2, v222 dst_sel:DWORD dst_unused:UNUSED_PAD src0_sel:DWORD src1_sel:BYTE_1\n\t" \
  "ds_read_u16 v234, v234 offset:10528\n\t" \
  "v_mfma_f32_16x16x32_bf16 v[172:175], v[208:211], v[108:111], 0\n\t" \
  "s_waitcnt lgkmcnt(1)\n\t" \
  "v_lshlrev_b32_sdwa v225, 1, v155 dst_sel:DWORD dst_unused:UNUSED_PAD src0_sel:DWORD src1_sel:WORD_1\n\t" \
  "v_readfirstlane_b32 s4, v246\n\t" \
  "ds_read_b32 v221, v221 offset:10016\n\t" \
  "v_mfma_f32_16x16x32_bf16 v[148:151], v[212:215], v[112:115], v[148:151]\n\t" \
  "s_cmpk_lt_u32 s4, 6144\n\t" \
  "v_lshlrev_b32_sdwa v224, 1, v154 dst_sel:DWORD dst_unused:UNUSED_PAD src0_sel:DWORD src1_sel:WORD_1\n\t" \
  "s_cbranch_scc1 .Lcnx_w1_7\n.Lcnx_g1_7:\n\t" \
  "ds_read_b32 v220, v220 offset:10016\n\t" \
  "v_mfma_f32_16x16x32_bf16 v[156:159], v[212:215], v[116:119], v[156:159]\n\t" \
  "v_lshlrev_b32_sdwa v227, 1, v153 dst_sel:DWORD dst_unused:UNUSED_PAD src0_sel:DWORD src1_sel:WORD_1\n\t" \
  "v_lshlrev_b32_sdwa v226, 1, v152 dst_sel:DWORD dst_unused:UNUSED_PAD src0_sel:DWORD src1_sel:WORD_1\n\t" \
  "ds_read_b64_tr_b16 v[208:209], v249 offset:30752\n\t" \
  "v_mfma_f32_16x16x32_bf16 v[164:167], v[212:215], v[120:123], v[164:167]\n\t" \
  "v_fma_mix_f32 v229, |v197|, v229, v229 op_sel:[0,1,0] op_sel_hi:[0,1,1] clamp\n\t" \
  "v_fma_mix_f32 v228, |v196|, v228, v228 op_sel:[0,1,0] op_sel_hi:[0,1,1] clamp\n\t" \
  "ds_read_b64_tr_b16 v[210:211], v249 offset:32288\n\t" \
  "ds_read_b32 v223, v223 offset:10016\n\t" \
  "v_mfma_f32_16x16x32_bf16 v[172:175], v[212:215], v[124:127], v[172:175]\n\t" \
  "v_fma_mix_f32 v231, |v199|, v231, v231 op_sel:[0,1,0] op_sel_hi:[0,1,1] clamp\n\t" \
  "v_fma_mix_f32 v230, |v198|, v230, v230 op_sel:[0,1,0] op_sel_hi:[0,1,1] clamp\n\t" \
  "s_waitcnt lgkmcnt(5)\n\t" \
  "ds_read_b64_tr_b16 v[212:213], v249 offset:30760\n\t" \
  "ds_read_b32 v222, v222 offset:10016\n\t" \
  "v_mfma_f32_16x16x32_bf16 v[148:151], v[216:219], v[128:131], v[148:151]\n\t" \
  "s_mov_b32 m0, s47\n\t" \
  "v_or_b32 v204, v232, v233\n\t" \
  "v_or_b32 v205, v234, v235\n\t" \
  "s_mov_b32 exec_hi, 0xffff\n\t" \
  "buffer_load_dwordx4 v252, s[16:19], s33 offen lds\n\t" \
  "v_mfma_f32_16x16x32_bf16 v[156:159], v[216:219], v[132:135], v[156:159]\n\t" \
  "s_mov_b32 exec_hi, -1\n\t" \
  "v_fma_f32 v197, |v197|, v229, v197 mul:4\n\t" \
  "v_fma_f32 v196, |v196|, v228, v196 mul:4\n\t" \
  "s_add_u32 s33, s33, s32\n\t" \
  "ds_read_b64_tr_b16 v[214:215], v249 offset:32296\n\t" \
  "v_mfma_f32_16x16x32_bf16 v[164:167], v[216:219], v[136:139], v[164:167]\n\t" \
  "v_fma_f32 v199, |v199|, v231, v199 mul:4\n\t" \
  "v_fma_f32 v198, |v198|, v230, v198 mul:4\n\t" \
  "ds_read_u16_d16_hi v225, v225 offset:10528\n\t" \
  "v_mfma_f32_16x16x32_bf16 v[172:175], v[216:219], v[140:143], v[172:175]\n\t" \
  "v_cvt_pk_bf16_f32 v196, v196, v197\n\t" \
  "v_cvt_pk_bf16_f32 v197, v198, v199\n\t" \
  "ds_read_b64_tr_b16 v[216:217], v249 offset:30816\n\t" \
  "s_waitcnt lgkmcnt(6)\n\t" \
  "v_mfma_f32_16x16x32_bf16 v[0:3], v[208:211], v[178:181], v[0:3]\n\t" \
  "v_cvt_pknorm_i16_f32 v228, |v162|, |v163|\n\t" \
  "v_cvt_pknorm_i16_f32 v230, |v160|, |v161|\n\t" \
  "ds_read_b64_tr_b16 v[218:219], v249 offset:32352\n\t" \
  "v_mfma_f32_16x16x32_bf16 v[24:27], v[208:211], v[186:189], v[24:27]\n\t" \
  "v_lshlrev_b32_sdwa v229, 2, v228 dst_sel:DWORD dst_unused:UNUSED_PAD src0_sel:DWORD src1_sel:BYTE_3\n\t" \
  "v_lshlrev_b32_sdwa v228, 2, v228 dst_sel:DWORD dst_unused:UNUSED_PAD src0_sel:DWORD src1_sel:BYTE_1\n\t" \
  "ds_read_u16 v224, v224 offset:10528\n\t" \
  "v_mfma_f32_16x16x32_bf16 v[48:51], v[208:211], v[194:197], v[48:51]\n\t" \
  "v_lshlrev_b32_sdwa v231, 2, v230 dst_sel:DWORD dst_unused:UNUSED_PAD src0_sel:DWORD src1_sel:BYTE_3\n\t" \
  "v_lshlrev_b32_sdwa v230, 2, v230 dst_sel:DWORD dst_unused:UNUSED_PAD src0_sel:DWORD src1_sel:BYTE_1\n\t" \
  "ds_read_u16_d16_hi v227, v227 offset:10528\n\t" \
  "v_mfma_f32_16x16x32_bf16 v[72:75], v[208:211], v[202:205], v[72:75]\n\t" \
  "s_add_u32 m0, s47, s35\n\t" \
  "v_lshlrev_b32_sdwa v233, 1, v171 dst_sel:DWORD dst_unused:UNUSED_PAD src0_sel:DWORD src1_sel:WORD_1\n\t" \
  "v_lshlrev_b32_sdwa v232, 1, v170 dst_sel:DWORD dst_unused:UNUSED_PAD src0_sel:DWORD src1_sel:WORD_1\n\t" \
  "s_waitcnt lgkmcnt(5)\n\t" \
  "s_mov_b32 exec_hi, 0xffff\n\t" \
  "buffer_load_dwordx4 v252, s[16:19], s34 offen lds\n\t" \
  "v_mfma_f32_16x16x32_bf16 v[4:7], v[212:215], v[178:181], v[4:7]\n\t" \
  "s_mov_b32 exec_hi, -1\n\t" \
  "v_lshlrev_b32_sdwa v235, 1, v169 dst_sel:DWORD dst_unused:UNUSED_PAD src0_sel:DWORD src1_sel:WORD_1\n\t" \
  "s_add_u32 s34, s34, s32\n\t" \
  "ds_read_b64_tr_b16 v[208:209], v249 offset:30824\n\t" \
  "ds_read_u16 v226, v226 offset:10528\n\t" \
  "v_mfma_f32_16x16x32_bf16 v[28:31], v[212:215], v[186:189], v[28:31]\n\t" \
  "v_lshlrev_b32_sdwa v234, 1, v168 dst_sel:DWORD dst_unused:UNUSED_PAD src0_sel:DWORD src1_sel:WORD_1\n\t" \
  "ds_read_b64_tr_b16 v[210:211], v249 offset:32360\n\t" \
  "ds_read_b32 v229, v229 offset:10016\n\t" \
  "v_mfma_f32_16x16x32_bf16 v[52:55], v[212:215], v[194:197], v[52:55]\n\t" \
  "v_fma_mix_f32 v221, |v147|, v221, v221 op_sel:[0,1,0] op_sel_hi:[0,1,1] clamp\n\t" \
  "v_fma_mix_f32 v220, |v146|, v220, v220 op_sel:[0,1,0] op_sel_hi:[0,1,1] clamp\n\t" \
  "ds_read_b32 v228, v228 offset:10016\n\t" \
  "ds_read_b32 v231, v231 offset:10016\n\t" \
  "v_mfma_f32_16x16x32_bf16 v[76:79], v[212:215], v[202:205], v[76:79]\n\t" \
  "v_fma_mix_f32 v223, |v145|, v223, v223 op_sel:[0,1,0] op_sel_hi:[0,1,1] clamp\n\t" \
  "v_fma_mix_f32 v222, |v144|, v222, v222 op_sel:[0,1,0] op_sel_hi:[0,1,1] clamp\n\t" \
  "s_waitcnt lgkmcnt(4)\n\t" \
  "ds_read_b64_tr_b16 v[212:213], v249 offset:30880\n\t" \
  "ds_read_b32 v230, v230 offset:10016\n\t" \
  "v_mfma_f32_16x16x32_bf16 v[8:11], v[216:219], v[178:181], v[8:11]\n\t" \
  "v_or_b32 v155, v224, v225\n\t" \
  "v_or_b32 v154, v226, v227\n\t" \
  "ds_read_b64_tr_b16 v[214:215], v249 offset:32416\n\t" \
  "v_mfma_f32_16x16x32_bf16 v[32:35], v[216:219], v[186:189], v[32:35]\n\t" \
  "v_fma_f32 v147, |v147|, v221, v147 mul:4\n\t" \
  "v_fma_f32 v146, |v146|, v220, v146 mul:4\n\t" \
  "buffer_load_dwordx4 v[236:239], v250, s[8:11], s14 offen offset:0 nt\n\t" \
  "ds_read_u16_d16_hi v233, v233 offset:10528\n\t" \
  "v_mfma_f32_16x16x32_bf16 v[56:59], v[216:219], v[194:197], v[56:59]\n\t" \
  "v_fma_f32 v145, |v145|, v223, v145 mul:4\n\t" \
  "v_fma_f32 v144, |v144|, v222, v144 mul:4\n\t" \
  "ds_read_u16 v232, v232 offset:10528\n\t" \
  "v_mfma_f32_16x16x32_bf16 v[80:83], v[216:219], v[202:205], v[80:83]\n\t" \
  "v_cvt_pk_bf16_f32 v147, v146, v147\n\t" \
  "v_cvt_pk_bf16_f32 v146, v144, v145\n\t" \
  "ds_read_b64_tr_b16 v[216:217], v249 offset:30888\n\t" \
  "s_waitcnt lgkmcnt(9)\n\t" \
  "v_mfma_f32_16x16x32_bf16 v[12:15], v[208:211], v[178:181], v[12:15]\n\t" \
  "v_cvt_pknorm_i16_f32 v220, |v148|, |v149|\n\t" \
  "v_cvt_pknorm_i16_f32 v222, |v150|, |v151|\n\t" \
  "ds_read_b64_tr_b16 v[218:219], v249 offset:32424\n\t" \
  "v_mfma_f32_16x16x32_bf16 v[36:39], v[208:211], v[186:189], v[36:39]\n\t" \
  "v_lshlrev_b32_sdwa v221, 2, v220 dst_sel:DWORD dst_unused:UNUSED_PAD src0_sel:DWORD src1_sel:BYTE_3\n\t" \
  "v_lshlrev_b32_sdwa v220, 2, v220 dst_sel:DWORD dst_unused:UNUSED_PAD src0_sel:DWORD src1_sel:BYTE_1\n\t" \
  "ds_read_u16_d16_hi v235, v235 offset:10528\n\t" \
  "v_mfma_f32_16x16x32_bf16 v[60:63], v[208:211], v[194:197], v[60:63]\n\t" \
  "v_lshlrev_b32_sdwa v223, 2, v222 dst_sel:DWORD dst_unused:UNUSED_PAD src0_sel:DWORD src1_sel:BYTE_3\n\t" \
  "v_lshlrev_b32_sdwa v222, 2, v222 dst_sel:DWORD dst_unused:UNUSED_PAD src0_sel:DWORD src1_sel:BYTE_1\n\t" \
  "ds_read_u16 v234, v234 offset:10528\n\t" \
  "v_mfma_f32_16x16x32_bf16 v[84:87], v[208:211], v[202:205], v[84:87]\n\t" \
  "v_lshlrev_b32_sdwa v225, 1, v157 dst_sel:DWORD dst_unused:UNUSED_PAD src0_sel:DWORD src1_sel:WORD_1\n\t" \
  "v_lshlrev_b32_sdwa v224, 1, v156 dst_sel:DWORD dst_unused:UNUSED_PAD src0_sel:DWORD src1_sel:WORD_1\n\t" \
  "ds_read_b64_tr_b16 v[208:209], v248 offset:0\n\t" \
  "s_waitcnt lgkmcnt(7)\n\t" \
  "v_mfma_f32_16x16x32_bf16 v[16:19], v[212:215], v[178:181], v[16:19]\n\t" \
  "v_lshlrev_b32_sdwa v227, 1, v159 dst_sel:DWORD dst_unused:UNUSED_PAD src0_sel:DWORD src1_sel:WORD_1\n\t" \
  "ds_read_b64_tr_b16 v[210:211], v248 offset:1024\n\t" \
  "ds_read_b32 v221, v221 offset:10016\n\t" \
  "v_mfma_f32_16x16x32_bf16 v[40:43], v[212:215], v[186:189], v[40:43]\n\t" \
  "v_lshlrev_b32_sdwa v226, 1, v158 dst_sel:DWORD dst_unused:UNUSED_PAD src0_sel:DWORD src1_sel:WORD_1\n\t" \
  "v_fma_mix_f32 v229, |v163|, v229, v229 op_sel:[0,1,0] op_sel_hi:[0,1,1] clamp\n\t" \
  "ds_read_b32 v220, v220 offset:10016\n\t" \
  "v_mfma_f32_16x16x32_bf16 v[64:67], v[212:215], v[194:197], v[64:67]\n\t" \
  "v_fma_mix_f32 v228, |v162|, v228, v228 op_sel:[0,1,0] op_sel_hi:[0,1,1] clamp\n\t" \
  "ds_read_b32 v223, v223 offset:10016\n\t" \
  "ds_read_b32 v222, v222 offset:10016\n\t" \
  "v_mfma_f32_16x16x32_bf16 v[88:91], v[212:215], v[202:205], v[88:91]\n\t" \
  "v_fma_mix_f32 v231, |v161|, v231, v231 op_sel:[0,1,0] op_sel_hi:[0,1,1] clamp\n\t" \
  "v_fma_mix_f32 v230, |v160|, v230, v230 op_sel:[0,1,0] op_sel_hi:[0,1,1] clamp\n\t" \
  "s_waitcnt lgkmcnt(6)\n\t" \
  "ds_read_b64_tr_b16 v[212:213], v248 offset:3072\n\t" \
  "ds_read_u16_d16_hi v225, v225 offset:10528\n\t" \
  "v_mfma_f32_16x16x32_bf16 v[20:23], v[216:219], v[178:181], v[20:23]\n\t" \
  "v_or_b32 v171, v232, v233\n\t" \
  "v_or_b32 v170, v234, v235\n\t" \
  "ds_read_b64_tr_b16 v[214:215], v248 offset:4096\n\t" \
  "v_mfma_f32_16x16x32_bf16 v[44:47], v[216:219], v[186:189], v[44:47]\n\t" \
  "v_fma_f32 v163, |v163|, v229, v163 mul:4\n\t" \
  "v_fma_f32 v162, |v162|, v228, v162 mul:4\n\t" \
  "ds_read_u16 v224, v224 offset:10528\n\t" \
  "v_mfma_f32_16x16x32_bf16 v[68:71], v[216:219], v[194:197], v[68:71]\n\t" \
  "v_fma_f32 v161, |v161|, v231, v161 mul:4\n\t" \
  "v_fma_f32 v160, |v160|, v230, v160 mul:4\n\t" \
  "ds_read_u16_d16_hi v227, v227 offset:10528\n\t" \
  "v_mfma_f32_16x16x32_bf16 v[92:95], v[216:219], v[202:205], v[92:95]\n\t" \
  "v_cvt_pk_bf16_f32 v163, v162, v163\n\t" \
  "v_cvt_pk_bf16_f32 v162, v160, v161\n\t" \
  "ds_read_b64_tr_b16 v[216:217], v248 offset:2048\n\t" \
  "s_waitcnt lgkmcnt(10)\n\t" \
  "v_mfma_f32_16x16x32_bf16 v[176:179], v[208:211], v[96:99], 0\n\t" \
  "v_cvt_pknorm_i16_f32 v228, |v164|, |v165|\n\t" \
  "v_cvt_pknorm_i16_f32 v230, |v166|, |v167|\n\t" \
  "ds_read_b64_tr_b16 v[218:219], v248 offset:5120\n\t" \
  "v_mfma_f32_16x16x32_bf16 v[184:187], v[208:211], v[100:103], 0\n\t" \
  "v_lshlrev_b32_sdwa v229, 2, v228 dst_sel:DWORD dst_unused:UNUSED_PAD src0_sel:DWORD src1_sel:BYTE_3\n\t" \
  "v_lshlrev_b32_sdwa v228, 2, v228 dst_sel:DWORD dst_unused:UNUSED_PAD src0_sel:DWORD src1_sel:BYTE_1\n\t" \
  "ds_read_u16 v226, v226 offset:10528\n\t" \
  "v_mfma_f32_16x16x32_bf16 v[192:195], v[208:211], v[104:107], 0\n\t" \
  "v_lshlrev_b32_sdwa v231, 2, v230 dst_sel:DWORD dst_unused:UNUSED_PAD src0_sel:DWORD src1_sel:BYTE_3\n\t" \
  "v_lshlrev_b32_sdwa v230, 2, v230 dst_sel:DWORD dst_unused:UNUSED_PAD src0_sel:DWORD src1_sel:BYTE_1\n\t" \
  "ds_read_b32 v229, v229 offset:10016\n\t" \
  "v_mfma_f32_16x16x32_bf16 v[200:203], v[208:211], v[108:111], 0\n\t" \
  "v_lshlrev_b32_sdwa v233, 1, v173 dst_sel:DWORD dst_unused:UNUSED_PAD src0_sel:DWORD src1_sel:WORD_1\n\t" \
  "v_lshlrev_b32_sdwa v232, 1, v172 dst_sel:DWORD dst_unused:UNUSED_PAD src0_sel:DWORD src1_sel:WORD_1\n\t" \
  "ds_read_b64_tr_b16 v[208:209], v248 offset:512\n\t" \
  "s_waitcnt lgkmcnt(7)\n\t" \
  "v_mfma_f32_16x16x32_bf16 v[176:179], v[212:215], v[112:115], v[176:179]\n\t" \
  "v_lshlrev_b32_sdwa v235, 1, v175 dst_sel:DWORD dst_unused:UNUSED_PAD src0_sel:DWORD src1_sel:WORD_1\n\t" \
  "v_lshlrev_b32_sdwa v234, 1, v174 dst_sel:DWORD dst_unused:UNUSED_PAD src0_sel:DWORD src1_sel:WORD_1\n\t" \
  "ds_read_b64_tr_b16 v[210:211], v248 offset:1536\n\t" \
  "v_mfma_f32_16x16x32_bf16 v[184:187], v[212:215], v[116:119], v[184:187]\n\t" \
  "v_fma_mix_f32 v221, |v149|, v221, v221 op_sel:[0,1,0] op_sel_hi:[0,1,1] clamp\n\t" \
  "ds_read_b32 v228, v228 offset:10016\n\t" \
  "ds_read_b32 v231, v231 offset:10016\n\t" \
  "v_mfma_f32_16x16x32_bf16 v[192:195], v[212:215], v[120:123], v[192:195]\n\t" \
  "v_fma_mix_f32 v220, |v148|, v220, v220 op_sel:[0,1,0] op_sel_hi:[0,1,1] clamp\n\t" \
  "v_fma_mix_f32 v223, |v151|, v223, v223 op_sel:[0,1,0] op_sel_hi:[0,1,1] clamp\n\t" \
  "ds_read_b32 v230, v230 offset:10016\n\t" \
  "v_mfma_f32_16x16x32_bf16 v[200:203], v[212:215], v[124:127], v[200:203]\n\t" \
  "v_fma_mix_f32 v222, |v150|, v222, v222 op_sel:[0,1,0] op_sel_hi:[0,1,1] clamp\n\t" \
  "s_waitcnt lgkmcnt(6)\n\t" \
  "ds_read_b64_tr_b16 v[212:213], v248 offset:3584\n\t" \
  "ds_read_u16_d16_hi v233, v233 offset:10528\n\t" \
  "v_mfma_f32_16x16x32_bf16 v[176:179], v[216:219], v[128:131], v[176:179]\n\t" \
  "v_or_b32 v156, v224, v225\n\t" \
  "v_or_b32 v157, v226, v227\n\t" \
  "ds_read_b64_tr_b16 v[214:215], v248 offset:4608\n\t" \
  "v_mfma_f32_16x16x32_bf16 v[184:187], v[216:219], v[132:135], v[184:187]\n\t" \
  "v_fma_f32 v149, |v149|, v221, v149 mul:4\n\t" \
  "v_fma_f32 v148, |v148|, v220, v148 mul:4\n\t" \
  "ds_read_u16 v232, v232 offset:10528\n\t" \
  "v_mfma_f32_16x16x32_bf16 v[192:195], v[216:219], v[136:139], v[192:195]\n\t" \
  "v_fma_f32 v151, |v151|, v223, v151 mul:4\n\t" \
  "v_fma_f32 v150, |v150|, v222, v150 mul:4\n\t" \
  "ds_read_u16_d16_hi v235, v235 offset:10528\n\t" \
  "v_mfma_f32_16x16x32_bf16 v[200:203], v[216:219], v[140:143], v[200:203]\n\t" \
  "v_cvt_pk_bf16_f32 v148, v148, v149\n\t" \
  "v_cvt_pk_bf16_f32 v149, v150, v151\n\t" \
  "ds_read_b64_tr_b16 v[216:217], v248 offset:2560\n\t" \
  "s_waitcnt lgkmcnt(9)\n\t" \
  "v_mfma_f32_16x16x32_bf16 v[180:183], v[208:211], v[96:99], 0\n\t" \
  "v_cvt_pknorm_i16_f32 v220, |v178|, |v179|\n\t" \
  "v_cvt_pknorm_i16_f32 v222, |v176|, |v177|\n\t" \
  "s_waitcnt vmcnt(0)\n\t" \
  "ds_read_b64_tr_b16 v[218:219], v248 offset:5632\n\t" \
  "v_mfma_f32_16x16x32_bf16 v[188:191], v[208:211], v[100:103], 0\n\t" \
  "v_lshlrev_b32_sdwa v221, 2, v220 dst_sel:DWORD dst_unused:UNUSED_PAD src0_sel:DWORD src1_sel:BYTE_3\n\t" \
  "v_lshlrev_b32_sdwa v220, 2, v220 dst_sel:DWORD dst_unused:UNUSED_PAD src0_sel:DWORD src1_sel:BYTE_1\n\t" \
  "s_mov_b64 exec, 3\n\t" \
  "ds_add_rtn_u32 v246, v251, v250 offset:47192\n\t" \
  "v_mfma_f32_16x16x32_bf16 v[196:199], v[208:211], v[104:107], 0\n\t" \
  "s_mov_b64 exec, -1\n\t" \
  "v_lshlrev_b32_sdwa v223, 2, v222 dst_sel:DWORD dst_unused:UNUSED_PAD src0_sel:DWORD src1_sel:BYTE_3\n\t" \
  "v_lshlrev_b32_sdwa v222, 2, v222 dst_sel:DWORD dst_unused:UNUSED_PAD src0_sel:DWORD src1_sel:BYTE_1\n\t" \
  "ds_read_u16 v234, v234 offset:10528\n\t" \
  "v_mfma_f32_16x16x32_bf16 v[204:207], v[208:211], v[108:111], 0\n\t" \
  "s_waitcnt lgkmcnt(1)\n\t" \
  "v_lshlrev_b32_sdwa v225, 1, v187 dst_sel:DWORD dst_unused:UNUSED_PAD src0_sel:DWORD src1_sel:WORD_1\n\t" \
  "v_readfirstlane_b32 s4, v246\n\t" \
  "ds_read_b32 v221, v221 offset:10016\n\t" \
  "v_mfma_f32_16x16x32_bf16 v[180:183], v[212:215], v[112:115], v[180:183]\n\t" \
  "s_cmpk_lt_u32 s4, 6144\n\t" \
  "v_lshlrev_b32_sdwa v224, 1, v186 dst_sel:DWORD dst_unused:UNUSED_PAD src0_sel:DWORD src1_sel:WORD_1\n\t" \
  "s_cbranch_scc1 .Lcnx_w1_8\n.Lcnx_g1_8:\n\t" \
  "ds_read_b32 v220, v220 offset:10016\n\t" \
  "v_mfma_f32_16x16x32_bf16 v[188:191], v[212:215], v[116:119], v[188:191]\n\t" \
  "v_lshlrev_b32_sdwa v227, 1, v185 dst_sel:DWORD dst_unused:UNUSED_PAD src0_sel:DWORD src1_sel:WORD_1\n\t" \
  "v_lshlrev_b32_sdwa v226, 1, v184 dst_sel:DWORD dst_unused:UNUSED_PAD src0_sel:DWORD src1_sel:WORD_1\n\t" \
  "ds_read_b64_tr_b16 v[208:209], v249 offset:6152\n\t" \
  "v_mfma_f32_16x16x32_bf16 v[196:199], v[212:215], v[120:123], v[196:199]\n\t" \
  "v_fma_mix_f32 v229, |v165|, v229, v229 op_sel:[0,1,0] op_sel_hi:[0,1,1] clamp\n\t" \
  "v_fma_mix_f32 v228, |v164|, v228, v228 op_sel:[0,1,0] op_sel_hi:[0,1,1] clamp\n\t" \
  "ds_read_b64_tr_b16 v[210:211], v249 offset:7688\n\t" \
  "ds_read_b32 v223, v223 offset:10016\n\t" \
  "v_mfma_f32_16x16x32_bf16 v[204:207], v[212:215], v[124:127], v[204:207]\n\t" \
  "v_fma_mix_f32 v231, |v167|, v231, v231 op_sel:[0,1,0] op_sel_hi:[0,1,1] clamp\n\t" \
  "v_fma_mix_f32 v230, |v166|, v230, v230 op_sel:[0,1,0] op_sel_hi:[0,1,1] clamp\n\t" \
  "s_waitcnt lgkmcnt(5)\n\t" \
  "ds_read_b64_tr_b16 v[212:213], v249 offset:6160\n\t" \
  "ds_read_b32 v222, v222 offset:10016\n\t" \
  "v_mfma_f32_16x16x32_bf16 v[180:183], v[216:219], v[128:131], v[180:183]\n\t" \
  "v_or_b32 v172, v232, v233\n\t" \
  "v_or_b32 v173, v234, v235\n\t" \
  "ds_read_b64_tr_b16 v[214:215], v249 offset:7696\n\t" \
  "v_mfma_f32_16x16x32_bf16 v[188:191], v[216:219], v[132:135], v[188:191]\n\t" \
  "v_fma_f32 v165, |v165|, v229, v165 mul:4\n\t" \
  "v_fma_f32 v164, |v164|, v228, v164 mul:4\n\t" \
  "ds_read_u16_d16_hi v225, v225 offset:10528\n\t" \
  "v_mfma_f32_16x16x32_bf16 v[196:199], v[216:219], v[136:139], v[196:199]\n\t" \
  "v_fma_f32 v167, |v167|, v231, v167 mul:4\n\t" \
  "v_fma_f32 v166, |v166|, v230, v166 mul:4\n\t" \
  "ds_read_u16 v224, v224 offset:10528\n\t" \
  "v_mfma_f32_16x16x32_bf16 v[204:207], v[216:219], v[140:143], v[204:207]\n\t" \
  "v_cvt_pk_bf16_f32 v164, v164, v165\n\t" \
  "v_cvt_pk_bf16_f32 v165, v166, v167\n\t" \
  "ds_read_b64_tr_b16 v[216:217], v249 offset:6216\n\t" \
  "v_mfma_f32_4x4x4_16b_bf16 v[48:51], v[242:243], v[236:237], v[48:51]\n\t" \
  "v_mfma_f32_4x4x4_16b_bf16 v[52:55], v[242:243], v[238:239], v[52:55]\n\t" \
  "v_cvt_pknorm_i16_f32 v228, |v194|, |v195|\n\t" \
  "v_cvt_pknorm_i16_f32 v230, |v192|, |v193|\n\t" \
  "ds_read_b64_tr_b16 v[218:219], v249 offset:7752\n\t" \
  "s_waitcnt lgkmcnt(8)\n\t" \
  "v_mfma_f32_16x16x32_bf16 v[0:3], v[208:211], v[146:149], v[0:3]\n\t" \
  "v_lshlrev_b32_sdwa v229, 2, v228 dst_sel:DWORD dst_unused:UNUSED_PAD src0_sel:DWORD src1_sel:BYTE_3\n\t" \
  "v_lshlrev_b32_sdwa v228, 2, v228 dst_sel:DWORD dst_unused:UNUSED_PAD src0_sel:DWORD src1_sel:BYTE_1\n\t" \
  "ds_read_u16_d16_hi v227, v227 offset:10528\n\t" \
  "v_mfma_f32_16x16x32_bf16 v[24:27], v[208:211], v[154:157], v[24:27]\n\t" \
  "v_lshlrev_b32_sdwa v231, 2, v230 dst_sel:DWORD dst_unused:UNUSED_PAD src0_sel:DWORD src1_sel:BYTE_3\n\t" \
  "v_lshlrev_b32_sdwa v230, 2, v230 dst_sel:DWORD dst_unused:UNUSED_PAD src0_sel:DWORD src1_sel:BYTE_1\n\t" \
  "ds_read_u16 v226, v226 offset:10528\n\t" \
  "v_mfma_f32_16x16x32_bf16 v[48:51], v[208:211], v[162:165], v[48:51]\n\t" \
  "v_lshlrev_b32_sdwa v233, 1, v203 dst_sel:DWORD dst_unused:UNUSED_PAD src0_sel:DWORD src1_sel:WORD_1\n\t" \
  "v_lshlrev_b32_sdwa v232, 1, v202 dst_sel:DWORD dst_unused:UNUSED_PAD src0_sel:DWORD src1_sel:WORD_1\n\t" \
  "ds_read_b32 v229, v229 offset:10016\n\t" \
  "v_mfma_f32_16x16x32_bf16 v[72:75], v[208:211], v[170:173], v[72:75]\n\t" \
  "v_lshlrev_b32_sdwa v235, 1, v201 dst_sel:DWORD dst_unused:UNUSED_PAD src0_sel:DWORD src1_sel:WORD_1\n\t" \
  "s_waitcnt lgkmcnt(7)\n\t" \
  "ds_read_b64_tr_b16 v[208:209], v249 offset:6224\n\t" \
  "ds_read_b32 v228, v228 offset:10016\n\t" \
  "v_mfma_f32_16x16x32_bf16 v[4:7], v[212:215], v[146:149], v[4:7]\n\t" \
  "v_lshlrev_b32_sdwa v234, 1, v200 dst_sel:DWORD dst_unused:UNUSED_PAD src0_sel:DWORD src1_sel:WORD_1\n\t" \
  "v_fma_mix_f32 v221, |v179|, v221, v221 op_sel:[0,1,0] op_sel_hi:[0,1,1] clamp\n\t" \
  "ds_read_b64_tr_b16 v[210:211], v249 offset:7760\n\t" \
  "v_mfma_f32_16x16x32_bf16 v[28:31], v[212:215], v[154:157], v[28:31]\n\t" \
  "v_fma_mix_f32 v220, |v178|, v220, v220 op_sel:[0,1,0] op_sel_hi:[0,1,1] clamp\n\t" \
  "ds_read_b32 v231, v231 offset:10016\n\t" \
  "ds_read_b32 v230, v230 offset:10016\n\t" \
  "v_mfma_f32_16x16x32_bf16 v[52:55], v[212:215], v[162:165], v[52:55]\n\t" \
  "v_fma_mix_f32 v223, |v177|, v223, v223 op_sel:[0,1,0] op_sel_hi:[0,1,1] clamp\n\t" \
  "v_fma_mix_f32 v222, |v176|, v222, v222 op_sel:[0,1,0] op_sel_hi:[0,1,1] clamp\n\t" \
  "ds_read_u16_d16_hi v233, v233 offset:10528\n\t" \
  "s_waitcnt lgkmcnt(7)\n\t" \
  "v_mfma_f32_16x16x32_bf16 v[76:79], v[212:215], v[170:173], v[76:79]\n\t" \
  "v_or_b32 v187, v224, v225\n\t" \
  "v_or_b32 v186, v226, v227\n\t" \
  "ds_read_b64_tr_b16 v[212:213], v249 offset:6280\n\t" \
  "v_mfma_f32_16x16x32_bf16 v[8:11], v[216:219], v[146:149], v[8:11]\n\t" \
  "v_fma_f32 v179, |v179|, v221, v179 mul:4\n\t" \
  "v_fma_f32 v178, |v178|, v220, v178 mul:4\n\t" \
  "buffer_load_dwordx4 v[236:239], v250, s[8:11], s14 offen offset:64 nt\n\t" \
  "ds_read_b64_tr_b16 v[214:215], v249 offset:7816\n\t" \
  "v_mfma_f32_16x16x32_bf16 v[32:35], v[216:219], v[154:157], v[32:35]\n\t" \
  "v_fma_f32 v177, |v177|, v223, v177 mul:4\n\t" \
  "v_fma_f32 v176, |v176|, v222, v176 mul:4\n\t" \
  "ds_read_u16 v232, v232 offset:10528\n\t" \
  "v_mfma_f32_16x16x32_bf16 v[56:59], v[216:219], v[162:165], v[56:59]\n\t" \
  "v_cvt_pk_bf16_f32 v179, v178, v179\n\t" \
  "v_cvt_pk_bf16_f32 v178, v176, v177\n\t" \
  "ds_read_u16_d16_hi v235, v235 offset:10528\n\t" \
  "v_mfma_f32_16x16x32_bf16 v[80:83], v[216:219], v[170:173], v[80:83]\n\t" \
  "v_cvt_pknorm_i16_f32 v220, |v180|, |v181|\n\t" \
  "v_cvt_pknorm_i16_f32 v222, |v182|, |v183|\n\t" \
  "ds_read_b64_tr_b16 v[216:217], v249 offset:6288\n\t" \
  "s_waitcnt lgkmcnt(8)\n\t" \
  "v_mfma_f32_16x16x32_bf16 v[12:15], v[208:211], v[146:149], v[12:15]\n\t" \
  "v_lshlrev_b32_sdwa v221, 2, v220 dst_sel:DWORD dst_unused:UNUSED_PAD src0_sel:DWORD src1_sel:BYTE_3\n\t" \
  "v_lshlrev_b32_sdwa v220, 2, v220 dst_sel:DWORD dst_unused:UNUSED_PAD src0_sel:DWORD src1_sel:BYTE_1\n\t" \
  "ds_read_b64_tr_b16 v[218:219], v249 offset:7824\n\t" \
  "v_mfma_f32_16x16x32_bf16 v[36:39], v[208:211], v[154:157], v[36:39]\n\t" \
  "v_lshlrev_b32_sdwa v223, 2, v222 dst_sel:DWORD dst_unused:UNUSED_PAD src0_sel:DWORD src1_sel:BYTE_3\n\t" \
  "v_lshlrev_b32_sdwa v222, 2, v222 dst_sel:DWORD dst_unused:UNUSED_PAD src0_sel:DWORD src1_sel:BYTE_1\n\t" \
  "ds_read_u16 v234, v234 offset:10528\n\t" \
  "v_mfma_f32_16x16x32_bf16 v[60:63], v[208:211], v[162:165], v[60:63]\n\t" \
  "v_lshlrev_b32_sdwa v225, 1, v189 dst_sel:DWORD dst_unused:UNUSED_PAD src0_sel:DWORD src1_sel:WORD_1\n\t" \
  "v_lshlrev_b32_sdwa v224, 1, v188 dst_sel:DWORD dst_unused:UNUSED_PAD src0_sel:DWORD src1_sel:WORD_1\n\t" \
  "ds_read_b32 v221, v221 offset:10016\n\t" \
  "v_mfma_f32_16x16x32_bf16 v[84:87], v[208:211], v[170:173], v[84:87]\n\t" \
  "v_lshlrev_b32_sdwa v227, 1, v191 dst_sel:DWORD dst_unused:UNUSED_PAD src0_sel:DWORD src1_sel:WORD_1\n\t" \
  "s_waitcnt lgkmcnt(6)\n\t" \
  "ds_read_b64_tr_b16 v[208:209], v248 offset:43048\n\t" \
  "ds_read_b32 v220, v220 offset:10016\n\t" \
  "v_mfma_f32_16x16x32_bf16 v[16:19], v[212:215], v[146:149], v[16:19]\n\t" \
  "v_lshlrev_b32_sdwa v226, 1, v190 dst_sel:DWORD dst_unused:UNUSED_PAD src0_sel:DWORD src1_sel:WORD_1\n\t" \
  "v_fma_mix_f32 v229, |v195|, v229, v229 op_sel:[0,1,0] op_sel_hi:[0,1,1] clamp\n\t" \
  "ds_read_b64_tr_b16 v[210:211], v248 offset:44072\n\t" \
  "v_mfma_f32_16x16x32_bf16 v[40:43], v[212:215], v[154:157], v[40:43]\n\t" \
  "v_fma_mix_f32 v228, |v194|, v228, v228 op_sel:[0,1,0] op_sel_hi:[0,1,1] clamp\n\t" \
  "ds_read_b32 v223, v223 offset:10016\n\t" \
  "ds_read_b32 v222, v222 offset:10016\n\t" \
  "v_mfma_f32_16x16x32_bf16 v[64:67], v[212:215], v[162:165], v[64:67]\n\t" \
  "v_fma_mix_f32 v231, |v193|, v231, v231 op_sel:[0,1,0] op_sel_hi:[0,1,1] clamp\n\t" \
  "v_fma_mix_f32 v230, |v192|, v230, v230 op_sel:[0,1,0] op_sel_hi:[0,1,1] clamp\n\t" \
  "ds_read_u16_d16_hi v225, v225 offset:10528\n\t" \
  "s_waitcnt lgkmcnt(7)\n\t" \
  "v_mfma_f32_16x16x32_bf16 v[88:91], v[212:215], v[170:173], v[88:91]\n\t" \
  "v_or_b32 v203, v232, v233\n\t" \
  "v_or_b32 v202, v234, v235\n\t" \
  "ds_read_b64_tr_b16 v[212:213], v248 offset:46120\n\t" \
  "v_mfma_f32_16x16x32_bf16 v[20:23], v[216:219], v[146:149], v[20:23]\n\t" \
  "v_fma_f32 v195, |v195|, v229, v195 mul:4\n\t" \
  "v_fma_f32 v194, |v194|, v228, v194 mul:4\n\t" \
  "ds_read_b64_tr_b16 v[214:215], v248 offset:47144\n\t" \
  "v_mfma_f32_16x16x32_bf16 v[44:47], v[216:219], v[154:157], v[44:47]\n\t" \
  "v_fma_f32 v193, |v193|, v231, v193 mul:4\n\t" \
  "v_fma_f32 v192, |v192|, v230, v192 mul:4\n\t" \
  "ds_read_u16 v224, v224 offset:10528\n\t" \
  "v_mfma_f32_16x16x32_bf16 v[68:71], v[216:219], v[162:165], v[68:71]\n\t" \
  "v_cvt_pk_bf16_f32 v195, v194, v195\n\t" \
  "v_cvt_pk_bf16_f32 v194, v192, v193\n\t" \
  "ds_read_u16_d16_hi v227, v227 offset:10528\n\t" \
  "v_mfma_f32_16x16x32_bf16 v[92:95], v[216:219], v[170:173], v[92:95]\n\t" \
  "v_cvt_pknorm_i16_f32 v228, |v196|, |v197|\n\t" \
  "v_cvt_pknorm_i16_f32 v230, |v198|, |v199|\n\t" \
  "ds_read_b64_tr_b16 v[216:217], v248 offset:45096\n\t" \
  "s_waitcnt lgkmcnt(8)\n\t" \
  "v_mfma_f32_16x16x32_bf16 v[144:147], v[208:211], v[96:99], 0\n\t" \
  "v_lshlrev_b32_sdwa v229, 2, v228 dst_sel:DWORD dst_unused:UNUSED_PAD src0_sel:DWORD src1_sel:BYTE_3\n\t" \
  "v_lshlrev_b32_sdwa v228, 2, v228 dst_sel:DWORD dst_unused:UNUSED_PAD src0_sel:DWORD src1_sel:BYTE_1\n\t" \
  "ds_read_b64_tr_b16 v[218:219], v248 offset:48168\n\t" \
  "v_mfma_f32_16x16x32_bf16 v[152:155], v[208:211], v[100:103], 0\n\t" \
  "v_lshlrev_b32_sdwa v231, 2, v230 dst_sel:DWORD dst_unused:UNUSED_PAD src0_sel:DWORD src1_sel:BYTE_3\n\t" \
  "v_lshlrev_b32_sdwa v230, 2, v230 dst_sel:DWORD dst_unused:UNUSED_PAD src0_sel:DWORD src1_sel:BYTE_1\n\t" \
  "ds_read_u16 v226, v226 offset:10528\n\t" \
  "v_mfma_f32_16x16x32_bf16 v[160:163], v[208:211], v[104:107], 0\n\t" \
  "v_lshlrev_b32_sdwa v233, 1, v205 dst_sel:DWORD dst_unused:UNUSED_PAD src0_sel:DWORD src1_sel:WORD_1\n\t" \
  "ds_read_b32 v229, v229 offset:10016\n\t" \
  "v_mfma_f32_16x16x32_bf16 v[168:171], v[208:211], v[108:111], 0\n\t" \
  "v_lshlrev_b32_sdwa v232, 1, v204 dst_sel:DWORD dst_unused:UNUSED_PAD src0_sel:DWORD src1_sel:WORD_1\n\t" \
  "v_lshlrev_b32_sdwa v235, 1, v207 dst_sel:DWORD dst_unused:UNUSED_PAD src0_sel:DWORD src1_sel:WORD_1\n\t" \
  "ds_read_b64_tr_b16 v[208:209], v248 offset:43560\n\t" \
  "s_waitcnt lgkmcnt(7)\n\t" \
  "v_mfma_f32_16x16x32_bf16 v[144:147], v[212:215], v[112:115], v[144:147]\n\t" \
  "v_lshlrev_b32_sdwa v234, 1, v206 dst_sel:DWORD dst_unused:UNUSED_PAD src0_sel:DWORD src1_sel:WORD_1\n\t" \
  "ds_read_b64_tr_b16 v[210:211], v248 offset:44584\n\t" \
  "ds_read_b32 v228, v228 offset:10016\n\t" \
  "v_mfma_f32_16x16x32_bf16 v[152:155], v[212:215], v[116:119], v[152:155]\n\t" \
  "v_fma_mix_f32 v221, |v181|, v221, v221 op_sel:[0,1,0] op_sel_hi:[0,1,1] clamp\n\t" \
  "v_fma_mix_f32 v220, |v180|, v220, v220 op_sel:[0,1,0] op_sel_hi:[0,1,1] clamp\n\t" \
  "ds_read_b32 v231, v231 offset:10016\n\t" \
  "v_mfma_f32_16x16x32_bf16 v[160:163], v[212:215], v[120:123], v[160:163]\n\t" \
  "v_fma_mix_f32 v223, |v183|, v223, v223 op_sel:[0,1,0] op_sel_hi:[0,1,1] clamp\n\t" \
  "v_fma_mix_f32 v222, |v182|, v222, v222 op_sel:[0,1,0] op_sel_hi:[0,1,1] clamp\n\t" \
  "ds_read_b32 v230, v230 offset:10016\n\t" \
  "v_mfma_f32_16x16x32_bf16 v[168:171], v[212:215], v[124:127], v[168:171]\n\t" \
  "s_waitcnt lgkmcnt(6)\n\t" \
  "v_or_b32 v188, v224, v225\n\t" \
  "ds_read_b64_tr_b16 v[212:213], v248 offset:46632\n\t" \
  "ds_read_u16_d16_hi v233, v233 offset:10528\n\t" \
  "v_mfma_f32_16x16x32_bf16 v[144:147], v[216:219], v[128:131], v[144:147]\n\t" \
  "v_or_b32 v189, v226, v227\n\t" \
  "v_fma_f32 v181, |v181|, v221, v181 mul:4\n\t" \
  "ds_read_b64_tr_b16 v[214:215], v248 offset:47656\n\t" \
  "v_mfma_f32_16x16x32_bf16 v[152:155], v[216:219], v[132:135], v[152:155]\n\t" \
  "v_fma_f32 v180, |v180|, v220, v180 mul:4\n\t" \
  "v_fma_f32 v183, |v183|, v223, v183 mul:4\n\t" \
  "ds_read_u16 v232, v232 offset:10528\n\t" \
  "v_mfma_f32_16x16x32_bf16 v[160:163], v[216:219], v[136:139], v[160:163]\n\t" \
  "v_fma_f32 v182, |v182|, v222, v182 mul:4\n\t" \
  "v_cvt_pk_bf16_f32 v180, v180, v181\n\t" \
  "ds_read_u16_d16_hi v235, v235 offset:10528\n\t" \
  "v_mfma_f32_16x16x32_bf16 v[168:171], v[216:219], v[140:143], v[168:171]\n\t" \
  "v_cvt_pk_bf16_f32 v181, v182, v183\n\t" \
  "v_cvt_pknorm_i16_f32 v220, |v146|, |v147|\n\t" \
  "ds_read_b64_tr_b16 v[216:217], v248 offset:45608\n\t" \
  "s_waitcnt lgkmcnt(9)\n\t" \
  "v_mfma_f32_16x16x32_bf16 v[148:151], v[208:211], v[96:99], 0\n\t" \
  "v_cvt_pknorm_i16_f32 v222, |v144|, |v145|\n\t" \
  "v_lshlrev_b32_sdwa v221, 2, v220 dst_sel:DWORD dst_unused:UNUSED_PAD src0_sel:DWORD src1_sel:BYTE_3\n\t" \
  "s_waitcnt vmcnt(0)\n\t" \
  "ds_read_b64_tr_b16 v[218:219], v248 offset:48680\n\t" \
  "v_mfma_f32_16x16x32_bf16 v[156:159], v[208:211], v[100:103], 0\n\t" \
  "v_lshlrev_b32_sdwa v220, 2, v220 dst_sel:DWORD dst_unused:UNUSED_PAD src0_sel:DWORD src1_sel:BYTE_1\n\t" \
  "v_lshlrev_b32_sdwa v223, 2, v222 dst_sel:DWORD dst_unused:UNUSED_PAD src0_sel:DWORD src1_sel:BYTE_3\n\t" \
  "s_mov_b64 exec, 3\n\t" \
  "ds_add_rtn_u32 v246, v251, v250 offset:47200\n\t" \
  "v_mfma_f32_16x16x32_bf16 v[164:167], v[208:211], v[104:107], 0\n\t" \
  "s_mov_b64 exec, -1\n\t" \
  "v_lshlrev_b32_sdwa v222, 2, v222 dst_sel:DWORD dst_unused:UNUSED_PAD src0_sel:DWORD src1_sel:BYTE_1\n\t" \
  "v_lshlrev_b32_sdwa v225, 1, v155 dst_sel:DWORD dst_unused:UNUSED_PAD src0_sel:DWORD src1_sel:WORD_1\n\t" \
  "ds_read_u16 v234, v234 offset:10528\n\t" \
  "v_mfma_f32_16x16x32_bf16 v[172:175], v[208:211], v[108:111], 0\n\t" \
  "s_waitcnt lgkmcnt(1)\n\t" \
  "v_lshlrev_b32_sdwa v224, 1, v154 dst_sel:DWORD dst_unused:UNUSED_PAD src0_sel:DWORD src1_sel:WORD_1\n\t" \
  "v_readfirstlane_b32 s4, v246\n\t" \
  "ds_read_b32 v221, v221 offset:10016\n\t" \
  "v_mfma_f32_16x16x32_bf16 v[148:151], v[212:215], v[112:115], v[148:151]\n\t" \
  "s_cmpk_lt_u32 s4, 6144\n\t" \
  "v_lshlrev_b32_sdwa v227, 1, v153 dst_sel:DWORD dst_unused:UNUSED_PAD src0_sel:DWORD src1_sel:WORD_1\n\t" \
  "s_cbranch_scc1 .Lcnx_w1_9\n.Lcnx_g1_9:\n\t" \
  "ds_read_b32 v220, v220 offset:10016\n\t" \
  "v_mfma_f32_16x16x32_bf16 v[156:159], v[212:215], v[116:119], v[156:159]\n\t" \
  "v_lshlrev_b32_sdwa v226, 1, v152 dst_sel:DWORD dst_unused:UNUSED_PAD src0_sel:DWORD src1_sel:WORD_1\n\t" \
  "ds_read_b64_tr_b16 v[208:209], v249 offset:12304\n\t" \
  "ds_read_b32 v223, v223 offset:10016\n\t" \
  "v_mfma_f32_16x16x32_bf16 v[164:167], v[212:215], v[120:123], v[164:167]\n\t" \
  "v_fma_mix_f32 v229, |v197|, v229, v229 op_sel:[0,1,0] op_sel_hi:[0,1,1] clamp\n\t" \
  "v_fma_mix_f32 v228, |v196|, v228, v228 op_sel:[0,1,0] op_sel_hi:[0,1,1] clamp\n\t" \
  "ds_read_b64_tr_b16 v[210:211], v249 offset:13840\n\t" \
  "v_mfma_f32_16x16x32_bf16 v[172:175], v[212:215], v[124:127], v[172:175]\n\t" \
  "v_fma_mix_f32 v231, |v199|, v231, v231 op_sel:[0,1,0] op_sel_hi:[0,1,1] clamp\n\t" \
  "v_fma_mix_f32 v230, |v198|, v230, v230 op_sel:[0,1,0] op_sel_hi:[0,1,1] clamp\n\t" \
  "s_waitcnt lgkmcnt(5)\n\t" \
  "ds_read_b64_tr_b16 v[212:213], v249 offset:12312\n\t" \
  "ds_read_b32 v222, v222 offset:10016\n\t" \
  "v_mfma_f32_16x16x32_bf16 v[148:151], v[216:219], v[128:131], v[148:151]\n\t" \
  "v_or_b32 v204, v232, v233\n\t" \
  "v_or_b32 v205, v234, v235\n\t" \
  "ds_read_b64_tr_b16 v[214:215], v249 offset:13848\n\t" \
  "v_mfma_f32_16x16x32_bf16 v[156:159], v[216:219], v[132:135], v[156:159]\n\t" \
  "v_fma_f32 v197, |v197|, v229, v197 mul:4\n\t" \
  "v_fma_f32 v196, |v196|, v228, v196 mul:4\n\t" \
  "ds_read_u16_d16_hi v225, v225 offset:10528\n\t" \
  "v_mfma_f32_16x16x32_bf16 v[164:167], v[216:219], v[136:139], v[164:167]\n\t" \
  "v_fma_f32 v199, |v199|, v231, v199 mul:4\n\t" \
  "v_fma_f32 v198, |v198|, v230, v198 mul:4\n\t" \
  "ds_read_u16 v224, v224 offset:10528\n\t" \
  "v_mfma_f32_16x16x32_bf16 v[172:175], v[216:219], v[140:143], v[172:175]\n\t" \
  "v_cvt_pk_bf16_f32 v196, v196, v197\n\t" \
  "v_cvt_pk_bf16_f32 v197, v198, v199\n\t" \
  "ds_read_b64_tr_b16 v[216:217], v249 offset:12368\n\t" \
  "v_mfma_f32_4x4x4_16b_bf16 v[56:59], v[242:243], v[236:237], v[56:59]\n\t" \
  "v_mfma_f32_4x4x4_16b_bf16 v[60:63], v[242:243], v[238:239], v[60:63]\n\t" \
  "ds_read_b64_tr_b16 v[218:219], v249 offset:13904\n\t" \
  "v_cvt_pknorm_i16_f32 v228, |v162|, |v163|\n\t" \
  "s_setprio 0\n\t" \
  "s_cmp_ge_u32 s39, 2048\n\t" \
  "s_cbranch_scc1 .Lcnx_p\n\ts_setprio 3\n.Lcnx_p:\n\t" \
  "v_cvt_pknorm_i16_f32 v230, |v160|, |v161|\n\t" \
  "s_waitcnt lgkmcnt(7)\n\t" \
  "v_mfma_f32_16x16x32_bf16 v[0:3], v[208:211], v[178:181], v[0:3]\n\t" \
  "ds_read_u16_d16_hi v227, v227 offset:10528\n\t" \
  "v_lshlrev_b32_sdwa v229, 2, v228 dst_sel:DWORD dst_unused:UNUSED_PAD src0_sel:DWORD src1_sel:BYTE_3\n\t" \
  "v_lshlrev_b32_sdwa v228, 2, v228 dst_sel:DWORD dst_unused:UNUSED_PAD src0_sel:DWORD src1_sel:BYTE_1\n\t" \
  "v_mfma_f32_16x16x32_bf16 v[24:27], v[208:211], v[186:189], v[24:27]\n\t" \
  "ds_read_u16 v226, v226 offset:10528\n\t" \
  "v_lshlrev_b32_sdwa v231, 2, v230 dst_sel:DWORD dst_unused:UNUSED_PAD src0_sel:DWORD src1_sel:BYTE_3\n\t" \
  "v_lshlrev_b32_sdwa v230, 2, v230 dst_sel:DWORD dst_unused:UNUSED_PAD src0_sel:DWORD src1_sel:BYTE_1\n\t" \
  "v_mfma_f32_16x16x32_bf16 v[48:51], v[208:211], v[194:197], v[48:51]\n\t" \
  "ds_read_b32 v229, v229 offset:10016\n\t" \
  "v_lshlrev_b32_sdwa v233, 1, v171 dst_sel:DWORD dst_unused:UNUSED_PAD src0_sel:DWORD src1_sel:WORD_1\n\t" \
  "v_lshlrev_b32_sdwa v232, 1, v170 dst_sel:DWORD dst_unused:UNUSED_PAD src0_sel:DWORD src1_sel:WORD_1\n\t" \
  "v_mfma_f32_16x16x32_bf16 v[72:75], v[208:211], v[202:205], v[72:75]\n\t" \
  "ds_read_b64_tr_b16 v[208:209], v249 offset:12376\n\t" \
  "ds_read_b32 v228, v228 offset:10016\n\t" \
  "v_lshlrev_b32_sdwa v235, 1, v169 dst_sel:DWORD dst_unused:UNUSED_PAD src0_sel:DWORD src1_sel:WORD_1\n\t" \
  "s_waitcnt lgkmcnt(9)\n\t" \
  "v_mfma_f32_16x16x32_bf16 v[4:7], v[212:215], v[178:181], v[4:7]\n\t" \
  "ds_read_b64_tr_b16 v[210:211], v249 offset:13912\n\t" \
  "v_lshlrev_b32_sdwa v234, 1, v168 dst_sel:DWORD dst_unused:UNUSED_PAD src0_sel:DWORD src1_sel:WORD_1\n\t" \
  "v_fma_mix_f32 v221, |v147|, v221, v221 op_sel:[0,1,0] op_sel_hi:[0,1,1] clamp\n\t" \
  "v_mfma_f32_16x16x32_bf16 v[28:31], v[212:215], v[186:189], v[28:31]\n\t" \
  "ds_read_b32 v231, v231 offset:10016\n\t" \
  "ds_read_b32 v230, v230 offset:10016\n\t" \
  "v_fma_mix_f32 v220, |v146|, v220, v220 op_sel:[0,1,0] op_sel_hi:[0,1,1] clamp\n\t" \
  "v_mfma_f32_16x16x32_bf16 v[52:55], v[212:215], v[194:197], v[52:55]\n\t" \
  "ds_read_u16_d16_hi v233, v233 offset:10528\n\t" \
  "v_fma_mix_f32 v223, |v145|, v223, v223 op_sel:[0,1,0] op_sel_hi:[0,1,1] clamp\n\t" \
  "v_fma_mix_f32 v222, |v144|, v222, v222 op_sel:[0,1,0] op_sel_hi:[0,1,1] clamp\n\t" \
  "s_waitcnt lgkmcnt(7)\n\t" \
  "v_mfma_f32_16x16x32_bf16 v[76:79], v[212:215], v[202:205], v[76:79]\n\t" \
  "ds_read_b64_tr_b16 v[212:213], v249 offset:12432\n\t" \
  "v_or_b32 v155, v224, v225\n\t" \
  "v_or_b32 v154, v226, v227\n\t" \
  "v_mfma_f32_16x16x32_bf16 v[8:11], v[216:219], v[178:181], v[8:11]\n\t" \
  "ds_read_b64_tr_b16 v[214:215], v249 offset:13968\n\t" \
  "buffer_load_dwordx4 v[236:239], v250, s[8:11], s14 offen offset:128 nt\n\t" \
  "v_fma_f32 v147, |v147|, v221, v147 mul:4\n\t" \
  "v_mfma_f32_16x16x32_bf16 v[32:35], v[216:219], v[186:189], v[32:35]\n\t" \
  "ds_read_u16 v232, v232 offset:10528\n\t" \
  "v_fma_f32 v146, |v146|, v220, v146 mul:4\n\t" \
  "v_fma_f32 v145, |v145|, v223, v145 mul:4\n\t" \
  "v_mfma_f32_16x16x32_bf16 v[56:59], v[216:219], v[194:197], v[56:59]\n\t" \
  "ds_read_u16_d16_hi v235, v235 offset:10528\n\t" \
  "v_fma_f32 v144, |v144|, v222, v144 mul:4\n\t" \
  "v_cvt_pk_bf16_f32 v147, v146, v147\n\t" \
  "v_mfma_f32_16x16x32_bf16 v[80:83], v[216:219], v[202:205], v[80:83]\n\t" \
  "ds_read_b64_tr_b16 v[216:217], v249 offset:12440\n\t" \
  "v_cvt_pk_bf16_f32 v146, v144, v145\n\t" \
  "v_cvt_pknorm_i16_f32 v220, |v148|, |v149|\n\t" \
  "s_waitcnt lgkmcnt(8)\n\t" \
  "v_mfma_f32_16x16x32_bf16 v[12:15], v[208:211], v[178:181], v[12:15]\n\t" \
  "ds_read_b64_tr_b16 v[218:219], v249 offset:13976\n\t" \
  "v_cvt_pknorm_i16_f32 v222, |v150|, |v151|\n\t" \
  "v_lshlrev_b32_sdwa v221, 2, v220 dst_sel:DWORD dst_unused:UNUSED_PAD src0_sel:DWORD src1_sel:BYTE_3\n\t" \
  "v_mfma_f32_16x16x32_bf16 v[36:39], v[208:211], v[186:189], v[36:39]\n\t" \
  "ds_read_u16 v234, v234 offset:10528\n\t" \
  "v_lshlrev_b32_sdwa v220, 2, v220 dst_sel:DWORD dst_unused:UNUSED_PAD src0_sel:DWORD src1_sel:BYTE_1\n\t" \
  "v_lshlrev_b32_sdwa v223, 2, v222 dst_sel:DWORD dst_unused:UNUSED_PAD src0_sel:DWORD src1_sel:BYTE_3\n\t" \
  "v_mfma_f32_16x16x32_bf16 v[60:63], v[208:211], v[194:197], v[60:63]\n\t" \
  "ds_read_b32 v221, v221 offset:10016\n\t" \
  "v_lshlrev_b32_sdwa v222, 2, v222 dst_sel:DWORD dst_unused:UNUSED_PAD src0_sel:DWORD src1_sel:BYTE_1\n\t" \
  "v_lshlrev_b32_sdwa v225, 1, v157 dst_sel:DWORD dst_unused:UNUSED_PAD src0_sel:DWORD src1_sel:WORD_1\n\t" \
  "v_mfma_f32_16x16x32_bf16 v[84:87], v[208:211], v[202:205], v[84:87]\n\t" \
  "ds_read_b64_tr_b16 v[208:209], v248 offset:49192\n\t" \
  "v_lshlrev_b32_sdwa v224, 1, v156 dst_sel:DWORD dst_unused:UNUSED_PAD src0_sel:DWORD src1_sel:WORD_1\n\t" \
  "v_lshlrev_b32_sdwa v227, 1, v159 dst_sel:DWORD dst_unused:UNUSED_PAD src0_sel:DWORD src1_sel:WORD_1\n\t" \
  "s_waitcnt lgkmcnt(7)\n\t" \
  "v_mfma_f32_16x16x32_bf16 v[16:19], v[212:215], v[178:181], v[16:19]\n\t" \
  "ds_read_b64_tr_b16 v[210:211], v248 offset:50216\n\t" \
  "ds_read_b32 v220, v220 offset:10016\n\t" \
  "v_lshlrev_b32_sdwa v226, 1, v158 dst_sel:DWORD dst_unused:UNUSED_PAD src0_sel:DWORD src1_sel:WORD_1\n\t" \
  "v_mfma_f32_16x16x32_bf16 v[40:43], v[212:215], v[186:189], v[40:43]\n\t" \
  "ds_read_b32 v223, v223 offset:10016\n\t" \
  "v_fma_mix_f32 v229, |v163|, v229, v229 op_sel:[0,1,0] op_sel_hi:[0,1,1] clamp\n\t" \
  "v_fma_mix_f32 v228, |v162|, v228, v228 op_sel:[0,1,0] op_sel_hi:[0,1,1] clamp\n\t" \
  "v_mfma_f32_16x16x32_bf16 v[64:67], v[212:215], v[194:197], v[64:67]\n\t" \
  "ds_read_b32 v222, v222 offset:10016\n\t" \
  "v_fma_mix_f32 v231, |v161|, v231, v231 op_sel:[0,1,0] op_sel_hi:[0,1,1] clamp\n\t" \
  "v_fma_mix_f32 v230, |v160|, v230, v230 op_sel:[0,1,0] op_sel_hi:[0,1,1] clamp\n\t" \
  "s_waitcnt lgkmcnt(6)\n\t" \
  "v_mfma_f32_16x16x32_bf16 v[88:91], v[212:215], v[202:205], v[88:91]\n\t" \
  "ds_read_b64_tr_b16 v[212:213], v248 offset:52264\n\t" \
  "ds_read_u16_d16_hi v225, v225 offset:10528\n\t" \
  "v_or_b32 v171, v232, v233\n\t" \
  "v_or_b32 v170, v234, v235\n\t" \
  "v_mfma_f32_16x16x32_bf16 v[20:23], v[216:219], v[178:181], v[20:23]\n\t" \
  "ds_read_b64_tr_b16 v[214:215], v248 offset:53288\n\t" \
  "v_fma_f32 v163, |v163|, v229, v163 mul:4\n\t" \
  "v_fma_f32 v162, |v162|, v228, v162 mul:4\n\t" \
  "v_mfma_f32_16x16x32_bf16 v[44:47], v[216:219], v[186:189], v[44:47]\n\t" \
  "ds_read_u16 v224, v224 offset:10528\n\t" \
  "v_fma_f32 v161, |v161|, v231, v161 mul:4\n\t" \
  "v_fma_f32 v160, |v160|, v230, v160 mul:4\n\t" \
  "v_mfma_f32_16x16x32_bf16 v[68:71], v[216:219], v[194:197], v[68:71]\n\t" \
  "ds_read_u16_d16_hi v227, v227 offset:10528\n\t" \
  "v_cvt_pk_bf16_f32 v163, v162, v163\n\t" \
  "v_cvt_pk_bf16_f32 v162, v160, v161\n\t" \
  "v_mfma_f32_16x16x32_bf16 v[92:95], v[216:219], v[202:205], v[92:95]\n\t" \
  "ds_read_b64_tr_b16 v[216:217], v248 offset:51240\n\t" \
  "v_cvt_pknorm_i16_f32 v228, |v164|, |v165|\n\t" \
  "v_cvt_pknorm_i16_f32 v230, |v166|, |v167|\n\t" \
  "s_waitcnt lgkmcnt(9)\n\t" \
  "v_mfma_f32_16x16x32_bf16 v[176:179], v[208:211], v[96:99], 0\n\t" \
  "ds_read_b64_tr_b16 v[218:219], v248 offset:54312\n\t" \
  "v_lshlrev_b32_sdwa v229, 2, v228 dst_sel:DWORD dst_unused:UNUSED_PAD src0_sel:DWORD src1_sel:BYTE_3\n\t" \
  "v_lshlrev_b32_sdwa v228, 2, v228 dst_sel:DWORD dst_unused:UNUSED_PAD src0_sel:DWORD src1_sel:BYTE_1\n\t" \
  "v_mfma_f32_16x16x32_bf16 v[184:187], v[208:211], v[100:103], 0\n\t" \
  "ds_read_u16 v226, v226 offset:10528\n\t" \
  "v_lshlrev_b32_sdwa v231, 2, v230 dst_sel:DWORD dst_unused:UNUSED_PAD src0_sel:DWORD src1_sel:BYTE_3\n\t" \
  "v_lshlrev_b32_sdwa v230, 2, v230 dst_sel:DWORD dst_unused:UNUSED_PAD src0_sel:DWORD src1_sel:BYTE_1\n\t" \
  "v_mfma_f32_16x16x32_bf16 v[192:195], v[208:211], v[104:107], 0\n\t" \
  "ds_read_b32 v229, v229 offset:10016\n\t" \
  "v_lshlrev_b32_sdwa v233, 1, v173 dst_sel:DWORD dst_unused:UNUSED_PAD src0_sel:DWORD src1_sel:WORD_1\n\t" \
  "v_lshlrev_b32_sdwa v232, 1, v172 dst_sel:DWORD dst_unused:UNUSED_PAD src0_sel:DWORD src1_sel:WORD_1\n\t" \
  "v_mfma_f32_16x16x32_bf16 v[200:203], v[208:211], v[108:111], 0\n\t" \
  "ds_read_b64_tr_b16 v[208:209], v248 offset:49704\n\t" \
  "ds_read_b32 v228, v228 offset:10016\n\t" \
  "v_lshlrev_b32_sdwa v235, 1, v175 dst_sel:DWORD dst_unused:UNUSED_PAD src0_sel:DWORD src1_sel:WORD_1\n\t" \
  "s_waitcnt lgkmcnt(8)\n\t" \
  "v_mfma_f32_16x16x32_bf16 v[176:179], v[212:215], v[112:115], v[176:179]\n\t" \
  "ds_read_b64_tr_b16 v[210:211], v248 offset:50728\n\t" \
  "v_lshlrev_b32_sdwa v234, 1, v174 dst_sel:DWORD dst_unused:UNUSED_PAD src0_sel:DWORD src1_sel:WORD_1\n\t" \
  "v_fma_mix_f32 v221, |v149|, v221, v221 op_sel:[0,1,0] op_sel_hi:[0,1,1] clamp\n\t" \
  "v_mfma_f32_16x16x32_bf16 v[184:187], v[212:215], v[116:119], v[184:187]\n\t" \
  "ds_read_b32 v231, v231 offset:10016\n\t" \
  "ds_read_b32 v230, v230 offset:10016\n\t" \
  "v_fma_mix_f32 v220, |v148|, v220, v220 op_sel:[0,1,0] op_sel_hi:[0,1,1] clamp\n\t" \
  "v_mfma_f32_16x16x32_bf16 v[192:195], v[212:215], v[120:123], v[192:195]\n\t" \
  "ds_read_u16_d16_hi v233, v233 offset:10528\n\t" \
  "v_fma_mix_f32 v223, |v151|, v223, v223 op_sel:[0,1,0] op_sel_hi:[0,1,1] clamp\n\t" \
  "v_fma_mix_f32 v222, |v150|, v222, v222 op_sel:[0,1,0] op_sel_hi:[0,1,1] clamp\n\t" \
  "s_waitcnt lgkmcnt(7)\n\t" \
  "v_mfma_f32_16x16x32_bf16 v[200:203], v[212:215], v[124:127], v[200:203]\n\t" \
  "ds_read_b64_tr_b16 v[212:213], v248 offset:52776\n\t" \
  "v_or_b32 v156, v224, v225\n\t" \
  "v_or_b32 v157, v226, v227\n\t" \
  "v_mfma_f32_16x16x32_bf16 v[176:179], v[216:219], v[128:131], v[176:179]\n\t" \
  "ds_read_b64_tr_b16 v[214:215], v248 offset:53800\n\t" \
  "v_fma_f32 v149, |v149|, v221, v149 mul:4\n\t" \
  "v_fma_f32 v148, |v148|, v220, v148 mul:4\n\t" \
  "v_mfma_f32_16x16x32_bf16 v[184:187], v[216:219], v[132:135], v[184:187]\n\t" \
  "ds_read_u16 v232, v232 offset:10528\n\t" \
  "v_fma_f32 v151, |v151|, v223, v151 mul:4\n\t" \
  "v_fma_f32 v150, |v150|, v222, v150 mul:4\n\t" \
  "v_mfma_f32_16x16x32_bf16 v[192:195], v[216:219], v[136:139], v[192:195]\n\t" \
  "ds_read_u16_d16_hi v235, v235 offset:10528\n\t" \
  "v_cvt_pk_bf16_f32 v148, v148, v149\n\t" \
  "v_cvt_pk_bf16_f32 v149, v150, v151\n\t" \
  "v_mfma_f32_16x16x32_bf16 v[200:203], v[216:219], v[140:143], v[200:203]\n\t" \
  "ds_read_b64_tr_b16 v[216:217], v248 offset:51752\n\t" \
  "v_cvt_pknorm_i16_f32 v220, |v178|, |v179|\n\t" \
  "v_cvt_pknorm_i16_f32 v222, |v176|, |v177|\n\t" \
  "s_waitcnt lgkmcnt(8)\n\t" \
  "v_mfma_f32_16x16x32_bf16 v[180:183], v[208:211], v[96:99], 0\n\t" \
  "ds_read_b64_tr_b16 v[218:219], v248 offset:54824\n\t" \
  "v_lshlrev_b32_sdwa v221, 2, v220 dst_sel:DWORD dst_unused:UNUSED_PAD src0_sel:DWORD src1_sel:BYTE_3\n\t" \
  "s_waitcnt vmcnt(0)\n\t" \
  "v_lshlrev_b32_sdwa v220, 2, v220 dst_sel:DWORD dst_unused:UNUSED_PAD src0_sel:DWORD src1_sel:BYTE_1\n\t" \
  "v_mfma_f32_16x16x32_bf16 v[188:191], v[208:211], v[100:103], 0\n\t" \
  "ds_read_u16 v234, v234 offset:10528\n\t" \
  "v_lshlrev_b32_sdwa v223, 2, v222 dst_sel:DWORD dst_unused:UNUSED_PAD src0_sel:DWORD src1_sel:BYTE_3\n\t" \
  "v_lshlrev_b32_sdwa v222, 2, v222 dst_sel:DWORD dst_unused:UNUSED_PAD src0_sel:DWORD src1_sel:BYTE_1\n\t" \
  "v_mfma_f32_16x16x32_bf16 v[196:199], v[208:211], v[104:107], 0\n\t" \
  "ds_read_b32 v221, v221 offset:10016\n\t" \
  "v_lshlrev_b32_sdwa v225, 1, v187 dst_sel:DWORD dst_unused:UNUSED_PAD src0_sel:DWORD src1_sel:WORD_1\n\t" \
  "v_lshlrev_b32_sdwa v224, 1, v186 dst_sel:DWORD dst_unused:UNUSED_PAD src0_sel:DWORD src1_sel:WORD_1\n\t" \
  "v_mfma_f32_16x16x32_bf16 v[204:207], v[208:211], v[108:111], 0\n\t" \
  "ds_read_b32 v220, v220 offset:10016\n\t" \
  "v_lshlrev_b32_sdwa v227, 1, v185 dst_sel:DWORD dst_unused:UNUSED_PAD src0_sel:DWORD src1_sel:WORD_1\n\t" \
  "v_lshlrev_b32_sdwa v226, 1, v184 dst_sel:DWORD dst_unused:UNUSED_PAD src0_sel:DWORD src1_sel:WORD_1\n\t" \
  "s_waitcnt lgkmcnt(7)\n\t" \
  "v_mfma_f32_16x16x32_bf16 v[180:183], v[212:215], v[112:115], v[180:183]\n\t" \
  "ds_read_b32 v223, v223 offset:10016\n\t" \
  "v_add_u32 v248, -81480, v248\n\t" \
  "v_add_u32 v249, -81480, v249\n\t" \
  "v_mfma_f32_16x16x32_bf16 v[188:191], v[212:215], v[116:119], v[188:191]\n\t" \
  "ds_read_b64_tr_b16 v[208:209], v249 offset:24600\n\t" \
  "ds_read_b32 v222, v222 offset:10016\n\t" \
  "v_fma_mix_f32 v229, |v165|, v229, v229 op_sel:[0,1,0] op_sel_hi:[0,1,1] clamp\n\t" \
  "v_mfma_f32_16x16x32_bf16 v[196:199], v[212:215], v[120:123], v[196:199]\n\t" \
  "ds_read_b64_tr_b16 v[210:211], v249 offset:26136\n\t" \
  "v_fma_mix_f32 v228, |v164|, v228, v228 op_sel:[0,1,0] op_sel_hi:[0,1,1] clamp\n\t" \
  "v_fma_mix_f32 v231, |v167|, v231, v231 op_sel:[0,1,0] op_sel_hi:[0,1,1] clamp\n\t" \
  "v_mfma_f32_16x16x32_bf16 v[204:207], v[212:215], v[124:127], v[204:207]\n\t" \
  "ds_read_b64_tr_b16 v[212:213], v249 offset:24608\n\t" \
  "ds_read_u16_d16_hi v225, v225 offset:10528\n\t" \
  "v_fma_mix_f32 v230, |v166|, v230, v230 op_sel:[0,1,0] op_sel_hi:[0,1,1] clamp\n\t" \
  "s_waitcnt lgkmcnt(8)\n\t" \
  "v_mfma_f32_16x16x32_bf16 v[180:183], v[216:219], v[128:131], v[180:183]\n\t" \
  "ds_read_b64_tr_b16 v[214:215], v249 offset:26144\n\t" \
  "v_or_b32 v172, v232, v233\n\t" \
  "v_or_b32 v173, v234, v235\n\t" \
  "v_mfma_f32_16x16x32_bf16 v[188:191], v[216:219], v[132:135], v[188:191]\n\t" \
  "ds_read_u16 v224, v224 offset:10528\n\t" \
  "v_fma_f32 v165, |v165|, v229, v165 mul:4\n\t" \
  "v_fma_f32 v164, |v164|, v228, v164 mul:4\n\t" \
  "v_mfma_f32_16x16x32_bf16 v[196:199], v[216:219], v[136:139], v[196:199]\n\t" \
  "ds_read_u16_d16_hi v227, v227 offset:10528\n\t" \
  "v_fma_f32 v167, |v167|, v231, v167 mul:4\n\t" \
  "v_fma_f32 v166, |v166|, v230, v166 mul:4\n\t" \
  "v_mfma_f32_16x16x32_bf16 v[204:207], v[216:219], v[140:143], v[204:207]\n\t" \
  "ds_read_b64_tr_b16 v[216:217], v249 offset:24664\n\t" \
  "v_cvt_pk_bf16_f32 v164, v164, v165\n\t" \
  "v_cvt_pk_bf16_f32 v165, v166, v167\n\t" \
  "v_mfma_f32_4x4x4_16b_bf16 v[64:67], v[242:243], v[236:237], v[64:67]\n\t" \
  "v_mfma_f32_4x4x4_16b_bf16 v[68:71], v[242:243], v[238:239], v[68:71]\n\t" \
  "ds_read_b64_tr_b16 v[218:219], v249 offset:26200\n\t" \
  "v_cvt_pknorm_i16_f32 v228, |v194|, |v195|\n\t" \
  "v_cvt_pknorm_i16_f32 v230, |v192|, |v193|\n\t" \
  "s_waitcnt lgkmcnt(7)\n\t" \
  "v_mfma_f32_16x16x32_bf16 v[0:3], v[208:211], v[146:149], v[0:3]\n\t" \
  "ds_read_u16 v226, v226 offset:10528\n\t" \
  "v_lshlrev_b32_sdwa v229, 2, v228 dst_sel:DWORD dst_unused:UNUSED_PAD src0_sel:DWORD src1_sel:BYTE_3\n\t" \
  "v_lshlrev_b32_sdwa v228, 2, v228 dst_sel:DWORD dst_unused:UNUSED_PAD src0_sel:DWORD src1_sel:BYTE_1\n\t" \
  "v_mfma_f32_16x16x32_bf16 v[24:27], v[208:211], v[154:157], v[24:27]\n\t" \
  "ds_read_b32 v229, v229 offset:10016\n\t" \
  "v_lshlrev_b32_sdwa v231, 2, v230 dst_sel:DWORD dst_unused:UNUSED_PAD src0_sel:DWORD src1_sel:BYTE_3\n\t" \
  "v_lshlrev_b32_sdwa v230, 2, v230 dst_sel:DWORD dst_unused:UNUSED_PAD src0_sel:DWORD src1_sel:BYTE_1\n\t" \
  "v_mfma_f32_16x16x32_bf16 v[48:51], v[208:211], v[162:165], v[48:51]\n\t" \
  "ds_read_b32 v228, v228 offset:10016\n\t" \
  "v_lshlrev_b32_sdwa v233, 1, v203 dst_sel:DWORD dst_unused:UNUSED_PAD src0_sel:DWORD src1_sel:WORD_1\n\t" \
  "v_lshlrev_b32_sdwa v232, 1, v202 dst_sel:DWORD dst_unused:UNUSED_PAD src0_sel:DWORD src1_sel:WORD_1\n\t" \
  "v_mfma_f32_16x16x32_bf16 v[72:75], v[208:211], v[170:173], v[72:75]\n\t" \
  "ds_read_b64_tr_b16 v[208:209], v249 offset:24672\n\t" \
  "v_lshlrev_b32_sdwa v235, 1, v201 dst_sel:DWORD dst_unused:UNUSED_PAD src0_sel:DWORD src1_sel:WORD_1\n\t" \
  "s_waitcnt lgkmcnt(8)\n\t" \
  "v_mfma_f32_16x16x32_bf16 v[4:7], v[212:215], v[146:149], v[4:7]\n\t" \
  "ds_read_b64_tr_b16 v[210:211], v249 offset:26208\n\t" \
  "v_lshlrev_b32_sdwa v234, 1, v200 dst_sel:DWORD dst_unused:UNUSED_PAD src0_sel:DWORD src1_sel:WORD_1\n\t" \
  "v_fma_mix_f32 v221, |v179|, v221, v221 op_sel:[0,1,0] op_sel_hi:[0,1,1] clamp\n\t" \
  "v_mfma_f32_16x16x32_bf16 v[28:31], v[212:215], v[154:157], v[28:31]\n\t" \
  "ds_read_b32 v231, v231 offset:10016\n\t" \
  "ds_read_b32 v230, v230 offset:10016\n\t" \
  "v_fma_mix_f32 v220, |v178|, v220, v220 op_sel:[0,1,0] op_sel_hi:[0,1,1] clamp\n\t" \
  "v_mfma_f32_16x16x32_bf16 v[52:55], v[212:215], v[162:165], v[52:55]\n\t" \
  "ds_read_u16_d16_hi v233, v233 offset:10528\n\t" \
  "v_fma_mix_f32 v223, |v177|, v223, v223 op_sel:[0,1,0] op_sel_hi:[0,1,1] clamp\n\t" \
  "v_fma_mix_f32 v222, |v176|, v222, v222 op_sel:[0,1,0] op_sel_hi:[0,1,1] clamp\n\t" \
  "s_waitcnt lgkmcnt(7)\n\t" \
  "v_mfma_f32_16x16x32_bf16 v[76:79], v[212:215], v[170:173], v[76:79]\n\t" \
  "ds_read_b64_tr_b16 v[212:213], v249 offset:24728\n\t" \
  "v_or_b32 v187, v224, v225\n\t" \
  "v_or_b32 v186, v226, v227\n\t" \
  "v_mfma_f32_16x16x32_bf16 v[8:11], v[216:219], v[146:149], v[8:11]\n\t" \
  "ds_read_b64_tr_b16 v[214:215], v249 offset:26264\n\t" \
  "v_fma_f32 v179, |v179|, v221, v179 mul:4\n\t" \
  "v_fma_f32 v178, |v178|, v220, v178 mul:4\n\t" \
  "v_mfma_f32_16x16x32_bf16 v[32:35], v[216:219], v[154:157], v[32:35]\n\t" \
  "ds_read_u16 v232, v232 offset:10528\n\t" \
  "v_fma_f32 v177, |v177|, v223, v177 mul:4\n\t" \
  "v_fma_f32 v176, |v176|, v222, v176 mul:4\n\t" \
  "v_mfma_f32_16x16x32_bf16 v[56:59], v[216:219], v[162:165], v[56:59]\n\t" \
  "ds_read_u16_d16_hi v235, v235 offset:10528\n\t" \
  "v_cvt_pk_bf16_f32 v179, v178, v179\n\t" \
  "v_cvt_pk_bf16_f32 v178, v176, v177\n\t" \
  "v_mfma_f32_16x16x32_bf16 v[80:83], v[216:219], v[170:173], v[80:83]\n\t" \
  "ds_read_b64_tr_b16 v[216:217], v249 offset:24736\n\t" \
  "v_cvt_pknorm_i16_f32 v220, |v180|, |v181|\n\t" \
  "v_cvt_pknorm_i16_f32 v222, |v182|, |v183|\n\t" \
  "s_waitcnt lgkmcnt(8)\n\t" \
  "v_mfma_f32_16x16x32_bf16 v[12:15], v[208:211], v[146:149], v[12:15]\n\t" \
  "ds_read_b64_tr_b16 v[218:219], v249 offset:26272\n\t" \
  "v_lshlrev_b32_sdwa v221, 2, v220 dst_sel:DWORD dst_unused:UNUSED_PAD src0_sel:DWORD src1_sel:BYTE_3\n\t" \
  "v_lshlrev_b32_sdwa v220, 2, v220 dst_sel:DWORD dst_unused:UNUSED_PAD src0_sel:DWORD src1_sel:BYTE_1\n\t" \
  "v_mfma_f32_16x16x32_bf16 v[36:39], v[208:211], v[154:157], v[36:39]\n\t" \
  "ds_read_u16 v234, v234 offset:10528\n\t" \
  "v_lshlrev_b32_sdwa v223, 2, v222 dst_sel:DWORD dst_unused:UNUSED_PAD src0_sel:DWORD src1_sel:BYTE_3\n\t" \
  "v_mfma_f32_16x16x32_bf16 v[60:63], v[208:211], v[162:165], v[60:63]\n\t" \
  "ds_read_b32 v221, v221 offset:10016\n\t" \
  "v_lshlrev_b32_sdwa v222, 2, v222 dst_sel:DWORD dst_unused:UNUSED_PAD src0_sel:DWORD src1_sel:BYTE_1\n\t" \
  "v_lshlrev_b32_sdwa v225, 1, v189 dst_sel:DWORD dst_unused:UNUSED_PAD src0_sel:DWORD src1_sel:WORD_1\n\t" \
  "v_mfma_f32_16x16x32_bf16 v[84:87], v[208:211], v[170:173], v[84:87]\n\t" \
  "ds_read_b64_tr_b16 v[208:209], v248 offset:36904\n\t" \
  "v_lshlrev_b32_sdwa v224, 1, v188 dst_sel:DWORD dst_unused:UNUSED_PAD src0_sel:DWORD src1_sel:WORD_1\n\t" \
  "v_lshlrev_b32_sdwa v227, 1, v191 dst_sel:DWORD dst_unused:UNUSED_PAD src0_sel:DWORD src1_sel:WORD_1\n\t" \
  "s_waitcnt lgkmcnt(7)\n\t" \
  "v_mfma_f32_16x16x32_bf16 v[16:19], v[212:215], v[146:149], v[16:19]\n\t" \
  "ds_read_b64_tr_b16 v[210:211], v248 offset:37928\n\t" \
  "ds_read_b32 v220, v220 offset:10016\n\t" \
  "v_lshlrev_b32_sdwa v226, 1, v190 dst_sel:DWORD dst_unused:UNUSED_PAD src0_sel:DWORD src1_sel:WORD_1\n\t" \
  "v_mfma_f32_16x16x32_bf16 v[40:43], v[212:215], v[154:157], v[40:43]\n\t" \
  "ds_read_b32 v223, v223 offset:10016\n\t" \
  "v_fma_mix_f32 v229, |v195|, v229, v229 op_sel:[0,1,0] op_sel_hi:[0,1,1] clamp\n\t" \
  "v_fma_mix_f32 v228, |v194|, v228, v228 op_sel:[0,1,0] op_sel_hi:[0,1,1] clamp\n\t" \
  "v_mfma_f32_16x16x32_bf16 v[64:67], v[212:215], v[162:165], v[64:67]\n\t" \
  "ds_read_b32 v222, v222 offset:10016\n\t" \
  "v_fma_mix_f32 v231, |v193|, v231, v231 op_sel:[0,1,0] op_sel_hi:[0,1,1] clamp\n\t" \
  "v_fma_mix_f32 v230, |v192|, v230, v230 op_sel:[0,1,0] op_sel_hi:[0,1,1] clamp\n\t" \
  "v_mfma_f32_16x16x32_bf16 v[88:91], v[212:215], v[170:173], v[88:91]\n\t" \
  "ds_read_b64_tr_b16 v[212:213], v248 offset:39976\n\t" \
  "ds_read_u16_d16_hi v225, v225 offset:10528\n\t" \
  "s_waitcnt lgkmcnt(8)\n\t" \
  "v_or_b32 v203, v232, v233\n\t" \
  "v_mfma_f32_16x16x32_bf16 v[20:23], v[216:219], v[146:149], v[20:23]\n\t" \
  "ds_read_b64_tr_b16 v[214:215], v248 offset:41000\n\t" \
  "v_or_b32 v202, v234, v235\n\t" \
  "v_fma_f32 v195, |v195|, v229, v195 mul:4\n\t" \
  "v_mfma_f32_16x16x32_bf16 v[44:47], v[216:219], v[154:157], v[44:47]\n\t" \
  "ds_read_u16 v224, v224 offset:10528\n\t" \
  "v_fma_f32 v194, |v194|, v228, v194 mul:4\n\t" \
  "v_fma_f32 v193, |v193|, v231, v193 mul:4\n\t" \
  "v_mfma_f32_16x16x32_bf16 v[68:71], v[216:219], v[162:165], v[68:71]\n\t" \
  "ds_read_u16_d16_hi v227, v227 offset:10528\n\t" \
  "v_fma_f32 v192, |v192|, v230, v192 mul:4\n\t" \
  "v_cvt_pk_bf16_f32 v195, v194, v195\n\t" \
  "v_mfma_f32_16x16x32_bf16 v[92:95], v[216:219], v[170:173], v[92:95]\n\t" \
  "ds_read_b64_tr_b16 v[216:217], v248 offset:38952\n\t" \
  "v_cvt_pk_bf16_f32 v194, v192, v193\n\t" \
  "v_cvt_pknorm_i16_f32 v228, |v196|, |v197|\n\t" \
  "s_waitcnt lgkmcnt(9)\n\t" \
  "v_mfma_f32_16x16x32_bf16 v[144:147], v[208:211], v[96:99], 0\n\t" \
  "ds_read_b64_tr_b16 v[218:219], v248 offset:42024\n\t" \
  "v_cvt_pknorm_i16_f32 v230, |v198|, |v199|\n\t" \
  "v_lshlrev_b32_sdwa v229, 2, v228 dst_sel:DWORD dst_unused:UNUSED_PAD src0_sel:DWORD src1_sel:BYTE_3\n\t" \
  "v_mfma_f32_16x16x32_bf16 v[152:155], v[208:211], v[100:103], 0\n\t" \
  "ds_read_u16 v226, v226 offset:10528\n\t" \
  "v_lshlrev_b32_sdwa v228, 2, v228 dst_sel:DWORD dst_unused:UNUSED_PAD src0_sel:DWORD src1_sel:BYTE_1\n\t" \
  "v_lshlrev_b32_sdwa v231, 2, v230 dst_sel:DWORD dst_unused:UNUSED_PAD src0_sel:DWORD src1_sel:BYTE_3\n\t" \
  "v_mfma_f32_16x16x32_bf16 v[160:163], v[208:211], v[104:107], 0\n\t" \
  "ds_read_b32 v229, v229 offset:10016\n\t" \
  "v_lshlrev_b32_sdwa v230, 2, v230 dst_sel:DWORD dst_unused:UNUSED_PAD src0_sel:DWORD src1_sel:BYTE_1\n\t" \
  "v_lshlrev_b32_sdwa v233, 1, v205 dst_sel:DWORD dst_unused:UNUSED_PAD src0_sel:DWORD src1_sel:WORD_1\n\t" \
  "v_mfma_f32_16x16x32_bf16 v[168:171], v[208:211], v[108:111], 0\n\t" \
  "ds_read_b64_tr_b16 v[208:209], v248 offset:37416\n\t" \
  "v_lshlrev_b32_sdwa v232, 1, v204 dst_sel:DWORD dst_unused:UNUSED_PAD src0_sel:DWORD src1_sel:WORD_1\n\t" \
  "v_lshlrev_b32_sdwa v235, 1, v207 dst_sel:DWORD dst_unused:UNUSED_PAD src0_sel:DWORD src1_sel:WORD_1\n\t" \
  "s_waitcnt lgkmcnt(7)\n\t" \
  "v_mfma_f32_16x16x32_bf16 v[144:147], v[212:215], v[112:115], v[144:147]\n\t" \
  "ds_read_b64_tr_b16 v[210:211], v248 offset:38440\n\t" \
  "ds_read_b32 v228, v228 offset:10016\n\t" \
  "v_lshlrev_b32_sdwa v234, 1, v206 dst_sel:DWORD dst_unused:UNUSED_PAD src0_sel:DWORD src1_sel:WORD_1\n\t" \
  "v_mfma_f32_16x16x32_bf16 v[152:155], v[212:215], v[116:119], v[152:155]\n\t" \
  "ds_read_b32 v231, v231 offset:10016\n\t" \
  "v_fma_mix_f32 v221, |v181|, v221, v221 op_sel:[0,1,0] op_sel_hi:[0,1,1] clamp\n\t" \
  "v_fma_mix_f32 v220, |v180|, v220, v220 op_sel:[0,1,0] op_sel_hi:[0,1,1] clamp\n\t" \
  "v_mfma_f32_16x16x32_bf16 v[160:163], v[212:215], v[120:123], v[160:163]\n\t" \
  "ds_read_b32 v230, v230 offset:10016\n\t" \
  "v_fma_mix_f32 v223, |v183|, v223, v223 op_sel:[0,1,0] op_sel_hi:[0,1,1] clamp\n\t" \
  "v_fma_mix_f32 v222, |v182|, v222, v222 op_sel:[0,1,0] op_sel_hi:[0,1,1] clamp\n\t" \
  "v_mfma_f32_16x16x32_bf16 v[168:171], v[212:215], v[124:127], v[168:171]\n\t" \
  "ds_read_b64_tr_b16 v[212:213], v248 offset:40488\n\t" \
  "ds_read_u16_d16_hi v233, v233 offset:10528\n\t" \
  "s_waitcnt lgkmcnt(8)\n\t" \
  "v_or_b32 v188, v224, v225\n\t" \
  "v_mfma_f32_16x16x32_bf16 v[144:147], v[216:219], v[128:131], v[144:147]\n\t" \
  "ds_read_b64_tr_b16 v[214:215], v248 offset:41512\n\t" \
  "v_or_b32 v189, v226, v227\n\t" \
  "v_fma_f32 v181, |v181|, v221, v181 mul:4\n\t" \
  "v_mfma_f32_16x16x32_bf16 v[152:155], v[216:219], v[132:135], v[152:155]\n\t" \
  "ds_read_u16 v232, v232 offset:10528\n\t" \
  "v_fma_f32 v180, |v180|, v220, v180 mul:4\n\t" \
  "v_fma_f32 v183, |v183|, v223, v183 mul:4\n\t" \
  "v_mfma_f32_16x16x32_bf16 v[160:163], v[216:219], v[136:139], v[160:163]\n\t" \
  "ds_read_u16_d16_hi v235, v235 offset:10528\n\t" \
  "v_fma_f32 v182, |v182|, v222, v182 mul:4\n\t" \
  "v_cvt_pk_bf16_f32 v180, v180, v181\n\t" \
  "v_mfma_f32_16x16x32_bf16 v[168:171], v[216:219], v[140:143], v[168:171]\n\t" \
  "ds_read_b64_tr_b16 v[216:217], v248 offset:39464\n\t" \
  "v_cvt_pk_bf16_f32 v181, v182, v183\n\t" \
  "v_cvt_pknorm_i16_f32 v220, |v146|, |v147|\n\t" \
  "s_waitcnt lgkmcnt(9)\n\t" \
  "v_mfma_f32_16x16x32_bf16 v[148:151], v[208:211], v[96:99], 0\n\t" \
  "ds_read_b64_tr_b16 v[218:219], v248 offset:42536\n\t" \
  "v_cvt_pknorm_i16_f32 v222, |v144|, |v145|\n\t" \
  "v_lshlrev_b32_sdwa v221, 2, v220 dst_sel:DWORD dst_unused:UNUSED_PAD src0_sel:DWORD src1_sel:BYTE_3\n\t" \
  "v_mfma_f32_16x16x32_bf16 v[156:159], v[208:211], v[100:103], 0\n\t" \
  "ds_read_u16 v234, v234 offset:10528\n\t" \
  "v_lshlrev_b32_sdwa v220, 2, v220 dst_sel:DWORD dst_unused:UNUSED_PAD src0_sel:DWORD src1_sel:BYTE_1\n\t" \
  "v_lshlrev_b32_sdwa v223, 2, v222 dst_sel:DWORD dst_unused:UNUSED_PAD src0_sel:DWORD src1_sel:BYTE_3\n\t" \
  "v_mfma_f32_16x16x32_bf16 v[164:167], v[208:211], v[104:107], 0\n\t" \
  "ds_read_b32 v221, v221 offset:10016\n\t" \
  "v_lshlrev_b32_sdwa v222, 2, v222 dst_sel:DWORD dst_unused:UNUSED_PAD src0_sel:DWORD src1_sel:BYTE_1\n\t" \
  "v_mfma_f32_16x16x32_bf16 v[172:175], v[208:211], v[108:111], 0\n\t" \
  "ds_read_b32 v220, v220 offset:10016\n\t" \
  "v_lshlrev_b32_sdwa v225, 1, v155 dst_sel:DWORD dst_unused:UNUSED_PAD src0_sel:DWORD src1_sel:WORD_1\n\t" \
  "v_lshlrev_b32_sdwa v224, 1, v154 dst_sel:DWORD dst_unused:UNUSED_PAD src0_sel:DWORD src1_sel:WORD_1\n\t" \
  "s_waitcnt lgkmcnt(7)\n\t" \
  "v_mfma_f32_16x16x32_bf16 v[148:151], v[212:215], v[112:115], v[148:151]\n\t" \
  "ds_read_b32 v223, v223 offset:10016\n\t" \
  "v_lshlrev_b32_sdwa v227, 1, v153 dst_sel:DWORD dst_unused:UNUSED_PAD src0_sel:DWORD src1_sel:WORD_1\n\t" \
  "v_lshlrev_b32_sdwa v226, 1, v152 dst_sel:DWORD dst_unused:UNUSED_PAD src0_sel:DWORD src1_sel:WORD_1\n\t" \
  "v_mfma_f32_16x16x32_bf16 v[156:159], v[212:215], v[116:119], v[156:159]\n\t" \
  "ds_read_b64_tr_b16 v[208:209], v249 offset:30752\n\t" \
  "ds_read_b32 v222, v222 offset:10016\n\t" \
  "v_fma_mix_f32 v229, |v197|, v229, v229 op_sel:[0,1,0] op_sel_hi:[0,1,1] clamp\n\t" \
  "v_mfma_f32_16x16x32_bf16 v[164:167], v[212:215], v[120:123], v[164:167]\n\t" \
  "ds_read_b64_tr_b16 v[210:211], v249 offset:32288\n\t" \
  "v_fma_mix_f32 v228, |v196|, v228, v228 op_sel:[0,1,0] op_sel_hi:[0,1,1] clamp\n\t" \
  "v_fma_mix_f32 v231, |v199|, v231, v231 op_sel:[0,1,0] op_sel_hi:[0,1,1] clamp\n\t" \
  "v_mfma_f32_16x16x32_bf16 v[172:175], v[212:215], v[124:127], v[172:175]\n\t" \
  "ds_read_b64_tr_b16 v[212:213], v249 offset:30760\n\t" \
  "ds_read_u16_d16_hi v225, v225 offset:10528\n\t" \
  "v_fma_mix_f32 v230, |v198|, v230, v230 op_sel:[0,1,0] op_sel_hi:[0,1,1] clamp\n\t" \
  "s_waitcnt lgkmcnt(8)\n\t" \
  "v_mfma_f32_16x16x32_bf16 v[148:151], v[216:219], v[128:131], v[148:151]\n\t" \
  "ds_read_b64_tr_b16 v[214:215], v249 offset:32296\n\t" \
  "v_or_b32 v204, v232, v233\n\t" \
  "v_or_b32 v205, v234, v235\n\t" \
  "v_mfma_f32_16x16x32_bf16 v[156:159], v[216:219], v[132:135], v[156:159]\n\t" \
  "ds_read_u16 v224, v224 offset:10528\n\t" \
  "v_fma_f32 v197, |v197|, v229, v197 mul:4\n\t" \
  "v_fma_f32 v196, |v196|, v228, v196 mul:4\n\t" \
  "v_mfma_f32_16x16x32_bf16 v[164:167], v[216:219], v[136:139], v[164:167]\n\t" \
  "ds_read_u16_d16_hi v227, v227 offset:10528\n\t" \
  "v_fma_f32 v199, |v199|, v231, v199 mul:4\n\t" \
  "v_fma_f32 v198, |v198|, v230, v198 mul:4\n\t" \
  "v_mfma_f32_16x16x32_bf16 v[172:175], v[216:219], v[140:143], v[172:175]\n\t" \
  "ds_read_b64_tr_b16 v[216:217], v249 offset:30816\n\t" \
  "v_cvt_pk_bf16_f32 v196, v196, v197\n\t" \
  "v_cvt_pk_bf16_f32 v197, v198, v199\n\t" \
  "s_waitcnt lgkmcnt(6)\n\t" \
  "v_mfma_f32_16x16x32_bf16 v[0:3], v[208:211], v[178:181], v[0:3]\n\t" \
  "ds_read_b64_tr_b16 v[218:219], v249 offset:32352\n\t" \
  "v_cvt_pknorm_i16_f32 v228, |v162|, |v163|\n\t" \
  "v_cvt_pknorm_i16_f32 v230, |v160|, |v161|\n\t" \
  "v_mfma_f32_16x16x32_bf16 v[24:27], v[208:211], v[186:189], v[24:27]\n\t" \
  "ds_read_u16 v226, v226 offset:10528\n\t" \
  "v_lshlrev_b32_sdwa v229, 2, v228 dst_sel:DWORD dst_unused:UNUSED_PAD src0_sel:DWORD src1_sel:BYTE_3\n\t" \
  "v_lshlrev_b32_sdwa v228, 2, v228 dst_sel:DWORD dst_unused:UNUSED_PAD src0_sel:DWORD src1_sel:BYTE_1\n\t" \
  "v_mfma_f32_16x16x32_bf16 v[48:51], v[208:211], v[194:197], v[48:51]\n\t" \
  "ds_read_b32 v229, v229 offset:10016\n\t" \
  "v_lshlrev_b32_sdwa v231, 2, v230 dst_sel:DWORD dst_unused:UNUSED_PAD src0_sel:DWORD src1_sel:BYTE_3\n\t" \
  "v_lshlrev_b32_sdwa v230, 2, v230 dst_sel:DWORD dst_unused:UNUSED_PAD src0_sel:DWORD src1_sel:BYTE_1\n\t" \
  "v_mfma_f32_16x16x32_bf16 v[72:75], v[208:211], v[202:205], v[72:75]\n\t" \
  "ds_read_b64_tr_b16 v[208:209], v249 offset:30824\n\t" \
  "v_lshlrev_b32_sdwa v233, 1, v171 dst_sel:DWORD dst_unused:UNUSED_PAD src0_sel:DWORD src1_sel:WORD_1\n\t" \
  "v_lshlrev_b32_sdwa v232, 1, v170 dst_sel:DWORD dst_unused:UNUSED_PAD src0_sel:DWORD src1_sel:WORD_1\n\t" \
  "s_waitcnt lgkmcnt(7)\n\t" \
  "v_mfma_f32_16x16x32_bf16 v[4:7], v[212:215], v[178:181], v[4:7]\n\t" \
  "ds_read_b64_tr_b16 v[210:211], v249 offset:32360\n\t" \
  "v_lshlrev_b32_sdwa v235, 1, v169 dst_sel:DWORD dst_unused:UNUSED_PAD src0_sel:DWORD src1_sel:WORD_1\n\t" \
  "v_lshlrev_b32_sdwa v234, 1, v168 dst_sel:DWORD dst_unused:UNUSED_PAD src0_sel:DWORD src1_sel:WORD_1\n\t" \
  "v_mfma_f32_16x16x32_bf16 v[28:31], v[212:215], v[186:189], v[28:31]\n\t" \
  "ds_read_b32 v228, v228 offset:10016\n\t" \
  "ds_read_b32 v231, v231 offset:10016\n\t" \
  "v_fma_mix_f32 v221, |v147|, v221, v221 op_sel:[0,1,0] op_sel_hi:[0,1,1] clamp\n\t" \
  "v_mfma_f32_16x16x32_bf16 v[52:55], v[212:215], v[194:197], v[52:55]\n\t" \
  "ds_read_b32 v230, v230 offset:10016\n\t" \
  "v_fma_mix_f32 v220, |v146|, v220, v220 op_sel:[0,1,0] op_sel_hi:[0,1,1] clamp\n\t" \
  "v_fma_mix_f32 v223, |v145|, v223, v223 op_sel:[0,1,0] op_sel_hi:[0,1,1] clamp\n\t" \
  "v_mfma_f32_16x16x32_bf16 v[76:79], v[212:215], v[202:205], v[76:79]\n\t" \
  "ds_read_b64_tr_b16 v[212:213], v249 offset:30880\n\t" \
  "ds_read_u16_d16_hi v233, v233 offset:10528\n\t" \
  "v_fma_mix_f32 v222, |v144|, v222, v222 op_sel:[0,1,0] op_sel_hi:[0,1,1] clamp\n\t" \
  "s_waitcnt lgkmcnt(8)\n\t" \
  "v_mfma_f32_16x16x32_bf16 v[8:11], v[216:219], v[178:181], v[8:11]\n\t" \
  "ds_read_b64_tr_b16 v[214:215], v249 offset:32416\n\t" \
  "v_or_b32 v155, v224, v225\n\t" \
  "v_or_b32 v154, v226, v227\n\t" \
  "v_mfma_f32_16x16x32_bf16 v[32:35], v[216:219], v[186:189], v[32:35]\n\t" \
  "ds_read_u16 v232, v232 offset:10528\n\t" \
  "v_fma_f32 v147, |v147|, v221, v147 mul:4\n\t" \
  "v_fma_f32 v146, |v146|, v220, v146 mul:4\n\t" \
  "v_mfma_f32_16x16x32_bf16 v[56:59], v[216:219], v[194:197], v[56:59]\n\t" \
  "ds_read_u16_d16_hi v235, v235 offset:10528\n\t" \
  "v_fma_f32 v145, |v145|, v223, v145 mul:4\n\t" \
  "v_fma_f32 v144, |v144|, v222, v144 mul:4\n\t" \
  "v_mfma_f32_16x16x32_bf16 v[80:83], v[216:219], v[202:205], v[80:83]\n\t" \
  "ds_read_b64_tr_b16 v[216:217], v249 offset:30888\n\t" \
  "v_cvt_pk_bf16_f32 v147, v146, v147\n\t" \
  "v_cvt_pk_bf16_f32 v146, v144, v145\n\t" \
  "s_waitcnt lgkmcnt(9)\n\t" \
  "v_mfma_f32_16x16x32_bf16 v[12:15], v[208:211], v[178:181], v[12:15]\n\t" \
  "ds_read_b64_tr_b16 v[218:219], v249 offset:32424\n\t" \
  "v_cvt_pknorm_i16_f32 v220, |v148|, |v149|\n\t" \
  "v_cvt_pknorm_i16_f32 v222, |v150|, |v151|\n\t" \
  "v_mfma_f32_16x16x32_bf16 v[36:39], v[208:211], v[186:189], v[36:39]\n\t" \
  "ds_read_u16 v234, v234 offset:10528\n\t" \
  "v_lshlrev_b32_sdwa v221, 2, v220 dst_sel:DWORD dst_unused:UNUSED_PAD src0_sel:DWORD src1_sel:BYTE_3\n\t" \
  "v_lshlrev_b32_sdwa v220, 2, v220 dst_sel:DWORD dst_unused:UNUSED_PAD src0_sel:DWORD src1_sel:BYTE_1\n\t" \
  "v_mfma_f32_16x16x32_bf16 v[60:63], v[208:211], v[194:197], v[60:63]\n\t" \
  "ds_read_b32 v221, v221 offset:10016\n\t" \
  "v_lshlrev_b32_sdwa v223, 2, v222 dst_sel:DWORD dst_unused:UNUSED_PAD src0_sel:DWORD src1_sel:BYTE_3\n\t" \
  "v_lshlrev_b32_sdwa v222, 2, v222 dst_sel:DWORD dst_unused:UNUSED_PAD src0_sel:DWORD src1_sel:BYTE_1\n\t" \
  "v_mfma_f32_16x16x32_bf16 v[84:87], v[208:211], v[202:205], v[84:87]\n\t" \
  "ds_read_b64_tr_b16 v[208:209], v248 offset:0\n\t" \
  "v_lshlrev_b32_sdwa v225, 1, v157 dst_sel:DWORD dst_unused:UNUSED_PAD src0_sel:DWORD src1_sel:WORD_1\n\t" \
  "v_lshlrev_b32_sdwa v224, 1, v156 dst_sel:DWORD dst_unused:UNUSED_PAD src0_sel:DWORD src1_sel:WORD_1\n\t" \
  "s_waitcnt lgkmcnt(7)\n\t" \
  "v_mfma_f32_16x16x32_bf16 v[16:19], v[212:215], v[178:181], v[16:19]\n\t" \
  "ds_read_b64_tr_b16 v[210:211], v248 offset:1024\n\t" \
  "v_lshlrev_b32_sdwa v227, 1, v159 dst_sel:DWORD dst_unused:UNUSED_PAD src0_sel:DWORD src1_sel:WORD_1\n\t" \
  "v_lshlrev_b32_sdwa v226, 1, v158 dst_sel:DWORD dst_unused:UNUSED_PAD src0_sel:DWORD src1_sel:WORD_1\n\t" \
  "v_mfma_f32_16x16x32_bf16 v[40:43], v[212:215], v[186:189], v[40:43]\n\t" \
  "ds_read_b32 v220, v220 offset:10016\n\t" \
  "ds_read_b32 v223, v223 offset:10016\n\t" \
  "v_fma_mix_f32 v229, |v163|, v229, v229 op_sel:[0,1,0] op_sel_hi:[0,1,1] clamp\n\t" \
  "v_mfma_f32_16x16x32_bf16 v[64:67], v[212:215], v[194:197], v[64:67]\n\t" \
  "ds_read_b32 v222, v222 offset:10016\n\t" \
  "v_fma_mix_f32 v228, |v162|, v228, v228 op_sel:[0,1,0] op_sel_hi:[0,1,1] clamp\n\t" \
  "v_fma_mix_f32 v231, |v161|, v231, v231 op_sel:[0,1,0] op_sel_hi:[0,1,1] clamp\n\t" \
  "v_mfma_f32_16x16x32_bf16 v[88:91], v[212:215], v[202:205], v[88:91]\n\t" \
  "ds_read_b64_tr_b16 v[212:213], v248 offset:3072\n\t" \
  "ds_read_u16_d16_hi v225, v225 offset:10528\n\t" \
  "v_fma_mix_f32 v230, |v160|, v230, v230 op_sel:[0,1,0] op_sel_hi:[0,1,1] clamp\n\t" \
  "s_waitcnt lgkmcnt(8)\n\t" \
  "v_mfma_f32_16x16x32_bf16 v[20:23], v[216:219], v[178:181], v[20:23]\n\t" \
  "ds_read_b64_tr_b16 v[214:215], v248 offset:4096\n\t" \
  "v_or_b32 v171, v232, v233\n\t" \
  "v_or_b32 v170, v234, v235\n\t" \
  "v_mfma_f32_16x16x32_bf16 v[44:47], v[216:219], v[186:189], v[44:47]\n\t" \
  "ds_read_u16 v224, v224 offset:10528\n\t" \
  "v_fma_f32 v163, |v163|, v229, v163 mul:4\n\t" \
  "v_fma_f32 v162, |v162|, v228, v162 mul:4\n\t" \
  "v_mfma_f32_16x16x32_bf16 v[68:71], v[216:219], v[194:197], v[68:71]\n\t" \
  "ds_read_u16_d16_hi v227, v227 offset:10528\n\t" \
  "v_fma_f32 v161, |v161|, v231, v161 mul:4\n\t" \
  "v_fma_f32 v160, |v160|, v230, v160 mul:4\n\t" \
  "v_mfma_f32_16x16x32_bf16 v[92:95], v[216:219], v[202:205], v[92:95]\n\t" \
  "ds_read_b64_tr_b16 v[216:217], v248 offset:2048\n\t" \
  "v_cvt_pk_bf16_f32 v163, v162, v163\n\t" \
  "v_cvt_pk_bf16_f32 v162, v160, v161\n\t" \
  "s_waitcnt lgkmcnt(9)\n\t" \
  "v_mfma_f32_16x16x32_bf16 v[176:179], v[208:211], v[96:99], 0\n\t" \
  "ds_read_b64_tr_b16 v[218:219], v248 offset:5120\n\t" \
  "v_cvt_pknorm_i16_f32 v228, |v164|, |v165|\n\t" \
  "v_cvt_pknorm_i16_f32 v230, |v166|, |v167|\n\t" \
  "v_mfma_f32_16x16x32_bf16 v[184:187], v[208:211], v[100:103], 0\n\t" \
  "ds_read_u16 v226, v226 offset:10528\n\t" \
  "v_lshlrev_b32_sdwa v229, 2, v228 dst_sel:DWORD dst_unused:UNUSED_PAD src0_sel:DWORD src1_sel:BYTE_3\n\t" \
  "v_lshlrev_b32_sdwa v228, 2, v228 dst_sel:DWORD dst_unused:UNUSED_PAD src0_sel:DWORD src1_sel:BYTE_1\n\t" \
  "v_mfma_f32_16x16x32_bf16 v[192:195], v[208:211], v[104:107], 0\n\t" \
  "ds_read_b32 v229, v229 offset:10016\n\t" \
  "v_lshlrev_b32_sdwa v231, 2, v230 dst_sel:DWORD dst_unused:UNUSED_PAD src0_sel:DWORD src1_sel:BYTE_3\n\t" \
  "v_lshlrev_b32_sdwa v230, 2, v230 dst_sel:DWORD dst_unused:UNUSED_PAD src0_sel:DWORD src1_sel:BYTE_1\n\t" \
  "v_mfma_f32_16x16x32_bf16 v[200:203], v[208:211], v[108:111], 0\n\t" \
  "ds_read_b64_tr_b16 v[208:209], v248 offset:512\n\t" \
  "v_lshlrev_b32_sdwa v233, 1, v173 dst_sel:DWORD dst_unused:UNUSED_PAD src0_sel:DWORD src1_sel:WORD_1\n\t" \
  "v_lshlrev_b32_sdwa v232, 1, v172 dst_sel:DWORD dst_unused:UNUSED_PAD src0_sel:DWORD src1_sel:WORD_1\n\t" \
  "s_waitcnt lgkmcnt(7)\n\t" \
  "v_mfma_f32_16x16x32_bf16 v[176:179], v[212:215], v[112:115], v[176:179]\n\t" \
  "ds_read_b64_tr_b16 v[210:211], v248 offset:1536\n\t" \
  "v_lshlrev_b32_sdwa v235, 1, v175 dst_sel:DWORD dst_unused:UNUSED_PAD src0_sel:DWORD src1_sel:WORD_1\n\t" \
  "v_lshlrev_b32_sdwa v234, 1, v174 dst_sel:DWORD dst_unused:UNUSED_PAD src0_sel:DWORD src1_sel:WORD_1\n\t" \
  "v_mfma_f32_16x16x32_bf16 v[184:187], v[212:215], v[116:119], v[184:187]\n\t" \
  "ds_read_b32 v228, v228 offset:10016\n\t" \
  "ds_read_b32 v231, v231 offset:10016\n\t" \
  "v_fma_mix_f32 v221, |v149|, v221, v221 op_sel:[0,1,0] op_sel_hi:[0,1,1] clamp\n\t" \
  "v_mfma_f32_16x16x32_bf16 v[192:195], v[212:215], v[120:123], v[192:195]\n\t" \
  "ds_read_b32 v230, v230 offset:10016\n\t" \
  "v_fma_mix_f32 v220, |v148|, v220, v220 op_sel:[0,1,0] op_sel_hi:[0,1,1] clamp\n\t" \
  "v_fma_mix_f32 v223, |v151|, v223, v223 op_sel:[0,1,0] op_sel_hi:[0,1,1] clamp\n\t" \
  "v_mfma_f32_16x16x32_bf16 v[200:203], v[212:215], v[124:127], v[200:203]\n\t" \
  "ds_read_b64_tr_b16 v[212:213], v248 offset:3584\n\t" \
  "ds_read_u16_d16_hi v233, v233 offset:10528\n\t" \
  "v_fma_mix_f32 v222, |v150|, v222, v222 op_sel:[0,1,0] op_sel_hi:[0,1,1] clamp\n\t" \
  "s_waitcnt lgkmcnt(8)\n\t" \
  "v_mfma_f32_16x16x32_bf16 v[176:179], v[216:219], v[128:131], v[176:179]\n\t" \
  "ds_read_b64_tr_b16 v[214:215], v248 offset:4608\n\t" \
  "v_or_b32 v156, v224, v225\n\t" \
  "v_or_b32 v157, v226, v227\n\t" \
  "v_mfma_f32_16x16x32_bf16 v[184:187], v[216:219], v[132:135], v[184:187]\n\t" \
  "ds_read_u16 v232, v232 offset:10528\n\t" \
  "v_fma_f32 v149, |v149|, v221, v149 mul:4\n\t" \
  "v_fma_f32 v148, |v148|, v220, v148 mul:4\n\t" \
  "v_mfma_f32_16x16x32_bf16 v[192:195], v[216:219], v[136:139], v[192:195]\n\t" \
  "ds_read_u16_d16_hi v235, v235 offset:10528\n\t" \
  "v_fma_f32 v151, |v151|, v223, v151 mul:4\n\t" \
  "v_fma_f32 v150, |v150|, v222, v150 mul:4\n\t" \
  "v_mfma_f32_16x16x32_bf16 v[200:203], v[216:219], v[140:143], v[200:203]\n\t" \
  "ds_read_b64_tr_b16 v[216:217], v248 offset:2560\n\t" \
  "v_cvt_pk_bf16_f32 v148, v148, v149\n\t" \
  "v_cvt_pk_bf16_f32 v149, v150, v151\n\t" \
  "s_waitcnt lgkmcnt(9)\n\t" \
  "v_mfma_f32_16x16x32_bf16 v[180:183], v[208:211], v[96:99], 0\n\t" \
  "ds_read_b64_tr_b16 v[218:219], v248 offset:5632\n\t" \
  "v_cvt_pknorm_i16_f32 v220, |v178|, |v179|\n\t" \
  "v_cvt_pknorm_i16_f32 v222, |v176|, |v177|\n\t" \
  "v_mfma_f32_16x16x32_bf16 v[188:191], v[208:211], v[100:103], 0\n\t" \
  "ds_read_u16 v234, v234 offset:10528\n\t" \
  "v_lshlrev_b32_sdwa v221, 2, v220 dst_sel:DWORD dst_unused:UNUSED_PAD src0_sel:DWORD src1_sel:BYTE_3\n\t" \
  "v_lshlrev_b32_sdwa v220, 2, v220 dst_sel:DWORD dst_unused:UNUSED_PAD src0_sel:DWORD src1_sel:BYTE_1\n\t" \
  "v_mfma_f32_16x16x32_bf16 v[196:199], v[208:211], v[104:107], 0\n\t" \
  "ds_read_b32 v221, v221 offset:10016\n\t" \
  "v_lshlrev_b32_sdwa v223, 2, v222 dst_sel:DWORD dst_unused:UNUSED_PAD src0_sel:DWORD src1_sel:BYTE_3\n\t" \
  "v_lshlrev_b32_sdwa v222, 2, v222 dst_sel:DWORD dst_unused:UNUSED_PAD src0_sel:DWORD src1_sel:BYTE_1\n\t" \
  "v_mfma_f32_16x16x32_bf16 v[204:207], v[208:211], v[108:111], 0\n\t" \
  "ds_read_b32 v220, v220 offset:10016\n\t" \
  "v_lshlrev_b32_sdwa v225, 1, v187 dst_sel:DWORD dst_unused:UNUSED_PAD src0_sel:DWORD src1_sel:WORD_1\n\t" \
  "v_lshlrev_b32_sdwa v224, 1, v186 dst_sel:DWORD dst_unused:UNUSED_PAD src0_sel:DWORD src1_sel:WORD_1\n\t" \
  "s_waitcnt lgkmcnt(7)\n\t" \
  "v_mfma_f32_16x16x32_bf16 v[180:183], v[212:215], v[112:115], v[180:183]\n\t" \
  "ds_read_b32 v223, v223 offset:10016\n\t" \
  "v_lshlrev_b32_sdwa v227, 1, v185 dst_sel:DWORD dst_unused:UNUSED_PAD src0_sel:DWORD src1_sel:WORD_1\n\t" \
  "v_lshlrev_b32_sdwa v226, 1, v184 dst_sel:DWORD dst_unused:UNUSED_PAD src0_sel:DWORD src1_sel:WORD_1\n\t" \
  "v_mfma_f32_16x16x32_bf16 v[188:191], v[212:215], v[116:119], v[188:191]\n\t" \
  "ds_read_b64_tr_b16 v[208:209], v249 offset:6152\n\t" \
  "ds_read_b32 v222, v222 offset:10016\n\t" \
  "v_fma_mix_f32 v229, |v165|, v229, v229 op_sel:[0,1,0] op_sel_hi:[0,1,1] clamp\n\t" \
  "v_mfma_f32_16x16x32_bf16 v[196:199], v[212:215], v[120:123], v[196:199]\n\t" \
  "ds_read_b64_tr_b16 v[210:211], v249 offset:7688\n\t" \
  "v_fma_mix_f32 v228, |v164|, v228, v228 op_sel:[0,1,0] op_sel_hi:[0,1,1] clamp\n\t" \
  "v_fma_mix_f32 v231, |v167|, v231, v231 op_sel:[0,1,0] op_sel_hi:[0,1,1] clamp\n\t" \
  "v_mfma_f32_16x16x32_bf16 v[204:207], v[212:215], v[124:127], v[204:207]\n\t" \
  "ds_read_b64_tr_b16 v[212:213], v249 offset:6160\n\t" \
  "ds_read_u16_d16_hi v225, v225 offset:10528\n\t" \
  "v_fma_mix_f32 v230, |v166|, v230, v230 op_sel:[0,1,0] op_sel_hi:[0,1,1] clamp\n\t" \
  "s_waitcnt lgkmcnt(8)\n\t" \
  "v_mfma_f32_16x16x32_bf16 v[180:183], v[216:219], v[128:131], v[180:183]\n\t" \
  "ds_read_b64_tr_b16 v[214:215], v249 offset:7696\n\t" \
  "v_or_b32 v172, v232, v233\n\t" \
  "v_or_b32 v173, v234, v235\n\t" \
  "v_mfma_f32_16x16x32_bf16 v[188:191], v[216:219], v[132:135], v[188:191]\n\t" \
  "ds_read_u16 v224, v224 offset:10528\n\t" \
  "v_fma_f32 v165, |v165|, v229, v165 mul:4\n\t" \
  "v_fma_f32 v164, |v164|, v228, v164 mul:4\n\t" \
  "v_mfma_f32_16x16x32_bf16 v[196:199], v[216:219], v[136:139], v[196:199]\n\t" \
  "ds_read_u16_d16_hi v227, v227 offset:10528\n\t" \
  "v_fma_f32 v167, |v167|, v231, v167 mul:4\n\t" \
  "v_fma_f32 v166, |v166|, v230, v166 mul:4\n\t" \
  "v_mfma_f32_16x16x32_bf16 v[204:207], v[216:219], v[140:143], v[204:207]\n\t" \
  "ds_read_b64_tr_b16 v[216:217], v249 offset:6216\n\t" \
  "v_cvt_pk_bf16_f32 v164, v164, v165\n\t" \
  "v_cvt_pk_bf16_f32 v165, v166, v167\n\t" \
  "s_waitcnt lgkmcnt(6)\n\t" \
  "v_mfma_f32_16x16x32_bf16 v[0:3], v[208:211], v[146:149], v[0:3]\n\t" \
  "ds_read_b64_tr_b16 v[218:219], v249 offset:7752\n\t" \
  "v_cvt_pknorm_i16_f32 v228, |v194|, |v195|\n\t" \
  "v_cvt_pknorm_i16_f32 v230, |v192|, |v193|\n\t" \
  "v_lshlrev_b32_sdwa v229, 2, v228 dst_sel:DWORD dst_unused:UNUSED_PAD src0_sel:DWORD src1_sel:BYTE_3\n\t" \
  "v_mfma_f32_16x16x32_bf16 v[24:27], v[208:211], v[154:157], v[24:27]\n\t" \
  "ds_read_u16 v226, v226 offset:10528\n\t" \
  "v_lshlrev_b32_sdwa v228, 2, v228 dst_sel:DWORD dst_unused:UNUSED_PAD src0_sel:DWORD src1_sel:BYTE_1\n\t" \
  "v_lshlrev_b32_sdwa v231, 2, v230 dst_sel:DWORD dst_unused:UNUSED_PAD src0_sel:DWORD src1_sel:BYTE_3\n\t" \
  "v_lshlrev_b32_sdwa v230, 2, v230 dst_sel:DWORD dst_unused:UNUSED_PAD src0_sel:DWORD src1_sel:BYTE_1\n\t" \
  "v_mfma_f32_16x16x32_bf16 v[48:51], v[208:211], v[162:165], v[48:51]\n\t" \
  "ds_read_b32 v229, v229 offset:10016\n\t" \
  "v_lshlrev_b32_sdwa v233, 1, v203 dst_sel:DWORD dst_unused:UNUSED_PAD src0_sel:DWORD src1_sel:WORD_1\n\t" \
  "v_lshlrev_b32_sdwa v232, 1, v202 dst_sel:DWORD dst_unused:UNUSED_PAD src0_sel:DWORD src1_sel:WORD_1\n\t" \
  "v_lshlrev_b32_sdwa v235, 1, v201 dst_sel:DWORD dst_unused:UNUSED_PAD src0_sel:DWORD src1_sel:WORD_1\n\t" \
  "v_mfma_f32_16x16x32_bf16 v[72:75], v[208:211], v[170:173], v[72:75]\n\t" \
  "ds_read_b64_tr_b16 v[208:209], v249 offset:6224\n\t" \
  "ds_read_b32 v228, v228 offset:10016\n\t" \
  "v_lshlrev_b32_sdwa v234, 1, v200 dst_sel:DWORD dst_unused:UNUSED_PAD src0_sel:DWORD src1_sel:WORD_1\n\t" \
  "v_fma_mix_f32 v221, |v179|, v221, v221 op_sel:[0,1,0] op_sel_hi:[0,1,1] clamp\n\t" \
  "v_fma_mix_f32 v220, |v178|, v220, v220 op_sel:[0,1,0] op_sel_hi:[0,1,1] clamp\n\t" \
  "s_waitcnt lgkmcnt(8)\n\t" \
  "v_mfma_f32_16x16x32_bf16 v[4:7], v[212:215], v[146:149], v[4:7]\n\t" \
  "ds_read_b64_tr_b16 v[210:211], v249 offset:7760\n\t" \
  "ds_read_b32 v231, v231 offset:10016\n\t" \
  "v_fma_mix_f32 v223, |v177|, v223, v223 op_sel:[0,1,0] op_sel_hi:[0,1,1] clamp\n\t" \
  "v_fma_mix_f32 v222, |v176|, v222, v222 op_sel:[0,1,0] op_sel_hi:[0,1,1] clamp\n\t" \
  "s_waitcnt lgkmcnt(5)\n\t" \
  "v_or_b32 v187, v224, v225\n\t" \
  "v_mfma_f32_16x16x32_bf16 v[28:31], v[212:215], v[154:157], v[28:31]\n\t" \
  "ds_read_b32 v230, v230 offset:10016\n\t" \
  "v_or_b32 v186, v226, v227\n\t" \
  "v_fma_f32 v179, |v179|, v221, v179 mul:4\n\t" \
  "v_fma_f32 v178, |v178|, v220, v178 mul:4\n\t" \
  "v_mfma_f32_16x16x32_bf16 v[52:55], v[212:215], v[162:165], v[52:55]\n\t" \
  "ds_read_u16_d16_hi v233, v233 offset:10528\n\t" \
  "v_fma_f32 v177, |v177|, v223, v177 mul:4\n\t" \
  "v_fma_f32 v176, |v176|, v222, v176 mul:4\n\t" \
  "v_cvt_pk_bf16_f32 v179, v178, v179\n\t" \
  "v_mfma_f32_16x16x32_bf16 v[76:79], v[212:215], v[170:173], v[76:79]\n\t" \
  "ds_read_b64_tr_b16 v[212:213], v249 offset:6280\n\t" \
  "v_cvt_pk_bf16_f32 v178, v176, v177\n\t" \
  "v_cvt_pknorm_i16_f32 v220, |v180|, |v181|\n\t" \
  "v_cvt_pknorm_i16_f32 v222, |v182|, |v183|\n\t" \
  "v_mfma_f32_16x16x32_bf16 v[8:11], v[216:219], v[146:149], v[8:11]\n\t" \
  "ds_read_b64_tr_b16 v[214:215], v249 offset:7816\n\t" \
  "v_lshlrev_b32_sdwa v221, 2, v220 dst_sel:DWORD dst_unused:UNUSED_PAD src0_sel:DWORD src1_sel:BYTE_3\n\t" \
  "v_lshlrev_b32_sdwa v220, 2, v220 dst_sel:DWORD dst_unused:UNUSED_PAD src0_sel:DWORD src1_sel:BYTE_1\n\t" \
  "v_lshlrev_b32_sdwa v223, 2, v222 dst_sel:DWORD dst_unused:UNUSED_PAD src0_sel:DWORD src1_sel:BYTE_3\n\t" \
  "v_mfma_f32_16x16x32_bf16 v[32:35], v[216:219], v[154:157], v[32:35]\n\t" \
  "ds_read_u16 v232, v232 offset:10528\n\t" \
  "v_lshlrev_b32_sdwa v222, 2, v222 dst_sel:DWORD dst_unused:UNUSED_PAD src0_sel:DWORD src1_sel:BYTE_1\n\t" \
  "v_lshlrev_b32_sdwa v225, 1, v189 dst_sel:DWORD dst_unused:UNUSED_PAD src0_sel:DWORD src1_sel:WORD_1\n\t" \
  "v_lshlrev_b32_sdwa v224, 1, v188 dst_sel:DWORD dst_unused:UNUSED_PAD src0_sel:DWORD src1_sel:WORD_1\n\t" \
  "v_mfma_f32_16x16x32_bf16 v[56:59], v[216:219], v[162:165], v[56:59]\n\t" \
  "ds_read_u16_d16_hi v235, v235 offset:10528\n\t" \
  "ds_read_u16 v234, v234 offset:10528\n\t" \
  "v_lshlrev_b32_sdwa v227, 1, v191 dst_sel:DWORD dst_unused:UNUSED_PAD src0_sel:DWORD src1_sel:WORD_1\n\t" \
  "v_lshlrev_b32_sdwa v226, 1, v190 dst_sel:DWORD dst_unused:UNUSED_PAD src0_sel:DWORD src1_sel:WORD_1\n\t" \
  "s_waitcnt lgkmcnt(6)\n\t" \
  "v_fma_mix_f32 v229, |v195|, v229, v229 op_sel:[0,1,0] op_sel_hi:[0,1,1] clamp\n\t" \
  "v_mfma_f32_16x16x32_bf16 v[80:83], v[216:219], v[170:173], v[80:83]\n\t" \
  "ds_read_b64_tr_b16 v[216:217], v249 offset:6288\n\t" \
  "ds_read_b32 v221, v221 offset:10016\n\t" \
  "v_fma_mix_f32 v228, |v194|, v228, v228 op_sel:[0,1,0] op_sel_hi:[0,1,1] clamp\n\t" \
  "v_fma_mix_f32 v231, |v193|, v231, v231 op_sel:[0,1,0] op_sel_hi:[0,1,1] clamp\n\t" \
  "v_fma_mix_f32 v230, |v192|, v230, v230 op_sel:[0,1,0] op_sel_hi:[0,1,1] clamp\n\t" \
  "s_waitcnt lgkmcnt(2)\n\t" \
  "v_mfma_f32_16x16x32_bf16 v[12:15], v[208:211], v[146:149], v[12:15]\n\t" \
  "ds_read_b64_tr_b16 v[218:219], v249 offset:7824\n\t" \
  "ds_read_b32 v220, v220 offset:10016\n\t" \
  "v_or_b32 v203, v232, v233\n\t" \
  "v_or_b32 v202, v234, v235\n\t" \
  "v_fma_f32 v195, |v195|, v229, v195 mul:4\n\t" \
  "v_fma_f32 v194, |v194|, v228, v194 mul:4\n\t" \
  "v_mfma_f32_16x16x32_bf16 v[36:39], v[208:211], v[154:157], v[36:39]\n\t" \
  "ds_read_b32 v223, v223 offset:10016\n\t" \
  "ds_read_b32 v222, v222 offset:10016\n\t" \
  "v_fma_f32 v193, |v193|, v231, v193 mul:4\n\t" \
  "v_fma_f32 v192, |v192|, v230, v192 mul:4\n\t" \
  "v_cvt_pk_bf16_f32 v195, v194, v195\n\t" \
  "v_mfma_f32_16x16x32_bf16 v[60:63], v[208:211], v[162:165], v[60:63]\n\t" \
  "ds_read_u16_d16_hi v225, v225 offset:10528\n\t" \
  "v_cvt_pk_bf16_f32 v194, v192, v193\n\t" \
  "v_cvt_pknorm_i16_f32 v228, |v196|, |v197|\n\t" \
  "v_cvt_pknorm_i16_f32 v230, |v198|, |v199|\n\t" \
  "v_lshlrev_b32_sdwa v229, 2, v228 dst_sel:DWORD dst_unused:UNUSED_PAD src0_sel:DWORD src1_sel:BYTE_3\n\t" \
  "v_mfma_f32_16x16x32_bf16 v[84:87], v[208:211], v[170:173], v[84:87]\n\t" \
  "ds_read_u16 v224, v224 offset:10528\n\t" \
  "v_lshlrev_b32_sdwa v228, 2, v228 dst_sel:DWORD dst_unused:UNUSED_PAD src0_sel:DWORD src1_sel:BYTE_1\n\t" \
  "v_lshlrev_b32_sdwa v231, 2, v230 dst_sel:DWORD dst_unused:UNUSED_PAD src0_sel:DWORD src1_sel:BYTE_3\n\t" \
  "v_lshlrev_b32_sdwa v230, 2, v230 dst_sel:DWORD dst_unused:UNUSED_PAD src0_sel:DWORD src1_sel:BYTE_1\n\t" \
  "v_mfma_f32_16x16x32_bf16 v[16:19], v[212:215], v[146:149], v[16:19]\n\t" \
  "ds_read_u16_d16_hi v227, v227 offset:10528\n\t" \
  "v_lshlrev_b32_sdwa v233, 1, v205 dst_sel:DWORD dst_unused:UNUSED_PAD src0_sel:DWORD src1_sel:WORD_1\n\t" \
  "v_lshlrev_b32_sdwa v232, 1, v204 dst_sel:DWORD dst_unused:UNUSED_PAD src0_sel:DWORD src1_sel:WORD_1\n\t" \
  "v_lshlrev_b32_sdwa v235, 1, v207 dst_sel:DWORD dst_unused:UNUSED_PAD src0_sel:DWORD src1_sel:WORD_1\n\t" \
  "v_lshlrev_b32_sdwa v234, 1, v206 dst_sel:DWORD dst_unused:UNUSED_PAD src0_sel:DWORD src1_sel:WORD_1\n\t" \
  "s_waitcnt lgkmcnt(3)\n\t" \
  "v_mfma_f32_16x16x32_bf16 v[40:43], v[212:215], v[154:157], v[40:43]\n\t" \
  "ds_read_b64_tr_b16 v[208:209], v249 offset:12304\n\t" \
  "ds_read_u16 v226, v226 offset:10528\n\t" \
  "v_fma_mix_f32 v221, |v181|, v221, v221 op_sel:[0,1,0] op_sel_hi:[0,1,1] clamp\n\t" \
  "v_fma_mix_f32 v220, |v180|, v220, v220 op_sel:[0,1,0] op_sel_hi:[0,1,1] clamp\n\t" \
  "v_fma_mix_f32 v223, |v183|, v223, v223 op_sel:[0,1,0] op_sel_hi:[0,1,1] clamp\n\t" \
  "v_mfma_f32_16x16x32_bf16 v[64:67], v[212:215], v[162:165], v[64:67]\n\t" \
  "ds_read_b64_tr_b16 v[210:211], v249 offset:13840\n\t" \
  "ds_read_b32 v229, v229 offset:10016\n\t" \
  "v_fma_mix_f32 v222, |v182|, v222, v222 op_sel:[0,1,0] op_sel_hi:[0,1,1] clamp\n\t" \
  "s_waitcnt lgkmcnt(2)\n\t" \
  "v_or_b32 v188, v224, v225\n\t" \
  "v_or_b32 v189, v226, v227\n\t" \
  "v_fma_f32 v181, |v181|, v221, v181 mul:4\n\t" \
  "v_mfma_f32_16x16x32_bf16 v[88:91], v[212:215], v[170:173], v[88:91]\n\t" \
  "ds_read_b64_tr_b16 v[212:213], v249 offset:12312\n\t" \
  "ds_read_b32 v228, v228 offset:10016\n\t" \
  "v_fma_f32 v180, |v180|, v220, v180 mul:4\n\t" \
  "v_fma_f32 v183, |v183|, v223, v183 mul:4\n\t" \
  "v_fma_f32 v182, |v182|, v222, v182 mul:4\n\t" \
  "v_mfma_f32_16x16x32_bf16 v[20:23], v[216:219], v[146:149], v[20:23]\n\t" \
  "ds_read_b64_tr_b16 v[214:215], v249 offset:13848\n\t" \
  "ds_read_b32 v231, v231 offset:10016\n\t" \
  "v_cvt_pk_bf16_f32 v180, v180, v181\n\t" \
  "v_cvt_pk_bf16_f32 v181, v182, v183\n\t" \
  "ds_read_b32 v230, v230 offset:10016\n\t" \
  "s_waitcnt lgkmcnt(0)\n\t" \
  "v_fma_mix_f32 v229, |v197|, v229, v229 op_sel:[0,1,0] op_sel_hi:[0,1,1] clamp\n\t" \
  "v_fma_mix_f32 v228, |v196|, v228, v228 op_sel:[0,1,0] op_sel_hi:[0,1,1] clamp\n\t" \
  "v_mfma_f32_16x16x32_bf16 v[44:47], v[216:219], v[154:157], v[44:47]\n\t" \
  "ds_read_u16_d16_hi v233, v233 offset:10528\n\t" \
  "ds_read_u16 v232, v232 offset:10528\n\t" \
  "ds_read_u16_d16_hi v235, v235 offset:10528\n\t" \
  "ds_read_u16 v234, v234 offset:10528\n\t" \
  "v_fma_mix_f32 v231, |v199|, v231, v231 op_sel:[0,1,0] op_sel_hi:[0,1,1] clamp\n\t" \
  "v_fma_mix_f32 v230, |v198|, v230, v230 op_sel:[0,1,0] op_sel_hi:[0,1,1] clamp\n\t" \
  "s_waitcnt lgkmcnt(0)\n\t" \
  "v_or_b32 v204, v232, v233\n\t" \
  "v_mfma_f32_16x16x32_bf16 v[68:71], v[216:219], v[162:165], v[68:71]\n\t" \
  "v_or_b32 v205, v234, v235\n\t" \
  "v_fma_f32 v197, |v197|, v229, v197 mul:4\n\t" \
  "v_fma_f32 v196, |v196|, v228, v196 mul:4\n\t" \
  "v_fma_f32 v199, |v199|, v231, v199 mul:4\n\t" \
  "v_mfma_f32_16x16x32_bf16 v[92:95], v[216:219], v[170:173], v[92:95]\n\t" \
  "ds_read_b64_tr_b16 v[216:217], v249 offset:12368\n\t" \
  "v_fma_f32 v198, |v198|, v230, v198 mul:4\n\t" \
  "v_cvt_pk_bf16_f32 v196, v196, v197\n\t" \
  "v_cvt_pk_bf16_f32 v197, v198, v199\n\t" \
  "v_mfma_f32_16x16x32_bf16 v[0:3], v[208:211], v[178:181], v[0:3]\n\t" \
  "ds_read_b64_tr_b16 v[218:219], v249 offset:13904\n\t" \
  "v_mfma_f32_16x16x32_bf16 v[24:27], v[208:211], v[186:189], v[24:27]\n\t" \
  "v_mfma_f32_16x16x32_bf16 v[48:51], v[208:211], v[194:197], v[48:51]\n\t" \
  "v_mfma_f32_16x16x32_bf16 v[72:75], v[208:211], v[202:205], v[72:75]\n\t" \
  "ds_read_b64_tr_b16 v[208:209], v249 offset:12376\n\t" \
  "v_mfma_f32_16x16x32_bf16 v[4:7], v[212:215], v[178:181], v[4:7]\n\t" \
  "ds_read_b64_tr_b16 v[210:211], v249 offset:13912\n\t" \
  "v_mfma_f32_16x16x32_bf16 v[28:31], v[212:215], v[186:189], v[28:31]\n\t" \
  "v_mfma_f32_16x16x32_bf16 v[52:55], v[212:215], v[194:197], v[52:55]\n\t" \
  "v_mfma_f32_16x16x32_bf16 v[76:79], v[212:215], v[202:205], v[76:79]\n\t" \
  "ds_read_b64_tr_b16 v[212:213], v249 offset:12432\n\t" \
  "s_waitcnt lgkmcnt(3)\n\t" \
  "v_mfma_f32_16x16x32_bf16 v[8:11], v[216:219], v[178:181], v[8:11]\n\t" \
  "ds_read_b64_tr_b16 v[214:215], v249 offset:13968\n\t" \
  "v_mfma_f32_16x16x32_bf16 v[32:35], v[216:219], v[186:189], v[32:35]\n\t" \
  "v_mfma_f32_16x16x32_bf16 v[56:59], v[216:219], v[194:197], v[56:59]\n\t" \
  "v_mfma_f32_16x16x32_bf16 v[80:83], v[216:219], v[202:205], v[80:83]\n\t" \
  "ds_read_b64_tr_b16 v[216:217], v249 offset:12440\n\t" \
  "v_cvt_pk_bf16_f32 v0, v0, v1\n\t" \
  "v_cvt_pk_bf16_f32 v1, v2, v3\n\t" \
  "v_cvt_pk_bf16_f32 v2, v4, v5\n\t" \
  "v_cvt_pk_bf16_f32 v3, v6, v7\n\t" \
  "buffer_store_dwordx4 v[0:3], v250, s[20:23], s63 offen offset:0 sc1\n\t" \
  "v_cvt_pk_bf16_f32 v24, v24, v25\n\t" \
  "v_cvt_pk_bf16_f32 v25, v26, v27\n\t" \
  "v_cvt_pk_bf16_f32 v26, v28, v29\n\t" \
  "v_cvt_pk_bf16_f32 v27, v30, v31\n\t" \
  "buffer_store_dwordx4 v[24:27], v250, s[20:23], s62 offen offset:0 sc1\n\t" \
  "v_cvt_pk_bf16_f32 v48, v48, v49\n\t" \
  "v_cvt_pk_bf16_f32 v49, v50, v51\n\t" \
  "v_cvt_pk_bf16_f32 v50, v52, v53\n\t" \
  "v_cvt_pk_bf16_f32 v51, v54, v55\n\t" \
  "buffer_store_dwordx4 v[48:51], v250, s[20:23], s14 offen offset:0 sc1\n\t" \
  "v_cvt_pk_bf16_f32 v72, v72, v73\n\t" \
  "v_cvt_pk_bf16_f32 v73, v74, v75\n\t" \
  "v_cvt_pk_bf16_f32 v74, v76, v77\n\t" \
  "v_cvt_pk_bf16_f32 v75, v78, v79\n\t" \
  "buffer_store_dwordx4 v[72:75], v250, s[20:23], s15 offen offset:0 sc1\n\t" \
  "s_waitcnt lgkmcnt(3)\n\t" \
  "v_mfma_f32_16x16x32_bf16 v[12:15], v[208:211], v[178:181], v[12:15]\n\t" \
  "ds_read_b64_tr_b16 v[218:219], v249 offset:13976\n\t" \
  "v_mfma_f32_16x16x32_bf16 v[36:39], v[208:211], v[186:189], v[36:39]\n\t" \
  "v_mfma_f32_16x16x32_bf16 v[60:63], v[208:211], v[194:197], v[60:63]\n\t" \
  "v_mfma_f32_16x16x32_bf16 v[84:87], v[208:211], v[202:205], v[84:87]\n\t" \
  "s_waitcnt lgkmcnt(2)\n\t" \
  "v_mfma_f32_16x16x32_bf16 v[16:19], v[212:215], v[178:181], v[16:19]\n\t" \
  "v_mfma_f32_16x16x32_bf16 v[40:43], v[212:215], v[186:189], v[40:43]\n\t" \
  "v_mfma_f32_16x16x32_bf16 v[64:67], v[212:215], v[194:197], v[64:67]\n\t" \
  "v_mfma_f32_16x16x32_bf16 v[88:91], v[212:215], v[202:205], v[88:91]\n\t" \
  "v_cvt_pk_bf16_f32 v8, v8, v9\n\t" \
  "v_cvt_pk_bf16_f32 v9, v10, v11\n\t" \
  "v_cvt_pk_bf16_f32 v10, v12, v13\n\t" \
  "v_cvt_pk_bf16_f32 v11, v14, v15\n\t" \
  "buffer_store_dwordx4 v[8:11], v250, s[20:23], s63 offen offset:64 sc1\n\t" \
  "v_cvt_pk_bf16_f32 v32, v32, v33\n\t" \
  "v_cvt_pk_bf16_f32 v33, v34, v35\n\t" \
  "v_cvt_pk_bf16_f32 v34, v36, v37\n\t" \
  "v_cvt_pk_bf16_f32 v35, v38, v39\n\t" \
  "buffer_store_dwordx4 v[32:35], v250, s[20:23], s62 offen offset:64 sc1\n\t" \
  "v_cvt_pk_bf16_f32 v56, v56, v57\n\t" \
  "v_cvt_pk_bf16_f32 v57, v58, v59\n\t" \
  "v_cvt_pk_bf16_f32 v58, v60, v61\n\t" \
  "v_cvt_pk_bf16_f32 v59, v62, v63\n\t" \
  "buffer_store_dwordx4 v[56:59], v250, s[20:23], s14 offen offset:64 sc1\n\t" \
  "v_cvt_pk_bf16_f32 v80, v80, v81\n\t" \
  "v_cvt_pk_bf16_f32 v81, v82, v83\n\t" \
  "v_cvt_pk_bf16_f32 v82, v84, v85\n\t" \
  "v_cvt_pk_bf16_f32 v83, v86, v87\n\t" \
  "buffer_store_dwordx4 v[80:83], v250, s[20:23], s15 offen offset:64 sc1\n\t" \
  "s_waitcnt lgkmcnt(0)\n\t" \
  "v_mfma_f32_16x16x32_bf16 v[20:23], v[216:219], v[178:181], v[20:23]\n\t" \
  "v_mfma_f32_16x16x32_bf16 v[44:47], v[216:219], v[186:189], v[44:47]\n\t" \
  "v_mfma_f32_16x16x32_bf16 v[68:71], v[216:219], v[194:197], v[68:71]\n\t" \
  "v_mfma_f32_16x16x32_bf16 v[92:95], v[216:219], v[202:205], v[92:95]\n\t" \
  "v_cvt_pk_bf16_f32 v16, v16, v17\n\t" \
  "v_cvt_pk_bf16_f32 v17, v18, v19\n\t" \
  "s_nop 2\n\t" \
  "v_cvt_pk_bf16_f32 v18, v20, v21\n\t" \
  "v_cvt_pk_bf16_f32 v19, v22, v23\n\t" \
  "buffer_store_dwordx4 v[16:19], v250, s[20:23], s63 offen offset:128 sc1\n\t" \
  "v_cvt_pk_bf16_f32 v40, v40, v41\n\t" \
  "v_cvt_pk_bf16_f32 v41, v42, v43\n\t" \
  "v_cvt_pk_bf16_f32 v42, v44, v45\n\t" \
  "v_cvt_pk_bf16_f32 v43, v46, v47\n\t" \
  "buffer_store_dwordx4 v[40:43], v250, s[20:23], s62 offen offset:128 sc1\n\t" \
  "v_cvt_pk_bf16_f32 v64, v64, v65\n\t" \
  "v_cvt_pk_bf16_f32 v65, v66, v67\n\t" \
  "v_cvt_pk_bf16_f32 v66, v68, v69\n\t" \
  "v_cvt_pk_bf16_f32 v67, v70, v71\n\t" \
  "buffer_store_dwordx4 v[64:67], v250, s[20:23], s14 offen offset:128 sc1\n\t" \
  "v_cvt_pk_bf16_f32 v88, v88, v89\n\t" \
  "v_cvt_pk_bf16_f32 v89, v90, v91\n\t" \
  "v_cvt_pk_bf16_f32 v90, v92, v93\n\t" \
  "v_cvt_pk_bf16_f32 v91, v94, v95\n\t" \
  "buffer_store_dwordx4 v[88:91], v250, s[20:23], s15 offen offset:128 sc1\n\t" \
  "s_waitcnt vmcnt(0)\n\t" \
  ".Lcnx_end:\n\t" \
  ""


__device__ __forceinline__ unsigned lds_addr(const char* p) { return (unsigned)(uintptr_t)(lds_ptr_t)p; }
__device__ __forceinline__ unsigned sg(unsigned v) { return __builtin_amdgcn_readfirstlane(v); }

constexpr double tanh_u(double y) {
    const double v = 8 * y, a = 2 * 0.79788456080286536 * (v + 0.044715 * v * v * v) / 1024;
    double m = 1;
    for (int i = 12; i > 1; --i) m = 1 + m * a / i;
    m *= a;
    for (int i = 0; i < 10; ++i) m *= m + 2;
    return m / (m + 2);
}
constexpr double h2d(int h) {
    if (h < 1024) return 0;
    double x = (1024 + (h & 1023)) * 0x1p-24;
    for (int e = 1; e < (h >> 10); ++e) x *= 2;
    return x;
}
constexpr int d2h(double x) {
    int h = 0;
    for (int s = 1 << 14; s; s >>= 1) if (h2d(h + s) <= x) h += s;
    return h;
}
constexpr unsigned ip_entry(int k) {
    if (k == 127) return 0x3c003c00u;
    const double y0 = k ? (256 * k - 0.5) / 32767 : 0, dy = ((256 * k + 255.5) / 32767 - y0) / 8;
    double t[9] = {};
    for (int i = 0; i < 9; ++i) t[i] = tanh_u(y0 + i * dy);
    const double b0 = (t[8] - t[0]) / (8 * dy), ym = y0 + 4 * dy, tm = (t[4] + (t[0] + t[8]) / 2) / 2;
    unsigned best = 0;
    double err = 1;
    for (int ha = d2h(tm - b0 * ym) - 1, i = 0; i < 4; ++i, ++ha)
        for (int hb = d2h((tm - h2d(ha)) / ym), j = 0; j < 2; ++j, ++hb) {
            double e = 0;
            for (int n = 0; n < 9; ++n) {
                const double s = h2d(ha) + h2d(hb) * (y0 + n * dy), d = (s < 1 ? s : 1) - t[n];
                e = d > e ? d : -d > e ? -d : e;
            }
            if (e < err) err = e, best = (ha < 1024 ? 0 : ha) | (hb < 1024 ? 0 : hb) << 16;
        }
    return best;
}
constexpr unsigned g_entry(int e, bool neg) {
    double y = e ? ((e & 127) + 0.5 + (e >> 7 ? 128 : 0)) * 0x1p-23 / (1 + 1.0 / 384) : 0;
    for (int i = 1; i < (e >> 7); ++i) y *= 2;
    const double t = y < 1 ? tanh_u(y) : 1;
    const unsigned u = __builtin_bit_cast(unsigned, (float)(4 * y * (neg ? t - 1 : t + 1)));
    return (u + 0x7fff + (u >> 16 & 1)) >> 16;
}
struct TanhTab { unsigned v[1024]; };
constexpr TanhTab gelu_tab(int k0) {
    TanhTab t = {};
    for (int k = k0; k < k0 + 1024 && k < 128 + GSZ / 2; ++k) {
        const int e = 2 * (k < 2048 ? k - 128 : k - 2048);
        t.v[k - k0] = k < 128 ? ip_entry(k) : g_entry(e, k >= 2048) | g_entry(e + 1, k >= 2048) << 16;
    }
    return t;
}
struct WaveTab { unsigned v192[64][4], v244[64][4], v16[64][4], s32[32]; };
struct RegTab { unsigned v240[64][4], v248[64][4]; WaveTab w[8]; };
constexpr unsigned cpos(unsigned w, unsigned px) { return (w >> 1) * 1024 + (px ^ (w & 2) << 2) * 16 + ((w ^ px >> 4) & 1) * 8; }
constexpr RegTab reg_tab() {
    RegTab t = {};
    for (unsigned l = 0; l < 64; ++l) {
        const unsigned G = l >> 4, n = l & 15, q = n >> 2, p = l & 3;
        t.v240[l][2 + (p >> 1)] = p & 1 ? 0x3f800000u : 0x3f80u;
        t.v248[l][0] = WB + (l >> 2) * 32 + (p & 1) * 16 + (p >> 1) * 8;
        t.v248[l][1] = WB + (G & 1) * 3080 + ((G >> 1) * 4 + q) * 192 + p * 16;
        t.v248[l][2] = (n & 7) * 384 + (n >> 3) * 192 + G * 16;
        t.v248[l][3] = PB + l * 8;
        for (unsigned w = 0; w < 8; ++w) {
            t.w[w].v244[l][2] = XOFF + n * 400 + (G & 1) * XROW + (G >> 1) * 192 + w * 24;
            t.w[w].v244[l][3] = dw_pos(3 * w) + p * 192 + (1 - (q >> 1)) * 24 + (1 + (G & 1) - (q & 1)) * 8 + ((G ^ p) >> 1) * 4;
            const unsigned pl = PTOP + (w >> 2) * 12288, blk = (w & 3) * 16;
            t.w[w].v192[l][0] = XOFF + cpos(w, (G & 2) * 16 + (n & 8) * 2 + (G & 1) * 8 + (n & 7));
            t.w[w].v192[l][1] = t.w[w].v192[l][0] + PTOP - XOFF;
            t.w[w].v192[l][2] = pl + (cpos((G & 1) * 4 + (G & 2), blk + n) & ~8u);
            t.w[w].v192[l][3] = pl + cpos(p, blk + (l >> 2));
            const unsigned u = (w & 3) * 48 + l, j = u & 63;
            t.w[w].v16[l][0] = w < 4 ? (4 * (u >> 6) + 12 * ((j >> 3) & 3) + ((j >> 1) & 3)) * (HID * 2) + (2 * (j & 1) + (j >> 5)) * 16 : u * 16;
            const unsigned d = dw_pos(w * 8 + (l >> 5) * 4 + p) + ((l >> 2) & 7) * 24;
            t.w[w].v16[l][1] = w < 3 ? d + (l & 1) * 192 : (l & 4 ? DW2 + 11 * 768 : 13 * 768) + p * 8;
            t.w[w].v16[l][2] = d + (~l & 1) * 192;
            t.w[w].v16[l][3] = l & 1 ? 0x2b800000u : 0x2b80u;
        }
    }
    for (unsigned w = 0; w < 8; ++w) {
        const bool w1 = w < 4;
        const unsigned dm = w1 ? 3072 : 3080, wl = WB + (w & 3) * 768, wc = w1 ? 64 : 6144;
        const unsigned rp[4] = {x_pos(3 + (w >> 1 ^ 2)), x_pos(7 + (w >> 1)), x_pos((11 + (w >> 1)) % 12), x_pos(3 + (w >> 1))};
        const unsigned s[32] = {wc, 0, w1 ? 48u * HID * 2 : 3072, dm, dm + XOFF - WB, w >> 2, w, w * 512,
                                0xaaaaaaaau, 0xaaaaaaaau, 0xccccccccu, 0xccccccccu,
                                wl + (w1 ? WA + 12304 : WA), wl + (w1 ? 0 : WA + 6152), wl + (w1 ? WA + 18448 : 6152),
                                wl + (w1 ? WA + 18448 + 6144 : 12304), w1 ? -11 * wc : wc, w1 ? wc : -11 * wc,
                                0u + XOFF - WB, 4 * wc, ~0x00f0000fu, ~0xf0000f00u,
                                (w >> 2 ? PTOP - 12288u : XHI + 6 * XROW) - XOFF - 4 * 12288, 0x07060302, dw_pos(3 * w + 2) - dw_pos(3 * w), 0,
                                rp[1] - rp[0], rp[2] - rp[1], rp[3] - rp[2], rp[0] + 592 + (w & 1) * 3200, 0, 8 * (unsigned)sizeof(WaveTab) - 2 * PB};
        for (int i = 0; i < 32; ++i) t.w[w].s32[i] = s[i];
    }
    return t;
}
struct Tables { RegTab r; TanhTab t[4]; };
static_assert(sizeof(WaveTab) == 64 * 50 && sizeof(RegTab) == 27648, "table layout");
__device__ const __attribute__((aligned(128))) Tables TABLES = {reg_tab(), {gelu_tab(0), gelu_tab(1024), gelu_tab(2048), gelu_tab(3072)}};

#define R10(r, a) r #a "0", r #a "1", r #a "2", r #a "3", r #a "4", r #a "5", r #a "6", r #a "7", r #a "8", r #a "9"
#define V10(a) R10("v", a)
#define S10(a) R10("s", a)
#define MAIN_CLOB "memory", "scc", "vcc", "v1", "v2", "v3", "v4", "v5", "v6", "v7", "v8", "v9", V10(1), V10(2), V10(3), V10(4), V10(5),  \
    V10(6), V10(7), V10(8), V10(9), V10(10), V10(11), V10(12), V10(13), V10(14), V10(15), V10(16), V10(17), V10(18), V10(19),        \
    V10(20), V10(21), V10(22), V10(23), V10(24), "v250", "v251", "v252", "v253", "v254", "s4", "s5", "s8", "s9", S10(1),       \
    S10(2), S10(3), S10(4), S10(5), "s60", "s61", "s62", "s63", "s64", "s65", "s66", "s67", "s68", "s69"

__global__ __launch_bounds__(512, 2) void convnext_kernel(const __hip_bfloat16* __restrict__ x,
                                                          const __hip_bfloat16* __restrict__ dw,
                                                          const __hip_bfloat16* __restrict__ w1,
                                                          const __hip_bfloat16* __restrict__ w2,
                                                          __hip_bfloat16* __restrict__ out) {
    __shared__ __attribute__((aligned(16))) char smem[LDS_BYTES];

    unsigned tid = threadIdx.x, lds = lds_addr(smem);
    const unsigned bid = blockIdx.x;
    asm volatile(MAIN_ASM
                 : "+{v0}"(tid), "+{s3}"(lds)
                 : "{s[0:1]}"(__builtin_amdgcn_kernarg_segment_ptr()), "{s2}"(bid), "{s[6:7]}"(TABLES.r.w)
                 : MAIN_CLOB);

}

}

extern "C" void kernel_launch(void* const* d_in, const int* in_sizes, int n_in, void* d_out, int out_size,
                              void* d_ws, size_t ws_size, hipStream_t stream) {
    (void)in_sizes; (void)n_in; (void)out_size; (void)d_ws; (void)ws_size;
    const __hip_bfloat16* x = (const __hip_bfloat16*)d_in[0];
    const __hip_bfloat16* dw = (const __hip_bfloat16*)d_in[1];
    const __hip_bfloat16* w1 = (const __hip_bfloat16*)d_in[2];
    const __hip_bfloat16* w2 = (const __hip_bfloat16*)d_in[3];
    __hip_bfloat16* out = (__hip_bfloat16*)d_out;
    convnext_kernel<<<256, 512, 0, stream>>>(x, dw, w1, w2, out);
}
```

```cpp
#include <hip/hip_runtime.h>
#include <hip/hip_bf16.h>
#include <stdint.h>

namespace {

typedef __attribute__((address_space(3))) void* lds_ptr_t;

constexpr int IMG = 512;
constexpr int CH = 96;
constexpr int HID = 384;
constexpr int ROWB = IMG * CH * 2;
constexpr int IPOFF = 13 * 768 + 32;
constexpr int GOFF = IPOFF + 512;
constexpr int GSZ = 30 * 256;
constexpr int DW2 = GOFF + GSZ;
constexpr int XROW = 8200;
constexpr int XOFF = GOFF + 65536 - 6 * XROW;
constexpr int XHI = XOFF + GSZ;
constexpr int PB = XHI + 10 * XROW;
constexpr int WB = XHI + 9 * XROW;
constexpr int WA = 3 * XROW;
constexpr int PTOP = XHI + 12 * XROW - 12288;
constexpr int CNTOFF = WB + WA + 18448 + 12288;
constexpr int LDS_BYTES = 160 * 1024;
static_assert(XOFF == 26864 && CNTOFF - PB == 47136 && DW2 + 11 * 768 + 32 <= XOFF && CNTOFF + 144 <= LDS_BYTES, "LDS map");
constexpr unsigned dw_pos(unsigned u) { return u < 12 ? u * 768 : u == 14 ? 12 * 768 : DW2 + (u - 12 - (u > 14)) * 768; }
constexpr unsigned x_pos(unsigned r) { return (r < 6 ? XOFF : XHI) + r * XROW; }
constexpr unsigned OOB = 0x40000000u;

#define MAIN_ASM \
  "s_load_dwordx2 s[8:9], s[0:1], 0x0\n\t" \
  "s_load_dwordx2 s[12:13], s[0:1], 0x8\n\t" \
  "s_load_dwordx2 s[20:21], s[0:1], 0x20\n\t" \
  "s_load_dwordx4 s[32:35], s[0:1], 0x10\n\t" \
  "v_readfirstlane_b32 s5, v0\n\t" \
  "v_and_b32 v21, 63, v0\n\t" \
  "s_mov_b32 m0, 107744\n\t" \
  "s_cmp_lt_u32 s5, 256\n\t" \
  "s_cselect_b32 s3, 16, 24\n\t" \
  "v_lshlrev_b32 v22, 4, v21\n\t" \
  "s_load_dwordx2 s[16:17], s[0:1], s3\n\t" \
  "s_mul_i32 s4, s5, 50\n\t" \
  "s_add_u32 s66, s6, s4\n\t" \
  "s_addc_u32 s67, s7, 0\n\t" \
  "global_load_dwordx4 v[244:247], v22, s[66:67] offset:1024\n\t" \
  "global_load_dwordx4 v[16:19], v22, s[66:67] offset:2048\n\t" \
  "global_load_dwordx4 v[240:243], v22, s[6:7] offset:-2048\n\t" \
  "global_load_dwordx4 v[248:251], v22, s[6:7] offset:-1024\n\t" \
  "global_load_dwordx4 v[192:195], v22, s[66:67]\n\t" \
  "s_cmp_lg_u32 s5, 192\n\t" \
  "s_cbranch_scc1 .Lcnx_nc\n\t" \
  "s_getpc_b64 s[14:15]\n\t" \
  ".Lcnx_pc:\n\t" \
  "s_lshr_b32 s3, s2, 3\n\t" \
  "s_lshl_b32 s3, s3, 11\n\t" \
  "v_lshl_add_u32 v1, v21, 5, s3\n\t" \
  "v_min_u32 v1, .Lcnx_end-.Lcnx_pc, v1\n\t" \
  "global_load_lds_dword v1, s[14:15]\n\t" \
  ".Lcnx_nc:\n\t" \
  "v_mul_u32_u24 v254, 0x147b, v0\n\t" \
  "s_bfe_u32 s27, s2, 0x40003\n\t" \
  "v_bfe_u32 v1, v0, 2, 3\n\t" \
  "s_mul_i32 s27, s27, 6144\n\t" \
  "v_lshrrev_b32 v254, 17, v254\n\t" \
  "s_sub_u32 s3, s27, 576\n\t" \
  "v_and_b32 v20, 3, v0\n\t" \
  "s_add_u32 s4, s27, 6720\n\t" \
  "v_sub_u32 v254, v0, v254\n\t" \
  "s_min_u32 s4, s4, 0x18000\n\t" \
  "v_and_b32 v2, 0x1e0, v0\n\t" \
  "s_and_b32 s24, s2, 7\n\t" \
  "v_lshl_add_u32 v254, v254, 4, s3\n\t" \
  "s_lshl_b32 s24, s24, 6\n\t" \
  "v_lshl_or_b32 v20, v20, 3, v2\n\t" \
  "s_mov_b32 s26, 0x18000\n\t" \
  "v_cmp_gt_u32 vcc, s4, v254\n\t" \
  "s_cmp_ge_u32 s2, 128\n\t" \
  "s_cselect_b32 s3, 7, 0\n\t" \
  "s_cselect_b32 s4, 0, 7\n\t" \
  "v_add_u32 v1, s3, v1\n\t" \
  "s_cselect_b32 s3, 28, 35\n\t" \
  "v_cndmask_b32 v254, 2.0, v254, vcc\n\t" \
  "s_cselect_b32 s26, s26, 0xfffe8000\n\t" \
  "v_xor_b32 v1, s4, v1\n\t" \
  "s_add_u32 s24, s24, s3\n\t" \
  "v_and_b32 v1, 7, v1\n\t" \
  "s_movk_i32 s3, 1344\n\t" \
  "s_mul_i32 s24, s24, 0x18000\n\t" \
  "v_mad_u32_u24 v1, v1, s3, v20\n\t" \
  "s_lshl4_add_u32 s25, s5, 26864\n\t" \
  "s_mov_b32 s10, 0x3000000\n\t" \
  "s_mov_b32 s11, 0x20000\n\t" \
  "s_mov_b32 s14, 9408\n\t" \
  "s_mov_b32 s15, 0x20000\n\t" \
  "s_mov_b32 s18, 73728\n\t" \
  "s_mov_b32 s19, 0x20000\n\t" \
  "s_mov_b32 s22, 0x3000000\n\t" \
  "s_mov_b32 s23, 0x20000\n\t" \
  "s_add_u32 s3, s24, s26\n\t" \
  "s_add_u32 s3, s3, s27\n\t" \
  "s_mul_i32 s4, s26, 10\n\t" \
  "v_mov_b32 v24, s3\n\t" \
  "v_mad_i32_i24 v27, v21, s4, v24\n\t" \
  "s_setreg_imm32_b32 hwreg(HW_REG_MODE, 4, 2), 0\n\t" \
  "s_setreg_imm32_b32 hwreg(HW_REG_MODE, 9, 1), 0\n\t" \
  "s_lshl_b32 s65, s26, 4\n\t" \
  "s_lshr_b32 s3, s5, 7\n\t" \
  "s_add_u32 s3, s3, 8\n\t" \
  "s_mul_i32 s3, s3, s26\n\t" \
  "s_add_u32 s29, s24, s27\n\t" \
  "s_add_u32 s29, s29, s3\n\t" \
  "s_and_b32 s3, s5, 64\n\t" \
  "s_mul_i32 s3, s3, 48\n\t" \
  "s_add_u32 s29, s29, s3\n\t" \
  "s_mul_i32 s3, s26, 9\n\t" \
  "s_add_u32 s27, s24, s65\n\t" \
  "s_mov_b32 s30, 0x01010101\n\t" \
  "s_mov_b32 s31, 0x01010101\n\t" \
  "s_add_u32 s27, s27, s3\n\t" \
  "s_mul_i32 s64, s26, 22\n\t" \
  "s_sub_u32 s64, s64, s65\n\t" \
  "s_cmp_ge_u32 s5, 256\n\t" \
  "s_cselect_b32 s3, -6, -2\n\t" \
  "s_mul_i32 s3, s3, s26\n\t" \
  "s_add_u32 s28, s29, s3\n\t" \
  "s_mov_b64 vcc, 0\n\t" \
  "s_waitcnt lgkmcnt(0)\n\t" \
  "v_add_co_u32 v24, vcc, s8, v27\n\t" \
  "v_mov_b32 v25, s9\n\t" \
  "v_addc_co_u32 v25, vcc, 0, v25, vcc\n\t" \
  "v_writelane_b32 v24, s12, 3\n\t" \
  "v_writelane_b32 v25, s13, 3\n\t" \
  "v_writelane_b32 v24, s32, 4\n\t" \
  "v_writelane_b32 v25, s33, 4\n\t" \
  "v_writelane_b32 v24, s34, 5\n\t" \
  "v_writelane_b32 v25, s35, 5\n\t" \
  "s_mov_b64 exec, 63\n\t" \
  "global_load_dword v26, v[24:25], off\n\t" \
  "s_mov_b64 exec, -1\n\t" \
  "s_mov_b64 vcc, 0\n\t" \
  "s_and_b32 s13, s13, 0xffff\n\t" \
  "s_and_b32 s9, s9, 0xffff\n\t" \
  "s_and_b32 s17, s17, 0xffff\n\t" \
  "s_cmp_ge_u32 s5, 192\n\t" \
  "s_cbranch_scc1 .Lcnx_nd\n\t" \
  "buffer_load_dwordx2 v[2:3], v1, s[12:15], 0 offen offset:0\n\t" \
  "buffer_load_dwordx2 v[4:5], v1, s[12:15], 0 offen offset:192\n\t" \
  "buffer_load_dwordx2 v[6:7], v1, s[12:15], 0 offen offset:384\n\t" \
  "buffer_load_dwordx2 v[8:9], v1, s[12:15], 0 offen offset:576\n\t" \
  "buffer_load_dwordx2 v[10:11], v1, s[12:15], 0 offen offset:768\n\t" \
  "buffer_load_dwordx2 v[12:13], v1, s[12:15], 0 offen offset:960\n\t" \
  "buffer_load_dwordx2 v[14:15], v1, s[12:15], 0 offen offset:1152\n\t" \
  ".Lcnx_nd:\n\t" \
  "s_and_b32 s3, s5, 0xc0\n\t" \
  "s_cmp_lg_u32 s3, 64\n\t" \
  "s_cbranch_scc1 .Lcnx_nw\n\t" \
  "s_lshr_b32 s3, s2, 3\n\t" \
  "s_mul_i32 s3, s3, 2304\n\t" \
  "s_mov_b32 m0, 107744\n\t" \
  "v_mul_u32_u24 v21, 36, v21\n\t" \
  "buffer_load_dword v21, s[16:19], s3 offen lds\n\t" \
  ".Lcnx_nw:\n\t" \
  "s_add_u32 m0, s25, 0\n\t" \
  "s_add_u32 s24, s24, s26\n\t" \
  "buffer_load_dwordx4 v254, s[8:11], s24 offen lds\n\t" \
  "s_add_u32 m0, s25, 8200\n\t" \
  "s_add_u32 s24, s24, s26\n\t" \
  "buffer_load_dwordx4 v254, s[8:11], s24 offen lds\n\t" \
  "s_add_u32 m0, s25, 16400\n\t" \
  "s_add_u32 s24, s24, s26\n\t" \
  "buffer_load_dwordx4 v254, s[8:11], s24 offen lds\n\t" \
  "s_add_u32 m0, s25, 24600\n\t" \
  "s_add_u32 s24, s24, s26\n\t" \
  "buffer_load_dwordx4 v254, s[8:11], s24 offen lds\n\t" \
  "s_load_dwordx16 s[32:47], s[66:67], 0xc00\n\t" \
  "s_load_dwordx16 s[48:63], s[66:67], 0xc40\n\t" \
  "s_and_b32 s21, s21, 0xffff\n\t" \
  "s_waitcnt vmcnt(5)\n\t" \
  "v_mfma_f32_32x32x16_bf16 v[144:159], v[242:245], v[240:243], 0\n\t" \
  "v_mfma_f32_32x32x16_bf16 v[160:175], v[242:245], v[240:243], 0\n\t" \
  "v_bfe_u32 v234, v251, 3, 2\n\t" \
  "v_mul_u32_u24 v234, 0x5555, v234\n\t" \
  "v_mov_b32 v252, v16\n\t" \
  "s_lshl2_add_u32 s12, s26, s29\n\t" \
  "s_lshl3_add_u32 s13, s26, s29\n\t" \
  "v_mov_b32 v253, v246\n\t" \
  "s_mov_b32 s68, 0x07060302\n\t" \
  "s_cmp_gt_u32 s5, 192\n\t" \
  "s_cbranch_scc1 .Lcnx_e2\n\t" \
  "s_cmp_eq_u32 s5, 192\n\t" \
  "s_cbranch_scc1 .Lcnx_e1\n\t" \
  "v_xor_b32 v23, 0x2b802b80, v19\n\t" \
  "s_waitcnt vmcnt(4)\n\t" \
  "v_dot2_f32_bf16 v48, v2, v19, 0\n\t" \
  "v_dot2_f32_bf16 v50, v4, v19, 0\n\t" \
  "v_dot2_f32_bf16 v52, v6, v19, 0\n\t" \
  "v_dot2_f32_bf16 v54, v8, v19, 0\n\t" \
  "v_dot2_f32_bf16 v56, v10, v19, 0\n\t" \
  "v_dot2_f32_bf16 v58, v12, v19, 0\n\t" \
  "v_dot2_f32_bf16 v60, v14, v19, 0\n\t" \
  "v_dot2_f32_bf16 v2, v2, v23, 0\n\t" \
  "v_dot2_f32_bf16 v4, v4, v23, 0\n\t" \
  "v_dot2_f32_bf16 v6, v6, v23, 0\n\t" \
  "v_dot2_f32_bf16 v8, v8, v23, 0\n\t" \
  "v_dot2_f32_bf16 v10, v10, v23, 0\n\t" \
  "v_dot2_f32_bf16 v12, v12, v23, 0\n\t" \
  "v_dot2_f32_bf16 v14, v14, v23, 0\n\t" \
  "v_perm_b32 v24, v50, 0, s68\n\t" \
  "v_perm_b32 v25, v58, v54, s68\n\t" \
  "v_perm_b32 v26, v52, v48, s68\n\t" \
  "v_perm_b32 v27, v60, v56, s68\n\t" \
  "v_perm_b32 v28, v54, v50, s68\n\t" \
  "v_perm_b32 v29, 0, v58, s68\n\t" \
  "ds_write_b64 v17, v[24:25] offset:0\n\t" \
  "ds_write_b64 v17, v[26:27] offset:8\n\t" \
  "ds_write_b64 v17, v[28:29] offset:16\n\t" \
  "v_dot2_f32_bf16 v49, v3, v19, 0\n\t" \
  "v_dot2_f32_bf16 v51, v5, v19, 0\n\t" \
  "v_dot2_f32_bf16 v53, v7, v19, 0\n\t" \
  "v_dot2_f32_bf16 v55, v9, v19, 0\n\t" \
  "v_dot2_f32_bf16 v57, v11, v19, 0\n\t" \
  "v_dot2_f32_bf16 v59, v13, v19, 0\n\t" \
  "v_dot2_f32_bf16 v61, v15, v19, 0\n\t" \
  "v_perm_b32 v30, v4, 0, s68\n\t" \
  "v_perm_b32 v31, v12, v8, s68\n\t" \
  "v_perm_b32 v32, v6, v2, s68\n\t" \
  "v_perm_b32 v33, v14, v10, s68\n\t" \
  "v_perm_b32 v34, v8, v4, s68\n\t" \
  "v_perm_b32 v35, 0, v12, s68\n\t" \
  "ds_write_b64 v18, v[30:31] offset:0\n\t" \
  "ds_write_b64 v18, v[32:33] offset:8\n\t" \
  "ds_write_b64 v18, v[34:35] offset:16\n\t" \
  "v_dot2_f32_bf16 v3, v3, v23, 0\n\t" \
  "v_dot2_f32_bf16 v5, v5, v23, 0\n\t" \
  "v_dot2_f32_bf16 v7, v7, v23, 0\n\t" \
  "v_dot2_f32_bf16 v9, v9, v23, 0\n\t" \
  "v_dot2_f32_bf16 v11, v11, v23, 0\n\t" \
  "v_dot2_f32_bf16 v13, v13, v23, 0\n\t" \
  "v_dot2_f32_bf16 v15, v15, v23, 0\n\t" \
  "v_perm_b32 v37, v51, 0, s68\n\t" \
  "v_perm_b32 v36, v59, v55, s68\n\t" \
  "v_perm_b32 v39, v53, v49, s68\n\t" \
  "v_perm_b32 v38, v61, v57, s68\n\t" \
  "v_perm_b32 v41, v55, v51, s68\n\t" \
  "v_perm_b32 v40, 0, v59, s68\n\t" \
  "ds_write_b64 v17, v[36:37] offset:384\n\t" \
  "ds_write_b64 v17, v[38:39] offset:392\n\t" \
  "ds_write_b64 v17, v[40:41] offset:400\n\t" \
  "v_perm_b32 v43, v5, 0, s68\n\t" \
  "v_perm_b32 v42, v13, v9, s68\n\t" \
  "v_perm_b32 v45, v7, v3, s68\n\t" \
  "v_perm_b32 v44, v15, v11, s68\n\t" \
  "v_perm_b32 v47, v9, v5, s68\n\t" \
  "v_perm_b32 v46, 0, v13, s68\n\t" \
  "ds_write_b64 v18, v[42:43] offset:384\n\t" \
  "ds_write_b64 v18, v[44:45] offset:392\n\t" \
  "ds_write_b64 v18, v[46:47] offset:400\n\t" \
  "s_branch .Lcnx_e3\n\t" \
  ".Lcnx_e1:\n\t" \
  "s_mov_b64 exec, 0x3ffff\n\t" \
  "ds_write_b64 v251, v[240:241] offset:47136\n\t" \
  "s_mov_b64 exec, 0xff\n\t" \
  "ds_write_b64 v17, v[240:241]\n\t" \
  "s_mov_b64 exec, -1\n\t" \
  "s_branch .Lcnx_e3\n\t" \
  ".Lcnx_e2:\n\t" \
  "s_setprio 1\n\t" \
  ".Lcnx_e3:\n\t" \
  "s_add_u32 m0, s25, 32800\n\t" \
  "s_add_u32 s24, s24, s26\n\t" \
  "buffer_load_dwordx4 v254, s[8:11], s24 offen lds\n\t" \
  "s_add_u32 m0, s25, 41000\n\t" \
  "s_add_u32 s24, s24, s26\n\t" \
  "buffer_load_dwordx4 v254, s[8:11], s24 offen lds\n\t" \
  "s_add_u32 m0, s25, 56880\n\t" \
  "s_add_u32 s24, s24, s26\n\t" \
  "buffer_load_dwordx4 v254, s[8:11], s24 offen lds\n\t" \
  "s_add_u32 m0, s25, 65080\n\t" \
  "s_add_u32 s24, s24, s26\n\t" \
  "buffer_load_dwordx4 v254, s[8:11], s24 offen lds\n\t" \
  ".p2align 3\n\t" \
  "s_waitcnt vmcnt(6) lgkmcnt(0)\n\t" \
  "s_barrier\n\t" \
  "ds_read_b64 v[222:223], v253 offset:1216\n\t" \
  "ds_read2_b32 v[114:115], v247 offset0:192 offset1:198\n\t" \
  "ds_read_b64 v[214:215], v253 offset:1208\n\t" \
  "ds_read2_b32 v[98:99], v247 offset0:0 offset1:6\n\t" \
  "s_sub_u32 s66, s66, 233088\n\t" \
  "s_subb_u32 s67, s67, 0\n\t" \
  "v_add_u32 v232, s56, v247\n\t" \
  "ds_read2_b32 v[130:131], v232 offset0:0 offset1:6\n\t" \
  "v_mfma_f32_32x32x16_bf16 v[176:191], v[242:245], v[240:243], 0\n\t" \
  "ds_read_b64 v[206:207], v253 offset:1200\n\t" \
  "ds_read_b64 v[212:213], v253 offset:808\n\t" \
  "ds_read2_b32 v[122:123], v247 offset0:216 offset1:222\n\t" \
  "ds_read_b64 v[202:203], v253 offset:400\n\t" \
  "ds_read_b64 v[210:211], v253 offset:408\n\t" \
  "ds_read_b64 v[220:221], v253 offset:816\n\t" \
  "ds_read_b64 v[200:201], v253 offset:0\n\t" \
  "ds_read2_b32 v[106:107], v247 offset0:24 offset1:30\n\t" \
  "ds_read_b64 v[208:209], v253 offset:8\n\t" \
  "s_add_u32 m0, s25, 73280\n\t" \
  "s_add_u32 s24, s24, s26\n\t" \
  "s_waitcnt lgkmcnt(10)\n\t" \
  "ds_read2_b32 v[102:103], v247 offset0:12 offset1:18\n\t" \
  "buffer_load_dwordx4 v254, s[8:11], s24 offen lds\n\t" \
  "ds_read2_b32 v[118:119], v247 offset0:204 offset1:210\n\t" \
  "s_add_u32 m0, s25, 81480\n\t" \
  "ds_read2_b32 v[138:139], v232 offset0:24 offset1:30\n\t" \
  "ds_read2_b32 v[142:143], v232 offset0:36 offset1:42\n\t" \
  "v_and_b32 v96, 0xffff, v98\n\t" \
  "s_add_u32 s24, s24, s26\n\t" \
  "buffer_load_dwordx4 v254, s[8:11], s24 offen lds\n\t" \
  "s_add_u32 m0, s25, 89680\n\t" \
  "ds_read_b64 v[216:217], v253 offset:16\n\t" \
  "ds_read_b64 v[204:205], v253 offset:800\n\t" \
  "v_and_b32 v112, 0xffff, v114\n\t" \
  "v_add_u32 v233, s39, v251\n\t" \
  "s_add_u32 s24, s24, s26\n\t" \
  "s_waitcnt lgkmcnt(5)\n\t" \
  "buffer_load_dwordx4 v254, s[8:11], s24 offen lds\n\t" \
  "s_add_u32 m0, s25, 97880\n\t" \
  "ds_read_b64 v[218:219], v253 offset:416\n\t" \
  "ds_read2_b32 v[126:127], v247 offset0:228 offset1:234\n\t" \
  "v_and_b32 v97, 0xffff0000, v98\n\t" \
  "v_and_b32 v100, 0xffff, v102\n\t" \
  "v_and_b32 v98, 0xffff, v99\n\t" \
  "v_and_b32 v99, 0xffff0000, v99\n\t" \
  "s_add_u32 s24, s24, s26\n\t" \
  "buffer_load_dwordx4 v254, s[8:11], s24 offen lds\n\t" \
  "ds_read2_b32 v[134:135], v232 offset0:12 offset1:18\n\t" \
  "v_add_u32 v232, 56880, v253\n\t" \
  "s_waitcnt lgkmcnt(3)\n\t" \
  "v_smfmac_f32_16x16x64_bf16 v[144:147], v[96:99], v[200:207], v234\n\t" \
  "ds_read2_b32 v[110:111], v247 offset0:36 offset1:42\n\t" \
  "v_and_b32 v105, 0xffff0000, v106\n\t" \
  "v_and_b32 v113, 0xffff0000, v114\n\t" \
  "v_and_b32 v114, 0xffff, v115\n\t" \
  "v_and_b32 v128, 0xffff, v130\n\t" \
  "v_and_b32 v101, 0xffff0000, v102\n\t" \
  "v_and_b32 v102, 0xffff, v103\n\t" \
  "v_and_b32 v115, 0xffff0000, v115\n\t" \
  "v_and_b32 v103, 0xffff0000, v103\n\t" \
  "v_and_b32 v129, 0xffff0000, v130\n\t" \
  "v_and_b32 v130, 0xffff, v131\n\t" \
  "v_and_b32 v131, 0xffff0000, v131\n\t" \
  ".p2align 3\n\t" \
  "s_waitcnt vmcnt(8) lgkmcnt(0)\n\t" \
  "s_barrier\n\t" \
  "v_and_b32 v116, 0xffff, v118\n\t" \
  "ds_read_b64 v[200:201], v253 offset:16408\n\t" \
  "ds_read_b64 v[228:229], v253 offset:17200\n\t" \
  "v_and_b32 v104, 0xffff, v106\n\t" \
  "v_and_b32 v106, 0xffff, v107\n\t" \
  "ds_read_b64 v[224:225], v253 offset:16400\n\t" \
  "v_smfmac_f32_16x16x64_bf16 v[152:155], v[128:131], v[216:223], v234\n\t" \
  "ds_read_b64 v[226:227], v253 offset:16800\n\t" \
  "ds_read_b64 v[206:207], v253 offset:17608\n\t" \
  "v_smfmac_f32_16x16x64_bf16 v[148:151], v[112:115], v[208:215], v234\n\t" \
  "ds_read_b64 v[230:231], v253 offset:17600\n\t" \
  "ds_read_b64 v[204:205], v253 offset:17208\n\t" \
  "ds_read_b64 v[202:203], v253 offset:16808\n\t" \
  "v_and_b32 v117, 0xffff0000, v118\n\t" \
  "v_and_b32 v118, 0xffff, v119\n\t" \
  "v_and_b32 v133, 0xffff0000, v134\n\t" \
  "v_and_b32 v132, 0xffff, v134\n\t" \
  "v_and_b32 v119, 0xffff0000, v119\n\t" \
  "v_and_b32 v107, 0xffff0000, v107\n\t" \
  "s_cmp_eq_u32 s37, 1\n\t" \
  "s_waitcnt lgkmcnt(2)\n\t" \
  "ds_read_b64 v[210:211], v253 offset:16816\n\t" \
  "ds_read_b64 v[214:215], v253 offset:17616\n\t" \
  "ds_read_b64 v[212:213], v253 offset:17216\n\t" \
  "ds_read_b64 v[208:209], v253 offset:16416\n\t" \
  "v_lshlrev_b32 v235, 1, v251\n\t" \
  "v_and_b32 v236, 0x70, v235\n\t" \
  "v_bfe_u32 v235, v250, 6, 1\n\t" \
  "v_lshl_add_u32 v235, v235, 4, v250\n\t" \
  "v_smfmac_f32_16x16x64_bf16 v[156:159], v[96:99], v[224:231], v234\n\t" \
  "s_cselect_b64 s[68:69], -1, 0\n\t" \
  "v_and_b32 v134, 0xffff, v135\n\t" \
  "v_and_b32 v135, 0xffff0000, v135\n\t" \
  "v_smfmac_f32_16x16x64_bf16 v[144:147], v[100:103], v[224:231], v234\n\t" \
  ".p2align 3\n\t" \
  "s_waitcnt vmcnt(5) lgkmcnt(0)\n\t" \
  "s_barrier\n\t" \
  "ds_read_b64 v[216:217], v253 offset:32800\n\t" \
  "ds_read_b64 v[222:223], v253 offset:34000\n\t" \
  "v_add3_u32 v235, v235, v236, s61\n\t" \
  "ds_read_b64 v[218:219], v253 offset:33200\n\t" \
  "s_add_u32 s24, s24, s26\n\t" \
  "s_add_u32 m0, s25, 0\n\t" \
  "ds_read_b64 v[78:79], v235 offset:8\n\t" \
  "v_smfmac_f32_16x16x64_bf16 v[160:163], v[112:115], v[200:207], v234\n\t" \
  "buffer_load_dwordx4 v254, s[8:11], s24 offen lds\n\t" \
  "ds_read_b64 v[76:77], v235 offset:0\n\t" \
  "s_add_u32 s24, s24, s26\n\t" \
  "ds_read_b64 v[224:225], v253 offset:32808\n\t" \
  "v_smfmac_f32_16x16x64_bf16 v[148:151], v[116:119], v[200:207], v234\n\t" \
  "ds_read_b64 v[226:227], v253 offset:33208\n\t" \
  "ds_read_b64 v[220:221], v253 offset:33600\n\t" \
  "s_add_u32 m0, s25, 8200\n\t" \
  "v_and_b32 v121, 0xffff0000, v122\n\t" \
  "v_and_b32 v120, 0xffff, v122\n\t" \
  "v_and_b32 v136, 0xffff, v138\n\t" \
  "v_and_b32 v122, 0xffff, v123\n\t" \
  "v_and_b32 v123, 0xffff0000, v123\n\t" \
  "buffer_load_dwordx4 v254, s[8:11], s24 offen lds\n\t" \
  "ds_read_b64 v[200:201], v253 offset:32816\n\t" \
  "ds_read_b64 v[228:229], v253 offset:33608\n\t" \
  "v_smfmac_f32_16x16x64_bf16 v[164:167], v[128:131], v[208:215], v234\n\t" \
  "ds_read_b64 v[202:203], v253 offset:33216\n\t" \
  "ds_read_b64 v[204:205], v253 offset:33616\n\t" \
  "v_and_b32 v137, 0xffff0000, v138\n\t" \
  "v_and_b32 v108, 0xffff, v110\n\t" \
  "ds_read_b64 v[84:85], v235 offset:64\n\t" \
  "v_and_b32 v138, 0xffff, v139\n\t" \
  "v_and_b32 v139, 0xffff0000, v139\n\t" \
  "ds_read_b64 v[94:95], v235 offset:136\n\t" \
  "s_waitcnt lgkmcnt(6)\n\t" \
  "v_smfmac_f32_16x16x64_bf16 v[168:171], v[96:99], v[216:223], v234\n\t" \
  "s_add_u32 s24, s24, s26\n\t" \
  "ds_read_b64 v[230:231], v253 offset:34008\n\t" \
  "ds_read_b64 v[86:87], v235 offset:72\n\t" \
  "ds_read_b64 v[206:207], v253 offset:34016\n\t" \
  "v_smfmac_f32_16x16x64_bf16 v[152:155], v[132:135], v[208:215], v234\n\t" \
  "v_and_b32 v109, 0xffff0000, v110\n\t" \
  "v_mfma_f32_32x32x16_bf16 v[0:15], v[242:245], v[240:243], 0\n\t" \
  "ds_read_b64 v[92:93], v235 offset:128\n\t" \
  "s_add_u32 m0, s25, 16400\n\t" \
  "v_and_b32 v110, 0xffff, v111\n\t" \
  "v_and_b32 v111, 0xffff0000, v111\n\t" \
  "v_add_u32 v235, s58, v235\n\t" \
  "v_smfmac_f32_16x16x64_bf16 v[156:159], v[100:103], v[216:223], v234\n\t" \
  "buffer_load_dwordx4 v254, s[8:11], s24 offen lds\n\t" \
  "v_and_b32 v124, 0xffff, v126\n\t" \
  "v_smfmac_f32_16x16x64_bf16 v[144:147], v[104:107], v[216:223], v234\n\t" \
  ".p2align 3\n\t" \
  "s_waitcnt vmcnt(7) lgkmcnt(0)\n\t" \
  "s_barrier\n\t" \
  "v_smfmac_f32_16x16x64_bf16 v[172:175], v[112:115], v[224:231], v234\n\t" \
  "s_add_u32 m0, s25, 24600\n\t" \
  "ds_read_b64 v[210:211], v232 offset:400\n\t" \
  "s_add_u32 s24, s24, s26\n\t" \
  "ds_read_b64 v[208:209], v232 offset:0\n\t" \
  "v_and_b32 v125, 0xffff0000, v126\n\t" \
  "v_and_b32 v126, 0xffff, v127\n\t" \
  "v_and_b32 v127, 0xffff0000, v127\n\t" \
  "v_smfmac_f32_16x16x64_bf16 v[160:163], v[116:119], v[224:231], v234\n\t" \
  "buffer_load_dwordx4 v254, s[8:11], s24 offen lds\n\t" \
  "ds_read_b64 v[212:213], v232 offset:800\n\t" \
  "v_smfmac_f32_16x16x64_bf16 v[148:151], v[120:123], v[224:231], v234\n\t" \
  "ds_read_b64 v[218:219], v232 offset:408\n\t" \
  "v_smfmac_f32_16x16x64_bf16 v[164:167], v[132:135], v[200:207], v234\n\t" \
  "s_add_u32 m0, s25, 32800\n\t" \
  "s_add_u32 s24, s24, s26\n\t" \
  "ds_read_b64 v[214:215], v232 offset:1200\n\t" \
  "v_smfmac_f32_16x16x64_bf16 v[176:179], v[128:131], v[200:207], v234\n\t" \
  "ds_read_b64 v[220:221], v232 offset:808\n\t" \
  "v_and_b32 v140, 0xffff, v142\n\t" \
  "ds_read_b64 v[222:223], v232 offset:1208\n\t" \
  "ds_read_b64 v[216:217], v232 offset:8\n\t" \
  "v_and_b32 v141, 0xffff0000, v142\n\t" \
  "v_and_b32 v142, 0xffff, v143\n\t" \
  "v_and_b32 v143, 0xffff0000, v143\n\t" \
  "ds_read_b64 v[224:225], v232 offset:16\n\t" \
  "ds_read_b64 v[228:229], v232 offset:816\n\t" \
  "v_smfmac_f32_16x16x64_bf16 v[152:155], v[136:139], v[200:207], v234\n\t" \
  "buffer_load_dwordx4 v254, s[8:11], s24 offen lds\n\t" \
  "s_waitcnt lgkmcnt(5)\n\t" \
  "v_smfmac_f32_16x16x64_bf16 v[180:183], v[96:99], v[208:215], v234\n\t" \
  "ds_read_b64 v[226:227], v232 offset:416\n\t" \
  "v_mov_b32 v197, 0x3f003f00\n\t" \
  "v_smfmac_f32_16x16x64_bf16 v[168:171], v[100:103], v[208:215], v234\n\t" \
  "ds_read_b64 v[230:231], v232 offset:1216\n\t" \
  "v_mov_b32 v196, 0x3f003f00\n\t" \
  "v_mfma_f32_32x32x16_bf16 v[16:31], v[242:245], v[240:243], 0\n\t" \
  "s_add_u32 m0, s25, 41000\n\t" \
  "s_add_u32 s24, s24, s26\n\t" \
  "buffer_load_dwordx4 v254, s[8:11], s24 offen lds\n\t" \
  "v_mov_b32 v199, 0x3f003f00\n\t" \
  "v_mov_b32 v198, 0x3f003f00\n\t" \
  "v_smfmac_f32_16x16x64_bf16 v[144:147], v[108:111], v[208:215], v234\n\t" \
  ".p2align 3\n\t" \
  "s_waitcnt vmcnt(7) lgkmcnt(0)\n\t" \
  "s_barrier\n\t" \
  "ds_read_b64 v[202:203], v232 offset:16800\n\t" \
  "ds_read_b64 v[200:201], v232 offset:16400\n\t" \
  "v_smfmac_f32_16x16x64_bf16 v[184:187], v[112:115], v[216:223], v234\n\t" \
  "v_smfmac_f32_16x16x64_bf16 v[172:175], v[116:119], v[216:223], v234\n\t" \
  "ds_read_b64 v[204:205], v232 offset:17200\n\t" \
  "ds_read_b64 v[206:207], v232 offset:17600\n\t" \
  "v_smfmac_f32_16x16x64_bf16 v[156:159], v[104:107], v[208:215], v234\n\t" \
  "ds_read_b64 v[210:211], v232 offset:16808\n\t" \
  "s_add_u32 s24, s24, s26\n\t" \
  "v_cvt_pk_bf16_f32 v144, v144, v145\n\t" \
  "v_smfmac_f32_16x16x64_bf16 v[148:151], v[124:127], v[216:223], v234\n\t" \
  "ds_read_b64 v[72:73], v235 offset:0\n\t" \
  "s_add_u32 m0, s25, 56880\n\t" \
  "v_smfmac_f32_16x16x64_bf16 v[160:163], v[120:123], v[216:223], v234\n\t" \
  "ds_read_b64 v[208:209], v232 offset:16408\n\t" \
  "v_cvt_pk_bf16_f32 v145, v146, v147\n\t" \
  "ds_read_b64 v[214:215], v232 offset:17608\n\t" \
  "buffer_load_dwordx4 v254, s[8:11], s24 offen lds\n\t" \
  "ds_read_b64 v[74:75], v235 offset:8\n\t" \
  "v_smfmac_f32_16x16x64_bf16 v[164:167], v[136:139], v[224:231], v234\n\t" \
  "ds_read_b64 v[80:81], v235 offset:64\n\t" \
  "v_cvt_pk_bf16_f32 v148, v148, v149\n\t" \
  "v_cvt_pk_bf16_f32 v149, v150, v151\n\t" \
  "v_smfmac_f32_16x16x64_bf16 v[176:179], v[132:135], v[224:231], v234\n\t" \
  "ds_read_b64 v[218:219], v232 offset:16816\n\t" \
  "ds_read_b64 v[212:213], v232 offset:17208\n\t" \
  "v_smfmac_f32_16x16x64_bf16 v[188:191], v[128:131], v[224:231], v234\n\t" \
  "v_smfmac_f32_16x16x64_bf16 v[152:155], v[140:143], v[224:231], v234\n\t" \
  "ds_read_b64 v[88:89], v235 offset:128\n\t" \
  "ds_read_b64 v[220:221], v232 offset:17216\n\t" \
  "s_waitcnt lgkmcnt(10)\n\t" \
  "v_smfmac_f32_16x16x64_bf16 v[180:183], v[100:103], v[200:207], v234\n\t" \
  "ds_read_b64 v[222:223], v232 offset:17616\n\t" \
  "ds_read_b64 v[82:83], v235 offset:72\n\t" \
  "v_smfmac_f32_16x16x64_bf16 v[0:3], v[96:99], v[200:207], v234\n\t" \
  "ds_read_b64 v[90:91], v235 offset:136\n\t" \
  "v_mfma_f32_32x32x16_bf16 v[32:47], v[242:245], v[240:243], 0\n\t" \
  "ds_read_b64 v[216:217], v232 offset:16416\n\t" \
  "v_cvt_pk_bf16_f32 v152, v152, v153\n\t" \
  "s_add_u32 s24, s24, s26\n\t" \
  "v_cvt_pk_bf16_f32 v153, v154, v155\n\t" \
  "v_smfmac_f32_16x16x64_bf16 v[168:171], v[104:107], v[200:207], v234\n\t" \
  "v_add_u32 v235, s59, v235\n\t" \
  "v_smfmac_f32_16x16x64_bf16 v[156:159], v[108:111], v[200:207], v234\n\t" \
  ".p2align 3\n\t" \
  "s_waitcnt vmcnt(6) lgkmcnt(0)\n\t" \
  "s_barrier\n\t" \
  "s_add_u32 m0, s25, 65080\n\t" \
  "ds_read_b64 v[226:227], v232 offset:33200\n\t" \
  "buffer_load_dwordx4 v254, s[8:11], s24 offen lds\n\t" \
  "v_smfmac_f32_16x16x64_bf16 v[4:7], v[112:115], v[208:215], v234\n\t" \
  "ds_read_b64 v[48:49], v235 offset:0\n\t" \
  "ds_read_b64 v[206:207], v232 offset:34008\n\t" \
  "v_smfmac_f32_16x16x64_bf16 v[184:187], v[116:119], v[208:215], v234\n\t" \
  "s_add_u32 m0, s25, 73280\n\t" \
  "ds_read_b64 v[224:225], v232 offset:32800\n\t" \
  "s_add_u32 s24, s24, s26\n\t" \
  "buffer_load_dwordx4 v254, s[8:11], s24 offen lds\n\t" \
  "ds_read_b64 v[230:231], v232 offset:34000\n\t" \
  "ds_read_b64 v[50:51], v235 offset:8\n\t" \
  "ds_read_b64 v[228:229], v232 offset:33600\n\t" \
  "v_smfmac_f32_16x16x64_bf16 v[172:175], v[120:123], v[208:215], v234\n\t" \
  "ds_read_b64 v[200:201], v232 offset:32808\n\t" \
  "s_add_u32 m0, s25, 81480\n\t" \
  "s_add_u32 s24, s24, s26\n\t" \
  "buffer_load_dwordx4 v254, s[8:11], s24 offen lds\n\t" \
  "v_cvt_pk_bf16_f32 v156, v156, v157\n\t" \
  "v_smfmac_f32_16x16x64_bf16 v[8:11], v[128:131], v[216:223], v234\n\t" \
  "v_smfmac_f32_16x16x64_bf16 v[160:163], v[124:127], v[208:215], v234\n\t" \
  "ds_read_b64 v[66:67], v235 offset:136\n\t" \
  "ds_read_b64 v[56:57], v235 offset:64\n\t" \
  "v_smfmac_f32_16x16x64_bf16 v[188:191], v[132:135], v[216:223], v234\n\t" \
  "ds_read_b64 v[210:211], v232 offset:33216\n\t" \
  "v_smfmac_f32_16x16x64_bf16 v[164:167], v[140:143], v[216:223], v234\n\t" \
  "ds_read_b64 v[204:205], v232 offset:33608\n\t" \
  "ds_read_b64 v[214:215], v232 offset:34016\n\t" \
  "ds_read_b64 v[208:209], v232 offset:32816\n\t" \
  "s_waitcnt lgkmcnt(7)\n\t" \
  "v_cvt_pk_bf16_f32 v157, v158, v159\n\t" \
  "ds_read_b64 v[58:59], v235 offset:72\n\t" \
  "v_smfmac_f32_16x16x64_bf16 v[0:3], v[100:103], v[224:231], v234\n\t" \
  "ds_read_b64 v[202:203], v232 offset:33208\n\t" \
  "v_cvt_pk_bf16_f32 v160, v160, v161\n\t" \
  "v_cvt_pk_bf16_f32 v161, v162, v163\n\t" \
  "v_smfmac_f32_16x16x64_bf16 v[176:179], v[136:139], v[216:223], v234\n\t" \
  "ds_read_b64 v[212:213], v232 offset:33616\n\t" \
  "ds_read_b64 v[64:65], v235 offset:128\n\t" \
  "v_smfmac_f32_16x16x64_bf16 v[12:15], v[96:99], v[224:231], v234\n\t" \
  "v_cvt_pk_bf16_f32 v164, v164, v165\n\t" \
  "v_cvt_pk_bf16_f32 v165, v166, v167\n\t" \
  "v_smfmac_f32_16x16x64_bf16 v[168:171], v[108:111], v[224:231], v234\n\t" \
  "v_smfmac_f32_16x16x64_bf16 v[180:183], v[104:107], v[224:231], v234\n\t" \
  ".p2align 3\n\t" \
  "s_waitcnt vmcnt(7) lgkmcnt(0)\n\t" \
  "s_barrier\n\t" \
  "ds_read_b64 v[216:217], v253 offset:0\n\t" \
  "ds_read_b64 v[218:219], v253 offset:400\n\t" \
  "ds_read_b64 v[220:221], v253 offset:800\n\t" \
  "ds_write_b64 v193, v[144:145] offset:12288\n\t" \
  "v_smfmac_f32_16x16x64_bf16 v[16:19], v[112:115], v[200:207], v234\n\t" \
  "v_smfmac_f32_16x16x64_bf16 v[4:7], v[116:119], v[200:207], v234\n\t" \
  "ds_write_b64 v193, v[148:149] offset:16384\n\t" \
  "v_smfmac_f32_16x16x64_bf16 v[184:187], v[120:123], v[200:207], v234\n\t" \
  "ds_write_b64 v193, v[156:157] offset:0\n\t" \
  "v_smfmac_f32_16x16x64_bf16 v[20:23], v[128:131], v[208:215], v234\n\t" \
  "ds_write_b64 v193, v[152:153] offset:20480\n\t" \
  "v_cvt_pk_bf16_f32 v168, v168, v169\n\t" \
  "v_cvt_pk_bf16_f32 v169, v170, v171\n\t" \
  "ds_read_b64 v[226:227], v253 offset:408\n\t" \
  "ds_write_b64 v193, v[164:165] offset:8192\n\t" \
  "ds_read_b64 v[230:231], v253 offset:1208\n\t" \
  "v_smfmac_f32_16x16x64_bf16 v[172:175], v[124:127], v[200:207], v234\n\t" \
  "ds_read_b64 v[222:223], v253 offset:1200\n\t" \
  "ds_read_b64 v[224:225], v253 offset:8\n\t" \
  "v_smfmac_f32_16x16x64_bf16 v[8:11], v[132:135], v[208:215], v234\n\t" \
  "s_mov_b64 exec, s[68:69]\n\t" \
  "ds_read_b64 v[48:49], v235 offset:0\n\t" \
  "ds_read_b64 v[50:51], v235 offset:8\n\t" \
  "ds_read_b64 v[56:57], v235 offset:64\n\t" \
  "s_mov_b64 exec, -1\n\t" \
  "v_smfmac_f32_16x16x64_bf16 v[176:179], v[140:143], v[208:215], v234\n\t" \
  "ds_read_b64 v[200:201], v253 offset:16\n\t" \
  "ds_write_b64 v193, v[160:161] offset:4096\n\t" \
  "v_smfmac_f32_16x16x64_bf16 v[188:191], v[136:139], v[208:215], v234\n\t" \
  "s_waitcnt lgkmcnt(6)\n\t" \
  "ds_read_b64 v[202:203], v253 offset:416\n\t" \
  "ds_read_b64 v[228:229], v253 offset:808\n\t" \
  "v_smfmac_f32_16x16x64_bf16 v[24:27], v[96:99], v[216:223], v234\n\t" \
  "ds_read_b64 v[204:205], v253 offset:816\n\t" \
  "ds_read_b64 v[206:207], v253 offset:1216\n\t" \
  "v_smfmac_f32_16x16x64_bf16 v[0:3], v[104:107], v[216:223], v234\n\t" \
  "s_mov_b64 exec, s[68:69]\n\t" \
  "ds_read_b64 v[58:59], v235 offset:72\n\t" \
  "ds_read_b64 v[64:65], v235 offset:128\n\t" \
  "ds_read_b64 v[66:67], v235 offset:136\n\t" \
  "s_mov_b64 exec, -1\n\t" \
  "v_cvt_pk_bf16_f32 v176, v176, v177\n\t" \
  "v_add_u32 v235, s60, v235\n\t" \
  "v_cvt_pk_bf16_f32 v172, v172, v173\n\t" \
  "v_cvt_pk_bf16_f32 v173, v174, v175\n\t" \
  "v_smfmac_f32_16x16x64_bf16 v[12:15], v[100:103], v[216:223], v234\n\t" \
  "v_cvt_pk_bf16_f32 v177, v178, v179\n\t" \
  "v_smfmac_f32_16x16x64_bf16 v[180:183], v[108:111], v[216:223], v234\n\t" \
  ".p2align 3\n\t" \
  "s_waitcnt vmcnt(6) lgkmcnt(0)\n\t" \
  "s_barrier\n\t" \
  "ds_write_b64 v192, v[168:169] offset:0\n\t" \
  "v_smfmac_f32_16x16x64_bf16 v[28:31], v[112:115], v[224:231], v234\n\t" \
  "ds_read_b64 v[210:211], v253 offset:16800\n\t" \
  "ds_read_b128 v[144:147], v194 offset:0\n\t" \
  "v_smfmac_f32_16x16x64_bf16 v[16:19], v[116:119], v[224:231], v234\n\t" \
  "ds_read_b64 v[208:209], v253 offset:16400\n\t" \
  "ds_write_b64 v192, v[176:177] offset:8192\n\t" \
  "v_cvt_pk_bf16_f32 v180, v180, v181\n\t" \
  "v_cvt_pk_bf16_f32 v181, v182, v183\n\t" \
  "ds_write_b64 v192, v[172:173] offset:4096\n\t" \
  "v_smfmac_f32_16x16x64_bf16 v[4:7], v[120:123], v[224:231], v234\n\t" \
  "ds_read_b128 v[148:151], v194 offset:4096\n\t" \
  "s_waitcnt lgkmcnt(4)\n\t" \
  "v_mfma_f32_16x16x32_bf16 v[168:171], v[144:147], v[144:147], 0\n\t" \
  "ds_read_b64 v[214:215], v253 offset:17600\n\t" \
  "ds_read_b64 v[212:213], v253 offset:17200\n\t" \
  "v_smfmac_f32_16x16x64_bf16 v[184:187], v[124:127], v[224:231], v234\n\t" \
  "ds_read_b128 v[152:155], v194 offset:8192\n\t" \
  "v_add_u32 v194, -93792, v194\n\t" \
  "v_smfmac_f32_16x16x64_bf16 v[32:35], v[128:131], v[200:207], v234\n\t" \
  "ds_read_b64 v[220:221], v253 offset:17208\n\t" \
  "ds_read_b64 v[218:219], v253 offset:16808\n\t" \
  "v_smfmac_f32_16x16x64_bf16 v[20:23], v[132:135], v[200:207], v234\n\t" \
  "ds_read_b64 v[230:231], v253 offset:17616\n\t" \
  "s_waitcnt lgkmcnt(6)\n\t" \
  "v_mfma_f32_16x16x32_bf16 v[168:171], v[148:151], v[148:151], v[168:171]\n\t" \
  "s_waitcnt lgkmcnt(4)\n\t" \
  "v_smfmac_f32_16x16x64_bf16 v[36:39], v[96:99], v[208:215], v234\n\t" \
  "ds_read_b64 v[216:217], v253 offset:16408\n\t" \
  "ds_read_b64 v[224:225], v253 offset:16416\n\t" \
  "v_smfmac_f32_16x16x64_bf16 v[8:11], v[136:139], v[200:207], v234\n\t" \
  "ds_read_b64 v[226:227], v253 offset:16816\n\t" \
  "v_cvt_pk_bf16_f32 v184, v184, v185\n\t" \
  "v_cvt_pk_bf16_f32 v185, v186, v187\n\t" \
  "v_mfma_f32_16x16x32_bf16 v[172:175], v[196:199], v[144:147], 0\n\t" \
  "ds_read_b64_tr_b16 v[162:163], v195 offset:6144\n\t" \
  "ds_read_b64_tr_b16 v[156:157], v195 offset:0\n\t" \
  "ds_read_b64 v[222:223], v253 offset:17608\n\t" \
  "ds_read_b64 v[228:229], v253 offset:17216\n\t" \
  "v_smfmac_f32_16x16x64_bf16 v[188:191], v[140:143], v[200:207], v234\n\t" \
  "v_mfma_f32_16x16x32_bf16 v[172:175], v[196:199], v[148:151], v[172:175]\n\t" \
  "s_waitcnt lgkmcnt(10)\n\t" \
  "ds_read_b64_tr_b16 v[160:161], v195 offset:2048\n\t" \
  "ds_read_b64_tr_b16 v[164:165], v195 offset:8192\n\t" \
  "v_mfma_f32_16x16x32_bf16 v[168:171], v[152:155], v[152:155], v[168:171]\n\t" \
  "ds_read_b64_tr_b16 v[158:159], v195 offset:4096\n\t" \
  "ds_read_b64_tr_b16 v[166:167], v195 offset:10240\n\t" \
  "v_smfmac_f32_16x16x64_bf16 v[12:15], v[104:107], v[208:215], v234\n\t" \
  "v_cvt_pk_bf16_f32 v188, v188, v189\n\t" \
  "v_cvt_pk_bf16_f32 v189, v190, v191\n\t" \
  "v_mfma_f32_16x16x32_bf16 v[172:175], v[196:199], v[152:155], v[172:175]\n\t" \
  "v_smfmac_f32_16x16x64_bf16 v[0:3], v[108:111], v[208:215], v234\n\t" \
  ".p2align 3\n\t" \
  "s_waitcnt vmcnt(3) lgkmcnt(0)\n\t" \
  "s_barrier\n\t" \
  "ds_write_b64 v192, v[180:181] offset:12288\n\t" \
  "v_lshl_add_u32 v238, v233, 1, s63\n\t" \
  "v_add_u32 v239, 0x2000, v238\n\t" \
  "s_lshl1_add_u32 m0, s39, 10016\n\t" \
  "ds_write_b64 v192, v[184:185] offset:16384\n\t" \
  "v_smfmac_f32_16x16x64_bf16 v[24:27], v[100:103], v[208:215], v234\n\t" \
  "ds_write_b64 v192, v[188:189] offset:20480\n\t" \
  "global_load_lds_dwordx4 v238, s[6:7]\n\t" \
  "s_add_u32 m0, m0, 66048\n\t" \
  "v_cvt_pk_bf16_f32 v0, v0, v1\n\t" \
  "s_cmp_eq_u32 s39, 3584\n\t" \
  "s_cselect_b32 exec_hi, 0, -1\n\t" \
  "global_load_lds_dwordx4 v239, s[6:7]\n\t" \
  "s_mov_b64 exec, -1\n\t" \
  "ds_read_b64 v[60:61], v235 offset:64\n\t" \
  "v_smfmac_f32_16x16x64_bf16 v[40:43], v[112:115], v[216:223], v234\n\t" \
  "ds_read_b64 v[202:203], v253 offset:33200\n\t" \
  "v_smfmac_f32_16x16x64_bf16 v[28:31], v[116:119], v[216:223], v234\n\t" \
  "ds_read_b64 v[200:201], v253 offset:32800\n\t" \
  "ds_read_b64 v[204:205], v253 offset:33600\n\t" \
  "v_smfmac_f32_16x16x64_bf16 v[16:19], v[120:123], v[216:223], v234\n\t" \
  "v_mov_b32 v174, 0x1127c5ac\n\t" \
  "ds_read_b64 v[206:207], v253 offset:34000\n\t" \
  "ds_read_b64 v[52:53], v235 offset:0\n\t" \
  "v_cvt_pk_bf16_f32 v1, v2, v3\n\t" \
  "v_cndmask_b32 v168, v168, v169, s[40:41]\n\t" \
  "v_cndmask_b32 v170, v170, v171, s[40:41]\n\t" \
  "v_smfmac_f32_16x16x64_bf16 v[4:7], v[124:127], v[216:223], v234\n\t" \
  "ds_read_b64 v[222:223], v253 offset:34016\n\t" \
  "s_waitcnt lgkmcnt(2)\n\t" \
  "ds_read_b64 v[214:215], v253 offset:34008\n\t" \
  "s_not_b64 vcc, s[52:53]\n\t" \
  "v_smfmac_f32_16x16x64_bf16 v[32:35], v[132:135], v[224:231], v234\n\t" \
  "ds_read_b64 v[68:69], v235 offset:128\n\t" \
  "ds_read_b64 v[218:219], v253 offset:33216\n\t" \
  "v_smfmac_f32_16x16x64_bf16 v[12:15], v[108:111], v[200:207], v234\n\t" \
  "v_smfmac_f32_16x16x64_bf16 v[8:11], v[140:143], v[224:231], v234\n\t" \
  "ds_read_b64 v[54:55], v235 offset:8\n\t" \
  "ds_read_b64 v[212:213], v253 offset:33608\n\t" \
  "v_smfmac_f32_16x16x64_bf16 v[44:47], v[128:131], v[224:231], v234\n\t" \
  "ds_read_b64 v[62:63], v235 offset:72\n\t" \
  "v_smfmac_f32_16x16x64_bf16 v[20:23], v[136:139], v[224:231], v234\n\t" \
  "ds_read_b64 v[220:221], v253 offset:33616\n\t" \
  "ds_read_b64 v[216:217], v253 offset:32816\n\t" \
  "v_add_u32 v195, -93792, v195\n\t" \
  "v_smfmac_f32_16x16x64_bf16 v[24:27], v[104:107], v[200:207], v234\n\t" \
  "ds_read_b64 v[70:71], v235 offset:136\n\t" \
  "v_cvt_pk_bf16_f32 v8, v8, v9\n\t" \
  "ds_read_b64 v[210:211], v253 offset:33208\n\t" \
  "v_cvt_pk_bf16_f32 v4, v4, v5\n\t" \
  "v_cvt_pk_bf16_f32 v9, v10, v11\n\t" \
  "v_cvt_pk_bf16_f32 v5, v6, v7\n\t" \
  "v_smfmac_f32_16x16x64_bf16 v[36:39], v[100:103], v[200:207], v234\n\t" \
  "ds_read_b64 v[208:209], v253 offset:32808\n\t" \
  "v_cndmask_b32 v173, v168, v170, s[42:43]\n\t" \
  ".p2align 3\n\t" \
  "s_waitcnt vmcnt(2) lgkmcnt(0)\n\t" \
  "s_barrier\n\t" \
  "s_cmp_lg_u32 s37, 0\n\t" \
  "ds_read_b128 v[180:183], v194 offset:0\n\t" \
  "s_cbranch_scc1 .Lcnx_c2\n\t" \
  "s_mov_b32 m0, s46\n\t" \
  "s_mov_b32 exec_hi, 0xffff\n\t" \
  "buffer_load_dwordx4 v252, s[16:19], s33 offen lds\n\t" \
  "s_mov_b32 exec_hi, -1\n\t" \
  "s_add_u32 s33, s33, s32\n\t" \
  "s_add_u32 m0, s46, s35\n\t" \
  "s_mov_b32 exec_hi, 0xffff\n\t" \
  "buffer_load_dwordx4 v252, s[16:19], s34 offen lds\n\t" \
  "s_mov_b32 exec_hi, -1\n\t" \
  "s_add_u32 s34, s34, s32\n\t" \
  "s_mov_b32 m0, s47\n\t" \
  "s_mov_b32 exec_hi, 0xffff\n\t" \
  "buffer_load_dwordx4 v252, s[16:19], s33 offen lds\n\t" \
  "s_mov_b32 exec_hi, -1\n\t" \
  "s_add_u32 s33, s33, s32\n\t" \
  "s_add_u32 m0, s47, s35\n\t" \
  "s_mov_b32 exec_hi, 0xffff\n\t" \
  "buffer_load_dwordx4 v252, s[16:19], s34 offen lds\n\t" \
  "s_mov_b32 exec_hi, -1\n\t" \
  "s_add_u32 s34, s34, s32\n\t" \
  ".Lcnx_c2:\n\t" \
  "ds_read_b128 v[184:187], v194 offset:4096\n\t" \
  "v_smfmac_f32_16x16x64_bf16 v[20:23], v[140:143], v[216:223], v234\n\t" \
  "ds_write_b64 v192, v[0:1] offset:24576\n\t" \
  "ds_read_b64 v[224:225], v232 offset:0\n\t" \
  "ds_read_b64 v[230:231], v232 offset:1200\n\t" \
  "ds_read_b128 v[188:191], v194 offset:8192\n\t" \
  "v_smfmac_f32_16x16x64_bf16 v[28:31], v[120:123], v[208:215], v234\n\t" \
  "ds_read_b64 v[228:229], v232 offset:800\n\t" \
  "s_waitcnt lgkmcnt(6)\n\t" \
  "v_smfmac_f32_16x16x64_bf16 v[44:47], v[132:135], v[216:223], v234\n\t" \
  "ds_read_b64 v[204:205], v232 offset:808\n\t" \
  "ds_write_b64 v192, v[8:9] offset:32768\n\t" \
  "v_smfmac_f32_16x16x64_bf16 v[16:19], v[124:127], v[208:215], v234\n\t" \
  "ds_read_b64 v[206:207], v232 offset:1208\n\t" \
  "v_mfma_f32_16x16x32_bf16 v[176:179], v[180:183], v[180:183], 0\n\t" \
  "ds_read_b64 v[226:227], v232 offset:400\n\t" \
  "ds_read_b64 v[202:203], v232 offset:408\n\t" \
  "v_smfmac_f32_16x16x64_bf16 v[40:43], v[116:119], v[208:215], v234\n\t" \
  "ds_read_b64 v[212:213], v232 offset:816\n\t" \
  "ds_write_b64 v192, v[4:5] offset:28672\n\t" \
  "s_nop 0\n\t" \
  "v_cvt_pk_bf16_f32 v16, v16, v17\n\t" \
  "v_cvt_pk_bf16_f32 v12, v12, v13\n\t" \
  "v_cvt_pk_bf16_f32 v13, v14, v15\n\t" \
  "v_mfma_f32_16x16x32_bf16 v[168:171], v[196:199], v[180:183], 0\n\t" \
  "ds_write_b64 v192, v[12:13] offset:36864\n\t" \
  "s_waitcnt lgkmcnt(13)\n\t" \
  "v_mfma_f32_16x16x32_bf16 v[176:179], v[184:187], v[184:187], v[176:179]\n\t" \
  "s_waitcnt lgkmcnt(9)\n\t" \
  "ds_read_b64 v[214:215], v232 offset:1216\n\t" \
  "ds_read_b64 v[200:201], v232 offset:8\n\t" \
  "v_mfma_f32_16x16x32_bf16 v[168:171], v[196:199], v[184:187], v[168:171]\n\t" \
  "v_cvt_pk_bf16_f32 v17, v18, v19\n\t" \
  "v_mfma_f32_16x16x32_bf16 v[176:179], v[188:191], v[188:191], v[176:179]\n\t" \
  "ds_write_b64 v192, v[16:17] offset:40960\n\t" \
  "v_mfma_f32_16x16x32_bf16 v[168:171], v[196:199], v[188:191], v[168:171]\n\t" \
  "ds_read_b64 v[210:211], v232 offset:416\n\t" \
  "s_waitcnt lgkmcnt(8)\n\t" \
  "ds_read_b64 v[208:209], v232 offset:16\n\t" \
  "v_mov_b32 v183, 0\n\t" \
  "v_mov_b32 v187, 0\n\t" \
  "v_cvt_pk_bf16_f32 v20, v20, v21\n\t" \
  "v_smfmac_f32_16x16x64_bf16 v[36:39], v[104:107], v[224:231], v234\n\t" \
  "v_cndmask_b32 v178, v178, v179, s[40:41]\n\t" \
  "v_cndmask_b32 v176, v176, v177, s[40:41]\n\t" \
  "v_cndmask_b32 v176, v176, v178, s[42:43]\n\t" \
  "v_smfmac_f32_16x16x64_bf16 v[24:27], v[108:111], v[224:231], v234\n\t" \
  "v_cndmask_b32_dpp v172, v168, v172, vcc row_ror:8 row_mask:0xf bank_mask:0xf\n\t" \
  "v_cndmask_b32_dpp v173, v176, v173, vcc row_ror:8 row_mask:0xf bank_mask:0xf\n\t" \
  "v_mul_f32 v172, 0x3daaaaab, v172\n\t" \
  "v_smfmac_f32_16x16x64_bf16 v[32:35], v[136:139], v[216:223], v234\n\t" \
  ".p2align 3\n\t" \
  "s_waitcnt vmcnt(0) lgkmcnt(0)\n\t" \
  "s_barrier\n\t" \
  "ds_read_b64 v[222:223], v232 offset:17600\n\t" \
  "v_smfmac_f32_16x16x64_bf16 v[40:43], v[120:123], v[200:207], v234\n\t" \
  "ds_read_b64 v[216:217], v232 offset:16400\n\t" \
  "v_fmamk_f32 v173, v173, 0x3e2aaaab, v174\n\t" \
  "v_fma_f32 v173, -v172, v172, v173\n\t" \
  "ds_read_b64 v[220:221], v232 offset:17200\n\t" \
  "ds_read_b64 v[218:219], v232 offset:16800\n\t" \
  "ds_read_b64 v[226:227], v232 offset:16808\n\t" \
  "ds_read_b64 v[224:225], v232 offset:16408\n\t" \
  "v_cvt_pk_bf16_f32 v21, v22, v23\n\t" \
  "ds_write_b64 v192, v[20:21] offset:45056\n\t" \
  "v_max_f32 v173, v174, v173\n\t" \
  "v_rsq_f32 v173, v173 div:2\n\t" \
  "s_mov_b64 exec, s[52:53]\n\t" \
  "v_mul_f32 v173, 0x08805555, v173\n\t" \
  "s_mov_b64 exec, -1\n\t" \
  "s_mov_b64 vcc, s[40:41]\n\t" \
  "v_cvt_pk_bf16_f32 v175, v173, v173\n\t" \
  "v_and_b32 v173, 0xffff0000, v175\n\t" \
  "v_mul_f32 v174, -v172, v173 div:2\n\t" \
  "v_cndmask_b32_sdwa v175, v175, v173, vcc dst_sel:DWORD dst_unused:UNUSED_PAD src0_sel:WORD_1 src1_sel:DWORD\n\t" \
  "v_smfmac_f32_16x16x64_bf16 v[44:47], v[136:139], v[208:215], v234\n\t" \
  "s_or_b64 vcc, s[52:53], s[42:43]\n\t" \
  "ds_read_b64 v[230:231], v232 offset:17608\n\t" \
  "v_smfmac_f32_16x16x64_bf16 v[28:31], v[124:127], v[200:207], v234\n\t" \
  "ds_read_b64 v[200:201], v232 offset:16416\n\t" \
  "ds_read_b64 v[228:229], v232 offset:17208\n\t" \
  "v_cvt_pk_bf16_f32 v24, v24, v25\n\t" \
  "v_cvt_pk_bf16_f32 v25, v26, v27\n\t" \
  "v_cndmask_b32 v180, v175, v240, vcc\n\t" \
  "v_and_b32 v172, 0xffff0000, v174\n\t" \
  "v_sub_f32 v174, v174, v172\n\t" \
  "v_smfmac_f32_16x16x64_bf16 v[32:35], v[140:143], v[208:215], v234\n\t" \
  "ds_write_b64 v192, v[24:25] offset:56880\n\t" \
  "ds_read_b64 v[202:203], v232 offset:16816\n\t" \
  "v_perm_b32 v174, v172, v174, s55\n\t" \
  "v_cndmask_b32_dpp v184, v175, v240, vcc row_ror:8 row_mask:0xf bank_mask:0xf\n\t" \
  "s_mov_b64 vcc, s[52:53]\n\t" \
  "ds_read_b64_tr_b16 v[146:147], v195 offset:4096\n\t" \
  "ds_read_b64_tr_b16 v[144:145], v195 offset:0\n\t" \
  "s_waitcnt lgkmcnt(10)\n\t" \
  "v_smfmac_f32_16x16x64_bf16 v[36:39], v[108:111], v[216:223], v234\n\t" \
  "ds_read_b64 v[204:205], v232 offset:17216\n\t" \
  "ds_read_b64 v[206:207], v232 offset:17616\n\t" \
  "v_cvt_pk_bf16_f32 v28, v28, v29\n\t" \
  "v_cndmask_b32_dpp v186, v174, v240, vcc row_ror:8 row_mask:0xf bank_mask:0xf\n\t" \
  "v_cndmask_b32 v182, v174, v240, vcc\n\t" \
  "ds_read_b64_tr_b16 v[148:149], v195 offset:2048\n\t" \
  "s_orn2_b64 vcc, s[52:53], s[42:43]\n\t" \
  "v_cndmask_b32 v181, v175, v240, vcc\n\t" \
  "v_cndmask_b32_dpp v185, v175, v240, vcc row_ror:8 row_mask:0xf bank_mask:0xf\n\t" \
  "v_cvt_pk_bf16_f32 v29, v30, v31\n\t" \
  "s_waitcnt lgkmcnt(1)\n\t" \
  "v_smfmac_f32_16x16x64_bf16 v[44:47], v[140:143], v[200:207], v234\n\t" \
  "ds_read_b64_tr_b16 v[150:151], v195 offset:6144\n\t" \
  "v_smfmac_f32_16x16x64_bf16 v[40:43], v[124:127], v[224:231], v234\n\t" \
  "ds_write_b64 v192, v[28:29] offset:60976\n\t" \
  "v_mfma_f32_16x16x16_bf16 v[4:7], v[196:197], v[186:187], 0\n\t" \
  "ds_read_b64_tr_b16 v[154:155], v195 offset:10240\n\t" \
  "ds_read_b64_tr_b16 v[152:153], v195 offset:8192\n\t" \
  "v_mfma_f32_16x16x16_bf16 v[0:3], v[196:197], v[182:183], 0\n\t" \
  "v_cvt_pk_bf16_f32 v32, v32, v33\n\t" \
  "v_cvt_pk_bf16_f32 v33, v34, v35\n\t" \
  "v_mfma_f32_16x16x16_bf16 v[188:191], v[156:157], v[180:181], v[0:3]\n\t" \
  "ds_write_b64 v192, v[32:33] offset:65072\n\t" \
  "v_mfma_f32_16x16x16_bf16 v[176:179], v[158:159], v[180:181], v[0:3]\n\t" \
  "v_cvt_pk_bf16_f32 v36, v36, v37\n\t" \
  "v_cvt_pk_bf16_f32 v37, v38, v39\n\t" \
  "v_mfma_f32_16x16x16_bf16 v[168:171], v[160:161], v[180:181], v[0:3]\n\t" \
  "ds_write_b64 v193, v[36:37] offset:0\n\t" \
  "v_cvt_pk_bf16_f32 v40, v40, v41\n\t" \
  "s_nop 0\n\t" \
  "v_cvt_pk_bf16_f32 v97, v190, v191\n\t" \
  "v_cvt_pk_bf16_f32 v41, v42, v43\n\t" \
  "ds_write_b64 v193, v[40:41] offset:4096\n\t" \
  "v_cvt_pk_bf16_f32 v96, v188, v189\n\t" \
  "v_mfma_f32_16x16x16_bf16 v[172:175], v[162:163], v[180:181], v[0:3]\n\t" \
  "v_cvt_pk_bf16_f32 v44, v44, v45\n\t" \
  "v_cvt_pk_bf16_f32 v45, v46, v47\n\t" \
  "v_mfma_f32_16x16x16_bf16 v[188:191], v[164:165], v[180:181], v[0:3]\n\t" \
  "ds_write_b64 v193, v[44:45] offset:8192\n\t" \
  "v_cvt_pk_bf16_f32 v98, v176, v177\n\t" \
  "v_cvt_pk_bf16_f32 v113, v170, v171\n\t" \
  "v_cvt_pk_bf16_f32 v99, v178, v179\n\t" \
  "v_mfma_f32_16x16x16_bf16 v[176:179], v[166:167], v[180:181], v[0:3]\n\t" \
  "s_nop 2\n\t" \
  "v_cvt_pk_bf16_f32 v128, v188, v189\n\t" \
  "v_cvt_pk_bf16_f32 v112, v168, v169\n\t" \
  "v_cvt_pk_bf16_f32 v129, v190, v191\n\t" \
  "s_nop 1\n\t" \
  "v_cvt_pk_bf16_f32 v131, v178, v179\n\t" \
  "v_cvt_pk_bf16_f32 v130, v176, v177\n\t" \
  ".p2align 3\n\t" \
  "s_waitcnt vmcnt(0) lgkmcnt(0)\n\t" \
  "s_barrier\n\t" \
  "ds_read_b128 v[104:107], v194 offset:24576\n\t" \
  "v_cvt_pk_bf16_f32 v115, v174, v175\n\t" \
  "ds_read_b64_tr_b16 v[210:211], v248 offset:44072\n\t" \
  "ds_read_b128 v[120:123], v194 offset:28672\n\t" \
  "ds_read_b64_tr_b16 v[208:209], v248 offset:43048\n\t" \
  "v_cvt_pk_bf16_f32 v114, v172, v173\n\t" \
  "v_mfma_f32_16x16x16_bf16 v[168:171], v[144:145], v[184:185], v[4:7]\n\t" \
  "ds_read_b64_tr_b16 v[216:217], v248 offset:45096\n\t" \
  "v_mfma_f32_16x16x16_bf16 v[172:175], v[146:147], v[184:185], v[4:7]\n\t" \
  "ds_read_b64_tr_b16 v[214:215], v248 offset:47144\n\t" \
  "s_mov_b32 m0, s44\n\t" \
  "s_mov_b32 exec_hi, 0xffff\n\t" \
  "buffer_load_dwordx4 v252, s[16:19], s33 offen lds\n\t" \
  "s_mov_b32 exec_hi, -1\n\t" \
  "ds_read_b64_tr_b16 v[212:213], v248 offset:46120\n\t" \
  "ds_read_b64_tr_b16 v[218:219], v248 offset:48168\n\t" \
  "s_add_u32 s33, s33, s32\n\t" \
  "v_mfma_f32_16x16x16_bf16 v[188:191], v[148:149], v[184:185], v[4:7]\n\t" \
  "ds_read_b64_tr_b16 v[160:161], v248 offset:43560\n\t" \
  "ds_read_b64_tr_b16 v[162:163], v248 offset:44584\n\t" \
  "v_cvt_pk_bf16_f32 v100, v168, v169\n\t" \
  "ds_read_b64_tr_b16 v[164:165], v248 offset:46632\n\t" \
  "v_mfma_f32_16x16x16_bf16 v[176:179], v[150:151], v[184:185], v[4:7]\n\t" \
  "v_cvt_pk_bf16_f32 v102, v172, v173\n\t" \
  "ds_read_b64_tr_b16 v[166:167], v248 offset:47656\n\t" \
  "v_cvt_pk_bf16_f32 v103, v174, v175\n\t" \
  "v_cvt_pk_bf16_f32 v117, v190, v191\n\t" \
  "v_mfma_f32_16x16x16_bf16 v[172:175], v[154:155], v[184:185], v[4:7]\n\t" \
  "ds_read_b64_tr_b16 v[24:25], v195 offset:24576\n\t" \
  "ds_read_b64_tr_b16 v[26:27], v195 offset:28672\n\t" \
  "v_cvt_pk_bf16_f32 v101, v170, v171\n\t" \
  "v_mfma_f32_16x16x16_bf16 v[168:171], v[152:153], v[184:185], v[4:7]\n\t" \
  "ds_read_b64_tr_b16 v[232:233], v248 offset:45608\n\t" \
  "ds_read_b64_tr_b16 v[234:235], v248 offset:48680\n\t" \
  "v_cvt_pk_bf16_f32 v119, v178, v179\n\t" \
  "v_cvt_pk_bf16_f32 v116, v188, v189\n\t" \
  "v_cvt_pk_bf16_f32 v118, v176, v177\n\t" \
  "s_waitcnt lgkmcnt(12)\n\t" \
  "v_mfma_f32_16x16x32_bf16 v[144:147], v[208:211], v[96:99], 0\n\t" \
  "ds_read_b64_tr_b16 v[28:29], v195 offset:26624\n\t" \
  "ds_read_b128 v[136:139], v194 offset:32768\n\t" \
  "v_add_u32 v194, s54, v194\n\t" \
  "ds_read_b128 v[108:111], v194 offset:49152\n\t" \
  "ds_read_b64_tr_b16 v[30:31], v195 offset:30720\n\t" \
  "v_cvt_pk_bf16_f32 v132, v168, v169\n\t" \
  "v_cvt_pk_bf16_f32 v133, v170, v171\n\t" \
  "s_waitcnt lgkmcnt(13)\n\t" \
  "v_mfma_f32_16x16x32_bf16 v[144:147], v[212:215], v[112:115], v[144:147]\n\t" \
  "ds_read_b64_tr_b16 v[32:33], v195 offset:32768\n\t" \
  "ds_read_b64_tr_b16 v[34:35], v195 offset:34816\n\t" \
  "s_waitcnt lgkmcnt(4)\n\t" \
  "ds_read_b128 v[124:127], v194 offset:53248\n\t" \
  "ds_read_b128 v[140:143], v194 offset:57344\n\t" \
  "v_cvt_pk_bf16_f32 v135, v174, v175\n\t" \
  "v_cvt_pk_bf16_f32 v134, v172, v173\n\t" \
  "v_mfma_f32_16x16x32_bf16 v[152:155], v[208:211], v[100:103], 0\n\t" \
  "v_add_u32 v195, s54, v195\n\t" \
  "ds_read_b64_tr_b16 v[38:39], v195 offset:53248\n\t" \
  "v_mfma_f32_16x16x32_bf16 v[144:147], v[216:219], v[128:131], v[144:147]\n\t" \
  "ds_read_b64_tr_b16 v[36:37], v195 offset:49152\n\t" \
  "ds_read_b64_tr_b16 v[40:41], v195 offset:51200\n\t" \
  "s_add_u32 m0, s44, s35\n\t" \
  "s_mov_b32 exec_hi, 0xffff\n\t" \
  "buffer_load_dwordx4 v252, s[16:19], s34 offen lds\n\t" \
  "s_mov_b32 exec_hi, -1\n\t" \
  "s_add_u32 s34, s34, s32\n\t" \
  "v_mfma_f32_16x16x32_bf16 v[152:155], v[212:215], v[116:119], v[152:155]\n\t" \
  "ds_read_b64_tr_b16 v[42:43], v195 offset:55296\n\t" \
  "v_mfma_f32_16x16x32_bf16 v[152:155], v[216:219], v[132:135], v[152:155]\n\t" \
  "ds_read_b64_tr_b16 v[44:45], v195 offset:57344\n\t" \
  "v_mfma_f32_16x16x32_bf16 v[8:11], v[104:107], v[104:107], 0\n\t" \
  "ds_read_b64_tr_b16 v[46:47], v195 offset:59392\n\t" \
  "s_mov_b32 m0, s45\n\t" \
  "s_mov_b32 exec_hi, 0xffff\n\t" \
  "buffer_load_dwordx4 v252, s[16:19], s33 offen lds\n\t" \
  "s_mov_b32 exec_hi, -1\n\t" \
  "s_add_u32 s33, s33, s32\n\t" \
  "v_mfma_f32_16x16x32_bf16 v[12:15], v[196:199], v[104:107], 0\n\t" \
  "v_cvt_pknorm_i16_f32 v222, |v144|, |v145|\n\t" \
  "v_cvt_pknorm_i16_f32 v220, |v146|, |v147|\n\t" \
  "v_mfma_f32_16x16x32_bf16 v[8:11], v[120:123], v[120:123], v[8:11]\n\t" \
  "v_lshlrev_b32_sdwa v221, 2, v220 dst_sel:DWORD dst_unused:UNUSED_PAD src0_sel:DWORD src1_sel:BYTE_3\n\t" \
  "v_lshlrev_b32_sdwa v220, 2, v220 dst_sel:DWORD dst_unused:UNUSED_PAD src0_sel:DWORD src1_sel:BYTE_1\n\t" \
  "v_mfma_f32_16x16x32_bf16 v[12:15], v[196:199], v[120:123], v[12:15]\n\t" \
  "ds_read_b32 v220, v220 offset:10016\n\t" \
  "v_lshlrev_b32_sdwa v223, 2, v222 dst_sel:DWORD dst_unused:UNUSED_PAD src0_sel:DWORD src1_sel:BYTE_3\n\t" \
  "ds_read_b32 v221, v221 offset:10016\n\t" \
  "ds_read_b32 v223, v223 offset:10016\n\t" \
  "v_lshlrev_b32_sdwa v227, 1, v153 dst_sel:DWORD dst_unused:UNUSED_PAD src0_sel:DWORD src1_sel:WORD_1\n\t" \
  "v_lshlrev_b32_sdwa v222, 2, v222 dst_sel:DWORD dst_unused:UNUSED_PAD src0_sel:DWORD src1_sel:BYTE_1\n\t" \
  "v_lshlrev_b32_sdwa v224, 1, v154 dst_sel:DWORD dst_unused:UNUSED_PAD src0_sel:DWORD src1_sel:WORD_1\n\t" \
  "v_mfma_f32_16x16x32_bf16 v[12:15], v[196:199], v[136:139], v[12:15]\n\t" \
  "v_lshlrev_b32_sdwa v226, 1, v152 dst_sel:DWORD dst_unused:UNUSED_PAD src0_sel:DWORD src1_sel:WORD_1\n\t" \
  "v_mfma_f32_16x16x32_bf16 v[8:11], v[136:139], v[136:139], v[8:11]\n\t" \
  "v_lshlrev_b32_sdwa v225, 1, v155 dst_sel:DWORD dst_unused:UNUSED_PAD src0_sel:DWORD src1_sel:WORD_1\n\t" \
  "s_waitcnt lgkmcnt(14)\n\t" \
  "v_mfma_f32_16x16x32_bf16 v[16:19], v[108:111], v[108:111], 0\n\t" \
  "ds_read_b32 v222, v222 offset:10016\n\t" \
  "v_mfma_f32_16x16x32_bf16 v[20:23], v[196:199], v[108:111], 0\n\t" \
  "ds_read_u16_d16_hi v225, v225 offset:10528\n\t" \
  "s_add_u32 m0, s45, s35\n\t" \
  "s_mov_b32 exec_hi, 0xffff\n\t" \
  "buffer_load_dwordx4 v252, s[16:19], s34 offen lds\n\t" \
  "s_mov_b32 exec_hi, -1\n\t" \
  "s_add_u32 s34, s34, s32\n\t" \
  "s_waitcnt lgkmcnt(12)\n\t" \
  "v_cndmask_b32 v10, v10, v11, s[40:41]\n\t" \
  "v_mfma_f32_16x16x32_bf16 v[16:19], v[124:127], v[124:127], v[16:19]\n\t" \
  "ds_read_u16 v224, v224 offset:10528\n\t" \
  "v_cndmask_b32 v8, v8, v9, s[40:41]\n\t" \
  "s_waitcnt lgkmcnt(5)\n\t" \
  "v_cndmask_b32 v13, v8, v10, s[42:43]\n\t" \
  "v_mov_b32 v14, 0x1127c5ac\n\t" \
  "v_mfma_f32_16x16x32_bf16 v[20:23], v[196:199], v[124:127], v[20:23]\n\t" \
  "s_not_b64 vcc, s[52:53]\n\t" \
  "ds_read_u16_d16_hi v227, v227 offset:10528\n\t" \
  "v_mfma_f32_16x16x32_bf16 v[16:19], v[140:143], v[140:143], v[16:19]\n\t" \
  "ds_read_u16 v226, v226 offset:10528\n\t" \
  "s_waitcnt lgkmcnt(4)\n\t" \
  "v_fma_mix_f32 v221, |v147|, v221, v221 op_sel:[0,1,0] op_sel_hi:[0,1,1] clamp\n\t" \
  "v_fma_mix_f32 v220, |v146|, v220, v220 op_sel:[0,1,0] op_sel_hi:[0,1,1] clamp\n\t" \
  "v_mfma_f32_16x16x32_bf16 v[20:23], v[196:199], v[140:143], v[20:23]\n\t" \
  "v_fma_mix_f32 v223, |v145|, v223, v223 op_sel:[0,1,0] op_sel_hi:[0,1,1] clamp\n\t" \
  "v_fma_mix_f32 v222, |v144|, v222, v222 op_sel:[0,1,0] op_sel_hi:[0,1,1] clamp\n\t" \
  "v_mfma_f32_16x16x32_bf16 v[148:151], v[160:163], v[96:99], 0\n\t" \
  "s_waitcnt lgkmcnt(0)\n\t" \
  "v_or_b32 v155, v224, v225\n\t" \
  "v_or_b32 v154, v226, v227\n\t" \
  "v_mfma_f32_16x16x32_bf16 v[156:159], v[160:163], v[100:103], 0\n\t" \
  "v_cndmask_b32 v18, v18, v19, s[40:41]\n\t" \
  "v_cndmask_b32 v16, v16, v17, s[40:41]\n\t" \
  "v_cndmask_b32 v16, v16, v18, s[42:43]\n\t" \
  "v_cndmask_b32_dpp v12, v20, v12, vcc row_ror:8 row_mask:0xf bank_mask:0xf\n\t" \
  "v_mul_f32 v12, 0x3daaaaab, v12\n\t" \
  "v_mfma_f32_16x16x32_bf16 v[148:151], v[164:167], v[112:115], v[148:151]\n\t" \
  "v_cndmask_b32_dpp v13, v16, v13, vcc row_ror:8 row_mask:0xf bank_mask:0xf\n\t" \
  "v_fmamk_f32 v13, v13, 0x3e2aaaab, v14\n\t" \
  "v_fma_f32 v13, -v12, v12, v13\n\t" \
  "v_max_f32 v13, v14, v13\n\t" \
  "v_rsq_f32 v13, v13 div:2\n\t" \
  "v_mfma_f32_16x16x32_bf16 v[156:159], v[164:167], v[116:119], v[156:159]\n\t" \
  "s_mov_b64 exec, s[52:53]\n\t" \
  "v_mul_f32 v13, 0x08805555, v13\n\t" \
  "s_mov_b64 exec, -1\n\t" \
  "s_mov_b64 vcc, s[40:41]\n\t" \
  "v_cvt_pk_bf16_f32 v15, v13, v13\n\t" \
  "v_and_b32 v13, 0xffff0000, v15\n\t" \
  "v_mul_f32 v14, -v12, v13 div:2\n\t" \
  "v_cndmask_b32_sdwa v15, v15, v13, vcc dst_sel:DWORD dst_unused:UNUSED_PAD src0_sel:WORD_1 src1_sel:DWORD\n\t" \
  "s_or_b64 vcc, s[52:53], s[42:43]\n\t" \
  "v_and_b32 v12, 0xffff0000, v14\n\t" \
  "v_mfma_f32_16x16x32_bf16 v[148:151], v[232:235], v[128:131], v[148:151]\n\t" \
  "v_sub_f32 v14, v14, v12\n\t" \
  "v_cndmask_b32 v180, v15, v240, vcc\n\t" \
  "v_mfma_f32_16x16x32_bf16 v[156:159], v[232:235], v[132:135], v[156:159]\n\t" \
  "v_perm_b32 v14, v12, v14, s55\n\t" \
  "v_cndmask_b32_dpp v184, v15, v240, vcc row_ror:8 row_mask:0xf bank_mask:0xf\n\t" \
  "s_mov_b64 vcc, s[52:53]\n\t" \
  "v_cndmask_b32_dpp v186, v14, v240, vcc row_ror:8 row_mask:0xf bank_mask:0xf\n\t" \
  "v_cndmask_b32 v182, v14, v240, vcc\n\t" \
  "s_orn2_b64 vcc, s[52:53], s[42:43]\n\t" \
  "v_mfma_f32_16x16x16_bf16 v[4:7], v[196:197], v[186:187], 0\n\t" \
  "v_fma_f32 v145, |v145|, v223, v145 mul:4\n\t" \
  "v_fma_f32 v146, |v146|, v220, v146 mul:4\n\t" \
  "v_cndmask_b32 v181, v15, v240, vcc\n\t" \
  "v_mfma_f32_16x16x16_bf16 v[0:3], v[196:197], v[182:183], 0\n\t" \
  "v_cndmask_b32_dpp v185, v15, v240, vcc row_ror:8 row_mask:0xf bank_mask:0xf\n\t" \
  "v_fma_f32 v147, |v147|, v221, v147 mul:4\n\t" \
  "s_mov_b64 vcc, 0\n\t" \
  "v_mfma_f32_16x16x16_bf16 v[188:191], v[24:25], v[180:181], v[0:3]\n\t" \
  "v_fma_f32 v144, |v144|, v222, v144 mul:4\n\t" \
  "v_mfma_f32_16x16x16_bf16 v[176:179], v[26:27], v[180:181], v[0:3]\n\t" \
  "v_cvt_pknorm_i16_f32 v228, |v148|, |v149|\n\t" \
  "v_cvt_pk_bf16_f32 v147, v146, v147\n\t" \
  "s_nop 3\n\t" \
  "v_cvt_pk_bf16_f32 v105, v190, v191\n\t" \
  "v_mfma_f32_16x16x16_bf16 v[204:207], v[38:39], v[184:185], v[4:7]\n\t" \
  "v_mfma_f32_16x16x16_bf16 v[200:203], v[36:37], v[184:185], v[4:7]\n\t" \
  "v_cvt_pk_bf16_f32 v106, v176, v177\n\t" \
  "v_cvt_pk_bf16_f32 v107, v178, v179\n\t" \
  "v_cvt_pk_bf16_f32 v104, v188, v189\n\t" \
  "v_mfma_f32_16x16x16_bf16 v[196:199], v[28:29], v[180:181], v[0:3]\n\t" \
  "s_nop 3\n\t" \
  "v_cvt_pk_bf16_f32 v108, v200, v201\n\t" \
  "v_cvt_pk_bf16_f32 v109, v202, v203\n\t" \
  "v_mfma_f32_16x16x16_bf16 v[188:191], v[30:31], v[180:181], v[0:3]\n\t" \
  "v_cvt_pk_bf16_f32 v110, v204, v205\n\t" \
  "v_mfma_f32_16x16x32_bf16 v[160:163], v[208:211], v[104:107], 0\n\t" \
  "v_cvt_pk_bf16_f32 v120, v196, v197\n\t" \
  "v_cvt_pk_bf16_f32 v121, v198, v199\n\t" \
  "s_nop 3\n\t" \
  "v_cvt_pk_bf16_f32 v123, v190, v191\n\t" \
  "v_mfma_f32_16x16x16_bf16 v[200:203], v[42:43], v[184:185], v[4:7]\n\t" \
  "v_cvt_pk_bf16_f32 v111, v206, v207\n\t" \
  "v_mfma_f32_16x16x16_bf16 v[176:179], v[40:41], v[184:185], v[4:7]\n\t" \
  "v_cvt_pk_bf16_f32 v146, v144, v145\n\t" \
  "v_cvt_pk_bf16_f32 v122, v188, v189\n\t" \
  "v_lshlrev_b32_sdwa v229, 2, v228 dst_sel:DWORD dst_unused:UNUSED_PAD src0_sel:DWORD src1_sel:BYTE_3\n\t" \
  "v_mfma_f32_16x16x32_bf16 v[168:171], v[208:211], v[108:111], 0\n\t" \
  "ds_read_b64_tr_b16 v[208:209], v248 offset:43560\n\t" \
  "v_cvt_pknorm_i16_f32 v230, |v150|, |v151|\n\t" \
  "s_nop 1\n\t" \
  "v_cvt_pk_bf16_f32 v124, v176, v177\n\t" \
  "v_cvt_pk_bf16_f32 v125, v178, v179\n\t" \
  "v_mfma_f32_16x16x16_bf16 v[204:207], v[32:33], v[180:181], v[0:3]\n\t" \
  "ds_read_b64_tr_b16 v[210:211], v248 offset:44584\n\t" \
  "v_cvt_pk_bf16_f32 v126, v200, v201\n\t" \
  "v_cvt_pk_bf16_f32 v127, v202, v203\n\t" \
  "v_mfma_f32_16x16x16_bf16 v[196:199], v[34:35], v[180:181], v[0:3]\n\t" \
  "v_lshlrev_b32_sdwa v228, 2, v228 dst_sel:DWORD dst_unused:UNUSED_PAD src0_sel:DWORD src1_sel:BYTE_1\n\t" \
  "v_lshlrev_b32_sdwa v231, 2, v230 dst_sel:DWORD dst_unused:UNUSED_PAD src0_sel:DWORD src1_sel:BYTE_3\n\t" \
  "v_mfma_f32_16x16x32_bf16 v[160:163], v[212:215], v[120:123], v[160:163]\n\t" \
  "ds_read_b32 v229, v229 offset:10016\n\t" \
  "v_cvt_pk_bf16_f32 v136, v204, v205\n\t" \
  "v_cvt_pk_bf16_f32 v137, v206, v207\n\t" \
  "v_mfma_f32_16x16x16_bf16 v[188:191], v[44:45], v[184:185], v[4:7]\n\t" \
  "ds_read_b32 v228, v228 offset:10016\n\t" \
  "v_cvt_pk_bf16_f32 v138, v196, v197\n\t" \
  "v_cvt_pk_bf16_f32 v139, v198, v199\n\t" \
  "v_mfma_f32_16x16x16_bf16 v[176:179], v[46:47], v[184:185], v[4:7]\n\t" \
  "s_nop 3\n\t" \
  "v_cvt_pk_bf16_f32 v140, v188, v189\n\t" \
  "v_lshlrev_b32_sdwa v230, 2, v230 dst_sel:DWORD dst_unused:UNUSED_PAD src0_sel:DWORD src1_sel:BYTE_1\n\t" \
  "v_lshlrev_b32_sdwa v233, 1, v157 dst_sel:DWORD dst_unused:UNUSED_PAD src0_sel:DWORD src1_sel:WORD_1\n\t" \
  "v_mfma_f32_16x16x32_bf16 v[168:171], v[212:215], v[124:127], v[168:171]\n\t" \
  "ds_read_b64_tr_b16 v[214:215], v248 offset:47656\n\t" \
  "ds_read_b64_tr_b16 v[212:213], v248 offset:46632\n\t" \
  "v_cvt_pk_bf16_f32 v141, v190, v191\n\t" \
  "v_mfma_f32_16x16x32_bf16 v[160:163], v[216:219], v[136:139], v[160:163]\n\t" \
  "v_cvt_pk_bf16_f32 v142, v176, v177\n\t" \
  "v_cvt_pk_bf16_f32 v143, v178, v179\n\t" \
  "v_lshlrev_b32_sdwa v232, 1, v156 dst_sel:DWORD dst_unused:UNUSED_PAD src0_sel:DWORD src1_sel:WORD_1\n\t" \
  "v_lshlrev_b32_sdwa v235, 1, v159 dst_sel:DWORD dst_unused:UNUSED_PAD src0_sel:DWORD src1_sel:WORD_1\n\t" \
  "s_waitcnt lgkmcnt(4)\n\t" \
  "v_mfma_f32_16x16x32_bf16 v[168:171], v[216:219], v[140:143], v[168:171]\n\t" \
  "ds_read_b64_tr_b16 v[216:217], v248 offset:45608\n\t" \
  "ds_read_b64_tr_b16 v[218:219], v248 offset:48680\n\t" \
  "s_waitcnt vmcnt(2)\n\t" \
  "s_mov_b64 exec, 3\n\t" \
  "ds_add_rtn_u32 v246, v251, v250 offset:47136\n\t" \
  "s_mov_b64 exec, -1\n\t" \
  "v_mfma_f32_16x16x32_bf16 v[164:167], v[208:211], v[104:107], 0\n\t" \
  "ds_read_b32 v231, v231 offset:10016\n\t" \
  "v_lshlrev_b32_sdwa v234, 1, v158 dst_sel:DWORD dst_unused:UNUSED_PAD src0_sel:DWORD src1_sel:WORD_1\n\t" \
  "v_mfma_f32_16x16x32_bf16 v[172:175], v[208:211], v[108:111], 0\n\t" \
  "ds_read_b64_tr_b16 v[208:209], v248 offset:49192\n\t" \
  "ds_read_b64_tr_b16 v[210:211], v248 offset:50216\n\t" \
  "s_waitcnt lgkmcnt(6)\n\t" \
  "v_mfma_f32_16x16x32_bf16 v[164:167], v[212:215], v[120:123], v[164:167]\n\t" \
  "ds_read_b32 v230, v230 offset:10016\n\t" \
  "v_mfma_f32_16x16x32_bf16 v[172:175], v[212:215], v[124:127], v[172:175]\n\t" \
  "ds_read_b64_tr_b16 v[212:213], v248 offset:52264\n\t" \
  "ds_read_b64_tr_b16 v[214:215], v248 offset:53288\n\t" \
  "s_waitcnt lgkmcnt(7)\n\t" \
  "v_mfma_f32_16x16x32_bf16 v[164:167], v[216:219], v[136:139], v[164:167]\n\t" \
  "ds_read_u16_d16_hi v233, v233 offset:10528\n\t" \
  "v_mfma_f32_16x16x32_bf16 v[172:175], v[216:219], v[140:143], v[172:175]\n\t" \
  "ds_read_b64_tr_b16 v[216:217], v248 offset:51240\n\t" \
  "v_mfma_f32_4x4x4_16b_bf16 v[0:3], v[242:243], v[76:77], 0\n\t" \
  "v_cvt_pknorm_i16_f32 v222, |v160|, |v161|\n\t" \
  "v_mfma_f32_4x4x4_16b_bf16 v[4:7], v[242:243], v[78:79], 0\n\t" \
  "v_cvt_pknorm_i16_f32 v220, |v162|, |v163|\n\t" \
  "s_setprio 0\n\t" \
  "ds_read_b64_tr_b16 v[218:219], v248 offset:54312\n\t" \
  "v_mfma_f32_4x4x4_16b_bf16 v[8:11], v[242:243], v[84:85], 0\n\t" \
  "v_mfma_f32_4x4x4_16b_bf16 v[12:15], v[242:243], v[86:87], 0\n\t" \
  "v_lshlrev_b32_sdwa v221, 2, v220 dst_sel:DWORD dst_unused:UNUSED_PAD src0_sel:DWORD src1_sel:BYTE_3\n\t" \
  "v_lshlrev_b32_sdwa v220, 2, v220 dst_sel:DWORD dst_unused:UNUSED_PAD src0_sel:DWORD src1_sel:BYTE_1\n\t" \
  "ds_read_u16 v232, v232 offset:10528\n\t" \
  "ds_read_u16_d16_hi v235, v235 offset:10528\n\t" \
  "v_mfma_f32_4x4x4_16b_bf16 v[16:19], v[242:243], v[92:93], 0\n\t" \
  "v_mfma_f32_4x4x4_16b_bf16 v[20:23], v[242:243], v[94:95], 0\n\t" \
  "v_lshlrev_b32_sdwa v223, 2, v222 dst_sel:DWORD dst_unused:UNUSED_PAD src0_sel:DWORD src1_sel:BYTE_3\n\t" \
  "v_lshlrev_b32_sdwa v222, 2, v222 dst_sel:DWORD dst_unused:UNUSED_PAD src0_sel:DWORD src1_sel:BYTE_1\n\t" \
  "v_mfma_f32_4x4x4_16b_bf16 v[24:27], v[242:243], v[72:73], 0\n\t" \
  "v_mfma_f32_4x4x4_16b_bf16 v[28:31], v[242:243], v[74:75], 0\n\t" \
  "v_lshlrev_b32_sdwa v225, 1, v171 dst_sel:DWORD dst_unused:UNUSED_PAD src0_sel:DWORD src1_sel:WORD_1\n\t" \
  "v_lshlrev_b32_sdwa v224, 1, v170 dst_sel:DWORD dst_unused:UNUSED_PAD src0_sel:DWORD src1_sel:WORD_1\n\t" \
  "s_waitcnt lgkmcnt(7)\n\t" \
  "ds_read_u16 v234, v234 offset:10528\n\t" \
  "v_mfma_f32_4x4x4_16b_bf16 v[36:39], v[242:243], v[82:83], 0\n\t" \
  "v_mfma_f32_4x4x4_16b_bf16 v[32:35], v[242:243], v[80:81], 0\n\t" \
  "ds_read_b32 v221, v221 offset:10016\n\t" \
  "v_lshlrev_b32_sdwa v227, 1, v169 dst_sel:DWORD dst_unused:UNUSED_PAD src0_sel:DWORD src1_sel:WORD_1\n\t" \
  "v_lshlrev_b32_sdwa v226, 1, v168 dst_sel:DWORD dst_unused:UNUSED_PAD src0_sel:DWORD src1_sel:WORD_1\n\t" \
  "v_mfma_f32_4x4x4_16b_bf16 v[40:43], v[242:243], v[88:89], 0\n\t" \
  "v_mfma_f32_4x4x4_16b_bf16 v[44:47], v[242:243], v[90:91], 0\n\t" \
  "v_fma_mix_f32 v229, |v149|, v229, v229 op_sel:[0,1,0] op_sel_hi:[0,1,1] clamp\n\t" \
  "ds_read_b32 v220, v220 offset:10016\n\t" \
  "v_fma_mix_f32 v230, |v150|, v230, v230 op_sel:[0,1,0] op_sel_hi:[0,1,1] clamp\n\t" \
  "v_fma_mix_f32 v228, |v148|, v228, v228 op_sel:[0,1,0] op_sel_hi:[0,1,1] clamp\n\t" \
  "ds_read_b32 v223, v223 offset:10016\n\t" \
  "v_mfma_f32_4x4x4_16b_bf16 v[72:75], v[242:243], v[52:53], 0\n\t" \
  "v_mfma_f32_4x4x4_16b_bf16 v[76:79], v[242:243], v[54:55], 0\n\t" \
  "v_fma_mix_f32 v231, |v151|, v231, v231 op_sel:[0,1,0] op_sel_hi:[0,1,1] clamp\n\t" \
  "ds_read_b32 v222, v222 offset:10016\n\t" \
  "v_mfma_f32_4x4x4_16b_bf16 v[52:55], v[242:243], v[50:51], 0\n\t" \
  "s_waitcnt lgkmcnt(4)\n\t" \
  "v_mfma_f32_4x4x4_16b_bf16 v[48:51], v[242:243], v[48:49], 0\n\t" \
  "ds_read_u16_d16_hi v225, v225 offset:10528\n\t" \
  "v_or_b32 v156, v232, v233\n\t" \
  "v_mfma_f32_4x4x4_16b_bf16 v[80:83], v[242:243], v[60:61], 0\n\t" \
  "v_mfma_f32_4x4x4_16b_bf16 v[84:87], v[242:243], v[62:63], 0\n\t" \
  "v_or_b32 v157, v234, v235\n\t" \
  "v_fma_f32 v149, |v149|, v229, v149 mul:4\n\t" \
  "ds_read_u16 v224, v224 offset:10528\n\t" \
  "v_mfma_f32_4x4x4_16b_bf16 v[60:63], v[242:243], v[58:59], 0\n\t" \
  "v_mfma_f32_4x4x4_16b_bf16 v[56:59], v[242:243], v[56:57], 0\n\t" \
  "v_fma_f32 v148, |v148|, v228, v148 mul:4\n\t" \
  "v_fma_f32 v151, |v151|, v231, v151 mul:4\n\t" \
  "ds_read_u16_d16_hi v227, v227 offset:10528\n\t" \
  "v_mfma_f32_4x4x4_16b_bf16 v[88:91], v[242:243], v[68:69], 0\n\t" \
  "v_mfma_f32_4x4x4_16b_bf16 v[92:95], v[242:243], v[70:71], 0\n\t" \
  "v_fma_f32 v150, |v150|, v230, v150 mul:4\n\t" \
  "v_cvt_pk_bf16_f32 v148, v148, v149\n\t" \
  "v_mfma_f32_4x4x4_16b_bf16 v[68:71], v[242:243], v[66:67], 0\n\t" \
  "v_mfma_f32_4x4x4_16b_bf16 v[64:67], v[242:243], v[64:65], 0\n\t" \
  "ds_read_u16 v226, v226 offset:10528\n\t" \
  "v_cvt_pknorm_i16_f32 v230, |v166|, |v167|\n\t" \
  "v_lshlrev_b32_sdwa v231, 2, v230 dst_sel:DWORD dst_unused:UNUSED_PAD src0_sel:DWORD src1_sel:BYTE_3\n\t" \
  "v_mfma_f32_16x16x32_bf16 v[192:195], v[208:211], v[104:107], 0\n\t" \
  "v_lshlrev_b32_sdwa v230, 2, v230 dst_sel:DWORD dst_unused:UNUSED_PAD src0_sel:DWORD src1_sel:BYTE_1\n\t" \
  "v_cvt_pknorm_i16_f32 v228, |v164|, |v165|\n\t" \
  "v_lshlrev_b32_sdwa v229, 2, v228 dst_sel:DWORD dst_unused:UNUSED_PAD src0_sel:DWORD src1_sel:BYTE_3\n\t" \
  "v_cvt_pk_bf16_f32 v149, v150, v151\n\t" \
  "v_mfma_f32_16x16x32_bf16 v[200:203], v[208:211], v[108:111], 0\n\t" \
  "s_add_u32 s27, s27, s26\n\t" \
  "v_lshlrev_b32_sdwa v233, 1, v173 dst_sel:DWORD dst_unused:UNUSED_PAD src0_sel:DWORD src1_sel:WORD_1\n\t" \
  "v_mfma_f32_16x16x32_bf16 v[184:187], v[208:211], v[100:103], 0\n\t" \
  "v_mfma_f32_16x16x32_bf16 v[176:179], v[208:211], v[96:99], 0\n\t" \
  "v_lshlrev_b32_sdwa v228, 2, v228 dst_sel:DWORD dst_unused:UNUSED_PAD src0_sel:DWORD src1_sel:BYTE_1\n\t" \
  "v_lshlrev_b32_sdwa v232, 1, v172 dst_sel:DWORD dst_unused:UNUSED_PAD src0_sel:DWORD src1_sel:WORD_1\n\t" \
  "ds_read_b64_tr_b16 v[208:209], v248 offset:49704\n\t" \
  "v_mfma_f32_16x16x32_bf16 v[176:179], v[212:215], v[112:115], v[176:179]\n\t" \
  "v_lshlrev_b32_sdwa v235, 1, v175 dst_sel:DWORD dst_unused:UNUSED_PAD src0_sel:DWORD src1_sel:WORD_1\n\t" \
  "v_lshlrev_b32_sdwa v234, 1, v174 dst_sel:DWORD dst_unused:UNUSED_PAD src0_sel:DWORD src1_sel:WORD_1\n\t" \
  "ds_read_b64_tr_b16 v[210:211], v248 offset:50728\n\t" \
  "s_waitcnt lgkmcnt(6)\n\t" \
  "ds_read_b32 v229, v229 offset:10016\n\t" \
  "v_mfma_f32_16x16x32_bf16 v[184:187], v[212:215], v[116:119], v[184:187]\n\t" \
  "v_fma_mix_f32 v220, |v162|, v220, v220 op_sel:[0,1,0] op_sel_hi:[0,1,1] clamp\n\t" \
  "s_mov_b64 exec, s[30:31]\n\t" \
  "buffer_load_dword v237, v254, s[8:11], s27 offen\n\t" \
  "v_mfma_f32_16x16x32_bf16 v[192:195], v[212:215], v[120:123], v[192:195]\n\t" \
  "s_mov_b64 exec, -1\n\t" \
  "v_fma_mix_f32 v223, |v161|, v223, v223 op_sel:[0,1,0] op_sel_hi:[0,1,1] clamp\n\t" \
  "v_fma_mix_f32 v221, |v163|, v221, v221 op_sel:[0,1,0] op_sel_hi:[0,1,1] clamp\n\t" \
  "ds_read_b32 v228, v228 offset:10016\n\t" \
  "ds_read_b32 v231, v231 offset:10016\n\t" \
  "v_mfma_f32_16x16x32_bf16 v[200:203], v[212:215], v[124:127], v[200:203]\n\t" \
  "v_fma_mix_f32 v222, |v160|, v222, v222 op_sel:[0,1,0] op_sel_hi:[0,1,1] clamp\n\t" \
  "s_waitcnt lgkmcnt(5)\n\t" \
  "ds_read_b64_tr_b16 v[212:213], v248 offset:52776\n\t" \
  "ds_read_b32 v230, v230 offset:10016\n\t" \
  "v_mfma_f32_16x16x32_bf16 v[176:179], v[216:219], v[128:131], v[176:179]\n\t" \
  "v_or_b32 v171, v224, v225\n\t" \
  "v_or_b32 v170, v226, v227\n\t" \
  "ds_read_b64_tr_b16 v[214:215], v248 offset:53800\n\t" \
  "v_mfma_f32_16x16x32_bf16 v[184:187], v[216:219], v[132:135], v[184:187]\n\t" \
  "v_fma_f32 v163, |v163|, v221, v163 mul:4\n\t" \
  "v_fma_f32 v162, |v162|, v220, v162 mul:4\n\t" \
  "v_mfma_f32_16x16x32_bf16 v[192:195], v[216:219], v[136:139], v[192:195]\n\t" \
  "v_fma_f32 v161, |v161|, v223, v161 mul:4\n\t" \
  "v_fma_f32 v160, |v160|, v222, v160 mul:4\n\t" \
  "ds_read_u16 v232, v232 offset:10528\n\t" \
  "ds_read_u16_d16_hi v233, v233 offset:10528\n\t" \
  "v_mfma_f32_16x16x32_bf16 v[200:203], v[216:219], v[140:143], v[200:203]\n\t" \
  "v_cvt_pk_bf16_f32 v163, v162, v163\n\t" \
  "v_cvt_pk_bf16_f32 v162, v160, v161\n\t" \
  "ds_read_b64_tr_b16 v[216:217], v248 offset:51752\n\t" \
  "s_waitcnt lgkmcnt(9)\n\t" \
  "v_mfma_f32_16x16x32_bf16 v[180:183], v[208:211], v[96:99], 0\n\t" \
  "v_cvt_pknorm_i16_f32 v220, |v178|, |v179|\n\t" \
  "v_cvt_pknorm_i16_f32 v222, |v176|, |v177|\n\t" \
  "s_waitcnt vmcnt(1)\n\t" \
  "ds_read_b64_tr_b16 v[218:219], v248 offset:54824\n\t" \
  "v_mfma_f32_16x16x32_bf16 v[188:191], v[208:211], v[100:103], 0\n\t" \
  "v_lshlrev_b32_sdwa v221, 2, v220 dst_sel:DWORD dst_unused:UNUSED_PAD src0_sel:DWORD src1_sel:BYTE_3\n\t" \
  "v_lshlrev_b32_sdwa v220, 2, v220 dst_sel:DWORD dst_unused:UNUSED_PAD src0_sel:DWORD src1_sel:BYTE_1\n\t" \
  "s_mov_b64 exec, 3\n\t" \
  "ds_add_rtn_u32 v246, v251, v250 offset:47144\n\t" \
  "v_mfma_f32_16x16x32_bf16 v[196:199], v[208:211], v[104:107], 0\n\t" \
  "s_mov_b64 exec, -1\n\t" \
  "v_lshlrev_b32_sdwa v223, 2, v222 dst_sel:DWORD dst_unused:UNUSED_PAD src0_sel:DWORD src1_sel:BYTE_3\n\t" \
  "v_lshlrev_b32_sdwa v222, 2, v222 dst_sel:DWORD dst_unused:UNUSED_PAD src0_sel:DWORD src1_sel:BYTE_1\n\t" \
  "ds_read_u16_d16_hi v235, v235 offset:10528\n\t" \
  "v_mfma_f32_16x16x32_bf16 v[204:207], v[208:211], v[108:111], 0\n\t" \
  "s_waitcnt lgkmcnt(1)\n\t" \
  "v_lshlrev_b32_sdwa v225, 1, v187 dst_sel:DWORD dst_unused:UNUSED_PAD src0_sel:DWORD src1_sel:WORD_1\n\t" \
  "v_readfirstlane_b32 s4, v246\n\t" \
  "ds_read_u16 v234, v234 offset:10528\n\t" \
  "v_mfma_f32_16x16x32_bf16 v[180:183], v[212:215], v[112:115], v[180:183]\n\t" \
  "s_cmpk_lt_u32 s4, 3072\n\t" \
  "v_lshlrev_b32_sdwa v224, 1, v186 dst_sel:DWORD dst_unused:UNUSED_PAD src0_sel:DWORD src1_sel:WORD_1\n\t" \
  "s_cbranch_scc1 .Lcnx_w0_2\n\t" \
  ".Lcnx_g0_2:\n\t" \
  "ds_read_b32 v221, v221 offset:10016\n\t" \
  "v_mfma_f32_16x16x32_bf16 v[188:191], v[212:215], v[116:119], v[188:191]\n\t" \
  "v_lshlrev_b32_sdwa v227, 1, v185 dst_sel:DWORD dst_unused:UNUSED_PAD src0_sel:DWORD src1_sel:WORD_1\n\t" \
  "v_lshlrev_b32_sdwa v226, 1, v184 dst_sel:DWORD dst_unused:UNUSED_PAD src0_sel:DWORD src1_sel:WORD_1\n\t" \
  "ds_read_b64_tr_b16 v[208:209], v249 offset:24600\n\t" \
  "ds_read_b32 v220, v220 offset:10016\n\t" \
  "v_mfma_f32_16x16x32_bf16 v[196:199], v[212:215], v[120:123], v[196:199]\n\t" \
  "v_fma_mix_f32 v229, |v165|, v229, v229 op_sel:[0,1,0] op_sel_hi:[0,1,1] clamp\n\t" \
  "v_fma_mix_f32 v228, |v164|, v228, v228 op_sel:[0,1,0] op_sel_hi:[0,1,1] clamp\n\t" \
  "ds_read_b32 v223, v223 offset:10016\n\t" \
  "ds_read_b64_tr_b16 v[210:211], v249 offset:26136\n\t" \
  "v_mfma_f32_16x16x32_bf16 v[204:207], v[212:215], v[124:127], v[204:207]\n\t" \
  "v_fma_mix_f32 v230, |v166|, v230, v230 op_sel:[0,1,0] op_sel_hi:[0,1,1] clamp\n\t" \
  "v_fma_mix_f32 v231, |v167|, v231, v231 op_sel:[0,1,0] op_sel_hi:[0,1,1] clamp\n\t" \
  "s_waitcnt lgkmcnt(5)\n\t" \
  "ds_read_b64_tr_b16 v[212:213], v249 offset:24608\n\t" \
  "ds_read_b32 v222, v222 offset:10016\n\t" \
  "v_mfma_f32_16x16x32_bf16 v[180:183], v[216:219], v[128:131], v[180:183]\n\t" \
  "s_mov_b32 m0, s46\n\t" \
  "v_or_b32 v172, v232, v233\n\t" \
  "v_or_b32 v173, v234, v235\n\t" \
  "s_mov_b32 exec_hi, 0xffff\n\t" \
  "buffer_load_dwordx4 v252, s[16:19], s33 offen lds\n\t" \
  "v_mfma_f32_16x16x32_bf16 v[188:191], v[216:219], v[132:135], v[188:191]\n\t" \
  "s_mov_b32 exec_hi, -1\n\t" \
  "v_fma_f32 v164, |v164|, v228, v164 mul:4\n\t" \
  "v_fma_f32 v165, |v165|, v229, v165 mul:4\n\t" \
  "s_add_u32 s33, s33, s32\n\t" \
  "ds_read_b64_tr_b16 v[214:215], v249 offset:26144\n\t" \
  "ds_read_u16_d16_hi v225, v225 offset:10528\n\t" \
  "v_mfma_f32_16x16x32_bf16 v[196:199], v[216:219], v[136:139], v[196:199]\n\t" \
  "v_fma_f32 v167, |v167|, v231, v167 mul:4\n\t" \
  "v_fma_f32 v166, |v166|, v230, v166 mul:4\n\t" \
  "ds_read_u16_d16_hi v227, v227 offset:10528\n\t" \
  "s_waitcnt lgkmcnt(5)\n\t" \
  "v_mfma_f32_16x16x32_bf16 v[0:3], v[208:211], v[146:149], v[0:3]\n\t" \
  "v_cvt_pknorm_i16_f32 v228, |v194|, |v195|\n\t" \
  "v_cvt_pknorm_i16_f32 v230, |v192|, |v193|\n\t" \
  "v_mfma_f32_16x16x32_bf16 v[204:207], v[216:219], v[140:143], v[204:207]\n\t" \
  "v_cvt_pk_bf16_f32 v164, v164, v165\n\t" \
  "v_cvt_pk_bf16_f32 v165, v166, v167\n\t" \
  "ds_read_b64_tr_b16 v[216:217], v249 offset:24664\n\t" \
  "ds_read_b64_tr_b16 v[218:219], v249 offset:26200\n\t" \
  "v_mfma_f32_16x16x32_bf16 v[24:27], v[208:211], v[154:157], v[24:27]\n\t" \
  "v_lshlrev_b32_sdwa v229, 2, v228 dst_sel:DWORD dst_unused:UNUSED_PAD src0_sel:DWORD src1_sel:BYTE_3\n\t" \
  "v_lshlrev_b32_sdwa v228, 2, v228 dst_sel:DWORD dst_unused:UNUSED_PAD src0_sel:DWORD src1_sel:BYTE_1\n\t" \
  "ds_read_u16 v224, v224 offset:10528\n\t" \
  "v_mfma_f32_16x16x32_bf16 v[48:51], v[208:211], v[162:165], v[48:51]\n\t" \
  "v_lshlrev_b32_sdwa v231, 2, v230 dst_sel:DWORD dst_unused:UNUSED_PAD src0_sel:DWORD src1_sel:BYTE_3\n\t" \
  "v_lshlrev_b32_sdwa v230, 2, v230 dst_sel:DWORD dst_unused:UNUSED_PAD src0_sel:DWORD src1_sel:BYTE_1\n\t" \
  "v_mfma_f32_16x16x32_bf16 v[72:75], v[208:211], v[170:173], v[72:75]\n\t" \
  "s_add_u32 m0, s46, s35\n\t" \
  "v_lshlrev_b32_sdwa v233, 1, v203 dst_sel:DWORD dst_unused:UNUSED_PAD src0_sel:DWORD src1_sel:WORD_1\n\t" \
  "s_waitcnt lgkmcnt(5)\n\t" \
  "s_mov_b32 exec_hi, 0xffff\n\t" \
  "buffer_load_dwordx4 v252, s[16:19], s34 offen lds\n\t" \
  "v_mfma_f32_16x16x32_bf16 v[4:7], v[212:215], v[146:149], v[4:7]\n\t" \
  "s_mov_b32 exec_hi, -1\n\t" \
  "v_lshlrev_b32_sdwa v232, 1, v202 dst_sel:DWORD dst_unused:UNUSED_PAD src0_sel:DWORD src1_sel:WORD_1\n\t" \
  "v_lshlrev_b32_sdwa v235, 1, v201 dst_sel:DWORD dst_unused:UNUSED_PAD src0_sel:DWORD src1_sel:WORD_1\n\t" \
  "ds_read_b64_tr_b16 v[208:209], v249 offset:24672\n\t" \
  "ds_read_u16 v226, v226 offset:10528\n\t" \
  "v_mfma_f32_16x16x32_bf16 v[28:31], v[212:215], v[154:157], v[28:31]\n\t" \
  "v_lshlrev_b32_sdwa v234, 1, v200 dst_sel:DWORD dst_unused:UNUSED_PAD src0_sel:DWORD src1_sel:WORD_1\n\t" \
  "s_add_u32 s34, s34, s32\n\t" \
  "ds_read_b64_tr_b16 v[210:211], v249 offset:26208\n\t" \
  "ds_read_b32 v229, v229 offset:10016\n\t" \
  "v_mfma_f32_16x16x32_bf16 v[52:55], v[212:215], v[162:165], v[52:55]\n\t" \
  "v_fma_mix_f32 v221, |v179|, v221, v221 op_sel:[0,1,0] op_sel_hi:[0,1,1] clamp\n\t" \
  "v_fma_mix_f32 v220, |v178|, v220, v220 op_sel:[0,1,0] op_sel_hi:[0,1,1] clamp\n\t" \
  "ds_read_b32 v228, v228 offset:10016\n\t" \
  "ds_read_b32 v231, v231 offset:10016\n\t" \
  "v_mfma_f32_16x16x32_bf16 v[76:79], v[212:215], v[170:173], v[76:79]\n\t" \
  "v_fma_mix_f32 v223, |v177|, v223, v223 op_sel:[0,1,0] op_sel_hi:[0,1,1] clamp\n\t" \
  "v_fma_mix_f32 v222, |v176|, v222, v222 op_sel:[0,1,0] op_sel_hi:[0,1,1] clamp\n\t" \
  "s_waitcnt lgkmcnt(4)\n\t" \
  "ds_read_b64_tr_b16 v[212:213], v249 offset:24728\n\t" \
  "ds_read_b32 v230, v230 offset:10016\n\t" \
  "v_mfma_f32_16x16x32_bf16 v[8:11], v[216:219], v[146:149], v[8:11]\n\t" \
  "v_or_b32 v187, v224, v225\n\t" \
  "v_or_b32 v186, v226, v227\n\t" \
  "ds_read_b64_tr_b16 v[214:215], v249 offset:26264\n\t" \
  "v_mfma_f32_16x16x32_bf16 v[32:35], v[216:219], v[154:157], v[32:35]\n\t" \
  "v_fma_f32 v179, |v179|, v221, v179 mul:4\n\t" \
  "v_fma_f32 v178, |v178|, v220, v178 mul:4\n\t" \
  "ds_read_u16_d16_hi v233, v233 offset:10528\n\t" \
  "v_mfma_f32_16x16x32_bf16 v[56:59], v[216:219], v[162:165], v[56:59]\n\t" \
  "s_add_u32 m0, s25, 0\n\t" \
  "v_fma_f32 v177, |v177|, v223, v177 mul:4\n\t" \
  "v_fma_f32 v176, |v176|, v222, v176 mul:4\n\t" \
  "s_add_u32 s24, s24, s26\n\t" \
  "s_sub_u32 s24, s24, s64\n\t" \
  "ds_read_u16 v232, v232 offset:10528\n\t" \
  "v_mfma_f32_16x16x32_bf16 v[80:83], v[216:219], v[170:173], v[80:83]\n\t" \
  "v_cvt_pk_bf16_f32 v179, v178, v179\n\t" \
  "v_cvt_pk_bf16_f32 v178, v176, v177\n\t" \
  "s_waitcnt lgkmcnt(8)\n\t" \
  "ds_read_b64_tr_b16 v[216:217], v249 offset:24736\n\t" \
  "buffer_load_dwordx4 v254, s[8:11], s24 offen nt lds\n\t" \
  "v_mfma_f32_16x16x32_bf16 v[12:15], v[208:211], v[146:149], v[12:15]\n\t" \
  "v_cvt_pknorm_i16_f32 v220, |v180|, |v181|\n\t" \
  "v_cvt_pknorm_i16_f32 v222, |v182|, |v183|\n\t" \
  "ds_read_b64_tr_b16 v[218:219], v249 offset:26272\n\t" \
  "v_mfma_f32_16x16x32_bf16 v[36:39], v[208:211], v[154:157], v[36:39]\n\t" \
  "v_lshlrev_b32_sdwa v221, 2, v220 dst_sel:DWORD dst_unused:UNUSED_PAD src0_sel:DWORD src1_sel:BYTE_3\n\t" \
  "v_lshlrev_b32_sdwa v220, 2, v220 dst_sel:DWORD dst_unused:UNUSED_PAD src0_sel:DWORD src1_sel:BYTE_1\n\t" \
  "ds_read_u16_d16_hi v235, v235 offset:10528\n\t" \
  "v_mfma_f32_16x16x32_bf16 v[60:63], v[208:211], v[162:165], v[60:63]\n\t" \
  "v_lshlrev_b32_sdwa v223, 2, v222 dst_sel:DWORD dst_unused:UNUSED_PAD src0_sel:DWORD src1_sel:BYTE_3\n\t" \
  "v_lshlrev_b32_sdwa v222, 2, v222 dst_sel:DWORD dst_unused:UNUSED_PAD src0_sel:DWORD src1_sel:BYTE_1\n\t" \
  "ds_read_u16 v234, v234 offset:10528\n\t" \
  "v_mfma_f32_16x16x32_bf16 v[84:87], v[208:211], v[170:173], v[84:87]\n\t" \
  "v_lshlrev_b32_sdwa v225, 1, v189 dst_sel:DWORD dst_unused:UNUSED_PAD src0_sel:DWORD src1_sel:WORD_1\n\t" \
  "v_lshlrev_b32_sdwa v224, 1, v188 dst_sel:DWORD dst_unused:UNUSED_PAD src0_sel:DWORD src1_sel:WORD_1\n\t" \
  "ds_read_b64_tr_b16 v[208:209], v248 offset:36904\n\t" \
  "s_waitcnt lgkmcnt(7)\n\t" \
  "v_mfma_f32_16x16x32_bf16 v[16:19], v[212:215], v[146:149], v[16:19]\n\t" \
  "v_lshlrev_b32_sdwa v227, 1, v191 dst_sel:DWORD dst_unused:UNUSED_PAD src0_sel:DWORD src1_sel:WORD_1\n\t" \
  "v_lshlrev_b32_sdwa v226, 1, v190 dst_sel:DWORD dst_unused:UNUSED_PAD src0_sel:DWORD src1_sel:WORD_1\n\t" \
  "ds_read_b64_tr_b16 v[210:211], v248 offset:37928\n\t" \
  "ds_read_b32 v221, v221 offset:10016\n\t" \
  "v_mfma_f32_16x16x32_bf16 v[40:43], v[212:215], v[154:157], v[40:43]\n\t" \
  "s_add_u32 s27, s27, s26\n\t" \
  "v_fma_mix_f32 v229, |v195|, v229, v229 op_sel:[0,1,0] op_sel_hi:[0,1,1] clamp\n\t" \
  "s_mov_b64 exec, s[30:31]\n\t" \
  "buffer_load_dword v237, v254, s[8:11], s27 offen\n\t" \
  "v_mfma_f32_16x16x32_bf16 v[64:67], v[212:215], v[162:165], v[64:67]\n\t" \
  "s_mov_b64 exec, -1\n\t" \
  "v_fma_mix_f32 v228, |v194|, v228, v228 op_sel:[0,1,0] op_sel_hi:[0,1,1] clamp\n\t" \
  "v_fma_mix_f32 v230, |v192|, v230, v230 op_sel:[0,1,0] op_sel_hi:[0,1,1] clamp\n\t" \
  "ds_read_b32 v220, v220 offset:10016\n\t" \
  "ds_read_b32 v223, v223 offset:10016\n\t" \
  "v_mfma_f32_16x16x32_bf16 v[88:91], v[212:215], v[170:173], v[88:91]\n\t" \
  "v_fma_mix_f32 v231, |v193|, v231, v231 op_sel:[0,1,0] op_sel_hi:[0,1,1] clamp\n\t" \
  "s_waitcnt lgkmcnt(5)\n\t" \
  "ds_read_b64_tr_b16 v[212:213], v248 offset:39976\n\t" \
  "ds_read_b32 v222, v222 offset:10016\n\t" \
  "v_mfma_f32_16x16x32_bf16 v[20:23], v[216:219], v[146:149], v[20:23]\n\t" \
  "v_or_b32 v203, v232, v233\n\t" \
  "v_or_b32 v202, v234, v235\n\t" \
  "ds_read_b64_tr_b16 v[214:215], v248 offset:41000\n\t" \
  "v_mfma_f32_16x16x32_bf16 v[44:47], v[216:219], v[154:157], v[44:47]\n\t" \
  "v_fma_f32 v195, |v195|, v229, v195 mul:4\n\t" \
  "v_fma_f32 v194, |v194|, v228, v194 mul:4\n\t" \
  "ds_read_u16_d16_hi v225, v225 offset:10528\n\t" \
  "v_mfma_f32_16x16x32_bf16 v[68:71], v[216:219], v[162:165], v[68:71]\n\t" \
  "v_fma_f32 v193, |v193|, v231, v193 mul:4\n\t" \
  "v_fma_f32 v192, |v192|, v230, v192 mul:4\n\t" \
  "ds_read_u16_d16_hi v227, v227 offset:10528\n\t" \
  "ds_read_u16 v224, v224 offset:10528\n\t" \
  "v_mfma_f32_16x16x32_bf16 v[92:95], v[216:219], v[170:173], v[92:95]\n\t" \
  "v_cvt_pk_bf16_f32 v195, v194, v195\n\t" \
  "v_cvt_pk_bf16_f32 v194, v192, v193\n\t" \
  "s_waitcnt lgkmcnt(9)\n\t" \
  "ds_read_b64_tr_b16 v[216:217], v248 offset:38952\n\t" \
  "v_mfma_f32_16x16x32_bf16 v[144:147], v[208:211], v[96:99], 0\n\t" \
  "v_cvt_pknorm_i16_f32 v228, |v196|, |v197|\n\t" \
  "v_cvt_pknorm_i16_f32 v230, |v198|, |v199|\n\t" \
  "ds_read_b64_tr_b16 v[218:219], v248 offset:42024\n\t" \
  "v_mfma_f32_16x16x32_bf16 v[152:155], v[208:211], v[100:103], 0\n\t" \
  "v_lshlrev_b32_sdwa v229, 2, v228 dst_sel:DWORD dst_unused:UNUSED_PAD src0_sel:DWORD src1_sel:BYTE_3\n\t" \
  "v_lshlrev_b32_sdwa v228, 2, v228 dst_sel:DWORD dst_unused:UNUSED_PAD src0_sel:DWORD src1_sel:BYTE_1\n\t" \
  "v_mfma_f32_16x16x32_bf16 v[160:163], v[208:211], v[104:107], 0\n\t" \
  "v_lshlrev_b32_sdwa v231, 2, v230 dst_sel:DWORD dst_unused:UNUSED_PAD src0_sel:DWORD src1_sel:BYTE_3\n\t" \
  "v_lshlrev_b32_sdwa v230, 2, v230 dst_sel:DWORD dst_unused:UNUSED_PAD src0_sel:DWORD src1_sel:BYTE_1\n\t" \
  "ds_read_u16 v226, v226 offset:10528\n\t" \
  "v_mfma_f32_16x16x32_bf16 v[168:171], v[208:211], v[108:111], 0\n\t" \
  "s_waitcnt lgkmcnt(6)\n\t" \
  "v_lshlrev_b32_sdwa v233, 1, v205 dst_sel:DWORD dst_unused:UNUSED_PAD src0_sel:DWORD src1_sel:WORD_1\n\t" \
  "v_lshlrev_b32_sdwa v232, 1, v204 dst_sel:DWORD dst_unused:UNUSED_PAD src0_sel:DWORD src1_sel:WORD_1\n\t" \
  "ds_read_b64_tr_b16 v[208:209], v248 offset:37416\n\t" \
  "v_mfma_f32_16x16x32_bf16 v[144:147], v[212:215], v[112:115], v[144:147]\n\t" \
  "v_lshlrev_b32_sdwa v235, 1, v207 dst_sel:DWORD dst_unused:UNUSED_PAD src0_sel:DWORD src1_sel:WORD_1\n\t" \
  "v_lshlrev_b32_sdwa v234, 1, v206 dst_sel:DWORD dst_unused:UNUSED_PAD src0_sel:DWORD src1_sel:WORD_1\n\t" \
  "ds_read_b64_tr_b16 v[210:211], v248 offset:38440\n\t" \
  "ds_read_b32 v229, v229 offset:10016\n\t" \
  "v_mfma_f32_16x16x32_bf16 v[152:155], v[212:215], v[116:119], v[152:155]\n\t" \
  "v_fma_mix_f32 v221, |v181|, v221, v221 op_sel:[0,1,0] op_sel_hi:[0,1,1] clamp\n\t" \
  "ds_read_b32 v231, v231 offset:10016\n\t" \
  "v_mfma_f32_16x16x32_bf16 v[160:163], v[212:215], v[120:123], v[160:163]\n\t" \
  "v_fma_mix_f32 v223, |v183|, v223, v223 op_sel:[0,1,0] op_sel_hi:[0,1,1] clamp\n\t" \
  "v_fma_mix_f32 v220, |v180|, v220, v220 op_sel:[0,1,0] op_sel_hi:[0,1,1] clamp\n\t" \
  "ds_read_b32 v228, v228 offset:10016\n\t" \
  "ds_read_b32 v230, v230 offset:10016\n\t" \
  "v_mfma_f32_16x16x32_bf16 v[168:171], v[212:215], v[124:127], v[168:171]\n\t" \
  "v_fma_mix_f32 v222, |v182|, v222, v222 op_sel:[0,1,0] op_sel_hi:[0,1,1] clamp\n\t" \
  "s_waitcnt lgkmcnt(6)\n\t" \
  "ds_read_b64_tr_b16 v[212:213], v248 offset:40488\n\t" \
  "ds_read_u16_d16_hi v233, v233 offset:10528\n\t" \
  "v_mfma_f32_16x16x32_bf16 v[144:147], v[216:219], v[128:131], v[144:147]\n\t" \
  "v_or_b32 v188, v224, v225\n\t" \
  "v_or_b32 v189, v226, v227\n\t" \
  "ds_read_b64_tr_b16 v[214:215], v248 offset:41512\n\t" \
  "v_mfma_f32_16x16x32_bf16 v[152:155], v[216:219], v[132:135], v[152:155]\n\t" \
  "v_fma_f32 v181, |v181|, v221, v181 mul:4\n\t" \
  "v_fma_f32 v180, |v180|, v220, v180 mul:4\n\t" \
  "ds_read_u16 v232, v232 offset:10528\n\t" \
  "v_mfma_f32_16x16x32_bf16 v[160:163], v[216:219], v[136:139], v[160:163]\n\t" \
  "v_fma_f32 v183, |v183|, v223, v183 mul:4\n\t" \
  "v_fma_f32 v182, |v182|, v222, v182 mul:4\n\t" \
  "ds_read_u16_d16_hi v235, v235 offset:10528\n\t" \
  "v_mfma_f32_16x16x32_bf16 v[168:171], v[216:219], v[140:143], v[168:171]\n\t" \
  "v_cvt_pk_bf16_f32 v180, v180, v181\n\t" \
  "v_cvt_pk_bf16_f32 v181, v182, v183\n\t" \
  "ds_read_b64_tr_b16 v[216:217], v248 offset:39464\n\t" \
  "s_waitcnt lgkmcnt(10)\n\t" \
  "v_mfma_f32_16x16x32_bf16 v[148:151], v[208:211], v[96:99], 0\n\t" \
  "v_cvt_pknorm_i16_f32 v220, |v146|, |v147|\n\t" \
  "v_cvt_pknorm_i16_f32 v222, |v144|, |v145|\n\t" \
  "s_nop 0\n\t" \
  "s_waitcnt vmcnt(2)\n\t" \
  "ds_read_b64_tr_b16 v[218:219], v248 offset:42536\n\t" \
  "v_mfma_f32_16x16x32_bf16 v[156:159], v[208:211], v[100:103], 0\n\t" \
  "s_waitcnt lgkmcnt(4)\n\t" \
  "v_lshlrev_b32_sdwa v221, 2, v220 dst_sel:DWORD dst_unused:UNUSED_PAD src0_sel:DWORD src1_sel:BYTE_3\n\t" \
  "v_lshlrev_b32_sdwa v220, 2, v220 dst_sel:DWORD dst_unused:UNUSED_PAD src0_sel:DWORD src1_sel:BYTE_1\n\t" \
  "s_mov_b64 exec, 3\n\t" \
  "ds_add_rtn_u32 v246, v251, v250 offset:47152\n\t" \
  "v_mfma_f32_16x16x32_bf16 v[164:167], v[208:211], v[104:107], 0\n\t" \
  "s_mov_b64 exec, -1\n\t" \
  "v_lshlrev_b32_sdwa v223, 2, v222 dst_sel:DWORD dst_unused:UNUSED_PAD src0_sel:DWORD src1_sel:BYTE_3\n\t" \
  "v_lshlrev_b32_sdwa v222, 2, v222 dst_sel:DWORD dst_unused:UNUSED_PAD src0_sel:DWORD src1_sel:BYTE_1\n\t" \
  "s_setprio 2\n\t" \
  "s_setprio 0\n\t" \
  "ds_read_u16 v234, v234 offset:10528\n\t" \
  "v_mfma_f32_16x16x32_bf16 v[156:159], v[212:215], v[116:119], v[156:159]\n\t" \
  "v_lshlrev_b32_sdwa v226, 1, v152 dst_sel:DWORD dst_unused:UNUSED_PAD src0_sel:DWORD src1_sel:WORD_1\n\t" \
  "v_lshlrev_b32_sdwa v227, 1, v153 dst_sel:DWORD dst_unused:UNUSED_PAD src0_sel:DWORD src1_sel:WORD_1\n\t" \
  "ds_read_b32 v223, v223 offset:10016\n\t" \
  "v_mfma_f32_16x16x32_bf16 v[148:151], v[212:215], v[112:115], v[148:151]\n\t" \
  "s_waitcnt lgkmcnt(2)\n\t" \
  "v_lshlrev_b32_sdwa v225, 1, v155 dst_sel:DWORD dst_unused:UNUSED_PAD src0_sel:DWORD src1_sel:WORD_1\n\t" \
  "v_readfirstlane_b32 s4, v246\n\t" \
  "ds_read_b32 v221, v221 offset:10016\n\t" \
  "v_mfma_f32_16x16x32_bf16 v[172:175], v[208:211], v[108:111], 0\n\t" \
  "s_cmpk_lt_u32 s4, 3072\n\t" \
  "v_lshlrev_b32_sdwa v224, 1, v154 dst_sel:DWORD dst_unused:UNUSED_PAD src0_sel:DWORD src1_sel:WORD_1\n\t" \
  "s_cbranch_scc1 .Lcnx_w0_3\n\t" \
  ".Lcnx_g0_3:\n\t" \
  "ds_read_b32 v220, v220 offset:10016\n\t" \
  "v_mfma_f32_16x16x32_bf16 v[164:167], v[212:215], v[120:123], v[164:167]\n\t" \
  "v_fma_mix_f32 v228, |v196|, v228, v228 op_sel:[0,1,0] op_sel_hi:[0,1,1] clamp\n\t" \
  "v_fma_mix_f32 v229, |v197|, v229, v229 op_sel:[0,1,0] op_sel_hi:[0,1,1] clamp\n\t" \
  "ds_read_b64_tr_b16 v[210:211], v249 offset:32288\n\t" \
  "ds_read_b64_tr_b16 v[208:209], v249 offset:30752\n\t" \
  "v_mfma_f32_16x16x32_bf16 v[172:175], v[212:215], v[124:127], v[172:175]\n\t" \
  "v_fma_mix_f32 v230, |v198|, v230, v230 op_sel:[0,1,0] op_sel_hi:[0,1,1] clamp\n\t" \
  "v_fma_mix_f32 v231, |v199|, v231, v231 op_sel:[0,1,0] op_sel_hi:[0,1,1] clamp\n\t" \
  "s_waitcnt lgkmcnt(5)\n\t" \
  "ds_read_b64_tr_b16 v[212:213], v249 offset:30760\n\t" \
  "ds_read_b32 v222, v222 offset:10016\n\t" \
  "v_mfma_f32_16x16x32_bf16 v[148:151], v[216:219], v[128:131], v[148:151]\n\t" \
  "s_mov_b32 m0, s47\n\t" \
  "v_or_b32 v204, v232, v233\n\t" \
  "v_or_b32 v205, v234, v235\n\t" \
  "s_mov_b32 exec_hi, 0xffff\n\t" \
  "buffer_load_dwordx4 v252, s[16:19], s33 offen lds\n\t" \
  "v_mfma_f32_16x16x32_bf16 v[156:159], v[216:219], v[132:135], v[156:159]\n\t" \
  "s_mov_b32 exec_hi, -1\n\t" \
  "v_fma_f32 v197, |v197|, v229, v197 mul:4\n\t" \
  "v_fma_f32 v196, |v196|, v228, v196 mul:4\n\t" \
  "s_add_u32 s33, s33, s32\n\t" \
  "ds_read_u16_d16_hi v227, v227 offset:10528\n\t" \
  "ds_read_b64_tr_b16 v[214:215], v249 offset:32296\n\t" \
  "v_mfma_f32_16x16x32_bf16 v[164:167], v[216:219], v[136:139], v[164:167]\n\t" \
  "v_fma_f32 v198, |v198|, v230, v198 mul:4\n\t" \
  "v_fma_f32 v199, |v199|, v231, v199 mul:4\n\t" \
  "s_waitcnt lgkmcnt(4)\n\t" \
  "ds_read_u16_d16_hi v225, v225 offset:10528\n\t" \
  "v_mfma_f32_16x16x32_bf16 v[172:175], v[216:219], v[140:143], v[172:175]\n\t" \
  "v_cvt_pk_bf16_f32 v196, v196, v197\n\t" \
  "v_cvt_pk_bf16_f32 v197, v198, v199\n\t" \
  "ds_read_b64_tr_b16 v[216:217], v249 offset:30816\n\t" \
  "v_mfma_f32_16x16x32_bf16 v[0:3], v[208:211], v[178:181], v[0:3]\n\t" \
  "v_cvt_pknorm_i16_f32 v228, |v162|, |v163|\n\t" \
  "v_cvt_pknorm_i16_f32 v230, |v160|, |v161|\n\t" \
  "ds_read_b64_tr_b16 v[218:219], v249 offset:32352\n\t" \
  "v_mfma_f32_16x16x32_bf16 v[24:27], v[208:211], v[186:189], v[24:27]\n\t" \
  "v_lshlrev_b32_sdwa v229, 2, v228 dst_sel:DWORD dst_unused:UNUSED_PAD src0_sel:DWORD src1_sel:BYTE_3\n\t" \
  "v_lshlrev_b32_sdwa v228, 2, v228 dst_sel:DWORD dst_unused:UNUSED_PAD src0_sel:DWORD src1_sel:BYTE_1\n\t" \
  "ds_read_u16 v224, v224 offset:10528\n\t" \
  "v_mfma_f32_16x16x32_bf16 v[48:51], v[208:211], v[194:197], v[48:51]\n\t" \
  "v_lshlrev_b32_sdwa v231, 2, v230 dst_sel:DWORD dst_unused:UNUSED_PAD src0_sel:DWORD src1_sel:BYTE_3\n\t" \
  "s_add_u32 m0, s47, s35\n\t" \
  "s_setprio 1\n\t" \
  "s_waitcnt lgkmcnt(4)\n\t" \
  "ds_read_b32 v228, v228 offset:10016\n\t" \
  "v_mfma_f32_16x16x32_bf16 v[72:75], v[208:211], v[202:205], v[72:75]\n\t" \
  "v_lshlrev_b32_sdwa v232, 1, v170 dst_sel:DWORD dst_unused:UNUSED_PAD src0_sel:DWORD src1_sel:WORD_1\n\t" \
  "v_lshlrev_b32_sdwa v233, 1, v171 dst_sel:DWORD dst_unused:UNUSED_PAD src0_sel:DWORD src1_sel:WORD_1\n\t" \
  "s_mov_b32 exec_hi, 0xffff\n\t" \
  "buffer_load_dwordx4 v252, s[16:19], s34 offen lds\n\t" \
  "v_mfma_f32_16x16x32_bf16 v[4:7], v[212:215], v[178:181], v[4:7]\n\t" \
  "s_mov_b32 exec_hi, -1\n\t" \
  "s_add_u32 s34, s34, s32\n\t" \
  "v_lshlrev_b32_sdwa v235, 1, v169 dst_sel:DWORD dst_unused:UNUSED_PAD src0_sel:DWORD src1_sel:WORD_1\n\t" \
  "ds_read_u16 v226, v226 offset:10528\n\t" \
  "ds_read_b64_tr_b16 v[208:209], v249 offset:30824\n\t" \
  "v_mfma_f32_16x16x32_bf16 v[28:31], v[212:215], v[186:189], v[28:31]\n\t" \
  "v_lshlrev_b32_sdwa v234, 1, v168 dst_sel:DWORD dst_unused:UNUSED_PAD src0_sel:DWORD src1_sel:WORD_1\n\t" \
  "v_lshlrev_b32_sdwa v230, 2, v230 dst_sel:DWORD dst_unused:UNUSED_PAD src0_sel:DWORD src1_sel:BYTE_1\n\t" \
  "ds_read_b64_tr_b16 v[210:211], v249 offset:32360\n\t" \
  "ds_read_b32 v229, v229 offset:10016\n\t" \
  "v_mfma_f32_16x16x32_bf16 v[76:79], v[212:215], v[202:205], v[76:79]\n\t" \
  "v_fma_mix_f32 v223, |v145|, v223, v223 op_sel:[0,1,0] op_sel_hi:[0,1,1] clamp\n\t" \
  "v_fma_mix_f32 v222, |v144|, v222, v222 op_sel:[0,1,0] op_sel_hi:[0,1,1] clamp\n\t" \
  "ds_read_b32 v230, v230 offset:10016\n\t" \
  "v_mfma_f32_16x16x32_bf16 v[52:55], v[212:215], v[194:197], v[52:55]\n\t" \
  "v_fma_mix_f32 v220, |v146|, v220, v220 op_sel:[0,1,0] op_sel_hi:[0,1,1] clamp\n\t" \
  "v_fma_mix_f32 v221, |v147|, v221, v221 op_sel:[0,1,0] op_sel_hi:[0,1,1] clamp\n\t" \
  "s_setprio 3\n\t" \
  "s_waitcnt lgkmcnt(4)\n\t" \
  "ds_read_b32 v231, v231 offset:10016\n\t" \
  "ds_read_b64_tr_b16 v[212:213], v249 offset:30880\n\t" \
  "v_mfma_f32_16x16x32_bf16 v[8:11], v[216:219], v[178:181], v[8:11]\n\t" \
  "v_or_b32 v155, v224, v225\n\t" \
  "v_or_b32 v154, v226, v227\n\t" \
  "s_setprio 1\n\t" \
  "ds_read_u16_d16_hi v233, v233 offset:10528\n\t" \
  "ds_read_b64_tr_b16 v[214:215], v249 offset:32416\n\t" \
  "v_mfma_f32_16x16x32_bf16 v[32:35], v[216:219], v[186:189], v[32:35]\n\t" \
  "s_add_u32 s24, s24, s26\n\t" \
  "v_fma_f32 v146, |v146|, v220, v146 mul:4\n\t" \
  "v_fma_f32 v147, |v147|, v221, v147 mul:4\n\t" \
  "v_mfma_f32_16x16x32_bf16 v[56:59], v[216:219], v[194:197], v[56:59]\n\t" \
  "v_fma_f32 v145, |v145|, v223, v145 mul:4\n\t" \
  "v_fma_f32 v144, |v144|, v222, v144 mul:4\n\t" \
  "ds_read_u16 v232, v232 offset:10528\n\t" \
  "v_mfma_f32_16x16x32_bf16 v[80:83], v[216:219], v[202:205], v[80:83]\n\t" \
  "s_add_u32 m0, s25, 8200\n\t" \
  "v_cvt_pk_bf16_f32 v147, v146, v147\n\t" \
  "v_cvt_pk_bf16_f32 v146, v144, v145\n\t" \
  "s_setprio 0\n\t" \
  "s_waitcnt lgkmcnt(7)\n\t" \
  "buffer_load_dwordx4 v254, s[8:11], s24 offen nt lds\n\t" \
  "v_mfma_f32_16x16x32_bf16 v[12:15], v[208:211], v[178:181], v[12:15]\n\t" \
  "v_cvt_pknorm_i16_f32 v220, |v148|, |v149|\n\t" \
  "v_cvt_pknorm_i16_f32 v222, |v150|, |v151|\n\t" \
  "ds_read_b64_tr_b16 v[216:217], v249 offset:30888\n\t" \
  "ds_read_b64_tr_b16 v[218:219], v249 offset:32424\n\t" \
  "v_mfma_f32_16x16x32_bf16 v[36:39], v[208:211], v[186:189], v[36:39]\n\t" \
  "v_lshlrev_b32_sdwa v221, 2, v220 dst_sel:DWORD dst_unused:UNUSED_PAD src0_sel:DWORD src1_sel:BYTE_3\n\t" \
  "v_lshlrev_b32_sdwa v220, 2, v220 dst_sel:DWORD dst_unused:UNUSED_PAD src0_sel:DWORD src1_sel:BYTE_1\n\t" \
  "ds_read_u16_d16_hi v235, v235 offset:10528\n\t" \
  "v_mfma_f32_16x16x32_bf16 v[60:63], v[208:211], v[194:197], v[60:63]\n\t" \
  "v_lshlrev_b32_sdwa v223, 2, v222 dst_sel:DWORD dst_unused:UNUSED_PAD src0_sel:DWORD src1_sel:BYTE_3\n\t" \
  "v_lshlrev_b32_sdwa v222, 2, v222 dst_sel:DWORD dst_unused:UNUSED_PAD src0_sel:DWORD src1_sel:BYTE_1\n\t" \
  "ds_read_u16 v234, v234 offset:10528\n\t" \
  "s_waitcnt lgkmcnt(5)\n\t" \
  "v_mfma_f32_16x16x32_bf16 v[84:87], v[208:211], v[202:205], v[84:87]\n\t" \
  "s_setprio 1\n\t" \
  "v_lshlrev_b32_sdwa v225, 1, v157 dst_sel:DWORD dst_unused:UNUSED_PAD src0_sel:DWORD src1_sel:WORD_1\n\t" \
  "v_lshlrev_b32_sdwa v224, 1, v156 dst_sel:DWORD dst_unused:UNUSED_PAD src0_sel:DWORD src1_sel:WORD_1\n\t" \
  "ds_read_b64_tr_b16 v[208:209], v248 offset:0\n\t" \
  "ds_read_b32 v220, v220 offset:10016\n\t" \
  "v_mfma_f32_16x16x32_bf16 v[16:19], v[212:215], v[178:181], v[16:19]\n\t" \
  "v_nop\n\t" \
  "s_add_u32 s27, s27, s26\n\t" \
  "s_mov_b64 exec, s[30:31]\n\t" \
  "buffer_load_dword v237, v254, s[8:11], s27 offen\n\t" \
  "v_mfma_f32_16x16x32_bf16 v[64:67], v[212:215], v[194:197], v[64:67]\n\t" \
  "s_mov_b64 exec, -1\n\t" \
  "v_fma_mix_f32 v229, |v163|, v229, v229 op_sel:[0,1,0] op_sel_hi:[0,1,1] clamp\n\t" \
  "v_lshlrev_b32_sdwa v227, 1, v159 dst_sel:DWORD dst_unused:UNUSED_PAD src0_sel:DWORD src1_sel:WORD_1\n\t" \
  "s_setprio 2\n\t" \
  "ds_read_b32 v223, v223 offset:10016\n\t" \
  "ds_read_b32 v221, v221 offset:10016\n\t" \
  "v_mfma_f32_16x16x32_bf16 v[88:91], v[212:215], v[202:205], v[88:91]\n\t" \
  "s_setprio 1\n\t" \
  "v_fma_mix_f32 v231, |v161|, v231, v231 op_sel:[0,1,0] op_sel_hi:[0,1,1] clamp\n\t" \
  "v_fma_mix_f32 v230, |v160|, v230, v230 op_sel:[0,1,0] op_sel_hi:[0,1,1] clamp\n\t" \
  "ds_read_b64_tr_b16 v[210:211], v248 offset:1024\n\t" \
  "v_mfma_f32_16x16x32_bf16 v[40:43], v[212:215], v[186:189], v[40:43]\n\t" \
  "s_waitcnt lgkmcnt(5)\n\t" \
  "v_fma_mix_f32 v228, |v162|, v228, v228 op_sel:[0,1,0] op_sel_hi:[0,1,1] clamp\n\t" \
  "v_lshlrev_b32_sdwa v226, 1, v158 dst_sel:DWORD dst_unused:UNUSED_PAD src0_sel:DWORD src1_sel:WORD_1\n\t" \
  "ds_read_b32 v222, v222 offset:10016\n\t" \
  "ds_read_b64_tr_b16 v[212:213], v248 offset:3072\n\t" \
  "v_mfma_f32_16x16x32_bf16 v[20:23], v[216:219], v[178:181], v[20:23]\n\t" \
  "v_or_b32 v171, v232, v233\n\t" \
  "v_or_b32 v170, v234, v235\n\t" \
  "ds_read_u16_d16_hi v225, v225 offset:10528\n\t" \
  "v_mfma_f32_16x16x32_bf16 v[68:71], v[216:219], v[194:197], v[68:71]\n\t" \
  "s_setprio 2\n\t" \
  "v_fma_f32 v160, |v160|, v230, v160 mul:4\n\t" \
  "v_fma_f32 v163, |v163|, v229, v163 mul:4\n\t" \
  "s_waitcnt lgkmcnt(3)\n\t" \
  "ds_read_u16 v224, v224 offset:10528\n\t" \
  "v_mfma_f32_16x16x32_bf16 v[176:179], v[208:211], v[96:99], 0\n\t" \
  "v_lshlrev_b32_sdwa v232, 1, v172 dst_sel:DWORD dst_unused:UNUSED_PAD src0_sel:DWORD src1_sel:WORD_1\n\t" \
  "v_fma_f32 v161, |v161|, v231, v161 mul:4\n\t" \
  "ds_read_b64_tr_b16 v[214:215], v248 offset:4096\n\t" \
  "v_mfma_f32_16x16x32_bf16 v[44:47], v[216:219], v[186:189], v[44:47]\n\t" \
  "v_fma_f32 v162, |v162|, v228, v162 mul:4\n\t" \
  "v_cvt_pknorm_i16_f32 v230, |v166|, |v167|\n\t" \
  "ds_read_u16_d16_hi v227, v227 offset:10528\n\t" \
  "v_mfma_f32_16x16x32_bf16 v[92:95], v[216:219], v[202:205], v[92:95]\n\t" \
  "v_cvt_pk_bf16_f32 v163, v162, v163\n\t" \
  "v_cvt_pk_bf16_f32 v162, v160, v161\n\t" \
  "ds_read_b64_tr_b16 v[216:217], v248 offset:2048\n\t" \
  "v_mfma_f32_16x16x32_bf16 v[192:195], v[208:211], v[104:107], 0\n\t" \
  "v_cvt_pknorm_i16_f32 v228, |v164|, |v165|\n\t" \
  "v_lshlrev_b32_sdwa v231, 2, v230 dst_sel:DWORD dst_unused:UNUSED_PAD src0_sel:DWORD src1_sel:BYTE_3\n\t" \
  "ds_read_b64_tr_b16 v[218:219], v248 offset:5120\n\t" \
  "v_mfma_f32_16x16x32_bf16 v[184:187], v[208:211], v[100:103], 0\n\t" \
  "v_lshlrev_b32_sdwa v229, 2, v228 dst_sel:DWORD dst_unused:UNUSED_PAD src0_sel:DWORD src1_sel:BYTE_3\n\t" \
  "v_lshlrev_b32_sdwa v228, 2, v228 dst_sel:DWORD dst_unused:UNUSED_PAD src0_sel:DWORD src1_sel:BYTE_1\n\t" \
  "ds_read_u16 v226, v226 offset:10528\n\t" \
  "s_waitcnt lgkmcnt(4)\n\t" \
  "v_mfma_f32_16x16x32_bf16 v[200:203], v[208:211], v[108:111], 0\n\t" \
  "v_lshlrev_b32_sdwa v230, 2, v230 dst_sel:DWORD dst_unused:UNUSED_PAD src0_sel:DWORD src1_sel:BYTE_1\n\t" \
  "v_lshlrev_b32_sdwa v233, 1, v173 dst_sel:DWORD dst_unused:UNUSED_PAD src0_sel:DWORD src1_sel:WORD_1\n\t" \
  "ds_read_b64_tr_b16 v[208:209], v248 offset:512\n\t" \
  "v_mfma_f32_16x16x32_bf16 v[176:179], v[212:215], v[112:115], v[176:179]\n\t" \
  "v_lshlrev_b32_sdwa v235, 1, v175 dst_sel:DWORD dst_unused:UNUSED_PAD src0_sel:DWORD src1_sel:WORD_1\n\t" \
  "v_fma_mix_f32 v221, |v149|, v221, v221 op_sel:[0,1,0] op_sel_hi:[0,1,1] clamp\n\t" \
  "ds_read_b32 v231, v231 offset:10016\n\t" \
  "v_mfma_f32_16x16x32_bf16 v[200:203], v[212:215], v[124:127], v[200:203]\n\t" \
  "v_lshlrev_b32_sdwa v234, 1, v174 dst_sel:DWORD dst_unused:UNUSED_PAD src0_sel:DWORD src1_sel:WORD_1\n\t" \
  "ds_read_b32 v228, v228 offset:10016\n\t" \
  "ds_read_b32 v229, v229 offset:10016\n\t" \
  "v_mfma_f32_16x16x32_bf16 v[192:195], v[212:215], v[120:123], v[192:195]\n\t" \
  "s_setprio 1\n\t" \
  "v_fma_mix_f32 v220, |v148|, v220, v220 op_sel:[0,1,0] op_sel_hi:[0,1,1] clamp\n\t" \
  "ds_read_b64_tr_b16 v[210:211], v248 offset:1536\n\t" \
  "v_mfma_f32_16x16x32_bf16 v[184:187], v[212:215], v[116:119], v[184:187]\n\t" \
  "v_fma_mix_f32 v222, |v150|, v222, v222 op_sel:[0,1,0] op_sel_hi:[0,1,1] clamp\n\t" \
  "v_fma_mix_f32 v223, |v151|, v223, v223 op_sel:[0,1,0] op_sel_hi:[0,1,1] clamp\n\t" \
  "s_waitcnt lgkmcnt(6)\n\t" \
  "ds_read_b64_tr_b16 v[212:213], v248 offset:3584\n\t" \
  "ds_read_u16_d16_hi v233, v233 offset:10528\n\t" \
  "v_mfma_f32_16x16x32_bf16 v[176:179], v[216:219], v[128:131], v[176:179]\n\t" \
  "s_waitcnt lgkmcnt(7)\n\t" \
  "v_or_b32 v156, v224, v225\n\t" \
  "v_or_b32 v157, v226, v227\n\t" \
  "ds_read_b64_tr_b16 v[214:215], v248 offset:4608\n\t" \
  "v_mfma_f32_16x16x32_bf16 v[192:195], v[216:219], v[136:139], v[192:195]\n\t" \
  "v_fma_f32 v150, |v150|, v222, v150 mul:4\n\t" \
  "v_fma_f32 v151, |v151|, v223, v151 mul:4\n\t" \
  "s_setprio 0\n\t" \
  "ds_read_b32 v230, v230 offset:10016\n\t" \
  "v_mfma_f32_16x16x32_bf16 v[184:187], v[216:219], v[132:135], v[184:187]\n\t" \
  "v_fma_f32 v149, |v149|, v221, v149 mul:4\n\t" \
  "v_fma_f32 v148, |v148|, v220, v148 mul:4\n\t" \
  "ds_read_u16 v232, v232 offset:10528\n\t" \
  "ds_read_u16_d16_hi v235, v235 offset:10528\n\t" \
  "v_mfma_f32_16x16x32_bf16 v[200:203], v[216:219], v[140:143], v[200:203]\n\t" \
  "v_cvt_pk_bf16_f32 v148, v148, v149\n\t" \
  "v_cvt_pk_bf16_f32 v149, v150, v151\n\t" \
  "s_waitcnt lgkmcnt(6)\n\t" \
  "ds_read_b64_tr_b16 v[216:217], v248 offset:2560\n\t" \
  "v_mfma_f32_16x16x32_bf16 v[180:183], v[208:211], v[96:99], 0\n\t" \
  "v_cvt_pknorm_i16_f32 v222, |v176|, |v177|\n\t" \
  "v_cvt_pknorm_i16_f32 v220, |v178|, |v179|\n\t" \
  "s_nop 0\n\t" \
  "s_waitcnt vmcnt(2)\n\t" \
  "ds_read_b64_tr_b16 v[218:219], v248 offset:5632\n\t" \
  "v_mfma_f32_16x16x32_bf16 v[188:191], v[208:211], v[100:103], 0\n\t" \
  "s_waitcnt lgkmcnt(5)\n\t" \
  "v_lshlrev_b32_sdwa v221, 2, v220 dst_sel:DWORD dst_unused:UNUSED_PAD src0_sel:DWORD src1_sel:BYTE_3\n\t" \
  "v_lshlrev_b32_sdwa v220, 2, v220 dst_sel:DWORD dst_unused:UNUSED_PAD src0_sel:DWORD src1_sel:BYTE_1\n\t" \
  "s_mov_b64 exec, 3\n\t" \
  "ds_add_rtn_u32 v246, v251, v250 offset:47160\n\t" \
  "v_mfma_f32_16x16x32_bf16 v[196:199], v[208:211], v[104:107], 0\n\t" \
  "s_mov_b64 exec, -1\n\t" \
  "v_lshlrev_b32_sdwa v223, 2, v222 dst_sel:DWORD dst_unused:UNUSED_PAD src0_sel:DWORD src1_sel:BYTE_3\n\t" \
  "v_lshlrev_b32_sdwa v222, 2, v222 dst_sel:DWORD dst_unused:UNUSED_PAD src0_sel:DWORD src1_sel:BYTE_1\n\t" \
  "s_setprio 2\n\t" \
  "s_setprio 0\n\t" \
  "ds_read_u16 v234, v234 offset:10528\n\t" \
  "v_mfma_f32_16x16x32_bf16 v[188:191], v[212:215], v[116:119], v[188:191]\n\t" \
  "v_lshlrev_b32_sdwa v226, 1, v184 dst_sel:DWORD dst_unused:UNUSED_PAD src0_sel:DWORD src1_sel:WORD_1\n\t" \
  "v_lshlrev_b32_sdwa v227, 1, v185 dst_sel:DWORD dst_unused:UNUSED_PAD src0_sel:DWORD src1_sel:WORD_1\n\t" \
  "ds_read_b32 v223, v223 offset:10016\n\t" \
  "v_mfma_f32_16x16x32_bf16 v[180:183], v[212:215], v[112:115], v[180:183]\n\t" \
  "s_waitcnt lgkmcnt(2)\n\t" \
  "v_lshlrev_b32_sdwa v225, 1, v187 dst_sel:DWORD dst_unused:UNUSED_PAD src0_sel:DWORD src1_sel:WORD_1\n\t" \
  "v_readfirstlane_b32 s4, v246\n\t" \
  "ds_read_b32 v221, v221 offset:10016\n\t" \
  "v_mfma_f32_16x16x32_bf16 v[204:207], v[208:211], v[108:111], 0\n\t" \
  "s_cmpk_lt_u32 s4, 3072\n\t" \
  "v_lshlrev_b32_sdwa v224, 1, v186 dst_sel:DWORD dst_unused:UNUSED_PAD src0_sel:DWORD src1_sel:WORD_1\n\t" \
  "s_cbranch_scc1 .Lcnx_w0_4\n\t" \
  ".Lcnx_g0_4:\n\t" \
  "ds_read_b32 v220, v220 offset:10016\n\t" \
  "v_mfma_f32_16x16x32_bf16 v[196:199], v[212:215], v[120:123], v[196:199]\n\t" \
  "v_fma_mix_f32 v228, |v164|, v228, v228 op_sel:[0,1,0] op_sel_hi:[0,1,1] clamp\n\t" \
  "v_fma_mix_f32 v229, |v165|, v229, v229 op_sel:[0,1,0] op_sel_hi:[0,1,1] clamp\n\t" \
  "ds_read_b64_tr_b16 v[210:211], v249 offset:7688\n\t" \
  "ds_read_b64_tr_b16 v[208:209], v249 offset:6152\n\t" \
  "v_mfma_f32_16x16x32_bf16 v[204:207], v[212:215], v[124:127], v[204:207]\n\t" \
  "v_fma_mix_f32 v230, |v166|, v230, v230 op_sel:[0,1,0] op_sel_hi:[0,1,1] clamp\n\t" \
  "v_fma_mix_f32 v231, |v167|, v231, v231 op_sel:[0,1,0] op_sel_hi:[0,1,1] clamp\n\t" \
  "s_waitcnt lgkmcnt(5)\n\t" \
  "ds_read_b64_tr_b16 v[212:213], v249 offset:6160\n\t" \
  "ds_read_b32 v222, v222 offset:10016\n\t" \
  "v_mfma_f32_16x16x32_bf16 v[180:183], v[216:219], v[128:131], v[180:183]\n\t" \
  "s_mov_b32 m0, s44\n\t" \
  "v_or_b32 v172, v232, v233\n\t" \
  "v_or_b32 v173, v234, v235\n\t" \
  "s_mov_b32 exec_hi, 0xffff\n\t" \
  "buffer_load_dwordx4 v252, s[16:19], s33 offen lds\n\t" \
  "v_mfma_f32_16x16x32_bf16 v[188:191], v[216:219], v[132:135], v[188:191]\n\t" \
  "s_mov_b32 exec_hi, -1\n\t" \
  "v_fma_f32 v165, |v165|, v229, v165 mul:4\n\t" \
  "v_fma_f32 v164, |v164|, v228, v164 mul:4\n\t" \
  "s_add_u32 s33, s33, s32\n\t" \
  "ds_read_u16_d16_hi v227, v227 offset:10528\n\t" \
  "ds_read_b64_tr_b16 v[214:215], v249 offset:7696\n\t" \
  "v_mfma_f32_16x16x32_bf16 v[196:199], v[216:219], v[136:139], v[196:199]\n\t" \
  "v_fma_f32 v166, |v166|, v230, v166 mul:4\n\t" \
  "v_fma_f32 v167, |v167|, v231, v167 mul:4\n\t" \
  "s_waitcnt lgkmcnt(4)\n\t" \
  "ds_read_u16_d16_hi v225, v225 offset:10528\n\t" \
  "v_mfma_f32_16x16x32_bf16 v[204:207], v[216:219], v[140:143], v[204:207]\n\t" \
  "v_cvt_pk_bf16_f32 v164, v164, v165\n\t" \
  "v_cvt_pk_bf16_f32 v165, v166, v167\n\t" \
  "ds_read_b64_tr_b16 v[216:217], v249 offset:6216\n\t" \
  "v_mfma_f32_16x16x32_bf16 v[0:3], v[208:211], v[146:149], v[0:3]\n\t" \
  "v_cvt_pknorm_i16_f32 v228, |v194|, |v195|\n\t" \
  "v_cvt_pknorm_i16_f32 v230, |v192|, |v193|\n\t" \
  "ds_read_b64_tr_b16 v[218:219], v249 offset:7752\n\t" \
  "v_mfma_f32_16x16x32_bf16 v[24:27], v[208:211], v[154:157], v[24:27]\n\t" \
  "v_lshlrev_b32_sdwa v229, 2, v228 dst_sel:DWORD dst_unused:UNUSED_PAD src0_sel:DWORD src1_sel:BYTE_3\n\t" \
  "v_lshlrev_b32_sdwa v228, 2, v228 dst_sel:DWORD dst_unused:UNUSED_PAD src0_sel:DWORD src1_sel:BYTE_1\n\t" \
  "ds_read_u16 v224, v224 offset:10528\n\t" \
  "v_mfma_f32_16x16x32_bf16 v[48:51], v[208:211], v[162:165], v[48:51]\n\t" \
  "v_lshlrev_b32_sdwa v231, 2, v230 dst_sel:DWORD dst_unused:UNUSED_PAD src0_sel:DWORD src1_sel:BYTE_3\n\t" \
  "s_add_u32 m0, s44, s35\n\t" \
  "s_setprio 1\n\t" \
  "s_waitcnt lgkmcnt(4)\n\t" \
  "ds_read_b32 v228, v228 offset:10016\n\t" \
  "v_mfma_f32_16x16x32_bf16 v[72:75], v[208:211], v[170:173], v[72:75]\n\t" \
  "v_lshlrev_b32_sdwa v232, 1, v202 dst_sel:DWORD dst_unused:UNUSED_PAD src0_sel:DWORD src1_sel:WORD_1\n\t" \
  "v_lshlrev_b32_sdwa v233, 1, v203 dst_sel:DWORD dst_unused:UNUSED_PAD src0_sel:DWORD src1_sel:WORD_1\n\t" \
  "s_mov_b32 exec_hi, 0xffff\n\t" \
  "buffer_load_dwordx4 v252, s[16:19], s34 offen lds\n\t" \
  "v_mfma_f32_16x16x32_bf16 v[4:7], v[212:215], v[146:149], v[4:7]\n\t" \
  "s_mov_b32 exec_hi, -1\n\t" \
  "s_add_u32 s34, s34, s32\n\t" \
  "v_lshlrev_b32_sdwa v235, 1, v201 dst_sel:DWORD dst_unused:UNUSED_PAD src0_sel:DWORD src1_sel:WORD_1\n\t" \
  "ds_read_u16 v226, v226 offset:10528\n\t" \
  "ds_read_b64_tr_b16 v[208:209], v249 offset:6224\n\t" \
  "v_mfma_f32_16x16x32_bf16 v[28:31], v[212:215], v[154:157], v[28:31]\n\t" \
  "v_lshlrev_b32_sdwa v234, 1, v200 dst_sel:DWORD dst_unused:UNUSED_PAD src0_sel:DWORD src1_sel:WORD_1\n\t" \
  "v_lshlrev_b32_sdwa v230, 2, v230 dst_sel:DWORD dst_unused:UNUSED_PAD src0_sel:DWORD src1_sel:BYTE_1\n\t" \
  "ds_read_b64_tr_b16 v[210:211], v249 offset:7760\n\t" \
  "ds_read_b32 v229, v229 offset:10016\n\t" \
  "v_mfma_f32_16x16x32_bf16 v[76:79], v[212:215], v[170:173], v[76:79]\n\t" \
  "v_fma_mix_f32 v223, |v177|, v223, v223 op_sel:[0,1,0] op_sel_hi:[0,1,1] clamp\n\t" \
  "v_fma_mix_f32 v222, |v176|, v222, v222 op_sel:[0,1,0] op_sel_hi:[0,1,1] clamp\n\t" \
  "ds_read_b32 v230, v230 offset:10016\n\t" \
  "v_mfma_f32_16x16x32_bf16 v[52:55], v[212:215], v[162:165], v[52:55]\n\t" \
  "v_fma_mix_f32 v220, |v178|, v220, v220 op_sel:[0,1,0] op_sel_hi:[0,1,1] clamp\n\t" \
  "v_fma_mix_f32 v221, |v179|, v221, v221 op_sel:[0,1,0] op_sel_hi:[0,1,1] clamp\n\t" \
  "s_setprio 3\n\t" \
  "s_waitcnt lgkmcnt(4)\n\t" \
  "ds_read_b32 v231, v231 offset:10016\n\t" \
  "ds_read_b64_tr_b16 v[212:213], v249 offset:6280\n\t" \
  "v_mfma_f32_16x16x32_bf16 v[8:11], v[216:219], v[146:149], v[8:11]\n\t" \
  "v_or_b32 v187, v224, v225\n\t" \
  "v_or_b32 v186, v226, v227\n\t" \
  "s_setprio 1\n\t" \
  "ds_read_u16_d16_hi v233, v233 offset:10528\n\t" \
  "ds_read_b64_tr_b16 v[214:215], v249 offset:7816\n\t" \
  "v_mfma_f32_16x16x32_bf16 v[32:35], v[216:219], v[154:157], v[32:35]\n\t" \
  "s_add_u32 s24, s24, s26\n\t" \
  "v_fma_f32 v178, |v178|, v220, v178 mul:4\n\t" \
  "v_fma_f32 v179, |v179|, v221, v179 mul:4\n\t" \
  "v_mfma_f32_16x16x32_bf16 v[56:59], v[216:219], v[162:165], v[56:59]\n\t" \
  "v_fma_f32 v177, |v177|, v223, v177 mul:4\n\t" \
  "v_fma_f32 v176, |v176|, v222, v176 mul:4\n\t" \
  "ds_read_u16 v232, v232 offset:10528\n\t" \
  "v_mfma_f32_16x16x32_bf16 v[80:83], v[216:219], v[170:173], v[80:83]\n\t" \
  "s_add_u32 m0, s25, 16400\n\t" \
  "v_cvt_pk_bf16_f32 v179, v178, v179\n\t" \
  "v_cvt_pk_bf16_f32 v178, v176, v177\n\t" \
  "s_setprio 0\n\t" \
  "s_waitcnt lgkmcnt(7)\n\t" \
  "buffer_load_dwordx4 v254, s[8:11], s24 offen nt lds\n\t" \
  "v_mfma_f32_16x16x32_bf16 v[12:15], v[208:211], v[146:149], v[12:15]\n\t" \
  "v_cvt_pknorm_i16_f32 v220, |v180|, |v181|\n\t" \
  "v_cvt_pknorm_i16_f32 v222, |v182|, |v183|\n\t" \
  "ds_read_b64_tr_b16 v[216:217], v249 offset:6288\n\t" \
  "ds_read_b64_tr_b16 v[218:219], v249 offset:7824\n\t" \
  "v_mfma_f32_16x16x32_bf16 v[36:39], v[208:211], v[154:157], v[36:39]\n\t" \
  "v_lshlrev_b32_sdwa v221, 2, v220 dst_sel:DWORD dst_unused:UNUSED_PAD src0_sel:DWORD src1_sel:BYTE_3\n\t" \
  "v_lshlrev_b32_sdwa v220, 2, v220 dst_sel:DWORD dst_unused:UNUSED_PAD src0_sel:DWORD src1_sel:BYTE_1\n\t" \
  "ds_read_u16_d16_hi v235, v235 offset:10528\n\t" \
  "v_mfma_f32_16x16x32_bf16 v[60:63], v[208:211], v[162:165], v[60:63]\n\t" \
  "v_lshlrev_b32_sdwa v223, 2, v222 dst_sel:DWORD dst_unused:UNUSED_PAD src0_sel:DWORD src1_sel:BYTE_3\n\t" \
  "v_lshlrev_b32_sdwa v222, 2, v222 dst_sel:DWORD dst_unused:UNUSED_PAD src0_sel:DWORD src1_sel:BYTE_1\n\t" \
  "ds_read_u16 v234, v234 offset:10528\n\t" \
  "s_waitcnt lgkmcnt(5)\n\t" \
  "v_mfma_f32_16x16x32_bf16 v[84:87], v[208:211], v[170:173], v[84:87]\n\t" \
  "s_setprio 1\n\t" \
  "v_lshlrev_b32_sdwa v225, 1, v189 dst_sel:DWORD dst_unused:UNUSED_PAD src0_sel:DWORD src1_sel:WORD_1\n\t" \
  "v_lshlrev_b32_sdwa v224, 1, v188 dst_sel:DWORD dst_unused:UNUSED_PAD src0_sel:DWORD src1_sel:WORD_1\n\t" \
  "ds_read_b64_tr_b16 v[208:209], v248 offset:43048\n\t" \
  "ds_read_b32 v220, v220 offset:10016\n\t" \
  "v_mfma_f32_16x16x32_bf16 v[16:19], v[212:215], v[146:149], v[16:19]\n\t" \
  "v_nop\n\t" \
  "s_add_u32 s27, s27, s26\n\t" \
  "s_mov_b64 exec, s[30:31]\n\t" \
  "buffer_load_dword v237, v254, s[8:11], s27 offen\n\t" \
  "v_mfma_f32_16x16x32_bf16 v[64:67], v[212:215], v[162:165], v[64:67]\n\t" \
  "s_mov_b64 exec, -1\n\t" \
  "v_fma_mix_f32 v229, |v195|, v229, v229 op_sel:[0,1,0] op_sel_hi:[0,1,1] clamp\n\t" \
  "v_lshlrev_b32_sdwa v227, 1, v191 dst_sel:DWORD dst_unused:UNUSED_PAD src0_sel:DWORD src1_sel:WORD_1\n\t" \
  "s_setprio 2\n\t" \
  "ds_read_b32 v223, v223 offset:10016\n\t" \
  "ds_read_b32 v221, v221 offset:10016\n\t" \
  "v_mfma_f32_16x16x32_bf16 v[88:91], v[212:215], v[170:173], v[88:91]\n\t" \
  "s_setprio 1\n\t" \
  "v_fma_mix_f32 v231, |v193|, v231, v231 op_sel:[0,1,0] op_sel_hi:[0,1,1] clamp\n\t" \
  "v_fma_mix_f32 v230, |v192|, v230, v230 op_sel:[0,1,0] op_sel_hi:[0,1,1] clamp\n\t" \
  "ds_read_b64_tr_b16 v[210:211], v248 offset:44072\n\t" \
  "v_mfma_f32_16x16x32_bf16 v[40:43], v[212:215], v[154:157], v[40:43]\n\t" \
  "s_waitcnt lgkmcnt(5)\n\t" \
  "v_fma_mix_f32 v228, |v194|, v228, v228 op_sel:[0,1,0] op_sel_hi:[0,1,1] clamp\n\t" \
  "v_lshlrev_b32_sdwa v226, 1, v190 dst_sel:DWORD dst_unused:UNUSED_PAD src0_sel:DWORD src1_sel:WORD_1\n\t" \
  "ds_read_b32 v222, v222 offset:10016\n\t" \
  "ds_read_b64_tr_b16 v[212:213], v248 offset:46120\n\t" \
  "v_mfma_f32_16x16x32_bf16 v[20:23], v[216:219], v[146:149], v[20:23]\n\t" \
  "v_or_b32 v203, v232, v233\n\t" \
  "v_or_b32 v202, v234, v235\n\t" \
  "ds_read_u16_d16_hi v225, v225 offset:10528\n\t" \
  "v_mfma_f32_16x16x32_bf16 v[68:71], v[216:219], v[162:165], v[68:71]\n\t" \
  "s_setprio 2\n\t" \
  "v_fma_f32 v192, |v192|, v230, v192 mul:4\n\t" \
  "v_fma_f32 v195, |v195|, v229, v195 mul:4\n\t" \
  "s_waitcnt lgkmcnt(3)\n\t" \
  "ds_read_u16 v224, v224 offset:10528\n\t" \
  "v_mfma_f32_16x16x32_bf16 v[144:147], v[208:211], v[96:99], 0\n\t" \
  "v_lshlrev_b32_sdwa v232, 1, v204 dst_sel:DWORD dst_unused:UNUSED_PAD src0_sel:DWORD src1_sel:WORD_1\n\t" \
  "v_fma_f32 v193, |v193|, v231, v193 mul:4\n\t" \
  "ds_read_b64_tr_b16 v[214:215], v248 offset:47144\n\t" \
  "v_mfma_f32_16x16x32_bf16 v[44:47], v[216:219], v[154:157], v[44:47]\n\t" \
  "v_fma_f32 v194, |v194|, v228, v194 mul:4\n\t" \
  "v_cvt_pknorm_i16_f32 v230, |v198|, |v199|\n\t" \
  "ds_read_u16_d16_hi v227, v227 offset:10528\n\t" \
  "v_mfma_f32_16x16x32_bf16 v[92:95], v[216:219], v[170:173], v[92:95]\n\t" \
  "v_cvt_pk_bf16_f32 v195, v194, v195\n\t" \
  "v_cvt_pk_bf16_f32 v194, v192, v193\n\t" \
  "ds_read_b64_tr_b16 v[216:217], v248 offset:45096\n\t" \
  "v_mfma_f32_16x16x32_bf16 v[160:163], v[208:211], v[104:107], 0\n\t" \
  "v_cvt_pknorm_i16_f32 v228, |v196|, |v197|\n\t" \
  "v_lshlrev_b32_sdwa v231, 2, v230 dst_sel:DWORD dst_unused:UNUSED_PAD src0_sel:DWORD src1_sel:BYTE_3\n\t" \
  "ds_read_b64_tr_b16 v[218:219], v248 offset:48168\n\t" \
  "v_mfma_f32_16x16x32_bf16 v[152:155], v[208:211], v[100:103], 0\n\t" \
  "v_lshlrev_b32_sdwa v229, 2, v228 dst_sel:DWORD dst_unused:UNUSED_PAD src0_sel:DWORD src1_sel:BYTE_3\n\t" \
  "v_lshlrev_b32_sdwa v228, 2, v228 dst_sel:DWORD dst_unused:UNUSED_PAD src0_sel:DWORD src1_sel:BYTE_1\n\t" \
  "ds_read_u16 v226, v226 offset:10528\n\t" \
  "s_waitcnt lgkmcnt(4)\n\t" \
  "v_mfma_f32_16x16x32_bf16 v[168:171], v[208:211], v[108:111], 0\n\t" \
  "v_lshlrev_b32_sdwa v230, 2, v230 dst_sel:DWORD dst_unused:UNUSED_PAD src0_sel:DWORD src1_sel:BYTE_1\n\t" \
  "v_lshlrev_b32_sdwa v233, 1, v205 dst_sel:DWORD dst_unused:UNUSED_PAD src0_sel:DWORD src1_sel:WORD_1\n\t" \
  "ds_read_b64_tr_b16 v[208:209], v248 offset:43560\n\t" \
  "v_mfma_f32_16x16x32_bf16 v[144:147], v[212:215], v[112:115], v[144:147]\n\t" \
  "v_lshlrev_b32_sdwa v235, 1, v207 dst_sel:DWORD dst_unused:UNUSED_PAD src0_sel:DWORD src1_sel:WORD_1\n\t" \
  "v_fma_mix_f32 v221, |v181|, v221, v221 op_sel:[0,1,0] op_sel_hi:[0,1,1] clamp\n\t" \
  "ds_read_b32 v231, v231 offset:10016\n\t" \
  "v_mfma_f32_16x16x32_bf16 v[168:171], v[212:215], v[124:127], v[168:171]\n\t" \
  "v_lshlrev_b32_sdwa v234, 1, v206 dst_sel:DWORD dst_unused:UNUSED_PAD src0_sel:DWORD src1_sel:WORD_1\n\t" \
  "ds_read_b32 v228, v228 offset:10016\n\t" \
  "ds_read_b32 v229, v229 offset:10016\n\t" \
  "v_mfma_f32_16x16x32_bf16 v[160:163], v[212:215], v[120:123], v[160:163]\n\t" \
  "s_setprio 1\n\t" \
  "v_fma_mix_f32 v220, |v180|, v220, v220 op_sel:[0,1,0] op_sel_hi:[0,1,1] clamp\n\t" \
  "ds_read_b64_tr_b16 v[210:211], v248 offset:44584\n\t" \
  "v_mfma_f32_16x16x32_bf16 v[152:155], v[212:215], v[116:119], v[152:155]\n\t" \
  "v_fma_mix_f32 v222, |v182|, v222, v222 op_sel:[0,1,0] op_sel_hi:[0,1,1] clamp\n\t" \
  "v_fma_mix_f32 v223, |v183|, v223, v223 op_sel:[0,1,0] op_sel_hi:[0,1,1] clamp\n\t" \
  "s_waitcnt lgkmcnt(6)\n\t" \
  "ds_read_b64_tr_b16 v[212:213], v248 offset:46632\n\t" \
  "ds_read_u16_d16_hi v233, v233 offset:10528\n\t" \
  "v_mfma_f32_16x16x32_bf16 v[144:147], v[216:219], v[128:131], v[144:147]\n\t" \
  "s_waitcnt lgkmcnt(7)\n\t" \
  "v_or_b32 v188, v224, v225\n\t" \
  "v_or_b32 v189, v226, v227\n\t" \
  "ds_read_b64_tr_b16 v[214:215], v248 offset:47656\n\t" \
  "v_mfma_f32_16x16x32_bf16 v[160:163], v[216:219], v[136:139], v[160:163]\n\t" \
  "v_fma_f32 v182, |v182|, v222, v182 mul:4\n\t" \
  "v_fma_f32 v183, |v183|, v223, v183 mul:4\n\t" \
  "s_setprio 0\n\t" \
  "ds_read_b32 v230, v230 offset:10016\n\t" \
  "v_mfma_f32_16x16x32_bf16 v[152:155], v[216:219], v[132:135], v[152:155]\n\t" \
  "v_fma_f32 v181, |v181|, v221, v181 mul:4\n\t" \
  "v_fma_f32 v180, |v180|, v220, v180 mul:4\n\t" \
  "ds_read_u16 v232, v232 offset:10528\n\t" \
  "ds_read_u16_d16_hi v235, v235 offset:10528\n\t" \
  "v_mfma_f32_16x16x32_bf16 v[168:171], v[216:219], v[140:143], v[168:171]\n\t" \
  "v_cvt_pk_bf16_f32 v180, v180, v181\n\t" \
  "v_cvt_pk_bf16_f32 v181, v182, v183\n\t" \
  "s_waitcnt lgkmcnt(6)\n\t" \
  "ds_read_b64_tr_b16 v[216:217], v248 offset:45608\n\t" \
  "v_mfma_f32_16x16x32_bf16 v[148:151], v[208:211], v[96:99], 0\n\t" \
  "v_cvt_pknorm_i16_f32 v222, |v144|, |v145|\n\t" \
  "v_cvt_pknorm_i16_f32 v220, |v146|, |v147|\n\t" \
  "s_nop 0\n\t" \
  "s_waitcnt vmcnt(2)\n\t" \
  "ds_read_b64_tr_b16 v[218:219], v248 offset:48680\n\t" \
  "v_mfma_f32_16x16x32_bf16 v[156:159], v[208:211], v[100:103], 0\n\t" \
  "s_waitcnt lgkmcnt(5)\n\t" \
  "v_lshlrev_b32_sdwa v221, 2, v220 dst_sel:DWORD dst_unused:UNUSED_PAD src0_sel:DWORD src1_sel:BYTE_3\n\t" \
  "v_lshlrev_b32_sdwa v220, 2, v220 dst_sel:DWORD dst_unused:UNUSED_PAD src0_sel:DWORD src1_sel:BYTE_1\n\t" \
  "s_mov_b64 exec, 3\n\t" \
  "ds_add_rtn_u32 v246, v251, v250 offset:47168\n\t" \
  "v_mfma_f32_16x16x32_bf16 v[164:167], v[208:211], v[104:107], 0\n\t" \
  "s_mov_b64 exec, -1\n\t" \
  "v_lshlrev_b32_sdwa v223, 2, v222 dst_sel:DWORD dst_unused:UNUSED_PAD src0_sel:DWORD src1_sel:BYTE_3\n\t" \
  "v_lshlrev_b32_sdwa v222, 2, v222 dst_sel:DWORD dst_unused:UNUSED_PAD src0_sel:DWORD src1_sel:BYTE_1\n\t" \
  "s_setprio 2\n\t" \
  "s_setprio 0\n\t" \
  "ds_read_u16 v234, v234 offset:10528\n\t" \
  "v_mfma_f32_16x16x32_bf16 v[156:159], v[212:215], v[116:119], v[156:159]\n\t" \
  "v_lshlrev_b32_sdwa v226, 1, v152 dst_sel:DWORD dst_unused:UNUSED_PAD src0_sel:DWORD src1_sel:WORD_1\n\t" \
  "v_lshlrev_b32_sdwa v227, 1, v153 dst_sel:DWORD dst_unused:UNUSED_PAD src0_sel:DWORD src1_sel:WORD_1\n\t" \
  "ds_read_b32 v223, v223 offset:10016\n\t" \
  "v_mfma_f32_16x16x32_bf16 v[148:151], v[212:215], v[112:115], v[148:151]\n\t" \
  "s_waitcnt lgkmcnt(2)\n\t" \
  "v_lshlrev_b32_sdwa v225, 1, v155 dst_sel:DWORD dst_unused:UNUSED_PAD src0_sel:DWORD src1_sel:WORD_1\n\t" \
  "v_readfirstlane_b32 s4, v246\n\t" \
  "ds_read_b32 v221, v221 offset:10016\n\t" \
  "v_mfma_f32_16x16x32_bf16 v[172:175], v[208:211], v[108:111], 0\n\t" \
  "s_cmpk_lt_u32 s4, 3072\n\t" \
  "v_lshlrev_b32_sdwa v224, 1, v154 dst_sel:DWORD dst_unused:UNUSED_PAD src0_sel:DWORD src1_sel:WORD_1\n\t" \
  "s_cbranch_scc1 .Lcnx_w0_5\n\t" \
  ".Lcnx_g0_5:\n\t" \
  "ds_read_b32 v220, v220 offset:10016\n\t" \
  "v_mfma_f32_16x16x32_bf16 v[164:167], v[212:215], v[120:123], v[164:167]\n\t" \
  "v_fma_mix_f32 v228, |v196|, v228, v228 op_sel:[0,1,0] op_sel_hi:[0,1,1] clamp\n\t" \
  "v_fma_mix_f32 v229, |v197|, v229, v229 op_sel:[0,1,0] op_sel_hi:[0,1,1] clamp\n\t" \
  "ds_read_b64_tr_b16 v[210:211], v249 offset:13840\n\t" \
  "ds_read_b64_tr_b16 v[208:209], v249 offset:12304\n\t" \
  "v_mfma_f32_16x16x32_bf16 v[172:175], v[212:215], v[124:127], v[172:175]\n\t" \
  "v_fma_mix_f32 v230, |v198|, v230, v230 op_sel:[0,1,0] op_sel_hi:[0,1,1] clamp\n\t" \
  "v_fma_mix_f32 v231, |v199|, v231, v231 op_sel:[0,1,0] op_sel_hi:[0,1,1] clamp\n\t" \
  "s_waitcnt lgkmcnt(5)\n\t" \
  "ds_read_b64_tr_b16 v[212:213], v249 offset:12312\n\t" \
  "ds_read_b32 v222, v222 offset:10016\n\t" \
  "v_mfma_f32_16x16x32_bf16 v[148:151], v[216:219], v[128:131], v[148:151]\n\t" \
  "s_mov_b32 m0, s45\n\t" \
  "v_or_b32 v204, v232, v233\n\t" \
  "v_or_b32 v205, v234, v235\n\t" \
  "s_mov_b32 exec_hi, 0xffff\n\t" \
  "buffer_load_dwordx4 v252, s[16:19], s33 offen lds\n\t" \
  "v_mfma_f32_16x16x32_bf16 v[156:159], v[216:219], v[132:135], v[156:159]\n\t" \
  "s_mov_b32 exec_hi, -1\n\t" \
  "v_fma_f32 v197, |v197|, v229, v197 mul:4\n\t" \
  "v_fma_f32 v196, |v196|, v228, v196 mul:4\n\t" \
  "s_add_u32 s33, s33, s32\n\t" \
  "ds_read_u16_d16_hi v227, v227 offset:10528\n\t" \
  "ds_read_b64_tr_b16 v[214:215], v249 offset:13848\n\t" \
  "v_mfma_f32_16x16x32_bf16 v[164:167], v[216:219], v[136:139], v[164:167]\n\t" \
  "v_fma_f32 v198, |v198|, v230, v198 mul:4\n\t" \
  "v_fma_f32 v199, |v199|, v231, v199 mul:4\n\t" \
  "s_waitcnt lgkmcnt(4)\n\t" \
  "ds_read_u16_d16_hi v225, v225 offset:10528\n\t" \
  "v_mfma_f32_16x16x32_bf16 v[172:175], v[216:219], v[140:143], v[172:175]\n\t" \
  "v_cvt_pk_bf16_f32 v196, v196, v197\n\t" \
  "v_cvt_pk_bf16_f32 v197, v198, v199\n\t" \
  "ds_read_b64_tr_b16 v[216:217], v249 offset:12368\n\t" \
  "v_mfma_f32_16x16x32_bf16 v[0:3], v[208:211], v[178:181], v[0:3]\n\t" \
  "v_cvt_pknorm_i16_f32 v228, |v162|, |v163|\n\t" \
  "v_cvt_pknorm_i16_f32 v230, |v160|, |v161|\n\t" \
  "ds_read_b64_tr_b16 v[218:219], v249 offset:13904\n\t" \
  "v_mfma_f32_16x16x32_bf16 v[24:27], v[208:211], v[186:189], v[24:27]\n\t" \
  "v_lshlrev_b32_sdwa v229, 2, v228 dst_sel:DWORD dst_unused:UNUSED_PAD src0_sel:DWORD src1_sel:BYTE_3\n\t" \
  "v_lshlrev_b32_sdwa v228, 2, v228 dst_sel:DWORD dst_unused:UNUSED_PAD src0_sel:DWORD src1_sel:BYTE_1\n\t" \
  "ds_read_u16 v224, v224 offset:10528\n\t" \
  "v_mfma_f32_16x16x32_bf16 v[48:51], v[208:211], v[194:197], v[48:51]\n\t" \
  "v_lshlrev_b32_sdwa v231, 2, v230 dst_sel:DWORD dst_unused:UNUSED_PAD src0_sel:DWORD src1_sel:BYTE_3\n\t" \
  "s_add_u32 m0, s45, s35\n\t" \
  "s_setprio 1\n\t" \
  "s_waitcnt lgkmcnt(4)\n\t" \
  "ds_read_b32 v228, v228 offset:10016\n\t" \
  "v_mfma_f32_16x16x32_bf16 v[72:75], v[208:211], v[202:205], v[72:75]\n\t" \
  "v_lshlrev_b32_sdwa v232, 1, v170 dst_sel:DWORD dst_unused:UNUSED_PAD src0_sel:DWORD src1_sel:WORD_1\n\t" \
  "v_lshlrev_b32_sdwa v233, 1, v171 dst_sel:DWORD dst_unused:UNUSED_PAD src0_sel:DWORD src1_sel:WORD_1\n\t" \
  "s_mov_b32 exec_hi, 0xffff\n\t" \
  "buffer_load_dwordx4 v252, s[16:19], s34 offen lds\n\t" \
  "v_mfma_f32_16x16x32_bf16 v[4:7], v[212:215], v[178:181], v[4:7]\n\t" \
  "s_mov_b32 exec_hi, -1\n\t" \
  "s_add_u32 s34, s34, s32\n\t" \
  "v_lshlrev_b32_sdwa v235, 1, v169 dst_sel:DWORD dst_unused:UNUSED_PAD src0_sel:DWORD src1_sel:WORD_1\n\t" \
  "ds_read_u16 v226, v226 offset:10528\n\t" \
  "ds_read_b64_tr_b16 v[208:209], v249 offset:12376\n\t" \
  "v_mfma_f32_16x16x32_bf16 v[28:31], v[212:215], v[186:189], v[28:31]\n\t" \
  "v_lshlrev_b32_sdwa v234, 1, v168 dst_sel:DWORD dst_unused:UNUSED_PAD src0_sel:DWORD src1_sel:WORD_1\n\t" \
  "v_lshlrev_b32_sdwa v230, 2, v230 dst_sel:DWORD dst_unused:UNUSED_PAD src0_sel:DWORD src1_sel:BYTE_1\n\t" \
  "ds_read_b64_tr_b16 v[210:211], v249 offset:13912\n\t" \
  "ds_read_b32 v229, v229 offset:10016\n\t" \
  "v_mfma_f32_16x16x32_bf16 v[76:79], v[212:215], v[202:205], v[76:79]\n\t" \
  "v_fma_mix_f32 v223, |v145|, v223, v223 op_sel:[0,1,0] op_sel_hi:[0,1,1] clamp\n\t" \
  "v_fma_mix_f32 v222, |v144|, v222, v222 op_sel:[0,1,0] op_sel_hi:[0,1,1] clamp\n\t" \
  "ds_read_b32 v230, v230 offset:10016\n\t" \
  "v_mfma_f32_16x16x32_bf16 v[52:55], v[212:215], v[194:197], v[52:55]\n\t" \
  "v_fma_mix_f32 v220, |v146|, v220, v220 op_sel:[0,1,0] op_sel_hi:[0,1,1] clamp\n\t" \
  "v_fma_mix_f32 v221, |v147|, v221, v221 op_sel:[0,1,0] op_sel_hi:[0,1,1] clamp\n\t" \
  "s_setprio 3\n\t" \
  "s_waitcnt lgkmcnt(4)\n\t" \
  "ds_read_b32 v231, v231 offset:10016\n\t" \
  "ds_read_b64_tr_b16 v[212:213], v249 offset:12432\n\t" \
  "v_mfma_f32_16x16x32_bf16 v[8:11], v[216:219], v[178:181], v[8:11]\n\t" \
  "v_or_b32 v155, v224, v225\n\t" \
  "v_or_b32 v154, v226, v227\n\t" \
  "s_setprio 1\n\t" \
  "ds_read_u16_d16_hi v233, v233 offset:10528\n\t" \
  "ds_read_b64_tr_b16 v[214:215], v249 offset:13968\n\t" \
  "v_mfma_f32_16x16x32_bf16 v[32:35], v[216:219], v[186:189], v[32:35]\n\t" \
  "s_add_u32 s24, s24, s26\n\t" \
  "v_fma_f32 v146, |v146|, v220, v146 mul:4\n\t" \
  "v_fma_f32 v147, |v147|, v221, v147 mul:4\n\t" \
  "v_mfma_f32_16x16x32_bf16 v[56:59], v[216:219], v[194:197], v[56:59]\n\t" \
  "v_fma_f32 v145, |v145|, v223, v145 mul:4\n\t" \
  "v_fma_f32 v144, |v144|, v222, v144 mul:4\n\t" \
  "ds_read_u16 v232, v232 offset:10528\n\t" \
  "v_mfma_f32_16x16x32_bf16 v[80:83], v[216:219], v[202:205], v[80:83]\n\t" \
  "s_add_u32 m0, s25, 24600\n\t" \
  "v_cvt_pk_bf16_f32 v147, v146, v147\n\t" \
  "v_cvt_pk_bf16_f32 v146, v144, v145\n\t" \
  "s_setprio 0\n\t" \
  "s_waitcnt lgkmcnt(7)\n\t" \
  "buffer_load_dwordx4 v254, s[8:11], s24 offen lds\n\t" \
  "v_mfma_f32_16x16x32_bf16 v[12:15], v[208:211], v[178:181], v[12:15]\n\t" \
  "v_cvt_pknorm_i16_f32 v220, |v148|, |v149|\n\t" \
  "v_cvt_pknorm_i16_f32 v222, |v150|, |v151|\n\t" \
  "ds_read_b64_tr_b16 v[216:217], v249 offset:12440\n\t" \
  "ds_read_b64_tr_b16 v[218:219], v249 offset:13976\n\t" \
  "v_mfma_f32_16x16x32_bf16 v[36:39], v[208:211], v[186:189], v[36:39]\n\t" \
  "v_lshlrev_b32_sdwa v221, 2, v220 dst_sel:DWORD dst_unused:UNUSED_PAD src0_sel:DWORD src1_sel:BYTE_3\n\t" \
  "v_lshlrev_b32_sdwa v220, 2, v220 dst_sel:DWORD dst_unused:UNUSED_PAD src0_sel:DWORD src1_sel:BYTE_1\n\t" \
  "ds_read_u16_d16_hi v235, v235 offset:10528\n\t" \
  "v_mfma_f32_16x16x32_bf16 v[60:63], v[208:211], v[194:197], v[60:63]\n\t" \
  "v_lshlrev_b32_sdwa v223, 2, v222 dst_sel:DWORD dst_unused:UNUSED_PAD src0_sel:DWORD src1_sel:BYTE_3\n\t" \
  "v_lshlrev_b32_sdwa v222, 2, v222 dst_sel:DWORD dst_unused:UNUSED_PAD src0_sel:DWORD src1_sel:BYTE_1\n\t" \
  "ds_read_u16 v234, v234 offset:10528\n\t" \
  "s_waitcnt lgkmcnt(5)\n\t" \
  "v_mfma_f32_16x16x32_bf16 v[84:87], v[208:211], v[202:205], v[84:87]\n\t" \
  "s_setprio 1\n\t" \
  "v_lshlrev_b32_sdwa v225, 1, v157 dst_sel:DWORD dst_unused:UNUSED_PAD src0_sel:DWORD src1_sel:WORD_1\n\t" \
  "v_lshlrev_b32_sdwa v224, 1, v156 dst_sel:DWORD dst_unused:UNUSED_PAD src0_sel:DWORD src1_sel:WORD_1\n\t" \
  "ds_read_b64_tr_b16 v[208:209], v248 offset:49192\n\t" \
  "ds_read_b32 v220, v220 offset:10016\n\t" \
  "v_mfma_f32_16x16x32_bf16 v[16:19], v[212:215], v[178:181], v[16:19]\n\t" \
  "v_nop\n\t" \
  "s_add_u32 s27, s27, s26\n\t" \
  "s_mov_b64 exec, s[30:31]\n\t" \
  "buffer_load_dword v237, v254, s[8:11], s27 offen\n\t" \
  "v_mfma_f32_16x16x32_bf16 v[64:67], v[212:215], v[194:197], v[64:67]\n\t" \
  "s_mov_b64 exec, -1\n\t" \
  "v_fma_mix_f32 v229, |v163|, v229, v229 op_sel:[0,1,0] op_sel_hi:[0,1,1] clamp\n\t" \
  "v_lshlrev_b32_sdwa v227, 1, v159 dst_sel:DWORD dst_unused:UNUSED_PAD src0_sel:DWORD src1_sel:WORD_1\n\t" \
  "s_setprio 2\n\t" \
  "ds_read_b32 v223, v223 offset:10016\n\t" \
  "ds_read_b32 v221, v221 offset:10016\n\t" \
  "v_mfma_f32_16x16x32_bf16 v[88:91], v[212:215], v[202:205], v[88:91]\n\t" \
  "s_setprio 1\n\t" \
  "v_fma_mix_f32 v231, |v161|, v231, v231 op_sel:[0,1,0] op_sel_hi:[0,1,1] clamp\n\t" \
  "v_fma_mix_f32 v230, |v160|, v230, v230 op_sel:[0,1,0] op_sel_hi:[0,1,1] clamp\n\t" \
  "ds_read_b64_tr_b16 v[210:211], v248 offset:50216\n\t" \
  "v_mfma_f32_16x16x32_bf16 v[40:43], v[212:215], v[186:189], v[40:43]\n\t" \
  "s_waitcnt lgkmcnt(5)\n\t" \
  "v_fma_mix_f32 v228, |v162|, v228, v228 op_sel:[0,1,0] op_sel_hi:[0,1,1] clamp\n\t" \
  "v_lshlrev_b32_sdwa v226, 1, v158 dst_sel:DWORD dst_unused:UNUSED_PAD src0_sel:DWORD src1_sel:WORD_1\n\t" \
  "ds_read_b32 v222, v222 offset:10016\n\t" \
  "ds_read_b64_tr_b16 v[212:213], v248 offset:52264\n\t" \
  "v_mfma_f32_16x16x32_bf16 v[20:23], v[216:219], v[178:181], v[20:23]\n\t" \
  "v_or_b32 v171, v232, v233\n\t" \
  "v_or_b32 v170, v234, v235\n\t" \
  "ds_read_u16_d16_hi v225, v225 offset:10528\n\t" \
  "v_mfma_f32_16x16x32_bf16 v[68:71], v[216:219], v[194:197], v[68:71]\n\t" \
  "s_setprio 2\n\t" \
  "v_fma_f32 v160, |v160|, v230, v160 mul:4\n\t" \
  "v_fma_f32 v163, |v163|, v229, v163 mul:4\n\t" \
  "s_waitcnt lgkmcnt(3)\n\t" \
  "ds_read_u16 v224, v224 offset:10528\n\t" \
  "v_mfma_f32_16x16x32_bf16 v[176:179], v[208:211], v[96:99], 0\n\t" \
  "v_lshlrev_b32_sdwa v232, 1, v172 dst_sel:DWORD dst_unused:UNUSED_PAD src0_sel:DWORD src1_sel:WORD_1\n\t" \
  "v_fma_f32 v161, |v161|, v231, v161 mul:4\n\t" \
  "ds_read_b64_tr_b16 v[214:215], v248 offset:53288\n\t" \
  "v_mfma_f32_16x16x32_bf16 v[44:47], v[216:219], v[186:189], v[44:47]\n\t" \
  "v_fma_f32 v162, |v162|, v228, v162 mul:4\n\t" \
  "v_cvt_pknorm_i16_f32 v230, |v166|, |v167|\n\t" \
  "ds_read_u16_d16_hi v227, v227 offset:10528\n\t" \
  "v_mfma_f32_16x16x32_bf16 v[92:95], v[216:219], v[202:205], v[92:95]\n\t" \
  "v_cvt_pk_bf16_f32 v163, v162, v163\n\t" \
  "v_cvt_pk_bf16_f32 v162, v160, v161\n\t" \
  "ds_read_b64_tr_b16 v[216:217], v248 offset:51240\n\t" \
  "v_mfma_f32_16x16x32_bf16 v[192:195], v[208:211], v[104:107], 0\n\t" \
  "v_cvt_pknorm_i16_f32 v228, |v164|, |v165|\n\t" \
  "v_lshlrev_b32_sdwa v231, 2, v230 dst_sel:DWORD dst_unused:UNUSED_PAD src0_sel:DWORD src1_sel:BYTE_3\n\t" \
  "ds_read_b64_tr_b16 v[218:219], v248 offset:54312\n\t" \
  "v_mfma_f32_16x16x32_bf16 v[184:187], v[208:211], v[100:103], 0\n\t" \
  "v_lshlrev_b32_sdwa v229, 2, v228 dst_sel:DWORD dst_unused:UNUSED_PAD src0_sel:DWORD src1_sel:BYTE_3\n\t" \
  "v_lshlrev_b32_sdwa v228, 2, v228 dst_sel:DWORD dst_unused:UNUSED_PAD src0_sel:DWORD src1_sel:BYTE_1\n\t" \
  "ds_read_u16 v226, v226 offset:10528\n\t" \
  "s_waitcnt lgkmcnt(4)\n\t" \
  "v_mfma_f32_16x16x32_bf16 v[200:203], v[208:211], v[108:111], 0\n\t" \
  "v_lshlrev_b32_sdwa v230, 2, v230 dst_sel:DWORD dst_unused:UNUSED_PAD src0_sel:DWORD src1_sel:BYTE_1\n\t" \
  "v_lshlrev_b32_sdwa v233, 1, v173 dst_sel:DWORD dst_unused:UNUSED_PAD src0_sel:DWORD src1_sel:WORD_1\n\t" \
  "ds_read_b64_tr_b16 v[208:209], v248 offset:49704\n\t" \
  "v_mfma_f32_16x16x32_bf16 v[176:179], v[212:215], v[112:115], v[176:179]\n\t" \
  "v_lshlrev_b32_sdwa v235, 1, v175 dst_sel:DWORD dst_unused:UNUSED_PAD src0_sel:DWORD src1_sel:WORD_1\n\t" \
  "v_fma_mix_f32 v221, |v149|, v221, v221 op_sel:[0,1,0] op_sel_hi:[0,1,1] clamp\n\t" \
  "ds_read_b32 v231, v231 offset:10016\n\t" \
  "v_mfma_f32_16x16x32_bf16 v[200:203], v[212:215], v[124:127], v[200:203]\n\t" \
  "v_lshlrev_b32_sdwa v234, 1, v174 dst_sel:DWORD dst_unused:UNUSED_PAD src0_sel:DWORD src1_sel:WORD_1\n\t" \
  "ds_read_b32 v228, v228 offset:10016\n\t" \
  "ds_read_b32 v229, v229 offset:10016\n\t" \
  "v_mfma_f32_16x16x32_bf16 v[192:195], v[212:215], v[120:123], v[192:195]\n\t" \
  "s_setprio 1\n\t" \
  "v_fma_mix_f32 v220, |v148|, v220, v220 op_sel:[0,1,0] op_sel_hi:[0,1,1] clamp\n\t" \
  "ds_read_b64_tr_b16 v[210:211], v248 offset:50728\n\t" \
  "v_mfma_f32_16x16x32_bf16 v[184:187], v[212:215], v[116:119], v[184:187]\n\t" \
  "v_fma_mix_f32 v222, |v150|, v222, v222 op_sel:[0,1,0] op_sel_hi:[0,1,1] clamp\n\t" \
  "v_fma_mix_f32 v223, |v151|, v223, v223 op_sel:[0,1,0] op_sel_hi:[0,1,1] clamp\n\t" \
  "s_waitcnt lgkmcnt(6)\n\t" \
  "ds_read_b64_tr_b16 v[212:213], v248 offset:52776\n\t" \
  "ds_read_u16_d16_hi v233, v233 offset:10528\n\t" \
  "v_mfma_f32_16x16x32_bf16 v[176:179], v[216:219], v[128:131], v[176:179]\n\t" \
  "s_waitcnt lgkmcnt(7)\n\t" \
  "v_or_b32 v156, v224, v225\n\t" \
  "v_or_b32 v157, v226, v227\n\t" \
  "ds_read_b64_tr_b16 v[214:215], v248 offset:53800\n\t" \
  "v_mfma_f32_16x16x32_bf16 v[192:195], v[216:219], v[136:139], v[192:195]\n\t" \
  "v_fma_f32 v150, |v150|, v222, v150 mul:4\n\t" \
  "v_fma_f32 v151, |v151|, v223, v151 mul:4\n\t" \
  "s_setprio 0\n\t" \
  "ds_read_b32 v230, v230 offset:10016\n\t" \
  "v_mfma_f32_16x16x32_bf16 v[184:187], v[216:219], v[132:135], v[184:187]\n\t" \
  "v_fma_f32 v149, |v149|, v221, v149 mul:4\n\t" \
  "v_fma_f32 v148, |v148|, v220, v148 mul:4\n\t" \
  "ds_read_u16 v232, v232 offset:10528\n\t" \
  "ds_read_u16_d16_hi v235, v235 offset:10528\n\t" \
  "v_mfma_f32_16x16x32_bf16 v[200:203], v[216:219], v[140:143], v[200:203]\n\t" \
  "v_cvt_pk_bf16_f32 v148, v148, v149\n\t" \
  "v_cvt_pk_bf16_f32 v149, v150, v151\n\t" \
  "s_waitcnt lgkmcnt(6)\n\t" \
  "ds_read_b64_tr_b16 v[216:217], v248 offset:51752\n\t" \
  "v_mfma_f32_16x16x32_bf16 v[180:183], v[208:211], v[96:99], 0\n\t" \
  "v_cvt_pknorm_i16_f32 v222, |v176|, |v177|\n\t" \
  "v_cvt_pknorm_i16_f32 v220, |v178|, |v179|\n\t" \
  "s_nop 0\n\t" \
  "s_waitcnt vmcnt(2)\n\t" \
  "ds_read_b64_tr_b16 v[218:219], v248 offset:54824\n\t" \
  "v_mfma_f32_16x16x32_bf16 v[188:191], v[208:211], v[100:103], 0\n\t" \
  "s_waitcnt lgkmcnt(5)\n\t" \
  "v_lshlrev_b32_sdwa v221, 2, v220 dst_sel:DWORD dst_unused:UNUSED_PAD src0_sel:DWORD src1_sel:BYTE_3\n\t" \
  "v_lshlrev_b32_sdwa v220, 2, v220 dst_sel:DWORD dst_unused:UNUSED_PAD src0_sel:DWORD src1_sel:BYTE_1\n\t" \
  "s_mov_b64 exec, 3\n\t" \
  "ds_add_rtn_u32 v246, v251, v250 offset:47176\n\t" \
  "v_mfma_f32_16x16x32_bf16 v[196:199], v[208:211], v[104:107], 0\n\t" \
  "s_mov_b64 exec, -1\n\t" \
  "v_lshlrev_b32_sdwa v223, 2, v222 dst_sel:DWORD dst_unused:UNUSED_PAD src0_sel:DWORD src1_sel:BYTE_3\n\t" \
  "v_lshlrev_b32_sdwa v222, 2, v222 dst_sel:DWORD dst_unused:UNUSED_PAD src0_sel:DWORD src1_sel:BYTE_1\n\t" \
  "s_setprio 2\n\t" \
  "s_setprio 0\n\t" \
  "ds_read_u16 v234, v234 offset:10528\n\t" \
  "v_mfma_f32_16x16x32_bf16 v[188:191], v[212:215], v[116:119], v[188:191]\n\t" \
  "v_lshlrev_b32_sdwa v226, 1, v184 dst_sel:DWORD dst_unused:UNUSED_PAD src0_sel:DWORD src1_sel:WORD_1\n\t" \
  "v_lshlrev_b32_sdwa v227, 1, v185 dst_sel:DWORD dst_unused:UNUSED_PAD src0_sel:DWORD src1_sel:WORD_1\n\t" \
  "ds_read_b32 v223, v223 offset:10016\n\t" \
  "v_mfma_f32_16x16x32_bf16 v[180:183], v[212:215], v[112:115], v[180:183]\n\t" \
  "s_waitcnt lgkmcnt(2)\n\t" \
  "v_lshlrev_b32_sdwa v225, 1, v187 dst_sel:DWORD dst_unused:UNUSED_PAD src0_sel:DWORD src1_sel:WORD_1\n\t" \
  "v_readfirstlane_b32 s4, v246\n\t" \
  "ds_read_b32 v221, v221 offset:10016\n\t" \
  "v_mfma_f32_16x16x32_bf16 v[204:207], v[208:211], v[108:111], 0\n\t" \
  "s_cmpk_lt_u32 s4, 3072\n\t" \
  "v_lshlrev_b32_sdwa v224, 1, v186 dst_sel:DWORD dst_unused:UNUSED_PAD src0_sel:DWORD src1_sel:WORD_1\n\t" \
  "s_cbranch_scc1 .Lcnx_w0_6\n\t" \
  ".Lcnx_g0_6:\n\t" \
  "ds_read_b32 v220, v220 offset:10016\n\t" \
  "v_mfma_f32_16x16x32_bf16 v[196:199], v[212:215], v[120:123], v[196:199]\n\t" \
  "v_fma_mix_f32 v228, |v164|, v228, v228 op_sel:[0,1,0] op_sel_hi:[0,1,1] clamp\n\t" \
  "v_fma_mix_f32 v229, |v165|, v229, v229 op_sel:[0,1,0] op_sel_hi:[0,1,1] clamp\n\t" \
  "ds_read_b64_tr_b16 v[210:211], v249 offset:26136\n\t" \
  "ds_read_b64_tr_b16 v[208:209], v249 offset:24600\n\t" \
  "v_mfma_f32_16x16x32_bf16 v[204:207], v[212:215], v[124:127], v[204:207]\n\t" \
  "v_fma_mix_f32 v230, |v166|, v230, v230 op_sel:[0,1,0] op_sel_hi:[0,1,1] clamp\n\t" \
  "v_fma_mix_f32 v231, |v167|, v231, v231 op_sel:[0,1,0] op_sel_hi:[0,1,1] clamp\n\t" \
  "s_waitcnt lgkmcnt(5)\n\t" \
  "ds_read_b64_tr_b16 v[212:213], v249 offset:24608\n\t" \
  "ds_read_b32 v222, v222 offset:10016\n\t" \
  "v_mfma_f32_16x16x32_bf16 v[180:183], v[216:219], v[128:131], v[180:183]\n\t" \
  "s_mov_b32 m0, s46\n\t" \
  "v_or_b32 v172, v232, v233\n\t" \
  "v_or_b32 v173, v234, v235\n\t" \
  "s_mov_b32 exec_hi, 0xffff\n\t" \
  "buffer_load_dwordx4 v252, s[16:19], s33 offen lds\n\t" \
  "v_mfma_f32_16x16x32_bf16 v[188:191], v[216:219], v[132:135], v[188:191]\n\t" \
  "s_mov_b32 exec_hi, -1\n\t" \
  "v_fma_f32 v165, |v165|, v229, v165 mul:4\n\t" \
  "v_fma_f32 v164, |v164|, v228, v164 mul:4\n\t" \
  "s_add_u32 s33, s33, s32\n\t" \
  "ds_read_u16_d16_hi v227, v227 offset:10528\n\t" \
  "ds_read_b64_tr_b16 v[214:215], v249 offset:26144\n\t" \
  "v_mfma_f32_16x16x32_bf16 v[196:199], v[216:219], v[136:139], v[196:199]\n\t" \
  "v_fma_f32 v166, |v166|, v230, v166 mul:4\n\t" \
  "v_fma_f32 v167, |v167|, v231, v167 mul:4\n\t" \
  "s_waitcnt lgkmcnt(4)\n\t" \
  "ds_read_u16_d16_hi v225, v225 offset:10528\n\t" \
  "v_mfma_f32_16x16x32_bf16 v[204:207], v[216:219], v[140:143], v[204:207]\n\t" \
  "v_cvt_pk_bf16_f32 v164, v164, v165\n\t" \
  "v_cvt_pk_bf16_f32 v165, v166, v167\n\t" \
  "ds_read_b64_tr_b16 v[216:217], v249 offset:24664\n\t" \
  "v_mfma_f32_16x16x32_bf16 v[0:3], v[208:211], v[146:149], v[0:3]\n\t" \
  "v_cvt_pknorm_i16_f32 v228, |v194|, |v195|\n\t" \
  "v_cvt_pknorm_i16_f32 v230, |v192|, |v193|\n\t" \
  "ds_read_b64_tr_b16 v[218:219], v249 offset:26200\n\t" \
  "v_mfma_f32_16x16x32_bf16 v[24:27], v[208:211], v[154:157], v[24:27]\n\t" \
  "v_lshlrev_b32_sdwa v229, 2, v228 dst_sel:DWORD dst_unused:UNUSED_PAD src0_sel:DWORD src1_sel:BYTE_3\n\t" \
  "v_lshlrev_b32_sdwa v228, 2, v228 dst_sel:DWORD dst_unused:UNUSED_PAD src0_sel:DWORD src1_sel:BYTE_1\n\t" \
  "ds_read_u16 v224, v224 offset:10528\n\t" \
  "v_mfma_f32_16x16x32_bf16 v[48:51], v[208:211], v[162:165], v[48:51]\n\t" \
  "v_lshlrev_b32_sdwa v231, 2, v230 dst_sel:DWORD dst_unused:UNUSED_PAD src0_sel:DWORD src1_sel:BYTE_3\n\t" \
  "s_add_u32 m0, s46, s35\n\t" \
  "s_setprio 1\n\t" \
  "s_waitcnt lgkmcnt(4)\n\t" \
  "ds_read_b32 v228, v228 offset:10016\n\t" \
  "v_mfma_f32_16x16x32_bf16 v[72:75], v[208:211], v[170:173], v[72:75]\n\t" \
  "v_lshlrev_b32_sdwa v232, 1, v202 dst_sel:DWORD dst_unused:UNUSED_PAD src0_sel:DWORD src1_sel:WORD_1\n\t" \
  "v_lshlrev_b32_sdwa v233, 1, v203 dst_sel:DWORD dst_unused:UNUSED_PAD src0_sel:DWORD src1_sel:WORD_1\n\t" \
  "s_mov_b32 exec_hi, 0xffff\n\t" \
  "buffer_load_dwordx4 v252, s[16:19], s34 offen lds\n\t" \
  "v_mfma_f32_16x16x32_bf16 v[4:7], v[212:215], v[146:149], v[4:7]\n\t" \
  "s_mov_b32 exec_hi, -1\n\t" \
  "s_add_u32 s34, s34, s32\n\t" \
  "v_lshlrev_b32_sdwa v235, 1, v201 dst_sel:DWORD dst_unused:UNUSED_PAD src0_sel:DWORD src1_sel:WORD_1\n\t" \
  "ds_read_u16 v226, v226 offset:10528\n\t" \
  "ds_read_b64_tr_b16 v[208:209], v249 offset:24672\n\t" \
  "v_mfma_f32_16x16x32_bf16 v[28:31], v[212:215], v[154:157], v[28:31]\n\t" \
  "v_lshlrev_b32_sdwa v234, 1, v200 dst_sel:DWORD dst_unused:UNUSED_PAD src0_sel:DWORD src1_sel:WORD_1\n\t" \
  "v_lshlrev_b32_sdwa v230, 2, v230 dst_sel:DWORD dst_unused:UNUSED_PAD src0_sel:DWORD src1_sel:BYTE_1\n\t" \
  "ds_read_b64_tr_b16 v[210:211], v249 offset:26208\n\t" \
  "ds_read_b32 v229, v229 offset:10016\n\t" \
  "v_mfma_f32_16x16x32_bf16 v[76:79], v[212:215], v[170:173], v[76:79]\n\t" \
  "v_fma_mix_f32 v223, |v177|, v223, v223 op_sel:[0,1,0] op_sel_hi:[0,1,1] clamp\n\t" \
  "v_fma_mix_f32 v222, |v176|, v222, v222 op_sel:[0,1,0] op_sel_hi:[0,1,1] clamp\n\t" \
  "ds_read_b32 v230, v230 offset:10016\n\t" \
  "v_mfma_f32_16x16x32_bf16 v[52:55], v[212:215], v[162:165], v[52:55]\n\t" \
  "v_fma_mix_f32 v220, |v178|, v220, v220 op_sel:[0,1,0] op_sel_hi:[0,1,1] clamp\n\t" \
  "v_fma_mix_f32 v221, |v179|, v221, v221 op_sel:[0,1,0] op_sel_hi:[0,1,1] clamp\n\t" \
  "s_setprio 3\n\t" \
  "s_waitcnt lgkmcnt(4)\n\t" \
  "ds_read_b32 v231, v231 offset:10016\n\t" \
  "ds_read_b64_tr_b16 v[212:213], v249 offset:24728\n\t" \
  "v_mfma_f32_16x16x32_bf16 v[8:11], v[216:219], v[146:149], v[8:11]\n\t" \
  "v_or_b32 v187, v224, v225\n\t" \
  "v_or_b32 v186, v226, v227\n\t" \
  "s_setprio 1\n\t" \
  "ds_read_u16_d16_hi v233, v233 offset:10528\n\t" \
  "ds_read_b64_tr_b16 v[214:215], v249 offset:26264\n\t" \
  "v_mfma_f32_16x16x32_bf16 v[32:35], v[216:219], v[154:157], v[32:35]\n\t" \
  "s_add_u32 s24, s24, s26\n\t" \
  "v_fma_f32 v178, |v178|, v220, v178 mul:4\n\t" \
  "v_fma_f32 v179, |v179|, v221, v179 mul:4\n\t" \
  "v_mfma_f32_16x16x32_bf16 v[56:59], v[216:219], v[162:165], v[56:59]\n\t" \
  "v_fma_f32 v177, |v177|, v223, v177 mul:4\n\t" \
  "v_fma_f32 v176, |v176|, v222, v176 mul:4\n\t" \
  "ds_read_u16 v232, v232 offset:10528\n\t" \
  "v_mfma_f32_16x16x32_bf16 v[80:83], v[216:219], v[170:173], v[80:83]\n\t" \
  "s_add_u32 m0, s25, 32800\n\t" \
  "v_cvt_pk_bf16_f32 v179, v178, v179\n\t" \
  "v_cvt_pk_bf16_f32 v178, v176, v177\n\t" \
  "s_setprio 0\n\t" \
  "s_waitcnt lgkmcnt(7)\n\t" \
  "buffer_load_dwordx4 v254, s[8:11], s24 offen lds\n\t" \
  "v_mfma_f32_16x16x32_bf16 v[12:15], v[208:211], v[146:149], v[12:15]\n\t" \
  "v_cvt_pknorm_i16_f32 v220, |v180|, |v181|\n\t" \
  "v_cvt_pknorm_i16_f32 v222, |v182|, |v183|\n\t" \
  "ds_read_b64_tr_b16 v[216:217], v249 offset:24736\n\t" \
  "ds_read_b64_tr_b16 v[218:219], v249 offset:26272\n\t" \
  "v_mfma_f32_16x16x32_bf16 v[36:39], v[208:211], v[154:157], v[36:39]\n\t" \
  "v_lshlrev_b32_sdwa v221, 2, v220 dst_sel:DWORD dst_unused:UNUSED_PAD src0_sel:DWORD src1_sel:BYTE_3\n\t" \
  "v_lshlrev_b32_sdwa v220, 2, v220 dst_sel:DWORD dst_unused:UNUSED_PAD src0_sel:DWORD src1_sel:BYTE_1\n\t" \
  "ds_read_u16_d16_hi v235, v235 offset:10528\n\t" \
  "v_mfma_f32_16x16x32_bf16 v[60:63], v[208:211], v[162:165], v[60:63]\n\t" \
  "v_lshlrev_b32_sdwa v223, 2, v222 dst_sel:DWORD dst_unused:UNUSED_PAD src0_sel:DWORD src1_sel:BYTE_3\n\t" \
  "v_lshlrev_b32_sdwa v222, 2, v222 dst_sel:DWORD dst_unused:UNUSED_PAD src0_sel:DWORD src1_sel:BYTE_1\n\t" \
  "ds_read_u16 v234, v234 offset:10528\n\t" \
  "s_waitcnt lgkmcnt(5)\n\t" \
  "v_mfma_f32_16x16x32_bf16 v[84:87], v[208:211], v[170:173], v[84:87]\n\t" \
  "s_setprio 1\n\t" \
  "v_lshlrev_b32_sdwa v225, 1, v189 dst_sel:DWORD dst_unused:UNUSED_PAD src0_sel:DWORD src1_sel:WORD_1\n\t" \
  "v_lshlrev_b32_sdwa v224, 1, v188 dst_sel:DWORD dst_unused:UNUSED_PAD src0_sel:DWORD src1_sel:WORD_1\n\t" \
  "ds_read_b64_tr_b16 v[208:209], v248 offset:36904\n\t" \
  "ds_read_b32 v220, v220 offset:10016\n\t" \
  "v_mfma_f32_16x16x32_bf16 v[16:19], v[212:215], v[146:149], v[16:19]\n\t" \
  "v_nop\n\t" \
  "s_add_u32 s27, s27, s26\n\t" \
  "s_mov_b64 exec, s[30:31]\n\t" \
  "buffer_load_dword v237, v254, s[8:11], s27 offen\n\t" \
  "v_mfma_f32_16x16x32_bf16 v[64:67], v[212:215], v[162:165], v[64:67]\n\t" \
  "s_mov_b64 exec, -1\n\t" \
  "v_fma_mix_f32 v229, |v195|, v229, v229 op_sel:[0,1,0] op_sel_hi:[0,1,1] clamp\n\t" \
  "v_lshlrev_b32_sdwa v227, 1, v191 dst_sel:DWORD dst_unused:UNUSED_PAD src0_sel:DWORD src1_sel:WORD_1\n\t" \
  "s_setprio 2\n\t" \
  "ds_read_b32 v223, v223 offset:10016\n\t" \
  "ds_read_b32 v221, v221 offset:10016\n\t" \
  "v_mfma_f32_16x16x32_bf16 v[88:91], v[212:215], v[170:173], v[88:91]\n\t" \
  "s_setprio 1\n\t" \
  "v_fma_mix_f32 v231, |v193|, v231, v231 op_sel:[0,1,0] op_sel_hi:[0,1,1] clamp\n\t" \
  "v_fma_mix_f32 v230, |v192|, v230, v230 op_sel:[0,1,0] op_sel_hi:[0,1,1] clamp\n\t" \
  "ds_read_b64_tr_b16 v[210:211], v248 offset:37928\n\t" \
  "v_mfma_f32_16x16x32_bf16 v[40:43], v[212:215], v[154:157], v[40:43]\n\t" \
  "s_waitcnt lgkmcnt(5)\n\t" \
  "v_fma_mix_f32 v228, |v194|, v228, v228 op_sel:[0,1,0] op_sel_hi:[0,1,1] clamp\n\t" \
  "v_lshlrev_b32_sdwa v226, 1, v190 dst_sel:DWORD dst_unused:UNUSED_PAD src0_sel:DWORD src1_sel:WORD_1\n\t" \
  "ds_read_b32 v222, v222 offset:10016\n\t" \
  "ds_read_b64_tr_b16 v[212:213], v248 offset:39976\n\t" \
  "v_mfma_f32_16x16x32_bf16 v[20:23], v[216:219], v[146:149], v[20:23]\n\t" \
  "v_or_b32 v203, v232, v233\n\t" \
  "v_or_b32 v202, v234, v235\n\t" \
  "ds_read_u16_d16_hi v225, v225 offset:10528\n\t" \
  "v_mfma_f32_16x16x32_bf16 v[68:71], v[216:219], v[162:165], v[68:71]\n\t" \
  "s_setprio 2\n\t" \
  "v_fma_f32 v192, |v192|, v230, v192 mul:4\n\t" \
  "v_fma_f32 v195, |v195|, v229, v195 mul:4\n\t" \
  "s_waitcnt lgkmcnt(3)\n\t" \
  "ds_read_u16 v224, v224 offset:10528\n\t" \
  "v_mfma_f32_16x16x32_bf16 v[144:147], v[208:211], v[96:99], 0\n\t" \
  "v_lshlrev_b32_sdwa v232, 1, v204 dst_sel:DWORD dst_unused:UNUSED_PAD src0_sel:DWORD src1_sel:WORD_1\n\t" \
  "v_fma_f32 v193, |v193|, v231, v193 mul:4\n\t" \
  "ds_read_b64_tr_b16 v[214:215], v248 offset:41000\n\t" \
  "v_mfma_f32_16x16x32_bf16 v[44:47], v[216:219], v[154:157], v[44:47]\n\t" \
  "v_fma_f32 v194, |v194|, v228, v194 mul:4\n\t" \
  "v_cvt_pknorm_i16_f32 v230, |v198|, |v199|\n\t" \
  "ds_read_u16_d16_hi v227, v227 offset:10528\n\t" \
  "v_mfma_f32_16x16x32_bf16 v[92:95], v[216:219], v[170:173], v[92:95]\n\t" \
  "v_cvt_pk_bf16_f32 v195, v194, v195\n\t" \
  "v_cvt_pk_bf16_f32 v194, v192, v193\n\t" \
  "ds_read_b64_tr_b16 v[216:217], v248 offset:38952\n\t" \
  "v_mfma_f32_16x16x32_bf16 v[160:163], v[208:211], v[104:107], 0\n\t" \
  "v_cvt_pknorm_i16_f32 v228, |v196|, |v197|\n\t" \
  "v_lshlrev_b32_sdwa v231, 2, v230 dst_sel:DWORD dst_unused:UNUSED_PAD src0_sel:DWORD src1_sel:BYTE_3\n\t" \
  "ds_read_b64_tr_b16 v[218:219], v248 offset:42024\n\t" \
  "v_mfma_f32_16x16x32_bf16 v[152:155], v[208:211], v[100:103], 0\n\t" \
  "v_lshlrev_b32_sdwa v229, 2, v228 dst_sel:DWORD dst_unused:UNUSED_PAD src0_sel:DWORD src1_sel:BYTE_3\n\t" \
  "v_lshlrev_b32_sdwa v228, 2, v228 dst_sel:DWORD dst_unused:UNUSED_PAD src0_sel:DWORD src1_sel:BYTE_1\n\t" \
  "ds_read_u16 v226, v226 offset:10528\n\t" \
  "s_waitcnt lgkmcnt(4)\n\t" \
  "v_mfma_f32_16x16x32_bf16 v[168:171], v[208:211], v[108:111], 0\n\t" \
  "v_lshlrev_b32_sdwa v230, 2, v230 dst_sel:DWORD dst_unused:UNUSED_PAD src0_sel:DWORD src1_sel:BYTE_1\n\t" \
  "v_lshlrev_b32_sdwa v233, 1, v205 dst_sel:DWORD dst_unused:UNUSED_PAD src0_sel:DWORD src1_sel:WORD_1\n\t" \
  "ds_read_b64_tr_b16 v[208:209], v248 offset:37416\n\t" \
  "v_mfma_f32_16x16x32_bf16 v[144:147], v[212:215], v[112:115], v[144:147]\n\t" \
  "v_lshlrev_b32_sdwa v235, 1, v207 dst_sel:DWORD dst_unused:UNUSED_PAD src0_sel:DWORD src1_sel:WORD_1\n\t" \
  "v_fma_mix_f32 v221, |v181|, v221, v221 op_sel:[0,1,0] op_sel_hi:[0,1,1] clamp\n\t" \
  "ds_read_b32 v231, v231 offset:10016\n\t" \
  "v_mfma_f32_16x16x32_bf16 v[168:171], v[212:215], v[124:127], v[168:171]\n\t" \
  "v_lshlrev_b32_sdwa v234, 1, v206 dst_sel:DWORD dst_unused:UNUSED_PAD src0_sel:DWORD src1_sel:WORD_1\n\t" \
  "ds_read_b32 v228, v228 offset:10016\n\t" \
  "ds_read_b32 v229, v229 offset:10016\n\t" \
  "v_mfma_f32_16x16x32_bf16 v[160:163], v[212:215], v[120:123], v[160:163]\n\t" \
  "s_setprio 1\n\t" \
  "v_fma_mix_f32 v220, |v180|, v220, v220 op_sel:[0,1,0] op_sel_hi:[0,1,1] clamp\n\t" \
  "ds_read_b64_tr_b16 v[210:211], v248 offset:38440\n\t" \
  "v_mfma_f32_16x16x32_bf16 v[152:155], v[212:215], v[116:119], v[152:155]\n\t" \
  "v_fma_mix_f32 v222, |v182|, v222, v222 op_sel:[0,1,0] op_sel_hi:[0,1,1] clamp\n\t" \
  "v_fma_mix_f32 v223, |v183|, v223, v223 op_sel:[0,1,0] op_sel_hi:[0,1,1] clamp\n\t" \
  "s_waitcnt lgkmcnt(6)\n\t" \
  "ds_read_b64_tr_b16 v[212:213], v248 offset:40488\n\t" \
  "ds_read_u16_d16_hi v233, v233 offset:10528\n\t" \
  "v_mfma_f32_16x16x32_bf16 v[144:147], v[216:219], v[128:131], v[144:147]\n\t" \
  "s_waitcnt lgkmcnt(7)\n\t" \
  "v_or_b32 v188, v224, v225\n\t" \
  "v_or_b32 v189, v226, v227\n\t" \
  "ds_read_b64_tr_b16 v[214:215], v248 offset:41512\n\t" \
  "v_mfma_f32_16x16x32_bf16 v[160:163], v[216:219], v[136:139], v[160:163]\n\t" \
  "v_fma_f32 v182, |v182|, v222, v182 mul:4\n\t" \
  "v_fma_f32 v183, |v183|, v223, v183 mul:4\n\t" \
  "s_setprio 0\n\t" \
  "ds_read_b32 v230, v230 offset:10016\n\t" \
  "v_mfma_f32_16x16x32_bf16 v[152:155], v[216:219], v[132:135], v[152:155]\n\t" \
  "v_fma_f32 v181, |v181|, v221, v181 mul:4\n\t" \
  "v_fma_f32 v180, |v180|, v220, v180 mul:4\n\t" \
  "ds_read_u16 v232, v232 offset:10528\n\t" \
  "ds_read_u16_d16_hi v235, v235 offset:10528\n\t" \
  "v_mfma_f32_16x16x32_bf16 v[168:171], v[216:219], v[140:143], v[168:171]\n\t" \
  "v_cvt_pk_bf16_f32 v180, v180, v181\n\t" \
  "v_cvt_pk_bf16_f32 v181, v182, v183\n\t" \
  "s_waitcnt lgkmcnt(6)\n\t" \
  "ds_read_b64_tr_b16 v[216:217], v248 offset:39464\n\t" \
  "v_mfma_f32_16x16x32_bf16 v[148:151], v[208:211], v[96:99], 0\n\t" \
  "v_cvt_pknorm_i16_f32 v222, |v144|, |v145|\n\t" \
  "v_cvt_pknorm_i16_f32 v220, |v146|, |v147|\n\t" \
  "s_nop 0\n\t" \
  "s_waitcnt vmcnt(2)\n\t" \
  "ds_read_b64_tr_b16 v[218:219], v248 offset:42536\n\t" \
  "v_mfma_f32_16x16x32_bf16 v[156:159], v[208:211], v[100:103], 0\n\t" \
  "s_waitcnt lgkmcnt(5)\n\t" \
  "v_lshlrev_b32_sdwa v221, 2, v220 dst_sel:DWORD dst_unused:UNUSED_PAD src0_sel:DWORD src1_sel:BYTE_3\n\t" \
  "v_lshlrev_b32_sdwa v220, 2, v220 dst_sel:DWORD dst_unused:UNUSED_PAD src0_sel:DWORD src1_sel:BYTE_1\n\t" \
  "s_mov_b64 exec, 3\n\t" \
  "ds_add_rtn_u32 v246, v251, v250 offset:47184\n\t" \
  "v_mfma_f32_16x16x32_bf16 v[164:167], v[208:211], v[104:107], 0\n\t" \
  "s_mov_b64 exec, -1\n\t" \
  "v_lshlrev_b32_sdwa v223, 2, v222 dst_sel:DWORD dst_unused:UNUSED_PAD src0_sel:DWORD src1_sel:BYTE_3\n\t" \
  "v_lshlrev_b32_sdwa v222, 2, v222 dst_sel:DWORD dst_unused:UNUSED_PAD src0_sel:DWORD src1_sel:BYTE_1\n\t" \
  "s_setprio 2\n\t" \
  "s_setprio 0\n\t" \
  "ds_read_u16 v234, v234 offset:10528\n\t" \
  "v_mfma_f32_16x16x32_bf16 v[156:159], v[212:215], v[116:119], v[156:159]\n\t" \
  "v_lshlrev_b32_sdwa v226, 1, v152 dst_sel:DWORD dst_unused:UNUSED_PAD src0_sel:DWORD src1_sel:WORD_1\n\t" \
  "v_lshlrev_b32_sdwa v227, 1, v153 dst_sel:DWORD dst_unused:UNUSED_PAD src0_sel:DWORD src1_sel:WORD_1\n\t" \
  "ds_read_b32 v223, v223 offset:10016\n\t" \
  "v_mfma_f32_16x16x32_bf16 v[148:151], v[212:215], v[112:115], v[148:151]\n\t" \
  "s_waitcnt lgkmcnt(2)\n\t" \
  "v_lshlrev_b32_sdwa v225, 1, v155 dst_sel:DWORD dst_unused:UNUSED_PAD src0_sel:DWORD src1_sel:WORD_1\n\t" \
  "v_readfirstlane_b32 s4, v246\n\t" \
  "ds_read_b32 v221, v221 offset:10016\n\t" \
  "v_mfma_f32_16x16x32_bf16 v[172:175], v[208:211], v[108:111], 0\n\t" \
  "s_cmpk_lt_u32 s4, 3072\n\t" \
  "v_lshlrev_b32_sdwa v224, 1, v154 dst_sel:DWORD dst_unused:UNUSED_PAD src0_sel:DWORD src1_sel:WORD_1\n\t" \
  "s_cbranch_scc1 .Lcnx_w0_7\n\t" \
  ".Lcnx_g0_7:\n\t" \
  "ds_read_b32 v220, v220 offset:10016\n\t" \
  "v_mfma_f32_16x16x32_bf16 v[164:167], v[212:215], v[120:123], v[164:167]\n\t" \
  "v_fma_mix_f32 v228, |v196|, v228, v228 op_sel:[0,1,0] op_sel_hi:[0,1,1] clamp\n\t" \
  "v_fma_mix_f32 v229, |v197|, v229, v229 op_sel:[0,1,0] op_sel_hi:[0,1,1] clamp\n\t" \
  "ds_read_b64_tr_b16 v[210:211], v249 offset:32288\n\t" \
  "ds_read_b64_tr_b16 v[208:209], v249 offset:30752\n\t" \
  "v_mfma_f32_16x16x32_bf16 v[172:175], v[212:215], v[124:127], v[172:175]\n\t" \
  "v_fma_mix_f32 v230, |v198|, v230, v230 op_sel:[0,1,0] op_sel_hi:[0,1,1] clamp\n\t" \
  "v_fma_mix_f32 v231, |v199|, v231, v231 op_sel:[0,1,0] op_sel_hi:[0,1,1] clamp\n\t" \
  "s_waitcnt lgkmcnt(5)\n\t" \
  "ds_read_b64_tr_b16 v[212:213], v249 offset:30760\n\t" \
  "ds_read_b32 v222, v222 offset:10016\n\t" \
  "v_mfma_f32_16x16x32_bf16 v[148:151], v[216:219], v[128:131], v[148:151]\n\t" \
  "s_mov_b32 m0, s47\n\t" \
  "v_or_b32 v204, v232, v233\n\t" \
  "v_or_b32 v205, v234, v235\n\t" \
  "s_mov_b32 exec_hi, 0xffff\n\t" \
  "buffer_load_dwordx4 v252, s[16:19], s33 offen lds\n\t" \
  "v_mfma_f32_16x16x32_bf16 v[156:159], v[216:219], v[132:135], v[156:159]\n\t" \
  "s_mov_b32 exec_hi, -1\n\t" \
  "v_fma_f32 v197, |v197|, v229, v197 mul:4\n\t" \
  "v_fma_f32 v196, |v196|, v228, v196 mul:4\n\t" \
  "s_add_u32 s33, s33, s32\n\t" \
  "ds_read_u16_d16_hi v227, v227 offset:10528\n\t" \
  "ds_read_b64_tr_b16 v[214:215], v249 offset:32296\n\t" \
  "v_mfma_f32_16x16x32_bf16 v[164:167], v[216:219], v[136:139], v[164:167]\n\t" \
  "v_fma_f32 v198, |v198|, v230, v198 mul:4\n\t" \
  "v_fma_f32 v199, |v199|, v231, v199 mul:4\n\t" \
  "s_waitcnt lgkmcnt(4)\n\t" \
  "ds_read_u16_d16_hi v225, v225 offset:10528\n\t" \
  "v_mfma_f32_16x16x32_bf16 v[172:175], v[216:219], v[140:143], v[172:175]\n\t" \
  "v_cvt_pk_bf16_f32 v196, v196, v197\n\t" \
  "v_cvt_pk_bf16_f32 v197, v198, v199\n\t" \
  "ds_read_b64_tr_b16 v[216:217], v249 offset:30816\n\t" \
  "v_mfma_f32_16x16x32_bf16 v[0:3], v[208:211], v[178:181], v[0:3]\n\t" \
  "v_cvt_pknorm_i16_f32 v228, |v162|, |v163|\n\t" \
  "v_cvt_pknorm_i16_f32 v230, |v160|, |v161|\n\t" \
  "ds_read_b64_tr_b16 v[218:219], v249 offset:32352\n\t" \
  "v_mfma_f32_16x16x32_bf16 v[24:27], v[208:211], v[186:189], v[24:27]\n\t" \
  "v_lshlrev_b32_sdwa v229, 2, v228 dst_sel:DWORD dst_unused:UNUSED_PAD src0_sel:DWORD src1_sel:BYTE_3\n\t" \
  "v_lshlrev_b32_sdwa v228, 2, v228 dst_sel:DWORD dst_unused:UNUSED_PAD src0_sel:DWORD src1_sel:BYTE_1\n\t" \
  "ds_read_u16 v224, v224 offset:10528\n\t" \
  "v_mfma_f32_16x16x32_bf16 v[48:51], v[208:211], v[194:197], v[48:51]\n\t" \
  "v_lshlrev_b32_sdwa v231, 2, v230 dst_sel:DWORD dst_unused:UNUSED_PAD src0_sel:DWORD src1_sel:BYTE_3\n\t" \
  "s_add_u32 m0, s47, s35\n\t" \
  "s_setprio 1\n\t" \
  "s_waitcnt lgkmcnt(4)\n\t" \
  "ds_read_b32 v228, v228 offset:10016\n\t" \
  "v_mfma_f32_16x16x32_bf16 v[72:75], v[208:211], v[202:205], v[72:75]\n\t" \
  "v_lshlrev_b32_sdwa v232, 1, v170 dst_sel:DWORD dst_unused:UNUSED_PAD src0_sel:DWORD src1_sel:WORD_1\n\t" \
  "v_lshlrev_b32_sdwa v233, 1, v171 dst_sel:DWORD dst_unused:UNUSED_PAD src0_sel:DWORD src1_sel:WORD_1\n\t" \
  "s_mov_b32 exec_hi, 0xffff\n\t" \
  "buffer_load_dwordx4 v252, s[16:19], s34 offen lds\n\t" \
  "v_mfma_f32_16x16x32_bf16 v[4:7], v[212:215], v[178:181], v[4:7]\n\t" \
  "s_mov_b32 exec_hi, -1\n\t" \
  "s_add_u32 s34, s34, s32\n\t" \
  "v_lshlrev_b32_sdwa v235, 1, v169 dst_sel:DWORD dst_unused:UNUSED_PAD src0_sel:DWORD src1_sel:WORD_1\n\t" \
  "ds_read_u16 v226, v226 offset:10528\n\t" \
  "ds_read_b64_tr_b16 v[208:209], v249 offset:30824\n\t" \
  "v_mfma_f32_16x16x32_bf16 v[28:31], v[212:215], v[186:189], v[28:31]\n\t" \
  "v_lshlrev_b32_sdwa v234, 1, v168 dst_sel:DWORD dst_unused:UNUSED_PAD src0_sel:DWORD src1_sel:WORD_1\n\t" \
  "v_lshlrev_b32_sdwa v230, 2, v230 dst_sel:DWORD dst_unused:UNUSED_PAD src0_sel:DWORD src1_sel:BYTE_1\n\t" \
  "ds_read_b64_tr_b16 v[210:211], v249 offset:32360\n\t" \
  "ds_read_b32 v229, v229 offset:10016\n\t" \
  "v_mfma_f32_16x16x32_bf16 v[76:79], v[212:215], v[202:205], v[76:79]\n\t" \
  "v_fma_mix_f32 v223, |v145|, v223, v223 op_sel:[0,1,0] op_sel_hi:[0,1,1] clamp\n\t" \
  "v_fma_mix_f32 v222, |v144|, v222, v222 op_sel:[0,1,0] op_sel_hi:[0,1,1] clamp\n\t" \
  "ds_read_b32 v230, v230 offset:10016\n\t" \
  "v_mfma_f32_16x16x32_bf16 v[52:55], v[212:215], v[194:197], v[52:55]\n\t" \
  "v_fma_mix_f32 v220, |v146|, v220, v220 op_sel:[0,1,0] op_sel_hi:[0,1,1] clamp\n\t" \
  "v_fma_mix_f32 v221, |v147|, v221, v221 op_sel:[0,1,0] op_sel_hi:[0,1,1] clamp\n\t" \
  "s_setprio 3\n\t" \
  "s_waitcnt lgkmcnt(4)\n\t" \
  "ds_read_b32 v231, v231 offset:10016\n\t" \
  "ds_read_b64_tr_b16 v[212:213], v249 offset:30880\n\t" \
  "v_mfma_f32_16x16x32_bf16 v[8:11], v[216:219], v[178:181], v[8:11]\n\t" \
  "v_or_b32 v155, v224, v225\n\t" \
  "v_or_b32 v154, v226, v227\n\t" \
  "s_setprio 1\n\t" \
  "ds_read_u16_d16_hi v233, v233 offset:10528\n\t" \
  "ds_read_b64_tr_b16 v[214:215], v249 offset:32416\n\t" \
  "v_mfma_f32_16x16x32_bf16 v[32:35], v[216:219], v[186:189], v[32:35]\n\t" \
  "s_add_u32 s24, s24, s26\n\t" \
  "v_fma_f32 v146, |v146|, v220, v146 mul:4\n\t" \
  "v_fma_f32 v147, |v147|, v221, v147 mul:4\n\t" \
  "v_mfma_f32_16x16x32_bf16 v[56:59], v[216:219], v[194:197], v[56:59]\n\t" \
  "v_fma_f32 v145, |v145|, v223, v145 mul:4\n\t" \
  "v_fma_f32 v144, |v144|, v222, v144 mul:4\n\t" \
  "ds_read_u16 v232, v232 offset:10528\n\t" \
  "v_mfma_f32_16x16x32_bf16 v[80:83], v[216:219], v[202:205], v[80:83]\n\t" \
  "s_add_u32 m0, s25, 41000\n\t" \
  "v_cvt_pk_bf16_f32 v147, v146, v147\n\t" \
  "v_cvt_pk_bf16_f32 v146, v144, v145\n\t" \
  "s_setprio 0\n\t" \
  "s_waitcnt lgkmcnt(7)\n\t" \
  "buffer_load_dwordx4 v254, s[8:11], s24 offen lds\n\t" \
  "v_mfma_f32_16x16x32_bf16 v[12:15], v[208:211], v[178:181], v[12:15]\n\t" \
  "v_cvt_pknorm_i16_f32 v220, |v148|, |v149|\n\t" \
  "v_cvt_pknorm_i16_f32 v222, |v150|, |v151|\n\t" \
  "ds_read_b64_tr_b16 v[216:217], v249 offset:30888\n\t" \
  "ds_read_b64_tr_b16 v[218:219], v249 offset:32424\n\t" \
  "v_mfma_f32_16x16x32_bf16 v[36:39], v[208:211], v[186:189], v[36:39]\n\t" \
  "v_lshlrev_b32_sdwa v221, 2, v220 dst_sel:DWORD dst_unused:UNUSED_PAD src0_sel:DWORD src1_sel:BYTE_3\n\t" \
  "v_lshlrev_b32_sdwa v220, 2, v220 dst_sel:DWORD dst_unused:UNUSED_PAD src0_sel:DWORD src1_sel:BYTE_1\n\t" \
  "ds_read_u16_d16_hi v235, v235 offset:10528\n\t" \
  "v_mfma_f32_16x16x32_bf16 v[60:63], v[208:211], v[194:197], v[60:63]\n\t" \
  "v_lshlrev_b32_sdwa v223, 2, v222 dst_sel:DWORD dst_unused:UNUSED_PAD src0_sel:DWORD src1_sel:BYTE_3\n\t" \
  "v_lshlrev_b32_sdwa v222, 2, v222 dst_sel:DWORD dst_unused:UNUSED_PAD src0_sel:DWORD src1_sel:BYTE_1\n\t" \
  "ds_read_u16 v234, v234 offset:10528\n\t" \
  "s_waitcnt lgkmcnt(5)\n\t" \
  "v_mfma_f32_16x16x32_bf16 v[84:87], v[208:211], v[202:205], v[84:87]\n\t" \
  "s_setprio 1\n\t" \
  "v_lshlrev_b32_sdwa v225, 1, v157 dst_sel:DWORD dst_unused:UNUSED_PAD src0_sel:DWORD src1_sel:WORD_1\n\t" \
  "v_lshlrev_b32_sdwa v224, 1, v156 dst_sel:DWORD dst_unused:UNUSED_PAD src0_sel:DWORD src1_sel:WORD_1\n\t" \
  "ds_read_b64_tr_b16 v[208:209], v248 offset:0\n\t" \
  "ds_read_b32 v220, v220 offset:10016\n\t" \
  "v_mfma_f32_16x16x32_bf16 v[16:19], v[212:215], v[178:181], v[16:19]\n\t" \
  "v_nop\n\t" \
  "s_add_u32 s27, s27, s26\n\t" \
  "s_mov_b64 exec, s[30:31]\n\t" \
  "buffer_load_dword v237, v254, s[8:11], s27 offen\n\t" \
  "v_mfma_f32_16x16x32_bf16 v[64:67], v[212:215], v[194:197], v[64:67]\n\t" \
  "s_mov_b64 exec, -1\n\t" \
  "v_fma_mix_f32 v229, |v163|, v229, v229 op_sel:[0,1,0] op_sel_hi:[0,1,1] clamp\n\t" \
  "v_lshlrev_b32_sdwa v227, 1, v159 dst_sel:DWORD dst_unused:UNUSED_PAD src0_sel:DWORD src1_sel:WORD_1\n\t" \
  "s_setprio 2\n\t" \
  "ds_read_b32 v223, v223 offset:10016\n\t" \
  "ds_read_b32 v221, v221 offset:10016\n\t" \
  "v_mfma_f32_16x16x32_bf16 v[88:91], v[212:215], v[202:205], v[88:91]\n\t" \
  "s_setprio 1\n\t" \
  "v_fma_mix_f32 v231, |v161|, v231, v231 op_sel:[0,1,0] op_sel_hi:[0,1,1] clamp\n\t" \
  "v_fma_mix_f32 v230, |v160|, v230, v230 op_sel:[0,1,0] op_sel_hi:[0,1,1] clamp\n\t" \
  "ds_read_b64_tr_b16 v[210:211], v248 offset:1024\n\t" \
  "v_mfma_f32_16x16x32_bf16 v[40:43], v[212:215], v[186:189], v[40:43]\n\t" \
  "s_waitcnt lgkmcnt(5)\n\t" \
  "v_fma_mix_f32 v228, |v162|, v228, v228 op_sel:[0,1,0] op_sel_hi:[0,1,1] clamp\n\t" \
  "v_lshlrev_b32_sdwa v226, 1, v158 dst_sel:DWORD dst_unused:UNUSED_PAD src0_sel:DWORD src1_sel:WORD_1\n\t" \
  "ds_read_b32 v222, v222 offset:10016\n\t" \
  "ds_read_b64_tr_b16 v[212:213], v248 offset:3072\n\t" \
  "v_mfma_f32_16x16x32_bf16 v[20:23], v[216:219], v[178:181], v[20:23]\n\t" \
  "v_or_b32 v171, v232, v233\n\t" \
  "v_or_b32 v170, v234, v235\n\t" \
  "ds_read_u16_d16_hi v225, v225 offset:10528\n\t" \
  "v_mfma_f32_16x16x32_bf16 v[68:71], v[216:219], v[194:197], v[68:71]\n\t" \
  "s_setprio 2\n\t" \
  "v_fma_f32 v160, |v160|, v230, v160 mul:4\n\t" \
  "v_fma_f32 v163, |v163|, v229, v163 mul:4\n\t" \
  "s_waitcnt lgkmcnt(3)\n\t" \
  "ds_read_u16 v224, v224 offset:10528\n\t" \
  "v_mfma_f32_16x16x32_bf16 v[176:179], v[208:211], v[96:99], 0\n\t" \
  "v_lshlrev_b32_sdwa v232, 1, v172 dst_sel:DWORD dst_unused:UNUSED_PAD src0_sel:DWORD src1_sel:WORD_1\n\t" \
  "v_fma_f32 v161, |v161|, v231, v161 mul:4\n\t" \
  "ds_read_b64_tr_b16 v[214:215], v248 offset:4096\n\t" \
  "v_mfma_f32_16x16x32_bf16 v[44:47], v[216:219], v[186:189], v[44:47]\n\t" \
  "v_fma_f32 v162, |v162|, v228, v162 mul:4\n\t" \
  "v_cvt_pknorm_i16_f32 v230, |v166|, |v167|\n\t" \
  "ds_read_u16_d16_hi v227, v227 offset:10528\n\t" \
  "v_mfma_f32_16x16x32_bf16 v[92:95], v[216:219], v[202:205], v[92:95]\n\t" \
  "v_cvt_pk_bf16_f32 v163, v162, v163\n\t" \
  "v_cvt_pk_bf16_f32 v162, v160, v161\n\t" \
  "ds_read_b64_tr_b16 v[216:217], v248 offset:2048\n\t" \
  "v_mfma_f32_16x16x32_bf16 v[192:195], v[208:211], v[104:107], 0\n\t" \
  "v_cvt_pknorm_i16_f32 v228, |v164|, |v165|\n\t" \
  "v_lshlrev_b32_sdwa v231, 2, v230 dst_sel:DWORD dst_unused:UNUSED_PAD src0_sel:DWORD src1_sel:BYTE_3\n\t" \
  "ds_read_b64_tr_b16 v[218:219], v248 offset:5120\n\t" \
  "v_mfma_f32_16x16x32_bf16 v[184:187], v[208:211], v[100:103], 0\n\t" \
  "v_lshlrev_b32_sdwa v229, 2, v228 dst_sel:DWORD dst_unused:UNUSED_PAD src0_sel:DWORD src1_sel:BYTE_3\n\t" \
  "v_lshlrev_b32_sdwa v228, 2, v228 dst_sel:DWORD dst_unused:UNUSED_PAD src0_sel:DWORD src1_sel:BYTE_1\n\t" \
  "ds_read_u16 v226, v226 offset:10528\n\t" \
  "s_waitcnt lgkmcnt(4)\n\t" \
  "v_mfma_f32_16x16x32_bf16 v[200:203], v[208:211], v[108:111], 0\n\t" \
  "v_lshlrev_b32_sdwa v230, 2, v230 dst_sel:DWORD dst_unused:UNUSED_PAD src0_sel:DWORD src1_sel:BYTE_1\n\t" \
  "v_lshlrev_b32_sdwa v233, 1, v173 dst_sel:DWORD dst_unused:UNUSED_PAD src0_sel:DWORD src1_sel:WORD_1\n\t" \
  "ds_read_b64_tr_b16 v[208:209], v248 offset:512\n\t" \
  "v_mfma_f32_16x16x32_bf16 v[176:179], v[212:215], v[112:115], v[176:179]\n\t" \
  "v_lshlrev_b32_sdwa v235, 1, v175 dst_sel:DWORD dst_unused:UNUSED_PAD src0_sel:DWORD src1_sel:WORD_1\n\t" \
  "v_fma_mix_f32 v221, |v149|, v221, v221 op_sel:[0,1,0] op_sel_hi:[0,1,1] clamp\n\t" \
  "ds_read_b32 v231, v231 offset:10016\n\t" \
  "v_mfma_f32_16x16x32_bf16 v[200:203], v[212:215], v[124:127], v[200:203]\n\t" \
  "v_lshlrev_b32_sdwa v234, 1, v174 dst_sel:DWORD dst_unused:UNUSED_PAD src0_sel:DWORD src1_sel:WORD_1\n\t" \
  "ds_read_b32 v228, v228 offset:10016\n\t" \
  "ds_read_b32 v229, v229 offset:10016\n\t" \
  "v_mfma_f32_16x16x32_bf16 v[192:195], v[212:215], v[120:123], v[192:195]\n\t" \
  "s_setprio 1\n\t" \
  "v_fma_mix_f32 v220, |v148|, v220, v220 op_sel:[0,1,0] op_sel_hi:[0,1,1] clamp\n\t" \
  "ds_read_b64_tr_b16 v[210:211], v248 offset:1536\n\t" \
  "v_mfma_f32_16x16x32_bf16 v[184:187], v[212:215], v[116:119], v[184:187]\n\t" \
  "v_fma_mix_f32 v222, |v150|, v222, v222 op_sel:[0,1,0] op_sel_hi:[0,1,1] clamp\n\t" \
  "v_fma_mix_f32 v223, |v151|, v223, v223 op_sel:[0,1,0] op_sel_hi:[0,1,1] clamp\n\t" \
  "s_waitcnt lgkmcnt(6)\n\t" \
  "ds_read_b64_tr_b16 v[212:213], v248 offset:3584\n\t" \
  "ds_read_u16_d16_hi v233, v233 offset:10528\n\t" \
  "v_mfma_f32_16x16x32_bf16 v[176:179], v[216:219], v[128:131], v[176:179]\n\t" \
  "s_waitcnt lgkmcnt(7)\n\t" \
  "v_or_b32 v156, v224, v225\n\t" \
  "v_or_b32 v157, v226, v227\n\t" \
  "ds_read_b64_tr_b16 v[214:215], v248 offset:4608\n\t" \
  "v_mfma_f32_16x16x32_bf16 v[192:195], v[216:219], v[136:139], v[192:195]\n\t" \
  "v_fma_f32 v150, |v150|, v222, v150 mul:4\n\t" \
  "v_fma_f32 v151, |v151|, v223, v151 mul:4\n\t" \
  "s_setprio 0\n\t" \
  "ds_read_b32 v230, v230 offset:10016\n\t" \
  "v_mfma_f32_16x16x32_bf16 v[184:187], v[216:219], v[132:135], v[184:187]\n\t" \
  "v_fma_f32 v149, |v149|, v221, v149 mul:4\n\t" \
  "v_fma_f32 v148, |v148|, v220, v148 mul:4\n\t" \
  "ds_read_u16 v232, v232 offset:10528\n\t" \
  "ds_read_u16_d16_hi v235, v235 offset:10528\n\t" \
  "v_mfma_f32_16x16x32_bf16 v[200:203], v[216:219], v[140:143], v[200:203]\n\t" \
  "v_cvt_pk_bf16_f32 v148, v148, v149\n\t" \
  "v_cvt_pk_bf16_f32 v149, v150, v151\n\t" \
  "s_waitcnt lgkmcnt(6)\n\t" \
  "ds_read_b64_tr_b16 v[216:217], v248 offset:2560\n\t" \
  "v_mfma_f32_16x16x32_bf16 v[180:183], v[208:211], v[96:99], 0\n\t" \
  "v_cvt_pknorm_i16_f32 v222, |v176|, |v177|\n\t" \
  "v_cvt_pknorm_i16_f32 v220, |v178|, |v179|\n\t" \
  "s_nop 0\n\t" \
  "s_waitcnt vmcnt(2)\n\t" \
  "ds_read_b64_tr_b16 v[218:219], v248 offset:5632\n\t" \
  "v_mfma_f32_16x16x32_bf16 v[188:191], v[208:211], v[100:103], 0\n\t" \
  "s_waitcnt lgkmcnt(5)\n\t" \
  "v_lshlrev_b32_sdwa v221, 2, v220 dst_sel:DWORD dst_unused:UNUSED_PAD src0_sel:DWORD src1_sel:BYTE_3\n\t" \
  "v_lshlrev_b32_sdwa v220, 2, v220 dst_sel:DWORD dst_unused:UNUSED_PAD src0_sel:DWORD src1_sel:BYTE_1\n\t" \
  "s_mov_b64 exec, 3\n\t" \
  "ds_add_rtn_u32 v246, v251, v250 offset:47192\n\t" \
  "v_mfma_f32_16x16x32_bf16 v[196:199], v[208:211], v[104:107], 0\n\t" \
  "s_mov_b64 exec, -1\n\t" \
  "v_lshlrev_b32_sdwa v223, 2, v222 dst_sel:DWORD dst_unused:UNUSED_PAD src0_sel:DWORD src1_sel:BYTE_3\n\t" \
  "v_lshlrev_b32_sdwa v222, 2, v222 dst_sel:DWORD dst_unused:UNUSED_PAD src0_sel:DWORD src1_sel:BYTE_1\n\t" \
  "s_setprio 2\n\t" \
  "s_setprio 0\n\t" \
  "ds_read_u16 v234, v234 offset:10528\n\t" \
  "v_mfma_f32_16x16x32_bf16 v[188:191], v[212:215], v[116:119], v[188:191]\n\t" \
  "v_lshlrev_b32_sdwa v226, 1, v184 dst_sel:DWORD dst_unused:UNUSED_PAD src0_sel:DWORD src1_sel:WORD_1\n\t" \
  "v_lshlrev_b32_sdwa v227, 1, v185 dst_sel:DWORD dst_unused:UNUSED_PAD src0_sel:DWORD src1_sel:WORD_1\n\t" \
  "ds_read_b32 v223, v223 offset:10016\n\t" \
  "v_mfma_f32_16x16x32_bf16 v[180:183], v[212:215], v[112:115], v[180:183]\n\t" \
  "s_waitcnt lgkmcnt(2)\n\t" \
  "v_lshlrev_b32_sdwa v225, 1, v187 dst_sel:DWORD dst_unused:UNUSED_PAD src0_sel:DWORD src1_sel:WORD_1\n\t" \
  "v_readfirstlane_b32 s4, v246\n\t" \
  "ds_read_b32 v221, v221 offset:10016\n\t" \
  "v_mfma_f32_16x16x32_bf16 v[204:207], v[208:211], v[108:111], 0\n\t" \
  "s_cmpk_lt_u32 s4, 3072\n\t" \
  "v_lshlrev_b32_sdwa v224, 1, v186 dst_sel:DWORD dst_unused:UNUSED_PAD src0_sel:DWORD src1_sel:WORD_1\n\t" \
  "s_cbranch_scc1 .Lcnx_w0_8\n\t" \
  ".Lcnx_g0_8:\n\t" \
  "ds_read_b32 v220, v220 offset:10016\n\t" \
  "v_mfma_f32_16x16x32_bf16 v[196:199], v[212:215], v[120:123], v[196:199]\n\t" \
  "v_fma_mix_f32 v228, |v164|, v228, v228 op_sel:[0,1,0] op_sel_hi:[0,1,1] clamp\n\t" \
  "v_fma_mix_f32 v229, |v165|, v229, v229 op_sel:[0,1,0] op_sel_hi:[0,1,1] clamp\n\t" \
  "ds_read_b64_tr_b16 v[210:211], v249 offset:7688\n\t" \
  "ds_read_b64_tr_b16 v[208:209], v249 offset:6152\n\t" \
  "v_mfma_f32_16x16x32_bf16 v[204:207], v[212:215], v[124:127], v[204:207]\n\t" \
  "v_fma_mix_f32 v230, |v166|, v230, v230 op_sel:[0,1,0] op_sel_hi:[0,1,1] clamp\n\t" \
  "v_fma_mix_f32 v231, |v167|, v231, v231 op_sel:[0,1,0] op_sel_hi:[0,1,1] clamp\n\t" \
  "s_waitcnt lgkmcnt(5)\n\t" \
  "ds_read_b64_tr_b16 v[212:213], v249 offset:6160\n\t" \
  "ds_read_b32 v222, v222 offset:10016\n\t" \
  "v_mfma_f32_16x16x32_bf16 v[180:183], v[216:219], v[128:131], v[180:183]\n\t" \
  "s_mov_b32 m0, s44\n\t" \
  "v_or_b32 v172, v232, v233\n\t" \
  "v_or_b32 v173, v234, v235\n\t" \
  "s_mov_b32 exec_hi, 0xffff\n\t" \
  "buffer_load_dwordx4 v252, s[16:19], s33 offen lds\n\t" \
  "v_mfma_f32_16x16x32_bf16 v[188:191], v[216:219], v[132:135], v[188:191]\n\t" \
  "s_mov_b32 exec_hi, -1\n\t" \
  "v_fma_f32 v165, |v165|, v229, v165 mul:4\n\t" \
  "v_fma_f32 v164, |v164|, v228, v164 mul:4\n\t" \
  "s_add_u32 s33, s33, s32\n\t" \
  "ds_read_u16_d16_hi v227, v227 offset:10528\n\t" \
  "ds_read_b64_tr_b16 v[214:215], v249 offset:7696\n\t" \
  "v_mfma_f32_16x16x32_bf16 v[196:199], v[216:219], v[136:139], v[196:199]\n\t" \
  "v_fma_f32 v166, |v166|, v230, v166 mul:4\n\t" \
  "v_fma_f32 v167, |v167|, v231, v167 mul:4\n\t" \
  "s_waitcnt lgkmcnt(4)\n\t" \
  "ds_read_u16_d16_hi v225, v225 offset:10528\n\t" \
  "v_mfma_f32_16x16x32_bf16 v[204:207], v[216:219], v[140:143], v[204:207]\n\t" \
  "v_cvt_pk_bf16_f32 v164, v164, v165\n\t" \
  "v_cvt_pk_bf16_f32 v165, v166, v167\n\t" \
  "ds_read_b64_tr_b16 v[216:217], v249 offset:6216\n\t" \
  "v_mfma_f32_16x16x32_bf16 v[0:3], v[208:211], v[146:149], v[0:3]\n\t" \
  "v_cvt_pknorm_i16_f32 v228, |v194|, |v195|\n\t" \
  "v_cvt_pknorm_i16_f32 v230, |v192|, |v193|\n\t" \
  "ds_read_b64_tr_b16 v[218:219], v249 offset:7752\n\t" \
  "v_mfma_f32_16x16x32_bf16 v[24:27], v[208:211], v[154:157], v[24:27]\n\t" \
  "v_lshlrev_b32_sdwa v229, 2, v228 dst_sel:DWORD dst_unused:UNUSED_PAD src0_sel:DWORD src1_sel:BYTE_3\n\t" \
  "v_lshlrev_b32_sdwa v228, 2, v228 dst_sel:DWORD dst_unused:UNUSED_PAD src0_sel:DWORD src1_sel:BYTE_1\n\t" \
  "ds_read_u16 v224, v224 offset:10528\n\t" \
  "v_mfma_f32_16x16x32_bf16 v[48:51], v[208:211], v[162:165], v[48:51]\n\t" \
  "v_lshlrev_b32_sdwa v231, 2, v230 dst_sel:DWORD dst_unused:UNUSED_PAD src0_sel:DWORD src1_sel:BYTE_3\n\t" \
  "s_add_u32 m0, s44, s35\n\t" \
  "s_setprio 1\n\t" \
  "s_waitcnt lgkmcnt(4)\n\t" \
  "ds_read_b32 v228, v228 offset:10016\n\t" \
  "v_mfma_f32_16x16x32_bf16 v[72:75], v[208:211], v[170:173], v[72:75]\n\t" \
  "v_lshlrev_b32_sdwa v232, 1, v202 dst_sel:DWORD dst_unused:UNUSED_PAD src0_sel:DWORD src1_sel:WORD_1\n\t" \
  "v_lshlrev_b32_sdwa v233, 1, v203 dst_sel:DWORD dst_unused:UNUSED_PAD src0_sel:DWORD src1_sel:WORD_1\n\t" \
  "s_mov_b32 exec_hi, 0xffff\n\t" \
  "buffer_load_dwordx4 v252, s[16:19], s34 offen lds\n\t" \
  "v_mfma_f32_16x16x32_bf16 v[4:7], v[212:215], v[146:149], v[4:7]\n\t" \
  "s_mov_b32 exec_hi, -1\n\t" \
  "s_add_u32 s34, s34, s32\n\t" \
  "v_lshlrev_b32_sdwa v235, 1, v201 dst_sel:DWORD dst_unused:UNUSED_PAD src0_sel:DWORD src1_sel:WORD_1\n\t" \
  "ds_read_u16 v226, v226 offset:10528\n\t" \
  "ds_read_b64_tr_b16 v[208:209], v249 offset:6224\n\t" \
  "v_mfma_f32_16x16x32_bf16 v[28:31], v[212:215], v[154:157], v[28:31]\n\t" \
  "v_lshlrev_b32_sdwa v234, 1, v200 dst_sel:DWORD dst_unused:UNUSED_PAD src0_sel:DWORD src1_sel:WORD_1\n\t" \
  "v_lshlrev_b32_sdwa v230, 2, v230 dst_sel:DWORD dst_unused:UNUSED_PAD src0_sel:DWORD src1_sel:BYTE_1\n\t" \
  "ds_read_b64_tr_b16 v[210:211], v249 offset:7760\n\t" \
  "ds_read_b32 v229, v229 offset:10016\n\t" \
  "v_mfma_f32_16x16x32_bf16 v[76:79], v[212:215], v[170:173], v[76:79]\n\t" \
  "v_fma_mix_f32 v223, |v177|, v223, v223 op_sel:[0,1,0] op_sel_hi:[0,1,1] clamp\n\t" \
  "v_fma_mix_f32 v222, |v176|, v222, v222 op_sel:[0,1,0] op_sel_hi:[0,1,1] clamp\n\t" \
  "ds_read_b32 v230, v230 offset:10016\n\t" \
  "v_mfma_f32_16x16x32_bf16 v[52:55], v[212:215], v[162:165], v[52:55]\n\t" \
  "v_fma_mix_f32 v220, |v178|, v220, v220 op_sel:[0,1,0] op_sel_hi:[0,1,1] clamp\n\t" \
  "v_fma_mix_f32 v221, |v179|, v221, v221 op_sel:[0,1,0] op_sel_hi:[0,1,1] clamp\n\t" \
  "s_setprio 3\n\t" \
  "s_waitcnt lgkmcnt(4)\n\t" \
  "ds_read_b32 v231, v231 offset:10016\n\t" \
  "ds_read_b64_tr_b16 v[212:213], v249 offset:6280\n\t" \
  "v_mfma_f32_16x16x32_bf16 v[8:11], v[216:219], v[146:149], v[8:11]\n\t" \
  "v_or_b32 v187, v224, v225\n\t" \
  "v_or_b32 v186, v226, v227\n\t" \
  "s_setprio 1\n\t" \
  "ds_read_u16_d16_hi v233, v233 offset:10528\n\t" \
  "ds_read_b64_tr_b16 v[214:215], v249 offset:7816\n\t" \
  "v_mfma_f32_16x16x32_bf16 v[32:35], v[216:219], v[154:157], v[32:35]\n\t" \
  "s_add_u32 s24, s24, s26\n\t" \
  "v_fma_f32 v178, |v178|, v220, v178 mul:4\n\t" \
  "v_fma_f32 v179, |v179|, v221, v179 mul:4\n\t" \
  "v_mfma_f32_16x16x32_bf16 v[56:59], v[216:219], v[162:165], v[56:59]\n\t" \
  "v_fma_f32 v177, |v177|, v223, v177 mul:4\n\t" \
  "v_fma_f32 v176, |v176|, v222, v176 mul:4\n\t" \
  "ds_read_u16 v232, v232 offset:10528\n\t" \
  "v_mfma_f32_16x16x32_bf16 v[80:83], v[216:219], v[170:173], v[80:83]\n\t" \
  "s_add_u32 m0, s25, 56880\n\t" \
  "v_cvt_pk_bf16_f32 v179, v178, v179\n\t" \
  "v_cvt_pk_bf16_f32 v178, v176, v177\n\t" \
  "s_setprio 0\n\t" \
  "s_waitcnt lgkmcnt(7)\n\t" \
  "buffer_load_dwordx4 v254, s[8:11], s24 offen lds\n\t" \
  "v_mfma_f32_16x16x32_bf16 v[12:15], v[208:211], v[146:149], v[12:15]\n\t" \
  "v_cvt_pknorm_i16_f32 v220, |v180|, |v181|\n\t" \
  "v_cvt_pknorm_i16_f32 v222, |v182|, |v183|\n\t" \
  "ds_read_b64_tr_b16 v[216:217], v249 offset:6288\n\t" \
  "ds_read_b64_tr_b16 v[218:219], v249 offset:7824\n\t" \
  "v_mfma_f32_16x16x32_bf16 v[36:39], v[208:211], v[154:157], v[36:39]\n\t" \
  "v_lshlrev_b32_sdwa v221, 2, v220 dst_sel:DWORD dst_unused:UNUSED_PAD src0_sel:DWORD src1_sel:BYTE_3\n\t" \
  "v_lshlrev_b32_sdwa v220, 2, v220 dst_sel:DWORD dst_unused:UNUSED_PAD src0_sel:DWORD src1_sel:BYTE_1\n\t" \
  "ds_read_u16_d16_hi v235, v235 offset:10528\n\t" \
  "v_mfma_f32_16x16x32_bf16 v[60:63], v[208:211], v[162:165], v[60:63]\n\t" \
  "v_lshlrev_b32_sdwa v223, 2, v222 dst_sel:DWORD dst_unused:UNUSED_PAD src0_sel:DWORD src1_sel:BYTE_3\n\t" \
  "v_lshlrev_b32_sdwa v222, 2, v222 dst_sel:DWORD dst_unused:UNUSED_PAD src0_sel:DWORD src1_sel:BYTE_1\n\t" \
  "ds_read_u16 v234, v234 offset:10528\n\t" \
  "s_waitcnt lgkmcnt(5)\n\t" \
  "v_mfma_f32_16x16x32_bf16 v[84:87], v[208:211], v[170:173], v[84:87]\n\t" \
  "s_setprio 1\n\t" \
  "v_lshlrev_b32_sdwa v225, 1, v189 dst_sel:DWORD dst_unused:UNUSED_PAD src0_sel:DWORD src1_sel:WORD_1\n\t" \
  "v_lshlrev_b32_sdwa v224, 1, v188 dst_sel:DWORD dst_unused:UNUSED_PAD src0_sel:DWORD src1_sel:WORD_1\n\t" \
  "ds_read_b64_tr_b16 v[208:209], v248 offset:43048\n\t" \
  "ds_read_b32 v220, v220 offset:10016\n\t" \
  "v_mfma_f32_16x16x32_bf16 v[16:19], v[212:215], v[146:149], v[16:19]\n\t" \
  "v_nop\n\t" \
  "s_add_u32 s27, s27, s26\n\t" \
  "s_mov_b64 exec, s[30:31]\n\t" \
  "buffer_load_dword v237, v254, s[8:11], s27 offen\n\t" \
  "v_mfma_f32_16x16x32_bf16 v[64:67], v[212:215], v[162:165], v[64:67]\n\t" \
  "s_mov_b64 exec, -1\n\t" \
  "v_fma_mix_f32 v229, |v195|, v229, v229 op_sel:[0,1,0] op_sel_hi:[0,1,1] clamp\n\t" \
  "v_lshlrev_b32_sdwa v227, 1, v191 dst_sel:DWORD dst_unused:UNUSED_PAD src0_sel:DWORD src1_sel:WORD_1\n\t" \
  "s_setprio 2\n\t" \
  "ds_read_b32 v223, v223 offset:10016\n\t" \
  "ds_read_b32 v221, v221 offset:10016\n\t" \
  "v_mfma_f32_16x16x32_bf16 v[88:91], v[212:215], v[170:173], v[88:91]\n\t" \
  "s_setprio 1\n\t" \
  "v_fma_mix_f32 v231, |v193|, v231, v231 op_sel:[0,1,0] op_sel_hi:[0,1,1] clamp\n\t" \
  "v_fma_mix_f32 v230, |v192|, v230, v230 op_sel:[0,1,0] op_sel_hi:[0,1,1] clamp\n\t" \
  "ds_read_b64_tr_b16 v[210:211], v248 offset:44072\n\t" \
  "v_mfma_f32_16x16x32_bf16 v[40:43], v[212:215], v[154:157], v[40:43]\n\t" \
  "s_waitcnt lgkmcnt(5)\n\t" \
  "v_fma_mix_f32 v228, |v194|, v228, v228 op_sel:[0,1,0] op_sel_hi:[0,1,1] clamp\n\t" \
  "v_lshlrev_b32_sdwa v226, 1, v190 dst_sel:DWORD dst_unused:UNUSED_PAD src0_sel:DWORD src1_sel:WORD_1\n\t" \
  "ds_read_b32 v222, v222 offset:10016\n\t" \
  "ds_read_b64_tr_b16 v[212:213], v248 offset:46120\n\t" \
  "v_mfma_f32_16x16x32_bf16 v[20:23], v[216:219], v[146:149], v[20:23]\n\t" \
  "v_or_b32 v203, v232, v233\n\t" \
  "v_or_b32 v202, v234, v235\n\t" \
  "ds_read_u16_d16_hi v225, v225 offset:10528\n\t" \
  "v_mfma_f32_16x16x32_bf16 v[68:71], v[216:219], v[162:165], v[68:71]\n\t" \
  "s_setprio 2\n\t" \
  "v_fma_f32 v192, |v192|, v230, v192 mul:4\n\t" \
  "v_fma_f32 v195, |v195|, v229, v195 mul:4\n\t" \
  "s_waitcnt lgkmcnt(3)\n\t" \
  "ds_read_u16 v224, v224 offset:10528\n\t" \
  "v_mfma_f32_16x16x32_bf16 v[144:147], v[208:211], v[96:99], 0\n\t" \
  "v_lshlrev_b32_sdwa v232, 1, v204 dst_sel:DWORD dst_unused:UNUSED_PAD src0_sel:DWORD src1_sel:WORD_1\n\t" \
  "v_fma_f32 v193, |v193|, v231, v193 mul:4\n\t" \
  "ds_read_b64_tr_b16 v[214:215], v248 offset:47144\n\t" \
  "v_mfma_f32_16x16x32_bf16 v[44:47], v[216:219], v[154:157], v[44:47]\n\t" \
  "v_fma_f32 v194, |v194|, v228, v194 mul:4\n\t" \
  "v_cvt_pknorm_i16_f32 v230, |v198|, |v199|\n\t" \
  "ds_read_u16_d16_hi v227, v227 offset:10528\n\t" \
  "v_mfma_f32_16x16x32_bf16 v[92:95], v[216:219], v[170:173], v[92:95]\n\t" \
  "v_cvt_pk_bf16_f32 v195, v194, v195\n\t" \
  "v_cvt_pk_bf16_f32 v194, v192, v193\n\t" \
  "ds_read_b64_tr_b16 v[216:217], v248 offset:45096\n\t" \
  "v_mfma_f32_16x16x32_bf16 v[160:163], v[208:211], v[104:107], 0\n\t" \
  "v_cvt_pknorm_i16_f32 v228, |v196|, |v197|\n\t" \
  "v_lshlrev_b32_sdwa v231, 2, v230 dst_sel:DWORD dst_unused:UNUSED_PAD src0_sel:DWORD src1_sel:BYTE_3\n\t" \
  "ds_read_b64_tr_b16 v[218:219], v248 offset:48168\n\t" \
  "v_mfma_f32_16x16x32_bf16 v[152:155], v[208:211], v[100:103], 0\n\t" \
  "v_lshlrev_b32_sdwa v229, 2, v228 dst_sel:DWORD dst_unused:UNUSED_PAD src0_sel:DWORD src1_sel:BYTE_3\n\t" \
  "v_lshlrev_b32_sdwa v228, 2, v228 dst_sel:DWORD dst_unused:UNUSED_PAD src0_sel:DWORD src1_sel:BYTE_1\n\t" \
  "ds_read_u16 v226, v226 offset:10528\n\t" \
  "s_waitcnt lgkmcnt(4)\n\t" \
  "v_mfma_f32_16x16x32_bf16 v[168:171], v[208:211], v[108:111], 0\n\t" \
  "v_lshlrev_b32_sdwa v230, 2, v230 dst_sel:DWORD dst_unused:UNUSED_PAD src0_sel:DWORD src1_sel:BYTE_1\n\t" \
  "v_lshlrev_b32_sdwa v233, 1, v205 dst_sel:DWORD dst_unused:UNUSED_PAD src0_sel:DWORD src1_sel:WORD_1\n\t" \
  "ds_read_b64_tr_b16 v[208:209], v248 offset:43560\n\t" \
  "v_mfma_f32_16x16x32_bf16 v[144:147], v[212:215], v[112:115], v[144:147]\n\t" \
  "v_lshlrev_b32_sdwa v235, 1, v207 dst_sel:DWORD dst_unused:UNUSED_PAD src0_sel:DWORD src1_sel:WORD_1\n\t" \
  "v_fma_mix_f32 v221, |v181|, v221, v221 op_sel:[0,1,0] op_sel_hi:[0,1,1] clamp\n\t" \
  "ds_read_b32 v231, v231 offset:10016\n\t" \
  "v_mfma_f32_16x16x32_bf16 v[168:171], v[212:215], v[124:127], v[168:171]\n\t" \
  "v_lshlrev_b32_sdwa v234, 1, v206 dst_sel:DWORD dst_unused:UNUSED_PAD src0_sel:DWORD src1_sel:WORD_1\n\t" \
  "ds_read_b32 v228, v228 offset:10016\n\t" \
  "ds_read_b32 v229, v229 offset:10016\n\t" \
  "v_mfma_f32_16x16x32_bf16 v[160:163], v[212:215], v[120:123], v[160:163]\n\t" \
  "s_setprio 1\n\t" \
  "v_fma_mix_f32 v220, |v180|, v220, v220 op_sel:[0,1,0] op_sel_hi:[0,1,1] clamp\n\t" \
  "ds_read_b64_tr_b16 v[210:211], v248 offset:44584\n\t" \
  "v_mfma_f32_16x16x32_bf16 v[152:155], v[212:215], v[116:119], v[152:155]\n\t" \
  "v_fma_mix_f32 v222, |v182|, v222, v222 op_sel:[0,1,0] op_sel_hi:[0,1,1] clamp\n\t" \
  "v_fma_mix_f32 v223, |v183|, v223, v223 op_sel:[0,1,0] op_sel_hi:[0,1,1] clamp\n\t" \
  "s_waitcnt lgkmcnt(6)\n\t" \
  "ds_read_b64_tr_b16 v[212:213], v248 offset:46632\n\t" \
  "ds_read_u16_d16_hi v233, v233 offset:10528\n\t" \
  "v_mfma_f32_16x16x32_bf16 v[144:147], v[216:219], v[128:131], v[144:147]\n\t" \
  "s_waitcnt lgkmcnt(7)\n\t" \
  "v_or_b32 v188, v224, v225\n\t" \
  "v_or_b32 v189, v226, v227\n\t" \
  "ds_read_b64_tr_b16 v[214:215], v248 offset:47656\n\t" \
  "v_mfma_f32_16x16x32_bf16 v[160:163], v[216:219], v[136:139], v[160:163]\n\t" \
  "v_fma_f32 v182, |v182|, v222, v182 mul:4\n\t" \
  "v_fma_f32 v183, |v183|, v223, v183 mul:4\n\t" \
  "s_setprio 0\n\t" \
  "ds_read_b32 v230, v230 offset:10016\n\t" \
  "v_mfma_f32_16x16x32_bf16 v[152:155], v[216:219], v[132:135], v[152:155]\n\t" \
  "v_fma_f32 v181, |v181|, v221, v181 mul:4\n\t" \
  "v_fma_f32 v180, |v180|, v220, v180 mul:4\n\t" \
  "ds_read_u16 v232, v232 offset:10528\n\t" \
  "ds_read_u16_d16_hi v235, v235 offset:10528\n\t" \
  "v_mfma_f32_16x16x32_bf16 v[168:171], v[216:219], v[140:143], v[168:171]\n\t" \
  "v_cvt_pk_bf16_f32 v180, v180, v181\n\t" \
  "v_cvt_pk_bf16_f32 v181, v182, v183\n\t" \
  "s_waitcnt lgkmcnt(6)\n\t" \
  "ds_read_b64_tr_b16 v[216:217], v248 offset:45608\n\t" \
  "v_mfma_f32_16x16x32_bf16 v[148:151], v[208:211], v[96:99], 0\n\t" \
  "v_cvt_pknorm_i16_f32 v222, |v144|, |v145|\n\t" \
  "v_cvt_pknorm_i16_f32 v220, |v146|, |v147|\n\t" \
  "s_nop 0\n\t" \
  "s_waitcnt vmcnt(2)\n\t" \
  "ds_read_b64_tr_b16 v[218:219], v248 offset:48680\n\t" \
  "v_mfma_f32_16x16x32_bf16 v[156:159], v[208:211], v[100:103], 0\n\t" \
  "s_waitcnt lgkmcnt(5)\n\t" \
  "v_lshlrev_b32_sdwa v221, 2, v220 dst_sel:DWORD dst_unused:UNUSED_PAD src0_sel:DWORD src1_sel:BYTE_3\n\t" \
  "v_lshlrev_b32_sdwa v220, 2, v220 dst_sel:DWORD dst_unused:UNUSED_PAD src0_sel:DWORD src1_sel:BYTE_1\n\t" \
  "s_mov_b64 exec, 3\n\t" \
  "ds_add_rtn_u32 v246, v251, v250 offset:47200\n\t" \
  "v_mfma_f32_16x16x32_bf16 v[164:167], v[208:211], v[104:107], 0\n\t" \
  "s_mov_b64 exec, -1\n\t" \
  "v_lshlrev_b32_sdwa v223, 2, v222 dst_sel:DWORD dst_unused:UNUSED_PAD src0_sel:DWORD src1_sel:BYTE_3\n\t" \
  "v_lshlrev_b32_sdwa v222, 2, v222 dst_sel:DWORD dst_unused:UNUSED_PAD src0_sel:DWORD src1_sel:BYTE_1\n\t" \
  "s_setprio 2\n\t" \
  "s_setprio 0\n\t" \
  "ds_read_u16 v234, v234 offset:10528\n\t" \
  "v_mfma_f32_16x16x32_bf16 v[156:159], v[212:215], v[116:119], v[156:159]\n\t" \
  "v_lshlrev_b32_sdwa v226, 1, v152 dst_sel:DWORD dst_unused:UNUSED_PAD src0_sel:DWORD src1_sel:WORD_1\n\t" \
  "v_lshlrev_b32_sdwa v227, 1, v153 dst_sel:DWORD dst_unused:UNUSED_PAD src0_sel:DWORD src1_sel:WORD_1\n\t" \
  "ds_read_b32 v223, v223 offset:10016\n\t" \
  "v_mfma_f32_16x16x32_bf16 v[148:151], v[212:215], v[112:115], v[148:151]\n\t" \
  "s_waitcnt lgkmcnt(2)\n\t" \
  "v_lshlrev_b32_sdwa v225, 1, v155 dst_sel:DWORD dst_unused:UNUSED_PAD src0_sel:DWORD src1_sel:WORD_1\n\t" \
  "v_readfirstlane_b32 s4, v246\n\t" \
  "ds_read_b32 v221, v221 offset:10016\n\t" \
  "v_mfma_f32_16x16x32_bf16 v[172:175], v[208:211], v[108:111], 0\n\t" \
  "s_cmpk_lt_u32 s4, 3072\n\t" \
  "v_lshlrev_b32_sdwa v224, 1, v154 dst_sel:DWORD dst_unused:UNUSED_PAD src0_sel:DWORD src1_sel:WORD_1\n\t" \
  "s_cbranch_scc1 .Lcnx_w0_9\n\t" \
  ".Lcnx_g0_9:\n\t" \
  "ds_read_b32 v220, v220 offset:10016\n\t" \
  "v_mfma_f32_16x16x32_bf16 v[164:167], v[212:215], v[120:123], v[164:167]\n\t" \
  "v_fma_mix_f32 v228, |v196|, v228, v228 op_sel:[0,1,0] op_sel_hi:[0,1,1] clamp\n\t" \
  "v_fma_mix_f32 v229, |v197|, v229, v229 op_sel:[0,1,0] op_sel_hi:[0,1,1] clamp\n\t" \
  "ds_read_b64_tr_b16 v[210:211], v249 offset:13840\n\t" \
  "ds_read_b64_tr_b16 v[208:209], v249 offset:12304\n\t" \
  "v_mfma_f32_16x16x32_bf16 v[172:175], v[212:215], v[124:127], v[172:175]\n\t" \
  "v_fma_mix_f32 v230, |v198|, v230, v230 op_sel:[0,1,0] op_sel_hi:[0,1,1] clamp\n\t" \
  "v_fma_mix_f32 v231, |v199|, v231, v231 op_sel:[0,1,0] op_sel_hi:[0,1,1] clamp\n\t" \
  "s_waitcnt lgkmcnt(5)\n\t" \
  "ds_read_b64_tr_b16 v[212:213], v249 offset:12312\n\t" \
  "ds_read_b32 v222, v222 offset:10016\n\t" \
  "v_mfma_f32_16x16x32_bf16 v[148:151], v[216:219], v[128:131], v[148:151]\n\t" \
  "s_mov_b32 m0, s45\n\t" \
  "v_or_b32 v204, v232, v233\n\t" \
  "v_or_b32 v205, v234, v235\n\t" \
  "s_mov_b32 exec_hi, 0xffff\n\t" \
  "buffer_load_dwordx4 v252, s[16:19], s33 offen lds\n\t" \
  "v_mfma_f32_16x16x32_bf16 v[156:159], v[216:219], v[132:135], v[156:159]\n\t" \
  "s_mov_b32 exec_hi, -1\n\t" \
  "v_fma_f32 v197, |v197|, v229, v197 mul:4\n\t" \
  "v_fma_f32 v196, |v196|, v228, v196 mul:4\n\t" \
  "s_add_u32 s33, s33, s48\n\t" \
  "ds_read_u16_d16_hi v227, v227 offset:10528\n\t" \
  "ds_read_b64_tr_b16 v[214:215], v249 offset:13848\n\t" \
  "v_mfma_f32_16x16x32_bf16 v[164:167], v[216:219], v[136:139], v[164:167]\n\t" \
  "v_fma_f32 v198, |v198|, v230, v198 mul:4\n\t" \
  "v_fma_f32 v199, |v199|, v231, v199 mul:4\n\t" \
  "s_waitcnt lgkmcnt(4)\n\t" \
  "ds_read_u16_d16_hi v225, v225 offset:10528\n\t" \
  "v_mfma_f32_16x16x32_bf16 v[172:175], v[216:219], v[140:143], v[172:175]\n\t" \
  "v_cvt_pk_bf16_f32 v196, v196, v197\n\t" \
  "v_cvt_pk_bf16_f32 v197, v198, v199\n\t" \
  "ds_read_b64_tr_b16 v[216:217], v249 offset:12368\n\t" \
  "v_mfma_f32_16x16x32_bf16 v[0:3], v[208:211], v[178:181], v[0:3]\n\t" \
  "v_cvt_pknorm_i16_f32 v228, |v162|, |v163|\n\t" \
  "v_cvt_pknorm_i16_f32 v230, |v160|, |v161|\n\t" \
  "ds_read_b64_tr_b16 v[218:219], v249 offset:13904\n\t" \
  "v_mfma_f32_16x16x32_bf16 v[24:27], v[208:211], v[186:189], v[24:27]\n\t" \
  "v_lshlrev_b32_sdwa v229, 2, v228 dst_sel:DWORD dst_unused:UNUSED_PAD src0_sel:DWORD src1_sel:BYTE_3\n\t" \
  "v_lshlrev_b32_sdwa v228, 2, v228 dst_sel:DWORD dst_unused:UNUSED_PAD src0_sel:DWORD src1_sel:BYTE_1\n\t" \
  "ds_read_u16 v224, v224 offset:10528\n\t" \
  "v_mfma_f32_16x16x32_bf16 v[48:51], v[208:211], v[194:197], v[48:51]\n\t" \
  "v_lshlrev_b32_sdwa v231, 2, v230 dst_sel:DWORD dst_unused:UNUSED_PAD src0_sel:DWORD src1_sel:BYTE_3\n\t" \
  "s_add_u32 m0, s45, s35\n\t" \
  "s_setprio 1\n\t" \
  "s_waitcnt lgkmcnt(4)\n\t" \
  "ds_read_b32 v228, v228 offset:10016\n\t" \
  "v_mfma_f32_16x16x32_bf16 v[72:75], v[208:211], v[202:205], v[72:75]\n\t" \
  "v_lshlrev_b32_sdwa v232, 1, v170 dst_sel:DWORD dst_unused:UNUSED_PAD src0_sel:DWORD src1_sel:WORD_1\n\t" \
  "v_lshlrev_b32_sdwa v233, 1, v171 dst_sel:DWORD dst_unused:UNUSED_PAD src0_sel:DWORD src1_sel:WORD_1\n\t" \
  "s_mov_b32 exec_hi, 0xffff\n\t" \
  "buffer_load_dwordx4 v252, s[16:19], s34 offen lds\n\t" \
  "v_mfma_f32_16x16x32_bf16 v[4:7], v[212:215], v[178:181], v[4:7]\n\t" \
  "s_mov_b32 exec_hi, -1\n\t" \
  "s_add_u32 s34, s34, s48\n\t" \
  "v_lshlrev_b32_sdwa v235, 1, v169 dst_sel:DWORD dst_unused:UNUSED_PAD src0_sel:DWORD src1_sel:WORD_1\n\t" \
  "ds_read_u16 v226, v226 offset:10528\n\t" \
  "ds_read_b64_tr_b16 v[208:209], v249 offset:12376\n\t" \
  "v_mfma_f32_16x16x32_bf16 v[28:31], v[212:215], v[186:189], v[28:31]\n\t" \
  "v_lshlrev_b32_sdwa v234, 1, v168 dst_sel:DWORD dst_unused:UNUSED_PAD src0_sel:DWORD src1_sel:WORD_1\n\t" \
  "v_lshlrev_b32_sdwa v230, 2, v230 dst_sel:DWORD dst_unused:UNUSED_PAD src0_sel:DWORD src1_sel:BYTE_1\n\t" \
  "ds_read_b64_tr_b16 v[210:211], v249 offset:13912\n\t" \
  "ds_read_b32 v229, v229 offset:10016\n\t" \
  "v_mfma_f32_16x16x32_bf16 v[76:79], v[212:215], v[202:205], v[76:79]\n\t" \
  "v_fma_mix_f32 v223, |v145|, v223, v223 op_sel:[0,1,0] op_sel_hi:[0,1,1] clamp\n\t" \
  "v_fma_mix_f32 v222, |v144|, v222, v222 op_sel:[0,1,0] op_sel_hi:[0,1,1] clamp\n\t" \
  "ds_read_b32 v230, v230 offset:10016\n\t" \
  "v_mfma_f32_16x16x32_bf16 v[52:55], v[212:215], v[194:197], v[52:55]\n\t" \
  "v_fma_mix_f32 v220, |v146|, v220, v220 op_sel:[0,1,0] op_sel_hi:[0,1,1] clamp\n\t" \
  "v_fma_mix_f32 v221, |v147|, v221, v221 op_sel:[0,1,0] op_sel_hi:[0,1,1] clamp\n\t" \
  "s_setprio 3\n\t" \
  "s_waitcnt lgkmcnt(4)\n\t" \
  "ds_read_b32 v231, v231 offset:10016\n\t" \
  "ds_read_b64_tr_b16 v[212:213], v249 offset:12432\n\t" \
  "v_mfma_f32_16x16x32_bf16 v[8:11], v[216:219], v[178:181], v[8:11]\n\t" \
  "v_or_b32 v155, v224, v225\n\t" \
  "v_or_b32 v154, v226, v227\n\t" \
  "s_setprio 1\n\t" \
  "ds_read_u16_d16_hi v233, v233 offset:10528\n\t" \
  "ds_read_b64_tr_b16 v[214:215], v249 offset:13968\n\t" \
  "v_mfma_f32_16x16x32_bf16 v[32:35], v[216:219], v[186:189], v[32:35]\n\t" \
  "s_add_u32 s24, s24, s26\n\t" \
  "v_fma_f32 v146, |v146|, v220, v146 mul:4\n\t" \
  "v_fma_f32 v147, |v147|, v221, v147 mul:4\n\t" \
  "v_mfma_f32_16x16x32_bf16 v[56:59], v[216:219], v[194:197], v[56:59]\n\t" \
  "v_fma_f32 v145, |v145|, v223, v145 mul:4\n\t" \
  "v_fma_f32 v144, |v144|, v222, v144 mul:4\n\t" \
  "ds_read_u16 v232, v232 offset:10528\n\t" \
  "v_mfma_f32_16x16x32_bf16 v[80:83], v[216:219], v[202:205], v[80:83]\n\t" \
  "s_add_u32 m0, s25, 65080\n\t" \
  "v_cvt_pk_bf16_f32 v147, v146, v147\n\t" \
  "v_cvt_pk_bf16_f32 v146, v144, v145\n\t" \
  "s_setprio 0\n\t" \
  "s_waitcnt lgkmcnt(7)\n\t" \
  "buffer_load_dwordx4 v254, s[8:11], s24 offen lds\n\t" \
  "v_mfma_f32_16x16x32_bf16 v[12:15], v[208:211], v[178:181], v[12:15]\n\t" \
  "v_cvt_pknorm_i16_f32 v220, |v148|, |v149|\n\t" \
  "v_cvt_pknorm_i16_f32 v222, |v150|, |v151|\n\t" \
  "ds_read_b64_tr_b16 v[216:217], v249 offset:12440\n\t" \
  "ds_read_b64_tr_b16 v[218:219], v249 offset:13976\n\t" \
  "v_mfma_f32_16x16x32_bf16 v[36:39], v[208:211], v[186:189], v[36:39]\n\t" \
  "v_lshlrev_b32_sdwa v221, 2, v220 dst_sel:DWORD dst_unused:UNUSED_PAD src0_sel:DWORD src1_sel:BYTE_3\n\t" \
  "v_lshlrev_b32_sdwa v220, 2, v220 dst_sel:DWORD dst_unused:UNUSED_PAD src0_sel:DWORD src1_sel:BYTE_1\n\t" \
  "ds_read_u16_d16_hi v235, v235 offset:10528\n\t" \
  "v_mfma_f32_16x16x32_bf16 v[60:63], v[208:211], v[194:197], v[60:63]\n\t" \
  "v_lshlrev_b32_sdwa v223, 2, v222 dst_sel:DWORD dst_unused:UNUSED_PAD src0_sel:DWORD src1_sel:BYTE_3\n\t" \
  "v_lshlrev_b32_sdwa v222, 2, v222 dst_sel:DWORD dst_unused:UNUSED_PAD src0_sel:DWORD src1_sel:BYTE_1\n\t" \
  "ds_read_u16 v234, v234 offset:10528\n\t" \
  "s_waitcnt lgkmcnt(5)\n\t" \
  "v_mfma_f32_16x16x32_bf16 v[84:87], v[208:211], v[202:205], v[84:87]\n\t" \
  "s_setprio 1\n\t" \
  "v_lshlrev_b32_sdwa v225, 1, v157 dst_sel:DWORD dst_unused:UNUSED_PAD src0_sel:DWORD src1_sel:WORD_1\n\t" \
  "v_lshlrev_b32_sdwa v224, 1, v156 dst_sel:DWORD dst_unused:UNUSED_PAD src0_sel:DWORD src1_sel:WORD_1\n\t" \
  "ds_read_b64_tr_b16 v[208:209], v248 offset:49192\n\t" \
  "ds_read_b32 v220, v220 offset:10016\n\t" \
  "v_mfma_f32_16x16x32_bf16 v[16:19], v[212:215], v[178:181], v[16:19]\n\t" \
  "v_nop\n\t" \
  "s_add_u32 s27, s27, s26\n\t" \
  "s_mov_b64 exec, s[30:31]\n\t" \
  "buffer_load_dword v237, v254, s[8:11], s27 offen\n\t" \
  "v_mfma_f32_16x16x32_bf16 v[64:67], v[212:215], v[194:197], v[64:67]\n\t" \
  "s_mov_b64 exec, -1\n\t" \
  "v_fma_mix_f32 v229, |v163|, v229, v229 op_sel:[0,1,0] op_sel_hi:[0,1,1] clamp\n\t" \
  "v_lshlrev_b32_sdwa v227, 1, v159 dst_sel:DWORD dst_unused:UNUSED_PAD src0_sel:DWORD src1_sel:WORD_1\n\t" \
  "s_setprio 2\n\t" \
  "ds_read_b32 v223, v223 offset:10016\n\t" \
  "ds_read_b32 v221, v221 offset:10016\n\t" \
  "v_mfma_f32_16x16x32_bf16 v[88:91], v[212:215], v[202:205], v[88:91]\n\t" \
  "s_setprio 1\n\t" \
  "v_fma_mix_f32 v231, |v161|, v231, v231 op_sel:[0,1,0] op_sel_hi:[0,1,1] clamp\n\t" \
  "v_fma_mix_f32 v230, |v160|, v230, v230 op_sel:[0,1,0] op_sel_hi:[0,1,1] clamp\n\t" \
  "ds_read_b64_tr_b16 v[210:211], v248 offset:50216\n\t" \
  "v_mfma_f32_16x16x32_bf16 v[40:43], v[212:215], v[186:189], v[40:43]\n\t" \
  "s_waitcnt lgkmcnt(5)\n\t" \
  "v_fma_mix_f32 v228, |v162|, v228, v228 op_sel:[0,1,0] op_sel_hi:[0,1,1] clamp\n\t" \
  "v_lshlrev_b32_sdwa v226, 1, v158 dst_sel:DWORD dst_unused:UNUSED_PAD src0_sel:DWORD src1_sel:WORD_1\n\t" \
  "ds_read_b32 v222, v222 offset:10016\n\t" \
  "ds_read_b64_tr_b16 v[212:213], v248 offset:52264\n\t" \
  "v_mfma_f32_16x16x32_bf16 v[20:23], v[216:219], v[178:181], v[20:23]\n\t" \
  "v_or_b32 v171, v232, v233\n\t" \
  "v_or_b32 v170, v234, v235\n\t" \
  "ds_read_u16_d16_hi v225, v225 offset:10528\n\t" \
  "v_mfma_f32_16x16x32_bf16 v[68:71], v[216:219], v[194:197], v[68:71]\n\t" \
  "s_setprio 2\n\t" \
  "v_fma_f32 v160, |v160|, v230, v160 mul:4\n\t" \
  "v_fma_f32 v163, |v163|, v229, v163 mul:4\n\t" \
  "s_waitcnt lgkmcnt(3)\n\t" \
  "ds_read_u16 v224, v224 offset:10528\n\t" \
  "v_mfma_f32_16x16x32_bf16 v[176:179], v[208:211], v[96:99], 0\n\t" \
  "v_lshlrev_b32_sdwa v232, 1, v172 dst_sel:DWORD dst_unused:UNUSED_PAD src0_sel:DWORD src1_sel:WORD_1\n\t" \
  "v_fma_f32 v161, |v161|, v231, v161 mul:4\n\t" \
  "ds_read_b64_tr_b16 v[214:215], v248 offset:53288\n\t" \
  "v_mfma_f32_16x16x32_bf16 v[44:47], v[216:219], v[186:189], v[44:47]\n\t" \
  "v_fma_f32 v162, |v162|, v228, v162 mul:4\n\t" \
  "v_cvt_pknorm_i16_f32 v230, |v166|, |v167|\n\t" \
  "ds_read_u16_d16_hi v227, v227 offset:10528\n\t" \
  "v_mfma_f32_16x16x32_bf16 v[92:95], v[216:219], v[202:205], v[92:95]\n\t" \
  "v_cvt_pk_bf16_f32 v163, v162, v163\n\t" \
  "v_cvt_pk_bf16_f32 v162, v160, v161\n\t" \
  "ds_read_b64_tr_b16 v[216:217], v248 offset:51240\n\t" \
  "v_mfma_f32_16x16x32_bf16 v[192:195], v[208:211], v[104:107], 0\n\t" \
  "v_cvt_pknorm_i16_f32 v228, |v164|, |v165|\n\t" \
  "v_lshlrev_b32_sdwa v231, 2, v230 dst_sel:DWORD dst_unused:UNUSED_PAD src0_sel:DWORD src1_sel:BYTE_3\n\t" \
  "ds_read_b64_tr_b16 v[218:219], v248 offset:54312\n\t" \
  "v_mfma_f32_16x16x32_bf16 v[184:187], v[208:211], v[100:103], 0\n\t" \
  "v_lshlrev_b32_sdwa v229, 2, v228 dst_sel:DWORD dst_unused:UNUSED_PAD src0_sel:DWORD src1_sel:BYTE_3\n\t" \
  "v_lshlrev_b32_sdwa v228, 2, v228 dst_sel:DWORD dst_unused:UNUSED_PAD src0_sel:DWORD src1_sel:BYTE_1\n\t" \
  "ds_read_u16 v226, v226 offset:10528\n\t" \
  "s_waitcnt lgkmcnt(4)\n\t" \
  "v_mfma_f32_16x16x32_bf16 v[200:203], v[208:211], v[108:111], 0\n\t" \
  "v_lshlrev_b32_sdwa v230, 2, v230 dst_sel:DWORD dst_unused:UNUSED_PAD src0_sel:DWORD src1_sel:BYTE_1\n\t" \
  "v_lshlrev_b32_sdwa v233, 1, v173 dst_sel:DWORD dst_unused:UNUSED_PAD src0_sel:DWORD src1_sel:WORD_1\n\t" \
  "ds_read_b64_tr_b16 v[208:209], v248 offset:49704\n\t" \
  "v_mfma_f32_16x16x32_bf16 v[176:179], v[212:215], v[112:115], v[176:179]\n\t" \
  "v_lshlrev_b32_sdwa v235, 1, v175 dst_sel:DWORD dst_unused:UNUSED_PAD src0_sel:DWORD src1_sel:WORD_1\n\t" \
  "v_fma_mix_f32 v221, |v149|, v221, v221 op_sel:[0,1,0] op_sel_hi:[0,1,1] clamp\n\t" \
  "ds_read_b32 v231, v231 offset:10016\n\t" \
  "v_mfma_f32_16x16x32_bf16 v[200:203], v[212:215], v[124:127], v[200:203]\n\t" \
  "v_lshlrev_b32_sdwa v234, 1, v174 dst_sel:DWORD dst_unused:UNUSED_PAD src0_sel:DWORD src1_sel:WORD_1\n\t" \
  "ds_read_b32 v228, v228 offset:10016\n\t" \
  "ds_read_b32 v229, v229 offset:10016\n\t" \
  "v_mfma_f32_16x16x32_bf16 v[192:195], v[212:215], v[120:123], v[192:195]\n\t" \
  "s_setprio 1\n\t" \
  "v_fma_mix_f32 v220, |v148|, v220, v220 op_sel:[0,1,0] op_sel_hi:[0,1,1] clamp\n\t" \
  "ds_read_b64_tr_b16 v[210:211], v248 offset:50728\n\t" \
  "v_mfma_f32_16x16x32_bf16 v[184:187], v[212:215], v[116:119], v[184:187]\n\t" \
  "v_fma_mix_f32 v222, |v150|, v222, v222 op_sel:[0,1,0] op_sel_hi:[0,1,1] clamp\n\t" \
  "v_fma_mix_f32 v223, |v151|, v223, v223 op_sel:[0,1,0] op_sel_hi:[0,1,1] clamp\n\t" \
  "s_waitcnt lgkmcnt(6)\n\t" \
  "ds_read_b64_tr_b16 v[212:213], v248 offset:52776\n\t" \
  "ds_read_u16_d16_hi v233, v233 offset:10528\n\t" \
  "v_mfma_f32_16x16x32_bf16 v[176:179], v[216:219], v[128:131], v[176:179]\n\t" \
  "s_waitcnt lgkmcnt(7)\n\t" \
  "v_or_b32 v156, v224, v225\n\t" \
  "v_or_b32 v157, v226, v227\n\t" \
  "ds_read_b64_tr_b16 v[214:215], v248 offset:53800\n\t" \
  "v_mfma_f32_16x16x32_bf16 v[192:195], v[216:219], v[136:139], v[192:195]\n\t" \
  "v_fma_f32 v150, |v150|, v222, v150 mul:4\n\t" \
  "v_fma_f32 v151, |v151|, v223, v151 mul:4\n\t" \
  "s_setprio 0\n\t" \
  "ds_read_b32 v230, v230 offset:10016\n\t" \
  "v_mfma_f32_16x16x32_bf16 v[184:187], v[216:219], v[132:135], v[184:187]\n\t" \
  "v_fma_f32 v149, |v149|, v221, v149 mul:4\n\t" \
  "v_fma_f32 v148, |v148|, v220, v148 mul:4\n\t" \
  "ds_read_u16 v232, v232 offset:10528\n\t" \
  "ds_read_u16_d16_hi v235, v235 offset:10528\n\t" \
  "v_mfma_f32_16x16x32_bf16 v[200:203], v[216:219], v[140:143], v[200:203]\n\t" \
  "v_cvt_pk_bf16_f32 v148, v148, v149\n\t" \
  "v_cvt_pk_bf16_f32 v149, v150, v151\n\t" \
  "s_waitcnt lgkmcnt(6)\n\t" \
  "ds_read_b64_tr_b16 v[216:217], v248 offset:51752\n\t" \
  "v_mfma_f32_16x16x32_bf16 v[180:183], v[208:211], v[96:99], 0\n\t" \
  "v_cvt_pknorm_i16_f32 v222, |v176|, |v177|\n\t" \
  "v_cvt_pknorm_i16_f32 v220, |v178|, |v179|\n\t" \
  "s_nop 0\n\t" \
  "s_waitcnt vmcnt(2)\n\t" \
  "ds_read_b64_tr_b16 v[218:219], v248 offset:54824\n\t" \
  "v_mfma_f32_16x16x32_bf16 v[188:191], v[208:211], v[100:103], 0\n\t" \
  "s_waitcnt lgkmcnt(5)\n\t" \
  "v_lshlrev_b32_sdwa v221, 2, v220 dst_sel:DWORD dst_unused:UNUSED_PAD src0_sel:DWORD src1_sel:BYTE_3\n\t" \
  "v_lshlrev_b32_sdwa v220, 2, v220 dst_sel:DWORD dst_unused:UNUSED_PAD src0_sel:DWORD src1_sel:BYTE_1\n\t" \
  "s_mov_b64 exec, 3\n\t" \
  "ds_add_rtn_u32 v246, v251, v250 offset:47208\n\t" \
  "v_mfma_f32_16x16x32_bf16 v[196:199], v[208:211], v[104:107], 0\n\t" \
  "s_mov_b64 exec, -1\n\t" \
  "v_lshlrev_b32_sdwa v223, 2, v222 dst_sel:DWORD dst_unused:UNUSED_PAD src0_sel:DWORD src1_sel:BYTE_3\n\t" \
  "v_lshlrev_b32_sdwa v222, 2, v222 dst_sel:DWORD dst_unused:UNUSED_PAD src0_sel:DWORD src1_sel:BYTE_1\n\t" \
  "s_setprio 2\n\t" \
  "s_setprio 0\n\t" \
  "ds_read_u16 v234, v234 offset:10528\n\t" \
  "v_mfma_f32_16x16x32_bf16 v[188:191], v[212:215], v[116:119], v[188:191]\n\t" \
  "v_lshlrev_b32_sdwa v226, 1, v184 dst_sel:DWORD dst_unused:UNUSED_PAD src0_sel:DWORD src1_sel:WORD_1\n\t" \
  "v_lshlrev_b32_sdwa v227, 1, v185 dst_sel:DWORD dst_unused:UNUSED_PAD src0_sel:DWORD src1_sel:WORD_1\n\t" \
  "ds_read_b32 v223, v223 offset:10016\n\t" \
  "v_mfma_f32_16x16x32_bf16 v[180:183], v[212:215], v[112:115], v[180:183]\n\t" \
  "s_waitcnt lgkmcnt(2)\n\t" \
  "v_lshlrev_b32_sdwa v225, 1, v187 dst_sel:DWORD dst_unused:UNUSED_PAD src0_sel:DWORD src1_sel:WORD_1\n\t" \
  "v_readfirstlane_b32 s4, v246\n\t" \
  "ds_read_b32 v221, v221 offset:10016\n\t" \
  "v_mfma_f32_16x16x32_bf16 v[204:207], v[208:211], v[108:111], 0\n\t" \
  "s_cmpk_lt_u32 s4, 3072\n\t" \
  "v_lshlrev_b32_sdwa v224, 1, v186 dst_sel:DWORD dst_unused:UNUSED_PAD src0_sel:DWORD src1_sel:WORD_1\n\t" \
  "s_cbranch_scc1 .Lcnx_w0_10\n\t" \
  ".Lcnx_g0_10:\n\t" \
  "ds_read_b32 v220, v220 offset:10016\n\t" \
  "v_mfma_f32_16x16x32_bf16 v[196:199], v[212:215], v[120:123], v[196:199]\n\t" \
  "v_fma_mix_f32 v228, |v164|, v228, v228 op_sel:[0,1,0] op_sel_hi:[0,1,1] clamp\n\t" \
  "v_fma_mix_f32 v229, |v165|, v229, v229 op_sel:[0,1,0] op_sel_hi:[0,1,1] clamp\n\t" \
  "ds_read_b64_tr_b16 v[210:211], v249 offset:26136\n\t" \
  "ds_read_b64_tr_b16 v[208:209], v249 offset:24600\n\t" \
  "v_mfma_f32_16x16x32_bf16 v[204:207], v[212:215], v[124:127], v[204:207]\n\t" \
  "v_fma_mix_f32 v230, |v166|, v230, v230 op_sel:[0,1,0] op_sel_hi:[0,1,1] clamp\n\t" \
  "v_fma_mix_f32 v231, |v167|, v231, v231 op_sel:[0,1,0] op_sel_hi:[0,1,1] clamp\n\t" \
  "s_waitcnt lgkmcnt(5)\n\t" \
  "ds_read_b64_tr_b16 v[212:213], v249 offset:24608\n\t" \
  "ds_read_b32 v222, v222 offset:10016\n\t" \
  "v_mfma_f32_16x16x32_bf16 v[180:183], v[216:219], v[128:131], v[180:183]\n\t" \
  "s_mov_b32 m0, s46\n\t" \
  "v_or_b32 v172, v232, v233\n\t" \
  "v_or_b32 v173, v234, v235\n\t" \
  "s_mov_b32 exec_hi, 0xffff\n\t" \
  "buffer_load_dwordx4 v252, s[16:19], s33 offen lds\n\t" \
  "v_mfma_f32_16x16x32_bf16 v[188:191], v[216:219], v[132:135], v[188:191]\n\t" \
  "s_mov_b32 exec_hi, -1\n\t" \
  "v_fma_f32 v165, |v165|, v229, v165 mul:4\n\t" \
  "v_fma_f32 v164, |v164|, v228, v164 mul:4\n\t" \
  "s_add_u32 s33, s33, s32\n\t" \
  "ds_read_u16_d16_hi v227, v227 offset:10528\n\t" \
  "ds_read_b64_tr_b16 v[214:215], v249 offset:26144\n\t" \
  "v_mfma_f32_16x16x32_bf16 v[196:199], v[216:219], v[136:139], v[196:199]\n\t" \
  "v_fma_f32 v166, |v166|, v230, v166 mul:4\n\t" \
  "v_fma_f32 v167, |v167|, v231, v167 mul:4\n\t" \
  "s_waitcnt lgkmcnt(4)\n\t" \
  "ds_read_u16_d16_hi v225, v225 offset:10528\n\t" \
  "v_mfma_f32_16x16x32_bf16 v[204:207], v[216:219], v[140:143], v[204:207]\n\t" \
  "v_cvt_pk_bf16_f32 v164, v164, v165\n\t" \
  "v_cvt_pk_bf16_f32 v165, v166, v167\n\t" \
  "ds_read_b64_tr_b16 v[216:217], v249 offset:24664\n\t" \
  "v_mfma_f32_16x16x32_bf16 v[0:3], v[208:211], v[146:149], v[0:3]\n\t" \
  "v_cvt_pknorm_i16_f32 v228, |v194|, |v195|\n\t" \
  "v_cvt_pknorm_i16_f32 v230, |v192|, |v193|\n\t" \
  "ds_read_b64_tr_b16 v[218:219], v249 offset:26200\n\t" \
  "v_mfma_f32_16x16x32_bf16 v[24:27], v[208:211], v[154:157], v[24:27]\n\t" \
  "v_lshlrev_b32_sdwa v229, 2, v228 dst_sel:DWORD dst_unused:UNUSED_PAD src0_sel:DWORD src1_sel:BYTE_3\n\t" \
  "v_lshlrev_b32_sdwa v228, 2, v228 dst_sel:DWORD dst_unused:UNUSED_PAD src0_sel:DWORD src1_sel:BYTE_1\n\t" \
  "ds_read_u16 v224, v224 offset:10528\n\t" \
  "v_mfma_f32_16x16x32_bf16 v[48:51], v[208:211], v[162:165], v[48:51]\n\t" \
  "v_lshlrev_b32_sdwa v231, 2, v230 dst_sel:DWORD dst_unused:UNUSED_PAD src0_sel:DWORD src1_sel:BYTE_3\n\t" \
  "s_add_u32 m0, s46, s35\n\t" \
  "s_setprio 1\n\t" \
  "s_waitcnt lgkmcnt(4)\n\t" \
  "ds_read_b32 v228, v228 offset:10016\n\t" \
  "v_mfma_f32_16x16x32_bf16 v[72:75], v[208:211], v[170:173], v[72:75]\n\t" \
  "v_lshlrev_b32_sdwa v232, 1, v202 dst_sel:DWORD dst_unused:UNUSED_PAD src0_sel:DWORD src1_sel:WORD_1\n\t" \
  "v_lshlrev_b32_sdwa v233, 1, v203 dst_sel:DWORD dst_unused:UNUSED_PAD src0_sel:DWORD src1_sel:WORD_1\n\t" \
  "s_mov_b32 exec_hi, 0xffff\n\t" \
  "buffer_load_dwordx4 v252, s[16:19], s34 offen lds\n\t" \
  "v_mfma_f32_16x16x32_bf16 v[4:7], v[212:215], v[146:149], v[4:7]\n\t" \
  "s_mov_b32 exec_hi, -1\n\t" \
  "s_add_u32 s34, s34, s32\n\t" \
  "v_lshlrev_b32_sdwa v235, 1, v201 dst_sel:DWORD dst_unused:UNUSED_PAD src0_sel:DWORD src1_sel:WORD_1\n\t" \
  "ds_read_u16 v226, v226 offset:10528\n\t" \
  "ds_read_b64_tr_b16 v[208:209], v249 offset:24672\n\t" \
  "v_mfma_f32_16x16x32_bf16 v[28:31], v[212:215], v[154:157], v[28:31]\n\t" \
  "v_lshlrev_b32_sdwa v234, 1, v200 dst_sel:DWORD dst_unused:UNUSED_PAD src0_sel:DWORD src1_sel:WORD_1\n\t" \
  "v_lshlrev_b32_sdwa v230, 2, v230 dst_sel:DWORD dst_unused:UNUSED_PAD src0_sel:DWORD src1_sel:BYTE_1\n\t" \
  "ds_read_b64_tr_b16 v[210:211], v249 offset:26208\n\t" \
  "ds_read_b32 v229, v229 offset:10016\n\t" \
  "v_mfma_f32_16x16x32_bf16 v[76:79], v[212:215], v[170:173], v[76:79]\n\t" \
  "v_fma_mix_f32 v223, |v177|, v223, v223 op_sel:[0,1,0] op_sel_hi:[0,1,1] clamp\n\t" \
  "v_fma_mix_f32 v222, |v176|, v222, v222 op_sel:[0,1,0] op_sel_hi:[0,1,1] clamp\n\t" \
  "ds_read_b32 v230, v230 offset:10016\n\t" \
  "v_mfma_f32_16x16x32_bf16 v[52:55], v[212:215], v[162:165], v[52:55]\n\t" \
  "v_fma_mix_f32 v220, |v178|, v220, v220 op_sel:[0,1,0] op_sel_hi:[0,1,1] clamp\n\t" \
  "v_fma_mix_f32 v221, |v179|, v221, v221 op_sel:[0,1,0] op_sel_hi:[0,1,1] clamp\n\t" \
  "s_setprio 3\n\t" \
  "s_waitcnt lgkmcnt(4)\n\t" \
  "ds_read_b32 v231, v231 offset:10016\n\t" \
  "ds_read_b64_tr_b16 v[212:213], v249 offset:24728\n\t" \
  "v_mfma_f32_16x16x32_bf16 v[8:11], v[216:219], v[146:149], v[8:11]\n\t" \
  "v_or_b32 v187, v224, v225\n\t" \
  "v_or_b32 v186, v226, v227\n\t" \
  "s_setprio 1\n\t" \
  "ds_read_u16_d16_hi v233, v233 offset:10528\n\t" \
  "ds_read_b64_tr_b16 v[214:215], v249 offset:26264\n\t" \
  "v_mfma_f32_16x16x32_bf16 v[32:35], v[216:219], v[154:157], v[32:35]\n\t" \
  "s_add_u32 s24, s24, s26\n\t" \
  "v_fma_f32 v178, |v178|, v220, v178 mul:4\n\t" \
  "v_fma_f32 v179, |v179|, v221, v179 mul:4\n\t" \
  "v_mfma_f32_16x16x32_bf16 v[56:59], v[216:219], v[162:165], v[56:59]\n\t" \
  "v_fma_f32 v177, |v177|, v223, v177 mul:4\n\t" \
  "v_fma_f32 v176, |v176|, v222, v176 mul:4\n\t" \
  "ds_read_u16 v232, v232 offset:10528\n\t" \
  "v_mfma_f32_16x16x32_bf16 v[80:83], v[216:219], v[170:173], v[80:83]\n\t" \
  "s_add_u32 m0, s25, 73280\n\t" \
  "v_cvt_pk_bf16_f32 v179, v178, v179\n\t" \
  "v_cvt_pk_bf16_f32 v178, v176, v177\n\t" \
  "s_setprio 0\n\t" \
  "s_waitcnt lgkmcnt(7)\n\t" \
  "buffer_load_dwordx4 v254, s[8:11], s24 offen lds\n\t" \
  "v_mfma_f32_16x16x32_bf16 v[12:15], v[208:211], v[146:149], v[12:15]\n\t" \
  "v_cvt_pknorm_i16_f32 v220, |v180|, |v181|\n\t" \
  "v_cvt_pknorm_i16_f32 v222, |v182|, |v183|\n\t" \
  "ds_read_b64_tr_b16 v[216:217], v249 offset:24736\n\t" \
  "ds_read_b64_tr_b16 v[218:219], v249 offset:26272\n\t" \
  "v_mfma_f32_16x16x32_bf16 v[36:39], v[208:211], v[154:157], v[36:39]\n\t" \
  "v_lshlrev_b32_sdwa v221, 2, v220 dst_sel:DWORD dst_unused:UNUSED_PAD src0_sel:DWORD src1_sel:BYTE_3\n\t" \
  "v_lshlrev_b32_sdwa v220, 2, v220 dst_sel:DWORD dst_unused:UNUSED_PAD src0_sel:DWORD src1_sel:BYTE_1\n\t" \
  "ds_read_u16_d16_hi v235, v235 offset:10528\n\t" \
  "v_mfma_f32_16x16x32_bf16 v[60:63], v[208:211], v[162:165], v[60:63]\n\t" \
  "v_lshlrev_b32_sdwa v223, 2, v222 dst_sel:DWORD dst_unused:UNUSED_PAD src0_sel:DWORD src1_sel:BYTE_3\n\t" \
  "v_lshlrev_b32_sdwa v222, 2, v222 dst_sel:DWORD dst_unused:UNUSED_PAD src0_sel:DWORD src1_sel:BYTE_1\n\t" \
  "ds_read_u16 v234, v234 offset:10528\n\t" \
  "s_waitcnt lgkmcnt(5)\n\t" \
  "v_mfma_f32_16x16x32_bf16 v[84:87], v[208:211], v[170:173], v[84:87]\n\t" \
  "s_setprio 1\n\t" \
  "v_lshlrev_b32_sdwa v225, 1, v189 dst_sel:DWORD dst_unused:UNUSED_PAD src0_sel:DWORD src1_sel:WORD_1\n\t" \
  "v_lshlrev_b32_sdwa v224, 1, v188 dst_sel:DWORD dst_unused:UNUSED_PAD src0_sel:DWORD src1_sel:WORD_1\n\t" \
  "ds_read_b64_tr_b16 v[208:209], v248 offset:36904\n\t" \
  "ds_read_b32 v220, v220 offset:10016\n\t" \
  "v_mfma_f32_16x16x32_bf16 v[16:19], v[212:215], v[146:149], v[16:19]\n\t" \
  "v_nop\n\t" \
  "s_add_u32 s27, s27, s26\n\t" \
  "s_mov_b64 exec, s[30:31]\n\t" \
  "buffer_load_dword v237, v254, s[8:11], s27 offen\n\t" \
  "v_mfma_f32_16x16x32_bf16 v[64:67], v[212:215], v[162:165], v[64:67]\n\t" \
  "s_mov_b64 exec, -1\n\t" \
  "v_fma_mix_f32 v229, |v195|, v229, v229 op_sel:[0,1,0] op_sel_hi:[0,1,1] clamp\n\t" \
  "v_lshlrev_b32_sdwa v227, 1, v191 dst_sel:DWORD dst_unused:UNUSED_PAD src0_sel:DWORD src1_sel:WORD_1\n\t" \
  "s_setprio 2\n\t" \
  "ds_read_b32 v223, v223 offset:10016\n\t" \
  "ds_read_b32 v221, v221 offset:10016\n\t" \
  "v_mfma_f32_16x16x32_bf16 v[88:91], v[212:215], v[170:173], v[88:91]\n\t" \
  "s_setprio 1\n\t" \
  "v_fma_mix_f32 v231, |v193|, v231, v231 op_sel:[0,1,0] op_sel_hi:[0,1,1] clamp\n\t" \
  "v_fma_mix_f32 v230, |v192|, v230, v230 op_sel:[0,1,0] op_sel_hi:[0,1,1] clamp\n\t" \
  "ds_read_b64_tr_b16 v[210:211], v248 offset:37928\n\t" \
  "v_mfma_f32_16x16x32_bf16 v[40:43], v[212:215], v[154:157], v[40:43]\n\t" \
  "s_waitcnt lgkmcnt(5)\n\t" \
  "v_fma_mix_f32 v228, |v194|, v228, v228 op_sel:[0,1,0] op_sel_hi:[0,1,1] clamp\n\t" \
  "v_lshlrev_b32_sdwa v226, 1, v190 dst_sel:DWORD dst_unused:UNUSED_PAD src0_sel:DWORD src1_sel:WORD_1\n\t" \
  "ds_read_b32 v222, v222 offset:10016\n\t" \
  "ds_read_b64_tr_b16 v[212:213], v248 offset:39976\n\t" \
  "v_mfma_f32_16x16x32_bf16 v[20:23], v[216:219], v[146:149], v[20:23]\n\t" \
  "v_or_b32 v203, v232, v233\n\t" \
  "v_or_b32 v202, v234, v235\n\t" \
  "ds_read_u16_d16_hi v225, v225 offset:10528\n\t" \
  "v_mfma_f32_16x16x32_bf16 v[68:71], v[216:219], v[162:165], v[68:71]\n\t" \
  "v_fma_f32 v192, |v192|, v230, v192 mul:4\n\t" \
  "v_fma_f32 v193, |v193|, v231, v193 mul:4\n\t" \
  "ds_read_b64_tr_b16 v[214:215], v248 offset:41000\n\t" \
  "v_mfma_f32_16x16x32_bf16 v[44:47], v[216:219], v[154:157], v[44:47]\n\t" \
  "v_fma_f32 v194, |v194|, v228, v194 mul:4\n\t" \
  "v_fma_f32 v195, |v195|, v229, v195 mul:4\n\t" \
  "s_setprio 2\n\t" \
  "ds_read_u16 v224, v224 offset:10528\n\t" \
  "v_mfma_f32_16x16x32_bf16 v[92:95], v[216:219], v[170:173], v[92:95]\n\t" \
  "v_cvt_pk_bf16_f32 v195, v194, v195\n\t" \
  "v_cvt_pk_bf16_f32 v194, v192, v193\n\t" \
  "s_waitcnt lgkmcnt(5)\n\t" \
  "ds_read_b64_tr_b16 v[216:217], v248 offset:38952\n\t" \
  "v_mfma_f32_16x16x32_bf16 v[144:147], v[208:211], v[96:99], 0\n\t" \
  "v_cvt_pknorm_i16_f32 v230, |v198|, |v199|\n\t" \
  "v_cvt_pknorm_i16_f32 v228, |v196|, |v197|\n\t" \
  "ds_read_b64_tr_b16 v[218:219], v248 offset:42024\n\t" \
  "v_mfma_f32_16x16x32_bf16 v[152:155], v[208:211], v[100:103], 0\n\t" \
  "v_lshlrev_b32_sdwa v229, 2, v228 dst_sel:DWORD dst_unused:UNUSED_PAD src0_sel:DWORD src1_sel:BYTE_3\n\t" \
  "v_lshlrev_b32_sdwa v228, 2, v228 dst_sel:DWORD dst_unused:UNUSED_PAD src0_sel:DWORD src1_sel:BYTE_1\n\t" \
  "ds_read_u16_d16_hi v227, v227 offset:10528\n\t" \
  "v_mfma_f32_16x16x32_bf16 v[160:163], v[208:211], v[104:107], 0\n\t" \
  "s_cmp_eq_u32 s38, 4\n\t" \
  "v_lshlrev_b32_sdwa v231, 2, v230 dst_sel:DWORD dst_unused:UNUSED_PAD src0_sel:DWORD src1_sel:BYTE_3\n\t" \
  "s_cbranch_scc1 .Lcnx_t0\n\t" \
  ".Lcnx_u0:\n\t" \
  "ds_read_u16 v226, v226 offset:10528\n\t" \
  "v_mfma_f32_16x16x32_bf16 v[168:171], v[208:211], v[108:111], 0\n\t" \
  "v_lshlrev_b32_sdwa v230, 2, v230 dst_sel:DWORD dst_unused:UNUSED_PAD src0_sel:DWORD src1_sel:BYTE_1\n\t" \
  "v_lshlrev_b32_sdwa v233, 1, v205 dst_sel:DWORD dst_unused:UNUSED_PAD src0_sel:DWORD src1_sel:WORD_1\n\t" \
  "s_waitcnt lgkmcnt(5)\n\t" \
  "ds_read_b64_tr_b16 v[208:209], v248 offset:37416\n\t" \
  "v_mfma_f32_16x16x32_bf16 v[144:147], v[212:215], v[112:115], v[144:147]\n\t" \
  "v_lshlrev_b32_sdwa v232, 1, v204 dst_sel:DWORD dst_unused:UNUSED_PAD src0_sel:DWORD src1_sel:WORD_1\n\t" \
  "v_lshlrev_b32_sdwa v235, 1, v207 dst_sel:DWORD dst_unused:UNUSED_PAD src0_sel:DWORD src1_sel:WORD_1\n\t" \
  "ds_read_b64_tr_b16 v[210:211], v248 offset:38440\n\t" \
  "v_mfma_f32_16x16x32_bf16 v[152:155], v[212:215], v[116:119], v[152:155]\n\t" \
  "v_lshlrev_b32_sdwa v234, 1, v206 dst_sel:DWORD dst_unused:UNUSED_PAD src0_sel:DWORD src1_sel:WORD_1\n\t" \
  "v_fma_mix_f32 v221, |v181|, v221, v221 op_sel:[0,1,0] op_sel_hi:[0,1,1] clamp\n\t" \
  "ds_read_b32 v228, v228 offset:10016\n\t" \
  "ds_read_b32 v229, v229 offset:10016\n\t" \
  "v_mfma_f32_16x16x32_bf16 v[160:163], v[212:215], v[120:123], v[160:163]\n\t" \
  "s_setprio 1\n\t" \
  "v_fma_mix_f32 v223, |v183|, v223, v223 op_sel:[0,1,0] op_sel_hi:[0,1,1] clamp\n\t" \
  "v_fma_mix_f32 v220, |v180|, v220, v220 op_sel:[0,1,0] op_sel_hi:[0,1,1] clamp\n\t" \
  "ds_read_b32 v231, v231 offset:10016\n\t" \
  "v_mfma_f32_16x16x32_bf16 v[168:171], v[212:215], v[124:127], v[168:171]\n\t" \
  "v_fma_mix_f32 v222, |v182|, v222, v222 op_sel:[0,1,0] op_sel_hi:[0,1,1] clamp\n\t" \
  "s_waitcnt lgkmcnt(7)\n\t" \
  "ds_read_b64_tr_b16 v[212:213], v248 offset:40488\n\t" \
  "ds_read_u16_d16_hi v233, v233 offset:10528\n\t" \
  "v_mfma_f32_16x16x32_bf16 v[144:147], v[216:219], v[128:131], v[144:147]\n\t" \
  "s_waitcnt lgkmcnt(7)\n\t" \
  "v_or_b32 v188, v224, v225\n\t" \
  "v_or_b32 v189, v226, v227\n\t" \
  "ds_read_b64_tr_b16 v[214:215], v248 offset:41512\n\t" \
  "v_mfma_f32_16x16x32_bf16 v[160:163], v[216:219], v[136:139], v[160:163]\n\t" \
  "v_fma_f32 v182, |v182|, v222, v182 mul:4\n\t" \
  "v_fma_f32 v183, |v183|, v223, v183 mul:4\n\t" \
  "s_setprio 0\n\t" \
  "ds_read_b32 v230, v230 offset:10016\n\t" \
  "v_mfma_f32_16x16x32_bf16 v[152:155], v[216:219], v[132:135], v[152:155]\n\t" \
  "v_fma_f32 v181, |v181|, v221, v181 mul:4\n\t" \
  "v_fma_f32 v180, |v180|, v220, v180 mul:4\n\t" \
  "ds_read_u16 v232, v232 offset:10528\n\t" \
  "ds_read_u16_d16_hi v235, v235 offset:10528\n\t" \
  "v_mfma_f32_16x16x32_bf16 v[168:171], v[216:219], v[140:143], v[168:171]\n\t" \
  "v_cvt_pk_bf16_f32 v180, v180, v181\n\t" \
  "v_cvt_pk_bf16_f32 v181, v182, v183\n\t" \
  "s_waitcnt lgkmcnt(9)\n\t" \
  "ds_read_b64_tr_b16 v[216:217], v248 offset:39464\n\t" \
  "v_mfma_f32_16x16x32_bf16 v[148:151], v[208:211], v[96:99], 0\n\t" \
  "v_cvt_pknorm_i16_f32 v222, |v144|, |v145|\n\t" \
  "v_cvt_pknorm_i16_f32 v220, |v146|, |v147|\n\t" \
  "s_waitcnt vmcnt(2)\n\t" \
  "ds_read_b64_tr_b16 v[218:219], v248 offset:42536\n\t" \
  "v_mfma_f32_16x16x32_bf16 v[156:159], v[208:211], v[100:103], 0\n\t" \
  "v_lshlrev_b32_sdwa v221, 2, v220 dst_sel:DWORD dst_unused:UNUSED_PAD src0_sel:DWORD src1_sel:BYTE_3\n\t" \
  "v_lshlrev_b32_sdwa v220, 2, v220 dst_sel:DWORD dst_unused:UNUSED_PAD src0_sel:DWORD src1_sel:BYTE_1\n\t" \
  "s_mov_b64 exec, 3\n\t" \
  "ds_add_rtn_u32 v246, v251, v250 offset:47216\n\t" \
  "v_mfma_f32_16x16x32_bf16 v[164:167], v[208:211], v[104:107], 0\n\t" \
  "s_mov_b64 exec, -1\n\t" \
  "v_lshlrev_b32_sdwa v223, 2, v222 dst_sel:DWORD dst_unused:UNUSED_PAD src0_sel:DWORD src1_sel:BYTE_3\n\t" \
  "v_lshlrev_b32_sdwa v222, 2, v222 dst_sel:DWORD dst_unused:UNUSED_PAD src0_sel:DWORD src1_sel:BYTE_1\n\t" \
  "ds_read_b32 v220, v220 offset:10016\n\t" \
  "s_waitcnt lgkmcnt(7)\n\t" \
  "v_mfma_f32_16x16x32_bf16 v[156:159], v[212:215], v[116:119], v[156:159]\n\t" \
  "v_lshlrev_b32_sdwa v227, 1, v153 dst_sel:DWORD dst_unused:UNUSED_PAD src0_sel:DWORD src1_sel:WORD_1\n\t" \
  "v_lshlrev_b32_sdwa v226, 1, v152 dst_sel:DWORD dst_unused:UNUSED_PAD src0_sel:DWORD src1_sel:WORD_1\n\t" \
  "ds_read_u16 v234, v234 offset:10528\n\t" \
  "s_waitcnt lgkmcnt(2)\n\t" \
  "v_mfma_f32_16x16x32_bf16 v[172:175], v[208:211], v[108:111], 0\n\t" \
  "v_readfirstlane_b32 s4, v246\n\t" \
  "v_lshlrev_b32_sdwa v225, 1, v155 dst_sel:DWORD dst_unused:UNUSED_PAD src0_sel:DWORD src1_sel:WORD_1\n\t" \
  "ds_read_b32 v221, v221 offset:10016\n\t" \
  "v_mfma_f32_16x16x32_bf16 v[148:151], v[212:215], v[112:115], v[148:151]\n\t" \
  "s_cmpk_lt_u32 s4, 3072\n\t" \
  "v_lshlrev_b32_sdwa v224, 1, v154 dst_sel:DWORD dst_unused:UNUSED_PAD src0_sel:DWORD src1_sel:WORD_1\n\t" \
  "s_cbranch_scc1 .Lcnx_w0_11\n\t" \
  ".Lcnx_g0_11:\n\t" \
  "ds_read_b64_tr_b16 v[208:209], v249 offset:30752\n\t" \
  "v_mfma_f32_16x16x32_bf16 v[164:167], v[212:215], v[120:123], v[164:167]\n\t" \
  "v_fma_mix_f32 v229, |v197|, v229, v229 op_sel:[0,1,0] op_sel_hi:[0,1,1] clamp\n\t" \
  "v_fma_mix_f32 v228, |v196|, v228, v228 op_sel:[0,1,0] op_sel_hi:[0,1,1] clamp\n\t" \
  "ds_read_b64_tr_b16 v[210:211], v249 offset:32288\n\t" \
  "ds_read_b32 v223, v223 offset:10016\n\t" \
  "v_mfma_f32_16x16x32_bf16 v[172:175], v[212:215], v[124:127], v[172:175]\n\t" \
  "v_fma_mix_f32 v231, |v199|, v231, v231 op_sel:[0,1,0] op_sel_hi:[0,1,1] clamp\n\t" \
  "v_fma_mix_f32 v230, |v198|, v230, v230 op_sel:[0,1,0] op_sel_hi:[0,1,1] clamp\n\t" \
  "s_waitcnt lgkmcnt(4)\n\t" \
  "ds_read_b32 v222, v222 offset:10016\n\t" \
  "ds_read_b64_tr_b16 v[212:213], v249 offset:30760\n\t" \
  "v_mfma_f32_16x16x32_bf16 v[148:151], v[216:219], v[128:131], v[148:151]\n\t" \
  "s_mov_b32 m0, s47\n\t" \
  "v_or_b32 v204, v232, v233\n\t" \
  "v_or_b32 v205, v234, v235\n\t" \
  "s_mov_b32 exec_hi, 0xffff\n\t" \
  "buffer_load_dwordx4 v252, s[16:19], s33 offen lds\n\t" \
  "v_mfma_f32_16x16x32_bf16 v[156:159], v[216:219], v[132:135], v[156:159]\n\t" \
  "s_mov_b32 exec_hi, -1\n\t" \
  "v_fma_f32 v197, |v197|, v229, v197 mul:4\n\t" \
  "v_fma_f32 v196, |v196|, v228, v196 mul:4\n\t" \
  "s_add_u32 s33, s33, s49\n\t" \
  "ds_read_u16_d16_hi v225, v225 offset:10528\n\t" \
  "ds_read_b64_tr_b16 v[214:215], v249 offset:32296\n\t" \
  "v_mfma_f32_16x16x32_bf16 v[164:167], v[216:219], v[136:139], v[164:167]\n\t" \
  "v_fma_f32 v199, |v199|, v231, v199 mul:4\n\t" \
  "v_fma_f32 v198, |v198|, v230, v198 mul:4\n\t" \
  "ds_read_u16 v226, v226 offset:10528\n\t" \
  "v_mfma_f32_16x16x32_bf16 v[172:175], v[216:219], v[140:143], v[172:175]\n\t" \
  "v_cvt_pk_bf16_f32 v196, v196, v197\n\t" \
  "v_cvt_pk_bf16_f32 v197, v198, v199\n\t" \
  "ds_read_b64_tr_b16 v[216:217], v249 offset:30816\n\t" \
  "s_waitcnt lgkmcnt(7)\n\t" \
  "v_mfma_f32_16x16x32_bf16 v[0:3], v[208:211], v[178:181], v[0:3]\n\t" \
  "v_cvt_pknorm_i16_f32 v230, |v160|, |v161|\n\t" \
  "v_cvt_pknorm_i16_f32 v228, |v162|, |v163|\n\t" \
  "ds_read_b64_tr_b16 v[218:219], v249 offset:32352\n\t" \
  "v_mfma_f32_16x16x32_bf16 v[24:27], v[208:211], v[186:189], v[24:27]\n\t" \
  "v_lshlrev_b32_sdwa v229, 2, v228 dst_sel:DWORD dst_unused:UNUSED_PAD src0_sel:DWORD src1_sel:BYTE_3\n\t" \
  "v_lshlrev_b32_sdwa v228, 2, v228 dst_sel:DWORD dst_unused:UNUSED_PAD src0_sel:DWORD src1_sel:BYTE_1\n\t" \
  "ds_read_u16 v224, v224 offset:10528\n\t" \
  "v_mfma_f32_16x16x32_bf16 v[48:51], v[208:211], v[194:197], v[48:51]\n\t" \
  "v_lshlrev_b32_sdwa v231, 2, v230 dst_sel:DWORD dst_unused:UNUSED_PAD src0_sel:DWORD src1_sel:BYTE_3\n\t" \
  "v_lshlrev_b32_sdwa v230, 2, v230 dst_sel:DWORD dst_unused:UNUSED_PAD src0_sel:DWORD src1_sel:BYTE_1\n\t" \
  "ds_read_u16_d16_hi v227, v227 offset:10528\n\t" \
  "ds_read_b32 v228, v228 offset:10016\n\t" \
  "v_mfma_f32_16x16x32_bf16 v[72:75], v[208:211], v[202:205], v[72:75]\n\t" \
  "s_add_u32 m0, s47, s35\n\t" \
  "v_lshlrev_b32_sdwa v232, 1, v170 dst_sel:DWORD dst_unused:UNUSED_PAD src0_sel:DWORD src1_sel:WORD_1\n\t" \
  "s_waitcnt lgkmcnt(6)\n\t" \
  "s_mov_b32 exec_hi, 0xffff\n\t" \
  "buffer_load_dwordx4 v252, s[16:19], s34 offen lds\n\t" \
  "v_mfma_f32_16x16x32_bf16 v[4:7], v[212:215], v[178:181], v[4:7]\n\t" \
  "s_mov_b32 exec_hi, -1\n\t" \
  "v_lshlrev_b32_sdwa v233, 1, v171 dst_sel:DWORD dst_unused:UNUSED_PAD src0_sel:DWORD src1_sel:WORD_1\n\t" \
  "v_lshlrev_b32_sdwa v235, 1, v169 dst_sel:DWORD dst_unused:UNUSED_PAD src0_sel:DWORD src1_sel:WORD_1\n\t" \
  "s_add_u32 s34, s34, s49\n\t" \
  "ds_read_b64_tr_b16 v[208:209], v249 offset:30824\n\t" \
  "v_mfma_f32_16x16x32_bf16 v[28:31], v[212:215], v[186:189], v[28:31]\n\t" \
  "v_fma_mix_f32 v223, |v145|, v223, v223 op_sel:[0,1,0] op_sel_hi:[0,1,1] clamp\n\t" \
  "v_lshlrev_b32_sdwa v234, 1, v168 dst_sel:DWORD dst_unused:UNUSED_PAD src0_sel:DWORD src1_sel:WORD_1\n\t" \
  "ds_read_b32 v229, v229 offset:10016\n\t" \
  "v_mfma_f32_16x16x32_bf16 v[52:55], v[212:215], v[194:197], v[52:55]\n\t" \
  "v_fma_mix_f32 v220, |v146|, v220, v220 op_sel:[0,1,0] op_sel_hi:[0,1,1] clamp\n\t" \
  "v_fma_mix_f32 v221, |v147|, v221, v221 op_sel:[0,1,0] op_sel_hi:[0,1,1] clamp\n\t" \
  "ds_read_b32 v231, v231 offset:10016\n\t" \
  "ds_read_b64_tr_b16 v[210:211], v249 offset:32360\n\t" \
  "v_mfma_f32_16x16x32_bf16 v[76:79], v[212:215], v[202:205], v[76:79]\n\t" \
  "v_fma_mix_f32 v222, |v144|, v222, v222 op_sel:[0,1,0] op_sel_hi:[0,1,1] clamp\n\t" \
  "s_waitcnt lgkmcnt(5)\n\t" \
  "ds_read_b64_tr_b16 v[212:213], v249 offset:30880\n\t" \
  "ds_read_b32 v230, v230 offset:10016\n\t" \
  "v_mfma_f32_16x16x32_bf16 v[8:11], v[216:219], v[178:181], v[8:11]\n\t" \
  "v_or_b32 v155, v224, v225\n\t" \
  "v_or_b32 v154, v226, v227\n\t" \
  "ds_read_b64_tr_b16 v[214:215], v249 offset:32416\n\t" \
  "v_mfma_f32_16x16x32_bf16 v[32:35], v[216:219], v[186:189], v[32:35]\n\t" \
  "v_fma_f32 v147, |v147|, v221, v147 mul:4\n\t" \
  "v_fma_f32 v146, |v146|, v220, v146 mul:4\n\t" \
  "ds_read_u16_d16_hi v233, v233 offset:10528\n\t" \
  "v_mfma_f32_16x16x32_bf16 v[56:59], v[216:219], v[194:197], v[56:59]\n\t" \
  "v_fma_f32 v145, |v145|, v223, v145 mul:4\n\t" \
  "v_fma_f32 v144, |v144|, v222, v144 mul:4\n\t" \
  "ds_read_u16 v232, v232 offset:10528\n\t" \
  "v_mfma_f32_16x16x32_bf16 v[80:83], v[216:219], v[202:205], v[80:83]\n\t" \
  "v_cvt_pk_bf16_f32 v147, v146, v147\n\t" \
  "v_cvt_pk_bf16_f32 v146, v144, v145\n\t" \
  "ds_read_b64_tr_b16 v[216:217], v249 offset:30888\n\t" \
  "s_waitcnt lgkmcnt(6)\n\t" \
  "v_mfma_f32_16x16x32_bf16 v[12:15], v[208:211], v[178:181], v[12:15]\n\t" \
  "v_cvt_pknorm_i16_f32 v220, |v148|, |v149|\n\t" \
  "v_cvt_pknorm_i16_f32 v222, |v150|, |v151|\n\t" \
  "ds_read_b64_tr_b16 v[218:219], v249 offset:32424\n\t" \
  "v_mfma_f32_16x16x32_bf16 v[36:39], v[208:211], v[186:189], v[36:39]\n\t" \
  "v_lshlrev_b32_sdwa v221, 2, v220 dst_sel:DWORD dst_unused:UNUSED_PAD src0_sel:DWORD src1_sel:BYTE_3\n\t" \
  "v_lshlrev_b32_sdwa v220, 2, v220 dst_sel:DWORD dst_unused:UNUSED_PAD src0_sel:DWORD src1_sel:BYTE_1\n\t" \
  "ds_read_u16_d16_hi v235, v235 offset:10528\n\t" \
  "s_waitcnt lgkmcnt(5)\n\t" \
  "v_mfma_f32_16x16x32_bf16 v[60:63], v[208:211], v[194:197], v[60:63]\n\t" \
  "v_lshlrev_b32_sdwa v223, 2, v222 dst_sel:DWORD dst_unused:UNUSED_PAD src0_sel:DWORD src1_sel:BYTE_3\n\t" \
  "v_lshlrev_b32_sdwa v222, 2, v222 dst_sel:DWORD dst_unused:UNUSED_PAD src0_sel:DWORD src1_sel:BYTE_1\n\t" \
  "ds_read_u16 v234, v234 offset:10528\n\t" \
  "v_mfma_f32_16x16x32_bf16 v[84:87], v[208:211], v[202:205], v[84:87]\n\t" \
  "v_lshlrev_b32_sdwa v225, 1, v157 dst_sel:DWORD dst_unused:UNUSED_PAD src0_sel:DWORD src1_sel:WORD_1\n\t" \
  "v_lshlrev_b32_sdwa v224, 1, v156 dst_sel:DWORD dst_unused:UNUSED_PAD src0_sel:DWORD src1_sel:WORD_1\n\t" \
  "ds_read_b64_tr_b16 v[208:209], v248 offset:0\n\t" \
  "v_mfma_f32_16x16x32_bf16 v[16:19], v[212:215], v[178:181], v[16:19]\n\t" \
  "v_lshlrev_b32_sdwa v227, 1, v159 dst_sel:DWORD dst_unused:UNUSED_PAD src0_sel:DWORD src1_sel:WORD_1\n\t" \
  "v_lshlrev_b32_sdwa v226, 1, v158 dst_sel:DWORD dst_unused:UNUSED_PAD src0_sel:DWORD src1_sel:WORD_1\n\t" \
  "ds_read_b64_tr_b16 v[210:211], v248 offset:1024\n\t" \
  "ds_read_b32 v221, v221 offset:10016\n\t" \
  "v_mfma_f32_16x16x32_bf16 v[40:43], v[212:215], v[186:189], v[40:43]\n\t" \
  "v_fma_mix_f32 v229, |v163|, v229, v229 op_sel:[0,1,0] op_sel_hi:[0,1,1] clamp\n\t" \
  "s_add_u32 s27, s27, s26\n\t" \
  "s_mov_b64 exec, s[30:31]\n\t" \
  "buffer_load_dword v237, v254, s[8:11], s27 offen\n\t" \
  "v_mfma_f32_16x16x32_bf16 v[64:67], v[212:215], v[194:197], v[64:67]\n\t" \
  "s_mov_b64 exec, -1\n\t" \
  "v_fma_mix_f32 v230, |v160|, v230, v230 op_sel:[0,1,0] op_sel_hi:[0,1,1] clamp\n\t" \
  "v_fma_mix_f32 v228, |v162|, v228, v228 op_sel:[0,1,0] op_sel_hi:[0,1,1] clamp\n\t" \
  "ds_read_b32 v220, v220 offset:10016\n\t" \
  "ds_read_b32 v223, v223 offset:10016\n\t" \
  "v_mfma_f32_16x16x32_bf16 v[88:91], v[212:215], v[202:205], v[88:91]\n\t" \
  "v_fma_mix_f32 v231, |v161|, v231, v231 op_sel:[0,1,0] op_sel_hi:[0,1,1] clamp\n\t" \
  "s_waitcnt lgkmcnt(5)\n\t" \
  "ds_read_b64_tr_b16 v[212:213], v248 offset:3072\n\t" \
  "ds_read_b32 v222, v222 offset:10016\n\t" \
  "v_mfma_f32_16x16x32_bf16 v[20:23], v[216:219], v[178:181], v[20:23]\n\t" \
  "v_or_b32 v171, v232, v233\n\t" \
  "v_or_b32 v170, v234, v235\n\t" \
  "ds_read_b64_tr_b16 v[214:215], v248 offset:4096\n\t" \
  "v_mfma_f32_16x16x32_bf16 v[44:47], v[216:219], v[186:189], v[44:47]\n\t" \
  "v_fma_f32 v163, |v163|, v229, v163 mul:4\n\t" \
  "v_fma_f32 v162, |v162|, v228, v162 mul:4\n\t" \
  "ds_read_u16_d16_hi v225, v225 offset:10528\n\t" \
  "v_mfma_f32_16x16x32_bf16 v[68:71], v[216:219], v[194:197], v[68:71]\n\t" \
  "v_fma_f32 v161, |v161|, v231, v161 mul:4\n\t" \
  "v_fma_f32 v160, |v160|, v230, v160 mul:4\n\t" \
  "ds_read_u16 v224, v224 offset:10528\n\t" \
  "v_mfma_f32_16x16x32_bf16 v[92:95], v[216:219], v[202:205], v[92:95]\n\t" \
  "v_cvt_pk_bf16_f32 v163, v162, v163\n\t" \
  "v_cvt_pk_bf16_f32 v162, v160, v161\n\t" \
  "ds_read_b64_tr_b16 v[216:217], v248 offset:2048\n\t" \
  "s_waitcnt lgkmcnt(9)\n\t" \
  "v_mfma_f32_16x16x32_bf16 v[176:179], v[208:211], v[96:99], 0\n\t" \
  "v_cvt_pknorm_i16_f32 v228, |v164|, |v165|\n\t" \
  "v_cvt_pknorm_i16_f32 v230, |v166|, |v167|\n\t" \
  "ds_read_b64_tr_b16 v[218:219], v248 offset:5120\n\t" \
  "v_mfma_f32_16x16x32_bf16 v[184:187], v[208:211], v[100:103], 0\n\t" \
  "v_lshlrev_b32_sdwa v229, 2, v228 dst_sel:DWORD dst_unused:UNUSED_PAD src0_sel:DWORD src1_sel:BYTE_3\n\t" \
  "v_lshlrev_b32_sdwa v228, 2, v228 dst_sel:DWORD dst_unused:UNUSED_PAD src0_sel:DWORD src1_sel:BYTE_1\n\t" \
  "ds_read_u16_d16_hi v227, v227 offset:10528\n\t" \
  "v_mfma_f32_16x16x32_bf16 v[192:195], v[208:211], v[104:107], 0\n\t" \
  "v_lshlrev_b32_sdwa v231, 2, v230 dst_sel:DWORD dst_unused:UNUSED_PAD src0_sel:DWORD src1_sel:BYTE_3\n\t" \
  "v_lshlrev_b32_sdwa v230, 2, v230 dst_sel:DWORD dst_unused:UNUSED_PAD src0_sel:DWORD src1_sel:BYTE_1\n\t" \
  "ds_read_u16 v226, v226 offset:10528\n\t" \
  "v_mfma_f32_16x16x32_bf16 v[200:203], v[208:211], v[108:111], 0\n\t" \
  "v_lshlrev_b32_sdwa v233, 1, v173 dst_sel:DWORD dst_unused:UNUSED_PAD src0_sel:DWORD src1_sel:WORD_1\n\t" \
  "v_lshlrev_b32_sdwa v232, 1, v172 dst_sel:DWORD dst_unused:UNUSED_PAD src0_sel:DWORD src1_sel:WORD_1\n\t" \
  "ds_read_b64_tr_b16 v[208:209], v248 offset:512\n\t" \
  "s_waitcnt lgkmcnt(7)\n\t" \
  "v_mfma_f32_16x16x32_bf16 v[176:179], v[212:215], v[112:115], v[176:179]\n\t" \
  "v_lshlrev_b32_sdwa v235, 1, v175 dst_sel:DWORD dst_unused:UNUSED_PAD src0_sel:DWORD src1_sel:WORD_1\n\t" \
  "v_fma_mix_f32 v221, |v149|, v221, v221 op_sel:[0,1,0] op_sel_hi:[0,1,1] clamp\n\t" \
  "ds_read_b32 v228, v228 offset:10016\n\t" \
  "v_mfma_f32_16x16x32_bf16 v[192:195], v[212:215], v[120:123], v[192:195]\n\t" \
  "v_fma_mix_f32 v220, |v148|, v220, v220 op_sel:[0,1,0] op_sel_hi:[0,1,1] clamp\n\t" \
  "v_fma_mix_f32 v222, |v150|, v222, v222 op_sel:[0,1,0] op_sel_hi:[0,1,1] clamp\n\t" \
  "ds_read_b32 v230, v230 offset:10016\n\t" \
  "v_mfma_f32_16x16x32_bf16 v[200:203], v[212:215], v[124:127], v[200:203]\n\t" \
  "v_fma_mix_f32 v223, |v151|, v223, v223 op_sel:[0,1,0] op_sel_hi:[0,1,1] clamp\n\t" \
  "ds_read_b64_tr_b16 v[210:211], v248 offset:1536\n\t" \
  "ds_read_b32 v229, v229 offset:10016\n\t" \
  "v_mfma_f32_16x16x32_bf16 v[184:187], v[212:215], v[116:119], v[184:187]\n\t" \
  "s_waitcnt lgkmcnt(5)\n\t" \
  "v_lshlrev_b32_sdwa v234, 1, v174 dst_sel:DWORD dst_unused:UNUSED_PAD src0_sel:DWORD src1_sel:WORD_1\n\t" \
  "ds_read_b64_tr_b16 v[212:213], v248 offset:3584\n\t" \
  "ds_read_u16_d16_hi v233, v233 offset:10528\n\t" \
  "v_mfma_f32_16x16x32_bf16 v[176:179], v[216:219], v[128:131], v[176:179]\n\t" \
  "v_or_b32 v157, v226, v227\n\t" \
  "v_or_b32 v156, v224, v225\n\t" \
  "ds_read_b32 v231, v231 offset:10016\n\t" \
  "ds_read_b64_tr_b16 v[214:215], v248 offset:4608\n\t" \
  "v_mfma_f32_16x16x32_bf16 v[184:187], v[216:219], v[132:135], v[184:187]\n\t" \
  "v_fma_f32 v149, |v149|, v221, v149 mul:4\n\t" \
  "v_fma_f32 v148, |v148|, v220, v148 mul:4\n\t" \
  "ds_read_u16 v232, v232 offset:10528\n\t" \
  "v_mfma_f32_16x16x32_bf16 v[192:195], v[216:219], v[136:139], v[192:195]\n\t" \
  "v_fma_f32 v151, |v151|, v223, v151 mul:4\n\t" \
  "v_fma_f32 v150, |v150|, v222, v150 mul:4\n\t" \
  "v_mfma_f32_16x16x32_bf16 v[200:203], v[216:219], v[140:143], v[200:203]\n\t" \
  "v_cvt_pk_bf16_f32 v148, v148, v149\n\t" \
  "v_cvt_pk_bf16_f32 v149, v150, v151\n\t" \
  "ds_read_b64_tr_b16 v[216:217], v248 offset:2560\n\t" \
  "s_waitcnt lgkmcnt(7)\n\t" \
  "v_mfma_f32_16x16x32_bf16 v[180:183], v[208:211], v[96:99], 0\n\t" \
  "v_cvt_pknorm_i16_f32 v220, |v178|, |v179|\n\t" \
  "v_cvt_pknorm_i16_f32 v222, |v176|, |v177|\n\t" \
  "s_waitcnt vmcnt(1)\n\t" \
  "ds_read_b64_tr_b16 v[218:219], v248 offset:5632\n\t" \
  "v_mfma_f32_16x16x32_bf16 v[188:191], v[208:211], v[100:103], 0\n\t" \
  "v_lshlrev_b32_sdwa v221, 2, v220 dst_sel:DWORD dst_unused:UNUSED_PAD src0_sel:DWORD src1_sel:BYTE_3\n\t" \
  "v_lshlrev_b32_sdwa v220, 2, v220 dst_sel:DWORD dst_unused:UNUSED_PAD src0_sel:DWORD src1_sel:BYTE_1\n\t" \
  "s_mov_b64 exec, 3\n\t" \
  "ds_add_rtn_u32 v246, v251, v250 offset:47224\n\t" \
  "v_mfma_f32_16x16x32_bf16 v[196:199], v[208:211], v[104:107], 0\n\t" \
  "s_mov_b64 exec, -1\n\t" \
  "v_lshlrev_b32_sdwa v223, 2, v222 dst_sel:DWORD dst_unused:UNUSED_PAD src0_sel:DWORD src1_sel:BYTE_3\n\t" \
  "v_lshlrev_b32_sdwa v222, 2, v222 dst_sel:DWORD dst_unused:UNUSED_PAD src0_sel:DWORD src1_sel:BYTE_1\n\t" \
  "ds_read_u16 v234, v234 offset:10528\n\t" \
  "ds_read_u16_d16_hi v235, v235 offset:10528\n\t" \
  "s_waitcnt lgkmcnt(2)\n\t" \
  "v_mfma_f32_16x16x32_bf16 v[204:207], v[208:211], v[108:111], 0\n\t" \
  "v_lshlrev_b32_sdwa v225, 1, v187 dst_sel:DWORD dst_unused:UNUSED_PAD src0_sel:DWORD src1_sel:WORD_1\n\t" \
  "v_readfirstlane_b32 s4, v246\n\t" \
  "ds_read_b32 v221, v221 offset:10016\n\t" \
  "v_mfma_f32_16x16x32_bf16 v[180:183], v[212:215], v[112:115], v[180:183]\n\t" \
  "s_cmpk_lt_u32 s4, 3072\n\t" \
  "v_lshlrev_b32_sdwa v224, 1, v186 dst_sel:DWORD dst_unused:UNUSED_PAD src0_sel:DWORD src1_sel:WORD_1\n\t" \
  "s_cbranch_scc1 .Lcnx_w0_12\n\t" \
  ".Lcnx_g0_12:\n\t" \
  "ds_read_b32 v220, v220 offset:10016\n\t" \
  "v_mfma_f32_16x16x32_bf16 v[188:191], v[212:215], v[116:119], v[188:191]\n\t" \
  "v_lshlrev_b32_sdwa v227, 1, v185 dst_sel:DWORD dst_unused:UNUSED_PAD src0_sel:DWORD src1_sel:WORD_1\n\t" \
  "v_lshlrev_b32_sdwa v226, 1, v184 dst_sel:DWORD dst_unused:UNUSED_PAD src0_sel:DWORD src1_sel:WORD_1\n\t" \
  "ds_read_b64_tr_b16 v[208:209], v249 offset:6152\n\t" \
  "v_mfma_f32_16x16x32_bf16 v[196:199], v[212:215], v[120:123], v[196:199]\n\t" \
  "v_fma_mix_f32 v228, |v164|, v228, v228 op_sel:[0,1,0] op_sel_hi:[0,1,1] clamp\n\t" \
  "v_fma_mix_f32 v229, |v165|, v229, v229 op_sel:[0,1,0] op_sel_hi:[0,1,1] clamp\n\t" \
  "ds_read_b64_tr_b16 v[210:211], v249 offset:7688\n\t" \
  "ds_read_b32 v223, v223 offset:10016\n\t" \
  "v_mfma_f32_16x16x32_bf16 v[204:207], v[212:215], v[124:127], v[204:207]\n\t" \
  "v_fma_mix_f32 v231, |v167|, v231, v231 op_sel:[0,1,0] op_sel_hi:[0,1,1] clamp\n\t" \
  "v_fma_mix_f32 v230, |v166|, v230, v230 op_sel:[0,1,0] op_sel_hi:[0,1,1] clamp\n\t" \
  "s_waitcnt lgkmcnt(5)\n\t" \
  "ds_read_b32 v222, v222 offset:10016\n\t" \
  "ds_read_b64_tr_b16 v[212:213], v249 offset:6160\n\t" \
  "v_mfma_f32_16x16x32_bf16 v[180:183], v[216:219], v[128:131], v[180:183]\n\t" \
  "v_or_b32 v172, v232, v233\n\t" \
  "v_or_b32 v173, v234, v235\n\t" \
  "ds_read_b64_tr_b16 v[214:215], v249 offset:7696\n\t" \
  "ds_read_u16 v224, v224 offset:10528\n\t" \
  "v_mfma_f32_16x16x32_bf16 v[188:191], v[216:219], v[132:135], v[188:191]\n\t" \
  "v_fma_f32 v165, |v165|, v229, v165 mul:4\n\t" \
  "v_fma_f32 v164, |v164|, v228, v164 mul:4\n\t" \
  "ds_read_u16_d16_hi v225, v225 offset:10528\n\t" \
  "v_mfma_f32_16x16x32_bf16 v[196:199], v[216:219], v[136:139], v[196:199]\n\t" \
  "v_fma_f32 v167, |v167|, v231, v167 mul:4\n\t" \
  "v_fma_f32 v166, |v166|, v230, v166 mul:4\n\t" \
  "v_mfma_f32_16x16x32_bf16 v[204:207], v[216:219], v[140:143], v[204:207]\n\t" \
  "v_cvt_pk_bf16_f32 v164, v164, v165\n\t" \
  "v_cvt_pk_bf16_f32 v165, v166, v167\n\t" \
  "ds_read_b64_tr_b16 v[216:217], v249 offset:6216\n\t" \
  "s_waitcnt lgkmcnt(7)\n\t" \
  "v_mfma_f32_16x16x32_bf16 v[0:3], v[208:211], v[146:149], v[0:3]\n\t" \
  "ds_read_b64_tr_b16 v[218:219], v249 offset:7752\n\t" \
  "v_cvt_pknorm_i16_f32 v228, |v194|, |v195|\n\t" \
  "v_cvt_pknorm_i16_f32 v230, |v192|, |v193|\n\t" \
  "v_mfma_f32_16x16x32_bf16 v[24:27], v[208:211], v[154:157], v[24:27]\n\t" \
  "ds_read_u16_d16_hi v227, v227 offset:10528\n\t" \
  "v_lshlrev_b32_sdwa v233, 1, v203 dst_sel:DWORD dst_unused:UNUSED_PAD src0_sel:DWORD src1_sel:WORD_1\n\t" \
  "v_lshlrev_b32_sdwa v231, 2, v230 dst_sel:DWORD dst_unused:UNUSED_PAD src0_sel:DWORD src1_sel:BYTE_3\n\t" \
  "v_lshlrev_b32_sdwa v232, 1, v202 dst_sel:DWORD dst_unused:UNUSED_PAD src0_sel:DWORD src1_sel:WORD_1\n\t" \
  "v_lshlrev_b32_sdwa v229, 2, v228 dst_sel:DWORD dst_unused:UNUSED_PAD src0_sel:DWORD src1_sel:BYTE_3\n\t" \
  "v_lshlrev_b32_sdwa v228, 2, v228 dst_sel:DWORD dst_unused:UNUSED_PAD src0_sel:DWORD src1_sel:BYTE_1\n\t" \
  "v_mfma_f32_16x16x32_bf16 v[48:51], v[208:211], v[162:165], v[48:51]\n\t" \
  "ds_read_u16 v226, v226 offset:10528\n\t" \
  "ds_read_b32 v229, v229 offset:10016\n\t" \
  "v_fma_mix_f32 v221, |v179|, v221, v221 op_sel:[0,1,0] op_sel_hi:[0,1,1] clamp\n\t" \
  "v_lshlrev_b32_sdwa v230, 2, v230 dst_sel:DWORD dst_unused:UNUSED_PAD src0_sel:DWORD src1_sel:BYTE_1\n\t" \
  "v_lshlrev_b32_sdwa v235, 1, v201 dst_sel:DWORD dst_unused:UNUSED_PAD src0_sel:DWORD src1_sel:WORD_1\n\t" \
  "s_waitcnt lgkmcnt(9)\n\t" \
  "v_lshlrev_b32_sdwa v234, 1, v200 dst_sel:DWORD dst_unused:UNUSED_PAD src0_sel:DWORD src1_sel:WORD_1\n\t" \
  "v_fma_mix_f32 v220, |v178|, v220, v220 op_sel:[0,1,0] op_sel_hi:[0,1,1] clamp\n\t" \
  "v_mfma_f32_16x16x32_bf16 v[72:75], v[208:211], v[170:173], v[72:75]\n\t" \
  "s_waitcnt lgkmcnt(5)\n\t" \
  "ds_read_b64_tr_b16 v[208:209], v249 offset:6224\n\t" \
  "ds_read_b32 v228, v228 offset:10016\n\t" \
  "v_fma_mix_f32 v223, |v177|, v223, v223 op_sel:[0,1,0] op_sel_hi:[0,1,1] clamp\n\t" \
  "v_fma_mix_f32 v222, |v176|, v222, v222 op_sel:[0,1,0] op_sel_hi:[0,1,1] clamp\n\t" \
  "s_waitcnt lgkmcnt(3)\n\t" \
  "v_or_b32 v187, v224, v225\n\t" \
  "v_or_b32 v186, v226, v227\n\t" \
  "v_mfma_f32_16x16x32_bf16 v[4:7], v[212:215], v[146:149], v[4:7]\n\t" \
  "ds_read_b32 v231, v231 offset:10016\n\t" \
  "ds_read_b64_tr_b16 v[210:211], v249 offset:7760\n\t" \
  "v_fma_f32 v176, |v176|, v222, v176 mul:4\n\t" \
  "v_fma_f32 v179, |v179|, v221, v179 mul:4\n\t" \
  "v_fma_f32 v178, |v178|, v220, v178 mul:4\n\t" \
  "v_mfma_f32_16x16x32_bf16 v[28:31], v[212:215], v[154:157], v[28:31]\n\t" \
  "s_add_u32 s27, s27, s26\n\t" \
  "ds_read_u16_d16_hi v233, v233 offset:10528\n\t" \
  "v_fma_f32 v177, |v177|, v223, v177 mul:4\n\t" \
  "s_mov_b64 exec, s[30:31]\n\t" \
  "buffer_load_dword v237, v254, s[8:11], s27 offen\n\t" \
  "s_mov_b64 exec, -1\n\t" \
  "v_cvt_pk_bf16_f32 v179, v178, v179\n\t" \
  "v_mfma_f32_16x16x32_bf16 v[52:55], v[212:215], v[162:165], v[52:55]\n\t" \
  "ds_read_b32 v230, v230 offset:10016\n\t" \
  "v_cvt_pk_bf16_f32 v178, v176, v177\n\t" \
  "v_cvt_pknorm_i16_f32 v220, |v180|, |v181|\n\t" \
  "v_cvt_pknorm_i16_f32 v222, |v182|, |v183|\n\t" \
  "v_lshlrev_b32_sdwa v221, 2, v220 dst_sel:DWORD dst_unused:UNUSED_PAD src0_sel:DWORD src1_sel:BYTE_3\n\t" \
  "v_lshlrev_b32_sdwa v223, 2, v222 dst_sel:DWORD dst_unused:UNUSED_PAD src0_sel:DWORD src1_sel:BYTE_3\n\t" \
  "v_mfma_f32_16x16x32_bf16 v[76:79], v[212:215], v[170:173], v[76:79]\n\t" \
  "ds_read_b64_tr_b16 v[214:215], v249 offset:7816\n\t" \
  "ds_read_u16 v232, v232 offset:10528\n\t" \
  "v_lshlrev_b32_sdwa v220, 2, v220 dst_sel:DWORD dst_unused:UNUSED_PAD src0_sel:DWORD src1_sel:BYTE_1\n\t" \
  "v_lshlrev_b32_sdwa v226, 1, v190 dst_sel:DWORD dst_unused:UNUSED_PAD src0_sel:DWORD src1_sel:WORD_1\n\t" \
  "v_lshlrev_b32_sdwa v224, 1, v188 dst_sel:DWORD dst_unused:UNUSED_PAD src0_sel:DWORD src1_sel:WORD_1\n\t" \
  "v_mfma_f32_16x16x32_bf16 v[32:35], v[216:219], v[154:157], v[32:35]\n\t" \
  "s_waitcnt lgkmcnt(2)\n\t" \
  "v_lshlrev_b32_sdwa v222, 2, v222 dst_sel:DWORD dst_unused:UNUSED_PAD src0_sel:DWORD src1_sel:BYTE_1\n\t" \
  "v_lshlrev_b32_sdwa v225, 1, v189 dst_sel:DWORD dst_unused:UNUSED_PAD src0_sel:DWORD src1_sel:WORD_1\n\t" \
  "v_fma_mix_f32 v231, |v193|, v231, v231 op_sel:[0,1,0] op_sel_hi:[0,1,1] clamp\n\t" \
  "v_fma_mix_f32 v228, |v194|, v228, v228 op_sel:[0,1,0] op_sel_hi:[0,1,1] clamp\n\t" \
  "v_mfma_f32_16x16x32_bf16 v[56:59], v[216:219], v[162:165], v[56:59]\n\t" \
  "s_setprio 1\n\t" \
  "ds_read_u16 v234, v234 offset:10528\n\t" \
  "ds_read_b64_tr_b16 v[212:213], v249 offset:6280\n\t" \
  "ds_read_b32 v222, v222 offset:10016\n\t" \
  "v_fma_mix_f32 v230, |v192|, v230, v230 op_sel:[0,1,0] op_sel_hi:[0,1,1] clamp\n\t" \
  "v_fma_f32 v194, |v194|, v228, v194 mul:4\n\t" \
  "v_fma_mix_f32 v229, |v195|, v229, v229 op_sel:[0,1,0] op_sel_hi:[0,1,1] clamp\n\t" \
  "v_fma_f32 v195, |v195|, v229, v195 mul:4\n\t" \
  "v_cvt_pk_bf16_f32 v195, v194, v195\n\t" \
  "v_fma_f32 v192, |v192|, v230, v192 mul:4\n\t" \
  "v_mfma_f32_16x16x32_bf16 v[12:15], v[208:211], v[146:149], v[12:15]\n\t" \
  "ds_read_b32 v220, v220 offset:10016\n\t" \
  "ds_read_b32 v221, v221 offset:10016\n\t" \
  "v_mfma_f32_16x16x32_bf16 v[8:11], v[216:219], v[146:149], v[8:11]\n\t" \
  "ds_read_u16_d16_hi v235, v235 offset:10528\n\t" \
  "v_cvt_pknorm_i16_f32 v230, |v198|, |v199|\n\t" \
  "v_lshlrev_b32_sdwa v227, 1, v191 dst_sel:DWORD dst_unused:UNUSED_PAD src0_sel:DWORD src1_sel:WORD_1\n\t" \
  "v_fma_f32 v193, |v193|, v231, v193 mul:4\n\t" \
  "v_cvt_pk_bf16_f32 v194, v192, v193\n\t" \
  "s_waitcnt lgkmcnt(6)\n\t" \
  "v_or_b32 v203, v232, v233\n\t" \
  "v_cvt_pknorm_i16_f32 v228, |v196|, |v197|\n\t" \
  "v_mfma_f32_16x16x32_bf16 v[80:83], v[216:219], v[170:173], v[80:83]\n\t" \
  "ds_read_b64_tr_b16 v[216:217], v249 offset:6288\n\t" \
  "v_mfma_f32_16x16x32_bf16 v[60:63], v[208:211], v[162:165], v[60:63]\n\t" \
  "ds_read_u16_d16_hi v225, v225 offset:10528\n\t" \
  "ds_read_b64_tr_b16 v[218:219], v249 offset:7824\n\t" \
  "s_mov_b64 exec, 3\n\t" \
  "ds_add_rtn_u32 v246, v251, v250 offset:47232\n\t" \
  "s_mov_b64 exec, -1\n\t" \
  "v_lshlrev_b32_sdwa v229, 2, v228 dst_sel:DWORD dst_unused:UNUSED_PAD src0_sel:DWORD src1_sel:BYTE_3\n\t" \
  "v_lshlrev_b32_sdwa v231, 2, v230 dst_sel:DWORD dst_unused:UNUSED_PAD src0_sel:DWORD src1_sel:BYTE_3\n\t" \
  "v_lshlrev_b32_sdwa v228, 2, v228 dst_sel:DWORD dst_unused:UNUSED_PAD src0_sel:DWORD src1_sel:BYTE_1\n\t" \
  "v_lshlrev_b32_sdwa v230, 2, v230 dst_sel:DWORD dst_unused:UNUSED_PAD src0_sel:DWORD src1_sel:BYTE_1\n\t" \
  "v_lshlrev_b32_sdwa v233, 1, v205 dst_sel:DWORD dst_unused:UNUSED_PAD src0_sel:DWORD src1_sel:WORD_1\n\t" \
  "v_mfma_f32_16x16x32_bf16 v[36:39], v[208:211], v[154:157], v[36:39]\n\t" \
  "ds_read_u16_d16_hi v227, v227 offset:10528\n\t" \
  "ds_read_b32 v223, v223 offset:10016\n\t" \
  "v_mfma_f32_16x16x32_bf16 v[84:87], v[208:211], v[170:173], v[84:87]\n\t" \
  "s_waitcnt lgkmcnt(6)\n\t" \
  "ds_read_u16 v224, v224 offset:10528\n\t" \
  "v_or_b32 v202, v234, v235\n\t" \
  "s_waitcnt lgkmcnt(3)\n\t" \
  "v_lshlrev_b32_sdwa v232, 1, v204 dst_sel:DWORD dst_unused:UNUSED_PAD src0_sel:DWORD src1_sel:WORD_1\n\t" \
  "v_lshlrev_b32_sdwa v235, 1, v207 dst_sel:DWORD dst_unused:UNUSED_PAD src0_sel:DWORD src1_sel:WORD_1\n\t" \
  "v_readfirstlane_b32 s4, v246\n\t" \
  "v_mfma_f32_16x16x32_bf16 v[16:19], v[212:215], v[146:149], v[16:19]\n\t" \
  "ds_read_u16 v226, v226 offset:10528\n\t" \
  "ds_read_b32 v228, v228 offset:10016\n\t" \
  "v_mfma_f32_16x16x32_bf16 v[40:43], v[212:215], v[154:157], v[40:43]\n\t" \
  "ds_read_b32 v229, v229 offset:10016\n\t" \
  "s_cmpk_lt_u32 s4, 3072\n\t" \
  "s_cbranch_scc1 .Lcnx_w0_13\n\t" \
  ".Lcnx_g0_13:\n\t" \
  "ds_read_b64_tr_b16 v[208:209], v249 offset:12304\n\t" \
  "v_fma_mix_f32 v221, |v181|, v221, v221 op_sel:[0,1,0] op_sel_hi:[0,1,1] clamp\n\t" \
  "v_fma_mix_f32 v222, |v182|, v222, v222 op_sel:[0,1,0] op_sel_hi:[0,1,1] clamp\n\t" \
  "v_mfma_f32_16x16x32_bf16 v[64:67], v[212:215], v[162:165], v[64:67]\n\t" \
  "ds_read_b64_tr_b16 v[210:211], v249 offset:13840\n\t" \
  "ds_read_b32 v231, v231 offset:10016\n\t" \
  "s_waitcnt lgkmcnt(5)\n\t" \
  "v_or_b32 v188, v224, v225\n\t" \
  "v_or_b32 v189, v226, v227\n\t" \
  "v_mfma_f32_16x16x32_bf16 v[88:91], v[212:215], v[170:173], v[88:91]\n\t" \
  "ds_read_b64_tr_b16 v[212:213], v249 offset:12312\n\t" \
  "ds_read_b32 v230, v230 offset:10016\n\t" \
  "v_fma_f32 v181, |v181|, v221, v181 mul:4\n\t" \
  "v_fma_mix_f32 v220, |v180|, v220, v220 op_sel:[0,1,0] op_sel_hi:[0,1,1] clamp\n\t" \
  "ds_read_b64_tr_b16 v[214:215], v249 offset:13848\n\t" \
  "v_fma_f32 v182, |v182|, v222, v182 mul:4\n\t" \
  "v_lshlrev_b32_sdwa v234, 1, v206 dst_sel:DWORD dst_unused:UNUSED_PAD src0_sel:DWORD src1_sel:WORD_1\n\t" \
  "v_mfma_f32_16x16x32_bf16 v[20:23], v[216:219], v[146:149], v[20:23]\n\t" \
  "s_waitcnt lgkmcnt(7)\n\t" \
  "v_fma_mix_f32 v228, |v196|, v228, v228 op_sel:[0,1,0] op_sel_hi:[0,1,1] clamp\n\t" \
  "s_waitcnt lgkmcnt(6)\n\t" \
  "v_fma_mix_f32 v229, |v197|, v229, v229 op_sel:[0,1,0] op_sel_hi:[0,1,1] clamp\n\t" \
  "v_mfma_f32_16x16x32_bf16 v[44:47], v[216:219], v[154:157], v[44:47]\n\t" \
  "s_waitcnt lgkmcnt(1)\n\t" \
  "ds_read_u16_d16_hi v233, v233 offset:10528\n\t" \
  "ds_read_u16_d16_hi v235, v235 offset:10528\n\t" \
  "ds_read_u16 v232, v232 offset:10528\n\t" \
  "v_fma_mix_f32 v223, |v183|, v223, v223 op_sel:[0,1,0] op_sel_hi:[0,1,1] clamp\n\t" \
  "v_mfma_f32_32x32x16_bf16 v[144:159], v[242:245], v[240:243], 0\n\t" \
  "ds_read_u16 v234, v234 offset:10528\n\t" \
  "v_fma_f32 v183, |v183|, v223, v183 mul:4\n\t" \
  "v_fma_mix_f32 v231, |v199|, v231, v231 op_sel:[0,1,0] op_sel_hi:[0,1,1] clamp\n\t" \
  "v_fma_mix_f32 v230, |v198|, v230, v230 op_sel:[0,1,0] op_sel_hi:[0,1,1] clamp\n\t" \
  "v_mfma_f32_16x16x32_bf16 v[68:71], v[216:219], v[162:165], v[68:71]\n\t" \
  "v_fma_f32 v180, |v180|, v220, v180 mul:4\n\t" \
  "v_cvt_pk_bf16_f32 v180, v180, v181\n\t" \
  "v_cvt_pk_bf16_f32 v181, v182, v183\n\t" \
  "v_fma_f32 v199, |v199|, v231, v199 mul:4\n\t" \
  "v_fma_f32 v196, |v196|, v228, v196 mul:4\n\t" \
  "v_mfma_f32_16x16x32_bf16 v[92:95], v[216:219], v[170:173], v[92:95]\n\t" \
  "s_waitcnt lgkmcnt(1)\n\t" \
  "v_or_b32 v204, v232, v233\n\t" \
  "s_waitcnt lgkmcnt(0)\n\t" \
  "v_mfma_f32_32x32x16_bf16 v[160:175], v[242:245], v[240:243], 0\n\t" \
  "ds_read_b64_tr_b16 v[216:217], v249 offset:12368\n\t" \
  "v_or_b32 v205, v234, v235\n\t" \
  "v_fma_f32 v197, |v197|, v229, v197 mul:4\n\t" \
  "v_cvt_pk_bf16_f32 v196, v196, v197\n\t" \
  "v_fma_f32 v198, |v198|, v230, v198 mul:4\n\t" \
  "v_cvt_pk_bf16_f32 v197, v198, v199\n\t" \
  "v_mfma_f32_16x16x32_bf16 v[0:3], v[208:211], v[178:181], v[0:3]\n\t" \
  "ds_read_b64_tr_b16 v[218:219], v249 offset:13904\n\t" \
  "ds_read2_b32 v[114:115], v247 offset0:192 offset1:198\n\t" \
  "v_mfma_f32_16x16x32_bf16 v[48:51], v[208:211], v[194:197], v[48:51]\n\t" \
  "ds_read2_b32 v[102:103], v247 offset0:12 offset1:18\n\t" \
  "v_add_u32 v232, s56, v247\n\t" \
  "v_mfma_f32_16x16x32_bf16 v[24:27], v[208:211], v[186:189], v[24:27]\n\t" \
  "ds_read2_b32 v[98:99], v247 offset0:0 offset1:6\n\t" \
  "ds_read2_b32 v[134:135], v232 offset0:12 offset1:18\n\t" \
  "v_mfma_f32_16x16x32_bf16 v[72:75], v[208:211], v[202:205], v[72:75]\n\t" \
  "ds_read_b64_tr_b16 v[208:209], v249 offset:12376\n\t" \
  "ds_read2_b32 v[118:119], v247 offset0:204 offset1:210\n\t" \
  "ds_read2_b32 v[130:131], v232 offset0:0 offset1:6\n\t" \
  "v_mfma_f32_16x16x32_bf16 v[4:7], v[212:215], v[178:181], v[4:7]\n\t" \
  "ds_read_b64_tr_b16 v[210:211], v249 offset:13912\n\t" \
  "s_add_u32 s27, s27, s26\n\t" \
  "v_mfma_f32_16x16x32_bf16 v[28:31], v[212:215], v[186:189], v[28:31]\n\t" \
  "s_mov_b64 exec, s[30:31]\n\t" \
  "buffer_load_dword v237, v254, s[8:11], s27 offen\n\t" \
  "s_mov_b64 exec, -1\n\t" \
  "v_mfma_f32_16x16x32_bf16 v[52:55], v[212:215], v[194:197], v[52:55]\n\t" \
  "ds_read2_b32 v[122:123], v247 offset0:216 offset1:222\n\t" \
  "ds_read2_b32 v[106:107], v247 offset0:24 offset1:30\n\t" \
  "v_mfma_f32_16x16x32_bf16 v[76:79], v[212:215], v[202:205], v[76:79]\n\t" \
  "ds_read_b64_tr_b16 v[212:213], v249 offset:12432\n\t" \
  "s_waitcnt lgkmcnt(11)\n\t" \
  "v_mfma_f32_16x16x32_bf16 v[8:11], v[216:219], v[178:181], v[8:11]\n\t" \
  "ds_read_b64_tr_b16 v[214:215], v249 offset:13968\n\t" \
  "v_bfe_u32 v234, v251, 3, 2\n\t" \
  "v_mfma_f32_16x16x32_bf16 v[32:35], v[216:219], v[186:189], v[32:35]\n\t" \
  "ds_read2_b32 v[110:111], v247 offset0:36 offset1:42\n\t" \
  "v_mul_u32_u24 v234, 0x5555, v234\n\t" \
  "v_mfma_f32_16x16x32_bf16 v[56:59], v[216:219], v[194:197], v[56:59]\n\t" \
  "v_cvt_pk_bf16_f32 v48, v48, v49\n\t" \
  "v_cvt_pk_bf16_f32 v49, v50, v51\n\t" \
  "v_mfma_f32_16x16x32_bf16 v[80:83], v[216:219], v[202:205], v[80:83]\n\t" \
  "ds_read_b64_tr_b16 v[216:217], v249 offset:12440\n\t" \
  "ds_read2_b32 v[138:139], v232 offset0:24 offset1:30\n\t" \
  "s_waitcnt lgkmcnt(7)\n\t" \
  "ds_read2_b32 v[126:127], v247 offset0:228 offset1:234\n\t" \
  "v_cvt_pk_bf16_f32 v51, v54, v55\n\t" \
  "v_cvt_pk_bf16_f32 v50, v52, v53\n\t" \
  "v_cvt_pk_bf16_f32 v52, v0, v1\n\t" \
  "v_cvt_pk_bf16_f32 v72, v72, v73\n\t" \
  "v_cvt_pk_bf16_f32 v73, v74, v75\n\t" \
  "v_cvt_pk_bf16_f32 v74, v76, v77\n\t" \
  "v_cvt_pk_bf16_f32 v75, v78, v79\n\t" \
  "v_mfma_f32_16x16x32_bf16 v[12:15], v[208:211], v[178:181], v[12:15]\n\t" \
  "buffer_store_dwordx4 v[72:75], v250, s[20:23], s13 offen offset:0 sc1\n\t" \
  "ds_read_b64_tr_b16 v[218:219], v249 offset:13976\n\t" \
  "v_cvt_pk_bf16_f32 v53, v2, v3\n\t" \
  "v_cvt_pk_bf16_f32 v77, v26, v27\n\t" \
  "v_mfma_f32_16x16x32_bf16 v[36:39], v[208:211], v[186:189], v[36:39]\n\t" \
  "s_add_u32 s63, s28, s65\n\t" \
  "ds_read2_b32 v[142:143], v232 offset0:36 offset1:42\n\t" \
  "v_cvt_pk_bf16_f32 v55, v6, v7\n\t" \
  "v_mfma_f32_16x16x32_bf16 v[60:63], v[208:211], v[194:197], v[60:63]\n\t" \
  "v_cvt_pk_bf16_f32 v76, v24, v25\n\t" \
  "s_waitcnt lgkmcnt(6)\n\t" \
  "v_mfma_f32_16x16x32_bf16 v[64:67], v[212:215], v[194:197], v[64:67]\n\t" \
  "v_cvt_pk_bf16_f32 v78, v28, v29\n\t" \
  "v_cvt_pk_bf16_f32 v79, v30, v31\n\t" \
  "v_mfma_f32_16x16x32_bf16 v[88:91], v[212:215], v[202:205], v[88:91]\n\t" \
  "v_cvt_pk_bf16_f32 v56, v56, v57\n\t" \
  "v_cvt_pk_bf16_f32 v57, v58, v59\n\t" \
  "v_mfma_f32_16x16x32_bf16 v[84:87], v[208:211], v[202:205], v[84:87]\n\t" \
  "v_cvt_pk_bf16_f32 v58, v60, v61\n\t" \
  "v_cvt_pk_bf16_f32 v59, v62, v63\n\t" \
  "v_mfma_f32_16x16x32_bf16 v[16:19], v[212:215], v[178:181], v[16:19]\n\t" \
  "v_cvt_pk_bf16_f32 v63, v14, v15\n\t" \
  "v_mfma_f32_16x16x32_bf16 v[40:43], v[212:215], v[186:189], v[40:43]\n\t" \
  "v_cvt_pk_bf16_f32 v54, v4, v5\n\t" \
  "v_cvt_pk_bf16_f32 v80, v80, v81\n\t" \
  "s_waitcnt lgkmcnt(1)\n\t" \
  "v_cvt_pk_bf16_f32 v81, v82, v83\n\t" \
  "v_cvt_pk_bf16_f32 v83, v86, v87\n\t" \
  "v_cvt_pk_bf16_f32 v82, v84, v85\n\t" \
  "v_cvt_pk_bf16_f32 v85, v34, v35\n\t" \
  "v_mfma_f32_16x16x32_bf16 v[68:71], v[216:219], v[194:197], v[68:71]\n\t" \
  "s_setprio 0\n\t" \
  "buffer_store_dwordx4 v[80:83], v250, s[20:23], s13 offen offset:64 sc1\n\t" \
  "v_cvt_pk_bf16_f32 v84, v32, v33\n\t" \
  "v_cvt_pk_bf16_f32 v61, v10, v11\n\t" \
  "v_cvt_pk_bf16_f32 v88, v88, v89\n\t" \
  "v_mfma_f32_16x16x32_bf16 v[92:95], v[216:219], v[202:205], v[92:95]\n\t" \
  "ds_read_b64 v[212:213], v253 offset:808\n\t" \
  "v_cvt_pk_bf16_f32 v62, v12, v13\n\t" \
  "v_cvt_pk_bf16_f32 v64, v64, v65\n\t" \
  "v_mfma_f32_16x16x32_bf16 v[20:23], v[216:219], v[178:181], v[20:23]\n\t" \
  "s_add_u32 s62, s29, s65\n\t" \
  "v_cvt_pk_bf16_f32 v60, v8, v9\n\t" \
  "v_cvt_pk_bf16_f32 v89, v90, v91\n\t" \
  "v_cvt_pk_bf16_f32 v65, v66, v67\n\t" \
  "v_cvt_pk_bf16_f32 v90, v92, v93\n\t" \
  "v_mfma_f32_16x16x32_bf16 v[44:47], v[216:219], v[186:189], v[44:47]\n\t" \
  "s_add_u32 s14, s12, s65\n\t" \
  "ds_read_b64 v[218:219], v253 offset:416\n\t" \
  "v_cvt_pk_bf16_f32 v91, v94, v95\n\t" \
  "v_cvt_pk_bf16_f32 v87, v38, v39\n\t" \
  "buffer_store_dwordx4 v[88:91], v250, s[20:23], s13 offen offset:128 sc1\n\t" \
  "v_and_b32 v96, 0xffff, v98\n\t" \
  "v_and_b32 v97, 0xffff0000, v98\n\t" \
  "v_cvt_pk_bf16_f32 v67, v70, v71\n\t" \
  "v_cvt_pk_bf16_f32 v86, v36, v37\n\t" \
  "v_and_b32 v128, 0xffff, v130\n\t" \
  "v_cvt_pk_bf16_f32 v66, v68, v69\n\t" \
  "v_cvt_pk_bf16_f32 v71, v22, v23\n\t" \
  "v_cvt_pk_bf16_f32 v69, v18, v19\n\t" \
  "v_cvt_pk_bf16_f32 v95, v46, v47\n\t" \
  "v_lshlrev_b32 v198, 1, v251\n\t" \
  "global_load_dwordx4 v[192:195], v198, s[66:67]\n\t" \
  "v_and_b32 v98, 0xffff, v99\n\t" \
  "v_and_b32 v99, 0xffff0000, v99\n\t" \
  "s_branch .Lcnx_mid\n\t" \
  ".Lcnx_w0_2:\n\t" \
  "ds_read_b32 v246, v251 offset:47144\n\t" \
  "s_waitcnt lgkmcnt(0)\n\t" \
  "v_readfirstlane_b32 s4, v246\n\t" \
  "s_cmp_lt_u32 s4, 3072\n\t" \
  "s_cbranch_scc1 .Lcnx_w0_2\n\t" \
  "s_branch .Lcnx_g0_2\n\t" \
  ".Lcnx_w0_3:\n\t" \
  "ds_read_b32 v246, v251 offset:47152\n\t" \
  "s_waitcnt lgkmcnt(0)\n\t" \
  "v_readfirstlane_b32 s4, v246\n\t" \
  "s_cmp_lt_u32 s4, 3072\n\t" \
  "s_cbranch_scc1 .Lcnx_w0_3\n\t" \
  "s_branch .Lcnx_g0_3\n\t" \
  ".Lcnx_w0_4:\n\t" \
  "ds_read_b32 v246, v251 offset:47160\n\t" \
  "s_waitcnt lgkmcnt(0)\n\t" \
  "v_readfirstlane_b32 s4, v246\n\t" \
  "s_cmp_lt_u32 s4, 3072\n\t" \
  "s_cbranch_scc1 .Lcnx_w0_4\n\t" \
  "s_branch .Lcnx_g0_4\n\t" \
  ".Lcnx_w0_5:\n\t" \
  "ds_read_b32 v246, v251 offset:47168\n\t" \
  "s_waitcnt lgkmcnt(0)\n\t" \
  "v_readfirstlane_b32 s4, v246\n\t" \
  "s_cmp_lt_u32 s4, 3072\n\t" \
  "s_cbranch_scc1 .Lcnx_w0_5\n\t" \
  "s_branch .Lcnx_g0_5\n\t" \
  ".Lcnx_w0_6:\n\t" \
  "ds_read_b32 v246, v251 offset:47176\n\t" \
  "s_waitcnt lgkmcnt(0)\n\t" \
  "v_readfirstlane_b32 s4, v246\n\t" \
  "s_cmp_lt_u32 s4, 3072\n\t" \
  "s_cbranch_scc1 .Lcnx_w0_6\n\t" \
  "s_branch .Lcnx_g0_6\n\t" \
  ".Lcnx_w0_7:\n\t" \
  "ds_read_b32 v246, v251 offset:47184\n\t" \
  "s_waitcnt lgkmcnt(0)\n\t" \
  "v_readfirstlane_b32 s4, v246\n\t" \
  "s_cmp_lt_u32 s4, 3072\n\t" \
  "s_cbranch_scc1 .Lcnx_w0_7\n\t" \
  "s_branch .Lcnx_g0_7\n\t" \
  ".Lcnx_w0_8:\n\t" \
  "ds_read_b32 v246, v251 offset:47192\n\t" \
  "s_waitcnt lgkmcnt(0)\n\t" \
  "v_readfirstlane_b32 s4, v246\n\t" \
  "s_cmp_lt_u32 s4, 3072\n\t" \
  "s_cbranch_scc1 .Lcnx_w0_8\n\t" \
  "s_branch .Lcnx_g0_8\n\t" \
  ".Lcnx_w0_9:\n\t" \
  "ds_read_b32 v246, v251 offset:47200\n\t" \
  "s_waitcnt lgkmcnt(0)\n\t" \
  "v_readfirstlane_b32 s4, v246\n\t" \
  "s_cmp_lt_u32 s4, 3072\n\t" \
  "s_cbranch_scc1 .Lcnx_w0_9\n\t" \
  "s_branch .Lcnx_g0_9\n\t" \
  ".Lcnx_w0_10:\n\t" \
  "ds_read_b32 v246, v251 offset:47208\n\t" \
  "s_waitcnt lgkmcnt(0)\n\t" \
  "v_readfirstlane_b32 s4, v246\n\t" \
  "s_cmp_lt_u32 s4, 3072\n\t" \
  "s_cbranch_scc1 .Lcnx_w0_10\n\t" \
  "s_branch .Lcnx_g0_10\n\t" \
  ".Lcnx_t0:\n\t" \
  "v_add_u32 v246, -116544, v251\n\t" \
  "v_lshrrev_b32 v246, 3, v246\n\t" \
  "v_mul_i32_i24 v246, s26, v246\n\t" \
  "v_add_u32 v246, s28, v246\n\t" \
  "s_mov_b32 m0, 107744\n\t" \
  "s_mov_b64 exec, 0xffff\n\t" \
  "buffer_load_dword v246, s[20:23], 0 offen lds\n\t" \
  "s_mov_b64 exec, -1\n\t" \
  "s_branch .Lcnx_u0\n\t" \
  ".Lcnx_w0_11:\n\t" \
  "ds_read_b32 v246, v251 offset:47216\n\t" \
  "s_waitcnt lgkmcnt(0)\n\t" \
  "v_readfirstlane_b32 s4, v246\n\t" \
  "s_cmp_lt_u32 s4, 3072\n\t" \
  "s_cbranch_scc1 .Lcnx_w0_11\n\t" \
  "s_branch .Lcnx_g0_11\n\t" \
  ".Lcnx_w0_12:\n\t" \
  "ds_read_b32 v246, v251 offset:47224\n\t" \
  "s_waitcnt lgkmcnt(0)\n\t" \
  "v_readfirstlane_b32 s4, v246\n\t" \
  "s_cmp_lt_u32 s4, 3072\n\t" \
  "s_cbranch_scc1 .Lcnx_w0_12\n\t" \
  "s_branch .Lcnx_g0_12\n\t" \
  ".Lcnx_w0_13:\n\t" \
  "ds_read_b32 v246, v251 offset:47232\n\t" \
  "s_waitcnt lgkmcnt(0)\n\t" \
  "v_readfirstlane_b32 s4, v246\n\t" \
  "s_cmp_lt_u32 s4, 3072\n\t" \
  "s_cbranch_scc1 .Lcnx_w0_13\n\t" \
  "s_branch .Lcnx_g0_13\n\t" \
  ".Lcnx_w1_2:\n\t" \
  "ds_read_b32 v246, v251 offset:47144\n\t" \
  "s_waitcnt lgkmcnt(0)\n\t" \
  "v_readfirstlane_b32 s4, v246\n\t" \
  "s_cmp_lt_u32 s4, 6144\n\t" \
  "s_cbranch_scc1 .Lcnx_w1_2\n\t" \
  "s_branch .Lcnx_g1_2\n\t" \
  ".Lcnx_w1_3:\n\t" \
  "ds_read_b32 v246, v251 offset:47152\n\t" \
  "s_waitcnt lgkmcnt(0)\n\t" \
  "v_readfirstlane_b32 s4, v246\n\t" \
  "s_cmp_lt_u32 s4, 6144\n\t" \
  "s_cbranch_scc1 .Lcnx_w1_3\n\t" \
  "s_branch .Lcnx_g1_3\n\t" \
  ".Lcnx_w1_4:\n\t" \
  "ds_read_b32 v246, v251 offset:47160\n\t" \
  "s_waitcnt lgkmcnt(0)\n\t" \
  "v_readfirstlane_b32 s4, v246\n\t" \
  "s_cmp_lt_u32 s4, 6144\n\t" \
  "s_cbranch_scc1 .Lcnx_w1_4\n\t" \
  "s_branch .Lcnx_g1_4\n\t" \
  ".Lcnx_w1_5:\n\t" \
  "ds_read_b32 v246, v251 offset:47168\n\t" \
  "s_waitcnt lgkmcnt(0)\n\t" \
  "v_readfirstlane_b32 s4, v246\n\t" \
  "s_cmp_lt_u32 s4, 6144\n\t" \
  "s_cbranch_scc1 .Lcnx_w1_5\n\t" \
  "s_branch .Lcnx_g1_5\n\t" \
  ".Lcnx_w1_6:\n\t" \
  "ds_read_b32 v246, v251 offset:47176\n\t" \
  "s_waitcnt lgkmcnt(0)\n\t" \
  "v_readfirstlane_b32 s4, v246\n\t" \
  "s_cmp_lt_u32 s4, 6144\n\t" \
  "s_cbranch_scc1 .Lcnx_w1_6\n\t" \
  "s_branch .Lcnx_g1_6\n\t" \
  ".Lcnx_w1_7:\n\t" \
  "ds_read_b32 v246, v251 offset:47184\n\t" \
  "s_waitcnt lgkmcnt(0)\n\t" \
  "v_readfirstlane_b32 s4, v246\n\t" \
  "s_cmp_lt_u32 s4, 6144\n\t" \
  "s_cbranch_scc1 .Lcnx_w1_7\n\t" \
  "s_branch .Lcnx_g1_7\n\t" \
  ".Lcnx_w1_8:\n\t" \
  "ds_read_b32 v246, v251 offset:47192\n\t" \
  "s_waitcnt lgkmcnt(0)\n\t" \
  "v_readfirstlane_b32 s4, v246\n\t" \
  "s_cmp_lt_u32 s4, 6144\n\t" \
  "s_cbranch_scc1 .Lcnx_w1_8\n\t" \
  "s_branch .Lcnx_g1_8\n\t" \
  ".Lcnx_w1_9:\n\t" \
  "ds_read_b32 v246, v251 offset:47200\n\t" \
  "s_waitcnt lgkmcnt(0)\n\t" \
  "v_readfirstlane_b32 s4, v246\n\t" \
  "s_cmp_lt_u32 s4, 6144\n\t" \
  "s_cbranch_scc1 .Lcnx_w1_9\n\t" \
  "s_branch .Lcnx_g1_9\n\t" \
  ".Lcnx_mid:\n\t" \
  "ds_read_b64 v[208:209], v253 offset:8\n\t" \
  "ds_read_b64 v[204:205], v253 offset:800\n\t" \
  "ds_read_b64 v[200:201], v253 offset:0\n\t" \
  "v_and_b32 v113, 0xffff0000, v114\n\t" \
  "v_and_b32 v112, 0xffff, v114\n\t" \
  "v_add_u32 v233, s39, v251\n\t" \
  "v_cvt_pk_bf16_f32 v68, v16, v17\n\t" \
  "s_add_u32 s15, s13, s65\n\t" \
  "v_cvt_pk_bf16_f32 v93, v42, v43\n\t" \
  "v_cvt_pk_bf16_f32 v94, v44, v45\n\t" \
  "ds_read_b64 v[216:217], v253 offset:16\n\t" \
  "v_cvt_pk_bf16_f32 v70, v20, v21\n\t" \
  "v_add_u32 v232, 56880, v253\n\t" \
  "ds_read_b64 v[206:207], v253 offset:1200\n\t" \
  "ds_read_b64 v[202:203], v253 offset:400\n\t" \
  "v_mfma_f32_32x32x16_bf16 v[176:191], v[242:245], v[240:243], 0\n\t" \
  "s_add_u32 s24, s24, s26\n\t" \
  "ds_read_b64 v[210:211], v253 offset:408\n\t" \
  "ds_read_b64 v[214:215], v253 offset:1208\n\t" \
  "ds_read_b64 v[222:223], v253 offset:1216\n\t" \
  "ds_read_b64 v[220:221], v253 offset:816\n\t" \
  "v_cvt_pk_bf16_f32 v92, v40, v41\n\t" \
  "v_and_b32 v129, 0xffff0000, v130\n\t" \
  "v_and_b32 v101, 0xffff0000, v102\n\t" \
  "v_and_b32 v114, 0xffff, v115\n\t" \
  "v_and_b32 v115, 0xffff0000, v115\n\t" \
  "v_and_b32 v100, 0xffff, v102\n\t" \
  "v_and_b32 v130, 0xffff, v131\n\t" \
  "v_and_b32 v131, 0xffff0000, v131\n\t" \
  "s_waitcnt lgkmcnt(4)\n\t" \
  "v_smfmac_f32_16x16x64_bf16 v[144:147], v[96:99], v[200:207], v234\n\t" \
  ".p2align 3\n\t" \
  "s_waitcnt vmcnt(12) lgkmcnt(0)\n\t" \
  "s_barrier\n\t" \
  "ds_read_b64 v[204:205], v253 offset:17208\n\t" \
  "v_smfmac_f32_16x16x64_bf16 v[148:151], v[112:115], v[208:215], v234\n\t" \
  "ds_read_b64 v[214:215], v253 offset:17616\n\t" \
  "s_add_u32 m0, s25, 81480\n\t" \
  "ds_read_b64 v[206:207], v253 offset:17608\n\t" \
  "buffer_load_dwordx4 v254, s[8:11], s24 offen lds\n\t" \
  "ds_read_b64 v[202:203], v253 offset:16808\n\t" \
  "v_and_b32 v117, 0xffff0000, v118\n\t" \
  "v_and_b32 v104, 0xffff, v106\n\t" \
  "v_and_b32 v133, 0xffff0000, v134\n\t" \
  "ds_read_b64 v[208:209], v253 offset:16416\n\t" \
  "v_and_b32 v132, 0xffff, v134\n\t" \
  "v_and_b32 v116, 0xffff, v118\n\t" \
  "v_and_b32 v118, 0xffff, v119\n\t" \
  "s_add_u32 s24, s24, s26\n\t" \
  "v_and_b32 v105, 0xffff0000, v106\n\t" \
  "v_and_b32 v106, 0xffff, v107\n\t" \
  "s_setprio 3\n\t" \
  "s_setprio 1\n\t" \
  "ds_read_b64 v[228:229], v253 offset:17200\n\t" \
  "s_add_u32 m0, s25, 89680\n\t" \
  "ds_read_b64 v[200:201], v253 offset:16408\n\t" \
  "v_and_b32 v102, 0xffff, v103\n\t" \
  "v_and_b32 v103, 0xffff0000, v103\n\t" \
  "v_lshlrev_b32 v235, 1, v251\n\t" \
  "v_and_b32 v236, 0x70, v235\n\t" \
  "v_and_b32 v107, 0xffff0000, v107\n\t" \
  "ds_read_b64 v[224:225], v253 offset:16400\n\t" \
  "buffer_load_dwordx4 v254, s[8:11], s24 offen lds\n\t" \
  "ds_read_b64 v[210:211], v253 offset:16816\n\t" \
  "s_setprio 0\n\t" \
  "ds_read_b64 v[212:213], v253 offset:17216\n\t" \
  "ds_read_b64 v[230:231], v253 offset:17600\n\t" \
  "v_and_b32 v109, 0xffff0000, v110\n\t" \
  "v_and_b32 v134, 0xffff, v135\n\t" \
  "v_and_b32 v120, 0xffff, v122\n\t" \
  "v_and_b32 v121, 0xffff0000, v122\n\t" \
  "ds_read_b64 v[226:227], v253 offset:16800\n\t" \
  "v_bfe_u32 v235, v250, 6, 1\n\t" \
  "v_lshl_add_u32 v235, v235, 4, v250\n\t" \
  "s_add_u32 s24, s24, s26\n\t" \
  "v_add3_u32 v235, v235, v236, s61\n\t" \
  "s_add_u32 m0, s25, 97880\n\t" \
  "v_and_b32 v135, 0xffff0000, v135\n\t" \
  "s_waitcnt lgkmcnt(0)\n\t" \
  "buffer_load_dwordx4 v254, s[8:11], s24 offen lds\n\t" \
  "v_smfmac_f32_16x16x64_bf16 v[144:147], v[100:103], v[224:231], v234\n\t" \
  "s_setprio 0\n\t" \
  ".p2align 3\n\t" \
  "s_waitcnt vmcnt(12) lgkmcnt(0)\n\t" \
  "s_barrier\n\t" \
  "ds_read_b64 v[38:39], v235 offset:8\n\t" \
  "v_smfmac_f32_16x16x64_bf16 v[152:155], v[128:131], v[216:223], v234\n\t" \
  "v_and_b32 v122, 0xffff, v123\n\t" \
  "v_and_b32 v119, 0xffff0000, v119\n\t" \
  "ds_read_b64 v[216:217], v253 offset:32800\n\t" \
  "v_smfmac_f32_16x16x64_bf16 v[156:159], v[96:99], v[224:231], v234\n\t" \
  "ds_read_b64 v[226:227], v253 offset:33208\n\t" \
  "v_and_b32 v123, 0xffff0000, v123\n\t" \
  "s_add_u32 s24, s24, s26\n\t" \
  "s_add_u32 m0, s25, 0\n\t" \
  "ds_read_b64 v[36:37], v235 offset:0\n\t" \
  "buffer_load_dwordx4 v254, s[8:11], s24 offen lds\n\t" \
  "v_and_b32 v137, 0xffff0000, v138\n\t" \
  "ds_read_b64 v[220:221], v253 offset:33600\n\t" \
  "v_smfmac_f32_16x16x64_bf16 v[148:151], v[116:119], v[200:207], v234\n\t" \
  "s_add_u32 s24, s24, s26\n\t" \
  "ds_read_b64 v[224:225], v253 offset:32808\n\t" \
  "s_add_u32 m0, s25, 8200\n\t" \
  "s_setprio 1\n\t" \
  "v_smfmac_f32_16x16x64_bf16 v[160:163], v[112:115], v[200:207], v234\n\t" \
  "ds_read_b64 v[46:47], v235 offset:136\n\t" \
  "ds_read_b64 v[218:219], v253 offset:33200\n\t" \
  "buffer_load_dwordx4 v254, s[8:11], s24 offen lds\n\t" \
  "v_smfmac_f32_16x16x64_bf16 v[164:167], v[128:131], v[208:215], v234\n\t" \
  "s_add_u32 s24, s24, s26\n\t" \
  "ds_read_b64 v[202:203], v253 offset:33216\n\t" \
  "ds_read_b64 v[222:223], v253 offset:34000\n\t" \
  "v_and_b32 v108, 0xffff, v110\n\t" \
  "v_and_b32 v136, 0xffff, v138\n\t" \
  "v_and_b32 v110, 0xffff, v111\n\t" \
  "ds_read_b64 v[204:205], v253 offset:33616\n\t" \
  "s_waitcnt lgkmcnt(1)\n\t" \
  "ds_read_b64 v[200:201], v253 offset:32816\n\t" \
  "s_setprio 0\n\t" \
  "s_add_u32 m0, s25, 16400\n\t" \
  "v_smfmac_f32_16x16x64_bf16 v[156:159], v[100:103], v[216:223], v234\n\t" \
  "ds_read_b64 v[42:43], v235 offset:72\n\t" \
  "buffer_load_dwordx4 v254, s[8:11], s24 offen lds\n\t" \
  "v_smfmac_f32_16x16x64_bf16 v[152:155], v[132:135], v[208:215], v234\n\t" \
  "ds_read_b64 v[44:45], v235 offset:128\n\t" \
  "v_and_b32 v138, 0xffff, v139\n\t" \
  "s_setprio 0\n\t" \
  "v_smfmac_f32_16x16x64_bf16 v[168:171], v[96:99], v[216:223], v234\n\t" \
  "ds_read_b64 v[40:41], v235 offset:64\n\t" \
  "v_and_b32 v111, 0xffff0000, v111\n\t" \
  "v_add_u32 v235, s58, v235\n\t" \
  "ds_read_b64 v[228:229], v253 offset:33608\n\t" \
  "ds_read_b64 v[206:207], v253 offset:34016\n\t" \
  "v_mfma_f32_32x32x16_bf16 v[0:15], v[242:245], v[240:243], 0\n\t" \
  "ds_read_b64 v[230:231], v253 offset:34008\n\t" \
  "s_waitcnt lgkmcnt(0)\n\t" \
  "v_and_b32 v139, 0xffff0000, v139\n\t" \
  "v_smfmac_f32_16x16x64_bf16 v[144:147], v[104:107], v[216:223], v234\n\t" \
  ".p2align 3\n\t" \
  "s_waitcnt vmcnt(4) lgkmcnt(0)\n\t" \
  "s_barrier\n\t" \
  "ds_read_b64 v[218:219], v232 offset:408\n\t" \
  "v_smfmac_f32_16x16x64_bf16 v[160:163], v[116:119], v[224:231], v234\n\t" \
  "ds_read_b64 v[210:211], v232 offset:400\n\t" \
  "ds_read_b64 v[216:217], v232 offset:8\n\t" \
  "v_smfmac_f32_16x16x64_bf16 v[172:175], v[112:115], v[224:231], v234\n\t" \
  "s_add_u32 m0, s25, 24600\n\t" \
  "ds_read_b64 v[74:75], v235 offset:8\n\t" \
  "s_add_u32 s24, s24, s26\n\t" \
  "buffer_load_dwordx4 v254, s[8:11], s24 offen lds\n\t" \
  "ds_read_b64 v[220:221], v232 offset:808\n\t" \
  "v_smfmac_f32_16x16x64_bf16 v[148:151], v[120:123], v[224:231], v234\n\t" \
  "ds_read_b64 v[208:209], v232 offset:0\n\t" \
  "ds_read_b64 v[222:223], v232 offset:1208\n\t" \
  "v_and_b32 v124, 0xffff, v126\n\t" \
  "ds_read_b64 v[212:213], v232 offset:800\n\t" \
  "ds_read_b64 v[214:215], v232 offset:1200\n\t" \
  "v_and_b32 v125, 0xffff0000, v126\n\t" \
  "s_add_u32 s24, s24, s26\n\t" \
  "v_and_b32 v141, 0xffff0000, v142\n\t" \
  "s_add_u32 m0, s25, 32800\n\t" \
  "v_and_b32 v126, 0xffff, v127\n\t" \
  "buffer_load_dwordx4 v254, s[8:11], s24 offen lds\n\t" \
  "s_add_u32 m0, s25, 41000\n\t" \
  "v_smfmac_f32_16x16x64_bf16 v[176:179], v[128:131], v[200:207], v234\n\t" \
  "ds_read_b64 v[82:83], v235 offset:72\n\t" \
  "ds_read_b64 v[72:73], v235 offset:0\n\t" \
  "v_smfmac_f32_16x16x64_bf16 v[152:155], v[136:139], v[200:207], v234\n\t" \
  "v_mov_b32 v199, 0x3f003f00\n\t" \
  "v_smfmac_f32_16x16x64_bf16 v[164:167], v[132:135], v[200:207], v234\n\t" \
  "ds_read_b64 v[226:227], v232 offset:416\n\t" \
  "ds_read_b64 v[224:225], v232 offset:16\n\t" \
  "v_mfma_f32_32x32x16_bf16 v[16:31], v[242:245], v[240:243], 0\n\t" \
  "ds_read_b64 v[88:89], v235 offset:128\n\t" \
  "v_and_b32 v140, 0xffff, v142\n\t" \
  "v_and_b32 v142, 0xffff, v143\n\t" \
  "s_waitcnt lgkmcnt(5)\n\t" \
  "v_and_b32 v143, 0xffff0000, v143\n\t" \
  "v_mov_b32 v197, 0x3f003f00\n\t" \
  "v_and_b32 v127, 0xffff0000, v127\n\t" \
  "v_smfmac_f32_16x16x64_bf16 v[156:159], v[104:107], v[208:215], v234\n\t" \
  "ds_read_b64 v[90:91], v235 offset:136\n\t" \
  "ds_read_b64 v[228:229], v232 offset:816\n\t" \
  "v_smfmac_f32_16x16x64_bf16 v[144:147], v[108:111], v[208:215], v234\n\t" \
  "s_add_u32 s24, s24, s26\n\t" \
  "ds_read_b64 v[230:231], v232 offset:1216\n\t" \
  "ds_read_b64 v[80:81], v235 offset:64\n\t" \
  "buffer_load_dwordx4 v254, s[8:11], s24 offen lds\n\t" \
  "v_mov_b32 v196, 0x3f003f00\n\t" \
  "v_smfmac_f32_16x16x64_bf16 v[180:183], v[96:99], v[208:215], v234\n\t" \
  "v_mov_b32 v198, 0x3f003f00\n\t" \
  "v_smfmac_f32_16x16x64_bf16 v[172:175], v[116:119], v[216:223], v234\n\t" \
  "v_add_u32 v235, s59, v235\n\t" \
  "v_add_u32 v235, s60, v235\n\t" \
  "v_smfmac_f32_16x16x64_bf16 v[168:171], v[100:103], v[208:215], v234\n\t" \
  ".p2align 3\n\t" \
  "s_waitcnt vmcnt(8) lgkmcnt(0)\n\t" \
  "s_barrier\n\t" \
  "ds_read_b64 v[200:201], v232 offset:16400\n\t" \
  "s_add_u32 m0, s25, 56880\n\t" \
  "ds_read_b64 v[208:209], v232 offset:16408\n\t" \
  "v_smfmac_f32_16x16x64_bf16 v[184:187], v[112:115], v[216:223], v234\n\t" \
  "ds_read_b64 v[206:207], v232 offset:17600\n\t" \
  "ds_read_b64 v[204:205], v232 offset:17200\n\t" \
  "v_smfmac_f32_16x16x64_bf16 v[160:163], v[120:123], v[216:223], v234\n\t" \
  "ds_read_b64 v[202:203], v232 offset:16800\n\t" \
  "s_setprio 0\n\t" \
  "v_smfmac_f32_16x16x64_bf16 v[148:151], v[124:127], v[216:223], v234\n\t" \
  "s_add_u32 s24, s24, s26\n\t" \
  "buffer_load_dwordx4 v254, s[8:11], s24 offen lds\n\t" \
  "v_cvt_pk_bf16_f32 v144, v144, v145\n\t" \
  "v_cvt_pk_bf16_f32 v145, v146, v147\n\t" \
  "v_smfmac_f32_16x16x64_bf16 v[164:167], v[136:139], v[224:231], v234\n\t" \
  "ds_read_b64 v[216:217], v232 offset:16416\n\t" \
  "v_smfmac_f32_16x16x64_bf16 v[176:179], v[132:135], v[224:231], v234\n\t" \
  "s_add_u32 m0, s25, 65080\n\t" \
  "ds_read_b64 v[212:213], v232 offset:17208\n\t" \
  "ds_read_b64 v[210:211], v232 offset:16808\n\t" \
  "s_add_u32 s24, s24, s26\n\t" \
  "v_cvt_pk_bf16_f32 v148, v148, v149\n\t" \
  "v_cvt_pk_bf16_f32 v149, v150, v151\n\t" \
  "v_smfmac_f32_16x16x64_bf16 v[188:191], v[128:131], v[224:231], v234\n\t" \
  "ds_read_b64 v[220:221], v232 offset:17216\n\t" \
  "ds_read_b64 v[214:215], v232 offset:17608\n\t" \
  "v_smfmac_f32_16x16x64_bf16 v[152:155], v[140:143], v[224:231], v234\n\t" \
  "buffer_load_dwordx4 v254, s[8:11], s24 offen lds\n\t" \
  "s_waitcnt lgkmcnt(5)\n\t" \
  "ds_read_b64 v[222:223], v232 offset:17616\n\t" \
  "ds_read_b64 v[218:219], v232 offset:16816\n\t" \
  "v_smfmac_f32_16x16x64_bf16 v[0:3], v[96:99], v[200:207], v234\n\t" \
  "s_add_u32 s24, s24, s26\n\t" \
  "v_smfmac_f32_16x16x64_bf16 v[180:183], v[100:103], v[200:207], v234\n\t" \
  "s_nop 0\n\t" \
  "v_cvt_pk_bf16_f32 v152, v152, v153\n\t" \
  "v_cvt_pk_bf16_f32 v153, v154, v155\n\t" \
  "v_mfma_f32_16x16x32_bf16 v[32:35], v[242:245], v[240:243], 0\n\t" \
  ".p2align 3\n\t" \
  "s_waitcnt vmcnt(8) lgkmcnt(0)\n\t" \
  "s_barrier\n\t" \
  "s_add_u32 m0, s25, 73280\n\t" \
  "s_nop 0\n\t" \
  "buffer_load_dwordx4 v254, s[8:11], s24 offen lds\n\t" \
  "v_smfmac_f32_16x16x64_bf16 v[156:159], v[108:111], v[200:207], v234\n\t" \
  "s_add_u32 s24, s24, s26\n\t" \
  "v_smfmac_f32_16x16x64_bf16 v[4:7], v[112:115], v[208:215], v234\n\t" \
  "ds_read_b64 v[224:225], v232 offset:32800\n\t" \
  "s_add_u32 m0, s25, 81480\n\t" \
  "ds_read_b64 v[226:227], v232 offset:33200\n\t" \
  "buffer_load_dwordx4 v254, s[8:11], s24 offen lds\n\t" \
  "v_smfmac_f32_16x16x64_bf16 v[160:163], v[124:127], v[208:215], v234\n\t" \
  "ds_read_b64 v[230:231], v232 offset:34000\n\t" \
  "s_setprio 1\n\t" \
  "v_smfmac_f32_16x16x64_bf16 v[168:171], v[104:107], v[200:207], v234\n\t" \
  "ds_read_b64 v[202:203], v232 offset:33208\n\t" \
  "v_smfmac_f32_16x16x64_bf16 v[172:175], v[120:123], v[208:215], v234\n\t" \
  "ds_read_b64 v[228:229], v232 offset:33600\n\t" \
  "v_smfmac_f32_16x16x64_bf16 v[8:11], v[128:131], v[216:223], v234\n\t" \
  "s_nop 0\n\t" \
  "v_cvt_pk_bf16_f32 v160, v160, v161\n\t" \
  "v_smfmac_f32_16x16x64_bf16 v[184:187], v[116:119], v[208:215], v234\n\t" \
  "ds_read_b64 v[208:209], v232 offset:32816\n\t" \
  "v_cvt_pk_bf16_f32 v156, v156, v157\n\t" \
  "v_smfmac_f32_16x16x64_bf16 v[164:167], v[140:143], v[216:223], v234\n\t" \
  "ds_read_b64 v[210:211], v232 offset:33216\n\t" \
  "v_cvt_pk_bf16_f32 v157, v158, v159\n\t" \
  "v_smfmac_f32_16x16x64_bf16 v[188:191], v[132:135], v[216:223], v234\n\t" \
  "ds_read_b64 v[206:207], v232 offset:34008\n\t" \
  "buffer_store_dwordx4 v[76:79], v250, s[20:23], s29 offen offset:0 sc1\n\t" \
  "ds_read_b64 v[212:213], v232 offset:33616\n\t" \
  "v_cvt_pk_bf16_f32 v161, v162, v163\n\t" \
  "v_smfmac_f32_16x16x64_bf16 v[176:179], v[136:139], v[216:223], v234\n\t" \
  "s_waitcnt lgkmcnt(4)\n\t" \
  "ds_read_b64 v[214:215], v232 offset:34016\n\t" \
  "ds_read_b64 v[204:205], v232 offset:33608\n\t" \
  "v_smfmac_f32_16x16x64_bf16 v[12:15], v[96:99], v[224:231], v234\n\t" \
  "ds_read_b64 v[200:201], v232 offset:32808\n\t" \
  "v_cvt_pk_bf16_f32 v164, v164, v165\n\t" \
  "v_smfmac_f32_16x16x64_bf16 v[180:183], v[104:107], v[224:231], v234\n\t" \
  "s_setprio 0\n\t" \
  "v_cvt_pk_bf16_f32 v165, v166, v167\n\t" \
  "v_smfmac_f32_16x16x64_bf16 v[168:171], v[108:111], v[224:231], v234\n\t" \
  "v_smfmac_f32_16x16x64_bf16 v[0:3], v[100:103], v[224:231], v234\n\t" \
  ".p2align 3\n\t" \
  "s_waitcnt vmcnt(9) lgkmcnt(0)\n\t" \
  "s_barrier\n\t" \
  "ds_read_b64 v[228:229], v253 offset:808\n\t" \
  "v_smfmac_f32_16x16x64_bf16 v[176:179], v[140:143], v[208:215], v234\n\t" \
  "ds_write_b64 v193, v[148:149] offset:16384\n\t" \
  "v_smfmac_f32_16x16x64_bf16 v[4:7], v[116:119], v[200:207], v234\n\t" \
  "v_smfmac_f32_16x16x64_bf16 v[184:187], v[120:123], v[200:207], v234\n\t" \
  "ds_read_b64 v[220:221], v253 offset:800\n\t" \
  "v_smfmac_f32_16x16x64_bf16 v[16:19], v[112:115], v[200:207], v234\n\t" \
  "ds_read_b64 v[216:217], v253 offset:0\n\t" \
  "v_cvt_pk_bf16_f32 v168, v168, v169\n\t" \
  "buffer_store_dwordx4 v[84:87], v250, s[20:23], s29 offen offset:64 sc1\n\t" \
  "ds_read_b64 v[224:225], v253 offset:8\n\t" \
  "ds_read_b64 v[222:223], v253 offset:1200\n\t" \
  "v_cvt_pk_bf16_f32 v169, v170, v171\n\t" \
  "ds_read_b64 v[226:227], v253 offset:408\n\t" \
  "ds_read_b64 v[218:219], v253 offset:400\n\t" \
  "ds_write_b64 v193, v[152:153] offset:20480\n\t" \
  "v_smfmac_f32_16x16x64_bf16 v[172:175], v[124:127], v[200:207], v234\n\t" \
  "ds_read_b64 v[204:205], v253 offset:816\n\t" \
  "ds_read_b64 v[200:201], v253 offset:16\n\t" \
  "v_mfma_f32_16x16x32_bf16 v[76:79], v[242:245], v[240:243], 0\n\t" \
  "ds_read_b64 v[230:231], v253 offset:1208\n\t" \
  "ds_read_b64 v[202:203], v253 offset:416\n\t" \
  "v_smfmac_f32_16x16x64_bf16 v[20:23], v[128:131], v[208:215], v234\n\t" \
  "s_nop 1\n\t" \
  "v_cvt_pk_bf16_f32 v172, v172, v173\n\t" \
  "v_cvt_pk_bf16_f32 v173, v174, v175\n\t" \
  "v_smfmac_f32_16x16x64_bf16 v[8:11], v[132:135], v[208:215], v234\n\t" \
  "ds_write_b64 v193, v[160:161] offset:4096\n\t" \
  "v_mfma_f32_16x16x32_bf16 v[84:87], v[242:245], v[240:243], 0\n\t" \
  "s_waitcnt lgkmcnt(6)\n\t" \
  "v_cvt_pk_bf16_f32 v176, v176, v177\n\t" \
  "s_setprio 1\n\t" \
  "v_smfmac_f32_16x16x64_bf16 v[0:3], v[104:107], v[216:223], v234\n\t" \
  "ds_read_b64 v[206:207], v253 offset:1216\n\t" \
  "ds_write_b64 v193, v[156:157] offset:0\n\t" \
  "v_smfmac_f32_16x16x64_bf16 v[12:15], v[100:103], v[216:223], v234\n\t" \
  "s_setprio 0\n\t" \
  "s_waitcnt lgkmcnt(4)\n\t" \
  "v_smfmac_f32_16x16x64_bf16 v[28:31], v[112:115], v[224:231], v234\n\t" \
  "ds_write_b64 v193, v[164:165] offset:8192\n\t" \
  "ds_write_b64 v193, v[144:145] offset:12288\n\t" \
  "v_smfmac_f32_16x16x64_bf16 v[188:191], v[136:139], v[208:215], v234\n\t" \
  "v_smfmac_f32_16x16x64_bf16 v[24:27], v[96:99], v[216:223], v234\n\t" \
  "v_cvt_pk_bf16_f32 v177, v178, v179\n\t" \
  "v_smfmac_f32_16x16x64_bf16 v[180:183], v[108:111], v[216:223], v234\n\t" \
  "s_setprio 0\n\t" \
  ".p2align 3\n\t" \
  "s_waitcnt vmcnt(8) lgkmcnt(0)\n\t" \
  "s_barrier\n\t" \
  "ds_read_b64 v[208:209], v253 offset:16400\n\t" \
  "ds_write_b64 v192, v[168:169] offset:0\n\t" \
  "buffer_store_dwordx4 v[92:95], v250, s[20:23], s29 offen offset:128 sc1\n\t" \
  "ds_read_b64 v[212:213], v253 offset:17200\n\t" \
  "ds_read_b128 v[144:147], v194 offset:0\n\t" \
  "v_smfmac_f32_16x16x64_bf16 v[16:19], v[116:119], v[224:231], v234\n\t" \
  "v_cvt_pk_bf16_f32 v180, v180, v181\n\t" \
  "v_cvt_pk_bf16_f32 v181, v182, v183\n\t" \
  "ds_read_b64 v[216:217], v253 offset:16408\n\t" \
  "ds_read_b64 v[214:215], v253 offset:17600\n\t" \
  "v_smfmac_f32_16x16x64_bf16 v[184:187], v[124:127], v[224:231], v234\n\t" \
  "ds_read_b64 v[210:211], v253 offset:16800\n\t" \
  "ds_write_b64 v192, v[172:173] offset:4096\n\t" \
  "ds_read_b128 v[152:155], v194 offset:8192\n\t" \
  "v_smfmac_f32_16x16x64_bf16 v[4:7], v[120:123], v[224:231], v234\n\t" \
  "ds_write_b64 v192, v[176:177] offset:8192\n\t" \
  "v_mfma_f32_16x16x32_bf16 v[92:95], v[242:245], v[240:243], 0\n\t" \
  "s_waitcnt lgkmcnt(6)\n\t" \
  "ds_read_b128 v[148:151], v194 offset:4096\n\t" \
  "v_add_u32 v194, -93792, v194\n\t" \
  "v_smfmac_f32_16x16x64_bf16 v[32:35], v[128:131], v[200:207], v234\n\t" \
  "ds_read_b64 v[220:221], v253 offset:17208\n\t" \
  "ds_read_b64 v[218:219], v253 offset:16808\n\t" \
  "v_mfma_f32_16x16x32_bf16 v[168:171], v[144:147], v[144:147], 0\n\t" \
  "ds_read_b64 v[226:227], v253 offset:16816\n\t" \
  "v_cvt_pk_bf16_f32 v184, v184, v185\n\t" \
  "v_mfma_f32_16x16x32_bf16 v[172:175], v[196:199], v[144:147], 0\n\t" \
  "ds_read_b64 v[224:225], v253 offset:16416\n\t" \
  "ds_read_b64 v[222:223], v253 offset:17608\n\t" \
  "v_smfmac_f32_16x16x64_bf16 v[8:11], v[136:139], v[200:207], v234\n\t" \
  "s_setprio 0\n\t" \
  "v_smfmac_f32_16x16x64_bf16 v[188:191], v[140:143], v[200:207], v234\n\t" \
  "v_smfmac_f32_16x16x64_bf16 v[20:23], v[132:135], v[200:207], v234\n\t" \
  "ds_read_b64 v[230:231], v253 offset:17616\n\t" \
  "ds_read_b64_tr_b16 v[156:157], v195 offset:0\n\t" \
  "s_waitcnt lgkmcnt(7)\n\t" \
  "v_mfma_f32_16x16x32_bf16 v[168:171], v[148:151], v[148:151], v[168:171]\n\t" \
  "ds_read_b64_tr_b16 v[164:165], v195 offset:8192\n\t" \
  "v_mfma_f32_16x16x32_bf16 v[168:171], v[152:155], v[152:155], v[168:171]\n\t" \
  "s_setprio 0\n\t" \
  "ds_read_b64_tr_b16 v[160:161], v195 offset:2048\n\t" \
  "v_smfmac_f32_16x16x64_bf16 v[76:79], v[96:99], v[208:215], v234\n\t" \
  "s_setprio 0\n\t" \
  "ds_read_b64_tr_b16 v[158:159], v195 offset:4096\n\t" \
  "ds_read_b64 v[228:229], v253 offset:17216\n\t" \
  "v_mfma_f32_16x16x32_bf16 v[172:175], v[196:199], v[148:151], v[172:175]\n\t" \
  "s_setprio 0\n\t" \
  "ds_read_b64_tr_b16 v[162:163], v195 offset:6144\n\t" \
  "s_setprio 0\n\t" \
  "v_smfmac_f32_16x16x64_bf16 v[24:27], v[100:103], v[208:215], v234\n\t" \
  "s_setprio 0\n\t" \
  "buffer_store_dwordx4 v[48:51], v250, s[20:23], s12 offen offset:0 sc1\n\t" \
  "ds_read_b64_tr_b16 v[166:167], v195 offset:10240\n\t" \
  "v_cvt_pk_bf16_f32 v185, v186, v187\n\t" \
  "v_cvt_pk_bf16_f32 v188, v188, v189\n\t" \
  "v_smfmac_f32_16x16x64_bf16 v[12:15], v[104:107], v[208:215], v234\n\t" \
  "v_cvt_pk_bf16_f32 v189, v190, v191\n\t" \
  "v_mfma_f32_16x16x32_bf16 v[172:175], v[196:199], v[152:155], v[172:175]\n\t" \
  "v_smfmac_f32_16x16x64_bf16 v[0:3], v[108:111], v[208:215], v234\n\t" \
  ".p2align 3\n\t" \
  "s_waitcnt vmcnt(7) lgkmcnt(0)\n\t" \
  "s_barrier\n\t" \
  "ds_read_b64 v[202:203], v253 offset:33200\n\t" \
  "v_smfmac_f32_16x16x64_bf16 v[84:87], v[112:115], v[216:223], v234\n\t" \
  "ds_read_b64 v[200:201], v253 offset:32800\n\t" \
  "ds_read_b64 v[48:49], v235 offset:0\n\t" \
  "ds_write_b64 v192, v[180:181] offset:12288\n\t" \
  "v_smfmac_f32_16x16x64_bf16 v[28:31], v[116:119], v[216:223], v234\n\t" \
  "s_setprio 0\n\t" \
  "ds_write_b64 v192, v[184:185] offset:16384\n\t" \
  "v_smfmac_f32_16x16x64_bf16 v[32:35], v[132:135], v[224:231], v234\n\t" \
  "s_setprio 0\n\t" \
  "v_cndmask_b32 v168, v168, v169, s[40:41]\n\t" \
  "v_cndmask_b32 v170, v170, v171, s[40:41]\n\t" \
  "v_cndmask_b32 v173, v168, v170, s[42:43]\n\t" \
  "v_cvt_pk_bf16_f32 v0, v0, v1\n\t" \
  "ds_read_b64 v[50:51], v235 offset:8\n\t" \
  "buffer_store_dwordx4 v[56:59], v250, s[20:23], s12 offen offset:64 sc1\n\t" \
  "v_smfmac_f32_16x16x64_bf16 v[16:19], v[120:123], v[216:223], v234\n\t" \
  "ds_read_b64 v[204:205], v253 offset:33600\n\t" \
  "ds_read_b64 v[58:59], v235 offset:72\n\t" \
  "ds_write_b64 v192, v[188:189] offset:20480\n\t" \
  "v_mov_b32 v174, 0x1127c5ac\n\t" \
  "v_cvt_pk_bf16_f32 v1, v2, v3\n\t" \
  "v_smfmac_f32_16x16x64_bf16 v[4:7], v[124:127], v[216:223], v234\n\t" \
  "ds_read_b64 v[222:223], v253 offset:34016\n\t" \
  "ds_read_b64 v[56:57], v235 offset:64\n\t" \
  "ds_read_b64 v[210:211], v253 offset:33208\n\t" \
  "v_smfmac_f32_16x16x64_bf16 v[20:23], v[136:139], v[224:231], v234\n\t" \
  "ds_read_b64 v[216:217], v253 offset:32816\n\t" \
  "ds_read_b64 v[206:207], v253 offset:34000\n\t" \
  "v_smfmac_f32_16x16x64_bf16 v[92:95], v[128:131], v[224:231], v234\n\t" \
  "ds_read_b64 v[212:213], v253 offset:33608\n\t" \
  "s_not_b64 vcc, s[52:53]\n\t" \
  "v_cvt_pk_bf16_f32 v4, v4, v5\n\t" \
  "v_add_u32 v195, -93792, v195\n\t" \
  "v_smfmac_f32_16x16x64_bf16 v[8:11], v[140:143], v[224:231], v234\n\t" \
  "ds_read_b64 v[218:219], v253 offset:33216\n\t" \
  "ds_read_b64 v[214:215], v253 offset:34008\n\t" \
  "v_cvt_pk_bf16_f32 v5, v6, v7\n\t" \
  "ds_read_b64 v[238:239], v235 offset:136\n\t" \
  "s_waitcnt lgkmcnt(4)\n\t" \
  "ds_read_b64 v[220:221], v253 offset:33616\n\t" \
  "ds_read_b64 v[236:237], v235 offset:128\n\t" \
  "ds_read_b64 v[208:209], v253 offset:32808\n\t" \
  "v_smfmac_f32_16x16x64_bf16 v[12:15], v[108:111], v[200:207], v234\n\t" \
  "v_cvt_pk_bf16_f32 v8, v8, v9\n\t" \
  "v_cvt_pk_bf16_f32 v9, v10, v11\n\t" \
  "v_smfmac_f32_16x16x64_bf16 v[76:79], v[100:103], v[200:207], v234\n\t" \
  "v_smfmac_f32_16x16x64_bf16 v[24:27], v[104:107], v[200:207], v234\n\t" \
  ".p2align 3\n\t" \
  "s_waitcnt vmcnt(7) lgkmcnt(0)\n\t" \
  "s_barrier\n\t" \
  "ds_read_b128 v[188:191], v194 offset:8192\n\t" \
  "ds_read_b128 v[184:187], v194 offset:4096\n\t" \
  "v_smfmac_f32_16x16x64_bf16 v[84:87], v[116:119], v[208:215], v234\n\t" \
  "ds_write_b64 v192, v[0:1] offset:24576\n\t" \
  "ds_read_b64 v[224:225], v232 offset:0\n\t" \
  "ds_read_b128 v[180:183], v194 offset:0\n\t" \
  "v_smfmac_f32_16x16x64_bf16 v[28:31], v[120:123], v[208:215], v234\n\t" \
  "ds_read_b64 v[230:231], v232 offset:1200\n\t" \
  "ds_write_b64 v192, v[4:5] offset:28672\n\t" \
  "v_smfmac_f32_16x16x64_bf16 v[32:35], v[136:139], v[216:223], v234\n\t" \
  "ds_read_b64 v[226:227], v232 offset:400\n\t" \
  "ds_read_b64 v[206:207], v232 offset:1208\n\t" \
  "ds_read_b64 v[228:229], v232 offset:800\n\t" \
  "v_smfmac_f32_16x16x64_bf16 v[16:19], v[124:127], v[208:215], v234\n\t" \
  "s_waitcnt lgkmcnt(5)\n\t" \
  "ds_write_b64 v192, v[8:9] offset:32768\n\t" \
  "v_mfma_f32_16x16x32_bf16 v[168:171], v[196:199], v[180:183], 0\n\t" \
  "v_mfma_f32_16x16x32_bf16 v[176:179], v[180:183], v[180:183], 0\n\t" \
  "ds_read_b64 v[200:201], v232 offset:8\n\t" \
  "ds_read_b64 v[212:213], v232 offset:816\n\t" \
  "ds_read_b64 v[208:209], v232 offset:16\n\t" \
  "v_mfma_f32_16x16x32_bf16 v[176:179], v[184:187], v[184:187], v[176:179]\n\t" \
  "ds_read_b64 v[214:215], v232 offset:1216\n\t" \
  "v_cvt_pk_bf16_f32 v12, v12, v13\n\t" \
  "v_cvt_pk_bf16_f32 v13, v14, v15\n\t" \
  "v_smfmac_f32_16x16x64_bf16 v[92:95], v[132:135], v[216:223], v234\n\t" \
  "s_setprio 0\n\t" \
  "ds_write_b64 v192, v[12:13] offset:36864\n\t" \
  "v_mfma_f32_16x16x32_bf16 v[168:171], v[196:199], v[184:187], v[168:171]\n\t" \
  "ds_read_b64 v[202:203], v232 offset:408\n\t" \
  "ds_read_b64 v[204:205], v232 offset:808\n\t" \
  "ds_read_b64 v[210:211], v232 offset:416\n\t" \
  "v_smfmac_f32_16x16x64_bf16 v[20:23], v[140:143], v[216:223], v234\n\t" \
  "v_cvt_pk_bf16_f32 v16, v16, v17\n\t" \
  "v_cvt_pk_bf16_f32 v17, v18, v19\n\t" \
  "v_mfma_f32_16x16x32_bf16 v[176:179], v[188:191], v[188:191], v[176:179]\n\t" \
  "buffer_store_dwordx4 v[64:67], v250, s[20:23], s12 offen offset:128 sc1\n\t" \
  "ds_write_b64 v192, v[16:17] offset:40960\n\t" \
  "v_mfma_f32_16x16x32_bf16 v[168:171], v[196:199], v[188:191], v[168:171]\n\t" \
  "v_mov_b32 v183, 0\n\t" \
  "s_waitcnt lgkmcnt(10)\n\t" \
  "v_smfmac_f32_16x16x64_bf16 v[76:79], v[104:107], v[224:231], v234\n\t" \
  "s_nop 1\n\t" \
  "v_cndmask_b32 v178, v178, v179, s[40:41]\n\t" \
  "v_cndmask_b32 v176, v176, v177, s[40:41]\n\t" \
  "v_mov_b32 v187, 0\n\t" \
  "v_cndmask_b32 v176, v176, v178, s[42:43]\n\t" \
  "v_smfmac_f32_16x16x64_bf16 v[24:27], v[108:111], v[224:231], v234\n\t" \
  ".p2align 3\n\t" \
  "s_waitcnt vmcnt(6) lgkmcnt(0)\n\t" \
  "s_barrier\n\t" \
  "ds_read_b64 v[216:217], v232 offset:16400\n\t" \
  "v_smfmac_f32_16x16x64_bf16 v[84:87], v[120:123], v[200:207], v234\n\t" \
  "ds_read_b64 v[220:221], v232 offset:17200\n\t" \
  "v_cndmask_b32_dpp v173, v176, v173, vcc row_ror:8 row_mask:0xf bank_mask:0xf\n\t" \
  "v_fmamk_f32 v173, v173, 0x3e2aaaab, v174\n\t" \
  "v_cndmask_b32_dpp v172, v168, v172, vcc row_ror:8 row_mask:0xf bank_mask:0xf\n\t" \
  "v_mul_f32 v172, 0x3daaaaab, v172\n\t" \
  "ds_read_b64 v[222:223], v232 offset:17600\n\t" \
  "ds_read_b64 v[224:225], v232 offset:16408\n\t" \
  "ds_read_b64 v[218:219], v232 offset:16800\n\t" \
  "ds_read_b64 v[226:227], v232 offset:16808\n\t" \
  "v_cvt_pk_bf16_f32 v20, v20, v21\n\t" \
  "v_cvt_pk_bf16_f32 v21, v22, v23\n\t" \
  "v_fma_f32 v173, -v172, v172, v173\n\t" \
  "v_max_f32 v173, v174, v173\n\t" \
  "v_rsq_f32 v173, v173 div:2\n\t" \
  "s_mov_b64 exec, s[52:53]\n\t" \
  "v_mul_f32 v173, 0x08805555, v173\n\t" \
  "s_mov_b64 exec, -1\n\t" \
  "v_smfmac_f32_16x16x64_bf16 v[28:31], v[124:127], v[200:207], v234\n\t" \
  "s_mov_b64 vcc, s[40:41]\n\t" \
  "ds_read_b64 v[228:229], v232 offset:17208\n\t" \
  "ds_write_b64 v192, v[20:21] offset:45056\n\t" \
  "v_cvt_pk_bf16_f32 v175, v173, v173\n\t" \
  "v_and_b32 v173, 0xffff0000, v175\n\t" \
  "v_smfmac_f32_16x16x64_bf16 v[92:95], v[136:139], v[208:215], v234\n\t" \
  "ds_read_b64 v[230:231], v232 offset:17608\n\t" \
  "ds_read_b64 v[200:201], v232 offset:16416\n\t" \
  "v_cvt_pk_bf16_f32 v24, v24, v25\n\t" \
  "v_mul_f32 v174, -v172, v173 div:2\n\t" \
  "v_and_b32 v172, 0xffff0000, v174\n\t" \
  "v_cvt_pk_bf16_f32 v25, v26, v27\n\t" \
  "v_cndmask_b32_sdwa v175, v175, v173, vcc dst_sel:DWORD dst_unused:UNUSED_PAD src0_sel:WORD_1 src1_sel:DWORD\n\t" \
  "v_smfmac_f32_16x16x64_bf16 v[32:35], v[140:143], v[208:215], v234\n\t" \
  "ds_read_b64 v[202:203], v232 offset:16816\n\t" \
  "s_or_b64 vcc, s[52:53], s[42:43]\n\t" \
  "ds_write_b64 v192, v[24:25] offset:56880\n\t" \
  "v_cndmask_b32_dpp v184, v175, v240, vcc row_ror:8 row_mask:0xf bank_mask:0xf\n\t" \
  "v_sub_f32 v174, v174, v172\n\t" \
  "v_perm_b32 v174, v172, v174, s55\n\t" \
  "v_cndmask_b32 v180, v175, v240, vcc\n\t" \
  "s_mov_b64 vcc, s[52:53]\n\t" \
  "v_cndmask_b32 v182, v174, v240, vcc\n\t" \
  "ds_read_b64_tr_b16 v[146:147], v195 offset:4096\n\t" \
  "ds_read_b64_tr_b16 v[144:145], v195 offset:0\n\t" \
  "s_waitcnt lgkmcnt(9)\n\t" \
  "v_smfmac_f32_16x16x64_bf16 v[76:79], v[108:111], v[216:223], v234\n\t" \
  "ds_read_b64 v[206:207], v232 offset:17616\n\t" \
  "ds_read_b64 v[204:205], v232 offset:17216\n\t" \
  "v_cvt_pk_bf16_f32 v28, v28, v29\n\t" \
  "s_setprio 2\n\t" \
  "v_cndmask_b32_dpp v186, v174, v240, vcc row_ror:8 row_mask:0xf bank_mask:0xf\n\t" \
  "s_orn2_b64 vcc, s[52:53], s[42:43]\n\t" \
  "v_cvt_pk_bf16_f32 v29, v30, v31\n\t" \
  "v_cndmask_b32_dpp v185, v175, v240, vcc row_ror:8 row_mask:0xf bank_mask:0xf\n\t" \
  "v_cndmask_b32 v181, v175, v240, vcc\n\t" \
  "v_cvt_pk_bf16_f32 v76, v76, v77\n\t" \
  "v_mfma_f32_16x16x16_bf16 v[0:3], v[196:197], v[182:183], 0\n\t" \
  "s_setprio 3\n\t" \
  "ds_read_b64_tr_b16 v[154:155], v195 offset:10240\n\t" \
  "ds_read_b64_tr_b16 v[150:151], v195 offset:6144\n\t" \
  "s_waitcnt lgkmcnt(2)\n\t" \
  "v_smfmac_f32_16x16x64_bf16 v[92:95], v[140:143], v[200:207], v234\n\t" \
  "s_setprio 3\n\t" \
  "ds_read_b64_tr_b16 v[148:149], v195 offset:2048\n\t" \
  "ds_read_b64_tr_b16 v[152:153], v195 offset:8192\n\t" \
  "v_smfmac_f32_16x16x64_bf16 v[84:87], v[124:127], v[224:231], v234\n\t" \
  "ds_write_b64 v192, v[28:29] offset:60976\n\t" \
  "v_mfma_f32_16x16x16_bf16 v[4:7], v[196:197], v[186:187], 0\n\t" \
  "v_cvt_pk_bf16_f32 v32, v32, v33\n\t" \
  "v_cvt_pk_bf16_f32 v77, v78, v79\n\t" \
  "v_mfma_f32_16x16x16_bf16 v[188:191], v[156:157], v[180:181], v[0:3]\n\t" \
  "v_cvt_pk_bf16_f32 v33, v34, v35\n\t" \
  "s_nop 1\n\t" \
  "v_cvt_pk_bf16_f32 v84, v84, v85\n\t" \
  "v_cvt_pk_bf16_f32 v85, v86, v87\n\t" \
  "s_setprio 0\n\t" \
  "v_mfma_f32_16x16x16_bf16 v[168:171], v[160:161], v[180:181], v[0:3]\n\t" \
  "ds_write_b64 v193, v[84:85] offset:4096\n\t" \
  "ds_write_b64 v192, v[32:33] offset:65072\n\t" \
  "v_mfma_f32_16x16x16_bf16 v[176:179], v[158:159], v[180:181], v[0:3]\n\t" \
  "ds_write_b64 v193, v[76:77] offset:0\n\t" \
  "v_cvt_pk_bf16_f32 v97, v190, v191\n\t" \
  "v_cvt_pk_bf16_f32 v92, v92, v93\n\t" \
  "v_cvt_pk_bf16_f32 v93, v94, v95\n\t" \
  "v_mfma_f32_16x16x16_bf16 v[172:175], v[162:163], v[180:181], v[0:3]\n\t" \
  "s_and_b32 s69, s69, 0xffff\n\t" \
  "ds_write_b64 v193, v[92:93] offset:8192\n\t" \
  "v_cvt_pk_bf16_f32 v96, v188, v189\n\t" \
  "v_mfma_f32_16x16x16_bf16 v[188:191], v[164:165], v[180:181], v[0:3]\n\t" \
  "v_cvt_pk_bf16_f32 v112, v168, v169\n\t" \
  "v_cvt_pk_bf16_f32 v98, v176, v177\n\t" \
  "v_cvt_pk_bf16_f32 v99, v178, v179\n\t" \
  "v_mfma_f32_16x16x16_bf16 v[176:179], v[166:167], v[180:181], v[0:3]\n\t" \
  "v_cvt_pk_bf16_f32 v115, v174, v175\n\t" \
  "s_nop 2\n\t" \
  "v_cvt_pk_bf16_f32 v128, v188, v189\n\t" \
  "v_cvt_pk_bf16_f32 v113, v170, v171\n\t" \
  "s_nop 1\n\t" \
  "v_cvt_pk_bf16_f32 v131, v178, v179\n\t" \
  ".p2align 3\n\t" \
  "s_waitcnt lgkmcnt(0)\n\t" \
  "s_barrier\n\t" \
  "ds_read_b64_tr_b16 v[208:209], v248 offset:43048\n\t" \
  "s_mov_b32 m0, s44\n\t" \
  "ds_read_b128 v[104:107], v194 offset:24576\n\t" \
  "ds_read_b128 v[120:123], v194 offset:28672\n\t" \
  "v_cvt_pk_bf16_f32 v114, v172, v173\n\t" \
  "ds_read_b64_tr_b16 v[210:211], v248 offset:44072\n\t" \
  "s_mov_b32 exec_hi, 0xffff\n\t" \
  "buffer_load_dwordx4 v252, s[16:19], s33 offen lds\n\t" \
  "s_mov_b32 exec_hi, -1\n\t" \
  "v_cvt_pk_bf16_f32 v130, v176, v177\n\t" \
  "s_add_u32 s33, s33, s32\n\t" \
  "s_setprio 0\n\t" \
  "ds_read_b64_tr_b16 v[218:219], v248 offset:48168\n\t" \
  "ds_read_b128 v[136:139], v194 offset:32768\n\t" \
  "v_add_u32 v194, s54, v194\n\t" \
  "s_setprio 0\n\t" \
  "ds_read_b128 v[108:111], v194 offset:49152\n\t" \
  "ds_read_b64_tr_b16 v[214:215], v248 offset:47144\n\t" \
  "v_mfma_f32_16x16x16_bf16 v[172:175], v[146:147], v[184:185], v[4:7]\n\t" \
  "ds_read_b64_tr_b16 v[216:217], v248 offset:45096\n\t" \
  "v_cvt_pk_bf16_f32 v129, v190, v191\n\t" \
  "v_mfma_f32_16x16x16_bf16 v[168:171], v[144:145], v[184:185], v[4:7]\n\t" \
  "ds_read_b64_tr_b16 v[160:161], v248 offset:43560\n\t" \
  "ds_read_b64_tr_b16 v[162:163], v248 offset:44584\n\t" \
  "v_mfma_f32_16x16x16_bf16 v[188:191], v[148:149], v[184:185], v[4:7]\n\t" \
  "ds_read_b128 v[140:143], v194 offset:57344\n\t" \
  "ds_read_b64_tr_b16 v[212:213], v248 offset:46120\n\t" \
  "s_nop 2\n\t" \
  "v_cvt_pk_bf16_f32 v101, v170, v171\n\t" \
  "v_cvt_pk_bf16_f32 v100, v168, v169\n\t" \
  "v_mfma_f32_16x16x16_bf16 v[176:179], v[150:151], v[184:185], v[4:7]\n\t" \
  "ds_read_b64_tr_b16 v[164:165], v248 offset:46632\n\t" \
  "ds_read_b64_tr_b16 v[166:167], v248 offset:47656\n\t" \
  "v_cvt_pk_bf16_f32 v103, v174, v175\n\t" \
  "v_cvt_pk_bf16_f32 v102, v172, v173\n\t" \
  "v_mfma_f32_16x16x16_bf16 v[168:171], v[152:153], v[184:185], v[4:7]\n\t" \
  "ds_read_b64_tr_b16 v[232:233], v248 offset:45608\n\t" \
  "v_cvt_pk_bf16_f32 v116, v188, v189\n\t" \
  "ds_read_b64_tr_b16 v[234:235], v248 offset:48680\n\t" \
  "v_cvt_pk_bf16_f32 v117, v190, v191\n\t" \
  "v_mfma_f32_16x16x16_bf16 v[172:175], v[154:155], v[184:185], v[4:7]\n\t" \
  "ds_read_b64_tr_b16 v[24:25], v195 offset:24576\n\t" \
  "ds_read_b64_tr_b16 v[26:27], v195 offset:28672\n\t" \
  "v_cvt_pk_bf16_f32 v118, v176, v177\n\t" \
  "v_cvt_pk_bf16_f32 v119, v178, v179\n\t" \
  "s_waitcnt lgkmcnt(15)\n\t" \
  "v_mfma_f32_16x16x32_bf16 v[144:147], v[208:211], v[96:99], 0\n\t" \
  "s_waitcnt lgkmcnt(13)\n\t" \
  "ds_read_b64_tr_b16 v[30:31], v195 offset:30720\n\t" \
  "ds_read_b128 v[124:127], v194 offset:53248\n\t" \
  "ds_read_b64_tr_b16 v[28:29], v195 offset:26624\n\t" \
  "v_cvt_pk_bf16_f32 v133, v170, v171\n\t" \
  "v_cvt_pk_bf16_f32 v132, v168, v169\n\t" \
  "v_cvt_pk_bf16_f32 v135, v174, v175\n\t" \
  "s_waitcnt lgkmcnt(9)\n\t" \
  "v_mfma_f32_16x16x32_bf16 v[144:147], v[212:215], v[112:115], v[144:147]\n\t" \
  "ds_read_b64_tr_b16 v[32:33], v195 offset:32768\n\t" \
  "v_cvt_pk_bf16_f32 v134, v172, v173\n\t" \
  "v_mfma_f32_16x16x32_bf16 v[152:155], v[208:211], v[100:103], 0\n\t" \
  "ds_read_b64_tr_b16 v[34:35], v195 offset:34816\n\t" \
  "v_add_u32 v195, s54, v195\n\t" \
  "v_mfma_f32_16x16x32_bf16 v[144:147], v[216:219], v[128:131], v[144:147]\n\t" \
  "ds_read_b64_tr_b16 v[76:77], v195 offset:49152\n\t" \
  "ds_read_b64_tr_b16 v[78:79], v195 offset:53248\n\t" \
  "s_add_u32 m0, s44, s35\n\t" \
  "ds_read_b64_tr_b16 v[84:85], v195 offset:51200\n\t" \
  "s_mov_b32 exec_hi, 0xffff\n\t" \
  "buffer_load_dwordx4 v252, s[16:19], s34 offen lds\n\t" \
  "s_mov_b32 exec_hi, -1\n\t" \
  "v_mfma_f32_16x16x32_bf16 v[152:155], v[212:215], v[116:119], v[152:155]\n\t" \
  "s_add_u32 s34, s34, s32\n\t" \
  "ds_read_b64_tr_b16 v[86:87], v195 offset:55296\n\t" \
  "v_mfma_f32_16x16x32_bf16 v[8:11], v[104:107], v[104:107], 0\n\t" \
  "ds_read_b64_tr_b16 v[94:95], v195 offset:59392\n\t" \
  "s_mov_b32 m0, s45\n\t" \
  "s_mov_b32 exec_hi, 0xffff\n\t" \
  "buffer_load_dwordx4 v252, s[16:19], s33 offen lds\n\t" \
  "s_mov_b32 exec_hi, -1\n\t" \
  "v_mfma_f32_16x16x32_bf16 v[12:15], v[196:199], v[104:107], 0\n\t" \
  "v_cvt_pknorm_i16_f32 v220, |v146|, |v147|\n\t" \
  "v_mfma_f32_16x16x32_bf16 v[152:155], v[216:219], v[132:135], v[152:155]\n\t" \
  "ds_read_b64_tr_b16 v[92:93], v195 offset:57344\n\t" \
  "v_mfma_f32_16x16x32_bf16 v[8:11], v[120:123], v[120:123], v[8:11]\n\t" \
  "v_cvt_pknorm_i16_f32 v222, |v144|, |v145|\n\t" \
  "s_add_u32 s33, s33, s32\n\t" \
  "v_mfma_f32_16x16x32_bf16 v[12:15], v[196:199], v[120:123], v[12:15]\n\t" \
  "v_lshlrev_b32_sdwa v221, 2, v220 dst_sel:DWORD dst_unused:UNUSED_PAD src0_sel:DWORD src1_sel:BYTE_3\n\t" \
  "v_lshlrev_b32_sdwa v220, 2, v220 dst_sel:DWORD dst_unused:UNUSED_PAD src0_sel:DWORD src1_sel:BYTE_1\n\t" \
  "ds_read_b32 v221, v221 offset:10016\n\t" \
  "v_lshlrev_b32_sdwa v223, 2, v222 dst_sel:DWORD dst_unused:UNUSED_PAD src0_sel:DWORD src1_sel:BYTE_3\n\t" \
  "v_lshlrev_b32_sdwa v222, 2, v222 dst_sel:DWORD dst_unused:UNUSED_PAD src0_sel:DWORD src1_sel:BYTE_1\n\t" \
  "v_mfma_f32_16x16x32_bf16 v[8:11], v[136:139], v[136:139], v[8:11]\n\t" \
  "ds_read_b32 v220, v220 offset:10016\n\t" \
  "v_lshlrev_b32_sdwa v225, 1, v155 dst_sel:DWORD dst_unused:UNUSED_PAD src0_sel:DWORD src1_sel:WORD_1\n\t" \
  "v_mfma_f32_16x16x32_bf16 v[20:23], v[196:199], v[108:111], 0\n\t" \
  "ds_read_u16_d16_hi v225, v225 offset:10528\n\t" \
  "s_add_u32 m0, s45, s35\n\t" \
  "v_lshlrev_b32_sdwa v224, 1, v154 dst_sel:DWORD dst_unused:UNUSED_PAD src0_sel:DWORD src1_sel:WORD_1\n\t" \
  "v_mfma_f32_16x16x32_bf16 v[12:15], v[196:199], v[136:139], v[12:15]\n\t" \
  "ds_read_b32 v223, v223 offset:10016\n\t" \
  "v_lshlrev_b32_sdwa v226, 1, v152 dst_sel:DWORD dst_unused:UNUSED_PAD src0_sel:DWORD src1_sel:WORD_1\n\t" \
  "v_lshlrev_b32_sdwa v227, 1, v153 dst_sel:DWORD dst_unused:UNUSED_PAD src0_sel:DWORD src1_sel:WORD_1\n\t" \
  "v_cndmask_b32 v8, v8, v9, s[40:41]\n\t" \
  "v_mfma_f32_16x16x32_bf16 v[16:19], v[108:111], v[108:111], 0\n\t" \
  "ds_read_b32 v222, v222 offset:10016\n\t" \
  "s_mov_b32 exec_hi, 0xffff\n\t" \
  "buffer_load_dwordx4 v252, s[16:19], s34 offen lds\n\t" \
  "s_mov_b32 exec_hi, -1\n\t" \
  "s_add_u32 s34, s34, s32\n\t" \
  "v_cndmask_b32 v10, v10, v11, s[40:41]\n\t" \
  "s_waitcnt lgkmcnt(14)\n\t" \
  "v_mfma_f32_16x16x32_bf16 v[16:19], v[124:127], v[124:127], v[16:19]\n\t" \
  "ds_read_u16 v224, v224 offset:10528\n\t" \
  "v_cndmask_b32 v13, v8, v10, s[42:43]\n\t" \
  "v_mov_b32 v14, 0x1127c5ac\n\t" \
  "v_mfma_f32_16x16x32_bf16 v[20:23], v[196:199], v[124:127], v[20:23]\n\t" \
  "ds_read_u16 v226, v226 offset:10528\n\t" \
  "ds_read_u16_d16_hi v227, v227 offset:10528\n\t" \
  "s_not_b64 vcc, s[52:53]\n\t" \
  "v_mfma_f32_16x16x32_bf16 v[16:19], v[140:143], v[140:143], v[16:19]\n\t" \
  "s_waitcnt lgkmcnt(3)\n\t" \
  "v_fma_mix_f32 v221, |v147|, v221, v221 op_sel:[0,1,0] op_sel_hi:[0,1,1] clamp\n\t" \
  "v_fma_mix_f32 v220, |v146|, v220, v220 op_sel:[0,1,0] op_sel_hi:[0,1,1] clamp\n\t" \
  "v_mfma_f32_16x16x32_bf16 v[20:23], v[196:199], v[140:143], v[20:23]\n\t" \
  "buffer_store_dwordx4 v[52:55], v250, s[20:23], s28 offen offset:0 sc1\n\t" \
  "v_fma_mix_f32 v223, |v145|, v223, v223 op_sel:[0,1,0] op_sel_hi:[0,1,1] clamp\n\t" \
  "v_fma_mix_f32 v222, |v144|, v222, v222 op_sel:[0,1,0] op_sel_hi:[0,1,1] clamp\n\t" \
  "v_mfma_f32_16x16x32_bf16 v[148:151], v[160:163], v[96:99], 0\n\t" \
  "v_cndmask_b32 v18, v18, v19, s[40:41]\n\t" \
  "v_cndmask_b32 v16, v16, v17, s[40:41]\n\t" \
  "v_mfma_f32_16x16x32_bf16 v[156:159], v[160:163], v[100:103], 0\n\t" \
  "s_waitcnt lgkmcnt(0)\n\t" \
  "v_or_b32 v155, v224, v225\n\t" \
  "v_or_b32 v154, v226, v227\n\t" \
  "v_cndmask_b32 v16, v16, v18, s[42:43]\n\t" \
  "v_cndmask_b32_dpp v12, v20, v12, vcc row_ror:8 row_mask:0xf bank_mask:0xf\n\t" \
  "v_mul_f32 v12, 0x3daaaaab, v12\n\t" \
  "v_mfma_f32_16x16x32_bf16 v[148:151], v[164:167], v[112:115], v[148:151]\n\t" \
  "v_cndmask_b32_dpp v13, v16, v13, vcc row_ror:8 row_mask:0xf bank_mask:0xf\n\t" \
  "v_fmamk_f32 v13, v13, 0x3e2aaaab, v14\n\t" \
  "v_fma_f32 v13, -v12, v12, v13\n\t" \
  "v_max_f32 v13, v14, v13\n\t" \
  "v_rsq_f32 v13, v13 div:2\n\t" \
  "v_mfma_f32_16x16x32_bf16 v[156:159], v[164:167], v[116:119], v[156:159]\n\t" \
  "s_mov_b64 exec, s[52:53]\n\t" \
  "v_mul_f32 v13, 0x08805555, v13\n\t" \
  "s_mov_b64 exec, -1\n\t" \
  "v_cvt_pk_bf16_f32 v15, v13, v13\n\t" \
  "v_and_b32 v13, 0xffff0000, v15\n\t" \
  "s_mov_b64 vcc, s[40:41]\n\t" \
  "v_cndmask_b32_sdwa v15, v15, v13, vcc dst_sel:DWORD dst_unused:UNUSED_PAD src0_sel:WORD_1 src1_sel:DWORD\n\t" \
  "v_mul_f32 v14, -v12, v13 div:2\n\t" \
  "v_mfma_f32_16x16x32_bf16 v[148:151], v[232:235], v[128:131], v[148:151]\n\t" \
  "s_or_b64 vcc, s[52:53], s[42:43]\n\t" \
  "v_cndmask_b32_dpp v184, v15, v240, vcc row_ror:8 row_mask:0xf bank_mask:0xf\n\t" \
  "v_and_b32 v12, 0xffff0000, v14\n\t" \
  "v_sub_f32 v14, v14, v12\n\t" \
  "v_cndmask_b32 v180, v15, v240, vcc\n\t" \
  "s_mov_b64 vcc, s[52:53]\n\t" \
  "v_perm_b32 v14, v12, v14, s55\n\t" \
  "v_mfma_f32_16x16x32_bf16 v[156:159], v[232:235], v[132:135], v[156:159]\n\t" \
  "v_cndmask_b32 v182, v14, v240, vcc\n\t" \
  "v_cndmask_b32_dpp v186, v14, v240, vcc row_ror:8 row_mask:0xf bank_mask:0xf\n\t" \
  "s_orn2_b64 vcc, s[52:53], s[42:43]\n\t" \
  "v_cndmask_b32 v181, v15, v240, vcc\n\t" \
  "v_mfma_f32_16x16x16_bf16 v[0:3], v[196:197], v[182:183], 0\n\t" \
  "v_fma_f32 v147, |v147|, v221, v147 mul:4\n\t" \
  "v_cndmask_b32_dpp v185, v15, v240, vcc row_ror:8 row_mask:0xf bank_mask:0xf\n\t" \
  "v_mfma_f32_16x16x16_bf16 v[4:7], v[196:197], v[186:187], 0\n\t" \
  "s_mov_b64 vcc, 0\n\t" \
  "v_fma_f32 v146, |v146|, v220, v146 mul:4\n\t" \
  "v_fma_f32 v145, |v145|, v223, v145 mul:4\n\t" \
  "v_mfma_f32_16x16x16_bf16 v[188:191], v[24:25], v[180:181], v[0:3]\n\t" \
  "v_fma_f32 v144, |v144|, v222, v144 mul:4\n\t" \
  "v_cvt_pk_bf16_f32 v147, v146, v147\n\t" \
  "v_mfma_f32_16x16x16_bf16 v[176:179], v[26:27], v[180:181], v[0:3]\n\t" \
  "s_nop 4\n\t" \
  "v_cvt_pk_bf16_f32 v105, v190, v191\n\t" \
  "v_mfma_f32_16x16x16_bf16 v[200:203], v[76:77], v[184:185], v[4:7]\n\t" \
  "s_nop 0\n\t" \
  "v_cvt_pk_bf16_f32 v107, v178, v179\n\t" \
  "v_mfma_f32_16x16x16_bf16 v[204:207], v[78:79], v[184:185], v[4:7]\n\t" \
  "v_cvt_pk_bf16_f32 v146, v144, v145\n\t" \
  "v_cvt_pknorm_i16_f32 v228, |v148|, |v149|\n\t" \
  "v_cvt_pk_bf16_f32 v106, v176, v177\n\t" \
  "v_cvt_pk_bf16_f32 v104, v188, v189\n\t" \
  "s_nop 0\n\t" \
  "v_cvt_pk_bf16_f32 v108, v200, v201\n\t" \
  "v_mfma_f32_16x16x16_bf16 v[196:199], v[28:29], v[180:181], v[0:3]\n\t" \
  "buffer_store_dwordx4 v[60:63], v250, s[20:23], s28 offen offset:64 sc1\n\t" \
  "v_mfma_f32_16x16x16_bf16 v[188:191], v[30:31], v[180:181], v[0:3]\n\t" \
  "v_cvt_pk_bf16_f32 v110, v204, v205\n\t" \
  "v_cvt_pk_bf16_f32 v109, v202, v203\n\t" \
  "v_cvt_pk_bf16_f32 v111, v206, v207\n\t" \
  "v_mfma_f32_16x16x32_bf16 v[160:163], v[208:211], v[104:107], 0\n\t" \
  "s_nop 1\n\t" \
  "v_cvt_pk_bf16_f32 v120, v196, v197\n\t" \
  "v_cvt_pk_bf16_f32 v121, v198, v199\n\t" \
  "v_cvt_pk_bf16_f32 v123, v190, v191\n\t" \
  "v_mfma_f32_16x16x16_bf16 v[176:179], v[84:85], v[184:185], v[4:7]\n\t" \
  "v_cvt_pk_bf16_f32 v122, v188, v189\n\t" \
  "v_mfma_f32_16x16x16_bf16 v[200:203], v[86:87], v[184:185], v[4:7]\n\t" \
  "v_cvt_pknorm_i16_f32 v230, |v150|, |v151|\n\t" \
  "v_lshlrev_b32_sdwa v229, 2, v228 dst_sel:DWORD dst_unused:UNUSED_PAD src0_sel:DWORD src1_sel:BYTE_3\n\t" \
  "v_lshlrev_b32_sdwa v228, 2, v228 dst_sel:DWORD dst_unused:UNUSED_PAD src0_sel:DWORD src1_sel:BYTE_1\n\t" \
  "v_mfma_f32_16x16x32_bf16 v[168:171], v[208:211], v[108:111], 0\n\t" \
  "ds_read_b64_tr_b16 v[210:211], v248 offset:44584\n\t" \
  "ds_read_b64_tr_b16 v[208:209], v248 offset:43560\n\t" \
  "v_cvt_pk_bf16_f32 v124, v176, v177\n\t" \
  "v_mfma_f32_16x16x16_bf16 v[204:207], v[32:33], v[180:181], v[0:3]\n\t" \
  "v_mfma_f32_16x16x16_bf16 v[196:199], v[34:35], v[180:181], v[0:3]\n\t" \
  "v_cvt_pk_bf16_f32 v127, v202, v203\n\t" \
  "v_lshlrev_b32_sdwa v231, 2, v230 dst_sel:DWORD dst_unused:UNUSED_PAD src0_sel:DWORD src1_sel:BYTE_3\n\t" \
  "v_cvt_pk_bf16_f32 v125, v178, v179\n\t" \
  "v_mfma_f32_16x16x32_bf16 v[160:163], v[212:215], v[120:123], v[160:163]\n\t" \
  "ds_read_b32 v229, v229 offset:10016\n\t" \
  "v_cvt_pk_bf16_f32 v126, v200, v201\n\t" \
  "s_nop 0\n\t" \
  "v_cvt_pk_bf16_f32 v137, v206, v207\n\t" \
  "v_cvt_pk_bf16_f32 v136, v204, v205\n\t" \
  "v_mfma_f32_16x16x16_bf16 v[188:191], v[92:93], v[184:185], v[4:7]\n\t" \
  "ds_read_b32 v228, v228 offset:10016\n\t" \
  "v_cvt_pk_bf16_f32 v139, v198, v199\n\t" \
  "v_mfma_f32_16x16x16_bf16 v[176:179], v[94:95], v[184:185], v[4:7]\n\t" \
  "v_cvt_pk_bf16_f32 v138, v196, v197\n\t" \
  "v_lshlrev_b32_sdwa v230, 2, v230 dst_sel:DWORD dst_unused:UNUSED_PAD src0_sel:DWORD src1_sel:BYTE_1\n\t" \
  "v_lshlrev_b32_sdwa v233, 1, v157 dst_sel:DWORD dst_unused:UNUSED_PAD src0_sel:DWORD src1_sel:WORD_1\n\t" \
  "v_mfma_f32_16x16x32_bf16 v[168:171], v[212:215], v[124:127], v[168:171]\n\t" \
  "ds_read_b64_tr_b16 v[212:213], v248 offset:46632\n\t" \
  "ds_read_b64_tr_b16 v[214:215], v248 offset:47656\n\t" \
  "v_cvt_pk_bf16_f32 v141, v190, v191\n\t" \
  "v_cvt_pk_bf16_f32 v140, v188, v189\n\t" \
  "v_mfma_f32_16x16x32_bf16 v[160:163], v[216:219], v[136:139], v[160:163]\n\t" \
  "v_cvt_pk_bf16_f32 v142, v176, v177\n\t" \
  "v_cvt_pk_bf16_f32 v143, v178, v179\n\t" \
  "s_nop 1\n\t" \
  "v_mfma_f32_16x16x32_bf16 v[168:171], v[216:219], v[140:143], v[168:171]\n\t" \
  "ds_read_b64_tr_b16 v[218:219], v248 offset:48680\n\t" \
  "ds_read_b64_tr_b16 v[216:217], v248 offset:45608\n\t" \
  "v_lshlrev_b32_sdwa v232, 1, v156 dst_sel:DWORD dst_unused:UNUSED_PAD src0_sel:DWORD src1_sel:WORD_1\n\t" \
  "v_lshlrev_b32_sdwa v235, 1, v159 dst_sel:DWORD dst_unused:UNUSED_PAD src0_sel:DWORD src1_sel:WORD_1\n\t" \
  "s_waitcnt lgkmcnt(6)\n\t" \
  "s_waitcnt vmcnt(4)\n\t" \
  "s_mov_b64 exec, 3\n\t" \
  "ds_add_rtn_u32 v246, v251, v250 offset:47136\n\t" \
  "s_mov_b64 exec, -1\n\t" \
  "v_mfma_f32_16x16x32_bf16 v[164:167], v[208:211], v[104:107], 0\n\t" \
  "ds_read_b32 v231, v231 offset:10016\n\t" \
  "v_lshlrev_b32_sdwa v234, 1, v158 dst_sel:DWORD dst_unused:UNUSED_PAD src0_sel:DWORD src1_sel:WORD_1\n\t" \
  "v_mfma_f32_16x16x32_bf16 v[172:175], v[208:211], v[108:111], 0\n\t" \
  "ds_read_b64_tr_b16 v[210:211], v248 offset:50216\n\t" \
  "ds_read_b64_tr_b16 v[208:209], v248 offset:49192\n\t" \
  "s_waitcnt lgkmcnt(6)\n\t" \
  "v_mfma_f32_16x16x32_bf16 v[172:175], v[212:215], v[124:127], v[172:175]\n\t" \
  "v_mfma_f32_16x16x32_bf16 v[164:167], v[212:215], v[120:123], v[164:167]\n\t" \
  "ds_read_b64_tr_b16 v[212:213], v248 offset:52264\n\t" \
  "s_waitcnt lgkmcnt(5)\n\t" \
  "ds_read_b32 v230, v230 offset:10016\n\t" \
  "v_mfma_f32_16x16x32_bf16 v[172:175], v[216:219], v[140:143], v[172:175]\n\t" \
  "ds_read_b64_tr_b16 v[214:215], v248 offset:53288\n\t" \
  "v_mfma_f32_16x16x32_bf16 v[164:167], v[216:219], v[136:139], v[164:167]\n\t" \
  "ds_read_u16_d16_hi v233, v233 offset:10528\n\t" \
  "ds_read_b64_tr_b16 v[216:217], v248 offset:51240\n\t" \
  "s_setprio 1\n\t" \
  "ds_read_b64_tr_b16 v[218:219], v248 offset:54312\n\t" \
  "v_mfma_f32_4x4x4_16b_bf16 v[0:3], v[242:243], v[36:37], 0\n\t" \
  "v_mfma_f32_4x4x4_16b_bf16 v[16:19], v[242:243], v[44:45], 0\n\t" \
  "v_mfma_f32_4x4x4_16b_bf16 v[4:7], v[242:243], v[38:39], 0\n\t" \
  "v_cvt_pknorm_i16_f32 v220, |v162|, |v163|\n\t" \
  "v_cvt_pknorm_i16_f32 v222, |v160|, |v161|\n\t" \
  "buffer_store_dwordx4 v[68:71], v250, s[20:23], s28 offen offset:128 sc1\n\t" \
  "s_setprio 0\n\t" \
  "v_mfma_f32_4x4x4_16b_bf16 v[12:15], v[242:243], v[42:43], 0\n\t" \
  "v_mfma_f32_4x4x4_16b_bf16 v[8:11], v[242:243], v[40:41], 0\n\t" \
  "v_lshlrev_b32_sdwa v221, 2, v220 dst_sel:DWORD dst_unused:UNUSED_PAD src0_sel:DWORD src1_sel:BYTE_3\n\t" \
  "v_lshlrev_b32_sdwa v220, 2, v220 dst_sel:DWORD dst_unused:UNUSED_PAD src0_sel:DWORD src1_sel:BYTE_1\n\t" \
  "v_mfma_f32_4x4x4_16b_bf16 v[20:23], v[242:243], v[46:47], 0\n\t" \
  "v_lshlrev_b32_sdwa v223, 2, v222 dst_sel:DWORD dst_unused:UNUSED_PAD src0_sel:DWORD src1_sel:BYTE_3\n\t" \
  "v_lshlrev_b32_sdwa v222, 2, v222 dst_sel:DWORD dst_unused:UNUSED_PAD src0_sel:DWORD src1_sel:BYTE_1\n\t" \
  "s_setprio 2\n\t" \
  "ds_read_u16 v234, v234 offset:10528\n\t" \
  "s_setprio 0\n\t" \
  "ds_read_u16 v232, v232 offset:10528\n\t" \
  "v_mfma_f32_4x4x4_16b_bf16 v[28:31], v[242:243], v[74:75], 0\n\t" \
  "v_lshlrev_b32_sdwa v224, 1, v170 dst_sel:DWORD dst_unused:UNUSED_PAD src0_sel:DWORD src1_sel:WORD_1\n\t" \
  "v_lshlrev_b32_sdwa v225, 1, v171 dst_sel:DWORD dst_unused:UNUSED_PAD src0_sel:DWORD src1_sel:WORD_1\n\t" \
  "ds_read_u16_d16_hi v235, v235 offset:10528\n\t" \
  "v_mfma_f32_4x4x4_16b_bf16 v[24:27], v[242:243], v[72:73], 0\n\t" \
  "v_mfma_f32_4x4x4_16b_bf16 v[32:35], v[242:243], v[80:81], 0\n\t" \
  "v_lshlrev_b32_sdwa v227, 1, v169 dst_sel:DWORD dst_unused:UNUSED_PAD src0_sel:DWORD src1_sel:WORD_1\n\t" \
  "v_lshlrev_b32_sdwa v226, 1, v168 dst_sel:DWORD dst_unused:UNUSED_PAD src0_sel:DWORD src1_sel:WORD_1\n\t" \
  "s_waitcnt lgkmcnt(7)\n\t" \
  "ds_read_b32 v221, v221 offset:10016\n\t" \
  "ds_read_b32 v220, v220 offset:10016\n\t" \
  "v_mfma_f32_4x4x4_16b_bf16 v[76:79], v[242:243], v[50:51], 0\n\t" \
  "v_mfma_f32_4x4x4_16b_bf16 v[44:47], v[242:243], v[90:91], 0\n\t" \
  "ds_read_b32 v223, v223 offset:10016\n\t" \
  "v_fma_mix_f32 v228, |v148|, v228, v228 op_sel:[0,1,0] op_sel_hi:[0,1,1] clamp\n\t" \
  "v_mfma_f32_4x4x4_16b_bf16 v[36:39], v[242:243], v[82:83], 0\n\t" \
  "v_mfma_f32_4x4x4_16b_bf16 v[72:75], v[242:243], v[48:49], 0\n\t" \
  "v_mfma_f32_4x4x4_16b_bf16 v[40:43], v[242:243], v[88:89], 0\n\t" \
  "v_fma_mix_f32 v230, |v150|, v230, v230 op_sel:[0,1,0] op_sel_hi:[0,1,1] clamp\n\t" \
  "v_fma_mix_f32 v231, |v151|, v231, v231 op_sel:[0,1,0] op_sel_hi:[0,1,1] clamp\n\t" \
  "ds_read_b32 v222, v222 offset:10016\n\t" \
  "s_waitcnt lgkmcnt(4)\n\t" \
  "v_mfma_f32_4x4x4_16b_bf16 v[80:83], v[242:243], v[56:57], 0\n\t" \
  "v_mfma_f32_4x4x4_16b_bf16 v[84:87], v[242:243], v[58:59], 0\n\t" \
  "v_or_b32 v156, v232, v233\n\t" \
  "v_or_b32 v157, v234, v235\n\t" \
  "ds_read_u16_d16_hi v225, v225 offset:10528\n\t" \
  "v_fma_mix_f32 v229, |v149|, v229, v229 op_sel:[0,1,0] op_sel_hi:[0,1,1] clamp\n\t" \
  "v_mfma_f32_4x4x4_16b_bf16 v[88:91], v[242:243], v[236:237], 0\n\t" \
  "v_mfma_f32_4x4x4_16b_bf16 v[92:95], v[242:243], v[238:239], 0\n\t" \
  "v_fma_f32 v148, |v148|, v228, v148 mul:4\n\t" \
  "v_fma_f32 v149, |v149|, v229, v149 mul:4\n\t" \
  "ds_read_u16 v224, v224 offset:10528\n\t" \
  "v_mfma_f32_16x16x32_bf16 v[176:179], v[208:211], v[96:99], 0\n\t" \
  "v_fma_f32 v150, |v150|, v230, v150 mul:4\n\t" \
  "v_fma_f32 v151, |v151|, v231, v151 mul:4\n\t" \
  "ds_read_u16_d16_hi v227, v227 offset:10528\n\t" \
  "v_mfma_f32_16x16x32_bf16 v[184:187], v[208:211], v[100:103], 0\n\t" \
  "v_cvt_pk_bf16_f32 v148, v148, v149\n\t" \
  "v_cvt_pk_bf16_f32 v149, v150, v151\n\t" \
  "ds_read_u16 v226, v226 offset:10528\n\t" \
  "v_mfma_f32_16x16x32_bf16 v[192:195], v[208:211], v[104:107], 0\n\t" \
  "v_cvt_pknorm_i16_f32 v228, |v164|, |v165|\n\t" \
  "v_cvt_pknorm_i16_f32 v230, |v166|, |v167|\n\t" \
  "v_lshlrev_b32_sdwa v235, 1, v175 dst_sel:DWORD dst_unused:UNUSED_PAD src0_sel:DWORD src1_sel:WORD_1\n\t" \
  "v_mfma_f32_16x16x32_bf16 v[200:203], v[208:211], v[108:111], 0\n\t" \
  "v_lshlrev_b32_sdwa v229, 2, v228 dst_sel:DWORD dst_unused:UNUSED_PAD src0_sel:DWORD src1_sel:BYTE_3\n\t" \
  "ds_read_b32 v229, v229 offset:10016\n\t" \
  "v_mfma_f32_16x16x32_bf16 v[192:195], v[212:215], v[120:123], v[192:195]\n\t" \
  "v_lshlrev_b32_sdwa v234, 1, v174 dst_sel:DWORD dst_unused:UNUSED_PAD src0_sel:DWORD src1_sel:WORD_1\n\t" \
  "v_lshlrev_b32_sdwa v228, 2, v228 dst_sel:DWORD dst_unused:UNUSED_PAD src0_sel:DWORD src1_sel:BYTE_1\n\t" \
  "ds_read_b64_tr_b16 v[208:209], v248 offset:49704\n\t" \
  "s_setprio 3\n\t" \
  "v_mfma_f32_16x16x32_bf16 v[176:179], v[212:215], v[112:115], v[176:179]\n\t" \
  "v_lshlrev_b32_sdwa v231, 2, v230 dst_sel:DWORD dst_unused:UNUSED_PAD src0_sel:DWORD src1_sel:BYTE_3\n\t" \
  "v_lshlrev_b32_sdwa v230, 2, v230 dst_sel:DWORD dst_unused:UNUSED_PAD src0_sel:DWORD src1_sel:BYTE_1\n\t" \
  "s_setprio 1\n\t" \
  "ds_read_b64_tr_b16 v[210:211], v248 offset:50728\n\t" \
  "v_mfma_f32_16x16x32_bf16 v[184:187], v[212:215], v[116:119], v[184:187]\n\t" \
  "v_lshlrev_b32_sdwa v233, 1, v173 dst_sel:DWORD dst_unused:UNUSED_PAD src0_sel:DWORD src1_sel:WORD_1\n\t" \
  "v_lshlrev_b32_sdwa v232, 1, v172 dst_sel:DWORD dst_unused:UNUSED_PAD src0_sel:DWORD src1_sel:WORD_1\n\t" \
  "ds_read_b32 v228, v228 offset:10016\n\t" \
  "s_waitcnt lgkmcnt(8)\n\t" \
  "v_mfma_f32_16x16x32_bf16 v[200:203], v[212:215], v[124:127], v[200:203]\n\t" \
  "v_fma_mix_f32 v221, |v163|, v221, v221 op_sel:[0,1,0] op_sel_hi:[0,1,1] clamp\n\t" \
  "v_fma_mix_f32 v220, |v162|, v220, v220 op_sel:[0,1,0] op_sel_hi:[0,1,1] clamp\n\t" \
  "ds_read_b64_tr_b16 v[212:213], v248 offset:52776\n\t" \
  "ds_read_b32 v231, v231 offset:10016\n\t" \
  "v_mfma_f32_16x16x32_bf16 v[176:179], v[216:219], v[128:131], v[176:179]\n\t" \
  "s_waitcnt lgkmcnt(6)\n\t" \
  "v_fma_mix_f32 v223, |v161|, v223, v223 op_sel:[0,1,0] op_sel_hi:[0,1,1] clamp\n\t" \
  "v_fma_mix_f32 v222, |v160|, v222, v222 op_sel:[0,1,0] op_sel_hi:[0,1,1] clamp\n\t" \
  "ds_read_b64_tr_b16 v[214:215], v248 offset:53800\n\t" \
  "ds_read_b32 v230, v230 offset:10016\n\t" \
  "v_mfma_f32_16x16x32_bf16 v[184:187], v[216:219], v[132:135], v[184:187]\n\t" \
  "v_or_b32 v171, v224, v225\n\t" \
  "v_or_b32 v170, v226, v227\n\t" \
  "ds_read_u16 v232, v232 offset:10528\n\t" \
  "s_setprio 0\n\t" \
  "v_mfma_f32_16x16x32_bf16 v[200:203], v[216:219], v[140:143], v[200:203]\n\t" \
  "v_fma_f32 v161, |v161|, v223, v161 mul:4\n\t" \
  "v_fma_f32 v160, |v160|, v222, v160 mul:4\n\t" \
  "ds_read_u16_d16_hi v233, v233 offset:10528\n\t" \
  "v_mfma_f32_16x16x32_bf16 v[192:195], v[216:219], v[136:139], v[192:195]\n\t" \
  "v_fma_f32 v163, |v163|, v221, v163 mul:4\n\t" \
  "v_fma_f32 v162, |v162|, v220, v162 mul:4\n\t" \
  "ds_read_b64_tr_b16 v[216:217], v248 offset:51752\n\t" \
  "s_waitcnt lgkmcnt(8)\n\t" \
  "v_mfma_f32_16x16x32_bf16 v[180:183], v[208:211], v[96:99], 0\n\t" \
  "s_waitcnt vmcnt(3)\n\t" \
  "v_cvt_pk_bf16_f32 v163, v162, v163\n\t" \
  "v_cvt_pk_bf16_f32 v162, v160, v161\n\t" \
  "ds_read_b64_tr_b16 v[218:219], v248 offset:54824\n\t" \
  "v_mfma_f32_16x16x32_bf16 v[188:191], v[208:211], v[100:103], 0\n\t" \
  "v_cvt_pknorm_i16_f32 v220, |v178|, |v179|\n\t" \
  "s_mov_b64 exec, 3\n\t" \
  "ds_add_rtn_u32 v246, v251, v250 offset:47144\n\t" \
  "v_mfma_f32_16x16x32_bf16 v[196:199], v[208:211], v[104:107], 0\n\t" \
  "s_mov_b64 exec, -1\n\t" \
  "v_mfma_f32_16x16x32_bf16 v[204:207], v[208:211], v[108:111], 0\n\t" \
  "v_cvt_pknorm_i16_f32 v222, |v176|, |v177|\n\t" \
  "v_lshlrev_b32_sdwa v223, 2, v222 dst_sel:DWORD dst_unused:UNUSED_PAD src0_sel:DWORD src1_sel:BYTE_3\n\t" \
  "s_waitcnt lgkmcnt(0)\n\t" \
  "v_readfirstlane_b32 s4, v246\n\t" \
  "v_lshlrev_b32_sdwa v221, 2, v220 dst_sel:DWORD dst_unused:UNUSED_PAD src0_sel:DWORD src1_sel:BYTE_3\n\t" \
  "v_lshlrev_b32_sdwa v220, 2, v220 dst_sel:DWORD dst_unused:UNUSED_PAD src0_sel:DWORD src1_sel:BYTE_1\n\t" \
  "ds_read_u16 v234, v234 offset:10528\n\t" \
  "ds_read_u16_d16_hi v235, v235 offset:10528\n\t" \
  "v_mfma_f32_16x16x32_bf16 v[180:183], v[212:215], v[112:115], v[180:183]\n\t" \
  "s_cmpk_lt_u32 s4, 6144\n\t" \
  "v_lshlrev_b32_sdwa v222, 2, v222 dst_sel:DWORD dst_unused:UNUSED_PAD src0_sel:DWORD src1_sel:BYTE_1\n\t" \
  "s_cbranch_scc1 .Lcnx_w1_2\n\t" \
  ".Lcnx_g1_2:\n\t" \
  "ds_read_b64_tr_b16 v[210:211], v249 offset:26136\n\t" \
  "ds_read_b64_tr_b16 v[208:209], v249 offset:24600\n\t" \
  "v_mfma_f32_16x16x32_bf16 v[196:199], v[212:215], v[120:123], v[196:199]\n\t" \
  "v_lshlrev_b32_sdwa v227, 1, v185 dst_sel:DWORD dst_unused:UNUSED_PAD src0_sel:DWORD src1_sel:WORD_1\n\t" \
  "v_lshlrev_b32_sdwa v226, 1, v184 dst_sel:DWORD dst_unused:UNUSED_PAD src0_sel:DWORD src1_sel:WORD_1\n\t" \
  "v_mfma_f32_16x16x32_bf16 v[204:207], v[212:215], v[124:127], v[204:207]\n\t" \
  "v_fma_mix_f32 v229, |v165|, v229, v229 op_sel:[0,1,0] op_sel_hi:[0,1,1] clamp\n\t" \
  "v_fma_mix_f32 v228, |v164|, v228, v228 op_sel:[0,1,0] op_sel_hi:[0,1,1] clamp\n\t" \
  "ds_read_b32 v221, v221 offset:10016\n\t" \
  "v_mfma_f32_16x16x32_bf16 v[188:191], v[212:215], v[116:119], v[188:191]\n\t" \
  "s_mov_b32 m0, s46\n\t" \
  "v_lshlrev_b32_sdwa v224, 1, v186 dst_sel:DWORD dst_unused:UNUSED_PAD src0_sel:DWORD src1_sel:WORD_1\n\t" \
  "v_lshlrev_b32_sdwa v225, 1, v187 dst_sel:DWORD dst_unused:UNUSED_PAD src0_sel:DWORD src1_sel:WORD_1\n\t" \
  "ds_read_b64_tr_b16 v[212:213], v249 offset:24608\n\t" \
  "ds_read_b32 v220, v220 offset:10016\n\t" \
  "v_mfma_f32_16x16x32_bf16 v[180:183], v[216:219], v[128:131], v[180:183]\n\t" \
  "v_fma_mix_f32 v230, |v166|, v230, v230 op_sel:[0,1,0] op_sel_hi:[0,1,1] clamp\n\t" \
  "s_mov_b32 exec_hi, 0xffff\n\t" \
  "buffer_load_dwordx4 v252, s[16:19], s33 offen lds\n\t" \
  "v_mfma_f32_16x16x32_bf16 v[188:191], v[216:219], v[132:135], v[188:191]\n\t" \
  "s_mov_b32 exec_hi, -1\n\t" \
  "v_or_b32 v172, v232, v233\n\t" \
  "v_fma_mix_f32 v231, |v167|, v231, v231 op_sel:[0,1,0] op_sel_hi:[0,1,1] clamp\n\t" \
  "ds_read_b64_tr_b16 v[214:215], v249 offset:26144\n\t" \
  "ds_read_b32 v223, v223 offset:10016\n\t" \
  "v_mfma_f32_16x16x32_bf16 v[196:199], v[216:219], v[136:139], v[196:199]\n\t" \
  "s_add_u32 s33, s33, s32\n\t" \
  "v_fma_f32 v167, |v167|, v231, v167 mul:4\n\t" \
  "v_fma_f32 v164, |v164|, v228, v164 mul:4\n\t" \
  "ds_read_b32 v222, v222 offset:10016\n\t" \
  "s_waitcnt lgkmcnt(8)\n\t" \
  "v_mfma_f32_16x16x32_bf16 v[204:207], v[216:219], v[140:143], v[204:207]\n\t" \
  "v_fma_f32 v166, |v166|, v230, v166 mul:4\n\t" \
  "v_or_b32 v173, v234, v235\n\t" \
  "v_fma_f32 v165, |v165|, v229, v165 mul:4\n\t" \
  "v_cvt_pk_bf16_f32 v164, v164, v165\n\t" \
  "v_cvt_pk_bf16_f32 v165, v166, v167\n\t" \
  "s_waitcnt lgkmcnt(6)\n\t" \
  "ds_read_b64_tr_b16 v[216:217], v249 offset:24664\n\t" \
  "ds_read_u16_d16_hi v225, v225 offset:10528\n\t" \
  "v_mfma_f32_16x16x32_bf16 v[0:3], v[208:211], v[146:149], v[0:3]\n\t" \
  "v_cvt_pknorm_i16_f32 v228, |v194|, |v195|\n\t" \
  "v_cvt_pknorm_i16_f32 v230, |v192|, |v193|\n\t" \
  "ds_read_u16 v224, v224 offset:10528\n\t" \
  "ds_read_b64_tr_b16 v[218:219], v249 offset:26200\n\t" \
  "v_mfma_f32_16x16x32_bf16 v[48:51], v[208:211], v[162:165], 0\n\t" \
  "v_lshlrev_b32_sdwa v231, 2, v230 dst_sel:DWORD dst_unused:UNUSED_PAD src0_sel:DWORD src1_sel:BYTE_3\n\t" \
  "v_lshlrev_b32_sdwa v230, 2, v230 dst_sel:DWORD dst_unused:UNUSED_PAD src0_sel:DWORD src1_sel:BYTE_1\n\t" \
  "v_lshlrev_b32_sdwa v234, 1, v200 dst_sel:DWORD dst_unused:UNUSED_PAD src0_sel:DWORD src1_sel:WORD_1\n\t" \
  "v_mfma_f32_16x16x32_bf16 v[24:27], v[208:211], v[154:157], v[24:27]\n\t" \
  "v_lshlrev_b32_sdwa v229, 2, v228 dst_sel:DWORD dst_unused:UNUSED_PAD src0_sel:DWORD src1_sel:BYTE_3\n\t" \
  "v_lshlrev_b32_sdwa v228, 2, v228 dst_sel:DWORD dst_unused:UNUSED_PAD src0_sel:DWORD src1_sel:BYTE_1\n\t" \
  "ds_read_u16_d16_hi v227, v227 offset:10528\n\t" \
  "s_add_u32 m0, s46, s35\n\t" \
  "v_mfma_f32_16x16x32_bf16 v[72:75], v[208:211], v[170:173], v[72:75]\n\t" \
  "v_lshlrev_b32_sdwa v233, 1, v203 dst_sel:DWORD dst_unused:UNUSED_PAD src0_sel:DWORD src1_sel:WORD_1\n\t" \
  "s_waitcnt lgkmcnt(7)\n\t" \
  "s_mov_b32 exec_hi, 0xffff\n\t" \
  "buffer_load_dwordx4 v252, s[16:19], s34 offen lds\n\t" \
  "v_mfma_f32_16x16x32_bf16 v[4:7], v[212:215], v[146:149], v[4:7]\n\t" \
  "s_mov_b32 exec_hi, -1\n\t" \
  "v_lshlrev_b32_sdwa v235, 1, v201 dst_sel:DWORD dst_unused:UNUSED_PAD src0_sel:DWORD src1_sel:WORD_1\n\t" \
  "v_lshlrev_b32_sdwa v232, 1, v202 dst_sel:DWORD dst_unused:UNUSED_PAD src0_sel:DWORD src1_sel:WORD_1\n\t" \
  "s_add_u32 s34, s34, s32\n\t" \
  "ds_read_b64_tr_b16 v[208:209], v249 offset:24672\n\t" \
  "ds_read_u16 v226, v226 offset:10528\n\t" \
  "v_mfma_f32_16x16x32_bf16 v[28:31], v[212:215], v[154:157], v[28:31]\n\t" \
  "s_waitcnt lgkmcnt(7)\n\t" \
  "ds_read_b64_tr_b16 v[210:211], v249 offset:26208\n\t" \
  "ds_read_b32 v229, v229 offset:10016\n\t" \
  "v_mfma_f32_16x16x32_bf16 v[52:55], v[212:215], v[162:165], 0\n\t" \
  "v_fma_mix_f32 v221, |v179|, v221, v221 op_sel:[0,1,0] op_sel_hi:[0,1,1] clamp\n\t" \
  "v_fma_mix_f32 v220, |v178|, v220, v220 op_sel:[0,1,0] op_sel_hi:[0,1,1] clamp\n\t" \
  "ds_read_b32 v228, v228 offset:10016\n\t" \
  "ds_read_b32 v231, v231 offset:10016\n\t" \
  "v_mfma_f32_16x16x32_bf16 v[76:79], v[212:215], v[170:173], v[76:79]\n\t" \
  "v_fma_mix_f32 v223, |v177|, v223, v223 op_sel:[0,1,0] op_sel_hi:[0,1,1] clamp\n\t" \
  "v_fma_mix_f32 v222, |v176|, v222, v222 op_sel:[0,1,0] op_sel_hi:[0,1,1] clamp\n\t" \
  "s_waitcnt lgkmcnt(4)\n\t" \
  "ds_read_b64_tr_b16 v[212:213], v249 offset:24728\n\t" \
  "ds_read_b32 v230, v230 offset:10016\n\t" \
  "v_mfma_f32_16x16x32_bf16 v[8:11], v[216:219], v[146:149], v[8:11]\n\t" \
  "v_or_b32 v187, v224, v225\n\t" \
  "v_or_b32 v186, v226, v227\n\t" \
  "ds_read_b64_tr_b16 v[214:215], v249 offset:26264\n\t" \
  "v_mfma_f32_16x16x32_bf16 v[32:35], v[216:219], v[154:157], v[32:35]\n\t" \
  "v_fma_f32 v177, |v177|, v223, v177 mul:4\n\t" \
  "v_fma_f32 v176, |v176|, v222, v176 mul:4\n\t" \
  "s_setprio 1\n\t" \
  "ds_read_u16 v232, v232 offset:10528\n\t" \
  "s_setprio 0\n\t" \
  "v_mfma_f32_16x16x32_bf16 v[80:83], v[216:219], v[170:173], v[80:83]\n\t" \
  "v_fma_f32 v179, |v179|, v221, v179 mul:4\n\t" \
  "v_fma_f32 v178, |v178|, v220, v178 mul:4\n\t" \
  "ds_read_u16_d16_hi v233, v233 offset:10528\n\t" \
  "v_mfma_f32_16x16x32_bf16 v[56:59], v[216:219], v[162:165], 0\n\t" \
  "v_cvt_pk_bf16_f32 v179, v178, v179\n\t" \
  "v_cvt_pk_bf16_f32 v178, v176, v177\n\t" \
  "ds_read_b64_tr_b16 v[216:217], v249 offset:24736\n\t" \
  "s_waitcnt lgkmcnt(9)\n\t" \
  "v_mfma_f32_16x16x32_bf16 v[12:15], v[208:211], v[146:149], v[12:15]\n\t" \
  "v_cvt_pknorm_i16_f32 v220, |v180|, |v181|\n\t" \
  "v_cvt_pknorm_i16_f32 v222, |v182|, |v183|\n\t" \
  "ds_read_u16 v234, v234 offset:10528\n\t" \
  "v_mfma_f32_16x16x32_bf16 v[84:87], v[208:211], v[170:173], v[84:87]\n\t" \
  "v_lshlrev_b32_sdwa v225, 1, v189 dst_sel:DWORD dst_unused:UNUSED_PAD src0_sel:DWORD src1_sel:WORD_1\n\t" \
  "v_lshlrev_b32_sdwa v224, 1, v188 dst_sel:DWORD dst_unused:UNUSED_PAD src0_sel:DWORD src1_sel:WORD_1\n\t" \
  "ds_read_b64_tr_b16 v[218:219], v249 offset:26272\n\t" \
  "v_mfma_f32_16x16x32_bf16 v[36:39], v[208:211], v[154:157], v[36:39]\n\t" \
  "v_lshlrev_b32_sdwa v221, 2, v220 dst_sel:DWORD dst_unused:UNUSED_PAD src0_sel:DWORD src1_sel:BYTE_3\n\t" \
  "v_lshlrev_b32_sdwa v220, 2, v220 dst_sel:DWORD dst_unused:UNUSED_PAD src0_sel:DWORD src1_sel:BYTE_1\n\t" \
  "ds_read_u16_d16_hi v235, v235 offset:10528\n\t" \
  "v_mfma_f32_16x16x32_bf16 v[60:63], v[208:211], v[162:165], 0\n\t" \
  "v_lshlrev_b32_sdwa v223, 2, v222 dst_sel:DWORD dst_unused:UNUSED_PAD src0_sel:DWORD src1_sel:BYTE_3\n\t" \
  "v_lshlrev_b32_sdwa v222, 2, v222 dst_sel:DWORD dst_unused:UNUSED_PAD src0_sel:DWORD src1_sel:BYTE_1\n\t" \
  "s_setprio 1\n\t" \
  "ds_read_b64_tr_b16 v[208:209], v248 offset:36904\n\t" \
  "s_waitcnt lgkmcnt(7)\n\t" \
  "v_mfma_f32_16x16x32_bf16 v[16:19], v[212:215], v[146:149], v[16:19]\n\t" \
  "v_lshlrev_b32_sdwa v227, 1, v191 dst_sel:DWORD dst_unused:UNUSED_PAD src0_sel:DWORD src1_sel:WORD_1\n\t" \
  "v_fma_mix_f32 v229, |v195|, v229, v229 op_sel:[0,1,0] op_sel_hi:[0,1,1] clamp\n\t" \
  "s_setprio 0\n\t" \
  "ds_read_b64_tr_b16 v[210:211], v248 offset:37928\n\t" \
  "ds_read_b32 v221, v221 offset:10016\n\t" \
  "v_mfma_f32_16x16x32_bf16 v[40:43], v[212:215], v[154:157], v[40:43]\n\t" \
  "v_lshlrev_b32_sdwa v226, 1, v190 dst_sel:DWORD dst_unused:UNUSED_PAD src0_sel:DWORD src1_sel:WORD_1\n\t" \
  "ds_read_b32 v220, v220 offset:10016\n\t" \
  "v_mfma_f32_16x16x32_bf16 v[64:67], v[212:215], v[162:165], 0\n\t" \
  "v_fma_mix_f32 v228, |v194|, v228, v228 op_sel:[0,1,0] op_sel_hi:[0,1,1] clamp\n\t" \
  "ds_read_b32 v223, v223 offset:10016\n\t" \
  "ds_read_b32 v222, v222 offset:10016\n\t" \
  "v_mfma_f32_16x16x32_bf16 v[88:91], v[212:215], v[170:173], v[88:91]\n\t" \
  "v_fma_mix_f32 v231, |v193|, v231, v231 op_sel:[0,1,0] op_sel_hi:[0,1,1] clamp\n\t" \
  "v_fma_mix_f32 v230, |v192|, v230, v230 op_sel:[0,1,0] op_sel_hi:[0,1,1] clamp\n\t" \
  "s_waitcnt lgkmcnt(6)\n\t" \
  "ds_read_u16_d16_hi v225, v225 offset:10528\n\t" \
  "ds_read_b64_tr_b16 v[212:213], v248 offset:39976\n\t" \
  "v_mfma_f32_16x16x32_bf16 v[20:23], v[216:219], v[146:149], v[20:23]\n\t" \
  "v_or_b32 v203, v232, v233\n\t" \
  "v_or_b32 v202, v234, v235\n\t" \
  "ds_read_b64_tr_b16 v[214:215], v248 offset:41000\n\t" \
  "v_mfma_f32_16x16x32_bf16 v[44:47], v[216:219], v[154:157], v[44:47]\n\t" \
  "v_fma_f32 v195, |v195|, v229, v195 mul:4\n\t" \
  "v_fma_f32 v194, |v194|, v228, v194 mul:4\n\t" \
  "ds_read_u16 v224, v224 offset:10528\n\t" \
  "v_mfma_f32_16x16x32_bf16 v[68:71], v[216:219], v[162:165], 0\n\t" \
  "v_fma_f32 v193, |v193|, v231, v193 mul:4\n\t" \
  "v_fma_f32 v192, |v192|, v230, v192 mul:4\n\t" \
  "ds_read_u16_d16_hi v227, v227 offset:10528\n\t" \
  "v_mfma_f32_16x16x32_bf16 v[92:95], v[216:219], v[170:173], v[92:95]\n\t" \
  "v_cvt_pk_bf16_f32 v195, v194, v195\n\t" \
  "v_cvt_pk_bf16_f32 v194, v192, v193\n\t" \
  "ds_read_b64_tr_b16 v[216:217], v248 offset:38952\n\t" \
  "s_waitcnt lgkmcnt(10)\n\t" \
  "v_mfma_f32_16x16x32_bf16 v[144:147], v[208:211], v[96:99], 0\n\t" \
  "v_cvt_pknorm_i16_f32 v228, |v196|, |v197|\n\t" \
  "v_cvt_pknorm_i16_f32 v230, |v198|, |v199|\n\t" \
  "ds_read_b64_tr_b16 v[218:219], v248 offset:42024\n\t" \
  "v_mfma_f32_16x16x32_bf16 v[152:155], v[208:211], v[100:103], 0\n\t" \
  "v_lshlrev_b32_sdwa v229, 2, v228 dst_sel:DWORD dst_unused:UNUSED_PAD src0_sel:DWORD src1_sel:BYTE_3\n\t" \
  "v_lshlrev_b32_sdwa v228, 2, v228 dst_sel:DWORD dst_unused:UNUSED_PAD src0_sel:DWORD src1_sel:BYTE_1\n\t" \
  "v_mfma_f32_16x16x32_bf16 v[160:163], v[208:211], v[104:107], 0\n\t" \
  "v_lshlrev_b32_sdwa v231, 2, v230 dst_sel:DWORD dst_unused:UNUSED_PAD src0_sel:DWORD src1_sel:BYTE_3\n\t" \
  "v_lshlrev_b32_sdwa v230, 2, v230 dst_sel:DWORD dst_unused:UNUSED_PAD src0_sel:DWORD src1_sel:BYTE_1\n\t" \
  "ds_read_u16 v226, v226 offset:10528\n\t" \
  "ds_read_b32 v229, v229 offset:10016\n\t" \
  "v_mfma_f32_16x16x32_bf16 v[168:171], v[208:211], v[108:111], 0\n\t" \
  "v_lshlrev_b32_sdwa v233, 1, v205 dst_sel:DWORD dst_unused:UNUSED_PAD src0_sel:DWORD src1_sel:WORD_1\n\t" \
  "v_lshlrev_b32_sdwa v232, 1, v204 dst_sel:DWORD dst_unused:UNUSED_PAD src0_sel:DWORD src1_sel:WORD_1\n\t" \
  "s_waitcnt lgkmcnt(6)\n\t" \
  "ds_read_b64_tr_b16 v[208:209], v248 offset:37416\n\t" \
  "v_mfma_f32_16x16x32_bf16 v[144:147], v[212:215], v[112:115], v[144:147]\n\t" \
  "v_lshlrev_b32_sdwa v235, 1, v207 dst_sel:DWORD dst_unused:UNUSED_PAD src0_sel:DWORD src1_sel:WORD_1\n\t" \
  "v_lshlrev_b32_sdwa v234, 1, v206 dst_sel:DWORD dst_unused:UNUSED_PAD src0_sel:DWORD src1_sel:WORD_1\n\t" \
  "ds_read_b64_tr_b16 v[210:211], v248 offset:38440\n\t" \
  "v_mfma_f32_16x16x32_bf16 v[152:155], v[212:215], v[116:119], v[152:155]\n\t" \
  "v_fma_mix_f32 v221, |v181|, v221, v221 op_sel:[0,1,0] op_sel_hi:[0,1,1] clamp\n\t" \
  "v_fma_mix_f32 v223, |v183|, v223, v223 op_sel:[0,1,0] op_sel_hi:[0,1,1] clamp\n\t" \
  "ds_read_b32 v228, v228 offset:10016\n\t" \
  "ds_read_u16 v232, v232 offset:10528\n\t" \
  "v_mfma_f32_16x16x32_bf16 v[160:163], v[212:215], v[120:123], v[160:163]\n\t" \
  "v_fma_mix_f32 v220, |v180|, v220, v220 op_sel:[0,1,0] op_sel_hi:[0,1,1] clamp\n\t" \
  "s_waitcnt lgkmcnt(5)\n\t" \
  "ds_read_b32 v230, v230 offset:10016\n\t" \
  "ds_read_u16_d16_hi v233, v233 offset:10528\n\t" \
  "v_mfma_f32_16x16x32_bf16 v[168:171], v[212:215], v[124:127], v[168:171]\n\t" \
  "v_fma_mix_f32 v222, |v182|, v222, v222 op_sel:[0,1,0] op_sel_hi:[0,1,1] clamp\n\t" \
  "v_or_b32 v189, v226, v227\n\t" \
  "ds_read_b64_tr_b16 v[214:215], v248 offset:41512\n\t" \
  "v_mfma_f32_16x16x32_bf16 v[152:155], v[216:219], v[132:135], v[152:155]\n\t" \
  "v_fma_f32 v181, |v181|, v221, v181 mul:4\n\t" \
  "v_fma_f32 v180, |v180|, v220, v180 mul:4\n\t" \
  "ds_read_b64_tr_b16 v[212:213], v248 offset:40488\n\t" \
  "v_mfma_f32_16x16x32_bf16 v[144:147], v[216:219], v[128:131], v[144:147]\n\t" \
  "v_or_b32 v188, v224, v225\n\t" \
  "v_mfma_f32_16x16x32_bf16 v[160:163], v[216:219], v[136:139], v[160:163]\n\t" \
  "v_fma_f32 v183, |v183|, v223, v183 mul:4\n\t" \
  "v_fma_f32 v182, |v182|, v222, v182 mul:4\n\t" \
  "ds_read_u16_d16_hi v235, v235 offset:10528\n\t" \
  "ds_read_b32 v231, v231 offset:10016\n\t" \
  "v_mfma_f32_16x16x32_bf16 v[168:171], v[216:219], v[140:143], v[168:171]\n\t" \
  "v_cvt_pk_bf16_f32 v180, v180, v181\n\t" \
  "v_cvt_pk_bf16_f32 v181, v182, v183\n\t" \
  "ds_read_b64_tr_b16 v[216:217], v248 offset:39464\n\t" \
  "s_waitcnt lgkmcnt(9)\n\t" \
  "v_mfma_f32_16x16x32_bf16 v[148:151], v[208:211], v[96:99], 0\n\t" \
  "v_cvt_pknorm_i16_f32 v222, |v144|, |v145|\n\t" \
  "v_cvt_pknorm_i16_f32 v220, |v146|, |v147|\n\t" \
  "s_nop 0\n\t" \
  "s_waitcnt vmcnt(0)\n\t" \
  "ds_read_b64_tr_b16 v[218:219], v248 offset:42536\n\t" \
  "v_mfma_f32_16x16x32_bf16 v[156:159], v[208:211], v[100:103], 0\n\t" \
  "s_waitcnt lgkmcnt(4)\n\t" \
  "v_lshlrev_b32_sdwa v221, 2, v220 dst_sel:DWORD dst_unused:UNUSED_PAD src0_sel:DWORD src1_sel:BYTE_3\n\t" \
  "v_lshlrev_b32_sdwa v220, 2, v220 dst_sel:DWORD dst_unused:UNUSED_PAD src0_sel:DWORD src1_sel:BYTE_1\n\t" \
  "s_mov_b64 exec, 3\n\t" \
  "ds_add_rtn_u32 v246, v251, v250 offset:47152\n\t" \
  "v_mfma_f32_16x16x32_bf16 v[164:167], v[208:211], v[104:107], 0\n\t" \
  "s_mov_b64 exec, -1\n\t" \
  "v_lshlrev_b32_sdwa v223, 2, v222 dst_sel:DWORD dst_unused:UNUSED_PAD src0_sel:DWORD src1_sel:BYTE_3\n\t" \
  "v_lshlrev_b32_sdwa v222, 2, v222 dst_sel:DWORD dst_unused:UNUSED_PAD src0_sel:DWORD src1_sel:BYTE_1\n\t" \
  "s_setprio 2\n\t" \
  "ds_read_u16 v234, v234 offset:10528\n\t" \
  "s_setprio 0\n\t" \
  "v_mfma_f32_16x16x32_bf16 v[156:159], v[212:215], v[116:119], v[156:159]\n\t" \
  "v_lshlrev_b32_sdwa v227, 1, v153 dst_sel:DWORD dst_unused:UNUSED_PAD src0_sel:DWORD src1_sel:WORD_1\n\t" \
  "v_lshlrev_b32_sdwa v226, 1, v152 dst_sel:DWORD dst_unused:UNUSED_PAD src0_sel:DWORD src1_sel:WORD_1\n\t" \
  "ds_read_b32 v223, v223 offset:10016\n\t" \
  "v_mfma_f32_16x16x32_bf16 v[172:175], v[208:211], v[108:111], 0\n\t" \
  "s_waitcnt lgkmcnt(2)\n\t" \
  "v_lshlrev_b32_sdwa v225, 1, v155 dst_sel:DWORD dst_unused:UNUSED_PAD src0_sel:DWORD src1_sel:WORD_1\n\t" \
  "v_readfirstlane_b32 s4, v246\n\t" \
  "ds_read_b32 v221, v221 offset:10016\n\t" \
  "ds_read_b32 v220, v220 offset:10016\n\t" \
  "v_mfma_f32_16x16x32_bf16 v[148:151], v[212:215], v[112:115], v[148:151]\n\t" \
  "s_cmpk_lt_u32 s4, 6144\n\t" \
  "v_lshlrev_b32_sdwa v224, 1, v154 dst_sel:DWORD dst_unused:UNUSED_PAD src0_sel:DWORD src1_sel:WORD_1\n\t" \
  "s_cbranch_scc1 .Lcnx_w1_3\n\t" \
  ".Lcnx_g1_3:\n\t" \
  "v_mfma_f32_16x16x32_bf16 v[164:167], v[212:215], v[120:123], v[164:167]\n\t" \
  "v_fma_mix_f32 v229, |v197|, v229, v229 op_sel:[0,1,0] op_sel_hi:[0,1,1] clamp\n\t" \
  "v_fma_mix_f32 v228, |v196|, v228, v228 op_sel:[0,1,0] op_sel_hi:[0,1,1] clamp\n\t" \
  "ds_read_b64_tr_b16 v[208:209], v249 offset:30752\n\t" \
  "ds_read_b64_tr_b16 v[210:211], v249 offset:32288\n\t" \
  "v_mfma_f32_16x16x32_bf16 v[172:175], v[212:215], v[124:127], v[172:175]\n\t" \
  "v_fma_mix_f32 v230, |v198|, v230, v230 op_sel:[0,1,0] op_sel_hi:[0,1,1] clamp\n\t" \
  "v_fma_mix_f32 v231, |v199|, v231, v231 op_sel:[0,1,0] op_sel_hi:[0,1,1] clamp\n\t" \
  "s_waitcnt lgkmcnt(5)\n\t" \
  "ds_read_b64_tr_b16 v[212:213], v249 offset:30760\n\t" \
  "ds_read_b32 v222, v222 offset:10016\n\t" \
  "v_mfma_f32_16x16x32_bf16 v[148:151], v[216:219], v[128:131], v[148:151]\n\t" \
  "s_mov_b32 m0, s47\n\t" \
  "v_or_b32 v204, v232, v233\n\t" \
  "v_or_b32 v205, v234, v235\n\t" \
  "s_mov_b32 exec_hi, 0xffff\n\t" \
  "buffer_load_dwordx4 v252, s[16:19], s33 offen lds\n\t" \
  "v_mfma_f32_16x16x32_bf16 v[156:159], v[216:219], v[132:135], v[156:159]\n\t" \
  "s_mov_b32 exec_hi, -1\n\t" \
  "v_fma_f32 v197, |v197|, v229, v197 mul:4\n\t" \
  "v_fma_f32 v196, |v196|, v228, v196 mul:4\n\t" \
  "s_add_u32 s33, s33, s32\n\t" \
  "ds_read_b64_tr_b16 v[214:215], v249 offset:32296\n\t" \
  "ds_read_u16_d16_hi v227, v227 offset:10528\n\t" \
  "v_mfma_f32_16x16x32_bf16 v[164:167], v[216:219], v[136:139], v[164:167]\n\t" \
  "v_fma_f32 v198, |v198|, v230, v198 mul:4\n\t" \
  "v_fma_f32 v199, |v199|, v231, v199 mul:4\n\t" \
  "ds_read_u16_d16_hi v225, v225 offset:10528\n\t" \
  "v_mfma_f32_16x16x32_bf16 v[172:175], v[216:219], v[140:143], v[172:175]\n\t" \
  "v_cvt_pk_bf16_f32 v196, v196, v197\n\t" \
  "v_cvt_pk_bf16_f32 v197, v198, v199\n\t" \
  "s_waitcnt lgkmcnt(5)\n\t" \
  "ds_read_b64_tr_b16 v[216:217], v249 offset:30816\n\t" \
  "v_mfma_f32_16x16x32_bf16 v[0:3], v[208:211], v[178:181], v[0:3]\n\t" \
  "v_cvt_pknorm_i16_f32 v230, |v160|, |v161|\n\t" \
  "v_cvt_pknorm_i16_f32 v228, |v162|, |v163|\n\t" \
  "ds_read_b64_tr_b16 v[218:219], v249 offset:32352\n\t" \
  "v_mfma_f32_16x16x32_bf16 v[24:27], v[208:211], v[186:189], v[24:27]\n\t" \
  "v_lshlrev_b32_sdwa v229, 2, v228 dst_sel:DWORD dst_unused:UNUSED_PAD src0_sel:DWORD src1_sel:BYTE_3\n\t" \
  "v_lshlrev_b32_sdwa v228, 2, v228 dst_sel:DWORD dst_unused:UNUSED_PAD src0_sel:DWORD src1_sel:BYTE_1\n\t" \
  "ds_read_u16 v224, v224 offset:10528\n\t" \
  "v_mfma_f32_16x16x32_bf16 v[48:51], v[208:211], v[194:197], v[48:51]\n\t" \
  "v_lshlrev_b32_sdwa v231, 2, v230 dst_sel:DWORD dst_unused:UNUSED_PAD src0_sel:DWORD src1_sel:BYTE_3\n\t" \
  "s_add_u32 m0, s47, s35\n\t" \
  "s_setprio 1\n\t" \
  "s_waitcnt lgkmcnt(5)\n\t" \
  "ds_read_b32 v228, v228 offset:10016\n\t" \
  "v_mfma_f32_16x16x32_bf16 v[72:75], v[208:211], v[202:205], v[72:75]\n\t" \
  "v_lshlrev_b32_sdwa v233, 1, v171 dst_sel:DWORD dst_unused:UNUSED_PAD src0_sel:DWORD src1_sel:WORD_1\n\t" \
  "v_lshlrev_b32_sdwa v232, 1, v170 dst_sel:DWORD dst_unused:UNUSED_PAD src0_sel:DWORD src1_sel:WORD_1\n\t" \
  "s_mov_b32 exec_hi, 0xffff\n\t" \
  "buffer_load_dwordx4 v252, s[16:19], s34 offen lds\n\t" \
  "v_mfma_f32_16x16x32_bf16 v[4:7], v[212:215], v[178:181], v[4:7]\n\t" \
  "s_mov_b32 exec_hi, -1\n\t" \
  "s_add_u32 s34, s34, s32\n\t" \
  "v_lshlrev_b32_sdwa v235, 1, v169 dst_sel:DWORD dst_unused:UNUSED_PAD src0_sel:DWORD src1_sel:WORD_1\n\t" \
  "ds_read_b64_tr_b16 v[208:209], v249 offset:30824\n\t" \
  "ds_read_u16 v226, v226 offset:10528\n\t" \
  "v_mfma_f32_16x16x32_bf16 v[28:31], v[212:215], v[186:189], v[28:31]\n\t" \
  "v_lshlrev_b32_sdwa v234, 1, v168 dst_sel:DWORD dst_unused:UNUSED_PAD src0_sel:DWORD src1_sel:WORD_1\n\t" \
  "v_lshlrev_b32_sdwa v230, 2, v230 dst_sel:DWORD dst_unused:UNUSED_PAD src0_sel:DWORD src1_sel:BYTE_1\n\t" \
  "ds_read_b64_tr_b16 v[210:211], v249 offset:32360\n\t" \
  "v_mfma_f32_16x16x32_bf16 v[76:79], v[212:215], v[202:205], v[76:79]\n\t" \
  "v_fma_mix_f32 v223, |v145|, v223, v223 op_sel:[0,1,0] op_sel_hi:[0,1,1] clamp\n\t" \
  "v_fma_mix_f32 v222, |v144|, v222, v222 op_sel:[0,1,0] op_sel_hi:[0,1,1] clamp\n\t" \
  "ds_read_b32 v230, v230 offset:10016\n\t" \
  "v_mfma_f32_16x16x32_bf16 v[52:55], v[212:215], v[194:197], v[52:55]\n\t" \
  "v_fma_mix_f32 v221, |v147|, v221, v221 op_sel:[0,1,0] op_sel_hi:[0,1,1] clamp\n\t" \
  "v_fma_mix_f32 v220, |v146|, v220, v220 op_sel:[0,1,0] op_sel_hi:[0,1,1] clamp\n\t" \
  "s_waitcnt lgkmcnt(2)\n\t" \
  "ds_read_b64_tr_b16 v[212:213], v249 offset:30880\n\t" \
  "ds_read_b32 v231, v231 offset:10016\n\t" \
  "v_mfma_f32_16x16x32_bf16 v[8:11], v[216:219], v[178:181], v[8:11]\n\t" \
  "s_setprio 3\n\t" \
  "v_or_b32 v155, v224, v225\n\t" \
  "v_or_b32 v154, v226, v227\n\t" \
  "ds_read_u16_d16_hi v233, v233 offset:10528\n\t" \
  "v_mfma_f32_16x16x32_bf16 v[32:35], v[216:219], v[186:189], v[32:35]\n\t" \
  "v_fma_f32 v146, |v146|, v220, v146 mul:4\n\t" \
  "v_fma_f32 v147, |v147|, v221, v147 mul:4\n\t" \
  "ds_read_b32 v229, v229 offset:10016\n\t" \
  "v_mfma_f32_16x16x32_bf16 v[56:59], v[216:219], v[194:197], v[56:59]\n\t" \
  "v_fma_f32 v144, |v144|, v222, v144 mul:4\n\t" \
  "v_fma_f32 v145, |v145|, v223, v145 mul:4\n\t" \
  "s_add_u32 m0, s44, s50\n\t" \
  "ds_read_u16 v232, v232 offset:10528\n\t" \
  "ds_read_b64_tr_b16 v[214:215], v249 offset:32416\n\t" \
  "v_mfma_f32_16x16x32_bf16 v[80:83], v[216:219], v[202:205], v[80:83]\n\t" \
  "v_cvt_pk_bf16_f32 v147, v146, v147\n\t" \
  "v_cvt_pk_bf16_f32 v146, v144, v145\n\t" \
  "s_setprio 0\n\t" \
  "s_waitcnt lgkmcnt(7)\n\t" \
  "v_mfma_f32_16x16x32_bf16 v[12:15], v[208:211], v[178:181], v[12:15]\n\t" \
  "v_cvt_pknorm_i16_f32 v220, |v148|, |v149|\n\t" \
  "v_cvt_pknorm_i16_f32 v222, |v150|, |v151|\n\t" \
  "s_add_u32 s3, s33, s51\n\t" \
  "ds_read_b64_tr_b16 v[216:217], v249 offset:30888\n\t" \
  "v_mfma_f32_16x16x32_bf16 v[36:39], v[208:211], v[186:189], v[36:39]\n\t" \
  "v_lshlrev_b32_sdwa v221, 2, v220 dst_sel:DWORD dst_unused:UNUSED_PAD src0_sel:DWORD src1_sel:BYTE_3\n\t" \
  "v_lshlrev_b32_sdwa v220, 2, v220 dst_sel:DWORD dst_unused:UNUSED_PAD src0_sel:DWORD src1_sel:BYTE_1\n\t" \
  "s_setprio 1\n\t" \
  "ds_read_u16_d16_hi v235, v235 offset:10528\n\t" \
  "ds_read_b64_tr_b16 v[218:219], v249 offset:32424\n\t" \
  "v_mfma_f32_16x16x32_bf16 v[84:87], v[208:211], v[202:205], v[84:87]\n\t" \
  "v_lshlrev_b32_sdwa v224, 1, v156 dst_sel:DWORD dst_unused:UNUSED_PAD src0_sel:DWORD src1_sel:WORD_1\n\t" \
  "s_waitcnt lgkmcnt(3)\n\t" \
  "v_mfma_f32_16x16x32_bf16 v[60:63], v[208:211], v[194:197], v[60:63]\n\t" \
  "v_lshlrev_b32_sdwa v223, 2, v222 dst_sel:DWORD dst_unused:UNUSED_PAD src0_sel:DWORD src1_sel:BYTE_3\n\t" \
  "v_lshlrev_b32_sdwa v222, 2, v222 dst_sel:DWORD dst_unused:UNUSED_PAD src0_sel:DWORD src1_sel:BYTE_1\n\t" \
  "ds_read_u16 v234, v234 offset:10528\n\t" \
  "ds_read_b32 v220, v220 offset:10016\n\t" \
  "v_lshlrev_b32_sdwa v225, 1, v157 dst_sel:DWORD dst_unused:UNUSED_PAD src0_sel:DWORD src1_sel:WORD_1\n\t" \
  "s_mov_b32 exec_hi, 0xffff\n\t" \
  "buffer_load_dwordx4 v252, s[16:19], s3 offen lds\n\t" \
  "v_mfma_f32_16x16x32_bf16 v[16:19], v[212:215], v[178:181], v[16:19]\n\t" \
  "s_mov_b64 exec, -1\n\t" \
  "s_setprio 2\n\t" \
  "v_nop\n\t" \
  "ds_read_b64_tr_b16 v[208:209], v248 offset:0\n\t" \
  "v_mfma_f32_16x16x32_bf16 v[64:67], v[212:215], v[194:197], v[64:67]\n\t" \
  "v_fma_mix_f32 v229, |v163|, v229, v229 op_sel:[0,1,0] op_sel_hi:[0,1,1] clamp\n\t" \
  "v_lshlrev_b32_sdwa v227, 1, v159 dst_sel:DWORD dst_unused:UNUSED_PAD src0_sel:DWORD src1_sel:WORD_1\n\t" \
  "ds_read_b32 v221, v221 offset:10016\n\t" \
  "v_mfma_f32_16x16x32_bf16 v[40:43], v[212:215], v[186:189], v[40:43]\n\t" \
  "v_lshlrev_b32_sdwa v226, 1, v158 dst_sel:DWORD dst_unused:UNUSED_PAD src0_sel:DWORD src1_sel:WORD_1\n\t" \
  "v_fma_mix_f32 v228, |v162|, v228, v228 op_sel:[0,1,0] op_sel_hi:[0,1,1] clamp\n\t" \
  "ds_read_b32 v223, v223 offset:10016\n\t" \
  "ds_read_b64_tr_b16 v[210:211], v248 offset:1024\n\t" \
  "v_mfma_f32_16x16x32_bf16 v[88:91], v[212:215], v[202:205], v[88:91]\n\t" \
  "v_fma_mix_f32 v231, |v161|, v231, v231 op_sel:[0,1,0] op_sel_hi:[0,1,1] clamp\n\t" \
  "v_fma_mix_f32 v230, |v160|, v230, v230 op_sel:[0,1,0] op_sel_hi:[0,1,1] clamp\n\t" \
  "s_waitcnt lgkmcnt(5)\n\t" \
  "ds_read_b64_tr_b16 v[212:213], v248 offset:3072\n\t" \
  "ds_read_b32 v222, v222 offset:10016\n\t" \
  "v_mfma_f32_16x16x32_bf16 v[20:23], v[216:219], v[178:181], v[20:23]\n\t" \
  "v_or_b32 v171, v232, v233\n\t" \
  "v_or_b32 v170, v234, v235\n\t" \
  "ds_read_u16_d16_hi v225, v225 offset:10528\n\t" \
  "v_mfma_f32_16x16x32_bf16 v[68:71], v[216:219], v[194:197], v[68:71]\n\t" \
  "v_fma_f32 v161, |v161|, v231, v161 mul:4\n\t" \
  "v_fma_f32 v160, |v160|, v230, v160 mul:4\n\t" \
  "s_setprio 1\n\t" \
  "ds_read_b64_tr_b16 v[214:215], v248 offset:4096\n\t" \
  "s_add_u32 s3, s34, s51\n\t" \
  "v_mfma_f32_16x16x32_bf16 v[44:47], v[216:219], v[186:189], v[44:47]\n\t" \
  "v_fma_f32 v162, |v162|, v228, v162 mul:4\n\t" \
  "v_fma_f32 v163, |v163|, v229, v163 mul:4\n\t" \
  "s_add_u32 m0, s44, s36\n\t" \
  "ds_read_u16 v224, v224 offset:10528\n\t" \
  "s_setprio 2\n\t" \
  "s_waitcnt lgkmcnt(5)\n\t" \
  "v_cvt_pk_bf16_f32 v163, v162, v163\n\t" \
  "s_mov_b32 exec_hi, 0xffff\n\t" \
  "buffer_load_dwordx4 v252, s[16:19], s3 offen lds\n\t" \
  "v_mfma_f32_16x16x32_bf16 v[176:179], v[208:211], v[96:99], 0\n\t" \
  "s_mov_b64 exec, -1\n\t" \
  "v_lshlrev_b32_sdwa v232, 1, v172 dst_sel:DWORD dst_unused:UNUSED_PAD src0_sel:DWORD src1_sel:WORD_1\n\t" \
  "v_cvt_pknorm_i16_f32 v230, |v166|, |v167|\n\t" \
  "ds_read_u16_d16_hi v227, v227 offset:10528\n\t" \
  "v_mfma_f32_16x16x32_bf16 v[92:95], v[216:219], v[202:205], v[92:95]\n\t" \
  "v_nop\n\t" \
  "v_cvt_pk_bf16_f32 v162, v160, v161\n\t" \
  "ds_read_b64_tr_b16 v[216:217], v248 offset:2048\n\t" \
  "v_mfma_f32_16x16x32_bf16 v[192:195], v[208:211], v[104:107], 0\n\t" \
  "v_lshlrev_b32_sdwa v231, 2, v230 dst_sel:DWORD dst_unused:UNUSED_PAD src0_sel:DWORD src1_sel:BYTE_3\n\t" \
  "v_cvt_pknorm_i16_f32 v228, |v164|, |v165|\n\t" \
  "ds_read_b64_tr_b16 v[218:219], v248 offset:5120\n\t" \
  "v_mfma_f32_16x16x32_bf16 v[184:187], v[208:211], v[100:103], 0\n\t" \
  "v_lshlrev_b32_sdwa v229, 2, v228 dst_sel:DWORD dst_unused:UNUSED_PAD src0_sel:DWORD src1_sel:BYTE_3\n\t" \
  "v_lshlrev_b32_sdwa v228, 2, v228 dst_sel:DWORD dst_unused:UNUSED_PAD src0_sel:DWORD src1_sel:BYTE_1\n\t" \
  "ds_read_u16 v226, v226 offset:10528\n\t" \
  "v_mfma_f32_16x16x32_bf16 v[200:203], v[208:211], v[108:111], 0\n\t" \
  "v_nop\n\t" \
  "v_lshlrev_b32_sdwa v230, 2, v230 dst_sel:DWORD dst_unused:UNUSED_PAD src0_sel:DWORD src1_sel:BYTE_1\n\t" \
  "ds_read_b64_tr_b16 v[208:209], v248 offset:512\n\t" \
  "s_waitcnt lgkmcnt(6)\n\t" \
  "v_mfma_f32_16x16x32_bf16 v[176:179], v[212:215], v[112:115], v[176:179]\n\t" \
  "v_lshlrev_b32_sdwa v235, 1, v175 dst_sel:DWORD dst_unused:UNUSED_PAD src0_sel:DWORD src1_sel:WORD_1\n\t" \
  "v_fma_mix_f32 v221, |v149|, v221, v221 op_sel:[0,1,0] op_sel_hi:[0,1,1] clamp\n\t" \
  "ds_read_b32 v231, v231 offset:10016\n\t" \
  "ds_read_b32 v229, v229 offset:10016\n\t" \
  "v_mfma_f32_16x16x32_bf16 v[200:203], v[212:215], v[124:127], v[200:203]\n\t" \
  "v_lshlrev_b32_sdwa v233, 1, v173 dst_sel:DWORD dst_unused:UNUSED_PAD src0_sel:DWORD src1_sel:WORD_1\n\t" \
  "v_lshlrev_b32_sdwa v234, 1, v174 dst_sel:DWORD dst_unused:UNUSED_PAD src0_sel:DWORD src1_sel:WORD_1\n\t" \
  "s_setprio 3\n\t" \
  "s_setprio 1\n\t" \
  "ds_read_b32 v228, v228 offset:10016\n\t" \
  "v_mfma_f32_16x16x32_bf16 v[192:195], v[212:215], v[120:123], v[192:195]\n\t" \
  "v_fma_mix_f32 v220, |v148|, v220, v220 op_sel:[0,1,0] op_sel_hi:[0,1,1] clamp\n\t" \
  "v_fma_mix_f32 v223, |v151|, v223, v223 op_sel:[0,1,0] op_sel_hi:[0,1,1] clamp\n\t" \
  "ds_read_b64_tr_b16 v[210:211], v248 offset:1536\n\t" \
  "v_mfma_f32_16x16x32_bf16 v[184:187], v[212:215], v[116:119], v[184:187]\n\t" \
  "s_setprio 0\n\t" \
  "s_waitcnt lgkmcnt(6)\n\t" \
  "v_fma_mix_f32 v222, |v150|, v222, v222 op_sel:[0,1,0] op_sel_hi:[0,1,1] clamp\n\t" \
  "ds_read_b64_tr_b16 v[212:213], v248 offset:3584\n\t" \
  "ds_read_u16_d16_hi v233, v233 offset:10528\n\t" \
  "v_mfma_f32_16x16x32_bf16 v[176:179], v[216:219], v[128:131], v[176:179]\n\t" \
  "s_waitcnt lgkmcnt(7)\n\t" \
  "v_or_b32 v156, v224, v225\n\t" \
  "v_or_b32 v157, v226, v227\n\t" \
  "ds_read_b64_tr_b16 v[214:215], v248 offset:4608\n\t" \
  "ds_read_u16_d16_hi v235, v235 offset:10528\n\t" \
  "v_mfma_f32_16x16x32_bf16 v[192:195], v[216:219], v[136:139], v[192:195]\n\t" \
  "v_fma_f32 v151, |v151|, v223, v151 mul:4\n\t" \
  "v_fma_f32 v150, |v150|, v222, v150 mul:4\n\t" \
  "ds_read_b32 v230, v230 offset:10016\n\t" \
  "v_mfma_f32_16x16x32_bf16 v[184:187], v[216:219], v[132:135], v[184:187]\n\t" \
  "v_fma_f32 v149, |v149|, v221, v149 mul:4\n\t" \
  "v_fma_f32 v148, |v148|, v220, v148 mul:4\n\t" \
  "ds_read_u16 v232, v232 offset:10528\n\t" \
  "v_mfma_f32_16x16x32_bf16 v[200:203], v[216:219], v[140:143], v[200:203]\n\t" \
  "v_cvt_pk_bf16_f32 v148, v148, v149\n\t" \
  "v_cvt_pk_bf16_f32 v149, v150, v151\n\t" \
  "s_waitcnt lgkmcnt(6)\n\t" \
  "ds_read_b64_tr_b16 v[216:217], v248 offset:2560\n\t" \
  "v_mfma_f32_16x16x32_bf16 v[180:183], v[208:211], v[96:99], 0\n\t" \
  "v_cvt_pknorm_i16_f32 v222, |v176|, |v177|\n\t" \
  "v_cvt_pknorm_i16_f32 v220, |v178|, |v179|\n\t" \
  "s_nop 0\n\t" \
  "s_waitcnt vmcnt(2)\n\t" \
  "ds_read_b64_tr_b16 v[218:219], v248 offset:5632\n\t" \
  "v_mfma_f32_16x16x32_bf16 v[188:191], v[208:211], v[100:103], 0\n\t" \
  "s_waitcnt lgkmcnt(5)\n\t" \
  "v_lshlrev_b32_sdwa v221, 2, v220 dst_sel:DWORD dst_unused:UNUSED_PAD src0_sel:DWORD src1_sel:BYTE_3\n\t" \
  "v_lshlrev_b32_sdwa v220, 2, v220 dst_sel:DWORD dst_unused:UNUSED_PAD src0_sel:DWORD src1_sel:BYTE_1\n\t" \
  "s_mov_b64 exec, 3\n\t" \
  "ds_add_rtn_u32 v246, v251, v250 offset:47160\n\t" \
  "v_mfma_f32_16x16x32_bf16 v[196:199], v[208:211], v[104:107], 0\n\t" \
  "s_mov_b64 exec, -1\n\t" \
  "v_lshlrev_b32_sdwa v223, 2, v222 dst_sel:DWORD dst_unused:UNUSED_PAD src0_sel:DWORD src1_sel:BYTE_3\n\t" \
  "v_lshlrev_b32_sdwa v222, 2, v222 dst_sel:DWORD dst_unused:UNUSED_PAD src0_sel:DWORD src1_sel:BYTE_1\n\t" \
  "s_setprio 2\n\t" \
  "ds_read_u16 v234, v234 offset:10528\n\t" \
  "s_setprio 0\n\t" \
  "v_mfma_f32_16x16x32_bf16 v[188:191], v[212:215], v[116:119], v[188:191]\n\t" \
  "v_lshlrev_b32_sdwa v227, 1, v185 dst_sel:DWORD dst_unused:UNUSED_PAD src0_sel:DWORD src1_sel:WORD_1\n\t" \
  "v_lshlrev_b32_sdwa v226, 1, v184 dst_sel:DWORD dst_unused:UNUSED_PAD src0_sel:DWORD src1_sel:WORD_1\n\t" \
  "ds_read_b32 v223, v223 offset:10016\n\t" \
  "v_mfma_f32_16x16x32_bf16 v[204:207], v[208:211], v[108:111], 0\n\t" \
  "s_waitcnt lgkmcnt(2)\n\t" \
  "v_lshlrev_b32_sdwa v225, 1, v187 dst_sel:DWORD dst_unused:UNUSED_PAD src0_sel:DWORD src1_sel:WORD_1\n\t" \
  "v_readfirstlane_b32 s4, v246\n\t" \
  "ds_read_b32 v221, v221 offset:10016\n\t" \
  "ds_read_b32 v220, v220 offset:10016\n\t" \
  "v_mfma_f32_16x16x32_bf16 v[180:183], v[212:215], v[112:115], v[180:183]\n\t" \
  "s_cmpk_lt_u32 s4, 6144\n\t" \
  "v_lshlrev_b32_sdwa v224, 1, v186 dst_sel:DWORD dst_unused:UNUSED_PAD src0_sel:DWORD src1_sel:WORD_1\n\t" \
  "s_cbranch_scc1 .Lcnx_w1_4\n\t" \
  ".Lcnx_g1_4:\n\t" \
  "v_mfma_f32_16x16x32_bf16 v[196:199], v[212:215], v[120:123], v[196:199]\n\t" \
  "v_fma_mix_f32 v229, |v165|, v229, v229 op_sel:[0,1,0] op_sel_hi:[0,1,1] clamp\n\t" \
  "v_fma_mix_f32 v228, |v164|, v228, v228 op_sel:[0,1,0] op_sel_hi:[0,1,1] clamp\n\t" \
  "ds_read_b64_tr_b16 v[208:209], v249 offset:6152\n\t" \
  "ds_read_b64_tr_b16 v[210:211], v249 offset:7688\n\t" \
  "v_mfma_f32_16x16x32_bf16 v[204:207], v[212:215], v[124:127], v[204:207]\n\t" \
  "v_fma_mix_f32 v230, |v166|, v230, v230 op_sel:[0,1,0] op_sel_hi:[0,1,1] clamp\n\t" \
  "v_fma_mix_f32 v231, |v167|, v231, v231 op_sel:[0,1,0] op_sel_hi:[0,1,1] clamp\n\t" \
  "s_waitcnt lgkmcnt(5)\n\t" \
  "ds_read_b64_tr_b16 v[212:213], v249 offset:6160\n\t" \
  "ds_read_b32 v222, v222 offset:10016\n\t" \
  "v_mfma_f32_16x16x32_bf16 v[180:183], v[216:219], v[128:131], v[180:183]\n\t" \
  "s_mov_b32 m0, s44\n\t" \
  "v_or_b32 v172, v232, v233\n\t" \
  "v_or_b32 v173, v234, v235\n\t" \
  "s_mov_b32 exec_hi, 0xffff\n\t" \
  "buffer_load_dwordx4 v252, s[16:19], s33 offen lds\n\t" \
  "v_mfma_f32_16x16x32_bf16 v[188:191], v[216:219], v[132:135], v[188:191]\n\t" \
  "s_mov_b32 exec_hi, -1\n\t" \
  "v_fma_f32 v165, |v165|, v229, v165 mul:4\n\t" \
  "v_fma_f32 v164, |v164|, v228, v164 mul:4\n\t" \
  "s_add_u32 s33, s33, s32\n\t" \
  "ds_read_b64_tr_b16 v[214:215], v249 offset:7696\n\t" \
  "ds_read_u16_d16_hi v227, v227 offset:10528\n\t" \
  "v_mfma_f32_16x16x32_bf16 v[196:199], v[216:219], v[136:139], v[196:199]\n\t" \
  "v_fma_f32 v166, |v166|, v230, v166 mul:4\n\t" \
  "v_fma_f32 v167, |v167|, v231, v167 mul:4\n\t" \
  "ds_read_u16_d16_hi v225, v225 offset:10528\n\t" \
  "v_mfma_f32_16x16x32_bf16 v[204:207], v[216:219], v[140:143], v[204:207]\n\t" \
  "v_cvt_pk_bf16_f32 v164, v164, v165\n\t" \
  "v_cvt_pk_bf16_f32 v165, v166, v167\n\t" \
  "s_waitcnt lgkmcnt(5)\n\t" \
  "ds_read_b64_tr_b16 v[216:217], v249 offset:6216\n\t" \
  "v_mfma_f32_16x16x32_bf16 v[0:3], v[208:211], v[146:149], v[0:3]\n\t" \
  "v_cvt_pknorm_i16_f32 v230, |v192|, |v193|\n\t" \
  "v_cvt_pknorm_i16_f32 v228, |v194|, |v195|\n\t" \
  "ds_read_b64_tr_b16 v[218:219], v249 offset:7752\n\t" \
  "v_mfma_f32_16x16x32_bf16 v[24:27], v[208:211], v[154:157], v[24:27]\n\t" \
  "v_lshlrev_b32_sdwa v229, 2, v228 dst_sel:DWORD dst_unused:UNUSED_PAD src0_sel:DWORD src1_sel:BYTE_3\n\t" \
  "v_lshlrev_b32_sdwa v228, 2, v228 dst_sel:DWORD dst_unused:UNUSED_PAD src0_sel:DWORD src1_sel:BYTE_1\n\t" \
  "ds_read_u16 v224, v224 offset:10528\n\t" \
  "v_mfma_f32_16x16x32_bf16 v[48:51], v[208:211], v[162:165], v[48:51]\n\t" \
  "v_lshlrev_b32_sdwa v231, 2, v230 dst_sel:DWORD dst_unused:UNUSED_PAD src0_sel:DWORD src1_sel:BYTE_3\n\t" \
  "s_add_u32 m0, s44, s35\n\t" \
  "s_setprio 1\n\t" \
  "s_waitcnt lgkmcnt(5)\n\t" \
  "ds_read_b32 v228, v228 offset:10016\n\t" \
  "v_mfma_f32_16x16x32_bf16 v[72:75], v[208:211], v[170:173], v[72:75]\n\t" \
  "v_lshlrev_b32_sdwa v233, 1, v203 dst_sel:DWORD dst_unused:UNUSED_PAD src0_sel:DWORD src1_sel:WORD_1\n\t" \
  "v_lshlrev_b32_sdwa v232, 1, v202 dst_sel:DWORD dst_unused:UNUSED_PAD src0_sel:DWORD src1_sel:WORD_1\n\t" \
  "s_mov_b32 exec_hi, 0xffff\n\t" \
  "buffer_load_dwordx4 v252, s[16:19], s34 offen lds\n\t" \
  "v_mfma_f32_16x16x32_bf16 v[4:7], v[212:215], v[146:149], v[4:7]\n\t" \
  "s_mov_b32 exec_hi, -1\n\t" \
  "s_add_u32 s34, s34, s32\n\t" \
  "v_lshlrev_b32_sdwa v235, 1, v201 dst_sel:DWORD dst_unused:UNUSED_PAD src0_sel:DWORD src1_sel:WORD_1\n\t" \
  "ds_read_b64_tr_b16 v[208:209], v249 offset:6224\n\t" \
  "ds_read_u16 v226, v226 offset:10528\n\t" \
  "v_mfma_f32_16x16x32_bf16 v[28:31], v[212:215], v[154:157], v[28:31]\n\t" \
  "v_lshlrev_b32_sdwa v234, 1, v200 dst_sel:DWORD dst_unused:UNUSED_PAD src0_sel:DWORD src1_sel:WORD_1\n\t" \
  "v_lshlrev_b32_sdwa v230, 2, v230 dst_sel:DWORD dst_unused:UNUSED_PAD src0_sel:DWORD src1_sel:BYTE_1\n\t" \
  "ds_read_b64_tr_b16 v[210:211], v249 offset:7760\n\t" \
  "v_mfma_f32_16x16x32_bf16 v[76:79], v[212:215], v[170:173], v[76:79]\n\t" \
  "v_fma_mix_f32 v223, |v177|, v223, v223 op_sel:[0,1,0] op_sel_hi:[0,1,1] clamp\n\t" \
  "v_fma_mix_f32 v222, |v176|, v222, v222 op_sel:[0,1,0] op_sel_hi:[0,1,1] clamp\n\t" \
  "ds_read_b32 v230, v230 offset:10016\n\t" \
  "v_mfma_f32_16x16x32_bf16 v[52:55], v[212:215], v[162:165], v[52:55]\n\t" \
  "v_fma_mix_f32 v221, |v179|, v221, v221 op_sel:[0,1,0] op_sel_hi:[0,1,1] clamp\n\t" \
  "v_fma_mix_f32 v220, |v178|, v220, v220 op_sel:[0,1,0] op_sel_hi:[0,1,1] clamp\n\t" \
  "s_waitcnt lgkmcnt(2)\n\t" \
  "ds_read_b64_tr_b16 v[212:213], v249 offset:6280\n\t" \
  "ds_read_b32 v231, v231 offset:10016\n\t" \
  "v_mfma_f32_16x16x32_bf16 v[8:11], v[216:219], v[146:149], v[8:11]\n\t" \
  "s_setprio 3\n\t" \
  "v_or_b32 v187, v224, v225\n\t" \
  "v_or_b32 v186, v226, v227\n\t" \
  "ds_read_u16_d16_hi v233, v233 offset:10528\n\t" \
  "v_mfma_f32_16x16x32_bf16 v[32:35], v[216:219], v[154:157], v[32:35]\n\t" \
  "v_fma_f32 v178, |v178|, v220, v178 mul:4\n\t" \
  "v_fma_f32 v179, |v179|, v221, v179 mul:4\n\t" \
  "ds_read_b32 v229, v229 offset:10016\n\t" \
  "v_mfma_f32_16x16x32_bf16 v[56:59], v[216:219], v[162:165], v[56:59]\n\t" \
  "v_fma_f32 v176, |v176|, v222, v176 mul:4\n\t" \
  "v_fma_f32 v177, |v177|, v223, v177 mul:4\n\t" \
  "s_add_u32 m0, s45, s50\n\t" \
  "ds_read_u16 v232, v232 offset:10528\n\t" \
  "ds_read_b64_tr_b16 v[214:215], v249 offset:7816\n\t" \
  "v_mfma_f32_16x16x32_bf16 v[80:83], v[216:219], v[170:173], v[80:83]\n\t" \
  "v_cvt_pk_bf16_f32 v179, v178, v179\n\t" \
  "v_cvt_pk_bf16_f32 v178, v176, v177\n\t" \
  "s_setprio 0\n\t" \
  "s_waitcnt lgkmcnt(7)\n\t" \
  "v_mfma_f32_16x16x32_bf16 v[12:15], v[208:211], v[146:149], v[12:15]\n\t" \
  "v_cvt_pknorm_i16_f32 v220, |v180|, |v181|\n\t" \
  "v_cvt_pknorm_i16_f32 v222, |v182|, |v183|\n\t" \
  "s_add_u32 s3, s33, s51\n\t" \
  "ds_read_b64_tr_b16 v[216:217], v249 offset:6288\n\t" \
  "v_mfma_f32_16x16x32_bf16 v[36:39], v[208:211], v[154:157], v[36:39]\n\t" \
  "v_lshlrev_b32_sdwa v221, 2, v220 dst_sel:DWORD dst_unused:UNUSED_PAD src0_sel:DWORD src1_sel:BYTE_3\n\t" \
  "v_lshlrev_b32_sdwa v220, 2, v220 dst_sel:DWORD dst_unused:UNUSED_PAD src0_sel:DWORD src1_sel:BYTE_1\n\t" \
  "s_setprio 1\n\t" \
  "ds_read_u16_d16_hi v235, v235 offset:10528\n\t" \
  "ds_read_b64_tr_b16 v[218:219], v249 offset:7824\n\t" \
  "v_mfma_f32_16x16x32_bf16 v[84:87], v[208:211], v[170:173], v[84:87]\n\t" \
  "v_lshlrev_b32_sdwa v224, 1, v188 dst_sel:DWORD dst_unused:UNUSED_PAD src0_sel:DWORD src1_sel:WORD_1\n\t" \
  "s_waitcnt lgkmcnt(3)\n\t" \
  "v_mfma_f32_16x16x32_bf16 v[60:63], v[208:211], v[162:165], v[60:63]\n\t" \
  "v_lshlrev_b32_sdwa v223, 2, v222 dst_sel:DWORD dst_unused:UNUSED_PAD src0_sel:DWORD src1_sel:BYTE_3\n\t" \
  "v_lshlrev_b32_sdwa v222, 2, v222 dst_sel:DWORD dst_unused:UNUSED_PAD src0_sel:DWORD src1_sel:BYTE_1\n\t" \
  "ds_read_u16 v234, v234 offset:10528\n\t" \
  "ds_read_b32 v220, v220 offset:10016\n\t" \
  "v_lshlrev_b32_sdwa v225, 1, v189 dst_sel:DWORD dst_unused:UNUSED_PAD src0_sel:DWORD src1_sel:WORD_1\n\t" \
  "s_mov_b32 exec_hi, 0xffff\n\t" \
  "buffer_load_dwordx4 v252, s[16:19], s3 offen lds\n\t" \
  "v_mfma_f32_16x16x32_bf16 v[16:19], v[212:215], v[146:149], v[16:19]\n\t" \
  "s_mov_b64 exec, -1\n\t" \
  "s_setprio 2\n\t" \
  "v_nop\n\t" \
  "ds_read_b64_tr_b16 v[208:209], v248 offset:43048\n\t" \
  "v_mfma_f32_16x16x32_bf16 v[64:67], v[212:215], v[162:165], v[64:67]\n\t" \
  "v_fma_mix_f32 v229, |v195|, v229, v229 op_sel:[0,1,0] op_sel_hi:[0,1,1] clamp\n\t" \
  "v_lshlrev_b32_sdwa v227, 1, v191 dst_sel:DWORD dst_unused:UNUSED_PAD src0_sel:DWORD src1_sel:WORD_1\n\t" \
  "ds_read_b32 v221, v221 offset:10016\n\t" \
  "v_mfma_f32_16x16x32_bf16 v[40:43], v[212:215], v[154:157], v[40:43]\n\t" \
  "v_lshlrev_b32_sdwa v226, 1, v190 dst_sel:DWORD dst_unused:UNUSED_PAD src0_sel:DWORD src1_sel:WORD_1\n\t" \
  "v_fma_mix_f32 v228, |v194|, v228, v228 op_sel:[0,1,0] op_sel_hi:[0,1,1] clamp\n\t" \
  "ds_read_b32 v223, v223 offset:10016\n\t" \
  "ds_read_b64_tr_b16 v[210:211], v248 offset:44072\n\t" \
  "v_mfma_f32_16x16x32_bf16 v[88:91], v[212:215], v[170:173], v[88:91]\n\t" \
  "v_fma_mix_f32 v231, |v193|, v231, v231 op_sel:[0,1,0] op_sel_hi:[0,1,1] clamp\n\t" \
  "v_fma_mix_f32 v230, |v192|, v230, v230 op_sel:[0,1,0] op_sel_hi:[0,1,1] clamp\n\t" \
  "s_waitcnt lgkmcnt(5)\n\t" \
  "ds_read_b64_tr_b16 v[212:213], v248 offset:46120\n\t" \
  "ds_read_b32 v222, v222 offset:10016\n\t" \
  "v_mfma_f32_16x16x32_bf16 v[20:23], v[216:219], v[146:149], v[20:23]\n\t" \
  "v_or_b32 v203, v232, v233\n\t" \
  "v_or_b32 v202, v234, v235\n\t" \
  "ds_read_u16_d16_hi v225, v225 offset:10528\n\t" \
  "v_mfma_f32_16x16x32_bf16 v[68:71], v[216:219], v[162:165], v[68:71]\n\t" \
  "v_fma_f32 v193, |v193|, v231, v193 mul:4\n\t" \
  "v_fma_f32 v192, |v192|, v230, v192 mul:4\n\t" \
  "s_setprio 1\n\t" \
  "ds_read_b64_tr_b16 v[214:215], v248 offset:47144\n\t" \
  "s_add_u32 s3, s34, s51\n\t" \
  "v_mfma_f32_16x16x32_bf16 v[44:47], v[216:219], v[154:157], v[44:47]\n\t" \
  "v_fma_f32 v194, |v194|, v228, v194 mul:4\n\t" \
  "v_fma_f32 v195, |v195|, v229, v195 mul:4\n\t" \
  "s_add_u32 m0, s45, s36\n\t" \
  "ds_read_u16 v224, v224 offset:10528\n\t" \
  "s_setprio 2\n\t" \
  "s_waitcnt lgkmcnt(5)\n\t" \
  "v_cvt_pk_bf16_f32 v195, v194, v195\n\t" \
  "s_mov_b32 exec_hi, 0xffff\n\t" \
  "buffer_load_dwordx4 v252, s[16:19], s3 offen lds\n\t" \
  "v_mfma_f32_16x16x32_bf16 v[144:147], v[208:211], v[96:99], 0\n\t" \
  "s_mov_b64 exec, -1\n\t" \
  "v_lshlrev_b32_sdwa v232, 1, v204 dst_sel:DWORD dst_unused:UNUSED_PAD src0_sel:DWORD src1_sel:WORD_1\n\t" \
  "v_cvt_pknorm_i16_f32 v230, |v198|, |v199|\n\t" \
  "ds_read_u16_d16_hi v227, v227 offset:10528\n\t" \
  "v_mfma_f32_16x16x32_bf16 v[92:95], v[216:219], v[170:173], v[92:95]\n\t" \
  "v_nop\n\t" \
  "v_cvt_pk_bf16_f32 v194, v192, v193\n\t" \
  "ds_read_b64_tr_b16 v[216:217], v248 offset:45096\n\t" \
  "v_mfma_f32_16x16x32_bf16 v[160:163], v[208:211], v[104:107], 0\n\t" \
  "v_lshlrev_b32_sdwa v231, 2, v230 dst_sel:DWORD dst_unused:UNUSED_PAD src0_sel:DWORD src1_sel:BYTE_3\n\t" \
  "v_cvt_pknorm_i16_f32 v228, |v196|, |v197|\n\t" \
  "ds_read_b64_tr_b16 v[218:219], v248 offset:48168\n\t" \
  "v_mfma_f32_16x16x32_bf16 v[152:155], v[208:211], v[100:103], 0\n\t" \
  "v_lshlrev_b32_sdwa v229, 2, v228 dst_sel:DWORD dst_unused:UNUSED_PAD src0_sel:DWORD src1_sel:BYTE_3\n\t" \
  "v_lshlrev_b32_sdwa v228, 2, v228 dst_sel:DWORD dst_unused:UNUSED_PAD src0_sel:DWORD src1_sel:BYTE_1\n\t" \
  "ds_read_u16 v226, v226 offset:10528\n\t" \
  "v_mfma_f32_16x16x32_bf16 v[168:171], v[208:211], v[108:111], 0\n\t" \
  "v_nop\n\t" \
  "v_lshlrev_b32_sdwa v230, 2, v230 dst_sel:DWORD dst_unused:UNUSED_PAD src0_sel:DWORD src1_sel:BYTE_1\n\t" \
  "ds_read_b64_tr_b16 v[208:209], v248 offset:43560\n\t" \
  "s_waitcnt lgkmcnt(6)\n\t" \
  "v_mfma_f32_16x16x32_bf16 v[144:147], v[212:215], v[112:115], v[144:147]\n\t" \
  "v_lshlrev_b32_sdwa v235, 1, v207 dst_sel:DWORD dst_unused:UNUSED_PAD src0_sel:DWORD src1_sel:WORD_1\n\t" \
  "v_fma_mix_f32 v221, |v181|, v221, v221 op_sel:[0,1,0] op_sel_hi:[0,1,1] clamp\n\t" \
  "ds_read_b32 v231, v231 offset:10016\n\t" \
  "ds_read_b32 v229, v229 offset:10016\n\t" \
  "v_mfma_f32_16x16x32_bf16 v[168:171], v[212:215], v[124:127], v[168:171]\n\t" \
  "v_lshlrev_b32_sdwa v233, 1, v205 dst_sel:DWORD dst_unused:UNUSED_PAD src0_sel:DWORD src1_sel:WORD_1\n\t" \
  "v_lshlrev_b32_sdwa v234, 1, v206 dst_sel:DWORD dst_unused:UNUSED_PAD src0_sel:DWORD src1_sel:WORD_1\n\t" \
  "s_setprio 3\n\t" \
  "s_setprio 1\n\t" \
  "ds_read_b32 v228, v228 offset:10016\n\t" \
  "v_mfma_f32_16x16x32_bf16 v[160:163], v[212:215], v[120:123], v[160:163]\n\t" \
  "v_fma_mix_f32 v220, |v180|, v220, v220 op_sel:[0,1,0] op_sel_hi:[0,1,1] clamp\n\t" \
  "v_fma_mix_f32 v223, |v183|, v223, v223 op_sel:[0,1,0] op_sel_hi:[0,1,1] clamp\n\t" \
  "ds_read_b64_tr_b16 v[210:211], v248 offset:44584\n\t" \
  "v_mfma_f32_16x16x32_bf16 v[152:155], v[212:215], v[116:119], v[152:155]\n\t" \
  "s_setprio 0\n\t" \
  "s_waitcnt lgkmcnt(6)\n\t" \
  "v_fma_mix_f32 v222, |v182|, v222, v222 op_sel:[0,1,0] op_sel_hi:[0,1,1] clamp\n\t" \
  "ds_read_b64_tr_b16 v[212:213], v248 offset:46632\n\t" \
  "ds_read_u16_d16_hi v233, v233 offset:10528\n\t" \
  "v_mfma_f32_16x16x32_bf16 v[144:147], v[216:219], v[128:131], v[144:147]\n\t" \
  "s_waitcnt lgkmcnt(7)\n\t" \
  "v_or_b32 v188, v224, v225\n\t" \
  "v_or_b32 v189, v226, v227\n\t" \
  "ds_read_b64_tr_b16 v[214:215], v248 offset:47656\n\t" \
  "ds_read_u16_d16_hi v235, v235 offset:10528\n\t" \
  "v_mfma_f32_16x16x32_bf16 v[160:163], v[216:219], v[136:139], v[160:163]\n\t" \
  "v_fma_f32 v183, |v183|, v223, v183 mul:4\n\t" \
  "v_fma_f32 v182, |v182|, v222, v182 mul:4\n\t" \
  "ds_read_b32 v230, v230 offset:10016\n\t" \
  "v_mfma_f32_16x16x32_bf16 v[152:155], v[216:219], v[132:135], v[152:155]\n\t" \
  "v_fma_f32 v181, |v181|, v221, v181 mul:4\n\t" \
  "v_fma_f32 v180, |v180|, v220, v180 mul:4\n\t" \
  "ds_read_u16 v232, v232 offset:10528\n\t" \
  "v_mfma_f32_16x16x32_bf16 v[168:171], v[216:219], v[140:143], v[168:171]\n\t" \
  "v_cvt_pk_bf16_f32 v180, v180, v181\n\t" \
  "v_cvt_pk_bf16_f32 v181, v182, v183\n\t" \
  "s_waitcnt lgkmcnt(6)\n\t" \
  "ds_read_b64_tr_b16 v[216:217], v248 offset:45608\n\t" \
  "v_mfma_f32_16x16x32_bf16 v[148:151], v[208:211], v[96:99], 0\n\t" \
  "v_cvt_pknorm_i16_f32 v222, |v144|, |v145|\n\t" \
  "v_cvt_pknorm_i16_f32 v220, |v146|, |v147|\n\t" \
  "s_nop 0\n\t" \
  "s_waitcnt vmcnt(2)\n\t" \
  "ds_read_b64_tr_b16 v[218:219], v248 offset:48680\n\t" \
  "v_mfma_f32_16x16x32_bf16 v[156:159], v[208:211], v[100:103], 0\n\t" \
  "s_waitcnt lgkmcnt(5)\n\t" \
  "v_lshlrev_b32_sdwa v221, 2, v220 dst_sel:DWORD dst_unused:UNUSED_PAD src0_sel:DWORD src1_sel:BYTE_3\n\t" \
  "v_lshlrev_b32_sdwa v220, 2, v220 dst_sel:DWORD dst_unused:UNUSED_PAD src0_sel:DWORD src1_sel:BYTE_1\n\t" \
  "s_mov_b64 exec, 3\n\t" \
  "ds_add_rtn_u32 v246, v251, v250 offset:47168\n\t" \
  "v_mfma_f32_16x16x32_bf16 v[164:167], v[208:211], v[104:107], 0\n\t" \
  "s_mov_b64 exec, -1\n\t" \
  "v_lshlrev_b32_sdwa v223, 2, v222 dst_sel:DWORD dst_unused:UNUSED_PAD src0_sel:DWORD src1_sel:BYTE_3\n\t" \
  "v_lshlrev_b32_sdwa v222, 2, v222 dst_sel:DWORD dst_unused:UNUSED_PAD src0_sel:DWORD src1_sel:BYTE_1\n\t" \
  "s_setprio 2\n\t" \
  "ds_read_u16 v234, v234 offset:10528\n\t" \
  "s_setprio 0\n\t" \
  "v_mfma_f32_16x16x32_bf16 v[156:159], v[212:215], v[116:119], v[156:159]\n\t" \
  "v_lshlrev_b32_sdwa v227, 1, v153 dst_sel:DWORD dst_unused:UNUSED_PAD src0_sel:DWORD src1_sel:WORD_1\n\t" \
  "v_lshlrev_b32_sdwa v226, 1, v152 dst_sel:DWORD dst_unused:UNUSED_PAD src0_sel:DWORD src1_sel:WORD_1\n\t" \
  "ds_read_b32 v223, v223 offset:10016\n\t" \
  "v_mfma_f32_16x16x32_bf16 v[172:175], v[208:211], v[108:111], 0\n\t" \
  "s_waitcnt lgkmcnt(2)\n\t" \
  "v_lshlrev_b32_sdwa v225, 1, v155 dst_sel:DWORD dst_unused:UNUSED_PAD src0_sel:DWORD src1_sel:WORD_1\n\t" \
  "v_readfirstlane_b32 s4, v246\n\t" \
  "ds_read_b32 v221, v221 offset:10016\n\t" \
  "ds_read_b32 v220, v220 offset:10016\n\t" \
  "v_mfma_f32_16x16x32_bf16 v[148:151], v[212:215], v[112:115], v[148:151]\n\t" \
  "s_cmpk_lt_u32 s4, 6144\n\t" \
  "v_lshlrev_b32_sdwa v224, 1, v154 dst_sel:DWORD dst_unused:UNUSED_PAD src0_sel:DWORD src1_sel:WORD_1\n\t" \
  "s_cbranch_scc1 .Lcnx_w1_5\n\t" \
  ".Lcnx_g1_5:\n\t" \
  "v_mfma_f32_16x16x32_bf16 v[164:167], v[212:215], v[120:123], v[164:167]\n\t" \
  "v_fma_mix_f32 v229, |v197|, v229, v229 op_sel:[0,1,0] op_sel_hi:[0,1,1] clamp\n\t" \
  "v_fma_mix_f32 v228, |v196|, v228, v228 op_sel:[0,1,0] op_sel_hi:[0,1,1] clamp\n\t" \
  "ds_read_b64_tr_b16 v[208:209], v249 offset:12304\n\t" \
  "ds_read_b64_tr_b16 v[210:211], v249 offset:13840\n\t" \
  "v_mfma_f32_16x16x32_bf16 v[172:175], v[212:215], v[124:127], v[172:175]\n\t" \
  "v_fma_mix_f32 v230, |v198|, v230, v230 op_sel:[0,1,0] op_sel_hi:[0,1,1] clamp\n\t" \
  "v_fma_mix_f32 v231, |v199|, v231, v231 op_sel:[0,1,0] op_sel_hi:[0,1,1] clamp\n\t" \
  "s_waitcnt lgkmcnt(5)\n\t" \
  "ds_read_b64_tr_b16 v[212:213], v249 offset:12312\n\t" \
  "ds_read_b32 v222, v222 offset:10016\n\t" \
  "v_mfma_f32_16x16x32_bf16 v[148:151], v[216:219], v[128:131], v[148:151]\n\t" \
  "s_mov_b32 m0, s45\n\t" \
  "v_or_b32 v204, v232, v233\n\t" \
  "v_or_b32 v205, v234, v235\n\t" \
  "s_mov_b32 exec_hi, 0xffff\n\t" \
  "buffer_load_dwordx4 v252, s[16:19], s33 offen lds\n\t" \
  "v_mfma_f32_16x16x32_bf16 v[156:159], v[216:219], v[132:135], v[156:159]\n\t" \
  "s_mov_b32 exec_hi, -1\n\t" \
  "v_fma_f32 v197, |v197|, v229, v197 mul:4\n\t" \
  "v_fma_f32 v196, |v196|, v228, v196 mul:4\n\t" \
  "s_add_u32 s33, s33, s32\n\t" \
  "ds_read_b64_tr_b16 v[214:215], v249 offset:13848\n\t" \
  "ds_read_u16_d16_hi v227, v227 offset:10528\n\t" \
  "v_mfma_f32_16x16x32_bf16 v[164:167], v[216:219], v[136:139], v[164:167]\n\t" \
  "v_fma_f32 v198, |v198|, v230, v198 mul:4\n\t" \
  "v_fma_f32 v199, |v199|, v231, v199 mul:4\n\t" \
  "ds_read_u16_d16_hi v225, v225 offset:10528\n\t" \
  "v_mfma_f32_16x16x32_bf16 v[172:175], v[216:219], v[140:143], v[172:175]\n\t" \
  "v_cvt_pk_bf16_f32 v196, v196, v197\n\t" \
  "v_cvt_pk_bf16_f32 v197, v198, v199\n\t" \
  "s_waitcnt lgkmcnt(5)\n\t" \
  "ds_read_b64_tr_b16 v[216:217], v249 offset:12368\n\t" \
  "v_mfma_f32_16x16x32_bf16 v[0:3], v[208:211], v[178:181], v[0:3]\n\t" \
  "v_cvt_pknorm_i16_f32 v230, |v160|, |v161|\n\t" \
  "v_cvt_pknorm_i16_f32 v228, |v162|, |v163|\n\t" \
  "ds_read_b64_tr_b16 v[218:219], v249 offset:13904\n\t" \
  "v_mfma_f32_16x16x32_bf16 v[24:27], v[208:211], v[186:189], v[24:27]\n\t" \
  "v_lshlrev_b32_sdwa v229, 2, v228 dst_sel:DWORD dst_unused:UNUSED_PAD src0_sel:DWORD src1_sel:BYTE_3\n\t" \
  "v_lshlrev_b32_sdwa v228, 2, v228 dst_sel:DWORD dst_unused:UNUSED_PAD src0_sel:DWORD src1_sel:BYTE_1\n\t" \
  "ds_read_u16 v224, v224 offset:10528\n\t" \
  "v_mfma_f32_16x16x32_bf16 v[48:51], v[208:211], v[194:197], v[48:51]\n\t" \
  "v_lshlrev_b32_sdwa v231, 2, v230 dst_sel:DWORD dst_unused:UNUSED_PAD src0_sel:DWORD src1_sel:BYTE_3\n\t" \
  "s_add_u32 m0, s45, s35\n\t" \
  "s_setprio 1\n\t" \
  "s_waitcnt lgkmcnt(5)\n\t" \
  "ds_read_b32 v228, v228 offset:10016\n\t" \
  "v_mfma_f32_16x16x32_bf16 v[72:75], v[208:211], v[202:205], v[72:75]\n\t" \
  "v_lshlrev_b32_sdwa v233, 1, v171 dst_sel:DWORD dst_unused:UNUSED_PAD src0_sel:DWORD src1_sel:WORD_1\n\t" \
  "v_lshlrev_b32_sdwa v232, 1, v170 dst_sel:DWORD dst_unused:UNUSED_PAD src0_sel:DWORD src1_sel:WORD_1\n\t" \
  "s_mov_b32 exec_hi, 0xffff\n\t" \
  "buffer_load_dwordx4 v252, s[16:19], s34 offen lds\n\t" \
  "v_mfma_f32_16x16x32_bf16 v[4:7], v[212:215], v[178:181], v[4:7]\n\t" \
  "s_mov_b32 exec_hi, -1\n\t" \
  "s_add_u32 s34, s34, s32\n\t" \
  "v_lshlrev_b32_sdwa v235, 1, v169 dst_sel:DWORD dst_unused:UNUSED_PAD src0_sel:DWORD src1_sel:WORD_1\n\t" \
  "ds_read_b64_tr_b16 v[208:209], v249 offset:12376\n\t" \
  "ds_read_u16 v226, v226 offset:10528\n\t" \
  "v_mfma_f32_16x16x32_bf16 v[28:31], v[212:215], v[186:189], v[28:31]\n\t" \
  "v_lshlrev_b32_sdwa v234, 1, v168 dst_sel:DWORD dst_unused:UNUSED_PAD src0_sel:DWORD src1_sel:WORD_1\n\t" \
  "v_lshlrev_b32_sdwa v230, 2, v230 dst_sel:DWORD dst_unused:UNUSED_PAD src0_sel:DWORD src1_sel:BYTE_1\n\t" \
  "ds_read_b64_tr_b16 v[210:211], v249 offset:13912\n\t" \
  "v_mfma_f32_16x16x32_bf16 v[76:79], v[212:215], v[202:205], v[76:79]\n\t" \
  "v_fma_mix_f32 v223, |v145|, v223, v223 op_sel:[0,1,0] op_sel_hi:[0,1,1] clamp\n\t" \
  "v_fma_mix_f32 v222, |v144|, v222, v222 op_sel:[0,1,0] op_sel_hi:[0,1,1] clamp\n\t" \
  "ds_read_b32 v230, v230 offset:10016\n\t" \
  "v_mfma_f32_16x16x32_bf16 v[52:55], v[212:215], v[194:197], v[52:55]\n\t" \
  "v_fma_mix_f32 v221, |v147|, v221, v221 op_sel:[0,1,0] op_sel_hi:[0,1,1] clamp\n\t" \
  "v_fma_mix_f32 v220, |v146|, v220, v220 op_sel:[0,1,0] op_sel_hi:[0,1,1] clamp\n\t" \
  "s_waitcnt lgkmcnt(2)\n\t" \
  "ds_read_b64_tr_b16 v[212:213], v249 offset:12432\n\t" \
  "ds_read_b32 v231, v231 offset:10016\n\t" \
  "v_mfma_f32_16x16x32_bf16 v[8:11], v[216:219], v[178:181], v[8:11]\n\t" \
  "s_setprio 3\n\t" \
  "v_or_b32 v155, v224, v225\n\t" \
  "v_or_b32 v154, v226, v227\n\t" \
  "ds_read_u16_d16_hi v233, v233 offset:10528\n\t" \
  "v_mfma_f32_16x16x32_bf16 v[32:35], v[216:219], v[186:189], v[32:35]\n\t" \
  "v_fma_f32 v146, |v146|, v220, v146 mul:4\n\t" \
  "v_fma_f32 v147, |v147|, v221, v147 mul:4\n\t" \
  "ds_read_b32 v229, v229 offset:10016\n\t" \
  "v_mfma_f32_16x16x32_bf16 v[56:59], v[216:219], v[194:197], v[56:59]\n\t" \
  "v_fma_f32 v144, |v144|, v222, v144 mul:4\n\t" \
  "v_fma_f32 v145, |v145|, v223, v145 mul:4\n\t" \
  "s_add_u32 m0, s46, s50\n\t" \
  "ds_read_u16 v232, v232 offset:10528\n\t" \
  "ds_read_b64_tr_b16 v[214:215], v249 offset:13968\n\t" \
  "v_mfma_f32_16x16x32_bf16 v[80:83], v[216:219], v[202:205], v[80:83]\n\t" \
  "v_cvt_pk_bf16_f32 v147, v146, v147\n\t" \
  "v_cvt_pk_bf16_f32 v146, v144, v145\n\t" \
  "s_setprio 0\n\t" \
  "s_waitcnt lgkmcnt(7)\n\t" \
  "v_mfma_f32_16x16x32_bf16 v[12:15], v[208:211], v[178:181], v[12:15]\n\t" \
  "v_cvt_pknorm_i16_f32 v220, |v148|, |v149|\n\t" \
  "v_cvt_pknorm_i16_f32 v222, |v150|, |v151|\n\t" \
  "s_add_u32 s3, s33, s51\n\t" \
  "ds_read_b64_tr_b16 v[216:217], v249 offset:12440\n\t" \
  "v_mfma_f32_16x16x32_bf16 v[36:39], v[208:211], v[186:189], v[36:39]\n\t" \
  "v_lshlrev_b32_sdwa v221, 2, v220 dst_sel:DWORD dst_unused:UNUSED_PAD src0_sel:DWORD src1_sel:BYTE_3\n\t" \
  "v_lshlrev_b32_sdwa v220, 2, v220 dst_sel:DWORD dst_unused:UNUSED_PAD src0_sel:DWORD src1_sel:BYTE_1\n\t" \
  "s_setprio 1\n\t" \
  "ds_read_u16_d16_hi v235, v235 offset:10528\n\t" \
  "ds_read_b64_tr_b16 v[218:219], v249 offset:13976\n\t" \
  "v_mfma_f32_16x16x32_bf16 v[84:87], v[208:211], v[202:205], v[84:87]\n\t" \
  "v_lshlrev_b32_sdwa v224, 1, v156 dst_sel:DWORD dst_unused:UNUSED_PAD src0_sel:DWORD src1_sel:WORD_1\n\t" \
  "s_waitcnt lgkmcnt(3)\n\t" \
  "v_mfma_f32_16x16x32_bf16 v[60:63], v[208:211], v[194:197], v[60:63]\n\t" \
  "v_lshlrev_b32_sdwa v223, 2, v222 dst_sel:DWORD dst_unused:UNUSED_PAD src0_sel:DWORD src1_sel:BYTE_3\n\t" \
  "v_lshlrev_b32_sdwa v222, 2, v222 dst_sel:DWORD dst_unused:UNUSED_PAD src0_sel:DWORD src1_sel:BYTE_1\n\t" \
  "ds_read_u16 v234, v234 offset:10528\n\t" \
  "ds_read_b32 v220, v220 offset:10016\n\t" \
  "v_lshlrev_b32_sdwa v225, 1, v157 dst_sel:DWORD dst_unused:UNUSED_PAD src0_sel:DWORD src1_sel:WORD_1\n\t" \
  "s_mov_b64 exec, s[68:69]\n\t" \
  "buffer_load_dwordx4 v252, s[16:19], s3 offen lds\n\t" \
  "v_mfma_f32_16x16x32_bf16 v[16:19], v[212:215], v[178:181], v[16:19]\n\t" \
  "s_mov_b64 exec, -1\n\t" \
  "s_setprio 2\n\t" \
  "v_nop\n\t" \
  "ds_read_b64_tr_b16 v[208:209], v248 offset:49192\n\t" \
  "v_mfma_f32_16x16x32_bf16 v[64:67], v[212:215], v[194:197], v[64:67]\n\t" \
  "v_fma_mix_f32 v229, |v163|, v229, v229 op_sel:[0,1,0] op_sel_hi:[0,1,1] clamp\n\t" \
  "v_lshlrev_b32_sdwa v227, 1, v159 dst_sel:DWORD dst_unused:UNUSED_PAD src0_sel:DWORD src1_sel:WORD_1\n\t" \
  "ds_read_b32 v221, v221 offset:10016\n\t" \
  "v_mfma_f32_16x16x32_bf16 v[40:43], v[212:215], v[186:189], v[40:43]\n\t" \
  "v_lshlrev_b32_sdwa v226, 1, v158 dst_sel:DWORD dst_unused:UNUSED_PAD src0_sel:DWORD src1_sel:WORD_1\n\t" \
  "v_fma_mix_f32 v228, |v162|, v228, v228 op_sel:[0,1,0] op_sel_hi:[0,1,1] clamp\n\t" \
  "ds_read_b32 v223, v223 offset:10016\n\t" \
  "ds_read_b64_tr_b16 v[210:211], v248 offset:50216\n\t" \
  "v_mfma_f32_16x16x32_bf16 v[88:91], v[212:215], v[202:205], v[88:91]\n\t" \
  "v_fma_mix_f32 v231, |v161|, v231, v231 op_sel:[0,1,0] op_sel_hi:[0,1,1] clamp\n\t" \
  "v_fma_mix_f32 v230, |v160|, v230, v230 op_sel:[0,1,0] op_sel_hi:[0,1,1] clamp\n\t" \
  "s_waitcnt lgkmcnt(5)\n\t" \
  "ds_read_b64_tr_b16 v[212:213], v248 offset:52264\n\t" \
  "ds_read_b32 v222, v222 offset:10016\n\t" \
  "v_mfma_f32_16x16x32_bf16 v[20:23], v[216:219], v[178:181], v[20:23]\n\t" \
  "v_or_b32 v171, v232, v233\n\t" \
  "v_or_b32 v170, v234, v235\n\t" \
  "ds_read_u16_d16_hi v225, v225 offset:10528\n\t" \
  "v_mfma_f32_16x16x32_bf16 v[68:71], v[216:219], v[194:197], v[68:71]\n\t" \
  "v_fma_f32 v161, |v161|, v231, v161 mul:4\n\t" \
  "v_fma_f32 v160, |v160|, v230, v160 mul:4\n\t" \
  "s_setprio 1\n\t" \
  "ds_read_b64_tr_b16 v[214:215], v248 offset:53288\n\t" \
  "s_add_u32 s3, s34, s51\n\t" \
  "v_mfma_f32_16x16x32_bf16 v[44:47], v[216:219], v[186:189], v[44:47]\n\t" \
  "v_fma_f32 v162, |v162|, v228, v162 mul:4\n\t" \
  "v_fma_f32 v163, |v163|, v229, v163 mul:4\n\t" \
  "s_add_u32 m0, s46, s36\n\t" \
  "ds_read_u16 v224, v224 offset:10528\n\t" \
  "s_setprio 2\n\t" \
  "s_waitcnt lgkmcnt(5)\n\t" \
  "v_cvt_pk_bf16_f32 v163, v162, v163\n\t" \
  "s_mov_b64 exec, s[68:69]\n\t" \
  "buffer_load_dwordx4 v252, s[16:19], s3 offen lds\n\t" \
  "v_mfma_f32_16x16x32_bf16 v[176:179], v[208:211], v[96:99], 0\n\t" \
  "s_mov_b64 exec, -1\n\t" \
  "v_lshlrev_b32_sdwa v232, 1, v172 dst_sel:DWORD dst_unused:UNUSED_PAD src0_sel:DWORD src1_sel:WORD_1\n\t" \
  "v_cvt_pknorm_i16_f32 v230, |v166|, |v167|\n\t" \
  "ds_read_u16_d16_hi v227, v227 offset:10528\n\t" \
  "v_mfma_f32_16x16x32_bf16 v[92:95], v[216:219], v[202:205], v[92:95]\n\t" \
  "v_nop\n\t" \
  "v_cvt_pk_bf16_f32 v162, v160, v161\n\t" \
  "ds_read_b64_tr_b16 v[216:217], v248 offset:51240\n\t" \
  "v_mfma_f32_16x16x32_bf16 v[192:195], v[208:211], v[104:107], 0\n\t" \
  "v_lshlrev_b32_sdwa v231, 2, v230 dst_sel:DWORD dst_unused:UNUSED_PAD src0_sel:DWORD src1_sel:BYTE_3\n\t" \
  "v_cvt_pknorm_i16_f32 v228, |v164|, |v165|\n\t" \
  "ds_read_b64_tr_b16 v[218:219], v248 offset:54312\n\t" \
  "v_mfma_f32_16x16x32_bf16 v[184:187], v[208:211], v[100:103], 0\n\t" \
  "v_lshlrev_b32_sdwa v229, 2, v228 dst_sel:DWORD dst_unused:UNUSED_PAD src0_sel:DWORD src1_sel:BYTE_3\n\t" \
  "v_lshlrev_b32_sdwa v228, 2, v228 dst_sel:DWORD dst_unused:UNUSED_PAD src0_sel:DWORD src1_sel:BYTE_1\n\t" \
  "ds_read_u16 v226, v226 offset:10528\n\t" \
  "v_mfma_f32_16x16x32_bf16 v[200:203], v[208:211], v[108:111], 0\n\t" \
  "v_nop\n\t" \
  "v_lshlrev_b32_sdwa v230, 2, v230 dst_sel:DWORD dst_unused:UNUSED_PAD src0_sel:DWORD src1_sel:BYTE_1\n\t" \
  "ds_read_b64_tr_b16 v[208:209], v248 offset:49704\n\t" \
  "s_waitcnt lgkmcnt(6)\n\t" \
  "v_mfma_f32_16x16x32_bf16 v[176:179], v[212:215], v[112:115], v[176:179]\n\t" \
  "v_lshlrev_b32_sdwa v235, 1, v175 dst_sel:DWORD dst_unused:UNUSED_PAD src0_sel:DWORD src1_sel:WORD_1\n\t" \
  "v_fma_mix_f32 v221, |v149|, v221, v221 op_sel:[0,1,0] op_sel_hi:[0,1,1] clamp\n\t" \
  "ds_read_b32 v231, v231 offset:10016\n\t" \
  "ds_read_b32 v229, v229 offset:10016\n\t" \
  "v_mfma_f32_16x16x32_bf16 v[200:203], v[212:215], v[124:127], v[200:203]\n\t" \
  "v_lshlrev_b32_sdwa v233, 1, v173 dst_sel:DWORD dst_unused:UNUSED_PAD src0_sel:DWORD src1_sel:WORD_1\n\t" \
  "v_lshlrev_b32_sdwa v234, 1, v174 dst_sel:DWORD dst_unused:UNUSED_PAD src0_sel:DWORD src1_sel:WORD_1\n\t" \
  "s_setprio 3\n\t" \
  "s_setprio 1\n\t" \
  "ds_read_b32 v228, v228 offset:10016\n\t" \
  "v_mfma_f32_16x16x32_bf16 v[192:195], v[212:215], v[120:123], v[192:195]\n\t" \
  "v_fma_mix_f32 v220, |v148|, v220, v220 op_sel:[0,1,0] op_sel_hi:[0,1,1] clamp\n\t" \
  "v_fma_mix_f32 v223, |v151|, v223, v223 op_sel:[0,1,0] op_sel_hi:[0,1,1] clamp\n\t" \
  "ds_read_b64_tr_b16 v[210:211], v248 offset:50728\n\t" \
  "v_mfma_f32_16x16x32_bf16 v[184:187], v[212:215], v[116:119], v[184:187]\n\t" \
  "s_setprio 0\n\t" \
  "s_waitcnt lgkmcnt(6)\n\t" \
  "v_fma_mix_f32 v222, |v150|, v222, v222 op_sel:[0,1,0] op_sel_hi:[0,1,1] clamp\n\t" \
  "ds_read_b64_tr_b16 v[212:213], v248 offset:52776\n\t" \
  "ds_read_u16_d16_hi v233, v233 offset:10528\n\t" \
  "v_mfma_f32_16x16x32_bf16 v[176:179], v[216:219], v[128:131], v[176:179]\n\t" \
  "s_waitcnt lgkmcnt(7)\n\t" \
  "v_or_b32 v156, v224, v225\n\t" \
  "v_or_b32 v157, v226, v227\n\t" \
  "ds_read_b64_tr_b16 v[214:215], v248 offset:53800\n\t" \
  "ds_read_u16_d16_hi v235, v235 offset:10528\n\t" \
  "v_mfma_f32_16x16x32_bf16 v[192:195], v[216:219], v[136:139], v[192:195]\n\t" \
  "v_fma_f32 v151, |v151|, v223, v151 mul:4\n\t" \
  "v_fma_f32 v150, |v150|, v222, v150 mul:4\n\t" \
  "ds_read_b32 v230, v230 offset:10016\n\t" \
  "v_mfma_f32_16x16x32_bf16 v[184:187], v[216:219], v[132:135], v[184:187]\n\t" \
  "v_fma_f32 v149, |v149|, v221, v149 mul:4\n\t" \
  "v_fma_f32 v148, |v148|, v220, v148 mul:4\n\t" \
  "ds_read_u16 v232, v232 offset:10528\n\t" \
  "v_mfma_f32_16x16x32_bf16 v[200:203], v[216:219], v[140:143], v[200:203]\n\t" \
  "v_cvt_pk_bf16_f32 v148, v148, v149\n\t" \
  "v_cvt_pk_bf16_f32 v149, v150, v151\n\t" \
  "s_waitcnt lgkmcnt(6)\n\t" \
  "ds_read_b64_tr_b16 v[216:217], v248 offset:51752\n\t" \
  "v_mfma_f32_16x16x32_bf16 v[180:183], v[208:211], v[96:99], 0\n\t" \
  "v_cvt_pknorm_i16_f32 v222, |v176|, |v177|\n\t" \
  "v_cvt_pknorm_i16_f32 v220, |v178|, |v179|\n\t" \
  "s_nop 0\n\t" \
  "s_waitcnt vmcnt(2)\n\t" \
  "ds_read_b64_tr_b16 v[218:219], v248 offset:54824\n\t" \
  "v_mfma_f32_16x16x32_bf16 v[188:191], v[208:211], v[100:103], 0\n\t" \
  "s_waitcnt lgkmcnt(5)\n\t" \
  "v_lshlrev_b32_sdwa v221, 2, v220 dst_sel:DWORD dst_unused:UNUSED_PAD src0_sel:DWORD src1_sel:BYTE_3\n\t" \
  "v_lshlrev_b32_sdwa v220, 2, v220 dst_sel:DWORD dst_unused:UNUSED_PAD src0_sel:DWORD src1_sel:BYTE_1\n\t" \
  "s_mov_b64 exec, 3\n\t" \
  "ds_add_rtn_u32 v246, v251, v250 offset:47176\n\t" \
  "v_mfma_f32_16x16x32_bf16 v[196:199], v[208:211], v[104:107], 0\n\t" \
  "s_mov_b64 exec, -1\n\t" \
  "v_lshlrev_b32_sdwa v223, 2, v222 dst_sel:DWORD dst_unused:UNUSED_PAD src0_sel:DWORD src1_sel:BYTE_3\n\t" \
  "v_lshlrev_b32_sdwa v222, 2, v222 dst_sel:DWORD dst_unused:UNUSED_PAD src0_sel:DWORD src1_sel:BYTE_1\n\t" \
  "s_setprio 2\n\t" \
  "ds_read_u16 v234, v234 offset:10528\n\t" \
  "s_setprio 0\n\t" \
  "v_mfma_f32_16x16x32_bf16 v[188:191], v[212:215], v[116:119], v[188:191]\n\t" \
  "v_lshlrev_b32_sdwa v227, 1, v185 dst_sel:DWORD dst_unused:UNUSED_PAD src0_sel:DWORD src1_sel:WORD_1\n\t" \
  "v_lshlrev_b32_sdwa v226, 1, v184 dst_sel:DWORD dst_unused:UNUSED_PAD src0_sel:DWORD src1_sel:WORD_1\n\t" \
  "ds_read_b32 v223, v223 offset:10016\n\t" \
  "v_mfma_f32_16x16x32_bf16 v[204:207], v[208:211], v[108:111], 0\n\t" \
  "s_waitcnt lgkmcnt(2)\n\t" \
  "v_lshlrev_b32_sdwa v225, 1, v187 dst_sel:DWORD dst_unused:UNUSED_PAD src0_sel:DWORD src1_sel:WORD_1\n\t" \
  "v_readfirstlane_b32 s4, v246\n\t" \
  "ds_read_b32 v221, v221 offset:10016\n\t" \
  "ds_read_b32 v220, v220 offset:10016\n\t" \
  "v_mfma_f32_16x16x32_bf16 v[180:183], v[212:215], v[112:115], v[180:183]\n\t" \
  "s_cmpk_lt_u32 s4, 6144\n\t" \
  "v_lshlrev_b32_sdwa v224, 1, v186 dst_sel:DWORD dst_unused:UNUSED_PAD src0_sel:DWORD src1_sel:WORD_1\n\t" \
  "s_cbranch_scc1 .Lcnx_w1_6\n\t" \
  ".Lcnx_g1_6:\n\t" \
  "v_mfma_f32_16x16x32_bf16 v[196:199], v[212:215], v[120:123], v[196:199]\n\t" \
  "v_fma_mix_f32 v229, |v165|, v229, v229 op_sel:[0,1,0] op_sel_hi:[0,1,1] clamp\n\t" \
  "v_fma_mix_f32 v228, |v164|, v228, v228 op_sel:[0,1,0] op_sel_hi:[0,1,1] clamp\n\t" \
  "ds_read_b64_tr_b16 v[208:209], v249 offset:24600\n\t" \
  "ds_read_b64_tr_b16 v[210:211], v249 offset:26136\n\t" \
  "v_mfma_f32_16x16x32_bf16 v[204:207], v[212:215], v[124:127], v[204:207]\n\t" \
  "v_fma_mix_f32 v230, |v166|, v230, v230 op_sel:[0,1,0] op_sel_hi:[0,1,1] clamp\n\t" \
  "v_fma_mix_f32 v231, |v167|, v231, v231 op_sel:[0,1,0] op_sel_hi:[0,1,1] clamp\n\t" \
  "s_waitcnt lgkmcnt(5)\n\t" \
  "ds_read_b64_tr_b16 v[212:213], v249 offset:24608\n\t" \
  "ds_read_b32 v222, v222 offset:10016\n\t" \
  "v_mfma_f32_16x16x32_bf16 v[180:183], v[216:219], v[128:131], v[180:183]\n\t" \
  "s_mov_b32 m0, s46\n\t" \
  "v_or_b32 v172, v232, v233\n\t" \
  "v_or_b32 v173, v234, v235\n\t" \
  "s_mov_b32 exec_hi, 0xffff\n\t" \
  "buffer_load_dwordx4 v252, s[16:19], s33 offen lds\n\t" \
  "v_mfma_f32_16x16x32_bf16 v[188:191], v[216:219], v[132:135], v[188:191]\n\t" \
  "s_mov_b32 exec_hi, -1\n\t" \
  "v_fma_f32 v165, |v165|, v229, v165 mul:4\n\t" \
  "v_fma_f32 v164, |v164|, v228, v164 mul:4\n\t" \
  "s_add_u32 s33, s33, s32\n\t" \
  "ds_read_b64_tr_b16 v[214:215], v249 offset:26144\n\t" \
  "ds_read_u16_d16_hi v227, v227 offset:10528\n\t" \
  "v_mfma_f32_16x16x32_bf16 v[196:199], v[216:219], v[136:139], v[196:199]\n\t" \
  "v_fma_f32 v166, |v166|, v230, v166 mul:4\n\t" \
  "v_fma_f32 v167, |v167|, v231, v167 mul:4\n\t" \
  "ds_read_u16_d16_hi v225, v225 offset:10528\n\t" \
  "v_mfma_f32_16x16x32_bf16 v[204:207], v[216:219], v[140:143], v[204:207]\n\t" \
  "v_cvt_pk_bf16_f32 v164, v164, v165\n\t" \
  "v_cvt_pk_bf16_f32 v165, v166, v167\n\t" \
  "s_waitcnt lgkmcnt(5)\n\t" \
  "ds_read_b64_tr_b16 v[216:217], v249 offset:24664\n\t" \
  "v_mfma_f32_16x16x32_bf16 v[0:3], v[208:211], v[146:149], v[0:3]\n\t" \
  "v_cvt_pknorm_i16_f32 v230, |v192|, |v193|\n\t" \
  "v_cvt_pknorm_i16_f32 v228, |v194|, |v195|\n\t" \
  "ds_read_b64_tr_b16 v[218:219], v249 offset:26200\n\t" \
  "v_mfma_f32_16x16x32_bf16 v[24:27], v[208:211], v[154:157], v[24:27]\n\t" \
  "v_lshlrev_b32_sdwa v229, 2, v228 dst_sel:DWORD dst_unused:UNUSED_PAD src0_sel:DWORD src1_sel:BYTE_3\n\t" \
  "v_lshlrev_b32_sdwa v228, 2, v228 dst_sel:DWORD dst_unused:UNUSED_PAD src0_sel:DWORD src1_sel:BYTE_1\n\t" \
  "ds_read_u16 v224, v224 offset:10528\n\t" \
  "v_mfma_f32_16x16x32_bf16 v[48:51], v[208:211], v[162:165], v[48:51]\n\t" \
  "v_lshlrev_b32_sdwa v231, 2, v230 dst_sel:DWORD dst_unused:UNUSED_PAD src0_sel:DWORD src1_sel:BYTE_3\n\t" \
  "s_add_u32 m0, s46, s35\n\t" \
  "s_setprio 1\n\t" \
  "s_waitcnt lgkmcnt(5)\n\t" \
  "ds_read_b32 v228, v228 offset:10016\n\t" \
  "v_mfma_f32_16x16x32_bf16 v[72:75], v[208:211], v[170:173], v[72:75]\n\t" \
  "v_lshlrev_b32_sdwa v233, 1, v203 dst_sel:DWORD dst_unused:UNUSED_PAD src0_sel:DWORD src1_sel:WORD_1\n\t" \
  "v_lshlrev_b32_sdwa v232, 1, v202 dst_sel:DWORD dst_unused:UNUSED_PAD src0_sel:DWORD src1_sel:WORD_1\n\t" \
  "s_mov_b32 exec_hi, 0xffff\n\t" \
  "buffer_load_dwordx4 v252, s[16:19], s34 offen lds\n\t" \
  "v_mfma_f32_16x16x32_bf16 v[4:7], v[212:215], v[146:149], v[4:7]\n\t" \
  "s_mov_b32 exec_hi, -1\n\t" \
  "s_add_u32 s34, s34, s32\n\t" \
  "v_lshlrev_b32_sdwa v235, 1, v201 dst_sel:DWORD dst_unused:UNUSED_PAD src0_sel:DWORD src1_sel:WORD_1\n\t" \
  "ds_read_b64_tr_b16 v[208:209], v249 offset:24672\n\t" \
  "ds_read_u16 v226, v226 offset:10528\n\t" \
  "v_mfma_f32_16x16x32_bf16 v[28:31], v[212:215], v[154:157], v[28:31]\n\t" \
  "v_lshlrev_b32_sdwa v234, 1, v200 dst_sel:DWORD dst_unused:UNUSED_PAD src0_sel:DWORD src1_sel:WORD_1\n\t" \
  "v_lshlrev_b32_sdwa v230, 2, v230 dst_sel:DWORD dst_unused:UNUSED_PAD src0_sel:DWORD src1_sel:BYTE_1\n\t" \
  "ds_read_b64_tr_b16 v[210:211], v249 offset:26208\n\t" \
  "v_mfma_f32_16x16x32_bf16 v[76:79], v[212:215], v[170:173], v[76:79]\n\t" \
  "v_fma_mix_f32 v223, |v177|, v223, v223 op_sel:[0,1,0] op_sel_hi:[0,1,1] clamp\n\t" \
  "v_fma_mix_f32 v222, |v176|, v222, v222 op_sel:[0,1,0] op_sel_hi:[0,1,1] clamp\n\t" \
  "ds_read_b32 v230, v230 offset:10016\n\t" \
  "v_mfma_f32_16x16x32_bf16 v[52:55], v[212:215], v[162:165], v[52:55]\n\t" \
  "v_fma_mix_f32 v221, |v179|, v221, v221 op_sel:[0,1,0] op_sel_hi:[0,1,1] clamp\n\t" \
  "v_fma_mix_f32 v220, |v178|, v220, v220 op_sel:[0,1,0] op_sel_hi:[0,1,1] clamp\n\t" \
  "s_waitcnt lgkmcnt(2)\n\t" \
  "ds_read_b64_tr_b16 v[212:213], v249 offset:24728\n\t" \
  "ds_read_b32 v231, v231 offset:10016\n\t" \
  "v_mfma_f32_16x16x32_bf16 v[8:11], v[216:219], v[146:149], v[8:11]\n\t" \
  "s_setprio 3\n\t" \
  "v_or_b32 v187, v224, v225\n\t" \
  "v_or_b32 v186, v226, v227\n\t" \
  "ds_read_u16_d16_hi v233, v233 offset:10528\n\t" \
  "v_mfma_f32_16x16x32_bf16 v[32:35], v[216:219], v[154:157], v[32:35]\n\t" \
  "v_fma_f32 v178, |v178|, v220, v178 mul:4\n\t" \
  "v_fma_f32 v179, |v179|, v221, v179 mul:4\n\t" \
  "ds_read_b32 v229, v229 offset:10016\n\t" \
  "v_mfma_f32_16x16x32_bf16 v[56:59], v[216:219], v[162:165], v[56:59]\n\t" \
  "v_fma_f32 v176, |v176|, v222, v176 mul:4\n\t" \
  "v_fma_f32 v177, |v177|, v223, v177 mul:4\n\t" \
  "s_add_u32 m0, s47, s50\n\t" \
  "ds_read_u16 v232, v232 offset:10528\n\t" \
  "ds_read_b64_tr_b16 v[214:215], v249 offset:26264\n\t" \
  "v_mfma_f32_16x16x32_bf16 v[80:83], v[216:219], v[170:173], v[80:83]\n\t" \
  "v_cvt_pk_bf16_f32 v179, v178, v179\n\t" \
  "v_cvt_pk_bf16_f32 v178, v176, v177\n\t" \
  "s_setprio 0\n\t" \
  "s_waitcnt lgkmcnt(7)\n\t" \
  "v_mfma_f32_16x16x32_bf16 v[12:15], v[208:211], v[146:149], v[12:15]\n\t" \
  "v_cvt_pknorm_i16_f32 v220, |v180|, |v181|\n\t" \
  "v_cvt_pknorm_i16_f32 v222, |v182|, |v183|\n\t" \
  "s_add_u32 s3, s33, s51\n\t" \
  "ds_read_b64_tr_b16 v[216:217], v249 offset:24736\n\t" \
  "v_mfma_f32_16x16x32_bf16 v[36:39], v[208:211], v[154:157], v[36:39]\n\t" \
  "v_lshlrev_b32_sdwa v221, 2, v220 dst_sel:DWORD dst_unused:UNUSED_PAD src0_sel:DWORD src1_sel:BYTE_3\n\t" \
  "v_lshlrev_b32_sdwa v220, 2, v220 dst_sel:DWORD dst_unused:UNUSED_PAD src0_sel:DWORD src1_sel:BYTE_1\n\t" \
  "s_setprio 1\n\t" \
  "ds_read_u16_d16_hi v235, v235 offset:10528\n\t" \
  "ds_read_b64_tr_b16 v[218:219], v249 offset:26272\n\t" \
  "v_mfma_f32_16x16x32_bf16 v[84:87], v[208:211], v[170:173], v[84:87]\n\t" \
  "v_lshlrev_b32_sdwa v224, 1, v188 dst_sel:DWORD dst_unused:UNUSED_PAD src0_sel:DWORD src1_sel:WORD_1\n\t" \
  "s_waitcnt lgkmcnt(3)\n\t" \
  "v_mfma_f32_16x16x32_bf16 v[60:63], v[208:211], v[162:165], v[60:63]\n\t" \
  "v_lshlrev_b32_sdwa v223, 2, v222 dst_sel:DWORD dst_unused:UNUSED_PAD src0_sel:DWORD src1_sel:BYTE_3\n\t" \
  "v_lshlrev_b32_sdwa v222, 2, v222 dst_sel:DWORD dst_unused:UNUSED_PAD src0_sel:DWORD src1_sel:BYTE_1\n\t" \
  "ds_read_u16 v234, v234 offset:10528\n\t" \
  "ds_read_b32 v220, v220 offset:10016\n\t" \
  "v_lshlrev_b32_sdwa v225, 1, v189 dst_sel:DWORD dst_unused:UNUSED_PAD src0_sel:DWORD src1_sel:WORD_1\n\t" \
  "s_mov_b64 exec, s[68:69]\n\t" \
  "buffer_load_dwordx4 v252, s[16:19], s3 offen lds\n\t" \
  "v_mfma_f32_16x16x32_bf16 v[16:19], v[212:215], v[146:149], v[16:19]\n\t" \
  "s_mov_b64 exec, -1\n\t" \
  "s_setprio 2\n\t" \
  "v_nop\n\t" \
  "ds_read_b64_tr_b16 v[208:209], v248 offset:36904\n\t" \
  "v_mfma_f32_16x16x32_bf16 v[64:67], v[212:215], v[162:165], v[64:67]\n\t" \
  "v_fma_mix_f32 v229, |v195|, v229, v229 op_sel:[0,1,0] op_sel_hi:[0,1,1] clamp\n\t" \
  "v_lshlrev_b32_sdwa v227, 1, v191 dst_sel:DWORD dst_unused:UNUSED_PAD src0_sel:DWORD src1_sel:WORD_1\n\t" \
  "ds_read_b32 v221, v221 offset:10016\n\t" \
  "v_mfma_f32_16x16x32_bf16 v[40:43], v[212:215], v[154:157], v[40:43]\n\t" \
  "v_lshlrev_b32_sdwa v226, 1, v190 dst_sel:DWORD dst_unused:UNUSED_PAD src0_sel:DWORD src1_sel:WORD_1\n\t" \
  "v_fma_mix_f32 v228, |v194|, v228, v228 op_sel:[0,1,0] op_sel_hi:[0,1,1] clamp\n\t" \
  "ds_read_b32 v223, v223 offset:10016\n\t" \
  "ds_read_b64_tr_b16 v[210:211], v248 offset:37928\n\t" \
  "v_mfma_f32_16x16x32_bf16 v[88:91], v[212:215], v[170:173], v[88:91]\n\t" \
  "v_fma_mix_f32 v231, |v193|, v231, v231 op_sel:[0,1,0] op_sel_hi:[0,1,1] clamp\n\t" \
  "v_fma_mix_f32 v230, |v192|, v230, v230 op_sel:[0,1,0] op_sel_hi:[0,1,1] clamp\n\t" \
  "s_waitcnt lgkmcnt(5)\n\t" \
  "ds_read_b64_tr_b16 v[212:213], v248 offset:39976\n\t" \
  "ds_read_b32 v222, v222 offset:10016\n\t" \
  "v_mfma_f32_16x16x32_bf16 v[20:23], v[216:219], v[146:149], v[20:23]\n\t" \
  "v_or_b32 v203, v232, v233\n\t" \
  "v_or_b32 v202, v234, v235\n\t" \
  "ds_read_u16_d16_hi v225, v225 offset:10528\n\t" \
  "v_mfma_f32_16x16x32_bf16 v[68:71], v[216:219], v[162:165], v[68:71]\n\t" \
  "v_fma_f32 v193, |v193|, v231, v193 mul:4\n\t" \
  "v_fma_f32 v192, |v192|, v230, v192 mul:4\n\t" \
  "s_setprio 1\n\t" \
  "ds_read_b64_tr_b16 v[214:215], v248 offset:41000\n\t" \
  "s_add_u32 s3, s34, s51\n\t" \
  "v_mfma_f32_16x16x32_bf16 v[44:47], v[216:219], v[154:157], v[44:47]\n\t" \
  "v_fma_f32 v194, |v194|, v228, v194 mul:4\n\t" \
  "v_fma_f32 v195, |v195|, v229, v195 mul:4\n\t" \
  "s_add_u32 m0, s47, s36\n\t" \
  "ds_read_u16 v224, v224 offset:10528\n\t" \
  "s_setprio 2\n\t" \
  "s_waitcnt lgkmcnt(5)\n\t" \
  "v_cvt_pk_bf16_f32 v195, v194, v195\n\t" \
  "s_mov_b64 exec, s[68:69]\n\t" \
  "buffer_load_dwordx4 v252, s[16:19], s3 offen lds\n\t" \
  "v_mfma_f32_16x16x32_bf16 v[144:147], v[208:211], v[96:99], 0\n\t" \
  "s_mov_b64 exec, -1\n\t" \
  "v_lshlrev_b32_sdwa v232, 1, v204 dst_sel:DWORD dst_unused:UNUSED_PAD src0_sel:DWORD src1_sel:WORD_1\n\t" \
  "v_cvt_pknorm_i16_f32 v230, |v198|, |v199|\n\t" \
  "ds_read_u16_d16_hi v227, v227 offset:10528\n\t" \
  "v_mfma_f32_16x16x32_bf16 v[92:95], v[216:219], v[170:173], v[92:95]\n\t" \
  "v_nop\n\t" \
  "v_cvt_pk_bf16_f32 v194, v192, v193\n\t" \
  "ds_read_b64_tr_b16 v[216:217], v248 offset:38952\n\t" \
  "v_mfma_f32_16x16x32_bf16 v[160:163], v[208:211], v[104:107], 0\n\t" \
  "v_lshlrev_b32_sdwa v231, 2, v230 dst_sel:DWORD dst_unused:UNUSED_PAD src0_sel:DWORD src1_sel:BYTE_3\n\t" \
  "v_cvt_pknorm_i16_f32 v228, |v196|, |v197|\n\t" \
  "ds_read_b64_tr_b16 v[218:219], v248 offset:42024\n\t" \
  "v_mfma_f32_16x16x32_bf16 v[152:155], v[208:211], v[100:103], 0\n\t" \
  "v_lshlrev_b32_sdwa v229, 2, v228 dst_sel:DWORD dst_unused:UNUSED_PAD src0_sel:DWORD src1_sel:BYTE_3\n\t" \
  "v_lshlrev_b32_sdwa v228, 2, v228 dst_sel:DWORD dst_unused:UNUSED_PAD src0_sel:DWORD src1_sel:BYTE_1\n\t" \
  "ds_read_u16 v226, v226 offset:10528\n\t" \
  "v_mfma_f32_16x16x32_bf16 v[168:171], v[208:211], v[108:111], 0\n\t" \
  "v_nop\n\t" \
  "v_lshlrev_b32_sdwa v230, 2, v230 dst_sel:DWORD dst_unused:UNUSED_PAD src0_sel:DWORD src1_sel:BYTE_1\n\t" \
  "ds_read_b64_tr_b16 v[208:209], v248 offset:37416\n\t" \
  "s_waitcnt lgkmcnt(6)\n\t" \
  "v_mfma_f32_16x16x32_bf16 v[144:147], v[212:215], v[112:115], v[144:147]\n\t" \
  "v_lshlrev_b32_sdwa v235, 1, v207 dst_sel:DWORD dst_unused:UNUSED_PAD src0_sel:DWORD src1_sel:WORD_1\n\t" \
  "v_fma_mix_f32 v221, |v181|, v221, v221 op_sel:[0,1,0] op_sel_hi:[0,1,1] clamp\n\t" \
  "ds_read_b32 v231, v231 offset:10016\n\t" \
  "ds_read_b32 v229, v229 offset:10016\n\t" \
  "v_mfma_f32_16x16x32_bf16 v[168:171], v[212:215], v[124:127], v[168:171]\n\t" \
  "v_lshlrev_b32_sdwa v233, 1, v205 dst_sel:DWORD dst_unused:UNUSED_PAD src0_sel:DWORD src1_sel:WORD_1\n\t" \
  "v_lshlrev_b32_sdwa v234, 1, v206 dst_sel:DWORD dst_unused:UNUSED_PAD src0_sel:DWORD src1_sel:WORD_1\n\t" \
  "s_setprio 3\n\t" \
  "s_setprio 1\n\t" \
  "ds_read_b32 v228, v228 offset:10016\n\t" \
  "v_mfma_f32_16x16x32_bf16 v[160:163], v[212:215], v[120:123], v[160:163]\n\t" \
  "v_fma_mix_f32 v220, |v180|, v220, v220 op_sel:[0,1,0] op_sel_hi:[0,1,1] clamp\n\t" \
  "v_fma_mix_f32 v223, |v183|, v223, v223 op_sel:[0,1,0] op_sel_hi:[0,1,1] clamp\n\t" \
  "ds_read_b64_tr_b16 v[210:211], v248 offset:38440\n\t" \
  "v_mfma_f32_16x16x32_bf16 v[152:155], v[212:215], v[116:119], v[152:155]\n\t" \
  "s_setprio 0\n\t" \
  "s_waitcnt lgkmcnt(6)\n\t" \
  "v_fma_mix_f32 v222, |v182|, v222, v222 op_sel:[0,1,0] op_sel_hi:[0,1,1] clamp\n\t" \
  "ds_read_b64_tr_b16 v[212:213], v248 offset:40488\n\t" \
  "ds_read_u16_d16_hi v233, v233 offset:10528\n\t" \
  "v_mfma_f32_16x16x32_bf16 v[144:147], v[216:219], v[128:131], v[144:147]\n\t" \
  "s_waitcnt lgkmcnt(7)\n\t" \
  "v_or_b32 v188, v224, v225\n\t" \
  "v_or_b32 v189, v226, v227\n\t" \
  "ds_read_b64_tr_b16 v[214:215], v248 offset:41512\n\t" \
  "ds_read_u16_d16_hi v235, v235 offset:10528\n\t" \
  "v_mfma_f32_16x16x32_bf16 v[160:163], v[216:219], v[136:139], v[160:163]\n\t" \
  "v_fma_f32 v183, |v183|, v223, v183 mul:4\n\t" \
  "v_fma_f32 v182, |v182|, v222, v182 mul:4\n\t" \
  "ds_read_b32 v230, v230 offset:10016\n\t" \
  "v_mfma_f32_16x16x32_bf16 v[152:155], v[216:219], v[132:135], v[152:155]\n\t" \
  "v_fma_f32 v181, |v181|, v221, v181 mul:4\n\t" \
  "v_fma_f32 v180, |v180|, v220, v180 mul:4\n\t" \
  "ds_read_u16 v232, v232 offset:10528\n\t" \
  "v_mfma_f32_16x16x32_bf16 v[168:171], v[216:219], v[140:143], v[168:171]\n\t" \
  "v_cvt_pk_bf16_f32 v180, v180, v181\n\t" \
  "v_cvt_pk_bf16_f32 v181, v182, v183\n\t" \
  "s_waitcnt lgkmcnt(6)\n\t" \
  "ds_read_b64_tr_b16 v[216:217], v248 offset:39464\n\t" \
  "v_mfma_f32_16x16x32_bf16 v[148:151], v[208:211], v[96:99], 0\n\t" \
  "v_cvt_pknorm_i16_f32 v222, |v144|, |v145|\n\t" \
  "v_cvt_pknorm_i16_f32 v220, |v146|, |v147|\n\t" \
  "s_waitcnt vmcnt(2)\n\t" \
  "ds_read_b64_tr_b16 v[218:219], v248 offset:42536\n\t" \
  "v_mfma_f32_16x16x32_bf16 v[156:159], v[208:211], v[100:103], 0\n\t" \
  "v_lshlrev_b32_sdwa v221, 2, v220 dst_sel:DWORD dst_unused:UNUSED_PAD src0_sel:DWORD src1_sel:BYTE_3\n\t" \
  "v_lshlrev_b32_sdwa v220, 2, v220 dst_sel:DWORD dst_unused:UNUSED_PAD src0_sel:DWORD src1_sel:BYTE_1\n\t" \
  "s_mov_b64 exec, 3\n\t" \
  "ds_add_rtn_u32 v246, v251, v250 offset:47184\n\t" \
  "v_mfma_f32_16x16x32_bf16 v[164:167], v[208:211], v[104:107], 0\n\t" \
  "s_mov_b64 exec, -1\n\t" \
  "v_lshlrev_b32_sdwa v223, 2, v222 dst_sel:DWORD dst_unused:UNUSED_PAD src0_sel:DWORD src1_sel:BYTE_3\n\t" \
  "v_lshlrev_b32_sdwa v222, 2, v222 dst_sel:DWORD dst_unused:UNUSED_PAD src0_sel:DWORD src1_sel:BYTE_1\n\t" \
  "s_waitcnt lgkmcnt(6)\n\t" \
  "s_setprio 2\n\t" \
  "s_setprio 0\n\t" \
  "ds_read_u16 v234, v234 offset:10528\n\t" \
  "v_mfma_f32_16x16x32_bf16 v[156:159], v[212:215], v[116:119], v[156:159]\n\t" \
  "s_nop 0\n\t" \
  "v_lshlrev_b32_sdwa v227, 1, v153 dst_sel:DWORD dst_unused:UNUSED_PAD src0_sel:DWORD src1_sel:WORD_1\n\t" \
  "v_lshlrev_b32_sdwa v226, 1, v152 dst_sel:DWORD dst_unused:UNUSED_PAD src0_sel:DWORD src1_sel:WORD_1\n\t" \
  "ds_read_b32 v223, v223 offset:10016\n\t" \
  "v_mfma_f32_16x16x32_bf16 v[172:175], v[208:211], v[108:111], 0\n\t" \
  "s_waitcnt lgkmcnt(2)\n\t" \
  "v_lshlrev_b32_sdwa v225, 1, v155 dst_sel:DWORD dst_unused:UNUSED_PAD src0_sel:DWORD src1_sel:WORD_1\n\t" \
  "v_readfirstlane_b32 s4, v246\n\t" \
  "ds_read_b32 v221, v221 offset:10016\n\t" \
  "v_mfma_f32_16x16x32_bf16 v[148:151], v[212:215], v[112:115], v[148:151]\n\t" \
  "s_cmpk_lt_u32 s4, 6144\n\t" \
  "v_lshlrev_b32_sdwa v224, 1, v154 dst_sel:DWORD dst_unused:UNUSED_PAD src0_sel:DWORD src1_sel:WORD_1\n\t" \
  "s_cbranch_scc1 .Lcnx_w1_7\n\t" \
  ".Lcnx_g1_7:\n\t" \
  "ds_read_b32 v220, v220 offset:10016\n\t" \
  "v_mfma_f32_16x16x32_bf16 v[164:167], v[212:215], v[120:123], v[164:167]\n\t" \
  "v_fma_mix_f32 v228, |v196|, v228, v228 op_sel:[0,1,0] op_sel_hi:[0,1,1] clamp\n\t" \
  "v_fma_mix_f32 v229, |v197|, v229, v229 op_sel:[0,1,0] op_sel_hi:[0,1,1] clamp\n\t" \
  "ds_read_b64_tr_b16 v[208:209], v249 offset:30752\n\t" \
  "ds_read_b64_tr_b16 v[210:211], v249 offset:32288\n\t" \
  "v_mfma_f32_16x16x32_bf16 v[172:175], v[212:215], v[124:127], v[172:175]\n\t" \
  "v_fma_mix_f32 v230, |v198|, v230, v230 op_sel:[0,1,0] op_sel_hi:[0,1,1] clamp\n\t" \
  "v_fma_mix_f32 v231, |v199|, v231, v231 op_sel:[0,1,0] op_sel_hi:[0,1,1] clamp\n\t" \
  "s_waitcnt lgkmcnt(5)\n\t" \
  "ds_read_b64_tr_b16 v[212:213], v249 offset:30760\n\t" \
  "ds_read_b32 v222, v222 offset:10016\n\t" \
  "v_mfma_f32_16x16x32_bf16 v[148:151], v[216:219], v[128:131], v[148:151]\n\t" \
  "s_mov_b32 m0, s47\n\t" \
  "v_or_b32 v204, v232, v233\n\t" \
  "v_or_b32 v205, v234, v235\n\t" \
  "s_mov_b32 exec_hi, 0xffff\n\t" \
  "buffer_load_dwordx4 v252, s[16:19], s33 offen lds\n\t" \
  "v_mfma_f32_16x16x32_bf16 v[156:159], v[216:219], v[132:135], v[156:159]\n\t" \
  "s_mov_b32 exec_hi, -1\n\t" \
  "v_fma_f32 v197, |v197|, v229, v197 mul:4\n\t" \
  "v_fma_f32 v196, |v196|, v228, v196 mul:4\n\t" \
  "s_add_u32 s33, s33, s32\n\t" \
  "ds_read_b64_tr_b16 v[214:215], v249 offset:32296\n\t" \
  "ds_read_u16_d16_hi v227, v227 offset:10528\n\t" \
  "v_mfma_f32_16x16x32_bf16 v[164:167], v[216:219], v[136:139], v[164:167]\n\t" \
  "v_fma_f32 v198, |v198|, v230, v198 mul:4\n\t" \
  "v_fma_f32 v199, |v199|, v231, v199 mul:4\n\t" \
  "s_waitcnt lgkmcnt(4)\n\t" \
  "ds_read_u16_d16_hi v225, v225 offset:10528\n\t" \
  "v_mfma_f32_16x16x32_bf16 v[172:175], v[216:219], v[140:143], v[172:175]\n\t" \
  "v_cvt_pk_bf16_f32 v196, v196, v197\n\t" \
  "v_cvt_pk_bf16_f32 v197, v198, v199\n\t" \
  "ds_read_b64_tr_b16 v[216:217], v249 offset:30816\n\t" \
  "v_mfma_f32_16x16x32_bf16 v[0:3], v[208:211], v[178:181], v[0:3]\n\t" \
  "v_cvt_pknorm_i16_f32 v230, |v160|, |v161|\n\t" \
  "v_cvt_pknorm_i16_f32 v228, |v162|, |v163|\n\t" \
  "ds_read_b64_tr_b16 v[218:219], v249 offset:32352\n\t" \
  "v_mfma_f32_16x16x32_bf16 v[24:27], v[208:211], v[186:189], v[24:27]\n\t" \
  "v_lshlrev_b32_sdwa v229, 2, v228 dst_sel:DWORD dst_unused:UNUSED_PAD src0_sel:DWORD src1_sel:BYTE_3\n\t" \
  "v_lshlrev_b32_sdwa v228, 2, v228 dst_sel:DWORD dst_unused:UNUSED_PAD src0_sel:DWORD src1_sel:BYTE_1\n\t" \
  "ds_read_u16 v224, v224 offset:10528\n\t" \
  "v_mfma_f32_16x16x32_bf16 v[48:51], v[208:211], v[194:197], v[48:51]\n\t" \
  "s_add_u32 m0, s47, s35\n\t" \
  "s_setprio 1\n\t" \
  "v_lshlrev_b32_sdwa v231, 2, v230 dst_sel:DWORD dst_unused:UNUSED_PAD src0_sel:DWORD src1_sel:BYTE_3\n\t" \
  "s_waitcnt lgkmcnt(5)\n\t" \
  "ds_read_b32 v228, v228 offset:10016\n\t" \
  "v_mfma_f32_16x16x32_bf16 v[72:75], v[208:211], v[202:205], v[72:75]\n\t" \
  "v_lshlrev_b32_sdwa v232, 1, v170 dst_sel:DWORD dst_unused:UNUSED_PAD src0_sel:DWORD src1_sel:WORD_1\n\t" \
  "v_lshlrev_b32_sdwa v233, 1, v171 dst_sel:DWORD dst_unused:UNUSED_PAD src0_sel:DWORD src1_sel:WORD_1\n\t" \
  "s_mov_b32 exec_hi, 0xffff\n\t" \
  "buffer_load_dwordx4 v252, s[16:19], s34 offen lds\n\t" \
  "v_mfma_f32_16x16x32_bf16 v[4:7], v[212:215], v[178:181], v[4:7]\n\t" \
  "s_mov_b32 exec_hi, -1\n\t" \
  "s_add_u32 s34, s34, s32\n\t" \
  "v_lshlrev_b32_sdwa v235, 1, v169 dst_sel:DWORD dst_unused:UNUSED_PAD src0_sel:DWORD src1_sel:WORD_1\n\t" \
  "ds_read_b64_tr_b16 v[208:209], v249 offset:30824\n\t" \
  "ds_read_u16 v226, v226 offset:10528\n\t" \
  "v_mfma_f32_16x16x32_bf16 v[28:31], v[212:215], v[186:189], v[28:31]\n\t" \
  "v_lshlrev_b32_sdwa v234, 1, v168 dst_sel:DWORD dst_unused:UNUSED_PAD src0_sel:DWORD src1_sel:WORD_1\n\t" \
  "v_lshlrev_b32_sdwa v230, 2, v230 dst_sel:DWORD dst_unused:UNUSED_PAD src0_sel:DWORD src1_sel:BYTE_1\n\t" \
  "ds_read_b64_tr_b16 v[210:211], v249 offset:32360\n\t" \
  "ds_read_b32 v229, v229 offset:10016\n\t" \
  "v_mfma_f32_16x16x32_bf16 v[76:79], v[212:215], v[202:205], v[76:79]\n\t" \
  "v_fma_mix_f32 v223, |v145|, v223, v223 op_sel:[0,1,0] op_sel_hi:[0,1,1] clamp\n\t" \
  "v_fma_mix_f32 v222, |v144|, v222, v222 op_sel:[0,1,0] op_sel_hi:[0,1,1] clamp\n\t" \
  "ds_read_b32 v230, v230 offset:10016\n\t" \
  "v_mfma_f32_16x16x32_bf16 v[52:55], v[212:215], v[194:197], v[52:55]\n\t" \
  "v_fma_mix_f32 v221, |v147|, v221, v221 op_sel:[0,1,0] op_sel_hi:[0,1,1] clamp\n\t" \
  "v_fma_mix_f32 v220, |v146|, v220, v220 op_sel:[0,1,0] op_sel_hi:[0,1,1] clamp\n\t" \
  "s_waitcnt lgkmcnt(3)\n\t" \
  "s_setprio 3\n\t" \
  "ds_read_b64_tr_b16 v[212:213], v249 offset:30880\n\t" \
  "ds_read_b32 v231, v231 offset:10016\n\t" \
  "v_mfma_f32_16x16x32_bf16 v[8:11], v[216:219], v[178:181], v[8:11]\n\t" \
  "v_or_b32 v155, v224, v225\n\t" \
  "v_or_b32 v154, v226, v227\n\t" \
  "ds_read_u16_d16_hi v233, v233 offset:10528\n\t" \
  "v_mfma_f32_16x16x32_bf16 v[32:35], v[216:219], v[186:189], v[32:35]\n\t" \
  "v_fma_f32 v147, |v147|, v221, v147 mul:4\n\t" \
  "v_fma_f32 v146, |v146|, v220, v146 mul:4\n\t" \
  "buffer_load_dwordx4 v[236:239], v250, s[8:11], s14 offen offset:0 nt\n\t" \
  "ds_read_b64_tr_b16 v[214:215], v249 offset:32416\n\t" \
  "v_mfma_f32_16x16x32_bf16 v[56:59], v[216:219], v[194:197], v[56:59]\n\t" \
  "v_fma_f32 v145, |v145|, v223, v145 mul:4\n\t" \
  "v_fma_f32 v144, |v144|, v222, v144 mul:4\n\t" \
  "ds_read_u16 v232, v232 offset:10528\n\t" \
  "v_mfma_f32_16x16x32_bf16 v[80:83], v[216:219], v[202:205], v[80:83]\n\t" \
  "v_cvt_pk_bf16_f32 v147, v146, v147\n\t" \
  "v_cvt_pk_bf16_f32 v146, v144, v145\n\t" \
  "s_waitcnt lgkmcnt(7)\n\t" \
  "s_setprio 0\n\t" \
  "v_mfma_f32_16x16x32_bf16 v[12:15], v[208:211], v[178:181], v[12:15]\n\t" \
  "v_cvt_pknorm_i16_f32 v220, |v148|, |v149|\n\t" \
  "v_cvt_pknorm_i16_f32 v222, |v150|, |v151|\n\t" \
  "ds_read_b64_tr_b16 v[216:217], v249 offset:30888\n\t" \
  "ds_read_b64_tr_b16 v[218:219], v249 offset:32424\n\t" \
  "v_mfma_f32_16x16x32_bf16 v[36:39], v[208:211], v[186:189], v[36:39]\n\t" \
  "v_lshlrev_b32_sdwa v221, 2, v220 dst_sel:DWORD dst_unused:UNUSED_PAD src0_sel:DWORD src1_sel:BYTE_3\n\t" \
  "v_lshlrev_b32_sdwa v220, 2, v220 dst_sel:DWORD dst_unused:UNUSED_PAD src0_sel:DWORD src1_sel:BYTE_1\n\t" \
  "s_setprio 1\n\t" \
  "ds_read_u16_d16_hi v235, v235 offset:10528\n\t" \
  "v_mfma_f32_16x16x32_bf16 v[60:63], v[208:211], v[194:197], v[60:63]\n\t" \
  "v_lshlrev_b32_sdwa v223, 2, v222 dst_sel:DWORD dst_unused:UNUSED_PAD src0_sel:DWORD src1_sel:BYTE_3\n\t" \
  "v_lshlrev_b32_sdwa v222, 2, v222 dst_sel:DWORD dst_unused:UNUSED_PAD src0_sel:DWORD src1_sel:BYTE_1\n\t" \
  "s_waitcnt lgkmcnt(4)\n\t" \
  "ds_read_u16 v234, v234 offset:10528\n\t" \
  "v_mfma_f32_16x16x32_bf16 v[84:87], v[208:211], v[202:205], v[84:87]\n\t" \
  "v_lshlrev_b32_sdwa v224, 1, v156 dst_sel:DWORD dst_unused:UNUSED_PAD src0_sel:DWORD src1_sel:WORD_1\n\t" \
  "v_lshlrev_b32_sdwa v225, 1, v157 dst_sel:DWORD dst_unused:UNUSED_PAD src0_sel:DWORD src1_sel:WORD_1\n\t" \
  "ds_read_b32 v220, v220 offset:10016\n\t" \
  "ds_read_b64_tr_b16 v[208:209], v248 offset:0\n\t" \
  "v_mfma_f32_16x16x32_bf16 v[16:19], v[212:215], v[178:181], v[16:19]\n\t" \
  "v_nop\n\t" \
  "v_mfma_f32_16x16x32_bf16 v[64:67], v[212:215], v[194:197], v[64:67]\n\t" \
  "v_fma_mix_f32 v229, |v163|, v229, v229 op_sel:[0,1,0] op_sel_hi:[0,1,1] clamp\n\t" \
  "v_lshlrev_b32_sdwa v227, 1, v159 dst_sel:DWORD dst_unused:UNUSED_PAD src0_sel:DWORD src1_sel:WORD_1\n\t" \
  "s_setprio 2\n\t" \
  "ds_read_b32 v221, v221 offset:10016\n\t" \
  "ds_read_b64_tr_b16 v[210:211], v248 offset:1024\n\t" \
  "v_mfma_f32_16x16x32_bf16 v[40:43], v[212:215], v[186:189], v[40:43]\n\t" \
  "v_lshlrev_b32_sdwa v226, 1, v158 dst_sel:DWORD dst_unused:UNUSED_PAD src0_sel:DWORD src1_sel:WORD_1\n\t" \
  "v_fma_mix_f32 v228, |v162|, v228, v228 op_sel:[0,1,0] op_sel_hi:[0,1,1] clamp\n\t" \
  "ds_read_b32 v223, v223 offset:10016\n\t" \
  "v_mfma_f32_16x16x32_bf16 v[88:91], v[212:215], v[202:205], v[88:91]\n\t" \
  "s_setprio 1\n\t" \
  "v_fma_mix_f32 v231, |v161|, v231, v231 op_sel:[0,1,0] op_sel_hi:[0,1,1] clamp\n\t" \
  "v_fma_mix_f32 v230, |v160|, v230, v230 op_sel:[0,1,0] op_sel_hi:[0,1,1] clamp\n\t" \
  "s_waitcnt lgkmcnt(5)\n\t" \
  "ds_read_b64_tr_b16 v[212:213], v248 offset:3072\n\t" \
  "ds_read_b32 v222, v222 offset:10016\n\t" \
  "v_mfma_f32_16x16x32_bf16 v[20:23], v[216:219], v[178:181], v[20:23]\n\t" \
  "v_or_b32 v171, v232, v233\n\t" \
  "v_or_b32 v170, v234, v235\n\t" \
  "ds_read_u16_d16_hi v225, v225 offset:10528\n\t" \
  "v_mfma_f32_16x16x32_bf16 v[68:71], v[216:219], v[194:197], v[68:71]\n\t" \
  "v_fma_f32 v160, |v160|, v230, v160 mul:4\n\t" \
  "v_fma_f32 v161, |v161|, v231, v161 mul:4\n\t" \
  "ds_read_b64_tr_b16 v[214:215], v248 offset:4096\n\t" \
  "v_mfma_f32_16x16x32_bf16 v[44:47], v[216:219], v[186:189], v[44:47]\n\t" \
  "v_fma_f32 v162, |v162|, v228, v162 mul:4\n\t" \
  "v_fma_f32 v163, |v163|, v229, v163 mul:4\n\t" \
  "s_waitcnt lgkmcnt(5)\n\t" \
  "ds_read_u16 v224, v224 offset:10528\n\t" \
  "v_mfma_f32_16x16x32_bf16 v[176:179], v[208:211], v[96:99], 0\n\t" \
  "s_setprio 2\n\t" \
  "v_cvt_pknorm_i16_f32 v230, |v166|, |v167|\n\t" \
  "v_lshlrev_b32_sdwa v232, 1, v172 dst_sel:DWORD dst_unused:UNUSED_PAD src0_sel:DWORD src1_sel:WORD_1\n\t" \
  "ds_read_u16_d16_hi v227, v227 offset:10528\n\t" \
  "v_mfma_f32_16x16x32_bf16 v[92:95], v[216:219], v[202:205], v[92:95]\n\t" \
  "v_cvt_pk_bf16_f32 v163, v162, v163\n\t" \
  "v_cvt_pk_bf16_f32 v162, v160, v161\n\t" \
  "ds_read_b64_tr_b16 v[216:217], v248 offset:2048\n\t" \
  "v_mfma_f32_16x16x32_bf16 v[192:195], v[208:211], v[104:107], 0\n\t" \
  "v_cvt_pknorm_i16_f32 v228, |v164|, |v165|\n\t" \
  "v_lshlrev_b32_sdwa v231, 2, v230 dst_sel:DWORD dst_unused:UNUSED_PAD src0_sel:DWORD src1_sel:BYTE_3\n\t" \
  "ds_read_b64_tr_b16 v[218:219], v248 offset:5120\n\t" \
  "v_mfma_f32_16x16x32_bf16 v[184:187], v[208:211], v[100:103], 0\n\t" \
  "v_lshlrev_b32_sdwa v229, 2, v228 dst_sel:DWORD dst_unused:UNUSED_PAD src0_sel:DWORD src1_sel:BYTE_3\n\t" \
  "v_lshlrev_b32_sdwa v228, 2, v228 dst_sel:DWORD dst_unused:UNUSED_PAD src0_sel:DWORD src1_sel:BYTE_1\n\t" \
  "ds_read_u16 v226, v226 offset:10528\n\t" \
  "s_waitcnt lgkmcnt(5)\n\t" \
  "v_mfma_f32_16x16x32_bf16 v[176:179], v[212:215], v[112:115], v[176:179]\n\t" \
  "v_lshlrev_b32_sdwa v235, 1, v175 dst_sel:DWORD dst_unused:UNUSED_PAD src0_sel:DWORD src1_sel:WORD_1\n\t" \
  "v_fma_mix_f32 v221, |v149|, v221, v221 op_sel:[0,1,0] op_sel_hi:[0,1,1] clamp\n\t" \
  "ds_read_b32 v231, v231 offset:10016\n\t" \
  "v_mfma_f32_16x16x32_bf16 v[200:203], v[208:211], v[108:111], 0\n\t" \
  "v_lshlrev_b32_sdwa v230, 2, v230 dst_sel:DWORD dst_unused:UNUSED_PAD src0_sel:DWORD src1_sel:BYTE_1\n\t" \
  "v_lshlrev_b32_sdwa v233, 1, v173 dst_sel:DWORD dst_unused:UNUSED_PAD src0_sel:DWORD src1_sel:WORD_1\n\t" \
  "ds_read_b64_tr_b16 v[208:209], v248 offset:512\n\t" \
  "v_mfma_f32_16x16x32_bf16 v[200:203], v[212:215], v[124:127], v[200:203]\n\t" \
  "v_lshlrev_b32_sdwa v234, 1, v174 dst_sel:DWORD dst_unused:UNUSED_PAD src0_sel:DWORD src1_sel:WORD_1\n\t" \
  "ds_read_b32 v229, v229 offset:10016\n\t" \
  "ds_read_b32 v228, v228 offset:10016\n\t" \
  "v_mfma_f32_16x16x32_bf16 v[192:195], v[212:215], v[120:123], v[192:195]\n\t" \
  "s_setprio 1\n\t" \
  "v_fma_mix_f32 v223, |v151|, v223, v223 op_sel:[0,1,0] op_sel_hi:[0,1,1] clamp\n\t" \
  "v_fma_mix_f32 v220, |v148|, v220, v220 op_sel:[0,1,0] op_sel_hi:[0,1,1] clamp\n\t" \
  "ds_read_b64_tr_b16 v[210:211], v248 offset:1536\n\t" \
  "v_mfma_f32_16x16x32_bf16 v[184:187], v[212:215], v[116:119], v[184:187]\n\t" \
  "s_waitcnt lgkmcnt(6)\n\t" \
  "v_fma_mix_f32 v222, |v150|, v222, v222 op_sel:[0,1,0] op_sel_hi:[0,1,1] clamp\n\t" \
  "ds_read_b64_tr_b16 v[212:213], v248 offset:3584\n\t" \
  "ds_read_u16_d16_hi v233, v233 offset:10528\n\t" \
  "v_mfma_f32_16x16x32_bf16 v[176:179], v[216:219], v[128:131], v[176:179]\n\t" \
  "s_waitcnt lgkmcnt(7)\n\t" \
  "v_or_b32 v156, v224, v225\n\t" \
  "v_or_b32 v157, v226, v227\n\t" \
  "s_setprio 0\n\t" \
  "ds_read_b64_tr_b16 v[214:215], v248 offset:4608\n\t" \
  "v_mfma_f32_16x16x32_bf16 v[192:195], v[216:219], v[136:139], v[192:195]\n\t" \
  "v_fma_f32 v150, |v150|, v222, v150 mul:4\n\t" \
  "v_fma_f32 v151, |v151|, v223, v151 mul:4\n\t" \
  "ds_read_b32 v230, v230 offset:10016\n\t" \
  "v_mfma_f32_16x16x32_bf16 v[184:187], v[216:219], v[132:135], v[184:187]\n\t" \
  "v_fma_f32 v148, |v148|, v220, v148 mul:4\n\t" \
  "v_fma_f32 v149, |v149|, v221, v149 mul:4\n\t" \
  "ds_read_u16 v232, v232 offset:10528\n\t" \
  "s_waitcnt lgkmcnt(5)\n\t" \
  "ds_read_u16_d16_hi v235, v235 offset:10528\n\t" \
  "v_mfma_f32_16x16x32_bf16 v[200:203], v[216:219], v[140:143], v[200:203]\n\t" \
  "v_cvt_pk_bf16_f32 v148, v148, v149\n\t" \
  "v_cvt_pk_bf16_f32 v149, v150, v151\n\t" \
  "ds_read_b64_tr_b16 v[216:217], v248 offset:2560\n\t" \
  "v_mfma_f32_16x16x32_bf16 v[180:183], v[208:211], v[96:99], 0\n\t" \
  "v_cvt_pknorm_i16_f32 v220, |v178|, |v179|\n\t" \
  "v_cvt_pknorm_i16_f32 v222, |v176|, |v177|\n\t" \
  "s_waitcnt vmcnt(0)\n\t" \
  "ds_read_b64_tr_b16 v[218:219], v248 offset:5632\n\t" \
  "v_mfma_f32_16x16x32_bf16 v[188:191], v[208:211], v[100:103], 0\n\t" \
  "s_waitcnt lgkmcnt(5)\n\t" \
  "v_lshlrev_b32_sdwa v221, 2, v220 dst_sel:DWORD dst_unused:UNUSED_PAD src0_sel:DWORD src1_sel:BYTE_3\n\t" \
  "v_lshlrev_b32_sdwa v220, 2, v220 dst_sel:DWORD dst_unused:UNUSED_PAD src0_sel:DWORD src1_sel:BYTE_1\n\t" \
  "s_mov_b64 exec, 3\n\t" \
  "ds_add_rtn_u32 v246, v251, v250 offset:47192\n\t" \
  "v_mfma_f32_16x16x32_bf16 v[196:199], v[208:211], v[104:107], 0\n\t" \
  "s_mov_b64 exec, -1\n\t" \
  "v_lshlrev_b32_sdwa v223, 2, v222 dst_sel:DWORD dst_unused:UNUSED_PAD src0_sel:DWORD src1_sel:BYTE_3\n\t" \
  "v_lshlrev_b32_sdwa v222, 2, v222 dst_sel:DWORD dst_unused:UNUSED_PAD src0_sel:DWORD src1_sel:BYTE_1\n\t" \
  "s_setprio 2\n\t" \
  "s_setprio 0\n\t" \
  "ds_read_u16 v234, v234 offset:10528\n\t" \
  "v_mfma_f32_16x16x32_bf16 v[188:191], v[212:215], v[116:119], v[188:191]\n\t" \
  "s_nop 0\n\t" \
  "v_lshlrev_b32_sdwa v227, 1, v185 dst_sel:DWORD dst_unused:UNUSED_PAD src0_sel:DWORD src1_sel:WORD_1\n\t" \
  "v_lshlrev_b32_sdwa v226, 1, v184 dst_sel:DWORD dst_unused:UNUSED_PAD src0_sel:DWORD src1_sel:WORD_1\n\t" \
  "ds_read_b32 v223, v223 offset:10016\n\t" \
  "v_mfma_f32_16x16x32_bf16 v[204:207], v[208:211], v[108:111], 0\n\t" \
  "s_waitcnt lgkmcnt(2)\n\t" \
  "v_lshlrev_b32_sdwa v225, 1, v187 dst_sel:DWORD dst_unused:UNUSED_PAD src0_sel:DWORD src1_sel:WORD_1\n\t" \
  "v_readfirstlane_b32 s4, v246\n\t" \
  "ds_read_b32 v221, v221 offset:10016\n\t" \
  "v_mfma_f32_16x16x32_bf16 v[180:183], v[212:215], v[112:115], v[180:183]\n\t" \
  "s_cmpk_lt_u32 s4, 6144\n\t" \
  "v_lshlrev_b32_sdwa v224, 1, v186 dst_sel:DWORD dst_unused:UNUSED_PAD src0_sel:DWORD src1_sel:WORD_1\n\t" \
  "s_cbranch_scc1 .Lcnx_w1_8\n\t" \
  ".Lcnx_g1_8:\n\t" \
  "ds_read_b32 v220, v220 offset:10016\n\t" \
  "v_mfma_f32_16x16x32_bf16 v[196:199], v[212:215], v[120:123], v[196:199]\n\t" \
  "v_fma_mix_f32 v229, |v165|, v229, v229 op_sel:[0,1,0] op_sel_hi:[0,1,1] clamp\n\t" \
  "v_fma_mix_f32 v228, |v164|, v228, v228 op_sel:[0,1,0] op_sel_hi:[0,1,1] clamp\n\t" \
  "ds_read_b64_tr_b16 v[208:209], v249 offset:6152\n\t" \
  "ds_read_b64_tr_b16 v[210:211], v249 offset:7688\n\t" \
  "v_mfma_f32_16x16x32_bf16 v[204:207], v[212:215], v[124:127], v[204:207]\n\t" \
  "v_fma_mix_f32 v230, |v166|, v230, v230 op_sel:[0,1,0] op_sel_hi:[0,1,1] clamp\n\t" \
  "s_waitcnt lgkmcnt(5)\n\t" \
  "v_fma_mix_f32 v231, |v167|, v231, v231 op_sel:[0,1,0] op_sel_hi:[0,1,1] clamp\n\t" \
  "ds_read_b64_tr_b16 v[212:213], v249 offset:6160\n\t" \
  "ds_read_b32 v222, v222 offset:10016\n\t" \
  "v_mfma_f32_16x16x32_bf16 v[180:183], v[216:219], v[128:131], v[180:183]\n\t" \
  "v_or_b32 v172, v232, v233\n\t" \
  "v_or_b32 v173, v234, v235\n\t" \
  "v_mfma_f32_16x16x32_bf16 v[188:191], v[216:219], v[132:135], v[188:191]\n\t" \
  "v_fma_f32 v165, |v165|, v229, v165 mul:4\n\t" \
  "v_fma_f32 v164, |v164|, v228, v164 mul:4\n\t" \
  "ds_read_u16_d16_hi v227, v227 offset:10528\n\t" \
  "ds_read_b64_tr_b16 v[214:215], v249 offset:7696\n\t" \
  "v_mfma_f32_16x16x32_bf16 v[196:199], v[216:219], v[136:139], v[196:199]\n\t" \
  "v_fma_f32 v166, |v166|, v230, v166 mul:4\n\t" \
  "v_fma_f32 v167, |v167|, v231, v167 mul:4\n\t" \
  "ds_read_u16_d16_hi v225, v225 offset:10528\n\t" \
  "v_mfma_f32_16x16x32_bf16 v[204:207], v[216:219], v[140:143], v[204:207]\n\t" \
  "s_waitcnt lgkmcnt(5)\n\t" \
  "v_cvt_pk_bf16_f32 v164, v164, v165\n\t" \
  "v_cvt_pk_bf16_f32 v165, v166, v167\n\t" \
  "ds_read_b64_tr_b16 v[216:217], v249 offset:6216\n\t" \
  "v_mfma_f32_4x4x4_16b_bf16 v[48:51], v[242:243], v[236:237], v[48:51]\n\t" \
  "v_mfma_f32_4x4x4_16b_bf16 v[52:55], v[242:243], v[238:239], v[52:55]\n\t" \
  "v_mfma_f32_16x16x32_bf16 v[0:3], v[208:211], v[146:149], v[0:3]\n\t" \
  "v_cvt_pknorm_i16_f32 v230, |v192|, |v193|\n\t" \
  "v_cvt_pknorm_i16_f32 v228, |v194|, |v195|\n\t" \
  "ds_read_b64_tr_b16 v[218:219], v249 offset:7752\n\t" \
  "v_mfma_f32_16x16x32_bf16 v[24:27], v[208:211], v[154:157], v[24:27]\n\t" \
  "v_lshlrev_b32_sdwa v229, 2, v228 dst_sel:DWORD dst_unused:UNUSED_PAD src0_sel:DWORD src1_sel:BYTE_3\n\t" \
  "v_lshlrev_b32_sdwa v228, 2, v228 dst_sel:DWORD dst_unused:UNUSED_PAD src0_sel:DWORD src1_sel:BYTE_1\n\t" \
  "ds_read_u16 v224, v224 offset:10528\n\t" \
  "v_mfma_f32_16x16x32_bf16 v[48:51], v[208:211], v[162:165], v[48:51]\n\t" \
  "s_setprio 1\n\t" \
  "v_lshlrev_b32_sdwa v231, 2, v230 dst_sel:DWORD dst_unused:UNUSED_PAD src0_sel:DWORD src1_sel:BYTE_3\n\t" \
  "s_waitcnt lgkmcnt(4)\n\t" \
  "ds_read_b32 v228, v228 offset:10016\n\t" \
  "v_mfma_f32_16x16x32_bf16 v[72:75], v[208:211], v[170:173], v[72:75]\n\t" \
  "v_lshlrev_b32_sdwa v232, 1, v202 dst_sel:DWORD dst_unused:UNUSED_PAD src0_sel:DWORD src1_sel:WORD_1\n\t" \
  "v_lshlrev_b32_sdwa v233, 1, v203 dst_sel:DWORD dst_unused:UNUSED_PAD src0_sel:DWORD src1_sel:WORD_1\n\t" \
  "v_mfma_f32_16x16x32_bf16 v[4:7], v[212:215], v[146:149], v[4:7]\n\t" \
  "v_lshlrev_b32_sdwa v235, 1, v201 dst_sel:DWORD dst_unused:UNUSED_PAD src0_sel:DWORD src1_sel:WORD_1\n\t" \
  "ds_read_b64_tr_b16 v[208:209], v249 offset:6224\n\t" \
  "ds_read_u16 v226, v226 offset:10528\n\t" \
  "v_mfma_f32_16x16x32_bf16 v[28:31], v[212:215], v[154:157], v[28:31]\n\t" \
  "v_lshlrev_b32_sdwa v234, 1, v200 dst_sel:DWORD dst_unused:UNUSED_PAD src0_sel:DWORD src1_sel:WORD_1\n\t" \
  "v_lshlrev_b32_sdwa v230, 2, v230 dst_sel:DWORD dst_unused:UNUSED_PAD src0_sel:DWORD src1_sel:BYTE_1\n\t" \
  "ds_read_b64_tr_b16 v[210:211], v249 offset:7760\n\t" \
  "ds_read_b32 v229, v229 offset:10016\n\t" \
  "v_mfma_f32_16x16x32_bf16 v[76:79], v[212:215], v[170:173], v[76:79]\n\t" \
  "v_fma_mix_f32 v223, |v177|, v223, v223 op_sel:[0,1,0] op_sel_hi:[0,1,1] clamp\n\t" \
  "v_fma_mix_f32 v222, |v176|, v222, v222 op_sel:[0,1,0] op_sel_hi:[0,1,1] clamp\n\t" \
  "v_mfma_f32_16x16x32_bf16 v[52:55], v[212:215], v[162:165], v[52:55]\n\t" \
  "v_fma_mix_f32 v221, |v179|, v221, v221 op_sel:[0,1,0] op_sel_hi:[0,1,1] clamp\n\t" \
  "v_fma_mix_f32 v220, |v178|, v220, v220 op_sel:[0,1,0] op_sel_hi:[0,1,1] clamp\n\t" \
  "s_setprio 3\n\t" \
  "s_waitcnt lgkmcnt(2)\n\t" \
  "ds_read_b64_tr_b16 v[212:213], v249 offset:6280\n\t" \
  "ds_read_b32 v230, v230 offset:10016\n\t" \
  "ds_read_b32 v231, v231 offset:10016\n\t" \
  "v_mfma_f32_16x16x32_bf16 v[8:11], v[216:219], v[146:149], v[8:11]\n\t" \
  "v_or_b32 v187, v224, v225\n\t" \
  "v_or_b32 v186, v226, v227\n\t" \
  "ds_read_u16_d16_hi v233, v233 offset:10528\n\t" \
  "buffer_load_dwordx4 v[236:239], v250, s[8:11], s14 offen offset:64 nt\n\t" \
  "v_mfma_f32_16x16x32_bf16 v[32:35], v[216:219], v[154:157], v[32:35]\n\t" \
  "v_fma_f32 v179, |v179|, v221, v179 mul:4\n\t" \
  "v_fma_f32 v178, |v178|, v220, v178 mul:4\n\t" \
  "ds_read_b64_tr_b16 v[214:215], v249 offset:7816\n\t" \
  "v_mfma_f32_16x16x32_bf16 v[56:59], v[216:219], v[162:165], v[56:59]\n\t" \
  "v_fma_f32 v176, |v176|, v222, v176 mul:4\n\t" \
  "v_fma_f32 v177, |v177|, v223, v177 mul:4\n\t" \
  "ds_read_u16 v232, v232 offset:10528\n\t" \
  "v_mfma_f32_16x16x32_bf16 v[80:83], v[216:219], v[170:173], v[80:83]\n\t" \
  "v_cvt_pk_bf16_f32 v179, v178, v179\n\t" \
  "v_cvt_pk_bf16_f32 v178, v176, v177\n\t" \
  "s_waitcnt lgkmcnt(7)\n\t" \
  "s_setprio 0\n\t" \
  "v_mfma_f32_16x16x32_bf16 v[12:15], v[208:211], v[146:149], v[12:15]\n\t" \
  "v_cvt_pknorm_i16_f32 v220, |v180|, |v181|\n\t" \
  "v_cvt_pknorm_i16_f32 v222, |v182|, |v183|\n\t" \
  "ds_read_b64_tr_b16 v[216:217], v249 offset:6288\n\t" \
  "ds_read_b64_tr_b16 v[218:219], v249 offset:7824\n\t" \
  "v_mfma_f32_16x16x32_bf16 v[36:39], v[208:211], v[154:157], v[36:39]\n\t" \
  "v_lshlrev_b32_sdwa v221, 2, v220 dst_sel:DWORD dst_unused:UNUSED_PAD src0_sel:DWORD src1_sel:BYTE_3\n\t" \
  "v_lshlrev_b32_sdwa v220, 2, v220 dst_sel:DWORD dst_unused:UNUSED_PAD src0_sel:DWORD src1_sel:BYTE_1\n\t" \
  "ds_read_u16_d16_hi v235, v235 offset:10528\n\t" \
  "v_mfma_f32_16x16x32_bf16 v[60:63], v[208:211], v[162:165], v[60:63]\n\t" \
  "v_lshlrev_b32_sdwa v223, 2, v222 dst_sel:DWORD dst_unused:UNUSED_PAD src0_sel:DWORD src1_sel:BYTE_3\n\t" \
  "v_lshlrev_b32_sdwa v222, 2, v222 dst_sel:DWORD dst_unused:UNUSED_PAD src0_sel:DWORD src1_sel:BYTE_1\n\t" \
  "s_waitcnt lgkmcnt(4)\n\t" \
  "ds_read_u16 v234, v234 offset:10528\n\t" \
  "v_mfma_f32_16x16x32_bf16 v[84:87], v[208:211], v[170:173], v[84:87]\n\t" \
  "v_lshlrev_b32_sdwa v224, 1, v188 dst_sel:DWORD dst_unused:UNUSED_PAD src0_sel:DWORD src1_sel:WORD_1\n\t" \
  "v_lshlrev_b32_sdwa v225, 1, v189 dst_sel:DWORD dst_unused:UNUSED_PAD src0_sel:DWORD src1_sel:WORD_1\n\t" \
  "ds_read_b32 v220, v220 offset:10016\n\t" \
  "ds_read_b64_tr_b16 v[208:209], v248 offset:43048\n\t" \
  "v_mfma_f32_16x16x32_bf16 v[16:19], v[212:215], v[146:149], v[16:19]\n\t" \
  "v_nop\n\t" \
  "v_mfma_f32_16x16x32_bf16 v[64:67], v[212:215], v[162:165], v[64:67]\n\t" \
  "v_fma_mix_f32 v229, |v195|, v229, v229 op_sel:[0,1,0] op_sel_hi:[0,1,1] clamp\n\t" \
  "v_lshlrev_b32_sdwa v227, 1, v191 dst_sel:DWORD dst_unused:UNUSED_PAD src0_sel:DWORD src1_sel:WORD_1\n\t" \
  "ds_read_b32 v221, v221 offset:10016\n\t" \
  "ds_read_b64_tr_b16 v[210:211], v248 offset:44072\n\t" \
  "v_mfma_f32_16x16x32_bf16 v[40:43], v[212:215], v[154:157], v[40:43]\n\t" \
  "v_lshlrev_b32_sdwa v226, 1, v190 dst_sel:DWORD dst_unused:UNUSED_PAD src0_sel:DWORD src1_sel:WORD_1\n\t" \
  "v_fma_mix_f32 v228, |v194|, v228, v228 op_sel:[0,1,0] op_sel_hi:[0,1,1] clamp\n\t" \
  "ds_read_b32 v223, v223 offset:10016\n\t" \
  "v_mfma_f32_16x16x32_bf16 v[88:91], v[212:215], v[170:173], v[88:91]\n\t" \
  "v_fma_mix_f32 v231, |v193|, v231, v231 op_sel:[0,1,0] op_sel_hi:[0,1,1] clamp\n\t" \
  "v_fma_mix_f32 v230, |v192|, v230, v230 op_sel:[0,1,0] op_sel_hi:[0,1,1] clamp\n\t" \
  "s_waitcnt lgkmcnt(5)\n\t" \
  "ds_read_b64_tr_b16 v[212:213], v248 offset:46120\n\t" \
  "ds_read_b32 v222, v222 offset:10016\n\t" \
  "v_mfma_f32_16x16x32_bf16 v[20:23], v[216:219], v[146:149], v[20:23]\n\t" \
  "v_or_b32 v203, v232, v233\n\t" \
  "v_or_b32 v202, v234, v235\n\t" \
  "ds_read_u16_d16_hi v225, v225 offset:10528\n\t" \
  "v_mfma_f32_16x16x32_bf16 v[68:71], v[216:219], v[162:165], v[68:71]\n\t" \
  "v_fma_f32 v192, |v192|, v230, v192 mul:4\n\t" \
  "v_fma_f32 v193, |v193|, v231, v193 mul:4\n\t" \
  "ds_read_b64_tr_b16 v[214:215], v248 offset:47144\n\t" \
  "v_mfma_f32_16x16x32_bf16 v[44:47], v[216:219], v[154:157], v[44:47]\n\t" \
  "v_fma_f32 v194, |v194|, v228, v194 mul:4\n\t" \
  "v_fma_f32 v195, |v195|, v229, v195 mul:4\n\t" \
  "s_waitcnt lgkmcnt(5)\n\t" \
  "ds_read_u16 v224, v224 offset:10528\n\t" \
  "v_mfma_f32_16x16x32_bf16 v[144:147], v[208:211], v[96:99], 0\n\t" \
  "s_setprio 2\n\t" \
  "v_cvt_pknorm_i16_f32 v230, |v198|, |v199|\n\t" \
  "v_lshlrev_b32_sdwa v232, 1, v204 dst_sel:DWORD dst_unused:UNUSED_PAD src0_sel:DWORD src1_sel:WORD_1\n\t" \
  "ds_read_u16_d16_hi v227, v227 offset:10528\n\t" \
  "v_mfma_f32_16x16x32_bf16 v[92:95], v[216:219], v[170:173], v[92:95]\n\t" \
  "v_cvt_pk_bf16_f32 v195, v194, v195\n\t" \
  "v_cvt_pk_bf16_f32 v194, v192, v193\n\t" \
  "ds_read_b64_tr_b16 v[216:217], v248 offset:45096\n\t" \
  "v_mfma_f32_16x16x32_bf16 v[160:163], v[208:211], v[104:107], 0\n\t" \
  "v_cvt_pknorm_i16_f32 v228, |v196|, |v197|\n\t" \
  "v_lshlrev_b32_sdwa v231, 2, v230 dst_sel:DWORD dst_unused:UNUSED_PAD src0_sel:DWORD src1_sel:BYTE_3\n\t" \
  "ds_read_b64_tr_b16 v[218:219], v248 offset:48168\n\t" \
  "v_mfma_f32_16x16x32_bf16 v[152:155], v[208:211], v[100:103], 0\n\t" \
  "v_lshlrev_b32_sdwa v229, 2, v228 dst_sel:DWORD dst_unused:UNUSED_PAD src0_sel:DWORD src1_sel:BYTE_3\n\t" \
  "v_lshlrev_b32_sdwa v228, 2, v228 dst_sel:DWORD dst_unused:UNUSED_PAD src0_sel:DWORD src1_sel:BYTE_1\n\t" \
  "ds_read_u16 v226, v226 offset:10528\n\t" \
  "v_mfma_f32_16x16x32_bf16 v[168:171], v[208:211], v[108:111], 0\n\t" \
  "v_lshlrev_b32_sdwa v230, 2, v230 dst_sel:DWORD dst_unused:UNUSED_PAD src0_sel:DWORD src1_sel:BYTE_1\n\t" \
  "v_lshlrev_b32_sdwa v233, 1, v205 dst_sel:DWORD dst_unused:UNUSED_PAD src0_sel:DWORD src1_sel:WORD_1\n\t" \
  "ds_read_b64_tr_b16 v[208:209], v248 offset:43560\n\t" \
  "s_waitcnt lgkmcnt(6)\n\t" \
  "v_mfma_f32_16x16x32_bf16 v[144:147], v[212:215], v[112:115], v[144:147]\n\t" \
  "v_lshlrev_b32_sdwa v235, 1, v207 dst_sel:DWORD dst_unused:UNUSED_PAD src0_sel:DWORD src1_sel:WORD_1\n\t" \
  "v_fma_mix_f32 v221, |v181|, v221, v221 op_sel:[0,1,0] op_sel_hi:[0,1,1] clamp\n\t" \
  "ds_read_b32 v231, v231 offset:10016\n\t" \
  "v_lshlrev_b32_sdwa v234, 1, v206 dst_sel:DWORD dst_unused:UNUSED_PAD src0_sel:DWORD src1_sel:WORD_1\n\t" \
  "v_mfma_f32_16x16x32_bf16 v[168:171], v[212:215], v[124:127], v[168:171]\n\t" \
  "ds_read_b32 v228, v228 offset:10016\n\t" \
  "ds_read_b32 v229, v229 offset:10016\n\t" \
  "v_mfma_f32_16x16x32_bf16 v[160:163], v[212:215], v[120:123], v[160:163]\n\t" \
  "s_setprio 1\n\t" \
  "v_fma_mix_f32 v223, |v183|, v223, v223 op_sel:[0,1,0] op_sel_hi:[0,1,1] clamp\n\t" \
  "v_fma_mix_f32 v220, |v180|, v220, v220 op_sel:[0,1,0] op_sel_hi:[0,1,1] clamp\n\t" \
  "ds_read_b64_tr_b16 v[210:211], v248 offset:44584\n\t" \
  "v_mfma_f32_16x16x32_bf16 v[152:155], v[212:215], v[116:119], v[152:155]\n\t" \
  "s_waitcnt lgkmcnt(6)\n\t" \
  "ds_read_b64_tr_b16 v[212:213], v248 offset:46632\n\t" \
  "v_fma_mix_f32 v222, |v182|, v222, v222 op_sel:[0,1,0] op_sel_hi:[0,1,1] clamp\n\t" \
  "ds_read_u16_d16_hi v233, v233 offset:10528\n\t" \
  "v_mfma_f32_16x16x32_bf16 v[144:147], v[216:219], v[128:131], v[144:147]\n\t" \
  "s_waitcnt lgkmcnt(7)\n\t" \
  "v_or_b32 v188, v224, v225\n\t" \
  "v_or_b32 v189, v226, v227\n\t" \
  "s_setprio 0\n\t" \
  "v_mfma_f32_16x16x32_bf16 v[160:163], v[216:219], v[136:139], v[160:163]\n\t" \
  "v_fma_f32 v182, |v182|, v222, v182 mul:4\n\t" \
  "v_fma_f32 v183, |v183|, v223, v183 mul:4\n\t" \
  "ds_read_b32 v230, v230 offset:10016\n\t" \
  "v_mfma_f32_16x16x32_bf16 v[152:155], v[216:219], v[132:135], v[152:155]\n\t" \
  "v_fma_f32 v180, |v180|, v220, v180 mul:4\n\t" \
  "ds_read_u16_d16_hi v235, v235 offset:10528\n\t" \
  "v_fma_f32 v181, |v181|, v221, v181 mul:4\n\t" \
  "ds_read_u16 v232, v232 offset:10528\n\t" \
  "ds_read_b64_tr_b16 v[214:215], v248 offset:47656\n\t" \
  "v_mfma_f32_16x16x32_bf16 v[168:171], v[216:219], v[140:143], v[168:171]\n\t" \
  "v_cvt_pk_bf16_f32 v180, v180, v181\n\t" \
  "v_cvt_pk_bf16_f32 v181, v182, v183\n\t" \
  "s_waitcnt lgkmcnt(6)\n\t" \
  "ds_read_b64_tr_b16 v[216:217], v248 offset:45608\n\t" \
  "v_mfma_f32_16x16x32_bf16 v[148:151], v[208:211], v[96:99], 0\n\t" \
  "v_cvt_pknorm_i16_f32 v220, |v146|, |v147|\n\t" \
  "v_lshlrev_b32_sdwa v221, 2, v220 dst_sel:DWORD dst_unused:UNUSED_PAD src0_sel:DWORD src1_sel:BYTE_3\n\t" \
  "v_cvt_pknorm_i16_f32 v222, |v144|, |v145|\n\t" \
  "s_waitcnt vmcnt(0)\n\t" \
  "ds_read_b64_tr_b16 v[218:219], v248 offset:48680\n\t" \
  "s_mov_b64 exec, 3\n\t" \
  "ds_add_rtn_u32 v246, v251, v250 offset:47200\n\t" \
  "v_mfma_f32_16x16x32_bf16 v[156:159], v[208:211], v[100:103], 0\n\t" \
  "s_mov_b64 exec, -1\n\t" \
  "v_lshlrev_b32_sdwa v223, 2, v222 dst_sel:DWORD dst_unused:UNUSED_PAD src0_sel:DWORD src1_sel:BYTE_3\n\t" \
  "v_lshlrev_b32_sdwa v220, 2, v220 dst_sel:DWORD dst_unused:UNUSED_PAD src0_sel:DWORD src1_sel:BYTE_1\n\t" \
  "v_mfma_f32_16x16x32_bf16 v[164:167], v[208:211], v[104:107], 0\n\t" \
  "v_lshlrev_b32_sdwa v222, 2, v222 dst_sel:DWORD dst_unused:UNUSED_PAD src0_sel:DWORD src1_sel:BYTE_1\n\t" \
  "v_lshlrev_b32_sdwa v225, 1, v155 dst_sel:DWORD dst_unused:UNUSED_PAD src0_sel:DWORD src1_sel:WORD_1\n\t" \
  "ds_read_u16 v234, v234 offset:10528\n\t" \
  "v_mfma_f32_16x16x32_bf16 v[172:175], v[208:211], v[108:111], 0\n\t" \
  "s_waitcnt lgkmcnt(1)\n\t" \
  "v_lshlrev_b32_sdwa v224, 1, v154 dst_sel:DWORD dst_unused:UNUSED_PAD src0_sel:DWORD src1_sel:WORD_1\n\t" \
  "v_readfirstlane_b32 s4, v246\n\t" \
  "ds_read_b32 v221, v221 offset:10016\n\t" \
  "v_mfma_f32_16x16x32_bf16 v[148:151], v[212:215], v[112:115], v[148:151]\n\t" \
  "s_cmpk_lt_u32 s4, 6144\n\t" \
  "v_lshlrev_b32_sdwa v227, 1, v153 dst_sel:DWORD dst_unused:UNUSED_PAD src0_sel:DWORD src1_sel:WORD_1\n\t" \
  "ds_read_b32 v220, v220 offset:10016\n\t" \
  "v_mfma_f32_16x16x32_bf16 v[156:159], v[212:215], v[116:119], v[156:159]\n\t" \
  "v_lshlrev_b32_sdwa v226, 1, v152 dst_sel:DWORD dst_unused:UNUSED_PAD src0_sel:DWORD src1_sel:WORD_1\n\t" \
  "s_cbranch_scc1 .Lcnx_w1_9\n\t" \
  ".Lcnx_g1_9:\n\t" \
  "ds_read_b64_tr_b16 v[208:209], v249 offset:12304\n\t" \
  "ds_read_b32 v223, v223 offset:10016\n\t" \
  "v_mfma_f32_16x16x32_bf16 v[164:167], v[212:215], v[120:123], v[164:167]\n\t" \
  "v_fma_mix_f32 v229, |v197|, v229, v229 op_sel:[0,1,0] op_sel_hi:[0,1,1] clamp\n\t" \
  "v_fma_mix_f32 v228, |v196|, v228, v228 op_sel:[0,1,0] op_sel_hi:[0,1,1] clamp\n\t" \
  "ds_read_b64_tr_b16 v[210:211], v249 offset:13840\n\t" \
  "v_mfma_f32_16x16x32_bf16 v[172:175], v[212:215], v[124:127], v[172:175]\n\t" \
  "v_fma_mix_f32 v231, |v199|, v231, v231 op_sel:[0,1,0] op_sel_hi:[0,1,1] clamp\n\t" \
  "v_fma_mix_f32 v230, |v198|, v230, v230 op_sel:[0,1,0] op_sel_hi:[0,1,1] clamp\n\t" \
  "s_waitcnt lgkmcnt(5)\n\t" \
  "ds_read_b64_tr_b16 v[212:213], v249 offset:12312\n\t" \
  "ds_read_b32 v222, v222 offset:10016\n\t" \
  "v_mfma_f32_16x16x32_bf16 v[148:151], v[216:219], v[128:131], v[148:151]\n\t" \
  "v_or_b32 v204, v232, v233\n\t" \
  "v_or_b32 v205, v234, v235\n\t" \
  "ds_read_b64_tr_b16 v[214:215], v249 offset:13848\n\t" \
  "v_mfma_f32_16x16x32_bf16 v[156:159], v[216:219], v[132:135], v[156:159]\n\t" \
  "v_fma_f32 v197, |v197|, v229, v197 mul:4\n\t" \
  "v_fma_f32 v196, |v196|, v228, v196 mul:4\n\t" \
  "ds_read_u16_d16_hi v225, v225 offset:10528\n\t" \
  "v_mfma_f32_16x16x32_bf16 v[164:167], v[216:219], v[136:139], v[164:167]\n\t" \
  "v_fma_f32 v199, |v199|, v231, v199 mul:4\n\t" \
  "v_fma_f32 v198, |v198|, v230, v198 mul:4\n\t" \
  "ds_read_u16 v224, v224 offset:10528\n\t" \
  "v_mfma_f32_16x16x32_bf16 v[172:175], v[216:219], v[140:143], v[172:175]\n\t" \
  "v_cvt_pk_bf16_f32 v196, v196, v197\n\t" \
  "v_cvt_pk_bf16_f32 v197, v198, v199\n\t" \
  "ds_read_b64_tr_b16 v[216:217], v249 offset:12368\n\t" \
  "v_mfma_f32_4x4x4_16b_bf16 v[56:59], v[242:243], v[236:237], v[56:59]\n\t" \
  "v_mfma_f32_4x4x4_16b_bf16 v[60:63], v[242:243], v[238:239], v[60:63]\n\t" \
  "ds_read_b64_tr_b16 v[218:219], v249 offset:13904\n\t" \
  "v_cvt_pknorm_i16_f32 v228, |v162|, |v163|\n\t" \
  "s_setprio 0\n\t" \
  "s_cmp_ge_u32 s39, 2048\n\t" \
  "s_waitcnt lgkmcnt(7)\n\t" \
  "v_mfma_f32_16x16x32_bf16 v[72:75], v[208:211], v[202:205], v[72:75]\n\t" \
  "s_cbranch_scc1 .Lcnx_p\n\t" \
  "s_setprio 3\n\t" \
  ".Lcnx_p:\n\t" \
  "v_cvt_pknorm_i16_f32 v230, |v160|, |v161|\n\t" \
  "v_mfma_f32_16x16x32_bf16 v[0:3], v[208:211], v[178:181], v[0:3]\n\t" \
  "ds_read_u16_d16_hi v227, v227 offset:10528\n\t" \
  "v_lshlrev_b32_sdwa v229, 2, v228 dst_sel:DWORD dst_unused:UNUSED_PAD src0_sel:DWORD src1_sel:BYTE_3\n\t" \
  "v_lshlrev_b32_sdwa v228, 2, v228 dst_sel:DWORD dst_unused:UNUSED_PAD src0_sel:DWORD src1_sel:BYTE_1\n\t" \
  "v_mfma_f32_16x16x32_bf16 v[24:27], v[208:211], v[186:189], v[24:27]\n\t" \
  "ds_read_u16 v226, v226 offset:10528\n\t" \
  "v_lshlrev_b32_sdwa v231, 2, v230 dst_sel:DWORD dst_unused:UNUSED_PAD src0_sel:DWORD src1_sel:BYTE_3\n\t" \
  "v_lshlrev_b32_sdwa v230, 2, v230 dst_sel:DWORD dst_unused:UNUSED_PAD src0_sel:DWORD src1_sel:BYTE_1\n\t" \
  "s_waitcnt lgkmcnt(6)\n\t" \
  "v_mfma_f32_16x16x32_bf16 v[48:51], v[208:211], v[194:197], v[48:51]\n\t" \
  "ds_read_b32 v229, v229 offset:10016\n\t" \
  "v_lshlrev_b32_sdwa v233, 1, v171 dst_sel:DWORD dst_unused:UNUSED_PAD src0_sel:DWORD src1_sel:WORD_1\n\t" \
  "v_lshlrev_b32_sdwa v232, 1, v170 dst_sel:DWORD dst_unused:UNUSED_PAD src0_sel:DWORD src1_sel:WORD_1\n\t" \
  "ds_read_b64_tr_b16 v[208:209], v249 offset:12376\n\t" \
  "ds_read_b32 v228, v228 offset:10016\n\t" \
  "v_mfma_f32_16x16x32_bf16 v[4:7], v[212:215], v[178:181], v[4:7]\n\t" \
  "ds_read_b64_tr_b16 v[210:211], v249 offset:13912\n\t" \
  "v_lshlrev_b32_sdwa v235, 1, v169 dst_sel:DWORD dst_unused:UNUSED_PAD src0_sel:DWORD src1_sel:WORD_1\n\t" \
  "v_lshlrev_b32_sdwa v234, 1, v168 dst_sel:DWORD dst_unused:UNUSED_PAD src0_sel:DWORD src1_sel:WORD_1\n\t" \
  "v_fma_mix_f32 v221, |v147|, v221, v221 op_sel:[0,1,0] op_sel_hi:[0,1,1] clamp\n\t" \
  "v_mfma_f32_16x16x32_bf16 v[28:31], v[212:215], v[186:189], v[28:31]\n\t" \
  "ds_read_b32 v231, v231 offset:10016\n\t" \
  "v_mfma_f32_16x16x32_bf16 v[52:55], v[212:215], v[194:197], v[52:55]\n\t" \
  "ds_read_u16_d16_hi v233, v233 offset:10528\n\t" \
  "v_fma_mix_f32 v223, |v145|, v223, v223 op_sel:[0,1,0] op_sel_hi:[0,1,1] clamp\n\t" \
  "ds_read_b32 v230, v230 offset:10016\n\t" \
  "v_fma_mix_f32 v222, |v144|, v222, v222 op_sel:[0,1,0] op_sel_hi:[0,1,1] clamp\n\t" \
  "v_mfma_f32_16x16x32_bf16 v[76:79], v[212:215], v[202:205], v[76:79]\n\t" \
  "s_waitcnt lgkmcnt(7)\n\t" \
  "ds_read_b64_tr_b16 v[212:213], v249 offset:12432\n\t" \
  "v_or_b32 v155, v224, v225\n\t" \
  "v_mfma_f32_16x16x32_bf16 v[8:11], v[216:219], v[178:181], v[8:11]\n\t" \
  "v_or_b32 v154, v226, v227\n\t" \
  "v_fma_f32 v147, |v147|, v221, v147 mul:4\n\t" \
  "ds_read_b64_tr_b16 v[214:215], v249 offset:13968\n\t" \
  "buffer_load_dwordx4 v[236:239], v250, s[8:11], s14 offen offset:128 nt\n\t" \
  "v_mfma_f32_16x16x32_bf16 v[32:35], v[216:219], v[186:189], v[32:35]\n\t" \
  "ds_read_u16 v232, v232 offset:10528\n\t" \
  "v_fma_f32 v145, |v145|, v223, v145 mul:4\n\t" \
  "v_mfma_f32_16x16x32_bf16 v[56:59], v[216:219], v[194:197], v[56:59]\n\t" \
  "s_waitcnt lgkmcnt(9)\n\t" \
  "v_fma_mix_f32 v229, |v163|, v229, v229 op_sel:[0,1,0] op_sel_hi:[0,1,1] clamp\n\t" \
  "ds_read_u16_d16_hi v235, v235 offset:10528\n\t" \
  "v_fma_f32 v144, |v144|, v222, v144 mul:4\n\t" \
  "v_mfma_f32_16x16x32_bf16 v[80:83], v[216:219], v[202:205], v[80:83]\n\t" \
  "s_waitcnt lgkmcnt(7)\n\t" \
  "ds_read_b64_tr_b16 v[216:217], v249 offset:12440\n\t" \
  "v_fma_mix_f32 v220, |v146|, v220, v220 op_sel:[0,1,0] op_sel_hi:[0,1,1] clamp\n\t" \
  "v_fma_f32 v146, |v146|, v220, v146 mul:4\n\t" \
  "v_cvt_pk_bf16_f32 v147, v146, v147\n\t" \
  "v_cvt_pk_bf16_f32 v146, v144, v145\n\t" \
  "v_cvt_pknorm_i16_f32 v220, |v148|, |v149|\n\t" \
  "v_mfma_f32_16x16x32_bf16 v[12:15], v[208:211], v[178:181], v[12:15]\n\t" \
  "ds_read_b64_tr_b16 v[218:219], v249 offset:13976\n\t" \
  "v_cvt_pknorm_i16_f32 v222, |v150|, |v151|\n\t" \
  "v_lshlrev_b32_sdwa v221, 2, v220 dst_sel:DWORD dst_unused:UNUSED_PAD src0_sel:DWORD src1_sel:BYTE_3\n\t" \
  "v_mfma_f32_16x16x32_bf16 v[36:39], v[208:211], v[186:189], v[36:39]\n\t" \
  "ds_read_u16 v234, v234 offset:10528\n\t" \
  "v_lshlrev_b32_sdwa v220, 2, v220 dst_sel:DWORD dst_unused:UNUSED_PAD src0_sel:DWORD src1_sel:BYTE_1\n\t" \
  "v_lshlrev_b32_sdwa v223, 2, v222 dst_sel:DWORD dst_unused:UNUSED_PAD src0_sel:DWORD src1_sel:BYTE_3\n\t" \
  "v_mfma_f32_16x16x32_bf16 v[60:63], v[208:211], v[194:197], v[60:63]\n\t" \
  "ds_read_b32 v221, v221 offset:10016\n\t" \
  "v_lshlrev_b32_sdwa v222, 2, v222 dst_sel:DWORD dst_unused:UNUSED_PAD src0_sel:DWORD src1_sel:BYTE_1\n\t" \
  "v_lshlrev_b32_sdwa v225, 1, v157 dst_sel:DWORD dst_unused:UNUSED_PAD src0_sel:DWORD src1_sel:WORD_1\n\t" \
  "v_mfma_f32_16x16x32_bf16 v[84:87], v[208:211], v[202:205], v[84:87]\n\t" \
  "s_waitcnt lgkmcnt(6)\n\t" \
  "v_lshlrev_b32_sdwa v224, 1, v156 dst_sel:DWORD dst_unused:UNUSED_PAD src0_sel:DWORD src1_sel:WORD_1\n\t" \
  "v_lshlrev_b32_sdwa v227, 1, v159 dst_sel:DWORD dst_unused:UNUSED_PAD src0_sel:DWORD src1_sel:WORD_1\n\t" \
  "v_mfma_f32_16x16x32_bf16 v[16:19], v[212:215], v[178:181], v[16:19]\n\t" \
  "ds_read_b64_tr_b16 v[210:211], v248 offset:50216\n\t" \
  "ds_read_b32 v220, v220 offset:10016\n\t" \
  "v_lshlrev_b32_sdwa v226, 1, v158 dst_sel:DWORD dst_unused:UNUSED_PAD src0_sel:DWORD src1_sel:WORD_1\n\t" \
  "v_mfma_f32_16x16x32_bf16 v[40:43], v[212:215], v[186:189], v[40:43]\n\t" \
  "ds_read_b32 v223, v223 offset:10016\n\t" \
  "v_fma_mix_f32 v228, |v162|, v228, v228 op_sel:[0,1,0] op_sel_hi:[0,1,1] clamp\n\t" \
  "v_fma_mix_f32 v231, |v161|, v231, v231 op_sel:[0,1,0] op_sel_hi:[0,1,1] clamp\n\t" \
  "v_fma_mix_f32 v230, |v160|, v230, v230 op_sel:[0,1,0] op_sel_hi:[0,1,1] clamp\n\t" \
  "v_mfma_f32_16x16x32_bf16 v[88:91], v[212:215], v[202:205], v[88:91]\n\t" \
  "ds_read_b64_tr_b16 v[208:209], v248 offset:49192\n\t" \
  "ds_read_b32 v222, v222 offset:10016\n\t" \
  "v_mfma_f32_16x16x32_bf16 v[64:67], v[212:215], v[194:197], v[64:67]\n\t" \
  "s_waitcnt lgkmcnt(6)\n\t" \
  "ds_read_b64_tr_b16 v[212:213], v248 offset:52264\n\t" \
  "ds_read_u16_d16_hi v225, v225 offset:10528\n\t" \
  "v_or_b32 v171, v232, v233\n\t" \
  "v_mfma_f32_16x16x32_bf16 v[20:23], v[216:219], v[178:181], v[20:23]\n\t" \
  "v_or_b32 v170, v234, v235\n\t" \
  "ds_read_b64_tr_b16 v[214:215], v248 offset:53288\n\t" \
  "v_fma_f32 v163, |v163|, v229, v163 mul:4\n\t" \
  "v_fma_f32 v162, |v162|, v228, v162 mul:4\n\t" \
  "v_mfma_f32_16x16x32_bf16 v[68:71], v[216:219], v[194:197], v[68:71]\n\t" \
  "ds_read_u16_d16_hi v227, v227 offset:10528\n\t" \
  "v_cvt_pk_bf16_f32 v163, v162, v163\n\t" \
  "v_mfma_f32_16x16x32_bf16 v[44:47], v[216:219], v[186:189], v[44:47]\n\t" \
  "ds_read_u16 v224, v224 offset:10528\n\t" \
  "v_fma_f32 v161, |v161|, v231, v161 mul:4\n\t" \
  "v_fma_f32 v160, |v160|, v230, v160 mul:4\n\t" \
  "v_mfma_f32_16x16x32_bf16 v[92:95], v[216:219], v[202:205], v[92:95]\n\t" \
  "ds_read_b64_tr_b16 v[216:217], v248 offset:51240\n\t" \
  "v_cvt_pknorm_i16_f32 v228, |v164|, |v165|\n\t" \
  "v_cvt_pknorm_i16_f32 v230, |v166|, |v167|\n\t" \
  "v_cvt_pk_bf16_f32 v162, v160, v161\n\t" \
  "s_waitcnt lgkmcnt(7)\n\t" \
  "v_mfma_f32_16x16x32_bf16 v[176:179], v[208:211], v[96:99], 0\n\t" \
  "ds_read_b64_tr_b16 v[218:219], v248 offset:54312\n\t" \
  "v_lshlrev_b32_sdwa v229, 2, v228 dst_sel:DWORD dst_unused:UNUSED_PAD src0_sel:DWORD src1_sel:BYTE_3\n\t" \
  "v_lshlrev_b32_sdwa v228, 2, v228 dst_sel:DWORD dst_unused:UNUSED_PAD src0_sel:DWORD src1_sel:BYTE_1\n\t" \
  "v_mfma_f32_16x16x32_bf16 v[184:187], v[208:211], v[100:103], 0\n\t" \
  "v_lshlrev_b32_sdwa v231, 2, v230 dst_sel:DWORD dst_unused:UNUSED_PAD src0_sel:DWORD src1_sel:BYTE_3\n\t" \
  "ds_read_b32 v229, v229 offset:10016\n\t" \
  "v_lshlrev_b32_sdwa v230, 2, v230 dst_sel:DWORD dst_unused:UNUSED_PAD src0_sel:DWORD src1_sel:BYTE_1\n\t" \
  "v_mfma_f32_16x16x32_bf16 v[192:195], v[208:211], v[104:107], 0\n\t" \
  "ds_read_u16 v226, v226 offset:10528\n\t" \
  "v_lshlrev_b32_sdwa v233, 1, v173 dst_sel:DWORD dst_unused:UNUSED_PAD src0_sel:DWORD src1_sel:WORD_1\n\t" \
  "v_lshlrev_b32_sdwa v232, 1, v172 dst_sel:DWORD dst_unused:UNUSED_PAD src0_sel:DWORD src1_sel:WORD_1\n\t" \
  "v_mfma_f32_16x16x32_bf16 v[200:203], v[208:211], v[108:111], 0\n\t" \
  "ds_read_b32 v228, v228 offset:10016\n\t" \
  "v_lshlrev_b32_sdwa v235, 1, v175 dst_sel:DWORD dst_unused:UNUSED_PAD src0_sel:DWORD src1_sel:WORD_1\n\t" \
  "ds_read_b64_tr_b16 v[208:209], v248 offset:49704\n\t" \
  "s_waitcnt lgkmcnt(8)\n\t" \
  "v_mfma_f32_16x16x32_bf16 v[176:179], v[212:215], v[112:115], v[176:179]\n\t" \
  "ds_read_b64_tr_b16 v[210:211], v248 offset:50728\n\t" \
  "v_lshlrev_b32_sdwa v234, 1, v174 dst_sel:DWORD dst_unused:UNUSED_PAD src0_sel:DWORD src1_sel:WORD_1\n\t" \
  "v_fma_mix_f32 v221, |v149|, v221, v221 op_sel:[0,1,0] op_sel_hi:[0,1,1] clamp\n\t" \
  "v_mfma_f32_16x16x32_bf16 v[184:187], v[212:215], v[116:119], v[184:187]\n\t" \
  "ds_read_b32 v230, v230 offset:10016\n\t" \
  "ds_read_b32 v231, v231 offset:10016\n\t" \
  "v_fma_mix_f32 v220, |v148|, v220, v220 op_sel:[0,1,0] op_sel_hi:[0,1,1] clamp\n\t" \
  "v_mfma_f32_16x16x32_bf16 v[192:195], v[212:215], v[120:123], v[192:195]\n\t" \
  "ds_read_u16_d16_hi v233, v233 offset:10528\n\t" \
  "v_fma_mix_f32 v223, |v151|, v223, v223 op_sel:[0,1,0] op_sel_hi:[0,1,1] clamp\n\t" \
  "v_fma_mix_f32 v222, |v150|, v222, v222 op_sel:[0,1,0] op_sel_hi:[0,1,1] clamp\n\t" \
  "s_waitcnt lgkmcnt(6)\n\t" \
  "v_mfma_f32_16x16x32_bf16 v[200:203], v[212:215], v[124:127], v[200:203]\n\t" \
  "ds_read_b64_tr_b16 v[212:213], v248 offset:52776\n\t" \
  "v_or_b32 v156, v224, v225\n\t" \
  "v_or_b32 v157, v226, v227\n\t" \
  "v_mfma_f32_16x16x32_bf16 v[176:179], v[216:219], v[128:131], v[176:179]\n\t" \
  "ds_read_b64_tr_b16 v[214:215], v248 offset:53800\n\t" \
  "v_fma_f32 v148, |v148|, v220, v148 mul:4\n\t" \
  "v_fma_f32 v151, |v151|, v223, v151 mul:4\n\t" \
  "v_fma_f32 v149, |v149|, v221, v149 mul:4\n\t" \
  "v_mfma_f32_16x16x32_bf16 v[184:187], v[216:219], v[132:135], v[184:187]\n\t" \
  "ds_read_u16 v232, v232 offset:10528\n\t" \
  "v_fma_f32 v150, |v150|, v222, v150 mul:4\n\t" \
  "v_mfma_f32_16x16x32_bf16 v[192:195], v[216:219], v[136:139], v[192:195]\n\t" \
  "ds_read_u16_d16_hi v235, v235 offset:10528\n\t" \
  "v_cvt_pk_bf16_f32 v148, v148, v149\n\t" \
  "v_cvt_pk_bf16_f32 v149, v150, v151\n\t" \
  "v_mfma_f32_16x16x32_bf16 v[200:203], v[216:219], v[140:143], v[200:203]\n\t" \
  "ds_read_b64_tr_b16 v[216:217], v248 offset:51752\n\t" \
  "v_cvt_pknorm_i16_f32 v220, |v178|, |v179|\n\t" \
  "v_cvt_pknorm_i16_f32 v222, |v176|, |v177|\n\t" \
  "v_lshlrev_b32_sdwa v221, 2, v220 dst_sel:DWORD dst_unused:UNUSED_PAD src0_sel:DWORD src1_sel:BYTE_3\n\t" \
  "s_waitcnt lgkmcnt(8)\n\t" \
  "v_mfma_f32_16x16x32_bf16 v[180:183], v[208:211], v[96:99], 0\n\t" \
  "v_lshlrev_b32_sdwa v225, 1, v187 dst_sel:DWORD dst_unused:UNUSED_PAD src0_sel:DWORD src1_sel:WORD_1\n\t" \
  "ds_read_b64_tr_b16 v[218:219], v248 offset:54824\n\t" \
  "s_waitcnt vmcnt(0)\n\t" \
  "v_lshlrev_b32_sdwa v220, 2, v220 dst_sel:DWORD dst_unused:UNUSED_PAD src0_sel:DWORD src1_sel:BYTE_1\n\t" \
  "v_mfma_f32_16x16x32_bf16 v[188:191], v[208:211], v[100:103], 0\n\t" \
  "v_lshlrev_b32_sdwa v223, 2, v222 dst_sel:DWORD dst_unused:UNUSED_PAD src0_sel:DWORD src1_sel:BYTE_3\n\t" \
  "v_lshlrev_b32_sdwa v222, 2, v222 dst_sel:DWORD dst_unused:UNUSED_PAD src0_sel:DWORD src1_sel:BYTE_1\n\t" \
  "ds_read_u16 v234, v234 offset:10528\n\t" \
  "ds_read_b32 v220, v220 offset:10016\n\t" \
  "v_mfma_f32_16x16x32_bf16 v[196:199], v[208:211], v[104:107], 0\n\t" \
  "ds_read_b32 v221, v221 offset:10016\n\t" \
  "v_lshlrev_b32_sdwa v224, 1, v186 dst_sel:DWORD dst_unused:UNUSED_PAD src0_sel:DWORD src1_sel:WORD_1\n\t" \
  "v_mfma_f32_16x16x32_bf16 v[204:207], v[208:211], v[108:111], 0\n\t" \
  "s_waitcnt lgkmcnt(7)\n\t" \
  "v_mfma_f32_16x16x32_bf16 v[180:183], v[212:215], v[112:115], v[180:183]\n\t" \
  "ds_read_b32 v223, v223 offset:10016\n\t" \
  "v_add_u32 v249, -81480, v249\n\t" \
  "v_add_u32 v248, -81480, v248\n\t" \
  "v_lshlrev_b32_sdwa v227, 1, v185 dst_sel:DWORD dst_unused:UNUSED_PAD src0_sel:DWORD src1_sel:WORD_1\n\t" \
  "v_lshlrev_b32_sdwa v226, 1, v184 dst_sel:DWORD dst_unused:UNUSED_PAD src0_sel:DWORD src1_sel:WORD_1\n\t" \
  "v_mfma_f32_16x16x32_bf16 v[188:191], v[212:215], v[116:119], v[188:191]\n\t" \
  "ds_read_b64_tr_b16 v[208:209], v249 offset:24600\n\t" \
  "ds_read_b32 v222, v222 offset:10016\n\t" \
  "ds_read_b64_tr_b16 v[210:211], v249 offset:26136\n\t" \
  "v_fma_mix_f32 v229, |v165|, v229, v229 op_sel:[0,1,0] op_sel_hi:[0,1,1] clamp\n\t" \
  "v_mfma_f32_16x16x32_bf16 v[196:199], v[212:215], v[120:123], v[196:199]\n\t" \
  "v_fma_mix_f32 v228, |v164|, v228, v228 op_sel:[0,1,0] op_sel_hi:[0,1,1] clamp\n\t" \
  "v_fma_mix_f32 v231, |v167|, v231, v231 op_sel:[0,1,0] op_sel_hi:[0,1,1] clamp\n\t" \
  "v_mfma_f32_16x16x32_bf16 v[204:207], v[212:215], v[124:127], v[204:207]\n\t" \
  "ds_read_b64_tr_b16 v[212:213], v249 offset:24608\n\t" \
  "ds_read_u16_d16_hi v225, v225 offset:10528\n\t" \
  "s_waitcnt lgkmcnt(8)\n\t" \
  "v_fma_mix_f32 v230, |v166|, v230, v230 op_sel:[0,1,0] op_sel_hi:[0,1,1] clamp\n\t" \
  "v_mfma_f32_16x16x32_bf16 v[180:183], v[216:219], v[128:131], v[180:183]\n\t" \
  "ds_read_b64_tr_b16 v[214:215], v249 offset:26144\n\t" \
  "v_or_b32 v173, v234, v235\n\t" \
  "v_or_b32 v172, v232, v233\n\t" \
  "v_mfma_f32_16x16x32_bf16 v[188:191], v[216:219], v[132:135], v[188:191]\n\t" \
  "ds_read_u16 v224, v224 offset:10528\n\t" \
  "v_fma_f32 v165, |v165|, v229, v165 mul:4\n\t" \
  "v_mfma_f32_16x16x32_bf16 v[196:199], v[216:219], v[136:139], v[196:199]\n\t" \
  "ds_read_u16_d16_hi v227, v227 offset:10528\n\t" \
  "v_fma_f32 v167, |v167|, v231, v167 mul:4\n\t" \
  "v_fma_f32 v166, |v166|, v230, v166 mul:4\n\t" \
  "v_mfma_f32_16x16x32_bf16 v[204:207], v[216:219], v[140:143], v[204:207]\n\t" \
  "ds_read_b64_tr_b16 v[216:217], v249 offset:24664\n\t" \
  "v_fma_f32 v164, |v164|, v228, v164 mul:4\n\t" \
  "v_cvt_pk_bf16_f32 v164, v164, v165\n\t" \
  "v_cvt_pk_bf16_f32 v165, v166, v167\n\t" \
  "v_mfma_f32_4x4x4_16b_bf16 v[64:67], v[242:243], v[236:237], v[64:67]\n\t" \
  "v_cvt_pknorm_i16_f32 v230, |v192|, |v193|\n\t" \
  "v_mfma_f32_4x4x4_16b_bf16 v[68:71], v[242:243], v[238:239], v[68:71]\n\t" \
  "ds_read_b64_tr_b16 v[218:219], v249 offset:26200\n\t" \
  "v_cvt_pknorm_i16_f32 v228, |v194|, |v195|\n\t" \
  "v_lshlrev_b32_sdwa v235, 1, v201 dst_sel:DWORD dst_unused:UNUSED_PAD src0_sel:DWORD src1_sel:WORD_1\n\t" \
  "s_waitcnt lgkmcnt(7)\n\t" \
  "v_mfma_f32_16x16x32_bf16 v[0:3], v[208:211], v[146:149], v[0:3]\n\t" \
  "ds_read_u16 v226, v226 offset:10528\n\t" \
  "v_lshlrev_b32_sdwa v232, 1, v202 dst_sel:DWORD dst_unused:UNUSED_PAD src0_sel:DWORD src1_sel:WORD_1\n\t" \
  "v_lshlrev_b32_sdwa v229, 2, v228 dst_sel:DWORD dst_unused:UNUSED_PAD src0_sel:DWORD src1_sel:BYTE_3\n\t" \
  "v_lshlrev_b32_sdwa v228, 2, v228 dst_sel:DWORD dst_unused:UNUSED_PAD src0_sel:DWORD src1_sel:BYTE_1\n\t" \
  "v_mfma_f32_16x16x32_bf16 v[24:27], v[208:211], v[154:157], v[24:27]\n\t" \
  "ds_read_b32 v229, v229 offset:10016\n\t" \
  "v_lshlrev_b32_sdwa v231, 2, v230 dst_sel:DWORD dst_unused:UNUSED_PAD src0_sel:DWORD src1_sel:BYTE_3\n\t" \
  "v_lshlrev_b32_sdwa v230, 2, v230 dst_sel:DWORD dst_unused:UNUSED_PAD src0_sel:DWORD src1_sel:BYTE_1\n\t" \
  "v_mfma_f32_16x16x32_bf16 v[48:51], v[208:211], v[162:165], v[48:51]\n\t" \
  "ds_read_b32 v228, v228 offset:10016\n\t" \
  "v_lshlrev_b32_sdwa v233, 1, v203 dst_sel:DWORD dst_unused:UNUSED_PAD src0_sel:DWORD src1_sel:WORD_1\n\t" \
  "v_mfma_f32_16x16x32_bf16 v[72:75], v[208:211], v[170:173], v[72:75]\n\t" \
  "s_waitcnt lgkmcnt(7)\n\t" \
  "ds_read_b64_tr_b16 v[208:209], v249 offset:24672\n\t" \
  "v_mfma_f32_16x16x32_bf16 v[4:7], v[212:215], v[146:149], v[4:7]\n\t" \
  "ds_read_b64_tr_b16 v[210:211], v249 offset:26208\n\t" \
  "v_fma_mix_f32 v220, |v178|, v220, v220 op_sel:[0,1,0] op_sel_hi:[0,1,1] clamp\n\t" \
  "v_lshlrev_b32_sdwa v234, 1, v200 dst_sel:DWORD dst_unused:UNUSED_PAD src0_sel:DWORD src1_sel:WORD_1\n\t" \
  "v_fma_mix_f32 v221, |v179|, v221, v221 op_sel:[0,1,0] op_sel_hi:[0,1,1] clamp\n\t" \
  "v_mfma_f32_16x16x32_bf16 v[28:31], v[212:215], v[154:157], v[28:31]\n\t" \
  "ds_read_b32 v231, v231 offset:10016\n\t" \
  "ds_read_b32 v230, v230 offset:10016\n\t" \
  "v_mfma_f32_16x16x32_bf16 v[52:55], v[212:215], v[162:165], v[52:55]\n\t" \
  "s_waitcnt lgkmcnt(6)\n\t" \
  "ds_read_u16_d16_hi v233, v233 offset:10528\n\t" \
  "v_fma_mix_f32 v223, |v177|, v223, v223 op_sel:[0,1,0] op_sel_hi:[0,1,1] clamp\n\t" \
  "v_fma_mix_f32 v222, |v176|, v222, v222 op_sel:[0,1,0] op_sel_hi:[0,1,1] clamp\n\t" \
  "v_or_b32 v187, v224, v225\n\t" \
  "v_mfma_f32_16x16x32_bf16 v[76:79], v[212:215], v[170:173], v[76:79]\n\t" \
  "ds_read_b64_tr_b16 v[212:213], v249 offset:24728\n\t" \
  "v_or_b32 v186, v226, v227\n\t" \
  "v_mfma_f32_16x16x32_bf16 v[8:11], v[216:219], v[146:149], v[8:11]\n\t" \
  "ds_read_b64_tr_b16 v[214:215], v249 offset:26264\n\t" \
  "v_fma_f32 v179, |v179|, v221, v179 mul:4\n\t" \
  "v_mfma_f32_16x16x32_bf16 v[32:35], v[216:219], v[154:157], v[32:35]\n\t" \
  "v_fma_f32 v178, |v178|, v220, v178 mul:4\n\t" \
  "v_fma_f32 v177, |v177|, v223, v177 mul:4\n\t" \
  "ds_read_u16 v232, v232 offset:10528\n\t" \
  "v_fma_f32 v176, |v176|, v222, v176 mul:4\n\t" \
  "v_mfma_f32_16x16x32_bf16 v[56:59], v[216:219], v[162:165], v[56:59]\n\t" \
  "ds_read_u16_d16_hi v235, v235 offset:10528\n\t" \
  "v_cvt_pk_bf16_f32 v179, v178, v179\n\t" \
  "v_cvt_pk_bf16_f32 v178, v176, v177\n\t" \
  "v_mfma_f32_16x16x32_bf16 v[80:83], v[216:219], v[170:173], v[80:83]\n\t" \
  "s_waitcnt lgkmcnt(7)\n\t" \
  "v_cvt_pknorm_i16_f32 v220, |v180|, |v181|\n\t" \
  "v_cvt_pknorm_i16_f32 v222, |v182|, |v183|\n\t" \
  "ds_read_b64_tr_b16 v[216:217], v249 offset:24736\n\t" \
  "v_mfma_f32_16x16x32_bf16 v[12:15], v[208:211], v[146:149], v[12:15]\n\t" \
  "ds_read_b64_tr_b16 v[218:219], v249 offset:26272\n\t" \
  "v_lshlrev_b32_sdwa v221, 2, v220 dst_sel:DWORD dst_unused:UNUSED_PAD src0_sel:DWORD src1_sel:BYTE_3\n\t" \
  "v_lshlrev_b32_sdwa v220, 2, v220 dst_sel:DWORD dst_unused:UNUSED_PAD src0_sel:DWORD src1_sel:BYTE_1\n\t" \
  "v_mfma_f32_16x16x32_bf16 v[36:39], v[208:211], v[154:157], v[36:39]\n\t" \
  "ds_read_u16 v234, v234 offset:10528\n\t" \
  "v_lshlrev_b32_sdwa v223, 2, v222 dst_sel:DWORD dst_unused:UNUSED_PAD src0_sel:DWORD src1_sel:BYTE_3\n\t" \
  "v_mfma_f32_16x16x32_bf16 v[60:63], v[208:211], v[162:165], v[60:63]\n\t" \
  "ds_read_b32 v221, v221 offset:10016\n\t" \
  "v_lshlrev_b32_sdwa v222, 2, v222 dst_sel:DWORD dst_unused:UNUSED_PAD src0_sel:DWORD src1_sel:BYTE_1\n\t" \
  "v_lshlrev_b32_sdwa v225, 1, v189 dst_sel:DWORD dst_unused:UNUSED_PAD src0_sel:DWORD src1_sel:WORD_1\n\t" \
  "v_mfma_f32_16x16x32_bf16 v[84:87], v[208:211], v[170:173], v[84:87]\n\t" \
  "ds_read_b64_tr_b16 v[208:209], v248 offset:36904\n\t" \
  "v_lshlrev_b32_sdwa v224, 1, v188 dst_sel:DWORD dst_unused:UNUSED_PAD src0_sel:DWORD src1_sel:WORD_1\n\t" \
  "v_lshlrev_b32_sdwa v227, 1, v191 dst_sel:DWORD dst_unused:UNUSED_PAD src0_sel:DWORD src1_sel:WORD_1\n\t" \
  "s_waitcnt lgkmcnt(7)\n\t" \
  "v_mfma_f32_16x16x32_bf16 v[16:19], v[212:215], v[146:149], v[16:19]\n\t" \
  "ds_read_b32 v220, v220 offset:10016\n\t" \
  "v_lshlrev_b32_sdwa v226, 1, v190 dst_sel:DWORD dst_unused:UNUSED_PAD src0_sel:DWORD src1_sel:WORD_1\n\t" \
  "v_mfma_f32_16x16x32_bf16 v[40:43], v[212:215], v[154:157], v[40:43]\n\t" \
  "ds_read_b32 v223, v223 offset:10016\n\t" \
  "v_fma_mix_f32 v229, |v195|, v229, v229 op_sel:[0,1,0] op_sel_hi:[0,1,1] clamp\n\t" \
  "v_fma_mix_f32 v228, |v194|, v228, v228 op_sel:[0,1,0] op_sel_hi:[0,1,1] clamp\n\t" \
  "v_mfma_f32_16x16x32_bf16 v[64:67], v[212:215], v[162:165], v[64:67]\n\t" \
  "s_waitcnt lgkmcnt(5)\n\t" \
  "ds_read_b32 v222, v222 offset:10016\n\t" \
  "v_fma_mix_f32 v231, |v193|, v231, v231 op_sel:[0,1,0] op_sel_hi:[0,1,1] clamp\n\t" \
  "v_fma_mix_f32 v230, |v192|, v230, v230 op_sel:[0,1,0] op_sel_hi:[0,1,1] clamp\n\t" \
  "v_mfma_f32_16x16x32_bf16 v[88:91], v[212:215], v[170:173], v[88:91]\n\t" \
  "ds_read_b64_tr_b16 v[212:213], v248 offset:39976\n\t" \
  "ds_read_u16_d16_hi v225, v225 offset:10528\n\t" \
  "v_or_b32 v203, v232, v233\n\t" \
  "s_waitcnt lgkmcnt(7)\n\t" \
  "ds_read_b64_tr_b16 v[210:211], v248 offset:37928\n\t" \
  "v_mfma_f32_16x16x32_bf16 v[20:23], v[216:219], v[146:149], v[20:23]\n\t" \
  "ds_read_b64_tr_b16 v[214:215], v248 offset:41000\n\t" \
  "v_or_b32 v202, v234, v235\n\t" \
  "v_fma_f32 v195, |v195|, v229, v195 mul:4\n\t" \
  "v_mfma_f32_16x16x32_bf16 v[44:47], v[216:219], v[154:157], v[44:47]\n\t" \
  "ds_read_u16 v224, v224 offset:10528\n\t" \
  "v_fma_f32 v194, |v194|, v228, v194 mul:4\n\t" \
  "v_fma_f32 v193, |v193|, v231, v193 mul:4\n\t" \
  "v_mfma_f32_16x16x32_bf16 v[68:71], v[216:219], v[162:165], v[68:71]\n\t" \
  "ds_read_u16_d16_hi v227, v227 offset:10528\n\t" \
  "v_cvt_pk_bf16_f32 v195, v194, v195\n\t" \
  "v_fma_f32 v192, |v192|, v230, v192 mul:4\n\t" \
  "v_mfma_f32_16x16x32_bf16 v[92:95], v[216:219], v[170:173], v[92:95]\n\t" \
  "ds_read_b64_tr_b16 v[216:217], v248 offset:38952\n\t" \
  "v_cvt_pk_bf16_f32 v194, v192, v193\n\t" \
  "v_cvt_pknorm_i16_f32 v228, |v196|, |v197|\n\t" \
  "v_lshlrev_b32_sdwa v229, 2, v228 dst_sel:DWORD dst_unused:UNUSED_PAD src0_sel:DWORD src1_sel:BYTE_3\n\t" \
  "s_waitcnt lgkmcnt(4)\n\t" \
  "v_mfma_f32_16x16x32_bf16 v[144:147], v[208:211], v[96:99], 0\n\t" \
  "ds_read_b64_tr_b16 v[218:219], v248 offset:42024\n\t" \
  "v_cvt_pknorm_i16_f32 v230, |v198|, |v199|\n\t" \
  "v_mfma_f32_16x16x32_bf16 v[152:155], v[208:211], v[100:103], 0\n\t" \
  "ds_read_u16 v226, v226 offset:10528\n\t" \
  "v_lshlrev_b32_sdwa v228, 2, v228 dst_sel:DWORD dst_unused:UNUSED_PAD src0_sel:DWORD src1_sel:BYTE_1\n\t" \
  "v_mfma_f32_16x16x32_bf16 v[160:163], v[208:211], v[104:107], 0\n\t" \
  "v_lshlrev_b32_sdwa v231, 2, v230 dst_sel:DWORD dst_unused:UNUSED_PAD src0_sel:DWORD src1_sel:BYTE_3\n\t" \
  "v_lshlrev_b32_sdwa v230, 2, v230 dst_sel:DWORD dst_unused:UNUSED_PAD src0_sel:DWORD src1_sel:BYTE_1\n\t" \
  "v_lshlrev_b32_sdwa v233, 1, v205 dst_sel:DWORD dst_unused:UNUSED_PAD src0_sel:DWORD src1_sel:WORD_1\n\t" \
  "v_mfma_f32_16x16x32_bf16 v[168:171], v[208:211], v[108:111], 0\n\t" \
  "ds_read_b64_tr_b16 v[208:209], v248 offset:37416\n\t" \
  "v_lshlrev_b32_sdwa v232, 1, v204 dst_sel:DWORD dst_unused:UNUSED_PAD src0_sel:DWORD src1_sel:WORD_1\n\t" \
  "ds_read_b64_tr_b16 v[210:211], v248 offset:38440\n\t" \
  "v_lshlrev_b32_sdwa v235, 1, v207 dst_sel:DWORD dst_unused:UNUSED_PAD src0_sel:DWORD src1_sel:WORD_1\n\t" \
  "s_waitcnt lgkmcnt(7)\n\t" \
  "v_mfma_f32_16x16x32_bf16 v[144:147], v[212:215], v[112:115], v[144:147]\n\t" \
  "ds_read_b32 v228, v228 offset:10016\n\t" \
  "ds_read_b32 v229, v229 offset:10016\n\t" \
  "v_lshlrev_b32_sdwa v234, 1, v206 dst_sel:DWORD dst_unused:UNUSED_PAD src0_sel:DWORD src1_sel:WORD_1\n\t" \
  "v_mfma_f32_16x16x32_bf16 v[152:155], v[212:215], v[116:119], v[152:155]\n\t" \
  "ds_read_b32 v231, v231 offset:10016\n\t" \
  "v_fma_mix_f32 v221, |v181|, v221, v221 op_sel:[0,1,0] op_sel_hi:[0,1,1] clamp\n\t" \
  "v_fma_mix_f32 v220, |v180|, v220, v220 op_sel:[0,1,0] op_sel_hi:[0,1,1] clamp\n\t" \
  "v_mfma_f32_16x16x32_bf16 v[160:163], v[212:215], v[120:123], v[160:163]\n\t" \
  "ds_read_b32 v230, v230 offset:10016\n\t" \
  "ds_read_u16_d16_hi v233, v233 offset:10528\n\t" \
  "v_mfma_f32_16x16x32_bf16 v[168:171], v[212:215], v[124:127], v[168:171]\n\t" \
  "ds_read_b64_tr_b16 v[212:213], v248 offset:40488\n\t" \
  "v_fma_mix_f32 v223, |v183|, v223, v223 op_sel:[0,1,0] op_sel_hi:[0,1,1] clamp\n\t" \
  "s_waitcnt lgkmcnt(8)\n\t" \
  "v_fma_f32 v183, |v183|, v223, v183 mul:4\n\t" \
  "v_fma_mix_f32 v222, |v182|, v222, v222 op_sel:[0,1,0] op_sel_hi:[0,1,1] clamp\n\t" \
  "v_or_b32 v188, v224, v225\n\t" \
  "v_mfma_f32_16x16x32_bf16 v[144:147], v[216:219], v[128:131], v[144:147]\n\t" \
  "v_fma_f32 v181, |v181|, v221, v181 mul:4\n\t" \
  "v_fma_f32 v182, |v182|, v222, v182 mul:4\n\t" \
  "v_or_b32 v189, v226, v227\n\t" \
  "v_mfma_f32_16x16x32_bf16 v[152:155], v[216:219], v[132:135], v[152:155]\n\t" \
  "ds_read_b64_tr_b16 v[214:215], v248 offset:41512\n\t" \
  "ds_read_u16 v232, v232 offset:10528\n\t" \
  "v_fma_f32 v180, |v180|, v220, v180 mul:4\n\t" \
  "v_mfma_f32_16x16x32_bf16 v[160:163], v[216:219], v[136:139], v[160:163]\n\t" \
  "ds_read_u16_d16_hi v235, v235 offset:10528\n\t" \
  "v_cvt_pk_bf16_f32 v180, v180, v181\n\t" \
  "v_mfma_f32_16x16x32_bf16 v[168:171], v[216:219], v[140:143], v[168:171]\n\t" \
  "ds_read_b64_tr_b16 v[216:217], v248 offset:39464\n\t" \
  "v_cvt_pk_bf16_f32 v181, v182, v183\n\t" \
  "v_cvt_pknorm_i16_f32 v220, |v146|, |v147|\n\t" \
  "v_lshlrev_b32_sdwa v221, 2, v220 dst_sel:DWORD dst_unused:UNUSED_PAD src0_sel:DWORD src1_sel:BYTE_3\n\t" \
  "s_waitcnt lgkmcnt(10)\n\t" \
  "v_mfma_f32_16x16x32_bf16 v[148:151], v[208:211], v[96:99], 0\n\t" \
  "ds_read_b64_tr_b16 v[218:219], v248 offset:42536\n\t" \
  "v_cvt_pknorm_i16_f32 v222, |v144|, |v145|\n\t" \
  "v_mfma_f32_16x16x32_bf16 v[156:159], v[208:211], v[100:103], 0\n\t" \
  "ds_read_u16 v234, v234 offset:10528\n\t" \
  "v_lshlrev_b32_sdwa v220, 2, v220 dst_sel:DWORD dst_unused:UNUSED_PAD src0_sel:DWORD src1_sel:BYTE_1\n\t" \
  "v_lshlrev_b32_sdwa v223, 2, v222 dst_sel:DWORD dst_unused:UNUSED_PAD src0_sel:DWORD src1_sel:BYTE_3\n\t" \
  "v_mfma_f32_16x16x32_bf16 v[164:167], v[208:211], v[104:107], 0\n\t" \
  "ds_read_b32 v221, v221 offset:10016\n\t" \
  "v_lshlrev_b32_sdwa v222, 2, v222 dst_sel:DWORD dst_unused:UNUSED_PAD src0_sel:DWORD src1_sel:BYTE_1\n\t" \
  "v_mfma_f32_16x16x32_bf16 v[172:175], v[208:211], v[108:111], 0\n\t" \
  "ds_read_b32 v220, v220 offset:10016\n\t" \
  "v_lshlrev_b32_sdwa v226, 1, v152 dst_sel:DWORD dst_unused:UNUSED_PAD src0_sel:DWORD src1_sel:WORD_1\n\t" \
  "v_lshlrev_b32_sdwa v225, 1, v155 dst_sel:DWORD dst_unused:UNUSED_PAD src0_sel:DWORD src1_sel:WORD_1\n\t" \
  "s_waitcnt lgkmcnt(7)\n\t" \
  "v_mfma_f32_16x16x32_bf16 v[156:159], v[212:215], v[116:119], v[156:159]\n\t" \
  "ds_read_b64_tr_b16 v[208:209], v249 offset:30752\n\t" \
  "v_fma_mix_f32 v229, |v197|, v229, v229 op_sel:[0,1,0] op_sel_hi:[0,1,1] clamp\n\t" \
  "v_mfma_f32_16x16x32_bf16 v[164:167], v[212:215], v[120:123], v[164:167]\n\t" \
  "ds_read_b32 v222, v222 offset:10016\n\t" \
  "ds_read_b64_tr_b16 v[210:211], v249 offset:32288\n\t" \
  "v_fma_mix_f32 v228, |v196|, v228, v228 op_sel:[0,1,0] op_sel_hi:[0,1,1] clamp\n\t" \
  "v_fma_mix_f32 v231, |v199|, v231, v231 op_sel:[0,1,0] op_sel_hi:[0,1,1] clamp\n\t" \
  "s_waitcnt lgkmcnt(6)\n\t" \
  "v_mfma_f32_16x16x32_bf16 v[156:159], v[216:219], v[132:135], v[156:159]\n\t" \
  "v_lshlrev_b32_sdwa v224, 1, v154 dst_sel:DWORD dst_unused:UNUSED_PAD src0_sel:DWORD src1_sel:WORD_1\n\t" \
  "v_lshlrev_b32_sdwa v227, 1, v153 dst_sel:DWORD dst_unused:UNUSED_PAD src0_sel:DWORD src1_sel:WORD_1\n\t" \
  "v_mfma_f32_16x16x32_bf16 v[148:151], v[212:215], v[112:115], v[148:151]\n\t" \
  "ds_read_b32 v223, v223 offset:10016\n\t" \
  "ds_read_u16 v224, v224 offset:10528\n\t" \
  "v_fma_f32 v197, |v197|, v229, v197 mul:4\n\t" \
  "v_mfma_f32_16x16x32_bf16 v[172:175], v[212:215], v[124:127], v[172:175]\n\t" \
  "s_waitcnt lgkmcnt(7)\n\t" \
  "ds_read_b64_tr_b16 v[212:213], v249 offset:30760\n\t" \
  "ds_read_u16_d16_hi v225, v225 offset:10528\n\t" \
  "v_fma_mix_f32 v230, |v198|, v230, v230 op_sel:[0,1,0] op_sel_hi:[0,1,1] clamp\n\t" \
  "v_or_b32 v205, v234, v235\n\t" \
  "v_mfma_f32_16x16x32_bf16 v[148:151], v[216:219], v[128:131], v[148:151]\n\t" \
  "ds_read_b64_tr_b16 v[214:215], v249 offset:32296\n\t" \
  "v_or_b32 v204, v232, v233\n\t" \
  "v_fma_f32 v196, |v196|, v228, v196 mul:4\n\t" \
  "v_mfma_f32_16x16x32_bf16 v[164:167], v[216:219], v[136:139], v[164:167]\n\t" \
  "v_fma_f32 v199, |v199|, v231, v199 mul:4\n\t" \
  "v_fma_f32 v198, |v198|, v230, v198 mul:4\n\t" \
  "v_mfma_f32_16x16x32_bf16 v[172:175], v[216:219], v[140:143], v[172:175]\n\t" \
  "ds_read_b64_tr_b16 v[216:217], v249 offset:30816\n\t" \
  "ds_read_u16_d16_hi v227, v227 offset:10528\n\t" \
  "v_cvt_pk_bf16_f32 v196, v196, v197\n\t" \
  "s_waitcnt lgkmcnt(7)\n\t" \
  "v_mfma_f32_16x16x32_bf16 v[0:3], v[208:211], v[178:181], v[0:3]\n\t" \
  "v_cvt_pknorm_i16_f32 v228, |v162|, |v163|\n\t" \
  "v_cvt_pk_bf16_f32 v197, v198, v199\n\t" \
  "ds_read_b64_tr_b16 v[218:219], v249 offset:32352\n\t" \
  "v_cvt_pknorm_i16_f32 v230, |v160|, |v161|\n\t" \
  "v_lshlrev_b32_sdwa v229, 2, v228 dst_sel:DWORD dst_unused:UNUSED_PAD src0_sel:DWORD src1_sel:BYTE_3\n\t" \
  "v_mfma_f32_16x16x32_bf16 v[24:27], v[208:211], v[186:189], v[24:27]\n\t" \
  "ds_read_u16 v226, v226 offset:10528\n\t" \
  "v_lshlrev_b32_sdwa v228, 2, v228 dst_sel:DWORD dst_unused:UNUSED_PAD src0_sel:DWORD src1_sel:BYTE_1\n\t" \
  "v_mfma_f32_16x16x32_bf16 v[48:51], v[208:211], v[194:197], v[48:51]\n\t" \
  "ds_read_b32 v229, v229 offset:10016\n\t" \
  "v_lshlrev_b32_sdwa v231, 2, v230 dst_sel:DWORD dst_unused:UNUSED_PAD src0_sel:DWORD src1_sel:BYTE_3\n\t" \
  "v_lshlrev_b32_sdwa v230, 2, v230 dst_sel:DWORD dst_unused:UNUSED_PAD src0_sel:DWORD src1_sel:BYTE_1\n\t" \
  "v_mfma_f32_16x16x32_bf16 v[72:75], v[208:211], v[202:205], v[72:75]\n\t" \
  "ds_read_b64_tr_b16 v[208:209], v249 offset:30824\n\t" \
  "v_lshlrev_b32_sdwa v233, 1, v171 dst_sel:DWORD dst_unused:UNUSED_PAD src0_sel:DWORD src1_sel:WORD_1\n\t" \
  "v_lshlrev_b32_sdwa v232, 1, v170 dst_sel:DWORD dst_unused:UNUSED_PAD src0_sel:DWORD src1_sel:WORD_1\n\t" \
  "s_waitcnt lgkmcnt(6)\n\t" \
  "v_mfma_f32_16x16x32_bf16 v[4:7], v[212:215], v[178:181], v[4:7]\n\t" \
  "ds_read_b64_tr_b16 v[210:211], v249 offset:32360\n\t" \
  "v_lshlrev_b32_sdwa v235, 1, v169 dst_sel:DWORD dst_unused:UNUSED_PAD src0_sel:DWORD src1_sel:WORD_1\n\t" \
  "v_lshlrev_b32_sdwa v234, 1, v168 dst_sel:DWORD dst_unused:UNUSED_PAD src0_sel:DWORD src1_sel:WORD_1\n\t" \
  "v_mfma_f32_16x16x32_bf16 v[28:31], v[212:215], v[186:189], v[28:31]\n\t" \
  "ds_read_b32 v228, v228 offset:10016\n\t" \
  "ds_read_b32 v231, v231 offset:10016\n\t" \
  "v_fma_mix_f32 v221, |v147|, v221, v221 op_sel:[0,1,0] op_sel_hi:[0,1,1] clamp\n\t" \
  "v_mfma_f32_16x16x32_bf16 v[52:55], v[212:215], v[194:197], v[52:55]\n\t" \
  "ds_read_b32 v230, v230 offset:10016\n\t" \
  "v_fma_mix_f32 v220, |v146|, v220, v220 op_sel:[0,1,0] op_sel_hi:[0,1,1] clamp\n\t" \
  "v_fma_mix_f32 v223, |v145|, v223, v223 op_sel:[0,1,0] op_sel_hi:[0,1,1] clamp\n\t" \
  "v_mfma_f32_16x16x32_bf16 v[76:79], v[212:215], v[202:205], v[76:79]\n\t" \
  "ds_read_b64_tr_b16 v[212:213], v249 offset:30880\n\t" \
  "ds_read_u16_d16_hi v233, v233 offset:10528\n\t" \
  "v_fma_mix_f32 v222, |v144|, v222, v222 op_sel:[0,1,0] op_sel_hi:[0,1,1] clamp\n\t" \
  "s_waitcnt lgkmcnt(8)\n\t" \
  "v_mfma_f32_16x16x32_bf16 v[8:11], v[216:219], v[178:181], v[8:11]\n\t" \
  "ds_read_b64_tr_b16 v[214:215], v249 offset:32416\n\t" \
  "v_or_b32 v155, v224, v225\n\t" \
  "v_or_b32 v154, v226, v227\n\t" \
  "v_mfma_f32_16x16x32_bf16 v[32:35], v[216:219], v[186:189], v[32:35]\n\t" \
  "ds_read_u16 v232, v232 offset:10528\n\t" \
  "v_fma_f32 v147, |v147|, v221, v147 mul:4\n\t" \
  "v_mfma_f32_16x16x32_bf16 v[56:59], v[216:219], v[194:197], v[56:59]\n\t" \
  "ds_read_u16_d16_hi v235, v235 offset:10528\n\t" \
  "v_fma_f32 v145, |v145|, v223, v145 mul:4\n\t" \
  "v_fma_f32 v146, |v146|, v220, v146 mul:4\n\t" \
  "v_fma_f32 v144, |v144|, v222, v144 mul:4\n\t" \
  "v_mfma_f32_16x16x32_bf16 v[80:83], v[216:219], v[202:205], v[80:83]\n\t" \
  "ds_read_b64_tr_b16 v[216:217], v249 offset:30888\n\t" \
  "ds_read_b64_tr_b16 v[218:219], v249 offset:32424\n\t" \
  "v_cvt_pknorm_i16_f32 v220, |v148|, |v149|\n\t" \
  "v_cvt_pk_bf16_f32 v147, v146, v147\n\t" \
  "v_cvt_pk_bf16_f32 v146, v144, v145\n\t" \
  "s_waitcnt lgkmcnt(10)\n\t" \
  "v_mfma_f32_16x16x32_bf16 v[12:15], v[208:211], v[178:181], v[12:15]\n\t" \
  "v_cvt_pknorm_i16_f32 v222, |v150|, |v151|\n\t" \
  "v_mfma_f32_16x16x32_bf16 v[36:39], v[208:211], v[186:189], v[36:39]\n\t" \
  "ds_read_u16 v234, v234 offset:10528\n\t" \
  "v_lshlrev_b32_sdwa v221, 2, v220 dst_sel:DWORD dst_unused:UNUSED_PAD src0_sel:DWORD src1_sel:BYTE_3\n\t" \
  "v_mfma_f32_16x16x32_bf16 v[60:63], v[208:211], v[194:197], v[60:63]\n\t" \
  "ds_read_b32 v221, v221 offset:10016\n\t" \
  "v_lshlrev_b32_sdwa v220, 2, v220 dst_sel:DWORD dst_unused:UNUSED_PAD src0_sel:DWORD src1_sel:BYTE_1\n\t" \
  "v_lshlrev_b32_sdwa v223, 2, v222 dst_sel:DWORD dst_unused:UNUSED_PAD src0_sel:DWORD src1_sel:BYTE_3\n\t" \
  "v_lshlrev_b32_sdwa v222, 2, v222 dst_sel:DWORD dst_unused:UNUSED_PAD src0_sel:DWORD src1_sel:BYTE_1\n\t" \
  "v_mfma_f32_16x16x32_bf16 v[84:87], v[208:211], v[202:205], v[84:87]\n\t" \
  "ds_read_b32 v223, v223 offset:10016\n\t" \
  "ds_read_b64_tr_b16 v[208:209], v248 offset:0\n\t" \
  "v_lshlrev_b32_sdwa v225, 1, v157 dst_sel:DWORD dst_unused:UNUSED_PAD src0_sel:DWORD src1_sel:WORD_1\n\t" \
  "v_lshlrev_b32_sdwa v224, 1, v156 dst_sel:DWORD dst_unused:UNUSED_PAD src0_sel:DWORD src1_sel:WORD_1\n\t" \
  "s_waitcnt lgkmcnt(8)\n\t" \
  "v_mfma_f32_16x16x32_bf16 v[16:19], v[212:215], v[178:181], v[16:19]\n\t" \
  "ds_read_b32 v220, v220 offset:10016\n\t" \
  "v_fma_mix_f32 v229, |v163|, v229, v229 op_sel:[0,1,0] op_sel_hi:[0,1,1] clamp\n\t" \
  "v_mfma_f32_16x16x32_bf16 v[64:67], v[212:215], v[194:197], v[64:67]\n\t" \
  "ds_read_b64_tr_b16 v[210:211], v248 offset:1024\n\t" \
  "v_lshlrev_b32_sdwa v227, 1, v159 dst_sel:DWORD dst_unused:UNUSED_PAD src0_sel:DWORD src1_sel:WORD_1\n\t" \
  "v_lshlrev_b32_sdwa v226, 1, v158 dst_sel:DWORD dst_unused:UNUSED_PAD src0_sel:DWORD src1_sel:WORD_1\n\t" \
  "v_mfma_f32_16x16x32_bf16 v[40:43], v[212:215], v[186:189], v[40:43]\n\t" \
  "ds_read_b32 v222, v222 offset:10016\n\t" \
  "v_fma_mix_f32 v228, |v162|, v228, v228 op_sel:[0,1,0] op_sel_hi:[0,1,1] clamp\n\t" \
  "v_fma_mix_f32 v231, |v161|, v231, v231 op_sel:[0,1,0] op_sel_hi:[0,1,1] clamp\n\t" \
  "v_mfma_f32_16x16x32_bf16 v[88:91], v[212:215], v[202:205], v[88:91]\n\t" \
  "s_waitcnt lgkmcnt(6)\n\t" \
  "ds_read_b64_tr_b16 v[212:213], v248 offset:3072\n\t" \
  "ds_read_u16_d16_hi v225, v225 offset:10528\n\t" \
  "v_or_b32 v171, v232, v233\n\t" \
  "v_or_b32 v170, v234, v235\n\t" \
  "v_mfma_f32_16x16x32_bf16 v[44:47], v[216:219], v[186:189], v[44:47]\n\t" \
  "ds_read_u16 v224, v224 offset:10528\n\t" \
  "v_fma_f32 v163, |v163|, v229, v163 mul:4\n\t" \
  "v_fma_f32 v162, |v162|, v228, v162 mul:4\n\t" \
  "v_fma_f32 v161, |v161|, v231, v161 mul:4\n\t" \
  "v_mfma_f32_16x16x32_bf16 v[68:71], v[216:219], v[194:197], v[68:71]\n\t" \
  "ds_read_u16_d16_hi v227, v227 offset:10528\n\t" \
  "v_fma_mix_f32 v230, |v160|, v230, v230 op_sel:[0,1,0] op_sel_hi:[0,1,1] clamp\n\t" \
  "v_fma_f32 v160, |v160|, v230, v160 mul:4\n\t" \
  "v_mfma_f32_16x16x32_bf16 v[20:23], v[216:219], v[178:181], v[20:23]\n\t" \
  "ds_read_b64_tr_b16 v[214:215], v248 offset:4096\n\t" \
  "v_mfma_f32_16x16x32_bf16 v[92:95], v[216:219], v[202:205], v[92:95]\n\t" \
  "ds_read_b64_tr_b16 v[216:217], v248 offset:2048\n\t" \
  "v_cvt_pk_bf16_f32 v163, v162, v163\n\t" \
  "v_cvt_pk_bf16_f32 v162, v160, v161\n\t" \
  "s_waitcnt lgkmcnt(7)\n\t" \
  "v_mfma_f32_16x16x32_bf16 v[176:179], v[208:211], v[96:99], 0\n\t" \
  "ds_read_b64_tr_b16 v[218:219], v248 offset:5120\n\t" \
  "v_cvt_pknorm_i16_f32 v228, |v164|, |v165|\n\t" \
  "v_mfma_f32_16x16x32_bf16 v[184:187], v[208:211], v[100:103], 0\n\t" \
  "ds_read_u16 v226, v226 offset:10528\n\t" \
  "v_lshlrev_b32_sdwa v229, 2, v228 dst_sel:DWORD dst_unused:UNUSED_PAD src0_sel:DWORD src1_sel:BYTE_3\n\t" \
  "v_lshlrev_b32_sdwa v228, 2, v228 dst_sel:DWORD dst_unused:UNUSED_PAD src0_sel:DWORD src1_sel:BYTE_1\n\t" \
  "v_mfma_f32_16x16x32_bf16 v[192:195], v[208:211], v[104:107], 0\n\t" \
  "s_waitcnt lgkmcnt(3)\n\t" \
  "ds_read_b32 v229, v229 offset:10016\n\t" \
  "v_fma_mix_f32 v220, |v148|, v220, v220 op_sel:[0,1,0] op_sel_hi:[0,1,1] clamp\n\t" \
  "v_cvt_pknorm_i16_f32 v230, |v166|, |v167|\n\t" \
  "v_lshlrev_b32_sdwa v231, 2, v230 dst_sel:DWORD dst_unused:UNUSED_PAD src0_sel:DWORD src1_sel:BYTE_3\n\t" \
  "v_lshlrev_b32_sdwa v230, 2, v230 dst_sel:DWORD dst_unused:UNUSED_PAD src0_sel:DWORD src1_sel:BYTE_1\n\t" \
  "v_mfma_f32_16x16x32_bf16 v[200:203], v[208:211], v[108:111], 0\n\t" \
  "ds_read_b64_tr_b16 v[208:209], v248 offset:512\n\t" \
  "v_lshlrev_b32_sdwa v232, 1, v172 dst_sel:DWORD dst_unused:UNUSED_PAD src0_sel:DWORD src1_sel:WORD_1\n\t" \
  "v_mfma_f32_16x16x32_bf16 v[176:179], v[212:215], v[112:115], v[176:179]\n\t" \
  "s_waitcnt lgkmcnt(2)\n\t" \
  "ds_read_b64_tr_b16 v[210:211], v248 offset:1536\n\t" \
  "v_lshlrev_b32_sdwa v235, 1, v175 dst_sel:DWORD dst_unused:UNUSED_PAD src0_sel:DWORD src1_sel:WORD_1\n\t" \
  "v_lshlrev_b32_sdwa v234, 1, v174 dst_sel:DWORD dst_unused:UNUSED_PAD src0_sel:DWORD src1_sel:WORD_1\n\t" \
  "v_or_b32 v157, v226, v227\n\t" \
  "v_mfma_f32_16x16x32_bf16 v[184:187], v[212:215], v[116:119], v[184:187]\n\t" \
  "ds_read_b32 v228, v228 offset:10016\n\t" \
  "ds_read_b32 v231, v231 offset:10016\n\t" \
  "v_mfma_f32_16x16x32_bf16 v[192:195], v[212:215], v[120:123], v[192:195]\n\t" \
  "ds_read_b32 v230, v230 offset:10016\n\t" \
  "v_fma_mix_f32 v221, |v149|, v221, v221 op_sel:[0,1,0] op_sel_hi:[0,1,1] clamp\n\t" \
  "v_lshlrev_b32_sdwa v233, 1, v173 dst_sel:DWORD dst_unused:UNUSED_PAD src0_sel:DWORD src1_sel:WORD_1\n\t" \
  "v_fma_mix_f32 v223, |v151|, v223, v223 op_sel:[0,1,0] op_sel_hi:[0,1,1] clamp\n\t" \
  "v_mfma_f32_16x16x32_bf16 v[200:203], v[212:215], v[124:127], v[200:203]\n\t" \
  "ds_read_b64_tr_b16 v[212:213], v248 offset:3584\n\t" \
  "ds_read_u16_d16_hi v233, v233 offset:10528\n\t" \
  "v_fma_mix_f32 v222, |v150|, v222, v222 op_sel:[0,1,0] op_sel_hi:[0,1,1] clamp\n\t" \
  "v_mfma_f32_16x16x32_bf16 v[176:179], v[216:219], v[128:131], v[176:179]\n\t" \
  "ds_read_b64_tr_b16 v[214:215], v248 offset:4608\n\t" \
  "v_or_b32 v156, v224, v225\n\t" \
  "v_mfma_f32_16x16x32_bf16 v[184:187], v[216:219], v[132:135], v[184:187]\n\t" \
  "ds_read_u16 v232, v232 offset:10528\n\t" \
  "v_fma_f32 v151, |v151|, v223, v151 mul:4\n\t" \
  "v_fma_f32 v150, |v150|, v222, v150 mul:4\n\t" \
  "v_mfma_f32_16x16x32_bf16 v[200:203], v[216:219], v[140:143], v[200:203]\n\t" \
  "ds_read_u16_d16_hi v235, v235 offset:10528\n\t" \
  "v_fma_f32 v149, |v149|, v221, v149 mul:4\n\t" \
  "v_fma_f32 v148, |v148|, v220, v148 mul:4\n\t" \
  "v_mfma_f32_16x16x32_bf16 v[192:195], v[216:219], v[136:139], v[192:195]\n\t" \
  "ds_read_b64_tr_b16 v[216:217], v248 offset:2560\n\t" \
  "v_cvt_pk_bf16_f32 v148, v148, v149\n\t" \
  "v_cvt_pk_bf16_f32 v149, v150, v151\n\t" \
  "s_waitcnt lgkmcnt(9)\n\t" \
  "v_mfma_f32_16x16x32_bf16 v[180:183], v[208:211], v[96:99], 0\n\t" \
  "ds_read_b64_tr_b16 v[218:219], v248 offset:5632\n\t" \
  "v_cvt_pknorm_i16_f32 v220, |v178|, |v179|\n\t" \
  "v_mfma_f32_16x16x32_bf16 v[188:191], v[208:211], v[100:103], 0\n\t" \
  "ds_read_u16 v234, v234 offset:10528\n\t" \
  "v_lshlrev_b32_sdwa v221, 2, v220 dst_sel:DWORD dst_unused:UNUSED_PAD src0_sel:DWORD src1_sel:BYTE_3\n\t" \
  "v_lshlrev_b32_sdwa v220, 2, v220 dst_sel:DWORD dst_unused:UNUSED_PAD src0_sel:DWORD src1_sel:BYTE_1\n\t" \
  "v_cvt_pknorm_i16_f32 v222, |v176|, |v177|\n\t" \
  "v_mfma_f32_16x16x32_bf16 v[196:199], v[208:211], v[104:107], 0\n\t" \
  "ds_read_b32 v221, v221 offset:10016\n\t" \
  "v_lshlrev_b32_sdwa v223, 2, v222 dst_sel:DWORD dst_unused:UNUSED_PAD src0_sel:DWORD src1_sel:BYTE_3\n\t" \
  "v_lshlrev_b32_sdwa v222, 2, v222 dst_sel:DWORD dst_unused:UNUSED_PAD src0_sel:DWORD src1_sel:BYTE_1\n\t" \
  "v_mfma_f32_16x16x32_bf16 v[204:207], v[208:211], v[108:111], 0\n\t" \
  "ds_read_b32 v222, v222 offset:10016\n\t" \
  "v_fma_mix_f32 v229, |v165|, v229, v229 op_sel:[0,1,0] op_sel_hi:[0,1,1] clamp\n\t" \
  "s_waitcnt lgkmcnt(7)\n\t" \
  "v_mfma_f32_16x16x32_bf16 v[196:199], v[212:215], v[120:123], v[196:199]\n\t" \
  "ds_read_b64_tr_b16 v[210:211], v249 offset:7688\n\t" \
  "ds_read_b32 v220, v220 offset:10016\n\t" \
  "v_lshlrev_b32_sdwa v225, 1, v187 dst_sel:DWORD dst_unused:UNUSED_PAD src0_sel:DWORD src1_sel:WORD_1\n\t" \
  "v_lshlrev_b32_sdwa v224, 1, v186 dst_sel:DWORD dst_unused:UNUSED_PAD src0_sel:DWORD src1_sel:WORD_1\n\t" \
  "v_mfma_f32_16x16x32_bf16 v[180:183], v[212:215], v[112:115], v[180:183]\n\t" \
  "ds_read_b32 v223, v223 offset:10016\n\t" \
  "v_lshlrev_b32_sdwa v227, 1, v185 dst_sel:DWORD dst_unused:UNUSED_PAD src0_sel:DWORD src1_sel:WORD_1\n\t" \
  "v_lshlrev_b32_sdwa v226, 1, v184 dst_sel:DWORD dst_unused:UNUSED_PAD src0_sel:DWORD src1_sel:WORD_1\n\t" \
  "v_mfma_f32_16x16x32_bf16 v[188:191], v[212:215], v[116:119], v[188:191]\n\t" \
  "ds_read_b64_tr_b16 v[208:209], v249 offset:6152\n\t" \
  "v_fma_mix_f32 v228, |v164|, v228, v228 op_sel:[0,1,0] op_sel_hi:[0,1,1] clamp\n\t" \
  "v_fma_mix_f32 v231, |v167|, v231, v231 op_sel:[0,1,0] op_sel_hi:[0,1,1] clamp\n\t" \
  "v_mfma_f32_16x16x32_bf16 v[204:207], v[212:215], v[124:127], v[204:207]\n\t" \
  "ds_read_b64_tr_b16 v[212:213], v249 offset:6160\n\t" \
  "ds_read_u16_d16_hi v225, v225 offset:10528\n\t" \
  "v_fma_mix_f32 v230, |v166|, v230, v230 op_sel:[0,1,0] op_sel_hi:[0,1,1] clamp\n\t" \
  "s_waitcnt lgkmcnt(8)\n\t" \
  "v_or_b32 v172, v232, v233\n\t" \
  "v_mfma_f32_16x16x32_bf16 v[180:183], v[216:219], v[128:131], v[180:183]\n\t" \
  "ds_read_b64_tr_b16 v[214:215], v249 offset:7696\n\t" \
  "v_or_b32 v173, v234, v235\n\t" \
  "v_mfma_f32_16x16x32_bf16 v[188:191], v[216:219], v[132:135], v[188:191]\n\t" \
  "ds_read_u16_d16_hi v227, v227 offset:10528\n\t" \
  "v_fma_f32 v167, |v167|, v231, v167 mul:4\n\t" \
  "v_mfma_f32_16x16x32_bf16 v[204:207], v[216:219], v[140:143], v[204:207]\n\t" \
  "ds_read_u16 v224, v224 offset:10528\n\t" \
  "v_fma_f32 v165, |v165|, v229, v165 mul:4\n\t" \
  "v_fma_f32 v164, |v164|, v228, v164 mul:4\n\t" \
  "v_mfma_f32_16x16x32_bf16 v[196:199], v[216:219], v[136:139], v[196:199]\n\t" \
  "ds_read_b64_tr_b16 v[216:217], v249 offset:6216\n\t" \
  "v_cvt_pknorm_i16_f32 v228, |v194|, |v195|\n\t" \
  "v_lshlrev_b32_sdwa v229, 2, v228 dst_sel:DWORD dst_unused:UNUSED_PAD src0_sel:DWORD src1_sel:BYTE_3\n\t" \
  "ds_read_b32 v229, v229 offset:10016\n\t" \
  "v_cvt_pk_bf16_f32 v164, v164, v165\n\t" \
  "v_fma_f32 v166, |v166|, v230, v166 mul:4\n\t" \
  "v_cvt_pk_bf16_f32 v165, v166, v167\n\t" \
  "s_waitcnt lgkmcnt(7)\n\t" \
  "v_mfma_f32_16x16x32_bf16 v[0:3], v[208:211], v[146:149], v[0:3]\n\t" \
  "ds_read_b64_tr_b16 v[218:219], v249 offset:7752\n\t" \
  "v_lshlrev_b32_sdwa v233, 1, v203 dst_sel:DWORD dst_unused:UNUSED_PAD src0_sel:DWORD src1_sel:WORD_1\n\t" \
  "v_cvt_pknorm_i16_f32 v230, |v192|, |v193|\n\t" \
  "v_mfma_f32_16x16x32_bf16 v[24:27], v[208:211], v[154:157], v[24:27]\n\t" \
  "ds_read_u16 v226, v226 offset:10528\n\t" \
  "v_lshlrev_b32_sdwa v231, 2, v230 dst_sel:DWORD dst_unused:UNUSED_PAD src0_sel:DWORD src1_sel:BYTE_3\n\t" \
  "v_lshlrev_b32_sdwa v228, 2, v228 dst_sel:DWORD dst_unused:UNUSED_PAD src0_sel:DWORD src1_sel:BYTE_1\n\t" \
  "v_lshlrev_b32_sdwa v232, 1, v202 dst_sel:DWORD dst_unused:UNUSED_PAD src0_sel:DWORD src1_sel:WORD_1\n\t" \
  "v_mfma_f32_16x16x32_bf16 v[48:51], v[208:211], v[162:165], v[48:51]\n\t" \
  "v_lshlrev_b32_sdwa v230, 2, v230 dst_sel:DWORD dst_unused:UNUSED_PAD src0_sel:DWORD src1_sel:BYTE_1\n\t" \
  "v_lshlrev_b32_sdwa v235, 1, v201 dst_sel:DWORD dst_unused:UNUSED_PAD src0_sel:DWORD src1_sel:WORD_1\n\t" \
  "v_mfma_f32_16x16x32_bf16 v[72:75], v[208:211], v[170:173], v[72:75]\n\t" \
  "ds_read_b64_tr_b16 v[208:209], v249 offset:6224\n\t" \
  "ds_read_b32 v228, v228 offset:10016\n\t" \
  "v_fma_mix_f32 v221, |v179|, v221, v221 op_sel:[0,1,0] op_sel_hi:[0,1,1] clamp\n\t" \
  "s_waitcnt lgkmcnt(6)\n\t" \
  "v_or_b32 v187, v224, v225\n\t" \
  "v_fma_mix_f32 v220, |v178|, v220, v220 op_sel:[0,1,0] op_sel_hi:[0,1,1] clamp\n\t" \
  "v_lshlrev_b32_sdwa v234, 1, v200 dst_sel:DWORD dst_unused:UNUSED_PAD src0_sel:DWORD src1_sel:WORD_1\n\t" \
  "v_mfma_f32_16x16x32_bf16 v[4:7], v[212:215], v[146:149], v[4:7]\n\t" \
  "ds_read_b64_tr_b16 v[210:211], v249 offset:7760\n\t" \
  "ds_read_b32 v231, v231 offset:10016\n\t" \
  "v_fma_mix_f32 v223, |v177|, v223, v223 op_sel:[0,1,0] op_sel_hi:[0,1,1] clamp\n\t" \
  "v_fma_f32 v179, |v179|, v221, v179 mul:4\n\t" \
  "s_waitcnt lgkmcnt(4)\n\t" \
  "v_fma_mix_f32 v222, |v176|, v222, v222 op_sel:[0,1,0] op_sel_hi:[0,1,1] clamp\n\t" \
  "v_mfma_f32_16x16x32_bf16 v[28:31], v[212:215], v[154:157], v[28:31]\n\t" \
  "ds_read_b32 v230, v230 offset:10016\n\t" \
  "v_or_b32 v186, v226, v227\n\t" \
  "v_fma_f32 v178, |v178|, v220, v178 mul:4\n\t" \
  "v_mfma_f32_16x16x32_bf16 v[52:55], v[212:215], v[162:165], v[52:55]\n\t" \
  "ds_read_u16_d16_hi v233, v233 offset:10528\n\t" \
  "v_fma_f32 v177, |v177|, v223, v177 mul:4\n\t" \
  "v_fma_f32 v176, |v176|, v222, v176 mul:4\n\t" \
  "v_cvt_pk_bf16_f32 v179, v178, v179\n\t" \
  "v_cvt_pk_bf16_f32 v178, v176, v177\n\t" \
  "v_cvt_pknorm_i16_f32 v220, |v180|, |v181|\n\t" \
  "v_cvt_pknorm_i16_f32 v222, |v182|, |v183|\n\t" \
  "v_mfma_f32_16x16x32_bf16 v[76:79], v[212:215], v[170:173], v[76:79]\n\t" \
  "ds_read_b64_tr_b16 v[212:213], v249 offset:6280\n\t" \
  "v_lshlrev_b32_sdwa v221, 2, v220 dst_sel:DWORD dst_unused:UNUSED_PAD src0_sel:DWORD src1_sel:BYTE_3\n\t" \
  "v_mfma_f32_16x16x32_bf16 v[8:11], v[216:219], v[146:149], v[8:11]\n\t" \
  "ds_read_b64_tr_b16 v[214:215], v249 offset:7816\n\t" \
  "v_lshlrev_b32_sdwa v220, 2, v220 dst_sel:DWORD dst_unused:UNUSED_PAD src0_sel:DWORD src1_sel:BYTE_1\n\t" \
  "v_lshlrev_b32_sdwa v223, 2, v222 dst_sel:DWORD dst_unused:UNUSED_PAD src0_sel:DWORD src1_sel:BYTE_3\n\t" \
  "v_lshlrev_b32_sdwa v224, 1, v188 dst_sel:DWORD dst_unused:UNUSED_PAD src0_sel:DWORD src1_sel:WORD_1\n\t" \
  "v_mfma_f32_16x16x32_bf16 v[32:35], v[216:219], v[154:157], v[32:35]\n\t" \
  "ds_read_u16 v232, v232 offset:10528\n\t" \
  "v_lshlrev_b32_sdwa v222, 2, v222 dst_sel:DWORD dst_unused:UNUSED_PAD src0_sel:DWORD src1_sel:BYTE_1\n\t" \
  "v_lshlrev_b32_sdwa v225, 1, v189 dst_sel:DWORD dst_unused:UNUSED_PAD src0_sel:DWORD src1_sel:WORD_1\n\t" \
  "v_mfma_f32_16x16x32_bf16 v[56:59], v[216:219], v[162:165], v[56:59]\n\t" \
  "ds_read_u16_d16_hi v235, v235 offset:10528\n\t" \
  "ds_read_u16 v234, v234 offset:10528\n\t" \
  "v_lshlrev_b32_sdwa v227, 1, v191 dst_sel:DWORD dst_unused:UNUSED_PAD src0_sel:DWORD src1_sel:WORD_1\n\t" \
  "v_lshlrev_b32_sdwa v226, 1, v190 dst_sel:DWORD dst_unused:UNUSED_PAD src0_sel:DWORD src1_sel:WORD_1\n\t" \
  "v_fma_mix_f32 v229, |v195|, v229, v229 op_sel:[0,1,0] op_sel_hi:[0,1,1] clamp\n\t" \
  "v_mfma_f32_16x16x32_bf16 v[80:83], v[216:219], v[170:173], v[80:83]\n\t" \
  "ds_read_b32 v221, v221 offset:10016\n\t" \
  "s_waitcnt lgkmcnt(7)\n\t" \
  "ds_read_b64_tr_b16 v[216:217], v249 offset:6288\n\t" \
  "v_fma_mix_f32 v228, |v194|, v228, v228 op_sel:[0,1,0] op_sel_hi:[0,1,1] clamp\n\t" \
  "v_fma_mix_f32 v231, |v193|, v231, v231 op_sel:[0,1,0] op_sel_hi:[0,1,1] clamp\n\t" \
  "v_mfma_f32_16x16x32_bf16 v[12:15], v[208:211], v[146:149], v[12:15]\n\t" \
  "ds_read_b64_tr_b16 v[218:219], v249 offset:7824\n\t" \
  "ds_read_b32 v220, v220 offset:10016\n\t" \
  "v_fma_mix_f32 v230, |v192|, v230, v230 op_sel:[0,1,0] op_sel_hi:[0,1,1] clamp\n\t" \
  "v_fma_f32 v192, |v192|, v230, v192 mul:4\n\t" \
  "s_waitcnt lgkmcnt(6)\n\t" \
  "v_or_b32 v203, v232, v233\n\t" \
  "v_fma_f32 v194, |v194|, v228, v194 mul:4\n\t" \
  "v_fma_f32 v195, |v195|, v229, v195 mul:4\n\t" \
  "v_cvt_pk_bf16_f32 v195, v194, v195\n\t" \
  "v_mfma_f32_16x16x32_bf16 v[36:39], v[208:211], v[154:157], v[36:39]\n\t" \
  "ds_read_b32 v223, v223 offset:10016\n\t" \
  "ds_read_b32 v222, v222 offset:10016\n\t" \
  "v_mfma_f32_16x16x32_bf16 v[60:63], v[208:211], v[162:165], v[60:63]\n\t" \
  "ds_read_u16_d16_hi v225, v225 offset:10528\n\t" \
  "v_fma_f32 v193, |v193|, v231, v193 mul:4\n\t" \
  "v_cvt_pk_bf16_f32 v194, v192, v193\n\t" \
  "v_cvt_pknorm_i16_f32 v228, |v196|, |v197|\n\t" \
  "v_lshlrev_b32_sdwa v229, 2, v228 dst_sel:DWORD dst_unused:UNUSED_PAD src0_sel:DWORD src1_sel:BYTE_3\n\t" \
  "v_mfma_f32_16x16x32_bf16 v[84:87], v[208:211], v[170:173], v[84:87]\n\t" \
  "ds_read_u16 v224, v224 offset:10528\n\t" \
  "v_cvt_pknorm_i16_f32 v230, |v198|, |v199|\n\t" \
  "v_mfma_f32_16x16x32_bf16 v[16:19], v[212:215], v[146:149], v[16:19]\n\t" \
  "ds_read_u16_d16_hi v227, v227 offset:10528\n\t" \
  "v_lshlrev_b32_sdwa v232, 1, v204 dst_sel:DWORD dst_unused:UNUSED_PAD src0_sel:DWORD src1_sel:WORD_1\n\t" \
  "s_waitcnt lgkmcnt(9)\n\t" \
  "v_lshlrev_b32_sdwa v228, 2, v228 dst_sel:DWORD dst_unused:UNUSED_PAD src0_sel:DWORD src1_sel:BYTE_1\n\t" \
  "v_lshlrev_b32_sdwa v231, 2, v230 dst_sel:DWORD dst_unused:UNUSED_PAD src0_sel:DWORD src1_sel:BYTE_3\n\t" \
  "v_lshlrev_b32_sdwa v230, 2, v230 dst_sel:DWORD dst_unused:UNUSED_PAD src0_sel:DWORD src1_sel:BYTE_1\n\t" \
  "v_lshlrev_b32_sdwa v233, 1, v205 dst_sel:DWORD dst_unused:UNUSED_PAD src0_sel:DWORD src1_sel:WORD_1\n\t" \
  "v_or_b32 v202, v234, v235\n\t" \
  "v_lshlrev_b32_sdwa v235, 1, v207 dst_sel:DWORD dst_unused:UNUSED_PAD src0_sel:DWORD src1_sel:WORD_1\n\t" \
  "v_lshlrev_b32_sdwa v234, 1, v206 dst_sel:DWORD dst_unused:UNUSED_PAD src0_sel:DWORD src1_sel:WORD_1\n\t" \
  "v_mfma_f32_16x16x32_bf16 v[40:43], v[212:215], v[154:157], v[40:43]\n\t" \
  "s_waitcnt lgkmcnt(3)\n\t" \
  "ds_read_b32 v228, v228 offset:10016\n\t" \
  "ds_read_b64_tr_b16 v[208:209], v249 offset:12304\n\t" \
  "ds_read_u16 v226, v226 offset:10528\n\t" \
  "v_fma_mix_f32 v220, |v180|, v220, v220 op_sel:[0,1,0] op_sel_hi:[0,1,1] clamp\n\t" \
  "v_fma_mix_f32 v221, |v181|, v221, v221 op_sel:[0,1,0] op_sel_hi:[0,1,1] clamp\n\t" \
  "v_fma_mix_f32 v223, |v183|, v223, v223 op_sel:[0,1,0] op_sel_hi:[0,1,1] clamp\n\t" \
  "v_mfma_f32_16x16x32_bf16 v[64:67], v[212:215], v[162:165], v[64:67]\n\t" \
  "s_waitcnt lgkmcnt(4)\n\t" \
  "ds_read_b64_tr_b16 v[210:211], v249 offset:13840\n\t" \
  "ds_read_b32 v229, v229 offset:10016\n\t" \
  "v_fma_mix_f32 v222, |v182|, v222, v222 op_sel:[0,1,0] op_sel_hi:[0,1,1] clamp\n\t" \
  "v_or_b32 v188, v224, v225\n\t" \
  "ds_read_b32 v230, v230 offset:10016\n\t" \
  "v_fma_f32 v182, |v182|, v222, v182 mul:4\n\t" \
  "v_fma_f32 v183, |v183|, v223, v183 mul:4\n\t" \
  "v_fma_f32 v180, |v180|, v220, v180 mul:4\n\t" \
  "v_fma_f32 v181, |v181|, v221, v181 mul:4\n\t" \
  "v_mfma_f32_16x16x32_bf16 v[20:23], v[216:219], v[146:149], v[20:23]\n\t" \
  "ds_read_b32 v231, v231 offset:10016\n\t" \
  "v_cvt_pk_bf16_f32 v180, v180, v181\n\t" \
  "v_cvt_pk_bf16_f32 v181, v182, v183\n\t" \
  "s_waitcnt lgkmcnt(2)\n\t" \
  "v_fma_mix_f32 v229, |v197|, v229, v229 op_sel:[0,1,0] op_sel_hi:[0,1,1] clamp\n\t" \
  "v_mfma_f32_16x16x32_bf16 v[88:91], v[212:215], v[170:173], v[88:91]\n\t" \
  "ds_read_b64_tr_b16 v[214:215], v249 offset:13848\n\t" \
  "ds_read_b64_tr_b16 v[212:213], v249 offset:12312\n\t" \
  "v_fma_mix_f32 v228, |v196|, v228, v228 op_sel:[0,1,0] op_sel_hi:[0,1,1] clamp\n\t" \
  "v_mfma_f32_16x16x32_bf16 v[44:47], v[216:219], v[154:157], v[44:47]\n\t" \
  "ds_read_u16 v234, v234 offset:10528\n\t" \
  "s_waitcnt lgkmcnt(4)\n\t" \
  "ds_read_u16_d16_hi v233, v233 offset:10528\n\t" \
  "s_waitcnt lgkmcnt(4)\n\t" \
  "v_fma_mix_f32 v231, |v199|, v231, v231 op_sel:[0,1,0] op_sel_hi:[0,1,1] clamp\n\t" \
  "v_or_b32 v189, v226, v227\n\t" \
  "ds_read_u16_d16_hi v235, v235 offset:10528\n\t" \
  "v_fma_f32 v196, |v196|, v228, v196 mul:4\n\t" \
  "ds_read_u16 v232, v232 offset:10528\n\t" \
  "v_mfma_f32_16x16x32_bf16 v[68:71], v[216:219], v[162:165], v[68:71]\n\t" \
  "s_waitcnt lgkmcnt(4)\n\t" \
  "v_mfma_f32_16x16x32_bf16 v[4:7], v[212:215], v[178:181], v[4:7]\n\t" \
  "v_fma_mix_f32 v230, |v198|, v230, v230 op_sel:[0,1,0] op_sel_hi:[0,1,1] clamp\n\t" \
  "s_waitcnt lgkmcnt(1)\n\t" \
  "v_mfma_f32_16x16x32_bf16 v[92:95], v[216:219], v[170:173], v[92:95]\n\t" \
  "ds_read_b64_tr_b16 v[216:217], v249 offset:12368\n\t" \
  "v_or_b32 v205, v234, v235\n\t" \
  "s_waitcnt lgkmcnt(1)\n\t" \
  "v_or_b32 v204, v232, v233\n\t" \
  "v_fma_f32 v198, |v198|, v230, v198 mul:4\n\t" \
  "v_mfma_f32_16x16x32_bf16 v[0:3], v[208:211], v[178:181], v[0:3]\n\t" \
  "ds_read_b64_tr_b16 v[218:219], v249 offset:13904\n\t" \
  "v_fma_f32 v197, |v197|, v229, v197 mul:4\n\t" \
  "v_fma_f32 v199, |v199|, v231, v199 mul:4\n\t" \
  "v_mfma_f32_16x16x32_bf16 v[28:31], v[212:215], v[186:189], v[28:31]\n\t" \
  "v_mfma_f32_16x16x32_bf16 v[24:27], v[208:211], v[186:189], v[24:27]\n\t" \
  "v_cvt_pk_bf16_f32 v196, v196, v197\n\t" \
  "v_mfma_f32_16x16x32_bf16 v[76:79], v[212:215], v[202:205], v[76:79]\n\t" \
  "v_cvt_pk_bf16_f32 v197, v198, v199\n\t" \
  "s_nop 1\n\t" \
  "v_mfma_f32_16x16x32_bf16 v[52:55], v[212:215], v[194:197], v[52:55]\n\t" \
  "ds_read_b64_tr_b16 v[214:215], v249 offset:13968\n\t" \
  "ds_read_b64_tr_b16 v[212:213], v249 offset:12432\n\t" \
  "v_mfma_f32_16x16x32_bf16 v[48:51], v[208:211], v[194:197], v[48:51]\n\t" \
  "v_cvt_pk_bf16_f32 v0, v0, v1\n\t" \
  "s_waitcnt lgkmcnt(2)\n\t" \
  "v_mfma_f32_16x16x32_bf16 v[8:11], v[216:219], v[178:181], v[8:11]\n\t" \
  "v_cvt_pk_bf16_f32 v1, v2, v3\n\t" \
  "v_cvt_pk_bf16_f32 v2, v4, v5\n\t" \
  "v_cvt_pk_bf16_f32 v3, v6, v7\n\t" \
  "v_mfma_f32_16x16x32_bf16 v[72:75], v[208:211], v[202:205], v[72:75]\n\t" \
  "ds_read_b64_tr_b16 v[208:209], v249 offset:12376\n\t" \
  "v_mfma_f32_16x16x32_bf16 v[56:59], v[216:219], v[194:197], v[56:59]\n\t" \
  "ds_read_b64_tr_b16 v[210:211], v249 offset:13912\n\t" \
  "buffer_store_dwordx4 v[0:3], v250, s[20:23], s63 offen offset:0 sc1\n\t" \
  "v_mfma_f32_16x16x32_bf16 v[80:83], v[216:219], v[202:205], v[80:83]\n\t" \
  "v_cvt_pk_bf16_f32 v24, v24, v25\n\t" \
  "v_cvt_pk_bf16_f32 v25, v26, v27\n\t" \
  "v_cvt_pk_bf16_f32 v27, v30, v31\n\t" \
  "v_mfma_f32_16x16x32_bf16 v[32:35], v[216:219], v[186:189], v[32:35]\n\t" \
  "ds_read_b64_tr_b16 v[216:217], v249 offset:12440\n\t" \
  "s_waitcnt lgkmcnt(3)\n\t" \
  "v_cvt_pk_bf16_f32 v26, v28, v29\n\t" \
  "v_mfma_f32_16x16x32_bf16 v[16:19], v[212:215], v[178:181], v[16:19]\n\t" \
  "buffer_store_dwordx4 v[24:27], v250, s[20:23], s62 offen offset:0 sc1\n\t" \
  "v_cvt_pk_bf16_f32 v48, v48, v49\n\t" \
  "v_mfma_f32_16x16x32_bf16 v[40:43], v[212:215], v[186:189], v[40:43]\n\t" \
  "s_waitcnt lgkmcnt(1)\n\t" \
  "v_cvt_pk_bf16_f32 v49, v50, v51\n\t" \
  "v_cvt_pk_bf16_f32 v51, v54, v55\n\t" \
  "v_cvt_pk_bf16_f32 v50, v52, v53\n\t" \
  "v_mfma_f32_16x16x32_bf16 v[64:67], v[212:215], v[194:197], v[64:67]\n\t" \
  "v_mfma_f32_16x16x32_bf16 v[84:87], v[208:211], v[202:205], v[84:87]\n\t" \
  "v_cvt_pk_bf16_f32 v72, v72, v73\n\t" \
  "v_cvt_pk_bf16_f32 v73, v74, v75\n\t" \
  "v_mfma_f32_16x16x32_bf16 v[36:39], v[208:211], v[186:189], v[36:39]\n\t" \
  "v_cvt_pk_bf16_f32 v75, v78, v79\n\t" \
  "buffer_store_dwordx4 v[48:51], v250, s[20:23], s14 offen offset:0 sc1\n\t" \
  "s_nop 0\n\t" \
  "v_cvt_pk_bf16_f32 v8, v8, v9\n\t" \
  "v_mfma_f32_16x16x32_bf16 v[12:15], v[208:211], v[178:181], v[12:15]\n\t" \
  "ds_read_b64_tr_b16 v[218:219], v249 offset:13976\n\t" \
  "v_mfma_f32_16x16x32_bf16 v[88:91], v[212:215], v[202:205], v[88:91]\n\t" \
  "v_cvt_pk_bf16_f32 v74, v76, v77\n\t" \
  "v_cvt_pk_bf16_f32 v56, v56, v57\n\t" \
  "v_cvt_pk_bf16_f32 v9, v10, v11\n\t" \
  "v_mfma_f32_16x16x32_bf16 v[60:63], v[208:211], v[194:197], v[60:63]\n\t" \
  "s_nop 1\n\t" \
  "v_cvt_pk_bf16_f32 v11, v14, v15\n\t" \
  "s_waitcnt lgkmcnt(0)\n\t" \
  "v_cvt_pk_bf16_f32 v57, v58, v59\n\t" \
  "s_nop 2\n\t" \
  "v_cvt_pk_bf16_f32 v58, v60, v61\n\t" \
  "v_cvt_pk_bf16_f32 v80, v80, v81\n\t" \
  "v_mfma_f32_16x16x32_bf16 v[20:23], v[216:219], v[178:181], v[20:23]\n\t" \
  "v_cvt_pk_bf16_f32 v32, v32, v33\n\t" \
  "v_cvt_pk_bf16_f32 v59, v62, v63\n\t" \
  "v_cvt_pk_bf16_f32 v16, v16, v17\n\t" \
  "v_cvt_pk_bf16_f32 v17, v18, v19\n\t" \
  "buffer_store_dwordx4 v[72:75], v250, s[20:23], s15 offen offset:0 sc1\n\t" \
  "s_nop 2\n\t" \
  "v_cvt_pk_bf16_f32 v18, v20, v21\n\t" \
  "v_mfma_f32_16x16x32_bf16 v[68:71], v[216:219], v[194:197], v[68:71]\n\t" \
  "v_cvt_pk_bf16_f32 v40, v40, v41\n\t" \
  "v_cvt_pk_bf16_f32 v64, v64, v65\n\t" \
  "v_cvt_pk_bf16_f32 v33, v34, v35\n\t" \
  "v_cvt_pk_bf16_f32 v35, v38, v39\n\t" \
  "v_cvt_pk_bf16_f32 v34, v36, v37\n\t" \
  "v_cvt_pk_bf16_f32 v65, v66, v67\n\t" \
  "s_nop 1\n\t" \
  "v_cvt_pk_bf16_f32 v67, v70, v71\n\t" \
  "v_mfma_f32_16x16x32_bf16 v[44:47], v[216:219], v[186:189], v[44:47]\n\t" \
  "v_cvt_pk_bf16_f32 v41, v42, v43\n\t" \
  "v_mfma_f32_16x16x32_bf16 v[92:95], v[216:219], v[202:205], v[92:95]\n\t" \
  "v_cvt_pk_bf16_f32 v10, v12, v13\n\t" \
  "v_cvt_pk_bf16_f32 v88, v88, v89\n\t" \
  "v_cvt_pk_bf16_f32 v19, v22, v23\n\t" \
  "v_cvt_pk_bf16_f32 v81, v82, v83\n\t" \
  "v_cvt_pk_bf16_f32 v66, v68, v69\n\t" \
  "v_cvt_pk_bf16_f32 v89, v90, v91\n\t" \
  "s_nop 1\n\t" \
  "v_cvt_pk_bf16_f32 v90, v92, v93\n\t" \
  "v_cvt_pk_bf16_f32 v91, v94, v95\n\t" \
  "v_cvt_pk_bf16_f32 v42, v44, v45\n\t" \
  "v_cvt_pk_bf16_f32 v43, v46, v47\n\t" \
  "v_cvt_pk_bf16_f32 v82, v84, v85\n\t" \
  "v_cvt_pk_bf16_f32 v83, v86, v87\n\t" \
  "buffer_store_dwordx4 v[8:11], v250, s[20:23], s63 offen offset:64 sc1\n\t" \
  "buffer_store_dwordx4 v[32:35], v250, s[20:23], s62 offen offset:64 sc1\n\t" \
  "buffer_store_dwordx4 v[56:59], v250, s[20:23], s14 offen offset:64 sc1\n\t" \
  "buffer_store_dwordx4 v[80:83], v250, s[20:23], s15 offen offset:64 sc1\n\t" \
  "buffer_store_dwordx4 v[16:19], v250, s[20:23], s63 offen offset:128 sc1\n\t" \
  "buffer_store_dwordx4 v[40:43], v250, s[20:23], s62 offen offset:128 sc1\n\t" \
  "buffer_store_dwordx4 v[64:67], v250, s[20:23], s14 offen offset:128 sc1\n\t" \
  "buffer_store_dwordx4 v[88:91], v250, s[20:23], s15 offen offset:128 sc1\n\t" \
  "s_waitcnt vmcnt(0)\n\t" \
  ".Lcnx_end:\n\t" \
  ""


__device__ __forceinline__ unsigned lds_addr(const char* p) { return (unsigned)(uintptr_t)(lds_ptr_t)p; }
__device__ __forceinline__ unsigned sg(unsigned v) { return __builtin_amdgcn_readfirstlane(v); }

constexpr double tanh_u(double y) {
    const double v = 8 * y, a = 2 * 0.79788456080286536 * (v + 0.044715 * v * v * v) / 1024;
    double m = 1;
    for (int i = 12; i > 1; --i) m = 1 + m * a / i;
    m *= a;
    for (int i = 0; i < 10; ++i) m *= m + 2;
    return m / (m + 2);
}
constexpr double h2d(int h) {
    if (h < 1024) return 0;
    double x = (1024 + (h & 1023)) * 0x1p-24;
    for (int e = 1; e < (h >> 10); ++e) x *= 2;
    return x;
}
constexpr int d2h(double x) {
    int h = 0;
    for (int s = 1 << 14; s; s >>= 1) if (h2d(h + s) <= x) h += s;
    return h;
}
constexpr unsigned ip_entry(int k) {
    if (k == 127) return 0x3c003c00u;
    const double y0 = k ? (256 * k - 0.5) / 32767 : 0, dy = ((256 * k + 255.5) / 32767 - y0) / 8;
    double t[9] = {};
    for (int i = 0; i < 9; ++i) t[i] = tanh_u(y0 + i * dy);
    const double b0 = (t[8] - t[0]) / (8 * dy), ym = y0 + 4 * dy, tm = (t[4] + (t[0] + t[8]) / 2) / 2;
    unsigned best = 0;
    double err = 1;
    for (int ha = d2h(tm - b0 * ym) - 1, i = 0; i < 4; ++i, ++ha)
        for (int hb = d2h((tm - h2d(ha)) / ym), j = 0; j < 2; ++j, ++hb) {
            double e = 0;
            for (int n = 0; n < 9; ++n) {
                const double s = h2d(ha) + h2d(hb) * (y0 + n * dy), d = (s < 1 ? s : 1) - t[n];
                e = d > e ? d : -d > e ? -d : e;
            }
            if (e < err) err = e, best = (ha < 1024 ? 0 : ha) | (hb < 1024 ? 0 : hb) << 16;
        }
    return best;
}
constexpr unsigned g_entry(int e, bool neg) {
    double y = e ? ((e & 127) + 0.5 + (e >> 7 ? 128 : 0)) * 0x1p-23 / (1 + 1.0 / 384) : 0;
    for (int i = 1; i < (e >> 7); ++i) y *= 2;
    const double t = y < 1 ? tanh_u(y) : 1;
    const unsigned u = __builtin_bit_cast(unsigned, (float)(4 * y * (neg ? t - 1 : t + 1)));
    return (u + 0x7fff + (u >> 16 & 1)) >> 16;
}
struct TanhTab { unsigned v[1024]; };
constexpr TanhTab gelu_tab(int k0) {
    TanhTab t = {};
    for (int k = k0; k < k0 + 1024 && k < 128 + GSZ / 2; ++k) {
        const int e = 2 * (k < 2048 ? k - 128 : k - 2048);
        t.v[k - k0] = k < 128 ? ip_entry(k) : g_entry(e, k >= 2048) | g_entry(e + 1, k >= 2048) << 16;
    }
    return t;
}
struct WaveTab { unsigned v192[64][4], v244[64][4], v16[64][4], s32[32]; };
struct RegTab { unsigned v240[64][4], v248[64][4]; WaveTab w[8]; };
constexpr unsigned cpos(unsigned w, unsigned px) { return (w >> 1) * 1024 + (px ^ (w & 2) << 2) * 16 + ((w ^ px >> 4) & 1) * 8; }
constexpr RegTab reg_tab() {
    RegTab t = {};
    for (unsigned l = 0; l < 64; ++l) {
        const unsigned G = l >> 4, n = l & 15, q = n >> 2, p = l & 3;
        t.v240[l][2 + (p >> 1)] = p & 1 ? 0x3f800000u : 0x3f80u;
        t.v248[l][0] = WB + (l >> 2) * 32 + (p & 1) * 16 + (p >> 1) * 8;
        t.v248[l][1] = WB + (G & 1) * 3080 + ((G >> 1) * 4 + q) * 192 + p * 16;
        t.v248[l][2] = (n & 7) * 384 + (n >> 3) * 192 + G * 16;
        t.v248[l][3] = PB + l * 8;
        for (unsigned w = 0; w < 8; ++w) {
            t.w[w].v244[l][2] = XOFF + n * 400 + (G & 1) * XROW + (G >> 1) * 192 + w * 24;
            t.w[w].v244[l][3] = dw_pos(3 * w) + p * 192 + (1 - (q >> 1)) * 24 + (1 + (G & 1) - (q & 1)) * 8 + ((G ^ p) >> 1) * 4;
            const unsigned pl = PTOP + (w >> 2) * 12288, blk = (w & 3) * 16;
            t.w[w].v192[l][0] = XOFF + cpos(w, (G & 2) * 16 + (n & 8) * 2 + (G & 1) * 8 + (n & 7));
            t.w[w].v192[l][1] = t.w[w].v192[l][0] + PTOP - XOFF;
            t.w[w].v192[l][2] = pl + (cpos((G & 1) * 4 + (G & 2), blk + n) & ~8u);
            t.w[w].v192[l][3] = pl + cpos(p, blk + (l >> 2));
            const unsigned u = (w & 3) * 48 + l, j = u & 63;
            t.w[w].v16[l][0] = w < 4 ? (4 * (u >> 6) + 12 * ((j >> 3) & 3) + ((j >> 1) & 3)) * (HID * 2) + (2 * (j & 1) + (j >> 5)) * 16 : u * 16;
            const unsigned d = dw_pos(w * 8 + (l >> 5) * 4 + p) + ((l >> 2) & 7) * 24;
            t.w[w].v16[l][1] = w < 3 ? d + (l & 1) * 192 : (l & 4 ? DW2 + 11 * 768 : 13 * 768) + p * 8;
            t.w[w].v16[l][2] = d + (~l & 1) * 192;
            t.w[w].v16[l][3] = l & 1 ? 0x2b800000u : 0x2b80u;
        }
    }
    for (unsigned w = 0; w < 8; ++w) {
        const bool w1 = w < 4;
        const unsigned dm = w1 ? 3072 : 3080, wl = WB + (w & 3) * 768, wc = w1 ? 64 : 6144;
        const unsigned rp[4] = {x_pos(3 + (w >> 1 ^ 2)), x_pos(7 + (w >> 1)), x_pos((11 + (w >> 1)) % 12), x_pos(3 + (w >> 1))};
        const unsigned s[32] = {wc, 0, w1 ? 48u * HID * 2 : 3072, dm, dm + XOFF - WB, w >> 2, w, w * 512,
                                0xaaaaaaaau, 0xaaaaaaaau, 0xccccccccu, 0xccccccccu,
                                wl + (w1 ? WA + 12304 : WA), wl + (w1 ? 0 : WA + 6152), wl + (w1 ? WA + 18448 : 6152),
                                wl + (w1 ? WA + 18448 + 6144 : 12304), w1 ? -11 * wc : wc, w1 ? wc : -11 * wc,
                                0u + XOFF - WB, 4 * wc, ~0x00f0000fu, ~0xf0000f00u,
                                (w >> 2 ? PTOP - 12288u : XHI + 6 * XROW) - XOFF - 4 * 12288, 0x07060302, dw_pos(3 * w + 2) - dw_pos(3 * w), 0,
                                rp[1] - rp[0], rp[2] - rp[1], rp[3] - rp[2], rp[0] + 592 + (w & 1) * 3200, 0, 8 * (unsigned)sizeof(WaveTab) - 2 * PB};
        for (int i = 0; i < 32; ++i) t.w[w].s32[i] = s[i];
    }
    return t;
}
struct Tables { RegTab r; TanhTab t[4]; };
static_assert(sizeof(WaveTab) == 64 * 50 && sizeof(RegTab) == 27648, "table layout");
__device__ const __attribute__((aligned(128))) Tables TABLES = {reg_tab(), {gelu_tab(0), gelu_tab(1024), gelu_tab(2048), gelu_tab(3072)}};

#define R10(r, a) r #a "0", r #a "1", r #a "2", r #a "3", r #a "4", r #a "5", r #a "6", r #a "7", r #a "8", r #a "9"
#define V10(a) R10("v", a)
#define S10(a) R10("s", a)
#define MAIN_CLOB "memory", "scc", "vcc", "v1", "v2", "v3", "v4", "v5", "v6", "v7", "v8", "v9", V10(1), V10(2), V10(3), V10(4), V10(5),  \
    V10(6), V10(7), V10(8), V10(9), V10(10), V10(11), V10(12), V10(13), V10(14), V10(15), V10(16), V10(17), V10(18), V10(19),        \
    V10(20), V10(21), V10(22), V10(23), V10(24), "v250", "v251", "v252", "v253", "v254", "s4", "s5", "s8", "s9", S10(1),       \
    S10(2), S10(3), S10(4), S10(5), "s60", "s61", "s62", "s63", "s64", "s65", "s66", "s67", "s68", "s69"

__global__ __launch_bounds__(512, 2) void convnext_kernel(const __hip_bfloat16* __restrict__ x,
                                                          const __hip_bfloat16* __restrict__ dw,
                                                          const __hip_bfloat16* __restrict__ w1,
                                                          const __hip_bfloat16* __restrict__ w2,
                                                          __hip_bfloat16* __restrict__ out) {
    __shared__ __attribute__((aligned(16))) char smem[LDS_BYTES];

    unsigned tid = threadIdx.x, lds = lds_addr(smem);
    const unsigned bid = blockIdx.x;
    asm volatile(MAIN_ASM
                 : "+{v0}"(tid), "+{s3}"(lds)
                 : "{s[0:1]}"(__builtin_amdgcn_kernarg_segment_ptr()), "{s2}"(bid), "{s[6:7]}"(TABLES.r.w)
                 : MAIN_CLOB);

}

}

extern "C" void kernel_launch(void* const* d_in, const int* in_sizes, int n_in, void* d_out, int out_size,
                              void* d_ws, size_t ws_size, hipStream_t stream) {
    (void)in_sizes; (void)n_in; (void)out_size; (void)d_ws; (void)ws_size;
    const __hip_bfloat16* x = (const __hip_bfloat16*)d_in[0];
    const __hip_bfloat16* dw = (const __hip_bfloat16*)d_in[1];
    const __hip_bfloat16* w1 = (const __hip_bfloat16*)d_in[2];
    const __hip_bfloat16* w2 = (const __hip_bfloat16*)d_in[3];
    __hip_bfloat16* out = (__hip_bfloat16*)d_out;
    convnext_kernel<<<256, 512, 0, stream>>>(x, dw, w1, w2, out);
}
```

```cpp
#include <hip/hip_runtime.h>
#include <hip/hip_bf16.h>
#include <stdint.h>

namespace {

typedef __attribute__((address_space(3))) void* lds_ptr_t;

constexpr int IMG = 512;
constexpr int CH = 96;
constexpr int HID = 384;
constexpr int ROWB = IMG * CH * 2;
constexpr int IPOFF = 13 * 768 + 32;
constexpr int GOFF = IPOFF + 512;
constexpr int GSZ = 30 * 256;
constexpr int DW2 = GOFF + GSZ;
constexpr int XROW = 8200;
constexpr int XOFF = GOFF + 65536 - 6 * XROW;
constexpr int XHI = XOFF + GSZ;
constexpr int PB = XHI + 10 * XROW;
constexpr int WB = XHI + 9 * XROW;
constexpr int WA = 3 * XROW;
constexpr int PTOP = XHI + 12 * XROW - 12288;
constexpr int CNTOFF = WB + WA + 18448 + 12288;
constexpr int LDS_BYTES = 160 * 1024;
static_assert(XOFF == 26864 && CNTOFF - PB == 47136 && DW2 + 11 * 768 + 32 <= XOFF && CNTOFF + 144 <= LDS_BYTES, "LDS map");
constexpr unsigned dw_pos(unsigned u) { return u < 12 ? u * 768 : u == 14 ? 12 * 768 : DW2 + (u - 12 - (u > 14)) * 768; }
constexpr unsigned x_pos(unsigned r) { return (r < 6 ? XOFF : XHI) + r * XROW; }
constexpr unsigned OOB = 0x40000000u;

#define MAIN_ASM \
  "s_load_dwordx2 s[8:9], s[0:1], 0x0\n\t" \
  "s_load_dwordx2 s[12:13], s[0:1], 0x8\n\t" \
  "s_load_dwordx4 s[32:35], s[0:1], 0x10\n\t" \
  "s_setprio 3\n\t" \
  "v_readfirstlane_b32 s5, v0\n\t" \
  "v_mul_u32_u24 v254, 0x147b, v0\n\t" \
  "s_getpc_b64 s[6:7]\n\t" \
  ".Lcnx_pc:\n\t" \
  "s_add_u32 s6, s6, %c[tab]@rel32@lo+4\n\t" \
  "s_addc_u32 s7, s7, %c[tab]@rel32@hi+12\n\t" \
  "v_lshrrev_b32 v254, 17, v254\n\t" \
  "s_mul_i32 s4, s5, 34\n\t" \
  "s_bfe_u32 s27, s2, 0x40003\n\t" \
  "v_lshl_add_u32 v22, v0, 4, s4\n\t" \
  "s_mul_i32 s27, s27, 6144\n\t" \
  "global_load_dwordx4 v[244:247], v22, s[6:7] offset:1024\n\t" \
  "global_load_dwordx4 v[248:251], v22, s[6:7] offset:2048\n\t" \
  "global_load_dwordx4 v[192:195], v22, s[6:7]\n\t" \
  "s_sub_u32 s3, s27, 576\n\t" \
  "v_sub_u32 v254, v0, v254\n\t" \
  "s_add_u32 s4, s27, 6720\n\t" \
  "v_lshl_add_u32 v254, v254, 4, s3\n\t" \
  "s_min_u32 s4, s4, 0x18000\n\t" \
  "s_and_b32 s24, s2, 7\n\t" \
  "v_cmp_gt_u32 vcc, s4, v254\n\t" \
  "s_lshl_b32 s24, s24, 6\n\t" \
  "s_mov_b32 s26, 0x18000\n\t" \
  "s_cmp_ge_u32 s2, 128\n\t" \
  "s_cselect_b32 s3, 29, 34\n\t" \
  "v_cndmask_b32 v254, 2.0, v254, vcc\n\t" \
  "s_cselect_b32 s26, s26, 0xfffe8000\n\t" \
  "s_add_u32 s24, s24, s3\n\t" \
  "s_lshl4_add_u32 s25, s5, 26864\n\t" \
  "s_mul_i32 s24, s24, 0x18000\n\t" \
  "s_mov_b32 s10, 0x3000000\n\t" \
  "s_mov_b32 s11, 0x20000\n\t" \
  "s_mov_b32 m0, s25\n\t" \
  "s_cmp_ge_u32 s5, 192\n\t" \
  "s_cbranch_scc1 .Lcnx_nd\n\t" \
  "v_bfe_u32 v1, v0, 2, 3\n\t" \
  "s_cmp_ge_u32 s2, 128\n\t" \
  "v_and_b32 v20, 3, v0\n\t" \
  "s_cselect_b32 s3, 7, 0\n\t" \
  "v_and_b32 v2, 0x1e0, v0\n\t" \
  "s_cselect_b32 s4, 0, 7\n\t" \
  "v_add_u32 v1, s3, v1\n\t" \
  "s_mov_b32 s14, 9408\n\t" \
  "v_lshl_or_b32 v20, v20, 3, v2\n\t" \
  "v_xor_b32 v1, s4, v1\n\t" \
  "s_movk_i32 s3, 1344\n\t" \
  "v_and_b32 v1, 7, v1\n\t" \
  "s_mov_b32 s15, 0x20000\n\t" \
  "v_mad_u32_u24 v1, v1, s3, v20\n\t" \
  "s_waitcnt lgkmcnt(0)\n\t" \
  "buffer_load_dwordx2 v[2:3], v1, s[12:15], 0 offen offset:0\n\t" \
  "buffer_load_dwordx2 v[4:5], v1, s[12:15], 0 offen offset:192\n\t" \
  "buffer_load_dwordx2 v[6:7], v1, s[12:15], 0 offen offset:384\n\t" \
  "buffer_load_dwordx2 v[8:9], v1, s[12:15], 0 offen offset:576\n\t" \
  "buffer_load_dwordx2 v[10:11], v1, s[12:15], 0 offen offset:768\n\t" \
  "buffer_load_dwordx2 v[12:13], v1, s[12:15], 0 offen offset:960\n\t" \
  "buffer_load_dwordx2 v[14:15], v1, s[12:15], 0 offen offset:1152\n\t" \
  ".Lcnx_nd:\n\t" \
  "s_waitcnt lgkmcnt(0)\n\t" \
  "buffer_load_dwordx4 v254, s[8:11], s24 offen lds\n\t" \
  "s_add_u32 m0, s25, 8200\n\t" \
  "s_add_u32 s24, s24, s26\n\t" \
  "buffer_load_dwordx4 v254, s[8:11], s24 offen lds\n\t" \
  "s_and_b32 s3, s5, 0xc0\n\t" \
  "s_cmp_lg_u32 s3, 0xc0\n\t" \
  "s_cbranch_scc1 .Lcnx_nw\n\t" \
  "s_cmp_lt_u32 s5, 256\n\t" \
  "s_cselect_b64 s[16:17], s[32:33], s[34:35]\n\t" \
  "s_mov_b32 s18, 73728\n\t" \
  "s_mov_b32 s19, 0x20000\n\t" \
  "s_mul_i32 s3, s26, 8\n\t" \
  "s_add_u32 s4, s24, s27\n\t" \
  "s_add_u32 s4, s4, s3\n\t" \
  "s_load_dword s69, s[8:9], s4\n\t" \
  "s_add_u32 s4, s4, s3\n\t" \
  "s_load_dword s69, s[8:9], s4\n\t" \
  "v_and_b32 v21, 63, v0\n\t" \
  "s_lshr_b32 s3, s2, 3\n\t" \
  "s_mul_i32 s4, s3, 2304\n\t" \
  "s_mov_b32 m0, 107744\n\t" \
  "v_mul_u32_u24 v2, 36, v21\n\t" \
  "s_lshl_b32 s3, s3, 11\n\t" \
  "buffer_load_dword v2, s[16:19], s4 offen lds\n\t" \
  "s_getpc_b64 s[14:15]\n\t" \
  ".Lcnx_pc3:\n\t" \
  "v_lshl_add_u32 v1, v21, 5, s3\n\t" \
  "v_min_u32 v1, .Lcnx_end-.Lcnx_pc3, v1\n\t" \
  "global_load_lds_dword v1, s[14:15]\n\t" \
  ".Lcnx_nw:\n\t" \
  "s_setprio 2\n\t" \
  "s_lshr_b32 s3, s5, 7\n\t" \
  "s_add_u32 s3, s3, 6\n\t" \
  "s_mul_i32 s3, s3, s26\n\t" \
  "s_add_u32 s29, s24, s27\n\t" \
  "s_add_u32 s29, s29, s3\n\t" \
  "s_mul_i32 s3, s26, 23\n\t" \
  "s_add_u32 s27, s24, s3\n\t" \
  "s_load_dwordx2 s[20:21], s[0:1], 0x20\n\t" \
  "s_add_u32 m0, s25, 16400\n\t" \
  "s_add_u32 s24, s24, s26\n\t" \
  "buffer_load_dwordx4 v254, s[8:11], s24 offen lds\n\t" \
  "s_mul_i32 s4, s5, 50\n\t" \
  "s_add_u32 s66, s6, s4\n\t" \
  "s_addc_u32 s67, s7, 0\n\t" \
  "s_cmp_lt_u32 s5, 256\n\t" \
  "s_cselect_b64 s[16:17], s[32:33], s[34:35]\n\t" \
  "s_mov_b32 s18, 73728\n\t" \
  "s_mov_b32 s19, 0x20000\n\t" \
  "s_load_dwordx16 s[32:47], s[66:67], 0xc00\n\t" \
  "s_load_dwordx16 s[48:63], s[66:67], 0xc40\n\t" \
  "s_add_u32 m0, s25, 24600\n\t" \
  "s_add_u32 s24, s24, s26\n\t" \
  "buffer_load_dwordx4 v254, s[8:11], s24 offen lds\n\t" \
  "s_setprio 1\n\t" \
  "s_setreg_imm32_b32 hwreg(HW_REG_MODE, 4, 2), 0\n\t" \
  "s_setreg_imm32_b32 hwreg(HW_REG_MODE, 9, 1), 0\n\t" \
  "s_mov_b64 s[30:31], 0x3f80\n\t" \
  "v_lshlrev_b32 v242, 4, v0\n\t" \
  "v_mov_b64 v[240:241], 0\n\t" \
  "v_lshlrev_b64 v[242:243], v242, s[30:31]\n\t" \
  "v_mov_b64 v[238:239], 0\n\t" \
  "s_mov_b32 s22, 0x3000000\n\t" \
  "s_mov_b32 s23, 0x20000\n\t" \
  "s_add_u32 m0, s25, 32800\n\t" \
  "s_add_u32 s24, s24, s26\n\t" \
  "buffer_load_dwordx4 v254, s[8:11], s24 offen lds\n\t" \
  "s_setprio 0\n\t" \
  "v_mfma_f32_32x32x16_bf16 v[144:159], v[238:241], v[238:241], 0\n\t" \
  "v_mfma_f32_32x32x16_bf16 v[160:175], v[238:241], v[238:241], 0\n\t" \
  "v_and_b32 v234, 3, v0\n\t" \
  "s_lshl_b32 s65, s26, 4\n\t" \
  "v_mul_u32_u24 v234, 0x5555, v234\n\t" \
  "s_and_b32 s3, s5, 64\n\t" \
  "s_mul_i32 s3, s3, 48\n\t" \
  "s_add_u32 s29, s29, s3\n\t" \
  "s_mov_b32 s30, 0x01010101\n\t" \
  "s_mov_b32 s31, 0x01010101\n\t" \
  "s_mul_i32 s64, s26, 6\n\t" \
  "s_cmp_ge_u32 s5, 256\n\t" \
  "s_cselect_b32 s3, -6, -2\n\t" \
  "s_mul_i32 s3, s3, s26\n\t" \
  "s_add_u32 s28, s29, s3\n\t" \
  "s_mov_b64 vcc, 0\n\t" \
  "s_lshl2_add_u32 s12, s26, s29\n\t" \
  "s_lshl3_add_u32 s13, s26, s29\n\t" \
  "s_mov_b32 s68, 0x07060302\n\t" \
  "s_cmp_gt_u32 s5, 192\n\t" \
  "s_cbranch_scc1 .Lcnx_e2\n\t" \
  "s_cmp_eq_u32 s5, 192\n\t" \
  "s_cbranch_scc1 .Lcnx_e1\n\t" \
  "v_and_b32 v18, 1, v0\n\t" \
  "s_movk_i32 s3, 0x2b80\n\t" \
  "v_lshlrev_b32 v19, 4, v18\n\t" \
  "v_mul_i32_i24 v18, -384, v18\n\t" \
  "v_lshlrev_b32 v19, v19, s3\n\t" \
  "v_add_u32 v18, 192, v18\n\t" \
  "v_xor_b32 v23, 0x2b802b80, v19\n\t" \
  ".p2align 3\n\t" \
  "s_waitcnt vmcnt(5)\n\t" \
  "v_add_u32 v18, v18, v245\n\t" \
  "v_dot2_f32_bf16 v48, v2, v19, 0\n\t" \
  "v_dot2_f32_bf16 v50, v4, v19, 0\n\t" \
  "v_dot2_f32_bf16 v52, v6, v19, 0\n\t" \
  "v_dot2_f32_bf16 v54, v8, v19, 0\n\t" \
  "v_dot2_f32_bf16 v56, v10, v19, 0\n\t" \
  "v_dot2_f32_bf16 v58, v12, v19, 0\n\t" \
  "v_dot2_f32_bf16 v60, v14, v19, 0\n\t" \
  "v_dot2_f32_bf16 v2, v2, v23, 0\n\t" \
  "v_dot2_f32_bf16 v4, v4, v23, 0\n\t" \
  "v_dot2_f32_bf16 v6, v6, v23, 0\n\t" \
  "v_dot2_f32_bf16 v8, v8, v23, 0\n\t" \
  "v_dot2_f32_bf16 v10, v10, v23, 0\n\t" \
  "v_dot2_f32_bf16 v12, v12, v23, 0\n\t" \
  "v_dot2_f32_bf16 v14, v14, v23, 0\n\t" \
  "v_perm_b32 v24, v50, 0, s68\n\t" \
  "v_perm_b32 v25, v58, v54, s68\n\t" \
  "v_perm_b32 v26, v52, v48, s68\n\t" \
  "v_perm_b32 v27, v60, v56, s68\n\t" \
  "v_perm_b32 v28, v54, v50, s68\n\t" \
  "v_perm_b32 v29, 0, v58, s68\n\t" \
  "ds_write_b64 v245, v[24:25] offset:0\n\t" \
  "ds_write_b64 v245, v[26:27] offset:8\n\t" \
  "ds_write_b64 v245, v[28:29] offset:16\n\t" \
  "v_dot2_f32_bf16 v49, v3, v19, 0\n\t" \
  "v_dot2_f32_bf16 v51, v5, v19, 0\n\t" \
  "v_dot2_f32_bf16 v53, v7, v19, 0\n\t" \
  "v_dot2_f32_bf16 v55, v9, v19, 0\n\t" \
  "v_dot2_f32_bf16 v57, v11, v19, 0\n\t" \
  "v_dot2_f32_bf16 v59, v13, v19, 0\n\t" \
  "v_dot2_f32_bf16 v61, v15, v19, 0\n\t" \
  "v_perm_b32 v30, v4, 0, s68\n\t" \
  "v_perm_b32 v31, v12, v8, s68\n\t" \
  "v_perm_b32 v32, v6, v2, s68\n\t" \
  "v_perm_b32 v33, v14, v10, s68\n\t" \
  "v_perm_b32 v34, v8, v4, s68\n\t" \
  "v_perm_b32 v35, 0, v12, s68\n\t" \
  "ds_write_b64 v18, v[30:31] offset:0\n\t" \
  "ds_write_b64 v18, v[32:33] offset:8\n\t" \
  "ds_write_b64 v18, v[34:35] offset:16\n\t" \
  "v_dot2_f32_bf16 v3, v3, v23, 0\n\t" \
  "v_dot2_f32_bf16 v5, v5, v23, 0\n\t" \
  "v_dot2_f32_bf16 v7, v7, v23, 0\n\t" \
  "v_dot2_f32_bf16 v9, v9, v23, 0\n\t" \
  "v_dot2_f32_bf16 v11, v11, v23, 0\n\t" \
  "v_dot2_f32_bf16 v13, v13, v23, 0\n\t" \
  "v_dot2_f32_bf16 v15, v15, v23, 0\n\t" \
  "v_perm_b32 v37, v51, 0, s68\n\t" \
  "v_perm_b32 v36, v59, v55, s68\n\t" \
  "v_perm_b32 v39, v53, v49, s68\n\t" \
  "v_perm_b32 v38, v61, v57, s68\n\t" \
  "v_perm_b32 v41, v55, v51, s68\n\t" \
  "v_perm_b32 v40, 0, v59, s68\n\t" \
  "ds_write_b64 v245, v[36:37] offset:384\n\t" \
  "ds_write_b64 v245, v[38:39] offset:392\n\t" \
  "ds_write_b64 v245, v[40:41] offset:400\n\t" \
  "v_perm_b32 v43, v5, 0, s68\n\t" \
  "v_perm_b32 v42, v13, v9, s68\n\t" \
  "v_perm_b32 v45, v7, v3, s68\n\t" \
  "v_perm_b32 v44, v15, v11, s68\n\t" \
  "v_perm_b32 v47, v9, v5, s68\n\t" \
  "v_perm_b32 v46, 0, v13, s68\n\t" \
  "ds_write_b64 v18, v[42:43] offset:384\n\t" \
  "ds_write_b64 v18, v[44:45] offset:392\n\t" \
  "ds_write_b64 v18, v[46:47] offset:400\n\t" \
  "s_branch .Lcnx_e3\n\t" \
  ".Lcnx_e1:\n\t" \
  "v_lshlrev_b32 v21, 3, v21\n\t" \
  "s_mov_b64 exec, 0xf\n\t" \
  "ds_write_b64 v21, v[240:241] offset:9984\n\t" \
  "ds_write_b64 v21, v[240:241] offset:26656\n\t" \
  "s_mov_b64 exec, 0x3ffff\n\t" \
  "v_add_u32 v21, 116544, v21\n\t" \
  "ds_write_b64 v21, v[240:241] offset:47136\n\t" \
  "s_mov_b64 exec, -1\n\t" \
  "s_branch .Lcnx_e4\n\t" \
  ".Lcnx_e2:\n\t" \
  "s_setprio 1\n\t" \
  ".Lcnx_e4:\n\t" \
  ".Lcnx_e3:\n\t" \
  "s_add_u32 m0, s25, 41000\n\t" \
  "s_add_u32 s24, s24, s26\n\t" \
  "buffer_load_dwordx4 v254, s[8:11], s24 offen lds\n\t" \
  "s_add_u32 m0, s25, 56880\n\t" \
  "s_add_u32 s24, s24, s26\n\t" \
  "buffer_load_dwordx4 v254, s[8:11], s24 offen lds\n\t" \
  "s_add_u32 m0, s25, 65080\n\t" \
  "s_add_u32 s24, s24, s26\n\t" \
  "buffer_load_dwordx4 v254, s[8:11], s24 offen lds\n\t" \
  "s_waitcnt vmcnt(8)\n\t" \
  "v_mov_b32 v252, v244\n\t" \
  "v_mov_b32 v253, v246\n\t" \
  "v_mov_b64 v[244:245], 0\n\t" \
  ".p2align 3\n\t" \
  "s_waitcnt vmcnt(6) lgkmcnt(0)\n\t" \
  "s_barrier\n\t" \
  "ds_read_b64 v[222:223], v253 offset:1216\n\t" \
  "ds_read2_b32 v[114:115], v247 offset0:192 offset1:198\n\t" \
  "ds_read_b64 v[214:215], v253 offset:1208\n\t" \
  "ds_read2_b32 v[98:99], v247 offset0:0 offset1:6\n\t" \
  "s_sub_u32 s66, s66, 233088\n\t" \
  "s_subb_u32 s67, s67, 0\n\t" \
  "s_add_u32 s24, s24, s26\n\t" \
  "s_add_u32 m0, s25, 73280\n\t" \
  "v_mfma_f32_32x32x16_bf16 v[176:191], v[242:245], v[240:243], 0\n\t" \
  "ds_read_b64 v[212:213], v253 offset:808\n\t" \
  "ds_read_b64 v[206:207], v253 offset:1200\n\t" \
  "v_add_u32 v232, s56, v247\n\t" \
  "ds_read_b64 v[200:201], v253 offset:0\n\t" \
  "ds_read_b64 v[208:209], v253 offset:8\n\t" \
  "ds_read_b64 v[202:203], v253 offset:400\n\t" \
  "ds_read_b64 v[210:211], v253 offset:408\n\t" \
  "ds_read2_b32 v[102:103], v247 offset0:12 offset1:18\n\t" \
  "ds_read_b64 v[220:221], v253 offset:816\n\t" \
  "ds_read2_b32 v[130:131], v232 offset0:0 offset1:6\n\t" \
  "s_waitcnt lgkmcnt(9)\n\t" \
  "ds_read2_b32 v[106:107], v247 offset0:24 offset1:30\n\t" \
  "buffer_load_dwordx4 v254, s[8:11], s24 offen lds\n\t" \
  "s_add_u32 m0, s25, 81480\n\t" \
  "ds_read2_b32 v[118:119], v247 offset0:204 offset1:210\n\t" \
  "ds_read2_b32 v[138:139], v232 offset0:24 offset1:30\n\t" \
  "ds_read2_b32 v[142:143], v232 offset0:36 offset1:42\n\t" \
  "v_and_b32 v96, 0xffff, v98\n\t" \
  "s_add_u32 s24, s24, s26\n\t" \
  "buffer_load_dwordx4 v254, s[8:11], s24 offen lds\n\t" \
  "s_add_u32 m0, s25, 89680\n\t" \
  "ds_read_b64 v[204:205], v253 offset:800\n\t" \
  "ds_read_b64 v[216:217], v253 offset:16\n\t" \
  "v_and_b32 v112, 0xffff, v114\n\t" \
  "v_add_u32 v233, s39, v251\n\t" \
  "s_add_u32 s24, s24, s26\n\t" \
  "s_waitcnt lgkmcnt(8)\n\t" \
  "buffer_load_dwordx4 v254, s[8:11], s24 offen lds\n\t" \
  "s_add_u32 m0, s25, 97880\n\t" \
  "ds_read2_b32 v[126:127], v247 offset0:228 offset1:234\n\t" \
  "ds_read_b64 v[218:219], v253 offset:416\n\t" \
  "v_and_b32 v97, 0xffff0000, v98\n\t" \
  "v_and_b32 v100, 0xffff, v102\n\t" \
  "v_and_b32 v98, 0xffff, v99\n\t" \
  "v_and_b32 v99, 0xffff0000, v99\n\t" \
  "s_add_u32 s24, s24, s26\n\t" \
  "ds_read2_b32 v[122:123], v247 offset0:216 offset1:222\n\t" \
  "buffer_load_dwordx4 v254, s[8:11], s24 offen lds\n\t" \
  "s_waitcnt lgkmcnt(4)\n\t" \
  "ds_read2_b32 v[134:135], v232 offset0:12 offset1:18\n\t" \
  "v_and_b32 v105, 0xffff0000, v106\n\t" \
  "v_smfmac_f32_16x16x64_bf16 v[144:147], v[96:99], v[200:207], v234\n\t" \
  "ds_read2_b32 v[110:111], v247 offset0:36 offset1:42\n\t" \
  "v_and_b32 v113, 0xffff0000, v114\n\t" \
  "v_add_u32 v232, 56880, v253\n\t" \
  "v_and_b32 v129, 0xffff0000, v130\n\t" \
  "v_and_b32 v128, 0xffff, v130\n\t" \
  "v_and_b32 v114, 0xffff, v115\n\t" \
  "v_and_b32 v115, 0xffff0000, v115\n\t" \
  "v_and_b32 v130, 0xffff, v131\n\t" \
  "v_and_b32 v101, 0xffff0000, v102\n\t" \
  "v_and_b32 v102, 0xffff, v103\n\t" \
  "v_and_b32 v116, 0xffff, v118\n\t" \
  "v_and_b32 v103, 0xffff0000, v103\n\t" \
  ".p2align 3\n\t" \
  "s_waitcnt vmcnt(8) lgkmcnt(0)\n\t" \
  "s_barrier\n\t" \
  "v_and_b32 v104, 0xffff, v106\n\t" \
  "ds_read_b64 v[206:207], v253 offset:17608\n\t" \
  "ds_read_b64 v[226:227], v253 offset:16800\n\t" \
  "v_and_b32 v131, 0xffff0000, v131\n\t" \
  "v_and_b32 v106, 0xffff, v107\n\t" \
  "v_and_b32 v132, 0xffff, v134\n\t" \
  "ds_read_b64 v[228:229], v253 offset:17200\n\t" \
  "v_smfmac_f32_16x16x64_bf16 v[152:155], v[128:131], v[216:223], v234\n\t" \
  "ds_read_b64 v[230:231], v253 offset:17600\n\t" \
  "ds_read_b64 v[224:225], v253 offset:16400\n\t" \
  "v_smfmac_f32_16x16x64_bf16 v[148:151], v[112:115], v[208:215], v234\n\t" \
  "ds_read_b64 v[210:211], v253 offset:16816\n\t" \
  "ds_read_b64 v[200:201], v253 offset:16408\n\t" \
  "ds_read_b64 v[202:203], v253 offset:16808\n\t" \
  "v_and_b32 v107, 0xffff0000, v107\n\t" \
  "v_and_b32 v133, 0xffff0000, v134\n\t" \
  "v_and_b32 v117, 0xffff0000, v118\n\t" \
  "v_and_b32 v118, 0xffff, v119\n\t" \
  "v_and_b32 v134, 0xffff, v135\n\t" \
  "v_and_b32 v119, 0xffff0000, v119\n\t" \
  "s_waitcnt lgkmcnt(3)\n\t" \
  "ds_read_b64 v[212:213], v253 offset:17216\n\t" \
  "ds_read_b64 v[204:205], v253 offset:17208\n\t" \
  "ds_read_b64 v[208:209], v253 offset:16416\n\t" \
  "ds_read_b64 v[214:215], v253 offset:17616\n\t" \
  "v_lshlrev_b32 v235, 1, v251\n\t" \
  "v_and_b32 v236, 0x70, v235\n\t" \
  "v_bfe_u32 v235, v250, 6, 1\n\t" \
  "v_lshl_add_u32 v235, v235, 4, v250\n\t" \
  "v_smfmac_f32_16x16x64_bf16 v[156:159], v[96:99], v[224:231], v234\n\t" \
  "s_cmp_eq_u32 s37, 1\n\t" \
  "s_cselect_b64 s[68:69], -1, 0\n\t" \
  "v_and_b32 v135, 0xffff0000, v135\n\t" \
  "v_smfmac_f32_16x16x64_bf16 v[144:147], v[100:103], v[224:231], v234\n\t" \
  ".p2align 3\n\t" \
  "s_waitcnt vmcnt(5) lgkmcnt(0)\n\t" \
  "s_barrier\n\t" \
  "ds_read_b64 v[218:219], v253 offset:33200\n\t" \
  "v_and_b32 v120, 0xffff, v122\n\t" \
  "ds_read_b64 v[216:217], v253 offset:32800\n\t" \
  "v_add3_u32 v235, v235, v236, s61\n\t" \
  "ds_read_b64 v[78:79], v235 offset:8\n\t" \
  "v_smfmac_f32_16x16x64_bf16 v[160:163], v[112:115], v[200:207], v234\n\t" \
  "v_smfmac_f32_16x16x64_bf16 v[148:151], v[116:119], v[200:207], v234\n\t" \
  "ds_read_b64 v[222:223], v253 offset:34000\n\t" \
  "ds_read_b64 v[76:77], v235 offset:0\n\t" \
  "ds_read_b64 v[224:225], v253 offset:32808\n\t" \
  "v_and_b32 v121, 0xffff0000, v122\n\t" \
  "v_and_b32 v122, 0xffff, v123\n\t" \
  "ds_read_b64 v[220:221], v253 offset:33600\n\t" \
  "v_and_b32 v136, 0xffff, v138\n\t" \
  "ds_read_b64 v[226:227], v253 offset:33208\n\t" \
  "v_and_b32 v123, 0xffff0000, v123\n\t" \
  "v_and_b32 v108, 0xffff, v110\n\t" \
  "ds_read_b64 v[200:201], v253 offset:32816\n\t" \
  "v_smfmac_f32_16x16x64_bf16 v[164:167], v[128:131], v[208:215], v234\n\t" \
  "ds_read_b64 v[202:203], v253 offset:33216\n\t" \
  "v_and_b32 v137, 0xffff0000, v138\n\t" \
  "v_and_b32 v138, 0xffff, v139\n\t" \
  "v_and_b32 v139, 0xffff0000, v139\n\t" \
  "s_waitcnt lgkmcnt(3)\n\t" \
  "ds_read_b64 v[230:231], v253 offset:34008\n\t" \
  "ds_read_b64 v[94:95], v235 offset:136\n\t" \
  "ds_read_b64 v[84:85], v235 offset:64\n\t" \
  "v_and_b32 v109, 0xffff0000, v110\n\t" \
  "v_smfmac_f32_16x16x64_bf16 v[168:171], v[96:99], v[216:223], v234\n\t" \
  "s_add_u32 m0, s25, 0\n\t" \
  "s_add_u32 s24, s24, s26\n\t" \
  "buffer_load_dwordx4 v254, s[8:11], s24 offen lds\n\t" \
  "ds_read_b64 v[204:205], v253 offset:33616\n\t" \
  "v_mfma_f32_32x32x16_bf16 v[0:15], v[242:245], v[240:243], 0\n\t" \
  "ds_read_b64 v[92:93], v235 offset:128\n\t" \
  "ds_read_b64 v[228:229], v253 offset:33608\n\t" \
  "v_smfmac_f32_16x16x64_bf16 v[152:155], v[132:135], v[208:215], v234\n\t" \
  "ds_read_b64 v[86:87], v235 offset:72\n\t" \
  "ds_read_b64 v[206:207], v253 offset:34016\n\t" \
  "v_and_b32 v110, 0xffff, v111\n\t" \
  "v_and_b32 v124, 0xffff, v126\n\t" \
  "v_add_u32 v235, s58, v235\n\t" \
  "v_and_b32 v111, 0xffff0000, v111\n\t" \
  "v_smfmac_f32_16x16x64_bf16 v[156:159], v[100:103], v[216:223], v234\n\t" \
  "v_smfmac_f32_16x16x64_bf16 v[144:147], v[104:107], v[216:223], v234\n\t" \
  ".p2align 3\n\t" \
  "s_waitcnt vmcnt(5) lgkmcnt(0)\n\t" \
  "s_barrier\n\t" \
  "v_smfmac_f32_16x16x64_bf16 v[172:175], v[112:115], v[224:231], v234\n\t" \
  "ds_read_b64 v[210:211], v232 offset:400\n\t" \
  "ds_read_b64 v[208:209], v232 offset:0\n\t" \
  "v_and_b32 v125, 0xffff0000, v126\n\t" \
  "v_and_b32 v126, 0xffff, v127\n\t" \
  "v_and_b32 v127, 0xffff0000, v127\n\t" \
  "v_smfmac_f32_16x16x64_bf16 v[160:163], v[116:119], v[224:231], v234\n\t" \
  "s_add_u32 m0, s25, 8200\n\t" \
  "s_add_u32 s24, s24, s26\n\t" \
  "buffer_load_dwordx4 v254, s[8:11], s24 offen lds\n\t" \
  "ds_read_b64 v[214:215], v232 offset:1200\n\t" \
  "v_smfmac_f32_16x16x64_bf16 v[148:151], v[120:123], v[224:231], v234\n\t" \
  "ds_read_b64 v[218:219], v232 offset:408\n\t" \
  "ds_read_b64 v[212:213], v232 offset:800\n\t" \
  "v_smfmac_f32_16x16x64_bf16 v[176:179], v[128:131], v[200:207], v234\n\t" \
  "v_smfmac_f32_16x16x64_bf16 v[164:167], v[132:135], v[200:207], v234\n\t" \
  "ds_read_b64 v[220:221], v232 offset:808\n\t" \
  "v_and_b32 v140, 0xffff, v142\n\t" \
  "ds_read_b64 v[216:217], v232 offset:8\n\t" \
  "ds_read_b64 v[222:223], v232 offset:1208\n\t" \
  "v_and_b32 v141, 0xffff0000, v142\n\t" \
  "v_and_b32 v142, 0xffff, v143\n\t" \
  "v_and_b32 v143, 0xffff0000, v143\n\t" \
  "ds_read_b64 v[224:225], v232 offset:16\n\t" \
  "ds_read_b64 v[228:229], v232 offset:816\n\t" \
  "v_smfmac_f32_16x16x64_bf16 v[152:155], v[136:139], v[200:207], v234\n\t" \
  "s_waitcnt lgkmcnt(5)\n\t" \
  "v_smfmac_f32_16x16x64_bf16 v[180:183], v[96:99], v[208:215], v234\n\t" \
  "s_add_u32 m0, s25, 16400\n\t" \
  "s_add_u32 s24, s24, s26\n\t" \
  "buffer_load_dwordx4 v254, s[8:11], s24 offen lds\n\t" \
  "ds_read_b64 v[226:227], v232 offset:416\n\t" \
  "v_mov_b32 v197, 0x3f003f00\n\t" \
  "v_smfmac_f32_16x16x64_bf16 v[168:171], v[100:103], v[208:215], v234\n\t" \
  "ds_read_b64 v[230:231], v232 offset:1216\n\t" \
  "v_mov_b32 v196, 0x3f003f00\n\t" \
  "v_mov_b32 v198, 0x3f003f00\n\t" \
  "v_mfma_f32_32x32x16_bf16 v[16:31], v[242:245], v[240:243], 0\n\t" \
  "v_mov_b32 v199, 0x3f003f00\n\t" \
  "v_smfmac_f32_16x16x64_bf16 v[144:147], v[108:111], v[208:215], v234\n\t" \
  ".p2align 3\n\t" \
  "s_waitcnt vmcnt(4) lgkmcnt(0)\n\t" \
  "s_barrier\n\t" \
  "ds_read_b64 v[202:203], v232 offset:16800\n\t" \
  "ds_read_b64 v[200:201], v232 offset:16400\n\t" \
  "v_smfmac_f32_16x16x64_bf16 v[184:187], v[112:115], v[216:223], v234\n\t" \
  "ds_read_b64 v[206:207], v232 offset:17600\n\t" \
  "v_smfmac_f32_16x16x64_bf16 v[172:175], v[116:119], v[216:223], v234\n\t" \
  "ds_read_b64 v[204:205], v232 offset:17200\n\t" \
  "v_smfmac_f32_16x16x64_bf16 v[156:159], v[104:107], v[208:215], v234\n\t" \
  "ds_read_b64 v[210:211], v232 offset:16808\n\t" \
  "v_cvt_pk_bf16_f32 v144, v144, v145\n\t" \
  "v_smfmac_f32_16x16x64_bf16 v[148:151], v[124:127], v[216:223], v234\n\t" \
  "ds_read_b64 v[72:73], v235 offset:0\n\t" \
  "v_smfmac_f32_16x16x64_bf16 v[160:163], v[120:123], v[216:223], v234\n\t" \
  "ds_read_b64 v[208:209], v232 offset:16408\n\t" \
  "v_cvt_pk_bf16_f32 v145, v146, v147\n\t" \
  "ds_read_b64 v[214:215], v232 offset:17608\n\t" \
  "ds_read_b64 v[74:75], v235 offset:8\n\t" \
  "ds_read_b64 v[88:89], v235 offset:128\n\t" \
  "v_cvt_pk_bf16_f32 v148, v148, v149\n\t" \
  "v_cvt_pk_bf16_f32 v149, v150, v151\n\t" \
  "v_smfmac_f32_16x16x64_bf16 v[176:179], v[132:135], v[224:231], v234\n\t" \
  "ds_read_b64 v[218:219], v232 offset:16816\n\t" \
  "ds_read_b64 v[212:213], v232 offset:17208\n\t" \
  "v_smfmac_f32_16x16x64_bf16 v[188:191], v[128:131], v[224:231], v234\n\t" \
  "v_smfmac_f32_16x16x64_bf16 v[164:167], v[136:139], v[224:231], v234\n\t" \
  "ds_read_b64 v[80:81], v235 offset:64\n\t" \
  "v_smfmac_f32_16x16x64_bf16 v[152:155], v[140:143], v[224:231], v234\n\t" \
  "ds_read_b64 v[222:223], v232 offset:17616\n\t" \
  "ds_read_b64 v[220:221], v232 offset:17216\n\t" \
  "s_waitcnt lgkmcnt(11)\n\t" \
  "v_smfmac_f32_16x16x64_bf16 v[0:3], v[96:99], v[200:207], v234\n\t" \
  "ds_read_b64 v[82:83], v235 offset:72\n\t" \
  "ds_read_b64 v[90:91], v235 offset:136\n\t" \
  "v_smfmac_f32_16x16x64_bf16 v[180:183], v[100:103], v[200:207], v234\n\t" \
  "ds_read_b64 v[216:217], v232 offset:16416\n\t" \
  "v_mfma_f32_32x32x16_bf16 v[32:47], v[242:245], v[240:243], 0\n\t" \
  "v_add_u32 v235, s59, v235\n\t" \
  "v_cvt_pk_bf16_f32 v152, v152, v153\n\t" \
  "v_cvt_pk_bf16_f32 v153, v154, v155\n\t" \
  "v_smfmac_f32_16x16x64_bf16 v[156:159], v[108:111], v[200:207], v234\n\t" \
  "v_smfmac_f32_16x16x64_bf16 v[168:171], v[104:107], v[200:207], v234\n\t" \
  ".p2align 3\n\t" \
  "s_waitcnt vmcnt(2) lgkmcnt(0)\n\t" \
  "s_barrier\n\t" \
  "ds_read_b64 v[226:227], v232 offset:33200\n\t" \
  "ds_read_b64 v[224:225], v232 offset:32800\n\t" \
  "ds_read_b64 v[48:49], v235 offset:0\n\t" \
  "v_smfmac_f32_16x16x64_bf16 v[184:187], v[116:119], v[208:215], v234\n\t" \
  "s_add_u32 m0, s25, 24600\n\t" \
  "s_add_u32 s24, s24, s26\n\t" \
  "buffer_load_dwordx4 v254, s[8:11], s24 offen lds\n\t" \
  "ds_read_b64 v[206:207], v232 offset:34008\n\t" \
  "v_smfmac_f32_16x16x64_bf16 v[172:175], v[120:123], v[208:215], v234\n\t" \
  "ds_read_b64 v[204:205], v232 offset:33608\n\t" \
  "ds_read_b64 v[200:201], v232 offset:32808\n\t" \
  "ds_read_b64 v[230:231], v232 offset:34000\n\t" \
  "ds_read_b64 v[50:51], v235 offset:8\n\t" \
  "v_smfmac_f32_16x16x64_bf16 v[4:7], v[112:115], v[208:215], v234\n\t" \
  "ds_read_b64 v[228:229], v232 offset:33600\n\t" \
  "s_nop 0\n\t" \
  "v_smfmac_f32_16x16x64_bf16 v[164:167], v[140:143], v[216:223], v234\n\t" \
  "v_cvt_pk_bf16_f32 v156, v156, v157\n\t" \
  "v_cvt_pk_bf16_f32 v157, v158, v159\n\t" \
  "v_smfmac_f32_16x16x64_bf16 v[8:11], v[128:131], v[216:223], v234\n\t" \
  "ds_read_b64 v[66:67], v235 offset:136\n\t" \
  "v_smfmac_f32_16x16x64_bf16 v[160:163], v[124:127], v[208:215], v234\n\t" \
  "ds_read_b64 v[202:203], v232 offset:33208\n\t" \
  "ds_read_b64 v[64:65], v235 offset:128\n\t" \
  "v_smfmac_f32_16x16x64_bf16 v[188:191], v[132:135], v[216:223], v234\n\t" \
  "s_add_u32 m0, s25, 32800\n\t" \
  "s_add_u32 s24, s24, s26\n\t" \
  "buffer_load_dwordx4 v254, s[8:11], s24 offen lds\n\t" \
  "ds_read_b64 v[58:59], v235 offset:72\n\t" \
  "s_waitcnt lgkmcnt(4)\n\t" \
  "ds_read_b64 v[210:211], v232 offset:33216\n\t" \
  "ds_read_b64 v[212:213], v232 offset:33616\n\t" \
  "ds_read_b64 v[214:215], v232 offset:34016\n\t" \
  "v_smfmac_f32_16x16x64_bf16 v[0:3], v[100:103], v[224:231], v234\n\t" \
  "ds_read_b64 v[208:209], v232 offset:32816\n\t" \
  "ds_read_b64 v[56:57], v235 offset:64\n\t" \
  "v_smfmac_f32_16x16x64_bf16 v[12:15], v[96:99], v[224:231], v234\n\t" \
  "v_cvt_pk_bf16_f32 v164, v164, v165\n\t" \
  "v_cvt_pk_bf16_f32 v165, v166, v167\n\t" \
  "v_cvt_pk_bf16_f32 v160, v160, v161\n\t" \
  "v_smfmac_f32_16x16x64_bf16 v[168:171], v[108:111], v[224:231], v234\n\t" \
  "v_cvt_pk_bf16_f32 v161, v162, v163\n\t" \
  "v_smfmac_f32_16x16x64_bf16 v[180:183], v[104:107], v[224:231], v234\n\t" \
  "v_smfmac_f32_16x16x64_bf16 v[176:179], v[136:139], v[216:223], v234\n\t" \
  ".p2align 3\n\t" \
  "s_waitcnt vmcnt(2) lgkmcnt(0)\n\t" \
  "s_barrier\n\t" \
  "ds_read_b64 v[216:217], v253 offset:0\n\t" \
  "ds_read_b64 v[218:219], v253 offset:400\n\t" \
  "ds_read_b64 v[220:221], v253 offset:800\n\t" \
  "v_cvt_pk_bf16_f32 v168, v168, v169\n\t" \
  "ds_write_b64 v193, v[144:145] offset:12288\n\t" \
  "v_smfmac_f32_16x16x64_bf16 v[184:187], v[120:123], v[200:207], v234\n\t" \
  "s_add_u32 m0, s25, 41000\n\t" \
  "s_add_u32 s24, s24, s26\n\t" \
  "buffer_load_dwordx4 v254, s[8:11], s24 offen lds\n\t" \
  "v_smfmac_f32_16x16x64_bf16 v[20:23], v[128:131], v[208:215], v234\n\t" \
  "ds_write_b64 v193, v[152:153] offset:20480\n\t" \
  "v_smfmac_f32_16x16x64_bf16 v[4:7], v[116:119], v[200:207], v234\n\t" \
  "ds_write_b64 v193, v[156:157] offset:0\n\t" \
  "v_smfmac_f32_16x16x64_bf16 v[16:19], v[112:115], v[200:207], v234\n\t" \
  "ds_write_b64 v193, v[148:149] offset:16384\n\t" \
  "v_cvt_pk_bf16_f32 v169, v170, v171\n\t" \
  "ds_read_b64 v[226:227], v253 offset:408\n\t" \
  "ds_write_b64 v193, v[164:165] offset:8192\n\t" \
  "ds_read_b64 v[230:231], v253 offset:1208\n\t" \
  "v_smfmac_f32_16x16x64_bf16 v[172:175], v[124:127], v[200:207], v234\n\t" \
  "s_add_u32 m0, s25, 56880\n\t" \
  "s_add_u32 s24, s24, s26\n\t" \
  "buffer_load_dwordx4 v254, s[8:11], s24 offen lds\n\t" \
  "ds_read_b64 v[222:223], v253 offset:1200\n\t" \
  "ds_read_b64 v[224:225], v253 offset:8\n\t" \
  "v_smfmac_f32_16x16x64_bf16 v[8:11], v[132:135], v[208:215], v234\n\t" \
  "s_mov_b64 exec, s[68:69]\n\t" \
  "ds_read_b64 v[48:49], v235 offset:0\n\t" \
  "ds_read_b64 v[50:51], v235 offset:8\n\t" \
  "ds_read_b64 v[56:57], v235 offset:64\n\t" \
  "v_smfmac_f32_16x16x64_bf16 v[176:179], v[140:143], v[208:215], v234\n\t" \
  "s_mov_b64 exec, -1\n\t" \
  "ds_read_b64 v[200:201], v253 offset:16\n\t" \
  "ds_write_b64 v193, v[160:161] offset:4096\n\t" \
  "v_smfmac_f32_16x16x64_bf16 v[188:191], v[136:139], v[208:215], v234\n\t" \
  "s_waitcnt lgkmcnt(6)\n\t" \
  "ds_read_b64 v[202:203], v253 offset:416\n\t" \
  "ds_read_b64 v[228:229], v253 offset:808\n\t" \
  "v_smfmac_f32_16x16x64_bf16 v[24:27], v[96:99], v[216:223], v234\n\t" \
  "ds_read_b64 v[206:207], v253 offset:1216\n\t" \
  "ds_read_b64 v[204:205], v253 offset:816\n\t" \
  "v_smfmac_f32_16x16x64_bf16 v[0:3], v[104:107], v[216:223], v234\n\t" \
  "s_mov_b64 exec, s[68:69]\n\t" \
  "ds_read_b64 v[58:59], v235 offset:72\n\t" \
  "ds_read_b64 v[64:65], v235 offset:128\n\t" \
  "ds_read_b64 v[66:67], v235 offset:136\n\t" \
  "s_mov_b64 exec, -1\n\t" \
  "v_smfmac_f32_16x16x64_bf16 v[180:183], v[108:111], v[216:223], v234\n\t" \
  "v_add_u32 v235, s60, v235\n\t" \
  "v_cvt_pk_bf16_f32 v176, v176, v177\n\t" \
  "v_cvt_pk_bf16_f32 v177, v178, v179\n\t" \
  "v_cvt_pk_bf16_f32 v172, v172, v173\n\t" \
  "v_cvt_pk_bf16_f32 v173, v174, v175\n\t" \
  "v_smfmac_f32_16x16x64_bf16 v[12:15], v[100:103], v[216:223], v234\n\t" \
  ".p2align 3\n\t" \
  "s_waitcnt vmcnt(3) lgkmcnt(0)\n\t" \
  "s_barrier\n\t" \
  "ds_write_b64 v192, v[168:169] offset:0\n\t" \
  "v_smfmac_f32_16x16x64_bf16 v[28:31], v[112:115], v[224:231], v234\n\t" \
  "s_add_u32 m0, s25, 65080\n\t" \
  "s_add_u32 s24, s24, s26\n\t" \
  "buffer_load_dwordx4 v254, s[8:11], s24 offen lds\n\t" \
  "v_cvt_pk_bf16_f32 v180, v180, v181\n\t" \
  "ds_read_b128 v[144:147], v194 offset:0\n\t" \
  "ds_read_b64 v[210:211], v253 offset:16800\n\t" \
  "v_smfmac_f32_16x16x64_bf16 v[16:19], v[116:119], v[224:231], v234\n\t" \
  "ds_read_b64 v[208:209], v253 offset:16400\n\t" \
  "ds_write_b64 v192, v[176:177] offset:8192\n\t" \
  "v_cvt_pk_bf16_f32 v181, v182, v183\n\t" \
  "ds_write_b64 v192, v[172:173] offset:4096\n\t" \
  "v_smfmac_f32_16x16x64_bf16 v[4:7], v[120:123], v[224:231], v234\n\t" \
  "ds_read_b128 v[148:151], v194 offset:4096\n\t" \
  "s_waitcnt lgkmcnt(5)\n\t" \
  "v_mfma_f32_16x16x32_bf16 v[168:171], v[144:147], v[144:147], 0\n\t" \
  "ds_read_b64 v[212:213], v253 offset:17200\n\t" \
  "ds_read_b64 v[214:215], v253 offset:17600\n\t" \
  "v_smfmac_f32_16x16x64_bf16 v[184:187], v[124:127], v[224:231], v234\n\t" \
  "ds_read_b128 v[152:155], v194 offset:8192\n\t" \
  "v_add_u32 v194, -93792, v194\n\t" \
  "v_smfmac_f32_16x16x64_bf16 v[32:35], v[128:131], v[200:207], v234\n\t" \
  "s_add_u32 m0, s25, 73280\n\t" \
  "s_add_u32 s24, s24, s26\n\t" \
  "buffer_load_dwordx4 v254, s[8:11], s24 offen lds\n\t" \
  "ds_read_b64 v[220:221], v253 offset:17208\n\t" \
  "ds_read_b64 v[218:219], v253 offset:16808\n\t" \
  "v_smfmac_f32_16x16x64_bf16 v[20:23], v[132:135], v[200:207], v234\n\t" \
  "s_nop 0\n\t" \
  "ds_read_b64 v[226:227], v253 offset:16816\n\t" \
  "s_waitcnt lgkmcnt(6)\n\t" \
  "v_mfma_f32_16x16x32_bf16 v[168:171], v[148:151], v[148:151], v[168:171]\n\t" \
  "s_waitcnt lgkmcnt(4)\n\t" \
  "ds_read_b64 v[216:217], v253 offset:16408\n\t" \
  "v_smfmac_f32_16x16x64_bf16 v[36:39], v[96:99], v[208:215], v234\n\t" \
  "ds_read_b64 v[224:225], v253 offset:16416\n\t" \
  "ds_read_b64 v[230:231], v253 offset:17616\n\t" \
  "v_smfmac_f32_16x16x64_bf16 v[8:11], v[136:139], v[200:207], v234\n\t" \
  "v_cvt_pk_bf16_f32 v184, v184, v185\n\t" \
  "v_cvt_pk_bf16_f32 v185, v186, v187\n\t" \
  "v_mfma_f32_16x16x32_bf16 v[172:175], v[196:199], v[144:147], 0\n\t" \
  "ds_read_b64_tr_b16 v[162:163], v195 offset:6144\n\t" \
  "ds_read_b64_tr_b16 v[160:161], v195 offset:2048\n\t" \
  "ds_read_b64_tr_b16 v[166:167], v195 offset:10240\n\t" \
  "ds_read_b64 v[222:223], v253 offset:17608\n\t" \
  "v_smfmac_f32_16x16x64_bf16 v[188:191], v[140:143], v[200:207], v234\n\t" \
  "s_add_u32 m0, s25, 81480\n\t" \
  "s_add_u32 s24, s24, s26\n\t" \
  "buffer_load_dwordx4 v254, s[8:11], s24 offen lds\n\t" \
  "s_nop 0\n\t" \
  "v_mfma_f32_16x16x32_bf16 v[172:175], v[196:199], v[148:151], v[172:175]\n\t" \
  "s_waitcnt lgkmcnt(10)\n\t" \
  "ds_read_b64 v[228:229], v253 offset:17216\n\t" \
  "ds_read_b64_tr_b16 v[164:165], v195 offset:8192\n\t" \
  "v_mfma_f32_16x16x32_bf16 v[168:171], v[152:155], v[152:155], v[168:171]\n\t" \
  "ds_read_b64_tr_b16 v[158:159], v195 offset:4096\n\t" \
  "ds_read_b64_tr_b16 v[156:157], v195 offset:0\n\t" \
  "v_smfmac_f32_16x16x64_bf16 v[12:15], v[104:107], v[208:215], v234\n\t" \
  "v_cvt_pk_bf16_f32 v188, v188, v189\n\t" \
  "v_cvt_pk_bf16_f32 v189, v190, v191\n\t" \
  "v_mfma_f32_16x16x32_bf16 v[172:175], v[196:199], v[152:155], v[172:175]\n\t" \
  "v_smfmac_f32_16x16x64_bf16 v[0:3], v[108:111], v[208:215], v234\n\t" \
  ".p2align 3\n\t" \
  "s_waitcnt vmcnt(3) lgkmcnt(0)\n\t" \
  "s_barrier\n\t" \
  "ds_write_b64 v192, v[180:181] offset:12288\n\t" \
  "v_lshl_add_u32 v238, v233, 1, s63\n\t" \
  "v_add_u32 v239, 0x2000, v238\n\t" \
  "ds_write_b64 v192, v[184:185] offset:16384\n\t" \
  "v_smfmac_f32_16x16x64_bf16 v[24:27], v[100:103], v[208:215], v234\n\t" \
  "s_lshl1_add_u32 m0, s39, 10016\n\t" \
  "s_nop 0\n\t" \
  "global_load_lds_dwordx4 v238, s[6:7]\n\t" \
  "s_add_u32 m0, m0, 66048\n\t" \
  "s_cmp_eq_u32 s39, 3584\n\t" \
  "ds_write_b64 v192, v[188:189] offset:20480\n\t" \
  "v_cvt_pk_bf16_f32 v0, v0, v1\n\t" \
  "s_cselect_b32 exec_hi, 0, -1\n\t" \
  "global_load_lds_dwordx4 v239, s[6:7]\n\t" \
  "s_mov_b64 exec, -1\n\t" \
  "ds_read_b64 v[60:61], v235 offset:64\n\t" \
  "v_smfmac_f32_16x16x64_bf16 v[40:43], v[112:115], v[216:223], v234\n\t" \
  "ds_read_b64 v[200:201], v253 offset:32800\n\t" \
  "v_cndmask_b32 v170, v170, v171, s[40:41]\n\t" \
  "v_smfmac_f32_16x16x64_bf16 v[28:31], v[116:119], v[216:223], v234\n\t" \
  "ds_read_b64 v[206:207], v253 offset:34000\n\t" \
  "v_cndmask_b32 v168, v168, v169, s[40:41]\n\t" \
  "ds_read_b64 v[204:205], v253 offset:33600\n\t" \
  "ds_read_b64 v[202:203], v253 offset:33200\n\t" \
  "v_mov_b32 v174, 0x1127c5ac\n\t" \
  "ds_read_b64 v[52:53], v235 offset:0\n\t" \
  "v_smfmac_f32_16x16x64_bf16 v[16:19], v[120:123], v[216:223], v234\n\t" \
  "v_smfmac_f32_16x16x64_bf16 v[4:7], v[124:127], v[216:223], v234\n\t" \
  "ds_read_b64 v[222:223], v253 offset:34016\n\t" \
  "s_not_b64 vcc, s[52:53]\n\t" \
  "v_cvt_pk_bf16_f32 v1, v2, v3\n\t" \
  "v_smfmac_f32_16x16x64_bf16 v[8:11], v[140:143], v[224:231], v234\n\t" \
  "ds_read_b64 v[68:69], v235 offset:128\n\t" \
  "ds_read_b64 v[54:55], v235 offset:8\n\t" \
  "v_smfmac_f32_16x16x64_bf16 v[44:47], v[128:131], v[224:231], v234\n\t" \
  "ds_read_b64 v[216:217], v253 offset:32816\n\t" \
  "v_smfmac_f32_16x16x64_bf16 v[32:35], v[132:135], v[224:231], v234\n\t" \
  "ds_read_b64 v[214:215], v253 offset:34008\n\t" \
  "v_smfmac_f32_16x16x64_bf16 v[20:23], v[136:139], v[224:231], v234\n\t" \
  "ds_read_b64 v[218:219], v253 offset:33216\n\t" \
  "v_add_u32 v195, -93792, v195\n\t" \
  "s_waitcnt lgkmcnt(7)\n\t" \
  "ds_read_b64 v[70:71], v235 offset:136\n\t" \
  "v_cvt_pk_bf16_f32 v8, v8, v9\n\t" \
  "ds_read_b64 v[62:63], v235 offset:72\n\t" \
  "ds_read_b64 v[212:213], v253 offset:33608\n\t" \
  "v_smfmac_f32_16x16x64_bf16 v[12:15], v[108:111], v[200:207], v234\n\t" \
  "ds_read_b64 v[220:221], v253 offset:33616\n\t" \
  "v_smfmac_f32_16x16x64_bf16 v[24:27], v[104:107], v[200:207], v234\n\t" \
  "ds_read_b64 v[210:211], v253 offset:33208\n\t" \
  "v_cndmask_b32 v173, v168, v170, s[42:43]\n\t" \
  "ds_read_b64 v[208:209], v253 offset:32808\n\t" \
  "v_smfmac_f32_16x16x64_bf16 v[36:39], v[100:103], v[200:207], v234\n\t" \
  "v_cvt_pk_bf16_f32 v9, v10, v11\n\t" \
  "v_cvt_pk_bf16_f32 v4, v4, v5\n\t" \
  "v_cvt_pk_bf16_f32 v5, v6, v7\n\t" \
  ".p2align 3\n\t" \
  "s_waitcnt vmcnt(4) lgkmcnt(0)\n\t" \
  "s_barrier\n\t" \
  "s_cmp_lg_u32 s37, 0\n\t" \
  "ds_read_b128 v[180:183], v194 offset:0\n\t" \
  "s_cbranch_scc1 .Lcnx_c2\n\t" \
  "s_mov_b32 m0, s46\n\t" \
  "s_mov_b32 exec_hi, 0xffff\n\t" \
  "buffer_load_dwordx4 v252, s[16:19], s33 offen lds\n\t" \
  "s_mov_b32 exec_hi, -1\n\t" \
  "s_add_u32 s33, s33, s32\n\t" \
  "s_add_u32 m0, s46, s35\n\t" \
  "s_mov_b32 exec_hi, 0xffff\n\t" \
  "buffer_load_dwordx4 v252, s[16:19], s34 offen lds\n\t" \
  "s_mov_b32 exec_hi, -1\n\t" \
  "s_add_u32 s34, s34, s32\n\t" \
  "s_mov_b32 m0, s47\n\t" \
  "s_mov_b32 exec_hi, 0xffff\n\t" \
  "buffer_load_dwordx4 v252, s[16:19], s33 offen lds\n\t" \
  "s_mov_b32 exec_hi, -1\n\t" \
  "s_add_u32 s33, s33, s32\n\t" \
  "s_add_u32 m0, s47, s35\n\t" \
  "s_mov_b32 exec_hi, 0xffff\n\t" \
  "buffer_load_dwordx4 v252, s[16:19], s34 offen lds\n\t" \
  "s_mov_b32 exec_hi, -1\n\t" \
  "s_add_u32 s34, s34, s32\n\t" \
  ".Lcnx_c2:\n\t" \
  "ds_read_b64 v[230:231], v232 offset:1200\n\t" \
  "ds_write_b64 v192, v[0:1] offset:24576\n\t" \
  "v_smfmac_f32_16x16x64_bf16 v[20:23], v[140:143], v[216:223], v234\n\t" \
  "ds_read_b64 v[228:229], v232 offset:800\n\t" \
  "ds_read_b128 v[188:191], v194 offset:8192\n\t" \
  "ds_read_b128 v[184:187], v194 offset:4096\n\t" \
  "v_smfmac_f32_16x16x64_bf16 v[28:31], v[120:123], v[208:215], v234\n\t" \
  "ds_read_b64 v[224:225], v232 offset:0\n\t" \
  "ds_read_b64 v[202:203], v232 offset:408\n\t" \
  "v_smfmac_f32_16x16x64_bf16 v[44:47], v[132:135], v[216:223], v234\n\t" \
  "ds_read_b64 v[204:205], v232 offset:808\n\t" \
  "ds_write_b64 v192, v[8:9] offset:32768\n\t" \
  "v_smfmac_f32_16x16x64_bf16 v[16:19], v[124:127], v[208:215], v234\n\t" \
  "s_waitcnt lgkmcnt(9)\n\t" \
  "ds_read_b64 v[226:227], v232 offset:400\n\t" \
  "v_smfmac_f32_16x16x64_bf16 v[40:43], v[116:119], v[208:215], v234\n\t" \
  "ds_read_b64 v[212:213], v232 offset:816\n\t" \
  "ds_write_b64 v192, v[4:5] offset:28672\n\t" \
  "v_cvt_pk_bf16_f32 v12, v12, v13\n\t" \
  "v_mfma_f32_16x16x32_bf16 v[168:171], v[196:199], v[180:183], 0\n\t" \
  "s_waitcnt lgkmcnt(7)\n\t" \
  "v_cvt_pk_bf16_f32 v13, v14, v15\n\t" \
  "v_mfma_f32_16x16x32_bf16 v[176:179], v[180:183], v[180:183], 0\n\t" \
  "ds_write_b64 v192, v[12:13] offset:36864\n\t" \
  "v_mfma_f32_16x16x32_bf16 v[176:179], v[184:187], v[184:187], v[176:179]\n\t" \
  "ds_read_b64 v[208:209], v232 offset:16\n\t" \
  "v_cvt_pk_bf16_f32 v20, v20, v21\n\t" \
  "v_mfma_f32_16x16x32_bf16 v[168:171], v[196:199], v[184:187], v[168:171]\n\t" \
  "v_cvt_pk_bf16_f32 v16, v16, v17\n\t" \
  "v_cvt_pk_bf16_f32 v17, v18, v19\n\t" \
  "ds_write_b64 v192, v[16:17] offset:40960\n\t" \
  "v_mfma_f32_16x16x32_bf16 v[176:179], v[188:191], v[188:191], v[176:179]\n\t" \
  "ds_read_b64 v[200:201], v232 offset:8\n\t" \
  "v_mfma_f32_16x16x32_bf16 v[168:171], v[196:199], v[188:191], v[168:171]\n\t" \
  "s_waitcnt lgkmcnt(6)\n\t" \
  "ds_read_b64 v[206:207], v232 offset:1208\n\t" \
  "ds_read_b64 v[210:211], v232 offset:416\n\t" \
  "ds_read_b64 v[214:215], v232 offset:1216\n\t" \
  "v_smfmac_f32_16x16x64_bf16 v[36:39], v[104:107], v[224:231], v234\n\t" \
  "v_mov_b32 v187, 0\n\t" \
  "v_smfmac_f32_16x16x64_bf16 v[24:27], v[108:111], v[224:231], v234\n\t" \
  "v_cndmask_b32 v176, v176, v177, s[40:41]\n\t" \
  "v_cndmask_b32 v178, v178, v179, s[40:41]\n\t" \
  "v_cndmask_b32_dpp v172, v168, v172, vcc row_ror:8 row_mask:0xf bank_mask:0xf\n\t" \
  "v_cndmask_b32 v176, v176, v178, s[42:43]\n\t" \
  "v_mov_b32 v183, 0\n\t" \
  "s_nop 0\n\t" \
  "v_cndmask_b32_dpp v173, v176, v173, vcc row_ror:8 row_mask:0xf bank_mask:0xf\n\t" \
  "v_mul_f32 v172, 0x3daaaaab, v172\n\t" \
  "v_smfmac_f32_16x16x64_bf16 v[32:35], v[136:139], v[216:223], v234\n\t" \
  ".p2align 3\n\t" \
  "s_waitcnt vmcnt(0) lgkmcnt(0)\n\t" \
  "s_barrier\n\t" \
  "ds_read_b64 v[222:223], v232 offset:17600\n\t" \
  "v_smfmac_f32_16x16x64_bf16 v[40:43], v[120:123], v[200:207], v234\n\t" \
  "ds_read_b64 v[216:217], v232 offset:16400\n\t" \
  "v_fmamk_f32 v173, v173, 0x3e2aaaab, v174\n\t" \
  "ds_read_b64 v[226:227], v232 offset:16808\n\t" \
  "v_fma_f32 v173, -v172, v172, v173\n\t" \
  "s_mov_b64 vcc, s[40:41]\n\t" \
  "ds_read_b64 v[218:219], v232 offset:16800\n\t" \
  "ds_read_b64 v[220:221], v232 offset:17200\n\t" \
  "ds_read_b64 v[224:225], v232 offset:16408\n\t" \
  "v_cvt_pk_bf16_f32 v21, v22, v23\n\t" \
  "ds_write_b64 v192, v[20:21] offset:45056\n\t" \
  "v_max_f32 v173, v174, v173\n\t" \
  "v_rsq_f32 v173, v173 div:2\n\t" \
  "s_mov_b64 exec, s[52:53]\n\t" \
  "v_mul_f32 v173, 0x08805555, v173\n\t" \
  "s_mov_b64 exec, -1\n\t" \
  "v_cvt_pk_bf16_f32 v175, v173, v173\n\t" \
  "v_and_b32 v173, 0xffff0000, v175\n\t" \
  "v_cndmask_b32_sdwa v175, v175, v173, vcc dst_sel:DWORD dst_unused:UNUSED_PAD src0_sel:WORD_1 src1_sel:DWORD\n\t" \
  "v_mul_f32 v174, -v172, v173 div:2\n\t" \
  "v_smfmac_f32_16x16x64_bf16 v[44:47], v[136:139], v[208:215], v234\n\t" \
  "s_or_b64 vcc, s[52:53], s[42:43]\n\t" \
  "ds_read_b64 v[230:231], v232 offset:17608\n\t" \
  "v_smfmac_f32_16x16x64_bf16 v[28:31], v[124:127], v[200:207], v234\n\t" \
  "ds_read_b64 v[200:201], v232 offset:16416\n\t" \
  "ds_read_b64 v[228:229], v232 offset:17208\n\t" \
  "v_cndmask_b32 v180, v175, v240, vcc\n\t" \
  "v_cvt_pk_bf16_f32 v24, v24, v25\n\t" \
  "v_cvt_pk_bf16_f32 v25, v26, v27\n\t" \
  "v_and_b32 v172, 0xffff0000, v174\n\t" \
  "v_sub_f32 v174, v174, v172\n\t" \
  "v_smfmac_f32_16x16x64_bf16 v[32:35], v[140:143], v[208:215], v234\n\t" \
  "ds_read_b64 v[202:203], v232 offset:16816\n\t" \
  "ds_write_b64 v192, v[24:25] offset:56880\n\t" \
  "v_perm_b32 v174, v172, v174, s55\n\t" \
  "v_cndmask_b32_dpp v184, v175, v240, vcc row_ror:8 row_mask:0xf bank_mask:0xf\n\t" \
  "s_mov_b64 vcc, s[52:53]\n\t" \
  "ds_read_b64_tr_b16 v[144:145], v195 offset:0\n\t" \
  "ds_read_b64_tr_b16 v[146:147], v195 offset:4096\n\t" \
  "s_waitcnt lgkmcnt(9)\n\t" \
  "v_smfmac_f32_16x16x64_bf16 v[36:39], v[108:111], v[216:223], v234\n\t" \
  "ds_read_b64 v[204:205], v232 offset:17216\n\t" \
  "ds_read_b64 v[206:207], v232 offset:17616\n\t" \
  "v_cndmask_b32 v182, v174, v240, vcc\n\t" \
  "v_cvt_pk_bf16_f32 v28, v28, v29\n\t" \
  "v_cndmask_b32_dpp v186, v174, v240, vcc row_ror:8 row_mask:0xf bank_mask:0xf\n\t" \
  "ds_read_b64_tr_b16 v[148:149], v195 offset:2048\n\t" \
  "s_orn2_b64 vcc, s[52:53], s[42:43]\n\t" \
  "v_cndmask_b32 v181, v175, v240, vcc\n\t" \
  "v_cndmask_b32_dpp v185, v175, v240, vcc row_ror:8 row_mask:0xf bank_mask:0xf\n\t" \
  "v_cvt_pk_bf16_f32 v29, v30, v31\n\t" \
  "s_waitcnt lgkmcnt(1)\n\t" \
  "v_smfmac_f32_16x16x64_bf16 v[44:47], v[140:143], v[200:207], v234\n\t" \
  "ds_read_b64_tr_b16 v[150:151], v195 offset:6144\n\t" \
  "v_smfmac_f32_16x16x64_bf16 v[40:43], v[124:127], v[224:231], v234\n\t" \
  "ds_write_b64 v192, v[28:29] offset:60976\n\t" \
  "v_mfma_f32_16x16x16_bf16 v[4:7], v[196:197], v[186:187], 0\n\t" \
  "ds_read_b64_tr_b16 v[154:155], v195 offset:10240\n\t" \
  "ds_read_b64_tr_b16 v[152:153], v195 offset:8192\n\t" \
  "v_mfma_f32_16x16x16_bf16 v[0:3], v[196:197], v[182:183], 0\n\t" \
  "v_cvt_pk_bf16_f32 v32, v32, v33\n\t" \
  "v_cvt_pk_bf16_f32 v33, v34, v35\n\t" \
  "v_mfma_f32_16x16x16_bf16 v[188:191], v[156:157], v[180:181], v[0:3]\n\t" \
  "ds_write_b64 v192, v[32:33] offset:65072\n\t" \
  "v_mfma_f32_16x16x16_bf16 v[176:179], v[158:159], v[180:181], v[0:3]\n\t" \
  "v_cvt_pk_bf16_f32 v36, v36, v37\n\t" \
  "v_cvt_pk_bf16_f32 v44, v44, v45\n\t" \
  "v_cvt_pk_bf16_f32 v37, v38, v39\n\t" \
  "v_cvt_pk_bf16_f32 v45, v46, v47\n\t" \
  "s_nop 0\n\t" \
  "v_mfma_f32_16x16x16_bf16 v[168:171], v[160:161], v[180:181], v[0:3]\n\t" \
  "ds_write_b64 v193, v[36:37] offset:0\n\t" \
  "v_cvt_pk_bf16_f32 v40, v40, v41\n\t" \
  "v_cvt_pk_bf16_f32 v41, v42, v43\n\t" \
  "v_cvt_pk_bf16_f32 v96, v188, v189\n\t" \
  "v_mfma_f32_16x16x16_bf16 v[172:175], v[162:163], v[180:181], v[0:3]\n\t" \
  "ds_write_b64 v193, v[40:41] offset:4096\n\t" \
  "v_cvt_pk_bf16_f32 v98, v176, v177\n\t" \
  "v_cvt_pk_bf16_f32 v97, v190, v191\n\t" \
  "v_mfma_f32_16x16x16_bf16 v[188:191], v[164:165], v[180:181], v[0:3]\n\t" \
  "ds_write_b64 v193, v[44:45] offset:8192\n\t" \
  "v_cvt_pk_bf16_f32 v99, v178, v179\n\t" \
  "v_mfma_f32_16x16x16_bf16 v[176:179], v[166:167], v[180:181], v[0:3]\n\t" \
  "v_cvt_pk_bf16_f32 v112, v168, v169\n\t" \
  "v_cvt_pk_bf16_f32 v113, v170, v171\n\t" \
  "s_nop 2\n\t" \
  "v_cvt_pk_bf16_f32 v129, v190, v191\n\t" \
  "v_cvt_pk_bf16_f32 v128, v188, v189\n\t" \
  "s_nop 0\n\t" \
  "v_cvt_pk_bf16_f32 v130, v176, v177\n\t" \
  "v_cvt_pk_bf16_f32 v131, v178, v179\n\t" \
  ".p2align 3\n\t" \
  "s_waitcnt vmcnt(0) lgkmcnt(0)\n\t" \
  "s_barrier\n\t" \
  "ds_read_b128 v[104:107], v194 offset:24576\n\t" \
  "v_cvt_pk_bf16_f32 v115, v174, v175\n\t" \
  "ds_read_b64_tr_b16 v[210:211], v248 offset:44072\n\t" \
  "ds_read_b128 v[120:123], v194 offset:28672\n\t" \
  "ds_read_b64_tr_b16 v[208:209], v248 offset:43048\n\t" \
  "v_cvt_pk_bf16_f32 v114, v172, v173\n\t" \
  "v_mfma_f32_16x16x16_bf16 v[172:175], v[146:147], v[184:185], v[4:7]\n\t" \
  "ds_read_b64_tr_b16 v[214:215], v248 offset:47144\n\t" \
  "v_mfma_f32_16x16x16_bf16 v[168:171], v[144:145], v[184:185], v[4:7]\n\t" \
  "ds_read_b64_tr_b16 v[216:217], v248 offset:45096\n\t" \
  "s_mov_b32 m0, s44\n\t" \
  "s_mov_b32 exec_hi, 0xffff\n\t" \
  "buffer_load_dwordx4 v252, s[16:19], s33 offen lds\n\t" \
  "s_mov_b32 exec_hi, -1\n\t" \
  "ds_read_b64_tr_b16 v[212:213], v248 offset:46120\n\t" \
  "ds_read_b64_tr_b16 v[218:219], v248 offset:48168\n\t" \
  "s_add_u32 s33, s33, s32\n\t" \
  "v_mfma_f32_16x16x16_bf16 v[188:191], v[148:149], v[184:185], v[4:7]\n\t" \
  "ds_read_b64_tr_b16 v[160:161], v248 offset:43560\n\t" \
  "v_cvt_pk_bf16_f32 v100, v168, v169\n\t" \
  "ds_read_b64_tr_b16 v[164:165], v248 offset:46632\n\t" \
  "v_mfma_f32_16x16x16_bf16 v[176:179], v[150:151], v[184:185], v[4:7]\n\t" \
  "ds_read_b64_tr_b16 v[162:163], v248 offset:44584\n\t" \
  "v_cvt_pk_bf16_f32 v102, v172, v173\n\t" \
  "ds_read_b64_tr_b16 v[166:167], v248 offset:47656\n\t" \
  "v_cvt_pk_bf16_f32 v103, v174, v175\n\t" \
  "v_cvt_pk_bf16_f32 v101, v170, v171\n\t" \
  "v_mfma_f32_16x16x16_bf16 v[168:171], v[152:153], v[184:185], v[4:7]\n\t" \
  "ds_read_b64_tr_b16 v[232:233], v248 offset:45608\n\t" \
  "ds_read_b64_tr_b16 v[234:235], v248 offset:48680\n\t" \
  "v_cvt_pk_bf16_f32 v117, v190, v191\n\t" \
  "v_mfma_f32_16x16x16_bf16 v[172:175], v[154:155], v[184:185], v[4:7]\n\t" \
  "ds_read_b64_tr_b16 v[26:27], v195 offset:28672\n\t" \
  "ds_read_b64_tr_b16 v[24:25], v195 offset:24576\n\t" \
  "v_cvt_pk_bf16_f32 v118, v176, v177\n\t" \
  "v_cvt_pk_bf16_f32 v116, v188, v189\n\t" \
  "v_cvt_pk_bf16_f32 v119, v178, v179\n\t" \
  "s_waitcnt lgkmcnt(12)\n\t" \
  "v_mfma_f32_16x16x32_bf16 v[144:147], v[208:211], v[96:99], 0\n\t" \
  "ds_read_b64_tr_b16 v[28:29], v195 offset:26624\n\t" \
  "ds_read_b128 v[136:139], v194 offset:32768\n\t" \
  "v_add_u32 v194, s54, v194\n\t" \
  "ds_read_b128 v[108:111], v194 offset:49152\n\t" \
  "ds_read_b64_tr_b16 v[30:31], v195 offset:30720\n\t" \
  "v_cvt_pk_bf16_f32 v133, v170, v171\n\t" \
  "v_cvt_pk_bf16_f32 v132, v168, v169\n\t" \
  "s_waitcnt lgkmcnt(13)\n\t" \
  "v_mfma_f32_16x16x32_bf16 v[144:147], v[212:215], v[112:115], v[144:147]\n\t" \
  "ds_read_b128 v[124:127], v194 offset:53248\n\t" \
  "ds_read_b64_tr_b16 v[34:35], v195 offset:34816\n\t" \
  "s_waitcnt lgkmcnt(4)\n\t" \
  "ds_read_b64_tr_b16 v[32:33], v195 offset:32768\n\t" \
  "ds_read_b128 v[140:143], v194 offset:57344\n\t" \
  "v_cvt_pk_bf16_f32 v134, v172, v173\n\t" \
  "v_cvt_pk_bf16_f32 v135, v174, v175\n\t" \
  "v_mfma_f32_16x16x32_bf16 v[152:155], v[208:211], v[100:103], 0\n\t" \
  "v_add_u32 v195, s54, v195\n\t" \
  "ds_read_b64_tr_b16 v[38:39], v195 offset:53248\n\t" \
  "v_mfma_f32_16x16x32_bf16 v[144:147], v[216:219], v[128:131], v[144:147]\n\t" \
  "s_add_u32 m0, s44, s35\n\t" \
  "ds_read_b64_tr_b16 v[36:37], v195 offset:49152\n\t" \
  "ds_read_b64_tr_b16 v[40:41], v195 offset:51200\n\t" \
  "s_mov_b32 exec_hi, 0xffff\n\t" \
  "buffer_load_dwordx4 v252, s[16:19], s34 offen lds\n\t" \
  "s_mov_b32 exec_hi, -1\n\t" \
  "v_mfma_f32_16x16x32_bf16 v[152:155], v[212:215], v[116:119], v[152:155]\n\t" \
  "s_add_u32 s34, s34, s32\n\t" \
  "ds_read_b64_tr_b16 v[46:47], v195 offset:59392\n\t" \
  "v_mfma_f32_16x16x32_bf16 v[152:155], v[216:219], v[132:135], v[152:155]\n\t" \
  "ds_read_b64_tr_b16 v[42:43], v195 offset:55296\n\t" \
  "v_mfma_f32_16x16x32_bf16 v[8:11], v[104:107], v[104:107], 0\n\t" \
  "ds_read_b64_tr_b16 v[44:45], v195 offset:57344\n\t" \
  "s_mov_b32 m0, s45\n\t" \
  "s_mov_b32 exec_hi, 0xffff\n\t" \
  "buffer_load_dwordx4 v252, s[16:19], s33 offen lds\n\t" \
  "s_mov_b32 exec_hi, -1\n\t" \
  "v_cvt_pknorm_i16_f32 v222, |v144|, |v145|\n\t" \
  "v_cvt_pknorm_i16_f32 v220, |v146|, |v147|\n\t" \
  "v_mfma_f32_16x16x32_bf16 v[8:11], v[120:123], v[120:123], v[8:11]\n\t" \
  "v_lshlrev_b32_sdwa v221, 2, v220 dst_sel:DWORD dst_unused:UNUSED_PAD src0_sel:DWORD src1_sel:BYTE_3\n\t" \
  "v_mfma_f32_16x16x32_bf16 v[12:15], v[196:199], v[104:107], 0\n\t" \
  "s_add_u32 s33, s33, s32\n\t" \
  "v_lshlrev_b32_sdwa v220, 2, v220 dst_sel:DWORD dst_unused:UNUSED_PAD src0_sel:DWORD src1_sel:BYTE_1\n\t" \
  "v_mfma_f32_16x16x32_bf16 v[12:15], v[196:199], v[120:123], v[12:15]\n\t" \
  "ds_read_b32 v220, v220 offset:10016\n\t" \
  "v_lshlrev_b32_sdwa v223, 2, v222 dst_sel:DWORD dst_unused:UNUSED_PAD src0_sel:DWORD src1_sel:BYTE_3\n\t" \
  "ds_read_b32 v223, v223 offset:10016\n\t" \
  "ds_read_b32 v221, v221 offset:10016\n\t" \
  "s_add_u32 m0, s45, s35\n\t" \
  "v_lshlrev_b32_sdwa v227, 1, v153 dst_sel:DWORD dst_unused:UNUSED_PAD src0_sel:DWORD src1_sel:WORD_1\n\t" \
  "v_lshlrev_b32_sdwa v222, 2, v222 dst_sel:DWORD dst_unused:UNUSED_PAD src0_sel:DWORD src1_sel:BYTE_1\n\t" \
  "v_mfma_f32_16x16x32_bf16 v[12:15], v[196:199], v[136:139], v[12:15]\n\t" \
  "s_waitcnt lgkmcnt(14)\n\t" \
  "v_mfma_f32_16x16x32_bf16 v[8:11], v[136:139], v[136:139], v[8:11]\n\t" \
  "v_lshlrev_b32_sdwa v224, 1, v154 dst_sel:DWORD dst_unused:UNUSED_PAD src0_sel:DWORD src1_sel:WORD_1\n\t" \
  "v_lshlrev_b32_sdwa v225, 1, v155 dst_sel:DWORD dst_unused:UNUSED_PAD src0_sel:DWORD src1_sel:WORD_1\n\t" \
  "v_mfma_f32_16x16x32_bf16 v[16:19], v[108:111], v[108:111], 0\n\t" \
  "ds_read_b32 v222, v222 offset:10016\n\t" \
  "v_mfma_f32_16x16x32_bf16 v[20:23], v[196:199], v[108:111], 0\n\t" \
  "ds_read_u16_d16_hi v225, v225 offset:10528\n\t" \
  "v_lshlrev_b32_sdwa v226, 1, v152 dst_sel:DWORD dst_unused:UNUSED_PAD src0_sel:DWORD src1_sel:WORD_1\n\t" \
  "s_mov_b32 exec_hi, 0xffff\n\t" \
  "buffer_load_dwordx4 v252, s[16:19], s34 offen lds\n\t" \
  "s_mov_b32 exec_hi, -1\n\t" \
  "s_waitcnt lgkmcnt(14)\n\t" \
  "v_mov_b32 v14, 0x1127c5ac\n\t" \
  "v_cndmask_b32 v10, v10, v11, s[40:41]\n\t" \
  "v_mfma_f32_16x16x32_bf16 v[16:19], v[124:127], v[124:127], v[16:19]\n\t" \
  "ds_read_u16 v224, v224 offset:10528\n\t" \
  "v_cndmask_b32 v8, v8, v9, s[40:41]\n\t" \
  "s_add_u32 s34, s34, s32\n\t" \
  "v_cndmask_b32 v13, v8, v10, s[42:43]\n\t" \
  "v_mfma_f32_16x16x32_bf16 v[20:23], v[196:199], v[124:127], v[20:23]\n\t" \
  "s_waitcnt lgkmcnt(12)\n\t" \
  "ds_read_u16 v226, v226 offset:10528\n\t" \
  "s_not_b64 vcc, s[52:53]\n\t" \
  "s_waitcnt lgkmcnt(4)\n\t" \
  "ds_read_u16_d16_hi v227, v227 offset:10528\n\t" \
  "v_mfma_f32_16x16x32_bf16 v[16:19], v[140:143], v[140:143], v[16:19]\n\t" \
  "v_fma_mix_f32 v221, |v147|, v221, v221 op_sel:[0,1,0] op_sel_hi:[0,1,1] clamp\n\t" \
  "v_fma_mix_f32 v220, |v146|, v220, v220 op_sel:[0,1,0] op_sel_hi:[0,1,1] clamp\n\t" \
  "v_mfma_f32_16x16x32_bf16 v[20:23], v[196:199], v[140:143], v[20:23]\n\t" \
  "s_waitcnt lgkmcnt(2)\n\t" \
  "s_nop 3\n\t" \
  "v_cndmask_b32 v18, v18, v19, s[40:41]\n\t" \
  "v_fma_mix_f32 v223, |v145|, v223, v223 op_sel:[0,1,0] op_sel_hi:[0,1,1] clamp\n\t" \
  "v_mfma_f32_16x16x32_bf16 v[156:159], v[160:163], v[100:103], 0\n\t" \
  "v_cndmask_b32 v16, v16, v17, s[40:41]\n\t" \
  "v_fma_mix_f32 v222, |v144|, v222, v222 op_sel:[0,1,0] op_sel_hi:[0,1,1] clamp\n\t" \
  "v_mfma_f32_16x16x32_bf16 v[148:151], v[160:163], v[96:99], 0\n\t" \
  "s_waitcnt lgkmcnt(0)\n\t" \
  "v_or_b32 v155, v224, v225\n\t" \
  "v_or_b32 v154, v226, v227\n\t" \
  "v_cndmask_b32 v16, v16, v18, s[42:43]\n\t" \
  "v_cndmask_b32_dpp v12, v20, v12, vcc row_ror:8 row_mask:0xf bank_mask:0xf\n\t" \
  "v_mul_f32 v12, 0x3daaaaab, v12\n\t" \
  "v_mfma_f32_16x16x32_bf16 v[148:151], v[164:167], v[112:115], v[148:151]\n\t" \
  "v_cndmask_b32_dpp v13, v16, v13, vcc row_ror:8 row_mask:0xf bank_mask:0xf\n\t" \
  "v_fmamk_f32 v13, v13, 0x3e2aaaab, v14\n\t" \
  "v_fma_f32 v13, -v12, v12, v13\n\t" \
  "v_max_f32 v13, v14, v13\n\t" \
  "v_rsq_f32 v13, v13 div:2\n\t" \
  "v_mfma_f32_16x16x32_bf16 v[156:159], v[164:167], v[116:119], v[156:159]\n\t" \
  "s_mov_b64 vcc, s[40:41]\n\t" \
  "s_mov_b64 exec, s[52:53]\n\t" \
  "v_mul_f32 v13, 0x08805555, v13\n\t" \
  "s_mov_b64 exec, -1\n\t" \
  "v_cvt_pk_bf16_f32 v15, v13, v13\n\t" \
  "v_and_b32 v13, 0xffff0000, v15\n\t" \
  "v_mul_f32 v14, -v12, v13 div:2\n\t" \
  "v_cndmask_b32_sdwa v15, v15, v13, vcc dst_sel:DWORD dst_unused:UNUSED_PAD src0_sel:WORD_1 src1_sel:DWORD\n\t" \
  "s_or_b64 vcc, s[52:53], s[42:43]\n\t" \
  "v_cndmask_b32 v180, v15, v240, vcc\n\t" \
  "v_and_b32 v12, 0xffff0000, v14\n\t" \
  "v_mfma_f32_16x16x32_bf16 v[148:151], v[232:235], v[128:131], v[148:151]\n\t" \
  "v_sub_f32 v14, v14, v12\n\t" \
  "v_perm_b32 v14, v12, v14, s55\n\t" \
  "v_mfma_f32_16x16x32_bf16 v[156:159], v[232:235], v[132:135], v[156:159]\n\t" \
  "v_cndmask_b32_dpp v184, v15, v240, vcc row_ror:8 row_mask:0xf bank_mask:0xf\n\t" \
  "s_mov_b64 vcc, s[52:53]\n\t" \
  "v_cndmask_b32_dpp v186, v14, v240, vcc row_ror:8 row_mask:0xf bank_mask:0xf\n\t" \
  "v_cndmask_b32 v182, v14, v240, vcc\n\t" \
  "s_orn2_b64 vcc, s[52:53], s[42:43]\n\t" \
  "v_mfma_f32_16x16x16_bf16 v[4:7], v[196:197], v[186:187], 0\n\t" \
  "v_cndmask_b32 v181, v15, v240, vcc\n\t" \
  "v_cndmask_b32_dpp v185, v15, v240, vcc row_ror:8 row_mask:0xf bank_mask:0xf\n\t" \
  "v_mfma_f32_16x16x16_bf16 v[0:3], v[196:197], v[182:183], 0\n\t" \
  "v_lshlrev_b32_sdwa v235, 1, v159 dst_sel:DWORD dst_unused:UNUSED_PAD src0_sel:DWORD src1_sel:WORD_1\n\t" \
  "v_fma_f32 v144, |v144|, v222, v144 mul:4\n\t" \
  "v_mfma_f32_16x16x16_bf16 v[188:191], v[24:25], v[180:181], v[0:3]\n\t" \
  "v_fma_f32 v146, |v146|, v220, v146 mul:4\n\t" \
  "v_mfma_f32_16x16x16_bf16 v[176:179], v[26:27], v[180:181], v[0:3]\n\t" \
  "v_fma_f32 v147, |v147|, v221, v147 mul:4\n\t" \
  "v_cvt_pknorm_i16_f32 v228, |v148|, |v149|\n\t" \
  "s_mov_b64 vcc, 0\n\t" \
  "s_nop 2\n\t" \
  "v_cvt_pk_bf16_f32 v105, v190, v191\n\t" \
  "v_mfma_f32_16x16x16_bf16 v[204:207], v[38:39], v[184:185], v[4:7]\n\t" \
  "v_lshlrev_b32_sdwa v229, 2, v228 dst_sel:DWORD dst_unused:UNUSED_PAD src0_sel:DWORD src1_sel:BYTE_3\n\t" \
  "v_cvt_pk_bf16_f32 v107, v178, v179\n\t" \
  "v_mfma_f32_16x16x16_bf16 v[200:203], v[36:37], v[184:185], v[4:7]\n\t" \
  "v_cvt_pk_bf16_f32 v104, v188, v189\n\t" \
  "v_cvt_pk_bf16_f32 v106, v176, v177\n\t" \
  "v_mfma_f32_16x16x16_bf16 v[196:199], v[28:29], v[180:181], v[0:3]\n\t" \
  "v_fma_f32 v145, |v145|, v223, v145 mul:4\n\t" \
  "v_cvt_pk_bf16_f32 v147, v146, v147\n\t" \
  "v_cvt_pk_bf16_f32 v111, v206, v207\n\t" \
  "s_nop 1\n\t" \
  "v_cvt_pk_bf16_f32 v108, v200, v201\n\t" \
  "v_mfma_f32_16x16x32_bf16 v[160:163], v[208:211], v[104:107], 0\n\t" \
  "v_mfma_f32_16x16x16_bf16 v[188:191], v[30:31], v[180:181], v[0:3]\n\t" \
  "v_cvt_pk_bf16_f32 v109, v202, v203\n\t" \
  "v_cvt_pk_bf16_f32 v120, v196, v197\n\t" \
  "v_cvt_pk_bf16_f32 v110, v204, v205\n\t" \
  "s_nop 4\n\t" \
  "v_cvt_pk_bf16_f32 v123, v190, v191\n\t" \
  "v_mfma_f32_16x16x16_bf16 v[200:203], v[42:43], v[184:185], v[4:7]\n\t" \
  "v_cvt_pknorm_i16_f32 v230, |v150|, |v151|\n\t" \
  "v_mfma_f32_16x16x16_bf16 v[176:179], v[40:41], v[184:185], v[4:7]\n\t" \
  "v_cvt_pk_bf16_f32 v122, v188, v189\n\t" \
  "v_cvt_pk_bf16_f32 v121, v198, v199\n\t" \
  "v_cvt_pk_bf16_f32 v146, v144, v145\n\t" \
  "v_mfma_f32_16x16x32_bf16 v[168:171], v[208:211], v[108:111], 0\n\t" \
  "ds_read_b64_tr_b16 v[208:209], v248 offset:43560\n\t" \
  "ds_read_b64_tr_b16 v[210:211], v248 offset:44584\n\t" \
  "s_nop 1\n\t" \
  "v_cvt_pk_bf16_f32 v124, v176, v177\n\t" \
  "v_cvt_pk_bf16_f32 v125, v178, v179\n\t" \
  "v_mfma_f32_16x16x16_bf16 v[204:207], v[32:33], v[180:181], v[0:3]\n\t" \
  "v_cvt_pk_bf16_f32 v127, v202, v203\n\t" \
  "v_cvt_pk_bf16_f32 v126, v200, v201\n\t" \
  "v_mfma_f32_16x16x16_bf16 v[196:199], v[34:35], v[180:181], v[0:3]\n\t" \
  "v_lshlrev_b32_sdwa v228, 2, v228 dst_sel:DWORD dst_unused:UNUSED_PAD src0_sel:DWORD src1_sel:BYTE_1\n\t" \
  "v_lshlrev_b32_sdwa v231, 2, v230 dst_sel:DWORD dst_unused:UNUSED_PAD src0_sel:DWORD src1_sel:BYTE_3\n\t" \
  "v_lshlrev_b32_sdwa v230, 2, v230 dst_sel:DWORD dst_unused:UNUSED_PAD src0_sel:DWORD src1_sel:BYTE_1\n\t" \
  "v_mfma_f32_16x16x32_bf16 v[160:163], v[212:215], v[120:123], v[160:163]\n\t" \
  "ds_read_b32 v229, v229 offset:10016\n\t" \
  "v_cvt_pk_bf16_f32 v136, v204, v205\n\t" \
  "ds_read_b32 v228, v228 offset:10016\n\t" \
  "v_cvt_pk_bf16_f32 v137, v206, v207\n\t" \
  "v_mfma_f32_16x16x16_bf16 v[188:191], v[44:45], v[184:185], v[4:7]\n\t" \
  "v_cvt_pk_bf16_f32 v139, v198, v199\n\t" \
  "v_cvt_pk_bf16_f32 v138, v196, v197\n\t" \
  "v_mfma_f32_16x16x16_bf16 v[176:179], v[46:47], v[184:185], v[4:7]\n\t" \
  "s_nop 4\n\t" \
  "v_cvt_pk_bf16_f32 v140, v188, v189\n\t" \
  "v_lshlrev_b32_sdwa v233, 1, v157 dst_sel:DWORD dst_unused:UNUSED_PAD src0_sel:DWORD src1_sel:WORD_1\n\t" \
  "v_mfma_f32_16x16x32_bf16 v[168:171], v[212:215], v[124:127], v[168:171]\n\t" \
  "ds_read_b64_tr_b16 v[212:213], v248 offset:46632\n\t" \
  "ds_read_b64_tr_b16 v[214:215], v248 offset:47656\n\t" \
  "v_cvt_pk_bf16_f32 v142, v176, v177\n\t" \
  "v_cvt_pk_bf16_f32 v141, v190, v191\n\t" \
  "v_cvt_pk_bf16_f32 v143, v178, v179\n\t" \
  "v_mfma_f32_16x16x32_bf16 v[160:163], v[216:219], v[136:139], v[160:163]\n\t" \
  "s_waitcnt lgkmcnt(4)\n\t" \
  "v_lshlrev_b32_sdwa v232, 1, v156 dst_sel:DWORD dst_unused:UNUSED_PAD src0_sel:DWORD src1_sel:WORD_1\n\t" \
  "v_mfma_f32_16x16x32_bf16 v[168:171], v[216:219], v[140:143], v[168:171]\n\t" \
  "ds_read_b64_tr_b16 v[216:217], v248 offset:45608\n\t" \
  "ds_read_b64_tr_b16 v[218:219], v248 offset:48680\n\t" \
  "s_waitcnt vmcnt(2)\n\t" \
  "s_mov_b64 exec, 3\n\t" \
  "ds_add_rtn_u32 v246, v251, v250 offset:47136\n\t" \
  "s_mov_b64 exec, -1\n\t" \
  "v_mfma_f32_16x16x32_bf16 v[164:167], v[208:211], v[104:107], 0\n\t" \
  "ds_read_b32 v231, v231 offset:10016\n\t" \
  "v_lshlrev_b32_sdwa v234, 1, v158 dst_sel:DWORD dst_unused:UNUSED_PAD src0_sel:DWORD src1_sel:WORD_1\n\t" \
  "v_mfma_f32_16x16x32_bf16 v[172:175], v[208:211], v[108:111], 0\n\t" \
  "ds_read_b64_tr_b16 v[210:211], v248 offset:50216\n\t" \
  "ds_read_b64_tr_b16 v[208:209], v248 offset:49192\n\t" \
  "s_waitcnt lgkmcnt(6)\n\t" \
  "v_mfma_f32_16x16x32_bf16 v[172:175], v[212:215], v[124:127], v[172:175]\n\t" \
  "ds_read_u16_d16_hi v233, v233 offset:10528\n\t" \
  "v_mfma_f32_16x16x32_bf16 v[164:167], v[212:215], v[120:123], v[164:167]\n\t" \
  "ds_read_b32 v230, v230 offset:10016\n\t" \
  "ds_read_b64_tr_b16 v[212:213], v248 offset:52264\n\t" \
  "s_waitcnt lgkmcnt(7)\n\t" \
  "v_mfma_f32_16x16x32_bf16 v[172:175], v[216:219], v[140:143], v[172:175]\n\t" \
  "ds_read_b64_tr_b16 v[214:215], v248 offset:53288\n\t" \
  "v_mfma_f32_16x16x32_bf16 v[164:167], v[216:219], v[136:139], v[164:167]\n\t" \
  "ds_read_b64_tr_b16 v[216:217], v248 offset:51240\n\t" \
  "v_cvt_pknorm_i16_f32 v222, |v160|, |v161|\n\t" \
  "v_mfma_f32_4x4x4_16b_bf16 v[4:7], v[242:243], v[78:79], 0\n\t" \
  "ds_read_b64_tr_b16 v[218:219], v248 offset:54312\n\t" \
  "v_mfma_f32_4x4x4_16b_bf16 v[0:3], v[242:243], v[76:77], 0\n\t" \
  "v_cvt_pknorm_i16_f32 v220, |v162|, |v163|\n\t" \
  "s_setprio 0\n\t" \
  "v_mfma_f32_4x4x4_16b_bf16 v[12:15], v[242:243], v[86:87], 0\n\t" \
  "v_mfma_f32_4x4x4_16b_bf16 v[8:11], v[242:243], v[84:85], 0\n\t" \
  "v_lshlrev_b32_sdwa v221, 2, v220 dst_sel:DWORD dst_unused:UNUSED_PAD src0_sel:DWORD src1_sel:BYTE_3\n\t" \
  "ds_read_u16_d16_hi v235, v235 offset:10528\n\t" \
  "v_lshlrev_b32_sdwa v220, 2, v220 dst_sel:DWORD dst_unused:UNUSED_PAD src0_sel:DWORD src1_sel:BYTE_1\n\t" \
  "v_mfma_f32_4x4x4_16b_bf16 v[20:23], v[242:243], v[94:95], 0\n\t" \
  "v_mfma_f32_4x4x4_16b_bf16 v[36:39], v[242:243], v[82:83], 0\n\t" \
  "ds_read_u16 v232, v232 offset:10528\n\t" \
  "ds_read_u16 v234, v234 offset:10528\n\t" \
  "v_mfma_f32_4x4x4_16b_bf16 v[16:19], v[242:243], v[92:93], 0\n\t" \
  "v_lshlrev_b32_sdwa v225, 1, v171 dst_sel:DWORD dst_unused:UNUSED_PAD src0_sel:DWORD src1_sel:WORD_1\n\t" \
  "v_lshlrev_b32_sdwa v223, 2, v222 dst_sel:DWORD dst_unused:UNUSED_PAD src0_sel:DWORD src1_sel:BYTE_3\n\t" \
  "v_lshlrev_b32_sdwa v222, 2, v222 dst_sel:DWORD dst_unused:UNUSED_PAD src0_sel:DWORD src1_sel:BYTE_1\n\t" \
  "v_mfma_f32_4x4x4_16b_bf16 v[32:35], v[242:243], v[80:81], 0\n\t" \
  "s_setprio 0\n\t" \
  "v_mfma_f32_4x4x4_16b_bf16 v[28:31], v[242:243], v[74:75], 0\n\t" \
  "v_lshlrev_b32_sdwa v224, 1, v170 dst_sel:DWORD dst_unused:UNUSED_PAD src0_sel:DWORD src1_sel:WORD_1\n\t" \
  "v_mfma_f32_4x4x4_16b_bf16 v[24:27], v[242:243], v[72:73], 0\n\t" \
  "ds_read_b32 v221, v221 offset:10016\n\t" \
  "v_lshlrev_b32_sdwa v227, 1, v169 dst_sel:DWORD dst_unused:UNUSED_PAD src0_sel:DWORD src1_sel:WORD_1\n\t" \
  "s_waitcnt lgkmcnt(8)\n\t" \
  "v_lshlrev_b32_sdwa v226, 1, v168 dst_sel:DWORD dst_unused:UNUSED_PAD src0_sel:DWORD src1_sel:WORD_1\n\t" \
  "v_mfma_f32_4x4x4_16b_bf16 v[40:43], v[242:243], v[88:89], 0\n\t" \
  "v_mfma_f32_4x4x4_16b_bf16 v[44:47], v[242:243], v[90:91], 0\n\t" \
  "ds_read_b32 v220, v220 offset:10016\n\t" \
  "v_fma_mix_f32 v229, |v149|, v229, v229 op_sel:[0,1,0] op_sel_hi:[0,1,1] clamp\n\t" \
  "s_setprio 0\n\t" \
  "v_fma_mix_f32 v230, |v150|, v230, v230 op_sel:[0,1,0] op_sel_hi:[0,1,1] clamp\n\t" \
  "ds_read_b32 v223, v223 offset:10016\n\t" \
  "s_setprio 0\n\t" \
  "v_fma_mix_f32 v228, |v148|, v228, v228 op_sel:[0,1,0] op_sel_hi:[0,1,1] clamp\n\t" \
  "v_mfma_f32_4x4x4_16b_bf16 v[84:87], v[242:243], v[62:63], 0\n\t" \
  "v_fma_f32 v149, |v149|, v229, v149 mul:4\n\t" \
  "s_waitcnt lgkmcnt(3)\n\t" \
  "ds_read_b32 v222, v222 offset:10016\n\t" \
  "v_mfma_f32_4x4x4_16b_bf16 v[72:75], v[242:243], v[52:53], 0\n\t" \
  "v_mfma_f32_4x4x4_16b_bf16 v[80:83], v[242:243], v[60:61], 0\n\t" \
  "v_fma_mix_f32 v231, |v151|, v231, v231 op_sel:[0,1,0] op_sel_hi:[0,1,1] clamp\n\t" \
  "ds_read_u16_d16_hi v225, v225 offset:10528\n\t" \
  "v_or_b32 v157, v234, v235\n\t" \
  "v_mfma_f32_4x4x4_16b_bf16 v[76:79], v[242:243], v[54:55], 0\n\t" \
  "v_fma_f32 v151, |v151|, v231, v151 mul:4\n\t" \
  "v_mfma_f32_4x4x4_16b_bf16 v[52:55], v[242:243], v[50:51], 0\n\t" \
  "v_or_b32 v156, v232, v233\n\t" \
  "v_mfma_f32_4x4x4_16b_bf16 v[48:51], v[242:243], v[48:49], 0\n\t" \
  "v_mfma_f32_4x4x4_16b_bf16 v[60:63], v[242:243], v[58:59], 0\n\t" \
  "ds_read_u16 v224, v224 offset:10528\n\t" \
  "v_fma_f32 v148, |v148|, v228, v148 mul:4\n\t" \
  "ds_read_u16_d16_hi v227, v227 offset:10528\n\t" \
  "v_mfma_f32_4x4x4_16b_bf16 v[88:91], v[242:243], v[68:69], 0\n\t" \
  "v_mfma_f32_4x4x4_16b_bf16 v[56:59], v[242:243], v[56:57], 0\n\t" \
  "v_mfma_f32_4x4x4_16b_bf16 v[92:95], v[242:243], v[70:71], 0\n\t" \
  "v_fma_f32 v150, |v150|, v230, v150 mul:4\n\t" \
  "v_mfma_f32_4x4x4_16b_bf16 v[68:71], v[242:243], v[66:67], 0\n\t" \
  "v_mfma_f32_4x4x4_16b_bf16 v[64:67], v[242:243], v[64:65], 0\n\t" \
  "ds_read_u16 v226, v226 offset:10528\n\t" \
  "v_cvt_pknorm_i16_f32 v230, |v166|, |v167|\n\t" \
  "v_lshlrev_b32_sdwa v231, 2, v230 dst_sel:DWORD dst_unused:UNUSED_PAD src0_sel:DWORD src1_sel:BYTE_3\n\t" \
  "v_lshlrev_b32_sdwa v230, 2, v230 dst_sel:DWORD dst_unused:UNUSED_PAD src0_sel:DWORD src1_sel:BYTE_1\n\t" \
  "v_mfma_f32_16x16x32_bf16 v[192:195], v[208:211], v[104:107], 0\n\t" \
  "v_cvt_pk_bf16_f32 v148, v148, v149\n\t" \
  "v_cvt_pk_bf16_f32 v149, v150, v151\n\t" \
  "v_mfma_f32_16x16x32_bf16 v[176:179], v[208:211], v[96:99], 0\n\t" \
  "v_cvt_pknorm_i16_f32 v228, |v164|, |v165|\n\t" \
  "v_mfma_f32_16x16x32_bf16 v[184:187], v[208:211], v[100:103], 0\n\t" \
  "s_add_u32 s27, s27, s26\n\t" \
  "v_lshlrev_b32_sdwa v229, 2, v228 dst_sel:DWORD dst_unused:UNUSED_PAD src0_sel:DWORD src1_sel:BYTE_3\n\t" \
  "v_lshlrev_b32_sdwa v228, 2, v228 dst_sel:DWORD dst_unused:UNUSED_PAD src0_sel:DWORD src1_sel:BYTE_1\n\t" \
  "v_mfma_f32_16x16x32_bf16 v[200:203], v[208:211], v[108:111], 0\n\t" \
  "v_lshlrev_b32_sdwa v233, 1, v173 dst_sel:DWORD dst_unused:UNUSED_PAD src0_sel:DWORD src1_sel:WORD_1\n\t" \
  "v_lshlrev_b32_sdwa v232, 1, v172 dst_sel:DWORD dst_unused:UNUSED_PAD src0_sel:DWORD src1_sel:WORD_1\n\t" \
  "ds_read_b64_tr_b16 v[208:209], v248 offset:49704\n\t" \
  "v_mfma_f32_16x16x32_bf16 v[176:179], v[212:215], v[112:115], v[176:179]\n\t" \
  "v_lshlrev_b32_sdwa v234, 1, v174 dst_sel:DWORD dst_unused:UNUSED_PAD src0_sel:DWORD src1_sel:WORD_1\n\t" \
  "v_lshlrev_b32_sdwa v235, 1, v175 dst_sel:DWORD dst_unused:UNUSED_PAD src0_sel:DWORD src1_sel:WORD_1\n\t" \
  "ds_read_b64_tr_b16 v[210:211], v248 offset:50728\n\t" \
  "s_setprio 3\n\t" \
  "ds_read_b32 v229, v229 offset:10016\n\t" \
  "v_mfma_f32_16x16x32_bf16 v[184:187], v[212:215], v[116:119], v[184:187]\n\t" \
  "s_waitcnt lgkmcnt(7)\n\t" \
  "s_setprio 0\n\t" \
  "v_fma_mix_f32 v220, |v162|, v220, v220 op_sel:[0,1,0] op_sel_hi:[0,1,1] clamp\n\t" \
  "s_mov_b64 exec, s[30:31]\n\t" \
  "buffer_load_dword v237, v254, s[8:11], s27 offen\n\t" \
  "v_mfma_f32_16x16x32_bf16 v[192:195], v[212:215], v[120:123], v[192:195]\n\t" \
  "s_mov_b64 exec, -1\n\t" \
  "v_fma_mix_f32 v223, |v161|, v223, v223 op_sel:[0,1,0] op_sel_hi:[0,1,1] clamp\n\t" \
  "v_fma_mix_f32 v221, |v163|, v221, v221 op_sel:[0,1,0] op_sel_hi:[0,1,1] clamp\n\t" \
  "ds_read_b32 v231, v231 offset:10016\n\t" \
  "ds_read_b32 v228, v228 offset:10016\n\t" \
  "v_mfma_f32_16x16x32_bf16 v[200:203], v[212:215], v[124:127], v[200:203]\n\t" \
  "ds_read_b64_tr_b16 v[212:213], v248 offset:52776\n\t" \
  "v_fma_mix_f32 v222, |v160|, v222, v222 op_sel:[0,1,0] op_sel_hi:[0,1,1] clamp\n\t" \
  "s_waitcnt lgkmcnt(6)\n\t" \
  "ds_read_b32 v230, v230 offset:10016\n\t" \
  "ds_read_b64_tr_b16 v[214:215], v248 offset:53800\n\t" \
  "v_mfma_f32_16x16x32_bf16 v[176:179], v[216:219], v[128:131], v[176:179]\n\t" \
  "v_or_b32 v170, v226, v227\n\t" \
  "v_or_b32 v171, v224, v225\n\t" \
  "ds_read_u16 v232, v232 offset:10528\n\t" \
  "s_setprio 0\n\t" \
  "v_mfma_f32_16x16x32_bf16 v[184:187], v[216:219], v[132:135], v[184:187]\n\t" \
  "v_fma_f32 v163, |v163|, v221, v163 mul:4\n\t" \
  "v_fma_f32 v162, |v162|, v220, v162 mul:4\n\t" \
  "v_mfma_f32_16x16x32_bf16 v[192:195], v[216:219], v[136:139], v[192:195]\n\t" \
  "v_fma_f32 v161, |v161|, v223, v161 mul:4\n\t" \
  "v_fma_f32 v160, |v160|, v222, v160 mul:4\n\t" \
  "ds_read_u16_d16_hi v233, v233 offset:10528\n\t" \
  "v_mfma_f32_16x16x32_bf16 v[200:203], v[216:219], v[140:143], v[200:203]\n\t" \
  "v_cvt_pk_bf16_f32 v163, v162, v163\n\t" \
  "v_cvt_pk_bf16_f32 v162, v160, v161\n\t" \
  "s_waitcnt lgkmcnt(8)\n\t" \
  "ds_read_b64_tr_b16 v[216:217], v248 offset:51752\n\t" \
  "v_mfma_f32_16x16x32_bf16 v[180:183], v[208:211], v[96:99], 0\n\t" \
  "v_cvt_pknorm_i16_f32 v222, |v176|, |v177|\n\t" \
  "v_cvt_pknorm_i16_f32 v220, |v178|, |v179|\n\t" \
  "s_waitcnt vmcnt(1)\n\t" \
  "ds_read_b64_tr_b16 v[218:219], v248 offset:54824\n\t" \
  "v_mfma_f32_16x16x32_bf16 v[188:191], v[208:211], v[100:103], 0\n\t" \
  "v_lshlrev_b32_sdwa v221, 2, v220 dst_sel:DWORD dst_unused:UNUSED_PAD src0_sel:DWORD src1_sel:BYTE_3\n\t" \
  "v_lshlrev_b32_sdwa v220, 2, v220 dst_sel:DWORD dst_unused:UNUSED_PAD src0_sel:DWORD src1_sel:BYTE_1\n\t" \
  "s_mov_b64 exec, 3\n\t" \
  "ds_add_rtn_u32 v246, v251, v250 offset:47144\n\t" \
  "v_mfma_f32_16x16x32_bf16 v[196:199], v[208:211], v[104:107], 0\n\t" \
  "s_mov_b64 exec, -1\n\t" \
  "v_lshlrev_b32_sdwa v223, 2, v222 dst_sel:DWORD dst_unused:UNUSED_PAD src0_sel:DWORD src1_sel:BYTE_3\n\t" \
  "v_lshlrev_b32_sdwa v222, 2, v222 dst_sel:DWORD dst_unused:UNUSED_PAD src0_sel:DWORD src1_sel:BYTE_1\n\t" \
  "s_setprio 2\n\t" \
  "s_setprio 0\n\t" \
  "ds_read_u16 v234, v234 offset:10528\n\t" \
  "s_waitcnt lgkmcnt(6)\n\t" \
  "v_mfma_f32_16x16x32_bf16 v[188:191], v[212:215], v[116:119], v[188:191]\n\t" \
  "v_lshlrev_b32_sdwa v227, 1, v185 dst_sel:DWORD dst_unused:UNUSED_PAD src0_sel:DWORD src1_sel:WORD_1\n\t" \
  "v_lshlrev_b32_sdwa v226, 1, v184 dst_sel:DWORD dst_unused:UNUSED_PAD src0_sel:DWORD src1_sel:WORD_1\n\t" \
  "s_waitcnt lgkmcnt(1)\n\t" \
  "ds_read_b32 v223, v223 offset:10016\n\t" \
  "v_mfma_f32_16x16x32_bf16 v[180:183], v[212:215], v[112:115], v[180:183]\n\t" \
  "v_readfirstlane_b32 s4, v246\n\t" \
  "v_lshlrev_b32_sdwa v225, 1, v187 dst_sel:DWORD dst_unused:UNUSED_PAD src0_sel:DWORD src1_sel:WORD_1\n\t" \
  "s_setprio 2\n\t" \
  "s_setprio 0\n\t" \
  "ds_read_b32 v222, v222 offset:10016\n\t" \
  "ds_read_b32 v221, v221 offset:10016\n\t" \
  "v_mfma_f32_16x16x32_bf16 v[204:207], v[208:211], v[108:111], 0\n\t" \
  "s_cmpk_lt_u32 s4, 3072\n\t" \
  "v_lshlrev_b32_sdwa v224, 1, v186 dst_sel:DWORD dst_unused:UNUSED_PAD src0_sel:DWORD src1_sel:WORD_1\n\t" \
  "s_cbranch_scc1 .Lcnx_w0_2\n\t" \
  ".Lcnx_g0_2:\n\t" \
  "ds_read_b32 v220, v220 offset:10016\n\t" \
  "v_mfma_f32_16x16x32_bf16 v[196:199], v[212:215], v[120:123], v[196:199]\n\t" \
  "s_nop 0\n\t" \
  "v_fma_mix_f32 v229, |v165|, v229, v229 op_sel:[0,1,0] op_sel_hi:[0,1,1] clamp\n\t" \
  "v_fma_mix_f32 v228, |v164|, v228, v228 op_sel:[0,1,0] op_sel_hi:[0,1,1] clamp\n\t" \
  "ds_read_b64_tr_b16 v[210:211], v249 offset:26136\n\t" \
  "ds_read_b64_tr_b16 v[208:209], v249 offset:24600\n\t" \
  "v_mfma_f32_16x16x32_bf16 v[204:207], v[212:215], v[124:127], v[204:207]\n\t" \
  "s_mov_b32 m0, s46\n\t" \
  "v_fma_mix_f32 v231, |v167|, v231, v231 op_sel:[0,1,0] op_sel_hi:[0,1,1] clamp\n\t" \
  "ds_read_u16_d16_hi v227, v227 offset:10528\n\t" \
  "ds_read_b64_tr_b16 v[212:213], v249 offset:24608\n\t" \
  "v_mfma_f32_16x16x32_bf16 v[180:183], v[216:219], v[128:131], v[180:183]\n\t" \
  "v_or_b32 v172, v232, v233\n\t" \
  "v_fma_mix_f32 v230, |v166|, v230, v230 op_sel:[0,1,0] op_sel_hi:[0,1,1] clamp\n\t" \
  "s_mov_b32 exec_hi, 0xffff\n\t" \
  "buffer_load_dwordx4 v252, s[16:19], s33 offen lds\n\t" \
  "v_mfma_f32_16x16x32_bf16 v[188:191], v[216:219], v[132:135], v[188:191]\n\t" \
  "s_mov_b32 exec_hi, -1\n\t" \
  "v_fma_f32 v165, |v165|, v229, v165 mul:4\n\t" \
  "v_fma_f32 v164, |v164|, v228, v164 mul:4\n\t" \
  "ds_read_b64_tr_b16 v[214:215], v249 offset:26144\n\t" \
  "v_mfma_f32_16x16x32_bf16 v[196:199], v[216:219], v[136:139], v[196:199]\n\t" \
  "v_fma_f32 v166, |v166|, v230, v166 mul:4\n\t" \
  "v_fma_f32 v167, |v167|, v231, v167 mul:4\n\t" \
  "ds_read_u16_d16_hi v225, v225 offset:10528\n\t" \
  "s_waitcnt lgkmcnt(4)\n\t" \
  "v_mfma_f32_16x16x32_bf16 v[204:207], v[216:219], v[140:143], v[204:207]\n\t" \
  "v_cvt_pk_bf16_f32 v164, v164, v165\n\t" \
  "v_cvt_pk_bf16_f32 v165, v166, v167\n\t" \
  "ds_read_b64_tr_b16 v[216:217], v249 offset:24664\n\t" \
  "v_cvt_pknorm_i16_f32 v230, |v192|, |v193|\n\t" \
  "v_mfma_f32_16x16x32_bf16 v[0:3], v[208:211], v[146:149], v[0:3]\n\t" \
  "v_cvt_pknorm_i16_f32 v228, |v194|, |v195|\n\t" \
  "s_setprio 0\n\t" \
  "ds_read_b64_tr_b16 v[218:219], v249 offset:26200\n\t" \
  "v_mfma_f32_16x16x32_bf16 v[24:27], v[208:211], v[154:157], v[24:27]\n\t" \
  "v_lshlrev_b32_sdwa v229, 2, v228 dst_sel:DWORD dst_unused:UNUSED_PAD src0_sel:DWORD src1_sel:BYTE_3\n\t" \
  "v_lshlrev_b32_sdwa v228, 2, v228 dst_sel:DWORD dst_unused:UNUSED_PAD src0_sel:DWORD src1_sel:BYTE_1\n\t" \
  "s_add_u32 s33, s33, s32\n\t" \
  "ds_read_u16 v224, v224 offset:10528\n\t" \
  "s_waitcnt lgkmcnt(4)\n\t" \
  "v_mfma_f32_16x16x32_bf16 v[48:51], v[208:211], v[162:165], v[48:51]\n\t" \
  "v_lshlrev_b32_sdwa v231, 2, v230 dst_sel:DWORD dst_unused:UNUSED_PAD src0_sel:DWORD src1_sel:BYTE_3\n\t" \
  "s_add_u32 m0, s46, s35\n\t" \
  "s_mov_b32 exec_hi, 0xffff\n\t" \
  "buffer_load_dwordx4 v252, s[16:19], s34 offen lds\n\t" \
  "v_mfma_f32_16x16x32_bf16 v[4:7], v[212:215], v[146:149], v[4:7]\n\t" \
  "s_mov_b32 exec_hi, -1\n\t" \
  "v_lshlrev_b32_sdwa v230, 2, v230 dst_sel:DWORD dst_unused:UNUSED_PAD src0_sel:DWORD src1_sel:BYTE_1\n\t" \
  "v_fma_mix_f32 v222, |v176|, v222, v222 op_sel:[0,1,0] op_sel_hi:[0,1,1] clamp\n\t" \
  "s_add_u32 s24, s24, s26\n\t" \
  "s_sub_u32 s24, s24, s64\n\t" \
  "ds_read_u16 v226, v226 offset:10528\n\t" \
  "v_mfma_f32_16x16x32_bf16 v[28:31], v[212:215], v[154:157], v[28:31]\n\t" \
  "s_setprio 1\n\t" \
  "s_setprio 1\n\t" \
  "s_setprio 3\n\t" \
  "ds_read_b32 v229, v229 offset:10016\n\t" \
  "ds_read_b32 v228, v228 offset:10016\n\t" \
  "v_lshlrev_b32_sdwa v232, 1, v202 dst_sel:DWORD dst_unused:UNUSED_PAD src0_sel:DWORD src1_sel:WORD_1\n\t" \
  "v_lshlrev_b32_sdwa v233, 1, v203 dst_sel:DWORD dst_unused:UNUSED_PAD src0_sel:DWORD src1_sel:WORD_1\n\t" \
  "ds_read_b32 v230, v230 offset:10016\n\t" \
  "v_mfma_f32_16x16x32_bf16 v[52:55], v[212:215], v[162:165], v[52:55]\n\t" \
  "v_fma_mix_f32 v221, |v179|, v221, v221 op_sel:[0,1,0] op_sel_hi:[0,1,1] clamp\n\t" \
  "v_fma_mix_f32 v220, |v178|, v220, v220 op_sel:[0,1,0] op_sel_hi:[0,1,1] clamp\n\t" \
  "s_add_u32 s34, s34, s32\n\t" \
  "s_setprio 3\n\t" \
  "s_waitcnt lgkmcnt(3)\n\t" \
  "v_fma_mix_f32 v223, |v177|, v223, v223 op_sel:[0,1,0] op_sel_hi:[0,1,1] clamp\n\t" \
  "v_mfma_f32_16x16x32_bf16 v[8:11], v[216:219], v[146:149], v[8:11]\n\t" \
  "v_or_b32 v187, v224, v225\n\t" \
  "v_or_b32 v186, v226, v227\n\t" \
  "ds_read_u16_d16_hi v233, v233 offset:10528\n\t" \
  "ds_read_b32 v231, v231 offset:10016\n\t" \
  "v_mfma_f32_16x16x32_bf16 v[32:35], v[216:219], v[154:157], v[32:35]\n\t" \
  "v_fma_f32 v178, |v178|, v220, v178 mul:4\n\t" \
  "s_setprio 1\n\t" \
  "v_mfma_f32_16x16x32_bf16 v[56:59], v[216:219], v[162:165], v[56:59]\n\t" \
  "ds_read_u16_d16_hi v235, v235 offset:10528\n\t" \
  "ds_read_u16 v232, v232 offset:10528\n\t" \
  "v_fma_f32 v179, |v179|, v221, v179 mul:4\n\t" \
  "v_fma_f32 v177, |v177|, v223, v177 mul:4\n\t" \
  "s_add_u32 m0, s25, 0\n\t" \
  "v_fma_f32 v176, |v176|, v222, v176 mul:4\n\t" \
  "v_cvt_pk_bf16_f32 v179, v178, v179\n\t" \
  "v_cvt_pk_bf16_f32 v178, v176, v177\n\t" \
  "buffer_load_dwordx4 v254, s[8:11], s24 offen nt lds\n\t" \
  "s_waitcnt lgkmcnt(1)\n\t" \
  "v_cvt_pknorm_i16_f32 v220, |v180|, |v181|\n\t" \
  "v_lshlrev_b32_sdwa v221, 2, v220 dst_sel:DWORD dst_unused:UNUSED_PAD src0_sel:DWORD src1_sel:BYTE_3\n\t" \
  "v_or_b32 v173, v234, v235\n\t" \
  "s_setprio 2\n\t" \
  "s_nop 0\n\t" \
  "v_mfma_f32_16x16x32_bf16 v[72:75], v[208:211], v[170:173], v[72:75]\n\t" \
  "s_nop 0\n\t" \
  "v_cvt_pknorm_i16_f32 v222, |v182|, |v183|\n\t" \
  "v_lshlrev_b32_sdwa v234, 1, v200 dst_sel:DWORD dst_unused:UNUSED_PAD src0_sel:DWORD src1_sel:WORD_1\n\t" \
  "ds_read_b64_tr_b16 v[210:211], v249 offset:26208\n\t" \
  "ds_read_b64_tr_b16 v[208:209], v249 offset:24672\n\t" \
  "v_lshlrev_b32_sdwa v220, 2, v220 dst_sel:DWORD dst_unused:UNUSED_PAD src0_sel:DWORD src1_sel:BYTE_1\n\t" \
  "s_setprio 0\n\t" \
  "v_mfma_f32_16x16x32_bf16 v[76:79], v[212:215], v[170:173], v[76:79]\n\t" \
  "ds_read_b64_tr_b16 v[212:213], v249 offset:24728\n\t" \
  "ds_read_b64_tr_b16 v[214:215], v249 offset:26264\n\t" \
  "v_mfma_f32_16x16x32_bf16 v[80:83], v[216:219], v[170:173], v[80:83]\n\t" \
  "ds_read_b64_tr_b16 v[218:219], v249 offset:26272\n\t" \
  "ds_read_b64_tr_b16 v[216:217], v249 offset:24736\n\t" \
  "s_waitcnt lgkmcnt(4)\n\t" \
  "v_mfma_f32_16x16x32_bf16 v[36:39], v[208:211], v[154:157], v[36:39]\n\t" \
  "v_lshlrev_b32_sdwa v235, 1, v201 dst_sel:DWORD dst_unused:UNUSED_PAD src0_sel:DWORD src1_sel:WORD_1\n\t" \
  "v_mfma_f32_16x16x32_bf16 v[12:15], v[208:211], v[146:149], v[12:15]\n\t" \
  "v_lshlrev_b32_sdwa v223, 2, v222 dst_sel:DWORD dst_unused:UNUSED_PAD src0_sel:DWORD src1_sel:BYTE_3\n\t" \
  "v_lshlrev_b32_sdwa v222, 2, v222 dst_sel:DWORD dst_unused:UNUSED_PAD src0_sel:DWORD src1_sel:BYTE_1\n\t" \
  "ds_read_u16 v234, v234 offset:10528\n\t" \
  "v_mfma_f32_16x16x32_bf16 v[84:87], v[208:211], v[170:173], v[84:87]\n\t" \
  "s_waitcnt lgkmcnt(3)\n\t" \
  "v_lshlrev_b32_sdwa v225, 1, v189 dst_sel:DWORD dst_unused:UNUSED_PAD src0_sel:DWORD src1_sel:WORD_1\n\t" \
  "v_lshlrev_b32_sdwa v224, 1, v188 dst_sel:DWORD dst_unused:UNUSED_PAD src0_sel:DWORD src1_sel:WORD_1\n\t" \
  "v_mfma_f32_16x16x32_bf16 v[60:63], v[208:211], v[162:165], v[60:63]\n\t" \
  "ds_read_b64_tr_b16 v[208:209], v248 offset:36904\n\t" \
  "ds_read_b32 v220, v220 offset:10016\n\t" \
  "s_add_u32 s27, s27, s26\n\t" \
  "v_mfma_f32_16x16x32_bf16 v[16:19], v[212:215], v[146:149], v[16:19]\n\t" \
  "s_mov_b64 exec, s[30:31]\n\t" \
  "buffer_load_dword v237, v254, s[8:11], s27 offen\n\t" \
  "v_mfma_f32_16x16x32_bf16 v[64:67], v[212:215], v[162:165], v[64:67]\n\t" \
  "s_mov_b64 exec, -1\n\t" \
  "v_lshlrev_b32_sdwa v227, 1, v191 dst_sel:DWORD dst_unused:UNUSED_PAD src0_sel:DWORD src1_sel:WORD_1\n\t" \
  "v_fma_mix_f32 v229, |v195|, v229, v229 op_sel:[0,1,0] op_sel_hi:[0,1,1] clamp\n\t" \
  "ds_read_b32 v221, v221 offset:10016\n\t" \
  "ds_read_b32 v223, v223 offset:10016\n\t" \
  "v_mfma_f32_16x16x32_bf16 v[88:91], v[212:215], v[170:173], v[88:91]\n\t" \
  "s_setprio 0\n\t" \
  "v_fma_mix_f32 v231, |v193|, v231, v231 op_sel:[0,1,0] op_sel_hi:[0,1,1] clamp\n\t" \
  "v_fma_mix_f32 v230, |v192|, v230, v230 op_sel:[0,1,0] op_sel_hi:[0,1,1] clamp\n\t" \
  "ds_read_b64_tr_b16 v[210:211], v248 offset:37928\n\t" \
  "ds_read_b32 v222, v222 offset:10016\n\t" \
  "v_mfma_f32_16x16x32_bf16 v[40:43], v[212:215], v[154:157], v[40:43]\n\t" \
  "s_waitcnt lgkmcnt(7)\n\t" \
  "ds_read_u16_d16_hi v225, v225 offset:10528\n\t" \
  "v_mfma_f32_16x16x32_bf16 v[68:71], v[216:219], v[162:165], v[68:71]\n\t" \
  "v_fma_mix_f32 v228, |v194|, v228, v228 op_sel:[0,1,0] op_sel_hi:[0,1,1] clamp\n\t" \
  "v_lshlrev_b32_sdwa v226, 1, v190 dst_sel:DWORD dst_unused:UNUSED_PAD src0_sel:DWORD src1_sel:WORD_1\n\t" \
  "s_setprio 2\n\t" \
  "s_setprio 3\n\t" \
  "ds_read_b64_tr_b16 v[212:213], v248 offset:39976\n\t" \
  "v_mfma_f32_16x16x32_bf16 v[20:23], v[216:219], v[146:149], v[20:23]\n\t" \
  "v_or_b32 v203, v232, v233\n\t" \
  "v_fma_f32 v195, |v195|, v229, v195 mul:4\n\t" \
  "ds_read_u16 v224, v224 offset:10528\n\t" \
  "v_fma_f32 v192, |v192|, v230, v192 mul:4\n\t" \
  "s_setprio 3\n\t" \
  "s_waitcnt lgkmcnt(4)\n\t" \
  "v_mfma_f32_16x16x32_bf16 v[144:147], v[208:211], v[96:99], 0\n\t" \
  "v_fma_f32 v193, |v193|, v231, v193 mul:4\n\t" \
  "v_lshlrev_b32_sdwa v232, 1, v204 dst_sel:DWORD dst_unused:UNUSED_PAD src0_sel:DWORD src1_sel:WORD_1\n\t" \
  "s_setprio 3\n\t" \
  "ds_read_b64_tr_b16 v[214:215], v248 offset:41000\n\t" \
  "v_mfma_f32_16x16x32_bf16 v[44:47], v[216:219], v[154:157], v[44:47]\n\t" \
  "v_fma_f32 v194, |v194|, v228, v194 mul:4\n\t" \
  "v_cvt_pknorm_i16_f32 v230, |v198|, |v199|\n\t" \
  "ds_read_u16_d16_hi v227, v227 offset:10528\n\t" \
  "v_mfma_f32_16x16x32_bf16 v[92:95], v[216:219], v[170:173], v[92:95]\n\t" \
  "v_cvt_pk_bf16_f32 v195, v194, v195\n\t" \
  "v_cvt_pk_bf16_f32 v194, v192, v193\n\t" \
  "s_setprio 2\n\t" \
  "ds_read_b64_tr_b16 v[216:217], v248 offset:38952\n\t" \
  "v_mfma_f32_16x16x32_bf16 v[160:163], v[208:211], v[104:107], 0\n\t" \
  "v_cvt_pknorm_i16_f32 v228, |v196|, |v197|\n\t" \
  "v_lshlrev_b32_sdwa v231, 2, v230 dst_sel:DWORD dst_unused:UNUSED_PAD src0_sel:DWORD src1_sel:BYTE_3\n\t" \
  "v_mfma_f32_16x16x32_bf16 v[152:155], v[208:211], v[100:103], 0\n\t" \
  "s_setprio 2\n\t" \
  "v_lshlrev_b32_sdwa v229, 2, v228 dst_sel:DWORD dst_unused:UNUSED_PAD src0_sel:DWORD src1_sel:BYTE_3\n\t" \
  "v_lshlrev_b32_sdwa v228, 2, v228 dst_sel:DWORD dst_unused:UNUSED_PAD src0_sel:DWORD src1_sel:BYTE_1\n\t" \
  "s_setprio 2\n\t" \
  "s_waitcnt lgkmcnt(2)\n\t" \
  "ds_read_b64_tr_b16 v[218:219], v248 offset:42024\n\t" \
  "ds_read_u16 v226, v226 offset:10528\n\t" \
  "ds_read_b32 v231, v231 offset:10016\n\t" \
  "ds_read_b32 v228, v228 offset:10016\n\t" \
  "v_mfma_f32_16x16x32_bf16 v[144:147], v[212:215], v[112:115], v[144:147]\n\t" \
  "v_fma_mix_f32 v221, |v181|, v221, v221 op_sel:[0,1,0] op_sel_hi:[0,1,1] clamp\n\t" \
  "v_lshlrev_b32_sdwa v233, 1, v205 dst_sel:DWORD dst_unused:UNUSED_PAD src0_sel:DWORD src1_sel:WORD_1\n\t" \
  "v_mfma_f32_16x16x32_bf16 v[168:171], v[208:211], v[108:111], 0\n\t" \
  "v_lshlrev_b32_sdwa v230, 2, v230 dst_sel:DWORD dst_unused:UNUSED_PAD src0_sel:DWORD src1_sel:BYTE_1\n\t" \
  "s_setprio 1\n\t" \
  "ds_read_b64_tr_b16 v[208:209], v248 offset:37416\n\t" \
  "v_mfma_f32_16x16x32_bf16 v[168:171], v[212:215], v[124:127], v[168:171]\n\t" \
  "v_fma_mix_f32 v222, |v182|, v222, v222 op_sel:[0,1,0] op_sel_hi:[0,1,1] clamp\n\t" \
  "ds_read_b32 v229, v229 offset:10016\n\t" \
  "v_fma_mix_f32 v220, |v180|, v220, v220 op_sel:[0,1,0] op_sel_hi:[0,1,1] clamp\n\t" \
  "v_mfma_f32_16x16x32_bf16 v[160:163], v[212:215], v[120:123], v[160:163]\n\t" \
  "s_nop 0\n\t" \
  "ds_read_u16_d16_hi v235, v235 offset:10528\n\t" \
  "ds_read_u16_d16_hi v233, v233 offset:10528\n\t" \
  "ds_read_b64_tr_b16 v[210:211], v248 offset:38440\n\t" \
  "v_fma_mix_f32 v223, |v183|, v223, v223 op_sel:[0,1,0] op_sel_hi:[0,1,1] clamp\n\t" \
  "v_or_b32 v188, v224, v225\n\t" \
  "v_mfma_f32_16x16x32_bf16 v[152:155], v[212:215], v[116:119], v[152:155]\n\t" \
  "s_waitcnt lgkmcnt(8)\n\t" \
  "ds_read_b64_tr_b16 v[212:213], v248 offset:40488\n\t" \
  "v_mfma_f32_16x16x32_bf16 v[144:147], v[216:219], v[128:131], v[144:147]\n\t" \
  "s_waitcnt lgkmcnt(8)\n\t" \
  "v_or_b32 v189, v226, v227\n\t" \
  "ds_read_b64_tr_b16 v[214:215], v248 offset:41512\n\t" \
  "s_waitcnt lgkmcnt(4)\n\t" \
  "v_mfma_f32_16x16x32_bf16 v[160:163], v[216:219], v[136:139], v[160:163]\n\t" \
  "v_fma_f32 v182, |v182|, v222, v182 mul:4\n\t" \
  "v_fma_f32 v183, |v183|, v223, v183 mul:4\n\t" \
  "s_setprio 1\n\t" \
  "s_setprio 0\n\t" \
  "ds_read_b32 v230, v230 offset:10016\n\t" \
  "v_or_b32 v202, v234, v235\n\t" \
  "v_lshlrev_b32_sdwa v235, 1, v207 dst_sel:DWORD dst_unused:UNUSED_PAD src0_sel:DWORD src1_sel:WORD_1\n\t" \
  "v_lshlrev_b32_sdwa v234, 1, v206 dst_sel:DWORD dst_unused:UNUSED_PAD src0_sel:DWORD src1_sel:WORD_1\n\t" \
  "v_mfma_f32_16x16x32_bf16 v[152:155], v[216:219], v[132:135], v[152:155]\n\t" \
  "s_waitcnt lgkmcnt(3)\n\t" \
  "v_fma_f32 v181, |v181|, v221, v181 mul:4\n\t" \
  "v_fma_f32 v180, |v180|, v220, v180 mul:4\n\t" \
  "ds_read_u16_d16_hi v235, v235 offset:10528\n\t" \
  "v_mfma_f32_16x16x32_bf16 v[168:171], v[216:219], v[140:143], v[168:171]\n\t" \
  "v_cvt_pk_bf16_f32 v180, v180, v181\n\t" \
  "v_cvt_pk_bf16_f32 v181, v182, v183\n\t" \
  "ds_read_u16 v232, v232 offset:10528\n\t" \
  "ds_read_b64_tr_b16 v[216:217], v248 offset:39464\n\t" \
  "v_mfma_f32_16x16x32_bf16 v[148:151], v[208:211], v[96:99], 0\n\t" \
  "v_cvt_pknorm_i16_f32 v222, |v144|, |v145|\n\t" \
  "v_cvt_pknorm_i16_f32 v220, |v146|, |v147|\n\t" \
  "s_setprio 0\n\t" \
  "s_waitcnt vmcnt(2)\n\t" \
  "ds_read_b64_tr_b16 v[218:219], v248 offset:42536\n\t" \
  "s_waitcnt lgkmcnt(5)\n\t" \
  "v_mfma_f32_16x16x32_bf16 v[156:159], v[208:211], v[100:103], 0\n\t" \
  "v_lshlrev_b32_sdwa v221, 2, v220 dst_sel:DWORD dst_unused:UNUSED_PAD src0_sel:DWORD src1_sel:BYTE_3\n\t" \
  "v_lshlrev_b32_sdwa v220, 2, v220 dst_sel:DWORD dst_unused:UNUSED_PAD src0_sel:DWORD src1_sel:BYTE_1\n\t" \
  "s_mov_b64 exec, 3\n\t" \
  "ds_add_rtn_u32 v246, v251, v250 offset:47152\n\t" \
  "v_mfma_f32_16x16x32_bf16 v[164:167], v[208:211], v[104:107], 0\n\t" \
  "s_mov_b64 exec, -1\n\t" \
  "v_lshlrev_b32_sdwa v223, 2, v222 dst_sel:DWORD dst_unused:UNUSED_PAD src0_sel:DWORD src1_sel:BYTE_3\n\t" \
  "v_lshlrev_b32_sdwa v222, 2, v222 dst_sel:DWORD dst_unused:UNUSED_PAD src0_sel:DWORD src1_sel:BYTE_1\n\t" \
  "ds_read_u16 v234, v234 offset:10528\n\t" \
  "v_mfma_f32_16x16x32_bf16 v[156:159], v[212:215], v[116:119], v[156:159]\n\t" \
  "v_lshlrev_b32_sdwa v226, 1, v152 dst_sel:DWORD dst_unused:UNUSED_PAD src0_sel:DWORD src1_sel:WORD_1\n\t" \
  "v_lshlrev_b32_sdwa v227, 1, v153 dst_sel:DWORD dst_unused:UNUSED_PAD src0_sel:DWORD src1_sel:WORD_1\n\t" \
  "ds_read_b32 v223, v223 offset:10016\n\t" \
  "v_mfma_f32_16x16x32_bf16 v[148:151], v[212:215], v[112:115], v[148:151]\n\t" \
  "s_waitcnt lgkmcnt(2)\n\t" \
  "v_lshlrev_b32_sdwa v225, 1, v155 dst_sel:DWORD dst_unused:UNUSED_PAD src0_sel:DWORD src1_sel:WORD_1\n\t" \
  "v_readfirstlane_b32 s4, v246\n\t" \
  "ds_read_b32 v221, v221 offset:10016\n\t" \
  "v_mfma_f32_16x16x32_bf16 v[172:175], v[208:211], v[108:111], 0\n\t" \
  "s_cmpk_lt_u32 s4, 3072\n\t" \
  "v_lshlrev_b32_sdwa v224, 1, v154 dst_sel:DWORD dst_unused:UNUSED_PAD src0_sel:DWORD src1_sel:WORD_1\n\t" \
  "s_cbranch_scc1 .Lcnx_w0_3\n\t" \
  ".Lcnx_g0_3:\n\t" \
  "ds_read_b32 v220, v220 offset:10016\n\t" \
  "v_mfma_f32_16x16x32_bf16 v[164:167], v[212:215], v[120:123], v[164:167]\n\t" \
  "v_fma_mix_f32 v229, |v197|, v229, v229 op_sel:[0,1,0] op_sel_hi:[0,1,1] clamp\n\t" \
  "v_fma_mix_f32 v228, |v196|, v228, v228 op_sel:[0,1,0] op_sel_hi:[0,1,1] clamp\n\t" \
  "ds_read_b64_tr_b16 v[210:211], v249 offset:32288\n\t" \
  "ds_read_b64_tr_b16 v[208:209], v249 offset:30752\n\t" \
  "v_mfma_f32_16x16x32_bf16 v[172:175], v[212:215], v[124:127], v[172:175]\n\t" \
  "v_fma_mix_f32 v230, |v198|, v230, v230 op_sel:[0,1,0] op_sel_hi:[0,1,1] clamp\n\t" \
  "v_fma_mix_f32 v231, |v199|, v231, v231 op_sel:[0,1,0] op_sel_hi:[0,1,1] clamp\n\t" \
  "s_waitcnt lgkmcnt(5)\n\t" \
  "ds_read_b32 v222, v222 offset:10016\n\t" \
  "ds_read_b64_tr_b16 v[212:213], v249 offset:30760\n\t" \
  "v_mfma_f32_16x16x32_bf16 v[148:151], v[216:219], v[128:131], v[148:151]\n\t" \
  "s_mov_b32 m0, s47\n\t" \
  "v_or_b32 v204, v232, v233\n\t" \
  "v_or_b32 v205, v234, v235\n\t" \
  "s_mov_b32 exec_hi, 0xffff\n\t" \
  "buffer_load_dwordx4 v252, s[16:19], s33 offen lds\n\t" \
  "v_mfma_f32_16x16x32_bf16 v[156:159], v[216:219], v[132:135], v[156:159]\n\t" \
  "s_mov_b32 exec_hi, -1\n\t" \
  "v_fma_f32 v196, |v196|, v228, v196 mul:4\n\t" \
  "v_fma_f32 v197, |v197|, v229, v197 mul:4\n\t" \
  "s_add_u32 s33, s33, s32\n\t" \
  "ds_read_u16_d16_hi v227, v227 offset:10528\n\t" \
  "ds_read_b64_tr_b16 v[214:215], v249 offset:32296\n\t" \
  "v_mfma_f32_16x16x32_bf16 v[164:167], v[216:219], v[136:139], v[164:167]\n\t" \
  "v_fma_f32 v198, |v198|, v230, v198 mul:4\n\t" \
  "v_fma_f32 v199, |v199|, v231, v199 mul:4\n\t" \
  "ds_read_u16_d16_hi v225, v225 offset:10528\n\t" \
  "v_mfma_f32_16x16x32_bf16 v[172:175], v[216:219], v[140:143], v[172:175]\n\t" \
  "v_cvt_pk_bf16_f32 v196, v196, v197\n\t" \
  "v_cvt_pk_bf16_f32 v197, v198, v199\n\t" \
  "ds_read_b64_tr_b16 v[216:217], v249 offset:30816\n\t" \
  "s_waitcnt lgkmcnt(6)\n\t" \
  "v_mfma_f32_16x16x32_bf16 v[0:3], v[208:211], v[178:181], v[0:3]\n\t" \
  "v_cvt_pknorm_i16_f32 v230, |v160|, |v161|\n\t" \
  "v_cvt_pknorm_i16_f32 v228, |v162|, |v163|\n\t" \
  "ds_read_b64_tr_b16 v[218:219], v249 offset:32352\n\t" \
  "v_mfma_f32_16x16x32_bf16 v[24:27], v[208:211], v[186:189], v[24:27]\n\t" \
  "v_lshlrev_b32_sdwa v229, 2, v228 dst_sel:DWORD dst_unused:UNUSED_PAD src0_sel:DWORD src1_sel:BYTE_3\n\t" \
  "v_lshlrev_b32_sdwa v228, 2, v228 dst_sel:DWORD dst_unused:UNUSED_PAD src0_sel:DWORD src1_sel:BYTE_1\n\t" \
  "ds_read_b32 v229, v229 offset:10016\n\t" \
  "ds_read_u16 v224, v224 offset:10528\n\t" \
  "v_mfma_f32_16x16x32_bf16 v[48:51], v[208:211], v[194:197], v[48:51]\n\t" \
  "v_lshlrev_b32_sdwa v231, 2, v230 dst_sel:DWORD dst_unused:UNUSED_PAD src0_sel:DWORD src1_sel:BYTE_3\n\t" \
  "s_setprio 1\n\t" \
  "s_add_u32 m0, s47, s35\n\t" \
  "s_waitcnt lgkmcnt(5)\n\t" \
  "ds_read_b32 v228, v228 offset:10016\n\t" \
  "v_mfma_f32_16x16x32_bf16 v[72:75], v[208:211], v[202:205], v[72:75]\n\t" \
  "v_lshlrev_b32_sdwa v232, 1, v170 dst_sel:DWORD dst_unused:UNUSED_PAD src0_sel:DWORD src1_sel:WORD_1\n\t" \
  "v_lshlrev_b32_sdwa v233, 1, v171 dst_sel:DWORD dst_unused:UNUSED_PAD src0_sel:DWORD src1_sel:WORD_1\n\t" \
  "s_mov_b32 exec_hi, 0xffff\n\t" \
  "buffer_load_dwordx4 v252, s[16:19], s34 offen lds\n\t" \
  "v_mfma_f32_16x16x32_bf16 v[4:7], v[212:215], v[178:181], v[4:7]\n\t" \
  "s_mov_b32 exec_hi, -1\n\t" \
  "s_add_u32 s34, s34, s32\n\t" \
  "v_lshlrev_b32_sdwa v235, 1, v169 dst_sel:DWORD dst_unused:UNUSED_PAD src0_sel:DWORD src1_sel:WORD_1\n\t" \
  "ds_read_u16 v226, v226 offset:10528\n\t" \
  "v_mfma_f32_16x16x32_bf16 v[28:31], v[212:215], v[186:189], v[28:31]\n\t" \
  "v_lshlrev_b32_sdwa v234, 1, v168 dst_sel:DWORD dst_unused:UNUSED_PAD src0_sel:DWORD src1_sel:WORD_1\n\t" \
  "v_lshlrev_b32_sdwa v230, 2, v230 dst_sel:DWORD dst_unused:UNUSED_PAD src0_sel:DWORD src1_sel:BYTE_1\n\t" \
  "ds_read_b32 v230, v230 offset:10016\n\t" \
  "ds_read_b64_tr_b16 v[208:209], v249 offset:30824\n\t" \
  "v_mfma_f32_16x16x32_bf16 v[52:55], v[212:215], v[194:197], v[52:55]\n\t" \
  "v_fma_mix_f32 v220, |v146|, v220, v220 op_sel:[0,1,0] op_sel_hi:[0,1,1] clamp\n\t" \
  "v_fma_mix_f32 v221, |v147|, v221, v221 op_sel:[0,1,0] op_sel_hi:[0,1,1] clamp\n\t" \
  "ds_read_b64_tr_b16 v[210:211], v249 offset:32360\n\t" \
  "v_mfma_f32_16x16x32_bf16 v[76:79], v[212:215], v[202:205], v[76:79]\n\t" \
  "s_waitcnt lgkmcnt(7)\n\t" \
  "v_fma_mix_f32 v223, |v145|, v223, v223 op_sel:[0,1,0] op_sel_hi:[0,1,1] clamp\n\t" \
  "v_fma_mix_f32 v222, |v144|, v222, v222 op_sel:[0,1,0] op_sel_hi:[0,1,1] clamp\n\t" \
  "s_add_u32 m0, s25, 8200\n\t" \
  "s_waitcnt lgkmcnt(3)\n\t" \
  "ds_read_b32 v231, v231 offset:10016\n\t" \
  "ds_read_b64_tr_b16 v[212:213], v249 offset:30880\n\t" \
  "v_mfma_f32_16x16x32_bf16 v[8:11], v[216:219], v[178:181], v[8:11]\n\t" \
  "v_or_b32 v155, v224, v225\n\t" \
  "v_or_b32 v154, v226, v227\n\t" \
  "s_setprio 1\n\t" \
  "s_setprio 0\n\t" \
  "ds_read_u16_d16_hi v233, v233 offset:10528\n\t" \
  "v_mfma_f32_16x16x32_bf16 v[56:59], v[216:219], v[194:197], v[56:59]\n\t" \
  "v_fma_f32 v144, |v144|, v222, v144 mul:4\n\t" \
  "v_fma_f32 v145, |v145|, v223, v145 mul:4\n\t" \
  "ds_read_u16 v232, v232 offset:10528\n\t" \
  "v_mfma_f32_16x16x32_bf16 v[32:35], v[216:219], v[186:189], v[32:35]\n\t" \
  "v_fma_f32 v146, |v146|, v220, v146 mul:4\n\t" \
  "v_fma_f32 v147, |v147|, v221, v147 mul:4\n\t" \
  "ds_read_b64_tr_b16 v[214:215], v249 offset:32416\n\t" \
  "v_mfma_f32_16x16x32_bf16 v[80:83], v[216:219], v[202:205], v[80:83]\n\t" \
  "s_waitcnt lgkmcnt(5)\n\t" \
  "v_cvt_pk_bf16_f32 v147, v146, v147\n\t" \
  "v_cvt_pk_bf16_f32 v146, v144, v145\n\t" \
  "s_add_u32 s24, s24, s26\n\t" \
  "buffer_load_dwordx4 v254, s[8:11], s24 offen nt lds\n\t" \
  "v_mfma_f32_16x16x32_bf16 v[12:15], v[208:211], v[178:181], v[12:15]\n\t" \
  "v_cvt_pknorm_i16_f32 v220, |v148|, |v149|\n\t" \
  "v_cvt_pknorm_i16_f32 v222, |v150|, |v151|\n\t" \
  "ds_read_b64_tr_b16 v[218:219], v249 offset:32424\n\t" \
  "ds_read_b64_tr_b16 v[216:217], v249 offset:30888\n\t" \
  "v_mfma_f32_16x16x32_bf16 v[36:39], v[208:211], v[186:189], v[36:39]\n\t" \
  "v_lshlrev_b32_sdwa v221, 2, v220 dst_sel:DWORD dst_unused:UNUSED_PAD src0_sel:DWORD src1_sel:BYTE_3\n\t" \
  "v_lshlrev_b32_sdwa v220, 2, v220 dst_sel:DWORD dst_unused:UNUSED_PAD src0_sel:DWORD src1_sel:BYTE_1\n\t" \
  "ds_read_u16_d16_hi v235, v235 offset:10528\n\t" \
  "v_mfma_f32_16x16x32_bf16 v[60:63], v[208:211], v[194:197], v[60:63]\n\t" \
  "s_waitcnt lgkmcnt(3)\n\t" \
  "v_lshlrev_b32_sdwa v223, 2, v222 dst_sel:DWORD dst_unused:UNUSED_PAD src0_sel:DWORD src1_sel:BYTE_3\n\t" \
  "v_lshlrev_b32_sdwa v222, 2, v222 dst_sel:DWORD dst_unused:UNUSED_PAD src0_sel:DWORD src1_sel:BYTE_1\n\t" \
  "ds_read_u16 v234, v234 offset:10528\n\t" \
  "v_mfma_f32_16x16x32_bf16 v[84:87], v[208:211], v[202:205], v[84:87]\n\t" \
  "v_lshlrev_b32_sdwa v224, 1, v156 dst_sel:DWORD dst_unused:UNUSED_PAD src0_sel:DWORD src1_sel:WORD_1\n\t" \
  "v_lshlrev_b32_sdwa v225, 1, v157 dst_sel:DWORD dst_unused:UNUSED_PAD src0_sel:DWORD src1_sel:WORD_1\n\t" \
  "ds_read_b32 v220, v220 offset:10016\n\t" \
  "ds_read_b64_tr_b16 v[208:209], v248 offset:0\n\t" \
  "v_mfma_f32_16x16x32_bf16 v[16:19], v[212:215], v[178:181], v[16:19]\n\t" \
  "v_nop\n\t" \
  "s_add_u32 s27, s27, s26\n\t" \
  "s_mov_b64 exec, s[30:31]\n\t" \
  "buffer_load_dword v237, v254, s[8:11], s27 offen\n\t" \
  "v_mfma_f32_16x16x32_bf16 v[64:67], v[212:215], v[194:197], v[64:67]\n\t" \
  "s_mov_b64 exec, -1\n\t" \
  "v_lshlrev_b32_sdwa v227, 1, v159 dst_sel:DWORD dst_unused:UNUSED_PAD src0_sel:DWORD src1_sel:WORD_1\n\t" \
  "v_fma_mix_f32 v229, |v163|, v229, v229 op_sel:[0,1,0] op_sel_hi:[0,1,1] clamp\n\t" \
  "ds_read_b32 v223, v223 offset:10016\n\t" \
  "ds_read_b32 v221, v221 offset:10016\n\t" \
  "v_mfma_f32_16x16x32_bf16 v[88:91], v[212:215], v[202:205], v[88:91]\n\t" \
  "s_setprio 1\n\t" \
  "v_fma_mix_f32 v230, |v160|, v230, v230 op_sel:[0,1,0] op_sel_hi:[0,1,1] clamp\n\t" \
  "v_fma_mix_f32 v231, |v161|, v231, v231 op_sel:[0,1,0] op_sel_hi:[0,1,1] clamp\n\t" \
  "s_setprio 2\n\t" \
  "s_waitcnt lgkmcnt(4)\n\t" \
  "ds_read_b64_tr_b16 v[210:211], v248 offset:1024\n\t" \
  "v_mfma_f32_16x16x32_bf16 v[40:43], v[212:215], v[186:189], v[40:43]\n\t" \
  "v_lshlrev_b32_sdwa v226, 1, v158 dst_sel:DWORD dst_unused:UNUSED_PAD src0_sel:DWORD src1_sel:WORD_1\n\t" \
  "v_fma_mix_f32 v228, |v162|, v228, v228 op_sel:[0,1,0] op_sel_hi:[0,1,1] clamp\n\t" \
  "ds_read_b32 v222, v222 offset:10016\n\t" \
  "ds_read_b64_tr_b16 v[212:213], v248 offset:3072\n\t" \
  "v_mfma_f32_16x16x32_bf16 v[20:23], v[216:219], v[178:181], v[20:23]\n\t" \
  "v_or_b32 v171, v232, v233\n\t" \
  "v_or_b32 v170, v234, v235\n\t" \
  "ds_read_u16_d16_hi v225, v225 offset:10528\n\t" \
  "v_mfma_f32_16x16x32_bf16 v[68:71], v[216:219], v[194:197], v[68:71]\n\t" \
  "v_fma_f32 v163, |v163|, v229, v163 mul:4\n\t" \
  "v_fma_f32 v160, |v160|, v230, v160 mul:4\n\t" \
  "s_waitcnt lgkmcnt(3)\n\t" \
  "ds_read_u16 v224, v224 offset:10528\n\t" \
  "v_mfma_f32_16x16x32_bf16 v[176:179], v[208:211], v[96:99], 0\n\t" \
  "v_lshlrev_b32_sdwa v232, 1, v172 dst_sel:DWORD dst_unused:UNUSED_PAD src0_sel:DWORD src1_sel:WORD_1\n\t" \
  "v_fma_f32 v161, |v161|, v231, v161 mul:4\n\t" \
  "ds_read_b64_tr_b16 v[214:215], v248 offset:4096\n\t" \
  "v_mfma_f32_16x16x32_bf16 v[44:47], v[216:219], v[186:189], v[44:47]\n\t" \
  "v_cvt_pknorm_i16_f32 v230, |v166|, |v167|\n\t" \
  "v_fma_f32 v162, |v162|, v228, v162 mul:4\n\t" \
  "ds_read_u16_d16_hi v227, v227 offset:10528\n\t" \
  "v_mfma_f32_16x16x32_bf16 v[92:95], v[216:219], v[202:205], v[92:95]\n\t" \
  "v_cvt_pk_bf16_f32 v163, v162, v163\n\t" \
  "v_cvt_pk_bf16_f32 v162, v160, v161\n\t" \
  "ds_read_b64_tr_b16 v[216:217], v248 offset:2048\n\t" \
  "v_mfma_f32_16x16x32_bf16 v[192:195], v[208:211], v[104:107], 0\n\t" \
  "v_lshlrev_b32_sdwa v231, 2, v230 dst_sel:DWORD dst_unused:UNUSED_PAD src0_sel:DWORD src1_sel:BYTE_3\n\t" \
  "v_cvt_pknorm_i16_f32 v228, |v164|, |v165|\n\t" \
  "s_setprio 2\n\t" \
  "ds_read_b64_tr_b16 v[218:219], v248 offset:5120\n\t" \
  "v_mfma_f32_16x16x32_bf16 v[184:187], v[208:211], v[100:103], 0\n\t" \
  "v_lshlrev_b32_sdwa v229, 2, v228 dst_sel:DWORD dst_unused:UNUSED_PAD src0_sel:DWORD src1_sel:BYTE_3\n\t" \
  "v_lshlrev_b32_sdwa v228, 2, v228 dst_sel:DWORD dst_unused:UNUSED_PAD src0_sel:DWORD src1_sel:BYTE_1\n\t" \
  "ds_read_u16 v226, v226 offset:10528\n\t" \
  "v_mfma_f32_16x16x32_bf16 v[200:203], v[208:211], v[108:111], 0\n\t" \
  "v_lshlrev_b32_sdwa v234, 1, v174 dst_sel:DWORD dst_unused:UNUSED_PAD src0_sel:DWORD src1_sel:WORD_1\n\t" \
  "v_lshlrev_b32_sdwa v233, 1, v173 dst_sel:DWORD dst_unused:UNUSED_PAD src0_sel:DWORD src1_sel:WORD_1\n\t" \
  "s_waitcnt lgkmcnt(4)\n\t" \
  "ds_read_b64_tr_b16 v[208:209], v248 offset:512\n\t" \
  "v_mfma_f32_16x16x32_bf16 v[176:179], v[212:215], v[112:115], v[176:179]\n\t" \
  "v_lshlrev_b32_sdwa v230, 2, v230 dst_sel:DWORD dst_unused:UNUSED_PAD src0_sel:DWORD src1_sel:BYTE_1\n\t" \
  "v_fma_mix_f32 v221, |v149|, v221, v221 op_sel:[0,1,0] op_sel_hi:[0,1,1] clamp\n\t" \
  "ds_read_b32 v231, v231 offset:10016\n\t" \
  "v_mfma_f32_16x16x32_bf16 v[200:203], v[212:215], v[124:127], v[200:203]\n\t" \
  "v_lshlrev_b32_sdwa v235, 1, v175 dst_sel:DWORD dst_unused:UNUSED_PAD src0_sel:DWORD src1_sel:WORD_1\n\t" \
  "ds_read_b32 v228, v228 offset:10016\n\t" \
  "ds_read_b32 v229, v229 offset:10016\n\t" \
  "v_mfma_f32_16x16x32_bf16 v[192:195], v[212:215], v[120:123], v[192:195]\n\t" \
  "v_fma_mix_f32 v220, |v148|, v220, v220 op_sel:[0,1,0] op_sel_hi:[0,1,1] clamp\n\t" \
  "s_waitcnt lgkmcnt(5)\n\t" \
  "ds_read_b64_tr_b16 v[210:211], v248 offset:1536\n\t" \
  "ds_read_u16_d16_hi v233, v233 offset:10528\n\t" \
  "v_mfma_f32_16x16x32_bf16 v[184:187], v[212:215], v[116:119], v[184:187]\n\t" \
  "v_fma_mix_f32 v223, |v151|, v223, v223 op_sel:[0,1,0] op_sel_hi:[0,1,1] clamp\n\t" \
  "v_fma_mix_f32 v222, |v150|, v222, v222 op_sel:[0,1,0] op_sel_hi:[0,1,1] clamp\n\t" \
  "s_waitcnt lgkmcnt(6)\n\t" \
  "ds_read_b64_tr_b16 v[212:213], v248 offset:3584\n\t" \
  "v_mfma_f32_16x16x32_bf16 v[176:179], v[216:219], v[128:131], v[176:179]\n\t" \
  "v_or_b32 v156, v224, v225\n\t" \
  "v_or_b32 v157, v226, v227\n\t" \
  "ds_read_b64_tr_b16 v[214:215], v248 offset:4608\n\t" \
  "v_mfma_f32_16x16x32_bf16 v[192:195], v[216:219], v[136:139], v[192:195]\n\t" \
  "s_nop 0\n\t" \
  "v_fma_f32 v150, |v150|, v222, v150 mul:4\n\t" \
  "v_fma_f32 v151, |v151|, v223, v151 mul:4\n\t" \
  "ds_read_b32 v230, v230 offset:10016\n\t" \
  "v_mfma_f32_16x16x32_bf16 v[184:187], v[216:219], v[132:135], v[184:187]\n\t" \
  "v_fma_f32 v149, |v149|, v221, v149 mul:4\n\t" \
  "v_fma_f32 v148, |v148|, v220, v148 mul:4\n\t" \
  "ds_read_u16_d16_hi v235, v235 offset:10528\n\t" \
  "ds_read_u16 v232, v232 offset:10528\n\t" \
  "v_mfma_f32_16x16x32_bf16 v[200:203], v[216:219], v[140:143], v[200:203]\n\t" \
  "v_cvt_pknorm_i16_f32 v220, |v178|, |v179|\n\t" \
  "v_cvt_pk_bf16_f32 v148, v148, v149\n\t" \
  "s_setprio 0\n\t" \
  "s_waitcnt lgkmcnt(6)\n\t" \
  "ds_read_b64_tr_b16 v[216:217], v248 offset:2560\n\t" \
  "v_mfma_f32_16x16x32_bf16 v[180:183], v[208:211], v[96:99], 0\n\t" \
  "v_cvt_pk_bf16_f32 v149, v150, v151\n\t" \
  "v_cvt_pknorm_i16_f32 v222, |v176|, |v177|\n\t" \
  "s_waitcnt vmcnt(2)\n\t" \
  "ds_read_b64_tr_b16 v[218:219], v248 offset:5632\n\t" \
  "s_waitcnt lgkmcnt(5)\n\t" \
  "v_mfma_f32_16x16x32_bf16 v[188:191], v[208:211], v[100:103], 0\n\t" \
  "v_lshlrev_b32_sdwa v221, 2, v220 dst_sel:DWORD dst_unused:UNUSED_PAD src0_sel:DWORD src1_sel:BYTE_3\n\t" \
  "v_lshlrev_b32_sdwa v220, 2, v220 dst_sel:DWORD dst_unused:UNUSED_PAD src0_sel:DWORD src1_sel:BYTE_1\n\t" \
  "s_mov_b64 exec, 3\n\t" \
  "ds_add_rtn_u32 v246, v251, v250 offset:47160\n\t" \
  "v_mfma_f32_16x16x32_bf16 v[196:199], v[208:211], v[104:107], 0\n\t" \
  "s_mov_b64 exec, -1\n\t" \
  "v_lshlrev_b32_sdwa v223, 2, v222 dst_sel:DWORD dst_unused:UNUSED_PAD src0_sel:DWORD src1_sel:BYTE_3\n\t" \
  "v_lshlrev_b32_sdwa v222, 2, v222 dst_sel:DWORD dst_unused:UNUSED_PAD src0_sel:DWORD src1_sel:BYTE_1\n\t" \
  "ds_read_u16 v234, v234 offset:10528\n\t" \
  "v_mfma_f32_16x16x32_bf16 v[188:191], v[212:215], v[116:119], v[188:191]\n\t" \
  "v_lshlrev_b32_sdwa v226, 1, v184 dst_sel:DWORD dst_unused:UNUSED_PAD src0_sel:DWORD src1_sel:WORD_1\n\t" \
  "v_lshlrev_b32_sdwa v227, 1, v185 dst_sel:DWORD dst_unused:UNUSED_PAD src0_sel:DWORD src1_sel:WORD_1\n\t" \
  "ds_read_b32 v223, v223 offset:10016\n\t" \
  "v_mfma_f32_16x16x32_bf16 v[180:183], v[212:215], v[112:115], v[180:183]\n\t" \
  "s_waitcnt lgkmcnt(2)\n\t" \
  "v_lshlrev_b32_sdwa v225, 1, v187 dst_sel:DWORD dst_unused:UNUSED_PAD src0_sel:DWORD src1_sel:WORD_1\n\t" \
  "v_readfirstlane_b32 s4, v246\n\t" \
  "ds_read_b32 v221, v221 offset:10016\n\t" \
  "v_mfma_f32_16x16x32_bf16 v[204:207], v[208:211], v[108:111], 0\n\t" \
  "s_cmpk_lt_u32 s4, 3072\n\t" \
  "v_lshlrev_b32_sdwa v224, 1, v186 dst_sel:DWORD dst_unused:UNUSED_PAD src0_sel:DWORD src1_sel:WORD_1\n\t" \
  "s_cbranch_scc1 .Lcnx_w0_4\n\t" \
  ".Lcnx_g0_4:\n\t" \
  "ds_read_b32 v220, v220 offset:10016\n\t" \
  "v_mfma_f32_16x16x32_bf16 v[196:199], v[212:215], v[120:123], v[196:199]\n\t" \
  "v_fma_mix_f32 v229, |v165|, v229, v229 op_sel:[0,1,0] op_sel_hi:[0,1,1] clamp\n\t" \
  "v_fma_mix_f32 v228, |v164|, v228, v228 op_sel:[0,1,0] op_sel_hi:[0,1,1] clamp\n\t" \
  "ds_read_b64_tr_b16 v[210:211], v249 offset:7688\n\t" \
  "ds_read_b64_tr_b16 v[208:209], v249 offset:6152\n\t" \
  "v_mfma_f32_16x16x32_bf16 v[204:207], v[212:215], v[124:127], v[204:207]\n\t" \
  "v_fma_mix_f32 v230, |v166|, v230, v230 op_sel:[0,1,0] op_sel_hi:[0,1,1] clamp\n\t" \
  "v_fma_mix_f32 v231, |v167|, v231, v231 op_sel:[0,1,0] op_sel_hi:[0,1,1] clamp\n\t" \
  "s_waitcnt lgkmcnt(5)\n\t" \
  "ds_read_b32 v222, v222 offset:10016\n\t" \
  "ds_read_b64_tr_b16 v[212:213], v249 offset:6160\n\t" \
  "v_mfma_f32_16x16x32_bf16 v[180:183], v[216:219], v[128:131], v[180:183]\n\t" \
  "s_mov_b32 m0, s44\n\t" \
  "v_or_b32 v172, v232, v233\n\t" \
  "v_or_b32 v173, v234, v235\n\t" \
  "s_mov_b32 exec_hi, 0xffff\n\t" \
  "buffer_load_dwordx4 v252, s[16:19], s33 offen lds\n\t" \
  "v_mfma_f32_16x16x32_bf16 v[188:191], v[216:219], v[132:135], v[188:191]\n\t" \
  "s_mov_b32 exec_hi, -1\n\t" \
  "v_fma_f32 v164, |v164|, v228, v164 mul:4\n\t" \
  "v_fma_f32 v165, |v165|, v229, v165 mul:4\n\t" \
  "s_add_u32 s33, s33, s32\n\t" \
  "ds_read_u16_d16_hi v227, v227 offset:10528\n\t" \
  "ds_read_b64_tr_b16 v[214:215], v249 offset:7696\n\t" \
  "v_mfma_f32_16x16x32_bf16 v[196:199], v[216:219], v[136:139], v[196:199]\n\t" \
  "v_fma_f32 v166, |v166|, v230, v166 mul:4\n\t" \
  "v_fma_f32 v167, |v167|, v231, v167 mul:4\n\t" \
  "ds_read_u16_d16_hi v225, v225 offset:10528\n\t" \
  "v_mfma_f32_16x16x32_bf16 v[204:207], v[216:219], v[140:143], v[204:207]\n\t" \
  "v_cvt_pk_bf16_f32 v164, v164, v165\n\t" \
  "v_cvt_pk_bf16_f32 v165, v166, v167\n\t" \
  "ds_read_b64_tr_b16 v[216:217], v249 offset:6216\n\t" \
  "s_waitcnt lgkmcnt(6)\n\t" \
  "v_mfma_f32_16x16x32_bf16 v[0:3], v[208:211], v[146:149], v[0:3]\n\t" \
  "v_cvt_pknorm_i16_f32 v230, |v192|, |v193|\n\t" \
  "v_cvt_pknorm_i16_f32 v228, |v194|, |v195|\n\t" \
  "ds_read_b64_tr_b16 v[218:219], v249 offset:7752\n\t" \
  "v_mfma_f32_16x16x32_bf16 v[24:27], v[208:211], v[154:157], v[24:27]\n\t" \
  "v_lshlrev_b32_sdwa v229, 2, v228 dst_sel:DWORD dst_unused:UNUSED_PAD src0_sel:DWORD src1_sel:BYTE_3\n\t" \
  "v_lshlrev_b32_sdwa v228, 2, v228 dst_sel:DWORD dst_unused:UNUSED_PAD src0_sel:DWORD src1_sel:BYTE_1\n\t" \
  "ds_read_b32 v229, v229 offset:10016\n\t" \
  "ds_read_u16 v224, v224 offset:10528\n\t" \
  "v_mfma_f32_16x16x32_bf16 v[48:51], v[208:211], v[162:165], v[48:51]\n\t" \
  "v_lshlrev_b32_sdwa v231, 2, v230 dst_sel:DWORD dst_unused:UNUSED_PAD src0_sel:DWORD src1_sel:BYTE_3\n\t" \
  "s_setprio 1\n\t" \
  "s_add_u32 m0, s44, s35\n\t" \
  "s_waitcnt lgkmcnt(5)\n\t" \
  "ds_read_b32 v228, v228 offset:10016\n\t" \
  "v_mfma_f32_16x16x32_bf16 v[72:75], v[208:211], v[170:173], v[72:75]\n\t" \
  "v_lshlrev_b32_sdwa v232, 1, v202 dst_sel:DWORD dst_unused:UNUSED_PAD src0_sel:DWORD src1_sel:WORD_1\n\t" \
  "v_lshlrev_b32_sdwa v233, 1, v203 dst_sel:DWORD dst_unused:UNUSED_PAD src0_sel:DWORD src1_sel:WORD_1\n\t" \
  "s_mov_b32 exec_hi, 0xffff\n\t" \
  "buffer_load_dwordx4 v252, s[16:19], s34 offen lds\n\t" \
  "v_mfma_f32_16x16x32_bf16 v[4:7], v[212:215], v[146:149], v[4:7]\n\t" \
  "s_mov_b32 exec_hi, -1\n\t" \
  "s_add_u32 s34, s34, s32\n\t" \
  "v_lshlrev_b32_sdwa v235, 1, v201 dst_sel:DWORD dst_unused:UNUSED_PAD src0_sel:DWORD src1_sel:WORD_1\n\t" \
  "ds_read_u16 v226, v226 offset:10528\n\t" \
  "v_mfma_f32_16x16x32_bf16 v[28:31], v[212:215], v[154:157], v[28:31]\n\t" \
  "v_lshlrev_b32_sdwa v234, 1, v200 dst_sel:DWORD dst_unused:UNUSED_PAD src0_sel:DWORD src1_sel:WORD_1\n\t" \
  "v_lshlrev_b32_sdwa v230, 2, v230 dst_sel:DWORD dst_unused:UNUSED_PAD src0_sel:DWORD src1_sel:BYTE_1\n\t" \
  "ds_read_b32 v230, v230 offset:10016\n\t" \
  "ds_read_b64_tr_b16 v[208:209], v249 offset:6224\n\t" \
  "v_mfma_f32_16x16x32_bf16 v[52:55], v[212:215], v[162:165], v[52:55]\n\t" \
  "v_fma_mix_f32 v220, |v178|, v220, v220 op_sel:[0,1,0] op_sel_hi:[0,1,1] clamp\n\t" \
  "v_fma_mix_f32 v221, |v179|, v221, v221 op_sel:[0,1,0] op_sel_hi:[0,1,1] clamp\n\t" \
  "ds_read_b64_tr_b16 v[210:211], v249 offset:7760\n\t" \
  "v_mfma_f32_16x16x32_bf16 v[76:79], v[212:215], v[170:173], v[76:79]\n\t" \
  "s_waitcnt lgkmcnt(7)\n\t" \
  "v_fma_mix_f32 v223, |v177|, v223, v223 op_sel:[0,1,0] op_sel_hi:[0,1,1] clamp\n\t" \
  "v_fma_mix_f32 v222, |v176|, v222, v222 op_sel:[0,1,0] op_sel_hi:[0,1,1] clamp\n\t" \
  "s_add_u32 m0, s25, 16400\n\t" \
  "s_waitcnt lgkmcnt(3)\n\t" \
  "ds_read_b32 v231, v231 offset:10016\n\t" \
  "ds_read_b64_tr_b16 v[212:213], v249 offset:6280\n\t" \
  "v_mfma_f32_16x16x32_bf16 v[8:11], v[216:219], v[146:149], v[8:11]\n\t" \
  "v_or_b32 v187, v224, v225\n\t" \
  "v_or_b32 v186, v226, v227\n\t" \
  "s_setprio 1\n\t" \
  "s_setprio 0\n\t" \
  "ds_read_u16_d16_hi v233, v233 offset:10528\n\t" \
  "v_mfma_f32_16x16x32_bf16 v[56:59], v[216:219], v[162:165], v[56:59]\n\t" \
  "v_fma_f32 v176, |v176|, v222, v176 mul:4\n\t" \
  "v_fma_f32 v177, |v177|, v223, v177 mul:4\n\t" \
  "ds_read_u16 v232, v232 offset:10528\n\t" \
  "v_mfma_f32_16x16x32_bf16 v[32:35], v[216:219], v[154:157], v[32:35]\n\t" \
  "v_fma_f32 v178, |v178|, v220, v178 mul:4\n\t" \
  "v_fma_f32 v179, |v179|, v221, v179 mul:4\n\t" \
  "ds_read_b64_tr_b16 v[214:215], v249 offset:7816\n\t" \
  "v_mfma_f32_16x16x32_bf16 v[80:83], v[216:219], v[170:173], v[80:83]\n\t" \
  "s_waitcnt lgkmcnt(5)\n\t" \
  "v_cvt_pk_bf16_f32 v179, v178, v179\n\t" \
  "v_cvt_pk_bf16_f32 v178, v176, v177\n\t" \
  "s_add_u32 s24, s24, s26\n\t" \
  "buffer_load_dwordx4 v254, s[8:11], s24 offen nt lds\n\t" \
  "v_mfma_f32_16x16x32_bf16 v[12:15], v[208:211], v[146:149], v[12:15]\n\t" \
  "v_cvt_pknorm_i16_f32 v220, |v180|, |v181|\n\t" \
  "v_cvt_pknorm_i16_f32 v222, |v182|, |v183|\n\t" \
  "ds_read_b64_tr_b16 v[218:219], v249 offset:7824\n\t" \
  "ds_read_b64_tr_b16 v[216:217], v249 offset:6288\n\t" \
  "v_mfma_f32_16x16x32_bf16 v[36:39], v[208:211], v[154:157], v[36:39]\n\t" \
  "v_lshlrev_b32_sdwa v221, 2, v220 dst_sel:DWORD dst_unused:UNUSED_PAD src0_sel:DWORD src1_sel:BYTE_3\n\t" \
  "v_lshlrev_b32_sdwa v220, 2, v220 dst_sel:DWORD dst_unused:UNUSED_PAD src0_sel:DWORD src1_sel:BYTE_1\n\t" \
  "ds_read_u16_d16_hi v235, v235 offset:10528\n\t" \
  "v_mfma_f32_16x16x32_bf16 v[60:63], v[208:211], v[162:165], v[60:63]\n\t" \
  "s_waitcnt lgkmcnt(3)\n\t" \
  "v_lshlrev_b32_sdwa v223, 2, v222 dst_sel:DWORD dst_unused:UNUSED_PAD src0_sel:DWORD src1_sel:BYTE_3\n\t" \
  "v_lshlrev_b32_sdwa v222, 2, v222 dst_sel:DWORD dst_unused:UNUSED_PAD src0_sel:DWORD src1_sel:BYTE_1\n\t" \
  "ds_read_u16 v234, v234 offset:10528\n\t" \
  "v_mfma_f32_16x16x32_bf16 v[84:87], v[208:211], v[170:173], v[84:87]\n\t" \
  "v_lshlrev_b32_sdwa v224, 1, v188 dst_sel:DWORD dst_unused:UNUSED_PAD src0_sel:DWORD src1_sel:WORD_1\n\t" \
  "v_lshlrev_b32_sdwa v225, 1, v189 dst_sel:DWORD dst_unused:UNUSED_PAD src0_sel:DWORD src1_sel:WORD_1\n\t" \
  "ds_read_b32 v220, v220 offset:10016\n\t" \
  "ds_read_b64_tr_b16 v[208:209], v248 offset:43048\n\t" \
  "v_mfma_f32_16x16x32_bf16 v[16:19], v[212:215], v[146:149], v[16:19]\n\t" \
  "v_nop\n\t" \
  "s_add_u32 s27, s27, s26\n\t" \
  "s_mov_b64 exec, s[30:31]\n\t" \
  "buffer_load_dword v237, v254, s[8:11], s27 offen\n\t" \
  "v_mfma_f32_16x16x32_bf16 v[64:67], v[212:215], v[162:165], v[64:67]\n\t" \
  "s_mov_b64 exec, -1\n\t" \
  "v_lshlrev_b32_sdwa v227, 1, v191 dst_sel:DWORD dst_unused:UNUSED_PAD src0_sel:DWORD src1_sel:WORD_1\n\t" \
  "v_fma_mix_f32 v229, |v195|, v229, v229 op_sel:[0,1,0] op_sel_hi:[0,1,1] clamp\n\t" \
  "ds_read_b32 v223, v223 offset:10016\n\t" \
  "ds_read_b32 v221, v221 offset:10016\n\t" \
  "v_mfma_f32_16x16x32_bf16 v[88:91], v[212:215], v[170:173], v[88:91]\n\t" \
  "s_setprio 1\n\t" \
  "v_fma_mix_f32 v230, |v192|, v230, v230 op_sel:[0,1,0] op_sel_hi:[0,1,1] clamp\n\t" \
  "v_fma_mix_f32 v231, |v193|, v231, v231 op_sel:[0,1,0] op_sel_hi:[0,1,1] clamp\n\t" \
  "s_setprio 2\n\t" \
  "s_waitcnt lgkmcnt(4)\n\t" \
  "ds_read_b64_tr_b16 v[210:211], v248 offset:44072\n\t" \
  "v_mfma_f32_16x16x32_bf16 v[40:43], v[212:215], v[154:157], v[40:43]\n\t" \
  "v_lshlrev_b32_sdwa v226, 1, v190 dst_sel:DWORD dst_unused:UNUSED_PAD src0_sel:DWORD src1_sel:WORD_1\n\t" \
  "v_fma_mix_f32 v228, |v194|, v228, v228 op_sel:[0,1,0] op_sel_hi:[0,1,1] clamp\n\t" \
  "ds_read_b32 v222, v222 offset:10016\n\t" \
  "ds_read_b64_tr_b16 v[212:213], v248 offset:46120\n\t" \
  "v_mfma_f32_16x16x32_bf16 v[20:23], v[216:219], v[146:149], v[20:23]\n\t" \
  "v_or_b32 v203, v232, v233\n\t" \
  "v_or_b32 v202, v234, v235\n\t" \
  "ds_read_u16_d16_hi v225, v225 offset:10528\n\t" \
  "v_mfma_f32_16x16x32_bf16 v[68:71], v[216:219], v[162:165], v[68:71]\n\t" \
  "v_fma_f32 v195, |v195|, v229, v195 mul:4\n\t" \
  "v_fma_f32 v192, |v192|, v230, v192 mul:4\n\t" \
  "s_waitcnt lgkmcnt(3)\n\t" \
  "ds_read_u16 v224, v224 offset:10528\n\t" \
  "v_mfma_f32_16x16x32_bf16 v[144:147], v[208:211], v[96:99], 0\n\t" \
  "v_lshlrev_b32_sdwa v232, 1, v204 dst_sel:DWORD dst_unused:UNUSED_PAD src0_sel:DWORD src1_sel:WORD_1\n\t" \
  "v_fma_f32 v193, |v193|, v231, v193 mul:4\n\t" \
  "ds_read_b64_tr_b16 v[214:215], v248 offset:47144\n\t" \
  "v_mfma_f32_16x16x32_bf16 v[44:47], v[216:219], v[154:157], v[44:47]\n\t" \
  "v_cvt_pknorm_i16_f32 v230, |v198|, |v199|\n\t" \
  "v_fma_f32 v194, |v194|, v228, v194 mul:4\n\t" \
  "ds_read_u16_d16_hi v227, v227 offset:10528\n\t" \
  "v_mfma_f32_16x16x32_bf16 v[92:95], v[216:219], v[170:173], v[92:95]\n\t" \
  "v_cvt_pk_bf16_f32 v195, v194, v195\n\t" \
  "v_cvt_pk_bf16_f32 v194, v192, v193\n\t" \
  "ds_read_b64_tr_b16 v[216:217], v248 offset:45096\n\t" \
  "v_mfma_f32_16x16x32_bf16 v[160:163], v[208:211], v[104:107], 0\n\t" \
  "v_lshlrev_b32_sdwa v231, 2, v230 dst_sel:DWORD dst_unused:UNUSED_PAD src0_sel:DWORD src1_sel:BYTE_3\n\t" \
  "v_cvt_pknorm_i16_f32 v228, |v196|, |v197|\n\t" \
  "s_setprio 2\n\t" \
  "ds_read_b64_tr_b16 v[218:219], v248 offset:48168\n\t" \
  "v_mfma_f32_16x16x32_bf16 v[152:155], v[208:211], v[100:103], 0\n\t" \
  "v_lshlrev_b32_sdwa v229, 2, v228 dst_sel:DWORD dst_unused:UNUSED_PAD src0_sel:DWORD src1_sel:BYTE_3\n\t" \
  "v_lshlrev_b32_sdwa v228, 2, v228 dst_sel:DWORD dst_unused:UNUSED_PAD src0_sel:DWORD src1_sel:BYTE_1\n\t" \
  "ds_read_u16 v226, v226 offset:10528\n\t" \
  "v_mfma_f32_16x16x32_bf16 v[168:171], v[208:211], v[108:111], 0\n\t" \
  "v_lshlrev_b32_sdwa v234, 1, v206 dst_sel:DWORD dst_unused:UNUSED_PAD src0_sel:DWORD src1_sel:WORD_1\n\t" \
  "v_lshlrev_b32_sdwa v233, 1, v205 dst_sel:DWORD dst_unused:UNUSED_PAD src0_sel:DWORD src1_sel:WORD_1\n\t" \
  "s_waitcnt lgkmcnt(4)\n\t" \
  "ds_read_b64_tr_b16 v[208:209], v248 offset:43560\n\t" \
  "v_mfma_f32_16x16x32_bf16 v[144:147], v[212:215], v[112:115], v[144:147]\n\t" \
  "v_lshlrev_b32_sdwa v230, 2, v230 dst_sel:DWORD dst_unused:UNUSED_PAD src0_sel:DWORD src1_sel:BYTE_1\n\t" \
  "v_fma_mix_f32 v221, |v181|, v221, v221 op_sel:[0,1,0] op_sel_hi:[0,1,1] clamp\n\t" \
  "ds_read_b32 v231, v231 offset:10016\n\t" \
  "v_mfma_f32_16x16x32_bf16 v[168:171], v[212:215], v[124:127], v[168:171]\n\t" \
  "v_lshlrev_b32_sdwa v235, 1, v207 dst_sel:DWORD dst_unused:UNUSED_PAD src0_sel:DWORD src1_sel:WORD_1\n\t" \
  "ds_read_b32 v228, v228 offset:10016\n\t" \
  "ds_read_b32 v229, v229 offset:10016\n\t" \
  "v_mfma_f32_16x16x32_bf16 v[160:163], v[212:215], v[120:123], v[160:163]\n\t" \
  "v_fma_mix_f32 v220, |v180|, v220, v220 op_sel:[0,1,0] op_sel_hi:[0,1,1] clamp\n\t" \
  "s_waitcnt lgkmcnt(5)\n\t" \
  "ds_read_b64_tr_b16 v[210:211], v248 offset:44584\n\t" \
  "ds_read_u16_d16_hi v233, v233 offset:10528\n\t" \
  "v_mfma_f32_16x16x32_bf16 v[152:155], v[212:215], v[116:119], v[152:155]\n\t" \
  "v_fma_mix_f32 v223, |v183|, v223, v223 op_sel:[0,1,0] op_sel_hi:[0,1,1] clamp\n\t" \
  "v_fma_mix_f32 v222, |v182|, v222, v222 op_sel:[0,1,0] op_sel_hi:[0,1,1] clamp\n\t" \
  "s_waitcnt lgkmcnt(6)\n\t" \
  "ds_read_b64_tr_b16 v[212:213], v248 offset:46632\n\t" \
  "v_mfma_f32_16x16x32_bf16 v[144:147], v[216:219], v[128:131], v[144:147]\n\t" \
  "v_or_b32 v188, v224, v225\n\t" \
  "v_or_b32 v189, v226, v227\n\t" \
  "ds_read_b64_tr_b16 v[214:215], v248 offset:47656\n\t" \
  "v_mfma_f32_16x16x32_bf16 v[160:163], v[216:219], v[136:139], v[160:163]\n\t" \
  "s_nop 0\n\t" \
  "v_fma_f32 v182, |v182|, v222, v182 mul:4\n\t" \
  "v_fma_f32 v183, |v183|, v223, v183 mul:4\n\t" \
  "ds_read_b32 v230, v230 offset:10016\n\t" \
  "v_mfma_f32_16x16x32_bf16 v[152:155], v[216:219], v[132:135], v[152:155]\n\t" \
  "v_fma_f32 v181, |v181|, v221, v181 mul:4\n\t" \
  "v_fma_f32 v180, |v180|, v220, v180 mul:4\n\t" \
  "ds_read_u16_d16_hi v235, v235 offset:10528\n\t" \
  "ds_read_u16 v232, v232 offset:10528\n\t" \
  "v_mfma_f32_16x16x32_bf16 v[168:171], v[216:219], v[140:143], v[168:171]\n\t" \
  "v_cvt_pknorm_i16_f32 v220, |v146|, |v147|\n\t" \
  "v_cvt_pk_bf16_f32 v180, v180, v181\n\t" \
  "s_setprio 0\n\t" \
  "s_waitcnt lgkmcnt(6)\n\t" \
  "ds_read_b64_tr_b16 v[216:217], v248 offset:45608\n\t" \
  "v_mfma_f32_16x16x32_bf16 v[148:151], v[208:211], v[96:99], 0\n\t" \
  "v_cvt_pk_bf16_f32 v181, v182, v183\n\t" \
  "v_cvt_pknorm_i16_f32 v222, |v144|, |v145|\n\t" \
  "s_waitcnt vmcnt(2)\n\t" \
  "ds_read_b64_tr_b16 v[218:219], v248 offset:48680\n\t" \
  "s_waitcnt lgkmcnt(5)\n\t" \
  "v_mfma_f32_16x16x32_bf16 v[156:159], v[208:211], v[100:103], 0\n\t" \
  "v_lshlrev_b32_sdwa v221, 2, v220 dst_sel:DWORD dst_unused:UNUSED_PAD src0_sel:DWORD src1_sel:BYTE_3\n\t" \
  "v_lshlrev_b32_sdwa v220, 2, v220 dst_sel:DWORD dst_unused:UNUSED_PAD src0_sel:DWORD src1_sel:BYTE_1\n\t" \
  "s_mov_b64 exec, 3\n\t" \
  "ds_add_rtn_u32 v246, v251, v250 offset:47168\n\t" \
  "v_mfma_f32_16x16x32_bf16 v[164:167], v[208:211], v[104:107], 0\n\t" \
  "s_mov_b64 exec, -1\n\t" \
  "v_lshlrev_b32_sdwa v223, 2, v222 dst_sel:DWORD dst_unused:UNUSED_PAD src0_sel:DWORD src1_sel:BYTE_3\n\t" \
  "v_lshlrev_b32_sdwa v222, 2, v222 dst_sel:DWORD dst_unused:UNUSED_PAD src0_sel:DWORD src1_sel:BYTE_1\n\t" \
  "ds_read_u16 v234, v234 offset:10528\n\t" \
  "v_mfma_f32_16x16x32_bf16 v[156:159], v[212:215], v[116:119], v[156:159]\n\t" \
  "v_lshlrev_b32_sdwa v226, 1, v152 dst_sel:DWORD dst_unused:UNUSED_PAD src0_sel:DWORD src1_sel:WORD_1\n\t" \
  "v_lshlrev_b32_sdwa v227, 1, v153 dst_sel:DWORD dst_unused:UNUSED_PAD src0_sel:DWORD src1_sel:WORD_1\n\t" \
  "ds_read_b32 v223, v223 offset:10016\n\t" \
  "v_mfma_f32_16x16x32_bf16 v[148:151], v[212:215], v[112:115], v[148:151]\n\t" \
  "s_waitcnt lgkmcnt(2)\n\t" \
  "v_lshlrev_b32_sdwa v225, 1, v155 dst_sel:DWORD dst_unused:UNUSED_PAD src0_sel:DWORD src1_sel:WORD_1\n\t" \
  "v_readfirstlane_b32 s4, v246\n\t" \
  "ds_read_b32 v221, v221 offset:10016\n\t" \
  "v_mfma_f32_16x16x32_bf16 v[172:175], v[208:211], v[108:111], 0\n\t" \
  "s_cmpk_lt_u32 s4, 3072\n\t" \
  "v_lshlrev_b32_sdwa v224, 1, v154 dst_sel:DWORD dst_unused:UNUSED_PAD src0_sel:DWORD src1_sel:WORD_1\n\t" \
  "s_cbranch_scc1 .Lcnx_w0_5\n\t" \
  ".Lcnx_g0_5:\n\t" \
  "ds_read_b32 v220, v220 offset:10016\n\t" \
  "v_mfma_f32_16x16x32_bf16 v[164:167], v[212:215], v[120:123], v[164:167]\n\t" \
  "v_fma_mix_f32 v229, |v197|, v229, v229 op_sel:[0,1,0] op_sel_hi:[0,1,1] clamp\n\t" \
  "v_fma_mix_f32 v228, |v196|, v228, v228 op_sel:[0,1,0] op_sel_hi:[0,1,1] clamp\n\t" \
  "ds_read_b64_tr_b16 v[210:211], v249 offset:13840\n\t" \
  "ds_read_b64_tr_b16 v[208:209], v249 offset:12304\n\t" \
  "v_mfma_f32_16x16x32_bf16 v[172:175], v[212:215], v[124:127], v[172:175]\n\t" \
  "v_fma_mix_f32 v230, |v198|, v230, v230 op_sel:[0,1,0] op_sel_hi:[0,1,1] clamp\n\t" \
  "v_fma_mix_f32 v231, |v199|, v231, v231 op_sel:[0,1,0] op_sel_hi:[0,1,1] clamp\n\t" \
  "s_waitcnt lgkmcnt(5)\n\t" \
  "ds_read_b32 v222, v222 offset:10016\n\t" \
  "ds_read_b64_tr_b16 v[212:213], v249 offset:12312\n\t" \
  "v_mfma_f32_16x16x32_bf16 v[148:151], v[216:219], v[128:131], v[148:151]\n\t" \
  "s_mov_b32 m0, s45\n\t" \
  "v_or_b32 v204, v232, v233\n\t" \
  "v_or_b32 v205, v234, v235\n\t" \
  "s_mov_b32 exec_hi, 0xffff\n\t" \
  "buffer_load_dwordx4 v252, s[16:19], s33 offen lds\n\t" \
  "v_mfma_f32_16x16x32_bf16 v[156:159], v[216:219], v[132:135], v[156:159]\n\t" \
  "s_mov_b32 exec_hi, -1\n\t" \
  "v_fma_f32 v196, |v196|, v228, v196 mul:4\n\t" \
  "v_fma_f32 v197, |v197|, v229, v197 mul:4\n\t" \
  "s_add_u32 s33, s33, s32\n\t" \
  "ds_read_u16_d16_hi v227, v227 offset:10528\n\t" \
  "ds_read_b64_tr_b16 v[214:215], v249 offset:13848\n\t" \
  "v_mfma_f32_16x16x32_bf16 v[164:167], v[216:219], v[136:139], v[164:167]\n\t" \
  "v_fma_f32 v198, |v198|, v230, v198 mul:4\n\t" \
  "v_fma_f32 v199, |v199|, v231, v199 mul:4\n\t" \
  "ds_read_u16_d16_hi v225, v225 offset:10528\n\t" \
  "v_mfma_f32_16x16x32_bf16 v[172:175], v[216:219], v[140:143], v[172:175]\n\t" \
  "v_cvt_pk_bf16_f32 v196, v196, v197\n\t" \
  "v_cvt_pk_bf16_f32 v197, v198, v199\n\t" \
  "ds_read_b64_tr_b16 v[216:217], v249 offset:12368\n\t" \
  "s_waitcnt lgkmcnt(6)\n\t" \
  "v_mfma_f32_16x16x32_bf16 v[0:3], v[208:211], v[178:181], v[0:3]\n\t" \
  "v_cvt_pknorm_i16_f32 v230, |v160|, |v161|\n\t" \
  "v_cvt_pknorm_i16_f32 v228, |v162|, |v163|\n\t" \
  "ds_read_b64_tr_b16 v[218:219], v249 offset:13904\n\t" \
  "v_mfma_f32_16x16x32_bf16 v[24:27], v[208:211], v[186:189], v[24:27]\n\t" \
  "v_lshlrev_b32_sdwa v229, 2, v228 dst_sel:DWORD dst_unused:UNUSED_PAD src0_sel:DWORD src1_sel:BYTE_3\n\t" \
  "v_lshlrev_b32_sdwa v228, 2, v228 dst_sel:DWORD dst_unused:UNUSED_PAD src0_sel:DWORD src1_sel:BYTE_1\n\t" \
  "ds_read_b32 v229, v229 offset:10016\n\t" \
  "ds_read_u16 v224, v224 offset:10528\n\t" \
  "v_mfma_f32_16x16x32_bf16 v[48:51], v[208:211], v[194:197], v[48:51]\n\t" \
  "v_lshlrev_b32_sdwa v231, 2, v230 dst_sel:DWORD dst_unused:UNUSED_PAD src0_sel:DWORD src1_sel:BYTE_3\n\t" \
  "s_setprio 1\n\t" \
  "s_add_u32 m0, s45, s35\n\t" \
  "s_waitcnt lgkmcnt(5)\n\t" \
  "ds_read_b32 v228, v228 offset:10016\n\t" \
  "v_mfma_f32_16x16x32_bf16 v[72:75], v[208:211], v[202:205], v[72:75]\n\t" \
  "v_lshlrev_b32_sdwa v232, 1, v170 dst_sel:DWORD dst_unused:UNUSED_PAD src0_sel:DWORD src1_sel:WORD_1\n\t" \
  "v_lshlrev_b32_sdwa v233, 1, v171 dst_sel:DWORD dst_unused:UNUSED_PAD src0_sel:DWORD src1_sel:WORD_1\n\t" \
  "s_mov_b32 exec_hi, 0xffff\n\t" \
  "buffer_load_dwordx4 v252, s[16:19], s34 offen lds\n\t" \
  "v_mfma_f32_16x16x32_bf16 v[4:7], v[212:215], v[178:181], v[4:7]\n\t" \
  "s_mov_b32 exec_hi, -1\n\t" \
  "s_add_u32 s34, s34, s32\n\t" \
  "v_lshlrev_b32_sdwa v235, 1, v169 dst_sel:DWORD dst_unused:UNUSED_PAD src0_sel:DWORD src1_sel:WORD_1\n\t" \
  "ds_read_u16 v226, v226 offset:10528\n\t" \
  "v_mfma_f32_16x16x32_bf16 v[28:31], v[212:215], v[186:189], v[28:31]\n\t" \
  "v_lshlrev_b32_sdwa v234, 1, v168 dst_sel:DWORD dst_unused:UNUSED_PAD src0_sel:DWORD src1_sel:WORD_1\n\t" \
  "v_lshlrev_b32_sdwa v230, 2, v230 dst_sel:DWORD dst_unused:UNUSED_PAD src0_sel:DWORD src1_sel:BYTE_1\n\t" \
  "ds_read_b32 v230, v230 offset:10016\n\t" \
  "ds_read_b64_tr_b16 v[208:209], v249 offset:12376\n\t" \
  "v_mfma_f32_16x16x32_bf16 v[52:55], v[212:215], v[194:197], v[52:55]\n\t" \
  "v_fma_mix_f32 v220, |v146|, v220, v220 op_sel:[0,1,0] op_sel_hi:[0,1,1] clamp\n\t" \
  "v_fma_mix_f32 v221, |v147|, v221, v221 op_sel:[0,1,0] op_sel_hi:[0,1,1] clamp\n\t" \
  "ds_read_b64_tr_b16 v[210:211], v249 offset:13912\n\t" \
  "v_mfma_f32_16x16x32_bf16 v[76:79], v[212:215], v[202:205], v[76:79]\n\t" \
  "s_waitcnt lgkmcnt(7)\n\t" \
  "v_fma_mix_f32 v223, |v145|, v223, v223 op_sel:[0,1,0] op_sel_hi:[0,1,1] clamp\n\t" \
  "v_fma_mix_f32 v222, |v144|, v222, v222 op_sel:[0,1,0] op_sel_hi:[0,1,1] clamp\n\t" \
  "s_add_u32 m0, s25, 24600\n\t" \
  "s_waitcnt lgkmcnt(3)\n\t" \
  "ds_read_b32 v231, v231 offset:10016\n\t" \
  "ds_read_b64_tr_b16 v[212:213], v249 offset:12432\n\t" \
  "v_mfma_f32_16x16x32_bf16 v[8:11], v[216:219], v[178:181], v[8:11]\n\t" \
  "v_or_b32 v155, v224, v225\n\t" \
  "v_or_b32 v154, v226, v227\n\t" \
  "s_setprio 1\n\t" \
  "s_setprio 0\n\t" \
  "ds_read_u16_d16_hi v233, v233 offset:10528\n\t" \
  "v_mfma_f32_16x16x32_bf16 v[56:59], v[216:219], v[194:197], v[56:59]\n\t" \
  "v_fma_f32 v144, |v144|, v222, v144 mul:4\n\t" \
  "v_fma_f32 v145, |v145|, v223, v145 mul:4\n\t" \
  "ds_read_u16 v232, v232 offset:10528\n\t" \
  "v_mfma_f32_16x16x32_bf16 v[32:35], v[216:219], v[186:189], v[32:35]\n\t" \
  "v_fma_f32 v146, |v146|, v220, v146 mul:4\n\t" \
  "v_fma_f32 v147, |v147|, v221, v147 mul:4\n\t" \
  "ds_read_b64_tr_b16 v[214:215], v249 offset:13968\n\t" \
  "v_mfma_f32_16x16x32_bf16 v[80:83], v[216:219], v[202:205], v[80:83]\n\t" \
  "s_waitcnt lgkmcnt(5)\n\t" \
  "v_cvt_pk_bf16_f32 v147, v146, v147\n\t" \
  "v_cvt_pk_bf16_f32 v146, v144, v145\n\t" \
  "s_add_u32 s24, s24, s26\n\t" \
  "buffer_load_dwordx4 v254, s[8:11], s24 offen lds\n\t" \
  "v_mfma_f32_16x16x32_bf16 v[12:15], v[208:211], v[178:181], v[12:15]\n\t" \
  "v_cvt_pknorm_i16_f32 v220, |v148|, |v149|\n\t" \
  "v_cvt_pknorm_i16_f32 v222, |v150|, |v151|\n\t" \
  "ds_read_b64_tr_b16 v[218:219], v249 offset:13976\n\t" \
  "ds_read_b64_tr_b16 v[216:217], v249 offset:12440\n\t" \
  "v_mfma_f32_16x16x32_bf16 v[36:39], v[208:211], v[186:189], v[36:39]\n\t" \
  "v_lshlrev_b32_sdwa v221, 2, v220 dst_sel:DWORD dst_unused:UNUSED_PAD src0_sel:DWORD src1_sel:BYTE_3\n\t" \
  "v_lshlrev_b32_sdwa v220, 2, v220 dst_sel:DWORD dst_unused:UNUSED_PAD src0_sel:DWORD src1_sel:BYTE_1\n\t" \
  "ds_read_u16_d16_hi v235, v235 offset:10528\n\t" \
  "v_mfma_f32_16x16x32_bf16 v[60:63], v[208:211], v[194:197], v[60:63]\n\t" \
  "s_waitcnt lgkmcnt(3)\n\t" \
  "v_lshlrev_b32_sdwa v223, 2, v222 dst_sel:DWORD dst_unused:UNUSED_PAD src0_sel:DWORD src1_sel:BYTE_3\n\t" \
  "v_lshlrev_b32_sdwa v222, 2, v222 dst_sel:DWORD dst_unused:UNUSED_PAD src0_sel:DWORD src1_sel:BYTE_1\n\t" \
  "ds_read_u16 v234, v234 offset:10528\n\t" \
  "v_mfma_f32_16x16x32_bf16 v[84:87], v[208:211], v[202:205], v[84:87]\n\t" \
  "v_lshlrev_b32_sdwa v224, 1, v156 dst_sel:DWORD dst_unused:UNUSED_PAD src0_sel:DWORD src1_sel:WORD_1\n\t" \
  "v_lshlrev_b32_sdwa v225, 1, v157 dst_sel:DWORD dst_unused:UNUSED_PAD src0_sel:DWORD src1_sel:WORD_1\n\t" \
  "ds_read_b32 v220, v220 offset:10016\n\t" \
  "ds_read_b64_tr_b16 v[208:209], v248 offset:49192\n\t" \
  "v_mfma_f32_16x16x32_bf16 v[16:19], v[212:215], v[178:181], v[16:19]\n\t" \
  "v_nop\n\t" \
  "s_add_u32 s27, s27, s26\n\t" \
  "s_mov_b64 exec, s[30:31]\n\t" \
  "buffer_load_dword v237, v254, s[8:11], s27 offen\n\t" \
  "v_mfma_f32_16x16x32_bf16 v[64:67], v[212:215], v[194:197], v[64:67]\n\t" \
  "s_mov_b64 exec, -1\n\t" \
  "v_lshlrev_b32_sdwa v227, 1, v159 dst_sel:DWORD dst_unused:UNUSED_PAD src0_sel:DWORD src1_sel:WORD_1\n\t" \
  "v_fma_mix_f32 v229, |v163|, v229, v229 op_sel:[0,1,0] op_sel_hi:[0,1,1] clamp\n\t" \
  "ds_read_b32 v223, v223 offset:10016\n\t" \
  "ds_read_b32 v221, v221 offset:10016\n\t" \
  "v_mfma_f32_16x16x32_bf16 v[88:91], v[212:215], v[202:205], v[88:91]\n\t" \
  "s_setprio 1\n\t" \
  "v_fma_mix_f32 v230, |v160|, v230, v230 op_sel:[0,1,0] op_sel_hi:[0,1,1] clamp\n\t" \
  "v_fma_mix_f32 v231, |v161|, v231, v231 op_sel:[0,1,0] op_sel_hi:[0,1,1] clamp\n\t" \
  "s_setprio 2\n\t" \
  "s_waitcnt lgkmcnt(4)\n\t" \
  "ds_read_b64_tr_b16 v[210:211], v248 offset:50216\n\t" \
  "v_mfma_f32_16x16x32_bf16 v[40:43], v[212:215], v[186:189], v[40:43]\n\t" \
  "v_lshlrev_b32_sdwa v226, 1, v158 dst_sel:DWORD dst_unused:UNUSED_PAD src0_sel:DWORD src1_sel:WORD_1\n\t" \
  "v_fma_mix_f32 v228, |v162|, v228, v228 op_sel:[0,1,0] op_sel_hi:[0,1,1] clamp\n\t" \
  "ds_read_b32 v222, v222 offset:10016\n\t" \
  "ds_read_b64_tr_b16 v[212:213], v248 offset:52264\n\t" \
  "v_mfma_f32_16x16x32_bf16 v[20:23], v[216:219], v[178:181], v[20:23]\n\t" \
  "v_or_b32 v171, v232, v233\n\t" \
  "v_or_b32 v170, v234, v235\n\t" \
  "ds_read_u16_d16_hi v225, v225 offset:10528\n\t" \
  "v_mfma_f32_16x16x32_bf16 v[68:71], v[216:219], v[194:197], v[68:71]\n\t" \
  "v_fma_f32 v163, |v163|, v229, v163 mul:4\n\t" \
  "v_fma_f32 v160, |v160|, v230, v160 mul:4\n\t" \
  "s_waitcnt lgkmcnt(3)\n\t" \
  "ds_read_u16 v224, v224 offset:10528\n\t" \
  "v_mfma_f32_16x16x32_bf16 v[176:179], v[208:211], v[96:99], 0\n\t" \
  "v_lshlrev_b32_sdwa v232, 1, v172 dst_sel:DWORD dst_unused:UNUSED_PAD src0_sel:DWORD src1_sel:WORD_1\n\t" \
  "v_fma_f32 v161, |v161|, v231, v161 mul:4\n\t" \
  "ds_read_b64_tr_b16 v[214:215], v248 offset:53288\n\t" \
  "v_mfma_f32_16x16x32_bf16 v[44:47], v[216:219], v[186:189], v[44:47]\n\t" \
  "v_cvt_pknorm_i16_f32 v230, |v166|, |v167|\n\t" \
  "v_fma_f32 v162, |v162|, v228, v162 mul:4\n\t" \
  "ds_read_u16_d16_hi v227, v227 offset:10528\n\t" \
  "v_mfma_f32_16x16x32_bf16 v[92:95], v[216:219], v[202:205], v[92:95]\n\t" \
  "v_cvt_pk_bf16_f32 v163, v162, v163\n\t" \
  "v_cvt_pk_bf16_f32 v162, v160, v161\n\t" \
  "ds_read_b64_tr_b16 v[216:217], v248 offset:51240\n\t" \
  "v_mfma_f32_16x16x32_bf16 v[192:195], v[208:211], v[104:107], 0\n\t" \
  "v_lshlrev_b32_sdwa v231, 2, v230 dst_sel:DWORD dst_unused:UNUSED_PAD src0_sel:DWORD src1_sel:BYTE_3\n\t" \
  "v_cvt_pknorm_i16_f32 v228, |v164|, |v165|\n\t" \
  "s_setprio 2\n\t" \
  "ds_read_b64_tr_b16 v[218:219], v248 offset:54312\n\t" \
  "v_mfma_f32_16x16x32_bf16 v[184:187], v[208:211], v[100:103], 0\n\t" \
  "v_lshlrev_b32_sdwa v229, 2, v228 dst_sel:DWORD dst_unused:UNUSED_PAD src0_sel:DWORD src1_sel:BYTE_3\n\t" \
  "v_lshlrev_b32_sdwa v228, 2, v228 dst_sel:DWORD dst_unused:UNUSED_PAD src0_sel:DWORD src1_sel:BYTE_1\n\t" \
  "ds_read_u16 v226, v226 offset:10528\n\t" \
  "v_mfma_f32_16x16x32_bf16 v[200:203], v[208:211], v[108:111], 0\n\t" \
  "v_lshlrev_b32_sdwa v234, 1, v174 dst_sel:DWORD dst_unused:UNUSED_PAD src0_sel:DWORD src1_sel:WORD_1\n\t" \
  "v_lshlrev_b32_sdwa v233, 1, v173 dst_sel:DWORD dst_unused:UNUSED_PAD src0_sel:DWORD src1_sel:WORD_1\n\t" \
  "s_waitcnt lgkmcnt(4)\n\t" \
  "ds_read_b64_tr_b16 v[208:209], v248 offset:49704\n\t" \
  "v_mfma_f32_16x16x32_bf16 v[176:179], v[212:215], v[112:115], v[176:179]\n\t" \
  "v_lshlrev_b32_sdwa v230, 2, v230 dst_sel:DWORD dst_unused:UNUSED_PAD src0_sel:DWORD src1_sel:BYTE_1\n\t" \
  "v_fma_mix_f32 v221, |v149|, v221, v221 op_sel:[0,1,0] op_sel_hi:[0,1,1] clamp\n\t" \
  "ds_read_b32 v231, v231 offset:10016\n\t" \
  "v_mfma_f32_16x16x32_bf16 v[200:203], v[212:215], v[124:127], v[200:203]\n\t" \
  "v_lshlrev_b32_sdwa v235, 1, v175 dst_sel:DWORD dst_unused:UNUSED_PAD src0_sel:DWORD src1_sel:WORD_1\n\t" \
  "ds_read_b32 v228, v228 offset:10016\n\t" \
  "ds_read_b32 v229, v229 offset:10016\n\t" \
  "v_mfma_f32_16x16x32_bf16 v[192:195], v[212:215], v[120:123], v[192:195]\n\t" \
  "v_fma_mix_f32 v220, |v148|, v220, v220 op_sel:[0,1,0] op_sel_hi:[0,1,1] clamp\n\t" \
  "s_waitcnt lgkmcnt(5)\n\t" \
  "ds_read_b64_tr_b16 v[210:211], v248 offset:50728\n\t" \
  "ds_read_u16_d16_hi v233, v233 offset:10528\n\t" \
  "v_mfma_f32_16x16x32_bf16 v[184:187], v[212:215], v[116:119], v[184:187]\n\t" \
  "v_fma_mix_f32 v223, |v151|, v223, v223 op_sel:[0,1,0] op_sel_hi:[0,1,1] clamp\n\t" \
  "v_fma_mix_f32 v222, |v150|, v222, v222 op_sel:[0,1,0] op_sel_hi:[0,1,1] clamp\n\t" \
  "s_waitcnt lgkmcnt(6)\n\t" \
  "ds_read_b64_tr_b16 v[212:213], v248 offset:52776\n\t" \
  "v_mfma_f32_16x16x32_bf16 v[176:179], v[216:219], v[128:131], v[176:179]\n\t" \
  "v_or_b32 v156, v224, v225\n\t" \
  "v_or_b32 v157, v226, v227\n\t" \
  "ds_read_b64_tr_b16 v[214:215], v248 offset:53800\n\t" \
  "v_mfma_f32_16x16x32_bf16 v[192:195], v[216:219], v[136:139], v[192:195]\n\t" \
  "s_nop 0\n\t" \
  "v_fma_f32 v150, |v150|, v222, v150 mul:4\n\t" \
  "v_fma_f32 v151, |v151|, v223, v151 mul:4\n\t" \
  "ds_read_b32 v230, v230 offset:10016\n\t" \
  "v_mfma_f32_16x16x32_bf16 v[184:187], v[216:219], v[132:135], v[184:187]\n\t" \
  "v_fma_f32 v149, |v149|, v221, v149 mul:4\n\t" \
  "v_fma_f32 v148, |v148|, v220, v148 mul:4\n\t" \
  "ds_read_u16_d16_hi v235, v235 offset:10528\n\t" \
  "ds_read_u16 v232, v232 offset:10528\n\t" \
  "v_mfma_f32_16x16x32_bf16 v[200:203], v[216:219], v[140:143], v[200:203]\n\t" \
  "v_cvt_pknorm_i16_f32 v220, |v178|, |v179|\n\t" \
  "v_cvt_pk_bf16_f32 v148, v148, v149\n\t" \
  "s_setprio 0\n\t" \
  "s_waitcnt lgkmcnt(6)\n\t" \
  "ds_read_b64_tr_b16 v[216:217], v248 offset:51752\n\t" \
  "v_mfma_f32_16x16x32_bf16 v[180:183], v[208:211], v[96:99], 0\n\t" \
  "v_cvt_pk_bf16_f32 v149, v150, v151\n\t" \
  "v_cvt_pknorm_i16_f32 v222, |v176|, |v177|\n\t" \
  "s_waitcnt vmcnt(2)\n\t" \
  "ds_read_b64_tr_b16 v[218:219], v248 offset:54824\n\t" \
  "s_waitcnt lgkmcnt(5)\n\t" \
  "v_mfma_f32_16x16x32_bf16 v[188:191], v[208:211], v[100:103], 0\n\t" \
  "v_lshlrev_b32_sdwa v221, 2, v220 dst_sel:DWORD dst_unused:UNUSED_PAD src0_sel:DWORD src1_sel:BYTE_3\n\t" \
  "v_lshlrev_b32_sdwa v220, 2, v220 dst_sel:DWORD dst_unused:UNUSED_PAD src0_sel:DWORD src1_sel:BYTE_1\n\t" \
  "s_mov_b64 exec, 3\n\t" \
  "ds_add_rtn_u32 v246, v251, v250 offset:47176\n\t" \
  "v_mfma_f32_16x16x32_bf16 v[196:199], v[208:211], v[104:107], 0\n\t" \
  "s_mov_b64 exec, -1\n\t" \
  "v_lshlrev_b32_sdwa v223, 2, v222 dst_sel:DWORD dst_unused:UNUSED_PAD src0_sel:DWORD src1_sel:BYTE_3\n\t" \
  "v_lshlrev_b32_sdwa v222, 2, v222 dst_sel:DWORD dst_unused:UNUSED_PAD src0_sel:DWORD src1_sel:BYTE_1\n\t" \
  "ds_read_u16 v234, v234 offset:10528\n\t" \
  "v_mfma_f32_16x16x32_bf16 v[188:191], v[212:215], v[116:119], v[188:191]\n\t" \
  "v_lshlrev_b32_sdwa v226, 1, v184 dst_sel:DWORD dst_unused:UNUSED_PAD src0_sel:DWORD src1_sel:WORD_1\n\t" \
  "v_lshlrev_b32_sdwa v227, 1, v185 dst_sel:DWORD dst_unused:UNUSED_PAD src0_sel:DWORD src1_sel:WORD_1\n\t" \
  "ds_read_b32 v223, v223 offset:10016\n\t" \
  "v_mfma_f32_16x16x32_bf16 v[180:183], v[212:215], v[112:115], v[180:183]\n\t" \
  "s_waitcnt lgkmcnt(2)\n\t" \
  "v_lshlrev_b32_sdwa v225, 1, v187 dst_sel:DWORD dst_unused:UNUSED_PAD src0_sel:DWORD src1_sel:WORD_1\n\t" \
  "v_readfirstlane_b32 s4, v246\n\t" \
  "ds_read_b32 v221, v221 offset:10016\n\t" \
  "v_mfma_f32_16x16x32_bf16 v[204:207], v[208:211], v[108:111], 0\n\t" \
  "s_cmpk_lt_u32 s4, 3072\n\t" \
  "v_lshlrev_b32_sdwa v224, 1, v186 dst_sel:DWORD dst_unused:UNUSED_PAD src0_sel:DWORD src1_sel:WORD_1\n\t" \
  "s_cbranch_scc1 .Lcnx_w0_6\n\t" \
  ".Lcnx_g0_6:\n\t" \
  "ds_read_b32 v220, v220 offset:10016\n\t" \
  "v_mfma_f32_16x16x32_bf16 v[196:199], v[212:215], v[120:123], v[196:199]\n\t" \
  "v_fma_mix_f32 v229, |v165|, v229, v229 op_sel:[0,1,0] op_sel_hi:[0,1,1] clamp\n\t" \
  "v_fma_mix_f32 v228, |v164|, v228, v228 op_sel:[0,1,0] op_sel_hi:[0,1,1] clamp\n\t" \
  "ds_read_b64_tr_b16 v[210:211], v249 offset:26136\n\t" \
  "ds_read_b64_tr_b16 v[208:209], v249 offset:24600\n\t" \
  "v_mfma_f32_16x16x32_bf16 v[204:207], v[212:215], v[124:127], v[204:207]\n\t" \
  "v_fma_mix_f32 v230, |v166|, v230, v230 op_sel:[0,1,0] op_sel_hi:[0,1,1] clamp\n\t" \
  "v_fma_mix_f32 v231, |v167|, v231, v231 op_sel:[0,1,0] op_sel_hi:[0,1,1] clamp\n\t" \
  "s_waitcnt lgkmcnt(5)\n\t" \
  "ds_read_b32 v222, v222 offset:10016\n\t" \
  "ds_read_b64_tr_b16 v[212:213], v249 offset:24608\n\t" \
  "v_mfma_f32_16x16x32_bf16 v[180:183], v[216:219], v[128:131], v[180:183]\n\t" \
  "s_mov_b32 m0, s46\n\t" \
  "v_or_b32 v172, v232, v233\n\t" \
  "v_or_b32 v173, v234, v235\n\t" \
  "s_mov_b32 exec_hi, 0xffff\n\t" \
  "buffer_load_dwordx4 v252, s[16:19], s33 offen lds\n\t" \
  "v_mfma_f32_16x16x32_bf16 v[188:191], v[216:219], v[132:135], v[188:191]\n\t" \
  "s_mov_b32 exec_hi, -1\n\t" \
  "v_fma_f32 v164, |v164|, v228, v164 mul:4\n\t" \
  "v_fma_f32 v165, |v165|, v229, v165 mul:4\n\t" \
  "s_add_u32 s33, s33, s32\n\t" \
  "ds_read_u16_d16_hi v227, v227 offset:10528\n\t" \
  "ds_read_b64_tr_b16 v[214:215], v249 offset:26144\n\t" \
  "v_mfma_f32_16x16x32_bf16 v[196:199], v[216:219], v[136:139], v[196:199]\n\t" \
  "v_fma_f32 v166, |v166|, v230, v166 mul:4\n\t" \
  "v_fma_f32 v167, |v167|, v231, v167 mul:4\n\t" \
  "ds_read_u16_d16_hi v225, v225 offset:10528\n\t" \
  "v_mfma_f32_16x16x32_bf16 v[204:207], v[216:219], v[140:143], v[204:207]\n\t" \
  "v_cvt_pk_bf16_f32 v164, v164, v165\n\t" \
  "v_cvt_pk_bf16_f32 v165, v166, v167\n\t" \
  "ds_read_b64_tr_b16 v[216:217], v249 offset:24664\n\t" \
  "s_waitcnt lgkmcnt(6)\n\t" \
  "v_mfma_f32_16x16x32_bf16 v[0:3], v[208:211], v[146:149], v[0:3]\n\t" \
  "v_cvt_pknorm_i16_f32 v230, |v192|, |v193|\n\t" \
  "v_cvt_pknorm_i16_f32 v228, |v194|, |v195|\n\t" \
  "ds_read_b64_tr_b16 v[218:219], v249 offset:26200\n\t" \
  "v_mfma_f32_16x16x32_bf16 v[24:27], v[208:211], v[154:157], v[24:27]\n\t" \
  "v_lshlrev_b32_sdwa v229, 2, v228 dst_sel:DWORD dst_unused:UNUSED_PAD src0_sel:DWORD src1_sel:BYTE_3\n\t" \
  "v_lshlrev_b32_sdwa v228, 2, v228 dst_sel:DWORD dst_unused:UNUSED_PAD src0_sel:DWORD src1_sel:BYTE_1\n\t" \
  "ds_read_b32 v229, v229 offset:10016\n\t" \
  "ds_read_u16 v224, v224 offset:10528\n\t" \
  "v_mfma_f32_16x16x32_bf16 v[48:51], v[208:211], v[162:165], v[48:51]\n\t" \
  "v_lshlrev_b32_sdwa v231, 2, v230 dst_sel:DWORD dst_unused:UNUSED_PAD src0_sel:DWORD src1_sel:BYTE_3\n\t" \
  "s_setprio 1\n\t" \
  "s_add_u32 m0, s46, s35\n\t" \
  "s_waitcnt lgkmcnt(5)\n\t" \
  "ds_read_b32 v228, v228 offset:10016\n\t" \
  "v_mfma_f32_16x16x32_bf16 v[72:75], v[208:211], v[170:173], v[72:75]\n\t" \
  "v_lshlrev_b32_sdwa v232, 1, v202 dst_sel:DWORD dst_unused:UNUSED_PAD src0_sel:DWORD src1_sel:WORD_1\n\t" \
  "v_lshlrev_b32_sdwa v233, 1, v203 dst_sel:DWORD dst_unused:UNUSED_PAD src0_sel:DWORD src1_sel:WORD_1\n\t" \
  "s_mov_b32 exec_hi, 0xffff\n\t" \
  "buffer_load_dwordx4 v252, s[16:19], s34 offen lds\n\t" \
  "v_mfma_f32_16x16x32_bf16 v[4:7], v[212:215], v[146:149], v[4:7]\n\t" \
  "s_mov_b32 exec_hi, -1\n\t" \
  "s_add_u32 s34, s34, s32\n\t" \
  "v_lshlrev_b32_sdwa v235, 1, v201 dst_sel:DWORD dst_unused:UNUSED_PAD src0_sel:DWORD src1_sel:WORD_1\n\t" \
  "ds_read_u16 v226, v226 offset:10528\n\t" \
  "v_mfma_f32_16x16x32_bf16 v[28:31], v[212:215], v[154:157], v[28:31]\n\t" \
  "v_lshlrev_b32_sdwa v234, 1, v200 dst_sel:DWORD dst_unused:UNUSED_PAD src0_sel:DWORD src1_sel:WORD_1\n\t" \
  "v_lshlrev_b32_sdwa v230, 2, v230 dst_sel:DWORD dst_unused:UNUSED_PAD src0_sel:DWORD src1_sel:BYTE_1\n\t" \
  "ds_read_b32 v230, v230 offset:10016\n\t" \
  "ds_read_b64_tr_b16 v[208:209], v249 offset:24672\n\t" \
  "v_mfma_f32_16x16x32_bf16 v[52:55], v[212:215], v[162:165], v[52:55]\n\t" \
  "v_fma_mix_f32 v220, |v178|, v220, v220 op_sel:[0,1,0] op_sel_hi:[0,1,1] clamp\n\t" \
  "v_fma_mix_f32 v221, |v179|, v221, v221 op_sel:[0,1,0] op_sel_hi:[0,1,1] clamp\n\t" \
  "ds_read_b64_tr_b16 v[210:211], v249 offset:26208\n\t" \
  "v_mfma_f32_16x16x32_bf16 v[76:79], v[212:215], v[170:173], v[76:79]\n\t" \
  "s_waitcnt lgkmcnt(7)\n\t" \
  "v_fma_mix_f32 v223, |v177|, v223, v223 op_sel:[0,1,0] op_sel_hi:[0,1,1] clamp\n\t" \
  "v_fma_mix_f32 v222, |v176|, v222, v222 op_sel:[0,1,0] op_sel_hi:[0,1,1] clamp\n\t" \
  "s_add_u32 m0, s25, 32800\n\t" \
  "s_waitcnt lgkmcnt(3)\n\t" \
  "ds_read_b32 v231, v231 offset:10016\n\t" \
  "ds_read_b64_tr_b16 v[212:213], v249 offset:24728\n\t" \
  "v_mfma_f32_16x16x32_bf16 v[8:11], v[216:219], v[146:149], v[8:11]\n\t" \
  "v_or_b32 v187, v224, v225\n\t" \
  "v_or_b32 v186, v226, v227\n\t" \
  "s_setprio 1\n\t" \
  "s_setprio 0\n\t" \
  "ds_read_u16_d16_hi v233, v233 offset:10528\n\t" \
  "v_mfma_f32_16x16x32_bf16 v[56:59], v[216:219], v[162:165], v[56:59]\n\t" \
  "v_fma_f32 v176, |v176|, v222, v176 mul:4\n\t" \
  "v_fma_f32 v177, |v177|, v223, v177 mul:4\n\t" \
  "ds_read_u16 v232, v232 offset:10528\n\t" \
  "v_mfma_f32_16x16x32_bf16 v[32:35], v[216:219], v[154:157], v[32:35]\n\t" \
  "v_fma_f32 v178, |v178|, v220, v178 mul:4\n\t" \
  "v_fma_f32 v179, |v179|, v221, v179 mul:4\n\t" \
  "ds_read_b64_tr_b16 v[214:215], v249 offset:26264\n\t" \
  "v_mfma_f32_16x16x32_bf16 v[80:83], v[216:219], v[170:173], v[80:83]\n\t" \
  "s_waitcnt lgkmcnt(5)\n\t" \
  "v_cvt_pk_bf16_f32 v179, v178, v179\n\t" \
  "v_cvt_pk_bf16_f32 v178, v176, v177\n\t" \
  "s_add_u32 s24, s24, s26\n\t" \
  "buffer_load_dwordx4 v254, s[8:11], s24 offen lds\n\t" \
  "v_mfma_f32_16x16x32_bf16 v[12:15], v[208:211], v[146:149], v[12:15]\n\t" \
  "v_cvt_pknorm_i16_f32 v220, |v180|, |v181|\n\t" \
  "v_cvt_pknorm_i16_f32 v222, |v182|, |v183|\n\t" \
  "ds_read_b64_tr_b16 v[218:219], v249 offset:26272\n\t" \
  "ds_read_b64_tr_b16 v[216:217], v249 offset:24736\n\t" \
  "v_mfma_f32_16x16x32_bf16 v[36:39], v[208:211], v[154:157], v[36:39]\n\t" \
  "v_lshlrev_b32_sdwa v221, 2, v220 dst_sel:DWORD dst_unused:UNUSED_PAD src0_sel:DWORD src1_sel:BYTE_3\n\t" \
  "v_lshlrev_b32_sdwa v220, 2, v220 dst_sel:DWORD dst_unused:UNUSED_PAD src0_sel:DWORD src1_sel:BYTE_1\n\t" \
  "ds_read_u16_d16_hi v235, v235 offset:10528\n\t" \
  "v_mfma_f32_16x16x32_bf16 v[60:63], v[208:211], v[162:165], v[60:63]\n\t" \
  "s_waitcnt lgkmcnt(3)\n\t" \
  "v_lshlrev_b32_sdwa v223, 2, v222 dst_sel:DWORD dst_unused:UNUSED_PAD src0_sel:DWORD src1_sel:BYTE_3\n\t" \
  "v_lshlrev_b32_sdwa v222, 2, v222 dst_sel:DWORD dst_unused:UNUSED_PAD src0_sel:DWORD src1_sel:BYTE_1\n\t" \
  "ds_read_u16 v234, v234 offset:10528\n\t" \
  "v_mfma_f32_16x16x32_bf16 v[84:87], v[208:211], v[170:173], v[84:87]\n\t" \
  "v_lshlrev_b32_sdwa v224, 1, v188 dst_sel:DWORD dst_unused:UNUSED_PAD src0_sel:DWORD src1_sel:WORD_1\n\t" \
  "v_lshlrev_b32_sdwa v225, 1, v189 dst_sel:DWORD dst_unused:UNUSED_PAD src0_sel:DWORD src1_sel:WORD_1\n\t" \
  "ds_read_b32 v220, v220 offset:10016\n\t" \
  "ds_read_b64_tr_b16 v[208:209], v248 offset:36904\n\t" \
  "v_mfma_f32_16x16x32_bf16 v[16:19], v[212:215], v[146:149], v[16:19]\n\t" \
  "v_nop\n\t" \
  "s_add_u32 s27, s27, s26\n\t" \
  "s_mov_b64 exec, s[30:31]\n\t" \
  "buffer_load_dword v237, v254, s[8:11], s27 offen\n\t" \
  "v_mfma_f32_16x16x32_bf16 v[64:67], v[212:215], v[162:165], v[64:67]\n\t" \
  "s_mov_b64 exec, -1\n\t" \
  "v_lshlrev_b32_sdwa v227, 1, v191 dst_sel:DWORD dst_unused:UNUSED_PAD src0_sel:DWORD src1_sel:WORD_1\n\t" \
  "v_fma_mix_f32 v229, |v195|, v229, v229 op_sel:[0,1,0] op_sel_hi:[0,1,1] clamp\n\t" \
  "ds_read_b32 v223, v223 offset:10016\n\t" \
  "ds_read_b32 v221, v221 offset:10016\n\t" \
  "v_mfma_f32_16x16x32_bf16 v[88:91], v[212:215], v[170:173], v[88:91]\n\t" \
  "s_setprio 1\n\t" \
  "v_fma_mix_f32 v230, |v192|, v230, v230 op_sel:[0,1,0] op_sel_hi:[0,1,1] clamp\n\t" \
  "v_fma_mix_f32 v231, |v193|, v231, v231 op_sel:[0,1,0] op_sel_hi:[0,1,1] clamp\n\t" \
  "s_setprio 2\n\t" \
  "s_waitcnt lgkmcnt(4)\n\t" \
  "ds_read_b64_tr_b16 v[210:211], v248 offset:37928\n\t" \
  "v_mfma_f32_16x16x32_bf16 v[40:43], v[212:215], v[154:157], v[40:43]\n\t" \
  "v_lshlrev_b32_sdwa v226, 1, v190 dst_sel:DWORD dst_unused:UNUSED_PAD src0_sel:DWORD src1_sel:WORD_1\n\t" \
  "v_fma_mix_f32 v228, |v194|, v228, v228 op_sel:[0,1,0] op_sel_hi:[0,1,1] clamp\n\t" \
  "ds_read_b32 v222, v222 offset:10016\n\t" \
  "ds_read_b64_tr_b16 v[212:213], v248 offset:39976\n\t" \
  "v_mfma_f32_16x16x32_bf16 v[20:23], v[216:219], v[146:149], v[20:23]\n\t" \
  "v_or_b32 v203, v232, v233\n\t" \
  "v_or_b32 v202, v234, v235\n\t" \
  "ds_read_u16_d16_hi v225, v225 offset:10528\n\t" \
  "v_mfma_f32_16x16x32_bf16 v[68:71], v[216:219], v[162:165], v[68:71]\n\t" \
  "v_fma_f32 v195, |v195|, v229, v195 mul:4\n\t" \
  "v_fma_f32 v192, |v192|, v230, v192 mul:4\n\t" \
  "s_waitcnt lgkmcnt(3)\n\t" \
  "ds_read_u16 v224, v224 offset:10528\n\t" \
  "v_mfma_f32_16x16x32_bf16 v[144:147], v[208:211], v[96:99], 0\n\t" \
  "v_lshlrev_b32_sdwa v232, 1, v204 dst_sel:DWORD dst_unused:UNUSED_PAD src0_sel:DWORD src1_sel:WORD_1\n\t" \
  "v_fma_f32 v193, |v193|, v231, v193 mul:4\n\t" \
  "ds_read_b64_tr_b16 v[214:215], v248 offset:41000\n\t" \
  "v_mfma_f32_16x16x32_bf16 v[44:47], v[216:219], v[154:157], v[44:47]\n\t" \
  "v_cvt_pknorm_i16_f32 v230, |v198|, |v199|\n\t" \
  "v_fma_f32 v194, |v194|, v228, v194 mul:4\n\t" \
  "ds_read_u16_d16_hi v227, v227 offset:10528\n\t" \
  "v_mfma_f32_16x16x32_bf16 v[92:95], v[216:219], v[170:173], v[92:95]\n\t" \
  "v_cvt_pk_bf16_f32 v195, v194, v195\n\t" \
  "v_cvt_pk_bf16_f32 v194, v192, v193\n\t" \
  "ds_read_b64_tr_b16 v[216:217], v248 offset:38952\n\t" \
  "v_mfma_f32_16x16x32_bf16 v[160:163], v[208:211], v[104:107], 0\n\t" \
  "v_lshlrev_b32_sdwa v231, 2, v230 dst_sel:DWORD dst_unused:UNUSED_PAD src0_sel:DWORD src1_sel:BYTE_3\n\t" \
  "v_cvt_pknorm_i16_f32 v228, |v196|, |v197|\n\t" \
  "s_setprio 2\n\t" \
  "ds_read_b64_tr_b16 v[218:219], v248 offset:42024\n\t" \
  "v_mfma_f32_16x16x32_bf16 v[152:155], v[208:211], v[100:103], 0\n\t" \
  "v_lshlrev_b32_sdwa v229, 2, v228 dst_sel:DWORD dst_unused:UNUSED_PAD src0_sel:DWORD src1_sel:BYTE_3\n\t" \
  "v_lshlrev_b32_sdwa v228, 2, v228 dst_sel:DWORD dst_unused:UNUSED_PAD src0_sel:DWORD src1_sel:BYTE_1\n\t" \
  "ds_read_u16 v226, v226 offset:10528\n\t" \
  "v_mfma_f32_16x16x32_bf16 v[168:171], v[208:211], v[108:111], 0\n\t" \
  "v_lshlrev_b32_sdwa v234, 1, v206 dst_sel:DWORD dst_unused:UNUSED_PAD src0_sel:DWORD src1_sel:WORD_1\n\t" \
  "v_lshlrev_b32_sdwa v233, 1, v205 dst_sel:DWORD dst_unused:UNUSED_PAD src0_sel:DWORD src1_sel:WORD_1\n\t" \
  "s_waitcnt lgkmcnt(4)\n\t" \
  "ds_read_b64_tr_b16 v[208:209], v248 offset:37416\n\t" \
  "v_mfma_f32_16x16x32_bf16 v[144:147], v[212:215], v[112:115], v[144:147]\n\t" \
  "v_lshlrev_b32_sdwa v230, 2, v230 dst_sel:DWORD dst_unused:UNUSED_PAD src0_sel:DWORD src1_sel:BYTE_1\n\t" \
  "v_fma_mix_f32 v221, |v181|, v221, v221 op_sel:[0,1,0] op_sel_hi:[0,1,1] clamp\n\t" \
  "ds_read_b32 v231, v231 offset:10016\n\t" \
  "v_mfma_f32_16x16x32_bf16 v[168:171], v[212:215], v[124:127], v[168:171]\n\t" \
  "v_lshlrev_b32_sdwa v235, 1, v207 dst_sel:DWORD dst_unused:UNUSED_PAD src0_sel:DWORD src1_sel:WORD_1\n\t" \
  "ds_read_b32 v228, v228 offset:10016\n\t" \
  "ds_read_b32 v229, v229 offset:10016\n\t" \
  "v_mfma_f32_16x16x32_bf16 v[160:163], v[212:215], v[120:123], v[160:163]\n\t" \
  "v_fma_mix_f32 v220, |v180|, v220, v220 op_sel:[0,1,0] op_sel_hi:[0,1,1] clamp\n\t" \
  "s_waitcnt lgkmcnt(5)\n\t" \
  "ds_read_b64_tr_b16 v[210:211], v248 offset:38440\n\t" \
  "ds_read_u16_d16_hi v233, v233 offset:10528\n\t" \
  "v_mfma_f32_16x16x32_bf16 v[152:155], v[212:215], v[116:119], v[152:155]\n\t" \
  "v_fma_mix_f32 v223, |v183|, v223, v223 op_sel:[0,1,0] op_sel_hi:[0,1,1] clamp\n\t" \
  "v_fma_mix_f32 v222, |v182|, v222, v222 op_sel:[0,1,0] op_sel_hi:[0,1,1] clamp\n\t" \
  "s_waitcnt lgkmcnt(6)\n\t" \
  "ds_read_b64_tr_b16 v[212:213], v248 offset:40488\n\t" \
  "v_mfma_f32_16x16x32_bf16 v[144:147], v[216:219], v[128:131], v[144:147]\n\t" \
  "v_or_b32 v188, v224, v225\n\t" \
  "v_or_b32 v189, v226, v227\n\t" \
  "ds_read_b64_tr_b16 v[214:215], v248 offset:41512\n\t" \
  "v_mfma_f32_16x16x32_bf16 v[160:163], v[216:219], v[136:139], v[160:163]\n\t" \
  "s_nop 0\n\t" \
  "v_fma_f32 v182, |v182|, v222, v182 mul:4\n\t" \
  "v_fma_f32 v183, |v183|, v223, v183 mul:4\n\t" \
  "ds_read_b32 v230, v230 offset:10016\n\t" \
  "v_mfma_f32_16x16x32_bf16 v[152:155], v[216:219], v[132:135], v[152:155]\n\t" \
  "v_fma_f32 v181, |v181|, v221, v181 mul:4\n\t" \
  "v_fma_f32 v180, |v180|, v220, v180 mul:4\n\t" \
  "ds_read_u16_d16_hi v235, v235 offset:10528\n\t" \
  "ds_read_u16 v232, v232 offset:10528\n\t" \
  "v_mfma_f32_16x16x32_bf16 v[168:171], v[216:219], v[140:143], v[168:171]\n\t" \
  "v_cvt_pknorm_i16_f32 v220, |v146|, |v147|\n\t" \
  "v_cvt_pk_bf16_f32 v180, v180, v181\n\t" \
  "s_setprio 0\n\t" \
  "s_waitcnt lgkmcnt(6)\n\t" \
  "ds_read_b64_tr_b16 v[216:217], v248 offset:39464\n\t" \
  "v_mfma_f32_16x16x32_bf16 v[148:151], v[208:211], v[96:99], 0\n\t" \
  "v_cvt_pk_bf16_f32 v181, v182, v183\n\t" \
  "v_cvt_pknorm_i16_f32 v222, |v144|, |v145|\n\t" \
  "s_waitcnt vmcnt(2)\n\t" \
  "ds_read_b64_tr_b16 v[218:219], v248 offset:42536\n\t" \
  "s_waitcnt lgkmcnt(5)\n\t" \
  "v_mfma_f32_16x16x32_bf16 v[156:159], v[208:211], v[100:103], 0\n\t" \
  "v_lshlrev_b32_sdwa v221, 2, v220 dst_sel:DWORD dst_unused:UNUSED_PAD src0_sel:DWORD src1_sel:BYTE_3\n\t" \
  "v_lshlrev_b32_sdwa v220, 2, v220 dst_sel:DWORD dst_unused:UNUSED_PAD src0_sel:DWORD src1_sel:BYTE_1\n\t" \
  "s_mov_b64 exec, 3\n\t" \
  "ds_add_rtn_u32 v246, v251, v250 offset:47184\n\t" \
  "v_mfma_f32_16x16x32_bf16 v[164:167], v[208:211], v[104:107], 0\n\t" \
  "s_mov_b64 exec, -1\n\t" \
  "v_lshlrev_b32_sdwa v223, 2, v222 dst_sel:DWORD dst_unused:UNUSED_PAD src0_sel:DWORD src1_sel:BYTE_3\n\t" \
  "v_lshlrev_b32_sdwa v222, 2, v222 dst_sel:DWORD dst_unused:UNUSED_PAD src0_sel:DWORD src1_sel:BYTE_1\n\t" \
  "ds_read_u16 v234, v234 offset:10528\n\t" \
  "v_mfma_f32_16x16x32_bf16 v[156:159], v[212:215], v[116:119], v[156:159]\n\t" \
  "v_lshlrev_b32_sdwa v226, 1, v152 dst_sel:DWORD dst_unused:UNUSED_PAD src0_sel:DWORD src1_sel:WORD_1\n\t" \
  "v_lshlrev_b32_sdwa v227, 1, v153 dst_sel:DWORD dst_unused:UNUSED_PAD src0_sel:DWORD src1_sel:WORD_1\n\t" \
  "ds_read_b32 v223, v223 offset:10016\n\t" \
  "v_mfma_f32_16x16x32_bf16 v[148:151], v[212:215], v[112:115], v[148:151]\n\t" \
  "s_waitcnt lgkmcnt(2)\n\t" \
  "v_lshlrev_b32_sdwa v225, 1, v155 dst_sel:DWORD dst_unused:UNUSED_PAD src0_sel:DWORD src1_sel:WORD_1\n\t" \
  "v_readfirstlane_b32 s4, v246\n\t" \
  "ds_read_b32 v221, v221 offset:10016\n\t" \
  "v_mfma_f32_16x16x32_bf16 v[172:175], v[208:211], v[108:111], 0\n\t" \
  "s_cmpk_lt_u32 s4, 3072\n\t" \
  "v_lshlrev_b32_sdwa v224, 1, v154 dst_sel:DWORD dst_unused:UNUSED_PAD src0_sel:DWORD src1_sel:WORD_1\n\t" \
  "s_cbranch_scc1 .Lcnx_w0_7\n\t" \
  ".Lcnx_g0_7:\n\t" \
  "ds_read_b32 v220, v220 offset:10016\n\t" \
  "v_mfma_f32_16x16x32_bf16 v[164:167], v[212:215], v[120:123], v[164:167]\n\t" \
  "v_fma_mix_f32 v229, |v197|, v229, v229 op_sel:[0,1,0] op_sel_hi:[0,1,1] clamp\n\t" \
  "v_fma_mix_f32 v228, |v196|, v228, v228 op_sel:[0,1,0] op_sel_hi:[0,1,1] clamp\n\t" \
  "ds_read_b64_tr_b16 v[210:211], v249 offset:32288\n\t" \
  "ds_read_b64_tr_b16 v[208:209], v249 offset:30752\n\t" \
  "v_mfma_f32_16x16x32_bf16 v[172:175], v[212:215], v[124:127], v[172:175]\n\t" \
  "v_fma_mix_f32 v230, |v198|, v230, v230 op_sel:[0,1,0] op_sel_hi:[0,1,1] clamp\n\t" \
  "v_fma_mix_f32 v231, |v199|, v231, v231 op_sel:[0,1,0] op_sel_hi:[0,1,1] clamp\n\t" \
  "s_waitcnt lgkmcnt(5)\n\t" \
  "ds_read_b32 v222, v222 offset:10016\n\t" \
  "ds_read_b64_tr_b16 v[212:213], v249 offset:30760\n\t" \
  "v_mfma_f32_16x16x32_bf16 v[148:151], v[216:219], v[128:131], v[148:151]\n\t" \
  "s_mov_b32 m0, s47\n\t" \
  "v_or_b32 v204, v232, v233\n\t" \
  "v_or_b32 v205, v234, v235\n\t" \
  "s_mov_b32 exec_hi, 0xffff\n\t" \
  "buffer_load_dwordx4 v252, s[16:19], s33 offen lds\n\t" \
  "v_mfma_f32_16x16x32_bf16 v[156:159], v[216:219], v[132:135], v[156:159]\n\t" \
  "s_mov_b32 exec_hi, -1\n\t" \
  "v_fma_f32 v196, |v196|, v228, v196 mul:4\n\t" \
  "v_fma_f32 v197, |v197|, v229, v197 mul:4\n\t" \
  "s_add_u32 s33, s33, s32\n\t" \
  "ds_read_u16_d16_hi v227, v227 offset:10528\n\t" \
  "ds_read_b64_tr_b16 v[214:215], v249 offset:32296\n\t" \
  "v_mfma_f32_16x16x32_bf16 v[164:167], v[216:219], v[136:139], v[164:167]\n\t" \
  "v_fma_f32 v198, |v198|, v230, v198 mul:4\n\t" \
  "v_fma_f32 v199, |v199|, v231, v199 mul:4\n\t" \
  "ds_read_u16_d16_hi v225, v225 offset:10528\n\t" \
  "v_mfma_f32_16x16x32_bf16 v[172:175], v[216:219], v[140:143], v[172:175]\n\t" \
  "v_cvt_pk_bf16_f32 v196, v196, v197\n\t" \
  "v_cvt_pk_bf16_f32 v197, v198, v199\n\t" \
  "ds_read_b64_tr_b16 v[216:217], v249 offset:30816\n\t" \
  "s_waitcnt lgkmcnt(6)\n\t" \
  "v_mfma_f32_16x16x32_bf16 v[0:3], v[208:211], v[178:181], v[0:3]\n\t" \
  "v_cvt_pknorm_i16_f32 v230, |v160|, |v161|\n\t" \
  "v_cvt_pknorm_i16_f32 v228, |v162|, |v163|\n\t" \
  "ds_read_b64_tr_b16 v[218:219], v249 offset:32352\n\t" \
  "v_mfma_f32_16x16x32_bf16 v[24:27], v[208:211], v[186:189], v[24:27]\n\t" \
  "v_lshlrev_b32_sdwa v229, 2, v228 dst_sel:DWORD dst_unused:UNUSED_PAD src0_sel:DWORD src1_sel:BYTE_3\n\t" \
  "v_lshlrev_b32_sdwa v228, 2, v228 dst_sel:DWORD dst_unused:UNUSED_PAD src0_sel:DWORD src1_sel:BYTE_1\n\t" \
  "ds_read_b32 v229, v229 offset:10016\n\t" \
  "ds_read_u16 v224, v224 offset:10528\n\t" \
  "v_mfma_f32_16x16x32_bf16 v[48:51], v[208:211], v[194:197], v[48:51]\n\t" \
  "v_lshlrev_b32_sdwa v231, 2, v230 dst_sel:DWORD dst_unused:UNUSED_PAD src0_sel:DWORD src1_sel:BYTE_3\n\t" \
  "s_setprio 1\n\t" \
  "s_add_u32 m0, s47, s35\n\t" \
  "s_waitcnt lgkmcnt(5)\n\t" \
  "ds_read_b32 v228, v228 offset:10016\n\t" \
  "v_mfma_f32_16x16x32_bf16 v[72:75], v[208:211], v[202:205], v[72:75]\n\t" \
  "v_lshlrev_b32_sdwa v232, 1, v170 dst_sel:DWORD dst_unused:UNUSED_PAD src0_sel:DWORD src1_sel:WORD_1\n\t" \
  "v_lshlrev_b32_sdwa v233, 1, v171 dst_sel:DWORD dst_unused:UNUSED_PAD src0_sel:DWORD src1_sel:WORD_1\n\t" \
  "s_mov_b32 exec_hi, 0xffff\n\t" \
  "buffer_load_dwordx4 v252, s[16:19], s34 offen lds\n\t" \
  "v_mfma_f32_16x16x32_bf16 v[4:7], v[212:215], v[178:181], v[4:7]\n\t" \
  "s_mov_b32 exec_hi, -1\n\t" \
  "s_add_u32 s34, s34, s32\n\t" \
  "v_lshlrev_b32_sdwa v235, 1, v169 dst_sel:DWORD dst_unused:UNUSED_PAD src0_sel:DWORD src1_sel:WORD_1\n\t" \
  "ds_read_u16 v226, v226 offset:10528\n\t" \
  "v_mfma_f32_16x16x32_bf16 v[28:31], v[212:215], v[186:189], v[28:31]\n\t" \
  "v_lshlrev_b32_sdwa v234, 1, v168 dst_sel:DWORD dst_unused:UNUSED_PAD src0_sel:DWORD src1_sel:WORD_1\n\t" \
  "v_lshlrev_b32_sdwa v230, 2, v230 dst_sel:DWORD dst_unused:UNUSED_PAD src0_sel:DWORD src1_sel:BYTE_1\n\t" \
  "ds_read_b32 v230, v230 offset:10016\n\t" \
  "ds_read_b64_tr_b16 v[208:209], v249 offset:30824\n\t" \
  "v_mfma_f32_16x16x32_bf16 v[52:55], v[212:215], v[194:197], v[52:55]\n\t" \
  "v_fma_mix_f32 v220, |v146|, v220, v220 op_sel:[0,1,0] op_sel_hi:[0,1,1] clamp\n\t" \
  "v_fma_mix_f32 v221, |v147|, v221, v221 op_sel:[0,1,0] op_sel_hi:[0,1,1] clamp\n\t" \
  "ds_read_b64_tr_b16 v[210:211], v249 offset:32360\n\t" \
  "v_mfma_f32_16x16x32_bf16 v[76:79], v[212:215], v[202:205], v[76:79]\n\t" \
  "s_waitcnt lgkmcnt(7)\n\t" \
  "v_fma_mix_f32 v223, |v145|, v223, v223 op_sel:[0,1,0] op_sel_hi:[0,1,1] clamp\n\t" \
  "v_fma_mix_f32 v222, |v144|, v222, v222 op_sel:[0,1,0] op_sel_hi:[0,1,1] clamp\n\t" \
  "s_add_u32 m0, s25, 41000\n\t" \
  "s_waitcnt lgkmcnt(3)\n\t" \
  "ds_read_b32 v231, v231 offset:10016\n\t" \
  "ds_read_b64_tr_b16 v[212:213], v249 offset:30880\n\t" \
  "v_mfma_f32_16x16x32_bf16 v[8:11], v[216:219], v[178:181], v[8:11]\n\t" \
  "v_or_b32 v155, v224, v225\n\t" \
  "v_or_b32 v154, v226, v227\n\t" \
  "s_setprio 1\n\t" \
  "s_setprio 0\n\t" \
  "ds_read_u16_d16_hi v233, v233 offset:10528\n\t" \
  "v_mfma_f32_16x16x32_bf16 v[56:59], v[216:219], v[194:197], v[56:59]\n\t" \
  "v_fma_f32 v144, |v144|, v222, v144 mul:4\n\t" \
  "v_fma_f32 v145, |v145|, v223, v145 mul:4\n\t" \
  "ds_read_u16 v232, v232 offset:10528\n\t" \
  "v_mfma_f32_16x16x32_bf16 v[32:35], v[216:219], v[186:189], v[32:35]\n\t" \
  "v_fma_f32 v146, |v146|, v220, v146 mul:4\n\t" \
  "v_fma_f32 v147, |v147|, v221, v147 mul:4\n\t" \
  "ds_read_b64_tr_b16 v[214:215], v249 offset:32416\n\t" \
  "v_mfma_f32_16x16x32_bf16 v[80:83], v[216:219], v[202:205], v[80:83]\n\t" \
  "s_waitcnt lgkmcnt(5)\n\t" \
  "v_cvt_pk_bf16_f32 v147, v146, v147\n\t" \
  "v_cvt_pk_bf16_f32 v146, v144, v145\n\t" \
  "s_add_u32 s24, s24, s26\n\t" \
  "buffer_load_dwordx4 v254, s[8:11], s24 offen lds\n\t" \
  "v_mfma_f32_16x16x32_bf16 v[12:15], v[208:211], v[178:181], v[12:15]\n\t" \
  "v_cvt_pknorm_i16_f32 v220, |v148|, |v149|\n\t" \
  "v_cvt_pknorm_i16_f32 v222, |v150|, |v151|\n\t" \
  "ds_read_b64_tr_b16 v[218:219], v249 offset:32424\n\t" \
  "ds_read_b64_tr_b16 v[216:217], v249 offset:30888\n\t" \
  "v_mfma_f32_16x16x32_bf16 v[36:39], v[208:211], v[186:189], v[36:39]\n\t" \
  "v_lshlrev_b32_sdwa v221, 2, v220 dst_sel:DWORD dst_unused:UNUSED_PAD src0_sel:DWORD src1_sel:BYTE_3\n\t" \
  "v_lshlrev_b32_sdwa v220, 2, v220 dst_sel:DWORD dst_unused:UNUSED_PAD src0_sel:DWORD src1_sel:BYTE_1\n\t" \
  "ds_read_u16_d16_hi v235, v235 offset:10528\n\t" \
  "v_mfma_f32_16x16x32_bf16 v[60:63], v[208:211], v[194:197], v[60:63]\n\t" \
  "s_waitcnt lgkmcnt(3)\n\t" \
  "v_lshlrev_b32_sdwa v223, 2, v222 dst_sel:DWORD dst_unused:UNUSED_PAD src0_sel:DWORD src1_sel:BYTE_3\n\t" \
  "v_lshlrev_b32_sdwa v222, 2, v222 dst_sel:DWORD dst_unused:UNUSED_PAD src0_sel:DWORD src1_sel:BYTE_1\n\t" \
  "ds_read_u16 v234, v234 offset:10528\n\t" \
  "v_mfma_f32_16x16x32_bf16 v[84:87], v[208:211], v[202:205], v[84:87]\n\t" \
  "v_lshlrev_b32_sdwa v224, 1, v156 dst_sel:DWORD dst_unused:UNUSED_PAD src0_sel:DWORD src1_sel:WORD_1\n\t" \
  "v_lshlrev_b32_sdwa v225, 1, v157 dst_sel:DWORD dst_unused:UNUSED_PAD src0_sel:DWORD src1_sel:WORD_1\n\t" \
  "ds_read_b32 v220, v220 offset:10016\n\t" \
  "ds_read_b64_tr_b16 v[208:209], v248 offset:0\n\t" \
  "v_mfma_f32_16x16x32_bf16 v[16:19], v[212:215], v[178:181], v[16:19]\n\t" \
  "v_nop\n\t" \
  "s_add_u32 s27, s27, s26\n\t" \
  "s_mov_b64 exec, s[30:31]\n\t" \
  "buffer_load_dword v237, v254, s[8:11], s27 offen\n\t" \
  "v_mfma_f32_16x16x32_bf16 v[64:67], v[212:215], v[194:197], v[64:67]\n\t" \
  "s_mov_b64 exec, -1\n\t" \
  "v_lshlrev_b32_sdwa v227, 1, v159 dst_sel:DWORD dst_unused:UNUSED_PAD src0_sel:DWORD src1_sel:WORD_1\n\t" \
  "v_fma_mix_f32 v229, |v163|, v229, v229 op_sel:[0,1,0] op_sel_hi:[0,1,1] clamp\n\t" \
  "ds_read_b32 v223, v223 offset:10016\n\t" \
  "ds_read_b32 v221, v221 offset:10016\n\t" \
  "v_mfma_f32_16x16x32_bf16 v[88:91], v[212:215], v[202:205], v[88:91]\n\t" \
  "s_setprio 1\n\t" \
  "v_fma_mix_f32 v230, |v160|, v230, v230 op_sel:[0,1,0] op_sel_hi:[0,1,1] clamp\n\t" \
  "v_fma_mix_f32 v231, |v161|, v231, v231 op_sel:[0,1,0] op_sel_hi:[0,1,1] clamp\n\t" \
  "s_setprio 2\n\t" \
  "s_waitcnt lgkmcnt(4)\n\t" \
  "ds_read_b64_tr_b16 v[210:211], v248 offset:1024\n\t" \
  "v_mfma_f32_16x16x32_bf16 v[40:43], v[212:215], v[186:189], v[40:43]\n\t" \
  "v_lshlrev_b32_sdwa v226, 1, v158 dst_sel:DWORD dst_unused:UNUSED_PAD src0_sel:DWORD src1_sel:WORD_1\n\t" \
  "v_fma_mix_f32 v228, |v162|, v228, v228 op_sel:[0,1,0] op_sel_hi:[0,1,1] clamp\n\t" \
  "ds_read_b32 v222, v222 offset:10016\n\t" \
  "ds_read_b64_tr_b16 v[212:213], v248 offset:3072\n\t" \
  "v_mfma_f32_16x16x32_bf16 v[20:23], v[216:219], v[178:181], v[20:23]\n\t" \
  "v_or_b32 v171, v232, v233\n\t" \
  "v_or_b32 v170, v234, v235\n\t" \
  "ds_read_u16_d16_hi v225, v225 offset:10528\n\t" \
  "v_mfma_f32_16x16x32_bf16 v[68:71], v[216:219], v[194:197], v[68:71]\n\t" \
  "v_fma_f32 v163, |v163|, v229, v163 mul:4\n\t" \
  "v_fma_f32 v160, |v160|, v230, v160 mul:4\n\t" \
  "s_waitcnt lgkmcnt(3)\n\t" \
  "ds_read_u16 v224, v224 offset:10528\n\t" \
  "v_mfma_f32_16x16x32_bf16 v[176:179], v[208:211], v[96:99], 0\n\t" \
  "v_lshlrev_b32_sdwa v232, 1, v172 dst_sel:DWORD dst_unused:UNUSED_PAD src0_sel:DWORD src1_sel:WORD_1\n\t" \
  "v_fma_f32 v161, |v161|, v231, v161 mul:4\n\t" \
  "ds_read_b64_tr_b16 v[214:215], v248 offset:4096\n\t" \
  "v_mfma_f32_16x16x32_bf16 v[44:47], v[216:219], v[186:189], v[44:47]\n\t" \
  "v_cvt_pknorm_i16_f32 v230, |v166|, |v167|\n\t" \
  "v_fma_f32 v162, |v162|, v228, v162 mul:4\n\t" \
  "ds_read_u16_d16_hi v227, v227 offset:10528\n\t" \
  "v_mfma_f32_16x16x32_bf16 v[92:95], v[216:219], v[202:205], v[92:95]\n\t" \
  "v_cvt_pk_bf16_f32 v163, v162, v163\n\t" \
  "v_cvt_pk_bf16_f32 v162, v160, v161\n\t" \
  "ds_read_b64_tr_b16 v[216:217], v248 offset:2048\n\t" \
  "v_mfma_f32_16x16x32_bf16 v[192:195], v[208:211], v[104:107], 0\n\t" \
  "v_lshlrev_b32_sdwa v231, 2, v230 dst_sel:DWORD dst_unused:UNUSED_PAD src0_sel:DWORD src1_sel:BYTE_3\n\t" \
  "v_cvt_pknorm_i16_f32 v228, |v164|, |v165|\n\t" \
  "s_setprio 2\n\t" \
  "ds_read_b64_tr_b16 v[218:219], v248 offset:5120\n\t" \
  "v_mfma_f32_16x16x32_bf16 v[184:187], v[208:211], v[100:103], 0\n\t" \
  "v_lshlrev_b32_sdwa v229, 2, v228 dst_sel:DWORD dst_unused:UNUSED_PAD src0_sel:DWORD src1_sel:BYTE_3\n\t" \
  "v_lshlrev_b32_sdwa v228, 2, v228 dst_sel:DWORD dst_unused:UNUSED_PAD src0_sel:DWORD src1_sel:BYTE_1\n\t" \
  "ds_read_u16 v226, v226 offset:10528\n\t" \
  "v_mfma_f32_16x16x32_bf16 v[200:203], v[208:211], v[108:111], 0\n\t" \
  "v_lshlrev_b32_sdwa v234, 1, v174 dst_sel:DWORD dst_unused:UNUSED_PAD src0_sel:DWORD src1_sel:WORD_1\n\t" \
  "v_lshlrev_b32_sdwa v233, 1, v173 dst_sel:DWORD dst_unused:UNUSED_PAD src0_sel:DWORD src1_sel:WORD_1\n\t" \
  "s_waitcnt lgkmcnt(4)\n\t" \
  "ds_read_b64_tr_b16 v[208:209], v248 offset:512\n\t" \
  "v_mfma_f32_16x16x32_bf16 v[176:179], v[212:215], v[112:115], v[176:179]\n\t" \
  "v_lshlrev_b32_sdwa v230, 2, v230 dst_sel:DWORD dst_unused:UNUSED_PAD src0_sel:DWORD src1_sel:BYTE_1\n\t" \
  "v_fma_mix_f32 v221, |v149|, v221, v221 op_sel:[0,1,0] op_sel_hi:[0,1,1] clamp\n\t" \
  "ds_read_b32 v231, v231 offset:10016\n\t" \
  "v_mfma_f32_16x16x32_bf16 v[200:203], v[212:215], v[124:127], v[200:203]\n\t" \
  "v_lshlrev_b32_sdwa v235, 1, v175 dst_sel:DWORD dst_unused:UNUSED_PAD src0_sel:DWORD src1_sel:WORD_1\n\t" \
  "ds_read_b32 v228, v228 offset:10016\n\t" \
  "ds_read_b32 v229, v229 offset:10016\n\t" \
  "v_mfma_f32_16x16x32_bf16 v[192:195], v[212:215], v[120:123], v[192:195]\n\t" \
  "v_fma_mix_f32 v220, |v148|, v220, v220 op_sel:[0,1,0] op_sel_hi:[0,1,1] clamp\n\t" \
  "s_waitcnt lgkmcnt(5)\n\t" \
  "ds_read_b64_tr_b16 v[210:211], v248 offset:1536\n\t" \
  "ds_read_u16_d16_hi v233, v233 offset:10528\n\t" \
  "v_mfma_f32_16x16x32_bf16 v[184:187], v[212:215], v[116:119], v[184:187]\n\t" \
  "v_fma_mix_f32 v223, |v151|, v223, v223 op_sel:[0,1,0] op_sel_hi:[0,1,1] clamp\n\t" \
  "v_fma_mix_f32 v222, |v150|, v222, v222 op_sel:[0,1,0] op_sel_hi:[0,1,1] clamp\n\t" \
  "s_waitcnt lgkmcnt(6)\n\t" \
  "ds_read_b64_tr_b16 v[212:213], v248 offset:3584\n\t" \
  "v_mfma_f32_16x16x32_bf16 v[176:179], v[216:219], v[128:131], v[176:179]\n\t" \
  "v_or_b32 v156, v224, v225\n\t" \
  "v_or_b32 v157, v226, v227\n\t" \
  "ds_read_b64_tr_b16 v[214:215], v248 offset:4608\n\t" \
  "v_mfma_f32_16x16x32_bf16 v[192:195], v[216:219], v[136:139], v[192:195]\n\t" \
  "s_nop 0\n\t" \
  "v_fma_f32 v150, |v150|, v222, v150 mul:4\n\t" \
  "v_fma_f32 v151, |v151|, v223, v151 mul:4\n\t" \
  "ds_read_b32 v230, v230 offset:10016\n\t" \
  "v_mfma_f32_16x16x32_bf16 v[184:187], v[216:219], v[132:135], v[184:187]\n\t" \
  "v_fma_f32 v149, |v149|, v221, v149 mul:4\n\t" \
  "v_fma_f32 v148, |v148|, v220, v148 mul:4\n\t" \
  "ds_read_u16_d16_hi v235, v235 offset:10528\n\t" \
  "ds_read_u16 v232, v232 offset:10528\n\t" \
  "v_mfma_f32_16x16x32_bf16 v[200:203], v[216:219], v[140:143], v[200:203]\n\t" \
  "v_cvt_pknorm_i16_f32 v220, |v178|, |v179|\n\t" \
  "v_cvt_pk_bf16_f32 v148, v148, v149\n\t" \
  "s_setprio 0\n\t" \
  "s_waitcnt lgkmcnt(6)\n\t" \
  "ds_read_b64_tr_b16 v[216:217], v248 offset:2560\n\t" \
  "v_mfma_f32_16x16x32_bf16 v[180:183], v[208:211], v[96:99], 0\n\t" \
  "v_cvt_pk_bf16_f32 v149, v150, v151\n\t" \
  "v_cvt_pknorm_i16_f32 v222, |v176|, |v177|\n\t" \
  "s_waitcnt vmcnt(2)\n\t" \
  "ds_read_b64_tr_b16 v[218:219], v248 offset:5632\n\t" \
  "s_waitcnt lgkmcnt(5)\n\t" \
  "v_mfma_f32_16x16x32_bf16 v[188:191], v[208:211], v[100:103], 0\n\t" \
  "v_lshlrev_b32_sdwa v221, 2, v220 dst_sel:DWORD dst_unused:UNUSED_PAD src0_sel:DWORD src1_sel:BYTE_3\n\t" \
  "v_lshlrev_b32_sdwa v220, 2, v220 dst_sel:DWORD dst_unused:UNUSED_PAD src0_sel:DWORD src1_sel:BYTE_1\n\t" \
  "s_mov_b64 exec, 3\n\t" \
  "ds_add_rtn_u32 v246, v251, v250 offset:47192\n\t" \
  "v_mfma_f32_16x16x32_bf16 v[196:199], v[208:211], v[104:107], 0\n\t" \
  "s_mov_b64 exec, -1\n\t" \
  "v_lshlrev_b32_sdwa v223, 2, v222 dst_sel:DWORD dst_unused:UNUSED_PAD src0_sel:DWORD src1_sel:BYTE_3\n\t" \
  "v_lshlrev_b32_sdwa v222, 2, v222 dst_sel:DWORD dst_unused:UNUSED_PAD src0_sel:DWORD src1_sel:BYTE_1\n\t" \
  "ds_read_u16 v234, v234 offset:10528\n\t" \
  "v_mfma_f32_16x16x32_bf16 v[188:191], v[212:215], v[116:119], v[188:191]\n\t" \
  "v_lshlrev_b32_sdwa v226, 1, v184 dst_sel:DWORD dst_unused:UNUSED_PAD src0_sel:DWORD src1_sel:WORD_1\n\t" \
  "v_lshlrev_b32_sdwa v227, 1, v185 dst_sel:DWORD dst_unused:UNUSED_PAD src0_sel:DWORD src1_sel:WORD_1\n\t" \
  "ds_read_b32 v223, v223 offset:10016\n\t" \
  "v_mfma_f32_16x16x32_bf16 v[180:183], v[212:215], v[112:115], v[180:183]\n\t" \
  "s_waitcnt lgkmcnt(2)\n\t" \
  "v_lshlrev_b32_sdwa v225, 1, v187 dst_sel:DWORD dst_unused:UNUSED_PAD src0_sel:DWORD src1_sel:WORD_1\n\t" \
  "v_readfirstlane_b32 s4, v246\n\t" \
  "ds_read_b32 v221, v221 offset:10016\n\t" \
  "v_mfma_f32_16x16x32_bf16 v[204:207], v[208:211], v[108:111], 0\n\t" \
  "s_cmpk_lt_u32 s4, 3072\n\t" \
  "v_lshlrev_b32_sdwa v224, 1, v186 dst_sel:DWORD dst_unused:UNUSED_PAD src0_sel:DWORD src1_sel:WORD_1\n\t" \
  "s_cbranch_scc1 .Lcnx_w0_8\n\t" \
  ".Lcnx_g0_8:\n\t" \
  "ds_read_b32 v220, v220 offset:10016\n\t" \
  "v_mfma_f32_16x16x32_bf16 v[196:199], v[212:215], v[120:123], v[196:199]\n\t" \
  "v_fma_mix_f32 v229, |v165|, v229, v229 op_sel:[0,1,0] op_sel_hi:[0,1,1] clamp\n\t" \
  "v_fma_mix_f32 v228, |v164|, v228, v228 op_sel:[0,1,0] op_sel_hi:[0,1,1] clamp\n\t" \
  "ds_read_b64_tr_b16 v[210:211], v249 offset:7688\n\t" \
  "ds_read_b64_tr_b16 v[208:209], v249 offset:6152\n\t" \
  "v_mfma_f32_16x16x32_bf16 v[204:207], v[212:215], v[124:127], v[204:207]\n\t" \
  "v_fma_mix_f32 v230, |v166|, v230, v230 op_sel:[0,1,0] op_sel_hi:[0,1,1] clamp\n\t" \
  "v_fma_mix_f32 v231, |v167|, v231, v231 op_sel:[0,1,0] op_sel_hi:[0,1,1] clamp\n\t" \
  "s_waitcnt lgkmcnt(5)\n\t" \
  "ds_read_b32 v222, v222 offset:10016\n\t" \
  "ds_read_b64_tr_b16 v[212:213], v249 offset:6160\n\t" \
  "v_mfma_f32_16x16x32_bf16 v[180:183], v[216:219], v[128:131], v[180:183]\n\t" \
  "s_mov_b32 m0, s44\n\t" \
  "v_or_b32 v172, v232, v233\n\t" \
  "v_or_b32 v173, v234, v235\n\t" \
  "s_mov_b32 exec_hi, 0xffff\n\t" \
  "buffer_load_dwordx4 v252, s[16:19], s33 offen lds\n\t" \
  "v_mfma_f32_16x16x32_bf16 v[188:191], v[216:219], v[132:135], v[188:191]\n\t" \
  "s_mov_b32 exec_hi, -1\n\t" \
  "v_fma_f32 v164, |v164|, v228, v164 mul:4\n\t" \
  "v_fma_f32 v165, |v165|, v229, v165 mul:4\n\t" \
  "s_add_u32 s33, s33, s32\n\t" \
  "ds_read_u16_d16_hi v227, v227 offset:10528\n\t" \
  "ds_read_b64_tr_b16 v[214:215], v249 offset:7696\n\t" \
  "v_mfma_f32_16x16x32_bf16 v[196:199], v[216:219], v[136:139], v[196:199]\n\t" \
  "v_fma_f32 v166, |v166|, v230, v166 mul:4\n\t" \
  "v_fma_f32 v167, |v167|, v231, v167 mul:4\n\t" \
  "ds_read_u16_d16_hi v225, v225 offset:10528\n\t" \
  "v_mfma_f32_16x16x32_bf16 v[204:207], v[216:219], v[140:143], v[204:207]\n\t" \
  "v_cvt_pk_bf16_f32 v164, v164, v165\n\t" \
  "v_cvt_pk_bf16_f32 v165, v166, v167\n\t" \
  "ds_read_b64_tr_b16 v[216:217], v249 offset:6216\n\t" \
  "s_waitcnt lgkmcnt(6)\n\t" \
  "v_mfma_f32_16x16x32_bf16 v[0:3], v[208:211], v[146:149], v[0:3]\n\t" \
  "v_cvt_pknorm_i16_f32 v230, |v192|, |v193|\n\t" \
  "v_cvt_pknorm_i16_f32 v228, |v194|, |v195|\n\t" \
  "ds_read_b64_tr_b16 v[218:219], v249 offset:7752\n\t" \
  "v_mfma_f32_16x16x32_bf16 v[24:27], v[208:211], v[154:157], v[24:27]\n\t" \
  "v_lshlrev_b32_sdwa v229, 2, v228 dst_sel:DWORD dst_unused:UNUSED_PAD src0_sel:DWORD src1_sel:BYTE_3\n\t" \
  "v_lshlrev_b32_sdwa v228, 2, v228 dst_sel:DWORD dst_unused:UNUSED_PAD src0_sel:DWORD src1_sel:BYTE_1\n\t" \
  "ds_read_b32 v229, v229 offset:10016\n\t" \
  "ds_read_u16 v224, v224 offset:10528\n\t" \
  "v_mfma_f32_16x16x32_bf16 v[48:51], v[208:211], v[162:165], v[48:51]\n\t" \
  "v_lshlrev_b32_sdwa v231, 2, v230 dst_sel:DWORD dst_unused:UNUSED_PAD src0_sel:DWORD src1_sel:BYTE_3\n\t" \
  "s_setprio 1\n\t" \
  "s_add_u32 m0, s44, s35\n\t" \
  "s_waitcnt lgkmcnt(5)\n\t" \
  "ds_read_b32 v228, v228 offset:10016\n\t" \
  "v_mfma_f32_16x16x32_bf16 v[72:75], v[208:211], v[170:173], v[72:75]\n\t" \
  "v_lshlrev_b32_sdwa v232, 1, v202 dst_sel:DWORD dst_unused:UNUSED_PAD src0_sel:DWORD src1_sel:WORD_1\n\t" \
  "v_lshlrev_b32_sdwa v233, 1, v203 dst_sel:DWORD dst_unused:UNUSED_PAD src0_sel:DWORD src1_sel:WORD_1\n\t" \
  "s_mov_b32 exec_hi, 0xffff\n\t" \
  "buffer_load_dwordx4 v252, s[16:19], s34 offen lds\n\t" \
  "v_mfma_f32_16x16x32_bf16 v[4:7], v[212:215], v[146:149], v[4:7]\n\t" \
  "s_mov_b32 exec_hi, -1\n\t" \
  "s_add_u32 s34, s34, s32\n\t" \
  "v_lshlrev_b32_sdwa v235, 1, v201 dst_sel:DWORD dst_unused:UNUSED_PAD src0_sel:DWORD src1_sel:WORD_1\n\t" \
  "ds_read_u16 v226, v226 offset:10528\n\t" \
  "v_mfma_f32_16x16x32_bf16 v[28:31], v[212:215], v[154:157], v[28:31]\n\t" \
  "v_lshlrev_b32_sdwa v234, 1, v200 dst_sel:DWORD dst_unused:UNUSED_PAD src0_sel:DWORD src1_sel:WORD_1\n\t" \
  "v_lshlrev_b32_sdwa v230, 2, v230 dst_sel:DWORD dst_unused:UNUSED_PAD src0_sel:DWORD src1_sel:BYTE_1\n\t" \
  "ds_read_b32 v230, v230 offset:10016\n\t" \
  "ds_read_b64_tr_b16 v[208:209], v249 offset:6224\n\t" \
  "v_mfma_f32_16x16x32_bf16 v[52:55], v[212:215], v[162:165], v[52:55]\n\t" \
  "v_fma_mix_f32 v220, |v178|, v220, v220 op_sel:[0,1,0] op_sel_hi:[0,1,1] clamp\n\t" \
  "v_fma_mix_f32 v221, |v179|, v221, v221 op_sel:[0,1,0] op_sel_hi:[0,1,1] clamp\n\t" \
  "ds_read_b64_tr_b16 v[210:211], v249 offset:7760\n\t" \
  "v_mfma_f32_16x16x32_bf16 v[76:79], v[212:215], v[170:173], v[76:79]\n\t" \
  "s_waitcnt lgkmcnt(7)\n\t" \
  "v_fma_mix_f32 v223, |v177|, v223, v223 op_sel:[0,1,0] op_sel_hi:[0,1,1] clamp\n\t" \
  "v_fma_mix_f32 v222, |v176|, v222, v222 op_sel:[0,1,0] op_sel_hi:[0,1,1] clamp\n\t" \
  "s_add_u32 m0, s25, 56880\n\t" \
  "s_waitcnt lgkmcnt(3)\n\t" \
  "ds_read_b32 v231, v231 offset:10016\n\t" \
  "ds_read_b64_tr_b16 v[212:213], v249 offset:6280\n\t" \
  "v_mfma_f32_16x16x32_bf16 v[8:11], v[216:219], v[146:149], v[8:11]\n\t" \
  "v_or_b32 v187, v224, v225\n\t" \
  "v_or_b32 v186, v226, v227\n\t" \
  "s_setprio 1\n\t" \
  "s_setprio 0\n\t" \
  "ds_read_u16_d16_hi v233, v233 offset:10528\n\t" \
  "v_mfma_f32_16x16x32_bf16 v[56:59], v[216:219], v[162:165], v[56:59]\n\t" \
  "v_fma_f32 v176, |v176|, v222, v176 mul:4\n\t" \
  "v_fma_f32 v177, |v177|, v223, v177 mul:4\n\t" \
  "ds_read_u16 v232, v232 offset:10528\n\t" \
  "v_mfma_f32_16x16x32_bf16 v[32:35], v[216:219], v[154:157], v[32:35]\n\t" \
  "v_fma_f32 v178, |v178|, v220, v178 mul:4\n\t" \
  "v_fma_f32 v179, |v179|, v221, v179 mul:4\n\t" \
  "ds_read_b64_tr_b16 v[214:215], v249 offset:7816\n\t" \
  "v_mfma_f32_16x16x32_bf16 v[80:83], v[216:219], v[170:173], v[80:83]\n\t" \
  "s_waitcnt lgkmcnt(5)\n\t" \
  "v_cvt_pk_bf16_f32 v179, v178, v179\n\t" \
  "v_cvt_pk_bf16_f32 v178, v176, v177\n\t" \
  "s_add_u32 s24, s24, s26\n\t" \
  "buffer_load_dwordx4 v254, s[8:11], s24 offen lds\n\t" \
  "v_mfma_f32_16x16x32_bf16 v[12:15], v[208:211], v[146:149], v[12:15]\n\t" \
  "v_cvt_pknorm_i16_f32 v220, |v180|, |v181|\n\t" \
  "v_cvt_pknorm_i16_f32 v222, |v182|, |v183|\n\t" \
  "ds_read_b64_tr_b16 v[218:219], v249 offset:7824\n\t" \
  "ds_read_b64_tr_b16 v[216:217], v249 offset:6288\n\t" \
  "v_mfma_f32_16x16x32_bf16 v[36:39], v[208:211], v[154:157], v[36:39]\n\t" \
  "v_lshlrev_b32_sdwa v221, 2, v220 dst_sel:DWORD dst_unused:UNUSED_PAD src0_sel:DWORD src1_sel:BYTE_3\n\t" \
  "v_lshlrev_b32_sdwa v220, 2, v220 dst_sel:DWORD dst_unused:UNUSED_PAD src0_sel:DWORD src1_sel:BYTE_1\n\t" \
  "ds_read_u16_d16_hi v235, v235 offset:10528\n\t" \
  "v_mfma_f32_16x16x32_bf16 v[60:63], v[208:211], v[162:165], v[60:63]\n\t" \
  "s_waitcnt lgkmcnt(3)\n\t" \
  "v_lshlrev_b32_sdwa v223, 2, v222 dst_sel:DWORD dst_unused:UNUSED_PAD src0_sel:DWORD src1_sel:BYTE_3\n\t" \
  "v_lshlrev_b32_sdwa v222, 2, v222 dst_sel:DWORD dst_unused:UNUSED_PAD src0_sel:DWORD src1_sel:BYTE_1\n\t" \
  "ds_read_u16 v234, v234 offset:10528\n\t" \
  "v_mfma_f32_16x16x32_bf16 v[84:87], v[208:211], v[170:173], v[84:87]\n\t" \
  "v_lshlrev_b32_sdwa v224, 1, v188 dst_sel:DWORD dst_unused:UNUSED_PAD src0_sel:DWORD src1_sel:WORD_1\n\t" \
  "v_lshlrev_b32_sdwa v225, 1, v189 dst_sel:DWORD dst_unused:UNUSED_PAD src0_sel:DWORD src1_sel:WORD_1\n\t" \
  "ds_read_b32 v220, v220 offset:10016\n\t" \
  "ds_read_b64_tr_b16 v[208:209], v248 offset:43048\n\t" \
  "v_mfma_f32_16x16x32_bf16 v[16:19], v[212:215], v[146:149], v[16:19]\n\t" \
  "v_nop\n\t" \
  "s_add_u32 s27, s27, s26\n\t" \
  "s_mov_b64 exec, s[30:31]\n\t" \
  "buffer_load_dword v237, v254, s[8:11], s27 offen\n\t" \
  "v_mfma_f32_16x16x32_bf16 v[64:67], v[212:215], v[162:165], v[64:67]\n\t" \
  "s_mov_b64 exec, -1\n\t" \
  "v_lshlrev_b32_sdwa v227, 1, v191 dst_sel:DWORD dst_unused:UNUSED_PAD src0_sel:DWORD src1_sel:WORD_1\n\t" \
  "v_fma_mix_f32 v229, |v195|, v229, v229 op_sel:[0,1,0] op_sel_hi:[0,1,1] clamp\n\t" \
  "ds_read_b32 v223, v223 offset:10016\n\t" \
  "ds_read_b32 v221, v221 offset:10016\n\t" \
  "v_mfma_f32_16x16x32_bf16 v[88:91], v[212:215], v[170:173], v[88:91]\n\t" \
  "s_setprio 1\n\t" \
  "v_fma_mix_f32 v230, |v192|, v230, v230 op_sel:[0,1,0] op_sel_hi:[0,1,1] clamp\n\t" \
  "v_fma_mix_f32 v231, |v193|, v231, v231 op_sel:[0,1,0] op_sel_hi:[0,1,1] clamp\n\t" \
  "s_setprio 2\n\t" \
  "s_waitcnt lgkmcnt(4)\n\t" \
  "ds_read_b64_tr_b16 v[210:211], v248 offset:44072\n\t" \
  "v_mfma_f32_16x16x32_bf16 v[40:43], v[212:215], v[154:157], v[40:43]\n\t" \
  "v_lshlrev_b32_sdwa v226, 1, v190 dst_sel:DWORD dst_unused:UNUSED_PAD src0_sel:DWORD src1_sel:WORD_1\n\t" \
  "v_fma_mix_f32 v228, |v194|, v228, v228 op_sel:[0,1,0] op_sel_hi:[0,1,1] clamp\n\t" \
  "ds_read_b32 v222, v222 offset:10016\n\t" \
  "ds_read_b64_tr_b16 v[212:213], v248 offset:46120\n\t" \
  "v_mfma_f32_16x16x32_bf16 v[20:23], v[216:219], v[146:149], v[20:23]\n\t" \
  "v_or_b32 v203, v232, v233\n\t" \
  "v_or_b32 v202, v234, v235\n\t" \
  "ds_read_u16_d16_hi v225, v225 offset:10528\n\t" \
  "v_mfma_f32_16x16x32_bf16 v[68:71], v[216:219], v[162:165], v[68:71]\n\t" \
  "v_fma_f32 v195, |v195|, v229, v195 mul:4\n\t" \
  "v_fma_f32 v192, |v192|, v230, v192 mul:4\n\t" \
  "s_waitcnt lgkmcnt(3)\n\t" \
  "ds_read_u16 v224, v224 offset:10528\n\t" \
  "v_mfma_f32_16x16x32_bf16 v[144:147], v[208:211], v[96:99], 0\n\t" \
  "v_lshlrev_b32_sdwa v232, 1, v204 dst_sel:DWORD dst_unused:UNUSED_PAD src0_sel:DWORD src1_sel:WORD_1\n\t" \
  "v_fma_f32 v193, |v193|, v231, v193 mul:4\n\t" \
  "ds_read_b64_tr_b16 v[214:215], v248 offset:47144\n\t" \
  "v_mfma_f32_16x16x32_bf16 v[44:47], v[216:219], v[154:157], v[44:47]\n\t" \
  "v_cvt_pknorm_i16_f32 v230, |v198|, |v199|\n\t" \
  "v_fma_f32 v194, |v194|, v228, v194 mul:4\n\t" \
  "ds_read_u16_d16_hi v227, v227 offset:10528\n\t" \
  "v_mfma_f32_16x16x32_bf16 v[92:95], v[216:219], v[170:173], v[92:95]\n\t" \
  "v_cvt_pk_bf16_f32 v195, v194, v195\n\t" \
  "v_cvt_pk_bf16_f32 v194, v192, v193\n\t" \
  "ds_read_b64_tr_b16 v[216:217], v248 offset:45096\n\t" \
  "v_mfma_f32_16x16x32_bf16 v[160:163], v[208:211], v[104:107], 0\n\t" \
  "v_lshlrev_b32_sdwa v231, 2, v230 dst_sel:DWORD dst_unused:UNUSED_PAD src0_sel:DWORD src1_sel:BYTE_3\n\t" \
  "v_cvt_pknorm_i16_f32 v228, |v196|, |v197|\n\t" \
  "s_setprio 2\n\t" \
  "ds_read_b64_tr_b16 v[218:219], v248 offset:48168\n\t" \
  "v_mfma_f32_16x16x32_bf16 v[152:155], v[208:211], v[100:103], 0\n\t" \
  "v_lshlrev_b32_sdwa v229, 2, v228 dst_sel:DWORD dst_unused:UNUSED_PAD src0_sel:DWORD src1_sel:BYTE_3\n\t" \
  "v_lshlrev_b32_sdwa v228, 2, v228 dst_sel:DWORD dst_unused:UNUSED_PAD src0_sel:DWORD src1_sel:BYTE_1\n\t" \
  "ds_read_u16 v226, v226 offset:10528\n\t" \
  "v_mfma_f32_16x16x32_bf16 v[168:171], v[208:211], v[108:111], 0\n\t" \
  "v_lshlrev_b32_sdwa v234, 1, v206 dst_sel:DWORD dst_unused:UNUSED_PAD src0_sel:DWORD src1_sel:WORD_1\n\t" \
  "v_lshlrev_b32_sdwa v233, 1, v205 dst_sel:DWORD dst_unused:UNUSED_PAD src0_sel:DWORD src1_sel:WORD_1\n\t" \
  "s_waitcnt lgkmcnt(4)\n\t" \
  "ds_read_b64_tr_b16 v[208:209], v248 offset:43560\n\t" \
  "v_mfma_f32_16x16x32_bf16 v[144:147], v[212:215], v[112:115], v[144:147]\n\t" \
  "v_lshlrev_b32_sdwa v230, 2, v230 dst_sel:DWORD dst_unused:UNUSED_PAD src0_sel:DWORD src1_sel:BYTE_1\n\t" \
  "v_fma_mix_f32 v221, |v181|, v221, v221 op_sel:[0,1,0] op_sel_hi:[0,1,1] clamp\n\t" \
  "ds_read_b32 v231, v231 offset:10016\n\t" \
  "v_mfma_f32_16x16x32_bf16 v[168:171], v[212:215], v[124:127], v[168:171]\n\t" \
  "v_lshlrev_b32_sdwa v235, 1, v207 dst_sel:DWORD dst_unused:UNUSED_PAD src0_sel:DWORD src1_sel:WORD_1\n\t" \
  "ds_read_b32 v228, v228 offset:10016\n\t" \
  "ds_read_b32 v229, v229 offset:10016\n\t" \
  "v_mfma_f32_16x16x32_bf16 v[160:163], v[212:215], v[120:123], v[160:163]\n\t" \
  "v_fma_mix_f32 v220, |v180|, v220, v220 op_sel:[0,1,0] op_sel_hi:[0,1,1] clamp\n\t" \
  "s_waitcnt lgkmcnt(5)\n\t" \
  "ds_read_b64_tr_b16 v[210:211], v248 offset:44584\n\t" \
  "ds_read_u16_d16_hi v233, v233 offset:10528\n\t" \
  "v_mfma_f32_16x16x32_bf16 v[152:155], v[212:215], v[116:119], v[152:155]\n\t" \
  "v_fma_mix_f32 v223, |v183|, v223, v223 op_sel:[0,1,0] op_sel_hi:[0,1,1] clamp\n\t" \
  "v_fma_mix_f32 v222, |v182|, v222, v222 op_sel:[0,1,0] op_sel_hi:[0,1,1] clamp\n\t" \
  "s_waitcnt lgkmcnt(6)\n\t" \
  "ds_read_b64_tr_b16 v[212:213], v248 offset:46632\n\t" \
  "v_mfma_f32_16x16x32_bf16 v[144:147], v[216:219], v[128:131], v[144:147]\n\t" \
  "v_or_b32 v188, v224, v225\n\t" \
  "v_or_b32 v189, v226, v227\n\t" \
  "ds_read_b64_tr_b16 v[214:215], v248 offset:47656\n\t" \
  "v_mfma_f32_16x16x32_bf16 v[160:163], v[216:219], v[136:139], v[160:163]\n\t" \
  "s_nop 0\n\t" \
  "v_fma_f32 v182, |v182|, v222, v182 mul:4\n\t" \
  "v_fma_f32 v183, |v183|, v223, v183 mul:4\n\t" \
  "ds_read_b32 v230, v230 offset:10016\n\t" \
  "v_mfma_f32_16x16x32_bf16 v[152:155], v[216:219], v[132:135], v[152:155]\n\t" \
  "v_fma_f32 v181, |v181|, v221, v181 mul:4\n\t" \
  "v_fma_f32 v180, |v180|, v220, v180 mul:4\n\t" \
  "ds_read_u16_d16_hi v235, v235 offset:10528\n\t" \
  "ds_read_u16 v232, v232 offset:10528\n\t" \
  "v_mfma_f32_16x16x32_bf16 v[168:171], v[216:219], v[140:143], v[168:171]\n\t" \
  "v_cvt_pknorm_i16_f32 v220, |v146|, |v147|\n\t" \
  "v_cvt_pk_bf16_f32 v180, v180, v181\n\t" \
  "s_setprio 0\n\t" \
  "s_waitcnt lgkmcnt(6)\n\t" \
  "ds_read_b64_tr_b16 v[216:217], v248 offset:45608\n\t" \
  "v_mfma_f32_16x16x32_bf16 v[148:151], v[208:211], v[96:99], 0\n\t" \
  "v_cvt_pk_bf16_f32 v181, v182, v183\n\t" \
  "v_cvt_pknorm_i16_f32 v222, |v144|, |v145|\n\t" \
  "s_waitcnt vmcnt(2)\n\t" \
  "ds_read_b64_tr_b16 v[218:219], v248 offset:48680\n\t" \
  "s_waitcnt lgkmcnt(5)\n\t" \
  "v_mfma_f32_16x16x32_bf16 v[156:159], v[208:211], v[100:103], 0\n\t" \
  "v_lshlrev_b32_sdwa v221, 2, v220 dst_sel:DWORD dst_unused:UNUSED_PAD src0_sel:DWORD src1_sel:BYTE_3\n\t" \
  "v_lshlrev_b32_sdwa v220, 2, v220 dst_sel:DWORD dst_unused:UNUSED_PAD src0_sel:DWORD src1_sel:BYTE_1\n\t" \
  "s_mov_b64 exec, 3\n\t" \
  "ds_add_rtn_u32 v246, v251, v250 offset:47200\n\t" \
  "v_mfma_f32_16x16x32_bf16 v[164:167], v[208:211], v[104:107], 0\n\t" \
  "s_mov_b64 exec, -1\n\t" \
  "v_lshlrev_b32_sdwa v223, 2, v222 dst_sel:DWORD dst_unused:UNUSED_PAD src0_sel:DWORD src1_sel:BYTE_3\n\t" \
  "v_lshlrev_b32_sdwa v222, 2, v222 dst_sel:DWORD dst_unused:UNUSED_PAD src0_sel:DWORD src1_sel:BYTE_1\n\t" \
  "ds_read_u16 v234, v234 offset:10528\n\t" \
  "v_mfma_f32_16x16x32_bf16 v[156:159], v[212:215], v[116:119], v[156:159]\n\t" \
  "v_lshlrev_b32_sdwa v226, 1, v152 dst_sel:DWORD dst_unused:UNUSED_PAD src0_sel:DWORD src1_sel:WORD_1\n\t" \
  "v_lshlrev_b32_sdwa v227, 1, v153 dst_sel:DWORD dst_unused:UNUSED_PAD src0_sel:DWORD src1_sel:WORD_1\n\t" \
  "ds_read_b32 v223, v223 offset:10016\n\t" \
  "v_mfma_f32_16x16x32_bf16 v[148:151], v[212:215], v[112:115], v[148:151]\n\t" \
  "s_waitcnt lgkmcnt(2)\n\t" \
  "v_lshlrev_b32_sdwa v225, 1, v155 dst_sel:DWORD dst_unused:UNUSED_PAD src0_sel:DWORD src1_sel:WORD_1\n\t" \
  "v_readfirstlane_b32 s4, v246\n\t" \
  "ds_read_b32 v221, v221 offset:10016\n\t" \
  "v_mfma_f32_16x16x32_bf16 v[172:175], v[208:211], v[108:111], 0\n\t" \
  "s_cmpk_lt_u32 s4, 3072\n\t" \
  "v_lshlrev_b32_sdwa v224, 1, v154 dst_sel:DWORD dst_unused:UNUSED_PAD src0_sel:DWORD src1_sel:WORD_1\n\t" \
  "s_cbranch_scc1 .Lcnx_w0_9\n\t" \
  ".Lcnx_g0_9:\n\t" \
  "ds_read_b32 v220, v220 offset:10016\n\t" \
  "v_mfma_f32_16x16x32_bf16 v[164:167], v[212:215], v[120:123], v[164:167]\n\t" \
  "v_fma_mix_f32 v229, |v197|, v229, v229 op_sel:[0,1,0] op_sel_hi:[0,1,1] clamp\n\t" \
  "v_fma_mix_f32 v228, |v196|, v228, v228 op_sel:[0,1,0] op_sel_hi:[0,1,1] clamp\n\t" \
  "ds_read_b64_tr_b16 v[210:211], v249 offset:13840\n\t" \
  "ds_read_b64_tr_b16 v[208:209], v249 offset:12304\n\t" \
  "v_mfma_f32_16x16x32_bf16 v[172:175], v[212:215], v[124:127], v[172:175]\n\t" \
  "v_fma_mix_f32 v230, |v198|, v230, v230 op_sel:[0,1,0] op_sel_hi:[0,1,1] clamp\n\t" \
  "v_fma_mix_f32 v231, |v199|, v231, v231 op_sel:[0,1,0] op_sel_hi:[0,1,1] clamp\n\t" \
  "s_waitcnt lgkmcnt(5)\n\t" \
  "ds_read_b32 v222, v222 offset:10016\n\t" \
  "ds_read_b64_tr_b16 v[212:213], v249 offset:12312\n\t" \
  "v_mfma_f32_16x16x32_bf16 v[148:151], v[216:219], v[128:131], v[148:151]\n\t" \
  "s_mov_b32 m0, s45\n\t" \
  "v_or_b32 v204, v232, v233\n\t" \
  "v_or_b32 v205, v234, v235\n\t" \
  "s_mov_b32 exec_hi, 0xffff\n\t" \
  "buffer_load_dwordx4 v252, s[16:19], s33 offen lds\n\t" \
  "v_mfma_f32_16x16x32_bf16 v[156:159], v[216:219], v[132:135], v[156:159]\n\t" \
  "s_mov_b32 exec_hi, -1\n\t" \
  "v_fma_f32 v196, |v196|, v228, v196 mul:4\n\t" \
  "v_fma_f32 v197, |v197|, v229, v197 mul:4\n\t" \
  "s_add_u32 s33, s33, s48\n\t" \
  "ds_read_u16_d16_hi v227, v227 offset:10528\n\t" \
  "ds_read_b64_tr_b16 v[214:215], v249 offset:13848\n\t" \
  "v_mfma_f32_16x16x32_bf16 v[164:167], v[216:219], v[136:139], v[164:167]\n\t" \
  "v_fma_f32 v198, |v198|, v230, v198 mul:4\n\t" \
  "v_fma_f32 v199, |v199|, v231, v199 mul:4\n\t" \
  "ds_read_u16_d16_hi v225, v225 offset:10528\n\t" \
  "v_mfma_f32_16x16x32_bf16 v[172:175], v[216:219], v[140:143], v[172:175]\n\t" \
  "v_cvt_pk_bf16_f32 v196, v196, v197\n\t" \
  "v_cvt_pk_bf16_f32 v197, v198, v199\n\t" \
  "ds_read_b64_tr_b16 v[216:217], v249 offset:12368\n\t" \
  "s_waitcnt lgkmcnt(6)\n\t" \
  "v_mfma_f32_16x16x32_bf16 v[0:3], v[208:211], v[178:181], v[0:3]\n\t" \
  "v_cvt_pknorm_i16_f32 v230, |v160|, |v161|\n\t" \
  "v_cvt_pknorm_i16_f32 v228, |v162|, |v163|\n\t" \
  "ds_read_b64_tr_b16 v[218:219], v249 offset:13904\n\t" \
  "v_mfma_f32_16x16x32_bf16 v[24:27], v[208:211], v[186:189], v[24:27]\n\t" \
  "v_lshlrev_b32_sdwa v229, 2, v228 dst_sel:DWORD dst_unused:UNUSED_PAD src0_sel:DWORD src1_sel:BYTE_3\n\t" \
  "v_lshlrev_b32_sdwa v228, 2, v228 dst_sel:DWORD dst_unused:UNUSED_PAD src0_sel:DWORD src1_sel:BYTE_1\n\t" \
  "ds_read_b32 v229, v229 offset:10016\n\t" \
  "ds_read_u16 v224, v224 offset:10528\n\t" \
  "v_mfma_f32_16x16x32_bf16 v[48:51], v[208:211], v[194:197], v[48:51]\n\t" \
  "v_lshlrev_b32_sdwa v231, 2, v230 dst_sel:DWORD dst_unused:UNUSED_PAD src0_sel:DWORD src1_sel:BYTE_3\n\t" \
  "s_setprio 1\n\t" \
  "s_add_u32 m0, s45, s35\n\t" \
  "s_waitcnt lgkmcnt(5)\n\t" \
  "ds_read_b32 v228, v228 offset:10016\n\t" \
  "v_mfma_f32_16x16x32_bf16 v[72:75], v[208:211], v[202:205], v[72:75]\n\t" \
  "v_lshlrev_b32_sdwa v232, 1, v170 dst_sel:DWORD dst_unused:UNUSED_PAD src0_sel:DWORD src1_sel:WORD_1\n\t" \
  "v_lshlrev_b32_sdwa v233, 1, v171 dst_sel:DWORD dst_unused:UNUSED_PAD src0_sel:DWORD src1_sel:WORD_1\n\t" \
  "s_mov_b32 exec_hi, 0xffff\n\t" \
  "buffer_load_dwordx4 v252, s[16:19], s34 offen lds\n\t" \
  "v_mfma_f32_16x16x32_bf16 v[4:7], v[212:215], v[178:181], v[4:7]\n\t" \
  "s_mov_b32 exec_hi, -1\n\t" \
  "s_add_u32 s34, s34, s48\n\t" \
  "v_lshlrev_b32_sdwa v235, 1, v169 dst_sel:DWORD dst_unused:UNUSED_PAD src0_sel:DWORD src1_sel:WORD_1\n\t" \
  "ds_read_u16 v226, v226 offset:10528\n\t" \
  "v_mfma_f32_16x16x32_bf16 v[28:31], v[212:215], v[186:189], v[28:31]\n\t" \
  "v_lshlrev_b32_sdwa v234, 1, v168 dst_sel:DWORD dst_unused:UNUSED_PAD src0_sel:DWORD src1_sel:WORD_1\n\t" \
  "v_lshlrev_b32_sdwa v230, 2, v230 dst_sel:DWORD dst_unused:UNUSED_PAD src0_sel:DWORD src1_sel:BYTE_1\n\t" \
  "ds_read_b32 v230, v230 offset:10016\n\t" \
  "ds_read_b64_tr_b16 v[208:209], v249 offset:12376\n\t" \
  "v_mfma_f32_16x16x32_bf16 v[52:55], v[212:215], v[194:197], v[52:55]\n\t" \
  "v_fma_mix_f32 v220, |v146|, v220, v220 op_sel:[0,1,0] op_sel_hi:[0,1,1] clamp\n\t" \
  "v_fma_mix_f32 v221, |v147|, v221, v221 op_sel:[0,1,0] op_sel_hi:[0,1,1] clamp\n\t" \
  "ds_read_b64_tr_b16 v[210:211], v249 offset:13912\n\t" \
  "v_mfma_f32_16x16x32_bf16 v[76:79], v[212:215], v[202:205], v[76:79]\n\t" \
  "s_waitcnt lgkmcnt(7)\n\t" \
  "v_fma_mix_f32 v223, |v145|, v223, v223 op_sel:[0,1,0] op_sel_hi:[0,1,1] clamp\n\t" \
  "v_fma_mix_f32 v222, |v144|, v222, v222 op_sel:[0,1,0] op_sel_hi:[0,1,1] clamp\n\t" \
  "s_add_u32 m0, s25, 65080\n\t" \
  "s_waitcnt lgkmcnt(3)\n\t" \
  "ds_read_b32 v231, v231 offset:10016\n\t" \
  "ds_read_b64_tr_b16 v[212:213], v249 offset:12432\n\t" \
  "v_mfma_f32_16x16x32_bf16 v[8:11], v[216:219], v[178:181], v[8:11]\n\t" \
  "v_or_b32 v155, v224, v225\n\t" \
  "v_or_b32 v154, v226, v227\n\t" \
  "s_setprio 1\n\t" \
  "s_setprio 0\n\t" \
  "ds_read_u16_d16_hi v233, v233 offset:10528\n\t" \
  "v_mfma_f32_16x16x32_bf16 v[56:59], v[216:219], v[194:197], v[56:59]\n\t" \
  "v_fma_f32 v144, |v144|, v222, v144 mul:4\n\t" \
  "v_fma_f32 v145, |v145|, v223, v145 mul:4\n\t" \
  "ds_read_u16 v232, v232 offset:10528\n\t" \
  "v_mfma_f32_16x16x32_bf16 v[32:35], v[216:219], v[186:189], v[32:35]\n\t" \
  "v_fma_f32 v146, |v146|, v220, v146 mul:4\n\t" \
  "v_fma_f32 v147, |v147|, v221, v147 mul:4\n\t" \
  "ds_read_b64_tr_b16 v[214:215], v249 offset:13968\n\t" \
  "v_mfma_f32_16x16x32_bf16 v[80:83], v[216:219], v[202:205], v[80:83]\n\t" \
  "s_waitcnt lgkmcnt(5)\n\t" \
  "v_cvt_pk_bf16_f32 v147, v146, v147\n\t" \
  "v_cvt_pk_bf16_f32 v146, v144, v145\n\t" \
  "s_add_u32 s24, s24, s26\n\t" \
  "buffer_load_dwordx4 v254, s[8:11], s24 offen lds\n\t" \
  "v_mfma_f32_16x16x32_bf16 v[12:15], v[208:211], v[178:181], v[12:15]\n\t" \
  "v_cvt_pknorm_i16_f32 v220, |v148|, |v149|\n\t" \
  "v_cvt_pknorm_i16_f32 v222, |v150|, |v151|\n\t" \
  "ds_read_b64_tr_b16 v[218:219], v249 offset:13976\n\t" \
  "ds_read_b64_tr_b16 v[216:217], v249 offset:12440\n\t" \
  "v_mfma_f32_16x16x32_bf16 v[36:39], v[208:211], v[186:189], v[36:39]\n\t" \
  "v_lshlrev_b32_sdwa v221, 2, v220 dst_sel:DWORD dst_unused:UNUSED_PAD src0_sel:DWORD src1_sel:BYTE_3\n\t" \
  "v_lshlrev_b32_sdwa v220, 2, v220 dst_sel:DWORD dst_unused:UNUSED_PAD src0_sel:DWORD src1_sel:BYTE_1\n\t" \
  "ds_read_u16_d16_hi v235, v235 offset:10528\n\t" \
  "v_mfma_f32_16x16x32_bf16 v[60:63], v[208:211], v[194:197], v[60:63]\n\t" \
  "s_waitcnt lgkmcnt(3)\n\t" \
  "v_lshlrev_b32_sdwa v223, 2, v222 dst_sel:DWORD dst_unused:UNUSED_PAD src0_sel:DWORD src1_sel:BYTE_3\n\t" \
  "v_lshlrev_b32_sdwa v222, 2, v222 dst_sel:DWORD dst_unused:UNUSED_PAD src0_sel:DWORD src1_sel:BYTE_1\n\t" \
  "ds_read_u16 v234, v234 offset:10528\n\t" \
  "v_mfma_f32_16x16x32_bf16 v[84:87], v[208:211], v[202:205], v[84:87]\n\t" \
  "v_lshlrev_b32_sdwa v224, 1, v156 dst_sel:DWORD dst_unused:UNUSED_PAD src0_sel:DWORD src1_sel:WORD_1\n\t" \
  "v_lshlrev_b32_sdwa v225, 1, v157 dst_sel:DWORD dst_unused:UNUSED_PAD src0_sel:DWORD src1_sel:WORD_1\n\t" \
  "ds_read_b32 v220, v220 offset:10016\n\t" \
  "ds_read_b64_tr_b16 v[208:209], v248 offset:49192\n\t" \
  "v_mfma_f32_16x16x32_bf16 v[16:19], v[212:215], v[178:181], v[16:19]\n\t" \
  "v_nop\n\t" \
  "s_add_u32 s27, s27, s26\n\t" \
  "s_mov_b64 exec, s[30:31]\n\t" \
  "buffer_load_dword v237, v254, s[8:11], s27 offen\n\t" \
  "v_mfma_f32_16x16x32_bf16 v[64:67], v[212:215], v[194:197], v[64:67]\n\t" \
  "s_mov_b64 exec, -1\n\t" \
  "v_lshlrev_b32_sdwa v227, 1, v159 dst_sel:DWORD dst_unused:UNUSED_PAD src0_sel:DWORD src1_sel:WORD_1\n\t" \
  "v_fma_mix_f32 v229, |v163|, v229, v229 op_sel:[0,1,0] op_sel_hi:[0,1,1] clamp\n\t" \
  "ds_read_b32 v223, v223 offset:10016\n\t" \
  "ds_read_b32 v221, v221 offset:10016\n\t" \
  "v_mfma_f32_16x16x32_bf16 v[88:91], v[212:215], v[202:205], v[88:91]\n\t" \
  "s_setprio 1\n\t" \
  "v_fma_mix_f32 v230, |v160|, v230, v230 op_sel:[0,1,0] op_sel_hi:[0,1,1] clamp\n\t" \
  "v_fma_mix_f32 v231, |v161|, v231, v231 op_sel:[0,1,0] op_sel_hi:[0,1,1] clamp\n\t" \
  "s_setprio 2\n\t" \
  "s_waitcnt lgkmcnt(4)\n\t" \
  "ds_read_b64_tr_b16 v[210:211], v248 offset:50216\n\t" \
  "v_mfma_f32_16x16x32_bf16 v[40:43], v[212:215], v[186:189], v[40:43]\n\t" \
  "v_lshlrev_b32_sdwa v226, 1, v158 dst_sel:DWORD dst_unused:UNUSED_PAD src0_sel:DWORD src1_sel:WORD_1\n\t" \
  "v_fma_mix_f32 v228, |v162|, v228, v228 op_sel:[0,1,0] op_sel_hi:[0,1,1] clamp\n\t" \
  "ds_read_b32 v222, v222 offset:10016\n\t" \
  "ds_read_b64_tr_b16 v[212:213], v248 offset:52264\n\t" \
  "v_mfma_f32_16x16x32_bf16 v[20:23], v[216:219], v[178:181], v[20:23]\n\t" \
  "v_or_b32 v171, v232, v233\n\t" \
  "v_or_b32 v170, v234, v235\n\t" \
  "ds_read_u16_d16_hi v225, v225 offset:10528\n\t" \
  "v_mfma_f32_16x16x32_bf16 v[68:71], v[216:219], v[194:197], v[68:71]\n\t" \
  "v_fma_f32 v163, |v163|, v229, v163 mul:4\n\t" \
  "v_fma_f32 v160, |v160|, v230, v160 mul:4\n\t" \
  "s_waitcnt lgkmcnt(3)\n\t" \
  "ds_read_u16 v224, v224 offset:10528\n\t" \
  "v_mfma_f32_16x16x32_bf16 v[176:179], v[208:211], v[96:99], 0\n\t" \
  "v_lshlrev_b32_sdwa v232, 1, v172 dst_sel:DWORD dst_unused:UNUSED_PAD src0_sel:DWORD src1_sel:WORD_1\n\t" \
  "v_fma_f32 v161, |v161|, v231, v161 mul:4\n\t" \
  "ds_read_b64_tr_b16 v[214:215], v248 offset:53288\n\t" \
  "v_mfma_f32_16x16x32_bf16 v[44:47], v[216:219], v[186:189], v[44:47]\n\t" \
  "v_cvt_pknorm_i16_f32 v230, |v166|, |v167|\n\t" \
  "v_fma_f32 v162, |v162|, v228, v162 mul:4\n\t" \
  "ds_read_u16_d16_hi v227, v227 offset:10528\n\t" \
  "v_mfma_f32_16x16x32_bf16 v[92:95], v[216:219], v[202:205], v[92:95]\n\t" \
  "v_cvt_pk_bf16_f32 v163, v162, v163\n\t" \
  "v_cvt_pk_bf16_f32 v162, v160, v161\n\t" \
  "ds_read_b64_tr_b16 v[216:217], v248 offset:51240\n\t" \
  "v_mfma_f32_16x16x32_bf16 v[192:195], v[208:211], v[104:107], 0\n\t" \
  "v_lshlrev_b32_sdwa v231, 2, v230 dst_sel:DWORD dst_unused:UNUSED_PAD src0_sel:DWORD src1_sel:BYTE_3\n\t" \
  "v_cvt_pknorm_i16_f32 v228, |v164|, |v165|\n\t" \
  "s_setprio 2\n\t" \
  "ds_read_b64_tr_b16 v[218:219], v248 offset:54312\n\t" \
  "v_mfma_f32_16x16x32_bf16 v[184:187], v[208:211], v[100:103], 0\n\t" \
  "v_lshlrev_b32_sdwa v229, 2, v228 dst_sel:DWORD dst_unused:UNUSED_PAD src0_sel:DWORD src1_sel:BYTE_3\n\t" \
  "v_lshlrev_b32_sdwa v228, 2, v228 dst_sel:DWORD dst_unused:UNUSED_PAD src0_sel:DWORD src1_sel:BYTE_1\n\t" \
  "ds_read_u16 v226, v226 offset:10528\n\t" \
  "v_mfma_f32_16x16x32_bf16 v[200:203], v[208:211], v[108:111], 0\n\t" \
  "v_lshlrev_b32_sdwa v234, 1, v174 dst_sel:DWORD dst_unused:UNUSED_PAD src0_sel:DWORD src1_sel:WORD_1\n\t" \
  "v_lshlrev_b32_sdwa v233, 1, v173 dst_sel:DWORD dst_unused:UNUSED_PAD src0_sel:DWORD src1_sel:WORD_1\n\t" \
  "s_waitcnt lgkmcnt(4)\n\t" \
  "ds_read_b64_tr_b16 v[208:209], v248 offset:49704\n\t" \
  "v_mfma_f32_16x16x32_bf16 v[176:179], v[212:215], v[112:115], v[176:179]\n\t" \
  "v_lshlrev_b32_sdwa v230, 2, v230 dst_sel:DWORD dst_unused:UNUSED_PAD src0_sel:DWORD src1_sel:BYTE_1\n\t" \
  "v_fma_mix_f32 v221, |v149|, v221, v221 op_sel:[0,1,0] op_sel_hi:[0,1,1] clamp\n\t" \
  "ds_read_b32 v231, v231 offset:10016\n\t" \
  "v_mfma_f32_16x16x32_bf16 v[200:203], v[212:215], v[124:127], v[200:203]\n\t" \
  "v_lshlrev_b32_sdwa v235, 1, v175 dst_sel:DWORD dst_unused:UNUSED_PAD src0_sel:DWORD src1_sel:WORD_1\n\t" \
  "ds_read_b32 v228, v228 offset:10016\n\t" \
  "ds_read_b32 v229, v229 offset:10016\n\t" \
  "v_mfma_f32_16x16x32_bf16 v[192:195], v[212:215], v[120:123], v[192:195]\n\t" \
  "v_fma_mix_f32 v220, |v148|, v220, v220 op_sel:[0,1,0] op_sel_hi:[0,1,1] clamp\n\t" \
  "s_waitcnt lgkmcnt(5)\n\t" \
  "ds_read_b64_tr_b16 v[210:211], v248 offset:50728\n\t" \
  "ds_read_u16_d16_hi v233, v233 offset:10528\n\t" \
  "v_mfma_f32_16x16x32_bf16 v[184:187], v[212:215], v[116:119], v[184:187]\n\t" \
  "v_fma_mix_f32 v223, |v151|, v223, v223 op_sel:[0,1,0] op_sel_hi:[0,1,1] clamp\n\t" \
  "v_fma_mix_f32 v222, |v150|, v222, v222 op_sel:[0,1,0] op_sel_hi:[0,1,1] clamp\n\t" \
  "s_waitcnt lgkmcnt(6)\n\t" \
  "ds_read_b64_tr_b16 v[212:213], v248 offset:52776\n\t" \
  "v_mfma_f32_16x16x32_bf16 v[176:179], v[216:219], v[128:131], v[176:179]\n\t" \
  "v_or_b32 v156, v224, v225\n\t" \
  "v_or_b32 v157, v226, v227\n\t" \
  "ds_read_b64_tr_b16 v[214:215], v248 offset:53800\n\t" \
  "v_mfma_f32_16x16x32_bf16 v[192:195], v[216:219], v[136:139], v[192:195]\n\t" \
  "s_nop 0\n\t" \
  "v_fma_f32 v150, |v150|, v222, v150 mul:4\n\t" \
  "v_fma_f32 v151, |v151|, v223, v151 mul:4\n\t" \
  "ds_read_b32 v230, v230 offset:10016\n\t" \
  "v_mfma_f32_16x16x32_bf16 v[184:187], v[216:219], v[132:135], v[184:187]\n\t" \
  "v_fma_f32 v149, |v149|, v221, v149 mul:4\n\t" \
  "v_fma_f32 v148, |v148|, v220, v148 mul:4\n\t" \
  "ds_read_u16_d16_hi v235, v235 offset:10528\n\t" \
  "ds_read_u16 v232, v232 offset:10528\n\t" \
  "v_mfma_f32_16x16x32_bf16 v[200:203], v[216:219], v[140:143], v[200:203]\n\t" \
  "v_cvt_pknorm_i16_f32 v220, |v178|, |v179|\n\t" \
  "v_cvt_pk_bf16_f32 v148, v148, v149\n\t" \
  "s_setprio 0\n\t" \
  "s_waitcnt lgkmcnt(6)\n\t" \
  "ds_read_b64_tr_b16 v[216:217], v248 offset:51752\n\t" \
  "v_mfma_f32_16x16x32_bf16 v[180:183], v[208:211], v[96:99], 0\n\t" \
  "v_cvt_pk_bf16_f32 v149, v150, v151\n\t" \
  "v_cvt_pknorm_i16_f32 v222, |v176|, |v177|\n\t" \
  "s_waitcnt vmcnt(2)\n\t" \
  "s_waitcnt lgkmcnt(4)\n\t" \
  "ds_read_b64_tr_b16 v[218:219], v248 offset:54824\n\t" \
  "v_mfma_f32_16x16x32_bf16 v[188:191], v[208:211], v[100:103], 0\n\t" \
  "v_lshlrev_b32_sdwa v221, 2, v220 dst_sel:DWORD dst_unused:UNUSED_PAD src0_sel:DWORD src1_sel:BYTE_3\n\t" \
  "v_lshlrev_b32_sdwa v220, 2, v220 dst_sel:DWORD dst_unused:UNUSED_PAD src0_sel:DWORD src1_sel:BYTE_1\n\t" \
  "s_mov_b64 exec, 3\n\t" \
  "ds_add_rtn_u32 v246, v251, v250 offset:47208\n\t" \
  "v_mfma_f32_16x16x32_bf16 v[196:199], v[208:211], v[104:107], 0\n\t" \
  "s_mov_b64 exec, -1\n\t" \
  "v_lshlrev_b32_sdwa v223, 2, v222 dst_sel:DWORD dst_unused:UNUSED_PAD src0_sel:DWORD src1_sel:BYTE_3\n\t" \
  "v_lshlrev_b32_sdwa v222, 2, v222 dst_sel:DWORD dst_unused:UNUSED_PAD src0_sel:DWORD src1_sel:BYTE_1\n\t" \
  "ds_read_u16 v234, v234 offset:10528\n\t" \
  "v_mfma_f32_16x16x32_bf16 v[188:191], v[212:215], v[116:119], v[188:191]\n\t" \
  "s_setprio 2\n\t" \
  "v_lshlrev_b32_sdwa v227, 1, v185 dst_sel:DWORD dst_unused:UNUSED_PAD src0_sel:DWORD src1_sel:WORD_1\n\t" \
  "v_lshlrev_b32_sdwa v226, 1, v184 dst_sel:DWORD dst_unused:UNUSED_PAD src0_sel:DWORD src1_sel:WORD_1\n\t" \
  "ds_read_b32 v223, v223 offset:10016\n\t" \
  "v_mfma_f32_16x16x32_bf16 v[180:183], v[212:215], v[112:115], v[180:183]\n\t" \
  "s_waitcnt lgkmcnt(2)\n\t" \
  "v_lshlrev_b32_sdwa v225, 1, v187 dst_sel:DWORD dst_unused:UNUSED_PAD src0_sel:DWORD src1_sel:WORD_1\n\t" \
  "v_readfirstlane_b32 s4, v246\n\t" \
  "s_setprio 0\n\t" \
  "ds_read_b32 v221, v221 offset:10016\n\t" \
  "v_mfma_f32_16x16x32_bf16 v[204:207], v[208:211], v[108:111], 0\n\t" \
  "s_cmpk_lt_u32 s4, 3072\n\t" \
  "v_lshlrev_b32_sdwa v224, 1, v186 dst_sel:DWORD dst_unused:UNUSED_PAD src0_sel:DWORD src1_sel:WORD_1\n\t" \
  "s_cbranch_scc1 .Lcnx_w0_10\n\t" \
  ".Lcnx_g0_10:\n\t" \
  "ds_read_b32 v220, v220 offset:10016\n\t" \
  "v_mfma_f32_16x16x32_bf16 v[196:199], v[212:215], v[120:123], v[196:199]\n\t" \
  "v_fma_mix_f32 v229, |v165|, v229, v229 op_sel:[0,1,0] op_sel_hi:[0,1,1] clamp\n\t" \
  "v_fma_mix_f32 v228, |v164|, v228, v228 op_sel:[0,1,0] op_sel_hi:[0,1,1] clamp\n\t" \
  "ds_read_b64_tr_b16 v[208:209], v249 offset:24600\n\t" \
  "ds_read_b64_tr_b16 v[210:211], v249 offset:26136\n\t" \
  "v_mfma_f32_16x16x32_bf16 v[204:207], v[212:215], v[124:127], v[204:207]\n\t" \
  "v_fma_mix_f32 v230, |v166|, v230, v230 op_sel:[0,1,0] op_sel_hi:[0,1,1] clamp\n\t" \
  "v_fma_mix_f32 v231, |v167|, v231, v231 op_sel:[0,1,0] op_sel_hi:[0,1,1] clamp\n\t" \
  "s_waitcnt lgkmcnt(5)\n\t" \
  "ds_read_b64_tr_b16 v[212:213], v249 offset:24608\n\t" \
  "ds_read_b32 v222, v222 offset:10016\n\t" \
  "v_mfma_f32_16x16x32_bf16 v[180:183], v[216:219], v[128:131], v[180:183]\n\t" \
  "s_mov_b32 m0, s46\n\t" \
  "v_or_b32 v172, v232, v233\n\t" \
  "v_or_b32 v173, v234, v235\n\t" \
  "s_mov_b32 exec_hi, 0xffff\n\t" \
  "buffer_load_dwordx4 v252, s[16:19], s33 offen lds\n\t" \
  "v_mfma_f32_16x16x32_bf16 v[188:191], v[216:219], v[132:135], v[188:191]\n\t" \
  "s_mov_b32 exec_hi, -1\n\t" \
  "v_fma_f32 v165, |v165|, v229, v165 mul:4\n\t" \
  "v_fma_f32 v164, |v164|, v228, v164 mul:4\n\t" \
  "s_add_u32 s33, s33, s32\n\t" \
  "ds_read_b64_tr_b16 v[214:215], v249 offset:26144\n\t" \
  "ds_read_u16_d16_hi v227, v227 offset:10528\n\t" \
  "v_mfma_f32_16x16x32_bf16 v[196:199], v[216:219], v[136:139], v[196:199]\n\t" \
  "v_fma_f32 v166, |v166|, v230, v166 mul:4\n\t" \
  "v_fma_f32 v167, |v167|, v231, v167 mul:4\n\t" \
  "ds_read_u16_d16_hi v225, v225 offset:10528\n\t" \
  "v_mfma_f32_16x16x32_bf16 v[204:207], v[216:219], v[140:143], v[204:207]\n\t" \
  "s_waitcnt lgkmcnt(5)\n\t" \
  "v_cvt_pk_bf16_f32 v164, v164, v165\n\t" \
  "v_cvt_pk_bf16_f32 v165, v166, v167\n\t" \
  "ds_read_b64_tr_b16 v[216:217], v249 offset:24664\n\t" \
  "v_mfma_f32_16x16x32_bf16 v[0:3], v[208:211], v[146:149], v[0:3]\n\t" \
  "v_cvt_pknorm_i16_f32 v230, |v192|, |v193|\n\t" \
  "v_cvt_pknorm_i16_f32 v228, |v194|, |v195|\n\t" \
  "ds_read_b64_tr_b16 v[218:219], v249 offset:26200\n\t" \
  "v_mfma_f32_16x16x32_bf16 v[24:27], v[208:211], v[154:157], v[24:27]\n\t" \
  "v_lshlrev_b32_sdwa v229, 2, v228 dst_sel:DWORD dst_unused:UNUSED_PAD src0_sel:DWORD src1_sel:BYTE_3\n\t" \
  "v_lshlrev_b32_sdwa v228, 2, v228 dst_sel:DWORD dst_unused:UNUSED_PAD src0_sel:DWORD src1_sel:BYTE_1\n\t" \
  "ds_read_u16 v224, v224 offset:10528\n\t" \
  "v_mfma_f32_16x16x32_bf16 v[48:51], v[208:211], v[162:165], v[48:51]\n\t" \
  "s_add_u32 m0, s46, s35\n\t" \
  "s_setprio 1\n\t" \
  "v_lshlrev_b32_sdwa v231, 2, v230 dst_sel:DWORD dst_unused:UNUSED_PAD src0_sel:DWORD src1_sel:BYTE_3\n\t" \
  "ds_read_b32 v228, v228 offset:10016\n\t" \
  "s_waitcnt lgkmcnt(6)\n\t" \
  "v_mfma_f32_16x16x32_bf16 v[72:75], v[208:211], v[170:173], v[72:75]\n\t" \
  "v_lshlrev_b32_sdwa v233, 1, v203 dst_sel:DWORD dst_unused:UNUSED_PAD src0_sel:DWORD src1_sel:WORD_1\n\t" \
  "v_lshlrev_b32_sdwa v232, 1, v202 dst_sel:DWORD dst_unused:UNUSED_PAD src0_sel:DWORD src1_sel:WORD_1\n\t" \
  "s_mov_b32 exec_hi, 0xffff\n\t" \
  "buffer_load_dwordx4 v252, s[16:19], s34 offen lds\n\t" \
  "v_mfma_f32_16x16x32_bf16 v[4:7], v[212:215], v[146:149], v[4:7]\n\t" \
  "s_mov_b32 exec_hi, -1\n\t" \
  "s_add_u32 s34, s34, s32\n\t" \
  "v_lshlrev_b32_sdwa v235, 1, v201 dst_sel:DWORD dst_unused:UNUSED_PAD src0_sel:DWORD src1_sel:WORD_1\n\t" \
  "ds_read_u16 v226, v226 offset:10528\n\t" \
  "ds_read_b64_tr_b16 v[208:209], v249 offset:24672\n\t" \
  "v_mfma_f32_16x16x32_bf16 v[28:31], v[212:215], v[154:157], v[28:31]\n\t" \
  "v_lshlrev_b32_sdwa v230, 2, v230 dst_sel:DWORD dst_unused:UNUSED_PAD src0_sel:DWORD src1_sel:BYTE_1\n\t" \
  "v_lshlrev_b32_sdwa v234, 1, v200 dst_sel:DWORD dst_unused:UNUSED_PAD src0_sel:DWORD src1_sel:WORD_1\n\t" \
  "s_setprio 3\n\t" \
  "s_setprio 1\n\t" \
  "ds_read_b64_tr_b16 v[210:211], v249 offset:26208\n\t" \
  "ds_read_b32 v229, v229 offset:10016\n\t" \
  "v_mfma_f32_16x16x32_bf16 v[76:79], v[212:215], v[170:173], v[76:79]\n\t" \
  "v_fma_mix_f32 v223, |v177|, v223, v223 op_sel:[0,1,0] op_sel_hi:[0,1,1] clamp\n\t" \
  "v_fma_mix_f32 v222, |v176|, v222, v222 op_sel:[0,1,0] op_sel_hi:[0,1,1] clamp\n\t" \
  "ds_read_b32 v231, v231 offset:10016\n\t" \
  "ds_read_b32 v230, v230 offset:10016\n\t" \
  "v_mfma_f32_16x16x32_bf16 v[52:55], v[212:215], v[162:165], v[52:55]\n\t" \
  "s_waitcnt lgkmcnt(5)\n\t" \
  "v_fma_mix_f32 v221, |v179|, v221, v221 op_sel:[0,1,0] op_sel_hi:[0,1,1] clamp\n\t" \
  "v_fma_mix_f32 v220, |v178|, v220, v220 op_sel:[0,1,0] op_sel_hi:[0,1,1] clamp\n\t" \
  "ds_read_b64_tr_b16 v[212:213], v249 offset:24728\n\t" \
  "v_mfma_f32_16x16x32_bf16 v[8:11], v[216:219], v[146:149], v[8:11]\n\t" \
  "v_or_b32 v187, v224, v225\n\t" \
  "v_or_b32 v186, v226, v227\n\t" \
  "s_add_u32 s24, s24, s26\n\t" \
  "ds_read_u16_d16_hi v233, v233 offset:10528\n\t" \
  "v_mfma_f32_16x16x32_bf16 v[32:35], v[216:219], v[154:157], v[32:35]\n\t" \
  "v_fma_f32 v179, |v179|, v221, v179 mul:4\n\t" \
  "v_fma_f32 v178, |v178|, v220, v178 mul:4\n\t" \
  "ds_read_b64_tr_b16 v[214:215], v249 offset:26264\n\t" \
  "v_mfma_f32_16x16x32_bf16 v[56:59], v[216:219], v[162:165], v[56:59]\n\t" \
  "s_waitcnt lgkmcnt(6)\n\t" \
  "v_fma_f32 v176, |v176|, v222, v176 mul:4\n\t" \
  "v_fma_f32 v177, |v177|, v223, v177 mul:4\n\t" \
  "s_add_u32 m0, s25, 73280\n\t" \
  "ds_read_u16 v232, v232 offset:10528\n\t" \
  "v_mfma_f32_16x16x32_bf16 v[80:83], v[216:219], v[170:173], v[80:83]\n\t" \
  "v_cvt_pk_bf16_f32 v179, v178, v179\n\t" \
  "v_cvt_pk_bf16_f32 v178, v176, v177\n\t" \
  "buffer_load_dwordx4 v254, s[8:11], s24 offen lds\n\t" \
  "v_mfma_f32_16x16x32_bf16 v[12:15], v[208:211], v[146:149], v[12:15]\n\t" \
  "s_setprio 0\n\t" \
  "v_cvt_pknorm_i16_f32 v220, |v180|, |v181|\n\t" \
  "v_cvt_pknorm_i16_f32 v222, |v182|, |v183|\n\t" \
  "ds_read_b64_tr_b16 v[216:217], v249 offset:24736\n\t" \
  "v_mfma_f32_16x16x32_bf16 v[36:39], v[208:211], v[154:157], v[36:39]\n\t" \
  "v_lshlrev_b32_sdwa v221, 2, v220 dst_sel:DWORD dst_unused:UNUSED_PAD src0_sel:DWORD src1_sel:BYTE_3\n\t" \
  "v_lshlrev_b32_sdwa v220, 2, v220 dst_sel:DWORD dst_unused:UNUSED_PAD src0_sel:DWORD src1_sel:BYTE_1\n\t" \
  "ds_read_u16_d16_hi v235, v235 offset:10528\n\t" \
  "ds_read_b64_tr_b16 v[218:219], v249 offset:26272\n\t" \
  "v_mfma_f32_16x16x32_bf16 v[60:63], v[208:211], v[162:165], v[60:63]\n\t" \
  "v_lshlrev_b32_sdwa v223, 2, v222 dst_sel:DWORD dst_unused:UNUSED_PAD src0_sel:DWORD src1_sel:BYTE_3\n\t" \
  "v_lshlrev_b32_sdwa v222, 2, v222 dst_sel:DWORD dst_unused:UNUSED_PAD src0_sel:DWORD src1_sel:BYTE_1\n\t" \
  "ds_read_u16 v234, v234 offset:10528\n\t" \
  "s_waitcnt lgkmcnt(5)\n\t" \
  "v_mfma_f32_16x16x32_bf16 v[84:87], v[208:211], v[170:173], v[84:87]\n\t" \
  "s_setprio 1\n\t" \
  "v_lshlrev_b32_sdwa v225, 1, v189 dst_sel:DWORD dst_unused:UNUSED_PAD src0_sel:DWORD src1_sel:WORD_1\n\t" \
  "v_lshlrev_b32_sdwa v224, 1, v188 dst_sel:DWORD dst_unused:UNUSED_PAD src0_sel:DWORD src1_sel:WORD_1\n\t" \
  "ds_read_b32 v220, v220 offset:10016\n\t" \
  "ds_read_b64_tr_b16 v[208:209], v248 offset:36904\n\t" \
  "v_mfma_f32_16x16x32_bf16 v[16:19], v[212:215], v[146:149], v[16:19]\n\t" \
  "s_add_u32 s27, s27, s26\n\t" \
  "v_nop\n\t" \
  "s_mov_b64 exec, s[30:31]\n\t" \
  "buffer_load_dword v237, v254, s[8:11], s27 offen\n\t" \
  "v_mfma_f32_16x16x32_bf16 v[64:67], v[212:215], v[162:165], v[64:67]\n\t" \
  "s_mov_b64 exec, -1\n\t" \
  "v_lshlrev_b32_sdwa v227, 1, v191 dst_sel:DWORD dst_unused:UNUSED_PAD src0_sel:DWORD src1_sel:WORD_1\n\t" \
  "v_fma_mix_f32 v229, |v195|, v229, v229 op_sel:[0,1,0] op_sel_hi:[0,1,1] clamp\n\t" \
  "s_setprio 2\n\t" \
  "ds_read_b32 v223, v223 offset:10016\n\t" \
  "ds_read_b32 v221, v221 offset:10016\n\t" \
  "v_mfma_f32_16x16x32_bf16 v[88:91], v[212:215], v[170:173], v[88:91]\n\t" \
  "s_setprio 1\n\t" \
  "v_fma_mix_f32 v230, |v192|, v230, v230 op_sel:[0,1,0] op_sel_hi:[0,1,1] clamp\n\t" \
  "v_fma_mix_f32 v231, |v193|, v231, v231 op_sel:[0,1,0] op_sel_hi:[0,1,1] clamp\n\t" \
  "ds_read_b64_tr_b16 v[210:211], v248 offset:37928\n\t" \
  "s_waitcnt lgkmcnt(5)\n\t" \
  "v_mfma_f32_16x16x32_bf16 v[40:43], v[212:215], v[154:157], v[40:43]\n\t" \
  "v_fma_mix_f32 v228, |v194|, v228, v228 op_sel:[0,1,0] op_sel_hi:[0,1,1] clamp\n\t" \
  "v_lshlrev_b32_sdwa v226, 1, v190 dst_sel:DWORD dst_unused:UNUSED_PAD src0_sel:DWORD src1_sel:WORD_1\n\t" \
  "ds_read_b32 v222, v222 offset:10016\n\t" \
  "ds_read_b64_tr_b16 v[212:213], v248 offset:39976\n\t" \
  "v_mfma_f32_16x16x32_bf16 v[20:23], v[216:219], v[146:149], v[20:23]\n\t" \
  "v_or_b32 v202, v234, v235\n\t" \
  "v_or_b32 v203, v232, v233\n\t" \
  "ds_read_u16_d16_hi v225, v225 offset:10528\n\t" \
  "v_mfma_f32_16x16x32_bf16 v[68:71], v[216:219], v[162:165], v[68:71]\n\t" \
  "v_fma_f32 v192, |v192|, v230, v192 mul:4\n\t" \
  "v_fma_f32 v193, |v193|, v231, v193 mul:4\n\t" \
  "ds_read_b64_tr_b16 v[214:215], v248 offset:41000\n\t" \
  "v_mfma_f32_16x16x32_bf16 v[44:47], v[216:219], v[154:157], v[44:47]\n\t" \
  "v_fma_f32 v195, |v195|, v229, v195 mul:4\n\t" \
  "v_fma_f32 v194, |v194|, v228, v194 mul:4\n\t" \
  "s_waitcnt lgkmcnt(4)\n\t" \
  "ds_read_u16 v224, v224 offset:10528\n\t" \
  "v_mfma_f32_16x16x32_bf16 v[92:95], v[216:219], v[170:173], v[92:95]\n\t" \
  "v_cvt_pk_bf16_f32 v195, v194, v195\n\t" \
  "v_cvt_pk_bf16_f32 v194, v192, v193\n\t" \
  "ds_read_b64_tr_b16 v[216:217], v248 offset:38952\n\t" \
  "v_mfma_f32_16x16x32_bf16 v[144:147], v[208:211], v[96:99], 0\n\t" \
  "v_cvt_pknorm_i16_f32 v228, |v196|, |v197|\n\t" \
  "v_cvt_pknorm_i16_f32 v230, |v198|, |v199|\n\t" \
  "ds_read_b64_tr_b16 v[218:219], v248 offset:42024\n\t" \
  "v_mfma_f32_16x16x32_bf16 v[152:155], v[208:211], v[100:103], 0\n\t" \
  "v_lshlrev_b32_sdwa v229, 2, v228 dst_sel:DWORD dst_unused:UNUSED_PAD src0_sel:DWORD src1_sel:BYTE_3\n\t" \
  "v_lshlrev_b32_sdwa v228, 2, v228 dst_sel:DWORD dst_unused:UNUSED_PAD src0_sel:DWORD src1_sel:BYTE_1\n\t" \
  "s_cmp_eq_u32 s38, 4\n\t" \
  "ds_read_u16_d16_hi v227, v227 offset:10528\n\t" \
  "v_mfma_f32_16x16x32_bf16 v[160:163], v[208:211], v[104:107], 0\n\t" \
  "v_lshlrev_b32_sdwa v231, 2, v230 dst_sel:DWORD dst_unused:UNUSED_PAD src0_sel:DWORD src1_sel:BYTE_3\n\t" \
  "s_cbranch_scc1 .Lcnx_t0\n\t" \
  ".Lcnx_u0:\n\t" \
  "ds_read_u16 v226, v226 offset:10528\n\t" \
  "v_mfma_f32_16x16x32_bf16 v[168:171], v[208:211], v[108:111], 0\n\t" \
  "v_lshlrev_b32_sdwa v233, 1, v205 dst_sel:DWORD dst_unused:UNUSED_PAD src0_sel:DWORD src1_sel:WORD_1\n\t" \
  "v_lshlrev_b32_sdwa v230, 2, v230 dst_sel:DWORD dst_unused:UNUSED_PAD src0_sel:DWORD src1_sel:BYTE_1\n\t" \
  "s_waitcnt lgkmcnt(5)\n\t" \
  "ds_read_b64_tr_b16 v[208:209], v248 offset:37416\n\t" \
  "v_mfma_f32_16x16x32_bf16 v[144:147], v[212:215], v[112:115], v[144:147]\n\t" \
  "v_lshlrev_b32_sdwa v235, 1, v207 dst_sel:DWORD dst_unused:UNUSED_PAD src0_sel:DWORD src1_sel:WORD_1\n\t" \
  "v_lshlrev_b32_sdwa v232, 1, v204 dst_sel:DWORD dst_unused:UNUSED_PAD src0_sel:DWORD src1_sel:WORD_1\n\t" \
  "ds_read_b64_tr_b16 v[210:211], v248 offset:38440\n\t" \
  "v_mfma_f32_16x16x32_bf16 v[152:155], v[212:215], v[116:119], v[152:155]\n\t" \
  "v_lshlrev_b32_sdwa v234, 1, v206 dst_sel:DWORD dst_unused:UNUSED_PAD src0_sel:DWORD src1_sel:WORD_1\n\t" \
  "v_fma_mix_f32 v221, |v181|, v221, v221 op_sel:[0,1,0] op_sel_hi:[0,1,1] clamp\n\t" \
  "s_setprio 2\n\t" \
  "ds_read_b32 v229, v229 offset:10016\n\t" \
  "ds_read_b32 v228, v228 offset:10016\n\t" \
  "v_mfma_f32_16x16x32_bf16 v[160:163], v[212:215], v[120:123], v[160:163]\n\t" \
  "v_fma_mix_f32 v220, |v180|, v220, v220 op_sel:[0,1,0] op_sel_hi:[0,1,1] clamp\n\t" \
  "v_fma_mix_f32 v223, |v183|, v223, v223 op_sel:[0,1,0] op_sel_hi:[0,1,1] clamp\n\t" \
  "s_waitcnt lgkmcnt(6)\n\t" \
  "ds_read_b32 v231, v231 offset:10016\n\t" \
  "ds_read_u16_d16_hi v233, v233 offset:10528\n\t" \
  "v_mfma_f32_16x16x32_bf16 v[168:171], v[212:215], v[124:127], v[168:171]\n\t" \
  "v_fma_mix_f32 v222, |v182|, v222, v222 op_sel:[0,1,0] op_sel_hi:[0,1,1] clamp\n\t" \
  "ds_read_b64_tr_b16 v[212:213], v248 offset:40488\n\t" \
  "s_waitcnt lgkmcnt(7)\n\t" \
  "v_mfma_f32_16x16x32_bf16 v[144:147], v[216:219], v[128:131], v[144:147]\n\t" \
  "v_or_b32 v189, v226, v227\n\t" \
  "v_or_b32 v188, v224, v225\n\t" \
  "ds_read_b64_tr_b16 v[214:215], v248 offset:41512\n\t" \
  "v_mfma_f32_16x16x32_bf16 v[160:163], v[216:219], v[136:139], v[160:163]\n\t" \
  "v_fma_f32 v183, |v183|, v223, v183 mul:4\n\t" \
  "v_fma_f32 v182, |v182|, v222, v182 mul:4\n\t" \
  "ds_read_b32 v230, v230 offset:10016\n\t" \
  "v_mfma_f32_16x16x32_bf16 v[152:155], v[216:219], v[132:135], v[152:155]\n\t" \
  "s_setprio 1\n\t" \
  "v_fma_f32 v180, |v180|, v220, v180 mul:4\n\t" \
  "v_fma_f32 v181, |v181|, v221, v181 mul:4\n\t" \
  "s_setprio 0\n\t" \
  "ds_read_u16_d16_hi v235, v235 offset:10528\n\t" \
  "ds_read_u16 v232, v232 offset:10528\n\t" \
  "v_mfma_f32_16x16x32_bf16 v[168:171], v[216:219], v[140:143], v[168:171]\n\t" \
  "v_cvt_pk_bf16_f32 v180, v180, v181\n\t" \
  "v_cvt_pk_bf16_f32 v181, v182, v183\n\t" \
  "s_waitcnt lgkmcnt(9)\n\t" \
  "ds_read_b64_tr_b16 v[216:217], v248 offset:39464\n\t" \
  "v_mfma_f32_16x16x32_bf16 v[148:151], v[208:211], v[96:99], 0\n\t" \
  "v_cvt_pknorm_i16_f32 v220, |v146|, |v147|\n\t" \
  "v_cvt_pknorm_i16_f32 v222, |v144|, |v145|\n\t" \
  "s_waitcnt vmcnt(2)\n\t" \
  "s_waitcnt lgkmcnt(4)\n\t" \
  "ds_read_b64_tr_b16 v[218:219], v248 offset:42536\n\t" \
  "v_mfma_f32_16x16x32_bf16 v[156:159], v[208:211], v[100:103], 0\n\t" \
  "v_lshlrev_b32_sdwa v221, 2, v220 dst_sel:DWORD dst_unused:UNUSED_PAD src0_sel:DWORD src1_sel:BYTE_3\n\t" \
  "v_lshlrev_b32_sdwa v220, 2, v220 dst_sel:DWORD dst_unused:UNUSED_PAD src0_sel:DWORD src1_sel:BYTE_1\n\t" \
  "s_mov_b64 exec, 3\n\t" \
  "ds_add_rtn_u32 v246, v251, v250 offset:47216\n\t" \
  "v_mfma_f32_16x16x32_bf16 v[164:167], v[208:211], v[104:107], 0\n\t" \
  "s_mov_b64 exec, -1\n\t" \
  "v_lshlrev_b32_sdwa v223, 2, v222 dst_sel:DWORD dst_unused:UNUSED_PAD src0_sel:DWORD src1_sel:BYTE_3\n\t" \
  "v_lshlrev_b32_sdwa v222, 2, v222 dst_sel:DWORD dst_unused:UNUSED_PAD src0_sel:DWORD src1_sel:BYTE_1\n\t" \
  "ds_read_u16 v234, v234 offset:10528\n\t" \
  "v_mfma_f32_16x16x32_bf16 v[156:159], v[212:215], v[116:119], v[156:159]\n\t" \
  "v_lshlrev_b32_sdwa v226, 1, v152 dst_sel:DWORD dst_unused:UNUSED_PAD src0_sel:DWORD src1_sel:WORD_1\n\t" \
  "v_lshlrev_b32_sdwa v227, 1, v153 dst_sel:DWORD dst_unused:UNUSED_PAD src0_sel:DWORD src1_sel:WORD_1\n\t" \
  "ds_read_b32 v223, v223 offset:10016\n\t" \
  "v_mfma_f32_16x16x32_bf16 v[148:151], v[212:215], v[112:115], v[148:151]\n\t" \
  "s_waitcnt lgkmcnt(2)\n\t" \
  "v_lshlrev_b32_sdwa v225, 1, v155 dst_sel:DWORD dst_unused:UNUSED_PAD src0_sel:DWORD src1_sel:WORD_1\n\t" \
  "v_readfirstlane_b32 s4, v246\n\t" \
  "ds_read_b32 v221, v221 offset:10016\n\t" \
  "v_mfma_f32_16x16x32_bf16 v[172:175], v[208:211], v[108:111], 0\n\t" \
  "s_cmpk_lt_u32 s4, 3072\n\t" \
  "v_lshlrev_b32_sdwa v224, 1, v154 dst_sel:DWORD dst_unused:UNUSED_PAD src0_sel:DWORD src1_sel:WORD_1\n\t" \
  "s_cbranch_scc1 .Lcnx_w0_11\n\t" \
  ".Lcnx_g0_11:\n\t" \
  "ds_read_b32 v220, v220 offset:10016\n\t" \
  "v_mfma_f32_16x16x32_bf16 v[164:167], v[212:215], v[120:123], v[164:167]\n\t" \
  "v_fma_mix_f32 v228, |v196|, v228, v228 op_sel:[0,1,0] op_sel_hi:[0,1,1] clamp\n\t" \
  "v_fma_mix_f32 v229, |v197|, v229, v229 op_sel:[0,1,0] op_sel_hi:[0,1,1] clamp\n\t" \
  "s_setprio 0\n\t" \
  "ds_read_b64_tr_b16 v[210:211], v249 offset:32288\n\t" \
  "ds_read_b64_tr_b16 v[208:209], v249 offset:30752\n\t" \
  "v_mfma_f32_16x16x32_bf16 v[172:175], v[212:215], v[124:127], v[172:175]\n\t" \
  "v_fma_mix_f32 v230, |v198|, v230, v230 op_sel:[0,1,0] op_sel_hi:[0,1,1] clamp\n\t" \
  "v_fma_mix_f32 v231, |v199|, v231, v231 op_sel:[0,1,0] op_sel_hi:[0,1,1] clamp\n\t" \
  "s_waitcnt lgkmcnt(5)\n\t" \
  "ds_read_b64_tr_b16 v[212:213], v249 offset:30760\n\t" \
  "ds_read_b32 v222, v222 offset:10016\n\t" \
  "v_mfma_f32_16x16x32_bf16 v[148:151], v[216:219], v[128:131], v[148:151]\n\t" \
  "s_mov_b32 m0, s47\n\t" \
  "v_or_b32 v204, v232, v233\n\t" \
  "v_or_b32 v205, v234, v235\n\t" \
  "s_mov_b32 exec_hi, 0xffff\n\t" \
  "buffer_load_dwordx4 v252, s[16:19], s33 offen lds\n\t" \
  "v_mfma_f32_16x16x32_bf16 v[156:159], v[216:219], v[132:135], v[156:159]\n\t" \
  "s_mov_b32 exec_hi, -1\n\t" \
  "v_fma_f32 v196, |v196|, v228, v196 mul:4\n\t" \
  "v_fma_f32 v197, |v197|, v229, v197 mul:4\n\t" \
  "s_add_u32 s33, s33, s49\n\t" \
  "ds_read_u16_d16_hi v227, v227 offset:10528\n\t" \
  "ds_read_b64_tr_b16 v[214:215], v249 offset:32296\n\t" \
  "v_mfma_f32_16x16x32_bf16 v[164:167], v[216:219], v[136:139], v[164:167]\n\t" \
  "v_fma_f32 v198, |v198|, v230, v198 mul:4\n\t" \
  "v_fma_f32 v199, |v199|, v231, v199 mul:4\n\t" \
  "ds_read_u16_d16_hi v225, v225 offset:10528\n\t" \
  "v_mfma_f32_16x16x32_bf16 v[172:175], v[216:219], v[140:143], v[172:175]\n\t" \
  "v_cvt_pk_bf16_f32 v196, v196, v197\n\t" \
  "v_cvt_pk_bf16_f32 v197, v198, v199\n\t" \
  "s_waitcnt lgkmcnt(5)\n\t" \
  "ds_read_b64_tr_b16 v[216:217], v249 offset:30816\n\t" \
  "v_mfma_f32_16x16x32_bf16 v[0:3], v[208:211], v[178:181], v[0:3]\n\t" \
  "v_cvt_pknorm_i16_f32 v230, |v160|, |v161|\n\t" \
  "v_cvt_pknorm_i16_f32 v228, |v162|, |v163|\n\t" \
  "ds_read_b64_tr_b16 v[218:219], v249 offset:32352\n\t" \
  "v_mfma_f32_16x16x32_bf16 v[24:27], v[208:211], v[186:189], v[24:27]\n\t" \
  "v_lshlrev_b32_sdwa v229, 2, v228 dst_sel:DWORD dst_unused:UNUSED_PAD src0_sel:DWORD src1_sel:BYTE_3\n\t" \
  "v_lshlrev_b32_sdwa v228, 2, v228 dst_sel:DWORD dst_unused:UNUSED_PAD src0_sel:DWORD src1_sel:BYTE_1\n\t" \
  "s_add_u32 m0, s47, s35\n\t" \
  "ds_read_b32 v228, v228 offset:10016\n\t" \
  "ds_read_u16 v224, v224 offset:10528\n\t" \
  "v_mfma_f32_16x16x32_bf16 v[48:51], v[208:211], v[194:197], v[48:51]\n\t" \
  "s_waitcnt lgkmcnt(5)\n\t" \
  "v_lshlrev_b32_sdwa v233, 1, v171 dst_sel:DWORD dst_unused:UNUSED_PAD src0_sel:DWORD src1_sel:WORD_1\n\t" \
  "v_mfma_f32_16x16x32_bf16 v[72:75], v[208:211], v[202:205], v[72:75]\n\t" \
  "v_lshlrev_b32_sdwa v232, 1, v170 dst_sel:DWORD dst_unused:UNUSED_PAD src0_sel:DWORD src1_sel:WORD_1\n\t" \
  "v_lshlrev_b32_sdwa v231, 2, v230 dst_sel:DWORD dst_unused:UNUSED_PAD src0_sel:DWORD src1_sel:BYTE_3\n\t" \
  "s_mov_b32 exec_hi, 0xffff\n\t" \
  "buffer_load_dwordx4 v252, s[16:19], s34 offen lds\n\t" \
  "v_mfma_f32_16x16x32_bf16 v[4:7], v[212:215], v[178:181], v[4:7]\n\t" \
  "s_mov_b32 exec_hi, -1\n\t" \
  "s_add_u32 s34, s34, s49\n\t" \
  "v_lshlrev_b32_sdwa v235, 1, v169 dst_sel:DWORD dst_unused:UNUSED_PAD src0_sel:DWORD src1_sel:WORD_1\n\t" \
  "ds_read_u16 v226, v226 offset:10528\n\t" \
  "ds_read_b64_tr_b16 v[208:209], v249 offset:30824\n\t" \
  "v_mfma_f32_16x16x32_bf16 v[28:31], v[212:215], v[186:189], v[28:31]\n\t" \
  "s_setprio 0\n\t" \
  "v_lshlrev_b32_sdwa v230, 2, v230 dst_sel:DWORD dst_unused:UNUSED_PAD src0_sel:DWORD src1_sel:BYTE_1\n\t" \
  "v_lshlrev_b32_sdwa v234, 1, v168 dst_sel:DWORD dst_unused:UNUSED_PAD src0_sel:DWORD src1_sel:WORD_1\n\t" \
  "ds_read_b32 v229, v229 offset:10016\n\t" \
  "ds_read_b64_tr_b16 v[210:211], v249 offset:32360\n\t" \
  "v_mfma_f32_16x16x32_bf16 v[76:79], v[212:215], v[202:205], v[76:79]\n\t" \
  "v_fma_mix_f32 v223, |v145|, v223, v223 op_sel:[0,1,0] op_sel_hi:[0,1,1] clamp\n\t" \
  "v_fma_mix_f32 v222, |v144|, v222, v222 op_sel:[0,1,0] op_sel_hi:[0,1,1] clamp\n\t" \
  "s_setprio 1\n\t" \
  "ds_read_b32 v230, v230 offset:10016\n\t" \
  "v_mfma_f32_16x16x32_bf16 v[52:55], v[212:215], v[194:197], v[52:55]\n\t" \
  "s_setprio 3\n\t" \
  "v_fma_mix_f32 v220, |v146|, v220, v220 op_sel:[0,1,0] op_sel_hi:[0,1,1] clamp\n\t" \
  "v_fma_mix_f32 v221, |v147|, v221, v221 op_sel:[0,1,0] op_sel_hi:[0,1,1] clamp\n\t" \
  "s_waitcnt lgkmcnt(4)\n\t" \
  "ds_read_b32 v231, v231 offset:10016\n\t" \
  "ds_read_b64_tr_b16 v[212:213], v249 offset:30880\n\t" \
  "v_mfma_f32_16x16x32_bf16 v[8:11], v[216:219], v[178:181], v[8:11]\n\t" \
  "v_or_b32 v155, v224, v225\n\t" \
  "v_or_b32 v154, v226, v227\n\t" \
  "ds_read_u16_d16_hi v233, v233 offset:10528\n\t" \
  "v_mfma_f32_16x16x32_bf16 v[32:35], v[216:219], v[186:189], v[32:35]\n\t" \
  "v_fma_f32 v145, |v145|, v223, v145 mul:4\n\t" \
  "v_fma_f32 v144, |v144|, v222, v144 mul:4\n\t" \
  "ds_read_u16 v232, v232 offset:10528\n\t" \
  "v_mfma_f32_16x16x32_bf16 v[80:83], v[216:219], v[202:205], v[80:83]\n\t" \
  "v_fma_f32 v146, |v146|, v220, v146 mul:4\n\t" \
  "v_fma_f32 v147, |v147|, v221, v147 mul:4\n\t" \
  "ds_read_b64_tr_b16 v[214:215], v249 offset:32416\n\t" \
  "v_mfma_f32_16x16x32_bf16 v[56:59], v[216:219], v[194:197], v[56:59]\n\t" \
  "s_waitcnt lgkmcnt(6)\n\t" \
  "v_cvt_pk_bf16_f32 v147, v146, v147\n\t" \
  "v_cvt_pk_bf16_f32 v146, v144, v145\n\t" \
  "v_mfma_f32_16x16x32_bf16 v[12:15], v[208:211], v[178:181], v[12:15]\n\t" \
  "v_cvt_pknorm_i16_f32 v220, |v148|, |v149|\n\t" \
  "v_cvt_pknorm_i16_f32 v222, |v150|, |v151|\n\t" \
  "ds_read_b64_tr_b16 v[216:217], v249 offset:30888\n\t" \
  "ds_read_b64_tr_b16 v[218:219], v249 offset:32424\n\t" \
  "v_mfma_f32_16x16x32_bf16 v[84:87], v[208:211], v[202:205], v[84:87]\n\t" \
  "v_lshlrev_b32_sdwa v221, 2, v220 dst_sel:DWORD dst_unused:UNUSED_PAD src0_sel:DWORD src1_sel:BYTE_3\n\t" \
  "v_lshlrev_b32_sdwa v220, 2, v220 dst_sel:DWORD dst_unused:UNUSED_PAD src0_sel:DWORD src1_sel:BYTE_1\n\t" \
  "ds_read_u16_d16_hi v235, v235 offset:10528\n\t" \
  "v_mfma_f32_16x16x32_bf16 v[60:63], v[208:211], v[194:197], v[60:63]\n\t" \
  "v_lshlrev_b32_sdwa v223, 2, v222 dst_sel:DWORD dst_unused:UNUSED_PAD src0_sel:DWORD src1_sel:BYTE_3\n\t" \
  "v_lshlrev_b32_sdwa v222, 2, v222 dst_sel:DWORD dst_unused:UNUSED_PAD src0_sel:DWORD src1_sel:BYTE_1\n\t" \
  "s_waitcnt lgkmcnt(3)\n\t" \
  "ds_read_u16 v234, v234 offset:10528\n\t" \
  "ds_read_b32 v220, v220 offset:10016\n\t" \
  "v_mfma_f32_16x16x32_bf16 v[16:19], v[212:215], v[178:181], v[16:19]\n\t" \
  "s_add_u32 s27, s27, s26\n\t" \
  "s_mov_b64 exec, s[30:31]\n\t" \
  "buffer_load_dword v237, v254, s[8:11], s27 offen\n\t" \
  "v_mfma_f32_16x16x32_bf16 v[64:67], v[212:215], v[194:197], v[64:67]\n\t" \
  "s_mov_b64 exec, -1\n\t" \
  "v_fma_mix_f32 v229, |v163|, v229, v229 op_sel:[0,1,0] op_sel_hi:[0,1,1] clamp\n\t" \
  "v_lshlrev_b32_sdwa v227, 1, v159 dst_sel:DWORD dst_unused:UNUSED_PAD src0_sel:DWORD src1_sel:WORD_1\n\t" \
  "ds_read_b32 v221, v221 offset:10016\n\t" \
  "ds_read_b32 v223, v223 offset:10016\n\t" \
  "v_mfma_f32_16x16x32_bf16 v[36:39], v[208:211], v[186:189], v[36:39]\n\t" \
  "v_lshlrev_b32_sdwa v224, 1, v156 dst_sel:DWORD dst_unused:UNUSED_PAD src0_sel:DWORD src1_sel:WORD_1\n\t" \
  "v_lshlrev_b32_sdwa v225, 1, v157 dst_sel:DWORD dst_unused:UNUSED_PAD src0_sel:DWORD src1_sel:WORD_1\n\t" \
  "ds_read_b64_tr_b16 v[208:209], v248 offset:0\n\t" \
  "v_mfma_f32_16x16x32_bf16 v[40:43], v[212:215], v[186:189], v[40:43]\n\t" \
  "v_fma_mix_f32 v228, |v162|, v228, v228 op_sel:[0,1,0] op_sel_hi:[0,1,1] clamp\n\t" \
  "v_lshlrev_b32_sdwa v226, 1, v158 dst_sel:DWORD dst_unused:UNUSED_PAD src0_sel:DWORD src1_sel:WORD_1\n\t" \
  "ds_read_b32 v222, v222 offset:10016\n\t" \
  "v_mfma_f32_16x16x32_bf16 v[88:91], v[212:215], v[202:205], v[88:91]\n\t" \
  "s_waitcnt lgkmcnt(5)\n\t" \
  "v_fma_mix_f32 v231, |v161|, v231, v231 op_sel:[0,1,0] op_sel_hi:[0,1,1] clamp\n\t" \
  "v_fma_mix_f32 v230, |v160|, v230, v230 op_sel:[0,1,0] op_sel_hi:[0,1,1] clamp\n\t" \
  "ds_read_b64_tr_b16 v[210:211], v248 offset:1024\n\t" \
  "v_mfma_f32_16x16x32_bf16 v[20:23], v[216:219], v[178:181], v[20:23]\n\t" \
  "v_or_b32 v170, v234, v235\n\t" \
  "v_or_b32 v171, v232, v233\n\t" \
  "ds_read_u16_d16_hi v225, v225 offset:10528\n\t" \
  "ds_read_b64_tr_b16 v[212:213], v248 offset:3072\n\t" \
  "v_mfma_f32_16x16x32_bf16 v[68:71], v[216:219], v[194:197], v[68:71]\n\t" \
  "v_fma_f32 v163, |v163|, v229, v163 mul:4\n\t" \
  "v_fma_f32 v160, |v160|, v230, v160 mul:4\n\t" \
  "s_setprio 2\n\t" \
  "ds_read_b64_tr_b16 v[214:215], v248 offset:4096\n\t" \
  "v_mfma_f32_16x16x32_bf16 v[44:47], v[216:219], v[186:189], v[44:47]\n\t" \
  "v_cvt_pknorm_i16_f32 v230, |v166|, |v167|\n\t" \
  "v_fma_f32 v162, |v162|, v228, v162 mul:4\n\t" \
  "ds_read_u16 v224, v224 offset:10528\n\t" \
  "s_waitcnt lgkmcnt(4)\n\t" \
  "v_mfma_f32_16x16x32_bf16 v[176:179], v[208:211], v[96:99], 0\n\t" \
  "v_fma_f32 v161, |v161|, v231, v161 mul:4\n\t" \
  "v_lshlrev_b32_sdwa v232, 1, v172 dst_sel:DWORD dst_unused:UNUSED_PAD src0_sel:DWORD src1_sel:WORD_1\n\t" \
  "v_mfma_f32_16x16x32_bf16 v[92:95], v[216:219], v[202:205], v[92:95]\n\t" \
  "v_cvt_pknorm_i16_f32 v228, |v164|, |v165|\n\t" \
  "v_lshlrev_b32_sdwa v231, 2, v230 dst_sel:DWORD dst_unused:UNUSED_PAD src0_sel:DWORD src1_sel:BYTE_3\n\t" \
  "ds_read_u16_d16_hi v227, v227 offset:10528\n\t" \
  "ds_read_b32 v231, v231 offset:10016\n\t" \
  "v_mfma_f32_16x16x32_bf16 v[200:203], v[208:211], v[108:111], 0\n\t" \
  "v_fma_mix_f32 v221, |v149|, v221, v221 op_sel:[0,1,0] op_sel_hi:[0,1,1] clamp\n\t" \
  "s_waitcnt lgkmcnt(3)\n\t" \
  "v_mfma_f32_16x16x32_bf16 v[200:203], v[212:215], v[124:127], v[200:203]\n\t" \
  "s_setprio 2\n\t" \
  "v_cvt_pk_bf16_f32 v163, v162, v163\n\t" \
  "v_cvt_pk_bf16_f32 v162, v160, v161\n\t" \
  "ds_read_b64_tr_b16 v[216:217], v248 offset:2048\n\t" \
  "ds_read_b64_tr_b16 v[218:219], v248 offset:5120\n\t" \
  "v_mfma_f32_16x16x32_bf16 v[192:195], v[208:211], v[104:107], 0\n\t" \
  "v_lshlrev_b32_sdwa v233, 1, v173 dst_sel:DWORD dst_unused:UNUSED_PAD src0_sel:DWORD src1_sel:WORD_1\n\t" \
  "v_lshlrev_b32_sdwa v230, 2, v230 dst_sel:DWORD dst_unused:UNUSED_PAD src0_sel:DWORD src1_sel:BYTE_1\n\t" \
  "v_mfma_f32_16x16x32_bf16 v[184:187], v[208:211], v[100:103], 0\n\t" \
  "v_lshlrev_b32_sdwa v229, 2, v228 dst_sel:DWORD dst_unused:UNUSED_PAD src0_sel:DWORD src1_sel:BYTE_3\n\t" \
  "v_lshlrev_b32_sdwa v228, 2, v228 dst_sel:DWORD dst_unused:UNUSED_PAD src0_sel:DWORD src1_sel:BYTE_1\n\t" \
  "ds_read_b32 v228, v228 offset:10016\n\t" \
  "ds_read_u16 v226, v226 offset:10528\n\t" \
  "v_mfma_f32_16x16x32_bf16 v[176:179], v[212:215], v[112:115], v[176:179]\n\t" \
  "v_lshlrev_b32_sdwa v235, 1, v175 dst_sel:DWORD dst_unused:UNUSED_PAD src0_sel:DWORD src1_sel:WORD_1\n\t" \
  "v_lshlrev_b32_sdwa v234, 1, v174 dst_sel:DWORD dst_unused:UNUSED_PAD src0_sel:DWORD src1_sel:WORD_1\n\t" \
  "ds_read_u16_d16_hi v233, v233 offset:10528\n\t" \
  "ds_read_b64_tr_b16 v[208:209], v248 offset:512\n\t" \
  "v_mfma_f32_16x16x32_bf16 v[192:195], v[212:215], v[120:123], v[192:195]\n\t" \
  "v_fma_mix_f32 v223, |v151|, v223, v223 op_sel:[0,1,0] op_sel_hi:[0,1,1] clamp\n\t" \
  "v_fma_mix_f32 v220, |v148|, v220, v220 op_sel:[0,1,0] op_sel_hi:[0,1,1] clamp\n\t" \
  "ds_read_b64_tr_b16 v[210:211], v248 offset:1536\n\t" \
  "v_mfma_f32_16x16x32_bf16 v[184:187], v[212:215], v[116:119], v[184:187]\n\t" \
  "v_fma_mix_f32 v222, |v150|, v222, v222 op_sel:[0,1,0] op_sel_hi:[0,1,1] clamp\n\t" \
  "v_fma_f32 v150, |v150|, v222, v150 mul:4\n\t" \
  "s_waitcnt lgkmcnt(5)\n\t" \
  "ds_read_b64_tr_b16 v[212:213], v248 offset:3584\n\t" \
  "ds_read_b32 v229, v229 offset:10016\n\t" \
  "v_mfma_f32_16x16x32_bf16 v[176:179], v[216:219], v[128:131], v[176:179]\n\t" \
  "v_or_b32 v156, v224, v225\n\t" \
  "v_fma_f32 v151, |v151|, v223, v151 mul:4\n\t" \
  "v_mfma_f32_16x16x32_bf16 v[192:195], v[216:219], v[136:139], v[192:195]\n\t" \
  "s_waitcnt lgkmcnt(5)\n\t" \
  "s_setprio 0\n\t" \
  "v_or_b32 v157, v226, v227\n\t" \
  "ds_read_b32 v230, v230 offset:10016\n\t" \
  "ds_read_b64_tr_b16 v[214:215], v248 offset:4608\n\t" \
  "v_mfma_f32_16x16x32_bf16 v[184:187], v[216:219], v[132:135], v[184:187]\n\t" \
  "v_fma_f32 v149, |v149|, v221, v149 mul:4\n\t" \
  "v_fma_f32 v148, |v148|, v220, v148 mul:4\n\t" \
  "ds_read_u16 v232, v232 offset:10528\n\t" \
  "v_mfma_f32_16x16x32_bf16 v[200:203], v[216:219], v[140:143], v[200:203]\n\t" \
  "s_waitcnt lgkmcnt(5)\n\t" \
  "v_cvt_pk_bf16_f32 v148, v148, v149\n\t" \
  "v_cvt_pk_bf16_f32 v149, v150, v151\n\t" \
  "ds_read_b64_tr_b16 v[216:217], v248 offset:2560\n\t" \
  "s_waitcnt vmcnt(1)\n\t" \
  "v_mfma_f32_16x16x32_bf16 v[180:183], v[208:211], v[96:99], 0\n\t" \
  "v_cvt_pknorm_i16_f32 v220, |v178|, |v179|\n\t" \
  "v_cvt_pknorm_i16_f32 v222, |v176|, |v177|\n\t" \
  "ds_read_b64_tr_b16 v[218:219], v248 offset:5632\n\t" \
  "v_mfma_f32_16x16x32_bf16 v[188:191], v[208:211], v[100:103], 0\n\t" \
  "v_lshlrev_b32_sdwa v221, 2, v220 dst_sel:DWORD dst_unused:UNUSED_PAD src0_sel:DWORD src1_sel:BYTE_3\n\t" \
  "v_lshlrev_b32_sdwa v220, 2, v220 dst_sel:DWORD dst_unused:UNUSED_PAD src0_sel:DWORD src1_sel:BYTE_1\n\t" \
  "s_mov_b64 exec, 3\n\t" \
  "ds_add_rtn_u32 v246, v251, v250 offset:47224\n\t" \
  "v_mfma_f32_16x16x32_bf16 v[196:199], v[208:211], v[104:107], 0\n\t" \
  "s_mov_b64 exec, -1\n\t" \
  "v_lshlrev_b32_sdwa v223, 2, v222 dst_sel:DWORD dst_unused:UNUSED_PAD src0_sel:DWORD src1_sel:BYTE_3\n\t" \
  "v_lshlrev_b32_sdwa v222, 2, v222 dst_sel:DWORD dst_unused:UNUSED_PAD src0_sel:DWORD src1_sel:BYTE_1\n\t" \
  "ds_read_u16 v234, v234 offset:10528\n\t" \
  "ds_read_u16_d16_hi v235, v235 offset:10528\n\t" \
  "v_mfma_f32_16x16x32_bf16 v[204:207], v[208:211], v[108:111], 0\n\t" \
  "s_waitcnt lgkmcnt(2)\n\t" \
  "v_lshlrev_b32_sdwa v225, 1, v187 dst_sel:DWORD dst_unused:UNUSED_PAD src0_sel:DWORD src1_sel:WORD_1\n\t" \
  "v_readfirstlane_b32 s4, v246\n\t" \
  "ds_read_b32 v221, v221 offset:10016\n\t" \
  "v_mfma_f32_16x16x32_bf16 v[180:183], v[212:215], v[112:115], v[180:183]\n\t" \
  "s_cmpk_lt_u32 s4, 3072\n\t" \
  "v_fma_mix_f32 v230, |v166|, v230, v230 op_sel:[0,1,0] op_sel_hi:[0,1,1] clamp\n\t" \
  "v_lshlrev_b32_sdwa v224, 1, v186 dst_sel:DWORD dst_unused:UNUSED_PAD src0_sel:DWORD src1_sel:WORD_1\n\t" \
  "s_cbranch_scc1 .Lcnx_w0_12\n\t" \
  ".Lcnx_g0_12:\n\t" \
  "ds_read_b64_tr_b16 v[208:209], v249 offset:6152\n\t" \
  "v_mfma_f32_16x16x32_bf16 v[196:199], v[212:215], v[120:123], v[196:199]\n\t" \
  "v_fma_mix_f32 v228, |v164|, v228, v228 op_sel:[0,1,0] op_sel_hi:[0,1,1] clamp\n\t" \
  "v_fma_mix_f32 v229, |v165|, v229, v229 op_sel:[0,1,0] op_sel_hi:[0,1,1] clamp\n\t" \
  "ds_read_b32 v220, v220 offset:10016\n\t" \
  "ds_read_b64_tr_b16 v[210:211], v249 offset:7688\n\t" \
  "v_mfma_f32_16x16x32_bf16 v[204:207], v[212:215], v[124:127], v[204:207]\n\t" \
  "ds_read_b32 v223, v223 offset:10016\n\t" \
  "v_lshlrev_b32_sdwa v226, 1, v184 dst_sel:DWORD dst_unused:UNUSED_PAD src0_sel:DWORD src1_sel:WORD_1\n\t" \
  "v_lshlrev_b32_sdwa v227, 1, v185 dst_sel:DWORD dst_unused:UNUSED_PAD src0_sel:DWORD src1_sel:WORD_1\n\t" \
  "v_fma_mix_f32 v231, |v167|, v231, v231 op_sel:[0,1,0] op_sel_hi:[0,1,1] clamp\n\t" \
  "v_mfma_f32_16x16x32_bf16 v[188:191], v[212:215], v[116:119], v[188:191]\n\t" \
  "s_waitcnt lgkmcnt(5)\n\t" \
  "ds_read_b64_tr_b16 v[212:213], v249 offset:6160\n\t" \
  "ds_read_b32 v222, v222 offset:10016\n\t" \
  "v_mfma_f32_16x16x32_bf16 v[180:183], v[216:219], v[128:131], v[180:183]\n\t" \
  "v_or_b32 v172, v232, v233\n\t" \
  "v_or_b32 v173, v234, v235\n\t" \
  "ds_read_u16 v224, v224 offset:10528\n\t" \
  "ds_read_b64_tr_b16 v[214:215], v249 offset:7696\n\t" \
  "v_mfma_f32_16x16x32_bf16 v[188:191], v[216:219], v[132:135], v[188:191]\n\t" \
  "v_fma_f32 v165, |v165|, v229, v165 mul:4\n\t" \
  "v_fma_f32 v164, |v164|, v228, v164 mul:4\n\t" \
  "ds_read_u16_d16_hi v225, v225 offset:10528\n\t" \
  "ds_read_u16_d16_hi v227, v227 offset:10528\n\t" \
  "v_mfma_f32_16x16x32_bf16 v[196:199], v[216:219], v[136:139], v[196:199]\n\t" \
  "v_fma_f32 v167, |v167|, v231, v167 mul:4\n\t" \
  "v_cvt_pk_bf16_f32 v164, v164, v165\n\t" \
  "v_fma_f32 v166, |v166|, v230, v166 mul:4\n\t" \
  "v_cvt_pk_bf16_f32 v165, v166, v167\n\t" \
  "v_mfma_f32_16x16x32_bf16 v[204:207], v[216:219], v[140:143], v[204:207]\n\t" \
  "s_waitcnt lgkmcnt(7)\n\t" \
  "ds_read_b64_tr_b16 v[216:217], v249 offset:6216\n\t" \
  "ds_read_b64_tr_b16 v[218:219], v249 offset:7752\n\t" \
  "v_mfma_f32_16x16x32_bf16 v[0:3], v[208:211], v[146:149], v[0:3]\n\t" \
  "v_cvt_pknorm_i16_f32 v230, |v192|, |v193|\n\t" \
  "v_lshlrev_b32_sdwa v231, 2, v230 dst_sel:DWORD dst_unused:UNUSED_PAD src0_sel:DWORD src1_sel:BYTE_3\n\t" \
  "v_mfma_f32_16x16x32_bf16 v[24:27], v[208:211], v[154:157], v[24:27]\n\t" \
  "v_lshlrev_b32_sdwa v234, 1, v200 dst_sel:DWORD dst_unused:UNUSED_PAD src0_sel:DWORD src1_sel:WORD_1\n\t" \
  "v_cvt_pknorm_i16_f32 v228, |v194|, |v195|\n\t" \
  "v_lshlrev_b32_sdwa v229, 2, v228 dst_sel:DWORD dst_unused:UNUSED_PAD src0_sel:DWORD src1_sel:BYTE_3\n\t" \
  "v_lshlrev_b32_sdwa v233, 1, v203 dst_sel:DWORD dst_unused:UNUSED_PAD src0_sel:DWORD src1_sel:WORD_1\n\t" \
  "v_mfma_f32_16x16x32_bf16 v[48:51], v[208:211], v[162:165], v[48:51]\n\t" \
  "ds_read_b32 v229, v229 offset:10016\n\t" \
  "ds_read_u16 v226, v226 offset:10528\n\t" \
  "v_lshlrev_b32_sdwa v235, 1, v201 dst_sel:DWORD dst_unused:UNUSED_PAD src0_sel:DWORD src1_sel:WORD_1\n\t" \
  "v_fma_mix_f32 v220, |v178|, v220, v220 op_sel:[0,1,0] op_sel_hi:[0,1,1] clamp\n\t" \
  "v_lshlrev_b32_sdwa v228, 2, v228 dst_sel:DWORD dst_unused:UNUSED_PAD src0_sel:DWORD src1_sel:BYTE_1\n\t" \
  "v_lshlrev_b32_sdwa v232, 1, v202 dst_sel:DWORD dst_unused:UNUSED_PAD src0_sel:DWORD src1_sel:WORD_1\n\t" \
  "v_fma_mix_f32 v221, |v179|, v221, v221 op_sel:[0,1,0] op_sel_hi:[0,1,1] clamp\n\t" \
  "v_fma_f32 v178, |v178|, v220, v178 mul:4\n\t" \
  "v_mfma_f32_16x16x32_bf16 v[72:75], v[208:211], v[170:173], v[72:75]\n\t" \
  "s_waitcnt lgkmcnt(8)\n\t" \
  "ds_read_b64_tr_b16 v[208:209], v249 offset:6224\n\t" \
  "ds_read_b32 v228, v228 offset:10016\n\t" \
  "s_waitcnt lgkmcnt(7)\n\t" \
  "v_lshlrev_b32_sdwa v230, 2, v230 dst_sel:DWORD dst_unused:UNUSED_PAD src0_sel:DWORD src1_sel:BYTE_1\n\t" \
  "v_fma_mix_f32 v223, |v177|, v223, v223 op_sel:[0,1,0] op_sel_hi:[0,1,1] clamp\n\t" \
  "v_or_b32 v187, v224, v225\n\t" \
  "v_fma_mix_f32 v222, |v176|, v222, v222 op_sel:[0,1,0] op_sel_hi:[0,1,1] clamp\n\t" \
  "v_fma_f32 v179, |v179|, v221, v179 mul:4\n\t" \
  "v_mfma_f32_16x16x32_bf16 v[4:7], v[212:215], v[146:149], v[4:7]\n\t" \
  "s_waitcnt lgkmcnt(2)\n\t" \
  "ds_read_b64_tr_b16 v[210:211], v249 offset:7760\n\t" \
  "v_or_b32 v186, v226, v227\n\t" \
  "v_mfma_f32_16x16x32_bf16 v[28:31], v[212:215], v[154:157], v[28:31]\n\t" \
  "s_add_u32 s27, s27, s26\n\t" \
  "ds_read_b32 v231, v231 offset:10016\n\t" \
  "ds_read_u16_d16_hi v233, v233 offset:10528\n\t" \
  "v_fma_f32 v176, |v176|, v222, v176 mul:4\n\t" \
  "v_cvt_pknorm_i16_f32 v222, |v182|, |v183|\n\t" \
  "s_mov_b64 exec, s[30:31]\n\t" \
  "buffer_load_dword v237, v254, s[8:11], s27 offen\n\t" \
  "s_mov_b64 exec, -1\n\t" \
  "v_mfma_f32_16x16x32_bf16 v[52:55], v[212:215], v[162:165], v[52:55]\n\t" \
  "ds_read_b32 v230, v230 offset:10016\n\t" \
  "v_lshlrev_b32_sdwa v227, 1, v191 dst_sel:DWORD dst_unused:UNUSED_PAD src0_sel:DWORD src1_sel:WORD_1\n\t" \
  "v_fma_f32 v177, |v177|, v223, v177 mul:4\n\t" \
  "v_cvt_pk_bf16_f32 v179, v178, v179\n\t" \
  "v_lshlrev_b32_sdwa v223, 2, v222 dst_sel:DWORD dst_unused:UNUSED_PAD src0_sel:DWORD src1_sel:BYTE_3\n\t" \
  "v_fma_mix_f32 v229, |v195|, v229, v229 op_sel:[0,1,0] op_sel_hi:[0,1,1] clamp\n\t" \
  "v_lshlrev_b32_sdwa v222, 2, v222 dst_sel:DWORD dst_unused:UNUSED_PAD src0_sel:DWORD src1_sel:BYTE_1\n\t" \
  "v_cvt_pknorm_i16_f32 v220, |v180|, |v181|\n\t" \
  "v_lshlrev_b32_sdwa v224, 1, v188 dst_sel:DWORD dst_unused:UNUSED_PAD src0_sel:DWORD src1_sel:WORD_1\n\t" \
  "v_mfma_f32_16x16x32_bf16 v[76:79], v[212:215], v[170:173], v[76:79]\n\t" \
  "ds_read_b64_tr_b16 v[214:215], v249 offset:7816\n\t" \
  "ds_read_u16 v232, v232 offset:10528\n\t" \
  "v_cvt_pk_bf16_f32 v178, v176, v177\n\t" \
  "v_lshlrev_b32_sdwa v226, 1, v190 dst_sel:DWORD dst_unused:UNUSED_PAD src0_sel:DWORD src1_sel:WORD_1\n\t" \
  "v_mfma_f32_16x16x32_bf16 v[32:35], v[216:219], v[154:157], v[32:35]\n\t" \
  "s_setprio 1\n\t" \
  "s_waitcnt lgkmcnt(2)\n\t" \
  "v_fma_mix_f32 v228, |v194|, v228, v228 op_sel:[0,1,0] op_sel_hi:[0,1,1] clamp\n\t" \
  "v_lshlrev_b32_sdwa v221, 2, v220 dst_sel:DWORD dst_unused:UNUSED_PAD src0_sel:DWORD src1_sel:BYTE_3\n\t" \
  "v_fma_mix_f32 v231, |v193|, v231, v231 op_sel:[0,1,0] op_sel_hi:[0,1,1] clamp\n\t" \
  "v_lshlrev_b32_sdwa v225, 1, v189 dst_sel:DWORD dst_unused:UNUSED_PAD src0_sel:DWORD src1_sel:WORD_1\n\t" \
  "v_mfma_f32_16x16x32_bf16 v[56:59], v[216:219], v[162:165], v[56:59]\n\t" \
  "ds_read_b32 v222, v222 offset:10016\n\t" \
  "ds_read_u16 v234, v234 offset:10528\n\t" \
  "ds_read_b64_tr_b16 v[212:213], v249 offset:6280\n\t" \
  "v_fma_f32 v194, |v194|, v228, v194 mul:4\n\t" \
  "v_fma_mix_f32 v230, |v192|, v230, v230 op_sel:[0,1,0] op_sel_hi:[0,1,1] clamp\n\t" \
  "v_fma_f32 v192, |v192|, v230, v192 mul:4\n\t" \
  "v_fma_f32 v195, |v195|, v229, v195 mul:4\n\t" \
  "v_lshlrev_b32_sdwa v220, 2, v220 dst_sel:DWORD dst_unused:UNUSED_PAD src0_sel:DWORD src1_sel:BYTE_1\n\t" \
  "v_mfma_f32_16x16x32_bf16 v[12:15], v[208:211], v[146:149], v[12:15]\n\t" \
  "ds_read_b32 v221, v221 offset:10016\n\t" \
  "ds_read_u16_d16_hi v225, v225 offset:10528\n\t" \
  "ds_read_u16_d16_hi v235, v235 offset:10528\n\t" \
  "ds_read_b32 v220, v220 offset:10016\n\t" \
  "v_mfma_f32_16x16x32_bf16 v[8:11], v[216:219], v[146:149], v[8:11]\n\t" \
  "s_waitcnt lgkmcnt(7)\n\t" \
  "v_or_b32 v203, v232, v233\n\t" \
  "v_cvt_pk_bf16_f32 v195, v194, v195\n\t" \
  "v_cvt_pknorm_i16_f32 v230, |v198|, |v199|\n\t" \
  "v_fma_f32 v193, |v193|, v231, v193 mul:4\n\t" \
  "v_cvt_pk_bf16_f32 v194, v192, v193\n\t" \
  "v_mfma_f32_16x16x32_bf16 v[80:83], v[216:219], v[170:173], v[80:83]\n\t" \
  "ds_read_b64_tr_b16 v[218:219], v249 offset:7824\n\t" \
  "ds_read_b64_tr_b16 v[216:217], v249 offset:6288\n\t" \
  "s_mov_b64 exec, 3\n\t" \
  "ds_add_rtn_u32 v246, v251, v250 offset:47232\n\t" \
  "s_mov_b64 exec, -1\n\t" \
  "v_lshlrev_b32_sdwa v232, 1, v204 dst_sel:DWORD dst_unused:UNUSED_PAD src0_sel:DWORD src1_sel:WORD_1\n\t" \
  "v_cvt_pknorm_i16_f32 v228, |v196|, |v197|\n\t" \
  "v_lshlrev_b32_sdwa v229, 2, v228 dst_sel:DWORD dst_unused:UNUSED_PAD src0_sel:DWORD src1_sel:BYTE_3\n\t" \
  "v_lshlrev_b32_sdwa v228, 2, v228 dst_sel:DWORD dst_unused:UNUSED_PAD src0_sel:DWORD src1_sel:BYTE_1\n\t" \
  "v_mfma_f32_16x16x32_bf16 v[36:39], v[208:211], v[154:157], v[36:39]\n\t" \
  "ds_read_u16 v224, v224 offset:10528\n\t" \
  "ds_read_b32 v223, v223 offset:10016\n\t" \
  "v_mfma_f32_16x16x32_bf16 v[60:63], v[208:211], v[162:165], v[60:63]\n\t" \
  "ds_read_u16_d16_hi v227, v227 offset:10528\n\t" \
  "ds_read_b32 v229, v229 offset:10016\n\t" \
  "s_waitcnt lgkmcnt(8)\n\t" \
  "v_lshlrev_b32_sdwa v231, 2, v230 dst_sel:DWORD dst_unused:UNUSED_PAD src0_sel:DWORD src1_sel:BYTE_3\n\t" \
  "v_or_b32 v202, v234, v235\n\t" \
  "v_lshlrev_b32_sdwa v230, 2, v230 dst_sel:DWORD dst_unused:UNUSED_PAD src0_sel:DWORD src1_sel:BYTE_1\n\t" \
  "v_mfma_f32_16x16x32_bf16 v[84:87], v[208:211], v[170:173], v[84:87]\n\t" \
  "s_waitcnt lgkmcnt(4)\n\t" \
  "ds_read_b32 v228, v228 offset:10016\n\t" \
  "v_lshlrev_b32_sdwa v233, 1, v205 dst_sel:DWORD dst_unused:UNUSED_PAD src0_sel:DWORD src1_sel:WORD_1\n\t" \
  "v_readfirstlane_b32 s4, v246\n\t" \
  "v_mfma_f32_16x16x32_bf16 v[40:43], v[212:215], v[154:157], v[40:43]\n\t" \
  "ds_read_u16 v226, v226 offset:10528\n\t" \
  "v_lshlrev_b32_sdwa v234, 1, v206 dst_sel:DWORD dst_unused:UNUSED_PAD src0_sel:DWORD src1_sel:WORD_1\n\t" \
  "v_mfma_f32_16x16x32_bf16 v[16:19], v[212:215], v[146:149], v[16:19]\n\t" \
  "s_cmpk_lt_u32 s4, 3072\n\t" \
  "ds_read_b32 v231, v231 offset:10016\n\t" \
  "ds_read_b32 v230, v230 offset:10016\n\t" \
  "v_mfma_f32_16x16x32_bf16 v[68:71], v[216:219], v[162:165], v[68:71]\n\t" \
  "s_cbranch_scc1 .Lcnx_w0_13\n\t" \
  ".Lcnx_g0_13:\n\t" \
  "ds_read_b64_tr_b16 v[210:211], v249 offset:13840\n\t" \
  "ds_read_b64_tr_b16 v[208:209], v249 offset:12304\n\t" \
  "v_fma_mix_f32 v222, |v182|, v222, v222 op_sel:[0,1,0] op_sel_hi:[0,1,1] clamp\n\t" \
  "v_fma_f32 v182, |v182|, v222, v182 mul:4\n\t" \
  "v_mfma_f32_16x16x32_bf16 v[64:67], v[212:215], v[162:165], v[64:67]\n\t" \
  "s_waitcnt lgkmcnt(9)\n\t" \
  "v_or_b32 v188, v224, v225\n\t" \
  "v_lshlrev_b32_sdwa v235, 1, v207 dst_sel:DWORD dst_unused:UNUSED_PAD src0_sel:DWORD src1_sel:WORD_1\n\t" \
  "v_mfma_f32_16x16x32_bf16 v[88:91], v[212:215], v[170:173], v[88:91]\n\t" \
  "s_waitcnt lgkmcnt(4)\n\t" \
  "ds_read_b64_tr_b16 v[212:213], v249 offset:12312\n\t" \
  "v_or_b32 v189, v226, v227\n\t" \
  "v_mfma_f32_16x16x32_bf16 v[20:23], v[216:219], v[146:149], v[20:23]\n\t" \
  "s_waitcnt lgkmcnt(3)\n\t" \
  "s_setprio 1\n\t" \
  "ds_read_b64_tr_b16 v[214:215], v249 offset:13848\n\t" \
  "v_fma_mix_f32 v220, |v180|, v220, v220 op_sel:[0,1,0] op_sel_hi:[0,1,1] clamp\n\t" \
  "v_mfma_f32_16x16x32_bf16 v[44:47], v[216:219], v[154:157], v[44:47]\n\t" \
  "ds_read_u16_d16_hi v233, v233 offset:10528\n\t" \
  "ds_read_u16 v232, v232 offset:10528\n\t" \
  "v_fma_mix_f32 v223, |v183|, v223, v223 op_sel:[0,1,0] op_sel_hi:[0,1,1] clamp\n\t" \
  "v_fma_f32 v183, |v183|, v223, v183 mul:4\n\t" \
  "v_fma_mix_f32 v230, |v198|, v230, v230 op_sel:[0,1,0] op_sel_hi:[0,1,1] clamp\n\t" \
  "v_fma_mix_f32 v228, |v196|, v228, v228 op_sel:[0,1,0] op_sel_hi:[0,1,1] clamp\n\t" \
  "v_fma_f32 v196, |v196|, v228, v196 mul:4\n\t" \
  "v_mfma_f32_32x32x16_bf16 v[144:159], v[242:245], v[240:243], 0\n\t" \
  "ds_read_u16 v234, v234 offset:10528\n\t" \
  "ds_read_u16_d16_hi v235, v235 offset:10528\n\t" \
  "v_fma_f32 v180, |v180|, v220, v180 mul:4\n\t" \
  "v_fma_mix_f32 v231, |v199|, v231, v231 op_sel:[0,1,0] op_sel_hi:[0,1,1] clamp\n\t" \
  "v_fma_mix_f32 v221, |v181|, v221, v221 op_sel:[0,1,0] op_sel_hi:[0,1,1] clamp\n\t" \
  "v_fma_f32 v181, |v181|, v221, v181 mul:4\n\t" \
  "v_fma_mix_f32 v229, |v197|, v229, v229 op_sel:[0,1,0] op_sel_hi:[0,1,1] clamp\n\t" \
  "v_fma_f32 v197, |v197|, v229, v197 mul:4\n\t" \
  "v_mfma_f32_16x16x32_bf16 v[92:95], v[216:219], v[170:173], v[92:95]\n\t" \
  "s_waitcnt lgkmcnt(2)\n\t" \
  "ds_read_b64_tr_b16 v[216:217], v249 offset:12368\n\t" \
  "v_fma_f32 v198, |v198|, v230, v198 mul:4\n\t" \
  "v_mfma_f32_32x32x16_bf16 v[160:175], v[242:245], v[240:243], 0\n\t" \
  "v_or_b32 v204, v232, v233\n\t" \
  "v_cvt_pk_bf16_f32 v180, v180, v181\n\t" \
  "v_cvt_pk_bf16_f32 v181, v182, v183\n\t" \
  "v_fma_f32 v199, |v199|, v231, v199 mul:4\n\t" \
  "v_cvt_pk_bf16_f32 v196, v196, v197\n\t" \
  "v_cvt_pk_bf16_f32 v197, v198, v199\n\t" \
  "v_mfma_f32_16x16x32_bf16 v[0:3], v[208:211], v[178:181], v[0:3]\n\t" \
  "s_waitcnt lgkmcnt(1)\n\t" \
  "ds_read_b64_tr_b16 v[218:219], v249 offset:13904\n\t" \
  "ds_read2_b32 v[114:115], v247 offset0:192 offset1:198\n\t" \
  "v_or_b32 v205, v234, v235\n\t" \
  "v_mfma_f32_16x16x32_bf16 v[48:51], v[208:211], v[194:197], v[48:51]\n\t" \
  "ds_read2_b32 v[98:99], v247 offset0:0 offset1:6\n\t" \
  "ds_read2_b32 v[102:103], v247 offset0:12 offset1:18\n\t" \
  "v_mfma_f32_16x16x32_bf16 v[24:27], v[208:211], v[186:189], v[24:27]\n\t" \
  "s_add_u32 s27, s27, s26\n\t" \
  "ds_read2_b32 v[118:119], v247 offset0:204 offset1:210\n\t" \
  "v_add_u32 v232, s56, v247\n\t" \
  "v_mfma_f32_16x16x32_bf16 v[28:31], v[212:215], v[186:189], v[28:31]\n\t" \
  "s_mov_b64 exec, s[30:31]\n\t" \
  "buffer_load_dword v237, v254, s[8:11], s27 offen\n\t" \
  "v_mfma_f32_16x16x32_bf16 v[76:79], v[212:215], v[202:205], v[76:79]\n\t" \
  "s_mov_b64 exec, -1\n\t" \
  "v_bfe_u32 v234, v251, 3, 2\n\t" \
  "v_mfma_f32_16x16x32_bf16 v[72:75], v[208:211], v[202:205], v[72:75]\n\t" \
  "ds_read_b64_tr_b16 v[210:211], v249 offset:13912\n\t" \
  "ds_read_b64_tr_b16 v[208:209], v249 offset:12376\n\t" \
  "v_mfma_f32_16x16x32_bf16 v[52:55], v[212:215], v[194:197], v[52:55]\n\t" \
  "ds_read2_b32 v[106:107], v247 offset0:24 offset1:30\n\t" \
  "ds_read2_b32 v[130:131], v232 offset0:0 offset1:6\n\t" \
  "v_mfma_f32_16x16x32_bf16 v[4:7], v[212:215], v[178:181], v[4:7]\n\t" \
  "s_waitcnt lgkmcnt(8)\n\t" \
  "ds_read_b64_tr_b16 v[212:213], v249 offset:12432\n\t" \
  "v_cvt_pk_bf16_f32 v48, v48, v49\n\t" \
  "v_mfma_f32_16x16x32_bf16 v[80:83], v[216:219], v[202:205], v[80:83]\n\t" \
  "ds_read2_b32 v[138:139], v232 offset0:24 offset1:30\n\t" \
  "v_cvt_pk_bf16_f32 v72, v72, v73\n\t" \
  "v_mfma_f32_16x16x32_bf16 v[8:11], v[216:219], v[178:181], v[8:11]\n\t" \
  "ds_read_b64_tr_b16 v[214:215], v249 offset:13968\n\t" \
  "ds_read2_b32 v[110:111], v247 offset0:36 offset1:42\n\t" \
  "v_mfma_f32_16x16x32_bf16 v[56:59], v[216:219], v[194:197], v[56:59]\n\t" \
  "s_waitcnt lgkmcnt(6)\n\t" \
  "v_cvt_pk_bf16_f32 v73, v74, v75\n\t" \
  "v_cvt_pk_bf16_f32 v74, v76, v77\n\t" \
  "v_cvt_pk_bf16_f32 v75, v78, v79\n\t" \
  "v_mfma_f32_16x16x32_bf16 v[12:15], v[208:211], v[178:181], v[12:15]\n\t" \
  "buffer_store_dwordx4 v[72:75], v250, s[20:23], s13 offen offset:0 sc1\n\t" \
  "v_cvt_pk_bf16_f32 v49, v50, v51\n\t" \
  "v_cvt_pk_bf16_f32 v50, v52, v53\n\t" \
  "v_cvt_pk_bf16_f32 v80, v80, v81\n\t" \
  "v_mfma_f32_16x16x32_bf16 v[36:39], v[208:211], v[186:189], v[36:39]\n\t" \
  "s_add_u32 s63, s28, s65\n\t" \
  "ds_read2_b32 v[134:135], v232 offset0:12 offset1:18\n\t" \
  "v_mul_u32_u24 v234, 0x5555, v234\n\t" \
  "v_mfma_f32_16x16x32_bf16 v[32:35], v[216:219], v[186:189], v[32:35]\n\t" \
  "ds_read_b64_tr_b16 v[218:219], v249 offset:13976\n\t" \
  "v_mfma_f32_16x16x32_bf16 v[84:87], v[208:211], v[202:205], v[84:87]\n\t" \
  "s_waitcnt lgkmcnt(3)\n\t" \
  "ds_read_b64_tr_b16 v[216:217], v249 offset:12440\n\t" \
  "v_mfma_f32_16x16x32_bf16 v[60:63], v[208:211], v[194:197], v[60:63]\n\t" \
  "s_add_u32 s62, s29, s65\n\t" \
  "ds_read2_b32 v[126:127], v247 offset0:228 offset1:234\n\t" \
  "v_cvt_pk_bf16_f32 v76, v24, v25\n\t" \
  "v_lshlrev_b32 v198, 1, v251\n\t" \
  "v_cvt_pk_bf16_f32 v81, v82, v83\n\t" \
  "v_cvt_pk_bf16_f32 v83, v86, v87\n\t" \
  "v_mfma_f32_16x16x32_bf16 v[16:19], v[212:215], v[178:181], v[16:19]\n\t" \
  "v_cvt_pk_bf16_f32 v87, v38, v39\n\t" \
  "v_cvt_pk_bf16_f32 v82, v84, v85\n\t" \
  "v_mfma_f32_16x16x32_bf16 v[88:91], v[212:215], v[202:205], v[88:91]\n\t" \
  "buffer_store_dwordx4 v[80:83], v250, s[20:23], s13 offen offset:64 sc1\n\t" \
  "v_cvt_pk_bf16_f32 v56, v56, v57\n\t" \
  "v_cvt_pk_bf16_f32 v51, v54, v55\n\t" \
  "s_setprio 0\n\t" \
  "v_cvt_pk_bf16_f32 v57, v58, v59\n\t" \
  "v_cvt_pk_bf16_f32 v58, v60, v61\n\t" \
  "v_cvt_pk_bf16_f32 v59, v62, v63\n\t" \
  "v_mfma_f32_16x16x32_bf16 v[40:43], v[212:215], v[186:189], v[40:43]\n\t" \
  "v_and_b32 v97, 0xffff0000, v98\n\t" \
  "s_waitcnt lgkmcnt(1)\n\t" \
  "v_mfma_f32_16x16x32_bf16 v[64:67], v[212:215], v[194:197], v[64:67]\n\t" \
  "ds_read_b64 v[206:207], v253 offset:1200\n\t" \
  "v_cvt_pk_bf16_f32 v88, v88, v89\n\t" \
  "v_mfma_f32_16x16x32_bf16 v[68:71], v[216:219], v[194:197], v[68:71]\n\t" \
  "v_cvt_pk_bf16_f32 v54, v4, v5\n\t" \
  "v_cvt_pk_bf16_f32 v61, v10, v11\n\t" \
  "v_cvt_pk_bf16_f32 v89, v90, v91\n\t" \
  "v_cvt_pk_bf16_f32 v84, v32, v33\n\t" \
  "v_mfma_f32_16x16x32_bf16 v[92:95], v[216:219], v[202:205], v[92:95]\n\t" \
  "ds_read2_b32 v[142:143], v232 offset0:36 offset1:42\n\t" \
  "global_load_dwordx4 v[192:195], v198, s[66:67]\n\t" \
  "ds_read_b64 v[202:203], v253 offset:400\n\t" \
  "v_mfma_f32_16x16x32_bf16 v[44:47], v[216:219], v[186:189], v[44:47]\n\t" \
  "s_nop 3\n\t" \
  "v_cvt_pk_bf16_f32 v91, v94, v95\n\t" \
  "v_cvt_pk_bf16_f32 v64, v64, v65\n\t" \
  "v_cvt_pk_bf16_f32 v90, v92, v93\n\t" \
  "buffer_store_dwordx4 v[88:91], v250, s[20:23], s13 offen offset:128 sc1\n\t" \
  "v_cvt_pk_bf16_f32 v65, v66, v67\n\t" \
  "v_cvt_pk_bf16_f32 v53, v2, v3\n\t" \
  "v_and_b32 v112, 0xffff, v114\n\t" \
  "v_cvt_pk_bf16_f32 v67, v70, v71\n\t" \
  "v_cvt_pk_bf16_f32 v62, v12, v13\n\t" \
  "v_cvt_pk_bf16_f32 v95, v46, v47\n\t" \
  "v_and_b32 v129, 0xffff0000, v130\n\t" \
  "s_add_u32 s14, s12, s65\n\t" \
  "v_cvt_pk_bf16_f32 v85, v34, v35\n\t" \
  "v_mfma_f32_16x16x32_bf16 v[20:23], v[216:219], v[178:181], v[20:23]\n\t" \
  "ds_read_b64 v[220:221], v253 offset:816\n\t" \
  "ds_read_b64 v[208:209], v253 offset:8\n\t" \
  "ds_read_b64 v[214:215], v253 offset:1208\n\t" \
  "ds_read2_b32 v[122:123], v247 offset0:216 offset1:222\n\t" \
  "v_cvt_pk_bf16_f32 v78, v28, v29\n\t" \
  "v_and_b32 v113, 0xffff0000, v114\n\t" \
  "v_and_b32 v114, 0xffff, v115\n\t" \
  "v_and_b32 v128, 0xffff, v130\n\t" \
  "v_and_b32 v96, 0xffff, v98\n\t" \
  "v_cvt_pk_bf16_f32 v93, v42, v43\n\t" \
  "v_and_b32 v98, 0xffff, v99\n\t" \
  "v_cvt_pk_bf16_f32 v60, v8, v9\n\t" \
  "ds_read_b64 v[218:219], v253 offset:416\n\t" \
  "ds_read_b64 v[200:201], v253 offset:0\n\t" \
  "v_cvt_pk_bf16_f32 v52, v0, v1\n\t" \
  "v_cvt_pk_bf16_f32 v66, v68, v69\n\t" \
  "v_cvt_pk_bf16_f32 v63, v14, v15\n\t" \
  "ds_read_b64 v[222:223], v253 offset:1216\n\t" \
  "ds_read_b64 v[212:213], v253 offset:808\n\t" \
  "v_cvt_pk_bf16_f32 v55, v6, v7\n\t" \
  "v_cvt_pk_bf16_f32 v71, v22, v23\n\t" \
  "v_and_b32 v130, 0xffff, v131\n\t" \
  "ds_read_b64 v[204:205], v253 offset:800\n\t" \
  "v_cvt_pk_bf16_f32 v86, v36, v37\n\t" \
  "v_mfma_f32_32x32x16_bf16 v[176:191], v[242:245], v[240:243], 0\n\t" \
  "ds_read_b64 v[216:217], v253 offset:16\n\t" \
  "ds_read_b64 v[210:211], v253 offset:408\n\t" \
  "v_and_b32 v100, 0xffff, v102\n\t" \
  "v_and_b32 v99, 0xffff0000, v99\n\t" \
  "s_add_u32 s15, s13, s65\n\t" \
  "v_and_b32 v115, 0xffff0000, v115\n\t" \
  "v_add_u32 v232, 56880, v253\n\t" \
  "v_and_b32 v131, 0xffff0000, v131\n\t" \
  "s_waitcnt lgkmcnt(2)\n\t" \
  "v_add_u32 v233, s39, v251\n\t" \
  "s_add_u32 s24, s24, s26\n\t" \
  "v_cvt_pk_bf16_f32 v79, v30, v31\n\t" \
  "v_cvt_pk_bf16_f32 v92, v40, v41\n\t" \
  "v_smfmac_f32_16x16x64_bf16 v[144:147], v[96:99], v[200:207], v234\n\t" \
  ".p2align 3\n\t" \
  "s_waitcnt vmcnt(12) lgkmcnt(0)\n\t" \
  "s_barrier\n\t" \
  "v_smfmac_f32_16x16x64_bf16 v[148:151], v[112:115], v[208:215], v234\n\t" \
  "ds_read_b64 v[214:215], v253 offset:17616\n\t" \
  "s_add_u32 m0, s25, 81480\n\t" \
  "s_nop 0\n\t" \
  "buffer_load_dwordx4 v254, s[8:11], s24 offen lds\n\t" \
  "ds_read_b64 v[226:227], v253 offset:16800\n\t" \
  "v_and_b32 v101, 0xffff0000, v102\n\t" \
  "v_cvt_pk_bf16_f32 v70, v20, v21\n\t" \
  "s_add_u32 m0, s25, 89680\n\t" \
  "ds_read_b64 v[202:203], v253 offset:16808\n\t" \
  "ds_read_b64 v[228:229], v253 offset:17200\n\t" \
  "ds_read_b64 v[204:205], v253 offset:17208\n\t" \
  "v_cvt_pk_bf16_f32 v94, v44, v45\n\t" \
  "s_add_u32 s24, s24, s26\n\t" \
  "v_and_b32 v116, 0xffff, v118\n\t" \
  "ds_read_b64 v[210:211], v253 offset:16816\n\t" \
  "buffer_load_dwordx4 v254, s[8:11], s24 offen lds\n\t" \
  "v_cvt_pk_bf16_f32 v68, v16, v17\n\t" \
  "v_and_b32 v104, 0xffff, v106\n\t" \
  "s_add_u32 m0, s25, 97880\n\t" \
  "v_lshlrev_b32 v235, 1, v251\n\t" \
  "ds_read_b64 v[208:209], v253 offset:16416\n\t" \
  "s_add_u32 s24, s24, s26\n\t" \
  "v_and_b32 v132, 0xffff, v134\n\t" \
  "v_and_b32 v121, 0xffff0000, v122\n\t" \
  "ds_read_b64 v[206:207], v253 offset:17608\n\t" \
  "ds_read_b64 v[230:231], v253 offset:17600\n\t" \
  "ds_read_b64 v[200:201], v253 offset:16408\n\t" \
  "ds_read_b64 v[224:225], v253 offset:16400\n\t" \
  "v_and_b32 v236, 0x70, v235\n\t" \
  "v_and_b32 v117, 0xffff0000, v118\n\t" \
  "v_and_b32 v118, 0xffff, v119\n\t" \
  "v_bfe_u32 v235, v250, 6, 1\n\t" \
  "v_lshl_add_u32 v235, v235, 4, v250\n\t" \
  "v_and_b32 v102, 0xffff, v103\n\t" \
  "v_cvt_pk_bf16_f32 v77, v26, v27\n\t" \
  "ds_read_b64 v[212:213], v253 offset:17216\n\t" \
  "v_and_b32 v120, 0xffff, v122\n\t" \
  "s_branch .Lcnx_mid\n\t" \
  ".Lcnx_w0_2:\n\t" \
  "ds_read_b32 v246, v251 offset:47144\n\t" \
  "s_waitcnt lgkmcnt(0)\n\t" \
  "v_readfirstlane_b32 s4, v246\n\t" \
  "s_cmp_lt_u32 s4, 3072\n\t" \
  "s_cbranch_scc1 .Lcnx_w0_2\n\t" \
  "s_branch .Lcnx_g0_2\n\t" \
  ".Lcnx_w0_3:\n\t" \
  "ds_read_b32 v246, v251 offset:47152\n\t" \
  "s_waitcnt lgkmcnt(0)\n\t" \
  "v_readfirstlane_b32 s4, v246\n\t" \
  "s_cmp_lt_u32 s4, 3072\n\t" \
  "s_cbranch_scc1 .Lcnx_w0_3\n\t" \
  "s_branch .Lcnx_g0_3\n\t" \
  ".Lcnx_w0_4:\n\t" \
  "ds_read_b32 v246, v251 offset:47160\n\t" \
  "s_waitcnt lgkmcnt(0)\n\t" \
  "v_readfirstlane_b32 s4, v246\n\t" \
  "s_cmp_lt_u32 s4, 3072\n\t" \
  "s_cbranch_scc1 .Lcnx_w0_4\n\t" \
  "s_branch .Lcnx_g0_4\n\t" \
  ".Lcnx_w0_5:\n\t" \
  "ds_read_b32 v246, v251 offset:47168\n\t" \
  "s_waitcnt lgkmcnt(0)\n\t" \
  "v_readfirstlane_b32 s4, v246\n\t" \
  "s_cmp_lt_u32 s4, 3072\n\t" \
  "s_cbranch_scc1 .Lcnx_w0_5\n\t" \
  "s_branch .Lcnx_g0_5\n\t" \
  ".Lcnx_w0_6:\n\t" \
  "ds_read_b32 v246, v251 offset:47176\n\t" \
  "s_waitcnt lgkmcnt(0)\n\t" \
  "v_readfirstlane_b32 s4, v246\n\t" \
  "s_cmp_lt_u32 s4, 3072\n\t" \
  "s_cbranch_scc1 .Lcnx_w0_6\n\t" \
  "s_branch .Lcnx_g0_6\n\t" \
  ".Lcnx_w0_7:\n\t" \
  "ds_read_b32 v246, v251 offset:47184\n\t" \
  "s_waitcnt lgkmcnt(0)\n\t" \
  "v_readfirstlane_b32 s4, v246\n\t" \
  "s_cmp_lt_u32 s4, 3072\n\t" \
  "s_cbranch_scc1 .Lcnx_w0_7\n\t" \
  "s_branch .Lcnx_g0_7\n\t" \
  ".Lcnx_w0_8:\n\t" \
  "ds_read_b32 v246, v251 offset:47192\n\t" \
  "s_waitcnt lgkmcnt(0)\n\t" \
  "v_readfirstlane_b32 s4, v246\n\t" \
  "s_cmp_lt_u32 s4, 3072\n\t" \
  "s_cbranch_scc1 .Lcnx_w0_8\n\t" \
  "s_branch .Lcnx_g0_8\n\t" \
  ".Lcnx_w0_9:\n\t" \
  "ds_read_b32 v246, v251 offset:47200\n\t" \
  "s_waitcnt lgkmcnt(0)\n\t" \
  "v_readfirstlane_b32 s4, v246\n\t" \
  "s_cmp_lt_u32 s4, 3072\n\t" \
  "s_cbranch_scc1 .Lcnx_w0_9\n\t" \
  "s_branch .Lcnx_g0_9\n\t" \
  ".Lcnx_w0_10:\n\t" \
  "ds_read_b32 v246, v251 offset:47208\n\t" \
  "s_waitcnt lgkmcnt(0)\n\t" \
  "v_readfirstlane_b32 s4, v246\n\t" \
  "s_cmp_lt_u32 s4, 3072\n\t" \
  "s_cbranch_scc1 .Lcnx_w0_10\n\t" \
  "s_branch .Lcnx_g0_10\n\t" \
  ".Lcnx_t0:\n\t" \
  "v_add_u32 v246, -116544, v251\n\t" \
  "v_lshrrev_b32 v246, 3, v246\n\t" \
  "v_mul_i32_i24 v246, s26, v246\n\t" \
  "v_add_u32 v246, s28, v246\n\t" \
  "s_mov_b32 m0, 107744\n\t" \
  "s_mov_b64 exec, 0xffff\n\t" \
  "buffer_load_dword v246, s[20:23], 0 offen lds\n\t" \
  "s_mov_b64 exec, -1\n\t" \
  "s_branch .Lcnx_u0\n\t" \
  ".Lcnx_w0_11:\n\t" \
  "ds_read_b32 v246, v251 offset:47216\n\t" \
  "s_waitcnt lgkmcnt(0)\n\t" \
  "v_readfirstlane_b32 s4, v246\n\t" \
  "s_cmp_lt_u32 s4, 3072\n\t" \
  "s_cbranch_scc1 .Lcnx_w0_11\n\t" \
  "s_branch .Lcnx_g0_11\n\t" \
  ".Lcnx_w0_12:\n\t" \
  "ds_read_b32 v246, v251 offset:47224\n\t" \
  "s_waitcnt lgkmcnt(0)\n\t" \
  "v_readfirstlane_b32 s4, v246\n\t" \
  "s_cmp_lt_u32 s4, 3072\n\t" \
  "s_cbranch_scc1 .Lcnx_w0_12\n\t" \
  "s_branch .Lcnx_g0_12\n\t" \
  ".Lcnx_w0_13:\n\t" \
  "ds_read_b32 v246, v251 offset:47232\n\t" \
  "s_waitcnt lgkmcnt(0)\n\t" \
  "v_readfirstlane_b32 s4, v246\n\t" \
  "s_cmp_lt_u32 s4, 3072\n\t" \
  "s_cbranch_scc1 .Lcnx_w0_13\n\t" \
  "s_branch .Lcnx_g0_13\n\t" \
  ".Lcnx_w1_2:\n\t" \
  "ds_read_b32 v246, v251 offset:47144\n\t" \
  "s_waitcnt lgkmcnt(0)\n\t" \
  "v_readfirstlane_b32 s4, v246\n\t" \
  "s_cmp_lt_u32 s4, 6144\n\t" \
  "s_cbranch_scc1 .Lcnx_w1_2\n\t" \
  "s_branch .Lcnx_g1_2\n\t" \
  ".Lcnx_w1_3:\n\t" \
  "ds_read_b32 v246, v251 offset:47152\n\t" \
  "s_waitcnt lgkmcnt(0)\n\t" \
  "v_readfirstlane_b32 s4, v246\n\t" \
  "s_cmp_lt_u32 s4, 6144\n\t" \
  "s_cbranch_scc1 .Lcnx_w1_3\n\t" \
  "s_branch .Lcnx_g1_3\n\t" \
  ".Lcnx_w1_4:\n\t" \
  "ds_read_b32 v246, v251 offset:47160\n\t" \
  "s_waitcnt lgkmcnt(0)\n\t" \
  "v_readfirstlane_b32 s4, v246\n\t" \
  "s_cmp_lt_u32 s4, 6144\n\t" \
  "s_cbranch_scc1 .Lcnx_w1_4\n\t" \
  "s_branch .Lcnx_g1_4\n\t" \
  ".Lcnx_w1_5:\n\t" \
  "ds_read_b32 v246, v251 offset:47168\n\t" \
  "s_waitcnt lgkmcnt(0)\n\t" \
  "v_readfirstlane_b32 s4, v246\n\t" \
  "s_cmp_lt_u32 s4, 6144\n\t" \
  "s_cbranch_scc1 .Lcnx_w1_5\n\t" \
  "s_branch .Lcnx_g1_5\n\t" \
  ".Lcnx_w1_6:\n\t" \
  "ds_read_b32 v246, v251 offset:47176\n\t" \
  "s_waitcnt lgkmcnt(0)\n\t" \
  "v_readfirstlane_b32 s4, v246\n\t" \
  "s_cmp_lt_u32 s4, 6144\n\t" \
  "s_cbranch_scc1 .Lcnx_w1_6\n\t" \
  "s_branch .Lcnx_g1_6\n\t" \
  ".Lcnx_w1_7:\n\t" \
  "ds_read_b32 v246, v251 offset:47184\n\t" \
  "s_waitcnt lgkmcnt(0)\n\t" \
  "v_readfirstlane_b32 s4, v246\n\t" \
  "s_cmp_lt_u32 s4, 6144\n\t" \
  "s_cbranch_scc1 .Lcnx_w1_7\n\t" \
  "s_branch .Lcnx_g1_7\n\t" \
  ".Lcnx_w1_8:\n\t" \
  "ds_read_b32 v246, v251 offset:47192\n\t" \
  "s_waitcnt lgkmcnt(0)\n\t" \
  "v_readfirstlane_b32 s4, v246\n\t" \
  "s_cmp_lt_u32 s4, 6144\n\t" \
  "s_cbranch_scc1 .Lcnx_w1_8\n\t" \
  "s_branch .Lcnx_g1_8\n\t" \
  ".Lcnx_w1_9:\n\t" \
  "ds_read_b32 v253, v251 offset:47200\n\t" \
  "s_waitcnt lgkmcnt(0)\n\t" \
  "v_readfirstlane_b32 s4, v253\n\t" \
  "s_cmp_lt_u32 s4, 6144\n\t" \
  "s_cbranch_scc1 .Lcnx_w1_9\n\t" \
  "s_branch .Lcnx_g1_9\n\t" \
  ".Lcnx_mid:\n\t" \
  "buffer_load_dwordx4 v254, s[8:11], s24 offen lds\n\t" \
  "v_and_b32 v103, 0xffff0000, v103\n\t" \
  "s_waitcnt lgkmcnt(1)\n\t" \
  "v_add3_u32 v235, v235, v236, s61\n\t" \
  "v_and_b32 v133, 0xffff0000, v134\n\t" \
  "v_and_b32 v119, 0xffff0000, v119\n\t" \
  "v_smfmac_f32_16x16x64_bf16 v[144:147], v[100:103], v[224:231], v234\n\t" \
  ".p2align 3\n\t" \
  "s_waitcnt vmcnt(12) lgkmcnt(0)\n\t" \
  "s_barrier\n\t" \
  "ds_read_b64 v[36:37], v235 offset:0\n\t" \
  "v_and_b32 v109, 0xffff0000, v110\n\t" \
  "v_and_b32 v105, 0xffff0000, v106\n\t" \
  "v_and_b32 v106, 0xffff, v107\n\t" \
  "v_and_b32 v107, 0xffff0000, v107\n\t" \
  "s_add_u32 m0, s25, 0\n\t" \
  "ds_read_b64 v[38:39], v235 offset:8\n\t" \
  "s_add_u32 s24, s24, s26\n\t" \
  "v_smfmac_f32_16x16x64_bf16 v[152:155], v[128:131], v[216:223], v234\n\t" \
  "buffer_load_dwordx4 v254, s[8:11], s24 offen lds\n\t" \
  "s_add_u32 m0, s25, 8200\n\t" \
  "ds_read_b64 v[220:221], v253 offset:33600\n\t" \
  "v_smfmac_f32_16x16x64_bf16 v[156:159], v[96:99], v[224:231], v234\n\t" \
  "ds_read_b64 v[226:227], v253 offset:33208\n\t" \
  "v_cvt_pk_bf16_f32 v69, v18, v19\n\t" \
  "v_and_b32 v134, 0xffff, v135\n\t" \
  "v_and_b32 v135, 0xffff0000, v135\n\t" \
  "v_smfmac_f32_16x16x64_bf16 v[148:151], v[116:119], v[200:207], v234\n\t" \
  "ds_read_b64 v[46:47], v235 offset:136\n\t" \
  "ds_read_b64 v[218:219], v253 offset:33200\n\t" \
  "v_smfmac_f32_16x16x64_bf16 v[160:163], v[112:115], v[200:207], v234\n\t" \
  "s_add_u32 s24, s24, s26\n\t" \
  "buffer_load_dwordx4 v254, s[8:11], s24 offen lds\n\t" \
  "ds_read_b64 v[200:201], v253 offset:32816\n\t" \
  "v_and_b32 v136, 0xffff, v138\n\t" \
  "v_smfmac_f32_16x16x64_bf16 v[164:167], v[128:131], v[208:215], v234\n\t" \
  "ds_read_b64 v[206:207], v253 offset:34016\n\t" \
  "s_add_u32 s24, s24, s26\n\t" \
  "ds_read_b64 v[204:205], v253 offset:33616\n\t" \
  "v_and_b32 v108, 0xffff, v110\n\t" \
  "v_and_b32 v122, 0xffff, v123\n\t" \
  "ds_read_b64 v[224:225], v253 offset:32808\n\t" \
  "v_and_b32 v137, 0xffff0000, v138\n\t" \
  "v_and_b32 v110, 0xffff, v111\n\t" \
  "v_and_b32 v138, 0xffff, v139\n\t" \
  "ds_read_b64 v[216:217], v253 offset:32800\n\t" \
  "ds_read_b64 v[222:223], v253 offset:34000\n\t" \
  "s_add_u32 m0, s25, 16400\n\t" \
  "ds_read_b64 v[228:229], v253 offset:33608\n\t" \
  "s_waitcnt lgkmcnt(1)\n\t" \
  "ds_read_b64 v[42:43], v235 offset:72\n\t" \
  "v_and_b32 v123, 0xffff0000, v123\n\t" \
  "v_and_b32 v139, 0xffff0000, v139\n\t" \
  "v_and_b32 v111, 0xffff0000, v111\n\t" \
  "buffer_load_dwordx4 v254, s[8:11], s24 offen lds\n\t" \
  "ds_read_b64 v[40:41], v235 offset:64\n\t" \
  "v_smfmac_f32_16x16x64_bf16 v[144:147], v[104:107], v[216:223], v234\n\t" \
  "ds_read_b64 v[44:45], v235 offset:128\n\t" \
  "v_smfmac_f32_16x16x64_bf16 v[156:159], v[100:103], v[216:223], v234\n\t" \
  "ds_read_b64 v[202:203], v253 offset:33216\n\t" \
  "ds_read_b64 v[230:231], v253 offset:34008\n\t" \
  "v_smfmac_f32_16x16x64_bf16 v[152:155], v[132:135], v[208:215], v234\n\t" \
  "v_mfma_f32_32x32x16_bf16 v[0:15], v[242:245], v[240:243], 0\n\t" \
  "v_add_u32 v235, s58, v235\n\t" \
  "v_smfmac_f32_16x16x64_bf16 v[168:171], v[96:99], v[216:223], v234\n\t" \
  ".p2align 3\n\t" \
  "s_waitcnt vmcnt(4) lgkmcnt(0)\n\t" \
  "s_barrier\n\t" \
  "ds_read_b64 v[218:219], v232 offset:408\n\t" \
  "v_smfmac_f32_16x16x64_bf16 v[160:163], v[116:119], v[224:231], v234\n\t" \
  "ds_read_b64 v[210:211], v232 offset:400\n\t" \
  "ds_read_b64 v[216:217], v232 offset:8\n\t" \
  "v_smfmac_f32_16x16x64_bf16 v[172:175], v[112:115], v[224:231], v234\n\t" \
  "s_add_u32 m0, s25, 24600\n\t" \
  "ds_read_b64 v[74:75], v235 offset:8\n\t" \
  "s_add_u32 s24, s24, s26\n\t" \
  "buffer_load_dwordx4 v254, s[8:11], s24 offen lds\n\t" \
  "ds_read_b64 v[220:221], v232 offset:808\n\t" \
  "v_smfmac_f32_16x16x64_bf16 v[148:151], v[120:123], v[224:231], v234\n\t" \
  "ds_read_b64 v[214:215], v232 offset:1200\n\t" \
  "ds_read_b64 v[212:213], v232 offset:800\n\t" \
  "v_and_b32 v141, 0xffff0000, v142\n\t" \
  "ds_read_b64 v[222:223], v232 offset:1208\n\t" \
  "v_and_b32 v125, 0xffff0000, v126\n\t" \
  "s_add_u32 m0, s25, 32800\n\t" \
  "s_add_u32 s24, s24, s26\n\t" \
  "buffer_load_dwordx4 v254, s[8:11], s24 offen lds\n\t" \
  "ds_read_b64 v[208:209], v232 offset:0\n\t" \
  "v_and_b32 v124, 0xffff, v126\n\t" \
  "v_and_b32 v126, 0xffff, v127\n\t" \
  "v_and_b32 v140, 0xffff, v142\n\t" \
  "v_smfmac_f32_16x16x64_bf16 v[176:179], v[128:131], v[200:207], v234\n\t" \
  "ds_read_b64 v[228:229], v232 offset:816\n\t" \
  "ds_read_b64 v[72:73], v235 offset:0\n\t" \
  "ds_read_b64 v[82:83], v235 offset:72\n\t" \
  "v_smfmac_f32_16x16x64_bf16 v[152:155], v[136:139], v[200:207], v234\n\t" \
  "s_add_u32 m0, s25, 41000\n\t" \
  "v_mov_b32 v199, 0x3f003f00\n\t" \
  "v_smfmac_f32_16x16x64_bf16 v[164:167], v[132:135], v[200:207], v234\n\t" \
  "s_add_u32 s24, s24, s26\n\t" \
  "ds_read_b64 v[224:225], v232 offset:16\n\t" \
  "ds_read_b64 v[226:227], v232 offset:416\n\t" \
  "v_mfma_f32_32x32x16_bf16 v[16:31], v[242:245], v[240:243], 0\n\t" \
  "s_waitcnt lgkmcnt(5)\n\t" \
  "ds_read_b64 v[88:89], v235 offset:128\n\t" \
  "ds_read_b64 v[80:81], v235 offset:64\n\t" \
  "v_mov_b32 v197, 0x3f003f00\n\t" \
  "v_and_b32 v142, 0xffff, v143\n\t" \
  "v_and_b32 v127, 0xffff0000, v127\n\t" \
  "v_and_b32 v143, 0xffff0000, v143\n\t" \
  "v_smfmac_f32_16x16x64_bf16 v[156:159], v[104:107], v[208:215], v234\n\t" \
  "ds_read_b64 v[90:91], v235 offset:136\n\t" \
  "v_smfmac_f32_16x16x64_bf16 v[144:147], v[108:111], v[208:215], v234\n\t" \
  "buffer_load_dwordx4 v254, s[8:11], s24 offen lds\n\t" \
  "ds_read_b64 v[230:231], v232 offset:1216\n\t" \
  "v_mov_b32 v196, 0x3f003f00\n\t" \
  "v_smfmac_f32_16x16x64_bf16 v[172:175], v[116:119], v[216:223], v234\n\t" \
  "v_mov_b32 v198, 0x3f003f00\n\t" \
  "v_add_u32 v235, s59, v235\n\t" \
  "v_smfmac_f32_16x16x64_bf16 v[168:171], v[100:103], v[208:215], v234\n\t" \
  "v_add_u32 v235, s60, v235\n\t" \
  "v_smfmac_f32_16x16x64_bf16 v[180:183], v[96:99], v[208:215], v234\n\t" \
  ".p2align 3\n\t" \
  "s_waitcnt vmcnt(8) lgkmcnt(0)\n\t" \
  "s_barrier\n\t" \
  "ds_read_b64 v[202:203], v232 offset:16800\n\t" \
  "ds_read_b64 v[200:201], v232 offset:16400\n\t" \
  "v_smfmac_f32_16x16x64_bf16 v[160:163], v[120:123], v[216:223], v234\n\t" \
  "ds_read_b64 v[206:207], v232 offset:17600\n\t" \
  "ds_read_b64 v[208:209], v232 offset:16408\n\t" \
  "v_smfmac_f32_16x16x64_bf16 v[184:187], v[112:115], v[216:223], v234\n\t" \
  "s_add_u32 m0, s25, 56880\n\t" \
  "ds_read_b64 v[204:205], v232 offset:17200\n\t" \
  "s_setprio 2\n\t" \
  "v_smfmac_f32_16x16x64_bf16 v[148:151], v[124:127], v[216:223], v234\n\t" \
  "s_add_u32 s24, s24, s26\n\t" \
  "buffer_load_dwordx4 v254, s[8:11], s24 offen lds\n\t" \
  "v_cvt_pk_bf16_f32 v144, v144, v145\n\t" \
  "v_cvt_pk_bf16_f32 v145, v146, v147\n\t" \
  "v_smfmac_f32_16x16x64_bf16 v[176:179], v[132:135], v[224:231], v234\n\t" \
  "ds_read_b64 v[212:213], v232 offset:17208\n\t" \
  "v_smfmac_f32_16x16x64_bf16 v[164:167], v[136:139], v[224:231], v234\n\t" \
  "s_add_u32 m0, s25, 65080\n\t" \
  "ds_read_b64 v[210:211], v232 offset:16808\n\t" \
  "ds_read_b64 v[216:217], v232 offset:16416\n\t" \
  "s_add_u32 s24, s24, s26\n\t" \
  "v_cvt_pk_bf16_f32 v148, v148, v149\n\t" \
  "v_cvt_pk_bf16_f32 v149, v150, v151\n\t" \
  "v_smfmac_f32_16x16x64_bf16 v[188:191], v[128:131], v[224:231], v234\n\t" \
  "ds_read_b64 v[220:221], v232 offset:17216\n\t" \
  "ds_read_b64 v[214:215], v232 offset:17608\n\t" \
  "v_smfmac_f32_16x16x64_bf16 v[152:155], v[140:143], v[224:231], v234\n\t" \
  "buffer_load_dwordx4 v254, s[8:11], s24 offen lds\n\t" \
  "s_waitcnt lgkmcnt(5)\n\t" \
  "ds_read_b64 v[218:219], v232 offset:16816\n\t" \
  "ds_read_b64 v[222:223], v232 offset:17616\n\t" \
  "v_smfmac_f32_16x16x64_bf16 v[180:183], v[100:103], v[200:207], v234\n\t" \
  "v_smfmac_f32_16x16x64_bf16 v[0:3], v[96:99], v[200:207], v234\n\t" \
  "s_setprio 0\n\t" \
  "s_add_u32 s24, s24, s26\n\t" \
  "v_cvt_pk_bf16_f32 v152, v152, v153\n\t" \
  "v_cvt_pk_bf16_f32 v153, v154, v155\n\t" \
  "v_mfma_f32_16x16x32_bf16 v[32:35], v[242:245], v[240:243], 0\n\t" \
  ".p2align 3\n\t" \
  "s_waitcnt vmcnt(8) lgkmcnt(0)\n\t" \
  "s_barrier\n\t" \
  "s_add_u32 m0, s25, 73280\n\t" \
  "s_nop 0\n\t" \
  "buffer_load_dwordx4 v254, s[8:11], s24 offen lds\n\t" \
  "v_smfmac_f32_16x16x64_bf16 v[156:159], v[108:111], v[200:207], v234\n\t" \
  "s_add_u32 s24, s24, s26\n\t" \
  "ds_read_b64 v[226:227], v232 offset:33200\n\t" \
  "v_smfmac_f32_16x16x64_bf16 v[4:7], v[112:115], v[208:215], v234\n\t" \
  "s_add_u32 m0, s25, 81480\n\t" \
  "ds_read_b64 v[228:229], v232 offset:33600\n\t" \
  "ds_read_b64 v[224:225], v232 offset:32800\n\t" \
  "buffer_load_dwordx4 v254, s[8:11], s24 offen lds\n\t" \
  "v_smfmac_f32_16x16x64_bf16 v[172:175], v[120:123], v[208:215], v234\n\t" \
  "ds_read_b64 v[230:231], v232 offset:34000\n\t" \
  "v_smfmac_f32_16x16x64_bf16 v[160:163], v[124:127], v[208:215], v234\n\t" \
  "v_smfmac_f32_16x16x64_bf16 v[8:11], v[128:131], v[216:223], v234\n\t" \
  "s_setprio 1\n\t" \
  "s_setprio 1\n\t" \
  "v_smfmac_f32_16x16x64_bf16 v[184:187], v[116:119], v[208:215], v234\n\t" \
  "ds_read_b64 v[208:209], v232 offset:32816\n\t" \
  "v_smfmac_f32_16x16x64_bf16 v[168:171], v[104:107], v[200:207], v234\n\t" \
  "ds_read_b64 v[200:201], v232 offset:32808\n\t" \
  "s_nop 0\n\t" \
  "v_cvt_pk_bf16_f32 v160, v160, v161\n\t" \
  "buffer_store_dwordx4 v[76:79], v250, s[20:23], s29 offen offset:0 sc1\n\t" \
  "ds_read_b64 v[210:211], v232 offset:33216\n\t" \
  "v_cvt_pk_bf16_f32 v156, v156, v157\n\t" \
  "v_smfmac_f32_16x16x64_bf16 v[188:191], v[132:135], v[216:223], v234\n\t" \
  "ds_read_b64 v[214:215], v232 offset:34016\n\t" \
  "ds_read_b64 v[212:213], v232 offset:33616\n\t" \
  "v_smfmac_f32_16x16x64_bf16 v[164:167], v[140:143], v[216:223], v234\n\t" \
  "s_waitcnt lgkmcnt(5)\n\t" \
  "ds_read_b64 v[204:205], v232 offset:33608\n\t" \
  "v_smfmac_f32_16x16x64_bf16 v[168:171], v[108:111], v[224:231], v234\n\t" \
  "ds_read_b64 v[206:207], v232 offset:34008\n\t" \
  "s_nop 3\n\t" \
  "v_cvt_pk_bf16_f32 v164, v164, v165\n\t" \
  "v_smfmac_f32_16x16x64_bf16 v[176:179], v[136:139], v[216:223], v234\n\t" \
  "s_setprio 0\n\t" \
  "ds_read_b64 v[202:203], v232 offset:33208\n\t" \
  "v_cvt_pk_bf16_f32 v157, v158, v159\n\t" \
  "v_smfmac_f32_16x16x64_bf16 v[12:15], v[96:99], v[224:231], v234\n\t" \
  "v_cvt_pk_bf16_f32 v165, v166, v167\n\t" \
  "v_smfmac_f32_16x16x64_bf16 v[180:183], v[104:107], v[224:231], v234\n\t" \
  "v_cvt_pk_bf16_f32 v161, v162, v163\n\t" \
  "v_smfmac_f32_16x16x64_bf16 v[0:3], v[100:103], v[224:231], v234\n\t" \
  ".p2align 3\n\t" \
  "s_waitcnt vmcnt(9) lgkmcnt(0)\n\t" \
  "s_barrier\n\t" \
  "ds_read_b64 v[228:229], v253 offset:808\n\t" \
  "v_smfmac_f32_16x16x64_bf16 v[176:179], v[140:143], v[208:215], v234\n\t" \
  "ds_write_b64 v193, v[148:149] offset:16384\n\t" \
  "v_smfmac_f32_16x16x64_bf16 v[4:7], v[116:119], v[200:207], v234\n\t" \
  "v_smfmac_f32_16x16x64_bf16 v[184:187], v[120:123], v[200:207], v234\n\t" \
  "ds_read_b64 v[220:221], v253 offset:800\n\t" \
  "v_smfmac_f32_16x16x64_bf16 v[16:19], v[112:115], v[200:207], v234\n\t" \
  "ds_read_b64 v[216:217], v253 offset:0\n\t" \
  "v_cvt_pk_bf16_f32 v168, v168, v169\n\t" \
  "buffer_store_dwordx4 v[84:87], v250, s[20:23], s29 offen offset:64 sc1\n\t" \
  "ds_read_b64 v[224:225], v253 offset:8\n\t" \
  "ds_read_b64 v[222:223], v253 offset:1200\n\t" \
  "v_cvt_pk_bf16_f32 v169, v170, v171\n\t" \
  "ds_read_b64 v[218:219], v253 offset:400\n\t" \
  "ds_read_b64 v[226:227], v253 offset:408\n\t" \
  "ds_write_b64 v193, v[152:153] offset:20480\n\t" \
  "v_smfmac_f32_16x16x64_bf16 v[172:175], v[124:127], v[200:207], v234\n\t" \
  "ds_read_b64 v[230:231], v253 offset:1208\n\t" \
  "ds_read_b64 v[202:203], v253 offset:416\n\t" \
  "v_mfma_f32_16x16x32_bf16 v[76:79], v[242:245], v[240:243], 0\n\t" \
  "ds_read_b64 v[200:201], v253 offset:16\n\t" \
  "ds_read_b64 v[204:205], v253 offset:816\n\t" \
  "v_smfmac_f32_16x16x64_bf16 v[8:11], v[132:135], v[208:215], v234\n\t" \
  "s_waitcnt lgkmcnt(6)\n\t" \
  "s_nop 0\n\t" \
  "v_cvt_pk_bf16_f32 v172, v172, v173\n\t" \
  "v_smfmac_f32_16x16x64_bf16 v[20:23], v[128:131], v[208:215], v234\n\t" \
  "ds_write_b64 v193, v[160:161] offset:4096\n\t" \
  "s_waitcnt lgkmcnt(4)\n\t" \
  "v_cvt_pk_bf16_f32 v176, v176, v177\n\t" \
  "v_smfmac_f32_16x16x64_bf16 v[0:3], v[104:107], v[216:223], v234\n\t" \
  "s_setprio 1\n\t" \
  "ds_read_b64 v[206:207], v253 offset:1216\n\t" \
  "ds_write_b64 v193, v[156:157] offset:0\n\t" \
  "v_smfmac_f32_16x16x64_bf16 v[12:15], v[100:103], v[216:223], v234\n\t" \
  "v_smfmac_f32_16x16x64_bf16 v[28:31], v[112:115], v[224:231], v234\n\t" \
  "ds_write_b64 v193, v[164:165] offset:8192\n\t" \
  "v_cvt_pk_bf16_f32 v177, v178, v179\n\t" \
  "v_smfmac_f32_16x16x64_bf16 v[180:183], v[108:111], v[216:223], v234\n\t" \
  "s_setprio 0\n\t" \
  "ds_write_b64 v193, v[144:145] offset:12288\n\t" \
  "v_mfma_f32_16x16x32_bf16 v[84:87], v[242:245], v[240:243], 0\n\t" \
  "v_cvt_pk_bf16_f32 v173, v174, v175\n\t" \
  "v_smfmac_f32_16x16x64_bf16 v[188:191], v[136:139], v[208:215], v234\n\t" \
  "v_smfmac_f32_16x16x64_bf16 v[24:27], v[96:99], v[216:223], v234\n\t" \
  ".p2align 3\n\t" \
  "s_waitcnt vmcnt(8) lgkmcnt(0)\n\t" \
  "s_barrier\n\t" \
  "ds_read_b64 v[208:209], v253 offset:16400\n\t" \
  "ds_write_b64 v192, v[168:169] offset:0\n\t" \
  "buffer_store_dwordx4 v[92:95], v250, s[20:23], s29 offen offset:128 sc1\n\t" \
  "ds_read_b64 v[212:213], v253 offset:17200\n\t" \
  "ds_read_b128 v[144:147], v194 offset:0\n\t" \
  "v_smfmac_f32_16x16x64_bf16 v[16:19], v[116:119], v[224:231], v234\n\t" \
  "v_cvt_pk_bf16_f32 v180, v180, v181\n\t" \
  "v_cvt_pk_bf16_f32 v181, v182, v183\n\t" \
  "ds_read_b64 v[214:215], v253 offset:17600\n\t" \
  "ds_read_b128 v[152:155], v194 offset:8192\n\t" \
  "v_smfmac_f32_16x16x64_bf16 v[184:187], v[124:127], v[224:231], v234\n\t" \
  "ds_read_b64 v[210:211], v253 offset:16800\n\t" \
  "ds_write_b64 v192, v[172:173] offset:4096\n\t" \
  "ds_read_b64 v[216:217], v253 offset:16408\n\t" \
  "v_smfmac_f32_16x16x64_bf16 v[4:7], v[120:123], v[224:231], v234\n\t" \
  "ds_write_b64 v192, v[176:177] offset:8192\n\t" \
  "v_mfma_f32_16x16x32_bf16 v[92:95], v[242:245], v[240:243], 0\n\t" \
  "ds_read_b128 v[148:151], v194 offset:4096\n\t" \
  "v_smfmac_f32_16x16x64_bf16 v[32:35], v[128:131], v[200:207], v234\n\t" \
  "s_waitcnt lgkmcnt(7)\n\t" \
  "ds_read_b64 v[220:221], v253 offset:17208\n\t" \
  "ds_read_b64 v[218:219], v253 offset:16808\n\t" \
  "v_mfma_f32_16x16x32_bf16 v[168:171], v[144:147], v[144:147], 0\n\t" \
  "ds_read_b64 v[226:227], v253 offset:16816\n\t" \
  "v_cvt_pk_bf16_f32 v184, v184, v185\n\t" \
  "v_mfma_f32_16x16x32_bf16 v[172:175], v[196:199], v[144:147], 0\n\t" \
  "s_setprio 0\n\t" \
  "ds_read_b64 v[224:225], v253 offset:16416\n\t" \
  "ds_read_b64 v[222:223], v253 offset:17608\n\t" \
  "v_smfmac_f32_16x16x64_bf16 v[8:11], v[136:139], v[200:207], v234\n\t" \
  "s_waitcnt lgkmcnt(5)\n\t" \
  "v_smfmac_f32_16x16x64_bf16 v[188:191], v[140:143], v[200:207], v234\n\t" \
  "v_smfmac_f32_16x16x64_bf16 v[20:23], v[132:135], v[200:207], v234\n\t" \
  "ds_read_b64 v[230:231], v253 offset:17616\n\t" \
  "ds_read_b64_tr_b16 v[156:157], v195 offset:0\n\t" \
  "v_mfma_f32_16x16x32_bf16 v[168:171], v[148:151], v[148:151], v[168:171]\n\t" \
  "ds_read_b64_tr_b16 v[164:165], v195 offset:8192\n\t" \
  "v_mfma_f32_16x16x32_bf16 v[168:171], v[152:155], v[152:155], v[168:171]\n\t" \
  "s_setprio 0\n\t" \
  "ds_read_b64_tr_b16 v[160:161], v195 offset:2048\n\t" \
  "s_setprio 0\n\t" \
  "v_smfmac_f32_16x16x64_bf16 v[76:79], v[96:99], v[208:215], v234\n\t" \
  "ds_read_b64_tr_b16 v[158:159], v195 offset:4096\n\t" \
  "ds_read_b64_tr_b16 v[162:163], v195 offset:6144\n\t" \
  "v_smfmac_f32_16x16x64_bf16 v[24:27], v[100:103], v[208:215], v234\n\t" \
  "ds_read_b64 v[228:229], v253 offset:17216\n\t" \
  "v_mfma_f32_16x16x32_bf16 v[172:175], v[196:199], v[148:151], v[172:175]\n\t" \
  "buffer_store_dwordx4 v[48:51], v250, s[20:23], s12 offen offset:0 sc1\n\t" \
  "ds_read_b64_tr_b16 v[166:167], v195 offset:10240\n\t" \
  "v_cvt_pk_bf16_f32 v188, v188, v189\n\t" \
  "v_cvt_pk_bf16_f32 v189, v190, v191\n\t" \
  "v_mfma_f32_16x16x32_bf16 v[172:175], v[196:199], v[152:155], v[172:175]\n\t" \
  "v_cvt_pk_bf16_f32 v185, v186, v187\n\t" \
  "v_add_u32 v194, -93792, v194\n\t" \
  "v_smfmac_f32_16x16x64_bf16 v[0:3], v[108:111], v[208:215], v234\n\t" \
  "v_smfmac_f32_16x16x64_bf16 v[12:15], v[104:107], v[208:215], v234\n\t" \
  ".p2align 3\n\t" \
  "s_waitcnt vmcnt(7) lgkmcnt(0)\n\t" \
  "s_barrier\n\t" \
  "ds_read_b64 v[202:203], v253 offset:33200\n\t" \
  "v_smfmac_f32_16x16x64_bf16 v[84:87], v[112:115], v[216:223], v234\n\t" \
  "ds_read_b64 v[48:49], v235 offset:0\n\t" \
  "ds_write_b64 v192, v[184:185] offset:16384\n\t" \
  "v_smfmac_f32_16x16x64_bf16 v[32:35], v[132:135], v[224:231], v234\n\t" \
  "ds_write_b64 v192, v[180:181] offset:12288\n\t" \
  "v_smfmac_f32_16x16x64_bf16 v[28:31], v[116:119], v[216:223], v234\n\t" \
  "v_cndmask_b32 v170, v170, v171, s[40:41]\n\t" \
  "ds_read_b64 v[200:201], v253 offset:32800\n\t" \
  "v_cvt_pk_bf16_f32 v0, v0, v1\n\t" \
  "v_cndmask_b32 v168, v168, v169, s[40:41]\n\t" \
  "ds_read_b64 v[50:51], v235 offset:8\n\t" \
  "buffer_store_dwordx4 v[56:59], v250, s[20:23], s12 offen offset:64 sc1\n\t" \
  "v_cvt_pk_bf16_f32 v1, v2, v3\n\t" \
  "v_smfmac_f32_16x16x64_bf16 v[16:19], v[120:123], v[216:223], v234\n\t" \
  "ds_read_b64 v[210:211], v253 offset:33208\n\t" \
  "ds_read_b64 v[204:205], v253 offset:33600\n\t" \
  "ds_read_b64 v[58:59], v235 offset:72\n\t" \
  "ds_write_b64 v192, v[188:189] offset:20480\n\t" \
  "v_cndmask_b32 v173, v168, v170, s[42:43]\n\t" \
  "v_mov_b32 v174, 0x1127c5ac\n\t" \
  "v_smfmac_f32_16x16x64_bf16 v[4:7], v[124:127], v[216:223], v234\n\t" \
  "ds_read_b64 v[216:217], v253 offset:32816\n\t" \
  "ds_read_b64 v[206:207], v253 offset:34000\n\t" \
  "ds_read_b64 v[56:57], v235 offset:64\n\t" \
  "ds_read_b64 v[222:223], v253 offset:34016\n\t" \
  "ds_read_b64 v[212:213], v253 offset:33608\n\t" \
  "v_smfmac_f32_16x16x64_bf16 v[92:95], v[128:131], v[224:231], v234\n\t" \
  "v_add_u32 v195, -93792, v195\n\t" \
  "ds_read_b64 v[218:219], v253 offset:33216\n\t" \
  "v_smfmac_f32_16x16x64_bf16 v[20:23], v[136:139], v[224:231], v234\n\t" \
  "ds_read_b64 v[214:215], v253 offset:34008\n\t" \
  "ds_read_b64 v[238:239], v235 offset:136\n\t" \
  "v_cvt_pk_bf16_f32 v4, v4, v5\n\t" \
  "v_cvt_pk_bf16_f32 v5, v6, v7\n\t" \
  "v_smfmac_f32_16x16x64_bf16 v[8:11], v[140:143], v[224:231], v234\n\t" \
  "s_waitcnt lgkmcnt(6)\n\t" \
  "ds_read_b64 v[220:221], v253 offset:33616\n\t" \
  "ds_read_b64 v[208:209], v253 offset:32808\n\t" \
  "v_smfmac_f32_16x16x64_bf16 v[12:15], v[108:111], v[200:207], v234\n\t" \
  "s_not_b64 vcc, s[52:53]\n\t" \
  "ds_read_b64 v[236:237], v235 offset:128\n\t" \
  "s_nop 1\n\t" \
  "v_cvt_pk_bf16_f32 v8, v8, v9\n\t" \
  "v_smfmac_f32_16x16x64_bf16 v[76:79], v[100:103], v[200:207], v234\n\t" \
  "v_cvt_pk_bf16_f32 v9, v10, v11\n\t" \
  "v_smfmac_f32_16x16x64_bf16 v[24:27], v[104:107], v[200:207], v234\n\t" \
  ".p2align 3\n\t" \
  "s_waitcnt vmcnt(7) lgkmcnt(0)\n\t" \
  "s_barrier\n\t" \
  "ds_read_b64 v[206:207], v232 offset:1208\n\t" \
  "v_smfmac_f32_16x16x64_bf16 v[16:19], v[124:127], v[208:215], v234\n\t" \
  "ds_read_b128 v[188:191], v194 offset:8192\n\t" \
  "ds_read_b128 v[184:187], v194 offset:4096\n\t" \
  "v_smfmac_f32_16x16x64_bf16 v[84:87], v[116:119], v[208:215], v234\n\t" \
  "ds_write_b64 v192, v[0:1] offset:24576\n\t" \
  "ds_read_b64 v[224:225], v232 offset:0\n\t" \
  "s_setprio 0\n\t" \
  "ds_read_b128 v[180:183], v194 offset:0\n\t" \
  "v_smfmac_f32_16x16x64_bf16 v[28:31], v[120:123], v[208:215], v234\n\t" \
  "ds_read_b64 v[230:231], v232 offset:1200\n\t" \
  "ds_write_b64 v192, v[4:5] offset:28672\n\t" \
  "v_smfmac_f32_16x16x64_bf16 v[32:35], v[136:139], v[216:223], v234\n\t" \
  "ds_read_b64 v[226:227], v232 offset:400\n\t" \
  "s_waitcnt lgkmcnt(3)\n\t" \
  "ds_write_b64 v192, v[8:9] offset:32768\n\t" \
  "v_mfma_f32_16x16x32_bf16 v[168:171], v[196:199], v[180:183], 0\n\t" \
  "v_mfma_f32_16x16x32_bf16 v[176:179], v[180:183], v[180:183], 0\n\t" \
  "ds_read_b64 v[228:229], v232 offset:800\n\t" \
  "ds_read_b64 v[210:211], v232 offset:416\n\t" \
  "ds_read_b64 v[212:213], v232 offset:816\n\t" \
  "ds_read_b64 v[214:215], v232 offset:1216\n\t" \
  "v_smfmac_f32_16x16x64_bf16 v[92:95], v[132:135], v[216:223], v234\n\t" \
  "ds_read_b64 v[208:209], v232 offset:16\n\t" \
  "v_cvt_pk_bf16_f32 v12, v12, v13\n\t" \
  "v_cvt_pk_bf16_f32 v13, v14, v15\n\t" \
  "v_mfma_f32_16x16x32_bf16 v[176:179], v[184:187], v[184:187], v[176:179]\n\t" \
  "ds_write_b64 v192, v[12:13] offset:36864\n\t" \
  "v_mfma_f32_16x16x32_bf16 v[168:171], v[196:199], v[184:187], v[168:171]\n\t" \
  "ds_read_b64 v[200:201], v232 offset:8\n\t" \
  "ds_read_b64 v[202:203], v232 offset:408\n\t" \
  "ds_read_b64 v[204:205], v232 offset:808\n\t" \
  "v_smfmac_f32_16x16x64_bf16 v[20:23], v[140:143], v[216:223], v234\n\t" \
  "v_cvt_pk_bf16_f32 v16, v16, v17\n\t" \
  "v_cvt_pk_bf16_f32 v17, v18, v19\n\t" \
  "v_mfma_f32_16x16x32_bf16 v[176:179], v[188:191], v[188:191], v[176:179]\n\t" \
  "buffer_store_dwordx4 v[64:67], v250, s[20:23], s12 offen offset:128 sc1\n\t" \
  "ds_write_b64 v192, v[16:17] offset:40960\n\t" \
  "v_mfma_f32_16x16x32_bf16 v[168:171], v[196:199], v[188:191], v[168:171]\n\t" \
  "s_waitcnt lgkmcnt(9)\n\t" \
  "v_mov_b32 v183, 0\n\t" \
  "v_smfmac_f32_16x16x64_bf16 v[76:79], v[104:107], v[224:231], v234\n\t" \
  "s_nop 1\n\t" \
  "v_cndmask_b32 v176, v176, v177, s[40:41]\n\t" \
  "v_cndmask_b32 v178, v178, v179, s[40:41]\n\t" \
  "v_cndmask_b32 v176, v176, v178, s[42:43]\n\t" \
  "v_mov_b32 v187, 0\n\t" \
  "v_cndmask_b32_dpp v172, v168, v172, vcc row_ror:8 row_mask:0xf bank_mask:0xf\n\t" \
  "v_smfmac_f32_16x16x64_bf16 v[24:27], v[108:111], v[224:231], v234\n\t" \
  ".p2align 3\n\t" \
  "s_waitcnt vmcnt(6) lgkmcnt(0)\n\t" \
  "s_barrier\n\t" \
  "ds_read_b64 v[220:221], v232 offset:17200\n\t" \
  "ds_read_b64 v[216:217], v232 offset:16400\n\t" \
  "v_smfmac_f32_16x16x64_bf16 v[84:87], v[120:123], v[200:207], v234\n\t" \
  "v_cndmask_b32_dpp v173, v176, v173, vcc row_ror:8 row_mask:0xf bank_mask:0xf\n\t" \
  "v_fmamk_f32 v173, v173, 0x3e2aaaab, v174\n\t" \
  "ds_read_b64 v[222:223], v232 offset:17600\n\t" \
  "ds_read_b64 v[218:219], v232 offset:16800\n\t" \
  "ds_read_b64 v[226:227], v232 offset:16808\n\t" \
  "ds_read_b64 v[224:225], v232 offset:16408\n\t" \
  "v_mul_f32 v172, 0x3daaaaab, v172\n\t" \
  "v_fma_f32 v173, -v172, v172, v173\n\t" \
  "v_max_f32 v173, v174, v173\n\t" \
  "v_cvt_pk_bf16_f32 v20, v20, v21\n\t" \
  "v_cvt_pk_bf16_f32 v21, v22, v23\n\t" \
  "v_rsq_f32 v173, v173 div:2\n\t" \
  "s_mov_b64 exec, s[52:53]\n\t" \
  "v_mul_f32 v173, 0x08805555, v173\n\t" \
  "s_mov_b64 exec, -1\n\t" \
  "v_smfmac_f32_16x16x64_bf16 v[28:31], v[124:127], v[200:207], v234\n\t" \
  "s_mov_b64 vcc, s[40:41]\n\t" \
  "ds_read_b64 v[228:229], v232 offset:17208\n\t" \
  "ds_write_b64 v192, v[20:21] offset:45056\n\t" \
  "v_cvt_pk_bf16_f32 v175, v173, v173\n\t" \
  "v_and_b32 v173, 0xffff0000, v175\n\t" \
  "v_smfmac_f32_16x16x64_bf16 v[92:95], v[136:139], v[208:215], v234\n\t" \
  "ds_read_b64 v[230:231], v232 offset:17608\n\t" \
  "ds_read_b64 v[200:201], v232 offset:16416\n\t" \
  "v_mul_f32 v174, -v172, v173 div:2\n\t" \
  "v_and_b32 v172, 0xffff0000, v174\n\t" \
  "v_cndmask_b32_sdwa v175, v175, v173, vcc dst_sel:DWORD dst_unused:UNUSED_PAD src0_sel:WORD_1 src1_sel:DWORD\n\t" \
  "v_cvt_pk_bf16_f32 v24, v24, v25\n\t" \
  "v_cvt_pk_bf16_f32 v25, v26, v27\n\t" \
  "v_smfmac_f32_16x16x64_bf16 v[32:35], v[140:143], v[208:215], v234\n\t" \
  "ds_read_b64 v[202:203], v232 offset:16816\n\t" \
  "s_or_b64 vcc, s[52:53], s[42:43]\n\t" \
  "ds_write_b64 v192, v[24:25] offset:56880\n\t" \
  "v_cndmask_b32 v180, v175, v240, vcc\n\t" \
  "v_sub_f32 v174, v174, v172\n\t" \
  "v_perm_b32 v174, v172, v174, s55\n\t" \
  "v_cndmask_b32_dpp v184, v175, v240, vcc row_ror:8 row_mask:0xf bank_mask:0xf\n\t" \
  "s_mov_b64 vcc, s[52:53]\n\t" \
  "s_waitcnt lgkmcnt(8)\n\t" \
  "v_smfmac_f32_16x16x64_bf16 v[76:79], v[108:111], v[216:223], v234\n\t" \
  "ds_read_b64 v[206:207], v232 offset:17616\n\t" \
  "ds_read_b64_tr_b16 v[144:145], v195 offset:0\n\t" \
  "ds_read_b64 v[204:205], v232 offset:17216\n\t" \
  "ds_read_b64_tr_b16 v[146:147], v195 offset:4096\n\t" \
  "v_cndmask_b32 v182, v174, v240, vcc\n\t" \
  "v_cndmask_b32_dpp v186, v174, v240, vcc row_ror:8 row_mask:0xf bank_mask:0xf\n\t" \
  "s_orn2_b64 vcc, s[52:53], s[42:43]\n\t" \
  "v_cndmask_b32 v181, v175, v240, vcc\n\t" \
  "v_cndmask_b32_dpp v185, v175, v240, vcc row_ror:8 row_mask:0xf bank_mask:0xf\n\t" \
  "v_mfma_f32_16x16x16_bf16 v[0:3], v[196:197], v[182:183], 0\n\t" \
  "v_cvt_pk_bf16_f32 v28, v28, v29\n\t" \
  "v_cvt_pk_bf16_f32 v29, v30, v31\n\t" \
  "v_cvt_pk_bf16_f32 v76, v76, v77\n\t" \
  "s_setprio 2\n\t" \
  "s_setprio 3\n\t" \
  "ds_read_b64_tr_b16 v[150:151], v195 offset:6144\n\t" \
  "ds_read_b64_tr_b16 v[154:155], v195 offset:10240\n\t" \
  "s_waitcnt lgkmcnt(3)\n\t" \
  "v_smfmac_f32_16x16x64_bf16 v[92:95], v[140:143], v[200:207], v234\n\t" \
  "ds_read_b64_tr_b16 v[152:153], v195 offset:8192\n\t" \
  "ds_read_b64_tr_b16 v[148:149], v195 offset:2048\n\t" \
  "v_smfmac_f32_16x16x64_bf16 v[84:87], v[124:127], v[224:231], v234\n\t" \
  "s_setprio 3\n\t" \
  "ds_write_b64 v192, v[28:29] offset:60976\n\t" \
  "v_mfma_f32_16x16x16_bf16 v[4:7], v[196:197], v[186:187], 0\n\t" \
  "v_cvt_pk_bf16_f32 v77, v78, v79\n\t" \
  "v_cvt_pk_bf16_f32 v32, v32, v33\n\t" \
  "v_mfma_f32_16x16x16_bf16 v[188:191], v[156:157], v[180:181], v[0:3]\n\t" \
  "v_cvt_pk_bf16_f32 v92, v92, v93\n\t" \
  "s_nop 0\n\t" \
  "v_cvt_pk_bf16_f32 v84, v84, v85\n\t" \
  "v_cvt_pk_bf16_f32 v85, v86, v87\n\t" \
  "v_cvt_pk_bf16_f32 v33, v34, v35\n\t" \
  "v_mfma_f32_16x16x16_bf16 v[168:171], v[160:161], v[180:181], v[0:3]\n\t" \
  "ds_write_b64 v192, v[32:33] offset:65072\n\t" \
  "ds_write_b64 v193, v[84:85] offset:4096\n\t" \
  "v_mfma_f32_16x16x16_bf16 v[176:179], v[158:159], v[180:181], v[0:3]\n\t" \
  "s_setprio 0\n\t" \
  "s_and_b32 s69, s69, 0xffff\n\t" \
  "ds_write_b64 v193, v[76:77] offset:0\n\t" \
  "v_cvt_pk_bf16_f32 v93, v94, v95\n\t" \
  "v_mfma_f32_16x16x16_bf16 v[172:175], v[162:163], v[180:181], v[0:3]\n\t" \
  "ds_write_b64 v193, v[92:93] offset:8192\n\t" \
  "v_cvt_pk_bf16_f32 v96, v188, v189\n\t" \
  "v_cvt_pk_bf16_f32 v97, v190, v191\n\t" \
  "v_mfma_f32_16x16x16_bf16 v[188:191], v[164:165], v[180:181], v[0:3]\n\t" \
  "v_cvt_pk_bf16_f32 v98, v176, v177\n\t" \
  "v_cvt_pk_bf16_f32 v99, v178, v179\n\t" \
  "v_mfma_f32_16x16x16_bf16 v[176:179], v[166:167], v[180:181], v[0:3]\n\t" \
  "v_cvt_pk_bf16_f32 v112, v168, v169\n\t" \
  "v_cvt_pk_bf16_f32 v113, v170, v171\n\t" \
  "v_cvt_pk_bf16_f32 v115, v174, v175\n\t" \
  "s_nop 1\n\t" \
  "v_cvt_pk_bf16_f32 v128, v188, v189\n\t" \
  "s_nop 1\n\t" \
  "v_cvt_pk_bf16_f32 v131, v178, v179\n\t" \
  ".p2align 3\n\t" \
  "s_waitcnt lgkmcnt(0)\n\t" \
  "s_barrier\n\t" \
  "ds_read_b64_tr_b16 v[210:211], v248 offset:44072\n\t" \
  "ds_read_b64_tr_b16 v[208:209], v248 offset:43048\n\t" \
  "s_mov_b32 m0, s44\n\t" \
  "ds_read_b128 v[104:107], v194 offset:24576\n\t" \
  "ds_read_b128 v[120:123], v194 offset:28672\n\t" \
  "s_setprio 0\n\t" \
  "v_cvt_pk_bf16_f32 v114, v172, v173\n\t" \
  "s_mov_b32 exec_hi, 0xffff\n\t" \
  "buffer_load_dwordx4 v252, s[16:19], s33 offen lds\n\t" \
  "s_mov_b32 exec_hi, -1\n\t" \
  "v_cvt_pk_bf16_f32 v130, v176, v177\n\t" \
  "ds_read_b64_tr_b16 v[218:219], v248 offset:48168\n\t" \
  "ds_read_b128 v[136:139], v194 offset:32768\n\t" \
  "v_add_u32 v194, s54, v194\n\t" \
  "s_setprio 0\n\t" \
  "ds_read_b128 v[108:111], v194 offset:49152\n\t" \
  "s_add_u32 s33, s33, s32\n\t" \
  "ds_read_b64_tr_b16 v[214:215], v248 offset:47144\n\t" \
  "v_mfma_f32_16x16x16_bf16 v[172:175], v[146:147], v[184:185], v[4:7]\n\t" \
  "ds_read_b64_tr_b16 v[216:217], v248 offset:45096\n\t" \
  "v_cvt_pk_bf16_f32 v129, v190, v191\n\t" \
  "v_mfma_f32_16x16x16_bf16 v[168:171], v[144:145], v[184:185], v[4:7]\n\t" \
  "ds_read_b64_tr_b16 v[160:161], v248 offset:43560\n\t" \
  "ds_read_b64_tr_b16 v[162:163], v248 offset:44584\n\t" \
  "v_mfma_f32_16x16x16_bf16 v[188:191], v[148:149], v[184:185], v[4:7]\n\t" \
  "ds_read_b128 v[140:143], v194 offset:57344\n\t" \
  "ds_read_b64_tr_b16 v[212:213], v248 offset:46120\n\t" \
  "s_nop 2\n\t" \
  "v_cvt_pk_bf16_f32 v101, v170, v171\n\t" \
  "v_cvt_pk_bf16_f32 v100, v168, v169\n\t" \
  "v_mfma_f32_16x16x16_bf16 v[176:179], v[150:151], v[184:185], v[4:7]\n\t" \
  "ds_read_b64_tr_b16 v[164:165], v248 offset:46632\n\t" \
  "ds_read_b64_tr_b16 v[166:167], v248 offset:47656\n\t" \
  "v_cvt_pk_bf16_f32 v102, v172, v173\n\t" \
  "v_cvt_pk_bf16_f32 v103, v174, v175\n\t" \
  "v_mfma_f32_16x16x16_bf16 v[168:171], v[152:153], v[184:185], v[4:7]\n\t" \
  "ds_read_b64_tr_b16 v[232:233], v248 offset:45608\n\t" \
  "v_cvt_pk_bf16_f32 v116, v188, v189\n\t" \
  "ds_read_b64_tr_b16 v[234:235], v248 offset:48680\n\t" \
  "v_cvt_pk_bf16_f32 v117, v190, v191\n\t" \
  "v_mfma_f32_16x16x16_bf16 v[172:175], v[154:155], v[184:185], v[4:7]\n\t" \
  "ds_read_b64_tr_b16 v[26:27], v195 offset:28672\n\t" \
  "ds_read_b64_tr_b16 v[24:25], v195 offset:24576\n\t" \
  "v_cvt_pk_bf16_f32 v119, v178, v179\n\t" \
  "v_cvt_pk_bf16_f32 v118, v176, v177\n\t" \
  "s_waitcnt lgkmcnt(15)\n\t" \
  "v_mfma_f32_16x16x32_bf16 v[144:147], v[208:211], v[96:99], 0\n\t" \
  "s_waitcnt lgkmcnt(13)\n\t" \
  "ds_read_b64_tr_b16 v[30:31], v195 offset:30720\n\t" \
  "ds_read_b64_tr_b16 v[28:29], v195 offset:26624\n\t" \
  "ds_read_b128 v[124:127], v194 offset:53248\n\t" \
  "v_cvt_pk_bf16_f32 v133, v170, v171\n\t" \
  "v_cvt_pk_bf16_f32 v135, v174, v175\n\t" \
  "v_cvt_pk_bf16_f32 v132, v168, v169\n\t" \
  "s_waitcnt lgkmcnt(9)\n\t" \
  "v_mfma_f32_16x16x32_bf16 v[144:147], v[212:215], v[112:115], v[144:147]\n\t" \
  "ds_read_b64_tr_b16 v[32:33], v195 offset:32768\n\t" \
  "v_cvt_pk_bf16_f32 v134, v172, v173\n\t" \
  "v_mfma_f32_16x16x32_bf16 v[152:155], v[208:211], v[100:103], 0\n\t" \
  "ds_read_b64_tr_b16 v[34:35], v195 offset:34816\n\t" \
  "v_add_u32 v195, s54, v195\n\t" \
  "v_mfma_f32_16x16x32_bf16 v[144:147], v[216:219], v[128:131], v[144:147]\n\t" \
  "ds_read_b64_tr_b16 v[76:77], v195 offset:49152\n\t" \
  "ds_read_b64_tr_b16 v[78:79], v195 offset:53248\n\t" \
  "s_add_u32 m0, s44, s35\n\t" \
  "ds_read_b64_tr_b16 v[84:85], v195 offset:51200\n\t" \
  "s_mov_b32 exec_hi, 0xffff\n\t" \
  "buffer_load_dwordx4 v252, s[16:19], s34 offen lds\n\t" \
  "s_mov_b32 exec_hi, -1\n\t" \
  "v_mfma_f32_16x16x32_bf16 v[8:11], v[104:107], v[104:107], 0\n\t" \
  "s_add_u32 s34, s34, s32\n\t" \
  "ds_read_b64_tr_b16 v[94:95], v195 offset:59392\n\t" \
  "v_mfma_f32_16x16x32_bf16 v[152:155], v[212:215], v[116:119], v[152:155]\n\t" \
  "s_mov_b32 m0, s45\n\t" \
  "s_mov_b32 exec_hi, 0xffff\n\t" \
  "buffer_load_dwordx4 v252, s[16:19], s33 offen lds\n\t" \
  "s_mov_b32 exec_hi, -1\n\t" \
  "s_add_u32 s33, s33, s32\n\t" \
  "v_mfma_f32_16x16x32_bf16 v[12:15], v[196:199], v[104:107], 0\n\t" \
  "ds_read_b64_tr_b16 v[86:87], v195 offset:55296\n\t" \
  "v_cvt_pknorm_i16_f32 v220, |v146|, |v147|\n\t" \
  "v_mfma_f32_16x16x32_bf16 v[152:155], v[216:219], v[132:135], v[152:155]\n\t" \
  "ds_read_b64_tr_b16 v[92:93], v195 offset:57344\n\t" \
  "v_mfma_f32_16x16x32_bf16 v[8:11], v[120:123], v[120:123], v[8:11]\n\t" \
  "v_cvt_pknorm_i16_f32 v222, |v144|, |v145|\n\t" \
  "v_mfma_f32_16x16x32_bf16 v[12:15], v[196:199], v[120:123], v[12:15]\n\t" \
  "v_lshlrev_b32_sdwa v221, 2, v220 dst_sel:DWORD dst_unused:UNUSED_PAD src0_sel:DWORD src1_sel:BYTE_3\n\t" \
  "v_lshlrev_b32_sdwa v220, 2, v220 dst_sel:DWORD dst_unused:UNUSED_PAD src0_sel:DWORD src1_sel:BYTE_1\n\t" \
  "v_lshlrev_b32_sdwa v223, 2, v222 dst_sel:DWORD dst_unused:UNUSED_PAD src0_sel:DWORD src1_sel:BYTE_3\n\t" \
  "ds_read_b32 v221, v221 offset:10016\n\t" \
  "v_lshlrev_b32_sdwa v222, 2, v222 dst_sel:DWORD dst_unused:UNUSED_PAD src0_sel:DWORD src1_sel:BYTE_1\n\t" \
  "v_mfma_f32_16x16x32_bf16 v[8:11], v[136:139], v[136:139], v[8:11]\n\t" \
  "ds_read_b32 v220, v220 offset:10016\n\t" \
  "v_lshlrev_b32_sdwa v225, 1, v155 dst_sel:DWORD dst_unused:UNUSED_PAD src0_sel:DWORD src1_sel:WORD_1\n\t" \
  "v_mfma_f32_16x16x32_bf16 v[20:23], v[196:199], v[108:111], 0\n\t" \
  "ds_read_u16_d16_hi v225, v225 offset:10528\n\t" \
  "v_lshlrev_b32_sdwa v224, 1, v154 dst_sel:DWORD dst_unused:UNUSED_PAD src0_sel:DWORD src1_sel:WORD_1\n\t" \
  "v_mfma_f32_16x16x32_bf16 v[12:15], v[196:199], v[136:139], v[12:15]\n\t" \
  "ds_read_b32 v223, v223 offset:10016\n\t" \
  "v_lshlrev_b32_sdwa v227, 1, v153 dst_sel:DWORD dst_unused:UNUSED_PAD src0_sel:DWORD src1_sel:WORD_1\n\t" \
  "s_add_u32 m0, s45, s35\n\t" \
  "v_lshlrev_b32_sdwa v226, 1, v152 dst_sel:DWORD dst_unused:UNUSED_PAD src0_sel:DWORD src1_sel:WORD_1\n\t" \
  "v_cndmask_b32 v8, v8, v9, s[40:41]\n\t" \
  "v_cndmask_b32 v10, v10, v11, s[40:41]\n\t" \
  "v_mfma_f32_16x16x32_bf16 v[16:19], v[108:111], v[108:111], 0\n\t" \
  "ds_read_b32 v222, v222 offset:10016\n\t" \
  "s_mov_b32 exec_hi, 0xffff\n\t" \
  "buffer_load_dwordx4 v252, s[16:19], s34 offen lds\n\t" \
  "s_mov_b32 exec_hi, -1\n\t" \
  "v_mov_b32 v14, 0x1127c5ac\n\t" \
  "s_waitcnt lgkmcnt(13)\n\t" \
  "v_mfma_f32_16x16x32_bf16 v[16:19], v[124:127], v[124:127], v[16:19]\n\t" \
  "s_add_u32 s34, s34, s32\n\t" \
  "ds_read_u16 v224, v224 offset:10528\n\t" \
  "v_cndmask_b32 v13, v8, v10, s[42:43]\n\t" \
  "v_mfma_f32_16x16x32_bf16 v[20:23], v[196:199], v[124:127], v[20:23]\n\t" \
  "ds_read_u16 v226, v226 offset:10528\n\t" \
  "ds_read_u16_d16_hi v227, v227 offset:10528\n\t" \
  "s_waitcnt lgkmcnt(3)\n\t" \
  "v_mfma_f32_16x16x32_bf16 v[16:19], v[140:143], v[140:143], v[16:19]\n\t" \
  "v_fma_mix_f32 v221, |v147|, v221, v221 op_sel:[0,1,0] op_sel_hi:[0,1,1] clamp\n\t" \
  "v_fma_mix_f32 v220, |v146|, v220, v220 op_sel:[0,1,0] op_sel_hi:[0,1,1] clamp\n\t" \
  "v_mfma_f32_16x16x32_bf16 v[20:23], v[196:199], v[140:143], v[20:23]\n\t" \
  "buffer_store_dwordx4 v[52:55], v250, s[20:23], s28 offen offset:0 sc1\n\t" \
  "v_fma_mix_f32 v222, |v144|, v222, v222 op_sel:[0,1,0] op_sel_hi:[0,1,1] clamp\n\t" \
  "v_fma_mix_f32 v223, |v145|, v223, v223 op_sel:[0,1,0] op_sel_hi:[0,1,1] clamp\n\t" \
  "v_mfma_f32_16x16x32_bf16 v[148:151], v[160:163], v[96:99], 0\n\t" \
  "s_not_b64 vcc, s[52:53]\n\t" \
  "v_cndmask_b32 v18, v18, v19, s[40:41]\n\t" \
  "v_cndmask_b32 v16, v16, v17, s[40:41]\n\t" \
  "v_mfma_f32_16x16x32_bf16 v[156:159], v[160:163], v[100:103], 0\n\t" \
  "s_waitcnt lgkmcnt(0)\n\t" \
  "v_or_b32 v155, v224, v225\n\t" \
  "v_or_b32 v154, v226, v227\n\t" \
  "v_cndmask_b32 v16, v16, v18, s[42:43]\n\t" \
  "v_cndmask_b32_dpp v12, v20, v12, vcc row_ror:8 row_mask:0xf bank_mask:0xf\n\t" \
  "v_mul_f32 v12, 0x3daaaaab, v12\n\t" \
  "v_mfma_f32_16x16x32_bf16 v[148:151], v[164:167], v[112:115], v[148:151]\n\t" \
  "v_cndmask_b32_dpp v13, v16, v13, vcc row_ror:8 row_mask:0xf bank_mask:0xf\n\t" \
  "v_fmamk_f32 v13, v13, 0x3e2aaaab, v14\n\t" \
  "v_fma_f32 v13, -v12, v12, v13\n\t" \
  "v_max_f32 v13, v14, v13\n\t" \
  "v_rsq_f32 v13, v13 div:2\n\t" \
  "v_mfma_f32_16x16x32_bf16 v[156:159], v[164:167], v[116:119], v[156:159]\n\t" \
  "s_mov_b64 exec, s[52:53]\n\t" \
  "v_mul_f32 v13, 0x08805555, v13\n\t" \
  "s_mov_b64 exec, -1\n\t" \
  "v_cvt_pk_bf16_f32 v15, v13, v13\n\t" \
  "v_and_b32 v13, 0xffff0000, v15\n\t" \
  "s_mov_b64 vcc, s[40:41]\n\t" \
  "v_mul_f32 v14, -v12, v13 div:2\n\t" \
  "v_cndmask_b32_sdwa v15, v15, v13, vcc dst_sel:DWORD dst_unused:UNUSED_PAD src0_sel:WORD_1 src1_sel:DWORD\n\t" \
  "v_mfma_f32_16x16x32_bf16 v[148:151], v[232:235], v[128:131], v[148:151]\n\t" \
  "s_or_b64 vcc, s[52:53], s[42:43]\n\t" \
  "v_and_b32 v12, 0xffff0000, v14\n\t" \
  "v_cndmask_b32_dpp v184, v15, v240, vcc row_ror:8 row_mask:0xf bank_mask:0xf\n\t" \
  "v_cndmask_b32 v180, v15, v240, vcc\n\t" \
  "s_mov_b64 vcc, s[52:53]\n\t" \
  "v_sub_f32 v14, v14, v12\n\t" \
  "v_perm_b32 v14, v12, v14, s55\n\t" \
  "v_cndmask_b32 v182, v14, v240, vcc\n\t" \
  "v_mfma_f32_16x16x32_bf16 v[156:159], v[232:235], v[132:135], v[156:159]\n\t" \
  "v_cndmask_b32_dpp v186, v14, v240, vcc row_ror:8 row_mask:0xf bank_mask:0xf\n\t" \
  "s_orn2_b64 vcc, s[52:53], s[42:43]\n\t" \
  "v_cndmask_b32 v181, v15, v240, vcc\n\t" \
  "v_mfma_f32_16x16x16_bf16 v[0:3], v[196:197], v[182:183], 0\n\t" \
  "v_cndmask_b32_dpp v185, v15, v240, vcc row_ror:8 row_mask:0xf bank_mask:0xf\n\t" \
  "v_fma_f32 v147, |v147|, v221, v147 mul:4\n\t" \
  "v_mfma_f32_16x16x16_bf16 v[4:7], v[196:197], v[186:187], 0\n\t" \
  "s_mov_b64 vcc, 0\n\t" \
  "v_fma_f32 v146, |v146|, v220, v146 mul:4\n\t" \
  "v_fma_f32 v145, |v145|, v223, v145 mul:4\n\t" \
  "v_mfma_f32_16x16x16_bf16 v[188:191], v[24:25], v[180:181], v[0:3]\n\t" \
  "v_cvt_pk_bf16_f32 v147, v146, v147\n\t" \
  "v_fma_f32 v144, |v144|, v222, v144 mul:4\n\t" \
  "v_cvt_pk_bf16_f32 v146, v144, v145\n\t" \
  "v_mfma_f32_16x16x16_bf16 v[176:179], v[26:27], v[180:181], v[0:3]\n\t" \
  "s_nop 3\n\t" \
  "v_cvt_pk_bf16_f32 v105, v190, v191\n\t" \
  "v_mfma_f32_16x16x16_bf16 v[200:203], v[76:77], v[184:185], v[4:7]\n\t" \
  "s_nop 1\n\t" \
  "v_cvt_pk_bf16_f32 v107, v178, v179\n\t" \
  "v_mfma_f32_16x16x16_bf16 v[204:207], v[78:79], v[184:185], v[4:7]\n\t" \
  "s_setprio 0\n\t" \
  "v_cvt_pk_bf16_f32 v106, v176, v177\n\t" \
  "v_cvt_pk_bf16_f32 v104, v188, v189\n\t" \
  "s_nop 0\n\t" \
  "v_cvt_pk_bf16_f32 v108, v200, v201\n\t" \
  "v_mfma_f32_16x16x16_bf16 v[196:199], v[28:29], v[180:181], v[0:3]\n\t" \
  "buffer_store_dwordx4 v[60:63], v250, s[20:23], s28 offen offset:64 sc1\n\t" \
  "v_mfma_f32_16x16x16_bf16 v[188:191], v[30:31], v[180:181], v[0:3]\n\t" \
  "s_setprio 0\n\t" \
  "v_cvt_pk_bf16_f32 v110, v204, v205\n\t" \
  "v_cvt_pk_bf16_f32 v109, v202, v203\n\t" \
  "v_mfma_f32_16x16x32_bf16 v[160:163], v[208:211], v[104:107], 0\n\t" \
  "s_nop 1\n\t" \
  "v_cvt_pk_bf16_f32 v120, v196, v197\n\t" \
  "v_cvt_pk_bf16_f32 v121, v198, v199\n\t" \
  "v_mfma_f32_16x16x16_bf16 v[196:199], v[34:35], v[180:181], v[0:3]\n\t" \
  "v_cvt_pknorm_i16_f32 v230, |v150|, |v151|\n\t" \
  "v_mfma_f32_16x16x16_bf16 v[176:179], v[84:85], v[184:185], v[4:7]\n\t" \
  "v_cvt_pk_bf16_f32 v122, v188, v189\n\t" \
  "v_cvt_pk_bf16_f32 v123, v190, v191\n\t" \
  "v_cvt_pk_bf16_f32 v111, v206, v207\n\t" \
  "v_mfma_f32_16x16x16_bf16 v[200:203], v[86:87], v[184:185], v[4:7]\n\t" \
  "v_cvt_pknorm_i16_f32 v228, |v148|, |v149|\n\t" \
  "v_lshlrev_b32_sdwa v229, 2, v228 dst_sel:DWORD dst_unused:UNUSED_PAD src0_sel:DWORD src1_sel:BYTE_3\n\t" \
  "v_mfma_f32_16x16x32_bf16 v[168:171], v[208:211], v[108:111], 0\n\t" \
  "v_lshlrev_b32_sdwa v228, 2, v228 dst_sel:DWORD dst_unused:UNUSED_PAD src0_sel:DWORD src1_sel:BYTE_1\n\t" \
  "ds_read_b64_tr_b16 v[210:211], v248 offset:44584\n\t" \
  "ds_read_b64_tr_b16 v[208:209], v248 offset:43560\n\t" \
  "v_cvt_pk_bf16_f32 v124, v176, v177\n\t" \
  "v_mfma_f32_16x16x16_bf16 v[204:207], v[32:33], v[180:181], v[0:3]\n\t" \
  "v_cvt_pk_bf16_f32 v126, v200, v201\n\t" \
  "v_cvt_pk_bf16_f32 v127, v202, v203\n\t" \
  "v_cvt_pk_bf16_f32 v139, v198, v199\n\t" \
  "v_lshlrev_b32_sdwa v231, 2, v230 dst_sel:DWORD dst_unused:UNUSED_PAD src0_sel:DWORD src1_sel:BYTE_3\n\t" \
  "v_mfma_f32_16x16x32_bf16 v[160:163], v[212:215], v[120:123], v[160:163]\n\t" \
  "ds_read_b32 v229, v229 offset:10016\n\t" \
  "s_nop 1\n\t" \
  "v_cvt_pk_bf16_f32 v136, v204, v205\n\t" \
  "v_cvt_pk_bf16_f32 v137, v206, v207\n\t" \
  "v_mfma_f32_16x16x16_bf16 v[188:191], v[92:93], v[184:185], v[4:7]\n\t" \
  "ds_read_b32 v228, v228 offset:10016\n\t" \
  "v_cvt_pk_bf16_f32 v125, v178, v179\n\t" \
  "v_mfma_f32_16x16x16_bf16 v[176:179], v[94:95], v[184:185], v[4:7]\n\t" \
  "v_lshlrev_b32_sdwa v230, 2, v230 dst_sel:DWORD dst_unused:UNUSED_PAD src0_sel:DWORD src1_sel:BYTE_1\n\t" \
  "v_cvt_pk_bf16_f32 v138, v196, v197\n\t" \
  "v_lshlrev_b32_sdwa v233, 1, v157 dst_sel:DWORD dst_unused:UNUSED_PAD src0_sel:DWORD src1_sel:WORD_1\n\t" \
  "v_mfma_f32_16x16x32_bf16 v[168:171], v[212:215], v[124:127], v[168:171]\n\t" \
  "ds_read_b64_tr_b16 v[212:213], v248 offset:46632\n\t" \
  "ds_read_b64_tr_b16 v[214:215], v248 offset:47656\n\t" \
  "v_cvt_pk_bf16_f32 v141, v190, v191\n\t" \
  "v_cvt_pk_bf16_f32 v140, v188, v189\n\t" \
  "v_mfma_f32_16x16x32_bf16 v[160:163], v[216:219], v[136:139], v[160:163]\n\t" \
  "v_cvt_pk_bf16_f32 v143, v178, v179\n\t" \
  "v_cvt_pk_bf16_f32 v142, v176, v177\n\t" \
  "s_nop 1\n\t" \
  "v_mfma_f32_16x16x32_bf16 v[168:171], v[216:219], v[140:143], v[168:171]\n\t" \
  "ds_read_b64_tr_b16 v[218:219], v248 offset:48680\n\t" \
  "ds_read_b64_tr_b16 v[216:217], v248 offset:45608\n\t" \
  "v_lshlrev_b32_sdwa v232, 1, v156 dst_sel:DWORD dst_unused:UNUSED_PAD src0_sel:DWORD src1_sel:WORD_1\n\t" \
  "v_lshlrev_b32_sdwa v235, 1, v159 dst_sel:DWORD dst_unused:UNUSED_PAD src0_sel:DWORD src1_sel:WORD_1\n\t" \
  "s_waitcnt lgkmcnt(6)\n\t" \
  "s_waitcnt vmcnt(4)\n\t" \
  "s_mov_b64 exec, 3\n\t" \
  "ds_add_rtn_u32 v246, v251, v250 offset:47136\n\t" \
  "s_mov_b64 exec, -1\n\t" \
  "v_mfma_f32_16x16x32_bf16 v[164:167], v[208:211], v[104:107], 0\n\t" \
  "v_lshlrev_b32_sdwa v234, 1, v158 dst_sel:DWORD dst_unused:UNUSED_PAD src0_sel:DWORD src1_sel:WORD_1\n\t" \
  "v_mfma_f32_16x16x32_bf16 v[172:175], v[208:211], v[108:111], 0\n\t" \
  "ds_read_b64_tr_b16 v[210:211], v248 offset:50216\n\t" \
  "ds_read_b64_tr_b16 v[208:209], v248 offset:49192\n\t" \
  "s_waitcnt lgkmcnt(5)\n\t" \
  "v_mfma_f32_16x16x32_bf16 v[172:175], v[212:215], v[124:127], v[172:175]\n\t" \
  "ds_read_b32 v231, v231 offset:10016\n\t" \
  "v_mfma_f32_16x16x32_bf16 v[164:167], v[212:215], v[120:123], v[164:167]\n\t" \
  "ds_read_b64_tr_b16 v[212:213], v248 offset:52264\n\t" \
  "ds_read_b64_tr_b16 v[214:215], v248 offset:53288\n\t" \
  "s_waitcnt lgkmcnt(6)\n\t" \
  "v_mfma_f32_16x16x32_bf16 v[164:167], v[216:219], v[136:139], v[164:167]\n\t" \
  "ds_read_b32 v230, v230 offset:10016\n\t" \
  "v_mfma_f32_16x16x32_bf16 v[172:175], v[216:219], v[140:143], v[172:175]\n\t" \
  "ds_read_b64_tr_b16 v[216:217], v248 offset:51240\n\t" \
  "ds_read_u16_d16_hi v233, v233 offset:10528\n\t" \
  "s_setprio 1\n\t" \
  "v_mfma_f32_4x4x4_16b_bf16 v[0:3], v[242:243], v[36:37], 0\n\t" \
  "ds_read_b64_tr_b16 v[218:219], v248 offset:54312\n\t" \
  "v_mfma_f32_4x4x4_16b_bf16 v[16:19], v[242:243], v[44:45], 0\n\t" \
  "v_mfma_f32_4x4x4_16b_bf16 v[4:7], v[242:243], v[38:39], 0\n\t" \
  "v_cvt_pknorm_i16_f32 v222, |v160|, |v161|\n\t" \
  "buffer_store_dwordx4 v[68:71], v250, s[20:23], s28 offen offset:128 sc1\n\t" \
  "v_mfma_f32_4x4x4_16b_bf16 v[12:15], v[242:243], v[42:43], 0\n\t" \
  "v_lshlrev_b32_sdwa v223, 2, v222 dst_sel:DWORD dst_unused:UNUSED_PAD src0_sel:DWORD src1_sel:BYTE_3\n\t" \
  "v_mfma_f32_4x4x4_16b_bf16 v[20:23], v[242:243], v[46:47], 0\n\t" \
  "v_cvt_pknorm_i16_f32 v220, |v162|, |v163|\n\t" \
  "s_setprio 0\n\t" \
  "v_lshlrev_b32_sdwa v222, 2, v222 dst_sel:DWORD dst_unused:UNUSED_PAD src0_sel:DWORD src1_sel:BYTE_1\n\t" \
  "v_mfma_f32_4x4x4_16b_bf16 v[8:11], v[242:243], v[40:41], 0\n\t" \
  "v_lshlrev_b32_sdwa v221, 2, v220 dst_sel:DWORD dst_unused:UNUSED_PAD src0_sel:DWORD src1_sel:BYTE_3\n\t" \
  "v_lshlrev_b32_sdwa v220, 2, v220 dst_sel:DWORD dst_unused:UNUSED_PAD src0_sel:DWORD src1_sel:BYTE_1\n\t" \
  "ds_read_u16 v232, v232 offset:10528\n\t" \
  "ds_read_u16 v234, v234 offset:10528\n\t" \
  "v_mfma_f32_4x4x4_16b_bf16 v[28:31], v[242:243], v[74:75], 0\n\t" \
  "v_lshlrev_b32_sdwa v224, 1, v170 dst_sel:DWORD dst_unused:UNUSED_PAD src0_sel:DWORD src1_sel:WORD_1\n\t" \
  "ds_read_u16_d16_hi v235, v235 offset:10528\n\t" \
  "v_lshlrev_b32_sdwa v225, 1, v171 dst_sel:DWORD dst_unused:UNUSED_PAD src0_sel:DWORD src1_sel:WORD_1\n\t" \
  "v_mfma_f32_4x4x4_16b_bf16 v[32:35], v[242:243], v[80:81], 0\n\t" \
  "v_lshlrev_b32_sdwa v226, 1, v168 dst_sel:DWORD dst_unused:UNUSED_PAD src0_sel:DWORD src1_sel:WORD_1\n\t" \
  "v_mfma_f32_4x4x4_16b_bf16 v[24:27], v[242:243], v[72:73], 0\n\t" \
  "s_setprio 0\n\t" \
  "v_lshlrev_b32_sdwa v227, 1, v169 dst_sel:DWORD dst_unused:UNUSED_PAD src0_sel:DWORD src1_sel:WORD_1\n\t" \
  "ds_read_b32 v221, v221 offset:10016\n\t" \
  "s_waitcnt lgkmcnt(7)\n\t" \
  "ds_read_b32 v220, v220 offset:10016\n\t" \
  "v_mfma_f32_4x4x4_16b_bf16 v[44:47], v[242:243], v[90:91], 0\n\t" \
  "v_mfma_f32_4x4x4_16b_bf16 v[76:79], v[242:243], v[50:51], 0\n\t" \
  "v_fma_mix_f32 v228, |v148|, v228, v228 op_sel:[0,1,0] op_sel_hi:[0,1,1] clamp\n\t" \
  "v_mfma_f32_4x4x4_16b_bf16 v[72:75], v[242:243], v[48:49], 0\n\t" \
  "ds_read_b32 v223, v223 offset:10016\n\t" \
  "v_mfma_f32_4x4x4_16b_bf16 v[40:43], v[242:243], v[88:89], 0\n\t" \
  "v_mfma_f32_4x4x4_16b_bf16 v[36:39], v[242:243], v[82:83], 0\n\t" \
  "v_fma_mix_f32 v230, |v150|, v230, v230 op_sel:[0,1,0] op_sel_hi:[0,1,1] clamp\n\t" \
  "v_fma_mix_f32 v231, |v151|, v231, v231 op_sel:[0,1,0] op_sel_hi:[0,1,1] clamp\n\t" \
  "ds_read_b32 v222, v222 offset:10016\n\t" \
  "s_waitcnt lgkmcnt(4)\n\t" \
  "v_mfma_f32_4x4x4_16b_bf16 v[80:83], v[242:243], v[56:57], 0\n\t" \
  "v_mfma_f32_4x4x4_16b_bf16 v[84:87], v[242:243], v[58:59], 0\n\t" \
  "v_or_b32 v156, v232, v233\n\t" \
  "v_or_b32 v157, v234, v235\n\t" \
  "ds_read_u16_d16_hi v225, v225 offset:10528\n\t" \
  "v_fma_mix_f32 v229, |v149|, v229, v229 op_sel:[0,1,0] op_sel_hi:[0,1,1] clamp\n\t" \
  "v_mfma_f32_4x4x4_16b_bf16 v[92:95], v[242:243], v[238:239], 0\n\t" \
  "v_mfma_f32_4x4x4_16b_bf16 v[88:91], v[242:243], v[236:237], 0\n\t" \
  "v_fma_f32 v149, |v149|, v229, v149 mul:4\n\t" \
  "ds_read_u16 v224, v224 offset:10528\n\t" \
  "v_fma_f32 v148, |v148|, v228, v148 mul:4\n\t" \
  "v_mfma_f32_16x16x32_bf16 v[176:179], v[208:211], v[96:99], 0\n\t" \
  "v_fma_f32 v151, |v151|, v231, v151 mul:4\n\t" \
  "v_fma_f32 v150, |v150|, v230, v150 mul:4\n\t" \
  "ds_read_u16_d16_hi v227, v227 offset:10528\n\t" \
  "v_mfma_f32_16x16x32_bf16 v[184:187], v[208:211], v[100:103], 0\n\t" \
  "v_cvt_pk_bf16_f32 v148, v148, v149\n\t" \
  "v_cvt_pk_bf16_f32 v149, v150, v151\n\t" \
  "ds_read_u16 v226, v226 offset:10528\n\t" \
  "v_mfma_f32_16x16x32_bf16 v[192:195], v[208:211], v[104:107], 0\n\t" \
  "v_cvt_pknorm_i16_f32 v228, |v164|, |v165|\n\t" \
  "v_lshlrev_b32_sdwa v235, 1, v175 dst_sel:DWORD dst_unused:UNUSED_PAD src0_sel:DWORD src1_sel:WORD_1\n\t" \
  "v_cvt_pknorm_i16_f32 v230, |v166|, |v167|\n\t" \
  "v_mfma_f32_16x16x32_bf16 v[200:203], v[208:211], v[108:111], 0\n\t" \
  "s_setprio 3\n\t" \
  "v_lshlrev_b32_sdwa v229, 2, v228 dst_sel:DWORD dst_unused:UNUSED_PAD src0_sel:DWORD src1_sel:BYTE_3\n\t" \
  "ds_read_b32 v229, v229 offset:10016\n\t" \
  "v_mfma_f32_16x16x32_bf16 v[192:195], v[212:215], v[120:123], v[192:195]\n\t" \
  "v_lshlrev_b32_sdwa v228, 2, v228 dst_sel:DWORD dst_unused:UNUSED_PAD src0_sel:DWORD src1_sel:BYTE_1\n\t" \
  "v_lshlrev_b32_sdwa v234, 1, v174 dst_sel:DWORD dst_unused:UNUSED_PAD src0_sel:DWORD src1_sel:WORD_1\n\t" \
  "ds_read_b64_tr_b16 v[208:209], v248 offset:49704\n\t" \
  "v_mfma_f32_16x16x32_bf16 v[176:179], v[212:215], v[112:115], v[176:179]\n\t" \
  "v_lshlrev_b32_sdwa v231, 2, v230 dst_sel:DWORD dst_unused:UNUSED_PAD src0_sel:DWORD src1_sel:BYTE_3\n\t" \
  "v_lshlrev_b32_sdwa v230, 2, v230 dst_sel:DWORD dst_unused:UNUSED_PAD src0_sel:DWORD src1_sel:BYTE_1\n\t" \
  "ds_read_b64_tr_b16 v[210:211], v248 offset:50728\n\t" \
  "v_mfma_f32_16x16x32_bf16 v[184:187], v[212:215], v[116:119], v[184:187]\n\t" \
  "v_lshlrev_b32_sdwa v232, 1, v172 dst_sel:DWORD dst_unused:UNUSED_PAD src0_sel:DWORD src1_sel:WORD_1\n\t" \
  "v_lshlrev_b32_sdwa v233, 1, v173 dst_sel:DWORD dst_unused:UNUSED_PAD src0_sel:DWORD src1_sel:WORD_1\n\t" \
  "ds_read_b32 v228, v228 offset:10016\n\t" \
  "v_mfma_f32_16x16x32_bf16 v[200:203], v[212:215], v[124:127], v[200:203]\n\t" \
  "s_waitcnt lgkmcnt(8)\n\t" \
  "v_fma_mix_f32 v221, |v163|, v221, v221 op_sel:[0,1,0] op_sel_hi:[0,1,1] clamp\n\t" \
  "v_fma_mix_f32 v220, |v162|, v220, v220 op_sel:[0,1,0] op_sel_hi:[0,1,1] clamp\n\t" \
  "ds_read_b32 v231, v231 offset:10016\n\t" \
  "ds_read_b64_tr_b16 v[212:213], v248 offset:52776\n\t" \
  "v_mfma_f32_16x16x32_bf16 v[176:179], v[216:219], v[128:131], v[176:179]\n\t" \
  "v_fma_mix_f32 v222, |v160|, v222, v222 op_sel:[0,1,0] op_sel_hi:[0,1,1] clamp\n\t" \
  "v_fma_mix_f32 v223, |v161|, v223, v223 op_sel:[0,1,0] op_sel_hi:[0,1,1] clamp\n\t" \
  "s_waitcnt lgkmcnt(6)\n\t" \
  "ds_read_b64_tr_b16 v[214:215], v248 offset:53800\n\t" \
  "ds_read_b32 v230, v230 offset:10016\n\t" \
  "v_mfma_f32_16x16x32_bf16 v[184:187], v[216:219], v[132:135], v[184:187]\n\t" \
  "v_or_b32 v171, v224, v225\n\t" \
  "v_or_b32 v170, v226, v227\n\t" \
  "s_setprio 0\n\t" \
  "s_setprio 0\n\t" \
  "ds_read_u16 v232, v232 offset:10528\n\t" \
  "v_mfma_f32_16x16x32_bf16 v[200:203], v[216:219], v[140:143], v[200:203]\n\t" \
  "v_fma_f32 v161, |v161|, v223, v161 mul:4\n\t" \
  "v_fma_f32 v160, |v160|, v222, v160 mul:4\n\t" \
  "ds_read_u16_d16_hi v233, v233 offset:10528\n\t" \
  "s_waitcnt lgkmcnt(7)\n\t" \
  "v_mfma_f32_16x16x32_bf16 v[192:195], v[216:219], v[136:139], v[192:195]\n\t" \
  "v_fma_f32 v163, |v163|, v221, v163 mul:4\n\t" \
  "v_fma_f32 v162, |v162|, v220, v162 mul:4\n\t" \
  "ds_read_b64_tr_b16 v[216:217], v248 offset:51752\n\t" \
  "v_mfma_f32_16x16x32_bf16 v[180:183], v[208:211], v[96:99], 0\n\t" \
  "s_waitcnt lgkmcnt(3)\n\t" \
  "v_cvt_pk_bf16_f32 v163, v162, v163\n\t" \
  "v_lshlrev_b32_sdwa v225, 1, v187 dst_sel:DWORD dst_unused:UNUSED_PAD src0_sel:DWORD src1_sel:WORD_1\n\t" \
  "s_waitcnt vmcnt(3)\n\t" \
  "ds_read_b64_tr_b16 v[218:219], v248 offset:54824\n\t" \
  "v_mfma_f32_16x16x32_bf16 v[188:191], v[208:211], v[100:103], 0\n\t" \
  "v_fma_mix_f32 v230, |v166|, v230, v230 op_sel:[0,1,0] op_sel_hi:[0,1,1] clamp\n\t" \
  "v_cvt_pknorm_i16_f32 v220, |v178|, |v179|\n\t" \
  "s_mov_b64 exec, 3\n\t" \
  "ds_add_rtn_u32 v246, v251, v250 offset:47144\n\t" \
  "v_mfma_f32_16x16x32_bf16 v[204:207], v[208:211], v[108:111], 0\n\t" \
  "s_mov_b64 exec, -1\n\t" \
  "v_cvt_pk_bf16_f32 v162, v160, v161\n\t" \
  "v_lshlrev_b32_sdwa v221, 2, v220 dst_sel:DWORD dst_unused:UNUSED_PAD src0_sel:DWORD src1_sel:BYTE_3\n\t" \
  "v_mfma_f32_16x16x32_bf16 v[196:199], v[208:211], v[104:107], 0\n\t" \
  "v_cvt_pknorm_i16_f32 v222, |v176|, |v177|\n\t" \
  "v_lshlrev_b32_sdwa v223, 2, v222 dst_sel:DWORD dst_unused:UNUSED_PAD src0_sel:DWORD src1_sel:BYTE_3\n\t" \
  "v_lshlrev_b32_sdwa v222, 2, v222 dst_sel:DWORD dst_unused:UNUSED_PAD src0_sel:DWORD src1_sel:BYTE_1\n\t" \
  "s_waitcnt lgkmcnt(0)\n\t" \
  "v_readfirstlane_b32 s4, v246\n\t" \
  "ds_read_u16_d16_hi v235, v235 offset:10528\n\t" \
  "ds_read_u16 v234, v234 offset:10528\n\t" \
  "v_mfma_f32_16x16x32_bf16 v[180:183], v[212:215], v[112:115], v[180:183]\n\t" \
  "s_cmpk_lt_u32 s4, 6144\n\t" \
  "v_lshlrev_b32_sdwa v220, 2, v220 dst_sel:DWORD dst_unused:UNUSED_PAD src0_sel:DWORD src1_sel:BYTE_1\n\t" \
  "s_cbranch_scc1 .Lcnx_w1_2\n\t" \
  ".Lcnx_g1_2:\n\t" \
  "ds_read_b64_tr_b16 v[210:211], v249 offset:26136\n\t" \
  "ds_read_b64_tr_b16 v[208:209], v249 offset:24600\n\t" \
  "v_mfma_f32_16x16x32_bf16 v[196:199], v[212:215], v[120:123], v[196:199]\n\t" \
  "v_lshlrev_b32_sdwa v226, 1, v184 dst_sel:DWORD dst_unused:UNUSED_PAD src0_sel:DWORD src1_sel:WORD_1\n\t" \
  "v_lshlrev_b32_sdwa v227, 1, v185 dst_sel:DWORD dst_unused:UNUSED_PAD src0_sel:DWORD src1_sel:WORD_1\n\t" \
  "v_mfma_f32_16x16x32_bf16 v[188:191], v[212:215], v[116:119], v[188:191]\n\t" \
  "v_fma_mix_f32 v228, |v164|, v228, v228 op_sel:[0,1,0] op_sel_hi:[0,1,1] clamp\n\t" \
  "v_lshlrev_b32_sdwa v224, 1, v186 dst_sel:DWORD dst_unused:UNUSED_PAD src0_sel:DWORD src1_sel:WORD_1\n\t" \
  "s_mov_b32 m0, s46\n\t" \
  "ds_read_b32 v220, v220 offset:10016\n\t" \
  "v_mfma_f32_16x16x32_bf16 v[204:207], v[212:215], v[124:127], v[204:207]\n\t" \
  "v_fma_mix_f32 v229, |v165|, v229, v229 op_sel:[0,1,0] op_sel_hi:[0,1,1] clamp\n\t" \
  "ds_read_b32 v221, v221 offset:10016\n\t" \
  "ds_read_b64_tr_b16 v[212:213], v249 offset:24608\n\t" \
  "v_mfma_f32_16x16x32_bf16 v[180:183], v[216:219], v[128:131], v[180:183]\n\t" \
  "s_mov_b32 exec_hi, 0xffff\n\t" \
  "buffer_load_dwordx4 v252, s[16:19], s33 offen lds\n\t" \
  "v_mfma_f32_16x16x32_bf16 v[188:191], v[216:219], v[132:135], v[188:191]\n\t" \
  "s_mov_b32 exec_hi, -1\n\t" \
  "v_fma_mix_f32 v231, |v167|, v231, v231 op_sel:[0,1,0] op_sel_hi:[0,1,1] clamp\n\t" \
  "v_or_b32 v172, v232, v233\n\t" \
  "ds_read_b32 v223, v223 offset:10016\n\t" \
  "ds_read_b64_tr_b16 v[214:215], v249 offset:26144\n\t" \
  "v_mfma_f32_16x16x32_bf16 v[196:199], v[216:219], v[136:139], v[196:199]\n\t" \
  "s_add_u32 s33, s33, s32\n\t" \
  "v_fma_f32 v167, |v167|, v231, v167 mul:4\n\t" \
  "v_fma_f32 v164, |v164|, v228, v164 mul:4\n\t" \
  "ds_read_b32 v222, v222 offset:10016\n\t" \
  "s_waitcnt lgkmcnt(8)\n\t" \
  "v_mfma_f32_16x16x32_bf16 v[204:207], v[216:219], v[140:143], v[204:207]\n\t" \
  "v_fma_f32 v166, |v166|, v230, v166 mul:4\n\t" \
  "v_or_b32 v173, v234, v235\n\t" \
  "v_fma_f32 v165, |v165|, v229, v165 mul:4\n\t" \
  "v_cvt_pk_bf16_f32 v164, v164, v165\n\t" \
  "s_waitcnt lgkmcnt(6)\n\t" \
  "v_cvt_pk_bf16_f32 v165, v166, v167\n\t" \
  "ds_read_u16_d16_hi v225, v225 offset:10528\n\t" \
  "ds_read_b64_tr_b16 v[216:217], v249 offset:24664\n\t" \
  "v_mfma_f32_16x16x32_bf16 v[0:3], v[208:211], v[146:149], v[0:3]\n\t" \
  "v_cvt_pknorm_i16_f32 v228, |v194|, |v195|\n\t" \
  "v_cvt_pknorm_i16_f32 v230, |v192|, |v193|\n\t" \
  "ds_read_u16 v224, v224 offset:10528\n\t" \
  "ds_read_b64_tr_b16 v[218:219], v249 offset:26200\n\t" \
  "v_mfma_f32_16x16x32_bf16 v[48:51], v[208:211], v[162:165], 0\n\t" \
  "v_lshlrev_b32_sdwa v231, 2, v230 dst_sel:DWORD dst_unused:UNUSED_PAD src0_sel:DWORD src1_sel:BYTE_3\n\t" \
  "v_lshlrev_b32_sdwa v230, 2, v230 dst_sel:DWORD dst_unused:UNUSED_PAD src0_sel:DWORD src1_sel:BYTE_1\n\t" \
  "v_lshlrev_b32_sdwa v234, 1, v200 dst_sel:DWORD dst_unused:UNUSED_PAD src0_sel:DWORD src1_sel:WORD_1\n\t" \
  "v_mfma_f32_16x16x32_bf16 v[24:27], v[208:211], v[154:157], v[24:27]\n\t" \
  "v_lshlrev_b32_sdwa v229, 2, v228 dst_sel:DWORD dst_unused:UNUSED_PAD src0_sel:DWORD src1_sel:BYTE_3\n\t" \
  "v_lshlrev_b32_sdwa v228, 2, v228 dst_sel:DWORD dst_unused:UNUSED_PAD src0_sel:DWORD src1_sel:BYTE_1\n\t" \
  "ds_read_u16_d16_hi v227, v227 offset:10528\n\t" \
  "s_add_u32 m0, s46, s35\n\t" \
  "v_mfma_f32_16x16x32_bf16 v[72:75], v[208:211], v[170:173], v[72:75]\n\t" \
  "v_lshlrev_b32_sdwa v233, 1, v203 dst_sel:DWORD dst_unused:UNUSED_PAD src0_sel:DWORD src1_sel:WORD_1\n\t" \
  "s_waitcnt lgkmcnt(6)\n\t" \
  "s_mov_b32 exec_hi, 0xffff\n\t" \
  "buffer_load_dwordx4 v252, s[16:19], s34 offen lds\n\t" \
  "v_mfma_f32_16x16x32_bf16 v[4:7], v[212:215], v[146:149], v[4:7]\n\t" \
  "s_mov_b32 exec_hi, -1\n\t" \
  "v_lshlrev_b32_sdwa v232, 1, v202 dst_sel:DWORD dst_unused:UNUSED_PAD src0_sel:DWORD src1_sel:WORD_1\n\t" \
  "v_lshlrev_b32_sdwa v235, 1, v201 dst_sel:DWORD dst_unused:UNUSED_PAD src0_sel:DWORD src1_sel:WORD_1\n\t" \
  "s_add_u32 s34, s34, s32\n\t" \
  "ds_read_b64_tr_b16 v[210:211], v249 offset:26208\n\t" \
  "ds_read_u16 v226, v226 offset:10528\n\t" \
  "v_mfma_f32_16x16x32_bf16 v[28:31], v[212:215], v[154:157], v[28:31]\n\t" \
  "s_waitcnt lgkmcnt(7)\n\t" \
  "ds_read_b64_tr_b16 v[208:209], v249 offset:24672\n\t" \
  "ds_read_b32 v229, v229 offset:10016\n\t" \
  "v_mfma_f32_16x16x32_bf16 v[52:55], v[212:215], v[162:165], 0\n\t" \
  "v_fma_mix_f32 v220, |v178|, v220, v220 op_sel:[0,1,0] op_sel_hi:[0,1,1] clamp\n\t" \
  "v_fma_mix_f32 v221, |v179|, v221, v221 op_sel:[0,1,0] op_sel_hi:[0,1,1] clamp\n\t" \
  "ds_read_b32 v228, v228 offset:10016\n\t" \
  "ds_read_b32 v231, v231 offset:10016\n\t" \
  "v_mfma_f32_16x16x32_bf16 v[76:79], v[212:215], v[170:173], v[76:79]\n\t" \
  "v_fma_mix_f32 v223, |v177|, v223, v223 op_sel:[0,1,0] op_sel_hi:[0,1,1] clamp\n\t" \
  "v_fma_mix_f32 v222, |v176|, v222, v222 op_sel:[0,1,0] op_sel_hi:[0,1,1] clamp\n\t" \
  "s_setprio 1\n\t" \
  "s_waitcnt lgkmcnt(4)\n\t" \
  "ds_read_b32 v230, v230 offset:10016\n\t" \
  "ds_read_b64_tr_b16 v[212:213], v249 offset:24728\n\t" \
  "v_mfma_f32_16x16x32_bf16 v[8:11], v[216:219], v[146:149], v[8:11]\n\t" \
  "v_or_b32 v187, v224, v225\n\t" \
  "v_or_b32 v186, v226, v227\n\t" \
  "ds_read_b64_tr_b16 v[214:215], v249 offset:26264\n\t" \
  "v_mfma_f32_16x16x32_bf16 v[56:59], v[216:219], v[162:165], 0\n\t" \
  "v_fma_f32 v179, |v179|, v221, v179 mul:4\n\t" \
  "v_fma_f32 v178, |v178|, v220, v178 mul:4\n\t" \
  "ds_read_u16_d16_hi v233, v233 offset:10528\n\t" \
  "v_mfma_f32_16x16x32_bf16 v[80:83], v[216:219], v[170:173], v[80:83]\n\t" \
  "v_fma_f32 v177, |v177|, v223, v177 mul:4\n\t" \
  "v_fma_f32 v176, |v176|, v222, v176 mul:4\n\t" \
  "ds_read_u16 v232, v232 offset:10528\n\t" \
  "v_mfma_f32_16x16x32_bf16 v[32:35], v[216:219], v[154:157], v[32:35]\n\t" \
  "v_cvt_pknorm_i16_f32 v222, |v182|, |v183|\n\t" \
  "v_cvt_pk_bf16_f32 v179, v178, v179\n\t" \
  "ds_read_b64_tr_b16 v[218:219], v249 offset:26272\n\t" \
  "s_waitcnt lgkmcnt(9)\n\t" \
  "v_mfma_f32_16x16x32_bf16 v[60:63], v[208:211], v[162:165], 0\n\t" \
  "v_lshlrev_b32_sdwa v223, 2, v222 dst_sel:DWORD dst_unused:UNUSED_PAD src0_sel:DWORD src1_sel:BYTE_3\n\t" \
  "v_cvt_pk_bf16_f32 v178, v176, v177\n\t" \
  "ds_read_b64_tr_b16 v[216:217], v249 offset:24736\n\t" \
  "v_mfma_f32_16x16x32_bf16 v[12:15], v[208:211], v[146:149], v[12:15]\n\t" \
  "v_lshlrev_b32_sdwa v222, 2, v222 dst_sel:DWORD dst_unused:UNUSED_PAD src0_sel:DWORD src1_sel:BYTE_1\n\t" \
  "v_cvt_pknorm_i16_f32 v220, |v180|, |v181|\n\t" \
  "ds_read_u16 v234, v234 offset:10528\n\t" \
  "s_waitcnt lgkmcnt(5)\n\t" \
  "v_mfma_f32_16x16x32_bf16 v[36:39], v[208:211], v[154:157], v[36:39]\n\t" \
  "v_lshlrev_b32_sdwa v221, 2, v220 dst_sel:DWORD dst_unused:UNUSED_PAD src0_sel:DWORD src1_sel:BYTE_3\n\t" \
  "v_lshlrev_b32_sdwa v220, 2, v220 dst_sel:DWORD dst_unused:UNUSED_PAD src0_sel:DWORD src1_sel:BYTE_1\n\t" \
  "ds_read_u16_d16_hi v235, v235 offset:10528\n\t" \
  "v_mfma_f32_16x16x32_bf16 v[84:87], v[208:211], v[170:173], v[84:87]\n\t" \
  "v_lshlrev_b32_sdwa v225, 1, v189 dst_sel:DWORD dst_unused:UNUSED_PAD src0_sel:DWORD src1_sel:WORD_1\n\t" \
  "v_lshlrev_b32_sdwa v224, 1, v188 dst_sel:DWORD dst_unused:UNUSED_PAD src0_sel:DWORD src1_sel:WORD_1\n\t" \
  "ds_read_b64_tr_b16 v[208:209], v248 offset:36904\n\t" \
  "v_mfma_f32_16x16x32_bf16 v[16:19], v[212:215], v[146:149], v[16:19]\n\t" \
  "v_lshlrev_b32_sdwa v227, 1, v191 dst_sel:DWORD dst_unused:UNUSED_PAD src0_sel:DWORD src1_sel:WORD_1\n\t" \
  "v_fma_mix_f32 v229, |v195|, v229, v229 op_sel:[0,1,0] op_sel_hi:[0,1,1] clamp\n\t" \
  "ds_read_b32 v221, v221 offset:10016\n\t" \
  "ds_read_b64_tr_b16 v[210:211], v248 offset:37928\n\t" \
  "v_mfma_f32_16x16x32_bf16 v[40:43], v[212:215], v[154:157], v[40:43]\n\t" \
  "v_lshlrev_b32_sdwa v226, 1, v190 dst_sel:DWORD dst_unused:UNUSED_PAD src0_sel:DWORD src1_sel:WORD_1\n\t" \
  "ds_read_b32 v222, v222 offset:10016\n\t" \
  "ds_read_b32 v223, v223 offset:10016\n\t" \
  "v_mfma_f32_16x16x32_bf16 v[88:91], v[212:215], v[170:173], v[88:91]\n\t" \
  "v_fma_mix_f32 v228, |v194|, v228, v228 op_sel:[0,1,0] op_sel_hi:[0,1,1] clamp\n\t" \
  "ds_read_b32 v220, v220 offset:10016\n\t" \
  "v_mfma_f32_16x16x32_bf16 v[64:67], v[212:215], v[162:165], 0\n\t" \
  "s_waitcnt lgkmcnt(6)\n\t" \
  "v_fma_mix_f32 v230, |v192|, v230, v230 op_sel:[0,1,0] op_sel_hi:[0,1,1] clamp\n\t" \
  "v_fma_mix_f32 v231, |v193|, v231, v231 op_sel:[0,1,0] op_sel_hi:[0,1,1] clamp\n\t" \
  "ds_read_b64_tr_b16 v[212:213], v248 offset:39976\n\t" \
  "ds_read_u16_d16_hi v225, v225 offset:10528\n\t" \
  "v_mfma_f32_16x16x32_bf16 v[20:23], v[216:219], v[146:149], v[20:23]\n\t" \
  "v_or_b32 v203, v232, v233\n\t" \
  "v_or_b32 v202, v234, v235\n\t" \
  "ds_read_u16_d16_hi v227, v227 offset:10528\n\t" \
  "v_mfma_f32_16x16x32_bf16 v[44:47], v[216:219], v[154:157], v[44:47]\n\t" \
  "v_fma_f32 v195, |v195|, v229, v195 mul:4\n\t" \
  "v_fma_f32 v194, |v194|, v228, v194 mul:4\n\t" \
  "ds_read_b64_tr_b16 v[214:215], v248 offset:41000\n\t" \
  "s_setprio 3\n\t" \
  "v_mfma_f32_16x16x32_bf16 v[68:71], v[216:219], v[162:165], 0\n\t" \
  "v_fma_f32 v193, |v193|, v231, v193 mul:4\n\t" \
  "v_fma_f32 v192, |v192|, v230, v192 mul:4\n\t" \
  "ds_read_u16 v224, v224 offset:10528\n\t" \
  "v_mfma_f32_16x16x32_bf16 v[92:95], v[216:219], v[170:173], v[92:95]\n\t" \
  "v_cvt_pk_bf16_f32 v195, v194, v195\n\t" \
  "v_cvt_pk_bf16_f32 v194, v192, v193\n\t" \
  "s_waitcnt lgkmcnt(8)\n\t" \
  "ds_read_b64_tr_b16 v[216:217], v248 offset:38952\n\t" \
  "v_mfma_f32_16x16x32_bf16 v[144:147], v[208:211], v[96:99], 0\n\t" \
  "v_cvt_pknorm_i16_f32 v230, |v198|, |v199|\n\t" \
  "v_cvt_pknorm_i16_f32 v228, |v196|, |v197|\n\t" \
  "ds_read_b64_tr_b16 v[218:219], v248 offset:42024\n\t" \
  "v_mfma_f32_16x16x32_bf16 v[152:155], v[208:211], v[100:103], 0\n\t" \
  "v_lshlrev_b32_sdwa v229, 2, v228 dst_sel:DWORD dst_unused:UNUSED_PAD src0_sel:DWORD src1_sel:BYTE_3\n\t" \
  "v_lshlrev_b32_sdwa v228, 2, v228 dst_sel:DWORD dst_unused:UNUSED_PAD src0_sel:DWORD src1_sel:BYTE_1\n\t" \
  "ds_read_b32 v228, v228 offset:10016\n\t" \
  "s_setprio 0\n\t" \
  "v_mfma_f32_16x16x32_bf16 v[160:163], v[208:211], v[104:107], 0\n\t" \
  "v_lshlrev_b32_sdwa v231, 2, v230 dst_sel:DWORD dst_unused:UNUSED_PAD src0_sel:DWORD src1_sel:BYTE_3\n\t" \
  "v_lshlrev_b32_sdwa v230, 2, v230 dst_sel:DWORD dst_unused:UNUSED_PAD src0_sel:DWORD src1_sel:BYTE_1\n\t" \
  "ds_read_u16 v226, v226 offset:10528\n\t" \
  "ds_read_b32 v229, v229 offset:10016\n\t" \
  "v_mfma_f32_16x16x32_bf16 v[168:171], v[208:211], v[108:111], 0\n\t" \
  "v_lshlrev_b32_sdwa v234, 1, v206 dst_sel:DWORD dst_unused:UNUSED_PAD src0_sel:DWORD src1_sel:WORD_1\n\t" \
  "v_lshlrev_b32_sdwa v233, 1, v205 dst_sel:DWORD dst_unused:UNUSED_PAD src0_sel:DWORD src1_sel:WORD_1\n\t" \
  "ds_read_b64_tr_b16 v[210:211], v248 offset:38440\n\t" \
  "s_waitcnt lgkmcnt(7)\n\t" \
  "v_mfma_f32_16x16x32_bf16 v[152:155], v[212:215], v[116:119], v[152:155]\n\t" \
  "v_lshlrev_b32_sdwa v232, 1, v204 dst_sel:DWORD dst_unused:UNUSED_PAD src0_sel:DWORD src1_sel:WORD_1\n\t" \
  "v_lshlrev_b32_sdwa v235, 1, v207 dst_sel:DWORD dst_unused:UNUSED_PAD src0_sel:DWORD src1_sel:WORD_1\n\t" \
  "v_mfma_f32_16x16x32_bf16 v[144:147], v[212:215], v[112:115], v[144:147]\n\t" \
  "v_fma_mix_f32 v221, |v181|, v221, v221 op_sel:[0,1,0] op_sel_hi:[0,1,1] clamp\n\t" \
  "v_fma_mix_f32 v220, |v180|, v220, v220 op_sel:[0,1,0] op_sel_hi:[0,1,1] clamp\n\t" \
  "ds_read_b64_tr_b16 v[208:209], v248 offset:37416\n\t" \
  "ds_read_u16 v232, v232 offset:10528\n\t" \
  "v_mfma_f32_16x16x32_bf16 v[160:163], v[212:215], v[120:123], v[160:163]\n\t" \
  "s_waitcnt lgkmcnt(4)\n\t" \
  "v_fma_mix_f32 v223, |v183|, v223, v223 op_sel:[0,1,0] op_sel_hi:[0,1,1] clamp\n\t" \
  "ds_read_b32 v230, v230 offset:10016\n\t" \
  "ds_read_u16_d16_hi v233, v233 offset:10528\n\t" \
  "v_mfma_f32_16x16x32_bf16 v[168:171], v[212:215], v[124:127], v[168:171]\n\t" \
  "v_or_b32 v189, v226, v227\n\t" \
  "v_fma_mix_f32 v222, |v182|, v222, v222 op_sel:[0,1,0] op_sel_hi:[0,1,1] clamp\n\t" \
  "ds_read_b64_tr_b16 v[212:213], v248 offset:40488\n\t" \
  "v_mfma_f32_16x16x32_bf16 v[144:147], v[216:219], v[128:131], v[144:147]\n\t" \
  "v_or_b32 v188, v224, v225\n\t" \
  "v_fma_f32 v183, |v183|, v223, v183 mul:4\n\t" \
  "ds_read_b64_tr_b16 v[214:215], v248 offset:41512\n\t" \
  "v_mfma_f32_16x16x32_bf16 v[160:163], v[216:219], v[136:139], v[160:163]\n\t" \
  "v_fma_f32 v182, |v182|, v222, v182 mul:4\n\t" \
  "v_mfma_f32_16x16x32_bf16 v[152:155], v[216:219], v[132:135], v[152:155]\n\t" \
  "v_fma_f32 v181, |v181|, v221, v181 mul:4\n\t" \
  "v_fma_f32 v180, |v180|, v220, v180 mul:4\n\t" \
  "ds_read_b32 v231, v231 offset:10016\n\t" \
  "ds_read_u16_d16_hi v235, v235 offset:10528\n\t" \
  "v_mfma_f32_16x16x32_bf16 v[168:171], v[216:219], v[140:143], v[168:171]\n\t" \
  "s_waitcnt lgkmcnt(7)\n\t" \
  "v_cvt_pk_bf16_f32 v180, v180, v181\n\t" \
  "v_cvt_pk_bf16_f32 v181, v182, v183\n\t" \
  "ds_read_b64_tr_b16 v[216:217], v248 offset:39464\n\t" \
  "v_mfma_f32_16x16x32_bf16 v[148:151], v[208:211], v[96:99], 0\n\t" \
  "v_cvt_pknorm_i16_f32 v220, |v146|, |v147|\n\t" \
  "v_cvt_pknorm_i16_f32 v222, |v144|, |v145|\n\t" \
  "s_waitcnt vmcnt(0)\n\t" \
  "ds_read_b64_tr_b16 v[218:219], v248 offset:42536\n\t" \
  "s_waitcnt lgkmcnt(4)\n\t" \
  "v_mfma_f32_16x16x32_bf16 v[156:159], v[208:211], v[100:103], 0\n\t" \
  "v_lshlrev_b32_sdwa v221, 2, v220 dst_sel:DWORD dst_unused:UNUSED_PAD src0_sel:DWORD src1_sel:BYTE_3\n\t" \
  "v_lshlrev_b32_sdwa v220, 2, v220 dst_sel:DWORD dst_unused:UNUSED_PAD src0_sel:DWORD src1_sel:BYTE_1\n\t" \
  "s_mov_b64 exec, 3\n\t" \
  "ds_add_rtn_u32 v246, v251, v250 offset:47152\n\t" \
  "v_mfma_f32_16x16x32_bf16 v[164:167], v[208:211], v[104:107], 0\n\t" \
  "s_mov_b64 exec, -1\n\t" \
  "v_lshlrev_b32_sdwa v223, 2, v222 dst_sel:DWORD dst_unused:UNUSED_PAD src0_sel:DWORD src1_sel:BYTE_3\n\t" \
  "v_lshlrev_b32_sdwa v222, 2, v222 dst_sel:DWORD dst_unused:UNUSED_PAD src0_sel:DWORD src1_sel:BYTE_1\n\t" \
  "ds_read_u16 v234, v234 offset:10528\n\t" \
  "v_mfma_f32_16x16x32_bf16 v[156:159], v[212:215], v[116:119], v[156:159]\n\t" \
  "v_lshlrev_b32_sdwa v227, 1, v153 dst_sel:DWORD dst_unused:UNUSED_PAD src0_sel:DWORD src1_sel:WORD_1\n\t" \
  "v_lshlrev_b32_sdwa v226, 1, v152 dst_sel:DWORD dst_unused:UNUSED_PAD src0_sel:DWORD src1_sel:WORD_1\n\t" \
  "ds_read_b32 v223, v223 offset:10016\n\t" \
  "v_mfma_f32_16x16x32_bf16 v[172:175], v[208:211], v[108:111], 0\n\t" \
  "s_waitcnt lgkmcnt(2)\n\t" \
  "v_lshlrev_b32_sdwa v225, 1, v155 dst_sel:DWORD dst_unused:UNUSED_PAD src0_sel:DWORD src1_sel:WORD_1\n\t" \
  "v_readfirstlane_b32 s4, v246\n\t" \
  "ds_read_b32 v220, v220 offset:10016\n\t" \
  "ds_read_b32 v221, v221 offset:10016\n\t" \
  "v_mfma_f32_16x16x32_bf16 v[148:151], v[212:215], v[112:115], v[148:151]\n\t" \
  "s_cmpk_lt_u32 s4, 6144\n\t" \
  "v_lshlrev_b32_sdwa v224, 1, v154 dst_sel:DWORD dst_unused:UNUSED_PAD src0_sel:DWORD src1_sel:WORD_1\n\t" \
  "s_cbranch_scc1 .Lcnx_w1_3\n\t" \
  ".Lcnx_g1_3:\n\t" \
  "v_mfma_f32_16x16x32_bf16 v[164:167], v[212:215], v[120:123], v[164:167]\n\t" \
  "v_fma_mix_f32 v229, |v197|, v229, v229 op_sel:[0,1,0] op_sel_hi:[0,1,1] clamp\n\t" \
  "v_fma_mix_f32 v228, |v196|, v228, v228 op_sel:[0,1,0] op_sel_hi:[0,1,1] clamp\n\t" \
  "ds_read_b64_tr_b16 v[208:209], v249 offset:30752\n\t" \
  "ds_read_b64_tr_b16 v[210:211], v249 offset:32288\n\t" \
  "v_mfma_f32_16x16x32_bf16 v[172:175], v[212:215], v[124:127], v[172:175]\n\t" \
  "v_fma_mix_f32 v230, |v198|, v230, v230 op_sel:[0,1,0] op_sel_hi:[0,1,1] clamp\n\t" \
  "v_fma_mix_f32 v231, |v199|, v231, v231 op_sel:[0,1,0] op_sel_hi:[0,1,1] clamp\n\t" \
  "s_waitcnt lgkmcnt(5)\n\t" \
  "ds_read_b64_tr_b16 v[212:213], v249 offset:30760\n\t" \
  "ds_read_b32 v222, v222 offset:10016\n\t" \
  "v_mfma_f32_16x16x32_bf16 v[148:151], v[216:219], v[128:131], v[148:151]\n\t" \
  "s_mov_b32 m0, s47\n\t" \
  "v_or_b32 v204, v232, v233\n\t" \
  "v_or_b32 v205, v234, v235\n\t" \
  "s_mov_b32 exec_hi, 0xffff\n\t" \
  "buffer_load_dwordx4 v252, s[16:19], s33 offen lds\n\t" \
  "v_mfma_f32_16x16x32_bf16 v[156:159], v[216:219], v[132:135], v[156:159]\n\t" \
  "s_mov_b32 exec_hi, -1\n\t" \
  "v_fma_f32 v197, |v197|, v229, v197 mul:4\n\t" \
  "v_fma_f32 v196, |v196|, v228, v196 mul:4\n\t" \
  "ds_read_b64_tr_b16 v[214:215], v249 offset:32296\n\t" \
  "ds_read_u16_d16_hi v227, v227 offset:10528\n\t" \
  "v_mfma_f32_16x16x32_bf16 v[164:167], v[216:219], v[136:139], v[164:167]\n\t" \
  "v_fma_f32 v198, |v198|, v230, v198 mul:4\n\t" \
  "v_fma_f32 v199, |v199|, v231, v199 mul:4\n\t" \
  "ds_read_u16_d16_hi v225, v225 offset:10528\n\t" \
  "v_mfma_f32_16x16x32_bf16 v[172:175], v[216:219], v[140:143], v[172:175]\n\t" \
  "s_add_u32 s33, s33, s32\n\t" \
  "v_cvt_pk_bf16_f32 v196, v196, v197\n\t" \
  "v_cvt_pk_bf16_f32 v197, v198, v199\n\t" \
  "s_waitcnt lgkmcnt(5)\n\t" \
  "s_setprio 1\n\t" \
  "ds_read_b64_tr_b16 v[216:217], v249 offset:30816\n\t" \
  "v_mfma_f32_16x16x32_bf16 v[0:3], v[208:211], v[178:181], v[0:3]\n\t" \
  "v_cvt_pknorm_i16_f32 v230, |v160|, |v161|\n\t" \
  "v_cvt_pknorm_i16_f32 v228, |v162|, |v163|\n\t" \
  "v_mfma_f32_16x16x32_bf16 v[24:27], v[208:211], v[186:189], v[24:27]\n\t" \
  "v_lshlrev_b32_sdwa v229, 2, v228 dst_sel:DWORD dst_unused:UNUSED_PAD src0_sel:DWORD src1_sel:BYTE_3\n\t" \
  "v_lshlrev_b32_sdwa v228, 2, v228 dst_sel:DWORD dst_unused:UNUSED_PAD src0_sel:DWORD src1_sel:BYTE_1\n\t" \
  "s_add_u32 m0, s47, s35\n\t" \
  "ds_read_u16 v224, v224 offset:10528\n\t" \
  "s_waitcnt lgkmcnt(4)\n\t" \
  "v_mfma_f32_16x16x32_bf16 v[48:51], v[208:211], v[194:197], v[48:51]\n\t" \
  "v_lshlrev_b32_sdwa v233, 1, v171 dst_sel:DWORD dst_unused:UNUSED_PAD src0_sel:DWORD src1_sel:WORD_1\n\t" \
  "v_lshlrev_b32_sdwa v231, 2, v230 dst_sel:DWORD dst_unused:UNUSED_PAD src0_sel:DWORD src1_sel:BYTE_3\n\t" \
  "ds_read_b32 v228, v228 offset:10016\n\t" \
  "ds_read_b64_tr_b16 v[218:219], v249 offset:32352\n\t" \
  "v_mfma_f32_16x16x32_bf16 v[72:75], v[208:211], v[202:205], v[72:75]\n\t" \
  "v_lshlrev_b32_sdwa v232, 1, v170 dst_sel:DWORD dst_unused:UNUSED_PAD src0_sel:DWORD src1_sel:WORD_1\n\t" \
  "s_mov_b32 exec_hi, 0xffff\n\t" \
  "buffer_load_dwordx4 v252, s[16:19], s34 offen lds\n\t" \
  "v_mfma_f32_16x16x32_bf16 v[4:7], v[212:215], v[178:181], v[4:7]\n\t" \
  "s_mov_b32 exec_hi, -1\n\t" \
  "s_add_u32 s34, s34, s32\n\t" \
  "v_lshlrev_b32_sdwa v235, 1, v169 dst_sel:DWORD dst_unused:UNUSED_PAD src0_sel:DWORD src1_sel:WORD_1\n\t" \
  "ds_read_u16 v226, v226 offset:10528\n\t" \
  "ds_read_b64_tr_b16 v[208:209], v249 offset:30824\n\t" \
  "v_mfma_f32_16x16x32_bf16 v[52:55], v[212:215], v[194:197], v[52:55]\n\t" \
  "v_fma_mix_f32 v220, |v146|, v220, v220 op_sel:[0,1,0] op_sel_hi:[0,1,1] clamp\n\t" \
  "v_fma_mix_f32 v221, |v147|, v221, v221 op_sel:[0,1,0] op_sel_hi:[0,1,1] clamp\n\t" \
  "ds_read_b64_tr_b16 v[210:211], v249 offset:32360\n\t" \
  "v_mfma_f32_16x16x32_bf16 v[76:79], v[212:215], v[202:205], v[76:79]\n\t" \
  "s_waitcnt lgkmcnt(3)\n\t" \
  "v_fma_mix_f32 v222, |v144|, v222, v222 op_sel:[0,1,0] op_sel_hi:[0,1,1] clamp\n\t" \
  "v_fma_mix_f32 v223, |v145|, v223, v223 op_sel:[0,1,0] op_sel_hi:[0,1,1] clamp\n\t" \
  "ds_read_b32 v231, v231 offset:10016\n\t" \
  "v_mfma_f32_16x16x32_bf16 v[28:31], v[212:215], v[186:189], v[28:31]\n\t" \
  "v_lshlrev_b32_sdwa v234, 1, v168 dst_sel:DWORD dst_unused:UNUSED_PAD src0_sel:DWORD src1_sel:WORD_1\n\t" \
  "v_lshlrev_b32_sdwa v230, 2, v230 dst_sel:DWORD dst_unused:UNUSED_PAD src0_sel:DWORD src1_sel:BYTE_1\n\t" \
  "s_waitcnt lgkmcnt(3)\n\t" \
  "ds_read_b32 v230, v230 offset:10016\n\t" \
  "ds_read_b64_tr_b16 v[212:213], v249 offset:30880\n\t" \
  "v_mfma_f32_16x16x32_bf16 v[8:11], v[216:219], v[178:181], v[8:11]\n\t" \
  "s_setprio 3\n\t" \
  "v_or_b32 v155, v224, v225\n\t" \
  "v_or_b32 v154, v226, v227\n\t" \
  "ds_read_u16_d16_hi v233, v233 offset:10528\n\t" \
  "v_mfma_f32_16x16x32_bf16 v[32:35], v[216:219], v[186:189], v[32:35]\n\t" \
  "v_fma_f32 v146, |v146|, v220, v146 mul:4\n\t" \
  "v_fma_f32 v147, |v147|, v221, v147 mul:4\n\t" \
  "ds_read_b32 v229, v229 offset:10016\n\t" \
  "v_mfma_f32_16x16x32_bf16 v[56:59], v[216:219], v[194:197], v[56:59]\n\t" \
  "s_add_u32 s3, s33, s51\n\t" \
  "v_fma_f32 v145, |v145|, v223, v145 mul:4\n\t" \
  "v_fma_f32 v144, |v144|, v222, v144 mul:4\n\t" \
  "ds_read_u16 v232, v232 offset:10528\n\t" \
  "ds_read_b64_tr_b16 v[214:215], v249 offset:32416\n\t" \
  "v_mfma_f32_16x16x32_bf16 v[80:83], v[216:219], v[202:205], v[80:83]\n\t" \
  "v_cvt_pk_bf16_f32 v147, v146, v147\n\t" \
  "v_cvt_pknorm_i16_f32 v220, |v148|, |v149|\n\t" \
  "s_waitcnt lgkmcnt(7)\n\t" \
  "v_cvt_pk_bf16_f32 v146, v144, v145\n\t" \
  "v_cvt_pknorm_i16_f32 v222, |v150|, |v151|\n\t" \
  "v_mfma_f32_16x16x32_bf16 v[36:39], v[208:211], v[186:189], v[36:39]\n\t" \
  "s_add_u32 m0, s44, s50\n\t" \
  "ds_read_b64_tr_b16 v[216:217], v249 offset:30888\n\t" \
  "ds_read_b64_tr_b16 v[218:219], v249 offset:32424\n\t" \
  "v_mfma_f32_16x16x32_bf16 v[12:15], v[208:211], v[178:181], v[12:15]\n\t" \
  "v_lshlrev_b32_sdwa v221, 2, v220 dst_sel:DWORD dst_unused:UNUSED_PAD src0_sel:DWORD src1_sel:BYTE_3\n\t" \
  "v_lshlrev_b32_sdwa v223, 2, v222 dst_sel:DWORD dst_unused:UNUSED_PAD src0_sel:DWORD src1_sel:BYTE_3\n\t" \
  "ds_read_u16_d16_hi v235, v235 offset:10528\n\t" \
  "v_mfma_f32_16x16x32_bf16 v[84:87], v[208:211], v[202:205], v[84:87]\n\t" \
  "s_waitcnt lgkmcnt(3)\n\t" \
  "v_lshlrev_b32_sdwa v222, 2, v222 dst_sel:DWORD dst_unused:UNUSED_PAD src0_sel:DWORD src1_sel:BYTE_1\n\t" \
  "v_lshlrev_b32_sdwa v220, 2, v220 dst_sel:DWORD dst_unused:UNUSED_PAD src0_sel:DWORD src1_sel:BYTE_1\n\t" \
  "ds_read_b32 v220, v220 offset:10016\n\t" \
  "ds_read_u16 v234, v234 offset:10528\n\t" \
  "v_mfma_f32_16x16x32_bf16 v[60:63], v[208:211], v[194:197], v[60:63]\n\t" \
  "v_lshlrev_b32_sdwa v224, 1, v156 dst_sel:DWORD dst_unused:UNUSED_PAD src0_sel:DWORD src1_sel:WORD_1\n\t" \
  "v_lshlrev_b32_sdwa v225, 1, v157 dst_sel:DWORD dst_unused:UNUSED_PAD src0_sel:DWORD src1_sel:WORD_1\n\t" \
  "s_mov_b32 exec_hi, 0xffff\n\t" \
  "buffer_load_dwordx4 v252, s[16:19], s3 offen lds\n\t" \
  "v_mfma_f32_16x16x32_bf16 v[16:19], v[212:215], v[178:181], v[16:19]\n\t" \
  "s_mov_b64 exec, -1\n\t" \
  "s_setprio 2\n\t" \
  "v_nop\n\t" \
  "ds_read_b32 v221, v221 offset:10016\n\t" \
  "v_mfma_f32_16x16x32_bf16 v[40:43], v[212:215], v[186:189], v[40:43]\n\t" \
  "v_fma_mix_f32 v228, |v162|, v228, v228 op_sel:[0,1,0] op_sel_hi:[0,1,1] clamp\n\t" \
  "v_lshlrev_b32_sdwa v226, 1, v158 dst_sel:DWORD dst_unused:UNUSED_PAD src0_sel:DWORD src1_sel:WORD_1\n\t" \
  "ds_read_b64_tr_b16 v[208:209], v248 offset:0\n\t" \
  "v_mfma_f32_16x16x32_bf16 v[88:91], v[212:215], v[202:205], v[88:91]\n\t" \
  "v_fma_mix_f32 v231, |v161|, v231, v231 op_sel:[0,1,0] op_sel_hi:[0,1,1] clamp\n\t" \
  "v_fma_mix_f32 v230, |v160|, v230, v230 op_sel:[0,1,0] op_sel_hi:[0,1,1] clamp\n\t" \
  "ds_read_b32 v223, v223 offset:10016\n\t" \
  "ds_read_b64_tr_b16 v[210:211], v248 offset:1024\n\t" \
  "v_mfma_f32_16x16x32_bf16 v[64:67], v[212:215], v[194:197], v[64:67]\n\t" \
  "s_waitcnt lgkmcnt(7)\n\t" \
  "v_fma_mix_f32 v229, |v163|, v229, v229 op_sel:[0,1,0] op_sel_hi:[0,1,1] clamp\n\t" \
  "v_lshlrev_b32_sdwa v227, 1, v159 dst_sel:DWORD dst_unused:UNUSED_PAD src0_sel:DWORD src1_sel:WORD_1\n\t" \
  "ds_read_b32 v222, v222 offset:10016\n\t" \
  "ds_read_b64_tr_b16 v[212:213], v248 offset:3072\n\t" \
  "v_mfma_f32_16x16x32_bf16 v[20:23], v[216:219], v[178:181], v[20:23]\n\t" \
  "s_waitcnt lgkmcnt(6)\n\t" \
  "v_or_b32 v171, v232, v233\n\t" \
  "v_or_b32 v170, v234, v235\n\t" \
  "ds_read_u16_d16_hi v225, v225 offset:10528\n\t" \
  "v_mfma_f32_16x16x32_bf16 v[68:71], v[216:219], v[194:197], v[68:71]\n\t" \
  "s_add_u32 s3, s34, s51\n\t" \
  "v_fma_f32 v163, |v163|, v229, v163 mul:4\n\t" \
  "v_fma_f32 v162, |v162|, v228, v162 mul:4\n\t" \
  "s_add_u32 m0, s44, s36\n\t" \
  "ds_read_u16 v224, v224 offset:10528\n\t" \
  "s_waitcnt lgkmcnt(4)\n\t" \
  "v_cvt_pk_bf16_f32 v163, v162, v163\n\t" \
  "s_mov_b32 exec_hi, 0xffff\n\t" \
  "buffer_load_dwordx4 v252, s[16:19], s3 offen lds\n\t" \
  "v_mfma_f32_16x16x32_bf16 v[176:179], v[208:211], v[96:99], 0\n\t" \
  "s_mov_b64 exec, -1\n\t" \
  "v_nop\n\t" \
  "v_fma_f32 v161, |v161|, v231, v161 mul:4\n\t" \
  "ds_read_u16_d16_hi v227, v227 offset:10528\n\t" \
  "v_mfma_f32_16x16x32_bf16 v[92:95], v[216:219], v[202:205], v[92:95]\n\t" \
  "v_fma_f32 v160, |v160|, v230, v160 mul:4\n\t" \
  "v_cvt_pk_bf16_f32 v162, v160, v161\n\t" \
  "ds_read_b64_tr_b16 v[214:215], v248 offset:4096\n\t" \
  "v_mfma_f32_16x16x32_bf16 v[44:47], v[216:219], v[186:189], v[44:47]\n\t" \
  "v_lshlrev_b32_sdwa v232, 1, v172 dst_sel:DWORD dst_unused:UNUSED_PAD src0_sel:DWORD src1_sel:WORD_1\n\t" \
  "v_cvt_pknorm_i16_f32 v230, |v166|, |v167|\n\t" \
  "ds_read_b64_tr_b16 v[216:217], v248 offset:2048\n\t" \
  "v_mfma_f32_16x16x32_bf16 v[192:195], v[208:211], v[104:107], 0\n\t" \
  "v_cvt_pknorm_i16_f32 v228, |v164|, |v165|\n\t" \
  "v_lshlrev_b32_sdwa v231, 2, v230 dst_sel:DWORD dst_unused:UNUSED_PAD src0_sel:DWORD src1_sel:BYTE_3\n\t" \
  "ds_read_b32 v231, v231 offset:10016\n\t" \
  "ds_read_b64_tr_b16 v[218:219], v248 offset:5120\n\t" \
  "v_mfma_f32_16x16x32_bf16 v[184:187], v[208:211], v[100:103], 0\n\t" \
  "v_lshlrev_b32_sdwa v229, 2, v228 dst_sel:DWORD dst_unused:UNUSED_PAD src0_sel:DWORD src1_sel:BYTE_3\n\t" \
  "v_lshlrev_b32_sdwa v228, 2, v228 dst_sel:DWORD dst_unused:UNUSED_PAD src0_sel:DWORD src1_sel:BYTE_1\n\t" \
  "ds_read_b32 v228, v228 offset:10016\n\t" \
  "ds_read_u16 v226, v226 offset:10528\n\t" \
  "v_mfma_f32_16x16x32_bf16 v[200:203], v[208:211], v[108:111], 0\n\t" \
  "v_lshlrev_b32_sdwa v230, 2, v230 dst_sel:DWORD dst_unused:UNUSED_PAD src0_sel:DWORD src1_sel:BYTE_1\n\t" \
  "v_nop\n\t" \
  "ds_read_b64_tr_b16 v[208:209], v248 offset:512\n\t" \
  "s_waitcnt lgkmcnt(6)\n\t" \
  "v_mfma_f32_16x16x32_bf16 v[176:179], v[212:215], v[112:115], v[176:179]\n\t" \
  "v_fma_mix_f32 v223, |v151|, v223, v223 op_sel:[0,1,0] op_sel_hi:[0,1,1] clamp\n\t" \
  "v_fma_mix_f32 v221, |v149|, v221, v221 op_sel:[0,1,0] op_sel_hi:[0,1,1] clamp\n\t" \
  "ds_read_b32 v229, v229 offset:10016\n\t" \
  "v_mfma_f32_16x16x32_bf16 v[200:203], v[212:215], v[124:127], v[200:203]\n\t" \
  "v_lshlrev_b32_sdwa v234, 1, v174 dst_sel:DWORD dst_unused:UNUSED_PAD src0_sel:DWORD src1_sel:WORD_1\n\t" \
  "v_lshlrev_b32_sdwa v235, 1, v175 dst_sel:DWORD dst_unused:UNUSED_PAD src0_sel:DWORD src1_sel:WORD_1\n\t" \
  "ds_read_b64_tr_b16 v[210:211], v248 offset:1536\n\t" \
  "v_mfma_f32_16x16x32_bf16 v[192:195], v[212:215], v[120:123], v[192:195]\n\t" \
  "v_fma_mix_f32 v220, |v148|, v220, v220 op_sel:[0,1,0] op_sel_hi:[0,1,1] clamp\n\t" \
  "v_lshlrev_b32_sdwa v233, 1, v173 dst_sel:DWORD dst_unused:UNUSED_PAD src0_sel:DWORD src1_sel:WORD_1\n\t" \
  "ds_read_u16_d16_hi v233, v233 offset:10528\n\t" \
  "s_setprio 1\n\t" \
  "v_mfma_f32_16x16x32_bf16 v[184:187], v[212:215], v[116:119], v[184:187]\n\t" \
  "s_waitcnt lgkmcnt(6)\n\t" \
  "s_setprio 0\n\t" \
  "v_fma_mix_f32 v222, |v150|, v222, v222 op_sel:[0,1,0] op_sel_hi:[0,1,1] clamp\n\t" \
  "s_waitcnt lgkmcnt(4)\n\t" \
  "ds_read_b64_tr_b16 v[212:213], v248 offset:3584\n\t" \
  "ds_read_b32 v230, v230 offset:10016\n\t" \
  "v_mfma_f32_16x16x32_bf16 v[176:179], v[216:219], v[128:131], v[176:179]\n\t" \
  "v_or_b32 v156, v224, v225\n\t" \
  "v_or_b32 v157, v226, v227\n\t" \
  "ds_read_b64_tr_b16 v[214:215], v248 offset:4608\n\t" \
  "v_mfma_f32_16x16x32_bf16 v[184:187], v[216:219], v[132:135], v[184:187]\n\t" \
  "v_fma_f32 v151, |v151|, v223, v151 mul:4\n\t" \
  "v_fma_f32 v150, |v150|, v222, v150 mul:4\n\t" \
  "ds_read_u16 v232, v232 offset:10528\n\t" \
  "v_mfma_f32_16x16x32_bf16 v[200:203], v[216:219], v[140:143], v[200:203]\n\t" \
  "v_fma_f32 v148, |v148|, v220, v148 mul:4\n\t" \
  "v_fma_f32 v149, |v149|, v221, v149 mul:4\n\t" \
  "ds_read_u16_d16_hi v235, v235 offset:10528\n\t" \
  "v_mfma_f32_16x16x32_bf16 v[192:195], v[216:219], v[136:139], v[192:195]\n\t" \
  "s_waitcnt lgkmcnt(6)\n\t" \
  "v_cvt_pknorm_i16_f32 v220, |v178|, |v179|\n\t" \
  "v_cvt_pk_bf16_f32 v148, v148, v149\n\t" \
  "ds_read_b64_tr_b16 v[216:217], v248 offset:2560\n\t" \
  "v_mfma_f32_16x16x32_bf16 v[180:183], v[208:211], v[96:99], 0\n\t" \
  "v_cvt_pk_bf16_f32 v149, v150, v151\n\t" \
  "v_cvt_pknorm_i16_f32 v222, |v176|, |v177|\n\t" \
  "s_waitcnt vmcnt(2)\n\t" \
  "ds_read_b64_tr_b16 v[218:219], v248 offset:5632\n\t" \
  "s_waitcnt lgkmcnt(4)\n\t" \
  "v_mfma_f32_16x16x32_bf16 v[188:191], v[208:211], v[100:103], 0\n\t" \
  "v_lshlrev_b32_sdwa v221, 2, v220 dst_sel:DWORD dst_unused:UNUSED_PAD src0_sel:DWORD src1_sel:BYTE_3\n\t" \
  "v_lshlrev_b32_sdwa v220, 2, v220 dst_sel:DWORD dst_unused:UNUSED_PAD src0_sel:DWORD src1_sel:BYTE_1\n\t" \
  "s_mov_b64 exec, 3\n\t" \
  "ds_add_rtn_u32 v246, v251, v250 offset:47160\n\t" \
  "v_mfma_f32_16x16x32_bf16 v[196:199], v[208:211], v[104:107], 0\n\t" \
  "s_mov_b64 exec, -1\n\t" \
  "v_lshlrev_b32_sdwa v223, 2, v222 dst_sel:DWORD dst_unused:UNUSED_PAD src0_sel:DWORD src1_sel:BYTE_3\n\t" \
  "v_lshlrev_b32_sdwa v222, 2, v222 dst_sel:DWORD dst_unused:UNUSED_PAD src0_sel:DWORD src1_sel:BYTE_1\n\t" \
  "ds_read_u16 v234, v234 offset:10528\n\t" \
  "v_mfma_f32_16x16x32_bf16 v[188:191], v[212:215], v[116:119], v[188:191]\n\t" \
  "v_lshlrev_b32_sdwa v227, 1, v185 dst_sel:DWORD dst_unused:UNUSED_PAD src0_sel:DWORD src1_sel:WORD_1\n\t" \
  "v_lshlrev_b32_sdwa v226, 1, v184 dst_sel:DWORD dst_unused:UNUSED_PAD src0_sel:DWORD src1_sel:WORD_1\n\t" \
  "ds_read_b32 v223, v223 offset:10016\n\t" \
  "v_mfma_f32_16x16x32_bf16 v[204:207], v[208:211], v[108:111], 0\n\t" \
  "s_waitcnt lgkmcnt(2)\n\t" \
  "v_lshlrev_b32_sdwa v225, 1, v187 dst_sel:DWORD dst_unused:UNUSED_PAD src0_sel:DWORD src1_sel:WORD_1\n\t" \
  "v_readfirstlane_b32 s4, v246\n\t" \
  "ds_read_b32 v220, v220 offset:10016\n\t" \
  "ds_read_b32 v221, v221 offset:10016\n\t" \
  "v_mfma_f32_16x16x32_bf16 v[180:183], v[212:215], v[112:115], v[180:183]\n\t" \
  "s_cmpk_lt_u32 s4, 6144\n\t" \
  "v_lshlrev_b32_sdwa v224, 1, v186 dst_sel:DWORD dst_unused:UNUSED_PAD src0_sel:DWORD src1_sel:WORD_1\n\t" \
  "s_cbranch_scc1 .Lcnx_w1_4\n\t" \
  ".Lcnx_g1_4:\n\t" \
  "v_mfma_f32_16x16x32_bf16 v[196:199], v[212:215], v[120:123], v[196:199]\n\t" \
  "v_fma_mix_f32 v229, |v165|, v229, v229 op_sel:[0,1,0] op_sel_hi:[0,1,1] clamp\n\t" \
  "v_fma_mix_f32 v228, |v164|, v228, v228 op_sel:[0,1,0] op_sel_hi:[0,1,1] clamp\n\t" \
  "ds_read_b64_tr_b16 v[208:209], v249 offset:6152\n\t" \
  "ds_read_b64_tr_b16 v[210:211], v249 offset:7688\n\t" \
  "v_mfma_f32_16x16x32_bf16 v[204:207], v[212:215], v[124:127], v[204:207]\n\t" \
  "v_fma_mix_f32 v230, |v166|, v230, v230 op_sel:[0,1,0] op_sel_hi:[0,1,1] clamp\n\t" \
  "v_fma_mix_f32 v231, |v167|, v231, v231 op_sel:[0,1,0] op_sel_hi:[0,1,1] clamp\n\t" \
  "s_waitcnt lgkmcnt(5)\n\t" \
  "ds_read_b64_tr_b16 v[212:213], v249 offset:6160\n\t" \
  "ds_read_b32 v222, v222 offset:10016\n\t" \
  "v_mfma_f32_16x16x32_bf16 v[180:183], v[216:219], v[128:131], v[180:183]\n\t" \
  "s_mov_b32 m0, s44\n\t" \
  "v_or_b32 v172, v232, v233\n\t" \
  "v_or_b32 v173, v234, v235\n\t" \
  "s_mov_b32 exec_hi, 0xffff\n\t" \
  "buffer_load_dwordx4 v252, s[16:19], s33 offen lds\n\t" \
  "v_mfma_f32_16x16x32_bf16 v[188:191], v[216:219], v[132:135], v[188:191]\n\t" \
  "s_mov_b32 exec_hi, -1\n\t" \
  "v_fma_f32 v165, |v165|, v229, v165 mul:4\n\t" \
  "v_fma_f32 v164, |v164|, v228, v164 mul:4\n\t" \
  "ds_read_b64_tr_b16 v[214:215], v249 offset:7696\n\t" \
  "ds_read_u16_d16_hi v227, v227 offset:10528\n\t" \
  "v_mfma_f32_16x16x32_bf16 v[196:199], v[216:219], v[136:139], v[196:199]\n\t" \
  "v_fma_f32 v166, |v166|, v230, v166 mul:4\n\t" \
  "v_fma_f32 v167, |v167|, v231, v167 mul:4\n\t" \
  "ds_read_u16_d16_hi v225, v225 offset:10528\n\t" \
  "v_mfma_f32_16x16x32_bf16 v[204:207], v[216:219], v[140:143], v[204:207]\n\t" \
  "s_add_u32 s33, s33, s32\n\t" \
  "v_cvt_pk_bf16_f32 v164, v164, v165\n\t" \
  "v_cvt_pk_bf16_f32 v165, v166, v167\n\t" \
  "s_waitcnt lgkmcnt(5)\n\t" \
  "s_setprio 1\n\t" \
  "ds_read_b64_tr_b16 v[216:217], v249 offset:6216\n\t" \
  "v_mfma_f32_16x16x32_bf16 v[0:3], v[208:211], v[146:149], v[0:3]\n\t" \
  "v_cvt_pknorm_i16_f32 v230, |v192|, |v193|\n\t" \
  "v_cvt_pknorm_i16_f32 v228, |v194|, |v195|\n\t" \
  "v_mfma_f32_16x16x32_bf16 v[24:27], v[208:211], v[154:157], v[24:27]\n\t" \
  "v_lshlrev_b32_sdwa v229, 2, v228 dst_sel:DWORD dst_unused:UNUSED_PAD src0_sel:DWORD src1_sel:BYTE_3\n\t" \
  "v_lshlrev_b32_sdwa v228, 2, v228 dst_sel:DWORD dst_unused:UNUSED_PAD src0_sel:DWORD src1_sel:BYTE_1\n\t" \
  "s_add_u32 m0, s44, s35\n\t" \
  "ds_read_u16 v224, v224 offset:10528\n\t" \
  "s_waitcnt lgkmcnt(4)\n\t" \
  "v_mfma_f32_16x16x32_bf16 v[48:51], v[208:211], v[162:165], v[48:51]\n\t" \
  "v_lshlrev_b32_sdwa v233, 1, v203 dst_sel:DWORD dst_unused:UNUSED_PAD src0_sel:DWORD src1_sel:WORD_1\n\t" \
  "v_lshlrev_b32_sdwa v231, 2, v230 dst_sel:DWORD dst_unused:UNUSED_PAD src0_sel:DWORD src1_sel:BYTE_3\n\t" \
  "ds_read_b32 v228, v228 offset:10016\n\t" \
  "ds_read_b64_tr_b16 v[218:219], v249 offset:7752\n\t" \
  "v_mfma_f32_16x16x32_bf16 v[72:75], v[208:211], v[170:173], v[72:75]\n\t" \
  "v_lshlrev_b32_sdwa v232, 1, v202 dst_sel:DWORD dst_unused:UNUSED_PAD src0_sel:DWORD src1_sel:WORD_1\n\t" \
  "s_mov_b32 exec_hi, 0xffff\n\t" \
  "buffer_load_dwordx4 v252, s[16:19], s34 offen lds\n\t" \
  "v_mfma_f32_16x16x32_bf16 v[4:7], v[212:215], v[146:149], v[4:7]\n\t" \
  "s_mov_b32 exec_hi, -1\n\t" \
  "s_add_u32 s34, s34, s32\n\t" \
  "v_lshlrev_b32_sdwa v235, 1, v201 dst_sel:DWORD dst_unused:UNUSED_PAD src0_sel:DWORD src1_sel:WORD_1\n\t" \
  "ds_read_u16 v226, v226 offset:10528\n\t" \
  "ds_read_b64_tr_b16 v[208:209], v249 offset:6224\n\t" \
  "v_mfma_f32_16x16x32_bf16 v[52:55], v[212:215], v[162:165], v[52:55]\n\t" \
  "v_fma_mix_f32 v220, |v178|, v220, v220 op_sel:[0,1,0] op_sel_hi:[0,1,1] clamp\n\t" \
  "v_fma_mix_f32 v221, |v179|, v221, v221 op_sel:[0,1,0] op_sel_hi:[0,1,1] clamp\n\t" \
  "ds_read_b64_tr_b16 v[210:211], v249 offset:7760\n\t" \
  "v_mfma_f32_16x16x32_bf16 v[76:79], v[212:215], v[170:173], v[76:79]\n\t" \
  "s_waitcnt lgkmcnt(3)\n\t" \
  "v_fma_mix_f32 v222, |v176|, v222, v222 op_sel:[0,1,0] op_sel_hi:[0,1,1] clamp\n\t" \
  "v_fma_mix_f32 v223, |v177|, v223, v223 op_sel:[0,1,0] op_sel_hi:[0,1,1] clamp\n\t" \
  "ds_read_b32 v231, v231 offset:10016\n\t" \
  "v_mfma_f32_16x16x32_bf16 v[28:31], v[212:215], v[154:157], v[28:31]\n\t" \
  "v_lshlrev_b32_sdwa v234, 1, v200 dst_sel:DWORD dst_unused:UNUSED_PAD src0_sel:DWORD src1_sel:WORD_1\n\t" \
  "v_lshlrev_b32_sdwa v230, 2, v230 dst_sel:DWORD dst_unused:UNUSED_PAD src0_sel:DWORD src1_sel:BYTE_1\n\t" \
  "s_waitcnt lgkmcnt(3)\n\t" \
  "ds_read_b32 v230, v230 offset:10016\n\t" \
  "ds_read_b64_tr_b16 v[212:213], v249 offset:6280\n\t" \
  "v_mfma_f32_16x16x32_bf16 v[8:11], v[216:219], v[146:149], v[8:11]\n\t" \
  "s_setprio 3\n\t" \
  "v_or_b32 v187, v224, v225\n\t" \
  "v_or_b32 v186, v226, v227\n\t" \
  "ds_read_u16_d16_hi v233, v233 offset:10528\n\t" \
  "v_mfma_f32_16x16x32_bf16 v[32:35], v[216:219], v[154:157], v[32:35]\n\t" \
  "v_fma_f32 v178, |v178|, v220, v178 mul:4\n\t" \
  "v_fma_f32 v179, |v179|, v221, v179 mul:4\n\t" \
  "ds_read_b32 v229, v229 offset:10016\n\t" \
  "v_mfma_f32_16x16x32_bf16 v[56:59], v[216:219], v[162:165], v[56:59]\n\t" \
  "s_add_u32 s3, s33, s51\n\t" \
  "v_fma_f32 v177, |v177|, v223, v177 mul:4\n\t" \
  "v_fma_f32 v176, |v176|, v222, v176 mul:4\n\t" \
  "ds_read_u16 v232, v232 offset:10528\n\t" \
  "ds_read_b64_tr_b16 v[214:215], v249 offset:7816\n\t" \
  "v_mfma_f32_16x16x32_bf16 v[80:83], v[216:219], v[170:173], v[80:83]\n\t" \
  "v_cvt_pk_bf16_f32 v179, v178, v179\n\t" \
  "v_cvt_pknorm_i16_f32 v220, |v180|, |v181|\n\t" \
  "s_waitcnt lgkmcnt(7)\n\t" \
  "v_cvt_pk_bf16_f32 v178, v176, v177\n\t" \
  "v_cvt_pknorm_i16_f32 v222, |v182|, |v183|\n\t" \
  "v_mfma_f32_16x16x32_bf16 v[36:39], v[208:211], v[154:157], v[36:39]\n\t" \
  "s_add_u32 m0, s45, s50\n\t" \
  "ds_read_b64_tr_b16 v[216:217], v249 offset:6288\n\t" \
  "ds_read_b64_tr_b16 v[218:219], v249 offset:7824\n\t" \
  "v_mfma_f32_16x16x32_bf16 v[12:15], v[208:211], v[146:149], v[12:15]\n\t" \
  "v_lshlrev_b32_sdwa v221, 2, v220 dst_sel:DWORD dst_unused:UNUSED_PAD src0_sel:DWORD src1_sel:BYTE_3\n\t" \
  "v_lshlrev_b32_sdwa v223, 2, v222 dst_sel:DWORD dst_unused:UNUSED_PAD src0_sel:DWORD src1_sel:BYTE_3\n\t" \
  "ds_read_u16_d16_hi v235, v235 offset:10528\n\t" \
  "v_mfma_f32_16x16x32_bf16 v[84:87], v[208:211], v[170:173], v[84:87]\n\t" \
  "s_waitcnt lgkmcnt(3)\n\t" \
  "v_lshlrev_b32_sdwa v222, 2, v222 dst_sel:DWORD dst_unused:UNUSED_PAD src0_sel:DWORD src1_sel:BYTE_1\n\t" \
  "v_lshlrev_b32_sdwa v220, 2, v220 dst_sel:DWORD dst_unused:UNUSED_PAD src0_sel:DWORD src1_sel:BYTE_1\n\t" \
  "ds_read_b32 v220, v220 offset:10016\n\t" \
  "ds_read_u16 v234, v234 offset:10528\n\t" \
  "v_mfma_f32_16x16x32_bf16 v[60:63], v[208:211], v[162:165], v[60:63]\n\t" \
  "v_lshlrev_b32_sdwa v224, 1, v188 dst_sel:DWORD dst_unused:UNUSED_PAD src0_sel:DWORD src1_sel:WORD_1\n\t" \
  "v_lshlrev_b32_sdwa v225, 1, v189 dst_sel:DWORD dst_unused:UNUSED_PAD src0_sel:DWORD src1_sel:WORD_1\n\t" \
  "s_mov_b32 exec_hi, 0xffff\n\t" \
  "buffer_load_dwordx4 v252, s[16:19], s3 offen lds\n\t" \
  "v_mfma_f32_16x16x32_bf16 v[16:19], v[212:215], v[146:149], v[16:19]\n\t" \
  "s_mov_b64 exec, -1\n\t" \
  "s_setprio 2\n\t" \
  "v_nop\n\t" \
  "ds_read_b32 v221, v221 offset:10016\n\t" \
  "v_mfma_f32_16x16x32_bf16 v[40:43], v[212:215], v[154:157], v[40:43]\n\t" \
  "v_fma_mix_f32 v228, |v194|, v228, v228 op_sel:[0,1,0] op_sel_hi:[0,1,1] clamp\n\t" \
  "v_lshlrev_b32_sdwa v226, 1, v190 dst_sel:DWORD dst_unused:UNUSED_PAD src0_sel:DWORD src1_sel:WORD_1\n\t" \
  "ds_read_b64_tr_b16 v[208:209], v248 offset:43048\n\t" \
  "v_mfma_f32_16x16x32_bf16 v[88:91], v[212:215], v[170:173], v[88:91]\n\t" \
  "v_fma_mix_f32 v231, |v193|, v231, v231 op_sel:[0,1,0] op_sel_hi:[0,1,1] clamp\n\t" \
  "v_fma_mix_f32 v230, |v192|, v230, v230 op_sel:[0,1,0] op_sel_hi:[0,1,1] clamp\n\t" \
  "ds_read_b32 v223, v223 offset:10016\n\t" \
  "ds_read_b64_tr_b16 v[210:211], v248 offset:44072\n\t" \
  "v_mfma_f32_16x16x32_bf16 v[64:67], v[212:215], v[162:165], v[64:67]\n\t" \
  "s_waitcnt lgkmcnt(7)\n\t" \
  "v_fma_mix_f32 v229, |v195|, v229, v229 op_sel:[0,1,0] op_sel_hi:[0,1,1] clamp\n\t" \
  "v_lshlrev_b32_sdwa v227, 1, v191 dst_sel:DWORD dst_unused:UNUSED_PAD src0_sel:DWORD src1_sel:WORD_1\n\t" \
  "ds_read_b32 v222, v222 offset:10016\n\t" \
  "ds_read_b64_tr_b16 v[212:213], v248 offset:46120\n\t" \
  "v_mfma_f32_16x16x32_bf16 v[20:23], v[216:219], v[146:149], v[20:23]\n\t" \
  "s_waitcnt lgkmcnt(6)\n\t" \
  "v_or_b32 v203, v232, v233\n\t" \
  "v_or_b32 v202, v234, v235\n\t" \
  "ds_read_u16_d16_hi v225, v225 offset:10528\n\t" \
  "v_mfma_f32_16x16x32_bf16 v[68:71], v[216:219], v[162:165], v[68:71]\n\t" \
  "s_add_u32 s3, s34, s51\n\t" \
  "v_fma_f32 v195, |v195|, v229, v195 mul:4\n\t" \
  "v_fma_f32 v194, |v194|, v228, v194 mul:4\n\t" \
  "s_add_u32 m0, s45, s36\n\t" \
  "ds_read_u16 v224, v224 offset:10528\n\t" \
  "s_waitcnt lgkmcnt(4)\n\t" \
  "v_cvt_pk_bf16_f32 v195, v194, v195\n\t" \
  "s_mov_b32 exec_hi, 0xffff\n\t" \
  "buffer_load_dwordx4 v252, s[16:19], s3 offen lds\n\t" \
  "v_mfma_f32_16x16x32_bf16 v[144:147], v[208:211], v[96:99], 0\n\t" \
  "s_mov_b64 exec, -1\n\t" \
  "v_nop\n\t" \
  "v_fma_f32 v193, |v193|, v231, v193 mul:4\n\t" \
  "ds_read_u16_d16_hi v227, v227 offset:10528\n\t" \
  "v_mfma_f32_16x16x32_bf16 v[92:95], v[216:219], v[170:173], v[92:95]\n\t" \
  "v_fma_f32 v192, |v192|, v230, v192 mul:4\n\t" \
  "v_cvt_pk_bf16_f32 v194, v192, v193\n\t" \
  "ds_read_b64_tr_b16 v[214:215], v248 offset:47144\n\t" \
  "v_mfma_f32_16x16x32_bf16 v[44:47], v[216:219], v[154:157], v[44:47]\n\t" \
  "v_lshlrev_b32_sdwa v232, 1, v204 dst_sel:DWORD dst_unused:UNUSED_PAD src0_sel:DWORD src1_sel:WORD_1\n\t" \
  "v_cvt_pknorm_i16_f32 v230, |v198|, |v199|\n\t" \
  "ds_read_b64_tr_b16 v[216:217], v248 offset:45096\n\t" \
  "v_mfma_f32_16x16x32_bf16 v[160:163], v[208:211], v[104:107], 0\n\t" \
  "v_cvt_pknorm_i16_f32 v228, |v196|, |v197|\n\t" \
  "v_lshlrev_b32_sdwa v231, 2, v230 dst_sel:DWORD dst_unused:UNUSED_PAD src0_sel:DWORD src1_sel:BYTE_3\n\t" \
  "ds_read_b32 v231, v231 offset:10016\n\t" \
  "ds_read_b64_tr_b16 v[218:219], v248 offset:48168\n\t" \
  "v_mfma_f32_16x16x32_bf16 v[152:155], v[208:211], v[100:103], 0\n\t" \
  "v_lshlrev_b32_sdwa v229, 2, v228 dst_sel:DWORD dst_unused:UNUSED_PAD src0_sel:DWORD src1_sel:BYTE_3\n\t" \
  "v_lshlrev_b32_sdwa v228, 2, v228 dst_sel:DWORD dst_unused:UNUSED_PAD src0_sel:DWORD src1_sel:BYTE_1\n\t" \
  "ds_read_b32 v228, v228 offset:10016\n\t" \
  "ds_read_u16 v226, v226 offset:10528\n\t" \
  "v_mfma_f32_16x16x32_bf16 v[168:171], v[208:211], v[108:111], 0\n\t" \
  "v_lshlrev_b32_sdwa v230, 2, v230 dst_sel:DWORD dst_unused:UNUSED_PAD src0_sel:DWORD src1_sel:BYTE_1\n\t" \
  "v_nop\n\t" \
  "ds_read_b64_tr_b16 v[208:209], v248 offset:43560\n\t" \
  "s_waitcnt lgkmcnt(6)\n\t" \
  "v_mfma_f32_16x16x32_bf16 v[144:147], v[212:215], v[112:115], v[144:147]\n\t" \
  "v_fma_mix_f32 v223, |v183|, v223, v223 op_sel:[0,1,0] op_sel_hi:[0,1,1] clamp\n\t" \
  "v_fma_mix_f32 v221, |v181|, v221, v221 op_sel:[0,1,0] op_sel_hi:[0,1,1] clamp\n\t" \
  "ds_read_b32 v229, v229 offset:10016\n\t" \
  "v_mfma_f32_16x16x32_bf16 v[168:171], v[212:215], v[124:127], v[168:171]\n\t" \
  "v_lshlrev_b32_sdwa v234, 1, v206 dst_sel:DWORD dst_unused:UNUSED_PAD src0_sel:DWORD src1_sel:WORD_1\n\t" \
  "v_lshlrev_b32_sdwa v235, 1, v207 dst_sel:DWORD dst_unused:UNUSED_PAD src0_sel:DWORD src1_sel:WORD_1\n\t" \
  "ds_read_b64_tr_b16 v[210:211], v248 offset:44584\n\t" \
  "v_mfma_f32_16x16x32_bf16 v[160:163], v[212:215], v[120:123], v[160:163]\n\t" \
  "v_fma_mix_f32 v220, |v180|, v220, v220 op_sel:[0,1,0] op_sel_hi:[0,1,1] clamp\n\t" \
  "v_lshlrev_b32_sdwa v233, 1, v205 dst_sel:DWORD dst_unused:UNUSED_PAD src0_sel:DWORD src1_sel:WORD_1\n\t" \
  "ds_read_u16_d16_hi v233, v233 offset:10528\n\t" \
  "s_setprio 1\n\t" \
  "v_mfma_f32_16x16x32_bf16 v[152:155], v[212:215], v[116:119], v[152:155]\n\t" \
  "s_waitcnt lgkmcnt(6)\n\t" \
  "s_setprio 0\n\t" \
  "v_fma_mix_f32 v222, |v182|, v222, v222 op_sel:[0,1,0] op_sel_hi:[0,1,1] clamp\n\t" \
  "s_waitcnt lgkmcnt(4)\n\t" \
  "ds_read_b64_tr_b16 v[212:213], v248 offset:46632\n\t" \
  "ds_read_b32 v230, v230 offset:10016\n\t" \
  "v_mfma_f32_16x16x32_bf16 v[144:147], v[216:219], v[128:131], v[144:147]\n\t" \
  "v_or_b32 v188, v224, v225\n\t" \
  "v_or_b32 v189, v226, v227\n\t" \
  "ds_read_b64_tr_b16 v[214:215], v248 offset:47656\n\t" \
  "v_mfma_f32_16x16x32_bf16 v[152:155], v[216:219], v[132:135], v[152:155]\n\t" \
  "v_fma_f32 v183, |v183|, v223, v183 mul:4\n\t" \
  "v_fma_f32 v182, |v182|, v222, v182 mul:4\n\t" \
  "ds_read_u16 v232, v232 offset:10528\n\t" \
  "v_mfma_f32_16x16x32_bf16 v[168:171], v[216:219], v[140:143], v[168:171]\n\t" \
  "v_fma_f32 v180, |v180|, v220, v180 mul:4\n\t" \
  "v_fma_f32 v181, |v181|, v221, v181 mul:4\n\t" \
  "ds_read_u16_d16_hi v235, v235 offset:10528\n\t" \
  "v_mfma_f32_16x16x32_bf16 v[160:163], v[216:219], v[136:139], v[160:163]\n\t" \
  "s_waitcnt lgkmcnt(6)\n\t" \
  "v_cvt_pknorm_i16_f32 v220, |v146|, |v147|\n\t" \
  "v_cvt_pk_bf16_f32 v180, v180, v181\n\t" \
  "ds_read_b64_tr_b16 v[216:217], v248 offset:45608\n\t" \
  "v_mfma_f32_16x16x32_bf16 v[148:151], v[208:211], v[96:99], 0\n\t" \
  "v_cvt_pk_bf16_f32 v181, v182, v183\n\t" \
  "v_cvt_pknorm_i16_f32 v222, |v144|, |v145|\n\t" \
  "s_waitcnt vmcnt(2)\n\t" \
  "ds_read_b64_tr_b16 v[218:219], v248 offset:48680\n\t" \
  "v_mfma_f32_16x16x32_bf16 v[156:159], v[208:211], v[100:103], 0\n\t" \
  "v_lshlrev_b32_sdwa v221, 2, v220 dst_sel:DWORD dst_unused:UNUSED_PAD src0_sel:DWORD src1_sel:BYTE_3\n\t" \
  "v_lshlrev_b32_sdwa v220, 2, v220 dst_sel:DWORD dst_unused:UNUSED_PAD src0_sel:DWORD src1_sel:BYTE_1\n\t" \
  "s_mov_b64 exec, 3\n\t" \
  "ds_add_rtn_u32 v246, v251, v250 offset:47168\n\t" \
  "v_mfma_f32_16x16x32_bf16 v[164:167], v[208:211], v[104:107], 0\n\t" \
  "s_mov_b64 exec, -1\n\t" \
  "v_lshlrev_b32_sdwa v223, 2, v222 dst_sel:DWORD dst_unused:UNUSED_PAD src0_sel:DWORD src1_sel:BYTE_3\n\t" \
  "v_lshlrev_b32_sdwa v222, 2, v222 dst_sel:DWORD dst_unused:UNUSED_PAD src0_sel:DWORD src1_sel:BYTE_1\n\t" \
  "s_waitcnt lgkmcnt(5)\n\t" \
  "s_setprio 2\n\t" \
  "ds_read_u16 v234, v234 offset:10528\n\t" \
  "v_mfma_f32_16x16x32_bf16 v[156:159], v[212:215], v[116:119], v[156:159]\n\t" \
  "v_lshlrev_b32_sdwa v227, 1, v153 dst_sel:DWORD dst_unused:UNUSED_PAD src0_sel:DWORD src1_sel:WORD_1\n\t" \
  "v_lshlrev_b32_sdwa v226, 1, v152 dst_sel:DWORD dst_unused:UNUSED_PAD src0_sel:DWORD src1_sel:WORD_1\n\t" \
  "ds_read_b32 v220, v220 offset:10016\n\t" \
  "ds_read_b32 v221, v221 offset:10016\n\t" \
  "v_mfma_f32_16x16x32_bf16 v[172:175], v[208:211], v[108:111], 0\n\t" \
  "s_waitcnt lgkmcnt(3)\n\t" \
  "v_lshlrev_b32_sdwa v225, 1, v155 dst_sel:DWORD dst_unused:UNUSED_PAD src0_sel:DWORD src1_sel:WORD_1\n\t" \
  "v_readfirstlane_b32 s4, v246\n\t" \
  "ds_read_b32 v223, v223 offset:10016\n\t" \
  "v_mfma_f32_16x16x32_bf16 v[148:151], v[212:215], v[112:115], v[148:151]\n\t" \
  "s_cmpk_lt_u32 s4, 6144\n\t" \
  "v_lshlrev_b32_sdwa v224, 1, v154 dst_sel:DWORD dst_unused:UNUSED_PAD src0_sel:DWORD src1_sel:WORD_1\n\t" \
  "s_cbranch_scc1 .Lcnx_w1_5\n\t" \
  ".Lcnx_g1_5:\n\t" \
  "s_setprio 0\n\t" \
  "v_mfma_f32_16x16x32_bf16 v[164:167], v[212:215], v[120:123], v[164:167]\n\t" \
  "v_fma_mix_f32 v228, |v196|, v228, v228 op_sel:[0,1,0] op_sel_hi:[0,1,1] clamp\n\t" \
  "v_fma_mix_f32 v229, |v197|, v229, v229 op_sel:[0,1,0] op_sel_hi:[0,1,1] clamp\n\t" \
  "ds_read_b64_tr_b16 v[208:209], v249 offset:12304\n\t" \
  "ds_read_b64_tr_b16 v[210:211], v249 offset:13840\n\t" \
  "v_mfma_f32_16x16x32_bf16 v[172:175], v[212:215], v[124:127], v[172:175]\n\t" \
  "v_fma_mix_f32 v231, |v199|, v231, v231 op_sel:[0,1,0] op_sel_hi:[0,1,1] clamp\n\t" \
  "v_fma_mix_f32 v230, |v198|, v230, v230 op_sel:[0,1,0] op_sel_hi:[0,1,1] clamp\n\t" \
  "s_waitcnt lgkmcnt(5)\n\t" \
  "ds_read_b64_tr_b16 v[212:213], v249 offset:12312\n\t" \
  "ds_read_b32 v222, v222 offset:10016\n\t" \
  "v_mfma_f32_16x16x32_bf16 v[148:151], v[216:219], v[128:131], v[148:151]\n\t" \
  "s_mov_b32 m0, s45\n\t" \
  "v_or_b32 v204, v232, v233\n\t" \
  "v_or_b32 v205, v234, v235\n\t" \
  "s_mov_b32 exec_hi, 0xffff\n\t" \
  "buffer_load_dwordx4 v252, s[16:19], s33 offen lds\n\t" \
  "v_mfma_f32_16x16x32_bf16 v[156:159], v[216:219], v[132:135], v[156:159]\n\t" \
  "s_mov_b32 exec_hi, -1\n\t" \
  "v_fma_f32 v196, |v196|, v228, v196 mul:4\n\t" \
  "v_fma_f32 v197, |v197|, v229, v197 mul:4\n\t" \
  "s_add_u32 s33, s33, s32\n\t" \
  "ds_read_u16_d16_hi v227, v227 offset:10528\n\t" \
  "v_mfma_f32_16x16x32_bf16 v[164:167], v[216:219], v[136:139], v[164:167]\n\t" \
  "v_fma_f32 v199, |v199|, v231, v199 mul:4\n\t" \
  "v_fma_f32 v198, |v198|, v230, v198 mul:4\n\t" \
  "ds_read_u16_d16_hi v225, v225 offset:10528\n\t" \
  "s_waitcnt lgkmcnt(4)\n\t" \
  "v_mfma_f32_16x16x32_bf16 v[172:175], v[216:219], v[140:143], v[172:175]\n\t" \
  "v_cvt_pk_bf16_f32 v196, v196, v197\n\t" \
  "v_cvt_pk_bf16_f32 v197, v198, v199\n\t" \
  "ds_read_b64_tr_b16 v[214:215], v249 offset:13848\n\t" \
  "ds_read_b64_tr_b16 v[216:217], v249 offset:12368\n\t" \
  "v_mfma_f32_16x16x32_bf16 v[0:3], v[208:211], v[178:181], v[0:3]\n\t" \
  "v_cvt_pknorm_i16_f32 v228, |v162|, |v163|\n\t" \
  "v_cvt_pknorm_i16_f32 v230, |v160|, |v161|\n\t" \
  "s_setprio 2\n\t" \
  "s_setprio 0\n\t" \
  "s_setprio 1\n\t" \
  "ds_read_b64_tr_b16 v[218:219], v249 offset:13904\n\t" \
  "v_mfma_f32_16x16x32_bf16 v[24:27], v[208:211], v[186:189], v[24:27]\n\t" \
  "v_lshlrev_b32_sdwa v229, 2, v228 dst_sel:DWORD dst_unused:UNUSED_PAD src0_sel:DWORD src1_sel:BYTE_3\n\t" \
  "v_lshlrev_b32_sdwa v228, 2, v228 dst_sel:DWORD dst_unused:UNUSED_PAD src0_sel:DWORD src1_sel:BYTE_1\n\t" \
  "s_add_u32 m0, s45, s35\n\t" \
  "ds_read_b32 v228, v228 offset:10016\n\t" \
  "ds_read_u16 v224, v224 offset:10528\n\t" \
  "v_mfma_f32_16x16x32_bf16 v[72:75], v[208:211], v[202:205], v[72:75]\n\t" \
  "s_waitcnt lgkmcnt(4)\n\t" \
  "v_lshlrev_b32_sdwa v232, 1, v170 dst_sel:DWORD dst_unused:UNUSED_PAD src0_sel:DWORD src1_sel:WORD_1\n\t" \
  "v_lshlrev_b32_sdwa v233, 1, v171 dst_sel:DWORD dst_unused:UNUSED_PAD src0_sel:DWORD src1_sel:WORD_1\n\t" \
  "v_mfma_f32_16x16x32_bf16 v[48:51], v[208:211], v[194:197], v[48:51]\n\t" \
  "v_lshlrev_b32_sdwa v231, 2, v230 dst_sel:DWORD dst_unused:UNUSED_PAD src0_sel:DWORD src1_sel:BYTE_3\n\t" \
  "s_mov_b32 exec_hi, 0xffff\n\t" \
  "buffer_load_dwordx4 v252, s[16:19], s34 offen lds\n\t" \
  "v_mfma_f32_16x16x32_bf16 v[4:7], v[212:215], v[178:181], v[4:7]\n\t" \
  "s_mov_b32 exec_hi, -1\n\t" \
  "s_add_u32 s34, s34, s32\n\t" \
  "v_lshlrev_b32_sdwa v235, 1, v169 dst_sel:DWORD dst_unused:UNUSED_PAD src0_sel:DWORD src1_sel:WORD_1\n\t" \
  "ds_read_b64_tr_b16 v[208:209], v249 offset:12376\n\t" \
  "ds_read_u16 v226, v226 offset:10528\n\t" \
  "v_mfma_f32_16x16x32_bf16 v[28:31], v[212:215], v[186:189], v[28:31]\n\t" \
  "v_lshlrev_b32_sdwa v234, 1, v168 dst_sel:DWORD dst_unused:UNUSED_PAD src0_sel:DWORD src1_sel:WORD_1\n\t" \
  "v_lshlrev_b32_sdwa v230, 2, v230 dst_sel:DWORD dst_unused:UNUSED_PAD src0_sel:DWORD src1_sel:BYTE_1\n\t" \
  "ds_read_b32 v230, v230 offset:10016\n\t" \
  "v_mfma_f32_16x16x32_bf16 v[52:55], v[212:215], v[194:197], v[52:55]\n\t" \
  "v_fma_mix_f32 v221, |v147|, v221, v221 op_sel:[0,1,0] op_sel_hi:[0,1,1] clamp\n\t" \
  "v_fma_mix_f32 v220, |v146|, v220, v220 op_sel:[0,1,0] op_sel_hi:[0,1,1] clamp\n\t" \
  "s_setprio 1\n\t" \
  "ds_read_b64_tr_b16 v[210:211], v249 offset:13912\n\t" \
  "s_add_u32 m0, s46, s50\n\t" \
  "v_mfma_f32_16x16x32_bf16 v[76:79], v[212:215], v[202:205], v[76:79]\n\t" \
  "v_fma_mix_f32 v223, |v145|, v223, v223 op_sel:[0,1,0] op_sel_hi:[0,1,1] clamp\n\t" \
  "v_fma_mix_f32 v222, |v144|, v222, v222 op_sel:[0,1,0] op_sel_hi:[0,1,1] clamp\n\t" \
  "s_waitcnt lgkmcnt(2)\n\t" \
  "s_setprio 1\n\t" \
  "ds_read_b32 v231, v231 offset:10016\n\t" \
  "ds_read_b64_tr_b16 v[212:213], v249 offset:12432\n\t" \
  "v_mfma_f32_16x16x32_bf16 v[8:11], v[216:219], v[178:181], v[8:11]\n\t" \
  "v_or_b32 v155, v224, v225\n\t" \
  "v_or_b32 v154, v226, v227\n\t" \
  "ds_read_b32 v229, v229 offset:10016\n\t" \
  "ds_read_u16_d16_hi v233, v233 offset:10528\n\t" \
  "v_mfma_f32_16x16x32_bf16 v[32:35], v[216:219], v[186:189], v[32:35]\n\t" \
  "s_setprio 1\n\t" \
  "v_fma_f32 v146, |v146|, v220, v146 mul:4\n\t" \
  "v_fma_f32 v147, |v147|, v221, v147 mul:4\n\t" \
  "s_setprio 3\n\t" \
  "s_setprio 3\n\t" \
  "buffer_load_dwordx4 v[236:239], v250, s[8:11], s14 offen offset:0 nt\n\t" \
  "v_mfma_f32_16x16x32_bf16 v[56:59], v[216:219], v[194:197], v[56:59]\n\t" \
  "v_fma_f32 v144, |v144|, v222, v144 mul:4\n\t" \
  "v_fma_f32 v145, |v145|, v223, v145 mul:4\n\t" \
  "ds_read_u16 v232, v232 offset:10528\n\t" \
  "v_mfma_f32_16x16x32_bf16 v[80:83], v[216:219], v[202:205], v[80:83]\n\t" \
  "s_waitcnt lgkmcnt(5)\n\t" \
  "v_cvt_pk_bf16_f32 v147, v146, v147\n\t" \
  "v_cvt_pk_bf16_f32 v146, v144, v145\n\t" \
  "ds_read_u16_d16_hi v235, v235 offset:10528\n\t" \
  "s_add_u32 s3, s33, s51\n\t" \
  "v_mfma_f32_16x16x32_bf16 v[12:15], v[208:211], v[178:181], v[12:15]\n\t" \
  "v_cvt_pknorm_i16_f32 v222, |v150|, |v151|\n\t" \
  "v_cvt_pknorm_i16_f32 v220, |v148|, |v149|\n\t" \
  "s_setprio 3\n\t" \
  "ds_read_b64_tr_b16 v[214:215], v249 offset:13968\n\t" \
  "ds_read_b64_tr_b16 v[216:217], v249 offset:12440\n\t" \
  "v_mfma_f32_16x16x32_bf16 v[36:39], v[208:211], v[186:189], v[36:39]\n\t" \
  "s_setprio 0\n\t" \
  "v_lshlrev_b32_sdwa v221, 2, v220 dst_sel:DWORD dst_unused:UNUSED_PAD src0_sel:DWORD src1_sel:BYTE_3\n\t" \
  "v_lshlrev_b32_sdwa v220, 2, v220 dst_sel:DWORD dst_unused:UNUSED_PAD src0_sel:DWORD src1_sel:BYTE_1\n\t" \
  "s_setprio 1\n\t" \
  "ds_read_b64_tr_b16 v[218:219], v249 offset:13976\n\t" \
  "v_mfma_f32_16x16x32_bf16 v[60:63], v[208:211], v[194:197], v[60:63]\n\t" \
  "s_setprio 1\n\t" \
  "v_lshlrev_b32_sdwa v223, 2, v222 dst_sel:DWORD dst_unused:UNUSED_PAD src0_sel:DWORD src1_sel:BYTE_3\n\t" \
  "v_lshlrev_b32_sdwa v222, 2, v222 dst_sel:DWORD dst_unused:UNUSED_PAD src0_sel:DWORD src1_sel:BYTE_1\n\t" \
  "ds_read_u16 v234, v234 offset:10528\n\t" \
  "ds_read_b32 v220, v220 offset:10016\n\t" \
  "v_mfma_f32_16x16x32_bf16 v[84:87], v[208:211], v[202:205], v[84:87]\n\t" \
  "s_waitcnt lgkmcnt(4)\n\t" \
  "v_lshlrev_b32_sdwa v224, 1, v156 dst_sel:DWORD dst_unused:UNUSED_PAD src0_sel:DWORD src1_sel:WORD_1\n\t" \
  "v_lshlrev_b32_sdwa v225, 1, v157 dst_sel:DWORD dst_unused:UNUSED_PAD src0_sel:DWORD src1_sel:WORD_1\n\t" \
  "s_mov_b64 exec, s[68:69]\n\t" \
  "buffer_load_dwordx4 v252, s[16:19], s3 offen lds\n\t" \
  "v_mfma_f32_16x16x32_bf16 v[16:19], v[212:215], v[178:181], v[16:19]\n\t" \
  "s_mov_b64 exec, -1\n\t" \
  "s_setprio 2\n\t" \
  "v_nop\n\t" \
  "ds_read_b32 v223, v223 offset:10016\n\t" \
  "v_mfma_f32_16x16x32_bf16 v[64:67], v[212:215], v[194:197], v[64:67]\n\t" \
  "v_fma_mix_f32 v229, |v163|, v229, v229 op_sel:[0,1,0] op_sel_hi:[0,1,1] clamp\n\t" \
  "v_lshlrev_b32_sdwa v227, 1, v159 dst_sel:DWORD dst_unused:UNUSED_PAD src0_sel:DWORD src1_sel:WORD_1\n\t" \
  "s_setprio 2\n\t" \
  "ds_read_b32 v221, v221 offset:10016\n\t" \
  "ds_read_b64_tr_b16 v[208:209], v248 offset:49192\n\t" \
  "v_mfma_f32_16x16x32_bf16 v[40:43], v[212:215], v[186:189], v[40:43]\n\t" \
  "v_lshlrev_b32_sdwa v226, 1, v158 dst_sel:DWORD dst_unused:UNUSED_PAD src0_sel:DWORD src1_sel:WORD_1\n\t" \
  "v_fma_mix_f32 v228, |v162|, v228, v228 op_sel:[0,1,0] op_sel_hi:[0,1,1] clamp\n\t" \
  "s_waitcnt lgkmcnt(4)\n\t" \
  "ds_read_b64_tr_b16 v[210:211], v248 offset:50216\n\t" \
  "ds_read_b32 v222, v222 offset:10016\n\t" \
  "v_mfma_f32_16x16x32_bf16 v[20:23], v[216:219], v[178:181], v[20:23]\n\t" \
  "v_or_b32 v171, v232, v233\n\t" \
  "v_or_b32 v170, v234, v235\n\t" \
  "ds_read_u16_d16_hi v225, v225 offset:10528\n\t" \
  "v_mfma_f32_16x16x32_bf16 v[88:91], v[212:215], v[202:205], v[88:91]\n\t" \
  "s_add_u32 m0, s46, s36\n\t" \
  "v_fma_mix_f32 v231, |v161|, v231, v231 op_sel:[0,1,0] op_sel_hi:[0,1,1] clamp\n\t" \
  "v_fma_mix_f32 v230, |v160|, v230, v230 op_sel:[0,1,0] op_sel_hi:[0,1,1] clamp\n\t" \
  "s_setprio 2\n\t" \
  "s_setprio 2\n\t" \
  "ds_read_b64_tr_b16 v[212:213], v248 offset:52264\n\t" \
  "v_mfma_f32_16x16x32_bf16 v[68:71], v[216:219], v[194:197], v[68:71]\n\t" \
  "v_fma_f32 v160, |v160|, v230, v160 mul:4\n\t" \
  "v_fma_f32 v163, |v163|, v229, v163 mul:4\n\t" \
  "ds_read_b64_tr_b16 v[214:215], v248 offset:53288\n\t" \
  "s_setprio 1\n\t" \
  "v_fma_f32 v162, |v162|, v228, v162 mul:4\n\t" \
  "s_setprio 2\n\t" \
  "v_cvt_pk_bf16_f32 v163, v162, v163\n\t" \
  "s_add_u32 s3, s34, s51\n\t" \
  "s_setprio 2\n\t" \
  "s_setprio 0\n\t" \
  "ds_read_u16 v224, v224 offset:10528\n\t" \
  "s_waitcnt lgkmcnt(5)\n\t" \
  "v_mfma_f32_16x16x32_bf16 v[44:47], v[216:219], v[186:189], v[44:47]\n\t" \
  "v_fma_f32 v161, |v161|, v231, v161 mul:4\n\t" \
  "v_cvt_pk_bf16_f32 v162, v160, v161\n\t" \
  "s_mov_b64 exec, s[68:69]\n\t" \
  "buffer_load_dwordx4 v252, s[16:19], s3 offen lds\n\t" \
  "v_mfma_f32_16x16x32_bf16 v[176:179], v[208:211], v[96:99], 0\n\t" \
  "s_mov_b64 exec, -1\n\t" \
  "v_lshlrev_b32_sdwa v232, 1, v172 dst_sel:DWORD dst_unused:UNUSED_PAD src0_sel:DWORD src1_sel:WORD_1\n\t" \
  "v_cvt_pknorm_i16_f32 v230, |v166|, |v167|\n\t" \
  "ds_read_u16_d16_hi v227, v227 offset:10528\n\t" \
  "s_setprio 2\n\t" \
  "v_mfma_f32_16x16x32_bf16 v[92:95], v[216:219], v[202:205], v[92:95]\n\t" \
  "v_nop\n\t" \
  "s_setprio 0\n\t" \
  "s_setprio 2\n\t" \
  "s_setprio 3\n\t" \
  "ds_read_b64_tr_b16 v[216:217], v248 offset:51240\n\t" \
  "v_mfma_f32_16x16x32_bf16 v[192:195], v[208:211], v[104:107], 0\n\t" \
  "v_cvt_pknorm_i16_f32 v228, |v164|, |v165|\n\t" \
  "v_lshlrev_b32_sdwa v231, 2, v230 dst_sel:DWORD dst_unused:UNUSED_PAD src0_sel:DWORD src1_sel:BYTE_3\n\t" \
  "ds_read_b32 v231, v231 offset:10016\n\t" \
  "v_mfma_f32_16x16x32_bf16 v[184:187], v[208:211], v[100:103], 0\n\t" \
  "s_waitcnt lgkmcnt(4)\n\t" \
  "v_lshlrev_b32_sdwa v229, 2, v228 dst_sel:DWORD dst_unused:UNUSED_PAD src0_sel:DWORD src1_sel:BYTE_3\n\t" \
  "v_lshlrev_b32_sdwa v228, 2, v228 dst_sel:DWORD dst_unused:UNUSED_PAD src0_sel:DWORD src1_sel:BYTE_1\n\t" \
  "ds_read_u16 v226, v226 offset:10528\n\t" \
  "ds_read_b64_tr_b16 v[218:219], v248 offset:54312\n\t" \
  "v_mfma_f32_16x16x32_bf16 v[200:203], v[208:211], v[108:111], 0\n\t" \
  "v_nop\n\t" \
  "v_lshlrev_b32_sdwa v235, 1, v175 dst_sel:DWORD dst_unused:UNUSED_PAD src0_sel:DWORD src1_sel:WORD_1\n\t" \
  "ds_read_b64_tr_b16 v[210:211], v248 offset:50728\n\t" \
  "v_mfma_f32_16x16x32_bf16 v[200:203], v[212:215], v[124:127], v[200:203]\n\t" \
  "s_setprio 1\n\t" \
  "s_setprio 2\n\t" \
  "v_lshlrev_b32_sdwa v230, 2, v230 dst_sel:DWORD dst_unused:UNUSED_PAD src0_sel:DWORD src1_sel:BYTE_1\n\t" \
  "ds_read_b64_tr_b16 v[208:209], v248 offset:49704\n\t" \
  "ds_read_b32 v229, v229 offset:10016\n\t" \
  "v_mfma_f32_16x16x32_bf16 v[176:179], v[212:215], v[112:115], v[176:179]\n\t" \
  "v_fma_mix_f32 v221, |v149|, v221, v221 op_sel:[0,1,0] op_sel_hi:[0,1,1] clamp\n\t" \
  "v_lshlrev_b32_sdwa v233, 1, v173 dst_sel:DWORD dst_unused:UNUSED_PAD src0_sel:DWORD src1_sel:WORD_1\n\t" \
  "ds_read_u16_d16_hi v233, v233 offset:10528\n\t" \
  "v_mfma_f32_16x16x32_bf16 v[184:187], v[212:215], v[116:119], v[184:187]\n\t" \
  "v_lshlrev_b32_sdwa v234, 1, v174 dst_sel:DWORD dst_unused:UNUSED_PAD src0_sel:DWORD src1_sel:WORD_1\n\t" \
  "v_fma_mix_f32 v222, |v150|, v222, v222 op_sel:[0,1,0] op_sel_hi:[0,1,1] clamp\n\t" \
  "s_waitcnt lgkmcnt(4)\n\t" \
  "s_setprio 0\n\t" \
  "ds_read_b32 v228, v228 offset:10016\n\t" \
  "v_mfma_f32_16x16x32_bf16 v[192:195], v[212:215], v[120:123], v[192:195]\n\t" \
  "v_fma_mix_f32 v223, |v151|, v223, v223 op_sel:[0,1,0] op_sel_hi:[0,1,1] clamp\n\t" \
  "v_fma_mix_f32 v220, |v148|, v220, v220 op_sel:[0,1,0] op_sel_hi:[0,1,1] clamp\n\t" \
  "ds_read_b64_tr_b16 v[212:213], v248 offset:52776\n\t" \
  "v_mfma_f32_16x16x32_bf16 v[176:179], v[216:219], v[128:131], v[176:179]\n\t" \
  "v_or_b32 v157, v226, v227\n\t" \
  "v_or_b32 v156, v224, v225\n\t" \
  "ds_read_b64_tr_b16 v[214:215], v248 offset:53800\n\t" \
  "ds_read_u16 v232, v232 offset:10528\n\t" \
  "v_mfma_f32_16x16x32_bf16 v[192:195], v[216:219], v[136:139], v[192:195]\n\t" \
  "v_fma_f32 v150, |v150|, v222, v150 mul:4\n\t" \
  "v_fma_f32 v151, |v151|, v223, v151 mul:4\n\t" \
  "ds_read_u16_d16_hi v235, v235 offset:10528\n\t" \
  "v_mfma_f32_16x16x32_bf16 v[184:187], v[216:219], v[132:135], v[184:187]\n\t" \
  "v_fma_f32 v149, |v149|, v221, v149 mul:4\n\t" \
  "v_fma_f32 v148, |v148|, v220, v148 mul:4\n\t" \
  "ds_read_b32 v230, v230 offset:10016\n\t" \
  "v_mfma_f32_16x16x32_bf16 v[200:203], v[216:219], v[140:143], v[200:203]\n\t" \
  "v_cvt_pk_bf16_f32 v148, v148, v149\n\t" \
  "v_cvt_pk_bf16_f32 v149, v150, v151\n\t" \
  "s_setprio 0\n\t" \
  "ds_read_b64_tr_b16 v[216:217], v248 offset:51752\n\t" \
  "s_waitcnt lgkmcnt(9)\n\t" \
  "v_mfma_f32_16x16x32_bf16 v[180:183], v[208:211], v[96:99], 0\n\t" \
  "v_cvt_pknorm_i16_f32 v220, |v178|, |v179|\n\t" \
  "v_cvt_pknorm_i16_f32 v222, |v176|, |v177|\n\t" \
  "s_waitcnt vmcnt(2)\n\t" \
  "ds_read_b64_tr_b16 v[218:219], v248 offset:54824\n\t" \
  "v_mfma_f32_16x16x32_bf16 v[188:191], v[208:211], v[100:103], 0\n\t" \
  "v_lshlrev_b32_sdwa v221, 2, v220 dst_sel:DWORD dst_unused:UNUSED_PAD src0_sel:DWORD src1_sel:BYTE_3\n\t" \
  "v_lshlrev_b32_sdwa v220, 2, v220 dst_sel:DWORD dst_unused:UNUSED_PAD src0_sel:DWORD src1_sel:BYTE_1\n\t" \
  "s_mov_b64 exec, 3\n\t" \
  "ds_add_rtn_u32 v246, v251, v250 offset:47176\n\t" \
  "v_mfma_f32_16x16x32_bf16 v[196:199], v[208:211], v[104:107], 0\n\t" \
  "s_mov_b64 exec, -1\n\t" \
  "v_lshlrev_b32_sdwa v226, 1, v184 dst_sel:DWORD dst_unused:UNUSED_PAD src0_sel:DWORD src1_sel:WORD_1\n\t" \
  "v_lshlrev_b32_sdwa v223, 2, v222 dst_sel:DWORD dst_unused:UNUSED_PAD src0_sel:DWORD src1_sel:BYTE_3\n\t" \
  "ds_read_b32 v223, v223 offset:10016\n\t" \
  "v_mfma_f32_16x16x32_bf16 v[204:207], v[208:211], v[108:111], 0\n\t" \
  "v_lshlrev_b32_sdwa v225, 1, v187 dst_sel:DWORD dst_unused:UNUSED_PAD src0_sel:DWORD src1_sel:WORD_1\n\t" \
  "v_lshlrev_b32_sdwa v222, 2, v222 dst_sel:DWORD dst_unused:UNUSED_PAD src0_sel:DWORD src1_sel:BYTE_1\n\t" \
  "ds_read_u16 v234, v234 offset:10528\n\t" \
  "s_waitcnt lgkmcnt(8)\n\t" \
  "v_mfma_f32_16x16x32_bf16 v[188:191], v[212:215], v[116:119], v[188:191]\n\t" \
  "s_waitcnt lgkmcnt(2)\n\t" \
  "v_lshlrev_b32_sdwa v227, 1, v185 dst_sel:DWORD dst_unused:UNUSED_PAD src0_sel:DWORD src1_sel:WORD_1\n\t" \
  "v_readfirstlane_b32 s4, v246\n\t" \
  "s_setprio 2\n\t" \
  "ds_read_b32 v221, v221 offset:10016\n\t" \
  "s_setprio 0\n\t" \
  "v_mfma_f32_16x16x32_bf16 v[180:183], v[212:215], v[112:115], v[180:183]\n\t" \
  "s_cmpk_lt_u32 s4, 6144\n\t" \
  "v_lshlrev_b32_sdwa v224, 1, v186 dst_sel:DWORD dst_unused:UNUSED_PAD src0_sel:DWORD src1_sel:WORD_1\n\t" \
  "s_cbranch_scc1 .Lcnx_w1_6\n\t" \
  ".Lcnx_g1_6:\n\t" \
  "v_mfma_f32_16x16x32_bf16 v[196:199], v[212:215], v[120:123], v[196:199]\n\t" \
  "v_fma_mix_f32 v228, |v164|, v228, v228 op_sel:[0,1,0] op_sel_hi:[0,1,1] clamp\n\t" \
  "v_fma_mix_f32 v229, |v165|, v229, v229 op_sel:[0,1,0] op_sel_hi:[0,1,1] clamp\n\t" \
  "ds_read_b64_tr_b16 v[210:211], v249 offset:26136\n\t" \
  "ds_read_b64_tr_b16 v[208:209], v249 offset:24600\n\t" \
  "v_mfma_f32_16x16x32_bf16 v[204:207], v[212:215], v[124:127], v[204:207]\n\t" \
  "v_fma_mix_f32 v231, |v167|, v231, v231 op_sel:[0,1,0] op_sel_hi:[0,1,1] clamp\n\t" \
  "v_fma_mix_f32 v230, |v166|, v230, v230 op_sel:[0,1,0] op_sel_hi:[0,1,1] clamp\n\t" \
  "s_waitcnt lgkmcnt(3)\n\t" \
  "ds_read_b32 v222, v222 offset:10016\n\t" \
  "ds_read_b64_tr_b16 v[212:213], v249 offset:24608\n\t" \
  "v_mfma_f32_16x16x32_bf16 v[180:183], v[216:219], v[128:131], v[180:183]\n\t" \
  "s_mov_b32 m0, s46\n\t" \
  "v_or_b32 v172, v232, v233\n\t" \
  "v_or_b32 v173, v234, v235\n\t" \
  "s_mov_b32 exec_hi, 0xffff\n\t" \
  "buffer_load_dwordx4 v252, s[16:19], s33 offen lds\n\t" \
  "v_mfma_f32_16x16x32_bf16 v[188:191], v[216:219], v[132:135], v[188:191]\n\t" \
  "s_mov_b32 exec_hi, -1\n\t" \
  "v_fma_f32 v165, |v165|, v229, v165 mul:4\n\t" \
  "v_fma_f32 v164, |v164|, v228, v164 mul:4\n\t" \
  "s_add_u32 s33, s33, s32\n\t" \
  "ds_read_u16_d16_hi v227, v227 offset:10528\n\t" \
  "ds_read_b32 v220, v220 offset:10016\n\t" \
  "v_mfma_f32_16x16x32_bf16 v[196:199], v[216:219], v[136:139], v[196:199]\n\t" \
  "v_fma_f32 v166, |v166|, v230, v166 mul:4\n\t" \
  "v_fma_f32 v167, |v167|, v231, v167 mul:4\n\t" \
  "ds_read_b64_tr_b16 v[214:215], v249 offset:26144\n\t" \
  "v_mfma_f32_16x16x32_bf16 v[204:207], v[216:219], v[140:143], v[204:207]\n\t" \
  "v_cvt_pk_bf16_f32 v164, v164, v165\n\t" \
  "v_cvt_pk_bf16_f32 v165, v166, v167\n\t" \
  "ds_read_u16_d16_hi v225, v225 offset:10528\n\t" \
  "ds_read_b64_tr_b16 v[216:217], v249 offset:24664\n\t" \
  "v_mfma_f32_4x4x4_16b_bf16 v[48:51], v[242:243], v[236:237], v[48:51]\n\t" \
  "s_waitcnt lgkmcnt(7)\n\t" \
  "v_mfma_f32_16x16x32_bf16 v[0:3], v[208:211], v[146:149], v[0:3]\n\t" \
  "v_mfma_f32_4x4x4_16b_bf16 v[52:55], v[242:243], v[238:239], v[52:55]\n\t" \
  "v_cvt_pknorm_i16_f32 v230, |v192|, |v193|\n\t" \
  "v_cvt_pknorm_i16_f32 v228, |v194|, |v195|\n\t" \
  "ds_read_u16 v224, v224 offset:10528\n\t" \
  "v_mfma_f32_16x16x32_bf16 v[48:51], v[208:211], v[162:165], v[48:51]\n\t" \
  "v_lshlrev_b32_sdwa v231, 2, v230 dst_sel:DWORD dst_unused:UNUSED_PAD src0_sel:DWORD src1_sel:BYTE_3\n\t" \
  "s_add_u32 m0, s46, s35\n\t" \
  "s_mov_b32 exec_hi, 0xffff\n\t" \
  "buffer_load_dwordx4 v252, s[16:19], s34 offen lds\n\t" \
  "v_mfma_f32_16x16x32_bf16 v[72:75], v[208:211], v[170:173], v[72:75]\n\t" \
  "s_mov_b32 exec_hi, -1\n\t" \
  "v_lshlrev_b32_sdwa v232, 1, v202 dst_sel:DWORD dst_unused:UNUSED_PAD src0_sel:DWORD src1_sel:WORD_1\n\t" \
  "s_setprio 3\n\t" \
  "s_setprio 1\n\t" \
  "s_waitcnt lgkmcnt(3)\n\t" \
  "ds_read_b64_tr_b16 v[218:219], v249 offset:26200\n\t" \
  "v_mfma_f32_16x16x32_bf16 v[24:27], v[208:211], v[154:157], v[24:27]\n\t" \
  "v_lshlrev_b32_sdwa v229, 2, v228 dst_sel:DWORD dst_unused:UNUSED_PAD src0_sel:DWORD src1_sel:BYTE_3\n\t" \
  "v_lshlrev_b32_sdwa v228, 2, v228 dst_sel:DWORD dst_unused:UNUSED_PAD src0_sel:DWORD src1_sel:BYTE_1\n\t" \
  "ds_read_u16 v226, v226 offset:10528\n\t" \
  "ds_read_b64_tr_b16 v[208:209], v249 offset:24672\n\t" \
  "v_mfma_f32_16x16x32_bf16 v[4:7], v[212:215], v[146:149], v[4:7]\n\t" \
  "v_lshlrev_b32_sdwa v233, 1, v203 dst_sel:DWORD dst_unused:UNUSED_PAD src0_sel:DWORD src1_sel:WORD_1\n\t" \
  "v_lshlrev_b32_sdwa v235, 1, v201 dst_sel:DWORD dst_unused:UNUSED_PAD src0_sel:DWORD src1_sel:WORD_1\n\t" \
  "s_setprio 3\n\t" \
  "ds_read_b64_tr_b16 v[210:211], v249 offset:26208\n\t" \
  "v_mfma_f32_16x16x32_bf16 v[28:31], v[212:215], v[154:157], v[28:31]\n\t" \
  "v_fma_mix_f32 v222, |v176|, v222, v222 op_sel:[0,1,0] op_sel_hi:[0,1,1] clamp\n\t" \
  "v_lshlrev_b32_sdwa v234, 1, v200 dst_sel:DWORD dst_unused:UNUSED_PAD src0_sel:DWORD src1_sel:WORD_1\n\t" \
  "s_add_u32 s34, s34, s32\n\t" \
  "ds_read_b32 v228, v228 offset:10016\n\t" \
  "v_mfma_f32_16x16x32_bf16 v[76:79], v[212:215], v[170:173], v[76:79]\n\t" \
  "v_lshlrev_b32_sdwa v230, 2, v230 dst_sel:DWORD dst_unused:UNUSED_PAD src0_sel:DWORD src1_sel:BYTE_1\n\t" \
  "v_fma_mix_f32 v223, |v177|, v223, v223 op_sel:[0,1,0] op_sel_hi:[0,1,1] clamp\n\t" \
  "ds_read_b32 v230, v230 offset:10016\n\t" \
  "ds_read_u16 v232, v232 offset:10528\n\t" \
  "v_mfma_f32_16x16x32_bf16 v[52:55], v[212:215], v[162:165], v[52:55]\n\t" \
  "v_fma_mix_f32 v220, |v178|, v220, v220 op_sel:[0,1,0] op_sel_hi:[0,1,1] clamp\n\t" \
  "v_fma_mix_f32 v221, |v179|, v221, v221 op_sel:[0,1,0] op_sel_hi:[0,1,1] clamp\n\t" \
  "s_waitcnt lgkmcnt(5)\n\t" \
  "s_setprio 3\n\t" \
  "ds_read_b64_tr_b16 v[212:213], v249 offset:24728\n\t" \
  "ds_read_b32 v231, v231 offset:10016\n\t" \
  "v_mfma_f32_16x16x32_bf16 v[8:11], v[216:219], v[146:149], v[8:11]\n\t" \
  "v_or_b32 v187, v224, v225\n\t" \
  "v_or_b32 v186, v226, v227\n\t" \
  "buffer_load_dwordx4 v[236:239], v250, s[8:11], s14 offen offset:64 nt\n\t" \
  "v_mfma_f32_16x16x32_bf16 v[32:35], v[216:219], v[154:157], v[32:35]\n\t" \
  "v_fma_f32 v179, |v179|, v221, v179 mul:4\n\t" \
  "v_fma_f32 v178, |v178|, v220, v178 mul:4\n\t" \
  "s_setprio 2\n\t" \
  "s_setprio 0\n\t" \
  "ds_read_b32 v229, v229 offset:10016\n\t" \
  "v_mfma_f32_16x16x32_bf16 v[56:59], v[216:219], v[162:165], v[56:59]\n\t" \
  "v_fma_f32 v176, |v176|, v222, v176 mul:4\n\t" \
  "v_fma_f32 v177, |v177|, v223, v177 mul:4\n\t" \
  "ds_read_u16_d16_hi v233, v233 offset:10528\n\t" \
  "ds_read_b64_tr_b16 v[214:215], v249 offset:26264\n\t" \
  "v_mfma_f32_16x16x32_bf16 v[80:83], v[216:219], v[170:173], v[80:83]\n\t" \
  "v_cvt_pk_bf16_f32 v179, v178, v179\n\t" \
  "v_cvt_pknorm_i16_f32 v220, |v180|, |v181|\n\t" \
  "s_waitcnt lgkmcnt(8)\n\t" \
  "s_setprio 0\n\t" \
  "s_add_u32 m0, s47, s50\n\t" \
  "ds_read_b64_tr_b16 v[216:217], v249 offset:24736\n\t" \
  "v_mfma_f32_16x16x32_bf16 v[36:39], v[208:211], v[154:157], v[36:39]\n\t" \
  "v_lshlrev_b32_sdwa v221, 2, v220 dst_sel:DWORD dst_unused:UNUSED_PAD src0_sel:DWORD src1_sel:BYTE_3\n\t" \
  "v_cvt_pk_bf16_f32 v178, v176, v177\n\t" \
  "ds_read_b64_tr_b16 v[218:219], v249 offset:26272\n\t" \
  "v_mfma_f32_16x16x32_bf16 v[12:15], v[208:211], v[146:149], v[12:15]\n\t" \
  "v_lshlrev_b32_sdwa v220, 2, v220 dst_sel:DWORD dst_unused:UNUSED_PAD src0_sel:DWORD src1_sel:BYTE_1\n\t" \
  "v_cvt_pknorm_i16_f32 v222, |v182|, |v183|\n\t" \
  "ds_read_u16_d16_hi v235, v235 offset:10528\n\t" \
  "v_mfma_f32_16x16x32_bf16 v[60:63], v[208:211], v[162:165], v[60:63]\n\t" \
  "s_add_u32 s3, s33, s51\n\t" \
  "s_setprio 2\n\t" \
  "v_lshlrev_b32_sdwa v225, 1, v189 dst_sel:DWORD dst_unused:UNUSED_PAD src0_sel:DWORD src1_sel:WORD_1\n\t" \
  "s_mov_b64 exec, s[68:69]\n\t" \
  "buffer_load_dwordx4 v252, s[16:19], s3 offen lds\n\t" \
  "v_mfma_f32_16x16x32_bf16 v[84:87], v[208:211], v[170:173], v[84:87]\n\t" \
  "s_mov_b64 exec, -1\n\t" \
  "v_lshlrev_b32_sdwa v223, 2, v222 dst_sel:DWORD dst_unused:UNUSED_PAD src0_sel:DWORD src1_sel:BYTE_3\n\t" \
  "v_lshlrev_b32_sdwa v222, 2, v222 dst_sel:DWORD dst_unused:UNUSED_PAD src0_sel:DWORD src1_sel:BYTE_1\n\t" \
  "s_waitcnt lgkmcnt(3)\n\t" \
  "ds_read_b32 v220, v220 offset:10016\n\t" \
  "ds_read_u16 v234, v234 offset:10528\n\t" \
  "v_mfma_f32_16x16x32_bf16 v[16:19], v[212:215], v[146:149], v[16:19]\n\t" \
  "v_lshlrev_b32_sdwa v224, 1, v188 dst_sel:DWORD dst_unused:UNUSED_PAD src0_sel:DWORD src1_sel:WORD_1\n\t" \
  "v_nop\n\t" \
  "ds_read_b32 v221, v221 offset:10016\n\t" \
  "v_mfma_f32_16x16x32_bf16 v[40:43], v[212:215], v[154:157], v[40:43]\n\t" \
  "v_lshlrev_b32_sdwa v226, 1, v190 dst_sel:DWORD dst_unused:UNUSED_PAD src0_sel:DWORD src1_sel:WORD_1\n\t" \
  "v_fma_mix_f32 v228, |v194|, v228, v228 op_sel:[0,1,0] op_sel_hi:[0,1,1] clamp\n\t" \
  "ds_read_b64_tr_b16 v[208:209], v248 offset:36904\n\t" \
  "s_setprio 0\n\t" \
  "v_mfma_f32_16x16x32_bf16 v[64:67], v[212:215], v[162:165], v[64:67]\n\t" \
  "v_lshlrev_b32_sdwa v227, 1, v191 dst_sel:DWORD dst_unused:UNUSED_PAD src0_sel:DWORD src1_sel:WORD_1\n\t" \
  "v_fma_mix_f32 v229, |v195|, v229, v229 op_sel:[0,1,0] op_sel_hi:[0,1,1] clamp\n\t" \
  "ds_read_b32 v223, v223 offset:10016\n\t" \
  "ds_read_b64_tr_b16 v[210:211], v248 offset:37928\n\t" \
  "v_mfma_f32_16x16x32_bf16 v[88:91], v[212:215], v[170:173], v[88:91]\n\t" \
  "v_fma_mix_f32 v230, |v192|, v230, v230 op_sel:[0,1,0] op_sel_hi:[0,1,1] clamp\n\t" \
  "v_fma_mix_f32 v231, |v193|, v231, v231 op_sel:[0,1,0] op_sel_hi:[0,1,1] clamp\n\t" \
  "s_waitcnt lgkmcnt(4)\n\t" \
  "s_setprio 2\n\t" \
  "ds_read_b32 v222, v222 offset:10016\n\t" \
  "ds_read_b64_tr_b16 v[212:213], v248 offset:39976\n\t" \
  "v_mfma_f32_16x16x32_bf16 v[20:23], v[216:219], v[146:149], v[20:23]\n\t" \
  "v_or_b32 v202, v234, v235\n\t" \
  "v_or_b32 v203, v232, v233\n\t" \
  "ds_read_b64_tr_b16 v[214:215], v248 offset:41000\n\t" \
  "ds_read_u16_d16_hi v225, v225 offset:10528\n\t" \
  "v_mfma_f32_16x16x32_bf16 v[44:47], v[216:219], v[154:157], v[44:47]\n\t" \
  "v_fma_f32 v194, |v194|, v228, v194 mul:4\n\t" \
  "v_fma_f32 v195, |v195|, v229, v195 mul:4\n\t" \
  "s_setprio 1\n\t" \
  "v_mfma_f32_16x16x32_bf16 v[68:71], v[216:219], v[162:165], v[68:71]\n\t" \
  "v_fma_f32 v193, |v193|, v231, v193 mul:4\n\t" \
  "v_fma_f32 v192, |v192|, v230, v192 mul:4\n\t" \
  "ds_read_u16 v224, v224 offset:10528\n\t" \
  "s_waitcnt lgkmcnt(5)\n\t" \
  "s_add_u32 s3, s34, s51\n\t" \
  "v_cvt_pk_bf16_f32 v195, v194, v195\n\t" \
  "s_add_u32 m0, s47, s36\n\t" \
  "s_mov_b64 exec, s[68:69]\n\t" \
  "buffer_load_dwordx4 v252, s[16:19], s3 offen lds\n\t" \
  "v_mfma_f32_16x16x32_bf16 v[92:95], v[216:219], v[170:173], v[92:95]\n\t" \
  "s_mov_b64 exec, -1\n\t" \
  "v_nop\n\t" \
  "v_cvt_pk_bf16_f32 v194, v192, v193\n\t" \
  "ds_read_b64_tr_b16 v[218:219], v248 offset:42024\n\t" \
  "v_mfma_f32_16x16x32_bf16 v[144:147], v[208:211], v[96:99], 0\n\t" \
  "s_nop 0\n\t" \
  "v_cvt_pknorm_i16_f32 v230, |v198|, |v199|\n\t" \
  "v_lshlrev_b32_sdwa v232, 1, v204 dst_sel:DWORD dst_unused:UNUSED_PAD src0_sel:DWORD src1_sel:WORD_1\n\t" \
  "ds_read_b64_tr_b16 v[216:217], v248 offset:38952\n\t" \
  "s_setprio 1\n\t" \
  "v_mfma_f32_16x16x32_bf16 v[160:163], v[208:211], v[104:107], 0\n\t" \
  "v_cvt_pknorm_i16_f32 v228, |v196|, |v197|\n\t" \
  "v_nop\n\t" \
  "ds_read_u16 v226, v226 offset:10528\n\t" \
  "v_mfma_f32_16x16x32_bf16 v[168:171], v[208:211], v[108:111], 0\n\t" \
  "v_lshlrev_b32_sdwa v231, 2, v230 dst_sel:DWORD dst_unused:UNUSED_PAD src0_sel:DWORD src1_sel:BYTE_3\n\t" \
  "v_lshlrev_b32_sdwa v230, 2, v230 dst_sel:DWORD dst_unused:UNUSED_PAD src0_sel:DWORD src1_sel:BYTE_1\n\t" \
  "ds_read_u16_d16_hi v227, v227 offset:10528\n\t" \
  "s_waitcnt lgkmcnt(6)\n\t" \
  "v_mfma_f32_16x16x32_bf16 v[152:155], v[208:211], v[100:103], 0\n\t" \
  "v_lshlrev_b32_sdwa v229, 2, v228 dst_sel:DWORD dst_unused:UNUSED_PAD src0_sel:DWORD src1_sel:BYTE_3\n\t" \
  "v_lshlrev_b32_sdwa v228, 2, v228 dst_sel:DWORD dst_unused:UNUSED_PAD src0_sel:DWORD src1_sel:BYTE_1\n\t" \
  "ds_read_b64_tr_b16 v[208:209], v248 offset:37416\n\t" \
  "v_mfma_f32_16x16x32_bf16 v[144:147], v[212:215], v[112:115], v[144:147]\n\t" \
  "v_fma_mix_f32 v221, |v181|, v221, v221 op_sel:[0,1,0] op_sel_hi:[0,1,1] clamp\n\t" \
  "v_lshlrev_b32_sdwa v233, 1, v205 dst_sel:DWORD dst_unused:UNUSED_PAD src0_sel:DWORD src1_sel:WORD_1\n\t" \
  "ds_read_b32 v228, v228 offset:10016\n\t" \
  "ds_read_b64_tr_b16 v[210:211], v248 offset:38440\n\t" \
  "v_mfma_f32_16x16x32_bf16 v[160:163], v[212:215], v[120:123], v[160:163]\n\t" \
  "v_fma_mix_f32 v223, |v183|, v223, v223 op_sel:[0,1,0] op_sel_hi:[0,1,1] clamp\n\t" \
  "v_lshlrev_b32_sdwa v235, 1, v207 dst_sel:DWORD dst_unused:UNUSED_PAD src0_sel:DWORD src1_sel:WORD_1\n\t" \
  "ds_read_b32 v231, v231 offset:10016\n\t" \
  "ds_read_u16_d16_hi v233, v233 offset:10528\n\t" \
  "v_mfma_f32_16x16x32_bf16 v[152:155], v[212:215], v[116:119], v[152:155]\n\t" \
  "v_fma_mix_f32 v222, |v182|, v222, v222 op_sel:[0,1,0] op_sel_hi:[0,1,1] clamp\n\t" \
  "v_mfma_f32_16x16x32_bf16 v[168:171], v[212:215], v[124:127], v[168:171]\n\t" \
  "v_fma_mix_f32 v220, |v180|, v220, v220 op_sel:[0,1,0] op_sel_hi:[0,1,1] clamp\n\t" \
  "v_lshlrev_b32_sdwa v234, 1, v206 dst_sel:DWORD dst_unused:UNUSED_PAD src0_sel:DWORD src1_sel:WORD_1\n\t" \
  "s_waitcnt lgkmcnt(7)\n\t" \
  "ds_read_b32 v229, v229 offset:10016\n\t" \
  "ds_read_b64_tr_b16 v[212:213], v248 offset:40488\n\t" \
  "v_mfma_f32_16x16x32_bf16 v[160:163], v[216:219], v[136:139], v[160:163]\n\t" \
  "v_fma_f32 v182, |v182|, v222, v182 mul:4\n\t" \
  "v_fma_f32 v183, |v183|, v223, v183 mul:4\n\t" \
  "ds_read_u16_d16_hi v235, v235 offset:10528\n\t" \
  "v_mfma_f32_16x16x32_bf16 v[144:147], v[216:219], v[128:131], v[144:147]\n\t" \
  "s_waitcnt lgkmcnt(8)\n\t" \
  "v_or_b32 v188, v224, v225\n\t" \
  "v_or_b32 v189, v226, v227\n\t" \
  "ds_read_b64_tr_b16 v[214:215], v248 offset:41512\n\t" \
  "v_mfma_f32_16x16x32_bf16 v[152:155], v[216:219], v[132:135], v[152:155]\n\t" \
  "v_fma_f32 v181, |v181|, v221, v181 mul:4\n\t" \
  "v_fma_f32 v180, |v180|, v220, v180 mul:4\n\t" \
  "s_setprio 0\n\t" \
  "ds_read_u16 v232, v232 offset:10528\n\t" \
  "ds_read_b32 v230, v230 offset:10016\n\t" \
  "v_mfma_f32_16x16x32_bf16 v[168:171], v[216:219], v[140:143], v[168:171]\n\t" \
  "v_cvt_pk_bf16_f32 v180, v180, v181\n\t" \
  "v_cvt_pk_bf16_f32 v181, v182, v183\n\t" \
  "s_waitcnt lgkmcnt(8)\n\t" \
  "ds_read_b64_tr_b16 v[216:217], v248 offset:39464\n\t" \
  "v_mfma_f32_16x16x32_bf16 v[148:151], v[208:211], v[96:99], 0\n\t" \
  "v_cvt_pknorm_i16_f32 v220, |v146|, |v147|\n\t" \
  "v_cvt_pknorm_i16_f32 v222, |v144|, |v145|\n\t" \
  "s_waitcnt vmcnt(2)\n\t" \
  "ds_read_b64_tr_b16 v[218:219], v248 offset:42536\n\t" \
  "v_mfma_f32_16x16x32_bf16 v[156:159], v[208:211], v[100:103], 0\n\t" \
  "v_lshlrev_b32_sdwa v221, 2, v220 dst_sel:DWORD dst_unused:UNUSED_PAD src0_sel:DWORD src1_sel:BYTE_3\n\t" \
  "v_lshlrev_b32_sdwa v220, 2, v220 dst_sel:DWORD dst_unused:UNUSED_PAD src0_sel:DWORD src1_sel:BYTE_1\n\t" \
  "s_mov_b64 exec, 3\n\t" \
  "ds_add_rtn_u32 v246, v251, v250 offset:47184\n\t" \
  "v_mfma_f32_16x16x32_bf16 v[164:167], v[208:211], v[104:107], 0\n\t" \
  "s_mov_b64 exec, -1\n\t" \
  "v_lshlrev_b32_sdwa v223, 2, v222 dst_sel:DWORD dst_unused:UNUSED_PAD src0_sel:DWORD src1_sel:BYTE_3\n\t" \
  "v_lshlrev_b32_sdwa v222, 2, v222 dst_sel:DWORD dst_unused:UNUSED_PAD src0_sel:DWORD src1_sel:BYTE_1\n\t" \
  "ds_read_u16 v234, v234 offset:10528\n\t" \
  "s_waitcnt lgkmcnt(6)\n\t" \
  "v_mfma_f32_16x16x32_bf16 v[156:159], v[212:215], v[116:119], v[156:159]\n\t" \
  "v_lshlrev_b32_sdwa v227, 1, v153 dst_sel:DWORD dst_unused:UNUSED_PAD src0_sel:DWORD src1_sel:WORD_1\n\t" \
  "v_lshlrev_b32_sdwa v226, 1, v152 dst_sel:DWORD dst_unused:UNUSED_PAD src0_sel:DWORD src1_sel:WORD_1\n\t" \
  "ds_read_b32 v220, v220 offset:10016\n\t" \
  "v_mfma_f32_16x16x32_bf16 v[172:175], v[208:211], v[108:111], 0\n\t" \
  "s_waitcnt lgkmcnt(2)\n\t" \
  "v_lshlrev_b32_sdwa v225, 1, v155 dst_sel:DWORD dst_unused:UNUSED_PAD src0_sel:DWORD src1_sel:WORD_1\n\t" \
  "v_readfirstlane_b32 s4, v246\n\t" \
  "s_setprio 2\n\t" \
  "s_setprio 0\n\t" \
  "ds_read_b32 v223, v223 offset:10016\n\t" \
  "ds_read_b32 v221, v221 offset:10016\n\t" \
  "v_mfma_f32_16x16x32_bf16 v[148:151], v[212:215], v[112:115], v[148:151]\n\t" \
  "s_cmpk_lt_u32 s4, 6144\n\t" \
  "v_lshlrev_b32_sdwa v224, 1, v154 dst_sel:DWORD dst_unused:UNUSED_PAD src0_sel:DWORD src1_sel:WORD_1\n\t" \
  "s_cbranch_scc1 .Lcnx_w1_7\n\t" \
  ".Lcnx_g1_7:\n\t" \
  "s_nop 0\n\t" \
  "v_mfma_f32_16x16x32_bf16 v[164:167], v[212:215], v[120:123], v[164:167]\n\t" \
  "v_fma_mix_f32 v228, |v196|, v228, v228 op_sel:[0,1,0] op_sel_hi:[0,1,1] clamp\n\t" \
  "v_fma_mix_f32 v229, |v197|, v229, v229 op_sel:[0,1,0] op_sel_hi:[0,1,1] clamp\n\t" \
  "ds_read_b64_tr_b16 v[210:211], v249 offset:32288\n\t" \
  "ds_read_b64_tr_b16 v[208:209], v249 offset:30752\n\t" \
  "v_mfma_f32_16x16x32_bf16 v[172:175], v[212:215], v[124:127], v[172:175]\n\t" \
  "v_fma_mix_f32 v230, |v198|, v230, v230 op_sel:[0,1,0] op_sel_hi:[0,1,1] clamp\n\t" \
  "v_fma_mix_f32 v231, |v199|, v231, v231 op_sel:[0,1,0] op_sel_hi:[0,1,1] clamp\n\t" \
  "s_waitcnt lgkmcnt(5)\n\t" \
  "ds_read_b32 v222, v222 offset:10016\n\t" \
  "ds_read_b64_tr_b16 v[212:213], v249 offset:30760\n\t" \
  "v_mfma_f32_16x16x32_bf16 v[148:151], v[216:219], v[128:131], v[148:151]\n\t" \
  "s_mov_b32 m0, s47\n\t" \
  "v_or_b32 v204, v232, v233\n\t" \
  "v_or_b32 v205, v234, v235\n\t" \
  "s_mov_b32 exec_hi, 0xffff\n\t" \
  "buffer_load_dwordx4 v252, s[16:19], s33 offen lds\n\t" \
  "v_mfma_f32_16x16x32_bf16 v[156:159], v[216:219], v[132:135], v[156:159]\n\t" \
  "s_mov_b32 exec_hi, -1\n\t" \
  "v_fma_f32 v197, |v197|, v229, v197 mul:4\n\t" \
  "v_fma_f32 v196, |v196|, v228, v196 mul:4\n\t" \
  "ds_read_b64_tr_b16 v[214:215], v249 offset:32296\n\t" \
  "v_mfma_f32_16x16x32_bf16 v[164:167], v[216:219], v[136:139], v[164:167]\n\t" \
  "s_add_u32 s33, s33, s32\n\t" \
  "v_fma_f32 v198, |v198|, v230, v198 mul:4\n\t" \
  "v_fma_f32 v199, |v199|, v231, v199 mul:4\n\t" \
  "ds_read_u16_d16_hi v225, v225 offset:10528\n\t" \
  "ds_read_u16_d16_hi v227, v227 offset:10528\n\t" \
  "v_mfma_f32_4x4x4_16b_bf16 v[56:59], v[242:243], v[236:237], v[56:59]\n\t" \
  "s_setprio 2\n\t" \
  "v_mfma_f32_16x16x32_bf16 v[172:175], v[216:219], v[140:143], v[172:175]\n\t" \
  "v_cvt_pknorm_i16_f32 v230, |v160|, |v161|\n\t" \
  "v_cvt_pknorm_i16_f32 v228, |v162|, |v163|\n\t" \
  "ds_read_b64_tr_b16 v[218:219], v249 offset:32352\n\t" \
  "s_waitcnt lgkmcnt(6)\n\t" \
  "v_mfma_f32_16x16x32_bf16 v[24:27], v[208:211], v[186:189], v[24:27]\n\t" \
  "v_cvt_pk_bf16_f32 v196, v196, v197\n\t" \
  "v_cvt_pk_bf16_f32 v197, v198, v199\n\t" \
  "s_setprio 0\n\t" \
  "ds_read_b64_tr_b16 v[216:217], v249 offset:30816\n\t" \
  "v_mfma_f32_16x16x32_bf16 v[0:3], v[208:211], v[178:181], v[0:3]\n\t" \
  "v_lshlrev_b32_sdwa v229, 2, v228 dst_sel:DWORD dst_unused:UNUSED_PAD src0_sel:DWORD src1_sel:BYTE_3\n\t" \
  "v_lshlrev_b32_sdwa v228, 2, v228 dst_sel:DWORD dst_unused:UNUSED_PAD src0_sel:DWORD src1_sel:BYTE_1\n\t" \
  "s_add_u32 m0, s47, s35\n\t" \
  "ds_read_u16 v224, v224 offset:10528\n\t" \
  "v_mfma_f32_16x16x32_bf16 v[48:51], v[208:211], v[194:197], v[48:51]\n\t" \
  "v_lshlrev_b32_sdwa v231, 2, v230 dst_sel:DWORD dst_unused:UNUSED_PAD src0_sel:DWORD src1_sel:BYTE_3\n\t" \
  "s_waitcnt lgkmcnt(5)\n\t" \
  "s_setprio 1\n\t" \
  "s_setprio 1\n\t" \
  "ds_read_b32 v228, v228 offset:10016\n\t" \
  "v_mfma_f32_4x4x4_16b_bf16 v[60:63], v[242:243], v[238:239], v[60:63]\n\t" \
  "v_lshlrev_b32_sdwa v233, 1, v171 dst_sel:DWORD dst_unused:UNUSED_PAD src0_sel:DWORD src1_sel:WORD_1\n\t" \
  "s_mov_b32 exec_hi, 0xffff\n\t" \
  "buffer_load_dwordx4 v252, s[16:19], s34 offen lds\n\t" \
  "v_mfma_f32_16x16x32_bf16 v[4:7], v[212:215], v[178:181], v[4:7]\n\t" \
  "s_mov_b32 exec_hi, -1\n\t" \
  "v_lshlrev_b32_sdwa v232, 1, v170 dst_sel:DWORD dst_unused:UNUSED_PAD src0_sel:DWORD src1_sel:WORD_1\n\t" \
  "ds_read_u16_d16_hi v233, v233 offset:10528\n\t" \
  "s_setprio 1\n\t" \
  "v_mfma_f32_16x16x32_bf16 v[72:75], v[208:211], v[202:205], v[72:75]\n\t" \
  "s_setprio 2\n\t" \
  "s_setprio 1\n\t" \
  "v_lshlrev_b32_sdwa v235, 1, v169 dst_sel:DWORD dst_unused:UNUSED_PAD src0_sel:DWORD src1_sel:WORD_1\n\t" \
  "s_add_u32 s34, s34, s32\n\t" \
  "ds_read_b64_tr_b16 v[208:209], v249 offset:30824\n\t" \
  "ds_read_u16 v226, v226 offset:10528\n\t" \
  "v_mfma_f32_16x16x32_bf16 v[28:31], v[212:215], v[186:189], v[28:31]\n\t" \
  "v_lshlrev_b32_sdwa v234, 1, v168 dst_sel:DWORD dst_unused:UNUSED_PAD src0_sel:DWORD src1_sel:WORD_1\n\t" \
  "v_lshlrev_b32_sdwa v230, 2, v230 dst_sel:DWORD dst_unused:UNUSED_PAD src0_sel:DWORD src1_sel:BYTE_1\n\t" \
  "s_setprio 3\n\t" \
  "ds_read_b64_tr_b16 v[210:211], v249 offset:32360\n\t" \
  "ds_read_b32 v229, v229 offset:10016\n\t" \
  "v_mfma_f32_16x16x32_bf16 v[76:79], v[212:215], v[202:205], v[76:79]\n\t" \
  "v_fma_mix_f32 v223, |v145|, v223, v223 op_sel:[0,1,0] op_sel_hi:[0,1,1] clamp\n\t" \
  "v_fma_mix_f32 v222, |v144|, v222, v222 op_sel:[0,1,0] op_sel_hi:[0,1,1] clamp\n\t" \
  "ds_read_b32 v230, v230 offset:10016\n\t" \
  "ds_read_b32 v231, v231 offset:10016\n\t" \
  "v_mfma_f32_16x16x32_bf16 v[52:55], v[212:215], v[194:197], v[52:55]\n\t" \
  "v_fma_mix_f32 v221, |v147|, v221, v221 op_sel:[0,1,0] op_sel_hi:[0,1,1] clamp\n\t" \
  "v_fma_mix_f32 v220, |v146|, v220, v220 op_sel:[0,1,0] op_sel_hi:[0,1,1] clamp\n\t" \
  "s_waitcnt lgkmcnt(9)\n\t" \
  "ds_read_b64_tr_b16 v[212:213], v249 offset:30880\n\t" \
  "v_mfma_f32_16x16x32_bf16 v[8:11], v[216:219], v[178:181], v[8:11]\n\t" \
  "s_waitcnt lgkmcnt(5)\n\t" \
  "v_fma_f32 v147, |v147|, v221, v147 mul:4\n\t" \
  "v_or_b32 v155, v224, v225\n\t" \
  "buffer_load_dwordx4 v[236:239], v250, s[8:11], s14 offen offset:128 nt\n\t" \
  "v_mfma_f32_16x16x32_bf16 v[32:35], v[216:219], v[186:189], v[32:35]\n\t" \
  "v_fma_f32 v146, |v146|, v220, v146 mul:4\n\t" \
  "v_or_b32 v154, v226, v227\n\t" \
  "ds_read_b64_tr_b16 v[214:215], v249 offset:32416\n\t" \
  "v_mfma_f32_16x16x32_bf16 v[56:59], v[216:219], v[194:197], v[56:59]\n\t" \
  "v_fma_f32 v145, |v145|, v223, v145 mul:4\n\t" \
  "v_fma_f32 v144, |v144|, v222, v144 mul:4\n\t" \
  "s_waitcnt lgkmcnt(5)\n\t" \
  "ds_read_u16 v232, v232 offset:10528\n\t" \
  "v_mfma_f32_16x16x32_bf16 v[12:15], v[208:211], v[178:181], v[12:15]\n\t" \
  "v_cvt_pknorm_i16_f32 v222, |v150|, |v151|\n\t" \
  "v_cvt_pk_bf16_f32 v147, v146, v147\n\t" \
  "v_cvt_pk_bf16_f32 v146, v144, v145\n\t" \
  "v_cvt_pknorm_i16_f32 v220, |v148|, |v149|\n\t" \
  "v_mfma_f32_16x16x32_bf16 v[80:83], v[216:219], v[202:205], v[80:83]\n\t" \
  "ds_read_b64_tr_b16 v[216:217], v249 offset:30888\n\t" \
  "v_mfma_f32_16x16x32_bf16 v[36:39], v[208:211], v[186:189], v[36:39]\n\t" \
  "s_setprio 0\n\t" \
  "v_lshlrev_b32_sdwa v221, 2, v220 dst_sel:DWORD dst_unused:UNUSED_PAD src0_sel:DWORD src1_sel:BYTE_3\n\t" \
  "v_lshlrev_b32_sdwa v220, 2, v220 dst_sel:DWORD dst_unused:UNUSED_PAD src0_sel:DWORD src1_sel:BYTE_1\n\t" \
  "s_setprio 1\n\t" \
  "ds_read_u16 v234, v234 offset:10528\n\t" \
  "ds_read_b64_tr_b16 v[218:219], v249 offset:32424\n\t" \
  "v_mfma_f32_16x16x32_bf16 v[84:87], v[208:211], v[202:205], v[84:87]\n\t" \
  "s_waitcnt lgkmcnt(4)\n\t" \
  "v_lshlrev_b32_sdwa v224, 1, v156 dst_sel:DWORD dst_unused:UNUSED_PAD src0_sel:DWORD src1_sel:WORD_1\n\t" \
  "v_lshlrev_b32_sdwa v225, 1, v157 dst_sel:DWORD dst_unused:UNUSED_PAD src0_sel:DWORD src1_sel:WORD_1\n\t" \
  "ds_read_u16_d16_hi v235, v235 offset:10528\n\t" \
  "v_mfma_f32_16x16x32_bf16 v[60:63], v[208:211], v[194:197], v[60:63]\n\t" \
  "v_lshlrev_b32_sdwa v223, 2, v222 dst_sel:DWORD dst_unused:UNUSED_PAD src0_sel:DWORD src1_sel:BYTE_3\n\t" \
  "v_lshlrev_b32_sdwa v222, 2, v222 dst_sel:DWORD dst_unused:UNUSED_PAD src0_sel:DWORD src1_sel:BYTE_1\n\t" \
  "ds_read_b64_tr_b16 v[208:209], v248 offset:0\n\t" \
  "ds_read_b32 v220, v220 offset:10016\n\t" \
  "v_mfma_f32_16x16x32_bf16 v[16:19], v[212:215], v[178:181], v[16:19]\n\t" \
  "v_mfma_f32_16x16x32_bf16 v[64:67], v[212:215], v[194:197], v[64:67]\n\t" \
  "v_fma_mix_f32 v229, |v163|, v229, v229 op_sel:[0,1,0] op_sel_hi:[0,1,1] clamp\n\t" \
  "v_lshlrev_b32_sdwa v227, 1, v159 dst_sel:DWORD dst_unused:UNUSED_PAD src0_sel:DWORD src1_sel:WORD_1\n\t" \
  "s_setprio 2\n\t" \
  "ds_read_b32 v221, v221 offset:10016\n\t" \
  "s_setprio 1\n\t" \
  "v_mfma_f32_16x16x32_bf16 v[40:43], v[212:215], v[186:189], v[40:43]\n\t" \
  "v_fma_mix_f32 v228, |v162|, v228, v228 op_sel:[0,1,0] op_sel_hi:[0,1,1] clamp\n\t" \
  "v_lshlrev_b32_sdwa v226, 1, v158 dst_sel:DWORD dst_unused:UNUSED_PAD src0_sel:DWORD src1_sel:WORD_1\n\t" \
  "ds_read_b64_tr_b16 v[210:211], v248 offset:1024\n\t" \
  "ds_read_b32 v223, v223 offset:10016\n\t" \
  "v_mfma_f32_16x16x32_bf16 v[88:91], v[212:215], v[202:205], v[88:91]\n\t" \
  "v_fma_mix_f32 v231, |v161|, v231, v231 op_sel:[0,1,0] op_sel_hi:[0,1,1] clamp\n\t" \
  "v_fma_mix_f32 v230, |v160|, v230, v230 op_sel:[0,1,0] op_sel_hi:[0,1,1] clamp\n\t" \
  "s_waitcnt lgkmcnt(5)\n\t" \
  "ds_read_b64_tr_b16 v[212:213], v248 offset:3072\n\t" \
  "ds_read_b32 v222, v222 offset:10016\n\t" \
  "v_mfma_f32_16x16x32_bf16 v[20:23], v[216:219], v[178:181], v[20:23]\n\t" \
  "v_or_b32 v171, v232, v233\n\t" \
  "v_or_b32 v170, v234, v235\n\t" \
  "ds_read_u16_d16_hi v225, v225 offset:10528\n\t" \
  "s_setprio 3\n\t" \
  "v_mfma_f32_16x16x32_bf16 v[68:71], v[216:219], v[194:197], v[68:71]\n\t" \
  "v_fma_f32 v162, |v162|, v228, v162 mul:4\n\t" \
  "v_fma_f32 v163, |v163|, v229, v163 mul:4\n\t" \
  "s_setprio 0\n\t" \
  "s_waitcnt lgkmcnt(4)\n\t" \
  "ds_read_u16 v224, v224 offset:10528\n\t" \
  "v_mfma_f32_16x16x32_bf16 v[176:179], v[208:211], v[96:99], 0\n\t" \
  "v_fma_f32 v160, |v160|, v230, v160 mul:4\n\t" \
  "v_fma_f32 v161, |v161|, v231, v161 mul:4\n\t" \
  "ds_read_b64_tr_b16 v[214:215], v248 offset:4096\n\t" \
  "v_mfma_f32_16x16x32_bf16 v[44:47], v[216:219], v[186:189], v[44:47]\n\t" \
  "v_lshlrev_b32_sdwa v232, 1, v172 dst_sel:DWORD dst_unused:UNUSED_PAD src0_sel:DWORD src1_sel:WORD_1\n\t" \
  "v_cvt_pknorm_i16_f32 v230, |v166|, |v167|\n\t" \
  "s_setprio 1\n\t" \
  "s_setprio 1\n\t" \
  "ds_read_u16_d16_hi v227, v227 offset:10528\n\t" \
  "v_mfma_f32_16x16x32_bf16 v[92:95], v[216:219], v[202:205], v[92:95]\n\t" \
  "s_setprio 2\n\t" \
  "v_cvt_pk_bf16_f32 v163, v162, v163\n\t" \
  "v_cvt_pk_bf16_f32 v162, v160, v161\n\t" \
  "ds_read_b64_tr_b16 v[216:217], v248 offset:2048\n\t" \
  "v_mfma_f32_16x16x32_bf16 v[192:195], v[208:211], v[104:107], 0\n\t" \
  "v_lshlrev_b32_sdwa v231, 2, v230 dst_sel:DWORD dst_unused:UNUSED_PAD src0_sel:DWORD src1_sel:BYTE_3\n\t" \
  "v_cvt_pknorm_i16_f32 v228, |v164|, |v165|\n\t" \
  "s_waitcnt lgkmcnt(2)\n\t" \
  "s_setprio 0\n\t" \
  "ds_read_b64_tr_b16 v[218:219], v248 offset:5120\n\t" \
  "v_mfma_f32_16x16x32_bf16 v[184:187], v[208:211], v[100:103], 0\n\t" \
  "v_lshlrev_b32_sdwa v229, 2, v228 dst_sel:DWORD dst_unused:UNUSED_PAD src0_sel:DWORD src1_sel:BYTE_3\n\t" \
  "v_lshlrev_b32_sdwa v228, 2, v228 dst_sel:DWORD dst_unused:UNUSED_PAD src0_sel:DWORD src1_sel:BYTE_1\n\t" \
  "ds_read_u16 v226, v226 offset:10528\n\t" \
  "v_mfma_f32_16x16x32_bf16 v[176:179], v[212:215], v[112:115], v[176:179]\n\t" \
  "v_lshlrev_b32_sdwa v235, 1, v175 dst_sel:DWORD dst_unused:UNUSED_PAD src0_sel:DWORD src1_sel:WORD_1\n\t" \
  "v_fma_mix_f32 v221, |v149|, v221, v221 op_sel:[0,1,0] op_sel_hi:[0,1,1] clamp\n\t" \
  "ds_read_b32 v231, v231 offset:10016\n\t" \
  "v_mfma_f32_16x16x32_bf16 v[200:203], v[208:211], v[108:111], 0\n\t" \
  "v_lshlrev_b32_sdwa v230, 2, v230 dst_sel:DWORD dst_unused:UNUSED_PAD src0_sel:DWORD src1_sel:BYTE_1\n\t" \
  "v_lshlrev_b32_sdwa v233, 1, v173 dst_sel:DWORD dst_unused:UNUSED_PAD src0_sel:DWORD src1_sel:WORD_1\n\t" \
  "ds_read_b64_tr_b16 v[208:209], v248 offset:512\n\t" \
  "v_mfma_f32_16x16x32_bf16 v[200:203], v[212:215], v[124:127], v[200:203]\n\t" \
  "s_setprio 1\n\t" \
  "s_setprio 1\n\t" \
  "v_lshlrev_b32_sdwa v234, 1, v174 dst_sel:DWORD dst_unused:UNUSED_PAD src0_sel:DWORD src1_sel:WORD_1\n\t" \
  "ds_read_b32 v229, v229 offset:10016\n\t" \
  "ds_read_b32 v228, v228 offset:10016\n\t" \
  "v_mfma_f32_16x16x32_bf16 v[184:187], v[212:215], v[116:119], v[184:187]\n\t" \
  "v_fma_mix_f32 v220, |v148|, v220, v220 op_sel:[0,1,0] op_sel_hi:[0,1,1] clamp\n\t" \
  "ds_read_u16_d16_hi v233, v233 offset:10528\n\t" \
  "v_mfma_f32_16x16x32_bf16 v[192:195], v[212:215], v[120:123], v[192:195]\n\t" \
  "v_fma_mix_f32 v223, |v151|, v223, v223 op_sel:[0,1,0] op_sel_hi:[0,1,1] clamp\n\t" \
  "v_fma_mix_f32 v222, |v150|, v222, v222 op_sel:[0,1,0] op_sel_hi:[0,1,1] clamp\n\t" \
  "s_waitcnt lgkmcnt(6)\n\t" \
  "ds_read_b64_tr_b16 v[210:211], v248 offset:1536\n\t" \
  "ds_read_b64_tr_b16 v[212:213], v248 offset:3584\n\t" \
  "v_mfma_f32_16x16x32_bf16 v[192:195], v[216:219], v[136:139], v[192:195]\n\t" \
  "v_or_b32 v156, v224, v225\n\t" \
  "v_fma_f32 v150, |v150|, v222, v150 mul:4\n\t" \
  "s_waitcnt lgkmcnt(7)\n\t" \
  "ds_read_b32 v230, v230 offset:10016\n\t" \
  "v_mfma_f32_16x16x32_bf16 v[176:179], v[216:219], v[128:131], v[176:179]\n\t" \
  "v_or_b32 v157, v226, v227\n\t" \
  "v_fma_f32 v149, |v149|, v221, v149 mul:4\n\t" \
  "ds_read_b64_tr_b16 v[214:215], v248 offset:4608\n\t" \
  "v_mfma_f32_16x16x32_bf16 v[184:187], v[216:219], v[132:135], v[184:187]\n\t" \
  "v_fma_f32 v151, |v151|, v223, v151 mul:4\n\t" \
  "v_fma_f32 v148, |v148|, v220, v148 mul:4\n\t" \
  "s_setprio 3\n\t" \
  "s_waitcnt lgkmcnt(3)\n\t" \
  "ds_read_u16 v232, v232 offset:10528\n\t" \
  "ds_read_u16_d16_hi v235, v235 offset:10528\n\t" \
  "v_mfma_f32_16x16x32_bf16 v[200:203], v[216:219], v[140:143], v[200:203]\n\t" \
  "s_setprio 0\n\t" \
  "v_cvt_pk_bf16_f32 v148, v148, v149\n\t" \
  "v_cvt_pk_bf16_f32 v149, v150, v151\n\t" \
  "s_setprio 0\n\t" \
  "ds_read_b64_tr_b16 v[216:217], v248 offset:2560\n\t" \
  "v_mfma_f32_16x16x32_bf16 v[180:183], v[208:211], v[96:99], 0\n\t" \
  "v_cvt_pknorm_i16_f32 v220, |v178|, |v179|\n\t" \
  "v_cvt_pknorm_i16_f32 v222, |v176|, |v177|\n\t" \
  "s_waitcnt vmcnt(0)\n\t" \
  "ds_read_b64_tr_b16 v[218:219], v248 offset:5632\n\t" \
  "v_mfma_f32_16x16x32_bf16 v[188:191], v[208:211], v[100:103], 0\n\t" \
  "v_lshlrev_b32_sdwa v221, 2, v220 dst_sel:DWORD dst_unused:UNUSED_PAD src0_sel:DWORD src1_sel:BYTE_3\n\t" \
  "v_lshlrev_b32_sdwa v220, 2, v220 dst_sel:DWORD dst_unused:UNUSED_PAD src0_sel:DWORD src1_sel:BYTE_1\n\t" \
  "s_mov_b64 exec, 3\n\t" \
  "ds_add_rtn_u32 v246, v251, v250 offset:47192\n\t" \
  "v_mfma_f32_16x16x32_bf16 v[196:199], v[208:211], v[104:107], 0\n\t" \
  "s_mov_b64 exec, -1\n\t" \
  "v_lshlrev_b32_sdwa v223, 2, v222 dst_sel:DWORD dst_unused:UNUSED_PAD src0_sel:DWORD src1_sel:BYTE_3\n\t" \
  "v_lshlrev_b32_sdwa v222, 2, v222 dst_sel:DWORD dst_unused:UNUSED_PAD src0_sel:DWORD src1_sel:BYTE_1\n\t" \
  "s_waitcnt lgkmcnt(5)\n\t" \
  "s_setprio 2\n\t" \
  "ds_read_u16 v234, v234 offset:10528\n\t" \
  "v_mfma_f32_16x16x32_bf16 v[188:191], v[212:215], v[116:119], v[188:191]\n\t" \
  "v_lshlrev_b32_sdwa v226, 1, v184 dst_sel:DWORD dst_unused:UNUSED_PAD src0_sel:DWORD src1_sel:WORD_1\n\t" \
  "v_fma_mix_f32 v229, |v165|, v229, v229 op_sel:[0,1,0] op_sel_hi:[0,1,1] clamp\n\t" \
  "ds_read_b32 v223, v223 offset:10016\n\t" \
  "v_mfma_f32_16x16x32_bf16 v[204:207], v[208:211], v[108:111], 0\n\t" \
  "s_waitcnt lgkmcnt(2)\n\t" \
  "v_lshlrev_b32_sdwa v227, 1, v185 dst_sel:DWORD dst_unused:UNUSED_PAD src0_sel:DWORD src1_sel:WORD_1\n\t" \
  "v_readfirstlane_b32 s4, v246\n\t" \
  "ds_read_b32 v221, v221 offset:10016\n\t" \
  "v_mfma_f32_16x16x32_bf16 v[180:183], v[212:215], v[112:115], v[180:183]\n\t" \
  "s_cmpk_lt_u32 s4, 6144\n\t" \
  "v_fma_mix_f32 v228, |v164|, v228, v228 op_sel:[0,1,0] op_sel_hi:[0,1,1] clamp\n\t" \
  "s_cbranch_scc1 .Lcnx_w1_8\n\t" \
  ".Lcnx_g1_8:\n\t" \
  "ds_read_b32 v220, v220 offset:10016\n\t" \
  "v_mfma_f32_16x16x32_bf16 v[204:207], v[212:215], v[124:127], v[204:207]\n\t" \
  "v_fma_f32 v164, |v164|, v228, v164 mul:4\n\t" \
  "v_lshlrev_b32_sdwa v225, 1, v187 dst_sel:DWORD dst_unused:UNUSED_PAD src0_sel:DWORD src1_sel:WORD_1\n\t" \
  "ds_read_b64_tr_b16 v[208:209], v249 offset:6152\n\t" \
  "ds_read_b64_tr_b16 v[210:211], v249 offset:7688\n\t" \
  "v_mfma_f32_16x16x32_bf16 v[196:199], v[212:215], v[120:123], v[196:199]\n\t" \
  "v_fma_mix_f32 v231, |v167|, v231, v231 op_sel:[0,1,0] op_sel_hi:[0,1,1] clamp\n\t" \
  "v_fma_f32 v167, |v167|, v231, v167 mul:4\n\t" \
  "ds_read_b32 v222, v222 offset:10016\n\t" \
  "ds_read_b64_tr_b16 v[212:213], v249 offset:6160\n\t" \
  "v_mfma_f32_16x16x32_bf16 v[180:183], v[216:219], v[128:131], v[180:183]\n\t" \
  "v_or_b32 v172, v232, v233\n\t" \
  "v_fma_mix_f32 v230, |v166|, v230, v230 op_sel:[0,1,0] op_sel_hi:[0,1,1] clamp\n\t" \
  "ds_read_u16_d16_hi v227, v227 offset:10528\n\t" \
  "ds_read_b64_tr_b16 v[214:215], v249 offset:7696\n\t" \
  "v_mfma_f32_16x16x32_bf16 v[188:191], v[216:219], v[132:135], v[188:191]\n\t" \
  "v_fma_f32 v165, |v165|, v229, v165 mul:4\n\t" \
  "v_cvt_pk_bf16_f32 v164, v164, v165\n\t" \
  "v_mfma_f32_16x16x32_bf16 v[204:207], v[216:219], v[140:143], v[204:207]\n\t" \
  "s_waitcnt lgkmcnt(9)\n\t" \
  "v_fma_f32 v166, |v166|, v230, v166 mul:4\n\t" \
  "v_cvt_pk_bf16_f32 v165, v166, v167\n\t" \
  "v_mfma_f32_16x16x32_bf16 v[196:199], v[216:219], v[136:139], v[196:199]\n\t" \
  "ds_read_b64_tr_b16 v[218:219], v249 offset:7752\n\t" \
  "v_cvt_pknorm_i16_f32 v230, |v192|, |v193|\n\t" \
  "s_waitcnt lgkmcnt(5)\n\t" \
  "v_or_b32 v173, v234, v235\n\t" \
  "v_mfma_f32_16x16x32_bf16 v[24:27], v[208:211], v[154:157], v[24:27]\n\t" \
  "ds_read_b64_tr_b16 v[216:217], v249 offset:6216\n\t" \
  "ds_read_u16_d16_hi v225, v225 offset:10528\n\t" \
  "v_cvt_pknorm_i16_f32 v228, |v194|, |v195|\n\t" \
  "v_mfma_f32_4x4x4_16b_bf16 v[64:67], v[242:243], v[236:237], v[64:67]\n\t" \
  "v_mfma_f32_4x4x4_16b_bf16 v[68:71], v[242:243], v[238:239], v[68:71]\n\t" \
  "v_lshlrev_b32_sdwa v235, 1, v201 dst_sel:DWORD dst_unused:UNUSED_PAD src0_sel:DWORD src1_sel:WORD_1\n\t" \
  "v_mfma_f32_16x16x32_bf16 v[0:3], v[208:211], v[146:149], v[0:3]\n\t" \
  "s_setprio 0\n\t" \
  "v_lshlrev_b32_sdwa v224, 1, v186 dst_sel:DWORD dst_unused:UNUSED_PAD src0_sel:DWORD src1_sel:WORD_1\n\t" \
  "v_lshlrev_b32_sdwa v234, 1, v200 dst_sel:DWORD dst_unused:UNUSED_PAD src0_sel:DWORD src1_sel:WORD_1\n\t" \
  "ds_read_u16 v224, v224 offset:10528\n\t" \
  "v_mfma_f32_16x16x32_bf16 v[48:51], v[208:211], v[162:165], v[48:51]\n\t" \
  "v_lshlrev_b32_sdwa v229, 2, v228 dst_sel:DWORD dst_unused:UNUSED_PAD src0_sel:DWORD src1_sel:BYTE_3\n\t" \
  "v_lshlrev_b32_sdwa v228, 2, v228 dst_sel:DWORD dst_unused:UNUSED_PAD src0_sel:DWORD src1_sel:BYTE_1\n\t" \
  "s_waitcnt lgkmcnt(4)\n\t" \
  "ds_read_u16 v226, v226 offset:10528\n\t" \
  "v_mfma_f32_16x16x32_bf16 v[72:75], v[208:211], v[170:173], v[72:75]\n\t" \
  "v_lshlrev_b32_sdwa v232, 1, v202 dst_sel:DWORD dst_unused:UNUSED_PAD src0_sel:DWORD src1_sel:WORD_1\n\t" \
  "v_fma_mix_f32 v223, |v177|, v223, v223 op_sel:[0,1,0] op_sel_hi:[0,1,1] clamp\n\t" \
  "ds_read_b64_tr_b16 v[210:211], v249 offset:7760\n\t" \
  "v_mfma_f32_16x16x32_bf16 v[76:79], v[212:215], v[170:173], v[76:79]\n\t" \
  "v_fma_mix_f32 v220, |v178|, v220, v220 op_sel:[0,1,0] op_sel_hi:[0,1,1] clamp\n\t" \
  "v_lshlrev_b32_sdwa v231, 2, v230 dst_sel:DWORD dst_unused:UNUSED_PAD src0_sel:DWORD src1_sel:BYTE_3\n\t" \
  "ds_read_b32 v231, v231 offset:10016\n\t" \
  "v_mfma_f32_16x16x32_bf16 v[4:7], v[212:215], v[146:149], v[4:7]\n\t" \
  "v_lshlrev_b32_sdwa v230, 2, v230 dst_sel:DWORD dst_unused:UNUSED_PAD src0_sel:DWORD src1_sel:BYTE_1\n\t" \
  "s_waitcnt lgkmcnt(2)\n\t" \
  "ds_read_b32 v230, v230 offset:10016\n\t" \
  "ds_read_b64_tr_b16 v[208:209], v249 offset:6224\n\t" \
  "v_mfma_f32_16x16x32_bf16 v[28:31], v[212:215], v[154:157], v[28:31]\n\t" \
  "v_or_b32 v186, v226, v227\n\t" \
  "v_fma_mix_f32 v222, |v176|, v222, v222 op_sel:[0,1,0] op_sel_hi:[0,1,1] clamp\n\t" \
  "ds_read_b32 v228, v228 offset:10016\n\t" \
  "v_mfma_f32_16x16x32_bf16 v[8:11], v[216:219], v[146:149], v[8:11]\n\t" \
  "v_fma_mix_f32 v221, |v179|, v221, v221 op_sel:[0,1,0] op_sel_hi:[0,1,1] clamp\n\t" \
  "v_lshlrev_b32_sdwa v233, 1, v203 dst_sel:DWORD dst_unused:UNUSED_PAD src0_sel:DWORD src1_sel:WORD_1\n\t" \
  "ds_read_u16_d16_hi v233, v233 offset:10528\n\t" \
  "ds_read_b32 v229, v229 offset:10016\n\t" \
  "v_mfma_f32_16x16x32_bf16 v[52:55], v[212:215], v[162:165], v[52:55]\n\t" \
  "v_fma_f32 v178, |v178|, v220, v178 mul:4\n\t" \
  "ds_read_b64_tr_b16 v[214:215], v249 offset:7816\n\t" \
  "v_mfma_f32_16x16x32_bf16 v[32:35], v[216:219], v[154:157], v[32:35]\n\t" \
  "v_fma_f32 v179, |v179|, v221, v179 mul:4\n\t" \
  "v_or_b32 v187, v224, v225\n\t" \
  "ds_read_b64_tr_b16 v[212:213], v249 offset:6280\n\t" \
  "v_mfma_f32_16x16x32_bf16 v[56:59], v[216:219], v[162:165], v[56:59]\n\t" \
  "v_fma_f32 v176, |v176|, v222, v176 mul:4\n\t" \
  "v_fma_f32 v177, |v177|, v223, v177 mul:4\n\t" \
  "s_waitcnt lgkmcnt(5)\n\t" \
  "ds_read_u16 v232, v232 offset:10528\n\t" \
  "v_mfma_f32_16x16x32_bf16 v[80:83], v[216:219], v[170:173], v[80:83]\n\t" \
  "v_cvt_pk_bf16_f32 v179, v178, v179\n\t" \
  "v_cvt_pk_bf16_f32 v178, v176, v177\n\t" \
  "ds_read_b64_tr_b16 v[218:219], v249 offset:7824\n\t" \
  "v_mfma_f32_16x16x32_bf16 v[12:15], v[208:211], v[146:149], v[12:15]\n\t" \
  "v_cvt_pknorm_i16_f32 v222, |v182|, |v183|\n\t" \
  "v_cvt_pknorm_i16_f32 v220, |v180|, |v181|\n\t" \
  "ds_read_u16_d16_hi v235, v235 offset:10528\n\t" \
  "v_mfma_f32_16x16x32_bf16 v[60:63], v[208:211], v[162:165], v[60:63]\n\t" \
  "v_lshlrev_b32_sdwa v223, 2, v222 dst_sel:DWORD dst_unused:UNUSED_PAD src0_sel:DWORD src1_sel:BYTE_3\n\t" \
  "v_lshlrev_b32_sdwa v221, 2, v220 dst_sel:DWORD dst_unused:UNUSED_PAD src0_sel:DWORD src1_sel:BYTE_3\n\t" \
  "ds_read_b64_tr_b16 v[216:217], v249 offset:6288\n\t" \
  "v_mfma_f32_16x16x32_bf16 v[36:39], v[208:211], v[154:157], v[36:39]\n\t" \
  "v_lshlrev_b32_sdwa v222, 2, v222 dst_sel:DWORD dst_unused:UNUSED_PAD src0_sel:DWORD src1_sel:BYTE_1\n\t" \
  "v_lshlrev_b32_sdwa v225, 1, v189 dst_sel:DWORD dst_unused:UNUSED_PAD src0_sel:DWORD src1_sel:WORD_1\n\t" \
  "ds_read_b32 v221, v221 offset:10016\n\t" \
  "ds_read_u16 v234, v234 offset:10528\n\t" \
  "v_mfma_f32_16x16x32_bf16 v[84:87], v[208:211], v[170:173], v[84:87]\n\t" \
  "s_waitcnt lgkmcnt(6)\n\t" \
  "v_lshlrev_b32_sdwa v220, 2, v220 dst_sel:DWORD dst_unused:UNUSED_PAD src0_sel:DWORD src1_sel:BYTE_1\n\t" \
  "v_add_u32 v254, -81480, v248\n\t" \
  "ds_read_b32 v223, v223 offset:10016\n\t" \
  "v_mfma_f32_16x16x32_bf16 v[88:91], v[212:215], v[170:173], v[88:91]\n\t" \
  "v_lshlrev_b32_sdwa v226, 1, v190 dst_sel:DWORD dst_unused:UNUSED_PAD src0_sel:DWORD src1_sel:WORD_1\n\t" \
  "ds_read_b64_tr_b16 v[208:209], v248 offset:43048\n\t" \
  "ds_read_b64_tr_b16 v[236:237], v254 offset:36904\n\t" \
  "v_mfma_f32_16x16x32_bf16 v[16:19], v[212:215], v[146:149], v[16:19]\n\t" \
  "v_fma_mix_f32 v231, |v193|, v231, v231 op_sel:[0,1,0] op_sel_hi:[0,1,1] clamp\n\t" \
  "v_fma_mix_f32 v230, |v192|, v230, v230 op_sel:[0,1,0] op_sel_hi:[0,1,1] clamp\n\t" \
  "ds_read_b32 v220, v220 offset:10016\n\t" \
  "ds_read_b64_tr_b16 v[210:211], v248 offset:44072\n\t" \
  "v_mfma_f32_16x16x32_bf16 v[64:67], v[212:215], v[162:165], v[64:67]\n\t" \
  "v_lshlrev_b32_sdwa v224, 1, v188 dst_sel:DWORD dst_unused:UNUSED_PAD src0_sel:DWORD src1_sel:WORD_1\n\t" \
  "ds_read_b64_tr_b16 v[238:239], v254 offset:37928\n\t" \
  "v_mfma_f32_16x16x32_bf16 v[40:43], v[212:215], v[154:157], v[40:43]\n\t" \
  "v_fma_f32 v192, |v192|, v230, v192 mul:4\n\t" \
  "v_lshlrev_b32_sdwa v227, 1, v191 dst_sel:DWORD dst_unused:UNUSED_PAD src0_sel:DWORD src1_sel:WORD_1\n\t" \
  "ds_read_b64_tr_b16 v[212:213], v248 offset:46120\n\t" \
  "ds_read_b64_tr_b16 v[240:241], v254 offset:39976\n\t" \
  "v_fma_mix_f32 v229, |v195|, v229, v229 op_sel:[0,1,0] op_sel_hi:[0,1,1] clamp\n\t" \
  "v_fma_f32 v195, |v195|, v229, v195 mul:4\n\t" \
  "s_waitcnt lgkmcnt(8)\n\t" \
  "ds_read_b64_tr_b16 v[214:215], v248 offset:47144\n\t" \
  "ds_read_u16_d16_hi v225, v225 offset:10528\n\t" \
  "v_mfma_f32_16x16x32_bf16 v[68:71], v[216:219], v[162:165], v[68:71]\n\t" \
  "v_fma_mix_f32 v228, |v194|, v228, v228 op_sel:[0,1,0] op_sel_hi:[0,1,1] clamp\n\t" \
  "ds_read_b32 v222, v222 offset:10016\n\t" \
  "v_mfma_f32_16x16x32_bf16 v[20:23], v[216:219], v[146:149], v[20:23]\n\t" \
  "v_or_b32 v202, v234, v235\n\t" \
  "v_fma_f32 v193, |v193|, v231, v193 mul:4\n\t" \
  "ds_read_b64_tr_b16 v[242:243], v254 offset:41000\n\t" \
  "v_mfma_f32_16x16x32_bf16 v[44:47], v[216:219], v[154:157], v[44:47]\n\t" \
  "v_or_b32 v203, v232, v233\n\t" \
  "v_fma_f32 v194, |v194|, v228, v194 mul:4\n\t" \
  "s_waitcnt lgkmcnt(7)\n\t" \
  "ds_read_u16 v224, v224 offset:10528\n\t" \
  "v_mfma_f32_16x16x32_bf16 v[144:147], v[208:211], v[96:99], 0\n\t" \
  "v_lshlrev_b32_sdwa v232, 1, v204 dst_sel:DWORD dst_unused:UNUSED_PAD src0_sel:DWORD src1_sel:WORD_1\n\t" \
  "v_cvt_pknorm_i16_f32 v230, |v198|, |v199|\n\t" \
  "ds_read_b64_tr_b16 v[244:245], v254 offset:38952\n\t" \
  "v_mfma_f32_16x16x32_bf16 v[92:95], v[216:219], v[170:173], v[92:95]\n\t" \
  "v_cvt_pk_bf16_f32 v195, v194, v195\n\t" \
  "v_cvt_pk_bf16_f32 v194, v192, v193\n\t" \
  "s_waitcnt lgkmcnt(8)\n\t" \
  "ds_read_b64_tr_b16 v[216:217], v248 offset:45096\n\t" \
  "v_mfma_f32_16x16x32_bf16 v[160:163], v[236:239], v[96:99], 0\n\t" \
  "s_setprio 3\n\t" \
  "v_lshlrev_b32_sdwa v231, 2, v230 dst_sel:DWORD dst_unused:UNUSED_PAD src0_sel:DWORD src1_sel:BYTE_3\n\t" \
  "v_cvt_pknorm_i16_f32 v228, |v196|, |v197|\n\t" \
  "ds_read_b32 v231, v231 offset:10016\n\t" \
  "ds_read_b64_tr_b16 v[246:247], v254 offset:42024\n\t" \
  "v_mfma_f32_16x16x32_bf16 v[152:155], v[208:211], v[100:103], 0\n\t" \
  "v_lshlrev_b32_sdwa v229, 2, v228 dst_sel:DWORD dst_unused:UNUSED_PAD src0_sel:DWORD src1_sel:BYTE_3\n\t" \
  "v_lshlrev_b32_sdwa v228, 2, v228 dst_sel:DWORD dst_unused:UNUSED_PAD src0_sel:DWORD src1_sel:BYTE_1\n\t" \
  "s_waitcnt lgkmcnt(8)\n\t" \
  "ds_read_u16 v226, v226 offset:10528\n\t" \
  "ds_read_b64_tr_b16 v[218:219], v248 offset:48168\n\t" \
  "v_mfma_f32_16x16x32_bf16 v[144:147], v[212:215], v[112:115], v[144:147]\n\t" \
  "v_lshlrev_b32_sdwa v235, 1, v207 dst_sel:DWORD dst_unused:UNUSED_PAD src0_sel:DWORD src1_sel:WORD_1\n\t" \
  "v_fma_mix_f32 v221, |v181|, v221, v221 op_sel:[0,1,0] op_sel_hi:[0,1,1] clamp\n\t" \
  "s_waitcnt lgkmcnt(7)\n\t" \
  "ds_read_b64_tr_b16 v[208:209], v248 offset:43560\n\t" \
  "ds_read_b32 v229, v229 offset:10016\n\t" \
  "v_mfma_f32_16x16x32_bf16 v[168:171], v[236:239], v[100:103], 0\n\t" \
  "v_lshlrev_b32_sdwa v233, 1, v205 dst_sel:DWORD dst_unused:UNUSED_PAD src0_sel:DWORD src1_sel:WORD_1\n\t" \
  "ds_read_u16_d16_hi v233, v233 offset:10528\n\t" \
  "ds_read_b64_tr_b16 v[238:239], v254 offset:38440\n\t" \
  "v_mfma_f32_16x16x32_bf16 v[168:171], v[240:243], v[116:119], v[168:171]\n\t" \
  "v_lshlrev_b32_sdwa v234, 1, v206 dst_sel:DWORD dst_unused:UNUSED_PAD src0_sel:DWORD src1_sel:WORD_1\n\t" \
  "v_fma_mix_f32 v220, |v180|, v220, v220 op_sel:[0,1,0] op_sel_hi:[0,1,1] clamp\n\t" \
  "s_setprio 0\n\t" \
  "ds_read_b32 v228, v228 offset:10016\n\t" \
  "v_lshlrev_b32_sdwa v230, 2, v230 dst_sel:DWORD dst_unused:UNUSED_PAD src0_sel:DWORD src1_sel:BYTE_1\n\t" \
  "ds_read_u16_d16_hi v227, v227 offset:10528\n\t" \
  "v_mfma_f32_16x16x32_bf16 v[160:163], v[240:243], v[112:115], v[160:163]\n\t" \
  "s_waitcnt lgkmcnt(6)\n\t" \
  "ds_read_b64_tr_b16 v[242:243], v254 offset:41512\n\t" \
  "ds_read_b64_tr_b16 v[236:237], v254 offset:37416\n\t" \
  "ds_read_b64_tr_b16 v[210:211], v248 offset:44584\n\t" \
  "v_mfma_f32_16x16x32_bf16 v[152:155], v[212:215], v[116:119], v[152:155]\n\t" \
  "s_setprio 0\n\t" \
  "v_fma_mix_f32 v222, |v182|, v222, v222 op_sel:[0,1,0] op_sel_hi:[0,1,1] clamp\n\t" \
  "v_fma_f32 v180, |v180|, v220, v180 mul:4\n\t" \
  "ds_read_b64_tr_b16 v[212:213], v248 offset:46632\n\t" \
  "v_mfma_f32_16x16x32_bf16 v[152:155], v[216:219], v[132:135], v[152:155]\n\t" \
  "v_fma_mix_f32 v223, |v183|, v223, v223 op_sel:[0,1,0] op_sel_hi:[0,1,1] clamp\n\t" \
  "v_or_b32 v188, v224, v225\n\t" \
  "ds_read_b64_tr_b16 v[240:241], v254 offset:40488\n\t" \
  "v_mfma_f32_16x16x32_bf16 v[160:163], v[244:247], v[128:131], v[160:163]\n\t" \
  "s_waitcnt lgkmcnt(5)\n\t" \
  "v_fma_f32 v183, |v183|, v223, v183 mul:4\n\t" \
  "v_fma_f32 v182, |v182|, v222, v182 mul:4\n\t" \
  "ds_read_u16_d16_hi v235, v235 offset:10528\n\t" \
  "v_mfma_f32_16x16x32_bf16 v[144:147], v[216:219], v[128:131], v[144:147]\n\t" \
  "v_or_b32 v189, v226, v227\n\t" \
  "v_fma_f32 v181, |v181|, v221, v181 mul:4\n\t" \
  "ds_read_u16 v232, v232 offset:10528\n\t" \
  "s_setprio 3\n\t" \
  "ds_read_b64_tr_b16 v[214:215], v248 offset:47656\n\t" \
  "v_cvt_pk_bf16_f32 v180, v180, v181\n\t" \
  "v_cvt_pk_bf16_f32 v181, v182, v183\n\t" \
  "s_waitcnt lgkmcnt(5)\n\t" \
  "ds_read_b64_tr_b16 v[216:217], v248 offset:45608\n\t" \
  "ds_read_b32 v230, v230 offset:10016\n\t" \
  "v_mfma_f32_16x16x32_bf16 v[168:171], v[244:247], v[132:135], v[168:171]\n\t" \
  "ds_read_b64_tr_b16 v[218:219], v248 offset:48680\n\t" \
  "v_cvt_pknorm_i16_f32 v222, |v144|, |v145|\n\t" \
  "v_mfma_f32_16x16x32_bf16 v[148:151], v[208:211], v[96:99], 0\n\t" \
  "v_lshlrev_b32_sdwa v223, 2, v222 dst_sel:DWORD dst_unused:UNUSED_PAD src0_sel:DWORD src1_sel:BYTE_3\n\t" \
  "v_cvt_pknorm_i16_f32 v220, |v146|, |v147|\n\t" \
  "ds_read_b64_tr_b16 v[244:245], v254 offset:39464\n\t" \
  "v_mfma_f32_16x16x32_bf16 v[164:167], v[236:239], v[96:99], 0\n\t" \
  "v_lshlrev_b32_sdwa v222, 2, v222 dst_sel:DWORD dst_unused:UNUSED_PAD src0_sel:DWORD src1_sel:BYTE_1\n\t" \
  "v_lshlrev_b32_sdwa v221, 2, v220 dst_sel:DWORD dst_unused:UNUSED_PAD src0_sel:DWORD src1_sel:BYTE_3\n\t" \
  "ds_read_b64_tr_b16 v[246:247], v254 offset:42536\n\t" \
  "v_mfma_f32_16x16x32_bf16 v[156:159], v[208:211], v[100:103], 0\n\t" \
  "v_lshlrev_b32_sdwa v220, 2, v220 dst_sel:DWORD dst_unused:UNUSED_PAD src0_sel:DWORD src1_sel:BYTE_1\n\t" \
  "s_mov_b64 exec, 3\n\t" \
  "ds_add_rtn_u32 v253, v251, v250 offset:47200\n\t" \
  "s_mov_b64 exec, -1\n\t" \
  "v_fma_mix_f32 v231, |v199|, v231, v231 op_sel:[0,1,0] op_sel_hi:[0,1,1] clamp\n\t" \
  "ds_read_u16 v234, v234 offset:10528\n\t" \
  "v_mfma_f32_16x16x32_bf16 v[172:175], v[236:239], v[100:103], 0\n\t" \
  "v_lshlrev_b32_sdwa v227, 1, v153 dst_sel:DWORD dst_unused:UNUSED_PAD src0_sel:DWORD src1_sel:WORD_1\n\t" \
  "v_lshlrev_b32_sdwa v225, 1, v155 dst_sel:DWORD dst_unused:UNUSED_PAD src0_sel:DWORD src1_sel:WORD_1\n\t" \
  "s_waitcnt lgkmcnt(1)\n\t" \
  "ds_read_b32 v221, v221 offset:10016\n\t" \
  "v_mfma_f32_16x16x32_bf16 v[156:159], v[212:215], v[116:119], v[156:159]\n\t" \
  "v_lshlrev_b32_sdwa v226, 1, v152 dst_sel:DWORD dst_unused:UNUSED_PAD src0_sel:DWORD src1_sel:WORD_1\n\t" \
  "v_readfirstlane_b32 s4, v253\n\t" \
  "ds_read_b32 v223, v223 offset:10016\n\t" \
  "v_mfma_f32_16x16x32_bf16 v[148:151], v[212:215], v[112:115], v[148:151]\n\t" \
  "s_cmpk_lt_u32 s4, 6144\n\t" \
  "ds_read_b32 v220, v220 offset:10016\n\t" \
  "v_lshlrev_b32_sdwa v224, 1, v154 dst_sel:DWORD dst_unused:UNUSED_PAD src0_sel:DWORD src1_sel:WORD_1\n\t" \
  "s_cbranch_scc1 .Lcnx_w1_9\n\t" \
  ".Lcnx_g1_9:\n\t" \
  "ds_read_b64_tr_b16 v[212:213], v249 offset:12312\n\t" \
  "v_mfma_f32_16x16x32_bf16 v[164:167], v[240:243], v[112:115], v[164:167]\n\t" \
  "v_fma_mix_f32 v228, |v196|, v228, v228 op_sel:[0,1,0] op_sel_hi:[0,1,1] clamp\n\t" \
  "v_fma_mix_f32 v229, |v197|, v229, v229 op_sel:[0,1,0] op_sel_hi:[0,1,1] clamp\n\t" \
  "s_cmp_ge_u32 s39, 2048\n\t" \
  "ds_read_b64_tr_b16 v[208:209], v249 offset:12304\n\t" \
  "v_mfma_f32_16x16x32_bf16 v[172:175], v[240:243], v[116:119], v[172:175]\n\t" \
  "v_fma_mix_f32 v230, |v198|, v230, v230 op_sel:[0,1,0] op_sel_hi:[0,1,1] clamp\n\t" \
  "v_fma_f32 v198, |v198|, v230, v198 mul:4\n\t" \
  "ds_read_b32 v222, v222 offset:10016\n\t" \
  "ds_read_b64_tr_b16 v[210:211], v249 offset:13840\n\t" \
  "v_mfma_f32_16x16x32_bf16 v[148:151], v[216:219], v[128:131], v[148:151]\n\t" \
  "v_or_b32 v204, v232, v233\n\t" \
  "ds_read_b64_tr_b16 v[214:215], v249 offset:13848\n\t" \
  "v_mfma_f32_16x16x32_bf16 v[156:159], v[216:219], v[132:135], v[156:159]\n\t" \
  "s_waitcnt lgkmcnt(8)\n\t" \
  "v_or_b32 v205, v234, v235\n\t" \
  "v_fma_f32 v196, |v196|, v228, v196 mul:4\n\t" \
  "ds_read_u16_d16_hi v225, v225 offset:10528\n\t" \
  "v_mfma_f32_16x16x32_bf16 v[164:167], v[244:247], v[128:131], v[164:167]\n\t" \
  "v_fma_f32 v199, |v199|, v231, v199 mul:4\n\t" \
  "v_cvt_pknorm_i16_f32 v228, |v162|, |v163|\n\t" \
  "ds_read_u16 v224, v224 offset:10528\n\t" \
  "s_waitcnt lgkmcnt(3)\n\t" \
  "v_mfma_f32_16x16x32_bf16 v[172:175], v[244:247], v[132:135], v[172:175]\n\t" \
  "v_fma_f32 v197, |v197|, v229, v197 mul:4\n\t" \
  "v_cvt_pk_bf16_f32 v196, v196, v197\n\t" \
  "ds_read_b64_tr_b16 v[218:219], v249 offset:13904\n\t" \
  "ds_read_b64_tr_b16 v[216:217], v249 offset:12368\n\t" \
  "v_lshlrev_b32_sdwa v229, 2, v228 dst_sel:DWORD dst_unused:UNUSED_PAD src0_sel:DWORD src1_sel:BYTE_3\n\t" \
  "v_lshlrev_b32_sdwa v228, 2, v228 dst_sel:DWORD dst_unused:UNUSED_PAD src0_sel:DWORD src1_sel:BYTE_1\n\t" \
  "ds_read_b32 v228, v228 offset:10016\n\t" \
  "v_mfma_f32_16x16x32_bf16 v[72:75], v[208:211], v[202:205], v[72:75]\n\t" \
  "s_cbranch_scc1 .Lcnx_p\n\t" \
  "s_setprio 3\n\t" \
  ".Lcnx_p:\n\t" \
  "v_cvt_pk_bf16_f32 v197, v198, v199\n\t" \
  "v_cvt_pknorm_i16_f32 v230, |v160|, |v161|\n\t" \
  "v_lshlrev_b32_sdwa v231, 2, v230 dst_sel:DWORD dst_unused:UNUSED_PAD src0_sel:DWORD src1_sel:BYTE_3\n\t" \
  "v_mfma_f32_16x16x32_bf16 v[0:3], v[208:211], v[178:181], v[0:3]\n\t" \
  "s_waitcnt lgkmcnt(1)\n\t" \
  "ds_read_u16_d16_hi v227, v227 offset:10528\n\t" \
  "v_mfma_f32_16x16x32_bf16 v[8:11], v[216:219], v[178:181], v[8:11]\n\t" \
  "v_fma_mix_f32 v221, |v147|, v221, v221 op_sel:[0,1,0] op_sel_hi:[0,1,1] clamp\n\t" \
  "v_fma_f32 v147, |v147|, v221, v147 mul:4\n\t" \
  "ds_read_u16 v226, v226 offset:10528\n\t" \
  "v_mfma_f32_16x16x32_bf16 v[48:51], v[208:211], v[194:197], v[48:51]\n\t" \
  "v_lshlrev_b32_sdwa v233, 1, v171 dst_sel:DWORD dst_unused:UNUSED_PAD src0_sel:DWORD src1_sel:WORD_1\n\t" \
  "v_lshlrev_b32_sdwa v232, 1, v170 dst_sel:DWORD dst_unused:UNUSED_PAD src0_sel:DWORD src1_sel:WORD_1\n\t" \
  "ds_read_b32 v229, v229 offset:10016\n\t" \
  "v_mfma_f32_16x16x32_bf16 v[24:27], v[208:211], v[186:189], v[24:27]\n\t" \
  "v_lshlrev_b32_sdwa v230, 2, v230 dst_sel:DWORD dst_unused:UNUSED_PAD src0_sel:DWORD src1_sel:BYTE_1\n\t" \
  "v_lshlrev_b32_sdwa v234, 1, v168 dst_sel:DWORD dst_unused:UNUSED_PAD src0_sel:DWORD src1_sel:WORD_1\n\t" \
  "ds_read_b64_tr_b16 v[208:209], v249 offset:12376\n\t" \
  "v_mfma_f32_16x16x32_bf16 v[28:31], v[212:215], v[186:189], v[28:31]\n\t" \
  "s_waitcnt lgkmcnt(2)\n\t" \
  "v_or_b32 v154, v226, v227\n\t" \
  "ds_read_b32 v231, v231 offset:10016\n\t" \
  "v_fma_mix_f32 v223, |v145|, v223, v223 op_sel:[0,1,0] op_sel_hi:[0,1,1] clamp\n\t" \
  "v_mfma_f32_16x16x32_bf16 v[76:79], v[212:215], v[202:205], v[76:79]\n\t" \
  "ds_read_b32 v230, v230 offset:10016\n\t" \
  "v_mfma_f32_16x16x32_bf16 v[4:7], v[212:215], v[178:181], v[4:7]\n\t" \
  "s_waitcnt lgkmcnt(3)\n\t" \
  "ds_read_u16_d16_hi v233, v233 offset:10528\n\t" \
  "v_fma_mix_f32 v222, |v144|, v222, v222 op_sel:[0,1,0] op_sel_hi:[0,1,1] clamp\n\t" \
  "ds_read_b64_tr_b16 v[210:211], v249 offset:13912\n\t" \
  "v_mfma_f32_16x16x32_bf16 v[52:55], v[212:215], v[194:197], v[52:55]\n\t" \
  "v_or_b32 v155, v224, v225\n\t" \
  "ds_read_b64_tr_b16 v[214:215], v249 offset:13968\n\t" \
  "v_mfma_f32_16x16x32_bf16 v[32:35], v[216:219], v[186:189], v[32:35]\n\t" \
  "ds_read_b64_tr_b16 v[212:213], v249 offset:12432\n\t" \
  "v_fma_mix_f32 v229, |v163|, v229, v229 op_sel:[0,1,0] op_sel_hi:[0,1,1] clamp\n\t" \
  "v_fma_f32 v145, |v145|, v223, v145 mul:4\n\t" \
  "ds_read_u16 v232, v232 offset:10528\n\t" \
  "v_mfma_f32_16x16x32_bf16 v[56:59], v[216:219], v[194:197], v[56:59]\n\t" \
  "v_lshlrev_b32_sdwa v235, 1, v169 dst_sel:DWORD dst_unused:UNUSED_PAD src0_sel:DWORD src1_sel:WORD_1\n\t" \
  "v_fma_mix_f32 v220, |v146|, v220, v220 op_sel:[0,1,0] op_sel_hi:[0,1,1] clamp\n\t" \
  "v_fma_f32 v144, |v144|, v222, v144 mul:4\n\t" \
  "ds_read_u16_d16_hi v235, v235 offset:10528\n\t" \
  "v_mfma_f32_16x16x32_bf16 v[80:83], v[216:219], v[202:205], v[80:83]\n\t" \
  "v_fma_f32 v146, |v146|, v220, v146 mul:4\n\t" \
  "v_cvt_pknorm_i16_f32 v222, |v150|, |v151|\n\t" \
  "v_cvt_pk_bf16_f32 v147, v146, v147\n\t" \
  "v_cvt_pk_bf16_f32 v146, v144, v145\n\t" \
  "s_waitcnt lgkmcnt(4)\n\t" \
  "v_mfma_f32_16x16x32_bf16 v[60:63], v[208:211], v[194:197], v[60:63]\n\t" \
  "ds_read_b64_tr_b16 v[216:217], v249 offset:12440\n\t" \
  "v_cvt_pknorm_i16_f32 v220, |v148|, |v149|\n\t" \
  "v_lshlrev_b32_sdwa v221, 2, v220 dst_sel:DWORD dst_unused:UNUSED_PAD src0_sel:DWORD src1_sel:BYTE_3\n\t" \
  "v_lshlrev_b32_sdwa v220, 2, v220 dst_sel:DWORD dst_unused:UNUSED_PAD src0_sel:DWORD src1_sel:BYTE_1\n\t" \
  "v_fma_mix_f32 v231, |v161|, v231, v231 op_sel:[0,1,0] op_sel_hi:[0,1,1] clamp\n\t" \
  "ds_read_b64_tr_b16 v[218:219], v249 offset:13976\n\t" \
  "v_mfma_f32_16x16x32_bf16 v[36:39], v[208:211], v[186:189], v[36:39]\n\t" \
  "ds_read_b64_tr_b16 v[238:239], v254 offset:1024\n\t" \
  "ds_read_b32 v220, v220 offset:10016\n\t" \
  "v_lshlrev_b32_sdwa v225, 1, v157 dst_sel:DWORD dst_unused:UNUSED_PAD src0_sel:DWORD src1_sel:WORD_1\n\t" \
  "v_lshlrev_b32_sdwa v224, 1, v156 dst_sel:DWORD dst_unused:UNUSED_PAD src0_sel:DWORD src1_sel:WORD_1\n\t" \
  "s_waitcnt lgkmcnt(6)\n\t" \
  "ds_read_u16 v234, v234 offset:10528\n\t" \
  "v_mfma_f32_16x16x32_bf16 v[40:43], v[212:215], v[186:189], v[40:43]\n\t" \
  "ds_read_b32 v221, v221 offset:10016\n\t" \
  "v_mfma_f32_16x16x32_bf16 v[12:15], v[208:211], v[178:181], v[12:15]\n\t" \
  "v_lshlrev_b32_sdwa v223, 2, v222 dst_sel:DWORD dst_unused:UNUSED_PAD src0_sel:DWORD src1_sel:BYTE_3\n\t" \
  "v_mfma_f32_16x16x32_bf16 v[84:87], v[208:211], v[202:205], v[84:87]\n\t" \
  "v_fma_mix_f32 v228, |v162|, v228, v228 op_sel:[0,1,0] op_sel_hi:[0,1,1] clamp\n\t" \
  "v_lshlrev_b32_sdwa v222, 2, v222 dst_sel:DWORD dst_unused:UNUSED_PAD src0_sel:DWORD src1_sel:BYTE_1\n\t" \
  "ds_read_b32 v223, v223 offset:10016\n\t" \
  "v_mfma_f32_16x16x32_bf16 v[16:19], v[212:215], v[178:181], v[16:19]\n\t" \
  "s_waitcnt lgkmcnt(5)\n\t" \
  "ds_read_b64_tr_b16 v[208:209], v248 offset:49192\n\t" \
  "v_fma_mix_f32 v230, |v160|, v230, v230 op_sel:[0,1,0] op_sel_hi:[0,1,1] clamp\n\t" \
  "ds_read_b64_tr_b16 v[210:211], v248 offset:50216\n\t" \
  "v_mfma_f32_16x16x32_bf16 v[20:23], v[216:219], v[178:181], v[20:23]\n\t" \
  "s_waitcnt lgkmcnt(4)\n\t" \
  "ds_read_b64_tr_b16 v[240:241], v254 offset:3072\n\t" \
  "v_lshlrev_b32_sdwa v226, 1, v158 dst_sel:DWORD dst_unused:UNUSED_PAD src0_sel:DWORD src1_sel:WORD_1\n\t" \
  "v_mfma_f32_16x16x32_bf16 v[88:91], v[212:215], v[202:205], v[88:91]\n\t" \
  "v_lshlrev_b32_sdwa v227, 1, v159 dst_sel:DWORD dst_unused:UNUSED_PAD src0_sel:DWORD src1_sel:WORD_1\n\t" \
  "v_or_b32 v170, v234, v235\n\t" \
  "ds_read_b32 v222, v222 offset:10016\n\t" \
  "ds_read_b64_tr_b16 v[236:237], v254 offset:0\n\t" \
  "v_mfma_f32_16x16x32_bf16 v[64:67], v[212:215], v[194:197], v[64:67]\n\t" \
  "ds_read_b64_tr_b16 v[212:213], v248 offset:52264\n\t" \
  "ds_read_b64_tr_b16 v[244:245], v254 offset:2048\n\t" \
  "v_mfma_f32_16x16x32_bf16 v[68:71], v[216:219], v[194:197], v[68:71]\n\t" \
  "ds_read_b64_tr_b16 v[214:215], v248 offset:53288\n\t" \
  "v_fma_f32 v160, |v160|, v230, v160 mul:4\n\t" \
  "v_fma_f32 v161, |v161|, v231, v161 mul:4\n\t" \
  "ds_read_u16_d16_hi v227, v227 offset:10528\n\t" \
  "ds_read_u16 v224, v224 offset:10528\n\t" \
  "v_fma_f32 v163, |v163|, v229, v163 mul:4\n\t" \
  "v_fma_f32 v162, |v162|, v228, v162 mul:4\n\t" \
  "v_cvt_pk_bf16_f32 v163, v162, v163\n\t" \
  "v_or_b32 v171, v232, v233\n\t" \
  "v_cvt_pknorm_i16_f32 v230, |v166|, |v167|\n\t" \
  "v_mfma_f32_16x16x32_bf16 v[44:47], v[216:219], v[186:189], v[44:47]\n\t" \
  "ds_read_b64_tr_b16 v[246:247], v254 offset:5120\n\t" \
  "s_waitcnt lgkmcnt(6)\n\t" \
  "v_mfma_f32_16x16x32_bf16 v[92:95], v[216:219], v[202:205], v[92:95]\n\t" \
  "ds_read_b64_tr_b16 v[218:219], v248 offset:54312\n\t" \
  "v_cvt_pk_bf16_f32 v162, v160, v161\n\t" \
  "v_lshlrev_b32_sdwa v231, 2, v230 dst_sel:DWORD dst_unused:UNUSED_PAD src0_sel:DWORD src1_sel:BYTE_3\n\t" \
  "ds_read_b64_tr_b16 v[216:217], v248 offset:51240\n\t" \
  "v_mfma_f32_16x16x32_bf16 v[200:203], v[236:239], v[100:103], 0\n\t" \
  "ds_read_b64_tr_b16 v[242:243], v254 offset:4096\n\t" \
  "v_lshlrev_b32_sdwa v235, 1, v175 dst_sel:DWORD dst_unused:UNUSED_PAD src0_sel:DWORD src1_sel:WORD_1\n\t" \
  "v_lshlrev_b32_sdwa v230, 2, v230 dst_sel:DWORD dst_unused:UNUSED_PAD src0_sel:DWORD src1_sel:BYTE_1\n\t" \
  "v_lshlrev_b32_sdwa v232, 1, v172 dst_sel:DWORD dst_unused:UNUSED_PAD src0_sel:DWORD src1_sel:WORD_1\n\t" \
  "v_cvt_pknorm_i16_f32 v228, |v164|, |v165|\n\t" \
  "v_lshlrev_b32_sdwa v229, 2, v228 dst_sel:DWORD dst_unused:UNUSED_PAD src0_sel:DWORD src1_sel:BYTE_3\n\t" \
  "v_mfma_f32_16x16x32_bf16 v[184:187], v[208:211], v[100:103], 0\n\t" \
  "ds_read_b32 v231, v231 offset:10016\n\t" \
  "v_mfma_f32_16x16x32_bf16 v[176:179], v[208:211], v[96:99], 0\n\t" \
  "ds_read_b64_tr_b16 v[210:211], v248 offset:50728\n\t" \
  "v_lshlrev_b32_sdwa v233, 1, v173 dst_sel:DWORD dst_unused:UNUSED_PAD src0_sel:DWORD src1_sel:WORD_1\n\t" \
  "s_waitcnt lgkmcnt(8)\n\t" \
  "ds_read_b32 v229, v229 offset:10016\n\t" \
  "v_mfma_f32_16x16x32_bf16 v[184:187], v[212:215], v[116:119], v[184:187]\n\t" \
  "ds_read_u16_d16_hi v225, v225 offset:10528\n\t" \
  "v_fma_mix_f32 v220, |v148|, v220, v220 op_sel:[0,1,0] op_sel_hi:[0,1,1] clamp\n\t" \
  "v_fma_f32 v148, |v148|, v220, v148 mul:4\n\t" \
  "v_mfma_f32_16x16x32_bf16 v[176:179], v[212:215], v[112:115], v[176:179]\n\t" \
  "s_waitcnt lgkmcnt(4)\n\t" \
  "ds_read_b64_tr_b16 v[208:209], v248 offset:49704\n\t" \
  "ds_read_u16 v226, v226 offset:10528\n\t" \
  "v_mfma_f32_16x16x32_bf16 v[192:195], v[236:239], v[96:99], 0\n\t" \
  "ds_read_b64_tr_b16 v[238:239], v254 offset:1536\n\t" \
  "v_lshlrev_b32_sdwa v234, 1, v174 dst_sel:DWORD dst_unused:UNUSED_PAD src0_sel:DWORD src1_sel:WORD_1\n\t" \
  "v_fma_mix_f32 v221, |v149|, v221, v221 op_sel:[0,1,0] op_sel_hi:[0,1,1] clamp\n\t" \
  "v_fma_f32 v149, |v149|, v221, v149 mul:4\n\t" \
  "v_fma_mix_f32 v222, |v150|, v222, v222 op_sel:[0,1,0] op_sel_hi:[0,1,1] clamp\n\t" \
  "v_mfma_f32_16x16x32_bf16 v[192:195], v[240:243], v[112:115], v[192:195]\n\t" \
  "ds_read_b32 v230, v230 offset:10016\n\t" \
  "ds_read_u16_d16_hi v233, v233 offset:10528\n\t" \
  "v_mfma_f32_16x16x32_bf16 v[200:203], v[240:243], v[116:119], v[200:203]\n\t" \
  "v_lshlrev_b32_sdwa v228, 2, v228 dst_sel:DWORD dst_unused:UNUSED_PAD src0_sel:DWORD src1_sel:BYTE_1\n\t" \
  "ds_read_u16_d16_hi v235, v235 offset:10528\n\t" \
  "ds_read_b32 v228, v228 offset:10016\n\t" \
  "v_mfma_f32_16x16x32_bf16 v[176:179], v[216:219], v[128:131], v[176:179]\n\t" \
  "ds_read_b64_tr_b16 v[236:237], v254 offset:512\n\t" \
  "ds_read_b64_tr_b16 v[214:215], v248 offset:53800\n\t" \
  "v_fma_mix_f32 v223, |v151|, v223, v223 op_sel:[0,1,0] op_sel_hi:[0,1,1] clamp\n\t" \
  "ds_read_u16 v232, v232 offset:10528\n\t" \
  "v_mfma_f32_16x16x32_bf16 v[184:187], v[216:219], v[132:135], v[184:187]\n\t" \
  "ds_read_b64_tr_b16 v[216:217], v248 offset:51752\n\t" \
  "v_cvt_pk_bf16_f32 v148, v148, v149\n\t" \
  "v_fma_f32 v150, |v150|, v222, v150 mul:4\n\t" \
  "s_waitcnt lgkmcnt(9)\n\t" \
  "v_or_b32 v157, v226, v227\n\t" \
  "s_waitcnt lgkmcnt(3)\n\t" \
  "v_or_b32 v156, v224, v225\n\t" \
  "v_mfma_f32_16x16x32_bf16 v[196:199], v[236:239], v[96:99], 0\n\t" \
  "ds_read_b64_tr_b16 v[242:243], v254 offset:4608\n\t" \
  "ds_read_b64_tr_b16 v[240:241], v254 offset:3584\n\t" \
  "v_mfma_f32_16x16x32_bf16 v[192:195], v[244:247], v[128:131], v[192:195]\n\t" \
  "ds_read_b64_tr_b16 v[212:213], v248 offset:52776\n\t" \
  "v_cvt_pknorm_i16_f32 v220, |v178|, |v179|\n\t" \
  "v_fma_f32 v151, |v151|, v223, v151 mul:4\n\t" \
  "v_cvt_pknorm_i16_f32 v222, |v176|, |v177|\n\t" \
  "v_lshlrev_b32_sdwa v225, 1, v187 dst_sel:DWORD dst_unused:UNUSED_PAD src0_sel:DWORD src1_sel:WORD_1\n\t" \
  "v_cvt_pk_bf16_f32 v149, v150, v151\n\t" \
  "v_lshlrev_b32_sdwa v223, 2, v222 dst_sel:DWORD dst_unused:UNUSED_PAD src0_sel:DWORD src1_sel:BYTE_3\n\t" \
  "v_mfma_f32_16x16x32_bf16 v[200:203], v[244:247], v[132:135], v[200:203]\n\t" \
  "ds_read_b64_tr_b16 v[244:245], v254 offset:2560\n\t" \
  "ds_read_b64_tr_b16 v[218:219], v248 offset:54824\n\t" \
  "v_mfma_f32_16x16x32_bf16 v[180:183], v[208:211], v[96:99], 0\n\t" \
  "ds_read_u16 v234, v234 offset:10528\n\t" \
  "v_lshlrev_b32_sdwa v221, 2, v220 dst_sel:DWORD dst_unused:UNUSED_PAD src0_sel:DWORD src1_sel:BYTE_3\n\t" \
  "v_add_u32 v249, -81480, v249\n\t" \
  "ds_read_b64_tr_b16 v[246:247], v254 offset:5632\n\t" \
  "v_mfma_f32_16x16x32_bf16 v[188:191], v[208:211], v[100:103], 0\n\t" \
  "ds_read_b32 v221, v221 offset:10016\n\t" \
  "v_fma_mix_f32 v231, |v167|, v231, v231 op_sel:[0,1,0] op_sel_hi:[0,1,1] clamp\n\t" \
  "v_lshlrev_b32_sdwa v220, 2, v220 dst_sel:DWORD dst_unused:UNUSED_PAD src0_sel:DWORD src1_sel:BYTE_1\n\t" \
  "v_fma_f32 v167, |v167|, v231, v167 mul:4\n\t" \
  "v_lshlrev_b32_sdwa v222, 2, v222 dst_sel:DWORD dst_unused:UNUSED_PAD src0_sel:DWORD src1_sel:BYTE_1\n\t" \
  "v_mfma_f32_16x16x32_bf16 v[204:207], v[236:239], v[100:103], 0\n\t" \
  "ds_read_b64_tr_b16 v[210:211], v249 offset:26136\n\t" \
  "s_waitcnt lgkmcnt(6)\n\t" \
  "v_mfma_f32_16x16x32_bf16 v[180:183], v[212:215], v[112:115], v[180:183]\n\t" \
  "v_lshlrev_b32_sdwa v226, 1, v184 dst_sel:DWORD dst_unused:UNUSED_PAD src0_sel:DWORD src1_sel:WORD_1\n\t" \
  "v_lshlrev_b32_sdwa v224, 1, v186 dst_sel:DWORD dst_unused:UNUSED_PAD src0_sel:DWORD src1_sel:WORD_1\n\t" \
  "ds_read_b32 v220, v220 offset:10016\n\t" \
  "v_mfma_f32_16x16x32_bf16 v[204:207], v[240:243], v[116:119], v[204:207]\n\t" \
  "ds_read_b64_tr_b16 v[208:209], v249 offset:24600\n\t" \
  "ds_read_b64_tr_b16 v[236:237], v249 offset:6152\n\t" \
  "v_lshlrev_b32_sdwa v227, 1, v185 dst_sel:DWORD dst_unused:UNUSED_PAD src0_sel:DWORD src1_sel:WORD_1\n\t" \
  "ds_read_b64_tr_b16 v[238:239], v249 offset:7688\n\t" \
  "ds_read_u16_d16_hi v225, v225 offset:10528\n\t" \
  "v_mfma_f32_16x16x32_bf16 v[188:191], v[212:215], v[116:119], v[188:191]\n\t" \
  "v_fma_mix_f32 v229, |v165|, v229, v229 op_sel:[0,1,0] op_sel_hi:[0,1,1] clamp\n\t" \
  "v_fma_f32 v165, |v165|, v229, v165 mul:4\n\t" \
  "ds_read_b32 v222, v222 offset:10016\n\t" \
  "ds_read_b64_tr_b16 v[212:213], v249 offset:24608\n\t" \
  "v_add_u32 v248, -81480, v248\n\t" \
  "s_waitcnt lgkmcnt(9)\n\t" \
  "ds_read_u16 v224, v224 offset:10528\n\t" \
  "v_fma_mix_f32 v228, |v164|, v228, v228 op_sel:[0,1,0] op_sel_hi:[0,1,1] clamp\n\t" \
  "v_fma_mix_f32 v230, |v166|, v230, v230 op_sel:[0,1,0] op_sel_hi:[0,1,1] clamp\n\t" \
  "v_mfma_f32_16x16x32_bf16 v[204:207], v[244:247], v[132:135], v[204:207]\n\t" \
  "ds_read_b64_tr_b16 v[214:215], v249 offset:26144\n\t" \
  "v_mfma_f32_16x16x32_bf16 v[196:199], v[240:243], v[112:115], v[196:199]\n\t" \
  "ds_read_b64_tr_b16 v[242:243], v249 offset:7696\n\t" \
  "v_or_b32 v173, v234, v235\n\t" \
  "ds_read_b64_tr_b16 v[240:241], v249 offset:6160\n\t" \
  "ds_read_u16_d16_hi v227, v227 offset:10528\n\t" \
  "v_mfma_f32_16x16x32_bf16 v[188:191], v[216:219], v[132:135], v[188:191]\n\t" \
  "v_fma_f32 v164, |v164|, v228, v164 mul:4\n\t" \
  "v_or_b32 v172, v232, v233\n\t" \
  "ds_read_b32 v223, v223 offset:10016\n\t" \
  "v_fma_f32 v166, |v166|, v230, v166 mul:4\n\t" \
  "v_mfma_f32_16x16x32_bf16 v[196:199], v[244:247], v[128:131], v[196:199]\n\t" \
  "s_waitcnt lgkmcnt(11)\n\t" \
  "ds_read_b64_tr_b16 v[244:245], v249 offset:6216\n\t" \
  "v_cvt_pk_bf16_f32 v164, v164, v165\n\t" \
  "v_cvt_pk_bf16_f32 v165, v166, v167\n\t" \
  "v_cvt_pknorm_i16_f32 v230, |v192|, |v193|\n\t" \
  "v_lshlrev_b32_sdwa v235, 1, v201 dst_sel:DWORD dst_unused:UNUSED_PAD src0_sel:DWORD src1_sel:WORD_1\n\t" \
  "v_mfma_f32_16x16x32_bf16 v[180:183], v[216:219], v[128:131], v[180:183]\n\t" \
  "ds_read_b64_tr_b16 v[218:219], v249 offset:26200\n\t" \
  "v_cvt_pknorm_i16_f32 v228, |v194|, |v195|\n\t" \
  "v_mfma_f32_16x16x32_bf16 v[0:3], v[208:211], v[146:149], v[0:3]\n\t" \
  "ds_read_b64_tr_b16 v[246:247], v249 offset:7752\n\t" \
  "ds_read_b64_tr_b16 v[216:217], v249 offset:24664\n\t" \
  "v_lshlrev_b32_sdwa v229, 2, v228 dst_sel:DWORD dst_unused:UNUSED_PAD src0_sel:DWORD src1_sel:BYTE_3\n\t" \
  "v_lshlrev_b32_sdwa v231, 2, v230 dst_sel:DWORD dst_unused:UNUSED_PAD src0_sel:DWORD src1_sel:BYTE_3\n\t" \
  "v_lshlrev_b32_sdwa v228, 2, v228 dst_sel:DWORD dst_unused:UNUSED_PAD src0_sel:DWORD src1_sel:BYTE_1\n\t" \
  "v_lshlrev_b32_sdwa v230, 2, v230 dst_sel:DWORD dst_unused:UNUSED_PAD src0_sel:DWORD src1_sel:BYTE_1\n\t" \
  "v_mfma_f32_16x16x32_bf16 v[24:27], v[208:211], v[154:157], v[24:27]\n\t" \
  "s_waitcnt lgkmcnt(8)\n\t" \
  "ds_read_b64_tr_b16 v[210:211], v249 offset:26208\n\t" \
  "v_mfma_f32_16x16x32_bf16 v[0:3], v[236:239], v[162:165], v[0:3]\n\t" \
  "ds_read_u16 v226, v226 offset:10528\n\t" \
  "v_fma_mix_f32 v220, |v178|, v220, v220 op_sel:[0,1,0] op_sel_hi:[0,1,1] clamp\n\t" \
  "v_mfma_f32_16x16x32_bf16 v[4:7], v[212:215], v[146:149], v[4:7]\n\t" \
  "ds_read_b64_tr_b16 v[208:209], v249 offset:24672\n\t" \
  "v_lshlrev_b32_sdwa v232, 1, v202 dst_sel:DWORD dst_unused:UNUSED_PAD src0_sel:DWORD src1_sel:WORD_1\n\t" \
  "s_waitcnt lgkmcnt(7)\n\t" \
  "v_fma_mix_f32 v223, |v177|, v223, v223 op_sel:[0,1,0] op_sel_hi:[0,1,1] clamp\n\t" \
  "v_lshlrev_b32_sdwa v233, 1, v203 dst_sel:DWORD dst_unused:UNUSED_PAD src0_sel:DWORD src1_sel:WORD_1\n\t" \
  "v_mfma_f32_16x16x32_bf16 v[28:31], v[212:215], v[154:157], v[28:31]\n\t" \
  "ds_read_b64_tr_b16 v[212:213], v249 offset:24728\n\t" \
  "v_fma_f32 v178, |v178|, v220, v178 mul:4\n\t" \
  "v_mfma_f32_16x16x32_bf16 v[24:27], v[236:239], v[170:173], v[24:27]\n\t" \
  "ds_read_b32 v229, v229 offset:10016\n\t" \
  "ds_read_b32 v231, v231 offset:10016\n\t" \
  "ds_read_b64_tr_b16 v[238:239], v249 offset:7760\n\t" \
  "v_lshlrev_b32_sdwa v234, 1, v200 dst_sel:DWORD dst_unused:UNUSED_PAD src0_sel:DWORD src1_sel:WORD_1\n\t" \
  "s_waitcnt lgkmcnt(5)\n\t" \
  "v_or_b32 v186, v226, v227\n\t" \
  "v_or_b32 v187, v224, v225\n\t" \
  "v_mfma_f32_16x16x32_bf16 v[28:31], v[240:243], v[170:173], v[28:31]\n\t" \
  "ds_read_b64_tr_b16 v[236:237], v249 offset:6224\n\t" \
  "v_mfma_f32_16x16x32_bf16 v[32:35], v[216:219], v[154:157], v[32:35]\n\t" \
  "ds_read_u16_d16_hi v233, v233 offset:10528\n\t" \
  "v_fma_mix_f32 v222, |v176|, v222, v222 op_sel:[0,1,0] op_sel_hi:[0,1,1] clamp\n\t" \
  "v_fma_f32 v177, |v177|, v223, v177 mul:4\n\t" \
  "v_fma_mix_f32 v221, |v179|, v221, v221 op_sel:[0,1,0] op_sel_hi:[0,1,1] clamp\n\t" \
  "v_mfma_f32_16x16x32_bf16 v[4:7], v[240:243], v[162:165], v[4:7]\n\t" \
  "ds_read_b64_tr_b16 v[242:243], v249 offset:7816\n\t" \
  "v_mfma_f32_16x16x32_bf16 v[8:11], v[216:219], v[146:149], v[8:11]\n\t" \
  "ds_read_b64_tr_b16 v[240:241], v249 offset:6280\n\t" \
  "ds_read_b32 v230, v230 offset:10016\n\t" \
  "v_add_u32 v254, 81480, v254\n\t" \
  "v_fma_f32 v176, |v176|, v222, v176 mul:4\n\t" \
  "v_mfma_f32_16x16x32_bf16 v[8:11], v[244:247], v[162:165], v[8:11]\n\t" \
  "ds_read_b32 v228, v228 offset:10016\n\t" \
  "ds_read_b64_tr_b16 v[218:219], v249 offset:26272\n\t" \
  "ds_read_b64_tr_b16 v[214:215], v249 offset:26264\n\t" \
  "v_fma_f32 v179, |v179|, v221, v179 mul:4\n\t" \
  "v_cvt_pk_bf16_f32 v179, v178, v179\n\t" \
  "v_cvt_pk_bf16_f32 v178, v176, v177\n\t" \
  "v_mfma_f32_16x16x32_bf16 v[32:35], v[244:247], v[170:173], v[32:35]\n\t" \
  "s_waitcnt lgkmcnt(12)\n\t" \
  "ds_read_b64_tr_b16 v[244:245], v249 offset:6288\n\t" \
  "ds_read_b64_tr_b16 v[216:217], v249 offset:24736\n\t" \
  "v_mfma_f32_16x16x32_bf16 v[36:39], v[208:211], v[154:157], v[36:39]\n\t" \
  "v_cvt_pknorm_i16_f32 v222, |v182|, |v183|\n\t" \
  "v_cvt_pknorm_i16_f32 v220, |v180|, |v181|\n\t" \
  "s_waitcnt lgkmcnt(9)\n\t" \
  "ds_read_u16 v234, v234 offset:10528\n\t" \
  "v_mfma_f32_16x16x32_bf16 v[12:15], v[208:211], v[146:149], v[12:15]\n\t" \
  "ds_read_b64_tr_b16 v[246:247], v249 offset:7824\n\t" \
  "v_lshlrev_b32_sdwa v223, 2, v222 dst_sel:DWORD dst_unused:UNUSED_PAD src0_sel:DWORD src1_sel:BYTE_3\n\t" \
  "v_lshlrev_b32_sdwa v221, 2, v220 dst_sel:DWORD dst_unused:UNUSED_PAD src0_sel:DWORD src1_sel:BYTE_3\n\t" \
  "v_lshlrev_b32_sdwa v222, 2, v222 dst_sel:DWORD dst_unused:UNUSED_PAD src0_sel:DWORD src1_sel:BYTE_1\n\t" \
  "v_mfma_f32_16x16x32_bf16 v[12:15], v[236:239], v[162:165], v[12:15]\n\t" \
  "s_waitcnt lgkmcnt(4)\n\t" \
  "ds_read_b32 v221, v221 offset:10016\n\t" \
  "ds_read_b32 v223, v223 offset:10016\n\t" \
  "v_mfma_f32_16x16x32_bf16 v[36:39], v[236:239], v[170:173], v[36:39]\n\t" \
  "ds_read_u16_d16_hi v235, v235 offset:10528\n\t" \
  "v_lshlrev_b32_sdwa v225, 1, v189 dst_sel:DWORD dst_unused:UNUSED_PAD src0_sel:DWORD src1_sel:WORD_1\n\t" \
  "v_mfma_f32_16x16x32_bf16 v[16:19], v[212:215], v[146:149], v[16:19]\n\t" \
  "ds_read_b32 v222, v222 offset:10016\n\t" \
  "v_lshlrev_b32_sdwa v227, 1, v191 dst_sel:DWORD dst_unused:UNUSED_PAD src0_sel:DWORD src1_sel:WORD_1\n\t" \
  "ds_read_b64_tr_b16 v[208:209], v248 offset:36904\n\t" \
  "v_fma_mix_f32 v228, |v194|, v228, v228 op_sel:[0,1,0] op_sel_hi:[0,1,1] clamp\n\t" \
  "v_lshlrev_b32_sdwa v226, 1, v190 dst_sel:DWORD dst_unused:UNUSED_PAD src0_sel:DWORD src1_sel:WORD_1\n\t" \
  "s_waitcnt lgkmcnt(7)\n\t" \
  "v_mfma_f32_16x16x32_bf16 v[20:23], v[216:219], v[146:149], v[20:23]\n\t" \
  "ds_read_u16 v232, v232 offset:10528\n\t" \
  "v_lshlrev_b32_sdwa v224, 1, v188 dst_sel:DWORD dst_unused:UNUSED_PAD src0_sel:DWORD src1_sel:WORD_1\n\t" \
  "s_waitcnt lgkmcnt(3)\n\t" \
  "v_or_b32 v202, v234, v235\n\t" \
  "v_lshlrev_b32_sdwa v220, 2, v220 dst_sel:DWORD dst_unused:UNUSED_PAD src0_sel:DWORD src1_sel:BYTE_1\n\t" \
  "v_mfma_f32_16x16x32_bf16 v[40:43], v[212:215], v[154:157], v[40:43]\n\t" \
  "ds_read_b64_tr_b16 v[214:215], v248 offset:41000\n\t" \
  "ds_read_b64_tr_b16 v[236:237], v254 offset:43048\n\t" \
  "v_mfma_f32_16x16x32_bf16 v[40:43], v[240:243], v[170:173], v[40:43]\n\t" \
  "ds_read_u16_d16_hi v225, v225 offset:10528\n\t" \
  "ds_read_b64_tr_b16 v[210:211], v248 offset:37928\n\t" \
  "v_fma_f32 v194, |v194|, v228, v194 mul:4\n\t" \
  "v_fma_mix_f32 v231, |v193|, v231, v231 op_sel:[0,1,0] op_sel_hi:[0,1,1] clamp\n\t" \
  "v_mfma_f32_16x16x32_bf16 v[16:19], v[240:243], v[162:165], v[16:19]\n\t" \
  "ds_read_b64_tr_b16 v[240:241], v254 offset:46120\n\t" \
  "ds_read_u16 v226, v226 offset:10528\n\t" \
  "ds_read_b32 v220, v220 offset:10016\n\t" \
  "v_mfma_f32_16x16x32_bf16 v[44:47], v[216:219], v[154:157], v[44:47]\n\t" \
  "ds_read_b64_tr_b16 v[218:219], v248 offset:42024\n\t" \
  "v_cvt_pknorm_i16_f32 v228, |v196|, |v197|\n\t" \
  "v_fma_mix_f32 v230, |v192|, v230, v230 op_sel:[0,1,0] op_sel_hi:[0,1,1] clamp\n\t" \
  "s_waitcnt lgkmcnt(4)\n\t" \
  "v_mfma_f32_16x16x32_bf16 v[144:147], v[208:211], v[104:107], 0\n\t" \
  "ds_read_u16_d16_hi v227, v227 offset:10528\n\t" \
  "ds_read_b64_tr_b16 v[238:239], v254 offset:44072\n\t" \
  "v_fma_mix_f32 v229, |v195|, v229, v229 op_sel:[0,1,0] op_sel_hi:[0,1,1] clamp\n\t" \
  "v_fma_f32 v195, |v195|, v229, v195 mul:4\n\t" \
  "v_cvt_pk_bf16_f32 v195, v194, v195\n\t" \
  "v_fma_f32 v192, |v192|, v230, v192 mul:4\n\t" \
  "v_mfma_f32_16x16x32_bf16 v[20:23], v[244:247], v[162:165], v[20:23]\n\t" \
  "ds_read_b64_tr_b16 v[216:217], v248 offset:38952\n\t" \
  "ds_read_u16 v224, v224 offset:10528\n\t" \
  "v_mfma_f32_16x16x32_bf16 v[44:47], v[244:247], v[170:173], v[44:47]\n\t" \
  "ds_read_b64_tr_b16 v[244:245], v254 offset:45096\n\t" \
  "ds_read_b64_tr_b16 v[242:243], v254 offset:47144\n\t" \
  "v_or_b32 v203, v232, v233\n\t" \
  "v_lshlrev_b32_sdwa v232, 1, v204 dst_sel:DWORD dst_unused:UNUSED_PAD src0_sel:DWORD src1_sel:WORD_1\n\t" \
  "v_fma_f32 v193, |v193|, v231, v193 mul:4\n\t" \
  "v_mfma_f32_16x16x32_bf16 v[152:155], v[208:211], v[108:111], 0\n\t" \
  "s_waitcnt lgkmcnt(4)\n\t" \
  "ds_read_b64_tr_b16 v[246:247], v254 offset:48168\n\t" \
  "ds_read_b64_tr_b16 v[210:211], v248 offset:38440\n\t" \
  "v_cvt_pk_bf16_f32 v194, v192, v193\n\t" \
  "v_lshlrev_b32_sdwa v233, 1, v205 dst_sel:DWORD dst_unused:UNUSED_PAD src0_sel:DWORD src1_sel:WORD_1\n\t" \
  "ds_read_b64_tr_b16 v[212:213], v248 offset:39976\n\t" \
  "v_fma_mix_f32 v222, |v182|, v222, v222 op_sel:[0,1,0] op_sel_hi:[0,1,1] clamp\n\t" \
  "v_mfma_f32_16x16x32_bf16 v[160:163], v[236:239], v[104:107], 0\n\t" \
  "v_lshlrev_b32_sdwa v229, 2, v228 dst_sel:DWORD dst_unused:UNUSED_PAD src0_sel:DWORD src1_sel:BYTE_3\n\t" \
  "v_lshlrev_b32_sdwa v228, 2, v228 dst_sel:DWORD dst_unused:UNUSED_PAD src0_sel:DWORD src1_sel:BYTE_1\n\t" \
  "v_cvt_pknorm_i16_f32 v230, |v198|, |v199|\n\t" \
  "v_lshlrev_b32_sdwa v231, 2, v230 dst_sel:DWORD dst_unused:UNUSED_PAD src0_sel:DWORD src1_sel:BYTE_3\n\t" \
  "v_mfma_f32_16x16x32_bf16 v[168:171], v[236:239], v[108:111], 0\n\t" \
  "ds_read_b64_tr_b16 v[238:239], v254 offset:44584\n\t" \
  "v_fma_f32 v182, |v182|, v222, v182 mul:4\n\t" \
  "s_waitcnt lgkmcnt(1)\n\t" \
  "ds_read_b32 v229, v229 offset:10016\n\t" \
  "v_lshlrev_b32_sdwa v230, 2, v230 dst_sel:DWORD dst_unused:UNUSED_PAD src0_sel:DWORD src1_sel:BYTE_1\n\t" \
  "v_fma_mix_f32 v220, |v180|, v220, v220 op_sel:[0,1,0] op_sel_hi:[0,1,1] clamp\n\t" \
  "v_mfma_f32_16x16x32_bf16 v[152:155], v[212:215], v[124:127], v[152:155]\n\t" \
  "ds_read_b64_tr_b16 v[236:237], v254 offset:43560\n\t" \
  "ds_read_u16_d16_hi v233, v233 offset:10528\n\t" \
  "v_fma_mix_f32 v223, |v183|, v223, v223 op_sel:[0,1,0] op_sel_hi:[0,1,1] clamp\n\t" \
  "v_fma_f32 v183, |v183|, v223, v183 mul:4\n\t" \
  "v_mfma_f32_16x16x32_bf16 v[168:171], v[240:243], v[124:127], v[168:171]\n\t" \
  "ds_read_b64_tr_b16 v[208:209], v248 offset:37416\n\t" \
  "ds_read_b32 v231, v231 offset:10016\n\t" \
  "v_mfma_f32_16x16x32_bf16 v[144:147], v[212:215], v[120:123], v[144:147]\n\t" \
  "ds_read_b32 v230, v230 offset:10016\n\t" \
  "v_fma_f32 v180, |v180|, v220, v180 mul:4\n\t" \
  "v_or_b32 v188, v224, v225\n\t" \
  "v_fma_mix_f32 v221, |v181|, v221, v221 op_sel:[0,1,0] op_sel_hi:[0,1,1] clamp\n\t" \
  "v_lshlrev_b32_sdwa v235, 1, v207 dst_sel:DWORD dst_unused:UNUSED_PAD src0_sel:DWORD src1_sel:WORD_1\n\t" \
  "v_mfma_f32_16x16x32_bf16 v[144:147], v[216:219], v[136:139], v[144:147]\n\t" \
  "ds_read_u16 v232, v232 offset:10528\n\t" \
  "v_or_b32 v189, v226, v227\n\t" \
  "v_mfma_f32_16x16x32_bf16 v[160:163], v[240:243], v[120:123], v[160:163]\n\t" \
  "ds_read_b64_tr_b16 v[212:213], v248 offset:40488\n\t" \
  "v_fma_f32 v181, |v181|, v221, v181 mul:4\n\t" \
  "v_mfma_f32_16x16x32_bf16 v[152:155], v[216:219], v[140:143], v[152:155]\n\t" \
  "ds_read_b64_tr_b16 v[240:241], v254 offset:46632\n\t" \
  "ds_read_b32 v228, v228 offset:10016\n\t" \
  "ds_read_b64_tr_b16 v[214:215], v248 offset:41512\n\t" \
  "ds_read_b64_tr_b16 v[242:243], v254 offset:47656\n\t" \
  "v_lshlrev_b32_sdwa v234, 1, v206 dst_sel:DWORD dst_unused:UNUSED_PAD src0_sel:DWORD src1_sel:WORD_1\n\t" \
  "v_mfma_f32_16x16x32_bf16 v[160:163], v[244:247], v[136:139], v[160:163]\n\t" \
  "ds_read_u16_d16_hi v235, v235 offset:10528\n\t" \
  "v_cvt_pknorm_i16_f32 v220, |v146|, |v147|\n\t" \
  "v_cvt_pk_bf16_f32 v180, v180, v181\n\t" \
  "v_cvt_pk_bf16_f32 v181, v182, v183\n\t" \
  "v_mfma_f32_16x16x32_bf16 v[168:171], v[244:247], v[140:143], v[168:171]\n\t" \
  "s_waitcnt lgkmcnt(3)\n\t" \
  "ds_read_b64_tr_b16 v[246:247], v254 offset:48680\n\t" \
  "ds_read_b64_tr_b16 v[244:245], v254 offset:45608\n\t" \
  "v_lshlrev_b32_sdwa v221, 2, v220 dst_sel:DWORD dst_unused:UNUSED_PAD src0_sel:DWORD src1_sel:BYTE_3\n\t" \
  "v_lshlrev_b32_sdwa v220, 2, v220 dst_sel:DWORD dst_unused:UNUSED_PAD src0_sel:DWORD src1_sel:BYTE_1\n\t" \
  "v_mfma_f32_16x16x32_bf16 v[148:151], v[208:211], v[104:107], 0\n\t" \
  "ds_read_b64_tr_b16 v[218:219], v248 offset:42536\n\t" \
  "v_lshlrev_b32_sdwa v225, 1, v155 dst_sel:DWORD dst_unused:UNUSED_PAD src0_sel:DWORD src1_sel:WORD_1\n\t" \
  "v_cvt_pknorm_i16_f32 v222, |v144|, |v145|\n\t" \
  "v_lshlrev_b32_sdwa v223, 2, v222 dst_sel:DWORD dst_unused:UNUSED_PAD src0_sel:DWORD src1_sel:BYTE_3\n\t" \
  "v_mfma_f32_16x16x32_bf16 v[156:159], v[208:211], v[108:111], 0\n\t" \
  "ds_read_b64_tr_b16 v[216:217], v248 offset:39464\n\t" \
  "ds_read_u16 v234, v234 offset:10528\n\t" \
  "v_fma_mix_f32 v231, |v199|, v231, v231 op_sel:[0,1,0] op_sel_hi:[0,1,1] clamp\n\t" \
  "v_fma_mix_f32 v230, |v198|, v230, v230 op_sel:[0,1,0] op_sel_hi:[0,1,1] clamp\n\t" \
  "v_fma_mix_f32 v228, |v196|, v228, v228 op_sel:[0,1,0] op_sel_hi:[0,1,1] clamp\n\t" \
  "v_mfma_f32_16x16x32_bf16 v[164:167], v[236:239], v[104:107], 0\n\t" \
  "ds_read_b32 v221, v221 offset:10016\n\t" \
  "v_lshlrev_b32_sdwa v227, 1, v153 dst_sel:DWORD dst_unused:UNUSED_PAD src0_sel:DWORD src1_sel:WORD_1\n\t" \
  "v_lshlrev_b32_sdwa v222, 2, v222 dst_sel:DWORD dst_unused:UNUSED_PAD src0_sel:DWORD src1_sel:BYTE_1\n\t" \
  "v_mfma_f32_16x16x32_bf16 v[172:175], v[236:239], v[108:111], 0\n\t" \
  "s_waitcnt lgkmcnt(8)\n\t" \
  "ds_read_b64_tr_b16 v[236:237], v249 offset:12304\n\t" \
  "ds_read_b32 v220, v220 offset:10016\n\t" \
  "ds_read_b32 v223, v223 offset:10016\n\t" \
  "ds_read_b32 v222, v222 offset:10016\n\t" \
  "ds_read_b64_tr_b16 v[238:239], v249 offset:13840\n\t" \
  "v_mfma_f32_16x16x32_bf16 v[156:159], v[212:215], v[124:127], v[156:159]\n\t" \
  "ds_read_b64_tr_b16 v[210:211], v249 offset:32288\n\t" \
  "v_fma_f32 v198, |v198|, v230, v198 mul:4\n\t" \
  "v_mfma_f32_16x16x32_bf16 v[148:151], v[212:215], v[120:123], v[148:151]\n\t" \
  "s_waitcnt lgkmcnt(13)\n\t" \
  "v_fma_f32 v196, |v196|, v228, v196 mul:4\n\t" \
  "v_cvt_pknorm_i16_f32 v230, |v160|, |v161|\n\t" \
  "v_fma_mix_f32 v229, |v197|, v229, v229 op_sel:[0,1,0] op_sel_hi:[0,1,1] clamp\n\t" \
  "v_lshlrev_b32_sdwa v224, 1, v154 dst_sel:DWORD dst_unused:UNUSED_PAD src0_sel:DWORD src1_sel:WORD_1\n\t" \
  "v_mfma_f32_16x16x32_bf16 v[164:167], v[240:243], v[120:123], v[164:167]\n\t" \
  "ds_read_b64_tr_b16 v[214:215], v249 offset:32296\n\t" \
  "ds_read_b64_tr_b16 v[212:213], v249 offset:30760\n\t" \
  "v_mfma_f32_16x16x32_bf16 v[172:175], v[240:243], v[124:127], v[172:175]\n\t" \
  "s_waitcnt lgkmcnt(9)\n\t" \
  "ds_read_b64_tr_b16 v[242:243], v249 offset:13848\n\t" \
  "v_lshlrev_b32_sdwa v226, 1, v152 dst_sel:DWORD dst_unused:UNUSED_PAD src0_sel:DWORD src1_sel:WORD_1\n\t" \
  "v_or_b32 v205, v234, v235\n\t" \
  "v_mfma_f32_16x16x32_bf16 v[172:175], v[244:247], v[140:143], v[172:175]\n\t" \
  "ds_read_u16_d16_hi v225, v225 offset:10528\n\t" \
  "ds_read_b64_tr_b16 v[208:209], v249 offset:30752\n\t" \
  "s_setprio 3\n\t" \
  "ds_read_u16 v224, v224 offset:10528\n\t" \
  "v_fma_f32 v199, |v199|, v231, v199 mul:4\n\t" \
  "v_fma_f32 v197, |v197|, v229, v197 mul:4\n\t" \
  "v_mfma_f32_16x16x32_bf16 v[148:151], v[216:219], v[136:139], v[148:151]\n\t" \
  "ds_read_u16_d16_hi v227, v227 offset:10528\n\t" \
  "ds_read_b64_tr_b16 v[240:241], v249 offset:12312\n\t" \
  "v_mfma_f32_16x16x32_bf16 v[156:159], v[216:219], v[140:143], v[156:159]\n\t" \
  "ds_read_b64_tr_b16 v[218:219], v249 offset:32352\n\t" \
  "v_or_b32 v204, v232, v233\n\t" \
  "v_mfma_f32_16x16x32_bf16 v[164:167], v[244:247], v[136:139], v[164:167]\n\t" \
  "s_waitcnt lgkmcnt(4)\n\t" \
  "ds_read_b64_tr_b16 v[244:245], v249 offset:12368\n\t" \
  "ds_read_b64_tr_b16 v[246:247], v249 offset:13904\n\t" \
  "ds_read_b64_tr_b16 v[216:217], v249 offset:30816\n\t" \
  "v_cvt_pknorm_i16_f32 v228, |v162|, |v163|\n\t" \
  "v_lshlrev_b32_sdwa v229, 2, v228 dst_sel:DWORD dst_unused:UNUSED_PAD src0_sel:DWORD src1_sel:BYTE_3\n\t" \
  "v_mfma_f32_16x16x32_bf16 v[24:27], v[208:211], v[186:189], v[24:27]\n\t" \
  "ds_read_b32 v229, v229 offset:10016\n\t" \
  "v_lshlrev_b32_sdwa v228, 2, v228 dst_sel:DWORD dst_unused:UNUSED_PAD src0_sel:DWORD src1_sel:BYTE_1\n\t" \
  "v_fma_mix_f32 v222, |v144|, v222, v222 op_sel:[0,1,0] op_sel_hi:[0,1,1] clamp\n\t" \
  "v_mfma_f32_16x16x32_bf16 v[4:7], v[212:215], v[178:181], v[4:7]\n\t" \
  "ds_read_b32 v228, v228 offset:10016\n\t" \
  "v_fma_f32 v144, |v144|, v222, v144 mul:4\n\t" \
  "v_cvt_pk_bf16_f32 v196, v196, v197\n\t" \
  "v_cvt_pk_bf16_f32 v197, v198, v199\n\t" \
  "v_lshlrev_b32_sdwa v233, 1, v171 dst_sel:DWORD dst_unused:UNUSED_PAD src0_sel:DWORD src1_sel:WORD_1\n\t" \
  "v_mfma_f32_16x16x32_bf16 v[0:3], v[208:211], v[178:181], v[0:3]\n\t" \
  "ds_read_b64_tr_b16 v[208:209], v249 offset:30824\n\t" \
  "v_nop\n\t" \
  "v_mfma_f32_16x16x32_bf16 v[0:3], v[236:239], v[194:197], v[0:3]\n\t" \
  "ds_read_u16 v226, v226 offset:10528\n\t" \
  "ds_read_b64_tr_b16 v[210:211], v249 offset:32360\n\t" \
  "v_mfma_f32_16x16x32_bf16 v[28:31], v[212:215], v[186:189], v[28:31]\n\t" \
  "s_waitcnt lgkmcnt(9)\n\t" \
  "ds_read_b64_tr_b16 v[212:213], v249 offset:30880\n\t" \
  "ds_read_b64_tr_b16 v[214:215], v249 offset:32416\n\t" \
  "v_fma_mix_f32 v221, |v147|, v221, v221 op_sel:[0,1,0] op_sel_hi:[0,1,1] clamp\n\t" \
  "v_lshlrev_b32_sdwa v231, 2, v230 dst_sel:DWORD dst_unused:UNUSED_PAD src0_sel:DWORD src1_sel:BYTE_3\n\t" \
  "v_lshlrev_b32_sdwa v230, 2, v230 dst_sel:DWORD dst_unused:UNUSED_PAD src0_sel:DWORD src1_sel:BYTE_1\n\t" \
  "v_mfma_f32_16x16x32_bf16 v[4:7], v[240:243], v[194:197], v[4:7]\n\t" \
  "ds_read_b32 v231, v231 offset:10016\n\t" \
  "v_lshlrev_b32_sdwa v232, 1, v170 dst_sel:DWORD dst_unused:UNUSED_PAD src0_sel:DWORD src1_sel:WORD_1\n\t" \
  "v_fma_mix_f32 v220, |v146|, v220, v220 op_sel:[0,1,0] op_sel_hi:[0,1,1] clamp\n\t" \
  "v_mfma_f32_16x16x32_bf16 v[28:31], v[240:243], v[202:205], v[28:31]\n\t" \
  "ds_read_b64_tr_b16 v[242:243], v249 offset:13968\n\t" \
  "v_lshlrev_b32_sdwa v235, 1, v169 dst_sel:DWORD dst_unused:UNUSED_PAD src0_sel:DWORD src1_sel:WORD_1\n\t" \
  "v_mfma_f32_16x16x32_bf16 v[24:27], v[236:239], v[202:205], v[24:27]\n\t" \
  "s_waitcnt lgkmcnt(4)\n\t" \
  "ds_read_b64_tr_b16 v[236:237], v249 offset:12376\n\t" \
  "v_lshlrev_b32_sdwa v234, 1, v168 dst_sel:DWORD dst_unused:UNUSED_PAD src0_sel:DWORD src1_sel:WORD_1\n\t" \
  "v_fma_mix_f32 v223, |v145|, v223, v223 op_sel:[0,1,0] op_sel_hi:[0,1,1] clamp\n\t" \
  "v_mfma_f32_16x16x32_bf16 v[36:39], v[208:211], v[186:189], v[36:39]\n\t" \
  "ds_read_b32 v230, v230 offset:10016\n\t" \
  "v_or_b32 v154, v226, v227\n\t" \
  "v_cvt_pknorm_i16_f32 v222, |v150|, |v151|\n\t" \
  "v_fma_f32 v145, |v145|, v223, v145 mul:4\n\t" \
  "v_lshlrev_b32_sdwa v223, 2, v222 dst_sel:DWORD dst_unused:UNUSED_PAD src0_sel:DWORD src1_sel:BYTE_3\n\t" \
  "v_cvt_pk_bf16_f32 v24, v24, v25\n\t" \
  "v_lshlrev_b32_sdwa v227, 1, v159 dst_sel:DWORD dst_unused:UNUSED_PAD src0_sel:DWORD src1_sel:WORD_1\n\t" \
  "v_mfma_f32_16x16x32_bf16 v[8:11], v[216:219], v[178:181], v[8:11]\n\t" \
  "ds_read_b64_tr_b16 v[240:241], v249 offset:12432\n\t" \
  "v_cvt_pk_bf16_f32 v0, v0, v1\n\t" \
  "v_mfma_f32_16x16x32_bf16 v[32:35], v[216:219], v[186:189], v[32:35]\n\t" \
  "ds_read_u16 v232, v232 offset:10528\n\t" \
  "ds_read_b64_tr_b16 v[238:239], v249 offset:13912\n\t" \
  "v_cvt_pk_bf16_f32 v1, v2, v3\n\t" \
  "v_cvt_pk_bf16_f32 v2, v4, v5\n\t" \
  "v_cvt_pk_bf16_f32 v3, v6, v7\n\t" \
  "v_mfma_f32_16x16x32_bf16 v[8:11], v[244:247], v[194:197], v[8:11]\n\t" \
  "ds_read_b64_tr_b16 v[218:219], v249 offset:32424\n\t" \
  "v_fma_mix_f32 v229, |v163|, v229, v229 op_sel:[0,1,0] op_sel_hi:[0,1,1] clamp\n\t" \
  "v_cvt_pk_bf16_f32 v25, v26, v27\n\t" \
  "v_cvt_pk_bf16_f32 v27, v30, v31\n\t" \
  "v_fma_f32 v147, |v147|, v221, v147 mul:4\n\t" \
  "v_mfma_f32_16x16x32_bf16 v[32:35], v[244:247], v[202:205], v[32:35]\n\t" \
  "ds_read_b64_tr_b16 v[246:247], v249 offset:13976\n\t" \
  "buffer_store_dwordx4 v[0:3], v250, s[20:23], s63 offen offset:0 sc1\n\t" \
  "ds_read_b64_tr_b16 v[244:245], v249 offset:12440\n\t" \
  "ds_read_u16_d16_hi v235, v235 offset:10528\n\t" \
  "v_or_b32 v155, v224, v225\n\t" \
  "v_cvt_pk_bf16_f32 v8, v8, v9\n\t" \
  "v_lshlrev_b32_sdwa v224, 1, v156 dst_sel:DWORD dst_unused:UNUSED_PAD src0_sel:DWORD src1_sel:WORD_1\n\t" \
  "v_mfma_f32_16x16x32_bf16 v[12:15], v[208:211], v[178:181], v[12:15]\n\t" \
  "s_waitcnt lgkmcnt(4)\n\t" \
  "v_lshlrev_b32_sdwa v222, 2, v222 dst_sel:DWORD dst_unused:UNUSED_PAD src0_sel:DWORD src1_sel:BYTE_1\n\t" \
  "v_cvt_pk_bf16_f32 v9, v10, v11\n\t" \
  "v_mfma_f32_16x16x32_bf16 v[12:15], v[236:239], v[194:197], v[12:15]\n\t" \
  "ds_read_u16_d16_hi v233, v233 offset:10528\n\t" \
  "ds_read_b32 v222, v222 offset:10016\n\t" \
  "v_fma_f32 v146, |v146|, v220, v146 mul:4\n\t" \
  "v_mfma_f32_16x16x32_bf16 v[16:19], v[212:215], v[178:181], v[16:19]\n\t" \
  "ds_read_b64_tr_b16 v[216:217], v249 offset:30888\n\t" \
  "v_lshlrev_b32_sdwa v225, 1, v157 dst_sel:DWORD dst_unused:UNUSED_PAD src0_sel:DWORD src1_sel:WORD_1\n\t" \
  "v_cvt_pk_bf16_f32 v147, v146, v147\n\t" \
  "v_cvt_pknorm_i16_f32 v220, |v148|, |v149|\n\t" \
  "v_lshlrev_b32_sdwa v221, 2, v220 dst_sel:DWORD dst_unused:UNUSED_PAD src0_sel:DWORD src1_sel:BYTE_3\n\t" \
  "v_mfma_f32_16x16x32_bf16 v[16:19], v[240:243], v[194:197], v[16:19]\n\t" \
  "ds_read_b32 v223, v223 offset:10016\n\t" \
  "ds_read_b64_tr_b16 v[208:209], v248 offset:0\n\t" \
  "ds_read_u16 v234, v234 offset:10528\n\t" \
  "v_mfma_f32_16x16x32_bf16 v[40:43], v[212:215], v[186:189], v[40:43]\n\t" \
  "ds_read_b32 v221, v221 offset:10016\n\t" \
  "v_fma_mix_f32 v228, |v162|, v228, v228 op_sel:[0,1,0] op_sel_hi:[0,1,1] clamp\n\t" \
  "s_waitcnt lgkmcnt(6)\n\t" \
  "v_fma_f32 v162, |v162|, v228, v162 mul:4\n\t" \
  "v_lshlrev_b32_sdwa v220, 2, v220 dst_sel:DWORD dst_unused:UNUSED_PAD src0_sel:DWORD src1_sel:BYTE_1\n\t" \
  "v_mfma_f32_16x16x32_bf16 v[36:39], v[236:239], v[202:205], v[36:39]\n\t" \
  "ds_read_b64_tr_b16 v[236:237], v254 offset:49192\n\t" \
  "v_lshlrev_b32_sdwa v226, 1, v158 dst_sel:DWORD dst_unused:UNUSED_PAD src0_sel:DWORD src1_sel:WORD_1\n\t" \
  "v_or_b32 v171, v232, v233\n\t" \
  "v_cvt_pk_bf16_f32 v10, v12, v13\n\t" \
  "v_cvt_pk_bf16_f32 v11, v14, v15\n\t" \
  "v_mfma_f32_16x16x32_bf16 v[40:43], v[240:243], v[202:205], v[40:43]\n\t" \
  "s_waitcnt lgkmcnt(2)\n\t" \
  "ds_read_b64_tr_b16 v[242:243], v254 offset:53288\n\t" \
  "ds_read_b64_tr_b16 v[210:211], v248 offset:1024\n\t" \
  "buffer_store_dwordx4 v[8:11], v250, s[20:23], s63 offen offset:64 sc1\n\t" \
  "ds_read_b32 v220, v220 offset:10016\n\t" \
  "ds_read_b64_tr_b16 v[240:241], v254 offset:52264\n\t" \
  "ds_read_b64_tr_b16 v[212:213], v248 offset:3072\n\t" \
  "v_or_b32 v170, v234, v235\n\t" \
  "v_cvt_pk_bf16_f32 v32, v32, v33\n\t" \
  "v_cvt_pk_bf16_f32 v16, v16, v17\n\t" \
  "v_mfma_f32_16x16x32_bf16 v[20:23], v[244:247], v[194:197], v[20:23]\n\t" \
  "ds_read_u16 v224, v224 offset:10528\n\t" \
  "v_fma_mix_f32 v231, |v161|, v231, v231 op_sel:[0,1,0] op_sel_hi:[0,1,1] clamp\n\t" \
  "v_cvt_pknorm_i16_f32 v228, |v164|, |v165|\n\t" \
  "v_cvt_pk_bf16_f32 v26, v28, v29\n\t" \
  "v_lshlrev_b32_sdwa v232, 1, v172 dst_sel:DWORD dst_unused:UNUSED_PAD src0_sel:DWORD src1_sel:WORD_1\n\t" \
  "v_mfma_f32_16x16x32_bf16 v[44:47], v[216:219], v[186:189], v[44:47]\n\t" \
  "ds_read_u16_d16_hi v227, v227 offset:10528\n\t" \
  "ds_read_b64_tr_b16 v[238:239], v254 offset:50216\n\t" \
  "v_fma_f32 v163, |v163|, v229, v163 mul:4\n\t" \
  "v_mfma_f32_16x16x32_bf16 v[20:23], v[216:219], v[178:181], v[20:23]\n\t" \
  "ds_read_b64_tr_b16 v[218:219], v248 offset:5120\n\t" \
  "v_cvt_pk_bf16_f32 v33, v34, v35\n\t" \
  "v_mfma_f32_16x16x32_bf16 v[44:47], v[244:247], v[202:205], v[44:47]\n\t" \
  "ds_read_b64_tr_b16 v[216:217], v248 offset:2048\n\t" \
  "ds_read_b64_tr_b16 v[214:215], v248 offset:4096\n\t" \
  "ds_read_b64_tr_b16 v[246:247], v254 offset:54312\n\t" \
  "v_cvt_pk_bf16_f32 v40, v40, v41\n\t" \
  "v_lshlrev_b32_sdwa v229, 2, v228 dst_sel:DWORD dst_unused:UNUSED_PAD src0_sel:DWORD src1_sel:BYTE_3\n\t" \
  "v_cvt_pk_bf16_f32 v35, v38, v39\n\t" \
  "s_waitcnt lgkmcnt(10)\n\t" \
  "v_cvt_pk_bf16_f32 v41, v42, v43\n\t" \
  "v_cvt_pk_bf16_f32 v163, v162, v163\n\t" \
  "v_mfma_f32_16x16x32_bf16 v[176:179], v[208:211], v[104:107], 0\n\t" \
  "v_cvt_pk_bf16_f32 v17, v18, v19\n\t" \
  "v_cvt_pk_bf16_f32 v19, v22, v23\n\t" \
  "v_fma_mix_f32 v230, |v160|, v230, v230 op_sel:[0,1,0] op_sel_hi:[0,1,1] clamp\n\t" \
  "v_mfma_f32_16x16x32_bf16 v[184:187], v[208:211], v[108:111], 0\n\t" \
  "s_waitcnt lgkmcnt(4)\n\t" \
  "ds_read_b64_tr_b16 v[210:211], v248 offset:1536\n\t" \
  "ds_read_b64_tr_b16 v[244:245], v254 offset:51240\n\t" \
  "ds_read_b64_tr_b16 v[208:209], v248 offset:512\n\t" \
  "ds_read_u16 v226, v226 offset:10528\n\t" \
  "v_fma_f32 v160, |v160|, v230, v160 mul:4\n\t" \
  "v_cvt_pknorm_i16_f32 v230, |v166|, |v167|\n\t" \
  "v_fma_f32 v161, |v161|, v231, v161 mul:4\n\t" \
  "v_cvt_pk_bf16_f32 v42, v44, v45\n\t" \
  "v_mfma_f32_16x16x32_bf16 v[192:195], v[236:239], v[104:107], 0\n\t" \
  "ds_read_u16 v232, v232 offset:10528\n\t" \
  "v_cvt_pk_bf16_f32 v146, v144, v145\n\t" \
  "v_lshlrev_b32_sdwa v231, 2, v230 dst_sel:DWORD dst_unused:UNUSED_PAD src0_sel:DWORD src1_sel:BYTE_3\n\t" \
  "v_mfma_f32_16x16x32_bf16 v[200:203], v[236:239], v[108:111], 0\n\t" \
  "ds_read_b32 v229, v229 offset:10016\n\t" \
  "ds_read_b64_tr_b16 v[236:237], v254 offset:49704\n\t" \
  "ds_read_u16_d16_hi v225, v225 offset:10528\n\t" \
  "v_fma_mix_f32 v223, |v151|, v223, v223 op_sel:[0,1,0] op_sel_hi:[0,1,1] clamp\n\t" \
  "v_mfma_f32_16x16x32_bf16 v[192:195], v[240:243], v[120:123], v[192:195]\n\t" \
  "s_waitcnt lgkmcnt(4)\n\t" \
  "ds_read_b64_tr_b16 v[238:239], v254 offset:50728\n\t" \
  "v_lshlrev_b32_sdwa v233, 1, v173 dst_sel:DWORD dst_unused:UNUSED_PAD src0_sel:DWORD src1_sel:WORD_1\n\t" \
  "v_cvt_pk_bf16_f32 v18, v20, v21\n\t" \
  "v_lshlrev_b32_sdwa v230, 2, v230 dst_sel:DWORD dst_unused:UNUSED_PAD src0_sel:DWORD src1_sel:BYTE_1\n\t" \
  "v_or_b32 v157, v226, v227\n\t" \
  "v_cvt_pk_bf16_f32 v34, v36, v37\n\t" \
  "v_mfma_f32_16x16x32_bf16 v[176:179], v[212:215], v[120:123], v[176:179]\n\t" \
  "ds_read_b32 v230, v230 offset:10016\n\t" \
  "v_lshlrev_b32_sdwa v235, 1, v175 dst_sel:DWORD dst_unused:UNUSED_PAD src0_sel:DWORD src1_sel:WORD_1\n\t" \
  "v_mfma_f32_16x16x32_bf16 v[184:187], v[212:215], v[124:127], v[184:187]\n\t" \
  "buffer_store_dwordx4 v[16:19], v250, s[20:23], s63 offen offset:128 sc1\n\t" \
  "ds_read_u16_d16_hi v233, v233 offset:10528\n\t" \
  "ds_read_b64_tr_b16 v[212:213], v248 offset:3584\n\t" \
  "v_lshlrev_b32_sdwa v228, 2, v228 dst_sel:DWORD dst_unused:UNUSED_PAD src0_sel:DWORD src1_sel:BYTE_1\n\t" \
  "v_fma_f32 v151, |v151|, v223, v151 mul:4\n\t" \
  "v_mfma_f32_16x16x32_bf16 v[176:179], v[216:219], v[136:139], v[176:179]\n\t" \
  "ds_read_b32 v228, v228 offset:10016\n\t" \
  "v_fma_mix_f32 v221, |v149|, v221, v221 op_sel:[0,1,0] op_sel_hi:[0,1,1] clamp\n\t" \
  "v_lshlrev_b32_sdwa v234, 1, v174 dst_sel:DWORD dst_unused:UNUSED_PAD src0_sel:DWORD src1_sel:WORD_1\n\t" \
  "v_cvt_pk_bf16_f32 v43, v46, v47\n\t" \
  "v_mfma_f32_16x16x32_bf16 v[200:203], v[240:243], v[124:127], v[200:203]\n\t" \
  "ds_read_b64_tr_b16 v[240:241], v254 offset:52776\n\t" \
  "ds_read_b64_tr_b16 v[242:243], v254 offset:53800\n\t" \
  "v_fma_f32 v149, |v149|, v221, v149 mul:4\n\t" \
  "v_fma_mix_f32 v220, |v148|, v220, v220 op_sel:[0,1,0] op_sel_hi:[0,1,1] clamp\n\t" \
  "v_fma_f32 v148, |v148|, v220, v148 mul:4\n\t" \
  "v_mfma_f32_16x16x32_bf16 v[200:203], v[244:247], v[140:143], v[200:203]\n\t" \
  "ds_read_b32 v231, v231 offset:10016\n\t" \
  "v_fma_mix_f32 v222, |v150|, v222, v222 op_sel:[0,1,0] op_sel_hi:[0,1,1] clamp\n\t" \
  "v_fma_f32 v150, |v150|, v222, v150 mul:4\n\t" \
  "v_cvt_pknorm_i16_f32 v222, |v176|, |v177|\n\t" \
  "v_cvt_pknorm_i16_f32 v220, |v178|, |v179|\n\t" \
  "v_mfma_f32_16x16x32_bf16 v[184:187], v[216:219], v[140:143], v[184:187]\n\t" \
  "ds_read_b64_tr_b16 v[214:215], v248 offset:4608\n\t" \
  "ds_read_b64_tr_b16 v[218:219], v248 offset:5632\n\t" \
  "ds_read_u16_d16_hi v235, v235 offset:10528\n\t" \
  "v_mfma_f32_16x16x32_bf16 v[192:195], v[244:247], v[136:139], v[192:195]\n\t" \
  "s_waitcnt lgkmcnt(9)\n\t" \
  "v_lshlrev_b32_sdwa v221, 2, v220 dst_sel:DWORD dst_unused:UNUSED_PAD src0_sel:DWORD src1_sel:BYTE_3\n\t" \
  "v_cvt_pk_bf16_f32 v162, v160, v161\n\t" \
  "v_cvt_pk_bf16_f32 v148, v148, v149\n\t" \
  "v_mfma_f32_16x16x32_bf16 v[180:183], v[208:211], v[104:107], 0\n\t" \
  "s_waitcnt lgkmcnt(3)\n\t" \
  "ds_read_b64_tr_b16 v[244:245], v254 offset:51752\n\t" \
  "v_fma_mix_f32 v230, |v166|, v230, v230 op_sel:[0,1,0] op_sel_hi:[0,1,1] clamp\n\t" \
  "v_lshlrev_b32_sdwa v226, 1, v184 dst_sel:DWORD dst_unused:UNUSED_PAD src0_sel:DWORD src1_sel:WORD_1\n\t" \
  "v_fma_mix_f32 v231, |v167|, v231, v231 op_sel:[0,1,0] op_sel_hi:[0,1,1] clamp\n\t" \
  "v_mfma_f32_16x16x32_bf16 v[188:191], v[208:211], v[108:111], 0\n\t" \
  "ds_read_b64_tr_b16 v[210:211], v249 offset:7688\n\t" \
  "ds_read_b64_tr_b16 v[246:247], v254 offset:54824\n\t" \
  "v_lshlrev_b32_sdwa v223, 2, v222 dst_sel:DWORD dst_unused:UNUSED_PAD src0_sel:DWORD src1_sel:BYTE_3\n\t" \
  "v_mfma_f32_16x16x32_bf16 v[204:207], v[236:239], v[108:111], 0\n\t" \
  "ds_read_b64_tr_b16 v[216:217], v248 offset:2560\n\t" \
  "v_or_b32 v156, v224, v225\n\t" \
  "v_lshlrev_b32_sdwa v225, 1, v187 dst_sel:DWORD dst_unused:UNUSED_PAD src0_sel:DWORD src1_sel:WORD_1\n\t" \
  "v_lshlrev_b32_sdwa v222, 2, v222 dst_sel:DWORD dst_unused:UNUSED_PAD src0_sel:DWORD src1_sel:BYTE_1\n\t" \
  "v_cvt_pk_bf16_f32 v149, v150, v151\n\t" \
  "v_mfma_f32_16x16x32_bf16 v[196:199], v[236:239], v[104:107], 0\n\t" \
  "buffer_store_dwordx4 v[24:27], v250, s[20:23], s62 offen offset:0 sc1\n\t" \
  "ds_read_b64_tr_b16 v[238:239], v249 offset:26136\n\t" \
  "ds_read_u16 v234, v234 offset:10528\n\t" \
  "ds_read_b32 v222, v222 offset:10016\n\t" \
  "ds_read_b32 v221, v221 offset:10016\n\t" \
  "v_or_b32 v172, v232, v233\n\t" \
  "v_lshlrev_b32_sdwa v233, 1, v203 dst_sel:DWORD dst_unused:UNUSED_PAD src0_sel:DWORD src1_sel:WORD_1\n\t" \
  "v_fma_mix_f32 v228, |v164|, v228, v228 op_sel:[0,1,0] op_sel_hi:[0,1,1] clamp\n\t" \
  "v_fma_f32 v164, |v164|, v228, v164 mul:4\n\t" \
  "v_mfma_f32_16x16x32_bf16 v[196:199], v[240:243], v[120:123], v[196:199]\n\t" \
  "s_waitcnt lgkmcnt(4)\n\t" \
  "ds_read_b64_tr_b16 v[236:237], v249 offset:24600\n\t" \
  "v_fma_f32 v167, |v167|, v231, v167 mul:4\n\t" \
  "v_mfma_f32_16x16x32_bf16 v[180:183], v[212:215], v[120:123], v[180:183]\n\t" \
  "ds_read_b64_tr_b16 v[208:209], v249 offset:6152\n\t" \
  "v_lshlrev_b32_sdwa v227, 1, v185 dst_sel:DWORD dst_unused:UNUSED_PAD src0_sel:DWORD src1_sel:WORD_1\n\t" \
  "v_mfma_f32_16x16x32_bf16 v[180:183], v[216:219], v[136:139], v[180:183]\n\t" \
  "v_fma_mix_f32 v229, |v165|, v229, v229 op_sel:[0,1,0] op_sel_hi:[0,1,1] clamp\n\t" \
  "v_lshlrev_b32_sdwa v220, 2, v220 dst_sel:DWORD dst_unused:UNUSED_PAD src0_sel:DWORD src1_sel:BYTE_1\n\t" \
  "v_mfma_f32_16x16x32_bf16 v[188:191], v[212:215], v[124:127], v[188:191]\n\t" \
  "ds_read_b64_tr_b16 v[214:215], v249 offset:7696\n\t" \
  "ds_read_u16_d16_hi v225, v225 offset:10528\n\t" \
  "v_fma_f32 v165, |v165|, v229, v165 mul:4\n\t" \
  "v_lshlrev_b32_sdwa v224, 1, v186 dst_sel:DWORD dst_unused:UNUSED_PAD src0_sel:DWORD src1_sel:WORD_1\n\t" \
  "v_mfma_f32_16x16x32_bf16 v[204:207], v[240:243], v[124:127], v[204:207]\n\t" \
  "s_nop 0\n\t" \
  "ds_read_b64_tr_b16 v[240:241], v249 offset:24608\n\t" \
  "ds_read_b64_tr_b16 v[242:243], v249 offset:26144\n\t" \
  "buffer_store_dwordx4 v[32:35], v250, s[20:23], s62 offen offset:64 sc1\n\t" \
  "ds_read_b32 v223, v223 offset:10016\n\t" \
  "ds_read_u16_d16_hi v227, v227 offset:10528\n\t" \
  "v_cvt_pknorm_i16_f32 v228, |v194|, |v195|\n\t" \
  "v_lshlrev_b32_sdwa v229, 2, v228 dst_sel:DWORD dst_unused:UNUSED_PAD src0_sel:DWORD src1_sel:BYTE_3\n\t" \
  "v_mfma_f32_16x16x32_bf16 v[196:199], v[244:247], v[136:139], v[196:199]\n\t" \
  "ds_read_b32 v229, v229 offset:10016\n\t" \
  "v_fma_f32 v166, |v166|, v230, v166 mul:4\n\t" \
  "v_mfma_f32_16x16x32_bf16 v[188:191], v[216:219], v[140:143], v[188:191]\n\t" \
  "s_waitcnt lgkmcnt(7)\n\t" \
  "ds_read_b64_tr_b16 v[218:219], v249 offset:7752\n\t" \
  "ds_read_b64_tr_b16 v[216:217], v249 offset:6216\n\t" \
  "v_lshlrev_b32_sdwa v228, 2, v228 dst_sel:DWORD dst_unused:UNUSED_PAD src0_sel:DWORD src1_sel:BYTE_1\n\t" \
  "v_cvt_pk_bf16_f32 v164, v164, v165\n\t" \
  "v_cvt_pk_bf16_f32 v165, v166, v167\n\t" \
  "v_mfma_f32_16x16x32_bf16 v[204:207], v[244:247], v[140:143], v[204:207]\n\t" \
  "ds_read_b64_tr_b16 v[212:213], v249 offset:6160\n\t" \
  "ds_read_u16 v224, v224 offset:10528\n\t" \
  "v_mfma_f32_16x16x32_bf16 v[72:75], v[208:211], v[154:157], v[72:75]\n\t" \
  "v_or_b32 v173, v234, v235\n\t" \
  "v_lshlrev_b32_sdwa v232, 1, v202 dst_sel:DWORD dst_unused:UNUSED_PAD src0_sel:DWORD src1_sel:WORD_1\n\t" \
  "v_mfma_f32_16x16x32_bf16 v[48:51], v[208:211], v[146:149], v[48:51]\n\t" \
  "ds_read_b64_tr_b16 v[246:247], v249 offset:26200\n\t" \
  "ds_read_b64_tr_b16 v[208:209], v249 offset:12304\n\t" \
  "ds_read_b32 v220, v220 offset:10016\n\t" \
  "ds_read_u16 v226, v226 offset:10528\n\t" \
  "v_mfma_f32_16x16x32_bf16 v[48:51], v[236:239], v[162:165], v[48:51]\n\t" \
  "s_waitcnt lgkmcnt(4)\n\t" \
  "v_or_b32 v187, v224, v225\n\t" \
  "v_lshlrev_b32_sdwa v234, 1, v200 dst_sel:DWORD dst_unused:UNUSED_PAD src0_sel:DWORD src1_sel:WORD_1\n\t" \
  "v_cvt_pknorm_i16_f32 v230, |v192|, |v193|\n\t" \
  "v_fma_mix_f32 v221, |v179|, v221, v221 op_sel:[0,1,0] op_sel_hi:[0,1,1] clamp\n\t" \
  "v_fma_f32 v179, |v179|, v221, v179 mul:4\n\t" \
  "v_mfma_f32_16x16x32_bf16 v[72:75], v[236:239], v[170:173], v[72:75]\n\t" \
  "ds_read_b64_tr_b16 v[236:237], v249 offset:30752\n\t" \
  "buffer_store_dwordx4 v[40:43], v250, s[20:23], s62 offen offset:128 sc1\n\t" \
  "ds_read_b64_tr_b16 v[244:245], v249 offset:24664\n\t" \
  "s_waitcnt lgkmcnt(2)\n\t" \
  "ds_read_u16 v232, v232 offset:10528\n\t" \
  "v_lshlrev_b32_sdwa v231, 2, v230 dst_sel:DWORD dst_unused:UNUSED_PAD src0_sel:DWORD src1_sel:BYTE_3\n\t" \
  "v_or_b32 v186, v226, v227\n\t" \
  "v_lshlrev_b32_sdwa v230, 2, v230 dst_sel:DWORD dst_unused:UNUSED_PAD src0_sel:DWORD src1_sel:BYTE_1\n\t" \
  "v_mfma_f32_16x16x32_bf16 v[52:55], v[212:215], v[146:149], v[52:55]\n\t" \
  "ds_read_b64_tr_b16 v[210:211], v249 offset:13840\n\t" \
  "ds_read_b32 v228, v228 offset:10016\n\t" \
  "v_fma_mix_f32 v220, |v178|, v220, v220 op_sel:[0,1,0] op_sel_hi:[0,1,1] clamp\n\t" \
  "v_fma_f32 v178, |v178|, v220, v178 mul:4\n\t" \
  "v_mfma_f32_16x16x32_bf16 v[76:79], v[212:215], v[154:157], v[76:79]\n\t" \
  "ds_read_b64_tr_b16 v[238:239], v249 offset:32288\n\t" \
  "ds_read_b32 v231, v231 offset:10016\n\t" \
  "v_lshlrev_b32_sdwa v224, 1, v188 dst_sel:DWORD dst_unused:UNUSED_PAD src0_sel:DWORD src1_sel:WORD_1\n\t" \
  "v_fma_mix_f32 v223, |v177|, v223, v223 op_sel:[0,1,0] op_sel_hi:[0,1,1] clamp\n\t" \
  "v_mfma_f32_16x16x32_bf16 v[76:79], v[240:243], v[170:173], v[76:79]\n\t" \
  "ds_read_b32 v230, v230 offset:10016\n\t" \
  "v_cvt_pknorm_i16_f32 v220, |v180|, |v181|\n\t" \
  "v_mfma_f32_16x16x32_bf16 v[52:55], v[240:243], v[162:165], v[52:55]\n\t" \
  "ds_read_b64_tr_b16 v[240:241], v249 offset:30760\n\t" \
  "ds_read_u16_d16_hi v233, v233 offset:10528\n\t" \
  "ds_read_b64_tr_b16 v[214:215], v249 offset:13848\n\t" \
  "v_fma_mix_f32 v229, |v195|, v229, v229 op_sel:[0,1,0] op_sel_hi:[0,1,1] clamp\n\t" \
  "v_lshlrev_b32_sdwa v221, 2, v220 dst_sel:DWORD dst_unused:UNUSED_PAD src0_sel:DWORD src1_sel:BYTE_3\n\t" \
  "v_lshlrev_b32_sdwa v235, 1, v201 dst_sel:DWORD dst_unused:UNUSED_PAD src0_sel:DWORD src1_sel:WORD_1\n\t" \
  "v_cvt_pk_bf16_f32 v179, v178, v179\n\t" \
  "v_mfma_f32_16x16x32_bf16 v[56:59], v[216:219], v[146:149], v[56:59]\n\t" \
  "s_waitcnt lgkmcnt(9)\n\t" \
  "ds_read_b64_tr_b16 v[212:213], v249 offset:12312\n\t" \
  "v_lshlrev_b32_sdwa v225, 1, v189 dst_sel:DWORD dst_unused:UNUSED_PAD src0_sel:DWORD src1_sel:WORD_1\n\t" \
  "v_lshlrev_b32_sdwa v220, 2, v220 dst_sel:DWORD dst_unused:UNUSED_PAD src0_sel:DWORD src1_sel:BYTE_1\n\t" \
  "v_mfma_f32_16x16x32_bf16 v[56:59], v[244:247], v[162:165], v[56:59]\n\t" \
  "s_waitcnt lgkmcnt(4)\n\t" \
  "ds_read_b64_tr_b16 v[242:243], v249 offset:32296\n\t" \
  "ds_read_u16 v234, v234 offset:10528\n\t" \
  "v_fma_f32 v195, |v195|, v229, v195 mul:4\n\t" \
  "v_fma_mix_f32 v230, |v192|, v230, v230 op_sel:[0,1,0] op_sel_hi:[0,1,1] clamp\n\t" \
  "v_fma_mix_f32 v231, |v193|, v231, v231 op_sel:[0,1,0] op_sel_hi:[0,1,1] clamp\n\t" \
  "v_fma_f32 v177, |v177|, v223, v177 mul:4\n\t" \
  "v_mfma_f32_16x16x32_bf16 v[80:83], v[216:219], v[154:157], v[80:83]\n\t" \
  "ds_read_b64_tr_b16 v[216:217], v249 offset:6224\n\t" \
  "ds_read_b64_tr_b16 v[218:219], v249 offset:7760\n\t" \
  "ds_read_b32 v220, v220 offset:10016\n\t" \
  "v_fma_f32 v193, |v193|, v231, v193 mul:4\n\t" \
  "v_fma_f32 v192, |v192|, v230, v192 mul:4\n\t" \
  "v_fma_mix_f32 v222, |v176|, v222, v222 op_sel:[0,1,0] op_sel_hi:[0,1,1] clamp\n\t" \
  "v_fma_f32 v176, |v176|, v222, v176 mul:4\n\t" \
  "v_cvt_pknorm_i16_f32 v222, |v182|, |v183|\n\t" \
  "v_lshlrev_b32_sdwa v223, 2, v222 dst_sel:DWORD dst_unused:UNUSED_PAD src0_sel:DWORD src1_sel:BYTE_3\n\t" \
  "v_lshlrev_b32_sdwa v222, 2, v222 dst_sel:DWORD dst_unused:UNUSED_PAD src0_sel:DWORD src1_sel:BYTE_1\n\t" \
  "v_cvt_pknorm_i16_f32 v230, |v198|, |v199|\n\t" \
  "v_lshlrev_b32_sdwa v231, 2, v230 dst_sel:DWORD dst_unused:UNUSED_PAD src0_sel:DWORD src1_sel:BYTE_3\n\t" \
  "v_cvt_pk_bf16_f32 v178, v176, v177\n\t" \
  "v_mfma_f32_16x16x32_bf16 v[80:83], v[244:247], v[170:173], v[80:83]\n\t" \
  "ds_read_b64_tr_b16 v[244:245], v249 offset:24672\n\t" \
  "ds_read_b32 v221, v221 offset:10016\n\t" \
  "ds_read_b64_tr_b16 v[246:247], v249 offset:26208\n\t" \
  "ds_read_u16_d16_hi v235, v235 offset:10528\n\t" \
  "ds_read_b32 v223, v223 offset:10016\n\t" \
  "v_fma_mix_f32 v228, |v194|, v228, v228 op_sel:[0,1,0] op_sel_hi:[0,1,1] clamp\n\t" \
  "v_fma_f32 v194, |v194|, v228, v194 mul:4\n\t" \
  "ds_read_u16 v224, v224 offset:10528\n\t" \
  "ds_read_b32 v222, v222 offset:10016\n\t" \
  "v_lshlrev_b32_sdwa v230, 2, v230 dst_sel:DWORD dst_unused:UNUSED_PAD src0_sel:DWORD src1_sel:BYTE_1\n\t" \
  "v_cvt_pknorm_i16_f32 v228, |v196|, |v197|\n\t" \
  "s_waitcnt lgkmcnt(7)\n\t" \
  "v_lshlrev_b32_sdwa v227, 1, v191 dst_sel:DWORD dst_unused:UNUSED_PAD src0_sel:DWORD src1_sel:WORD_1\n\t" \
  "v_lshlrev_b32_sdwa v229, 2, v228 dst_sel:DWORD dst_unused:UNUSED_PAD src0_sel:DWORD src1_sel:BYTE_3\n\t" \
  "v_lshlrev_b32_sdwa v228, 2, v228 dst_sel:DWORD dst_unused:UNUSED_PAD src0_sel:DWORD src1_sel:BYTE_1\n\t" \
  "v_fma_mix_f32 v220, |v180|, v220, v220 op_sel:[0,1,0] op_sel_hi:[0,1,1] clamp\n\t" \
  "s_waitcnt lgkmcnt(3)\n\t" \
  "ds_read_b32 v229, v229 offset:10016\n\t" \
  "ds_read_u16_d16_hi v225, v225 offset:10528\n\t" \
  "ds_read_b32 v228, v228 offset:10016\n\t" \
  "v_or_b32 v202, v234, v235\n\t" \
  "v_fma_mix_f32 v221, |v181|, v221, v221 op_sel:[0,1,0] op_sel_hi:[0,1,1] clamp\n\t" \
  "v_lshlrev_b32_sdwa v235, 1, v207 dst_sel:DWORD dst_unused:UNUSED_PAD src0_sel:DWORD src1_sel:WORD_1\n\t" \
  "v_lshlrev_b32_sdwa v226, 1, v190 dst_sel:DWORD dst_unused:UNUSED_PAD src0_sel:DWORD src1_sel:WORD_1\n\t" \
  "v_or_b32 v203, v232, v233\n\t" \
  "v_cvt_pk_bf16_f32 v195, v194, v195\n\t" \
  "v_lshlrev_b32_sdwa v232, 1, v204 dst_sel:DWORD dst_unused:UNUSED_PAD src0_sel:DWORD src1_sel:WORD_1\n\t" \
  "v_mfma_f32_16x16x32_bf16 v[84:87], v[216:219], v[154:157], v[84:87]\n\t" \
  "ds_read_b32 v230, v230 offset:10016\n\t" \
  "ds_read_u16_d16_hi v235, v235 offset:10528\n\t" \
  "ds_read_u16_d16_hi v227, v227 offset:10528\n\t" \
  "v_fma_f32 v181, |v181|, v221, v181 mul:4\n\t" \
  "v_lshlrev_b32_sdwa v233, 1, v205 dst_sel:DWORD dst_unused:UNUSED_PAD src0_sel:DWORD src1_sel:WORD_1\n\t" \
  "v_mfma_f32_16x16x32_bf16 v[60:63], v[216:219], v[146:149], v[60:63]\n\t" \
  "ds_read_b32 v231, v231 offset:10016\n\t" \
  "s_waitcnt lgkmcnt(5)\n\t" \
  "ds_read_u16 v226, v226 offset:10528\n\t" \
  "v_or_b32 v188, v224, v225\n\t" \
  "v_fma_mix_f32 v222, |v182|, v222, v222 op_sel:[0,1,0] op_sel_hi:[0,1,1] clamp\n\t" \
  "v_fma_f32 v182, |v182|, v222, v182 mul:4\n\t" \
  "v_fma_f32 v180, |v180|, v220, v180 mul:4\n\t" \
  "v_mfma_f32_16x16x32_bf16 v[84:87], v[244:247], v[170:173], v[84:87]\n\t" \
  "ds_read_u16_d16_hi v233, v233 offset:10528\n\t" \
  "ds_read_u16 v232, v232 offset:10528\n\t" \
  "v_lshlrev_b32_sdwa v234, 1, v206 dst_sel:DWORD dst_unused:UNUSED_PAD src0_sel:DWORD src1_sel:WORD_1\n\t" \
  "v_cvt_pk_bf16_f32 v180, v180, v181\n\t" \
  "v_mfma_f32_16x16x32_bf16 v[60:63], v[244:247], v[162:165], v[60:63]\n\t" \
  "ds_read_b64_tr_b16 v[216:217], v249 offset:6280\n\t" \
  "v_fma_mix_f32 v223, |v183|, v223, v223 op_sel:[0,1,0] op_sel_hi:[0,1,1] clamp\n\t" \
  "v_fma_f32 v183, |v183|, v223, v183 mul:4\n\t" \
  "v_cvt_pk_bf16_f32 v181, v182, v183\n\t" \
  "s_waitcnt lgkmcnt(3)\n\t" \
  "v_cvt_pk_bf16_f32 v194, v192, v193\n\t" \
  "v_mfma_f32_16x16x32_bf16 v[48:51], v[208:211], v[178:181], v[48:51]\n\t" \
  "ds_read_u16 v234, v234 offset:10528\n\t" \
  "v_fma_mix_f32 v229, |v197|, v229, v229 op_sel:[0,1,0] op_sel_hi:[0,1,1] clamp\n\t" \
  "v_or_b32 v189, v226, v227\n\t" \
  "v_fma_mix_f32 v230, |v198|, v230, v230 op_sel:[0,1,0] op_sel_hi:[0,1,1] clamp\n\t" \
  "v_fma_mix_f32 v228, |v196|, v228, v228 op_sel:[0,1,0] op_sel_hi:[0,1,1] clamp\n\t" \
  "v_mfma_f32_16x16x32_bf16 v[72:75], v[208:211], v[186:189], v[72:75]\n\t" \
  "ds_read_b64_tr_b16 v[208:209], v249 offset:12368\n\t" \
  "s_waitcnt lgkmcnt(3)\n\t" \
  "ds_read_b64_tr_b16 v[210:211], v249 offset:13904\n\t" \
  "v_or_b32 v204, v232, v233\n\t" \
  "v_fma_f32 v196, |v196|, v228, v196 mul:4\n\t" \
  "v_mfma_f32_16x16x32_bf16 v[52:55], v[212:215], v[178:181], v[52:55]\n\t" \
  "ds_read_b64_tr_b16 v[218:219], v249 offset:7816\n\t" \
  "s_waitcnt lgkmcnt(3)\n\t" \
  "v_or_b32 v205, v234, v235\n\t" \
  "v_fma_f32 v198, |v198|, v230, v198 mul:4\n\t" \
  "v_mfma_f32_16x16x32_bf16 v[76:79], v[212:215], v[186:189], v[76:79]\n\t" \
  "ds_read_b64_tr_b16 v[214:215], v249 offset:13912\n\t" \
  "ds_read_b64_tr_b16 v[212:213], v249 offset:12376\n\t" \
  "v_mfma_f32_16x16x32_bf16 v[72:75], v[236:239], v[202:205], v[72:75]\n\t" \
  "s_waitcnt lgkmcnt(3)\n\t" \
  "v_fma_f32 v197, |v197|, v229, v197 mul:4\n\t" \
  "v_cvt_pk_bf16_f32 v196, v196, v197\n\t" \
  "v_mfma_f32_16x16x32_bf16 v[76:79], v[240:243], v[202:205], v[76:79]\n\t" \
  "ds_read_b64_tr_b16 v[244:245], v249 offset:24728\n\t" \
  "ds_read_b64_tr_b16 v[246:247], v249 offset:26264\n\t" \
  "v_mfma_f32_16x16x32_bf16 v[80:83], v[208:211], v[186:189], v[80:83]\n\t" \
  "v_fma_mix_f32 v231, |v199|, v231, v231 op_sel:[0,1,0] op_sel_hi:[0,1,1] clamp\n\t" \
  "v_fma_f32 v199, |v199|, v231, v199 mul:4\n\t" \
  "v_cvt_pk_bf16_f32 v197, v198, v199\n\t" \
  "v_mfma_f32_16x16x32_bf16 v[56:59], v[208:211], v[178:181], v[56:59]\n\t" \
  "s_waitcnt lgkmcnt(2)\n\t" \
  "v_mfma_f32_16x16x32_bf16 v[48:51], v[236:239], v[194:197], v[48:51]\n\t" \
  "ds_read_b64_tr_b16 v[236:237], v249 offset:30816\n\t" \
  "v_cvt_pk_bf16_f32 v72, v72, v73\n\t" \
  "v_mfma_f32_16x16x32_bf16 v[60:63], v[212:215], v[178:181], v[60:63]\n\t" \
  "ds_read_b64_tr_b16 v[238:239], v249 offset:32352\n\t" \
  "v_cvt_pk_bf16_f32 v73, v74, v75\n\t" \
  "v_cvt_pk_bf16_f32 v75, v78, v79\n\t" \
  "v_cvt_pk_bf16_f32 v74, v76, v77\n\t" \
  "v_mfma_f32_16x16x32_bf16 v[52:55], v[240:243], v[194:197], v[52:55]\n\t" \
  "buffer_store_dwordx4 v[72:75], v250, s[20:23], s15 offen offset:0 sc1\n\t" \
  "ds_read_b64_tr_b16 v[242:243], v249 offset:32360\n\t" \
  "ds_read_b64_tr_b16 v[240:241], v249 offset:30824\n\t" \
  "v_cvt_pk_bf16_f32 v48, v48, v49\n\t" \
  "v_cvt_pk_bf16_f32 v49, v50, v51\n\t" \
  "v_mfma_f32_16x16x32_bf16 v[84:87], v[212:215], v[186:189], v[84:87]\n\t" \
  "s_waitcnt lgkmcnt(2)\n\t" \
  "ds_read_b64_tr_b16 v[214:215], v249 offset:13968\n\t" \
  "v_cvt_pk_bf16_f32 v50, v52, v53\n\t" \
  "v_mfma_f32_16x16x32_bf16 v[80:83], v[236:239], v[202:205], v[80:83]\n\t" \
  "v_cvt_pk_bf16_f32 v51, v54, v55\n\t" \
  "v_mfma_f32_16x16x32_bf16 v[56:59], v[236:239], v[194:197], v[56:59]\n\t" \
  "s_waitcnt lgkmcnt(1)\n\t" \
  "ds_read_b64_tr_b16 v[212:213], v249 offset:12432\n\t" \
  "v_mfma_f32_16x16x32_bf16 v[84:87], v[240:243], v[202:205], v[84:87]\n\t" \
  "ds_read_b64_tr_b16 v[208:209], v249 offset:6288\n\t" \
  "ds_read_b64_tr_b16 v[238:239], v249 offset:26272\n\t" \
  "v_mfma_f32_16x16x32_bf16 v[88:91], v[244:247], v[170:173], v[88:91]\n\t" \
  "ds_read_b64_tr_b16 v[236:237], v249 offset:24736\n\t" \
  "ds_read_b64_tr_b16 v[210:211], v249 offset:7824\n\t" \
  "v_mfma_f32_16x16x32_bf16 v[64:67], v[216:219], v[146:149], v[64:67]\n\t" \
  "v_cvt_pk_bf16_f32 v80, v80, v81\n\t" \
  "v_cvt_pk_bf16_f32 v81, v82, v83\n\t" \
  "v_cvt_pk_bf16_f32 v82, v84, v85\n\t" \
  "v_cvt_pk_bf16_f32 v83, v86, v87\n\t" \
  "v_mfma_f32_16x16x32_bf16 v[60:63], v[240:243], v[194:197], v[60:63]\n\t" \
  "buffer_store_dwordx4 v[80:83], v250, s[20:23], s15 offen offset:64 sc1\n\t" \
  "ds_read_b64_tr_b16 v[242:243], v249 offset:32416\n\t" \
  "ds_read_b64_tr_b16 v[240:241], v249 offset:30880\n\t" \
  "s_waitcnt lgkmcnt(6)\n\t" \
  "v_mfma_f32_16x16x32_bf16 v[64:67], v[244:247], v[162:165], v[64:67]\n\t" \
  "ds_read_b64_tr_b16 v[246:247], v249 offset:32424\n\t" \
  "v_cvt_pk_bf16_f32 v56, v56, v57\n\t" \
  "v_mfma_f32_16x16x32_bf16 v[88:91], v[216:219], v[154:157], v[88:91]\n\t" \
  "ds_read_b64_tr_b16 v[216:217], v249 offset:12440\n\t" \
  "v_mfma_f32_16x16x32_bf16 v[64:67], v[212:215], v[178:181], v[64:67]\n\t" \
  "buffer_store_dwordx4 v[48:51], v250, s[20:23], s14 offen offset:0 sc1\n\t" \
  "ds_read_b64_tr_b16 v[218:219], v249 offset:13976\n\t" \
  "s_setprio 0\n\t" \
  "v_mfma_f32_16x16x32_bf16 v[88:91], v[212:215], v[186:189], v[88:91]\n\t" \
  "s_waitcnt lgkmcnt(3)\n\t" \
  "v_mfma_f32_16x16x32_bf16 v[64:67], v[240:243], v[194:197], v[64:67]\n\t" \
  "v_mfma_f32_16x16x32_bf16 v[68:71], v[208:211], v[146:149], v[68:71]\n\t" \
  "v_cvt_pk_bf16_f32 v57, v58, v59\n\t" \
  "v_mfma_f32_16x16x32_bf16 v[92:95], v[236:239], v[170:173], v[92:95]\n\t" \
  "ds_read_b64_tr_b16 v[244:245], v249 offset:30888\n\t" \
  "v_mfma_f32_16x16x32_bf16 v[92:95], v[208:211], v[154:157], v[92:95]\n\t" \
  "s_waitcnt lgkmcnt(1)\n\t" \
  "v_mfma_f32_16x16x32_bf16 v[92:95], v[216:219], v[186:189], v[92:95]\n\t" \
  "s_nop 0\n\t" \
  "v_cvt_pk_bf16_f32 v64, v64, v65\n\t" \
  "v_mfma_f32_16x16x32_bf16 v[68:71], v[216:219], v[178:181], v[68:71]\n\t" \
  "s_waitcnt lgkmcnt(0)\n\t" \
  "v_cvt_pk_bf16_f32 v58, v60, v61\n\t" \
  "v_mfma_f32_16x16x32_bf16 v[88:91], v[240:243], v[202:205], v[88:91]\n\t" \
  "v_cvt_pk_bf16_f32 v59, v62, v63\n\t" \
  "v_mfma_f32_16x16x32_bf16 v[68:71], v[236:239], v[162:165], v[68:71]\n\t" \
  "v_mfma_f32_16x16x32_bf16 v[68:71], v[244:247], v[194:197], v[68:71]\n\t" \
  "v_cvt_pk_bf16_f32 v65, v66, v67\n\t" \
  "v_mfma_f32_16x16x32_bf16 v[92:95], v[244:247], v[202:205], v[92:95]\n\t" \
  "s_nop 2\n\t" \
  "v_cvt_pk_bf16_f32 v88, v88, v89\n\t" \
  "s_nop 1\n\t" \
  "v_cvt_pk_bf16_f32 v66, v68, v69\n\t" \
  "v_cvt_pk_bf16_f32 v89, v90, v91\n\t" \
  "v_cvt_pk_bf16_f32 v91, v94, v95\n\t" \
  "v_cvt_pk_bf16_f32 v90, v92, v93\n\t" \
  "v_cvt_pk_bf16_f32 v67, v70, v71\n\t" \
  "buffer_store_dwordx4 v[88:91], v250, s[20:23], s15 offen offset:128 sc1\n\t" \
  "buffer_store_dwordx4 v[56:59], v250, s[20:23], s14 offen offset:64 sc1\n\t" \
  "buffer_store_dwordx4 v[64:67], v250, s[20:23], s14 offen offset:128 sc1\n\t" \
  "s_waitcnt vmcnt(0)\n\t" \
  ".Lcnx_end:\n\t" \
  ""


__device__ __forceinline__ unsigned lds_addr(const char* p) { return (unsigned)(uintptr_t)(lds_ptr_t)p; }
__device__ __forceinline__ unsigned sg(unsigned v) { return __builtin_amdgcn_readfirstlane(v); }

constexpr double tanh_u(double y) {
    const double v = 8 * y, a = 2 * 0.79788456080286536 * (v + 0.044715 * v * v * v) / 1024;
    double m = 1;
    for (int i = 12; i > 1; --i) m = 1 + m * a / i;
    m *= a;
    for (int i = 0; i < 10; ++i) m *= m + 2;
    return m / (m + 2);
}
constexpr double h2d(int h) {
    if (h < 1024) return 0;
    double x = (1024 + (h & 1023)) * 0x1p-24;
    for (int e = 1; e < (h >> 10); ++e) x *= 2;
    return x;
}
constexpr int d2h(double x) {
    int h = 0;
    for (int s = 1 << 14; s; s >>= 1) if (h2d(h + s) <= x) h += s;
    return h;
}
constexpr unsigned ip_entry(int k) {
    if (k == 127) return 0x3c003c00u;
    const double y0 = k ? (256 * k - 0.5) / 32767 : 0, dy = ((256 * k + 255.5) / 32767 - y0) / 8;
    double t[9] = {};
    for (int i = 0; i < 9; ++i) t[i] = tanh_u(y0 + i * dy);
    const double b0 = (t[8] - t[0]) / (8 * dy), ym = y0 + 4 * dy, tm = (t[4] + (t[0] + t[8]) / 2) / 2;
    unsigned best = 0;
    double err = 1;
    for (int ha = d2h(tm - b0 * ym) - 1, i = 0; i < 4; ++i, ++ha)
        for (int hb = d2h((tm - h2d(ha)) / ym), j = 0; j < 2; ++j, ++hb) {
            double e = 0;
            for (int n = 0; n < 9; ++n) {
                const double s = h2d(ha) + h2d(hb) * (y0 + n * dy), d = (s < 1 ? s : 1) - t[n];
                e = d > e ? d : -d > e ? -d : e;
            }
            if (e < err) err = e, best = (ha < 1024 ? 0 : ha) | (hb < 1024 ? 0 : hb) << 16;
        }
    return best;
}
constexpr unsigned g_entry(int e, bool neg) {
    double y = e ? ((e & 127) + 0.5 + (e >> 7 ? 128 : 0)) * 0x1p-23 / (1 + 1.0 / 384) : 0;
    for (int i = 1; i < (e >> 7); ++i) y *= 2;
    const double t = y < 1 ? tanh_u(y) : 1;
    const unsigned u = __builtin_bit_cast(unsigned, (float)(4 * y * (neg ? t - 1 : t + 1)));
    return (u + 0x7fff + (u >> 16 & 1)) >> 16;
}
struct TanhTab { unsigned v[1024]; };
constexpr TanhTab gelu_tab(int k0) {
    TanhTab t = {};
    for (int k = k0; k < k0 + 1024 && k < 128 + GSZ / 2; ++k) {
        const int e = 2 * (k < 2048 ? k - 128 : k - 2048);
        t.v[k - k0] = k < 128 ? ip_entry(k) : g_entry(e, k >= 2048) | g_entry(e + 1, k >= 2048) << 16;
    }
    return t;
}
struct WaveTab { unsigned v192[64][4], v244[64][4], v248[64][4], s32[32]; };
struct RegTab { WaveTab w[8]; };
constexpr unsigned cpos(unsigned w, unsigned px) { return (w >> 1) * 1024 + (px ^ (w & 2) << 2) * 16 + ((w ^ px >> 4) & 1) * 8; }
constexpr RegTab reg_tab() {
    RegTab t = {};
    for (unsigned l = 0; l < 64; ++l) {
        const unsigned G = l >> 4, n = l & 15, q = n >> 2, p = l & 3;
        for (unsigned w = 0; w < 8; ++w) {
            t.w[w].v248[l][0] = WB + (l >> 2) * 32 + (p & 1) * 16 + (p >> 1) * 8;
            t.w[w].v248[l][1] = WB + (G & 1) * 3080 + ((G >> 1) * 4 + q) * 192 + p * 16;
            t.w[w].v248[l][2] = (n & 7) * 384 + (n >> 3) * 192 + G * 16;
            t.w[w].v248[l][3] = PB + l * 8;
            t.w[w].v244[l][2] = XOFF + n * 400 + (G & 1) * XROW + (G >> 1) * 192 + w * 24;
            t.w[w].v244[l][3] = dw_pos(3 * w) + p * 192 + (1 - (q >> 1)) * 24 + (1 + (G & 1) - (q & 1)) * 8 + ((G ^ p) >> 1) * 4;
            const unsigned pl = PTOP + (w >> 2) * 12288, blk = (w & 3) * 16;
            t.w[w].v192[l][0] = XOFF + cpos(w, (G & 2) * 16 + (n & 8) * 2 + (G & 1) * 8 + (n & 7));
            t.w[w].v192[l][1] = t.w[w].v192[l][0] + PTOP - XOFF;
            t.w[w].v192[l][2] = pl + (cpos((G & 1) * 4 + (G & 2), blk + n) & ~8u);
            t.w[w].v192[l][3] = pl + cpos(p, blk + (l >> 2));
            const unsigned u = (w & 3) * 48 + l, j = u & 63;
            t.w[w].v244[l][0] = w < 4 ? (4 * (u >> 6) + 12 * ((j >> 3) & 3) + ((j >> 1) & 3)) * (HID * 2) + (2 * (j & 1) + (j >> 5)) * 16 : u * 16;
            t.w[w].v244[l][1] = dw_pos(w * 8 + (l >> 5) * 4 + p) + ((l >> 2) & 7) * 24 + (l & 1) * 192;
        }
    }
    for (unsigned w = 0; w < 8; ++w) {
        const bool w1 = w < 4;
        const unsigned dm = w1 ? 3072 : 3080, wl = WB + (w & 3) * 768, wc = w1 ? 64 : 6144;
        const unsigned rp[4] = {x_pos(3 + (w >> 1 ^ 2)), x_pos(7 + (w >> 1)), x_pos((11 + (w >> 1)) % 12), x_pos(3 + (w >> 1))};
        const unsigned s[32] = {wc, 0, w1 ? 48u * HID * 2 : 3072, dm, dm + XOFF - WB, w >> 2, w, w * 512,
                                0xaaaaaaaau, 0xaaaaaaaau, 0xccccccccu, 0xccccccccu,
                                wl + (w1 ? WA + 12304 : WA), wl + (w1 ? 0 : WA + 6152), wl + (w1 ? WA + 18448 : 6152),
                                wl + (w1 ? WA + 18448 + 6144 : 12304), w1 ? -11 * wc : wc, w1 ? wc : -11 * wc,
                                0u + XOFF - WB, 4 * wc, ~0x00f0000fu, ~0xf0000f00u,
                                (w >> 2 ? PTOP - 12288u : XHI + 6 * XROW) - XOFF - 4 * 12288, 0x07060302, dw_pos(3 * w + 2) - dw_pos(3 * w), 0,
                                rp[1] - rp[0], rp[2] - rp[1], rp[3] - rp[2], rp[0] + 592 + (w & 1) * 3200, 0, 8 * (unsigned)sizeof(WaveTab) - 2 * PB};
        for (int i = 0; i < 32; ++i) t.w[w].s32[i] = s[i];
    }
    return t;
}
struct Tables { RegTab r; TanhTab t[4]; };
static_assert(sizeof(WaveTab) == 64 * 50 && sizeof(RegTab) == 25600, "table layout");
__device__ const __attribute__((aligned(128))) Tables TABLES = {reg_tab(), {gelu_tab(0), gelu_tab(1024), gelu_tab(2048), gelu_tab(3072)}};

#define R10(r, a) r #a "0", r #a "1", r #a "2", r #a "3", r #a "4", r #a "5", r #a "6", r #a "7", r #a "8", r #a "9"
#define V10(a) R10("v", a)
#define S10(a) R10("s", a)
#define MAIN_CLOB "memory", "scc", "vcc", "v1", "v2", "v3", "v4", "v5", "v6", "v7", "v8", "v9", V10(1), V10(2), V10(3), V10(4), V10(5),  \
    V10(6), V10(7), V10(8), V10(9), V10(10), V10(11), V10(12), V10(13), V10(14), V10(15), V10(16), V10(17), V10(18), V10(19),        \
    V10(20), V10(21), V10(22), V10(23), V10(24), "v250", "v251", "v252", "v253", "v254", "s4", "s5", "s6", "s7", "s8", "s9", S10(1),       \
    S10(2), S10(3), S10(4), S10(5), "s60", "s61", "s62", "s63", "s64", "s65", "s66", "s67", "s68", "s69"

__global__ __launch_bounds__(512, 2) void convnext_kernel(const __hip_bfloat16* __restrict__ x,
                                                          const __hip_bfloat16* __restrict__ dw,
                                                          const __hip_bfloat16* __restrict__ w1,
                                                          const __hip_bfloat16* __restrict__ w2,
                                                          __hip_bfloat16* __restrict__ out) {
    __shared__ __attribute__((aligned(16))) char smem[LDS_BYTES];

    unsigned tid = threadIdx.x, lds = lds_addr(smem);
    const unsigned bid = blockIdx.x;
    asm volatile(MAIN_ASM
                 : "+{v0}"(tid), "+{s3}"(lds)
                 : "{s[0:1]}"(__builtin_amdgcn_kernarg_segment_ptr()), "{s2}"(bid), [tab] "i"(&TABLES)
                 : MAIN_CLOB);

}

}

extern "C" void kernel_launch(void* const* d_in, const int* in_sizes, int n_in, void* d_out, int out_size,
                              void* d_ws, size_t ws_size, hipStream_t stream) {
    (void)in_sizes; (void)n_in; (void)out_size; (void)d_ws; (void)ws_size;
    const __hip_bfloat16* x = (const __hip_bfloat16*)d_in[0];
    const __hip_bfloat16* dw = (const __hip_bfloat16*)d_in[1];
    const __hip_bfloat16* w1 = (const __hip_bfloat16*)d_in[2];
    const __hip_bfloat16* w2 = (const __hip_bfloat16*)d_in[3];
    __hip_bfloat16* out = (__hip_bfloat16*)d_out;
    convnext_kernel<<<256, 512, 0, stream>>>(x, dw, w1, w2, out);
}
```
